```python
import torch

def _softmax(x, axis=-1):
    return torch.softmax(x, dim=axis)
def _gelu(x):
    return torch.nn.functional.gelu(x, approximate="tanh")
def _silu(x):
    return torch.nn.functional.silu(x)
def _sigmoid(x):
    return torch.sigmoid(x)
def _layernorm(x, axis=-1, eps=1e-5):
    m = x.mean(dim=axis, keepdim=True)
    v = x.var(dim=axis, keepdim=True, unbiased=False)
    return (x - m) / torch.sqrt(v + eps)
def _scatter_add(tgt, idx, src, axis=0):
    return tgt.clone().index_add_(axis, idx, src)
def _segment_sum(src, seg, n):
    out = torch.zeros((n,) + src.shape[1:], dtype=src.dtype)
    return out.index_add_(0, seg, src)
def _pad(x, pw):
    flat = tuple(v for pair in reversed(pw) for v in pair)
    return torch.nn.functional.pad(x, flat)

kernel_name = 'standalone__modern_conv_block_replknet__vision'

def setup_inputs():

    _w = (lambda a: (lambda t: t.to(torch.bfloat16) if t.is_floating_point() else t)(torch.as_tensor(a)))
    H = W = 512
    C = 96
    K = 3
    return {
        "x": _w((lambda *s: torch.randn(*s, dtype=torch.bfloat16) * 0.05)(1, H, W, C)),
        "dw_large": _w((lambda *s: torch.randn(*s, dtype=torch.bfloat16) * 0.4)(K, K, C) * (1.0 / K)),
        "dw_small": _w((lambda *s: torch.randn(*s, dtype=torch.bfloat16) * 0.4)(5, 5, C) * 0.2),
        "w1": _w((lambda *s: torch.randn(*s, dtype=torch.bfloat16) * 0.4)(C, 4 * C) * (C ** -0.5)),
        "w2": _w((lambda *s: torch.randn(*s, dtype=torch.bfloat16) * 0.1)(4 * C, C) * ((4 * C) ** -0.5)),
    }

def reference(x, dw_large, dw_small, w1, w2):
    B, H, W, C = x.shape
    Kl = dw_large.shape[0]
    pl = (Kl - 1) // 2
    xpl = _pad(x, ((0,0),(pl,pl),(pl,pl),(0,0)))
    out = torch.zeros_like(x)
    for i in range(Kl):
        for j in range(Kl):
            out = out + xpl[:, i:i+H, j:j+W, :] * dw_large[i, j][None, None, None, :]
    Ks = dw_small.shape[0]
    ps = (Ks - 1) // 2
    xps = _pad(x, ((0,0),(ps,ps),(ps,ps),(0,0)))
    for i in range(Ks):
        for j in range(Ks):
            out = out + xps[:, i:i+H, j:j+W, :] * dw_small[i, j][None, None, None, :]
    h = _layernorm(out, axis=-1)
    h = _gelu(torch.einsum('bhwc,ce->bhwe', h, w1))
    h = torch.einsum('bhwe,ec->bhwc', h, w2)
    return x + h
```

```cpp
#include <hip/hip_runtime.h>
#include <hip/hip_bf16.h>

typedef unsigned u32;
typedef unsigned short u16;
typedef unsigned long long u64;
typedef u32 u32x4 __attribute__((ext_vector_type(4)));
typedef u32 u32x8 __attribute__((ext_vector_type(8)));
typedef u32 u32x16 __attribute__((ext_vector_type(16)));
typedef _Float16 f16x2 __attribute__((ext_vector_type(2)));
typedef __bf16 bf16x2 __attribute__((ext_vector_type(2)));
typedef float f32x2 __attribute__((ext_vector_type(2)));
#define DI __device__ __forceinline__
#define LDSP(T, off) ((__attribute__((address_space(3))) T*)(smem3 + (off)))

constexpr int IMG = 512;
constexpr int CH = 96;
constexpr int HID = 384;
constexpr int PXB = 192;
constexpr int ROWB = 98304;
constexpr int TOEP_CS = 336;
constexpr int XT_RS = 320;
constexpr int XT_PP = 1360;
constexpr int XT_CG = 6736;
constexpr int GS = 6160;
constexpr int L_LUT = 0;
constexpr int L_TOEP = 8208;
constexpr int L_W = 40960;
constexpr int L_LUTN = 65536;
constexpr int L_XT = 81920;
constexpr int L_H = 108864;
constexpr int L_EX = 133504;
constexpr int L_MISC = 162752;
constexpr int L_TOTAL = 162816;
constexpr int LUTK = 106;
constexpr u32 XBYTES = IMG * IMG * PXB;
constexpr float GAMMA = -0.4684938781597355f;
constexpr float GC1 = -2.302208198144325f, GC3 = -0.1029432395800235f;
constexpr u32 MU_PK = 0x44ea44eau, CC_PK = 0xb77fb77fu;
__device__ const u32 CTAB[9 * 1024] = {
3722283144u,52428,108864,115024,3722283144u,56797,108880,115040,3722283144u,61166,108896,115056,3722283144u,52428,108912,115072,3722283144u,52428,108928,115088,3722283144u,56797,108944,115104,3722283144u,61166,108960,115120,3722283144u,52428,108976,115136,3722283144u,52428,108992,115152,3722283144u,56797,109008,115168,3722283144u,61166,109024,115184,3722283144u,52428,109040,115200,3722283144u,52428,109056,115216,3722283144u,56797,109072,115232,3722283144u,61166,109088,115248,3722283144u,52428,109104,115264,
3722283144u,52428,109312,115472,3722283144u,56797,109328,115488,3722283144u,61166,109344,115504,3722283144u,52428,109360,115520,3722283144u,52428,109248,115408,3722283144u,56797,109264,115424,3722283144u,61166,109280,115440,3722283144u,52428,109296,115456,3722283144u,52428,109184,115344,3722283144u,56797,109200,115360,3722283144u,61166,109216,115376,3722283144u,52428,109232,115392,3722283144u,52428,109120,115280,3722283144u,56797,109136,115296,3722283144u,61166,109152,115312,3722283144u,52428,109168,115328,
3722283144u,52428,109440,115600,3722283144u,56797,109456,115616,3722283144u,61166,109472,115632,3722283144u,52428,109488,115648,3722283144u,52428,109376,115536,3722283144u,56797,109392,115552,3722283144u,61166,109408,115568,3722283144u,52428,109424,115584,3722283144u,52428,109568,115728,3722283144u,56797,109584,115744,3722283144u,61166,109600,115760,3722283144u,52428,109616,115776,3722283144u,52428,109504,115664,3722283144u,56797,109520,115680,3722283144u,61166,109536,115696,3722283144u,52428,109552,115712,
3722283144u,52428,109760,115920,3722283144u,56797,109776,115936,3722283144u,61166,109792,115952,3722283144u,52428,109808,115968,3722283144u,52428,109824,115984,3722283144u,56797,109840,116000,3722283144u,61166,109856,116016,3722283144u,52428,109872,116032,3722283144u,52428,109632,115792,3722283144u,56797,109648,115808,3722283144u,61166,109664,115824,3722283144u,52428,109680,115840,3722283144u,52428,109696,115856,3722283144u,56797,109712,115872,3722283144u,61166,109728,115888,3722283144u,52428,109744,115904,
3722283144u,52428,115024,108864,3722283144u,56797,115040,108880,3722283144u,61166,115056,108896,3722283144u,52428,115072,108912,3722283144u,52428,115088,108928,3722283144u,56797,115104,108944,3722283144u,61166,115120,108960,3722283144u,52428,115136,108976,3722283144u,52428,115152,108992,3722283144u,56797,115168,109008,3722283144u,61166,115184,109024,3722283144u,52428,115200,109040,3722283144u,52428,115216,109056,3722283144u,56797,115232,109072,3722283144u,61166,115248,109088,3722283144u,52428,115264,109104,
3722283144u,52428,115472,109312,3722283144u,56797,115488,109328,3722283144u,61166,115504,109344,3722283144u,52428,115520,109360,3722283144u,52428,115408,109248,3722283144u,56797,115424,109264,3722283144u,61166,115440,109280,3722283144u,52428,115456,109296,3722283144u,52428,115344,109184,3722283144u,56797,115360,109200,3722283144u,61166,115376,109216,3722283144u,52428,115392,109232,3722283144u,52428,115280,109120,3722283144u,56797,115296,109136,3722283144u,61166,115312,109152,3722283144u,52428,115328,109168,
3722283144u,52428,115600,109440,3722283144u,56797,115616,109456,3722283144u,61166,115632,109472,3722283144u,52428,115648,109488,3722283144u,52428,115536,109376,3722283144u,56797,115552,109392,3722283144u,61166,115568,109408,3722283144u,52428,115584,109424,3722283144u,52428,115728,109568,3722283144u,56797,115744,109584,3722283144u,61166,115760,109600,3722283144u,52428,115776,109616,3722283144u,52428,115664,109504,3722283144u,56797,115680,109520,3722283144u,61166,115696,109536,3722283144u,52428,115712,109552,
3722283144u,52428,115920,109760,3722283144u,56797,115936,109776,3722283144u,61166,115952,109792,3722283144u,52428,115968,109808,3722283144u,52428,115984,109824,3722283144u,56797,116000,109840,3722283144u,61166,116016,109856,3722283144u,52428,116032,109872,3722283144u,52428,115792,109632,3722283144u,56797,115808,109648,3722283144u,61166,115824,109664,3722283144u,52428,115840,109680,3722283144u,52428,115856,109696,3722283144u,56797,115872,109712,3722283144u,61166,115888,109728,3722283144u,52428,115904,109744,
3722283144u,52428,121184,127344,3722283144u,56797,121200,127360,3722283144u,61166,121216,127376,3722283144u,52428,121232,127392,3722283144u,52428,121248,127408,3722283144u,56797,121264,127424,3722283144u,61166,121280,127440,3722283144u,52428,121296,127456,3722283144u,52428,121312,127472,3722283144u,56797,121328,127488,3722283144u,61166,121344,127504,3722283144u,52428,121360,127520,3722283144u,52428,121376,127536,3722283144u,56797,121392,127552,3722283144u,61166,121408,127568,3722283144u,52428,121424,127584,
3722283144u,52428,121632,127792,3722283144u,56797,121648,127808,3722283144u,61166,121664,127824,3722283144u,52428,121680,127840,3722283144u,52428,121568,127728,3722283144u,56797,121584,127744,3722283144u,61166,121600,127760,3722283144u,52428,121616,127776,3722283144u,52428,121504,127664,3722283144u,56797,121520,127680,3722283144u,61166,121536,127696,3722283144u,52428,121552,127712,3722283144u,52428,121440,127600,3722283144u,56797,121456,127616,3722283144u,61166,121472,127632,3722283144u,52428,121488,127648,
3722283144u,52428,121760,127920,3722283144u,56797,121776,127936,3722283144u,61166,121792,127952,3722283144u,52428,121808,127968,3722283144u,52428,121696,127856,3722283144u,56797,121712,127872,3722283144u,61166,121728,127888,3722283144u,52428,121744,127904,3722283144u,52428,121888,128048,3722283144u,56797,121904,128064,3722283144u,61166,121920,128080,3722283144u,52428,121936,128096,3722283144u,52428,121824,127984,3722283144u,56797,121840,128000,3722283144u,61166,121856,128016,3722283144u,52428,121872,128032,
3722283144u,52428,122080,128240,3722283144u,56797,122096,128256,3722283144u,61166,122112,128272,3722283144u,52428,122128,128288,3722283144u,52428,122144,128304,3722283144u,56797,122160,128320,3722283144u,61166,122176,128336,3722283144u,52428,122192,128352,3722283144u,52428,121952,128112,3722283144u,56797,121968,128128,3722283144u,61166,121984,128144,3722283144u,52428,122000,128160,3722283144u,52428,122016,128176,3722283144u,56797,122032,128192,3722283144u,61166,122048,128208,3722283144u,52428,122064,128224,
3722283144u,52428,127344,121184,3722283144u,56797,127360,121200,3722283144u,61166,127376,121216,3722283144u,52428,127392,121232,3722283144u,52428,127408,121248,3722283144u,56797,127424,121264,3722283144u,61166,127440,121280,3722283144u,52428,127456,121296,3722283144u,52428,127472,121312,3722283144u,56797,127488,121328,3722283144u,61166,127504,121344,3722283144u,52428,127520,121360,3722283144u,52428,127536,121376,3722283144u,56797,127552,121392,3722283144u,61166,127568,121408,3722283144u,52428,127584,121424,
3722283144u,52428,127792,121632,3722283144u,56797,127808,121648,3722283144u,61166,127824,121664,3722283144u,52428,127840,121680,3722283144u,52428,127728,121568,3722283144u,56797,127744,121584,3722283144u,61166,127760,121600,3722283144u,52428,127776,121616,3722283144u,52428,127664,121504,3722283144u,56797,127680,121520,3722283144u,61166,127696,121536,3722283144u,52428,127712,121552,3722283144u,52428,127600,121440,3722283144u,56797,127616,121456,3722283144u,61166,127632,121472,3722283144u,52428,127648,121488,
3722283144u,52428,127920,121760,3722283144u,56797,127936,121776,3722283144u,61166,127952,121792,3722283144u,52428,127968,121808,3722283144u,52428,127856,121696,3722283144u,56797,127872,121712,3722283144u,61166,127888,121728,3722283144u,52428,127904,121744,3722283144u,52428,128048,121888,3722283144u,56797,128064,121904,3722283144u,61166,128080,121920,3722283144u,52428,128096,121936,3722283144u,52428,127984,121824,3722283144u,56797,128000,121840,3722283144u,61166,128016,121856,3722283144u,52428,128032,121872,
3722283144u,52428,128240,122080,3722283144u,56797,128256,122096,3722283144u,61166,128272,122112,3722283144u,52428,128288,122128,3722283144u,52428,128304,122144,3722283144u,56797,128320,122160,3722283144u,61166,128336,122176,3722283144u,52428,128352,122192,3722283144u,52428,128112,121952,3722283144u,56797,128128,121968,3722283144u,61166,128144,121984,3722283144u,52428,128160,122000,3722283144u,52428,128176,122016,3722283144u,56797,128192,122032,3722283144u,61166,128208,122048,3722283144u,52428,128224,122064,
40960,393216,2147483648u,384,40976,393408,400,2147483664u,40992,393600,2147483680u,416,41008,393792,432,2147483696u,41024,393984,768,1152,41040,394176,1168,784,41056,394368,800,1184,41072,394560,1200,816,41088,394752,1536,1920,41104,394944,1936,1552,41120,395136,1568,1952,41136,395328,1968,1584,41152,395520,2304,2688,41168,395712,2704,2320,41184,395904,2336,2720,41200,396096,2736,2352,
41216,393232,3072,1073745280,41232,393424,1073745296,3088,41248,393616,3104,1073745312,41264,393808,1073745328,3120,41280,394000,2147876864u,393600,41296,394192,393616,2147876880u,41312,394384,2147876896u,393632,41328,394576,393648,2147876912u,41344,394768,393984,394368,41360,394960,394384,394000,41376,395152,394016,394400,41392,395344,394416,394032,41408,395536,394752,395136,41424,395728,395152,394768,41440,395920,394784,395168,41456,396112,395184,394800,
41472,393248,395520,395904,41488,393440,395920,395536,41504,393632,395552,395936,41520,393824,395952,395568,41536,394016,396288,1074138496,41552,394208,1074138512,396304,41568,394400,396320,1074138528,41584,394592,1074138544,396336,41600,394784,2148270080u,786816,41616,394976,786832,2148270096u,41632,395168,2148270112u,786848,41648,395360,786864,2148270128u,41664,395552,787200,787584,41680,395744,787600,787216,41696,395936,787232,787616,41712,396128,787632,787248,
41728,393264,787968,788352,41744,393456,788368,787984,41760,393648,788000,788384,41776,393840,788400,788016,41792,394032,788736,789120,41808,394224,789136,788752,41824,394416,788768,789152,41840,394608,789168,788784,41856,394800,789504,1074531712,41872,394992,1074531728,789520,41888,395184,789536,1074531744,41904,395376,1074531760,789552,41920,395568,789504,1074531712,41936,395760,1074531728,789520,41952,395952,789536,1074531744,41968,396144,1074531760,789552,
41984,0,2147581952u,98688,42000,192,98704,2147581968u,42016,384,2147581984u,98720,42032,576,98736,2147582000u,42048,768,99072,99456,42064,960,99472,99088,42080,1152,99104,99488,42096,1344,99504,99120,42112,1536,99840,100224,42128,1728,100240,99856,42144,1920,99872,100256,42160,2112,100272,99888,42176,2304,100608,100992,42192,2496,101008,100624,42208,2688,100640,101024,42224,2880,101040,100656,
42240,16,101376,1073843584,42256,208,1073843600,101392,42272,400,101408,1073843616,42288,592,1073843632,101424,42304,784,2147975168u,491904,42320,976,491920,2147975184u,42336,1168,2147975200u,491936,42352,1360,491952,2147975216u,42368,1552,492288,492672,42384,1744,492688,492304,42400,1936,492320,492704,42416,2128,492720,492336,42432,2320,493056,493440,42448,2512,493456,493072,42464,2704,493088,493472,42480,2896,493488,493104,
42496,32,493824,494208,42512,224,494224,493840,42528,416,493856,494240,42544,608,494256,493872,42560,800,494592,1074236800,42576,992,1074236816,494608,42592,1184,494624,1074236832,42608,1376,1074236848,494640,42624,1568,2148368384u,885120,42640,1760,885136,2148368400u,42656,1952,2148368416u,885152,42672,2144,885168,2148368432u,42688,2336,885504,885888,42704,2528,885904,885520,42720,2720,885536,885920,42736,2912,885936,885552,
42752,48,886272,886656,42768,240,886672,886288,42784,432,886304,886688,42800,624,886704,886320,42816,816,887040,887424,42832,1008,887440,887056,42848,1200,887072,887456,42864,1392,887472,887088,42880,1584,887808,1074630016,42896,1776,1074630032,887824,42912,1968,887840,1074630048,42928,2160,1074630064,887856,42944,2352,887808,1074630016,42960,2544,1074630032,887824,42976,2736,887840,1074630048,42992,2928,1074630064,887856,
40960,1179648,2147680256u,196992,40976,1179840,197008,2147680272u,40992,1180032,2147680288u,197024,41008,1180224,197040,2147680304u,41024,1180416,197376,197760,41040,1180608,197776,197392,41056,1180800,197408,197792,41072,1180992,197808,197424,41088,1181184,198144,198528,41104,1181376,198544,198160,41120,1181568,198176,198560,41136,1181760,198576,198192,41152,1181952,198912,199296,41168,1182144,199312,198928,41184,1182336,198944,199328,41200,1182528,199344,198960,
41216,1179664,199680,1073941888,41232,1179856,1073941904,199696,41248,1180048,199712,1073941920,41264,1180240,1073941936,199728,41280,1180432,2148073472u,590208,41296,1180624,590224,2148073488u,41312,1180816,2148073504u,590240,41328,1181008,590256,2148073520u,41344,1181200,590592,590976,41360,1181392,590992,590608,41376,1181584,590624,591008,41392,1181776,591024,590640,41408,1181968,591360,591744,41424,1182160,591760,591376,41440,1182352,591392,591776,41456,1182544,591792,591408,
41472,1179680,592128,592512,41488,1179872,592528,592144,41504,1180064,592160,592544,41520,1180256,592560,592176,41536,1180448,592896,1074335104,41552,1180640,1074335120,592912,41568,1180832,592928,1074335136,41584,1181024,1074335152,592944,41600,1181216,2148466688u,983424,41616,1181408,983440,2148466704u,41632,1181600,2148466720u,983456,41648,1181792,983472,2148466736u,41664,1181984,983808,984192,41680,1182176,984208,983824,41696,1182368,983840,984224,41712,1182560,984240,983856,
41728,1179696,984576,984960,41744,1179888,984976,984592,41760,1180080,984608,984992,41776,1180272,985008,984624,41792,1180464,985344,985728,41808,1180656,985744,985360,41824,1180848,985376,985760,41840,1181040,985776,985392,41856,1181232,986112,1074728320,41872,1181424,1074728336,986128,41888,1181616,986144,1074728352,41904,1181808,1074728368,986160,41920,1182000,986112,1074728320,41936,1182192,1074728336,986128,41952,1182384,986144,1074728352,41968,1182576,1074728368,986160,
41984,786432,2147778560u,295296,42000,786624,295312,2147778576u,42016,786816,2147778592u,295328,42032,787008,295344,2147778608u,42048,787200,295680,296064,42064,787392,296080,295696,42080,787584,295712,296096,42096,787776,296112,295728,42112,787968,296448,296832,42128,788160,296848,296464,42144,788352,296480,296864,42160,788544,296880,296496,42176,788736,297216,297600,42192,788928,297616,297232,42208,789120,297248,297632,42224,789312,297648,297264,
42240,786448,297984,1074040192,42256,786640,1074040208,298000,42272,786832,298016,1074040224,42288,787024,1074040240,298032,42304,787216,2148171776u,688512,42320,787408,688528,2148171792u,42336,787600,2148171808u,688544,42352,787792,688560,2148171824u,42368,787984,688896,689280,42384,788176,689296,688912,42400,788368,688928,689312,42416,788560,689328,688944,42432,788752,689664,690048,42448,788944,690064,689680,42464,789136,689696,690080,42480,789328,690096,689712,
42496,786464,690432,690816,42512,786656,690832,690448,42528,786848,690464,690848,42544,787040,690864,690480,42560,787232,691200,1074433408,42576,787424,1074433424,691216,42592,787616,691232,1074433440,42608,787808,1074433456,691248,42624,788000,2148564992u,1081728,42640,788192,1081744,2148565008u,42656,788384,2148565024u,1081760,42672,788576,1081776,2148565040u,42688,788768,1082112,1082496,42704,788960,1082512,1082128,42720,789152,1082144,1082528,42736,789344,1082544,1082160,
42752,786480,1082880,1083264,42768,786672,1083280,1082896,42784,786864,1082912,1083296,42800,787056,1083312,1082928,42816,787248,1083648,1084032,42832,787440,1084048,1083664,42848,787632,1083680,1084064,42864,787824,1084080,1083696,42880,788016,1084416,1074826624,42896,788208,1074826640,1084432,42912,788400,1084448,1074826656,42928,788592,1074826672,1084464,42944,788784,1084416,1074826624,42960,788976,1074826640,1084432,42976,789168,1084448,1074826656,42992,789360,1074826672,1084464,
81920,133504,81920,8208,88664,133952,83280,8192,95392,134080,84640,8192,102136,134400,86000,8192,81936,133520,81936,8224,88680,133968,83296,8864,95408,134096,84656,8864,102152,134416,86016,8864,81952,133536,81968,8240,88696,133984,83328,8864,95424,134112,84688,8864,102168,134432,86048,8864,81968,133552,81952,8256,88712,134000,83312,8192,95440,134128,84672,8192,102184,134448,86032,8192,
81984,133568,81936,8464,88728,133888,83296,8400,95456,134016,84656,8336,102200,134464,86016,8272,83280,133584,81952,8480,90024,133904,83312,8416,96752,134032,84672,8352,103496,134480,86032,8288,83296,133600,81984,8496,90040,133920,83344,8432,96768,134048,84704,8368,103512,134496,86064,8304,83312,133616,81968,8512,90056,133936,83328,8448,96784,134064,84688,8384,103528,134512,86048,8320,
83328,133632,83280,8272,90072,133824,84640,8208,96800,134208,86000,8192,103544,134272,87360,8192,83344,133648,83296,8288,90088,133840,84656,8224,96816,134224,86016,8864,103560,134288,87376,8864,84640,133664,83328,8304,91384,133856,84688,8240,98112,134240,86048,8864,104856,134304,87408,8864,84656,133680,83312,8320,91400,133872,84672,8256,98128,134256,86032,8192,104872,134320,87392,8192,
84672,133696,83296,8864,91416,133760,84656,8464,98144,134144,86016,8400,104888,134336,87376,8336,84688,133712,83312,8192,91432,133776,84672,8480,98160,134160,86032,8416,104904,134352,87392,8352,84704,133728,83344,8192,91448,133792,84704,8496,98176,134176,86064,8432,104920,134368,87424,8368,84704,133744,83328,8864,91448,133808,84688,8512,98176,134192,86048,8448,104920,134384,87408,8384,
82240,139664,88656,10896,88984,140112,90016,10880,95712,140240,91376,10880,102456,140560,92736,10880,82256,139680,88672,10912,89000,140128,90032,11552,95728,140256,91392,11552,102472,140576,92752,11552,82272,139696,88704,10928,89016,140144,90064,11552,95744,140272,91424,11552,102488,140592,92784,11552,82288,139712,88688,10944,89032,140160,90048,10880,95760,140288,91408,10880,102504,140608,92768,10880,
82304,139728,88672,11152,89048,140048,90032,11088,95776,140176,91392,11024,102520,140624,92752,10960,83600,139744,88688,11168,90344,140064,90048,11104,97072,140192,91408,11040,103816,140640,92768,10976,83616,139760,88720,11184,90360,140080,90080,11120,97088,140208,91440,11056,103832,140656,92800,10992,83632,139776,88704,11200,90376,140096,90064,11136,97104,140224,91424,11072,103848,140672,92784,11008,
83648,139792,90016,10960,90392,139984,91376,10896,97120,140368,92736,10880,103864,140432,94096,10880,83664,139808,90032,10976,90408,140000,91392,10912,97136,140384,92752,11552,103880,140448,94112,11552,84960,139824,90064,10992,91704,140016,91424,10928,98432,140400,92784,11552,105176,140464,94144,11552,84976,139840,90048,11008,91720,140032,91408,10944,98448,140416,92768,10880,105192,140480,94128,10880,
84992,139856,90032,11552,91736,139920,91392,11152,98464,140304,92752,11088,105208,140496,94112,11024,85008,139872,90048,10880,91752,139936,91408,11168,98480,140320,92768,11104,105224,140512,94128,11040,85024,139888,90080,10880,91768,139952,91440,11184,98496,140336,92800,11120,105240,140528,94160,11056,85024,139904,90064,11552,91768,139968,91424,11200,98496,140352,92784,11136,105240,140544,94144,11072,
82560,145824,95392,13584,89304,146272,96752,13568,96032,146400,98112,13568,102776,146720,99472,13568,82576,145840,95408,13600,89320,146288,96768,14240,96048,146416,98128,14240,102792,146736,99488,14240,82592,145856,95440,13616,89336,146304,96800,14240,96064,146432,98160,14240,102808,146752,99520,14240,82608,145872,95424,13632,89352,146320,96784,13568,96080,146448,98144,13568,102824,146768,99504,13568,
82624,145888,95408,13840,89368,146208,96768,13776,96096,146336,98128,13712,102840,146784,99488,13648,83920,145904,95424,13856,90664,146224,96784,13792,97392,146352,98144,13728,104136,146800,99504,13664,83936,145920,95456,13872,90680,146240,96816,13808,97408,146368,98176,13744,104152,146816,99536,13680,83952,145936,95440,13888,90696,146256,96800,13824,97424,146384,98160,13760,104168,146832,99520,13696,
83968,145952,96752,13648,90712,146144,98112,13584,97440,146528,99472,13568,104184,146592,100832,13568,83984,145968,96768,13664,90728,146160,98128,13600,97456,146544,99488,14240,104200,146608,100848,14240,85280,145984,96800,13680,92024,146176,98160,13616,98752,146560,99520,14240,105496,146624,100880,14240,85296,146000,96784,13696,92040,146192,98144,13632,98768,146576,99504,13568,105512,146640,100864,13568,
85312,146016,96768,14240,92056,146080,98128,13840,98784,146464,99488,13776,105528,146656,100848,13712,85328,146032,96784,13568,92072,146096,98144,13856,98800,146480,99504,13792,105544,146672,100864,13728,85344,146048,96816,13568,92088,146112,98176,13872,98816,146496,99536,13808,105560,146688,100896,13744,85344,146064,96800,14240,92088,146128,98160,13888,98816,146512,99520,13824,105560,146704,100880,13760,
82880,151984,102128,16272,89624,152432,103488,16256,96352,152560,104848,16256,103096,152880,106208,16256,82896,152000,102144,16288,89640,152448,103504,16928,96368,152576,104864,16928,103112,152896,106224,16928,82912,152016,102176,16304,89656,152464,103536,16928,96384,152592,104896,16928,103128,152912,106256,16928,82928,152032,102160,16320,89672,152480,103520,16256,96400,152608,104880,16256,103144,152928,106240,16256,
82944,152048,102144,16528,89688,152368,103504,16464,96416,152496,104864,16400,103160,152944,106224,16336,84240,152064,102160,16544,90984,152384,103520,16480,97712,152512,104880,16416,104456,152960,106240,16352,84256,152080,102192,16560,91000,152400,103552,16496,97728,152528,104912,16432,104472,152976,106272,16368,84272,152096,102176,16576,91016,152416,103536,16512,97744,152544,104896,16448,104488,152992,106256,16384,
84288,152112,103488,16336,91032,152304,104848,16272,97760,152688,106208,16256,104504,152752,107568,16256,84304,152128,103504,16352,91048,152320,104864,16288,97776,152704,106224,16928,104520,152768,107584,16928,85600,152144,103536,16368,92344,152336,104896,16304,99072,152720,106256,16928,105816,152784,107616,16928,85616,152160,103520,16384,92360,152352,104880,16320,99088,152736,106240,16256,105832,152800,107600,16256,
85632,152176,103504,16928,92376,152240,104864,16528,99104,152624,106224,16464,105848,152816,107584,16400,85648,152192,103520,16256,92392,152256,104880,16544,99120,152640,106240,16480,105864,152832,107600,16416,85664,152208,103552,16256,92408,152272,104912,16560,99136,152656,106272,16496,105880,152848,107632,16432,85664,152224,103536,16928,92408,152288,104896,16576,99136,152672,106256,16512,105880,152864,107616,16448,
8336,108864,0,393216,8272,115024,0,393232,8208,121184,0,393248,8864,127344,0,393264,8352,108928,0,393408,8288,115088,0,393424,8224,121248,0,393440,8528,127408,0,393456,8368,109056,0,393600,8304,115216,0,393616,8240,121376,0,393632,8528,127536,0,393648,8384,108992,0,393792,8320,115152,0,393808,8256,121312,0,393824,8864,127472,0,393840,
8528,108880,0,393984,8528,115040,0,394000,8464,121200,0,394016,8400,127360,0,394032,8864,108944,0,394176,8864,115104,0,394192,8480,121264,0,394208,8416,127424,0,394224,8864,109072,0,394368,8864,115232,0,394384,8496,121392,0,394400,8432,127552,0,394416,8528,109008,0,394560,8528,115168,0,394576,8512,121328,0,394592,8448,127488,0,394608,
8400,108896,0,394752,8336,115056,0,394768,8272,121216,0,394784,8208,127376,0,394800,8416,108960,0,394944,8352,115120,0,394960,8288,121280,0,394976,8224,127440,0,394992,8432,109088,0,395136,8368,115248,0,395152,8304,121408,0,395168,8240,127568,0,395184,8448,109024,0,395328,8384,115184,0,395344,8320,121344,0,395360,8256,127504,0,395376,
8528,108912,0,395520,8528,115072,0,395536,8528,121232,0,395552,8464,127392,0,395568,8864,108976,0,395712,8864,115136,0,395728,8864,121296,0,395744,8480,127456,0,395760,8864,109104,0,395904,8864,115264,0,395920,8864,121424,0,395936,8496,127584,0,395952,8528,109040,0,396096,8528,115200,0,396112,8528,121360,0,396128,8512,127520,0,396144,
11024,109312,0,0,10960,115472,0,16,10896,121632,0,32,11552,127792,0,48,11040,109248,0,192,10976,115408,0,208,10912,121568,0,224,11216,127728,0,240,11056,109120,0,384,10992,115280,0,400,10928,121440,0,416,11216,127600,0,432,11072,109184,0,576,11008,115344,0,592,10944,121504,0,608,11552,127664,0,624,
11216,109328,0,768,11216,115488,0,784,11152,121648,0,800,11088,127808,0,816,11552,109264,0,960,11552,115424,0,976,11168,121584,0,992,11104,127744,0,1008,11552,109136,0,1152,11552,115296,0,1168,11184,121456,0,1184,11120,127616,0,1200,11216,109200,0,1344,11216,115360,0,1360,11200,121520,0,1376,11136,127680,0,1392,
11088,109344,0,1536,11024,115504,0,1552,10960,121664,0,1568,10896,127824,0,1584,11104,109280,0,1728,11040,115440,0,1744,10976,121600,0,1760,10912,127760,0,1776,11120,109152,0,1920,11056,115312,0,1936,10992,121472,0,1952,10928,127632,0,1968,11136,109216,0,2112,11072,115376,0,2128,11008,121536,0,2144,10944,127696,0,2160,
11216,109360,0,2304,11216,115520,0,2320,11216,121680,0,2336,11152,127840,0,2352,11552,109296,0,2496,11552,115456,0,2512,11552,121616,0,2528,11168,127776,0,2544,11552,109168,0,2688,11552,115328,0,2704,11552,121488,0,2720,11184,127648,0,2736,11216,109232,0,2880,11216,115392,0,2896,11216,121552,0,2912,11200,127712,0,2928,
13712,109440,8,1179648,13648,115600,8,1179664,13584,121760,8,1179680,14240,127920,8,1179696,13728,109376,8,1179840,13664,115536,8,1179856,13600,121696,8,1179872,13904,127856,8,1179888,13744,109504,8,1180032,13680,115664,8,1180048,13616,121824,8,1180064,13904,127984,8,1180080,13760,109568,8,1180224,13696,115728,8,1180240,13632,121888,8,1180256,14240,128048,8,1180272,
13904,109456,8,1180416,13904,115616,8,1180432,13840,121776,8,1180448,13776,127936,8,1180464,14240,109392,8,1180608,14240,115552,8,1180624,13856,121712,8,1180640,13792,127872,8,1180656,14240,109520,8,1180800,14240,115680,8,1180816,13872,121840,8,1180832,13808,128000,8,1180848,13904,109584,8,1180992,13904,115744,8,1181008,13888,121904,8,1181024,13824,128064,8,1181040,
13776,109472,8,1181184,13712,115632,8,1181200,13648,121792,8,1181216,13584,127952,8,1181232,13792,109408,8,1181376,13728,115568,8,1181392,13664,121728,8,1181408,13600,127888,8,1181424,13808,109536,8,1181568,13744,115696,8,1181584,13680,121856,8,1181600,13616,128016,8,1181616,13824,109600,8,1181760,13760,115760,8,1181776,13696,121920,8,1181792,13632,128080,8,1181808,
13904,109488,8,1181952,13904,115648,8,1181968,13904,121808,8,1181984,13840,127968,8,1182000,14240,109424,8,1182144,14240,115584,8,1182160,14240,121744,8,1182176,13856,127904,8,1182192,14240,109552,8,1182336,14240,115712,8,1182352,14240,121872,8,1182368,13872,128032,8,1182384,13904,109616,8,1182528,13904,115776,8,1182544,13904,121936,8,1182560,13888,128096,8,1182576,
16400,109760,8,786432,16336,115920,8,786448,16272,122080,8,786464,16928,128240,8,786480,16416,109824,8,786624,16352,115984,8,786640,16288,122144,8,786656,16592,128304,8,786672,16432,109696,8,786816,16368,115856,8,786832,16304,122016,8,786848,16592,128176,8,786864,16448,109632,8,787008,16384,115792,8,787024,16320,121952,8,787040,16928,128112,8,787056,
16592,109776,8,787200,16592,115936,8,787216,16528,122096,8,787232,16464,128256,8,787248,16928,109840,8,787392,16928,116000,8,787408,16544,122160,8,787424,16480,128320,8,787440,16928,109712,8,787584,16928,115872,8,787600,16560,122032,8,787616,16496,128192,8,787632,16592,109648,8,787776,16592,115808,8,787792,16576,121968,8,787808,16512,128128,8,787824,
16464,109792,8,787968,16400,115952,8,787984,16336,122112,8,788000,16272,128272,8,788016,16480,109856,8,788160,16416,116016,8,788176,16352,122176,8,788192,16288,128336,8,788208,16496,109728,8,788352,16432,115888,8,788368,16368,122048,8,788384,16304,128208,8,788400,16512,109664,8,788544,16448,115824,8,788560,16384,121984,8,788576,16320,128144,8,788592,
16592,109808,8,788736,16592,115968,8,788752,16592,122128,8,788768,16528,128288,8,788784,16928,109872,8,788928,16928,116032,8,788944,16928,122192,8,788960,16544,128352,8,788976,16928,109744,8,789120,16928,115904,8,789136,16928,122064,8,789152,16560,128224,8,789168,16592,109680,8,789312,16592,115840,8,789328,16592,122000,8,789344,16576,128160,8,789360,
81920,40960,40960,40960,88656,40976,40976,40968,95392,40992,40992,40976,102128,41008,41008,40984,108864,41024,41024,41760,115600,41040,41040,41768,122336,41056,41056,41776,129072,41072,41072,41784,135808,41088,41088,42560,142544,41104,41104,42568,149280,41120,41120,42576,156016,41136,41136,42584,81924,41152,41152,43360,88660,41168,41168,43368,95396,41184,41184,43376,102132,41200,41200,43384,
108868,41760,41216,44160,115604,41776,41232,44168,122340,41728,41248,44176,129076,41744,41264,44184,135812,41824,41280,44960,142548,41840,41296,44968,149284,41792,41312,44976,156020,41808,41328,44984,81928,41888,41344,45760,88664,41904,41360,45768,95400,41856,41376,45776,102136,41872,41392,45784,108872,41952,41408,46560,115608,41968,41424,46568,122344,41920,41440,46576,129080,41936,41456,46584,
135816,42560,41472,47104,142552,42576,41488,47112,149288,42592,41504,47120,156024,42608,41520,47128,81932,42496,41536,47904,88668,42512,41552,47912,95404,42528,41568,47920,102140,42544,41584,47928,108876,42688,41600,48704,115612,42704,41616,48712,122348,42720,41632,48720,129084,42736,41648,48728,135820,42624,41664,49504,142556,42640,41680,49512,149292,42656,41696,49520,156028,42672,41712,49528,
81936,43360,41728,50304,88672,43376,41744,50312,95408,43328,41760,50320,102144,43344,41776,50328,108880,43296,41792,51104,115616,43312,41808,51112,122352,43264,41824,51120,129088,43280,41840,51128,135824,43488,41856,51904,142560,43504,41872,51912,149296,43456,41888,51920,156032,43472,41904,51928,81940,43424,41920,52704,88676,43440,41936,52712,95412,43392,41952,52720,102148,43408,41968,52728,
108884,44160,41984,41344,115620,44176,42000,41352,122356,44192,42016,41360,129092,44208,42032,41368,135828,44224,42048,42144,142564,44240,42064,42152,149300,44256,42080,42160,156036,44272,42096,42168,81944,44032,42112,42944,88680,44048,42128,42952,95416,44064,42144,42960,102152,44080,42160,42968,108888,44096,42176,43744,115624,44112,42192,43752,122360,44128,42208,43760,129096,44144,42224,43768,
135832,44960,42240,44288,142568,44976,42256,44296,149304,44928,42272,44304,156040,44944,42288,44312,81948,45024,42304,45088,88684,45040,42320,45096,95420,44992,42336,45104,102156,45008,42352,45112,108892,44832,42368,45888,115628,44848,42384,45896,122364,44800,42400,45904,129100,44816,42416,45912,135836,44896,42432,46688,142572,44912,42448,46696,149308,44864,42464,46704,156044,44880,42480,46712,
81952,45760,42496,47488,88688,45776,42512,47496,95424,45792,42528,47504,102160,45808,42544,47512,108896,45696,42560,48288,115632,45712,42576,48296,122368,45728,42592,48304,129104,45744,42608,48312,135840,45632,42624,49088,142576,45648,42640,49096,149312,45664,42656,49104,156048,45680,42672,49112,81956,45568,42688,49888,88692,45584,42704,49896,95428,45600,42720,49904,102164,45616,42736,49912,
108900,46560,42752,50432,115636,46576,42768,50440,122372,46528,42784,50448,129108,46544,42800,50456,135844,46496,42816,51232,142580,46512,42832,51240,149316,46464,42848,51248,156052,46480,42864,51256,81960,46432,42880,52032,88696,46448,42896,52040,95432,46400,42912,52048,102168,46416,42928,52056,108904,46368,42944,52832,115640,46384,42960,52840,122376,46336,42976,52848,129112,46352,42992,52856,
135848,47104,47104,40960,142584,47120,47120,40968,149320,47136,47136,40976,156056,47152,47152,40984,81964,47168,47168,41760,88700,47184,47184,41768,95436,47200,47200,41776,102172,47216,47216,41784,108908,47232,47232,42560,115644,47248,47248,42568,122380,47264,47264,42576,129116,47280,47280,42584,135852,47296,47296,43360,142588,47312,47312,43368,149324,47328,47328,43376,156060,47344,47344,43384,
81968,47904,47360,44160,88704,47920,47376,44168,95440,47872,47392,44176,102176,47888,47408,44184,108912,47968,47424,44960,115648,47984,47440,44968,122384,47936,47456,44976,129120,47952,47472,44984,135856,48032,47488,45760,142592,48048,47504,45768,149328,48000,47520,45776,156064,48016,47536,45784,81972,48096,47552,46560,88708,48112,47568,46568,95444,48064,47584,46576,102180,48080,47600,46584,
108916,48704,47616,47104,115652,48720,47632,47112,122388,48736,47648,47120,129124,48752,47664,47128,135860,48640,47680,47904,142596,48656,47696,47912,149332,48672,47712,47920,156068,48688,47728,47928,81976,48832,47744,48704,88712,48848,47760,48712,95448,48864,47776,48720,102184,48880,47792,48728,108920,48768,47808,49504,115656,48784,47824,49512,122392,48800,47840,49520,129128,48816,47856,49528,
135864,49504,47872,50304,142600,49520,47888,50312,149336,49472,47904,50320,156072,49488,47920,50328,81980,49440,47936,51104,88716,49456,47952,51112,95452,49408,47968,51120,102188,49424,47984,51128,108924,49632,48000,51904,115660,49648,48016,51912,122396,49600,48032,51920,129132,49616,48048,51928,135868,49568,48064,52704,142604,49584,48080,52712,149340,49536,48096,52720,156076,49552,48112,52728,
81984,50304,48128,41344,88720,50320,48144,41352,95456,50336,48160,41360,102192,50352,48176,41368,108928,50368,48192,42144,115664,50384,48208,42152,122400,50400,48224,42160,129136,50416,48240,42168,135872,50176,48256,42944,142608,50192,48272,42952,149344,50208,48288,42960,156080,50224,48304,42968,81988,50240,48320,43744,88724,50256,48336,43752,95460,50272,48352,43760,102196,50288,48368,43768,
108932,51104,48384,44288,115668,51120,48400,44296,122404,51072,48416,44304,129140,51088,48432,44312,135876,51168,48448,45088,142612,51184,48464,45096,149348,51136,48480,45104,156084,51152,48496,45112,81992,50976,48512,45888,88728,50992,48528,45896,95464,50944,48544,45904,102200,50960,48560,45912,108936,51040,48576,46688,115672,51056,48592,46696,122408,51008,48608,46704,129144,51024,48624,46712,
135880,51904,48640,47488,142616,51920,48656,47496,149352,51936,48672,47504,156088,51952,48688,47512,81996,51840,48704,48288,88732,51856,48720,48296,95468,51872,48736,48304,102204,51888,48752,48312,108940,51776,48768,49088,115676,51792,48784,49096,122412,51808,48800,49104,129148,51824,48816,49112,135884,51712,48832,49888,142620,51728,48848,49896,149356,51744,48864,49904,156092,51760,48880,49912,
156092,52704,48896,50432,156092,52720,48912,50440,156092,52672,48928,50448,156092,52688,48944,50456,156092,52640,48960,51232,156092,52656,48976,51240,156092,52608,48992,51248,156092,52624,49008,51256,156092,52576,49024,52032,156092,52592,49040,52040,156092,52544,49056,52048,156092,52560,49072,52056,156092,52512,49088,52832,156092,52528,49104,52840,156092,52480,49120,52848,156092,52496,49136,52856,
40960,8208,8192,41088,40976,8880,8528,41096,40992,9552,8864,41104,41008,10224,9200,41112,41152,10896,9536,41888,41168,11568,9872,41896,41184,12240,10208,41904,41200,12912,10544,41912,41344,13584,10880,42688,41360,14256,11216,42696,41376,14928,11552,42704,41392,15600,11888,42712,41536,16272,12224,43488,41552,16944,12560,43496,41568,17616,12896,43504,41584,18288,13232,43512,
41728,18960,13568,44032,41744,19632,13904,44040,41760,20304,14240,44048,41776,20976,14576,44056,41920,21648,14912,44832,41936,22320,15248,44840,41952,22992,15584,44848,41968,23664,15920,44856,42112,24336,16256,45632,42128,25008,16592,45640,42144,25680,16928,45648,42160,26352,17264,45656,42304,27024,17600,46432,42320,27696,17936,46440,42336,28368,18272,46448,42352,29040,18608,46456,
42496,29712,18944,47232,42512,30384,19280,47240,42528,31056,19616,47248,42544,31728,19952,47256,42688,32400,20288,48032,42704,33072,20624,48040,42720,33744,20960,48048,42736,34416,21296,48056,42880,35088,21632,48832,42896,35760,21968,48840,42912,36432,22304,48848,42928,37104,22640,48856,43072,37776,22976,49632,43088,38448,23312,49640,43104,39120,23648,49648,43120,39792,23984,49656,
43264,8272,24320,50176,43280,8944,24656,50184,43296,9616,24992,50192,43312,10288,25328,50200,43456,10960,25664,50976,43472,11632,26000,50984,43488,12304,26336,50992,43504,12976,26672,51000,43648,13648,27008,51776,43664,14320,27344,51784,43680,14992,27680,51792,43696,15664,28016,51800,43840,16336,28352,52576,43856,17008,28688,52584,43872,17680,29024,52592,43888,18352,29360,52600,
47104,19024,29696,41472,47120,19696,30032,41480,47136,20368,30368,41488,47152,21040,30704,41496,47296,21712,31040,42272,47312,22384,31376,42280,47328,23056,31712,42288,47344,23728,32048,42296,47488,24400,32384,43072,47504,25072,32720,43080,47520,25744,33056,43088,47536,26416,33392,43096,47680,27088,33728,43872,47696,27760,34064,43880,47712,28432,34400,43888,47728,29104,34736,43896,
47872,29776,35072,44672,47888,30448,35408,44680,47904,31120,35744,44688,47920,31792,36080,44696,48064,32464,36416,45472,48080,33136,36752,45480,48096,33808,37088,45488,48112,34480,37424,45496,48256,35152,37760,46272,48272,35824,38096,46280,48288,36496,38432,46288,48304,37168,38768,46296,48448,37840,39104,47072,48464,38512,39440,47080,48480,39184,39776,47088,48496,39856,40112,47096,
48640,8336,40448,47616,48656,9008,40784,47624,48672,9680,162752,47632,48688,10352,162752,47640,48832,11024,162752,48416,48848,11696,162752,48424,48864,12368,162752,48432,48880,13040,162752,48440,49024,13712,162752,49216,49040,14384,162752,49224,49056,15056,162752,49232,49072,15728,162752,49240,49216,16400,162752,50016,49232,17072,162752,50024,49248,17744,162752,50032,49264,18416,162752,50040,
49408,19088,162752,50816,49424,19760,162752,50824,49440,20432,162752,50832,49456,21104,162752,50840,49600,21776,162752,51616,49616,22448,162752,51624,49632,23120,162752,51632,49648,23792,162752,51640,49792,24464,162752,52416,49808,25136,162752,52424,49824,25808,162752,52432,49840,26480,162752,52440,49984,27152,162752,53216,50000,27824,162752,53224,50016,28496,162752,53232,50032,29168,162752,53240,
40960,29840,162752,41088,40976,30512,162752,41096,40992,31184,162752,41104,41008,31856,162752,41112,41152,32528,162752,41888,41168,33200,162752,41896,41184,33872,162752,41904,41200,34544,162752,41912,41344,35216,162752,42688,41360,35888,162752,42696,41376,36560,162752,42704,41392,37232,162752,42712,41536,37904,162752,43488,41552,38576,162752,43496,41568,39248,162752,43504,41584,39920,162752,43512,
41728,8400,162752,44032,41744,9072,162752,44040,41760,9744,162752,44048,41776,10416,162752,44056,41920,11088,162752,44832,41936,11760,162752,44840,41952,12432,162752,44848,41968,13104,162752,44856,42112,13776,162752,45632,42128,14448,162752,45640,42144,15120,162752,45648,42160,15792,162752,45656,42304,16464,162752,46432,42320,17136,162752,46440,42336,17808,162752,46448,42352,18480,162752,46456,
42496,19152,162752,47232,42512,19824,162752,47240,42528,20496,162752,47248,42544,21168,162752,47256,42688,21840,162752,48032,42704,22512,162752,48040,42720,23184,162752,48048,42736,23856,162752,48056,42880,24528,162752,48832,42896,25200,162752,48840,42912,25872,162752,48848,42928,26544,162752,48856,43072,27216,162752,49632,43088,27888,162752,49640,43104,28560,162752,49648,43120,29232,162752,49656,
43264,29904,162752,50176,43280,30576,162752,50184,43296,31248,162752,50192,43312,31920,162752,50200,43456,32592,162752,50976,43472,33264,162752,50984,43488,33936,162752,50992,43504,34608,162752,51000,43648,35280,162752,51776,43664,35952,162752,51784,43680,36624,162752,51792,43696,37296,162752,51800,43840,37968,162752,52576,43856,38640,162752,52584,43872,39312,162752,52592,43888,39984,162752,52600,
47104,8464,162752,41472,47120,9136,162752,41480,47136,9808,162752,41488,47152,10480,162752,41496,47296,11152,162752,42272,47312,11824,162752,42280,47328,12496,162752,42288,47344,13168,162752,42296,47488,13840,162752,43072,47504,14512,162752,43080,47520,15184,162752,43088,47536,15856,162752,43096,47680,16528,162752,43872,47696,17200,162752,43880,47712,17872,162752,43888,47728,18544,162752,43896,
47872,19216,162752,44672,47888,19888,162752,44680,47904,20560,162752,44688,47920,21232,162752,44696,48064,21904,162752,45472,48080,22576,162752,45480,48096,23248,162752,45488,48112,23920,162752,45496,48256,24592,162752,46272,48272,25264,162752,46280,48288,25936,162752,46288,48304,26608,162752,46296,48448,27280,162752,47072,48464,27952,162752,47080,48480,28624,162752,47088,48496,29296,162752,47096,
48640,29968,162752,47616,48656,30640,162752,47624,48672,31312,162752,47632,48688,31984,162752,47640,48832,32656,162752,48416,48848,33328,162752,48424,48864,34000,162752,48432,48880,34672,162752,48440,49024,35344,162752,49216,49040,36016,162752,49224,49056,36688,162752,49232,49072,37360,162752,49240,49216,38032,162752,50016,49232,38704,162752,50024,49248,39376,162752,50032,49264,40048,162752,50040,
49408,40048,162752,50816,49424,40048,162752,50824,49440,40048,162752,50832,49456,40048,162752,50840,49600,40048,162752,51616,49616,40048,162752,51624,49632,40048,162752,51632,49648,40048,162752,51640,49792,40048,162752,52416,49808,40048,162752,52424,49824,40048,162752,52432,49840,40048,162752,52440,49984,40048,162752,53216,50000,40048,162752,53224,50016,40048,162752,53232,50032,40048,162752,53240,
16256,0,0,0,16256,0,0,0,16256,0,0,0,1065353216,0,0,0,0,0,16256,0,0,0,16256,0,0,0,16256,0,0,0,1065353216,0,0,16256,0,0,0,16256,0,0,0,16256,0,0,0,1065353216,0,0,0,0,0,16256,0,0,0,16256,0,0,0,16256,0,0,0,1065353216,
0,0,0,0,0,0,0,0,0,0,0,0,0,0,0,0,0,0,0,0,0,0,0,0,0,0,0,0,0,0,0,0,0,0,0,0,0,0,0,0,0,0,0,0,0,0,0,0,0,0,0,0,0,0,0,0,0,0,0,0,0,0,0,0,
0,0,0,0,0,0,0,0,0,0,0,0,0,0,0,0,0,0,0,0,0,0,0,0,0,0,0,0,0,0,0,0,0,0,0,0,0,0,0,0,0,0,0,0,0,0,0,0,0,0,0,0,0,0,0,0,0,0,0,0,0,0,0,0,
16256,0,0,0,16256,0,0,0,16256,0,0,0,1065353216,0,0,0,0,0,16256,0,0,0,16256,0,0,0,16256,0,0,0,1065353216,0,0,16256,0,0,0,16256,0,0,0,16256,0,0,0,1065353216,0,0,0,0,0,16256,0,0,0,16256,0,0,0,16256,0,0,0,1065353216,
16256,0,0,0,16256,0,0,0,16256,0,0,0,1065353216,0,0,0,0,0,16256,0,0,0,16256,0,0,0,16256,0,0,0,1065353216,0,0,16256,0,0,0,16256,0,0,0,16256,0,0,0,1065353216,0,0,0,0,0,16256,0,0,0,16256,0,0,0,16256,0,0,0,1065353216,
0,0,0,0,0,0,0,0,0,0,0,0,0,0,0,0,0,0,0,0,0,0,0,0,0,0,0,0,0,0,0,0,0,0,0,0,0,0,0,0,0,0,0,0,0,0,0,0,0,0,0,0,0,0,0,0,0,0,0,0,0,0,0,0,
0,0,0,0,0,0,0,0,0,0,0,0,0,0,0,0,0,0,0,0,0,0,0,0,0,0,0,0,0,0,0,0,0,0,0,0,0,0,0,0,0,0,0,0,0,0,0,0,0,0,0,0,0,0,0,0,0,0,0,0,0,0,0,0,
16256,0,0,0,16256,0,0,0,16256,0,0,0,1065353216,0,0,0,0,0,16256,0,0,0,16256,0,0,0,16256,0,0,0,1065353216,0,0,16256,0,0,0,16256,0,0,0,16256,0,0,0,1065353216,0,0,0,0,0,16256,0,0,0,16256,0,0,0,16256,0,0,0,1065353216,
16256,0,0,0,16256,0,0,0,16256,0,0,0,1065353216,0,0,0,0,0,16256,0,0,0,16256,0,0,0,16256,0,0,0,1065353216,0,0,16256,0,0,0,16256,0,0,0,16256,0,0,0,1065353216,0,0,0,0,0,16256,0,0,0,16256,0,0,0,16256,0,0,0,1065353216,
0,0,0,0,0,0,0,0,0,0,0,0,0,0,0,0,0,0,0,0,0,0,0,0,0,0,0,0,0,0,0,0,0,0,0,0,0,0,0,0,0,0,0,0,0,0,0,0,0,0,0,0,0,0,0,0,0,0,0,0,0,0,0,0,
0,0,0,0,0,0,0,0,0,0,0,0,0,0,0,0,0,0,0,0,0,0,0,0,0,0,0,0,0,0,0,0,0,0,0,0,0,0,0,0,0,0,0,0,0,0,0,0,0,0,0,0,0,0,0,0,0,0,0,0,0,0,0,0,
16256,0,0,0,16256,0,0,0,16256,0,0,0,1065353216,0,0,0,0,0,16256,0,0,0,16256,0,0,0,16256,0,0,0,1065353216,0,0,16256,0,0,0,16256,0,0,0,16256,0,0,0,1065353216,0,0,0,0,0,16256,0,0,0,16256,0,0,0,16256,0,0,0,1065353216,
16256,0,0,0,16256,0,0,0,16256,0,0,0,1065353216,0,0,0,0,0,16256,0,0,0,16256,0,0,0,16256,0,0,0,1065353216,0,0,16256,0,0,0,16256,0,0,0,16256,0,0,0,1065353216,0,0,0,0,0,16256,0,0,0,16256,0,0,0,16256,0,0,0,1065353216,
0,0,0,0,0,0,0,0,0,0,0,0,0,0,0,0,0,0,0,0,0,0,0,0,0,0,0,0,0,0,0,0,0,0,0,0,0,0,0,0,0,0,0,0,0,0,0,0,0,0,0,0,0,0,0,0,0,0,0,0,0,0,0,0,
0,0,0,0,0,0,0,0,0,0,0,0,0,0,0,0,0,0,0,0,0,0,0,0,0,0,0,0,0,0,0,0,0,0,0,0,0,0,0,0,0,0,0,0,0,0,0,0,0,0,0,0,0,0,0,0,0,0,0,0,0,0,0,0,
16256,0,0,0,16256,0,0,0,16256,0,0,0,1065353216,0,0,0,0,0,16256,0,0,0,16256,0,0,0,16256,0,0,0,1065353216,0,0,16256,0,0,0,16256,0,0,0,16256,0,0,0,1065353216,0,0,0,0,0,16256,0,0,0,16256,0,0,0,16256,0,0,0,1065353216,
0,889206016,0,889206016,0,889206016,0,889206016,0,0,0,0,0,0,0,0,0,889206016,0,889206016,0,889206016,0,889206016,0,0,0,0,0,0,0,0,0,889206016,0,889206016,0,889206016,0,889206016,0,0,0,0,0,0,0,0,0,889206016,0,889206016,0,889206016,0,889206016,0,0,0,0,0,0,0,0,
0,889206016,0,889206016,0,889206016,0,889206016,0,0,0,0,0,0,0,0,0,889206016,0,889206016,0,889206016,0,889206016,0,0,0,0,0,0,0,0,0,889206016,0,889206016,0,889206016,0,889206016,0,0,0,0,0,0,0,0,0,889206016,0,889206016,0,889206016,0,889206016,0,0,0,0,0,0,0,0,
0,889206016,0,889206016,0,889206016,0,889206016,0,0,0,0,0,0,0,0,0,889206016,0,889206016,0,889206016,0,889206016,0,0,0,0,0,0,0,0,0,889206016,0,889206016,0,889206016,0,889206016,0,0,0,0,0,0,0,0,0,889206016,0,889206016,0,889206016,0,889206016,0,0,0,0,0,0,0,0,
0,889206016,0,889206016,0,889206016,0,889206016,0,0,0,0,0,0,0,0,0,889206016,0,889206016,0,889206016,0,889206016,0,0,0,0,0,0,0,0,0,889206016,0,889206016,0,889206016,0,889206016,0,0,0,0,0,0,0,0,0,889206016,0,889206016,0,889206016,0,889206016,0,0,0,0,0,0,0,0,
0,889206016,0,889206016,0,889206016,0,889206016,0,0,0,0,0,0,0,0,0,889206016,0,889206016,0,889206016,0,889206016,0,0,0,0,0,0,0,0,0,889206016,0,889206016,0,889206016,0,889206016,0,0,0,0,0,0,0,0,0,889206016,0,889206016,0,889206016,0,889206016,0,0,0,0,0,0,0,0,
0,889206016,0,889206016,0,889206016,0,889206016,0,0,0,0,0,0,0,0,0,889206016,0,889206016,0,889206016,0,889206016,0,0,0,0,0,0,0,0,0,889206016,0,889206016,0,889206016,0,889206016,0,0,0,0,0,0,0,0,0,889206016,0,889206016,0,889206016,0,889206016,0,0,0,0,0,0,0,0,
0,889206016,0,889206016,0,889206016,0,889206016,0,0,0,0,0,0,0,0,0,889206016,0,889206016,0,889206016,0,889206016,0,0,0,0,0,0,0,0,0,889206016,0,889206016,0,889206016,0,889206016,0,0,0,0,0,0,0,0,0,889206016,0,889206016,0,889206016,0,889206016,0,0,0,0,0,0,0,0,
0,889206016,0,889206016,0,889206016,0,889206016,0,0,0,0,0,0,0,0,0,889206016,0,889206016,0,889206016,0,889206016,0,0,0,0,0,0,0,0,0,889206016,0,889206016,0,889206016,0,889206016,0,0,0,0,0,0,0,0,0,889206016,0,889206016,0,889206016,0,889206016,0,0,0,0,0,0,0,0,
0,889206016,0,889206016,0,889206016,0,889206016,0,0,0,0,0,0,0,0,0,889206016,0,889206016,0,889206016,0,889206016,0,0,0,0,0,0,0,0,0,889206016,0,889206016,0,889206016,0,889206016,0,0,0,0,0,0,0,0,0,889206016,0,889206016,0,889206016,0,889206016,0,0,0,0,0,0,0,0,
0,889206016,0,889206016,0,889206016,0,889206016,0,0,0,0,0,0,0,0,0,889206016,0,889206016,0,889206016,0,889206016,0,0,0,0,0,0,0,0,0,889206016,0,889206016,0,889206016,0,889206016,0,0,0,0,0,0,0,0,0,889206016,0,889206016,0,889206016,0,889206016,0,0,0,0,0,0,0,0,
0,889206016,0,889206016,0,889206016,0,889206016,0,0,0,0,0,0,0,0,0,889206016,0,889206016,0,889206016,0,889206016,0,0,0,0,0,0,0,0,0,889206016,0,889206016,0,889206016,0,889206016,0,0,0,0,0,0,0,0,0,889206016,0,889206016,0,889206016,0,889206016,0,0,0,0,0,0,0,0,
0,889206016,0,889206016,0,889206016,0,889206016,0,0,0,0,0,0,0,0,0,889206016,0,889206016,0,889206016,0,889206016,0,0,0,0,0,0,0,0,0,889206016,0,889206016,0,889206016,0,889206016,0,0,0,0,0,0,0,0,0,889206016,0,889206016,0,889206016,0,889206016,0,0,0,0,0,0,0,0,
0,889206016,0,889206016,0,889206016,0,889206016,0,0,0,0,0,0,0,0,0,889206016,0,889206016,0,889206016,0,889206016,0,0,0,0,0,0,0,0,0,889206016,0,889206016,0,889206016,0,889206016,0,0,0,0,0,0,0,0,0,889206016,0,889206016,0,889206016,0,889206016,0,0,0,0,0,0,0,0,
0,889206016,0,889206016,0,889206016,0,889206016,0,0,0,0,0,0,0,0,0,889206016,0,889206016,0,889206016,0,889206016,0,0,0,0,0,0,0,0,0,889206016,0,889206016,0,889206016,0,889206016,0,0,0,0,0,0,0,0,0,889206016,0,889206016,0,889206016,0,889206016,0,0,0,0,0,0,0,0,
0,889206016,0,889206016,0,889206016,0,889206016,0,0,0,0,0,0,0,0,0,889206016,0,889206016,0,889206016,0,889206016,0,0,0,0,0,0,0,0,0,889206016,0,889206016,0,889206016,0,889206016,0,0,0,0,0,0,0,0,0,889206016,0,889206016,0,889206016,0,889206016,0,0,0,0,0,0,0,0,
0,889206016,0,889206016,0,889206016,0,889206016,0,0,0,0,0,0,0,0,0,889206016,0,889206016,0,889206016,0,889206016,0,0,0,0,0,0,0,0,0,889206016,0,889206016,0,889206016,0,889206016,0,0,0,0,0,0,0,0,0,889206016,0,889206016,0,889206016,0,889206016,0,0,0,0,0,0,0,0,
0,889206016,0,40960,0,889206016,0,40976,0,0,0,40992,0,0,0,41008,0,889206016,0,41024,0,889206016,0,41040,0,0,0,41056,0,0,0,41072,0,889206016,0,41088,0,889206016,0,41104,0,0,0,41120,0,0,0,41136,0,889206016,0,41152,0,889206016,0,41168,0,0,0,41184,0,0,0,41200,
0,889206016,0,41216,0,889206016,0,41232,0,0,0,41248,0,0,0,41264,0,889206016,0,41280,0,889206016,0,41296,0,0,0,41312,0,0,0,41328,0,889206016,0,41344,0,889206016,0,41360,0,0,0,41376,0,0,0,41392,0,889206016,0,41408,0,889206016,0,41424,0,0,0,41440,0,0,0,41456,
0,889206016,0,41472,0,889206016,0,41488,0,0,0,41504,0,0,0,41520,0,889206016,0,41536,0,889206016,0,41552,0,0,0,41568,0,0,0,41584,0,889206016,0,41600,0,889206016,0,41616,0,0,0,41632,0,0,0,41648,0,889206016,0,41664,0,889206016,0,41680,0,0,0,41696,0,0,0,41712,
0,889206016,0,41728,0,889206016,0,41744,0,0,0,41760,0,0,0,41776,0,889206016,0,41792,0,889206016,0,41808,0,0,0,41824,0,0,0,41840,0,889206016,0,41856,0,889206016,0,41872,0,0,0,41888,0,0,0,41904,0,889206016,0,41920,0,889206016,0,41936,0,0,0,41952,0,0,0,41968,
0,889206016,0,41984,0,889206016,0,42000,0,0,0,42016,0,0,0,42032,0,889206016,0,42048,0,889206016,0,42064,0,0,0,42080,0,0,0,42096,0,889206016,0,42112,0,889206016,0,42128,0,0,0,42144,0,0,0,42160,0,889206016,0,42176,0,889206016,0,42192,0,0,0,42208,0,0,0,42224,
0,889206016,0,42240,0,889206016,0,42256,0,0,0,42272,0,0,0,42288,0,889206016,0,42304,0,889206016,0,42320,0,0,0,42336,0,0,0,42352,0,889206016,0,42368,0,889206016,0,42384,0,0,0,42400,0,0,0,42416,0,889206016,0,42432,0,889206016,0,42448,0,0,0,42464,0,0,0,42480,
0,889206016,0,42496,0,889206016,0,42512,0,0,0,42528,0,0,0,42544,0,889206016,0,42560,0,889206016,0,42576,0,0,0,42592,0,0,0,42608,0,889206016,0,42624,0,889206016,0,42640,0,0,0,42656,0,0,0,42672,0,889206016,0,42688,0,889206016,0,42704,0,0,0,42720,0,0,0,42736,
0,889206016,0,42752,0,889206016,0,42768,0,0,0,42784,0,0,0,42800,0,889206016,0,42816,0,889206016,0,42832,0,0,0,42848,0,0,0,42864,0,889206016,0,42880,0,889206016,0,42896,0,0,0,42912,0,0,0,42928,0,889206016,0,42944,0,889206016,0,42960,0,0,0,42976,0,0,0,42992,
0,889206016,0,43008,0,889206016,0,43024,0,0,0,43040,0,0,0,43056,0,889206016,0,43072,0,889206016,0,43088,0,0,0,43104,0,0,0,43120,0,889206016,0,43136,0,889206016,0,43152,0,0,0,43168,0,0,0,43184,0,889206016,0,43200,0,889206016,0,43216,0,0,0,43232,0,0,0,43248,
0,889206016,0,43264,0,889206016,0,43280,0,0,0,43296,0,0,0,43312,0,889206016,0,43328,0,889206016,0,43344,0,0,0,43360,0,0,0,43376,0,889206016,0,43392,0,889206016,0,43408,0,0,0,43424,0,0,0,43440,0,889206016,0,43456,0,889206016,0,43472,0,0,0,43488,0,0,0,43504,
0,889206016,0,43520,0,889206016,0,43536,0,0,0,43552,0,0,0,43568,0,889206016,0,43584,0,889206016,0,43600,0,0,0,43616,0,0,0,43632,0,889206016,0,43648,0,889206016,0,43664,0,0,0,43680,0,0,0,43696,0,889206016,0,43712,0,889206016,0,43728,0,0,0,43744,0,0,0,43760,
0,889206016,0,43776,0,889206016,0,43792,0,0,0,43808,0,0,0,43824,0,889206016,0,43840,0,889206016,0,43856,0,0,0,43872,0,0,0,43888,0,889206016,0,43904,0,889206016,0,43920,0,0,0,43936,0,0,0,43952,0,889206016,0,43968,0,889206016,0,43984,0,0,0,44000,0,0,0,44016,
0,889206016,0,44032,0,889206016,0,44048,0,0,0,44064,0,0,0,44080,0,889206016,0,44096,0,889206016,0,44112,0,0,0,44128,0,0,0,44144,0,889206016,0,44160,0,889206016,0,44176,0,0,0,44192,0,0,0,44208,0,889206016,0,44224,0,889206016,0,44240,0,0,0,44256,0,0,0,44272,
0,889206016,0,44288,0,889206016,0,44304,0,0,0,44320,0,0,0,44336,0,889206016,0,44352,0,889206016,0,44368,0,0,0,44384,0,0,0,44400,0,889206016,0,44416,0,889206016,0,44432,0,0,0,44448,0,0,0,44464,0,889206016,0,44480,0,889206016,0,44496,0,0,0,44512,0,0,0,44528,
0,889206016,0,44544,0,889206016,0,44560,0,0,0,44576,0,0,0,44592,0,889206016,0,44608,0,889206016,0,44624,0,0,0,44640,0,0,0,44656,0,889206016,0,44672,0,889206016,0,44688,0,0,0,44704,0,0,0,44720,0,889206016,0,44736,0,889206016,0,44752,0,0,0,44768,0,0,0,44784,
0,889206016,0,44800,0,889206016,0,44816,0,0,0,44832,0,0,0,44848,0,889206016,0,44864,0,889206016,0,44880,0,0,0,44896,0,0,0,44912,0,889206016,0,44928,0,889206016,0,44944,0,0,0,44960,0,0,0,44976,0,889206016,0,44992,0,889206016,0,45008,0,0,0,45024,0,0,0,45040};

DI float bf_lo(u32 v) { return __uint_as_float(v << 16); }
DI u32 pk_bf16(float a, float b) { f32x2 t = {a, b}; return __builtin_bit_cast(u32, __builtin_convertvector(t, bf16x2)); }
DI u32 pk_f16(float a, float b) { f32x2 t = {a, b}; return __builtin_bit_cast(u32, __builtin_convertvector(t, f16x2)); }
DI u32x4 srd(const void* p, u32 n) { return (u32x4){(u32)(size_t)p, (u32)((size_t)p >> 32), n, 0x00020000u}; }

DI void slow_path(const u16* __restrict__ x, const u16* __restrict__ dwl, const u16* __restrict__ dws, const u16* __restrict__ w1,
                  const u16* __restrict__ w2, u16* __restrict__ out, float* sm, int t, int tx, int ty0) {
  float* o = sm;
  float* g = sm + 8 * CH;
  float* st = g + 8 * HID;
#pragma unroll 1
  for (int b = 0; b < 128; ++b) {
    const int y = ty0 * 16 + (b >> 1), x0 = tx * 16 + (b & 1) * 8;
    __syncthreads();
#pragma unroll 1
    for (int i = t; i < 8 * CH; i += 256) {
      const int p = i / CH, c = i - p * CH;
      float acc = 0.f;
#pragma unroll 1
      for (int a = 0; a < 5; ++a)
#pragma unroll 1
        for (int e = 0; e < 5; ++e) {
          const int yy = y + a - 2, xc = x0 + p + e - 2;
          if (yy < 0 || yy >= IMG || xc < 0 || xc >= IMG) continue;
          const float xv = bf_lo(x[(size_t)(yy * IMG + xc) * CH + c]);
          acc = __builtin_fmaf(bf_lo(dws[(a * 5 + e) * CH + c]), xv, acc);
          if (a >= 1 && a <= 3 && e >= 1 && e <= 3) acc = __builtin_fmaf(bf_lo(dwl[((a - 1) * 3 + (e - 1)) * CH + c]), xv, acc);
        }
      o[i] = acc;
    }
    __syncthreads();
    if (t < 8) {
      float m = 0.f, v = 0.f;
#pragma unroll 1
      for (int c = 0; c < CH; ++c) m += o[t * CH + c];
      m *= (1.0f / CH);
#pragma unroll 1
      for (int c = 0; c < CH; ++c) { const float d = o[t * CH + c] - m; v = __builtin_fmaf(d, d, v); }
      st[2 * t] = m; st[2 * t + 1] = __builtin_amdgcn_rsqf(v * (1.0f / CH) + 1e-5f);
    }
    __syncthreads();
#pragma unroll 1
    for (int i = t; i < 8 * HID; i += 256) {
      const int p = i / HID, e = i - p * HID;
      const float m = st[2 * p], r = st[2 * p + 1];
      float u = 0.f;
#pragma unroll 1
      for (int c = 0; c < CH; ++c) u = __builtin_fmaf((o[p * CH + c] - m) * r, bf_lo(w1[c * HID + e]), u);
      const float z = u * __builtin_fmaf(u * u, GC3, GC1);
      g[i] = u * __builtin_amdgcn_rcpf(1.0f + __builtin_amdgcn_exp2f(z));
    }
    __syncthreads();
#pragma unroll 1
    for (int i = t; i < 8 * CH; i += 256) {
      const int p = i / CH, c = i - p * CH;
      float acc = 0.f;
#pragma unroll 1
      for (int e = 0; e < HID; ++e) acc = __builtin_fmaf(g[p * HID + e], bf_lo(w2[e * CH + c]), acc);
      const size_t idx = (size_t)(y * IMG + x0 + p) * CH + c;
      out[idx] = (u16)pk_bf16(bf_lo(x[idx]) + acc, 0.f);
    }
  }
}

__global__ __launch_bounds__(256, 1) void replk_kernel(const u16* __restrict__ x, const u16* __restrict__ dwl,
    const u16* __restrict__ dws, const u16* __restrict__ w1, const u16* __restrict__ w2, u16* __restrict__ out
    ) {
  __shared__ __attribute__((aligned(16))) char smem[L_TOTAL];
  __attribute__((address_space(3))) char* smem3 = (__attribute__((address_space(3))) char*)smem;
  const int tid = threadIdx.x, lane = tid & 63, w = __builtin_amdgcn_readfirstlane(tid >> 6), bid = blockIdx.x;
  const int tx = bid >> 3, ty0 = (bid & 7) * 4;
  const int Y0 = ty0 * 16, X0 = tx * 16;

  const int t240 = min(tid, 239), px = (t240 * 5462) >> 16, tr = (t240 * 1366) >> 16;
  u32x8 PA;
  PA[0] = ((tx == 0 && px < 2) || (tx == 31 && px >= 18)) ? 0x80000000u : (u32)((Y0 * IMG + X0 - 2) * PXB + t240 * 16);
  PA[1] = (u32)((8 * (tid >> 6) + ((tid >> 4) & 3)) * (HID * 2) + (tid & 15) * 16);
  PA[2] = (u32)((tid >> 7) * (192 * PXB) + (tid & 127) * 16);
  PA[3] = (u32)(t240 * 4 + tr * (4 * PXB));
  PA[4] = (tr >= 1 && tr <= 3) ? (u32)(t240 * 4 + tr * (2 * PXB) - 3 * PXB) : 0x80000000u;
  PA[5] = (u32)(tid * 16);
  {
    u64 pc; u32 clen;
    asm volatile("s_getpc_b64 %0\n.Lreplk_pc0:\n s_mov_b32 %1, .Lreplk_end-.Lreplk_pc0-4" : "=s"(pc), "=s"(clen));
    const u64 base = w == 0 ? (u64)w1 : w == 1 ? (u64)w2 : pc;
    const u32 part = w < 2 ? (u32)(tx % 9) : (u32)((tx & 3) * 2 + (w & 1));
    const u64 pa = base + min(part * 8192u + lane * 128u, w < 2 ? 0x20000u : clen);
    PA[6] = (u32)pa; PA[7] = (u32)(pa >> 32);
  }
  const u32x4 rx = srd(x, XBYTES), rt = srd((const char*)x - 2 * ROWB, ty0 ? XBYTES : 0u);
  const u32x8 rw = {(u32)(size_t)w1, (u32)((size_t)w1 >> 32), CH * HID * 2, 0x00020000u, (u32)(size_t)w2, (u32)((size_t)w2 >> 32), CH * HID * 2, 0x00020000u};
  const u32x8 rd = {(u32)(size_t)dws, (u32)((size_t)dws >> 32), 25 * PXB, 0x00020000u, (u32)(size_t)dwl, (u32)((size_t)dwl >> 32), 9 * PXB, 0x00020000u};
  const u32x8 S = {MU_PK, CC_PK, __float_as_uint(GAMMA * 0x1p-106f), (u32)(w >> 1), 0xffffffffu, 0xffu, (u32)(L_XT - L_H + w * (XT_CG - GS)), (u32)(L_XT + w * 256 - (L_H + w * GS) / 4)};
  const u32x4 T = {(u32)(size_t)CTAB, (u32)((size_t)CTAB >> 32), (u32)(((Y0 + 14) * IMG + X0 - 2) * PXB), (u32)((Y0 * IMG + X0) * PXB)};
  u32 flg = (u32)(size_t)smem3 ? 1u : 0u;
  asm volatile(
    ".p2align 3\n"
    "s_add_u32 vcc_lo, s96, 16384\n"
    "s_addc_u32 vcc_hi, s97, 0\n"
    "global_load_dwordx4 v[216:219], v213, vcc\n"
    "s_add_u32 vcc_lo, s96, 20480\n"
    "s_addc_u32 vcc_hi, s97, 0\n"
    "global_load_dwordx4 v[220:223], v213, vcc\n"
    "buffer_load_dword v224, v211, s[88:91], 0 offen offset:0\n"
    "buffer_load_dword v225, v211, s[88:91], 0 offen offset:192\n"
    "buffer_load_dword v226, v211, s[88:91], 0 offen offset:384\n"
    "buffer_load_dword v227, v211, s[88:91], 0 offen offset:576\n"
    "buffer_load_dword v228, v211, s[88:91], 0 offen offset:768\n"
    "buffer_load_dword v229, v212, s[92:95], 0 offen offset:0\n"
    "buffer_load_dword v230, v212, s[92:95], 0 offen offset:192\n"
    "buffer_load_dword v231, v212, s[92:95], 0 offen offset:384\n"
    "global_load_dword v200, v[214:215], off\n"
    "s_mov_b32 s65, 0\n"
    "buffer_load_dwordx4 a[168:171], v208, s[76:79], s65 offen\n"
    "v_mov_b32_e32 v196, 0\n"
    "v_mov_b32_e64 v232, 0\n"
    "v_mov_b32_e64 v233, 0\n"
    "v_mov_b32_e64 v234, 0\n"
    "v_mov_b32_e64 v235, 0\n"
    "v_mov_b32_e32 v184, 0x75089ba4\n"
    "v_mov_b32_e32 v186, 0xbdd2d3e8\n"
    "v_mov_b32_e32 v180, 0xc0135761\n"
    "v_lshlrev_b32_e64 v182, 16, v213\n"
    "v_lshlrev_b32_e64 v163, 1, v213\n"
    "v_or_b32_e32 v144, 0x8000, v182\n"
    "v_or_b32_e32 v145, 0x18000, v182\n"
    "v_or_b32_e32 v146, 0x28000, v182\n"
    "v_or_b32_e32 v147, 0x38000, v182\n"
    "v_or_b32_e32 v148, 0x48000, v182\n"
    "v_or_b32_e32 v149, 0x58000, v182\n"
    "v_or_b32_e32 v150, 0x68000, v182\n"
    "v_or_b32_e32 v151, 0x78000, v182\n"
    "v_pk_mul_f32 v[144:145], v[144:145], v[184:185] op_sel_hi:[1,0]\n"
    "v_pk_mul_f32 v[146:147], v[146:147], v[184:185] op_sel_hi:[1,0]\n"
    "v_pk_mul_f32 v[148:149], v[148:149], v[184:185] op_sel_hi:[1,0]\n"
    "v_pk_mul_f32 v[150:151], v[150:151], v[184:185] op_sel_hi:[1,0]\n"
    "v_pk_mul_f32 v[152:153], v[144:145], v[144:145]\n"
    "v_pk_mul_f32 v[154:155], v[146:147], v[146:147]\n"
    "v_pk_mul_f32 v[156:157], v[148:149], v[148:149]\n"
    "v_pk_mul_f32 v[158:159], v[150:151], v[150:151]\n"
    "v_pk_fma_f32 v[152:153], v[152:153], v[186:187], v[180:181] op_sel_hi:[1,0,0]\n"
    "v_pk_fma_f32 v[154:155], v[154:155], v[186:187], v[180:181] op_sel_hi:[1,0,0]\n"
    "v_pk_fma_f32 v[156:157], v[156:157], v[186:187], v[180:181] op_sel_hi:[1,0,0]\n"
    "v_pk_fma_f32 v[158:159], v[158:159], v[186:187], v[180:181] op_sel_hi:[1,0,0]\n"
    "v_pk_mul_f32 v[152:153], v[152:153], v[144:145]\n"
    "v_pk_mul_f32 v[154:155], v[154:155], v[146:147]\n"
    "v_pk_mul_f32 v[156:157], v[156:157], v[148:149]\n"
    "v_pk_mul_f32 v[158:159], v[158:159], v[150:151]\n"
    "v_exp_f32_e64 v152, v152\n"
    "v_exp_f32_e64 v153, v153\n"
    "v_exp_f32_e64 v154, v154\n"
    "v_exp_f32_e64 v155, v155\n"
    "v_exp_f32_e64 v156, v156\n"
    "v_exp_f32_e64 v157, v157\n"
    "v_exp_f32_e64 v158, v158\n"
    "v_exp_f32_e64 v159, v159\n"
    "v_pk_add_f32 v[164:165], v[152:153], 1.0 op_sel_hi:[1,0]\n"
    "v_pk_add_f32 v[166:167], v[154:155], 1.0 op_sel_hi:[1,0]\n"
    "v_pk_add_f32 v[168:169], v[156:157], 1.0 op_sel_hi:[1,0]\n"
    "v_pk_add_f32 v[170:171], v[158:159], 1.0 op_sel_hi:[1,0]\n"
    "v_rcp_f32_e64 v164, v164\n"
    "v_rcp_f32_e64 v165, v165\n"
    "v_rcp_f32_e64 v166, v166\n"
    "v_rcp_f32_e64 v167, v167\n"
    "v_rcp_f32_e64 v168, v168\n"
    "v_rcp_f32_e64 v169, v169\n"
    "v_rcp_f32_e64 v170, v170\n"
    "v_rcp_f32_e64 v171, v171\n"
    "v_pk_mul_f32 v[164:165], v[164:165], v[144:145]\n"
    "v_pk_mul_f32 v[166:167], v[166:167], v[146:147]\n"
    "v_pk_mul_f32 v[168:169], v[168:169], v[148:149]\n"
    "v_pk_mul_f32 v[170:171], v[170:171], v[150:151]\n"
    "v_pk_mul_f32 v[152:153], v[152:153], v[164:165]\n"
    "v_pk_mul_f32 v[154:155], v[154:155], v[166:167]\n"
    "v_pk_mul_f32 v[156:157], v[156:157], v[168:169]\n"
    "v_pk_mul_f32 v[158:159], v[158:159], v[170:171]\n"
    "v_cvt_pk_f16_f32 v176, v164, v165\n"
    "v_cvt_pk_f16_f32 v177, v166, v167\n"
    "v_cvt_pk_f16_f32 v178, v168, v169\n"
    "v_cvt_pk_f16_f32 v179, v170, v171\n"
    "v_cvt_pk_f16_f32 v172, -v152, -v153\n"
    "v_cvt_pk_f16_f32 v173, -v154, -v155\n"
    "v_cvt_pk_f16_f32 v174, -v156, -v157\n"
    "v_cvt_pk_f16_f32 v175, -v158, -v159\n"
    "v_accvgpr_write_b32 a216, v176\n"
    "v_accvgpr_write_b32 a217, v177\n"
    "s_mov_b64 s[68:69], s[44:45]\n"
    "s_mov_b32 s70, 0\n"
    "s_mov_b32 s71, s47\n"
    "s_mov_b64 s[72:73], s[40:41]\n"
    "s_mov_b64 s[74:75], s[42:43]\n"
    "s_mov_b32 s57, 0\n"
    "s_mov_b32 s58, 196608\n"
    "s_mov_b32 s59, 0\n"
    "s_mov_b32 s60, 0\n"
    "s_mov_b32 s61, 1179648\n"
    "s_mov_b32 s62, 1572864\n"
    "s_mov_b32 s63, 2752512\n"
    "s_mov_b32 s64, 0\n"
    "s_nop 0\n"
    "s_add_u32 s100, s57, 98304\n"
    "s_add_u32 s101, s58, 98304\n"
    "s_add_u32 s39, s59, 98304\n"
    "v_accvgpr_write_b32 a218, v178\n"
    "v_accvgpr_write_b32 a219, v179\n"
    "ds_write_b128 v163, v[172:175] offset:0\n"
    "v_or_b32_e32 v144, 0x88000, v182\n"
    "v_or_b32_e32 v145, 0x98000, v182\n"
    "v_or_b32_e32 v146, 0xa8000, v182\n"
    "v_or_b32_e32 v147, 0xb8000, v182\n"
    "v_or_b32_e32 v148, 0xc8000, v182\n"
    "v_or_b32_e32 v149, 0xd8000, v182\n"
    "v_or_b32_e32 v150, 0xe8000, v182\n"
    "v_or_b32_e32 v151, 0xf8000, v182\n"
    "v_pk_mul_f32 v[144:145], v[144:145], v[184:185] op_sel_hi:[1,0]\n"
    "v_pk_mul_f32 v[146:147], v[146:147], v[184:185] op_sel_hi:[1,0]\n"
    "v_pk_mul_f32 v[148:149], v[148:149], v[184:185] op_sel_hi:[1,0]\n"
    "v_pk_mul_f32 v[150:151], v[150:151], v[184:185] op_sel_hi:[1,0]\n"
    "v_pk_mul_f32 v[152:153], v[144:145], v[144:145]\n"
    "v_pk_mul_f32 v[154:155], v[146:147], v[146:147]\n"
    "v_pk_mul_f32 v[156:157], v[148:149], v[148:149]\n"
    "v_pk_mul_f32 v[158:159], v[150:151], v[150:151]\n"
    "v_pk_fma_f32 v[152:153], v[152:153], v[186:187], v[180:181] op_sel_hi:[1,0,0]\n"
    "v_pk_fma_f32 v[154:155], v[154:155], v[186:187], v[180:181] op_sel_hi:[1,0,0]\n"
    "v_pk_fma_f32 v[156:157], v[156:157], v[186:187], v[180:181] op_sel_hi:[1,0,0]\n"
    "v_pk_fma_f32 v[158:159], v[158:159], v[186:187], v[180:181] op_sel_hi:[1,0,0]\n"
    "v_pk_mul_f32 v[152:153], v[152:153], v[144:145]\n"
    "v_pk_mul_f32 v[154:155], v[154:155], v[146:147]\n"
    "v_pk_mul_f32 v[156:157], v[156:157], v[148:149]\n"
    "v_pk_mul_f32 v[158:159], v[158:159], v[150:151]\n"
    "v_exp_f32_e64 v152, v152\n"
    "v_exp_f32_e64 v153, v153\n"
    "v_exp_f32_e64 v154, v154\n"
    "v_exp_f32_e64 v155, v155\n"
    "v_exp_f32_e64 v156, v156\n"
    "v_exp_f32_e64 v157, v157\n"
    "v_exp_f32_e64 v158, v158\n"
    "v_exp_f32_e64 v159, v159\n"
    "v_pk_add_f32 v[164:165], v[152:153], 1.0 op_sel_hi:[1,0]\n"
    "v_pk_add_f32 v[166:167], v[154:155], 1.0 op_sel_hi:[1,0]\n"
    "v_pk_add_f32 v[168:169], v[156:157], 1.0 op_sel_hi:[1,0]\n"
    "v_pk_add_f32 v[170:171], v[158:159], 1.0 op_sel_hi:[1,0]\n"
    "v_rcp_f32_e64 v164, v164\n"
    "v_rcp_f32_e64 v165, v165\n"
    "v_rcp_f32_e64 v166, v166\n"
    "v_rcp_f32_e64 v167, v167\n"
    "v_rcp_f32_e64 v168, v168\n"
    "v_rcp_f32_e64 v169, v169\n"
    "v_rcp_f32_e64 v170, v170\n"
    "v_rcp_f32_e64 v171, v171\n"
    "v_pk_mul_f32 v[164:165], v[164:165], v[144:145]\n"
    "v_pk_mul_f32 v[166:167], v[166:167], v[146:147]\n"
    "v_pk_mul_f32 v[168:169], v[168:169], v[148:149]\n"
    "v_pk_mul_f32 v[170:171], v[170:171], v[150:151]\n"
    "v_pk_mul_f32 v[152:153], v[152:153], v[164:165]\n"
    "v_pk_mul_f32 v[154:155], v[154:155], v[166:167]\n"
    "v_pk_mul_f32 v[156:157], v[156:157], v[168:169]\n"
    "v_pk_mul_f32 v[158:159], v[158:159], v[170:171]\n"
    "v_cvt_pk_f16_f32 v176, v164, v165\n"
    "v_cvt_pk_f16_f32 v177, v166, v167\n"
    "v_cvt_pk_f16_f32 v178, v168, v169\n"
    "v_cvt_pk_f16_f32 v179, v170, v171\n"
    "v_cvt_pk_f16_f32 v172, -v152, -v153\n"
    "v_cvt_pk_f16_f32 v173, -v154, -v155\n"
    "v_cvt_pk_f16_f32 v174, -v156, -v157\n"
    "v_cvt_pk_f16_f32 v175, -v158, -v159\n"
    "v_accvgpr_write_b32 a220, v176\n"
    "v_accvgpr_write_b32 a221, v177\n"
    "v_accvgpr_write_b32 a222, v178\n"
    "v_accvgpr_write_b32 a223, v179\n"
    "ds_write_b128 v163, v[172:175] offset:16\n"
    "v_add_u32_e32 v163, 65536, v163\n"
    "v_mov_b32_e64 v147, 0\n"
    "v_mov_b32_e64 v148, 0\n"
    "v_mov_b32_e64 v155, 0\n"
    "v_mov_b32_e64 v156, 0\n"
    "s_mov_b32 s65, 98304\n"
    "buffer_load_dwordx4 a[172:175], v208, s[76:79], s65 offen\n"
    "s_waitcnt vmcnt(12)\n"
    "s_nop 0\n"
    "v_xor_b32_e32 v211, 96, v219\n"
    "v_lshl_add_u32 v161, s51, 8, v211\n"
    "s_mov_b32 s65, 196608\n"
    "buffer_load_dwordx4 a[176:179], v208, s[40:43], s65 offen\n"
    "s_waitcnt vmcnt(12)\n"
    "s_nop 0\n"
    "ds_write_b128 v222, v[232:235]\n"
    "s_mov_b32 s65, 294912\n"
    "buffer_load_dwordx4 a[180:183], v208, s[40:43], s65 offen\n"
    "s_mov_b32 s65, 589824\n"
    "buffer_load_dwordx4 a[184:187], v208, s[40:43], s65 offen\n"
    "s_mov_b32 s65, 0\n"
    "s_nop 0\n"
    "buffer_load_dwordx4 v[0:3], v210, s[84:87], s65 offen\n"
    "v_lshl_add_u32 v162, s51, 3, v220\n"
    "s_mov_b32 s65, 2048\n"
    "buffer_load_dwordx4 v[4:7], v210, s[84:87], s65 offen\n"
    "s_waitcnt vmcnt(15)\n"
    "v_lshlrev_b32_e32 v188, 16, v224\n"
    "s_mov_b32 s65, 4096\n"
    "buffer_load_dwordx4 v[8:11], v210, s[84:87], s65 offen\n"
    "s_waitcnt vmcnt(15)\n"
    "v_lshlrev_b32_e32 v189, 16, v225\n"
    "s_mov_b32 s65, 6144\n"
    "buffer_load_dwordx4 v[12:15], v210, s[84:87], s65 offen\n"
    "s_waitcnt vmcnt(15)\n"
    "v_lshlrev_b32_e32 v190, 16, v226\n"
    "s_mov_b32 s65, 8192\n"
    "buffer_load_dwordx4 v[16:19], v210, s[84:87], s65 offen\n"
    "s_waitcnt vmcnt(15)\n"
    "v_lshlrev_b32_e32 v191, 16, v227\n"
    "s_mov_b32 s65, 10240\n"
    "buffer_load_dwordx4 v[20:23], v210, s[84:87], s65 offen\n"
    "s_waitcnt vmcnt(15)\n"
    "v_lshlrev_b32_e32 v192, 16, v228\n"
    "s_waitcnt vmcnt(14)\n"
    "v_lshlrev_b32_e32 v193, 16, v229\n"
    "s_waitcnt vmcnt(13)\n"
    "v_lshlrev_b32_e32 v194, 16, v230\n"
    "s_waitcnt vmcnt(12)\n"
    "v_lshlrev_b32_e32 v195, 16, v231\n"
    "v_add_f32_e64 v189, v189, v193\n"
    "v_add_f32_e64 v190, v190, v194\n"
    "v_add_f32_e64 v191, v191, v195\n"
    "v_cvt_pk_bf16_f32 v144, v188, v189\n"
    "v_cvt_pk_bf16_f32 v145, v190, v191\n"
    "v_cvt_pk_bf16_f32 v146, v192, 0\n"
    "v_cvt_pk_bf16_f32 v152, 0, v188\n"
    "v_cvt_pk_bf16_f32 v153, v189, v190\n"
    "v_cvt_pk_bf16_f32 v154, v191, v192\n"
    "v_cvt_pk_bf16_f32 v149, v188, v189\n"
    "v_cvt_pk_bf16_f32 v150, v190, v191\n"
    "v_cvt_pk_bf16_f32 v151, v192, 0\n"
    "v_cvt_pk_bf16_f32 v157, 0, v188\n"
    "v_cvt_pk_bf16_f32 v158, v189, v190\n"
    "s_mov_b32 s65, 688128\n"
    "buffer_load_dwordx4 a[188:191], v208, s[40:43], s65 offen\n"
    "ds_write_b128 v221, v[144:147] offset:0\n"
    "v_cvt_pk_bf16_f32 v159, v191, v192\n"
    "v_and_b32_e32 v188, 0xffff0000, v224\n"
    "v_and_b32_e32 v189, 0xffff0000, v225\n"
    "v_and_b32_e32 v190, 0xffff0000, v226\n"
    "v_and_b32_e32 v191, 0xffff0000, v227\n"
    "v_and_b32_e32 v192, 0xffff0000, v228\n"
    "v_and_b32_e32 v193, 0xffff0000, v229\n"
    "v_and_b32_e32 v194, 0xffff0000, v230\n"
    "ds_write_b128 v221, v[152:155] offset:16\n"
    "v_and_b32_e32 v195, 0xffff0000, v231\n"
    "v_add_f32_e64 v189, v189, v193\n"
    "v_add_f32_e64 v190, v190, v194\n"
    "s_mov_b32 s65, 983040\n"
    "buffer_load_dwordx4 a[192:195], v208, s[40:43], s65 offen\n"
    "v_add_f32_e64 v191, v191, v195\n"
    "ds_write_b128 v221, v[148:151] offset:32\n"
    "v_cvt_pk_bf16_f32 v144, v188, v189\n"
    "v_cvt_pk_bf16_f32 v145, v190, v191\n"
    "v_cvt_pk_bf16_f32 v146, v192, 0\n"
    "v_cvt_pk_bf16_f32 v152, 0, v188\n"
    "v_cvt_pk_bf16_f32 v153, v189, v190\n"
    "v_cvt_pk_bf16_f32 v154, v191, v192\n"
    "ds_write_b128 v221, v[156:159] offset:48\n"
    "v_cvt_pk_bf16_f32 v149, v188, v189\n"
    "v_cvt_pk_bf16_f32 v150, v190, v191\n"
    "s_mov_b32 s65, 1081344\n"
    "buffer_load_dwordx4 a[196:199], v208, s[40:43], s65 offen\n"
    "v_cvt_pk_bf16_f32 v151, v192, 0\n"
    "v_cvt_pk_bf16_f32 v157, 0, v188\n"
    "v_cvt_pk_bf16_f32 v158, v189, v190\n"
    "v_cvt_pk_bf16_f32 v159, v191, v192\n"
    "ds_write_b128 v221, v[144:147] offset:336\n"
    "v_xor_b32_e32 v212, 0, v223\n"
    "v_xor_b32_e32 v214, 64, v223\n"
    "v_xor_b32_e32 v215, 96, v223\n"
    "ds_write_b128 v221, v[152:155] offset:352\n"
    "s_mov_b32 s65, 1376256\n"
    "buffer_load_dwordx4 a[200:203], v208, s[40:43], s65 offen\n"
    "ds_write_b128 v221, v[148:151] offset:368\n"
    "ds_write_b128 v221, v[156:159] offset:384\n"
    "s_mov_b32 s65, 1474560\n"
    "buffer_load_dwordx4 a[204:207], v208, s[40:43], s65 offen\n"
    "s_waitcnt vmcnt(15)\n"
    "ds_write2_b32 v216, a168, a169 offset0:0 offset1:20\n"
    "ds_write2_b32 v216, a170, a171 offset0:40 offset1:60\n"
    "s_waitcnt vmcnt(14)\n"
    "ds_write2_b32 v216, a172, a173 offset0:80 offset1:100\n"
    "s_mov_b32 s65, 12288\n"
    "buffer_load_dwordx4 v[24:27], v210, s[84:87], s65 offen\n"
    "ds_write2_b32 v216, a174, a175 offset0:120 offset1:140\n"
    "v_add_u32_e32 v216, 1360, v216\n"
    "s_waitcnt vmcnt(14)\n"
    "ds_write2_b32 v216, a176, a177 offset0:0 offset1:20\n"
    "ds_write2_b32 v216, a178, a179 offset0:40 offset1:60\n"
    "s_mov_b32 s65, 14336\n"
    "buffer_load_dwordx4 v[28:31], v210, s[84:87], s65 offen\n"
    "s_waitcnt vmcnt(12)\n"
    "v_or3_b32 v196, v196, v0, v1\n"
    "ds_write2_b32 v216, a180, a181 offset0:80 offset1:100\n"
    "v_or3_b32 v196, v196, v2, v3\n"
    "v_and_b32_e32 v198, 0x7fff7fff, v0\n"
    "ds_write2_b32 v216, a182, a183 offset0:120 offset1:140\n"
    "v_add_u32_e32 v216, 1360, v216\n"
    "v_lshlrev_b32_e64 v232, 16, v0\n"
    "ds_write2_b32 v216, a184, a185 offset0:0 offset1:20\n"
    "v_lshlrev_b32_e64 v233, 16, v1\n"
    "v_lshlrev_b32_e64 v234, 16, v2\n"
    "s_mov_b32 s65, 16384\n"
    "buffer_load_dwordx4 v[32:35], v210, s[84:87], s65 offen\n"
    "ds_write2_b32 v216, a186, a187 offset0:40 offset1:60\n"
    "v_lshlrev_b32_e64 v235, 16, v3\n"
    "v_and_b32_e32 v0, 0xffff0000, v0\n"
    "v_and_b32_e32 v1, 0xffff0000, v1\n"
    "v_and_b32_e32 v2, 0xffff0000, v2\n"
    "v_and_b32_e32 v3, 0xffff0000, v3\n"
    "v_cvt_pk_f16_f32 v0, v232, v0\n"
    "v_cvt_pk_f16_f32 v1, v233, v1\n"
    "v_cvt_pk_f16_f32 v2, v234, v2\n"
    "v_cvt_pk_f16_f32 v3, v235, v3\n"
    "ds_write_b128 v218, v[0:3] offset:24576\n"
    "s_waitcnt vmcnt(12)\n"
    "v_or3_b32 v196, v196, v4, v5\n"
    "v_or3_b32 v196, v196, v6, v7\n"
    "v_lshlrev_b32_e32 v232, 16, v4\n"
    "s_mov_b32 s65, 18432\n"
    "buffer_load_dwordx4 v[36:39], v210, s[84:87], s65 offen\n"
    "v_lshlrev_b32_e64 v233, 16, v5\n"
    "v_lshlrev_b32_e64 v234, 16, v6\n"
    "v_lshlrev_b32_e64 v235, 16, v7\n"
    "v_and_b32_e32 v4, 0xffff0000, v4\n"
    "v_and_b32_e32 v5, 0xffff0000, v5\n"
    "v_and_b32_e32 v6, 0xffff0000, v6\n"
    "v_and_b32_e32 v7, 0xffff0000, v7\n"
    "v_cvt_pk_f16_f32 v4, v232, v4\n"
    "v_cvt_pk_f16_f32 v5, v233, v5\n"
    "v_cvt_pk_f16_f32 v6, v234, v6\n"
    "v_cvt_pk_f16_f32 v7, v235, v7\n"
    "ds_write_b128 v218, v[4:7] offset:26624\n"
    "s_waitcnt vmcnt(12)\n"
    "v_or3_b32 v196, v196, v8, v9\n"
    "v_or3_b32 v196, v196, v10, v11\n"
    "v_lshlrev_b32_e32 v232, 16, v8\n"
    "v_lshlrev_b32_e64 v233, 16, v9\n"
    "s_mov_b32 s65, 20480\n"
    "buffer_load_dwordx4 v[40:43], v210, s[84:87], s65 offen\n"
    "v_lshlrev_b32_e64 v234, 16, v10\n"
    "v_lshlrev_b32_e64 v235, 16, v11\n"
    "v_and_b32_e32 v8, 0xffff0000, v8\n"
    "v_and_b32_e32 v9, 0xffff0000, v9\n"
    "v_and_b32_e32 v10, 0xffff0000, v10\n"
    "v_and_b32_e32 v11, 0xffff0000, v11\n"
    "v_cvt_pk_f16_f32 v8, v232, v8\n"
    "v_cvt_pk_f16_f32 v9, v233, v9\n"
    "v_cvt_pk_f16_f32 v10, v234, v10\n"
    "v_cvt_pk_f16_f32 v11, v235, v11\n"
    "ds_write_b128 v218, v[8:11] offset:28672\n"
    "s_waitcnt vmcnt(12)\n"
    "v_or3_b32 v196, v196, v12, v13\n"
    "v_or3_b32 v196, v196, v14, v15\n"
    "v_lshlrev_b32_e32 v232, 16, v12\n"
    "v_lshlrev_b32_e64 v233, 16, v13\n"
    "v_lshlrev_b32_e64 v234, 16, v14\n"
    "s_mov_b32 s65, 22528\n"
    "buffer_load_dwordx4 v[44:47], v210, s[84:87], s65 offen\n"
    "v_lshlrev_b32_e64 v235, 16, v15\n"
    "v_and_b32_e32 v12, 0xffff0000, v12\n"
    "v_and_b32_e32 v13, 0xffff0000, v13\n"
    "v_and_b32_e32 v14, 0xffff0000, v14\n"
    "v_and_b32_e32 v15, 0xffff0000, v15\n"
    "v_cvt_pk_f16_f32 v12, v232, v12\n"
    "v_cvt_pk_f16_f32 v13, v233, v13\n"
    "v_cvt_pk_f16_f32 v14, v234, v14\n"
    "v_cvt_pk_f16_f32 v15, v235, v15\n"
    "ds_write_b128 v218, v[12:15] offset:0\n"
    "s_waitcnt vmcnt(12)\n"
    "v_or3_b32 v196, v196, v16, v17\n"
    "s_waitcnt lgkmcnt(1)\n"
    "s_barrier\n"
    "s_mov_b32 s65, 0\n"
    "buffer_load_dwordx4 a[208:211], v208, s[40:43], s65 offen\n"
    "ds_read_b64_tr_b16 a[0:1], v220 offset:24576\n"
    "ds_read_b64_tr_b16 a[2:3], v220 offset:27648\n"
    "ds_read_b64_tr_b16 a[4:5], v220 offset:24584\n"
    "v_or3_b32 v196, v196, v18, v19\n"
    "ds_read_b64_tr_b16 a[6:7], v220 offset:27656\n"
    "ds_read_b64_tr_b16 a[8:9], v220 offset:24640\n"
    "ds_read_b64_tr_b16 a[10:11], v220 offset:27712\n"
    "v_lshlrev_b32_e64 v232, 16, v16\n"
    "ds_read_b64_tr_b16 a[12:13], v220 offset:24648\n"
    "ds_read_b64_tr_b16 a[14:15], v220 offset:27720\n"
    "ds_read_b64_tr_b16 v[164:165], v162 offset:24704\n"
    "v_lshlrev_b32_e64 v233, 16, v17\n"
    "ds_read_b64_tr_b16 v[166:167], v162 offset:27776\n"
    "v_lshlrev_b32_e64 v234, 16, v18\n"
    "s_mov_b32 s65, 98304\n"
    "buffer_load_dwordx4 a[212:215], v208, s[40:43], s65 offen\n"
    "v_lshlrev_b32_e64 v235, 16, v19\n"
    "v_and_b32_e32 v16, 0xffff0000, v16\n"
    "v_and_b32_e32 v17, 0xffff0000, v17\n"
    "v_and_b32_e32 v18, 0xffff0000, v18\n"
    "v_and_b32_e32 v19, 0xffff0000, v19\n"
    "v_cvt_pk_f16_f32 v16, v232, v16\n"
    "v_cvt_pk_f16_f32 v17, v233, v17\n"
    "v_cvt_pk_f16_f32 v18, v234, v18\n"
    "v_cvt_pk_f16_f32 v19, v235, v19\n"
    "ds_write_b128 v218, v[16:19] offset:2048\n"
    "s_waitcnt vmcnt(13)\n"
    "v_or3_b32 v196, v196, v20, v21\n"
    "v_or3_b32 v196, v196, v22, v23\n"
    "v_lshlrev_b32_e32 v232, 16, v20\n"
    "v_lshlrev_b32_e64 v233, 16, v21\n"
    "v_lshlrev_b32_e64 v234, 16, v22\n"
    "v_lshlrev_b32_e64 v235, 16, v23\n"
    "s_mov_b32 s65, 393216\n"
    "buffer_load_dwordx4 a[224:227], v208, s[40:43], s65 offen\n"
    "v_and_b32_e32 v20, 0xffff0000, v20\n"
    "v_and_b32_e32 v21, 0xffff0000, v21\n"
    "v_and_b32_e32 v22, 0xffff0000, v22\n"
    "v_and_b32_e32 v23, 0xffff0000, v23\n"
    "v_cvt_pk_f16_f32 v20, v232, v20\n"
    "v_cvt_pk_f16_f32 v21, v233, v21\n"
    "v_cvt_pk_f16_f32 v22, v234, v22\n"
    "v_cvt_pk_f16_f32 v23, v235, v23\n"
    "ds_write_b128 v218, v[20:23] offset:4096\n"
    "s_waitcnt vmcnt(13)\n"
    "ds_write2_b32 v216, a188, a189 offset0:80 offset1:100\n"
    "s_mov_b32 s65, 491520\n"
    "buffer_load_dwordx4 a[228:231], v208, s[40:43], s65 offen\n"
    "ds_write2_b32 v216, a190, a191 offset0:120 offset1:140\n"
    "v_add_u32_e32 v216, 1360, v216\n"
    "s_waitcnt vmcnt(13)\n"
    "ds_write2_b32 v216, a192, a193 offset0:0 offset1:20\n"
    "s_waitcnt lgkmcnt(3)\n"
    "s_barrier\n"
    "s_mov_b32 s65, 786432\n"
    "buffer_load_dwordx4 a[232:235], v208, s[40:43], s65 offen\n"
    "ds_read_b64_tr_b16 a[16:17], v220 offset:0\n"
    "ds_read_b64_tr_b16 a[18:19], v220 offset:3072\n"
    "ds_read_b64_tr_b16 a[20:21], v220 offset:8\n"
    "ds_read_b64_tr_b16 a[22:23], v220 offset:3080\n"
    "ds_read_b64_tr_b16 a[24:25], v220 offset:64\n"
    "ds_read_b64_tr_b16 a[26:27], v220 offset:3136\n"
    "ds_read_b64_tr_b16 a[28:29], v220 offset:72\n"
    "ds_read_b64_tr_b16 a[30:31], v220 offset:3144\n"
    "ds_read_b64_tr_b16 v[168:169], v162 offset:128\n"
    "ds_read_b64_tr_b16 v[170:171], v162 offset:3200\n"
    "ds_write2_b32 v216, a194, a195 offset0:40 offset1:60\n"
    "s_mov_b32 s65, 24576\n"
    "buffer_load_dwordx4 v[48:51], v210, s[84:87], s65 offen\n"
    "s_waitcnt vmcnt(14)\n"
    "ds_write2_b32 v216, a196, a197 offset0:80 offset1:100\n"
    "ds_write2_b32 v216, a198, a199 offset0:120 offset1:140\n"
    "v_add_u32_e32 v216, 1360, v216\n"
    "s_waitcnt vmcnt(13)\n"
    "ds_write2_b32 v216, a200, a201 offset0:0 offset1:20\n"
    "s_mov_b32 s65, 26624\n"
    "buffer_load_dwordx4 v[52:55], v210, s[84:87], s65 offen\n"
    "ds_write2_b32 v216, a202, a203 offset0:40 offset1:60\n"
    "s_waitcnt vmcnt(12)\n"
    "v_or3_b32 v196, v196, v24, v25\n"
    "v_or3_b32 v196, v196, v26, v27\n"
    "v_lshlrev_b32_e32 v232, 16, v24\n"
    "ds_write2_b32 v216, a204, a205 offset0:80 offset1:100\n"
    "v_lshlrev_b32_e64 v233, 16, v25\n"
    "v_lshlrev_b32_e64 v234, 16, v26\n"
    "ds_write2_b32 v216, a206, a207 offset0:120 offset1:140\n"
    "v_add_u32_e32 v216, -4800, v216\n"
    "v_lshlrev_b32_e64 v235, 16, v27\n"
    "v_and_b32_e32 v24, 0xffff0000, v24\n"
    "v_and_b32_e32 v25, 0xffff0000, v25\n"
    "s_mov_b32 s65, 28672\n"
    "buffer_load_dwordx4 v[56:59], v210, s[84:87], s65 offen\n"
    "v_and_b32_e32 v26, 0xffff0000, v26\n"
    "v_and_b32_e32 v27, 0xffff0000, v27\n"
    "v_cvt_pk_f16_f32 v24, v232, v24\n"
    "v_cvt_pk_f16_f32 v25, v233, v25\n"
    "v_cvt_pk_f16_f32 v26, v234, v26\n"
    "v_cvt_pk_f16_f32 v27, v235, v27\n"
    "ds_write_b128 v218, v[24:27] offset:12288\n"
    "s_waitcnt vmcnt(12)\n"
    "v_or3_b32 v196, v196, v28, v29\n"
    "v_or3_b32 v196, v196, v30, v31\n"
    "v_lshlrev_b32_e32 v232, 16, v28\n"
    "v_lshlrev_b32_e64 v233, 16, v29\n"
    "v_lshlrev_b32_e64 v234, 16, v30\n"
    "v_lshlrev_b32_e64 v235, 16, v31\n"
    "v_and_b32_e32 v28, 0xffff0000, v28\n"
    "v_and_b32_e32 v29, 0xffff0000, v29\n"
    "v_and_b32_e32 v30, 0xffff0000, v30\n"
    "s_mov_b32 s65, 30720\n"
    "buffer_load_dwordx4 v[60:63], v210, s[84:87], s65 offen\n"
    "v_and_b32_e32 v31, 0xffff0000, v31\n"
    "v_cvt_pk_f16_f32 v28, v232, v28\n"
    "v_cvt_pk_f16_f32 v29, v233, v29\n"
    "v_cvt_pk_f16_f32 v30, v234, v30\n"
    "v_cvt_pk_f16_f32 v31, v235, v31\n"
    "ds_write_b128 v218, v[28:31] offset:14336\n"
    "s_waitcnt vmcnt(12)\n"
    "v_or3_b32 v196, v196, v32, v33\n"
    "v_or3_b32 v196, v196, v34, v35\n"
    "v_lshlrev_b32_e32 v232, 16, v32\n"
    "v_lshlrev_b32_e64 v233, 16, v33\n"
    "v_lshlrev_b32_e64 v234, 16, v34\n"
    "v_lshlrev_b32_e64 v235, 16, v35\n"
    "v_and_b32_e32 v32, 0xffff0000, v32\n"
    "v_and_b32_e32 v33, 0xffff0000, v33\n"
    "v_and_b32_e32 v34, 0xffff0000, v34\n"
    "v_and_b32_e32 v35, 0xffff0000, v35\n"
    "s_mov_b32 s65, 32768\n"
    "buffer_load_dwordx4 v[64:67], v210, s[84:87], s65 offen\n"
    "v_cvt_pk_f16_f32 v32, v232, v32\n"
    "v_cvt_pk_f16_f32 v33, v233, v33\n"
    "v_cvt_pk_f16_f32 v34, v234, v34\n"
    "v_cvt_pk_f16_f32 v35, v235, v35\n"
    "ds_write_b128 v218, v[32:35] offset:16384\n"
    "s_waitcnt vmcnt(12)\n"
    "v_or3_b32 v196, v196, v36, v37\n"
    "v_or3_b32 v196, v196, v38, v39\n"
    "v_lshlrev_b32_e32 v232, 16, v36\n"
    "v_lshlrev_b32_e64 v233, 16, v37\n"
    "v_lshlrev_b32_e64 v234, 16, v38\n"
    "v_lshlrev_b32_e64 v235, 16, v39\n"
    "v_and_b32_e32 v36, 0xffff0000, v36\n"
    "v_and_b32_e32 v37, 0xffff0000, v37\n"
    "v_and_b32_e32 v38, 0xffff0000, v38\n"
    "v_and_b32_e32 v39, 0xffff0000, v39\n"
    "v_cvt_pk_f16_f32 v36, v232, v36\n"
    "s_mov_b32 s65, 34816\n"
    "buffer_load_dwordx4 v[68:71], v210, s[84:87], s65 offen\n"
    "v_cvt_pk_f16_f32 v37, v233, v37\n"
    "v_cvt_pk_f16_f32 v38, v234, v38\n"
    "v_cvt_pk_f16_f32 v39, v235, v39\n"
    "ds_write_b128 v218, v[36:39] offset:24576\n"
    "s_waitcnt vmcnt(12)\n"
    "v_or3_b32 v196, v196, v40, v41\n"
    "s_waitcnt lgkmcnt(1)\n"
    "s_barrier\n"
    "s_nop 0\n"
    "ds_read_b64_tr_b16 a[32:33], v220 offset:12288\n"
    "ds_read_b64_tr_b16 a[34:35], v220 offset:15360\n"
    "ds_read_b64_tr_b16 a[36:37], v220 offset:12296\n"
    "s_mov_b32 s65, 884736\n"
    "buffer_load_dwordx4 a[236:239], v208, s[40:43], s65 offen\n"
    "ds_read_b64_tr_b16 a[38:39], v220 offset:15368\n"
    "ds_read_b64_tr_b16 a[40:41], v220 offset:12352\n"
    "v_or3_b32 v196, v196, v42, v43\n"
    "ds_read_b64_tr_b16 a[42:43], v220 offset:15424\n"
    "ds_read_b64_tr_b16 a[44:45], v220 offset:12360\n"
    "ds_read_b64_tr_b16 a[46:47], v220 offset:15432\n"
    "v_lshlrev_b32_e64 v232, 16, v40\n"
    "ds_read_b64_tr_b16 v[172:173], v162 offset:12416\n"
    "ds_read_b64_tr_b16 v[174:175], v162 offset:15488\n"
    "v_lshlrev_b32_e64 v233, 16, v41\n"
    "v_lshlrev_b32_e64 v234, 16, v42\n"
    "v_lshlrev_b32_e64 v235, 16, v43\n"
    "v_and_b32_e32 v40, 0xffff0000, v40\n"
    "v_and_b32_e32 v41, 0xffff0000, v41\n"
    "v_and_b32_e32 v42, 0xffff0000, v42\n"
    "v_and_b32_e32 v43, 0xffff0000, v43\n"
    "v_cvt_pk_f16_f32 v40, v232, v40\n"
    "v_cvt_pk_f16_f32 v41, v233, v41\n"
    "v_cvt_pk_f16_f32 v42, v234, v42\n"
    "s_mov_b32 s65, 1179648\n"
    "buffer_load_dwordx4 a[240:243], v208, s[40:43], s65 offen\n"
    "v_cvt_pk_f16_f32 v43, v235, v43\n"
    "ds_write_b128 v218, v[40:43] offset:26624\n"
    "s_waitcnt vmcnt(13)\n"
    "v_or3_b32 v196, v196, v44, v45\n"
    "v_or3_b32 v196, v196, v46, v47\n"
    "v_lshlrev_b32_e32 v232, 16, v44\n"
    "v_lshlrev_b32_e64 v233, 16, v45\n"
    "v_lshlrev_b32_e64 v234, 16, v46\n"
    "v_lshlrev_b32_e64 v235, 16, v47\n"
    "v_and_b32_e32 v44, 0xffff0000, v44\n"
    "v_and_b32_e32 v45, 0xffff0000, v45\n"
    "v_and_b32_e32 v46, 0xffff0000, v46\n"
    "v_and_b32_e32 v47, 0xffff0000, v47\n"
    "v_cvt_pk_f16_f32 v44, v232, v44\n"
    "v_cvt_pk_f16_f32 v45, v233, v45\n"
    "v_cvt_pk_f16_f32 v46, v234, v46\n"
    "v_cvt_pk_f16_f32 v47, v235, v47\n"
    "s_mov_b32 s65, 1277952\n"
    "buffer_load_dwordx4 a[244:247], v208, s[40:43], s65 offen\n"
    "ds_write_b128 v218, v[44:47] offset:28672\n"
    "v_xor_b32_e32 v210, 64, v219\n"
    "s_waitcnt vmcnt(13)\n"
    "ds_write2_b32 v216, a208, a209 offset0:0 offset1:20\n"
    "ds_write2_b32 v216, a210, a211 offset0:40 offset1:60\n"
    "s_mov_b32 s65, 1572864\n"
    "buffer_load_dwordx4 a[248:251], v208, s[40:43], s65 offen\n"
    "s_waitcnt vmcnt(13)\n"
    "ds_write2_b32 v216, a212, a213 offset0:80 offset1:100\n"
    "s_waitcnt lgkmcnt(3)\n"
    "s_barrier\n"
    "s_nop 0\n"
    "ds_read_b64_tr_b16 a[48:49], v220 offset:24576\n"
    "ds_read_b64_tr_b16 a[50:51], v220 offset:27648\n"
    "ds_read_b64_tr_b16 a[52:53], v220 offset:24584\n"
    "ds_read_b64_tr_b16 a[54:55], v220 offset:27656\n"
    "ds_read_b64_tr_b16 a[56:57], v220 offset:24640\n"
    "ds_read_b64_tr_b16 a[58:59], v220 offset:27712\n"
    "s_mov_b32 s65, 1671168\n"
    "buffer_load_dwordx4 a[252:255], v208, s[40:43], s65 offen\n"
    "ds_read_b64_tr_b16 a[60:61], v220 offset:24648\n"
    "ds_read_b64_tr_b16 a[62:63], v220 offset:27720\n"
    "v_xor_b32_e32 v208, 0, v219\n"
    "ds_read_b64_tr_b16 v[184:185], v162 offset:24704\n"
    "ds_read_b64_tr_b16 v[186:187], v162 offset:27776\n"
    "ds_write2_b32 v216, a214, a215 offset0:120 offset1:140\n"
    "v_add_u32_e32 v216, 1360, v216\n"
    "s_waitcnt vmcnt(13)\n"
    "ds_write2_b32 v216, a224, a225 offset0:0 offset1:20\n"
    "s_add_u32 vcc_lo, s96, 28672\n"
    "s_addc_u32 vcc_hi, s97, 0\n"
    "global_load_dwordx4 v[200:203], v213, vcc\n"
    "ds_write2_b32 v216, a226, a227 offset0:40 offset1:60\n"
    "s_waitcnt vmcnt(13)\n"
    "ds_write2_b32 v216, a228, a229 offset0:80 offset1:100\n"
    "ds_write2_b32 v216, a230, a231 offset0:120 offset1:140\n"
    "v_add_u32_e32 v216, 1360, v216\n"
    "s_waitcnt vmcnt(12)\n"
    "ds_write2_b32 v216, a232, a233 offset0:0 offset1:20\n"
    "s_add_u32 vcc_lo, s96, 32768\n"
    "s_addc_u32 vcc_hi, s97, 0\n"
    "global_load_dwordx4 v[204:207], v213, vcc\n"
    "s_waitcnt vmcnt(12)\n"
    "v_or3_b32 v196, v196, v48, v49\n"
    "ds_write2_b32 v216, a234, a235 offset0:40 offset1:60\n"
    "v_or3_b32 v196, v196, v50, v51\n"
    "v_lshlrev_b32_e32 v232, 16, v48\n"
    "v_lshlrev_b32_e64 v233, 16, v49\n"
    "v_lshlrev_b32_e64 v234, 16, v50\n"
    "v_lshlrev_b32_e64 v235, 16, v51\n"
    "v_and_b32_e32 v48, 0xffff0000, v48\n"
    "v_and_b32_e32 v49, 0xffff0000, v49\n"
    "v_and_b32_e32 v50, 0xffff0000, v50\n"
    "v_and_b32_e32 v51, 0xffff0000, v51\n"
    "v_cvt_pk_f16_f32 v48, v232, v48\n"
    "v_cvt_pk_f16_f32 v49, v233, v49\n"
    "s_add_u32 vcc_lo, s96, 0\n"
    "s_addc_u32 vcc_hi, s97, 0\n"
    "global_load_dwordx4 v[240:243], v213, vcc\n"
    "v_cvt_pk_f16_f32 v50, v234, v50\n"
    "v_cvt_pk_f16_f32 v51, v235, v51\n"
    "ds_write_b128 v218, v[48:51] offset:0\n"
    "s_waitcnt vmcnt(12)\n"
    "v_or3_b32 v196, v196, v52, v53\n"
    "v_or3_b32 v196, v196, v54, v55\n"
    "v_lshlrev_b32_e32 v232, 16, v52\n"
    "v_lshlrev_b32_e64 v233, 16, v53\n"
    "v_lshlrev_b32_e64 v234, 16, v54\n"
    "v_lshlrev_b32_e64 v235, 16, v55\n"
    "v_and_b32_e32 v52, 0xffff0000, v52\n"
    "v_and_b32_e32 v53, 0xffff0000, v53\n"
    "v_and_b32_e32 v54, 0xffff0000, v54\n"
    "v_and_b32_e32 v55, 0xffff0000, v55\n"
    "v_cvt_pk_f16_f32 v52, v232, v52\n"
    "v_cvt_pk_f16_f32 v53, v233, v53\n"
    "s_add_u32 vcc_lo, s96, 8192\n"
    "s_addc_u32 vcc_hi, s97, 0\n"
    "global_load_dwordx4 v[248:251], v213, vcc\n"
    "v_cvt_pk_f16_f32 v54, v234, v54\n"
    "v_cvt_pk_f16_f32 v55, v235, v55\n"
    "ds_write_b128 v218, v[52:55] offset:2048\n"
    "s_waitcnt vmcnt(12)\n"
    "v_or3_b32 v196, v196, v56, v57\n"
    "v_or3_b32 v196, v196, v58, v59\n"
    "v_lshlrev_b32_e64 v232, 16, v56\n"
    "v_lshlrev_b32_e64 v233, 16, v57\n"
    "v_lshlrev_b32_e64 v234, 16, v58\n"
    "v_lshlrev_b32_e64 v235, 16, v59\n"
    "v_and_b32_e32 v56, 0xffff0000, v56\n"
    "v_and_b32_e32 v57, 0xffff0000, v57\n"
    "v_and_b32_e32 v58, 0xffff0000, v58\n"
    "v_and_b32_e32 v59, 0xffff0000, v59\n"
    "v_cvt_pk_f16_f32 v56, v232, v56\n"
    "v_cvt_pk_f16_f32 v57, v233, v57\n"
    "s_add_u32 vcc_lo, s96, 12288\n"
    "s_addc_u32 vcc_hi, s97, 0\n"
    "global_load_dwordx4 v[252:255], v213, vcc\n"
    "v_cvt_pk_f16_f32 v58, v234, v58\n"
    "v_cvt_pk_f16_f32 v59, v235, v59\n"
    "ds_write_b128 v218, v[56:59] offset:4096\n"
    "s_waitcnt vmcnt(12)\n"
    "v_or3_b32 v196, v196, v60, v61\n"
    "v_or3_b32 v196, v196, v62, v63\n"
    "v_lshlrev_b32_e64 v232, 16, v60\n"
    "v_lshlrev_b32_e64 v233, 16, v61\n"
    "v_lshlrev_b32_e64 v234, 16, v62\n"
    "v_lshlrev_b32_e64 v235, 16, v63\n"
    "v_and_b32_e32 v60, 0xffff0000, v60\n"
    "v_and_b32_e32 v61, 0xffff0000, v61\n"
    "v_and_b32_e32 v62, 0xffff0000, v62\n"
    "v_and_b32_e32 v63, 0xffff0000, v63\n"
    "v_cvt_pk_f16_f32 v60, v232, v60\n"
    "v_cvt_pk_f16_f32 v61, v233, v61\n"
    "s_add_u32 vcc_lo, s96, 4096\n"
    "s_addc_u32 vcc_hi, s97, 0\n"
    "global_load_dwordx4 v[244:247], v213, vcc\n"
    "v_cvt_pk_f16_f32 v62, v234, v62\n"
    "v_cvt_pk_f16_f32 v63, v235, v63\n"
    "ds_write_b128 v218, v[60:63] offset:12288\n"
    "s_waitcnt vmcnt(12)\n"
    "v_or3_b32 v196, v196, v64, v65\n"
    "s_waitcnt lgkmcnt(1)\n"
    "s_barrier\n"
    "ds_read_b64_tr_b16 a[64:65], v220 offset:0\n"
    "ds_read_b64_tr_b16 a[66:67], v220 offset:3072\n"
    "ds_read_b64_tr_b16 a[68:69], v220 offset:8\n"
    "s_add_u32 vcc_lo, s96, 24576\n"
    "s_addc_u32 vcc_hi, s97, 0\n"
    "s_nop 0\n"
    "global_load_dwordx4 v[236:239], v213, vcc\n"
    "ds_read_b64_tr_b16 a[70:71], v220 offset:3080\n"
    "ds_read_b64_tr_b16 a[72:73], v220 offset:64\n"
    "ds_read_b64_tr_b16 a[74:75], v220 offset:3136\n"
    "v_or3_b32 v196, v196, v66, v67\n"
    "ds_read_b64_tr_b16 a[76:77], v220 offset:72\n"
    "ds_read_b64_tr_b16 a[78:79], v220 offset:3144\n"
    "ds_read_b64_tr_b16 v[188:189], v162 offset:128\n"
    "v_lshlrev_b32_e64 v232, 16, v64\n"
    "ds_read_b64_tr_b16 v[190:191], v162 offset:3200\n"
    "v_lshlrev_b32_e64 v233, 16, v65\n"
    "v_lshlrev_b32_e64 v234, 16, v66\n"
    "v_lshlrev_b32_e64 v235, 16, v67\n"
    "v_and_b32_e32 v64, 0xffff0000, v64\n"
    "v_and_b32_e32 v65, 0xffff0000, v65\n"
    "v_and_b32_e32 v66, 0xffff0000, v66\n"
    "v_and_b32_e32 v67, 0xffff0000, v67\n"
    "v_cvt_pk_f16_f32 v64, v232, v64\n"
    "v_cvt_pk_f16_f32 v65, v233, v65\n"
    "s_mov_b32 s65, 49152\n"
    "buffer_load_dwordx4 v[72:75], v209, s[80:83], s65 offen offset:0\n"
    "v_cvt_pk_f16_f32 v66, v234, v66\n"
    "v_cvt_pk_f16_f32 v67, v235, v67\n"
    "ds_write_b128 v218, v[64:67] offset:14336\n"
    "s_waitcnt vmcnt(13)\n"
    "v_or3_b32 v196, v196, v68, v69\n"
    "v_or3_b32 v196, v196, v70, v71\n"
    "v_lshlrev_b32_e32 v232, 16, v68\n"
    "v_lshlrev_b32_e64 v233, 16, v69\n"
    "v_lshlrev_b32_e64 v234, 16, v70\n"
    "v_lshlrev_b32_e64 v235, 16, v71\n"
    "v_and_b32_e32 v68, 0xffff0000, v68\n"
    "v_and_b32_e32 v69, 0xffff0000, v69\n"
    "v_and_b32_e32 v70, 0xffff0000, v70\n"
    "v_and_b32_e32 v71, 0xffff0000, v71\n"
    "v_cvt_pk_f16_f32 v68, v232, v68\n"
    "v_cvt_pk_f16_f32 v69, v233, v69\n"
    "v_cvt_pk_f16_f32 v70, v234, v70\n"
    "s_mov_b32 s65, 49152\n"
    "buffer_load_dwordx4 v[76:79], v209, s[80:83], s65 offen offset:256\n"
    "v_cvt_pk_f16_f32 v71, v235, v71\n"
    "ds_write_b128 v218, v[68:71] offset:16384\n"
    "v_xor_b32_e32 v213, 32, v223\n"
    "s_waitcnt vmcnt(13)\n"
    "ds_write2_b32 v216, a236, a237 offset0:80 offset1:100\n"
    "ds_write2_b32 v216, a238, a239 offset0:120 offset1:140\n"
    "s_mov_b32 s65, 49152\n"
    "buffer_load_dwordx4 v[80:83], v209, s[80:83], s65 offen offset:512\n"
    "v_add_u32_e32 v216, 1360, v216\n"
    "s_waitcnt vmcnt(13)\n"
    "ds_write2_b32 v216, a240, a241 offset0:0 offset1:20\n"
    "s_waitcnt lgkmcnt(3)\n"
    "s_barrier\n"
    "s_nop 0\n"
    "ds_read_b64_tr_b16 a[80:81], v220 offset:12288\n"
    "ds_read_b64_tr_b16 a[82:83], v220 offset:15360\n"
    "ds_read_b64_tr_b16 a[84:85], v220 offset:12296\n"
    "ds_read_b64_tr_b16 a[86:87], v220 offset:15368\n"
    "ds_read_b64_tr_b16 a[88:89], v220 offset:12352\n"
    "s_mov_b32 s65, 52224\n"
    "buffer_load_dwordx4 v[84:87], v209, s[80:83], s65 offen offset:0\n"
    "ds_read_b64_tr_b16 a[90:91], v220 offset:15424\n"
    "ds_read_b64_tr_b16 a[92:93], v220 offset:12360\n"
    "ds_read_b64_tr_b16 a[94:95], v220 offset:15432\n"
    "ds_read_b64_tr_b16 v[192:193], v162 offset:12416\n"
    "ds_read_b64_tr_b16 v[194:195], v162 offset:15488\n"
    "ds_write2_b32 v216, a242, a243 offset0:40 offset1:60\n"
    "s_waitcnt vmcnt(13)\n"
    "ds_write2_b32 v216, a244, a245 offset0:80 offset1:100\n"
    "s_mov_b32 s65, 52224\n"
    "buffer_load_dwordx4 v[88:91], v209, s[80:83], s65 offen offset:256\n"
    "ds_write2_b32 v216, a246, a247 offset0:120 offset1:140\n"
    "v_add_u32_e32 v216, 1360, v216\n"
    "s_waitcnt vmcnt(13)\n"
    "ds_write2_b32 v216, a248, a249 offset0:0 offset1:20\n"
    "ds_write2_b32 v216, a250, a251 offset0:40 offset1:60\n"
    "s_waitcnt vmcnt(12)\n"
    "ds_write2_b32 v216, a252, a253 offset0:80 offset1:100\n"
    "s_mov_b32 s65, 52224\n"
    "buffer_load_dwordx4 v[92:95], v209, s[80:83], s65 offen offset:512\n"
    "ds_write2_b32 v216, a254, a255 offset0:120 offset1:140\n"
    "v_add_u32_e32 v216, 1360, v216\n"
    "s_mov_b32 s65, 0\n"
    "buffer_load_dwordx4 v[96:99], v209, s[80:83], s65 offen offset:0\n"
    "s_mov_b32 s65, 0\n"
    "buffer_load_dwordx4 v[100:103], v209, s[80:83], s65 offen offset:256\n"
    "s_mov_b32 s65, 0\n"
    "buffer_load_dwordx4 v[104:107], v209, s[80:83], s65 offen offset:512\n"
    "s_mov_b32 s65, 3072\n"
    "buffer_load_dwordx4 v[108:111], v209, s[80:83], s65 offen offset:0\n"
    "s_waitcnt vmcnt(12)\n"
    "v_add_u32_e32 v255, s99, v255\n"
    "v_mov_b32_e32 v254, 0\n"
    "s_nop 1\n"
    "v_mfma_f32_32x32x1_2b_f32 v[0:31], v254, v254, 0\n"
    "v_mfma_f32_32x32x1_2b_f32 v[32:63], v254, v254, 0\n"
    "s_mov_b32 s65, 3072\n"
    "buffer_load_dwordx4 v[112:115], v209, s[80:83], s65 offen offset:256\n"
    "s_waitcnt vmcnt(12)\n"
    "v_and_b32_e32 v246, s67, v246\n"
    "v_and_b32_e64 v247, s67, v247\n"
    "v_add_u32_e64 v246, s98, v246\n"
    "v_add_u32_e64 v247, s98, v247\n"
    "v_add_u32_e64 v245, s99, v245\n"
    "s_mov_b32 s65, 3072\n"
    "buffer_load_dwordx4 v[116:119], v209, s[80:83], s65 offen offset:512\n"
    "s_mov_b32 s65, 24576\n"
    "buffer_load_dwordx4 v[120:123], v209, s[80:83], s65 offen offset:0\n"
    "s_waitcnt vmcnt(12)\n"
    "v_or3_b32 v196, v196, v72, v73\n"
    "v_or3_b32 v196, v196, v74, v75\n"
    "v_and_b32_e32 v197, 0x7fff7fff, v72\n"
    "v_add_u32_e32 v72, v201, v72\n"
    "v_add_u32_e64 v74, v201, v74\n"
    "ds_write_b128 v217, v[72:75] offset:24576\n"
    "s_mov_b32 s65, 24576\n"
    "buffer_load_dwordx4 v[124:127], v209, s[80:83], s65 offen offset:256\n"
    "s_waitcnt vmcnt(12)\n"
    "v_or3_b32 v196, v196, v76, v77\n"
    "v_or3_b32 v196, v196, v78, v79\n"
    "v_add_u32_e32 v76, v203, v76\n"
    "v_add_u32_e64 v78, v203, v78\n"
    "ds_write_b128 v217, v[76:79] offset:24832\n"
    "s_mov_b32 s65, 24576\n"
    "buffer_load_dwordx4 v[128:131], v209, s[80:83], s65 offen offset:512\n"
    "s_waitcnt vmcnt(12)\n"
    "v_or3_b32 v196, v196, v80, v81\n"
    "v_or3_b32 v196, v196, v82, v83\n"
    "v_add_u32_e32 v80, v205, v80\n"
    "v_add_u32_e64 v82, v205, v82\n"
    "ds_write_b128 v217, v[80:83] offset:25088\n"
    "s_mov_b32 s65, 27648\n"
    "buffer_load_dwordx4 v[132:135], v209, s[80:83], s65 offen offset:0\n"
    "s_waitcnt vmcnt(12)\n"
    "v_or3_b32 v196, v196, v84, v85\n"
    "v_or3_b32 v196, v196, v86, v87\n"
    "v_add_u32_e32 v84, v201, v84\n"
    "v_add_u32_e64 v86, v201, v86\n"
    "ds_write_b128 v217, v[84:87] offset:0\n"
    "s_waitcnt lgkmcnt(1)\n"
    "s_barrier\n"
    "ds_read_b64_tr_b16 a[96:97], v208 offset:24576\n"
    "ds_read_b64_tr_b16 a[100:101], v210 offset:24576\n"
    "ds_read_b64_tr_b16 v[180:181], v161 offset:24576\n"
    "s_mov_b32 s65, 27648\n"
    "buffer_load_dwordx4 v[136:139], v209, s[80:83], s65 offen offset:256\n"
    "ds_read_b64_tr_b16 a[104:105], v212 offset:24576\n"
    "ds_read_b64_tr_b16 a[108:109], v213 offset:24576\n"
    "s_waitcnt vmcnt(12)\n"
    "s_nop 0\n"
    "v_or3_b32 v196, v196, v88, v89\n"
    "ds_read_b64_tr_b16 a[112:113], v214 offset:24576\n"
    "ds_read_b64_tr_b16 a[116:117], v215 offset:24576\n"
    "ds_read_b64_tr_b16 a[120:121], v208 offset:24832\n"
    "ds_read_b64_tr_b16 a[124:125], v210 offset:24832\n"
    "v_or3_b32 v196, v196, v90, v91\n"
    "v_add_u32_e64 v88, v203, v88\n"
    "v_add_u32_e64 v90, v203, v90\n"
    "ds_write_b128 v217, v[88:91] offset:256\n"
    "s_mov_b32 s65, 27648\n"
    "buffer_load_dwordx4 v[140:143], v209, s[80:83], s65 offen offset:512\n"
    "s_waitcnt vmcnt(12)\n"
    "v_or3_b32 v196, v196, v92, v93\n"
    "v_or3_b32 v196, v196, v94, v95\n"
    "v_add_u32_e32 v92, v205, v92\n"
    "v_add_u32_e64 v94, v205, v94\n"
    "ds_write_b128 v217, v[92:95] offset:512\n"
    "v_mfma_f32_32x32x1_2b_f32 v[64:95], v254, v254, 0\n"
    "v_xor_b32_e32 v209, 32, v219\n"
    "v_lshl_add_u32 v160, s51, 8, v209\n"
    "v_add_u32_e32 v254, s54, v242\n"
    "ds_read_b64_tr_b16 v[176:177], v160 offset:24576\n"
    "buffer_load_dword v200, v246, s[40:43], s60 offen offset:0\n"
    "s_waitcnt vmcnt(12)\n"
    "v_or3_b32 v196, v196, v96, v97\n"
    "v_or3_b32 v196, v196, v98, v99\n"
    "v_add_u32_e32 v96, v201, v96\n"
    "v_add_u32_e64 v98, v201, v98\n"
    "ds_write_b128 v217, v[96:99] offset:12288\n"
    "s_waitcnt lgkmcnt(1)\n"
    "s_barrier\n"
    "buffer_load_dword v200, v246, s[40:43], s61 offen offset:0\n"
    "ds_read_b64_tr_b16 a[98:99], v208 offset:0\n"
    "ds_read_b64_tr_b16 v[178:179], v160 offset:0\n"
    "ds_read_b64_tr_b16 a[102:103], v210 offset:0\n"
    "s_waitcnt vmcnt(12)\n"
    "s_nop 0\n"
    "v_or3_b32 v196, v196, v100, v101\n"
    "ds_read_b64_tr_b16 v[182:183], v161 offset:0\n"
    "ds_read_b64_tr_b16 a[106:107], v212 offset:0\n"
    "ds_read_b64_tr_b16 a[110:111], v213 offset:0\n"
    "ds_read_b64_tr_b16 a[114:115], v214 offset:0\n"
    "v_or3_b32 v196, v196, v102, v103\n"
    "ds_read_b64_tr_b16 a[118:119], v215 offset:0\n"
    "ds_read_b64_tr_b16 a[122:123], v208 offset:256\n"
    "ds_read_b64_tr_b16 a[126:127], v210 offset:256\n"
    "v_add_u32_e64 v100, v203, v100\n"
    "buffer_load_dword v200, v246, s[40:43], s60 offen offset:128\n"
    "ds_read_b128 a[224:227], v250 offset:0\n"
    "ds_read_b128 a[228:231], v250 offset:320\n"
    "ds_read_b128 a[232:235], v251 offset:0\n"
    "v_add_u32_e64 v102, v203, v102\n"
    "ds_read_b128 a[236:239], v251 offset:336\n"
    "ds_read_b128 a[240:243], v250 offset:640\n"
    "ds_read_b128 a[244:247], v250 offset:960\n"
    "s_waitcnt vmcnt(12)\n"
    "s_nop 0\n"
    "v_or3_b32 v196, v196, v104, v105\n"
    "ds_read_b128 a[248:251], v252 offset:0\n"
    "ds_read_b128 a[252:255], v252 offset:336\n"
    "ds_read_b128 v[144:147], v250 offset:80\n"
    "ds_read_b128 v[148:151], v250 offset:400\n"
    "v_or3_b32 v196, v196, v106, v107\n"
    "ds_read_b128 v[152:155], v251 offset:672\n"
    "ds_read_b128 v[156:159], v251 offset:1008\n"
    "buffer_load_dword v200, v246, s[40:43], s61 offen offset:128\n"
    "ds_write_b128 v217, v[100:103] offset:12544\n"
    "v_add_u32_e64 v104, v205, v104\n"
    "v_add_u32_e32 v106, v205, v106\n"
    "s_waitcnt vmcnt(12)\n"
    "v_or3_b32 v196, v196, v108, v109\n"
    "v_or3_b32 v196, v196, v110, v111\n"
    "v_add_u32_e64 v108, v201, v108\n"
    "v_add_u32_e64 v110, v201, v110\n"
    "ds_write_b128 v217, v[104:107] offset:12800\n"
    "s_waitcnt lgkmcnt(10)\n"
    "s_nop 0\n"
    "v_smfmac_f32_16x16x64_bf16 v[0:3], a[232:235], a[224:231], v240\n"
    "buffer_load_dword v200, v246, s[40:43], s60 offen offset:256\n"
    "v_smfmac_f32_16x16x64_bf16 v[4:7], a[236:239], a[224:231], v240 abid:1\n"
    "ds_read_b128 a[224:227], v250 offset:720\n"
    "ds_read_b128 a[228:231], v250 offset:1040\n"
    "ds_read_b128 a[232:235], v252 offset:672\n"
    "s_waitcnt vmcnt(12)\n"
    "v_or3_b32 v196, v196, v112, v113\n"
    "s_waitcnt lgkmcnt(10)\n"
    "v_smfmac_f32_16x16x64_bf16 v[0:3], a[248:251], a[240:247], v240\n"
    "s_waitcnt lgkmcnt(9)\n"
    "s_nop 0\n"
    "ds_read_b128 a[236:239], v252 offset:1008\n"
    "v_smfmac_f32_16x16x64_bf16 v[4:7], a[252:255], a[240:247], v240 abid:1\n"
    "ds_read_b128 a[240:243], v250 offset:160\n"
    "ds_read_b128 a[244:247], v250 offset:480\n"
    "ds_read_b128 a[248:251], v251 offset:1344\n"
    "v_or3_b32 v196, v196, v114, v115\n"
    "ds_read_b128 a[252:255], v251 offset:1680\n"
    "buffer_load_dword v200, v246, s[40:43], s61 offen offset:256\n"
    "v_pk_mov_b32 v[224:225], v[0:1], v[0:1] op_sel:[0,1]\n"
    "v_pk_mov_b32 v[226:227], v[2:3], v[2:3] op_sel:[0,1]\n"
    "v_pk_mul_f32 v[228:229], v[0:1], v[0:1]\n"
    "v_pk_mul_f32 v[230:231], v[2:3], v[2:3]\n"
    "v_pk_add_f32 v[224:225], v[224:225], v[4:5]\n"
    "v_pk_add_f32 v[226:227], v[226:227], v[6:7]\n"
    "v_pk_fma_f32 v[228:229], v[4:5], v[4:5], v[228:229]\n"
    "v_pk_fma_f32 v[230:231], v[6:7], v[6:7], v[230:231]\n"
    "s_waitcnt lgkmcnt(9)\n"
    "v_smfmac_f32_16x16x64_bf16 v[8:11], v[152:155], v[144:151], v240\n"
    "ds_read_b128 v[152:155], v252 offset:1344\n"
    "v_smfmac_f32_16x16x64_bf16 v[12:15], v[156:159], v[144:151], v240 abid:1\n"
    "s_waitcnt lgkmcnt(9)\n"
    "ds_read_b128 v[144:147], v250 offset:800\n"
    "ds_read_b128 v[148:151], v250 offset:1120\n"
    "ds_read_b128 v[156:159], v252 offset:1680\n"
    "s_barrier\n"
    "ds_read_b64_tr_b16 a[128:129], v208 offset:12288\n"
    "ds_read_b64_tr_b16 a[132:133], v209 offset:12288\n"
    "ds_read_b64_tr_b16 a[136:137], v210 offset:12288\n"
    "v_add_u32_e32 v112, v203, v112\n"
    "ds_read_b64_tr_b16 a[140:141], v211 offset:12288\n"
    "s_waitcnt lgkmcnt(13)\n"
    "v_smfmac_f32_16x16x64_bf16 v[8:11], a[232:235], a[224:231], v240\n"
    "s_waitcnt lgkmcnt(12)\n"
    "ds_read_b128 a[232:235], v251 offset:2016\n"
    "v_smfmac_f32_16x16x64_bf16 v[12:15], a[236:239], a[224:231], v240 abid:1\n"
    "ds_read_b128 a[224:227], v250 offset:240\n"
    "ds_read_b128 a[228:231], v250 offset:560\n"
    "ds_read_b128 a[236:239], v251 offset:2352\n"
    "v_add_u32_e64 v114, v203, v114\n"
    "ds_read_b64_tr_b16 a[144:145], v212 offset:12288\n"
    "v_pk_add_f32 v[224:225], v[224:225], v[8:9]\n"
    "v_pk_add_f32 v[226:227], v[226:227], v[10:11]\n"
    "v_pk_fma_f32 v[228:229], v[8:9], v[8:9], v[228:229]\n"
    "v_pk_fma_f32 v[230:231], v[10:11], v[10:11], v[230:231]\n"
    "v_pk_add_f32 v[224:225], v[224:225], v[12:13]\n"
    "v_pk_add_f32 v[226:227], v[226:227], v[14:15]\n"
    "v_pk_fma_f32 v[228:229], v[12:13], v[12:13], v[228:229]\n"
    "v_pk_fma_f32 v[230:231], v[14:15], v[14:15], v[230:231]\n"
    "s_waitcnt lgkmcnt(12)\n"
    "v_smfmac_f32_16x16x64_bf16 v[16:19], a[248:251], a[240:247], v240\n"
    "ds_read_b128 a[248:251], v252 offset:2016\n"
    "v_smfmac_f32_16x16x64_bf16 v[20:23], a[252:255], a[240:247], v240 abid:1\n"
    "s_waitcnt lgkmcnt(11)\n"
    "ds_read_b128 a[240:243], v250 offset:880\n"
    "v_smfmac_f32_16x16x64_bf16 v[16:19], v[152:155], v[144:151], v240\n"
    "s_waitcnt lgkmcnt(11)\n"
    "s_nop 0\n"
    "ds_read_b128 a[244:247], v250 offset:1200\n"
    "v_smfmac_f32_16x16x64_bf16 v[20:23], v[156:159], v[144:151], v240 abid:1\n"
    "ds_read_b128 a[252:255], v252 offset:2352\n"
    "ds_read_b128 v[144:147], v250 offset:26944\n"
    "ds_read_b128 v[148:151], v250 offset:27264\n"
    "s_waitcnt vmcnt(12)\n"
    "s_nop 0\n"
    "v_or3_b32 v196, v196, v116, v117\n"
    "v_pk_add_f32 v[224:225], v[224:225], v[16:17]\n"
    "v_pk_add_f32 v[226:227], v[226:227], v[18:19]\n"
    "v_pk_fma_f32 v[228:229], v[16:17], v[16:17], v[228:229]\n"
    "v_pk_fma_f32 v[230:231], v[18:19], v[18:19], v[230:231]\n"
    "v_pk_add_f32 v[224:225], v[224:225], v[20:21]\n"
    "v_pk_add_f32 v[226:227], v[226:227], v[22:23]\n"
    "v_pk_fma_f32 v[228:229], v[20:21], v[20:21], v[228:229]\n"
    "v_pk_fma_f32 v[230:231], v[22:23], v[22:23], v[230:231]\n"
    "ds_read_b128 v[152:155], v251 offset:10752\n"
    "s_waitcnt lgkmcnt(9)\n"
    "v_smfmac_f32_16x16x64_bf16 v[24:27], a[232:235], a[224:231], v240\n"
    "s_waitcnt lgkmcnt(8)\n"
    "ds_read_b128 v[156:159], v251 offset:11088\n"
    "v_smfmac_f32_16x16x64_bf16 v[28:31], a[236:239], a[224:231], v240 abid:1\n"
    "ds_read_b128 a[224:227], v250 offset:27584\n"
    "ds_read_b128 a[228:231], v250 offset:27904\n"
    "ds_read_b128 a[232:235], v252 offset:10752\n"
    "v_or3_b32 v196, v196, v118, v119\n"
    "ds_read_b128 a[236:239], v252 offset:11088\n"
    "ds_read_b64_tr_b16 a[148:149], v213 offset:12288\n"
    "ds_read_b64_tr_b16 a[152:153], v214 offset:12288\n"
    "s_waitcnt lgkmcnt(11)\n"
    "v_smfmac_f32_16x16x64_bf16 v[24:27], a[248:251], a[240:247], v240\n"
    "s_waitcnt lgkmcnt(10)\n"
    "ds_read_b128 a[248:251], v251 offset:11424\n"
    "v_smfmac_f32_16x16x64_bf16 v[28:31], a[252:255], a[240:247], v240 abid:1\n"
    "ds_read_b128 a[240:243], v250 offset:27024\n"
    "ds_read_b128 a[244:247], v250 offset:27344\n"
    "ds_read_b128 a[252:255], v251 offset:11760\n"
    "v_add_u32_e64 v116, v205, v116\n"
    "ds_read_b64_tr_b16 a[156:157], v215 offset:12288\n"
    "v_pk_add_f32 v[224:225], v[224:225], v[24:25]\n"
    "v_pk_add_f32 v[226:227], v[226:227], v[26:27]\n"
    "v_pk_fma_f32 v[228:229], v[24:25], v[24:25], v[228:229]\n"
    "v_pk_fma_f32 v[230:231], v[26:27], v[26:27], v[230:231]\n"
    "v_pk_add_f32 v[224:225], v[224:225], v[28:29]\n"
    "v_pk_add_f32 v[226:227], v[226:227], v[30:31]\n"
    "v_pk_fma_f32 v[228:229], v[28:29], v[28:29], v[228:229]\n"
    "v_pk_fma_f32 v[230:231], v[30:31], v[30:31], v[230:231]\n"
    "s_waitcnt lgkmcnt(11)\n"
    "v_smfmac_f32_16x16x64_bf16 v[32:35], v[152:155], v[144:151], v240\n"
    "ds_read_b128 v[152:155], v252 offset:11424\n"
    "v_smfmac_f32_16x16x64_bf16 v[36:39], v[156:159], v[144:151], v240 abid:1\n"
    "ds_read_b128 v[144:147], v250 offset:27664\n"
    "s_waitcnt lgkmcnt(10)\n"
    "v_smfmac_f32_16x16x64_bf16 v[32:35], a[232:235], a[224:231], v240\n"
    "s_waitcnt lgkmcnt(9)\n"
    "s_nop 0\n"
    "ds_read_b128 v[148:151], v250 offset:27984\n"
    "v_smfmac_f32_16x16x64_bf16 v[36:39], a[236:239], a[224:231], v240 abid:1\n"
    "ds_read_b128 v[156:159], v252 offset:11760\n"
    "ds_read_b128 a[224:227], v250 offset:27104\n"
    "ds_read_b128 a[228:231], v250 offset:27424\n"
    "v_add_u32_e64 v118, v205, v118\n"
    "ds_read_b128 a[232:235], v251 offset:12096\n"
    "v_pk_add_f32 v[224:225], v[224:225], v[32:33]\n"
    "v_pk_add_f32 v[226:227], v[226:227], v[34:35]\n"
    "v_pk_fma_f32 v[228:229], v[32:33], v[32:33], v[228:229]\n"
    "v_pk_fma_f32 v[230:231], v[34:35], v[34:35], v[230:231]\n"
    "v_pk_add_f32 v[224:225], v[224:225], v[36:37]\n"
    "v_pk_add_f32 v[226:227], v[226:227], v[38:39]\n"
    "v_pk_fma_f32 v[228:229], v[36:37], v[36:37], v[228:229]\n"
    "v_pk_fma_f32 v[230:231], v[38:39], v[38:39], v[230:231]\n"
    "s_waitcnt lgkmcnt(9)\n"
    "v_smfmac_f32_16x16x64_bf16 v[40:43], a[248:251], a[240:247], v240\n"
    "s_waitcnt lgkmcnt(7)\n"
    "ds_read_b128 a[236:239], v251 offset:12432\n"
    "v_smfmac_f32_16x16x64_bf16 v[44:47], a[252:255], a[240:247], v240 abid:1\n"
    "ds_read_b128 a[240:243], v250 offset:27744\n"
    "ds_read_b128 a[244:247], v250 offset:28064\n"
    "ds_read_b128 a[248:251], v252 offset:12096\n"
    "s_waitcnt vmcnt(11)\n"
    "v_or3_b32 v196, v196, v120, v121\n"
    "ds_read_b128 a[252:255], v252 offset:12432\n"
    "s_waitcnt lgkmcnt(9)\n"
    "v_smfmac_f32_16x16x64_bf16 v[40:43], v[152:155], v[144:151], v240\n"
    "s_waitcnt lgkmcnt(8)\n"
    "s_nop 0\n"
    "ds_read_b64_tr_b16 a[160:161], v208 offset:12544\n"
    "v_smfmac_f32_16x16x64_bf16 v[44:47], v[156:159], v[144:151], v240 abid:1\n"
    "ds_read_b64_tr_b16 a[164:165], v209 offset:12544\n"
    "ds_read_b64_tr_b16 a[168:169], v210 offset:12544\n"
    "ds_read_b64_tr_b16 a[172:173], v211 offset:12544\n"
    "v_or3_b32 v196, v196, v122, v123\n"
    "ds_read_b128 v[144:147], v250 offset:27184\n"
    "v_pk_add_f32 v[224:225], v[224:225], v[40:41]\n"
    "v_pk_add_f32 v[226:227], v[226:227], v[42:43]\n"
    "v_pk_fma_f32 v[228:229], v[40:41], v[40:41], v[228:229]\n"
    "v_pk_fma_f32 v[230:231], v[42:43], v[42:43], v[230:231]\n"
    "v_pk_add_f32 v[224:225], v[224:225], v[44:45]\n"
    "v_pk_add_f32 v[226:227], v[226:227], v[46:47]\n"
    "v_pk_fma_f32 v[228:229], v[44:45], v[44:45], v[228:229]\n"
    "v_pk_fma_f32 v[230:231], v[46:47], v[46:47], v[230:231]\n"
    "s_waitcnt lgkmcnt(9)\n"
    "v_smfmac_f32_16x16x64_bf16 v[48:51], a[232:235], a[224:231], v240\n"
    "ds_read_b128 v[148:151], v250 offset:27504\n"
    "v_smfmac_f32_16x16x64_bf16 v[52:55], a[236:239], a[224:231], v240 abid:1\n"
    "ds_read_b128 v[152:155], v251 offset:12768\n"
    "s_waitcnt lgkmcnt(8)\n"
    "v_smfmac_f32_16x16x64_bf16 v[48:51], a[248:251], a[240:247], v240\n"
    "s_waitcnt lgkmcnt(7)\n"
    "s_nop 0\n"
    "ds_read_b128 v[156:159], v251 offset:13104\n"
    "v_smfmac_f32_16x16x64_bf16 v[52:55], a[252:255], a[240:247], v240 abid:1\n"
    "ds_read_b128 a[224:227], v250 offset:27824\n"
    "ds_read_b128 a[228:231], v250 offset:28144\n"
    "ds_read_b128 a[232:235], v252 offset:12768\n"
    "v_add_u32_e64 v120, v201, v120\n"
    "ds_read_b128 a[236:239], v252 offset:13104\n"
    "v_pk_add_f32 v[224:225], v[224:225], v[48:49]\n"
    "v_pk_add_f32 v[226:227], v[226:227], v[50:51]\n"
    "v_pk_fma_f32 v[228:229], v[48:49], v[48:49], v[228:229]\n"
    "v_pk_fma_f32 v[230:231], v[50:51], v[50:51], v[230:231]\n"
    "v_pk_add_f32 v[224:225], v[224:225], v[52:53]\n"
    "v_pk_add_f32 v[226:227], v[226:227], v[54:55]\n"
    "v_pk_fma_f32 v[228:229], v[52:53], v[52:53], v[228:229]\n"
    "v_pk_fma_f32 v[230:231], v[54:55], v[54:55], v[230:231]\n"
    "ds_read_b128 a[240:243], v250 offset:53888\n"
    "ds_read_b128 a[244:247], v250 offset:54208\n"
    "ds_read_b128 a[248:251], v251 offset:21504\n"
    "ds_read_b128 a[252:255], v251 offset:21840\n"
    "v_add_u32_e64 v122, v201, v122\n"
    "ds_write_b128 v217, v[108:111] offset:24576\n"
    "s_waitcnt lgkmcnt(9)\n"
    "v_smfmac_f32_16x16x64_bf16 v[56:59], v[152:155], v[144:151], v240\n"
    "s_waitcnt vmcnt(8)\n"
    "v_or3_b32 v196, v196, v124, v125\n"
    "v_smfmac_f32_16x16x64_bf16 v[60:63], v[156:159], v[144:151], v240 abid:1\n"
    "ds_read_b128 v[144:147], v250 offset:54528\n"
    "ds_read_b128 v[148:151], v250 offset:54848\n"
    "ds_read_b128 v[152:155], v252 offset:21504\n"
    "s_waitcnt lgkmcnt(9)\n"
    "v_smfmac_f32_16x16x64_bf16 v[56:59], a[232:235], a[224:231], v240\n"
    "s_waitcnt lgkmcnt(8)\n"
    "ds_read_b128 v[156:159], v252 offset:21840\n"
    "v_smfmac_f32_16x16x64_bf16 v[60:63], a[236:239], a[224:231], v240 abid:1\n"
    "ds_read_b128 a[224:227], v250 offset:53968\n"
    "ds_read_b128 a[228:231], v250 offset:54288\n"
    "ds_read_b128 a[232:235], v251 offset:22176\n"
    "v_or3_b32 v196, v196, v126, v127\n"
    "ds_read_b128 a[236:239], v251 offset:22512\n"
    "v_pk_add_f32 v[224:225], v[224:225], v[56:57]\n"
    "v_pk_add_f32 v[226:227], v[226:227], v[58:59]\n"
    "v_pk_fma_f32 v[228:229], v[56:57], v[56:57], v[228:229]\n"
    "v_pk_fma_f32 v[230:231], v[58:59], v[58:59], v[230:231]\n"
    "v_pk_add_f32 v[224:225], v[224:225], v[60:61]\n"
    "v_pk_add_f32 v[226:227], v[226:227], v[62:63]\n"
    "v_pk_fma_f32 v[228:229], v[60:61], v[60:61], v[228:229]\n"
    "v_pk_fma_f32 v[230:231], v[62:63], v[62:63], v[230:231]\n"
    "s_waitcnt lgkmcnt(9)\n"
    "s_nop 0\n"
    "v_smfmac_f32_16x16x64_bf16 v[64:67], a[248:251], a[240:247], v240\n"
    "ds_read_b128 a[248:251], v252 offset:22176\n"
    "v_smfmac_f32_16x16x64_bf16 v[68:71], a[252:255], a[240:247], v240 abid:1\n"
    "ds_read_b128 a[240:243], v250 offset:54608\n"
    "ds_read_b128 a[244:247], v250 offset:54928\n"
    "ds_read_b128 a[252:255], v252 offset:22512\n"
    "v_add_u32_e64 v124, v203, v124\n"
    "ds_write_b128 v217, v[112:115] offset:24832\n"
    "s_waitcnt lgkmcnt(10)\n"
    "v_smfmac_f32_16x16x64_bf16 v[64:67], v[152:155], v[144:151], v240\n"
    "s_waitcnt lgkmcnt(9)\n"
    "v_add_u32_e64 v126, v203, v126\n"
    "v_smfmac_f32_16x16x64_bf16 v[68:71], v[156:159], v[144:151], v240 abid:1\n"
    "ds_read_b128 v[144:147], v250 offset:54048\n"
    "ds_read_b128 v[148:151], v250 offset:54368\n"
    "ds_read_b128 v[152:155], v251 offset:22848\n"
    "v_or3_b32 v196, v196, v128, v129\n"
    "ds_read_b128 v[156:159], v251 offset:23184\n"
    "v_pk_add_f32 v[224:225], v[224:225], v[64:65]\n"
    "v_pk_add_f32 v[226:227], v[226:227], v[66:67]\n"
    "v_pk_fma_f32 v[228:229], v[64:65], v[64:65], v[228:229]\n"
    "v_pk_fma_f32 v[230:231], v[66:67], v[66:67], v[230:231]\n"
    "v_pk_add_f32 v[224:225], v[224:225], v[68:69]\n"
    "v_pk_add_f32 v[226:227], v[226:227], v[70:71]\n"
    "v_pk_fma_f32 v[228:229], v[68:69], v[68:69], v[228:229]\n"
    "v_pk_fma_f32 v[230:231], v[70:71], v[70:71], v[230:231]\n"
    "s_waitcnt lgkmcnt(8)\n"
    "v_smfmac_f32_16x16x64_bf16 v[72:75], a[232:235], a[224:231], v240\n"
    "ds_read_b128 a[232:235], v252 offset:22848\n"
    "v_smfmac_f32_16x16x64_bf16 v[76:79], a[236:239], a[224:231], v240 abid:1\n"
    "s_waitcnt lgkmcnt(7)\n"
    "ds_read_b128 a[224:227], v250 offset:54688\n"
    "v_smfmac_f32_16x16x64_bf16 v[72:75], a[248:251], a[240:247], v240\n"
    "s_waitcnt lgkmcnt(7)\n"
    "s_nop 0\n"
    "ds_read_b128 a[228:231], v250 offset:55008\n"
    "v_smfmac_f32_16x16x64_bf16 v[76:79], a[252:255], a[240:247], v240 abid:1\n"
    "ds_read_b128 a[236:239], v252 offset:23184\n"
    "ds_read_b128 a[240:243], v250 offset:54128\n"
    "ds_read_b128 a[244:247], v250 offset:54448\n"
    "v_or3_b32 v196, v196, v130, v131\n"
    "ds_read_b128 a[248:251], v251 offset:23520\n"
    "v_pk_add_f32 v[224:225], v[224:225], v[72:73]\n"
    "v_pk_add_f32 v[226:227], v[226:227], v[74:75]\n"
    "v_pk_fma_f32 v[228:229], v[72:73], v[72:73], v[228:229]\n"
    "v_pk_fma_f32 v[230:231], v[74:75], v[74:75], v[230:231]\n"
    "v_pk_add_f32 v[224:225], v[224:225], v[76:77]\n"
    "v_pk_add_f32 v[226:227], v[226:227], v[78:79]\n"
    "v_pk_fma_f32 v[228:229], v[76:77], v[76:77], v[228:229]\n"
    "v_pk_fma_f32 v[230:231], v[78:79], v[78:79], v[230:231]\n"
    "s_waitcnt lgkmcnt(8)\n"
    "v_smfmac_f32_16x16x64_bf16 v[80:83], v[152:155], v[144:151], v240\n"
    "s_waitcnt lgkmcnt(7)\n"
    "ds_read_b128 a[252:255], v251 offset:23856\n"
    "v_smfmac_f32_16x16x64_bf16 v[84:87], v[156:159], v[144:151], v240 abid:1\n"
    "ds_read_b128 v[144:147], v250 offset:54768\n"
    "ds_read_b128 v[148:151], v250 offset:55088\n"
    "ds_read_b128 v[152:155], v252 offset:23520\n"
    "v_add_u32_e64 v128, v205, v128\n"
    "ds_read_b128 v[156:159], v252 offset:23856\n"
    "ds_write_b128 v217, v[116:119] offset:25088\n"
    "s_waitcnt lgkmcnt(9)\n"
    "v_smfmac_f32_16x16x64_bf16 v[80:83], a[232:235], a[224:231], v240\n"
    "v_add_u32_e32 v130, v205, v130\n"
    "v_smfmac_f32_16x16x64_bf16 v[84:87], a[236:239], a[224:231], v240 abid:1\n"
    "v_or3_b32 v196, v196, v132, v133\n"
    "ds_write_b128 v217, v[120:123] offset:0\n"
    "s_waitcnt lgkmcnt(6)\n"
    "s_nop 0\n"
    "v_smfmac_f32_16x16x64_bf16 v[88:91], a[248:251], a[240:247], v240\n"
    "v_accvgpr_write_b32 a248, v236\n"
    "v_smfmac_f32_16x16x64_bf16 v[92:95], a[252:255], a[240:247], v240 abid:1\n"
    "v_accvgpr_write_b32 a249, v237\n"
    "v_accvgpr_write_b32 a250, v238\n"
    "v_accvgpr_write_b32 a251, v239\n"
    "ds_write_b128 v217, v[124:127] offset:256\n"
    "v_pk_add_f32 v[224:225], v[224:225], v[80:81]\n"
    "v_pk_add_f32 v[226:227], v[226:227], v[82:83]\n"
    "v_pk_fma_f32 v[228:229], v[80:81], v[80:81], v[228:229]\n"
    "v_pk_fma_f32 v[230:231], v[82:83], v[82:83], v[230:231]\n"
    "v_pk_add_f32 v[224:225], v[224:225], v[84:85]\n"
    "v_pk_add_f32 v[226:227], v[226:227], v[86:87]\n"
    "v_pk_fma_f32 v[228:229], v[84:85], v[84:85], v[228:229]\n"
    "v_pk_fma_f32 v[230:231], v[86:87], v[86:87], v[230:231]\n"
    "s_waitcnt lgkmcnt(2)\n"
    "v_smfmac_f32_16x16x64_bf16 v[88:91], v[152:155], v[144:151], v240\n"
    "s_barrier\n"
    "v_smfmac_f32_16x16x64_bf16 v[92:95], v[156:159], v[144:151], v240 abid:1\n"
    "ds_read_b64_tr_b16 a[130:131], v208 offset:24576\n"
    "ds_read_b64_tr_b16 a[134:135], v209 offset:24576\n"
    "ds_read_b64_tr_b16 a[138:139], v210 offset:24576\n"
    "v_or3_b32 v196, v196, v134, v135\n"
    "ds_read_b64_tr_b16 a[142:143], v211 offset:24576\n"
    "ds_read_b64_tr_b16 a[146:147], v212 offset:24576\n"
    "v_pk_add_f32 v[224:225], v[224:225], v[88:89]\n"
    "v_pk_add_f32 v[226:227], v[226:227], v[90:91]\n"
    "v_pk_fma_f32 v[228:229], v[88:89], v[88:89], v[228:229]\n"
    "v_pk_fma_f32 v[230:231], v[90:91], v[90:91], v[230:231]\n"
    "v_pk_add_f32 v[224:225], v[224:225], v[92:93]\n"
    "v_pk_add_f32 v[226:227], v[226:227], v[94:95]\n"
    "v_pk_fma_f32 v[228:229], v[92:93], v[92:93], v[228:229]\n"
    "v_pk_fma_f32 v[230:231], v[94:95], v[94:95], v[230:231]\n"
    "ds_write_b128 v254, v[224:227]\n"
    "v_add_u32_e64 v132, v201, v132\n"
    "v_add_u32_e32 v134, v201, v134\n"
    "s_waitcnt vmcnt(6)\n"
    "v_or3_b32 v196, v196, v136, v137\n"
    "v_or3_b32 v196, v196, v138, v139\n"
    "v_add_u32_e64 v136, v203, v136\n"
    "ds_write_b128 v254, v[228:231] offset:1024\n"
    "v_lshrrev_b32_e64 v254, 2, v242\n"
    "v_add_u32_e64 v254, s55, v254\n"
    "v_add_u32_e64 v138, v203, v138\n"
    "v_or3_b32 v196, v196, v140, v141\n"
    "v_or3_b32 v196, v196, v142, v143\n"
    "v_and_b32_e32 v196, 0x40004000, v196\n"
    "ds_read_b64_tr_b16 a[150:151], v213 offset:24576\n"
    "ds_read_b64_tr_b16 a[154:155], v214 offset:24576\n"
    "ds_read_b64_tr_b16 a[158:159], v215 offset:24576\n"
    "ds_read_b64_tr_b16 a[162:163], v208 offset:24832\n"
    "v_add_u32_e64 v140, v205, v140\n"
    "ds_read_b64_tr_b16 a[166:167], v209 offset:24832\n"
    "ds_read_b64_tr_b16 a[170:171], v210 offset:24832\n"
    "ds_read_b64_tr_b16 a[174:175], v211 offset:24832\n"
    "v_add_u32_e64 v142, v205, v142\n"
    "ds_write_b128 v217, v[128:131] offset:512\n"
    "s_waitcnt lgkmcnt(8)\n"
    "s_barrier\n"
    "ds_read_b32 v224, v254 offset:0\n"
    "ds_read_b32 v225, v254 offset:6736\n"
    "ds_read_b32 v226, v254 offset:13472\n"
    "ds_read_b32 v227, v254 offset:20208\n"
    "ds_read_b32 v228, v254 offset:1024\n"
    "ds_read_b32 v229, v254 offset:7760\n"
    "ds_read_b32 v230, v254 offset:14496\n"
    "ds_read_b32 v231, v254 offset:21232\n"
    "ds_write_b128 v217, v[132:135] offset:12288\n"
    "s_waitcnt lgkmcnt(9)\n"
    "s_barrier\n"
    "ds_write_b128 v163, a[216:219] offset:0\n"
    "s_waitcnt lgkmcnt(7)\n"
    "v_add_f32_e32 v224, v224, v225\n"
    "s_waitcnt lgkmcnt(6)\n"
    "v_add_f32_e32 v226, v226, v227\n"
    "v_add_f32_e64 v224, v224, v226\n"
    "v_mul_f32_e32 v224, 0x3c2aaaab, v224\n"
    "s_waitcnt lgkmcnt(4)\n"
    "v_add_f32_e32 v228, v228, v229\n"
    "ds_write_b128 v163, a[220:223] offset:16\n"
    "s_waitcnt lgkmcnt(2)\n"
    "v_add_f32_e32 v230, v230, v231\n"
    "v_add_f32_e64 v228, v228, v230\n"
    "v_mul_f32_e32 v228, 0x3c2aaaab, v228\n"
    "v_fma_f32 v228, -v224, v224, v228\n"
    "v_max_f32_e64 v228, 0, v228\n"
    "v_add_f32_e32 v228, 0x3727c5ac, v228\n"
    "v_rsq_f32_e64 v228, v228\n"
    "ds_read_b64_tr_b16 a[176:177], v208 offset:0\n"
    "v_mul_f32_e64 v228, s50, v228\n"
    "v_mul_f32_e64 v224, v228, -v224\n"
    "ds_write_b32 v254, v228\n"
    "ds_write_b32 v254, v224 offset:1024\n"
    "v_add_u32_e64 v254, s56, v242\n"
    "ds_read_b64_tr_b16 a[180:181], v209 offset:0\n"
    "ds_read_b64_tr_b16 a[184:185], v210 offset:0\n"
    "ds_read_b64_tr_b16 a[188:189], v211 offset:0\n"
    "ds_read_b64_tr_b16 a[192:193], v212 offset:0\n"
    "ds_read_b64_tr_b16 a[196:197], v213 offset:0\n"
    "ds_read_b64_tr_b16 a[200:201], v214 offset:0\n"
    "ds_read_b64_tr_b16 a[204:205], v215 offset:0\n"
    "ds_read_b64_tr_b16 a[208:209], v208 offset:256\n"
    "ds_read_b64_tr_b16 a[212:213], v209 offset:256\n"
    "ds_read_b64_tr_b16 a[216:217], v210 offset:256\n"
    "ds_read_b64_tr_b16 a[220:221], v211 offset:256\n"
    "ds_write_b128 v207, v[176:179] offset:32768\n"
    "s_waitcnt lgkmcnt(12)\n"
    "s_barrier\n"
    "ds_read_b128 v[224:227], v254\n"
    "ds_read_b128 v[228:231], v254 offset:1024\n"
    "ds_write_b128 v207, v[180:183] offset:36864\n"
    "ds_write_b128 v217, v[136:139] offset:12544\n"
    "v_cmp_le_u32_e32 vcc, 1, v196\n"
    "s_nop 1\n"
    "s_cmp_lg_u64 vcc, 0\n"
    "s_cselect_b32 vcc_lo, 1, 0\n"
    "s_or_b32 s66, s66, vcc_lo\n"
    "v_cmp_le_u32_e32 vcc, 981467136, v197\n"
    "s_nop 1\n"
    "s_cmp_lg_u64 vcc, 0\n"
    "s_cselect_b32 vcc_lo, 2, 0\n"
    "s_or_b32 s66, s66, vcc_lo\n"
    "v_cmp_le_u32_e32 vcc, 964689920, v198\n"
    "s_nop 1\n"
    "s_cmp_lg_u64 vcc, 0\n"
    "s_cselect_b32 vcc_lo, 4, 0\n"
    "s_or_b32 s66, s66, vcc_lo\n"
    "v_mov_b32_e32 v196, s66\n"
    "v_mov_b32_e32 v197, 162752\n"
    "s_mov_b64 exec, 1\n"
    "ds_or_b32 v197, v196\n"
    "s_mov_b64 exec, -1\n"
    "s_waitcnt lgkmcnt(1)\n"
    "s_nop 0\n"
    "v_pk_fma_f32 v[64:65], v[64:65], v[224:225], v[228:229]\n"
    "v_pk_fma_f32 v[66:67], v[66:67], v[226:227], v[230:231]\n"
    "v_pk_fma_f32 v[68:69], v[68:69], v[224:225], v[228:229]\n"
    "v_pk_fma_f32 v[70:71], v[70:71], v[226:227], v[230:231]\n"
    "v_pk_fma_f32 v[72:73], v[72:73], v[224:225], v[228:229]\n"
    "v_pk_fma_f32 v[74:75], v[74:75], v[226:227], v[230:231]\n"
    "v_pk_fma_f32 v[76:77], v[76:77], v[224:225], v[228:229]\n"
    "v_pk_fma_f32 v[78:79], v[78:79], v[226:227], v[230:231]\n"
    "v_pk_fma_f32 v[80:81], v[80:81], v[224:225], v[228:229]\n"
    "v_pk_fma_f32 v[82:83], v[82:83], v[226:227], v[230:231]\n"
    "v_pk_fma_f32 v[84:85], v[84:85], v[224:225], v[228:229]\n"
    "v_pk_fma_f32 v[86:87], v[86:87], v[226:227], v[230:231]\n"
    "v_pk_fma_f32 v[88:89], v[88:89], v[224:225], v[228:229]\n"
    "v_pk_fma_f32 v[90:91], v[90:91], v[226:227], v[230:231]\n"
    "v_pk_fma_f32 v[92:93], v[92:93], v[224:225], v[228:229]\n"
    "v_pk_fma_f32 v[94:95], v[94:95], v[226:227], v[230:231]\n"
    "v_cvt_pk_bf16_f32 v144, v64, v68\n"
    "v_cvt_pk_bf16_f32 v148, v65, v69\n"
    "v_cvt_pk_bf16_f32 v152, v66, v70\n"
    "v_cvt_pk_bf16_f32 v156, v67, v71\n"
    "v_cvt_pk_bf16_f32 v145, v72, v76\n"
    "v_cvt_pk_bf16_f32 v149, v73, v77\n"
    "v_cvt_pk_bf16_f32 v153, v74, v78\n"
    "v_cvt_pk_bf16_f32 v157, v75, v79\n"
    "v_cvt_pk_bf16_f32 v146, v80, v84\n"
    "v_cvt_pk_bf16_f32 v150, v81, v85\n"
    "v_cvt_pk_bf16_f32 v154, v82, v86\n"
    "v_cvt_pk_bf16_f32 v158, v83, v87\n"
    "v_cvt_pk_bf16_f32 v147, v88, v92\n"
    "ds_write_b128 v253, v[144:147] offset:2048\n"
    "v_cvt_pk_bf16_f32 v151, v89, v93\n"
    "v_cvt_pk_bf16_f32 v155, v90, v94\n"
    "v_cvt_pk_bf16_f32 v159, v91, v95\n"
    "v_pk_fma_f32 v[32:33], v[32:33], v[224:225], v[228:229]\n"
    "v_pk_fma_f32 v[34:35], v[34:35], v[226:227], v[230:231]\n"
    "v_pk_fma_f32 v[36:37], v[36:37], v[224:225], v[228:229]\n"
    "ds_write_b128 v253, v[148:151] offset:5120\n"
    "v_pk_fma_f32 v[38:39], v[38:39], v[226:227], v[230:231]\n"
    "v_pk_fma_f32 v[40:41], v[40:41], v[224:225], v[228:229]\n"
    "v_pk_fma_f32 v[42:43], v[42:43], v[226:227], v[230:231]\n"
    "v_pk_fma_f32 v[44:45], v[44:45], v[224:225], v[228:229]\n"
    "v_pk_fma_f32 v[46:47], v[46:47], v[226:227], v[230:231]\n"
    "v_pk_fma_f32 v[48:49], v[48:49], v[224:225], v[228:229]\n"
    "v_pk_fma_f32 v[50:51], v[50:51], v[226:227], v[230:231]\n"
    "v_pk_fma_f32 v[52:53], v[52:53], v[224:225], v[228:229]\n"
    "v_pk_fma_f32 v[54:55], v[54:55], v[226:227], v[230:231]\n"
    "v_pk_fma_f32 v[56:57], v[56:57], v[224:225], v[228:229]\n"
    "v_pk_fma_f32 v[58:59], v[58:59], v[226:227], v[230:231]\n"
    "v_pk_fma_f32 v[60:61], v[60:61], v[224:225], v[228:229]\n"
    "v_pk_fma_f32 v[62:63], v[62:63], v[226:227], v[230:231]\n"
    "v_cvt_pk_bf16_f32 v64, v32, v36\n"
    "v_cvt_pk_bf16_f32 v68, v33, v37\n"
    "v_cvt_pk_bf16_f32 v72, v34, v38\n"
    "v_cvt_pk_bf16_f32 v76, v35, v39\n"
    "v_cvt_pk_bf16_f32 v65, v40, v44\n"
    "v_cvt_pk_bf16_f32 v69, v41, v45\n"
    "v_cvt_pk_bf16_f32 v73, v42, v46\n"
    "v_cvt_pk_bf16_f32 v77, v43, v47\n"
    "v_cvt_pk_bf16_f32 v66, v48, v52\n"
    "v_cvt_pk_bf16_f32 v70, v49, v53\n"
    "v_cvt_pk_bf16_f32 v74, v50, v54\n"
    "v_cvt_pk_bf16_f32 v78, v51, v55\n"
    "v_cvt_pk_bf16_f32 v67, v56, v60\n"
    "ds_write_b128 v253, v[64:67] offset:1024\n"
    "v_cvt_pk_bf16_f32 v71, v57, v61\n"
    "v_cvt_pk_bf16_f32 v75, v58, v62\n"
    "v_cvt_pk_bf16_f32 v79, v59, v63\n"
    "v_pk_fma_f32 v[0:1], v[0:1], v[224:225], v[228:229]\n"
    "v_pk_fma_f32 v[2:3], v[2:3], v[226:227], v[230:231]\n"
    "v_pk_fma_f32 v[4:5], v[4:5], v[224:225], v[228:229]\n"
    "ds_write_b128 v253, v[68:71] offset:4096\n"
    "v_pk_fma_f32 v[6:7], v[6:7], v[226:227], v[230:231]\n"
    "v_pk_fma_f32 v[8:9], v[8:9], v[224:225], v[228:229]\n"
    "v_pk_fma_f32 v[10:11], v[10:11], v[226:227], v[230:231]\n"
    "v_pk_fma_f32 v[12:13], v[12:13], v[224:225], v[228:229]\n"
    "v_pk_fma_f32 v[14:15], v[14:15], v[226:227], v[230:231]\n"
    "v_pk_fma_f32 v[16:17], v[16:17], v[224:225], v[228:229]\n"
    "v_pk_fma_f32 v[18:19], v[18:19], v[226:227], v[230:231]\n"
    "v_pk_fma_f32 v[20:21], v[20:21], v[224:225], v[228:229]\n"
    "v_pk_fma_f32 v[22:23], v[22:23], v[226:227], v[230:231]\n"
    "v_pk_fma_f32 v[24:25], v[24:25], v[224:225], v[228:229]\n"
    "v_pk_fma_f32 v[26:27], v[26:27], v[226:227], v[230:231]\n"
    "v_pk_fma_f32 v[28:29], v[28:29], v[224:225], v[228:229]\n"
    "v_pk_fma_f32 v[30:31], v[30:31], v[226:227], v[230:231]\n"
    "v_cvt_pk_bf16_f32 v80, v0, v4\n"
    "v_cvt_pk_bf16_f32 v84, v1, v5\n"
    "v_cvt_pk_bf16_f32 v88, v2, v6\n"
    "v_cvt_pk_bf16_f32 v92, v3, v7\n"
    "v_cvt_pk_bf16_f32 v81, v8, v12\n"
    "v_cvt_pk_bf16_f32 v85, v9, v13\n"
    "v_cvt_pk_bf16_f32 v89, v10, v14\n"
    "v_cvt_pk_bf16_f32 v93, v11, v15\n"
    "v_cvt_pk_bf16_f32 v82, v16, v20\n"
    "v_cvt_pk_bf16_f32 v86, v17, v21\n"
    "v_cvt_pk_bf16_f32 v90, v18, v22\n"
    "v_cvt_pk_bf16_f32 v94, v19, v23\n"
    "v_cvt_pk_bf16_f32 v83, v24, v28\n"
    "ds_write_b128 v253, v[80:83] offset:0\n"
    "v_cvt_pk_bf16_f32 v87, v25, v29\n"
    "v_cvt_pk_bf16_f32 v91, v26, v30\n"
    "v_cvt_pk_bf16_f32 v95, v27, v31\n"
    "v_mov_b32_e64 v64, v152\n"
    "v_mov_b32_e64 v65, v153\n"
    "v_mov_b32_e64 v66, v154\n"
    "ds_write_b128 v253, v[84:87] offset:3072\n"
    "buffer_load_dwordx4 v[0:3], v246, s[40:43], s60 offen offset:0\n"
    "v_mov_b32_e64 v67, v155\n"
    "v_mov_b32_e64 v68, v156\n"
    "v_mov_b32_e64 v69, v157\n"
    "v_mov_b32_e64 v70, v158\n"
    "v_mov_b32_e64 v71, v159\n"
    "buffer_load_dwordx4 v[4:7], v246, s[40:43], s60 offen offset:192\n"
    "buffer_load_dwordx4 v[8:11], v247, s[40:43], s60 offen offset:0\n"
    "buffer_load_dwordx4 v[12:15], v247, s[40:43], s60 offen offset:192\n"
    "buffer_load_dwordx4 v[16:19], v246, s[40:43], s61 offen offset:0\n"
    "buffer_load_dwordx4 v[20:23], v246, s[40:43], s61 offen offset:192\n"
    "buffer_load_dwordx4 v[24:27], v247, s[40:43], s61 offen offset:0\n"
    "buffer_load_dwordx4 v[28:31], v247, s[40:43], s61 offen offset:192\n"
    "buffer_load_dwordx4 v[32:35], v246, s[40:43], s60 offen offset:64\n"
    "buffer_load_dwordx4 v[36:39], v246, s[40:43], s60 offen offset:256\n"
    "buffer_load_dwordx4 v[40:43], v247, s[40:43], s60 offen offset:64\n"
    "buffer_load_dwordx4 v[44:47], v247, s[40:43], s60 offen offset:256\n"
    "buffer_load_dwordx4 v[48:51], v246, s[40:43], s61 offen offset:64\n"
    "buffer_load_dwordx4 v[52:55], v246, s[40:43], s61 offen offset:256\n"
    "buffer_load_dwordx4 v[56:59], v247, s[40:43], s61 offen offset:64\n"
    "buffer_load_dwordx4 v[60:63], v247, s[40:43], s61 offen offset:256\n"
    "ds_write_b128 v217, v[140:143] offset:12800\n"
    "s_waitcnt lgkmcnt(0)\n"
    "s_barrier\n"
    "ds_read_b64_tr_b16 a[178:179], v208 offset:12288\n"
    "ds_read_b64_tr_b16 a[182:183], v209 offset:12288\n"
    "ds_read_b64_tr_b16 a[186:187], v210 offset:12288\n"
    "ds_read_b64_tr_b16 a[190:191], v211 offset:12288\n"
    "ds_read_b64_tr_b16 a[194:195], v212 offset:12288\n"
    "ds_read_b64_tr_b16 a[198:199], v213 offset:12288\n"
    "ds_read_b64_tr_b16 a[202:203], v214 offset:12288\n"
    "ds_read_b64_tr_b16 a[206:207], v215 offset:12288\n"
    "ds_read_b64_tr_b16 a[210:211], v208 offset:12544\n"
    "ds_read_b64_tr_b16 a[214:215], v209 offset:12544\n"
    "ds_read_b64_tr_b16 a[218:219], v210 offset:12544\n"
    "ds_read_b64_tr_b16 a[222:223], v211 offset:12544\n"
    "ds_write_b128 v207, v[164:167] offset:0\n"
    "ds_write_b128 v207, v[168:171] offset:4096\n"
    "ds_write_b128 v207, v[172:175] offset:8192\n"
    "ds_read_b32 v232, v197\n"
    "s_waitcnt lgkmcnt(1)\n"
    "s_barrier\n"
    "ds_read_b128 v[144:147], v242 offset:0\n"
    "ds_read_b128 v[148:151], v242 offset:1024\n"
    "ds_read_b128 v[152:155], v242 offset:2048\n"
    "ds_read_b128 v[156:159], v242 offset:3072\n"
    "ds_read_b128 v[160:163], v242 offset:4096\n"
    "ds_read_b128 v[164:167], v242 offset:5120\n"
    "ds_read_b128 a[240:243], v244 offset:32768\n"
    "ds_write_b128 v207, v[184:187] offset:12288\n"
    "ds_write_b128 v207, v[188:191] offset:16384\n"
    "s_waitcnt lgkmcnt(8)\n"
    "v_mfma_f32_16x16x32_bf16 v[168:171], a[128:131], v[144:147], 0\n"
    "s_waitcnt lgkmcnt(7)\n"
    "v_mfma_f32_16x16x32_bf16 v[168:171], a[176:179], v[148:151], v[168:171]\n"
    "s_waitcnt lgkmcnt(6)\n"
    "v_mfma_f32_16x16x32_bf16 v[168:171], a[96:99], v[152:155], v[168:171]\n"
    "ds_write_b128 v207, v[192:195] offset:20480\n"
    "v_mfma_f32_16x16x32_bf16 v[172:175], a[132:135], v[144:147], 0\n"
    "s_waitcnt lgkmcnt(5)\n"
    "v_mfma_f32_16x16x32_bf16 v[172:175], a[180:183], v[148:151], v[172:175]\n"
    "s_waitcnt lgkmcnt(3)\n"
    "v_mfma_f32_16x16x32_bf16 v[172:175], a[240:243], v[152:155], v[172:175]\n"
    "v_mfma_f32_16x16x32_bf16 v[176:179], a[128:131], v[156:159], 0\n"
    "v_mfma_f32_16x16x32_bf16 v[176:179], a[176:179], v[160:163], v[176:179]\n"
    "v_mfma_f32_16x16x32_bf16 v[176:179], a[96:99], v[164:167], v[176:179]\n"
    "v_mfma_f32_16x16x32_bf16 v[180:183], a[132:135], v[156:159], 0\n"
    "v_mfma_f32_16x16x32_bf16 v[180:183], a[180:183], v[160:163], v[180:183]\n"
    "s_waitcnt lgkmcnt(0)\n"
    "s_barrier\n"
    "v_mfma_f32_16x16x32_bf16 v[180:183], a[240:243], v[164:167], v[180:183]\n"
    "v_readfirstlane_b32 s66, v232\n"
    "s_nop 1\n"
    "s_cmp_lg_u32 s66, 6\n"
    "s_cbranch_scc1 98f\n"
    ".p2align 6\n"
    "1:\n"
    "v_mfma_f32_16x16x32_f16 v[140:143], a[244:247], v[188:191], v[140:143]\n"
    "ds_read_b128 a[240:243], v244 offset:36864\n"
    "ds_read_b128 v[236:239], v244 offset:20480\n"
    "ds_read_b128 a[244:247], v244 offset:22528\n"
    "v_cvt_pk_f16_f32 v184, v168, v169\n"
    "v_lshlrev_b32_sdwa v185, 1, v170 dst_sel:DWORD dst_unused:UNUSED_PAD src0_sel:DWORD src1_sel:WORD_1\n"
    "v_lshlrev_b32_sdwa v201, 1, v171 dst_sel:DWORD dst_unused:UNUSED_PAD src0_sel:DWORD src1_sel:WORD_1\n"
    "v_mfma_f32_16x16x32_bf16 v[168:171], a[136:139], v[144:147], 0\n"
    "ds_read_u16 v185, v185\n"
    "ds_read_u16_d16_hi v201, v201\n"
    "v_mfma_f32_16x16x32_bf16 v[168:171], a[184:187], v[148:151], v[168:171]\n"
    "v_lshlrev_b32_sdwa v186, 1, v172 dst_sel:DWORD dst_unused:UNUSED_PAD src0_sel:DWORD src1_sel:WORD_1\n"
    "v_lshlrev_b32_sdwa v202, 1, v173 dst_sel:DWORD dst_unused:UNUSED_PAD src0_sel:DWORD src1_sel:WORD_1\n"
    "v_mfma_f32_16x16x32_bf16 v[168:171], a[100:103], v[152:155], v[168:171]\n"
    "ds_read_u16 v186, v186\n"
    "ds_read_u16_d16_hi v202, v202\n"
    "s_waitcnt vmcnt(10)\n"
    "s_nop 0\n"
    "v_smfmac_f32_32x32x32_bf16 v[128:143], a[248:251], a[224:231], v241\n"
    "v_lshlrev_b32_sdwa v187, 1, v174 dst_sel:DWORD dst_unused:UNUSED_PAD src0_sel:DWORD src1_sel:WORD_1\n"
    "v_lshlrev_b32_sdwa v203, 1, v175 dst_sel:DWORD dst_unused:UNUSED_PAD src0_sel:DWORD src1_sel:WORD_1\n"
    "v_cvt_pk_f16_f32 v188, v176, v177\n"
    "v_lshlrev_b32_sdwa v189, 1, v178 dst_sel:DWORD dst_unused:UNUSED_PAD src0_sel:DWORD src1_sel:WORD_1\n"
    "v_lshlrev_b32_sdwa v205, 1, v179 dst_sel:DWORD dst_unused:UNUSED_PAD src0_sel:DWORD src1_sel:WORD_1\n"
    "v_mfma_f32_16x16x32_bf16 v[172:175], a[140:143], v[144:147], 0\n"
    "ds_read_u16 v187, v187\n"
    "v_lshlrev_b32_sdwa v190, 1, v180 dst_sel:DWORD dst_unused:UNUSED_PAD src0_sel:DWORD src1_sel:WORD_1\n"
    "v_mfma_f32_16x16x32_bf16 v[172:175], a[188:191], v[148:151], v[172:175]\n"
    "s_waitcnt lgkmcnt(7)\n"
    "s_nop 0\n"
    "ds_read_u16_d16_hi v203, v203\n"
    "v_lshlrev_b32_sdwa v206, 1, v181 dst_sel:DWORD dst_unused:UNUSED_PAD src0_sel:DWORD src1_sel:WORD_1\n"
    "v_mfma_f32_16x16x32_bf16 v[172:175], a[240:243], v[152:155], v[172:175]\n"
    "v_lshlrev_b32_sdwa v191, 1, v182 dst_sel:DWORD dst_unused:UNUSED_PAD src0_sel:DWORD src1_sel:WORD_1\n"
    "v_lshlrev_b32_sdwa v207, 1, v183 dst_sel:DWORD dst_unused:UNUSED_PAD src0_sel:DWORD src1_sel:WORD_1\n"
    "v_mfma_f32_16x16x32_bf16 v[176:179], a[136:139], v[156:159], 0\n"
    "ds_read_u16 v189, v189\n"
    "v_rcp_f16_sdwa v232, v234 dst_sel:WORD_1 dst_unused:UNUSED_PRESERVE src0_sel:WORD_1\n"
    "v_mfma_f32_16x16x32_bf16 v[176:179], a[184:187], v[160:163], v[176:179]\n"
    "ds_read_u16_d16_hi v205, v205\n"
    "v_rcp_f16_sdwa v233, v235 dst_sel:WORD_1 dst_unused:UNUSED_PRESERVE src0_sel:WORD_1\n"
    "v_mfma_f32_16x16x32_bf16 v[176:179], a[100:103], v[164:167], v[176:179]\n"
    "v_pk_mul_f16 v192, v192, v232\n"
    "v_pk_mul_f16 v196, v196, v233\n"
    "v_mfma_f32_16x16x32_bf16 v[180:183], a[140:143], v[156:159], 0\n"
    "s_waitcnt lgkmcnt(7)\n"
    "ds_read_u16 v190, v190\n"
    "buffer_load_dwordx4 a[232:235], v245, s[40:43], s57 offen offset:64\n"
    "v_mfma_f32_16x16x32_bf16 v[180:183], a[188:191], v[160:163], v[180:183]\n"
    "s_waitcnt lgkmcnt(7)\n"
    "ds_read_u16_d16_hi v206, v206\n"
    "v_or_b32_e64 v185, v185, v201\n"
    "v_mfma_f32_16x16x32_bf16 v[180:183], a[240:243], v[164:167], v[180:183]\n"
    "v_pk_fma_f16 v232, v184, v184, s48\n"
    "v_pk_fma_f16 v233, v188, v188, s48\n"
    "v_mfma_f32_16x16x32_f16 v[96:99], a[80:83], v[192:195], v[96:99]\n"
    "ds_read_u16 v191, v191\n"
    "v_pk_mul_f16 v232, v184, v232\n"
    "v_mfma_f32_16x16x32_f16 v[100:103], a[84:87], v[192:195], v[100:103]\n"
    "v_pk_mul_f16 v233, v188, v233\n"
    "v_lshlrev_b32_sdwa v201, 1, v171 dst_sel:DWORD dst_unused:UNUSED_PAD src0_sel:DWORD src1_sel:WORD_1\n"
    "v_mfma_f32_16x16x32_f16 v[112:115], a[88:91], v[192:195], v[112:115]\n"
    "s_waitcnt lgkmcnt(7)\n"
    "ds_read_u16_d16_hi v207, v207\n"
    "v_or_b32_e32 v186, v186, v202\n"
    "v_mfma_f32_16x16x32_f16 v[116:119], a[92:95], v[192:195], v[116:119]\n"
    "v_lshlrev_b32_sdwa v202, 1, v173 dst_sel:DWORD dst_unused:UNUSED_PAD src0_sel:DWORD src1_sel:WORD_1\n"
    "v_exp_f16_e64 v234, v232\n"
    "v_mfma_f32_16x16x32_f16 v[128:131], v[236:239], v[192:195], v[128:131]\n"
    "ds_read_u16_d16_hi v201, v201\n"
    "v_exp_f16_e64 v235, v233\n"
    "v_mfma_f32_16x16x32_f16 v[132:135], a[244:247], v[192:195], v[132:135]\n"
    "s_waitcnt lgkmcnt(7)\n"
    "ds_read_u16_d16_hi v202, v202\n"
    "v_or_b32_e32 v187, v187, v203\n"
    "v_mfma_f32_16x16x32_f16 v[104:107], a[80:83], v[196:199], v[104:107]\n"
    "v_cvt_pk_f16_f32 v192, v168, v169\n"
    "v_lshlrev_b32_sdwa v193, 1, v170 dst_sel:DWORD dst_unused:UNUSED_PAD src0_sel:DWORD src1_sel:WORD_1\n"
    "v_mfma_f32_16x16x32_f16 v[108:111], a[84:87], v[196:199], v[108:111]\n"
    "ds_read_u16 v193, v193\n"
    "v_lshlrev_b32_sdwa v194, 1, v172 dst_sel:DWORD dst_unused:UNUSED_PAD src0_sel:DWORD src1_sel:WORD_1\n"
    "v_mfma_f32_16x16x32_f16 v[120:123], a[88:91], v[196:199], v[120:123]\n"
    "s_waitcnt lgkmcnt(7)\n"
    "ds_read_u16 v194, v194\n"
    "v_or_b32_e32 v189, v189, v205\n"
    "v_mfma_f32_16x16x32_f16 v[124:127], a[92:95], v[196:199], v[124:127]\n"
    "v_lshlrev_b32_sdwa v195, 1, v174 dst_sel:DWORD dst_unused:UNUSED_PAD src0_sel:DWORD src1_sel:WORD_1\n"
    "v_lshlrev_b32_sdwa v203, 1, v175 dst_sel:DWORD dst_unused:UNUSED_PAD src0_sel:DWORD src1_sel:WORD_1\n"
    "v_mfma_f32_16x16x32_f16 v[136:139], v[236:239], v[196:199], v[136:139]\n"
    "ds_read_b128 v[236:239], v244 offset:0\n"
    "buffer_load_dwordx4 a[236:239], v245, s[40:43], s100 offen offset:64\n"
    "v_mfma_f32_16x16x32_f16 v[140:143], a[244:247], v[196:199], v[140:143]\n"
    "ds_read_u16 v195, v195\n"
    "v_cvt_pk_f16_f32 v196, v176, v177\n"
    "v_mfma_f32_16x16x32_bf16 v[168:171], a[144:147], v[144:147], 0\n"
    "s_waitcnt lgkmcnt(8)\n"
    "v_or_b32_e32 v190, v190, v206\n"
    "v_lshlrev_b32_sdwa v197, 1, v178 dst_sel:DWORD dst_unused:UNUSED_PAD src0_sel:DWORD src1_sel:WORD_1\n"
    "v_mfma_f32_16x16x32_bf16 v[168:171], a[192:195], v[148:151], v[168:171]\n"
    "ds_read_u16_d16_hi v203, v203\n"
    "v_lshlrev_b32_sdwa v205, 1, v179 dst_sel:DWORD dst_unused:UNUSED_PAD src0_sel:DWORD src1_sel:WORD_1\n"
    "v_mfma_f32_16x16x32_bf16 v[168:171], a[104:107], v[152:155], v[168:171]\n"
    "v_lshlrev_b32_sdwa v198, 1, v180 dst_sel:DWORD dst_unused:UNUSED_PAD src0_sel:DWORD src1_sel:WORD_1\n"
    "v_lshlrev_b32_sdwa v206, 1, v181 dst_sel:DWORD dst_unused:UNUSED_PAD src0_sel:DWORD src1_sel:WORD_1\n"
    "v_mfma_f32_16x16x32_bf16 v[172:175], a[148:151], v[144:147], 0\n"
    "s_waitcnt lgkmcnt(7)\n"
    "s_nop 0\n"
    "ds_read_b128 a[244:247], v244 offset:2048\n"
    "ds_read_u16 v197, v197\n"
    "v_mfma_f32_16x16x32_bf16 v[172:175], a[196:199], v[148:151], v[172:175]\n"
    "v_lshlrev_b32_sdwa v199, 1, v182 dst_sel:DWORD dst_unused:UNUSED_PAD src0_sel:DWORD src1_sel:WORD_1\n"
    "v_exp_f16_sdwa v234, v232 dst_sel:WORD_1 dst_unused:UNUSED_PRESERVE src0_sel:WORD_1\n"
    "v_mfma_f32_16x16x32_bf16 v[172:175], a[108:111], v[152:155], v[172:175]\n"
    "ds_read_u16_d16_hi v205, v205\n"
    "v_or_b32_e64 v191, v191, v207\n"
    "v_mfma_f32_16x16x32_bf16 v[176:179], a[144:147], v[156:159], 0\n"
    "v_lshlrev_b32_sdwa v207, 1, v183 dst_sel:DWORD dst_unused:UNUSED_PAD src0_sel:DWORD src1_sel:WORD_1\n"
    "v_exp_f16_sdwa v235, v233 dst_sel:WORD_1 dst_unused:UNUSED_PRESERVE src0_sel:WORD_1\n"
    "v_mfma_f32_16x16x32_bf16 v[176:179], a[192:195], v[160:163], v[176:179]\n"
    "ds_read_u16 v198, v198\n"
    "v_pk_fma_f16 v234, v234, s49, s49\n"
    "v_mfma_f32_16x16x32_bf16 v[176:179], a[104:107], v[164:167], v[176:179]\n"
    "s_waitcnt lgkmcnt(8)\n"
    "s_nop 0\n"
    "ds_read_u16_d16_hi v206, v206\n"
    "v_pk_fma_f16 v235, v235, s49, s49\n"
    "v_mfma_f32_16x16x32_bf16 v[180:183], a[148:151], v[156:159], 0\n"
    "v_cvt_pk_bf16_f32 v96, v96, v97\n"
    "v_cvt_pk_bf16_f32 v97, v98, v99\n"
    "v_mfma_f32_16x16x32_bf16 v[180:183], a[196:199], v[160:163], v[180:183]\n"
    "s_waitcnt lgkmcnt(7)\n"
    "ds_read_u16 v199, v199\n"
    "v_rcp_f16_e32 v232, v234\n"
    "v_mfma_f32_16x16x32_bf16 v[180:183], a[108:111], v[164:167], v[180:183]\n"
    "v_cvt_pk_bf16_f32 v98, v100, v101\n"
    "v_cvt_pk_bf16_f32 v99, v102, v103\n"
    "ds_write_b128 v242, v[96:99] offset:24640\n"
    "buffer_load_dwordx4 a[224:227], v245, s[40:43], s57 offen offset:0\n"
    "v_cvt_pk_bf16_f32 v112, v112, v113\n"
    "v_rcp_f16_e64 v233, v235\n"
    "v_cvt_pk_bf16_f32 v113, v114, v115\n"
    "v_cvt_pk_bf16_f32 v114, v116, v117\n"
    "v_cvt_pk_bf16_f32 v115, v118, v119\n"
    "ds_write_b128 v242, v[112:115] offset:26688\n"
    "v_rcp_f16_sdwa v232, v234 dst_sel:WORD_1 dst_unused:UNUSED_PRESERVE src0_sel:WORD_1\n"
    "v_cvt_pk_bf16_f32 v128, v128, v129\n"
    "v_cvt_pk_bf16_f32 v129, v130, v131\n"
    "v_cvt_pk_bf16_f32 v130, v132, v133\n"
    "v_or_b32_e64 v193, v193, v201\n"
    "v_cvt_pk_bf16_f32 v131, v134, v135\n"
    "ds_write_b128 v242, v[128:131] offset:28736\n"
    "v_rcp_f16_sdwa v233, v235 dst_sel:WORD_1 dst_unused:UNUSED_PRESERVE src0_sel:WORD_1\n"
    "v_cvt_pk_bf16_f32 v104, v104, v105\n"
    "v_cvt_pk_bf16_f32 v105, v106, v107\n"
    "v_cvt_pk_bf16_f32 v106, v108, v109\n"
    "v_pk_mul_f16 v184, v184, v232\n"
    "v_cvt_pk_bf16_f32 v107, v110, v111\n"
    "ds_write_b128 v242, v[104:107] offset:25664\n"
    "v_mfma_f32_16x16x32_f16 v[96:99], a[0:3], v[184:187], 0\n"
    "s_waitcnt lgkmcnt(3)\n"
    "v_or_b32_e32 v194, v194, v202\n"
    "v_cvt_pk_bf16_f32 v120, v120, v121\n"
    "v_mfma_f32_16x16x32_f16 v[100:103], a[4:7], v[184:187], 0\n"
    "ds_read_u16_d16_hi v207, v207\n"
    "buffer_load_dwordx4 a[228:231], v245, s[40:43], s100 offen offset:0\n"
    "v_mfma_f32_16x16x32_f16 v[112:115], a[8:11], v[184:187], 0\n"
    "v_cvt_pk_bf16_f32 v121, v122, v123\n"
    "v_pk_mul_f16 v188, v188, v233\n"
    "v_mfma_f32_16x16x32_f16 v[116:119], a[12:15], v[184:187], 0\n"
    "v_cvt_pk_bf16_f32 v122, v124, v125\n"
    "v_cvt_pk_bf16_f32 v123, v126, v127\n"
    "v_mfma_f32_16x16x32_f16 v[128:131], v[236:239], v[184:187], 0\n"
    "ds_write_b128 v242, v[120:123] offset:27712\n"
    "v_mfma_f32_16x16x32_f16 v[132:135], a[244:247], v[184:187], 0\n"
    "v_pk_fma_f16 v232, v192, v192, s48\n"
    "v_cvt_pk_bf16_f32 v136, v136, v137\n"
    "v_mfma_f32_16x16x32_f16 v[104:107], a[0:3], v[188:191], 0\n"
    "v_cvt_pk_bf16_f32 v137, v138, v139\n"
    "v_cvt_pk_bf16_f32 v138, v140, v141\n"
    "v_mfma_f32_16x16x32_f16 v[108:111], a[4:7], v[188:191], 0\n"
    "v_or_b32_e64 v195, v195, v203\n"
    "v_cvt_pk_bf16_f32 v139, v142, v143\n"
    "v_mfma_f32_16x16x32_f16 v[120:123], a[8:11], v[188:191], 0\n"
    "ds_write_b128 v242, v[136:139] offset:29760\n"
    "v_mfma_f32_16x16x32_f16 v[124:127], a[12:15], v[188:191], 0\n"
    "v_pk_fma_f16 v233, v196, v196, s48\n"
    "v_pk_mul_f16 v232, v192, v232\n"
    "v_mfma_f32_16x16x32_f16 v[136:139], v[236:239], v[188:191], 0\n"
    "s_waitcnt lgkmcnt(2)\n"
    "v_or_b32_e32 v197, v197, v205\n"
    "v_pk_mul_f16 v233, v196, v233\n"
    "v_mfma_f32_16x16x32_f16 v[140:143], a[244:247], v[188:191], 0\n"
    "ds_read_b128 a[252:255], v249 offset:0\n"
    "ds_read_b128 v[236:239], v244 offset:4096\n"
    "v_cvt_pk_f16_f32 v184, v168, v169\n"
    "v_lshlrev_b32_sdwa v185, 1, v170 dst_sel:DWORD dst_unused:UNUSED_PAD src0_sel:DWORD src1_sel:WORD_1\n"
    "v_lshlrev_b32_sdwa v201, 1, v171 dst_sel:DWORD dst_unused:UNUSED_PAD src0_sel:DWORD src1_sel:WORD_1\n"
    "v_mfma_f32_16x16x32_bf16 v[168:171], a[152:155], v[144:147], 0\n"
    "ds_read_u16 v185, v185\n"
    "ds_read_u16_d16_hi v201, v201\n"
    "v_mfma_f32_16x16x32_bf16 v[168:171], a[200:203], v[148:151], v[168:171]\n"
    "v_lshlrev_b32_sdwa v186, 1, v172 dst_sel:DWORD dst_unused:UNUSED_PAD src0_sel:DWORD src1_sel:WORD_1\n"
    "v_lshlrev_b32_sdwa v202, 1, v173 dst_sel:DWORD dst_unused:UNUSED_PAD src0_sel:DWORD src1_sel:WORD_1\n"
    "v_lshlrev_b32_sdwa v187, 1, v174 dst_sel:DWORD dst_unused:UNUSED_PAD src0_sel:DWORD src1_sel:WORD_1\n"
    "v_lshlrev_b32_sdwa v203, 1, v175 dst_sel:DWORD dst_unused:UNUSED_PAD src0_sel:DWORD src1_sel:WORD_1\n"
    "v_exp_f16_e64 v234, v232\n"
    "v_mfma_f32_16x16x32_bf16 v[168:171], a[112:115], v[152:155], v[168:171]\n"
    "ds_read_u16 v186, v186\n"
    "ds_read_u16_d16_hi v202, v202\n"
    "ds_write2_b32 v248, v0, v4 offset0:0 offset1:1\n"
    "v_mfma_f32_16x16x32_bf16 v[172:175], a[156:159], v[144:147], 0\n"
    "v_or_b32_e64 v198, v198, v206\n"
    "v_cvt_pk_f16_f32 v188, v176, v177\n"
    "v_lshlrev_b32_sdwa v189, 1, v178 dst_sel:DWORD dst_unused:UNUSED_PAD src0_sel:DWORD src1_sel:WORD_1\n"
    "v_exp_f16_e64 v235, v233\n"
    "v_mfma_f32_16x16x32_bf16 v[172:175], a[204:207], v[148:151], v[172:175]\n"
    "s_waitcnt lgkmcnt(6)\n"
    "s_nop 0\n"
    "ds_read_u16 v187, v187\n"
    "buffer_store_dwordx4 a[252:255], v255, s[68:71], s59 offen offset:0 sc1\n"
    "ds_read_u16_d16_hi v203, v203\n"
    "v_lshlrev_b32_sdwa v205, 1, v179 dst_sel:DWORD dst_unused:UNUSED_PAD src0_sel:DWORD src1_sel:WORD_1\n"
    "v_lshlrev_b32_sdwa v190, 1, v180 dst_sel:DWORD dst_unused:UNUSED_PAD src0_sel:DWORD src1_sel:WORD_1\n"
    "v_lshlrev_b32_sdwa v206, 1, v181 dst_sel:DWORD dst_unused:UNUSED_PAD src0_sel:DWORD src1_sel:WORD_1\n"
    "v_lshlrev_b32_sdwa v191, 1, v182 dst_sel:DWORD dst_unused:UNUSED_PAD src0_sel:DWORD src1_sel:WORD_1\n"
    "v_exp_f16_sdwa v234, v232 dst_sel:WORD_1 dst_unused:UNUSED_PRESERVE src0_sel:WORD_1\n"
    "v_mfma_f32_16x16x32_bf16 v[172:175], a[116:119], v[152:155], v[172:175]\n"
    "ds_read_b128 a[252:255], v249 offset:2048\n"
    "ds_read_b128 a[244:247], v244 offset:6144\n"
    "ds_write2_b32 v248, v1, v5 offset0:20 offset1:21\n"
    "v_mfma_f32_16x16x32_bf16 v[176:179], a[152:155], v[156:159], 0\n"
    "s_waitcnt lgkmcnt(8)\n"
    "v_or_b32_e32 v199, v199, v207\n"
    "v_lshlrev_b32_sdwa v207, 1, v183 dst_sel:DWORD dst_unused:UNUSED_PAD src0_sel:DWORD src1_sel:WORD_1\n"
    "v_exp_f16_sdwa v235, v233 dst_sel:WORD_1 dst_unused:UNUSED_PRESERVE src0_sel:WORD_1\n"
    "v_mfma_f32_16x16x32_bf16 v[176:179], a[200:203], v[160:163], v[176:179]\n"
    "ds_read_u16 v189, v189\n"
    "v_pk_fma_f16 v234, v234, s49, s49\n"
    "v_mfma_f32_16x16x32_bf16 v[176:179], a[112:115], v[164:167], v[176:179]\n"
    "ds_read_u16_d16_hi v205, v205\n"
    "v_pk_fma_f16 v235, v235, s49, s49\n"
    "v_mfma_f32_16x16x32_bf16 v[180:183], a[156:159], v[156:159], 0\n"
    "s_waitcnt lgkmcnt(7)\n"
    "v_rcp_f16_e32 v232, v234\n"
    "v_mfma_f32_16x16x32_bf16 v[180:183], a[204:207], v[160:163], v[180:183]\n"
    "ds_read_u16 v190, v190\n"
    "v_rcp_f16_e64 v233, v235\n"
    "v_mfma_f32_16x16x32_bf16 v[180:183], a[116:119], v[164:167], v[180:183]\n"
    "v_rcp_f16_sdwa v232, v234 dst_sel:WORD_1 dst_unused:UNUSED_PRESERVE src0_sel:WORD_1\n"
    "v_or_b32_e64 v185, v185, v201\n"
    "v_rcp_f16_sdwa v233, v235 dst_sel:WORD_1 dst_unused:UNUSED_PRESERVE src0_sel:WORD_1\n"
    "v_pk_mul_f16 v192, v192, v232\n"
    "v_or_b32_e64 v186, v186, v202\n"
    "v_pk_mul_f16 v196, v196, v233\n"
    "v_mfma_f32_16x16x32_f16 v[96:99], a[16:19], v[192:195], v[96:99]\n"
    "ds_read_u16_d16_hi v206, v206\n"
    "ds_read_u16 v191, v191\n"
    "s_waitcnt lgkmcnt(7)\n"
    "buffer_store_dwordx4 a[252:255], v255, s[68:71], s59 offen offset:64 sc1\n"
    "v_mfma_f32_16x16x32_f16 v[100:103], a[20:23], v[192:195], v[100:103]\n"
    "s_waitcnt lgkmcnt(6)\n"
    "ds_read_u16_d16_hi v207, v207\n"
    "v_pk_fma_f16 v232, v184, v184, s48\n"
    "v_mfma_f32_16x16x32_f16 v[112:115], a[24:27], v[192:195], v[112:115]\n"
    "ds_read_b128 a[252:255], v249 offset:4096\n"
    "v_or_b32_e64 v187, v187, v203\n"
    "v_mfma_f32_16x16x32_f16 v[116:119], a[28:31], v[192:195], v[116:119]\n"
    "ds_read_b128 a[240:243], v244 offset:34816\n"
    "v_pk_fma_f16 v233, v188, v188, s48\n"
    "v_mfma_f32_16x16x32_f16 v[128:131], v[236:239], v[192:195], v[128:131]\n"
    "ds_write2_b32 v248, v2, v6 offset0:40 offset1:41\n"
    "v_mfma_f32_16x16x32_f16 v[132:135], a[244:247], v[192:195], v[132:135]\n"
    "v_pk_mul_f16 v232, v184, v232\n"
    "v_pk_mul_f16 v233, v188, v233\n"
    "v_mfma_f32_16x16x32_f16 v[104:107], a[16:19], v[196:199], v[104:107]\n"
    "ds_write2_b32 v248, v3, v7 offset0:60 offset1:61\n"
    "v_mfma_f32_16x16x32_f16 v[108:111], a[20:23], v[196:199], v[108:111]\n"
    "s_waitcnt lgkmcnt(8)\n"
    "v_xor_b32_e32 v248, 8, v248\n"
    "v_or_b32_e64 v189, v189, v205\n"
    "v_mfma_f32_16x16x32_f16 v[120:123], a[24:27], v[196:199], v[120:123]\n"
    "ds_write2_b32 v248, v8, v12 offset0:0 offset1:1\n"
    "v_mfma_f32_16x16x32_f16 v[124:127], a[28:31], v[196:199], v[124:127]\n"
    "v_cvt_pk_f16_f32 v192, v168, v169\n"
    "v_lshlrev_b32_sdwa v193, 1, v170 dst_sel:DWORD dst_unused:UNUSED_PAD src0_sel:DWORD src1_sel:WORD_1\n"
    "v_mfma_f32_16x16x32_f16 v[136:139], v[236:239], v[196:199], v[136:139]\n"
    "s_waitcnt lgkmcnt(7)\n"
    "s_nop 0\n"
    "ds_read_u16 v193, v193\n"
    "v_lshlrev_b32_sdwa v201, 1, v171 dst_sel:DWORD dst_unused:UNUSED_PAD src0_sel:DWORD src1_sel:WORD_1\n"
    "v_mfma_f32_16x16x32_f16 v[140:143], a[244:247], v[196:199], v[140:143]\n"
    "ds_read_u16_d16_hi v201, v201\n"
    "v_lshlrev_b32_sdwa v194, 1, v172 dst_sel:DWORD dst_unused:UNUSED_PAD src0_sel:DWORD src1_sel:WORD_1\n"
    "v_mfma_f32_16x16x32_bf16 v[168:171], a[160:163], v[144:147], 0\n"
    "v_lshlrev_b32_sdwa v202, 1, v173 dst_sel:DWORD dst_unused:UNUSED_PAD src0_sel:DWORD src1_sel:WORD_1\n"
    "v_lshlrev_b32_sdwa v195, 1, v174 dst_sel:DWORD dst_unused:UNUSED_PAD src0_sel:DWORD src1_sel:WORD_1\n"
    "v_mfma_f32_16x16x32_bf16 v[168:171], a[208:211], v[148:151], v[168:171]\n"
    "s_waitcnt lgkmcnt(6)\n"
    "ds_read_u16 v194, v194\n"
    "buffer_store_dwordx4 a[252:255], v255, s[68:71], s59 offen offset:128 sc1\n"
    "ds_write2_b32 v248, v9, v13 offset0:20 offset1:21\n"
    "v_lshlrev_b32_sdwa v203, 1, v175 dst_sel:DWORD dst_unused:UNUSED_PAD src0_sel:DWORD src1_sel:WORD_1\n"
    "v_exp_f16_e32 v234, v232\n"
    "v_mfma_f32_16x16x32_bf16 v[168:171], a[120:123], v[152:155], v[168:171]\n"
    "ds_read_u16_d16_hi v202, v202\n"
    "v_or_b32_e64 v190, v190, v206\n"
    "v_mfma_f32_16x16x32_bf16 v[172:175], a[164:167], v[144:147], 0\n"
    "v_cvt_pk_f16_f32 v196, v176, v177\n"
    "v_lshlrev_b32_sdwa v197, 1, v178 dst_sel:DWORD dst_unused:UNUSED_PAD src0_sel:DWORD src1_sel:WORD_1\n"
    "v_exp_f16_e64 v235, v233\n"
    "v_mfma_f32_16x16x32_bf16 v[172:175], a[212:215], v[148:151], v[172:175]\n"
    "ds_read_b128 a[252:255], v249 offset:1024\n"
    "ds_read_b128 v[236:239], v244 offset:8192\n"
    "v_lshlrev_b32_sdwa v205, 1, v179 dst_sel:DWORD dst_unused:UNUSED_PAD src0_sel:DWORD src1_sel:WORD_1\n"
    "v_lshlrev_b32_sdwa v198, 1, v180 dst_sel:DWORD dst_unused:UNUSED_PAD src0_sel:DWORD src1_sel:WORD_1\n"
    "v_lshlrev_b32_sdwa v206, 1, v181 dst_sel:DWORD dst_unused:UNUSED_PAD src0_sel:DWORD src1_sel:WORD_1\n"
    "v_lshlrev_b32_sdwa v199, 1, v182 dst_sel:DWORD dst_unused:UNUSED_PAD src0_sel:DWORD src1_sel:WORD_1\n"
    "v_exp_f16_sdwa v234, v232 dst_sel:WORD_1 dst_unused:UNUSED_PRESERVE src0_sel:WORD_1\n"
    "s_waitcnt lgkmcnt(7)\n"
    "v_mfma_f32_16x16x32_bf16 v[172:175], a[240:243], v[152:155], v[172:175]\n"
    "ds_read_u16 v195, v195\n"
    "ds_read_u16_d16_hi v203, v203\n"
    "v_mfma_f32_16x16x32_bf16 v[176:179], a[160:163], v[156:159], 0\n"
    "v_or_b32_e32 v191, v191, v207\n"
    "v_lshlrev_b32_sdwa v207, 1, v183 dst_sel:DWORD dst_unused:UNUSED_PAD src0_sel:DWORD src1_sel:WORD_1\n"
    "v_exp_f16_sdwa v235, v233 dst_sel:WORD_1 dst_unused:UNUSED_PRESERVE src0_sel:WORD_1\n"
    "v_mfma_f32_16x16x32_bf16 v[176:179], a[208:211], v[160:163], v[176:179]\n"
    "ds_read_b128 a[244:247], v244 offset:10240\n"
    "ds_read_u16 v197, v197\n"
    "v_mfma_f32_16x16x32_bf16 v[176:179], a[120:123], v[164:167], v[176:179]\n"
    "s_waitcnt lgkmcnt(9)\n"
    "v_pk_fma_f16 v234, v234, s49, s49\n"
    "v_pk_fma_f16 v235, v235, s49, s49\n"
    "v_mfma_f32_16x16x32_bf16 v[180:183], a[164:167], v[156:159], 0\n"
    "ds_read_u16_d16_hi v205, v205\n"
    "v_rcp_f16_e32 v232, v234\n"
    "v_mfma_f32_16x16x32_bf16 v[180:183], a[212:215], v[160:163], v[180:183]\n"
    "v_rcp_f16_e64 v233, v235\n"
    "v_mfma_f32_16x16x32_bf16 v[180:183], a[240:243], v[164:167], v[180:183]\n"
    "ds_read_u16 v198, v198\n"
    "v_rcp_f16_sdwa v232, v234 dst_sel:WORD_1 dst_unused:UNUSED_PRESERVE src0_sel:WORD_1\n"
    "v_or_b32_e64 v193, v193, v201\n"
    "ds_write2_b32 v248, v10, v14 offset0:40 offset1:41\n"
    "s_waitcnt lgkmcnt(7)\n"
    "buffer_store_dwordx4 a[252:255], v255, s[68:71], s39 offen offset:0 sc1\n"
    "ds_write2_b32 v248, v11, v15 offset0:60 offset1:61\n"
    "v_add_u32_e32 v248, 4080, v248\n"
    "v_rcp_f16_sdwa v233, v235 dst_sel:WORD_1 dst_unused:UNUSED_PRESERVE src0_sel:WORD_1\n"
    "v_pk_mul_f16 v184, v184, v232\n"
    "s_waitcnt vmcnt(16)\n"
    "s_mov_b64 exec, s[52:53]\n"
    "ds_write2_b32 v248, v24, v28 offset0:0 offset1:1\n"
    "s_mov_b64 exec, -1\n"
    "v_mfma_f32_16x16x32_f16 v[96:99], a[32:35], v[184:187], v[96:99]\n"
    "s_waitcnt lgkmcnt(6)\n"
    "v_or_b32_e32 v194, v194, v202\n"
    "v_pk_mul_f16 v188, v188, v233\n"
    "v_mfma_f32_16x16x32_f16 v[100:103], a[36:39], v[184:187], v[100:103]\n"
    "ds_read_u16_d16_hi v206, v206\n"
    "ds_read_u16 v199, v199\n"
    "v_mfma_f32_16x16x32_f16 v[112:115], a[40:43], v[184:187], v[112:115]\n"
    "v_pk_fma_f16 v232, v192, v192, s48\n"
    "v_or_b32_e64 v195, v195, v203\n"
    "v_mfma_f32_16x16x32_f16 v[116:119], a[44:47], v[184:187], v[116:119]\n"
    "ds_read_u16_d16_hi v207, v207\n"
    "v_pk_fma_f16 v233, v196, v196, s48\n"
    "v_mfma_f32_16x16x32_f16 v[128:131], v[236:239], v[184:187], v[128:131]\n"
    "s_waitcnt lgkmcnt(7)\n"
    "v_mov_b32_e32 v254, 0\n"
    "v_pk_mul_f16 v232, v192, v232\n"
    "v_mfma_f32_16x16x32_f16 v[132:135], a[244:247], v[184:187], v[132:135]\n"
    "ds_read_b128 a[252:255], v249 offset:3072\n"
    "v_or_b32_e64 v197, v197, v205\n"
    "v_mfma_f32_16x16x32_f16 v[104:107], a[32:35], v[188:191], v[104:107]\n"
    "ds_read_b128 v[216:219], v251 offset:0\n"
    "v_pk_mul_f16 v233, v196, v233\n"
    "v_mfma_f32_16x16x32_f16 v[108:111], a[36:39], v[188:191], v[108:111]\n"
    "ds_read_b128 v[220:223], v251 offset:336\n"
    "v_cvt_pk_f16_f32 v184, v168, v169\n"
    "v_mfma_f32_16x16x32_f16 v[120:123], a[40:43], v[188:191], v[120:123]\n"
    "ds_read_b128 a[240:243], v244 offset:38912\n"
    "v_lshlrev_b32_sdwa v185, 1, v170 dst_sel:DWORD dst_unused:UNUSED_PAD src0_sel:DWORD src1_sel:WORD_1\n"
    "v_mfma_f32_16x16x32_f16 v[124:127], a[44:47], v[188:191], v[124:127]\n"
    "ds_read_u16 v185, v185\n"
    "v_lshlrev_b32_sdwa v201, 1, v171 dst_sel:DWORD dst_unused:UNUSED_PAD src0_sel:DWORD src1_sel:WORD_1\n"
    "v_mfma_f32_16x16x32_f16 v[136:139], v[236:239], v[188:191], v[136:139]\n"
    "v_lshlrev_b32_sdwa v186, 1, v172 dst_sel:DWORD dst_unused:UNUSED_PAD src0_sel:DWORD src1_sel:WORD_1\n"
    "v_lshlrev_b32_sdwa v202, 1, v173 dst_sel:DWORD dst_unused:UNUSED_PAD src0_sel:DWORD src1_sel:WORD_1\n"
    "v_mfma_f32_16x16x32_f16 v[140:143], a[244:247], v[188:191], v[140:143]\n"
    "ds_read_u16_d16_hi v201, v201\n"
    "v_lshlrev_b32_sdwa v187, 1, v174 dst_sel:DWORD dst_unused:UNUSED_PAD src0_sel:DWORD src1_sel:WORD_1\n"
    "v_mfma_f32_16x16x32_bf16 v[168:171], a[168:171], v[144:147], 0\n"
    "v_lshlrev_b32_sdwa v203, 1, v175 dst_sel:DWORD dst_unused:UNUSED_PAD src0_sel:DWORD src1_sel:WORD_1\n"
    "v_exp_f16_e64 v234, v232\n"
    "v_mfma_f32_16x16x32_bf16 v[168:171], a[216:219], v[148:151], v[168:171]\n"
    "ds_read_u16 v186, v186\n"
    "s_waitcnt lgkmcnt(9)\n"
    "v_or_b32_e32 v198, v198, v206\n"
    "v_mfma_f32_16x16x32_bf16 v[168:171], a[124:127], v[152:155], v[168:171]\n"
    "ds_read_u16_d16_hi v202, v202\n"
    "v_cvt_pk_f16_f32 v188, v176, v177\n"
    "v_mfma_f32_16x16x32_bf16 v[172:175], a[172:175], v[144:147], 0\n"
    "v_lshlrev_b32_sdwa v189, 1, v178 dst_sel:DWORD dst_unused:UNUSED_PAD src0_sel:DWORD src1_sel:WORD_1\n"
    "s_mov_b64 exec, s[52:53]\n"
    "ds_write2_b32 v248, v25, v29 offset0:20 offset1:21\n"
    "s_mov_b64 exec, -1\n"
    "v_exp_f16_e64 v235, v233\n"
    "v_mfma_f32_16x16x32_bf16 v[172:175], a[220:223], v[148:151], v[172:175]\n"
    "s_waitcnt lgkmcnt(7)\n"
    "ds_read_b128 v[236:239], v244 offset:12288\n"
    "buffer_store_dwordx4 a[252:255], v255, s[68:71], s39 offen offset:64 sc1\n"
    "s_mov_b64 exec, s[52:53]\n"
    "ds_write2_b32 v248, v26, v30 offset0:40 offset1:41\n"
    "s_mov_b64 exec, -1\n"
    "v_lshlrev_b32_sdwa v205, 1, v179 dst_sel:DWORD dst_unused:UNUSED_PAD src0_sel:DWORD src1_sel:WORD_1\n"
    "v_lshlrev_b32_sdwa v190, 1, v180 dst_sel:DWORD dst_unused:UNUSED_PAD src0_sel:DWORD src1_sel:WORD_1\n"
    "v_lshlrev_b32_sdwa v206, 1, v181 dst_sel:DWORD dst_unused:UNUSED_PAD src0_sel:DWORD src1_sel:WORD_1\n"
    "s_mov_b64 exec, s[52:53]\n"
    "ds_write2_b32 v248, v27, v31 offset0:60 offset1:61\n"
    "s_mov_b64 exec, -1\n"
    "v_xor_b32_e32 v248, 8, v248\n"
    "v_lshlrev_b32_sdwa v191, 1, v182 dst_sel:DWORD dst_unused:UNUSED_PAD src0_sel:DWORD src1_sel:WORD_1\n"
    "v_exp_f16_sdwa v234, v232 dst_sel:WORD_1 dst_unused:UNUSED_PRESERVE src0_sel:WORD_1\n"
    "s_waitcnt lgkmcnt(7)\n"
    "v_mfma_f32_16x16x32_bf16 v[172:175], a[240:243], v[152:155], v[172:175]\n"
    "s_waitcnt lgkmcnt(6)\n"
    "ds_read_b128 a[252:255], v249 offset:5120\n"
    "ds_read_u16 v187, v187\n"
    "v_mfma_f32_16x16x32_bf16 v[176:179], a[168:171], v[156:159], 0\n"
    "ds_read_u16_d16_hi v203, v203\n"
    "v_or_b32_e64 v199, v199, v207\n"
    "v_lshlrev_b32_sdwa v207, 1, v183 dst_sel:DWORD dst_unused:UNUSED_PAD src0_sel:DWORD src1_sel:WORD_1\n"
    "v_exp_f16_sdwa v235, v233 dst_sel:WORD_1 dst_unused:UNUSED_PRESERVE src0_sel:WORD_1\n"
    "v_mfma_f32_16x16x32_bf16 v[176:179], a[216:219], v[160:163], v[176:179]\n"
    "ds_read_b128 a[244:247], v244 offset:14336\n"
    "v_pk_fma_f16 v234, v234, s49, s49\n"
    "v_mfma_f32_16x16x32_bf16 v[176:179], a[124:127], v[164:167], v[176:179]\n"
    "s_waitcnt lgkmcnt(7)\n"
    "ds_read_u16 v189, v189\n"
    "v_pk_fma_f16 v235, v235, s49, s49\n"
    "v_mfma_f32_16x16x32_bf16 v[180:183], a[172:175], v[156:159], 0\n"
    "v_rcp_f16_e32 v232, v234\n"
    "v_mfma_f32_16x16x32_bf16 v[180:183], a[220:223], v[160:163], v[180:183]\n"
    "ds_read_u16_d16_hi v205, v205\n"
    "v_rcp_f16_e64 v233, v235\n"
    "v_mfma_f32_16x16x32_bf16 v[180:183], a[240:243], v[164:167], v[180:183]\n"
    "v_rcp_f16_sdwa v232, v234 dst_sel:WORD_1 dst_unused:UNUSED_PRESERVE src0_sel:WORD_1\n"
    "s_mov_b64 exec, s[52:53]\n"
    "ds_write2_b32 v248, v16, v20 offset0:0 offset1:1\n"
    "s_mov_b64 exec, -1\n"
    "v_or_b32_e64 v185, v185, v201\n"
    "v_rcp_f16_sdwa v233, v235 dst_sel:WORD_1 dst_unused:UNUSED_PRESERVE src0_sel:WORD_1\n"
    "s_mov_b64 exec, s[52:53]\n"
    "ds_write2_b32 v248, v17, v21 offset0:20 offset1:21\n"
    "s_mov_b64 exec, -1\n"
    "v_pk_mul_f16 v192, v192, v232\n"
    "v_or_b32_e64 v186, v186, v202\n"
    "v_pk_mul_f16 v196, v196, v233\n"
    "v_mfma_f32_16x16x32_f16 v[96:99], a[48:51], v[192:195], v[96:99]\n"
    "s_waitcnt lgkmcnt(7)\n"
    "ds_read_u16 v190, v190\n"
    "buffer_store_dwordx4 a[252:255], v255, s[68:71], s39 offen offset:128 sc1\n"
    "s_mov_b64 exec, s[52:53]\n"
    "ds_write2_b32 v248, v18, v22 offset0:40 offset1:41\n"
    "s_mov_b64 exec, -1\n"
    "v_mfma_f32_16x16x32_f16 v[100:103], a[52:55], v[192:195], v[100:103]\n"
    "s_waitcnt lgkmcnt(6)\n"
    "v_pk_fma_f16 v232, v184, v184, s48\n"
    "v_or_b32_e64 v187, v187, v203\n"
    "v_mfma_f32_16x16x32_f16 v[112:115], a[56:59], v[192:195], v[112:115]\n"
    "ds_read_b128 v[144:147], v243 offset:0\n"
    "ds_read_u16_d16_hi v206, v206\n"
    "v_mfma_f32_16x16x32_f16 v[116:119], a[60:63], v[192:195], v[116:119]\n"
    "v_pk_fma_f16 v233, v188, v188, s48\n"
    "v_pk_mul_f16 v232, v184, v232\n"
    "v_mfma_f32_16x16x32_f16 v[128:131], v[236:239], v[192:195], v[128:131]\n"
    "ds_read_b128 v[148:151], v243 offset:1024\n"
    "v_pk_mul_f16 v233, v188, v233\n"
    "v_mfma_f32_16x16x32_f16 v[132:135], a[244:247], v[192:195], v[132:135]\n"
    "ds_read_u16 v191, v191\n"
    "s_waitcnt lgkmcnt(7)\n"
    "v_or_b32_e32 v189, v189, v205\n"
    "v_mfma_f32_16x16x32_f16 v[104:107], a[48:51], v[196:199], v[104:107]\n"
    "ds_read_b128 v[152:155], v243 offset:2048\n"
    "v_cvt_pk_f16_f32 v192, v168, v169\n"
    "v_mfma_f32_16x16x32_f16 v[108:111], a[52:55], v[196:199], v[108:111]\n"
    "ds_read_u16_d16_hi v207, v207\n"
    "v_lshlrev_b32_sdwa v193, 1, v170 dst_sel:DWORD dst_unused:UNUSED_PAD src0_sel:DWORD src1_sel:WORD_1\n"
    "v_mfma_f32_16x16x32_f16 v[120:123], a[56:59], v[196:199], v[120:123]\n"
    "v_lshlrev_b32_sdwa v201, 1, v171 dst_sel:DWORD dst_unused:UNUSED_PAD src0_sel:DWORD src1_sel:WORD_1\n"
    "v_lshlrev_b32_sdwa v194, 1, v172 dst_sel:DWORD dst_unused:UNUSED_PAD src0_sel:DWORD src1_sel:WORD_1\n"
    "v_mfma_f32_16x16x32_f16 v[124:127], a[60:63], v[196:199], v[124:127]\n"
    "ds_read_b128 v[156:159], v243 offset:3072\n"
    "v_lshlrev_b32_sdwa v202, 1, v173 dst_sel:DWORD dst_unused:UNUSED_PAD src0_sel:DWORD src1_sel:WORD_1\n"
    "v_mfma_f32_16x16x32_f16 v[136:139], v[236:239], v[196:199], v[136:139]\n"
    "ds_read_b128 v[160:163], v243 offset:4096\n"
    "v_lshlrev_b32_sdwa v195, 1, v174 dst_sel:DWORD dst_unused:UNUSED_PAD src0_sel:DWORD src1_sel:WORD_1\n"
    "v_mfma_f32_16x16x32_f16 v[140:143], a[244:247], v[196:199], v[140:143]\n"
    "ds_read_b128 v[164:167], v243 offset:5120\n"
    "v_lshlrev_b32_sdwa v203, 1, v175 dst_sel:DWORD dst_unused:UNUSED_PAD src0_sel:DWORD src1_sel:WORD_1\n"
    "v_exp_f16_e32 v234, v232\n"
    "s_mov_b64 exec, s[52:53]\n"
    "ds_write2_b32 v248, v19, v23 offset0:60 offset1:61\n"
    "s_mov_b64 exec, -1\n"
    "v_mfma_f32_32x32x1_2b_f32 v[0:31], v254, v254, 0\n"
    "s_waitcnt lgkmcnt(9)\n"
    "v_subrev_u32_e32 v248, 4080, v248\n"
    "v_cvt_pk_f16_f32 v196, v176, v177\n"
    "v_lshlrev_b32_sdwa v197, 1, v178 dst_sel:DWORD dst_unused:UNUSED_PAD src0_sel:DWORD src1_sel:WORD_1\n"
    "v_exp_f16_e64 v235, v233\n"
    "v_lshlrev_b32_sdwa v205, 1, v179 dst_sel:DWORD dst_unused:UNUSED_PAD src0_sel:DWORD src1_sel:WORD_1\n"
    "v_lshlrev_b32_sdwa v198, 1, v180 dst_sel:DWORD dst_unused:UNUSED_PAD src0_sel:DWORD src1_sel:WORD_1\n"
    "s_waitcnt lgkmcnt(7)\n"
    "v_or_b32_e32 v190, v190, v206\n"
    "v_lshlrev_b32_sdwa v206, 1, v181 dst_sel:DWORD dst_unused:UNUSED_PAD src0_sel:DWORD src1_sel:WORD_1\n"
    "v_lshlrev_b32_sdwa v199, 1, v182 dst_sel:DWORD dst_unused:UNUSED_PAD src0_sel:DWORD src1_sel:WORD_1\n"
    "v_exp_f16_sdwa v234, v232 dst_sel:WORD_1 dst_unused:UNUSED_PRESERVE src0_sel:WORD_1\n"
    "v_exp_f16_sdwa v235, v233 dst_sel:WORD_1 dst_unused:UNUSED_PRESERVE src0_sel:WORD_1\n"
    "v_mfma_f32_16x16x32_bf16 v[168:171], a[128:131], v[144:147], 0\n"
    "ds_read_b128 a[240:243], v244 offset:32768\n"
    "ds_read_u16 v193, v193\n"
    "ds_read_u16_d16_hi v201, v201\n"
    "v_mfma_f32_16x16x32_bf16 v[168:171], a[176:179], v[148:151], v[168:171]\n"
    "s_waitcnt lgkmcnt(8)\n"
    "s_nop 0\n"
    "ds_read_u16 v194, v194\n"
    "v_pk_fma_f16 v234, v234, s49, s49\n"
    "v_mfma_f32_16x16x32_bf16 v[168:171], a[96:99], v[152:155], v[168:171]\n"
    "ds_read_u16_d16_hi v202, v202\n"
    "v_pk_fma_f16 v235, v235, s49, s49\n"
    "v_mfma_f32_16x16x32_bf16 v[172:175], a[132:135], v[144:147], 0\n"
    "s_waitcnt lgkmcnt(8)\n"
    "v_or_b32_e32 v191, v191, v207\n"
    "v_lshlrev_b32_sdwa v207, 1, v183 dst_sel:DWORD dst_unused:UNUSED_PAD src0_sel:DWORD src1_sel:WORD_1\n"
    "v_mfma_f32_16x16x32_bf16 v[172:175], a[180:183], v[148:151], v[172:175]\n"
    "ds_read_b128 v[236:239], v244 offset:16384\n"
    "v_rcp_f16_e64 v232, v234\n"
    "v_mfma_f32_16x16x32_bf16 v[176:179], a[128:131], v[156:159], 0\n"
    "s_waitcnt lgkmcnt(7)\n"
    "ds_read_u16 v195, v195\n"
    "v_rcp_f16_e64 v233, v235\n"
    "s_waitcnt lgkmcnt(7)\n"
    "s_barrier\n"
    "s_waitcnt lgkmcnt(6)\n"
    "v_mfma_f32_16x16x32_bf16 v[172:175], a[240:243], v[152:155], v[172:175]\n"
    "s_barrier\n"
    "v_mfma_f32_16x16x32_bf16 v[176:179], a[176:179], v[160:163], v[176:179]\n"
    "ds_read_b128 v[208:211], v250 offset:0\n"
    "ds_read_b128 v[212:215], v250 offset:320\n"
    "ds_write_b128 v253, v[64:67] offset:2048\n"
    "v_mfma_f32_16x16x32_bf16 v[176:179], a[96:99], v[164:167], v[176:179]\n"
    "s_waitcnt lgkmcnt(7)\n"
    "v_rcp_f16_sdwa v232, v234 dst_sel:WORD_1 dst_unused:UNUSED_PRESERVE src0_sel:WORD_1\n"
    "v_mfma_f32_16x16x32_bf16 v[180:183], a[132:135], v[156:159], 0\n"
    "v_or_b32_e64 v193, v193, v201\n"
    "v_rcp_f16_sdwa v233, v235 dst_sel:WORD_1 dst_unused:UNUSED_PRESERVE src0_sel:WORD_1\n"
    "v_mfma_f32_16x16x32_bf16 v[180:183], a[180:183], v[160:163], v[180:183]\n"
    "ds_read_u16_d16_hi v203, v203\n"
    "ds_read_b128 a[244:247], v244 offset:18432\n"
    "ds_write_b128 v253, v[68:71] offset:5120\n"
    "v_mfma_f32_16x16x32_bf16 v[180:183], a[240:243], v[164:167], v[180:183]\n"
    "s_waitcnt lgkmcnt(7)\n"
    "v_pk_mul_f16 v184, v184, v232\n"
    "v_or_b32_e32 v194, v194, v202\n"
    "v_pk_mul_f16 v188, v188, v233\n"
    "v_mfma_f32_16x16x32_f16 v[96:99], a[64:67], v[184:187], v[96:99]\n"
    "v_pk_fma_f16 v232, v192, v192, s48\n"
    "v_pk_fma_f16 v233, v196, v196, s48\n"
    "v_mfma_f32_16x16x32_f16 v[100:103], a[68:71], v[184:187], v[100:103]\n"
    "s_waitcnt lgkmcnt(4)\n"
    "s_nop 0\n"
    "ds_read_u16 v197, v197\n"
    "ds_read_u16_d16_hi v205, v205\n"
    "ds_write_b128 v253, v[72:75] offset:1024\n"
    "v_smfmac_f32_16x16x64_bf16 v[0:3], v[216:219], v[208:215], v240\n"
    "v_pk_mul_f16 v232, v192, v232\n"
    "v_smfmac_f32_16x16x64_bf16 v[4:7], v[220:223], v[208:215], v240 abid:1\n"
    "v_pk_mul_f16 v233, v196, v233\n"
    "v_mfma_f32_16x16x32_f16 v[112:115], a[72:75], v[184:187], v[112:115]\n"
    "ds_read_b128 v[208:211], v250 offset:640\n"
    "ds_read_b128 v[212:215], v250 offset:960\n"
    "ds_read_b128 v[216:219], v252 offset:0\n"
    "v_mfma_f32_16x16x32_f16 v[116:119], a[76:79], v[184:187], v[116:119]\n"
    "s_waitcnt lgkmcnt(7)\n"
    "ds_read_b128 v[220:223], v252 offset:336\n"
    "ds_read_u16 v198, v198\n"
    "v_mfma_f32_16x16x32_f16 v[128:131], v[236:239], v[184:187], v[128:131]\n"
    "v_or_b32_e32 v195, v195, v203\n"
    "v_lshlrev_b32_sdwa v201, 1, v171 dst_sel:DWORD dst_unused:UNUSED_PAD src0_sel:DWORD src1_sel:WORD_1\n"
    "v_mfma_f32_16x16x32_f16 v[132:135], a[244:247], v[184:187], v[132:135]\n"
    "ds_read_u16_d16_hi v206, v206\n"
    "v_cvt_pk_f16_f32 v184, v168, v169\n"
    "v_mfma_f32_16x16x32_f16 v[104:107], a[64:67], v[188:191], v[104:107]\n"
    "v_lshlrev_b32_sdwa v185, 1, v170 dst_sel:DWORD dst_unused:UNUSED_PAD src0_sel:DWORD src1_sel:WORD_1\n"
    "v_lshlrev_b32_sdwa v186, 1, v172 dst_sel:DWORD dst_unused:UNUSED_PAD src0_sel:DWORD src1_sel:WORD_1\n"
    "v_mfma_f32_16x16x32_f16 v[108:111], a[68:71], v[188:191], v[108:111]\n"
    "ds_read_u16 v199, v199\n"
    "v_lshlrev_b32_sdwa v202, 1, v173 dst_sel:DWORD dst_unused:UNUSED_PAD src0_sel:DWORD src1_sel:WORD_1\n"
    "v_mfma_f32_16x16x32_f16 v[120:123], a[72:75], v[188:191], v[120:123]\n"
    "v_lshlrev_b32_sdwa v187, 1, v174 dst_sel:DWORD dst_unused:UNUSED_PAD src0_sel:DWORD src1_sel:WORD_1\n"
    "v_lshlrev_b32_sdwa v203, 1, v175 dst_sel:DWORD dst_unused:UNUSED_PAD src0_sel:DWORD src1_sel:WORD_1\n"
    "v_mfma_f32_16x16x32_f16 v[124:127], a[76:79], v[188:191], v[124:127]\n"
    "ds_read_u16_d16_hi v207, v207\n"
    "v_exp_f16_e64 v234, v232\n"
    "v_mfma_f32_16x16x32_f16 v[136:139], v[236:239], v[188:191], v[136:139]\n"
    "s_waitcnt lgkmcnt(8)\n"
    "ds_read_b128 a[240:243], v244 offset:36864\n"
    "v_or_b32_e32 v197, v197, v205\n"
    "v_mfma_f32_16x16x32_f16 v[140:143], a[244:247], v[188:191], v[140:143]\n"
    "buffer_load_dwordx4 v[64:67], v246, s[40:43], s60 offen offset:128\n"
    "ds_read_u16 v185, v185\n"
    "v_mfma_f32_16x16x32_bf16 v[168:171], a[136:139], v[144:147], 0\n"
    "v_cvt_pk_f16_f32 v188, v176, v177\n"
    "v_lshlrev_b32_sdwa v189, 1, v178 dst_sel:DWORD dst_unused:UNUSED_PAD src0_sel:DWORD src1_sel:WORD_1\n"
    "v_mfma_f32_16x16x32_bf16 v[168:171], a[184:187], v[148:151], v[168:171]\n"
    "s_waitcnt lgkmcnt(7)\n"
    "ds_read_u16_d16_hi v201, v201\n"
    "v_exp_f16_e32 v235, v233\n"
    "v_smfmac_f32_16x16x64_bf16 v[0:3], v[216:219], v[208:215], v240\n"
    "s_waitcnt lgkmcnt(7)\n"
    "s_nop 0\n"
    "ds_read_b128 v[216:219], v251 offset:672\n"
    "v_smfmac_f32_16x16x64_bf16 v[4:7], v[220:223], v[208:215], v240 abid:1\n"
    "ds_read_b128 v[208:211], v250 offset:80\n"
    "v_mfma_f32_16x16x32_bf16 v[168:171], a[100:103], v[152:155], v[168:171]\n"
    "v_lshlrev_b32_sdwa v205, 1, v179 dst_sel:DWORD dst_unused:UNUSED_PAD src0_sel:DWORD src1_sel:WORD_1\n"
    "v_lshlrev_b32_sdwa v190, 1, v180 dst_sel:DWORD dst_unused:UNUSED_PAD src0_sel:DWORD src1_sel:WORD_1\n"
    "v_mfma_f32_16x16x32_bf16 v[172:175], a[140:143], v[144:147], 0\n"
    "ds_read_b128 v[212:215], v250 offset:400\n"
    "ds_read_b128 v[220:223], v251 offset:1008\n"
    "v_lshlrev_b32_sdwa v191, 1, v182 dst_sel:DWORD dst_unused:UNUSED_PAD src0_sel:DWORD src1_sel:WORD_1\n"
    "v_exp_f16_sdwa v234, v232 dst_sel:WORD_1 dst_unused:UNUSED_PRESERVE src0_sel:WORD_1\n"
    "v_pk_mov_b32 v[224:225], v[0:1], v[0:1] op_sel:[0,1]\n"
    "v_pk_mov_b32 v[226:227], v[2:3], v[2:3] op_sel:[0,1]\n"
    "v_pk_mul_f32 v[228:229], v[0:1], v[0:1]\n"
    "v_pk_mul_f32 v[230:231], v[2:3], v[2:3]\n"
    "v_pk_add_f32 v[224:225], v[224:225], v[4:5]\n"
    "v_pk_add_f32 v[226:227], v[226:227], v[6:7]\n"
    "v_pk_fma_f32 v[228:229], v[4:5], v[4:5], v[228:229]\n"
    "v_pk_fma_f32 v[230:231], v[6:7], v[6:7], v[230:231]\n"
    "v_mfma_f32_16x16x32_bf16 v[172:175], a[188:191], v[148:151], v[172:175]\n"
    "s_waitcnt lgkmcnt(8)\n"
    "ds_read_u16 v186, v186\n"
    "ds_read_u16_d16_hi v202, v202\n"
    "v_mfma_f32_16x16x32_bf16 v[176:179], a[136:139], v[156:159], 0\n"
    "s_waitcnt lgkmcnt(8)\n"
    "ds_read_b128 v[236:239], v244 offset:20480\n"
    "v_or_b32_e64 v198, v198, v206\n"
    "v_mfma_f32_16x16x32_bf16 v[172:175], a[240:243], v[152:155], v[172:175]\n"
    "ds_read_u16 v187, v187\n"
    "v_lshlrev_b32_sdwa v206, 1, v181 dst_sel:DWORD dst_unused:UNUSED_PAD src0_sel:DWORD src1_sel:WORD_1\n"
    "v_or_b32_e64 v199, v199, v207\n"
    "v_lshlrev_b32_sdwa v207, 1, v183 dst_sel:DWORD dst_unused:UNUSED_PAD src0_sel:DWORD src1_sel:WORD_1\n"
    "v_exp_f16_sdwa v235, v233 dst_sel:WORD_1 dst_unused:UNUSED_PRESERVE src0_sel:WORD_1\n"
    "v_mfma_f32_16x16x32_bf16 v[176:179], a[184:187], v[160:163], v[176:179]\n"
    "buffer_load_dwordx4 v[68:71], v246, s[40:43], s60 offen offset:320\n"
    "ds_read_u16_d16_hi v203, v203\n"
    "v_mfma_f32_16x16x32_bf16 v[176:179], a[100:103], v[164:167], v[176:179]\n"
    "v_pk_fma_f16 v234, v234, s49, s49\n"
    "v_pk_fma_f16 v235, v235, s49, s49\n"
    "v_mfma_f32_16x16x32_bf16 v[180:183], a[140:143], v[156:159], 0\n"
    "s_waitcnt lgkmcnt(8)\n"
    "ds_read_b128 a[244:247], v244 offset:22528\n"
    "v_rcp_f16_e32 v232, v234\n"
    "v_mfma_f32_16x16x32_bf16 v[180:183], a[188:191], v[160:163], v[180:183]\n"
    "ds_read_u16 v189, v189\n"
    "v_rcp_f16_e32 v233, v235\n"
    "s_waitcnt lgkmcnt(8)\n"
    "v_smfmac_f32_16x16x64_bf16 v[8:11], v[216:219], v[208:215], v240\n"
    "s_waitcnt lgkmcnt(7)\n"
    "ds_read_b128 v[216:219], v252 offset:672\n"
    "ds_read_u16_d16_hi v205, v205\n"
    "v_smfmac_f32_16x16x64_bf16 v[12:15], v[220:223], v[208:215], v240 abid:1\n"
    "v_rcp_f16_sdwa v232, v234 dst_sel:WORD_1 dst_unused:UNUSED_PRESERVE src0_sel:WORD_1\n"
    "v_mfma_f32_16x16x32_bf16 v[180:183], a[240:243], v[164:167], v[180:183]\n"
    "ds_read_b128 v[208:211], v250 offset:720\n"
    "v_or_b32_e32 v185, v185, v201\n"
    "v_rcp_f16_sdwa v233, v235 dst_sel:WORD_1 dst_unused:UNUSED_PRESERVE src0_sel:WORD_1\n"
    "ds_read_b128 v[212:215], v250 offset:1040\n"
    "ds_read_b128 v[220:223], v252 offset:1008\n"
    "v_pk_mul_f16 v192, v192, v232\n"
    "s_waitcnt lgkmcnt(9)\n"
    "v_or_b32_e32 v186, v186, v202\n"
    "v_mfma_f32_16x16x32_f16 v[96:99], a[80:83], v[192:195], v[96:99]\n"
    "ds_read_u16 v190, v190\n"
    "v_pk_mul_f16 v196, v196, v233\n"
    "v_mfma_f32_16x16x32_f16 v[100:103], a[84:87], v[192:195], v[100:103]\n"
    "v_pk_fma_f16 v232, v184, v184, s48\n"
    "v_pk_fma_f16 v233, v188, v188, s48\n"
    "v_mfma_f32_16x16x32_f16 v[112:115], a[88:91], v[192:195], v[112:115]\n"
    "ds_read_u16_d16_hi v206, v206\n"
    "v_pk_mul_f16 v232, v184, v232\n"
    "v_mfma_f32_16x16x32_f16 v[116:119], a[92:95], v[192:195], v[116:119]\n"
    "s_waitcnt lgkmcnt(8)\n"
    "ds_read_u16 v191, v191\n"
    "buffer_load_dwordx4 v[72:75], v247, s[40:43], s60 offen offset:128\n"
    "v_mfma_f32_16x16x32_f16 v[128:131], v[236:239], v[192:195], v[128:131]\n"
    "v_or_b32_e32 v187, v187, v203\n"
    "v_pk_mul_f16 v233, v188, v233\n"
    "v_mfma_f32_16x16x32_f16 v[132:135], a[244:247], v[192:195], v[132:135]\n"
    "ds_read_u16_d16_hi v207, v207\n"
    "v_cvt_pk_f16_f32 v192, v168, v169\n"
    "v_mfma_f32_16x16x32_f16 v[104:107], a[80:83], v[196:199], v[104:107]\n"
    "v_lshlrev_b32_sdwa v193, 1, v170 dst_sel:DWORD dst_unused:UNUSED_PAD src0_sel:DWORD src1_sel:WORD_1\n"
    "v_lshlrev_b32_sdwa v201, 1, v171 dst_sel:DWORD dst_unused:UNUSED_PAD src0_sel:DWORD src1_sel:WORD_1\n"
    "v_mfma_f32_16x16x32_f16 v[108:111], a[84:87], v[196:199], v[108:111]\n"
    "ds_read_u16 v193, v193\n"
    "v_lshlrev_b32_sdwa v194, 1, v172 dst_sel:DWORD dst_unused:UNUSED_PAD src0_sel:DWORD src1_sel:WORD_1\n"
    "v_mfma_f32_16x16x32_f16 v[120:123], a[88:91], v[196:199], v[120:123]\n"
    "v_lshlrev_b32_sdwa v202, 1, v173 dst_sel:DWORD dst_unused:UNUSED_PAD src0_sel:DWORD src1_sel:WORD_1\n"
    "v_lshlrev_b32_sdwa v195, 1, v174 dst_sel:DWORD dst_unused:UNUSED_PAD src0_sel:DWORD src1_sel:WORD_1\n"
    "v_mfma_f32_16x16x32_f16 v[124:127], a[92:95], v[196:199], v[124:127]\n"
    "s_waitcnt lgkmcnt(7)\n"
    "ds_read_u16_d16_hi v201, v201\n"
    "v_or_b32_e32 v189, v189, v205\n"
    "s_waitcnt lgkmcnt(7)\n"
    "v_smfmac_f32_16x16x64_bf16 v[8:11], v[216:219], v[208:215], v240\n"
    "s_waitcnt lgkmcnt(6)\n"
    "ds_read_b128 v[216:219], v251 offset:1344\n"
    "v_smfmac_f32_16x16x64_bf16 v[12:15], v[220:223], v[208:215], v240 abid:1\n"
    "ds_read_b128 v[208:211], v250 offset:160\n"
    "v_mfma_f32_16x16x32_f16 v[136:139], v[236:239], v[196:199], v[136:139]\n"
    "ds_read_b128 v[212:215], v250 offset:480\n"
    "v_lshlrev_b32_sdwa v203, 1, v175 dst_sel:DWORD dst_unused:UNUSED_PAD src0_sel:DWORD src1_sel:WORD_1\n"
    "v_mfma_f32_16x16x32_f16 v[140:143], a[244:247], v[196:199], v[140:143]\n"
    "ds_read_b128 v[220:223], v251 offset:1680\n"
    "v_exp_f16_e64 v234, v232\n"
    "ds_write_b128 v253, v[76:79] offset:4096\n"
    "v_pk_add_f32 v[224:225], v[224:225], v[8:9]\n"
    "v_pk_add_f32 v[226:227], v[226:227], v[10:11]\n"
    "v_pk_fma_f32 v[228:229], v[8:9], v[8:9], v[228:229]\n"
    "v_pk_fma_f32 v[230:231], v[10:11], v[10:11], v[230:231]\n"
    "v_pk_add_f32 v[224:225], v[224:225], v[12:13]\n"
    "v_pk_add_f32 v[226:227], v[226:227], v[14:15]\n"
    "v_pk_fma_f32 v[228:229], v[12:13], v[12:13], v[228:229]\n"
    "v_pk_fma_f32 v[230:231], v[14:15], v[14:15], v[230:231]\n"
    "v_mfma_f32_16x16x32_bf16 v[168:171], a[144:147], v[144:147], 0\n"
    "s_waitcnt lgkmcnt(8)\n"
    "buffer_load_dwordx4 v[76:79], v247, s[40:43], s60 offen offset:320\n"
    "ds_read_u16 v194, v194\n"
    "ds_write_b128 v253, v[88:91] offset:0\n"
    "v_mfma_f32_16x16x32_bf16 v[168:171], a[192:195], v[148:151], v[168:171]\n"
    "s_waitcnt lgkmcnt(8)\n"
    "v_or_b32_e64 v190, v190, v206\n"
    "v_cvt_pk_f16_f32 v196, v176, v177\n"
    "v_mfma_f32_16x16x32_bf16 v[168:171], a[104:107], v[152:155], v[168:171]\n"
    "v_lshlrev_b32_sdwa v197, 1, v178 dst_sel:DWORD dst_unused:UNUSED_PAD src0_sel:DWORD src1_sel:WORD_1\n"
    "v_exp_f16_e64 v235, v233\n"
    "v_mfma_f32_16x16x32_bf16 v[172:175], a[148:151], v[144:147], 0\n"
    "s_waitcnt lgkmcnt(6)\n"
    "ds_read_u16_d16_hi v202, v202\n"
    "ds_read_b128 v[236:239], v244 offset:0\n"
    "ds_write_b128 v253, v[92:95] offset:3072\n"
    "s_waitcnt lgkmcnt(7)\n"
    "v_smfmac_f32_16x16x64_bf16 v[16:19], v[216:219], v[208:215], v240\n"
    "s_waitcnt lgkmcnt(6)\n"
    "s_nop 0\n"
    "v_lshlrev_b32_sdwa v205, 1, v179 dst_sel:DWORD dst_unused:UNUSED_PAD src0_sel:DWORD src1_sel:WORD_1\n"
    "v_smfmac_f32_16x16x64_bf16 v[20:23], v[220:223], v[208:215], v240 abid:1\n"
    "v_lshlrev_b32_sdwa v198, 1, v180 dst_sel:DWORD dst_unused:UNUSED_PAD src0_sel:DWORD src1_sel:WORD_1\n"
    "v_mfma_f32_16x16x32_bf16 v[172:175], a[196:199], v[148:151], v[172:175]\n"
    "ds_read_b128 v[208:211], v250 offset:800\n"
    "ds_read_b128 v[212:215], v250 offset:1120\n"
    "ds_read_b128 v[216:219], v252 offset:1344\n"
    "v_lshlrev_b32_sdwa v206, 1, v181 dst_sel:DWORD dst_unused:UNUSED_PAD src0_sel:DWORD src1_sel:WORD_1\n"
    "ds_read_b128 v[220:223], v252 offset:1680\n"
    "ds_read_u16 v195, v195\n"
    "v_lshlrev_b32_sdwa v199, 1, v182 dst_sel:DWORD dst_unused:UNUSED_PAD src0_sel:DWORD src1_sel:WORD_1\n"
    "v_exp_f16_sdwa v234, v232 dst_sel:WORD_1 dst_unused:UNUSED_PRESERVE src0_sel:WORD_1\n"
    "v_mfma_f32_16x16x32_bf16 v[172:175], a[108:111], v[152:155], v[172:175]\n"
    "ds_read_u16_d16_hi v203, v203\n"
    "v_or_b32_e64 v191, v191, v207\n"
    "v_mfma_f32_16x16x32_bf16 v[176:179], a[144:147], v[156:159], 0\n"
    "buffer_load_dwordx4 v[80:83], v246, s[40:43], s61 offen offset:128\n"
    "v_lshlrev_b32_sdwa v207, 1, v183 dst_sel:DWORD dst_unused:UNUSED_PAD src0_sel:DWORD src1_sel:WORD_1\n"
    "v_exp_f16_sdwa v235, v233 dst_sel:WORD_1 dst_unused:UNUSED_PRESERVE src0_sel:WORD_1\n"
    "v_mfma_f32_16x16x32_bf16 v[176:179], a[192:195], v[160:163], v[176:179]\n"
    "ds_read_b128 a[244:247], v244 offset:2048\n"
    "ds_read_u16 v197, v197\n"
    "v_mfma_f32_16x16x32_bf16 v[176:179], a[104:107], v[164:167], v[176:179]\n"
    "s_waitcnt lgkmcnt(9)\n"
    "s_nop 0\n"
    "v_pk_fma_f16 v234, v234, s49, s49\n"
    "v_pk_fma_f16 v235, v235, s49, s49\n"
    "v_mfma_f32_16x16x32_bf16 v[180:183], a[148:151], v[156:159], 0\n"
    "ds_read_u16_d16_hi v205, v205\n"
    "v_cvt_pk_bf16_f32 v96, v96, v97\n"
    "v_mfma_f32_16x16x32_bf16 v[180:183], a[196:199], v[160:163], v[180:183]\n"
    "v_cvt_pk_bf16_f32 v97, v98, v99\n"
    "v_rcp_f16_e64 v232, v234\n"
    "v_mfma_f32_16x16x32_bf16 v[180:183], a[108:111], v[164:167], v[180:183]\n"
    "ds_read_u16 v198, v198\n"
    "v_cvt_pk_bf16_f32 v98, v100, v101\n"
    "v_cvt_pk_bf16_f32 v99, v102, v103\n"
    "v_cvt_pk_bf16_f32 v112, v112, v113\n"
    "ds_write_b128 v242, v[96:99] offset:24640\n"
    "s_waitcnt lgkmcnt(7)\n"
    "v_smfmac_f32_16x16x64_bf16 v[16:19], v[216:219], v[208:215], v240\n"
    "v_rcp_f16_e32 v233, v235\n"
    "v_smfmac_f32_16x16x64_bf16 v[20:23], v[220:223], v[208:215], v240 abid:1\n"
    "ds_read_b128 v[208:211], v250 offset:240\n"
    "ds_read_b128 v[212:215], v250 offset:560\n"
    "ds_read_b128 v[216:219], v251 offset:2016\n"
    "v_cvt_pk_bf16_f32 v113, v114, v115\n"
    "ds_read_b128 v[220:223], v251 offset:2352\n"
    "v_cvt_pk_bf16_f32 v114, v116, v117\n"
    "v_pk_add_f32 v[224:225], v[224:225], v[16:17]\n"
    "v_pk_add_f32 v[226:227], v[226:227], v[18:19]\n"
    "v_pk_fma_f32 v[228:229], v[16:17], v[16:17], v[228:229]\n"
    "v_pk_fma_f32 v[230:231], v[18:19], v[18:19], v[230:231]\n"
    "v_pk_add_f32 v[224:225], v[224:225], v[20:21]\n"
    "v_pk_add_f32 v[226:227], v[226:227], v[22:23]\n"
    "v_pk_fma_f32 v[228:229], v[20:21], v[20:21], v[228:229]\n"
    "v_pk_fma_f32 v[230:231], v[22:23], v[22:23], v[230:231]\n"
    "v_cvt_pk_bf16_f32 v115, v118, v119\n"
    "ds_write_b128 v242, v[112:115] offset:26688\n"
    "buffer_load_dwordx4 v[84:87], v246, s[40:43], s61 offen offset:320\n"
    "v_rcp_f16_sdwa v232, v234 dst_sel:WORD_1 dst_unused:UNUSED_PRESERVE src0_sel:WORD_1\n"
    "v_cvt_pk_bf16_f32 v128, v128, v129\n"
    "v_cvt_pk_bf16_f32 v129, v130, v131\n"
    "v_cvt_pk_bf16_f32 v130, v132, v133\n"
    "v_or_b32_e64 v193, v193, v201\n"
    "v_cvt_pk_bf16_f32 v131, v134, v135\n"
    "ds_write_b128 v242, v[128:131] offset:28736\n"
    "v_rcp_f16_sdwa v233, v235 dst_sel:WORD_1 dst_unused:UNUSED_PRESERVE src0_sel:WORD_1\n"
    "v_cvt_pk_bf16_f32 v104, v104, v105\n"
    "v_cvt_pk_bf16_f32 v105, v106, v107\n"
    "v_cvt_pk_bf16_f32 v106, v108, v109\n"
    "v_pk_mul_f16 v184, v184, v232\n"
    "v_cvt_pk_bf16_f32 v107, v110, v111\n"
    "ds_write_b128 v242, v[104:107] offset:25664\n"
    "s_waitcnt lgkmcnt(3)\n"
    "v_smfmac_f32_16x16x64_bf16 v[24:27], v[216:219], v[208:215], v240\n"
    "v_or_b32_e32 v194, v194, v202\n"
    "v_smfmac_f32_16x16x64_bf16 v[28:31], v[220:223], v[208:215], v240 abid:1\n"
    "ds_read_b128 v[208:211], v250 offset:880\n"
    "ds_read_b128 v[212:215], v250 offset:1200\n"
    "ds_read_b128 v[216:219], v252 offset:2016\n"
    "v_mfma_f32_16x16x32_f16 v[96:99], a[0:3], v[184:187], 0\n"
    "ds_read_b128 v[220:223], v252 offset:2352\n"
    "ds_read_u16_d16_hi v206, v206\n"
    "buffer_load_dwordx4 v[88:91], v247, s[40:43], s61 offen offset:128\n"
    "v_mfma_f32_16x16x32_f16 v[100:103], a[4:7], v[184:187], 0\n"
    "ds_read_u16 v199, v199\n"
    "v_cvt_pk_bf16_f32 v120, v120, v121\n"
    "v_mfma_f32_16x16x32_f16 v[112:115], a[8:11], v[184:187], 0\n"
    "ds_read_u16_d16_hi v207, v207\n"
    "v_cvt_pk_bf16_f32 v121, v122, v123\n"
    "v_mfma_f32_16x16x32_f16 v[116:119], a[12:15], v[184:187], 0\n"
    "v_pk_mul_f16 v188, v188, v233\n"
    "v_cvt_pk_bf16_f32 v122, v124, v125\n"
    "v_mfma_f32_16x16x32_f16 v[128:131], v[236:239], v[184:187], 0\n"
    "v_cvt_pk_bf16_f32 v123, v126, v127\n"
    "v_cvt_pk_bf16_f32 v136, v136, v137\n"
    "v_mfma_f32_16x16x32_f16 v[132:135], a[244:247], v[184:187], 0\n"
    "ds_write_b128 v242, v[120:123] offset:27712\n"
    "v_mfma_f32_16x16x32_f16 v[104:107], a[0:3], v[188:191], 0\n"
    "v_pk_fma_f16 v232, v192, v192, s48\n"
    "v_cvt_pk_bf16_f32 v137, v138, v139\n"
    "v_mfma_f32_16x16x32_f16 v[108:111], a[4:7], v[188:191], 0\n"
    "s_waitcnt lgkmcnt(6)\n"
    "s_barrier\n"
    "s_waitcnt lgkmcnt(4)\n"
    "v_smfmac_f32_16x16x64_bf16 v[24:27], v[216:219], v[208:215], v240\n"
    "s_waitcnt vmcnt(23)\n"
    "ds_write2_b32 v248, v32, v36 offset0:0 offset1:1\n"
    "v_smfmac_f32_16x16x64_bf16 v[28:31], v[220:223], v[208:215], v240 abid:1\n"
    "v_cvt_pk_bf16_f32 v138, v140, v141\n"
    "v_mfma_f32_16x16x32_f16 v[120:123], a[8:11], v[188:191], 0\n"
    "s_waitcnt lgkmcnt(4)\n"
    "ds_write2_b32 v248, v33, v37 offset0:20 offset1:21\n"
    "v_mfma_f32_16x16x32_f16 v[124:127], a[12:15], v[188:191], 0\n"
    "v_or_b32_e32 v195, v195, v203\n"
    "v_cvt_pk_bf16_f32 v139, v142, v143\n"
    "ds_write2_b32 v248, v34, v38 offset0:40 offset1:41\n"
    "v_pk_add_f32 v[224:225], v[224:225], v[24:25]\n"
    "v_pk_add_f32 v[226:227], v[226:227], v[26:27]\n"
    "v_pk_fma_f32 v[228:229], v[24:25], v[24:25], v[228:229]\n"
    "v_pk_fma_f32 v[230:231], v[26:27], v[26:27], v[230:231]\n"
    "v_pk_add_f32 v[224:225], v[224:225], v[28:29]\n"
    "v_pk_add_f32 v[226:227], v[226:227], v[30:31]\n"
    "v_pk_fma_f32 v[228:229], v[28:29], v[28:29], v[228:229]\n"
    "v_pk_fma_f32 v[230:231], v[30:31], v[30:31], v[230:231]\n"
    "ds_write2_b32 v248, v35, v39 offset0:60 offset1:61\n"
    "v_xor_b32_e64 v248, 8, v248\n"
    "v_pk_fma_f16 v233, v196, v196, s48\n"
    "v_mfma_f32_16x16x32_f16 v[140:143], a[244:247], v[188:191], 0\n"
    "s_waitcnt lgkmcnt(3)\n"
    "s_waitcnt vmcnt(21)\n"
    "ds_write2_b32 v248, v40, v44 offset0:0 offset1:1\n"
    "v_pk_mul_f16 v232, v192, v232\n"
    "s_waitcnt vmcnt(13)\n"
    "v_smfmac_f32_32x32x32_bf16 v[96:111], a[248:251], a[224:231], v241\n"
    "buffer_load_dwordx4 v[92:95], v247, s[40:43], s61 offen offset:320\n"
    "ds_write_b128 v242, v[136:139] offset:29760\n"
    "v_mfma_f32_16x16x32_f16 v[136:139], v[236:239], v[188:191], 0\n"
    "v_or_b32_e32 v197, v197, v205\n"
    "v_pk_mul_f16 v233, v196, v233\n"
    "v_smfmac_f32_32x32x32_bf16 v[112:127], a[248:251], a[232:239], v241\n"
    "v_cvt_pk_f16_f32 v184, v168, v169\n"
    "ds_write2_b32 v248, v41, v45 offset0:20 offset1:21\n"
    "v_lshlrev_b32_sdwa v185, 1, v170 dst_sel:DWORD dst_unused:UNUSED_PAD src0_sel:DWORD src1_sel:WORD_1\n"
    "v_lshlrev_b32_sdwa v201, 1, v171 dst_sel:DWORD dst_unused:UNUSED_PAD src0_sel:DWORD src1_sel:WORD_1\n"
    "v_mfma_f32_16x16x32_bf16 v[168:171], a[152:155], v[144:147], 0\n"
    "ds_read_u16 v185, v185\n"
    "ds_read_u16_d16_hi v201, v201\n"
    "ds_write2_b32 v248, v42, v46 offset0:40 offset1:41\n"
    "v_mfma_f32_16x16x32_bf16 v[168:171], a[200:203], v[148:151], v[168:171]\n"
    "v_lshlrev_b32_sdwa v186, 1, v172 dst_sel:DWORD dst_unused:UNUSED_PAD src0_sel:DWORD src1_sel:WORD_1\n"
    "v_lshlrev_b32_sdwa v202, 1, v173 dst_sel:DWORD dst_unused:UNUSED_PAD src0_sel:DWORD src1_sel:WORD_1\n"
    "v_lshlrev_b32_sdwa v187, 1, v174 dst_sel:DWORD dst_unused:UNUSED_PAD src0_sel:DWORD src1_sel:WORD_1\n"
    "v_lshlrev_b32_sdwa v203, 1, v175 dst_sel:DWORD dst_unused:UNUSED_PAD src0_sel:DWORD src1_sel:WORD_1\n"
    "ds_write2_b32 v248, v43, v47 offset0:60 offset1:61\n"
    "buffer_load_dwordx4 a[224:227], v245, s[40:43], s57 offen offset:128\n"
    "v_add_u32_e32 v248, 4080, v248\n"
    "v_exp_f16_e64 v234, v232\n"
    "v_mfma_f32_16x16x32_bf16 v[168:171], a[112:115], v[152:155], v[168:171]\n"
    "ds_read_u16 v186, v186\n"
    "ds_read_u16_d16_hi v202, v202\n"
    "v_mfma_f32_16x16x32_bf16 v[172:175], a[156:159], v[144:147], 0\n"
    "v_or_b32_e64 v198, v198, v206\n"
    "v_cvt_pk_f16_f32 v188, v176, v177\n"
    "v_lshlrev_b32_sdwa v189, 1, v178 dst_sel:DWORD dst_unused:UNUSED_PAD src0_sel:DWORD src1_sel:WORD_1\n"
    "v_exp_f16_e64 v235, v233\n"
    "v_mfma_f32_16x16x32_bf16 v[172:175], a[204:207], v[148:151], v[172:175]\n"
    "ds_read_b128 v[236:239], v244 offset:4096\n"
    "ds_read_u16 v187, v187\n"
    "v_lshlrev_b32_sdwa v205, 1, v179 dst_sel:DWORD dst_unused:UNUSED_PAD src0_sel:DWORD src1_sel:WORD_1\n"
    "v_lshlrev_b32_sdwa v190, 1, v180 dst_sel:DWORD dst_unused:UNUSED_PAD src0_sel:DWORD src1_sel:WORD_1\n"
    "v_lshlrev_b32_sdwa v206, 1, v181 dst_sel:DWORD dst_unused:UNUSED_PAD src0_sel:DWORD src1_sel:WORD_1\n"
    "v_lshlrev_b32_sdwa v191, 1, v182 dst_sel:DWORD dst_unused:UNUSED_PAD src0_sel:DWORD src1_sel:WORD_1\n"
    "s_mov_b64 exec, s[52:53]\n"
    "ds_write2_b32 v248, v56, v60 offset0:0 offset1:1\n"
    "s_mov_b64 exec, -1\n"
    "v_exp_f16_sdwa v234, v232 dst_sel:WORD_1 dst_unused:UNUSED_PRESERVE src0_sel:WORD_1\n"
    "v_mfma_f32_16x16x32_bf16 v[172:175], a[116:119], v[152:155], v[172:175]\n"
    "s_waitcnt lgkmcnt(6)\n"
    "ds_read_u16_d16_hi v203, v203\n"
    "v_or_b32_e32 v199, v199, v207\n"
    "v_mfma_f32_16x16x32_bf16 v[176:179], a[152:155], v[156:159], 0\n"
    "v_lshlrev_b32_sdwa v207, 1, v183 dst_sel:DWORD dst_unused:UNUSED_PAD src0_sel:DWORD src1_sel:WORD_1\n"
    "v_exp_f16_sdwa v235, v233 dst_sel:WORD_1 dst_unused:UNUSED_PRESERVE src0_sel:WORD_1\n"
    "v_mfma_f32_16x16x32_bf16 v[176:179], a[200:203], v[160:163], v[176:179]\n"
    "ds_read_b128 a[244:247], v244 offset:6144\n"
    "buffer_load_dwordx4 a[228:231], v245, s[40:43], s100 offen offset:128\n"
    "s_mov_b64 exec, s[52:53]\n"
    "ds_write2_b32 v248, v57, v61 offset0:20 offset1:21\n"
    "s_mov_b64 exec, -1\n"
    "v_mfma_f32_16x16x32_bf16 v[176:179], a[112:115], v[164:167], v[176:179]\n"
    "v_pk_fma_f16 v234, v234, s49, s49\n"
    "v_pk_fma_f16 v235, v235, s49, s49\n"
    "v_mfma_f32_16x16x32_bf16 v[180:183], a[156:159], v[156:159], 0\n"
    "ds_read_u16 v189, v189\n"
    "v_rcp_f16_e64 v232, v234\n"
    "v_mfma_f32_16x16x32_bf16 v[180:183], a[204:207], v[160:163], v[180:183]\n"
    "ds_read_u16_d16_hi v205, v205\n"
    "v_rcp_f16_e64 v233, v235\n"
    "v_mfma_f32_16x16x32_bf16 v[180:183], a[116:119], v[164:167], v[180:183]\n"
    "s_waitcnt lgkmcnt(7)\n"
    "ds_read_u16 v190, v190\n"
    "v_rcp_f16_sdwa v232, v234 dst_sel:WORD_1 dst_unused:UNUSED_PRESERVE src0_sel:WORD_1\n"
    "v_or_b32_e32 v185, v185, v201\n"
    "v_rcp_f16_sdwa v233, v235 dst_sel:WORD_1 dst_unused:UNUSED_PRESERVE src0_sel:WORD_1\n"
    "v_pk_mul_f16 v192, v192, v232\n"
    "s_mov_b64 exec, s[52:53]\n"
    "ds_write2_b32 v248, v58, v62 offset0:40 offset1:41\n"
    "s_mov_b64 exec, -1\n"
    "v_mfma_f32_16x16x32_f16 v[96:99], a[16:19], v[192:195], v[96:99]\n"
    "v_mov_b32_e64 v254, 0\n"
    "v_or_b32_e64 v186, v186, v202\n"
    "v_mfma_f32_16x16x32_f16 v[100:103], a[20:23], v[192:195], v[100:103]\n"
    "ds_read_u16_d16_hi v206, v206\n"
    "buffer_load_dword v200, v246, s[72:75], s62 offen offset:0\n"
    "v_mfma_f32_16x16x32_f16 v[112:115], a[24:27], v[192:195], v[112:115]\n"
    "s_waitcnt lgkmcnt(6)\n"
    "ds_read_u16 v191, v191\n"
    "v_pk_mul_f16 v196, v196, v233\n"
    "v_mfma_f32_16x16x32_f16 v[116:119], a[28:31], v[192:195], v[116:119]\n"
    "v_pk_fma_f16 v232, v184, v184, s48\n"
    "v_or_b32_e32 v187, v187, v203\n"
    "v_mfma_f32_16x16x32_f16 v[128:131], v[236:239], v[192:195], v[128:131]\n"
    "ds_read_u16_d16_hi v207, v207\n"
    "v_pk_fma_f16 v233, v188, v188, s48\n"
    "v_mfma_f32_16x16x32_f16 v[132:135], a[244:247], v[192:195], v[132:135]\n"
    "v_pk_mul_f16 v232, v184, v232\n"
    "v_pk_mul_f16 v233, v188, v233\n"
    "v_mfma_f32_16x16x32_f16 v[104:107], a[16:19], v[196:199], v[104:107]\n"
    "ds_read_b128 v[216:219], v251 offset:10752\n"
    "v_cvt_pk_f16_f32 v192, v168, v169\n"
    "v_mfma_f32_16x16x32_f16 v[108:111], a[20:23], v[196:199], v[108:111]\n"
    "ds_read_b128 v[220:223], v251 offset:11088\n"
    "v_lshlrev_b32_sdwa v193, 1, v170 dst_sel:DWORD dst_unused:UNUSED_PAD src0_sel:DWORD src1_sel:WORD_1\n"
    "v_mfma_f32_16x16x32_f16 v[120:123], a[24:27], v[196:199], v[120:123]\n"
    "ds_read_b128 a[240:243], v244 offset:34816\n"
    "v_lshlrev_b32_sdwa v201, 1, v171 dst_sel:DWORD dst_unused:UNUSED_PAD src0_sel:DWORD src1_sel:WORD_1\n"
    "v_mfma_f32_16x16x32_f16 v[124:127], a[28:31], v[196:199], v[124:127]\n"
    "s_waitcnt lgkmcnt(8)\n"
    "ds_read_u16 v193, v193\n"
    "v_or_b32_e32 v189, v189, v205\n"
    "v_mfma_f32_16x16x32_f16 v[136:139], v[236:239], v[196:199], v[136:139]\n"
    "v_lshlrev_b32_sdwa v194, 1, v172 dst_sel:DWORD dst_unused:UNUSED_PAD src0_sel:DWORD src1_sel:WORD_1\n"
    "v_lshlrev_b32_sdwa v202, 1, v173 dst_sel:DWORD dst_unused:UNUSED_PAD src0_sel:DWORD src1_sel:WORD_1\n"
    "v_mfma_f32_16x16x32_f16 v[140:143], a[244:247], v[196:199], v[140:143]\n"
    "ds_read_u16_d16_hi v201, v201\n"
    "v_lshlrev_b32_sdwa v195, 1, v174 dst_sel:DWORD dst_unused:UNUSED_PAD src0_sel:DWORD src1_sel:WORD_1\n"
    "v_mfma_f32_16x16x32_bf16 v[168:171], a[160:163], v[144:147], 0\n"
    "ds_read_u16 v194, v194\n"
    "v_lshlrev_b32_sdwa v203, 1, v175 dst_sel:DWORD dst_unused:UNUSED_PAD src0_sel:DWORD src1_sel:WORD_1\n"
    "v_mfma_f32_16x16x32_bf16 v[168:171], a[208:211], v[148:151], v[168:171]\n"
    "v_exp_f16_e64 v234, v232\n"
    "v_mfma_f32_16x16x32_bf16 v[168:171], a[120:123], v[152:155], v[168:171]\n"
    "ds_read_u16_d16_hi v202, v202\n"
    "s_waitcnt lgkmcnt(9)\n"
    "v_or_b32_e32 v190, v190, v206\n"
    "v_cvt_pk_f16_f32 v196, v176, v177\n"
    "v_lshlrev_b32_sdwa v197, 1, v178 dst_sel:DWORD dst_unused:UNUSED_PAD src0_sel:DWORD src1_sel:WORD_1\n"
    "v_exp_f16_e64 v235, v233\n"
    "v_mfma_f32_16x16x32_bf16 v[172:175], a[164:167], v[144:147], 0\n"
    "ds_read_b128 v[236:239], v244 offset:8192\n"
    "ds_read_u16 v195, v195\n"
    "v_mfma_f32_16x16x32_bf16 v[172:175], a[212:215], v[148:151], v[172:175]\n"
    "v_lshlrev_b32_sdwa v205, 1, v179 dst_sel:DWORD dst_unused:UNUSED_PAD src0_sel:DWORD src1_sel:WORD_1\n"
    "v_lshlrev_b32_sdwa v198, 1, v180 dst_sel:DWORD dst_unused:UNUSED_PAD src0_sel:DWORD src1_sel:WORD_1\n"
    "v_lshlrev_b32_sdwa v206, 1, v181 dst_sel:DWORD dst_unused:UNUSED_PAD src0_sel:DWORD src1_sel:WORD_1\n"
    "v_lshlrev_b32_sdwa v199, 1, v182 dst_sel:DWORD dst_unused:UNUSED_PAD src0_sel:DWORD src1_sel:WORD_1\n"
    "s_mov_b64 exec, s[52:53]\n"
    "ds_write2_b32 v248, v59, v63 offset0:60 offset1:61\n"
    "s_mov_b64 exec, -1\n"
    "v_xor_b32_e64 v248, 8, v248\n"
    "v_exp_f16_sdwa v234, v232 dst_sel:WORD_1 dst_unused:UNUSED_PRESERVE src0_sel:WORD_1\n"
    "s_waitcnt lgkmcnt(7)\n"
    "v_mfma_f32_16x16x32_bf16 v[172:175], a[240:243], v[152:155], v[172:175]\n"
    "ds_read_u16_d16_hi v203, v203\n"
    "v_or_b32_e32 v191, v191, v207\n"
    "v_mfma_f32_16x16x32_bf16 v[176:179], a[160:163], v[156:159], 0\n"
    "ds_read_b128 a[244:247], v244 offset:10240\n"
    "v_lshlrev_b32_sdwa v207, 1, v183 dst_sel:DWORD dst_unused:UNUSED_PAD src0_sel:DWORD src1_sel:WORD_1\n"
    "v_exp_f16_sdwa v235, v233 dst_sel:WORD_1 dst_unused:UNUSED_PRESERVE src0_sel:WORD_1\n"
    "v_mfma_f32_16x16x32_bf16 v[176:179], a[208:211], v[160:163], v[176:179]\n"
    "ds_read_u16 v197, v197\n"
    "v_pk_fma_f16 v234, v234, s49, s49\n"
    "v_mfma_f32_16x16x32_bf16 v[176:179], a[120:123], v[164:167], v[176:179]\n"
    "s_waitcnt lgkmcnt(8)\n"
    "v_pk_fma_f16 v235, v235, s49, s49\n"
    "v_rcp_f16_e32 v232, v234\n"
    "v_mfma_f32_16x16x32_bf16 v[180:183], a[164:167], v[156:159], 0\n"
    "ds_read_u16_d16_hi v205, v205\n"
    "v_rcp_f16_e64 v233, v235\n"
    "v_mfma_f32_16x16x32_bf16 v[180:183], a[212:215], v[160:163], v[180:183]\n"
    "ds_read_u16 v198, v198\n"
    "v_rcp_f16_sdwa v232, v234 dst_sel:WORD_1 dst_unused:UNUSED_PRESERVE src0_sel:WORD_1\n"
    "v_mfma_f32_16x16x32_bf16 v[180:183], a[240:243], v[164:167], v[180:183]\n"
    "s_waitcnt lgkmcnt(7)\n"
    "ds_read_u16_d16_hi v206, v206\n"
    "v_or_b32_e32 v193, v193, v201\n"
    "v_rcp_f16_sdwa v233, v235 dst_sel:WORD_1 dst_unused:UNUSED_PRESERVE src0_sel:WORD_1\n"
    "v_pk_mul_f16 v184, v184, v232\n"
    "v_or_b32_e64 v194, v194, v202\n"
    "v_pk_mul_f16 v188, v188, v233\n"
    "v_mfma_f32_16x16x32_f16 v[112:115], a[40:43], v[184:187], v[112:115]\n"
    "ds_read_u16 v199, v199\n"
    "buffer_load_dword v200, v246, s[72:75], s63 offen offset:0\n"
    "v_mfma_f32_16x16x32_f16 v[116:119], a[44:47], v[184:187], v[116:119]\n"
    "v_pk_fma_f16 v232, v192, v192, s48\n"
    "v_pk_fma_f16 v233, v196, v196, s48\n"
    "v_mfma_f32_16x16x32_f16 v[128:131], v[236:239], v[184:187], v[128:131]\n"
    "ds_read_u16_d16_hi v207, v207\n"
    "v_pk_mul_f16 v232, v192, v232\n"
    "v_mfma_f32_16x16x32_f16 v[120:123], a[40:43], v[188:191], v[120:123]\n"
    "ds_read_b128 a[240:243], v244 offset:38912\n"
    "v_pk_mul_f16 v233, v196, v233\n"
    "s_waitcnt lgkmcnt(7)\n"
    "v_mfma_f32_16x16x32_f16 v[132:135], a[244:247], v[184:187], v[132:135]\n"
    "s_mov_b64 exec, s[52:53]\n"
    "ds_write2_b32 v248, v48, v52 offset0:0 offset1:1\n"
    "s_mov_b64 exec, -1\n"
    "v_mfma_f32_16x16x32_f16 v[124:127], a[44:47], v[188:191], v[124:127]\n"
    "v_or_b32_e32 v195, v195, v203\n"
    "v_lshlrev_b32_sdwa v201, 1, v171 dst_sel:DWORD dst_unused:UNUSED_PAD src0_sel:DWORD src1_sel:WORD_1\n"
    "v_mfma_f32_16x16x32_f16 v[136:139], v[236:239], v[188:191], v[136:139]\n"
    "ds_read_u16_d16_hi v201, v201\n"
    "v_lshlrev_b32_sdwa v202, 1, v173 dst_sel:DWORD dst_unused:UNUSED_PAD src0_sel:DWORD src1_sel:WORD_1\n"
    "v_mfma_f32_16x16x32_f16 v[140:143], a[244:247], v[188:191], v[140:143]\n"
    "ds_read_u16_d16_hi v202, v202\n"
    "v_lshlrev_b32_sdwa v203, 1, v175 dst_sel:DWORD dst_unused:UNUSED_PAD src0_sel:DWORD src1_sel:WORD_1\n"
    "s_waitcnt lgkmcnt(8)\n"
    "v_or_b32_e32 v197, v197, v205\n"
    "v_exp_f16_e64 v234, v232\n"
    "v_exp_f16_e32 v235, v233\n"
    "s_mov_b64 exec, s[52:53]\n"
    "ds_write2_b32 v248, v49, v53 offset0:20 offset1:21\n"
    "s_mov_b64 exec, -1\n"
    "v_lshlrev_b32_sdwa v205, 1, v179 dst_sel:DWORD dst_unused:UNUSED_PAD src0_sel:DWORD src1_sel:WORD_1\n"
    "v_exp_f16_sdwa v234, v232 dst_sel:WORD_1 dst_unused:UNUSED_PRESERVE src0_sel:WORD_1\n"
    "s_mov_b64 exec, s[52:53]\n"
    "ds_write2_b32 v248, v50, v54 offset0:40 offset1:41\n"
    "s_mov_b64 exec, -1\n"
    "s_waitcnt lgkmcnt(8)\n"
    "v_or_b32_e64 v198, v198, v206\n"
    "v_lshlrev_b32_sdwa v206, 1, v181 dst_sel:DWORD dst_unused:UNUSED_PAD src0_sel:DWORD src1_sel:WORD_1\n"
    "s_mov_b64 exec, s[52:53]\n"
    "ds_write2_b32 v248, v51, v55 offset0:60 offset1:61\n"
    "s_mov_b64 exec, -1\n"
    "v_mfma_f32_32x32x1_2b_f32 v[32:63], v254, v254, 0\n"
    "s_waitcnt lgkmcnt(6)\n"
    "v_subrev_u32_e32 v248, 4080, v248\n"
    "v_or_b32_e32 v199, v199, v207\n"
    "v_lshlrev_b32_sdwa v207, 1, v183 dst_sel:DWORD dst_unused:UNUSED_PAD src0_sel:DWORD src1_sel:WORD_1\n"
    "v_exp_f16_sdwa v235, v233 dst_sel:WORD_1 dst_unused:UNUSED_PRESERVE src0_sel:WORD_1\n"
    "v_pk_fma_f16 v234, v234, s49, s49\n"
    "v_pk_fma_f16 v235, v235, s49, s49\n"
    "v_rcp_f16_e64 v232, v234\n"
    "v_rcp_f16_e64 v233, v235\n"
    "v_rcp_f16_sdwa v232, v234 dst_sel:WORD_1 dst_unused:UNUSED_PRESERVE src0_sel:WORD_1\n"
    "v_rcp_f16_sdwa v233, v235 dst_sel:WORD_1 dst_unused:UNUSED_PRESERVE src0_sel:WORD_1\n"
    "v_pk_mul_f16 v192, v192, v232\n"
    "v_pk_mul_f16 v196, v196, v233\n"
    "v_mov_b32_e64 v254, 0\n"
    "ds_read_b128 v[236:239], v244 offset:12288\n"
    "ds_read_u16_d16_hi v203, v203\n"
    "ds_read_b128 a[244:247], v244 offset:14336\n"
    "ds_read_u16_d16_hi v205, v205\n"
    "ds_read_u16_d16_hi v206, v206\n"
    "ds_read_u16_d16_hi v207, v207\n"
    "s_waitcnt lgkmcnt(6)\n"
    "s_barrier\n"
    "ds_read_b128 a[232:235], v243 offset:24640\n"
    "ds_read_b128 a[236:239], v243 offset:25664\n"
    "s_waitcnt lgkmcnt(7)\n"
    "v_mfma_f32_16x16x32_f16 v[128:131], v[236:239], v[192:195], v[128:131]\n"
    "ds_read_b128 v[208:211], v250 offset:0\n"
    "ds_read_b128 v[212:215], v250 offset:320\n"
    "v_mfma_f32_16x16x32_f16 v[136:139], v[236:239], v[196:199], v[136:139]\n"
    "ds_read_b128 v[236:239], v244 offset:16384\n"
    "s_waitcnt lgkmcnt(8)\n"
    "v_mfma_f32_16x16x32_f16 v[132:135], a[244:247], v[192:195], v[132:135]\n"
    "v_mfma_f32_16x16x32_f16 v[140:143], a[244:247], v[196:199], v[140:143]\n"
    "ds_read_b128 a[244:247], v244 offset:18432\n"
    "buffer_load_dword v200, v246, s[72:75], s62 offen offset:128\n"
    "s_waitcnt lgkmcnt(4)\n"
    "v_smfmac_f32_32x32x32_bf16 v[96:111], a[248:251], a[232:239], v241\n"
    "ds_read_b128 a[232:235], v243 offset:26688\n"
    "ds_read_b128 a[236:239], v243 offset:27712\n"
    "s_waitcnt lgkmcnt(4)\n"
    "v_smfmac_f32_16x16x64_bf16 v[32:35], v[216:219], v[208:215], v240\n"
    "ds_read_b128 v[216:219], v252 offset:10752\n"
    "v_smfmac_f32_16x16x64_bf16 v[36:39], v[220:223], v[208:215], v240 abid:1\n"
    "ds_read_b128 v[208:211], v250 offset:640\n"
    "ds_read_b128 v[212:215], v250 offset:960\n"
    "ds_read_b128 v[220:223], v252 offset:11088\n"
    "s_nop 0\n"
    "s_nop 0\n"
    "v_mfma_f32_16x16x32_f16 v[96:99], a[32:35], v[184:187], v[96:99]\n"
    "v_mfma_f32_16x16x32_f16 v[100:103], a[36:39], v[184:187], v[100:103]\n"
    "v_cvt_pk_f16_f32 v184, v168, v169\n"
    "v_lshlrev_b32_sdwa v185, 1, v170 dst_sel:DWORD dst_unused:UNUSED_PAD src0_sel:DWORD src1_sel:WORD_1\n"
    "v_mfma_f32_16x16x32_f16 v[104:107], a[32:35], v[188:191], v[104:107]\n"
    "ds_read_u16 v185, v185\n"
    "v_lshlrev_b32_sdwa v186, 1, v172 dst_sel:DWORD dst_unused:UNUSED_PAD src0_sel:DWORD src1_sel:WORD_1\n"
    "v_mfma_f32_16x16x32_f16 v[108:111], a[36:39], v[188:191], v[108:111]\n"
    "ds_read_u16 v186, v186\n"
    "v_lshlrev_b32_sdwa v187, 1, v174 dst_sel:DWORD dst_unused:UNUSED_PAD src0_sel:DWORD src1_sel:WORD_1\n"
    "v_mfma_f32_16x16x32_bf16 v[168:171], a[168:171], v[144:147], 0\n"
    "ds_read_u16 v187, v187\n"
    "v_cvt_pk_f16_f32 v188, v176, v177\n"
    "s_waitcnt lgkmcnt(7)\n"
    "s_nop 0\n"
    "v_smfmac_f32_32x32x32_bf16 v[112:127], a[248:251], a[232:239], v241\n"
    "ds_read_b128 a[232:235], v243 offset:28736\n"
    "ds_read_b128 a[236:239], v243 offset:29760\n"
    "v_lshlrev_b32_sdwa v189, 1, v178 dst_sel:DWORD dst_unused:UNUSED_PAD src0_sel:DWORD src1_sel:WORD_1\n"
    "v_lshlrev_b32_sdwa v190, 1, v180 dst_sel:DWORD dst_unused:UNUSED_PAD src0_sel:DWORD src1_sel:WORD_1\n"
    "v_lshlrev_b32_sdwa v191, 1, v182 dst_sel:DWORD dst_unused:UNUSED_PAD src0_sel:DWORD src1_sel:WORD_1\n"
    "v_mfma_f32_16x16x32_bf16 v[168:171], a[216:219], v[148:151], v[168:171]\n"
    "ds_read_u16 v189, v189\n"
    "ds_read_u16 v190, v190\n"
    "s_waitcnt lgkmcnt(8)\n"
    "v_smfmac_f32_16x16x64_bf16 v[32:35], v[216:219], v[208:215], v240\n"
    "s_waitcnt lgkmcnt(7)\n"
    "ds_read_b128 v[216:219], v251 offset:11424\n"
    "v_smfmac_f32_16x16x64_bf16 v[36:39], v[220:223], v[208:215], v240 abid:1\n"
    "ds_read_b128 v[208:211], v250 offset:80\n"
    "v_mfma_f32_16x16x32_bf16 v[168:171], a[124:127], v[152:155], v[168:171]\n"
    "v_pk_fma_f16 v232, v184, v184, s48\n"
    "v_pk_fma_f16 v233, v188, v188, s48\n"
    "v_mfma_f32_16x16x32_bf16 v[172:175], a[172:175], v[144:147], 0\n"
    "ds_read_b128 v[212:215], v250 offset:400\n"
    "ds_read_b128 v[220:223], v251 offset:11760\n"
    "s_waitcnt lgkmcnt(10)\n"
    "v_or_b32_e32 v185, v185, v201\n"
    "v_pk_mul_f16 v232, v184, v232\n"
    "v_pk_add_f32 v[224:225], v[224:225], v[32:33]\n"
    "v_pk_add_f32 v[226:227], v[226:227], v[34:35]\n"
    "v_pk_fma_f32 v[228:229], v[32:33], v[32:33], v[228:229]\n"
    "v_pk_fma_f32 v[230:231], v[34:35], v[34:35], v[230:231]\n"
    "v_pk_add_f32 v[224:225], v[224:225], v[36:37]\n"
    "v_pk_add_f32 v[226:227], v[226:227], v[38:39]\n"
    "v_pk_fma_f32 v[228:229], v[36:37], v[36:37], v[228:229]\n"
    "v_pk_fma_f32 v[230:231], v[38:39], v[38:39], v[230:231]\n"
    "v_mfma_f32_16x16x32_bf16 v[172:175], a[220:223], v[148:151], v[172:175]\n"
    "s_waitcnt lgkmcnt(9)\n"
    "ds_read_b128 v[144:147], v242 offset:0\n"
    "ds_read_b128 v[148:151], v242 offset:1024\n"
    "ds_read_u16 v191, v191\n"
    "v_mfma_f32_16x16x32_bf16 v[172:175], a[240:243], v[152:155], v[172:175]\n"
    "s_waitcnt lgkmcnt(9)\n"
    "ds_read_b128 v[152:155], v242 offset:2048\n"
    "v_or_b32_e64 v186, v186, v202\n"
    "v_mfma_f32_16x16x32_bf16 v[176:179], a[168:171], v[156:159], 0\n"
    "v_or_b32_e64 v187, v187, v203\n"
    "v_pk_mul_f16 v233, v188, v233\n"
    "v_mfma_f32_16x16x32_bf16 v[176:179], a[216:219], v[160:163], v[176:179]\n"
    "v_lshlrev_b32_sdwa v201, 1, v171 dst_sel:DWORD dst_unused:UNUSED_PAD src0_sel:DWORD src1_sel:WORD_1\n"
    "v_lshlrev_b32_sdwa v202, 1, v173 dst_sel:DWORD dst_unused:UNUSED_PAD src0_sel:DWORD src1_sel:WORD_1\n"
    "v_mfma_f32_16x16x32_bf16 v[176:179], a[124:127], v[164:167], v[176:179]\n"
    "s_waitcnt lgkmcnt(9)\n"
    "ds_read_u16_d16_hi v201, v201\n"
    "ds_read_u16_d16_hi v202, v202\n"
    "v_mfma_f32_16x16x32_bf16 v[180:183], a[172:175], v[156:159], 0\n"
    "v_or_b32_e32 v189, v189, v205\n"
    "v_lshlrev_b32_sdwa v203, 1, v175 dst_sel:DWORD dst_unused:UNUSED_PAD src0_sel:DWORD src1_sel:WORD_1\n"
    "v_mfma_f32_16x16x32_bf16 v[180:183], a[220:223], v[160:163], v[180:183]\n"
    "s_waitcnt lgkmcnt(9)\n"
    "ds_read_b128 v[156:159], v242 offset:3072\n"
    "v_exp_f16_e32 v234, v232\n"
    "v_mfma_f32_16x16x32_bf16 v[180:183], a[240:243], v[164:167], v[180:183]\n"
    "s_waitcnt lgkmcnt(8)\n"
    "ds_read_b128 v[160:163], v242 offset:4096\n"
    "ds_read_b128 v[164:167], v242 offset:5120\n"
    "v_smfmac_f32_16x16x64_bf16 v[40:43], v[216:219], v[208:215], v240\n"
    "s_waitcnt lgkmcnt(9)\n"
    "ds_read_b128 v[216:219], v252 offset:11424\n"
    "v_smfmac_f32_16x16x64_bf16 v[44:47], v[220:223], v[208:215], v240 abid:1\n"
    "ds_read_b128 v[208:211], v250 offset:720\n"
    "v_mfma_f32_16x16x32_f16 v[96:99], a[48:51], v[192:195], v[96:99]\n"
    "v_or_b32_e64 v190, v190, v206\n"
    "v_exp_f16_e64 v235, v233\n"
    "v_mfma_f32_16x16x32_f16 v[100:103], a[52:55], v[192:195], v[100:103]\n"
    "ds_read_b128 v[212:215], v250 offset:1040\n"
    "ds_read_b128 v[220:223], v252 offset:11760\n"
    "v_mfma_f32_16x16x32_f16 v[112:115], a[56:59], v[192:195], v[112:115]\n"
    "ds_read_b128 a[240:243], v244 offset:32768\n"
    "v_lshlrev_b32_sdwa v205, 1, v179 dst_sel:DWORD dst_unused:UNUSED_PAD src0_sel:DWORD src1_sel:WORD_1\n"
    "v_mfma_f32_16x16x32_f16 v[116:119], a[60:63], v[192:195], v[116:119]\n"
    "s_waitcnt lgkmcnt(10)\n"
    "s_nop 0\n"
    "ds_read_u16_d16_hi v203, v203\n"
    "v_cvt_pk_f16_f32 v192, v168, v169\n"
    "v_mfma_f32_16x16x32_f16 v[104:107], a[48:51], v[196:199], v[104:107]\n"
    "v_lshlrev_b32_sdwa v193, 1, v170 dst_sel:DWORD dst_unused:UNUSED_PAD src0_sel:DWORD src1_sel:WORD_1\n"
    "v_lshlrev_b32_sdwa v194, 1, v172 dst_sel:DWORD dst_unused:UNUSED_PAD src0_sel:DWORD src1_sel:WORD_1\n"
    "v_mfma_f32_16x16x32_f16 v[108:111], a[52:55], v[196:199], v[108:111]\n"
    "ds_read_u16 v193, v193\n"
    "v_lshlrev_b32_sdwa v195, 1, v174 dst_sel:DWORD dst_unused:UNUSED_PAD src0_sel:DWORD src1_sel:WORD_1\n"
    "v_mfma_f32_16x16x32_f16 v[120:123], a[56:59], v[196:199], v[120:123]\n"
    "v_lshlrev_b32_sdwa v206, 1, v181 dst_sel:DWORD dst_unused:UNUSED_PAD src0_sel:DWORD src1_sel:WORD_1\n"
    "v_exp_f16_sdwa v234, v232 dst_sel:WORD_1 dst_unused:UNUSED_PRESERVE src0_sel:WORD_1\n"
    "v_mfma_f32_16x16x32_f16 v[124:127], a[60:63], v[196:199], v[124:127]\n"
    "ds_read_u16 v194, v194\n"
    "v_cvt_pk_f16_f32 v196, v176, v177\n"
    "v_smfmac_f32_32x32x32_bf16 v[128:143], a[248:251], a[232:239], v241\n"
    "ds_read_u16 v195, v195\n"
    "v_lshlrev_b32_sdwa v197, 1, v178 dst_sel:DWORD dst_unused:UNUSED_PAD src0_sel:DWORD src1_sel:WORD_1\n"
    "v_lshlrev_b32_sdwa v198, 1, v180 dst_sel:DWORD dst_unused:UNUSED_PAD src0_sel:DWORD src1_sel:WORD_1\n"
    "v_lshlrev_b32_sdwa v199, 1, v182 dst_sel:DWORD dst_unused:UNUSED_PAD src0_sel:DWORD src1_sel:WORD_1\n"
    "v_or_b32_e64 v191, v191, v207\n"
    "v_mfma_f32_16x16x32_bf16 v[168:171], a[128:131], v[144:147], 0\n"
    "s_waitcnt lgkmcnt(8)\n"
    "ds_read_u16 v197, v197\n"
    "v_lshlrev_b32_sdwa v207, 1, v183 dst_sel:DWORD dst_unused:UNUSED_PAD src0_sel:DWORD src1_sel:WORD_1\n"
    "v_mfma_f32_16x16x32_bf16 v[168:171], a[176:179], v[148:151], v[168:171]\n"
    "v_exp_f16_sdwa v235, v233 dst_sel:WORD_1 dst_unused:UNUSED_PRESERVE src0_sel:WORD_1\n"
    "v_mfma_f32_16x16x32_bf16 v[168:171], a[96:99], v[152:155], v[168:171]\n"
    "s_waitcnt lgkmcnt(7)\n"
    "ds_read_u16_d16_hi v205, v205\n"
    "v_pk_fma_f16 v234, v234, s49, s49\n"
    "v_smfmac_f32_16x16x64_bf16 v[40:43], v[216:219], v[208:215], v240\n"
    "s_waitcnt lgkmcnt(7)\n"
    "ds_read_b128 v[216:219], v251 offset:12096\n"
    "v_smfmac_f32_16x16x64_bf16 v[44:47], v[220:223], v[208:215], v240 abid:1\n"
    "ds_read_b128 v[208:211], v250 offset:160\n"
    "v_mfma_f32_16x16x32_bf16 v[172:175], a[132:135], v[144:147], 0\n"
    "v_pk_fma_f16 v235, v235, s49, s49\n"
    "v_rcp_f16_e32 v232, v234\n"
    "v_mfma_f32_16x16x32_bf16 v[172:175], a[180:183], v[148:151], v[172:175]\n"
    "s_waitcnt lgkmcnt(8)\n"
    "ds_read_b128 v[212:215], v250 offset:480\n"
    "ds_read_b128 v[220:223], v251 offset:12432\n"
    "buffer_load_dword v200, v246, s[72:75], s63 offen offset:128\n"
    "v_rcp_f16_e32 v233, v235\n"
    "v_pk_add_f32 v[224:225], v[224:225], v[40:41]\n"
    "v_pk_add_f32 v[226:227], v[226:227], v[42:43]\n"
    "v_pk_fma_f32 v[228:229], v[40:41], v[40:41], v[228:229]\n"
    "v_pk_fma_f32 v[230:231], v[42:43], v[42:43], v[230:231]\n"
    "v_pk_add_f32 v[224:225], v[224:225], v[44:45]\n"
    "v_pk_add_f32 v[226:227], v[226:227], v[46:47]\n"
    "v_pk_fma_f32 v[228:229], v[44:45], v[44:45], v[228:229]\n"
    "v_pk_fma_f32 v[230:231], v[46:47], v[46:47], v[230:231]\n"
    "v_mfma_f32_16x16x32_bf16 v[172:175], a[240:243], v[152:155], v[172:175]\n"
    "s_waitcnt lgkmcnt(8)\n"
    "ds_read_u16 v198, v198\n"
    "ds_read_u16_d16_hi v206, v206\n"
    "v_mfma_f32_16x16x32_bf16 v[176:179], a[128:131], v[156:159], 0\n"
    "s_waitcnt lgkmcnt(9)\n"
    "ds_read_u16 v199, v199\n"
    "v_rcp_f16_sdwa v232, v234 dst_sel:WORD_1 dst_unused:UNUSED_PRESERVE src0_sel:WORD_1\n"
    "v_mfma_f32_16x16x32_bf16 v[176:179], a[176:179], v[160:163], v[176:179]\n"
    "s_waitcnt lgkmcnt(9)\n"
    "v_or_b32_e32 v193, v193, v201\n"
    "v_rcp_f16_sdwa v233, v235 dst_sel:WORD_1 dst_unused:UNUSED_PRESERVE src0_sel:WORD_1\n"
    "v_mfma_f32_16x16x32_bf16 v[176:179], a[96:99], v[164:167], v[176:179]\n"
    "ds_read_u16_d16_hi v207, v207\n"
    "v_pk_mul_f16 v184, v184, v232\n"
    "v_mfma_f32_16x16x32_bf16 v[180:183], a[132:135], v[156:159], 0\n"
    "v_or_b32_e64 v194, v194, v202\n"
    "v_pk_mul_f16 v188, v188, v233\n"
    "v_mfma_f32_16x16x32_bf16 v[180:183], a[180:183], v[160:163], v[180:183]\n"
    "s_waitcnt lgkmcnt(7)\n"
    "v_pk_fma_f16 v232, v192, v192, s48\n"
    "v_or_b32_e32 v195, v195, v203\n"
    "v_mfma_f32_16x16x32_bf16 v[180:183], a[240:243], v[164:167], v[180:183]\n"
    "ds_read_b128 a[240:243], v244 offset:36864\n"
    "v_pk_fma_f16 v233, v196, v196, s48\n"
    "v_mfma_f32_16x16x32_f16 v[96:99], a[64:67], v[184:187], v[96:99]\n"
    "s_waitcnt lgkmcnt(6)\n"
    "v_pk_mul_f16 v232, v192, v232\n"
    "v_or_b32_e32 v197, v197, v205\n"
    "v_smfmac_f32_16x16x64_bf16 v[48:51], v[216:219], v[208:215], v240\n"
    "s_waitcnt lgkmcnt(5)\n"
    "s_nop 0\n"
    "ds_read_b128 v[216:219], v252 offset:12096\n"
    "v_smfmac_f32_16x16x64_bf16 v[52:55], v[220:223], v[208:215], v240 abid:1\n"
    "ds_read_b128 v[208:211], v250 offset:800\n"
    "ds_read_b128 v[212:215], v250 offset:1120\n"
    "ds_read_b128 v[220:223], v252 offset:12432\n"
    "v_mfma_f32_16x16x32_f16 v[100:103], a[68:71], v[184:187], v[100:103]\n"
    "v_pk_mul_f16 v233, v196, v233\n"
    "v_lshlrev_b32_sdwa v201, 1, v171 dst_sel:DWORD dst_unused:UNUSED_PAD src0_sel:DWORD src1_sel:WORD_1\n"
    "v_mfma_f32_16x16x32_f16 v[112:115], a[72:75], v[184:187], v[112:115]\n"
    "ds_read_u16_d16_hi v201, v201\n"
    "v_lshlrev_b32_sdwa v202, 1, v173 dst_sel:DWORD dst_unused:UNUSED_PAD src0_sel:DWORD src1_sel:WORD_1\n"
    "v_mfma_f32_16x16x32_f16 v[116:119], a[76:79], v[184:187], v[116:119]\n"
    "s_waitcnt lgkmcnt(8)\n"
    "ds_read_u16_d16_hi v202, v202\n"
    "v_lshlrev_b32_sdwa v203, 1, v175 dst_sel:DWORD dst_unused:UNUSED_PAD src0_sel:DWORD src1_sel:WORD_1\n"
    "v_mfma_f32_16x16x32_f16 v[128:131], v[236:239], v[184:187], v[128:131]\n"
    "v_exp_f16_e32 v234, v232\n"
    "v_mfma_f32_16x16x32_f16 v[132:135], a[244:247], v[184:187], v[132:135]\n"
    "ds_read_u16_d16_hi v203, v203\n"
    "v_cvt_pk_f16_f32 v184, v168, v169\n"
    "v_mfma_f32_16x16x32_f16 v[104:107], a[64:67], v[188:191], v[104:107]\n"
    "v_lshlrev_b32_sdwa v185, 1, v170 dst_sel:DWORD dst_unused:UNUSED_PAD src0_sel:DWORD src1_sel:WORD_1\n"
    "v_lshlrev_b32_sdwa v186, 1, v172 dst_sel:DWORD dst_unused:UNUSED_PAD src0_sel:DWORD src1_sel:WORD_1\n"
    "v_mfma_f32_16x16x32_f16 v[108:111], a[68:71], v[188:191], v[108:111]\n"
    "s_waitcnt lgkmcnt(7)\n"
    "ds_read_u16 v185, v185\n"
    "ds_read_u16 v186, v186\n"
    "v_mfma_f32_16x16x32_f16 v[120:123], a[72:75], v[188:191], v[120:123]\n"
    "v_lshlrev_b32_sdwa v187, 1, v174 dst_sel:DWORD dst_unused:UNUSED_PAD src0_sel:DWORD src1_sel:WORD_1\n"
    "v_or_b32_e32 v198, v198, v206\n"
    "v_mfma_f32_16x16x32_f16 v[124:127], a[76:79], v[188:191], v[124:127]\n"
    "ds_read_u16 v187, v187\n"
    "v_exp_f16_e32 v235, v233\n"
    "s_waitcnt lgkmcnt(7)\n"
    "v_smfmac_f32_16x16x64_bf16 v[48:51], v[216:219], v[208:215], v240\n"
    "s_waitcnt lgkmcnt(6)\n"
    "s_nop 0\n"
    "ds_read_b128 v[216:219], v251 offset:12768\n"
    "v_smfmac_f32_16x16x64_bf16 v[52:55], v[220:223], v[208:215], v240 abid:1\n"
    "ds_read_b128 v[208:211], v250 offset:240\n"
    "ds_read_b128 v[212:215], v250 offset:560\n"
    "v_mfma_f32_16x16x32_f16 v[136:139], v[236:239], v[188:191], v[136:139]\n"
    "ds_read_b128 v[220:223], v251 offset:13104\n"
    "v_lshlrev_b32_sdwa v205, 1, v179 dst_sel:DWORD dst_unused:UNUSED_PAD src0_sel:DWORD src1_sel:WORD_1\n"
    "v_lshlrev_b32_sdwa v206, 1, v181 dst_sel:DWORD dst_unused:UNUSED_PAD src0_sel:DWORD src1_sel:WORD_1\n"
    "v_exp_f16_sdwa v234, v232 dst_sel:WORD_1 dst_unused:UNUSED_PRESERVE src0_sel:WORD_1\n"
    "v_pk_add_f32 v[224:225], v[224:225], v[48:49]\n"
    "v_pk_add_f32 v[226:227], v[226:227], v[50:51]\n"
    "v_pk_fma_f32 v[228:229], v[48:49], v[48:49], v[228:229]\n"
    "v_pk_fma_f32 v[230:231], v[50:51], v[50:51], v[230:231]\n"
    "v_pk_add_f32 v[224:225], v[224:225], v[52:53]\n"
    "v_pk_add_f32 v[226:227], v[226:227], v[54:55]\n"
    "v_pk_fma_f32 v[228:229], v[52:53], v[52:53], v[228:229]\n"
    "v_pk_fma_f32 v[230:231], v[54:55], v[54:55], v[230:231]\n"
    "v_mfma_f32_16x16x32_f16 v[140:143], a[244:247], v[188:191], v[140:143]\n"
    "ds_read_b128 v[236:239], v244 offset:20480\n"
    "ds_read_b128 a[244:247], v244 offset:22528\n"
    "ds_read_u16_d16_hi v205, v205\n"
    "v_mfma_f32_16x16x32_bf16 v[168:171], a[136:139], v[144:147], 0\n"
    "v_cvt_pk_f16_f32 v188, v176, v177\n"
    "v_lshlrev_b32_sdwa v189, 1, v178 dst_sel:DWORD dst_unused:UNUSED_PAD src0_sel:DWORD src1_sel:WORD_1\n"
    "v_mfma_f32_16x16x32_bf16 v[168:171], a[184:187], v[148:151], v[168:171]\n"
    "s_waitcnt lgkmcnt(9)\n"
    "ds_read_u16 v189, v189\n"
    "v_lshlrev_b32_sdwa v190, 1, v180 dst_sel:DWORD dst_unused:UNUSED_PAD src0_sel:DWORD src1_sel:WORD_1\n"
    "v_mfma_f32_16x16x32_bf16 v[168:171], a[100:103], v[152:155], v[168:171]\n"
    "v_lshlrev_b32_sdwa v191, 1, v182 dst_sel:DWORD dst_unused:UNUSED_PAD src0_sel:DWORD src1_sel:WORD_1\n"
    "v_or_b32_e32 v199, v199, v207\n"
    "v_mfma_f32_16x16x32_bf16 v[172:175], a[140:143], v[144:147], 0\n"
    "s_waitcnt lgkmcnt(9)\n"
    "v_lshlrev_b32_sdwa v207, 1, v183 dst_sel:DWORD dst_unused:UNUSED_PAD src0_sel:DWORD src1_sel:WORD_1\n"
    "v_exp_f16_sdwa v235, v233 dst_sel:WORD_1 dst_unused:UNUSED_PRESERVE src0_sel:WORD_1\n"
    "v_mfma_f32_16x16x32_bf16 v[172:175], a[188:191], v[148:151], v[172:175]\n"
    "s_waitcnt lgkmcnt(7)\n"
    "ds_read_u16 v190, v190\n"
    "v_pk_fma_f16 v234, v234, s49, s49\n"
    "v_mfma_f32_16x16x32_bf16 v[172:175], a[240:243], v[152:155], v[172:175]\n"
    "ds_read_u16_d16_hi v206, v206\n"
    "v_pk_fma_f16 v235, v235, s49, s49\n"
    "v_mfma_f32_16x16x32_bf16 v[176:179], a[136:139], v[156:159], 0\n"
    "s_waitcnt lgkmcnt(7)\n"
    "ds_read_u16 v191, v191\n"
    "buffer_load_dwordx4 a[232:235], v245, s[40:43], s58 offen offset:64\n"
    "v_smfmac_f32_16x16x64_bf16 v[56:59], v[216:219], v[208:215], v240\n"
    "s_waitcnt lgkmcnt(7)\n"
    "ds_read_b128 v[216:219], v252 offset:12768\n"
    "v_smfmac_f32_16x16x64_bf16 v[60:63], v[220:223], v[208:215], v240 abid:1\n"
    "ds_read_b128 v[208:211], v250 offset:880\n"
    "ds_read_b128 v[212:215], v250 offset:1200\n"
    "v_mfma_f32_16x16x32_bf16 v[176:179], a[184:187], v[160:163], v[176:179]\n"
    "v_rcp_f16_e64 v232, v234\n"
    "v_mfma_f32_16x16x32_bf16 v[176:179], a[100:103], v[164:167], v[176:179]\n"
    "ds_read_b128 v[220:223], v252 offset:13104\n"
    "ds_read_u16_d16_hi v207, v207\n"
    "s_waitcnt vmcnt(5)\n"
    "v_smfmac_f32_32x32x32_bf16 v[128:143], a[248:251], a[224:231], v241\n"
    "v_rcp_f16_e32 v233, v235\n"
    "v_rcp_f16_sdwa v232, v234 dst_sel:WORD_1 dst_unused:UNUSED_PRESERVE src0_sel:WORD_1\n"
    "v_or_b32_e64 v185, v185, v201\n"
    "v_mfma_f32_16x16x32_bf16 v[180:183], a[140:143], v[156:159], 0\n"
    "s_waitcnt lgkmcnt(8)\n"
    "v_rcp_f16_sdwa v233, v235 dst_sel:WORD_1 dst_unused:UNUSED_PRESERVE src0_sel:WORD_1\n"
    "v_mfma_f32_16x16x32_bf16 v[180:183], a[188:191], v[160:163], v[180:183]\n"
    "v_pk_mul_f16 v192, v192, v232\n"
    "v_or_b32_e32 v186, v186, v202\n"
    "v_mfma_f32_16x16x32_bf16 v[180:183], a[240:243], v[164:167], v[180:183]\n"
    "v_pk_mul_f16 v196, v196, v233\n"
    "v_pk_fma_f16 v232, v184, v184, s48\n"
    "v_mfma_f32_16x16x32_f16 v[96:99], a[80:83], v[192:195], v[96:99]\n"
    "buffer_load_dwordx4 a[236:239], v245, s[40:43], s101 offen offset:64\n"
    "v_or_b32_e64 v187, v187, v203\n"
    "v_mfma_f32_16x16x32_f16 v[100:103], a[84:87], v[192:195], v[100:103]\n"
    "s_waitcnt lgkmcnt(6)\n"
    "v_pk_fma_f16 v233, v188, v188, s48\n"
    "v_pk_mul_f16 v232, v184, v232\n"
    "v_mfma_f32_16x16x32_f16 v[112:115], a[88:91], v[192:195], v[112:115]\n"
    "v_or_b32_e32 v189, v189, v205\n"
    "v_pk_mul_f16 v233, v188, v233\n"
    "v_mfma_f32_16x16x32_f16 v[116:119], a[92:95], v[192:195], v[116:119]\n"
    "v_lshlrev_b32_sdwa v201, 1, v171 dst_sel:DWORD dst_unused:UNUSED_PAD src0_sel:DWORD src1_sel:WORD_1\n"
    "v_lshlrev_b32_sdwa v202, 1, v173 dst_sel:DWORD dst_unused:UNUSED_PAD src0_sel:DWORD src1_sel:WORD_1\n"
    "v_mfma_f32_16x16x32_f16 v[128:131], v[236:239], v[192:195], v[128:131]\n"
    "s_waitcnt lgkmcnt(2)\n"
    "s_barrier\n"
    "v_smfmac_f32_16x16x64_bf16 v[56:59], v[216:219], v[208:215], v240\n"
    "s_waitcnt lgkmcnt(1)\n"
    "ds_read_b128 v[216:219], v251 offset:21504\n"
    "ds_read_u16_d16_hi v201, v201\n"
    "ds_write2_b32 v248, v64, v68 offset0:0 offset1:1\n"
    "v_smfmac_f32_16x16x64_bf16 v[60:63], v[220:223], v[208:215], v240 abid:1\n"
    "s_waitcnt lgkmcnt(3)\n"
    "v_lshlrev_b32_sdwa v203, 1, v175 dst_sel:DWORD dst_unused:UNUSED_PAD src0_sel:DWORD src1_sel:WORD_1\n"
    "v_mfma_f32_16x16x32_f16 v[132:135], a[244:247], v[192:195], v[132:135]\n"
    "ds_read_b128 v[220:223], v251 offset:21840\n"
    "ds_read_u16_d16_hi v202, v202\n"
    "v_cvt_pk_f16_f32 v192, v168, v169\n"
    "ds_write2_b32 v248, v65, v69 offset0:20 offset1:21\n"
    "v_pk_add_f32 v[224:225], v[224:225], v[56:57]\n"
    "v_pk_add_f32 v[226:227], v[226:227], v[58:59]\n"
    "v_pk_fma_f32 v[228:229], v[56:57], v[56:57], v[228:229]\n"
    "v_pk_fma_f32 v[230:231], v[58:59], v[58:59], v[230:231]\n"
    "v_pk_add_f32 v[224:225], v[224:225], v[60:61]\n"
    "v_pk_add_f32 v[226:227], v[226:227], v[62:63]\n"
    "v_pk_fma_f32 v[228:229], v[60:61], v[60:61], v[228:229]\n"
    "v_pk_fma_f32 v[230:231], v[62:63], v[62:63], v[230:231]\n"
    "v_mfma_f32_16x16x32_f16 v[104:107], a[80:83], v[196:199], v[104:107]\n"
    "ds_write2_b32 v248, v66, v70 offset0:40 offset1:41\n"
    "v_mfma_f32_16x16x32_f16 v[108:111], a[84:87], v[196:199], v[108:111]\n"
    "v_lshlrev_b32_sdwa v193, 1, v170 dst_sel:DWORD dst_unused:UNUSED_PAD src0_sel:DWORD src1_sel:WORD_1\n"
    "v_lshlrev_b32_sdwa v194, 1, v172 dst_sel:DWORD dst_unused:UNUSED_PAD src0_sel:DWORD src1_sel:WORD_1\n"
    "v_mfma_f32_16x16x32_f16 v[120:123], a[88:91], v[196:199], v[120:123]\n"
    "ds_read_u16 v193, v193\n"
    "ds_read_u16 v194, v194\n"
    "v_mfma_f32_16x16x32_f16 v[124:127], a[92:95], v[196:199], v[124:127]\n"
    "v_lshlrev_b32_sdwa v195, 1, v174 dst_sel:DWORD dst_unused:UNUSED_PAD src0_sel:DWORD src1_sel:WORD_1\n"
    "v_exp_f16_e64 v234, v232\n"
    "v_mfma_f32_16x16x32_f16 v[136:139], v[236:239], v[196:199], v[136:139]\n"
    "ds_read_b128 v[236:239], v244 offset:0\n"
    "ds_read_u16 v195, v195\n"
    "buffer_load_dwordx4 a[224:227], v245, s[40:43], s58 offen offset:0\n"
    "v_mfma_f32_16x16x32_f16 v[140:143], a[244:247], v[196:199], v[140:143]\n"
    "v_or_b32_e64 v190, v190, v206\n"
    "v_cvt_pk_f16_f32 v196, v176, v177\n"
    "v_mfma_f32_16x16x32_bf16 v[168:171], a[144:147], v[144:147], 0\n"
    "ds_read_u16_d16_hi v203, v203\n"
    "v_lshlrev_b32_sdwa v197, 1, v178 dst_sel:DWORD dst_unused:UNUSED_PAD src0_sel:DWORD src1_sel:WORD_1\n"
    "v_mfma_f32_16x16x32_bf16 v[168:171], a[192:195], v[148:151], v[168:171]\n"
    "v_exp_f16_e64 v235, v233\n"
    "v_mfma_f32_16x16x32_bf16 v[168:171], a[104:107], v[152:155], v[168:171]\n"
    "ds_read_b128 a[244:247], v244 offset:2048\n"
    "v_lshlrev_b32_sdwa v205, 1, v179 dst_sel:DWORD dst_unused:UNUSED_PAD src0_sel:DWORD src1_sel:WORD_1\n"
    "v_mfma_f32_16x16x32_bf16 v[172:175], a[148:151], v[144:147], 0\n"
    "ds_read_u16 v197, v197\n"
    "v_lshlrev_b32_sdwa v198, 1, v180 dst_sel:DWORD dst_unused:UNUSED_PAD src0_sel:DWORD src1_sel:WORD_1\n"
    "v_mfma_f32_16x16x32_bf16 v[172:175], a[196:199], v[148:151], v[172:175]\n"
    "v_lshlrev_b32_sdwa v206, 1, v181 dst_sel:DWORD dst_unused:UNUSED_PAD src0_sel:DWORD src1_sel:WORD_1\n"
    "ds_write2_b32 v248, v67, v71 offset0:60 offset1:61\n"
    "v_xor_b32_e64 v248, 8, v248\n"
    "v_lshlrev_b32_sdwa v199, 1, v182 dst_sel:DWORD dst_unused:UNUSED_PAD src0_sel:DWORD src1_sel:WORD_1\n"
    "v_exp_f16_sdwa v234, v232 dst_sel:WORD_1 dst_unused:UNUSED_PRESERVE src0_sel:WORD_1\n"
    "v_mfma_f32_16x16x32_bf16 v[172:175], a[108:111], v[152:155], v[172:175]\n"
    "ds_read_u16_d16_hi v205, v205\n"
    "v_or_b32_e64 v191, v191, v207\n"
    "v_mfma_f32_16x16x32_bf16 v[176:179], a[144:147], v[156:159], 0\n"
    "s_waitcnt lgkmcnt(8)\n"
    "ds_write2_b32 v248, v72, v76 offset0:0 offset1:1\n"
    "v_lshlrev_b32_sdwa v207, 1, v183 dst_sel:DWORD dst_unused:UNUSED_PAD src0_sel:DWORD src1_sel:WORD_1\n"
    "v_exp_f16_sdwa v235, v233 dst_sel:WORD_1 dst_unused:UNUSED_PRESERVE src0_sel:WORD_1\n"
    "v_mfma_f32_16x16x32_bf16 v[176:179], a[192:195], v[160:163], v[176:179]\n"
    "s_waitcnt lgkmcnt(7)\n"
    "ds_read_u16 v198, v198\n"
    "v_pk_fma_f16 v234, v234, s49, s49\n"
    "v_mfma_f32_16x16x32_bf16 v[176:179], a[104:107], v[164:167], v[176:179]\n"
    "v_pk_fma_f16 v235, v235, s49, s49\n"
    "v_cvt_pk_bf16_f32 v96, v96, v97\n"
    "v_mfma_f32_16x16x32_bf16 v[180:183], a[148:151], v[156:159], 0\n"
    "ds_read_u16_d16_hi v206, v206\n"
    "v_cvt_pk_bf16_f32 v97, v98, v99\n"
    "v_mfma_f32_16x16x32_bf16 v[180:183], a[196:199], v[160:163], v[180:183]\n"
    "v_rcp_f16_e64 v232, v234\n"
    "v_mfma_f32_16x16x32_bf16 v[180:183], a[108:111], v[164:167], v[180:183]\n"
    "s_waitcnt lgkmcnt(6)\n"
    "s_nop 0\n"
    "ds_read_u16 v199, v199\n"
    "buffer_load_dwordx4 a[228:231], v245, s[40:43], s101 offen offset:0\n"
    "v_cvt_pk_bf16_f32 v98, v100, v101\n"
    "v_cvt_pk_bf16_f32 v99, v102, v103\n"
    "v_cvt_pk_bf16_f32 v112, v112, v113\n"
    "ds_write2_b32 v248, v73, v77 offset0:20 offset1:21\n"
    "v_rcp_f16_e64 v233, v235\n"
    "v_cvt_pk_bf16_f32 v113, v114, v115\n"
    "ds_write2_b32 v248, v74, v78 offset0:40 offset1:41\n"
    "v_cvt_pk_bf16_f32 v114, v116, v117\n"
    "v_cvt_pk_bf16_f32 v115, v118, v119\n"
    "v_rcp_f16_sdwa v232, v234 dst_sel:WORD_1 dst_unused:UNUSED_PRESERVE src0_sel:WORD_1\n"
    "ds_write2_b32 v248, v75, v79 offset0:60 offset1:61\n"
    "v_add_u32_e32 v248, 4080, v248\n"
    "v_cvt_pk_bf16_f32 v128, v128, v129\n"
    "s_mov_b64 exec, s[52:53]\n"
    "ds_write2_b32 v248, v88, v92 offset0:0 offset1:1\n"
    "s_mov_b64 exec, -1\n"
    "v_cvt_pk_bf16_f32 v129, v130, v131\n"
    "v_cvt_pk_bf16_f32 v130, v132, v133\n"
    "v_or_b32_e32 v193, v193, v201\n"
    "s_mov_b64 exec, s[52:53]\n"
    "ds_write2_b32 v248, v89, v93 offset0:20 offset1:21\n"
    "s_mov_b64 exec, -1\n"
    "v_cvt_pk_bf16_f32 v131, v134, v135\n"
    "v_rcp_f16_sdwa v233, v235 dst_sel:WORD_1 dst_unused:UNUSED_PRESERVE src0_sel:WORD_1\n"
    "s_mov_b64 exec, s[52:53]\n"
    "ds_write2_b32 v248, v90, v94 offset0:40 offset1:41\n"
    "s_mov_b64 exec, -1\n"
    "v_cvt_pk_bf16_f32 v104, v104, v105\n"
    "v_cvt_pk_bf16_f32 v105, v106, v107\n"
    "v_cvt_pk_bf16_f32 v106, v108, v109\n"
    "s_mov_b64 exec, s[52:53]\n"
    "ds_write2_b32 v248, v91, v95 offset0:60 offset1:61\n"
    "s_mov_b64 exec, -1\n"
    "v_xor_b32_e32 v248, 8, v248\n"
    "v_pk_mul_f16 v184, v184, v232\n"
    "v_cvt_pk_bf16_f32 v107, v110, v111\n"
    "s_mov_b64 exec, s[52:53]\n"
    "ds_write2_b32 v248, v80, v84 offset0:0 offset1:1\n"
    "s_mov_b64 exec, -1\n"
    "v_mfma_f32_16x16x32_f16 v[100:103], a[4:7], v[184:187], 0\n"
    "s_waitcnt lgkmcnt(6)\n"
    "v_or_b32_e32 v194, v194, v202\n"
    "v_cvt_pk_bf16_f32 v120, v120, v121\n"
    "v_mfma_f32_16x16x32_f16 v[116:119], a[12:15], v[184:187], 0\n"
    "ds_read_u16_d16_hi v207, v207\n"
    "v_cvt_pk_bf16_f32 v121, v122, v123\n"
    "v_mfma_f32_16x16x32_f16 v[132:135], a[244:247], v[184:187], 0\n"
    "s_mov_b64 exec, s[52:53]\n"
    "ds_write2_b32 v248, v81, v85 offset0:20 offset1:21\n"
    "s_mov_b64 exec, -1\n"
    "v_pk_mul_f16 v188, v188, v233\n"
    "v_cvt_pk_bf16_f32 v122, v124, v125\n"
    "v_cvt_pk_bf16_f32 v123, v126, v127\n"
    "v_mfma_f32_16x16x32_f16 v[108:111], a[4:7], v[188:191], 0\n"
    "s_mov_b64 exec, s[52:53]\n"
    "ds_write2_b32 v248, v82, v86 offset0:40 offset1:41\n"
    "s_mov_b64 exec, -1\n"
    "v_pk_fma_f16 v232, v192, v192, s48\n"
    "v_mfma_f32_16x16x32_f16 v[124:127], a[12:15], v[188:191], 0\n"
    "s_mov_b64 exec, s[52:53]\n"
    "ds_write2_b32 v248, v83, v87 offset0:60 offset1:61\n"
    "s_mov_b64 exec, -1\n"
    "v_mfma_f32_32x32x1_2b_f32 v[64:95], v254, v254, 0\n"
    "v_subrev_u32_e32 v248, 4080, v248\n"
    "v_cvt_pk_bf16_f32 v136, v136, v137\n"
    "ds_write_b128 v242, v[96:99] offset:24640\n"
    "v_cvt_pk_bf16_f32 v137, v138, v139\n"
    "v_cvt_pk_bf16_f32 v138, v140, v141\n"
    "v_or_b32_e64 v195, v195, v203\n"
    "v_cvt_pk_bf16_f32 v139, v142, v143\n"
    "v_pk_fma_f16 v233, v196, v196, s48\n"
    "v_mfma_f32_16x16x32_f16 v[96:99], a[0:3], v[184:187], 0\n"
    "ds_write_b128 v242, v[112:115] offset:26688\n"
    "v_mfma_f32_16x16x32_f16 v[112:115], a[8:11], v[184:187], 0\n"
    "v_pk_mul_f16 v232, v192, v232\n"
    "v_or_b32_e64 v197, v197, v205\n"
    "v_mfma_f32_16x16x32_f16 v[140:143], a[244:247], v[188:191], 0\n"
    "s_waitcnt lgkmcnt(4)\n"
    "s_nop 0\n"
    "v_pk_mul_f16 v233, v196, v233\n"
    "v_lshlrev_b32_sdwa v201, 1, v171 dst_sel:DWORD dst_unused:UNUSED_PAD src0_sel:DWORD src1_sel:WORD_1\n"
    "ds_write_b128 v242, v[128:131] offset:28736\n"
    "v_mfma_f32_16x16x32_f16 v[128:131], v[236:239], v[184:187], 0\n"
    "v_cvt_pk_f16_f32 v184, v168, v169\n"
    "v_lshlrev_b32_sdwa v185, 1, v170 dst_sel:DWORD dst_unused:UNUSED_PAD src0_sel:DWORD src1_sel:WORD_1\n"
    "v_mfma_f32_16x16x32_bf16 v[168:171], a[152:155], v[144:147], 0\n"
    "ds_read_u16 v185, v185\n"
    "s_waitcnt lgkmcnt(4)\n"
    "s_barrier\n"
    "v_mfma_f32_16x16x32_bf16 v[168:171], a[200:203], v[148:151], v[168:171]\n"
    "ds_read_b128 v[208:211], v250 offset:0\n"
    "ds_read_b128 v[212:215], v250 offset:320\n"
    "ds_write_b128 v242, v[104:107] offset:25664\n"
    "v_mfma_f32_16x16x32_f16 v[104:107], a[0:3], v[188:191], 0\n"
    "v_lshlrev_b32_sdwa v186, 1, v172 dst_sel:DWORD dst_unused:UNUSED_PAD src0_sel:DWORD src1_sel:WORD_1\n"
    "v_lshlrev_b32_sdwa v202, 1, v173 dst_sel:DWORD dst_unused:UNUSED_PAD src0_sel:DWORD src1_sel:WORD_1\n"
    "v_lshlrev_b32_sdwa v187, 1, v174 dst_sel:DWORD dst_unused:UNUSED_PAD src0_sel:DWORD src1_sel:WORD_1\n"
    "v_lshlrev_b32_sdwa v203, 1, v175 dst_sel:DWORD dst_unused:UNUSED_PAD src0_sel:DWORD src1_sel:WORD_1\n"
    "v_exp_f16_e64 v234, v232\n"
    "v_mfma_f32_16x16x32_bf16 v[168:171], a[112:115], v[152:155], v[168:171]\n"
    "ds_read_u16_d16_hi v201, v201\n"
    "ds_read_u16 v186, v186\n"
    "ds_write_b128 v242, v[120:123] offset:27712\n"
    "v_mfma_f32_16x16x32_f16 v[120:123], a[8:11], v[188:191], 0\n"
    "v_or_b32_e64 v198, v198, v206\n"
    "v_exp_f16_e64 v235, v233\n"
    "v_mfma_f32_16x16x32_bf16 v[172:175], a[156:159], v[144:147], 0\n"
    "v_lshlrev_b32_sdwa v205, 1, v179 dst_sel:DWORD dst_unused:UNUSED_PAD src0_sel:DWORD src1_sel:WORD_1\n"
    "v_lshlrev_b32_sdwa v206, 1, v181 dst_sel:DWORD dst_unused:UNUSED_PAD src0_sel:DWORD src1_sel:WORD_1\n"
    "v_mfma_f32_16x16x32_bf16 v[172:175], a[204:207], v[148:151], v[172:175]\n"
    "ds_read_u16_d16_hi v202, v202\n"
    "ds_read_u16 v187, v187\n"
    "ds_write_b128 v242, v[136:139] offset:29760\n"
    "s_waitcnt lgkmcnt(6)\n"
    "v_smfmac_f32_16x16x64_bf16 v[64:67], v[216:219], v[208:215], v240\n"
    "v_exp_f16_sdwa v234, v232 dst_sel:WORD_1 dst_unused:UNUSED_PRESERVE src0_sel:WORD_1\n"
    "v_smfmac_f32_16x16x64_bf16 v[68:71], v[220:223], v[208:215], v240 abid:1\n"
    "v_or_b32_e32 v199, v199, v207\n"
    "v_mfma_f32_16x16x32_f16 v[136:139], v[236:239], v[188:191], 0\n"
    "ds_read_b128 v[208:211], v250 offset:640\n"
    "ds_read_b128 v[212:215], v250 offset:960\n"
    "ds_read_b128 v[216:219], v252 offset:21504\n"
    "v_mfma_f32_16x16x32_bf16 v[172:175], a[116:119], v[152:155], v[172:175]\n"
    "s_waitcnt lgkmcnt(8)\n"
    "s_nop 0\n"
    "ds_read_b128 v[220:223], v252 offset:21840\n"
    "v_cvt_pk_f16_f32 v188, v176, v177\n"
    "v_lshlrev_b32_sdwa v189, 1, v178 dst_sel:DWORD dst_unused:UNUSED_PAD src0_sel:DWORD src1_sel:WORD_1\n"
    "v_mfma_f32_16x16x32_bf16 v[176:179], a[152:155], v[156:159], 0\n"
    "ds_read_b128 a[252:255], v249 offset:0\n"
    "ds_read_b128 v[236:239], v244 offset:4096\n"
    "v_lshlrev_b32_sdwa v190, 1, v180 dst_sel:DWORD dst_unused:UNUSED_PAD src0_sel:DWORD src1_sel:WORD_1\n"
    "v_lshlrev_b32_sdwa v191, 1, v182 dst_sel:DWORD dst_unused:UNUSED_PAD src0_sel:DWORD src1_sel:WORD_1\n"
    "v_lshlrev_b32_sdwa v207, 1, v183 dst_sel:DWORD dst_unused:UNUSED_PAD src0_sel:DWORD src1_sel:WORD_1\n"
    "v_exp_f16_sdwa v235, v233 dst_sel:WORD_1 dst_unused:UNUSED_PRESERVE src0_sel:WORD_1\n"
    "v_mfma_f32_16x16x32_bf16 v[176:179], a[200:203], v[160:163], v[176:179]\n"
    "ds_read_u16_d16_hi v203, v203\n"
    "v_pk_fma_f16 v234, v234, s49, s49\n"
    "v_mfma_f32_16x16x32_bf16 v[176:179], a[112:115], v[164:167], v[176:179]\n"
    "ds_read_b128 a[244:247], v244 offset:6144\n"
    "v_pk_fma_f16 v235, v235, s49, s49\n"
    "v_mfma_f32_16x16x32_bf16 v[180:183], a[156:159], v[156:159], 0\n"
    "s_waitcnt lgkmcnt(10)\n"
    "ds_read_u16 v189, v189\n"
    "v_rcp_f16_e32 v232, v234\n"
    "v_mfma_f32_16x16x32_bf16 v[180:183], a[204:207], v[160:163], v[180:183]\n"
    "v_rcp_f16_e64 v233, v235\n"
    "v_mfma_f32_16x16x32_bf16 v[180:183], a[116:119], v[164:167], v[180:183]\n"
    "ds_read_u16_d16_hi v205, v205\n"
    "v_rcp_f16_sdwa v232, v234 dst_sel:WORD_1 dst_unused:UNUSED_PRESERVE src0_sel:WORD_1\n"
    "v_or_b32_e64 v185, v185, v201\n"
    "v_rcp_f16_sdwa v233, v235 dst_sel:WORD_1 dst_unused:UNUSED_PRESERVE src0_sel:WORD_1\n"
    "v_pk_mul_f16 v192, v192, v232\n"
    "v_or_b32_e64 v186, v186, v202\n"
    "v_pk_mul_f16 v196, v196, v233\n"
    "v_mfma_f32_16x16x32_f16 v[96:99], a[16:19], v[192:195], v[96:99]\n"
    "s_waitcnt lgkmcnt(7)\n"
    "ds_read_u16 v190, v190\n"
    "ds_read_u16_d16_hi v206, v206\n"
    "v_smfmac_f32_16x16x64_bf16 v[64:67], v[216:219], v[208:215], v240\n"
    "s_waitcnt lgkmcnt(8)\n"
    "v_pk_fma_f16 v232, v184, v184, s48\n"
    "v_smfmac_f32_16x16x64_bf16 v[68:71], v[220:223], v[208:215], v240 abid:1\n"
    "s_waitcnt lgkmcnt(7)\n"
    "ds_read_b128 v[208:211], v250 offset:80\n"
    "buffer_store_dwordx4 a[252:255], v255, s[44:47], s57 offen offset:0 sc1\n"
    "ds_read_b128 v[212:215], v250 offset:400\n"
    "v_mfma_f32_16x16x32_f16 v[100:103], a[20:23], v[192:195], v[100:103]\n"
    "s_waitcnt lgkmcnt(8)\n"
    "ds_read_b128 v[216:219], v251 offset:22176\n"
    "ds_read_b128 v[220:223], v251 offset:22512\n"
    "v_pk_fma_f16 v233, v188, v188, s48\n"
    "v_pk_mul_f16 v232, v184, v232\n"
    "v_pk_mul_f16 v233, v188, v233\n"
    "v_pk_add_f32 v[224:225], v[224:225], v[64:65]\n"
    "v_pk_add_f32 v[226:227], v[226:227], v[66:67]\n"
    "v_pk_fma_f32 v[228:229], v[64:65], v[64:65], v[228:229]\n"
    "v_pk_fma_f32 v[230:231], v[66:67], v[66:67], v[230:231]\n"
    "v_pk_add_f32 v[224:225], v[224:225], v[68:69]\n"
    "v_pk_add_f32 v[226:227], v[226:227], v[70:71]\n"
    "v_pk_fma_f32 v[228:229], v[68:69], v[68:69], v[228:229]\n"
    "v_pk_fma_f32 v[230:231], v[70:71], v[70:71], v[230:231]\n"
    "v_mfma_f32_16x16x32_f16 v[112:115], a[24:27], v[192:195], v[112:115]\n"
    "s_waitcnt lgkmcnt(8)\n"
    "ds_read_b128 a[252:255], v249 offset:2048\n"
    "ds_read_u16 v191, v191\n"
    "ds_read_u16_d16_hi v207, v207\n"
    "v_mfma_f32_16x16x32_f16 v[116:119], a[28:31], v[192:195], v[116:119]\n"
    "v_or_b32_e32 v187, v187, v203\n"
    "v_lshlrev_b32_sdwa v201, 1, v171 dst_sel:DWORD dst_unused:UNUSED_PAD src0_sel:DWORD src1_sel:WORD_1\n"
    "v_mfma_f32_16x16x32_f16 v[128:131], v[236:239], v[192:195], v[128:131]\n"
    "s_waitcnt lgkmcnt(9)\n"
    "ds_read_b128 a[240:243], v244 offset:34816\n"
    "v_or_b32_e32 v189, v189, v205\n"
    "v_mfma_f32_16x16x32_f16 v[132:135], a[244:247], v[192:195], v[132:135]\n"
    "ds_read_u16_d16_hi v201, v201\n"
    "v_cvt_pk_f16_f32 v192, v168, v169\n"
    "v_mfma_f32_16x16x32_f16 v[104:107], a[16:19], v[196:199], v[104:107]\n"
    "v_lshlrev_b32_sdwa v193, 1, v170 dst_sel:DWORD dst_unused:UNUSED_PAD src0_sel:DWORD src1_sel:WORD_1\n"
    "v_lshlrev_b32_sdwa v194, 1, v172 dst_sel:DWORD dst_unused:UNUSED_PAD src0_sel:DWORD src1_sel:WORD_1\n"
    "v_mfma_f32_16x16x32_f16 v[108:111], a[20:23], v[196:199], v[108:111]\n"
    "ds_read_u16 v193, v193\n"
    "v_lshlrev_b32_sdwa v202, 1, v173 dst_sel:DWORD dst_unused:UNUSED_PAD src0_sel:DWORD src1_sel:WORD_1\n"
    "v_mfma_f32_16x16x32_f16 v[120:123], a[24:27], v[196:199], v[120:123]\n"
    "s_waitcnt lgkmcnt(9)\n"
    "v_lshlrev_b32_sdwa v195, 1, v174 dst_sel:DWORD dst_unused:UNUSED_PAD src0_sel:DWORD src1_sel:WORD_1\n"
    "v_lshlrev_b32_sdwa v203, 1, v175 dst_sel:DWORD dst_unused:UNUSED_PAD src0_sel:DWORD src1_sel:WORD_1\n"
    "v_mfma_f32_16x16x32_f16 v[124:127], a[28:31], v[196:199], v[124:127]\n"
    "ds_read_u16 v194, v194\n"
    "v_exp_f16_e64 v234, v232\n"
    "s_waitcnt lgkmcnt(8)\n"
    "v_smfmac_f32_16x16x64_bf16 v[72:75], v[216:219], v[208:215], v240\n"
    "s_waitcnt lgkmcnt(7)\n"
    "ds_read_b128 v[216:219], v252 offset:22176\n"
    "ds_read_u16_d16_hi v202, v202\n"
    "s_waitcnt lgkmcnt(8)\n"
    "buffer_store_dwordx4 a[252:255], v255, s[44:47], s57 offen offset:64 sc1\n"
    "v_smfmac_f32_16x16x64_bf16 v[76:79], v[220:223], v[208:215], v240 abid:1\n"
    "ds_read_b128 v[208:211], v250 offset:720\n"
    "ds_read_b128 v[212:215], v250 offset:1040\n"
    "v_mfma_f32_16x16x32_f16 v[136:139], v[236:239], v[196:199], v[136:139]\n"
    "v_or_b32_e64 v190, v190, v206\n"
    "v_exp_f16_e64 v235, v233\n"
    "v_mfma_f32_16x16x32_f16 v[140:143], a[244:247], v[196:199], v[140:143]\n"
    "ds_read_b128 v[220:223], v252 offset:22512\n"
    "ds_read_b128 a[252:255], v249 offset:4096\n"
    "v_mfma_f32_16x16x32_bf16 v[168:171], a[160:163], v[144:147], 0\n"
    "s_waitcnt lgkmcnt(9)\n"
    "s_nop 0\n"
    "ds_read_b128 v[236:239], v244 offset:8192\n"
    "v_cvt_pk_f16_f32 v196, v176, v177\n"
    "v_mfma_f32_16x16x32_bf16 v[168:171], a[208:211], v[148:151], v[168:171]\n"
    "ds_read_u16 v195, v195\n"
    "v_lshlrev_b32_sdwa v197, 1, v178 dst_sel:DWORD dst_unused:UNUSED_PAD src0_sel:DWORD src1_sel:WORD_1\n"
    "v_mfma_f32_16x16x32_bf16 v[168:171], a[120:123], v[152:155], v[168:171]\n"
    "v_lshlrev_b32_sdwa v205, 1, v179 dst_sel:DWORD dst_unused:UNUSED_PAD src0_sel:DWORD src1_sel:WORD_1\n"
    "v_lshlrev_b32_sdwa v198, 1, v180 dst_sel:DWORD dst_unused:UNUSED_PAD src0_sel:DWORD src1_sel:WORD_1\n"
    "v_mfma_f32_16x16x32_bf16 v[172:175], a[164:167], v[144:147], 0\n"
    "ds_read_u16_d16_hi v203, v203\n"
    "v_lshlrev_b32_sdwa v206, 1, v181 dst_sel:DWORD dst_unused:UNUSED_PAD src0_sel:DWORD src1_sel:WORD_1\n"
    "v_mfma_f32_16x16x32_bf16 v[172:175], a[212:215], v[148:151], v[172:175]\n"
    "s_waitcnt lgkmcnt(10)\n"
    "v_lshlrev_b32_sdwa v199, 1, v182 dst_sel:DWORD dst_unused:UNUSED_PAD src0_sel:DWORD src1_sel:WORD_1\n"
    "v_exp_f16_sdwa v234, v232 dst_sel:WORD_1 dst_unused:UNUSED_PRESERVE src0_sel:WORD_1\n"
    "v_mfma_f32_16x16x32_bf16 v[172:175], a[240:243], v[152:155], v[172:175]\n"
    "ds_read_b128 a[244:247], v244 offset:10240\n"
    "ds_read_u16 v197, v197\n"
    "v_mfma_f32_16x16x32_bf16 v[176:179], a[160:163], v[156:159], 0\n"
    "v_or_b32_e32 v191, v191, v207\n"
    "v_lshlrev_b32_sdwa v207, 1, v183 dst_sel:DWORD dst_unused:UNUSED_PAD src0_sel:DWORD src1_sel:WORD_1\n"
    "v_exp_f16_sdwa v235, v233 dst_sel:WORD_1 dst_unused:UNUSED_PRESERVE src0_sel:WORD_1\n"
    "v_mfma_f32_16x16x32_bf16 v[176:179], a[208:211], v[160:163], v[176:179]\n"
    "ds_read_u16_d16_hi v205, v205\n"
    "v_pk_fma_f16 v234, v234, s49, s49\n"
    "v_mfma_f32_16x16x32_bf16 v[176:179], a[120:123], v[164:167], v[176:179]\n"
    "s_waitcnt lgkmcnt(9)\n"
    "v_pk_fma_f16 v235, v235, s49, s49\n"
    "v_add_u32_e32 v254, s54, v242\n"
    "s_waitcnt lgkmcnt(8)\n"
    "v_smfmac_f32_16x16x64_bf16 v[72:75], v[216:219], v[208:215], v240\n"
    "s_waitcnt lgkmcnt(7)\n"
    "ds_read_b128 v[216:219], v251 offset:22848\n"
    "ds_read_u16 v198, v198\n"
    "v_smfmac_f32_16x16x64_bf16 v[76:79], v[220:223], v[208:215], v240 abid:1\n"
    "s_waitcnt lgkmcnt(8)\n"
    "s_nop 0\n"
    "buffer_store_dwordx4 a[252:255], v255, s[44:47], s57 offen offset:128 sc1\n"
    "ds_read_b128 v[208:211], v250 offset:160\n"
    "ds_read_b128 v[212:215], v250 offset:480\n"
    "v_pk_add_f32 v[224:225], v[224:225], v[72:73]\n"
    "v_pk_add_f32 v[226:227], v[226:227], v[74:75]\n"
    "v_pk_fma_f32 v[228:229], v[72:73], v[72:73], v[228:229]\n"
    "v_pk_fma_f32 v[230:231], v[74:75], v[74:75], v[230:231]\n"
    "v_pk_add_f32 v[224:225], v[224:225], v[76:77]\n"
    "v_pk_add_f32 v[226:227], v[226:227], v[78:79]\n"
    "v_pk_fma_f32 v[228:229], v[76:77], v[76:77], v[228:229]\n"
    "v_pk_fma_f32 v[230:231], v[78:79], v[78:79], v[230:231]\n"
    "v_mfma_f32_16x16x32_bf16 v[180:183], a[164:167], v[156:159], 0\n"
    "ds_read_b128 v[220:223], v251 offset:23184\n"
    "ds_read_b128 a[252:255], v249 offset:1024\n"
    "ds_read_u16_d16_hi v206, v206\n"
    "v_mfma_f32_16x16x32_bf16 v[180:183], a[212:215], v[160:163], v[180:183]\n"
    "s_waitcnt lgkmcnt(9)\n"
    "v_rcp_f16_e32 v232, v234\n"
    "v_mfma_f32_16x16x32_bf16 v[180:183], a[240:243], v[164:167], v[180:183]\n"
    "ds_read_u16 v199, v199\n"
    "v_rcp_f16_e64 v233, v235\n"
    "v_rcp_f16_sdwa v232, v234 dst_sel:WORD_1 dst_unused:UNUSED_PRESERVE src0_sel:WORD_1\n"
    "v_or_b32_e64 v193, v193, v201\n"
    "v_rcp_f16_sdwa v233, v235 dst_sel:WORD_1 dst_unused:UNUSED_PRESERVE src0_sel:WORD_1\n"
    "v_pk_mul_f16 v184, v184, v232\n"
    "v_or_b32_e64 v194, v194, v202\n"
    "v_pk_mul_f16 v188, v188, v233\n"
    "v_mfma_f32_16x16x32_f16 v[96:99], a[32:35], v[184:187], v[96:99]\n"
    "s_waitcnt lgkmcnt(7)\n"
    "ds_read_u16_d16_hi v207, v207\n"
    "ds_read_b128 a[240:243], v244 offset:38912\n"
    "v_mfma_f32_16x16x32_f16 v[100:103], a[36:39], v[184:187], v[100:103]\n"
    "v_pk_fma_f16 v232, v192, v192, s48\n"
    "v_or_b32_e32 v195, v195, v203\n"
    "v_mfma_f32_16x16x32_f16 v[112:115], a[40:43], v[184:187], v[112:115]\n"
    "v_pk_fma_f16 v233, v196, v196, s48\n"
    "v_pk_mul_f16 v232, v192, v232\n"
    "v_mfma_f32_16x16x32_f16 v[116:119], a[44:47], v[184:187], v[116:119]\n"
    "v_or_b32_e64 v197, v197, v205\n"
    "v_pk_mul_f16 v233, v196, v233\n"
    "s_waitcnt lgkmcnt(6)\n"
    "v_smfmac_f32_16x16x64_bf16 v[80:83], v[216:219], v[208:215], v240\n"
    "s_waitcnt lgkmcnt(5)\n"
    "ds_read_b128 v[216:219], v252 offset:22848\n"
    "v_smfmac_f32_16x16x64_bf16 v[84:87], v[220:223], v[208:215], v240 abid:1\n"
    "s_waitcnt lgkmcnt(5)\n"
    "ds_read_b128 v[208:211], v250 offset:800\n"
    "ds_read_b128 v[212:215], v250 offset:1120\n"
    "ds_read_b128 v[220:223], v252 offset:23184\n"
    "v_mfma_f32_16x16x32_f16 v[128:131], v[236:239], v[184:187], v[128:131]\n"
    "buffer_store_dwordx4 a[252:255], v255, s[44:47], s100 offen offset:0 sc1\n"
    "v_mfma_f32_16x16x32_f16 v[132:135], a[244:247], v[184:187], v[132:135]\n"
    "s_waitcnt lgkmcnt(7)\n"
    "ds_read_b128 a[252:255], v249 offset:3072\n"
    "v_cvt_pk_f16_f32 v184, v168, v169\n"
    "v_mfma_f32_16x16x32_f16 v[104:107], a[32:35], v[188:191], v[104:107]\n"
    "v_lshlrev_b32_sdwa v185, 1, v170 dst_sel:DWORD dst_unused:UNUSED_PAD src0_sel:DWORD src1_sel:WORD_1\n"
    "v_lshlrev_b32_sdwa v201, 1, v171 dst_sel:DWORD dst_unused:UNUSED_PAD src0_sel:DWORD src1_sel:WORD_1\n"
    "v_mfma_f32_16x16x32_f16 v[108:111], a[36:39], v[188:191], v[108:111]\n"
    "ds_read_u16 v185, v185\n"
    "ds_read_u16_d16_hi v201, v201\n"
    "v_mfma_f32_16x16x32_f16 v[120:123], a[40:43], v[188:191], v[120:123]\n"
    "v_lshlrev_b32_sdwa v186, 1, v172 dst_sel:DWORD dst_unused:UNUSED_PAD src0_sel:DWORD src1_sel:WORD_1\n"
    "v_lshlrev_b32_sdwa v202, 1, v173 dst_sel:DWORD dst_unused:UNUSED_PAD src0_sel:DWORD src1_sel:WORD_1\n"
    "v_mfma_f32_16x16x32_f16 v[124:127], a[44:47], v[188:191], v[124:127]\n"
    "s_waitcnt lgkmcnt(7)\n"
    "s_nop 0\n"
    "ds_read_u16 v186, v186\n"
    "ds_read_u16_d16_hi v202, v202\n"
    "v_mfma_f32_16x16x32_f16 v[136:139], v[236:239], v[188:191], v[136:139]\n"
    "ds_read_b128 v[236:239], v244 offset:12288\n"
    "v_lshlrev_b32_sdwa v187, 1, v174 dst_sel:DWORD dst_unused:UNUSED_PAD src0_sel:DWORD src1_sel:WORD_1\n"
    "v_mfma_f32_16x16x32_f16 v[140:143], a[244:247], v[188:191], v[140:143]\n"
    "ds_read_u16 v187, v187\n"
    "v_lshlrev_b32_sdwa v203, 1, v175 dst_sel:DWORD dst_unused:UNUSED_PAD src0_sel:DWORD src1_sel:WORD_1\n"
    "s_waitcnt lgkmcnt(8)\n"
    "v_smfmac_f32_16x16x64_bf16 v[80:83], v[216:219], v[208:215], v240\n"
    "s_waitcnt lgkmcnt(7)\n"
    "v_exp_f16_e64 v234, v232\n"
    "v_smfmac_f32_16x16x64_bf16 v[84:87], v[220:223], v[208:215], v240 abid:1\n"
    "s_waitcnt lgkmcnt(6)\n"
    "s_nop 0\n"
    "ds_read_b128 v[208:211], v250 offset:240\n"
    "ds_read_b128 v[212:215], v250 offset:560\n"
    "buffer_store_dwordx4 a[252:255], v255, s[44:47], s100 offen offset:64 sc1\n"
    "v_mfma_f32_16x16x32_bf16 v[168:171], a[168:171], v[144:147], 0\n"
    "ds_read_b128 v[216:219], v251 offset:23520\n"
    "ds_read_b128 v[220:223], v251 offset:23856\n"
    "v_or_b32_e64 v198, v198, v206\n"
    "v_cvt_pk_f16_f32 v188, v176, v177\n"
    "v_lshlrev_b32_sdwa v189, 1, v178 dst_sel:DWORD dst_unused:UNUSED_PAD src0_sel:DWORD src1_sel:WORD_1\n"
    "v_pk_add_f32 v[224:225], v[224:225], v[80:81]\n"
    "v_pk_add_f32 v[226:227], v[226:227], v[82:83]\n"
    "v_pk_fma_f32 v[228:229], v[80:81], v[80:81], v[228:229]\n"
    "v_pk_fma_f32 v[230:231], v[82:83], v[82:83], v[230:231]\n"
    "v_pk_add_f32 v[224:225], v[224:225], v[84:85]\n"
    "v_pk_add_f32 v[226:227], v[226:227], v[86:87]\n"
    "v_pk_fma_f32 v[228:229], v[84:85], v[84:85], v[228:229]\n"
    "v_pk_fma_f32 v[230:231], v[86:87], v[86:87], v[230:231]\n"
    "v_mfma_f32_16x16x32_bf16 v[168:171], a[216:219], v[148:151], v[168:171]\n"
    "s_waitcnt lgkmcnt(8)\n"
    "ds_read_b128 a[252:255], v249 offset:5120\n"
    "ds_read_u16_d16_hi v203, v203\n"
    "ds_read_b128 a[244:247], v244 offset:14336\n"
    "v_mfma_f32_16x16x32_bf16 v[168:171], a[124:127], v[152:155], v[168:171]\n"
    "v_exp_f16_e32 v235, v233\n"
    "v_mfma_f32_16x16x32_bf16 v[172:175], a[172:175], v[144:147], 0\n"
    "ds_read_u16 v189, v189\n"
    "v_lshlrev_b32_sdwa v205, 1, v179 dst_sel:DWORD dst_unused:UNUSED_PAD src0_sel:DWORD src1_sel:WORD_1\n"
    "v_mfma_f32_16x16x32_bf16 v[172:175], a[220:223], v[148:151], v[172:175]\n"
    "s_waitcnt lgkmcnt(9)\n"
    "s_nop 0\n"
    "v_lshlrev_b32_sdwa v190, 1, v180 dst_sel:DWORD dst_unused:UNUSED_PAD src0_sel:DWORD src1_sel:WORD_1\n"
    "v_lshlrev_b32_sdwa v206, 1, v181 dst_sel:DWORD dst_unused:UNUSED_PAD src0_sel:DWORD src1_sel:WORD_1\n"
    "v_lshlrev_b32_sdwa v191, 1, v182 dst_sel:DWORD dst_unused:UNUSED_PAD src0_sel:DWORD src1_sel:WORD_1\n"
    "v_exp_f16_sdwa v234, v232 dst_sel:WORD_1 dst_unused:UNUSED_PRESERVE src0_sel:WORD_1\n"
    "v_mfma_f32_16x16x32_bf16 v[172:175], a[240:243], v[152:155], v[172:175]\n"
    "ds_read_u16_d16_hi v205, v205\n"
    "v_or_b32_e64 v199, v199, v207\n"
    "v_mfma_f32_16x16x32_bf16 v[176:179], a[168:171], v[156:159], 0\n"
    "v_lshlrev_b32_sdwa v207, 1, v183 dst_sel:DWORD dst_unused:UNUSED_PAD src0_sel:DWORD src1_sel:WORD_1\n"
    "v_exp_f16_sdwa v235, v233 dst_sel:WORD_1 dst_unused:UNUSED_PRESERVE src0_sel:WORD_1\n"
    "v_mfma_f32_16x16x32_bf16 v[176:179], a[216:219], v[160:163], v[176:179]\n"
    "s_waitcnt lgkmcnt(6)\n"
    "ds_read_u16 v190, v190\n"
    "ds_read_b128 v[144:147], v243 offset:0\n"
    "v_smfmac_f32_16x16x64_bf16 v[88:91], v[216:219], v[208:215], v240\n"
    "s_waitcnt lgkmcnt(7)\n"
    "ds_read_b128 v[216:219], v252 offset:23520\n"
    "v_smfmac_f32_16x16x64_bf16 v[92:95], v[220:223], v[208:215], v240 abid:1\n"
    "s_waitcnt lgkmcnt(7)\n"
    "s_nop 0\n"
    "ds_read_b128 v[208:211], v250 offset:880\n"
    "ds_read_b128 v[212:215], v250 offset:1200\n"
    "ds_read_b128 v[220:223], v252 offset:23856\n"
    "v_mfma_f32_16x16x32_bf16 v[176:179], a[124:127], v[164:167], v[176:179]\n"
    "buffer_store_dwordx4 a[252:255], v255, s[44:47], s100 offen offset:128 sc1\n"
    "v_pk_fma_f16 v234, v234, s49, s49\n"
    "v_mfma_f32_16x16x32_bf16 v[180:183], a[172:175], v[156:159], 0\n"
    "s_waitcnt lgkmcnt(8)\n"
    "s_nop 0\n"
    "ds_read_u16_d16_hi v206, v206\n"
    "ds_read_b128 v[148:151], v243 offset:1024\n"
    "v_pk_fma_f16 v235, v235, s49, s49\n"
    "v_mfma_f32_16x16x32_bf16 v[180:183], a[220:223], v[160:163], v[180:183]\n"
    "ds_read_u16 v191, v191\n"
    "v_rcp_f16_e64 v232, v234\n"
    "v_mfma_f32_16x16x32_bf16 v[180:183], a[240:243], v[164:167], v[180:183]\n"
    "v_rcp_f16_e64 v233, v235\n"
    "v_rcp_f16_sdwa v232, v234 dst_sel:WORD_1 dst_unused:UNUSED_PRESERVE src0_sel:WORD_1\n"
    "v_or_b32_e64 v185, v185, v201\n"
    "v_rcp_f16_sdwa v233, v235 dst_sel:WORD_1 dst_unused:UNUSED_PRESERVE src0_sel:WORD_1\n"
    "v_pk_mul_f16 v192, v192, v232\n"
    "v_or_b32_e64 v186, v186, v202\n"
    "v_pk_mul_f16 v196, v196, v233\n"
    "v_mfma_f32_16x16x32_f16 v[96:99], a[48:51], v[192:195], v[96:99]\n"
    "s_waitcnt lgkmcnt(7)\n"
    "s_nop 0\n"
    "ds_read_b128 v[152:155], v243 offset:2048\n"
    "ds_read_u16_d16_hi v207, v207\n"
    "ds_read_b128 v[156:159], v243 offset:3072\n"
    "v_mfma_f32_16x16x32_f16 v[100:103], a[52:55], v[192:195], v[100:103]\n"
    "ds_read_b128 v[160:163], v243 offset:4096\n"
    "v_pk_fma_f16 v232, v184, v184, s48\n"
    "v_mfma_f32_16x16x32_f16 v[112:115], a[56:59], v[192:195], v[112:115]\n"
    "ds_read_b128 v[164:167], v243 offset:5120\n"
    "v_or_b32_e32 v187, v187, v203\n"
    "s_waitcnt lgkmcnt(9)\n"
    "v_smfmac_f32_16x16x64_bf16 v[88:91], v[216:219], v[208:215], v240\n"
    "s_waitcnt lgkmcnt(8)\n"
    "ds_read_b128 a[240:243], v244 offset:32768\n"
    "v_smfmac_f32_16x16x64_bf16 v[92:95], v[220:223], v[208:215], v240 abid:1\n"
    "v_pk_fma_f16 v233, v188, v188, s48\n"
    "v_mfma_f32_16x16x32_f16 v[116:119], a[60:63], v[192:195], v[116:119]\n"
    "v_pk_mul_f16 v232, v184, v232\n"
    "v_or_b32_e32 v189, v189, v205\n"
    "v_mfma_f32_16x16x32_f16 v[128:131], v[236:239], v[192:195], v[128:131]\n"
    "v_pk_mul_f16 v233, v188, v233\n"
    "v_lshlrev_b32_sdwa v201, 1, v171 dst_sel:DWORD dst_unused:UNUSED_PAD src0_sel:DWORD src1_sel:WORD_1\n"
    "v_lshlrev_b32_sdwa v202, 1, v173 dst_sel:DWORD dst_unused:UNUSED_PAD src0_sel:DWORD src1_sel:WORD_1\n"
    "v_lshlrev_b32_sdwa v203, 1, v175 dst_sel:DWORD dst_unused:UNUSED_PAD src0_sel:DWORD src1_sel:WORD_1\n"
    "v_exp_f16_e64 v234, v232\n"
    "v_pk_add_f32 v[224:225], v[224:225], v[88:89]\n"
    "v_pk_add_f32 v[226:227], v[226:227], v[90:91]\n"
    "v_pk_fma_f32 v[228:229], v[88:89], v[88:89], v[228:229]\n"
    "v_pk_fma_f32 v[230:231], v[90:91], v[90:91], v[230:231]\n"
    "v_pk_add_f32 v[224:225], v[224:225], v[92:93]\n"
    "v_pk_add_f32 v[226:227], v[226:227], v[94:95]\n"
    "v_pk_fma_f32 v[228:229], v[92:93], v[92:93], v[228:229]\n"
    "v_pk_fma_f32 v[230:231], v[94:95], v[94:95], v[230:231]\n"
    "v_mfma_f32_16x16x32_f16 v[132:135], a[244:247], v[192:195], v[132:135]\n"
    "s_waitcnt lgkmcnt(5)\n"
    "ds_read_u16_d16_hi v201, v201\n"
    "ds_read_u16_d16_hi v202, v202\n"
    "ds_write_b128 v254, v[224:227]\n"
    "v_mfma_f32_16x16x32_f16 v[104:107], a[48:51], v[196:199], v[104:107]\n"
    "s_waitcnt lgkmcnt(6)\n"
    "v_cvt_pk_f16_f32 v192, v168, v169\n"
    "v_lshlrev_b32_sdwa v193, 1, v170 dst_sel:DWORD dst_unused:UNUSED_PAD src0_sel:DWORD src1_sel:WORD_1\n"
    "v_mfma_f32_16x16x32_f16 v[108:111], a[52:55], v[196:199], v[108:111]\n"
    "v_lshlrev_b32_sdwa v194, 1, v172 dst_sel:DWORD dst_unused:UNUSED_PAD src0_sel:DWORD src1_sel:WORD_1\n"
    "v_lshlrev_b32_sdwa v195, 1, v174 dst_sel:DWORD dst_unused:UNUSED_PAD src0_sel:DWORD src1_sel:WORD_1\n"
    "v_mfma_f32_16x16x32_f16 v[120:123], a[56:59], v[196:199], v[120:123]\n"
    "ds_read_u16 v193, v193\n"
    "ds_read_u16 v194, v194\n"
    "v_mfma_f32_16x16x32_f16 v[124:127], a[60:63], v[196:199], v[124:127]\n"
    "ds_read_u16 v195, v195\n"
    "v_or_b32_e64 v190, v190, v206\n"
    "v_mfma_f32_16x16x32_f16 v[136:139], v[236:239], v[196:199], v[136:139]\n"
    "v_exp_f16_e64 v235, v233\n"
    "v_mfma_f32_16x16x32_f16 v[140:143], a[244:247], v[196:199], v[140:143]\n"
    "ds_read_b128 v[236:239], v244 offset:16384\n"
    "v_cvt_pk_f16_f32 v196, v176, v177\n"
    "v_mfma_f32_16x16x32_bf16 v[168:171], a[128:131], v[144:147], 0\n"
    "ds_read_u16_d16_hi v203, v203\n"
    "v_lshlrev_b32_sdwa v197, 1, v178 dst_sel:DWORD dst_unused:UNUSED_PAD src0_sel:DWORD src1_sel:WORD_1\n"
    "v_mfma_f32_16x16x32_bf16 v[168:171], a[176:179], v[148:151], v[168:171]\n"
    "v_lshlrev_b32_sdwa v205, 1, v179 dst_sel:DWORD dst_unused:UNUSED_PAD src0_sel:DWORD src1_sel:WORD_1\n"
    "v_lshlrev_b32_sdwa v198, 1, v180 dst_sel:DWORD dst_unused:UNUSED_PAD src0_sel:DWORD src1_sel:WORD_1\n"
    "v_mfma_f32_16x16x32_bf16 v[168:171], a[96:99], v[152:155], v[168:171]\n"
    "ds_read_b128 a[244:247], v244 offset:18432\n"
    "ds_read_u16 v197, v197\n"
    "v_mfma_f32_16x16x32_bf16 v[172:175], a[132:135], v[144:147], 0\n"
    "v_lshlrev_b32_sdwa v206, 1, v181 dst_sel:DWORD dst_unused:UNUSED_PAD src0_sel:DWORD src1_sel:WORD_1\n"
    "v_lshlrev_b32_sdwa v199, 1, v182 dst_sel:DWORD dst_unused:UNUSED_PAD src0_sel:DWORD src1_sel:WORD_1\n"
    "v_mfma_f32_16x16x32_bf16 v[172:175], a[180:183], v[148:151], v[172:175]\n"
    "ds_write_b128 v254, v[228:231] offset:1024\n"
    "v_lshrrev_b32_e64 v254, 2, v242\n"
    "v_exp_f16_sdwa v234, v232 dst_sel:WORD_1 dst_unused:UNUSED_PRESERVE src0_sel:WORD_1\n"
    "s_waitcnt lgkmcnt(8)\n"
    "v_mfma_f32_16x16x32_bf16 v[172:175], a[240:243], v[152:155], v[172:175]\n"
    "v_add_u32_e32 v254, s55, v254\n"
    "v_or_b32_e64 v191, v191, v207\n"
    "v_mfma_f32_16x16x32_bf16 v[176:179], a[128:131], v[156:159], 0\n"
    "s_waitcnt lgkmcnt(7)\n"
    "ds_read_u16_d16_hi v205, v205\n"
    "v_lshlrev_b32_sdwa v207, 1, v183 dst_sel:DWORD dst_unused:UNUSED_PAD src0_sel:DWORD src1_sel:WORD_1\n"
    "v_exp_f16_sdwa v235, v233 dst_sel:WORD_1 dst_unused:UNUSED_PRESERVE src0_sel:WORD_1\n"
    "v_mfma_f32_16x16x32_bf16 v[176:179], a[176:179], v[160:163], v[176:179]\n"
    "s_waitcnt lgkmcnt(7)\n"
    "ds_read_u16 v198, v198\n"
    "v_pk_fma_f16 v234, v234, s49, s49\n"
    "v_mfma_f32_16x16x32_bf16 v[176:179], a[96:99], v[164:167], v[176:179]\n"
    "ds_read_u16_d16_hi v206, v206\n"
    "v_pk_fma_f16 v235, v235, s49, s49\n"
    "v_mfma_f32_16x16x32_bf16 v[180:183], a[132:135], v[156:159], 0\n"
    "ds_read_u16 v199, v199\n"
    "v_rcp_f16_e64 v232, v234\n"
    "v_mfma_f32_16x16x32_bf16 v[180:183], a[180:183], v[160:163], v[180:183]\n"
    "v_rcp_f16_e64 v233, v235\n"
    "v_mfma_f32_16x16x32_bf16 v[180:183], a[240:243], v[164:167], v[180:183]\n"
    "s_waitcnt lgkmcnt(6)\n"
    "ds_read_u16_d16_hi v207, v207\n"
    "v_rcp_f16_sdwa v232, v234 dst_sel:WORD_1 dst_unused:UNUSED_PRESERVE src0_sel:WORD_1\n"
    "v_or_b32_e32 v193, v193, v201\n"
    "v_rcp_f16_sdwa v233, v235 dst_sel:WORD_1 dst_unused:UNUSED_PRESERVE src0_sel:WORD_1\n"
    "v_pk_mul_f16 v184, v184, v232\n"
    "v_or_b32_e64 v194, v194, v202\n"
    "v_pk_mul_f16 v188, v188, v233\n"
    "v_mfma_f32_16x16x32_f16 v[96:99], a[64:67], v[184:187], v[96:99]\n"
    "s_waitcnt lgkmcnt(5)\n"
    "s_barrier\n"
    "v_mfma_f32_16x16x32_f16 v[100:103], a[68:71], v[184:187], v[100:103]\n"
    "ds_read_b32 v224, v254 offset:0\n"
    "ds_read_b32 v225, v254 offset:6736\n"
    "ds_read_b32 v226, v254 offset:13472\n"
    "v_mfma_f32_16x16x32_f16 v[112:115], a[72:75], v[184:187], v[112:115]\n"
    "ds_read_b32 v227, v254 offset:20208\n"
    "ds_read_b32 v228, v254 offset:1024\n"
    "buffer_load_dword v200, v246, s[72:75], s62 offen offset:256\n"
    "v_mfma_f32_16x16x32_f16 v[116:119], a[76:79], v[184:187], v[116:119]\n"
    "s_waitcnt lgkmcnt(9)\n"
    "s_nop 0\n"
    "ds_read_b32 v229, v254 offset:7760\n"
    "ds_read_b32 v230, v254 offset:14496\n"
    "ds_read_b32 v231, v254 offset:21232\n"
    "v_mfma_f32_16x16x32_f16 v[128:131], v[236:239], v[184:187], v[128:131]\n"
    "ds_read_b128 a[240:243], v244 offset:36864\n"
    "v_pk_fma_f16 v232, v192, v192, s48\n"
    "v_mfma_f32_16x16x32_f16 v[132:135], a[244:247], v[184:187], v[132:135]\n"
    "v_or_b32_e64 v195, v195, v203\n"
    "v_pk_fma_f16 v233, v196, v196, s48\n"
    "v_mfma_f32_16x16x32_f16 v[104:107], a[64:67], v[188:191], v[104:107]\n"
    "s_waitcnt lgkmcnt(11)\n"
    "v_pk_mul_f16 v232, v192, v232\n"
    "v_or_b32_e32 v197, v197, v205\n"
    "v_mfma_f32_16x16x32_f16 v[108:111], a[68:71], v[188:191], v[108:111]\n"
    "v_pk_mul_f16 v233, v196, v233\n"
    "v_cvt_pk_f16_f32 v184, v168, v169\n"
    "v_mfma_f32_16x16x32_f16 v[120:123], a[72:75], v[188:191], v[120:123]\n"
    "v_lshlrev_b32_sdwa v185, 1, v170 dst_sel:DWORD dst_unused:UNUSED_PAD src0_sel:DWORD src1_sel:WORD_1\n"
    "v_lshlrev_b32_sdwa v201, 1, v171 dst_sel:DWORD dst_unused:UNUSED_PAD src0_sel:DWORD src1_sel:WORD_1\n"
    "v_mfma_f32_16x16x32_f16 v[124:127], a[76:79], v[188:191], v[124:127]\n"
    "s_waitcnt lgkmcnt(6)\n"
    "ds_read_u16 v185, v185\n"
    "ds_read_u16_d16_hi v201, v201\n"
    "v_mfma_f32_16x16x32_f16 v[136:139], v[236:239], v[188:191], v[136:139]\n"
    "s_waitcnt lgkmcnt(6)\n"
    "ds_read_b128 v[236:239], v244 offset:20480\n"
    "v_add_f32_e64 v224, v224, v225\n"
    "v_mfma_f32_16x16x32_f16 v[140:143], a[244:247], v[188:191], v[140:143]\n"
    "s_waitcnt lgkmcnt(5)\n"
    "v_add_f32_e32 v226, v226, v227\n"
    "v_add_f32_e64 v228, v228, v229\n"
    "v_mfma_f32_16x16x32_bf16 v[168:171], a[136:139], v[144:147], 0\n"
    "s_waitcnt lgkmcnt(4)\n"
    "v_add_f32_e32 v230, v230, v231\n"
    "v_add_f32_e64 v224, v224, v226\n"
    "v_mfma_f32_16x16x32_bf16 v[168:171], a[184:187], v[148:151], v[168:171]\n"
    "v_add_f32_e64 v228, v228, v230\n"
    "v_mul_f32_e32 v224, 0x3c2aaaab, v224\n"
    "v_mul_f32_e32 v228, 0x3c2aaaab, v228\n"
    "v_lshlrev_b32_sdwa v186, 1, v172 dst_sel:DWORD dst_unused:UNUSED_PAD src0_sel:DWORD src1_sel:WORD_1\n"
    "v_fma_f32 v228, -v224, v224, v228\n"
    "v_lshlrev_b32_sdwa v202, 1, v173 dst_sel:DWORD dst_unused:UNUSED_PAD src0_sel:DWORD src1_sel:WORD_1\n"
    "v_max_f32_e64 v228, 0, v228\n"
    "v_lshlrev_b32_sdwa v187, 1, v174 dst_sel:DWORD dst_unused:UNUSED_PAD src0_sel:DWORD src1_sel:WORD_1\n"
    "v_add_f32_e32 v228, 0x3727c5ac, v228\n"
    "v_lshlrev_b32_sdwa v203, 1, v175 dst_sel:DWORD dst_unused:UNUSED_PAD src0_sel:DWORD src1_sel:WORD_1\n"
    "v_rsq_f32_e64 v228, v228\n"
    "v_exp_f16_e64 v234, v232\n"
    "v_mfma_f32_16x16x32_bf16 v[168:171], a[100:103], v[152:155], v[168:171]\n"
    "ds_read_u16 v186, v186\n"
    "ds_read_u16_d16_hi v202, v202\n"
    "ds_read_u16 v187, v187\n"
    "v_mfma_f32_16x16x32_bf16 v[172:175], a[140:143], v[144:147], 0\n"
    "ds_read_u16_d16_hi v203, v203\n"
    "v_mul_f32_e64 v228, s50, v228\n"
    "v_or_b32_e64 v198, v198, v206\n"
    "v_mul_f32_e64 v224, v228, -v224\n"
    "v_cvt_pk_f16_f32 v188, v176, v177\n"
    "v_lshlrev_b32_sdwa v189, 1, v178 dst_sel:DWORD dst_unused:UNUSED_PAD src0_sel:DWORD src1_sel:WORD_1\n"
    "v_exp_f16_e64 v235, v233\n"
    "v_mfma_f32_16x16x32_bf16 v[172:175], a[188:191], v[148:151], v[172:175]\n"
    "s_waitcnt lgkmcnt(4)\n"
    "s_nop 0\n"
    "ds_read_b128 a[244:247], v244 offset:22528\n"
    "ds_read_u16 v189, v189\n"
    "v_lshlrev_b32_sdwa v205, 1, v179 dst_sel:DWORD dst_unused:UNUSED_PAD src0_sel:DWORD src1_sel:WORD_1\n"
    "v_lshlrev_b32_sdwa v190, 1, v180 dst_sel:DWORD dst_unused:UNUSED_PAD src0_sel:DWORD src1_sel:WORD_1\n"
    "v_lshlrev_b32_sdwa v206, 1, v181 dst_sel:DWORD dst_unused:UNUSED_PAD src0_sel:DWORD src1_sel:WORD_1\n"
    "v_lshlrev_b32_sdwa v191, 1, v182 dst_sel:DWORD dst_unused:UNUSED_PAD src0_sel:DWORD src1_sel:WORD_1\n"
    "v_exp_f16_sdwa v234, v232 dst_sel:WORD_1 dst_unused:UNUSED_PRESERVE src0_sel:WORD_1\n"
    "v_mfma_f32_16x16x32_bf16 v[172:175], a[240:243], v[152:155], v[172:175]\n"
    "ds_read_u16_d16_hi v205, v205\n"
    "v_or_b32_e64 v199, v199, v207\n"
    "v_mfma_f32_16x16x32_bf16 v[176:179], a[136:139], v[156:159], 0\n"
    "v_lshlrev_b32_sdwa v207, 1, v183 dst_sel:DWORD dst_unused:UNUSED_PAD src0_sel:DWORD src1_sel:WORD_1\n"
    "ds_write_b32 v254, v228\n"
    "ds_write_b32 v254, v224 offset:1024\n"
    "v_add_u32_e64 v254, s56, v242\n"
    "v_exp_f16_sdwa v235, v233 dst_sel:WORD_1 dst_unused:UNUSED_PRESERVE src0_sel:WORD_1\n"
    "v_mfma_f32_16x16x32_bf16 v[176:179], a[184:187], v[160:163], v[176:179]\n"
    "ds_read_u16 v190, v190\n"
    "v_pk_fma_f16 v234, v234, s49, s49\n"
    "v_mfma_f32_16x16x32_bf16 v[176:179], a[100:103], v[164:167], v[176:179]\n"
    "ds_read_u16_d16_hi v206, v206\n"
    "v_pk_fma_f16 v235, v235, s49, s49\n"
    "v_mfma_f32_16x16x32_bf16 v[180:183], a[140:143], v[156:159], 0\n"
    "s_waitcnt lgkmcnt(9)\n"
    "ds_read_u16 v191, v191\n"
    "v_rcp_f16_e32 v232, v234\n"
    "v_mfma_f32_16x16x32_bf16 v[180:183], a[188:191], v[160:163], v[180:183]\n"
    "v_rcp_f16_e64 v233, v235\n"
    "v_mfma_f32_16x16x32_bf16 v[180:183], a[240:243], v[164:167], v[180:183]\n"
    "ds_read_u16_d16_hi v207, v207\n"
    "buffer_load_dword v200, v246, s[72:75], s63 offen offset:256\n"
    "v_rcp_f16_sdwa v232, v234 dst_sel:WORD_1 dst_unused:UNUSED_PRESERVE src0_sel:WORD_1\n"
    "v_or_b32_e64 v185, v185, v201\n"
    "v_rcp_f16_sdwa v233, v235 dst_sel:WORD_1 dst_unused:UNUSED_PRESERVE src0_sel:WORD_1\n"
    "v_pk_mul_f16 v192, v192, v232\n"
    "v_or_b32_e64 v186, v186, v202\n"
    "v_pk_mul_f16 v196, v196, v233\n"
    "v_mfma_f32_16x16x32_f16 v[96:99], a[80:83], v[192:195], v[96:99]\n"
    "s_waitcnt lgkmcnt(8)\n"
    "v_pk_fma_f16 v232, v184, v184, s48\n"
    "v_or_b32_e32 v187, v187, v203\n"
    "v_mfma_f32_16x16x32_f16 v[100:103], a[84:87], v[192:195], v[100:103]\n"
    "v_pk_fma_f16 v233, v188, v188, s48\n"
    "s_waitcnt lgkmcnt(4)\n"
    "s_barrier\n"
    "v_mfma_f32_16x16x32_f16 v[112:115], a[88:91], v[192:195], v[112:115]\n"
    "ds_read_b128 v[224:227], v254\n"
    "ds_read_b128 v[228:231], v254 offset:1024\n"
    "v_mfma_f32_16x16x32_f16 v[116:119], a[92:95], v[192:195], v[116:119]\n"
    "v_pk_mul_f16 v232, v184, v232\n"
    "v_or_b32_e64 v189, v189, v205\n"
    "v_mfma_f32_16x16x32_f16 v[128:131], v[236:239], v[192:195], v[128:131]\n"
    "v_pk_mul_f16 v233, v188, v233\n"
    "v_lshlrev_b32_sdwa v201, 1, v171 dst_sel:DWORD dst_unused:UNUSED_PAD src0_sel:DWORD src1_sel:WORD_1\n"
    "v_mfma_f32_16x16x32_f16 v[132:135], a[244:247], v[192:195], v[132:135]\n"
    "s_waitcnt lgkmcnt(4)\n"
    "s_nop 0\n"
    "ds_read_u16_d16_hi v201, v201\n"
    "v_cvt_pk_f16_f32 v192, v168, v169\n"
    "v_mfma_f32_16x16x32_f16 v[104:107], a[80:83], v[196:199], v[104:107]\n"
    "v_lshlrev_b32_sdwa v193, 1, v170 dst_sel:DWORD dst_unused:UNUSED_PAD src0_sel:DWORD src1_sel:WORD_1\n"
    "v_lshlrev_b32_sdwa v194, 1, v172 dst_sel:DWORD dst_unused:UNUSED_PAD src0_sel:DWORD src1_sel:WORD_1\n"
    "v_mfma_f32_16x16x32_f16 v[108:111], a[84:87], v[196:199], v[108:111]\n"
    "ds_read_u16 v193, v193\n"
    "ds_read_u16 v194, v194\n"
    "v_mfma_f32_16x16x32_f16 v[120:123], a[88:91], v[196:199], v[120:123]\n"
    "v_lshlrev_b32_sdwa v202, 1, v173 dst_sel:DWORD dst_unused:UNUSED_PAD src0_sel:DWORD src1_sel:WORD_1\n"
    "v_lshlrev_b32_sdwa v195, 1, v174 dst_sel:DWORD dst_unused:UNUSED_PAD src0_sel:DWORD src1_sel:WORD_1\n"
    "v_mfma_f32_16x16x32_f16 v[124:127], a[92:95], v[196:199], v[124:127]\n"
    "s_waitcnt lgkmcnt(5)\n"
    "s_nop 0\n"
    "ds_read_u16_d16_hi v202, v202\n"
    "ds_read_u16 v195, v195\n"
    "v_mfma_f32_16x16x32_f16 v[136:139], v[236:239], v[196:199], v[136:139]\n"
    "ds_read_b128 v[236:239], v244 offset:0\n"
    "v_lshlrev_b32_sdwa v203, 1, v175 dst_sel:DWORD dst_unused:UNUSED_PAD src0_sel:DWORD src1_sel:WORD_1\n"
    "v_mfma_f32_16x16x32_f16 v[140:143], a[244:247], v[196:199], v[140:143]\n"
    "s_waitcnt lgkmcnt(6)\n"
    "v_exp_f16_e32 v234, v232\n"
    "v_or_b32_e64 v190, v190, v206\n"
    "v_cvt_pk_f16_f32 v196, v176, v177\n"
    "v_lshlrev_b32_sdwa v197, 1, v178 dst_sel:DWORD dst_unused:UNUSED_PAD src0_sel:DWORD src1_sel:WORD_1\n"
    "v_pk_fma_f32 v[64:65], v[64:65], v[224:225], v[228:229]\n"
    "v_pk_fma_f32 v[66:67], v[66:67], v[226:227], v[230:231]\n"
    "v_pk_fma_f32 v[68:69], v[68:69], v[224:225], v[228:229]\n"
    "v_pk_fma_f32 v[70:71], v[70:71], v[226:227], v[230:231]\n"
    "v_pk_fma_f32 v[72:73], v[72:73], v[224:225], v[228:229]\n"
    "v_pk_fma_f32 v[74:75], v[74:75], v[226:227], v[230:231]\n"
    "v_pk_fma_f32 v[76:77], v[76:77], v[224:225], v[228:229]\n"
    "v_pk_fma_f32 v[78:79], v[78:79], v[226:227], v[230:231]\n"
    "v_pk_fma_f32 v[80:81], v[80:81], v[224:225], v[228:229]\n"
    "v_pk_fma_f32 v[82:83], v[82:83], v[226:227], v[230:231]\n"
    "v_pk_fma_f32 v[84:85], v[84:85], v[224:225], v[228:229]\n"
    "v_pk_fma_f32 v[86:87], v[86:87], v[226:227], v[230:231]\n"
    "v_pk_fma_f32 v[88:89], v[88:89], v[224:225], v[228:229]\n"
    "v_pk_fma_f32 v[90:91], v[90:91], v[226:227], v[230:231]\n"
    "v_pk_fma_f32 v[92:93], v[92:93], v[224:225], v[228:229]\n"
    "v_pk_fma_f32 v[94:95], v[94:95], v[226:227], v[230:231]\n"
    "v_pk_fma_f32 v[0:1], v[0:1], v[224:225], v[228:229]\n"
    "v_pk_fma_f32 v[2:3], v[2:3], v[226:227], v[230:231]\n"
    "v_pk_fma_f32 v[4:5], v[4:5], v[224:225], v[228:229]\n"
    "v_pk_fma_f32 v[6:7], v[6:7], v[226:227], v[230:231]\n"
    "v_pk_fma_f32 v[8:9], v[8:9], v[224:225], v[228:229]\n"
    "v_pk_fma_f32 v[10:11], v[10:11], v[226:227], v[230:231]\n"
    "v_pk_fma_f32 v[12:13], v[12:13], v[224:225], v[228:229]\n"
    "v_pk_fma_f32 v[14:15], v[14:15], v[226:227], v[230:231]\n"
    "v_pk_fma_f32 v[16:17], v[16:17], v[224:225], v[228:229]\n"
    "v_pk_fma_f32 v[18:19], v[18:19], v[226:227], v[230:231]\n"
    "v_pk_fma_f32 v[20:21], v[20:21], v[224:225], v[228:229]\n"
    "v_pk_fma_f32 v[22:23], v[22:23], v[226:227], v[230:231]\n"
    "v_pk_fma_f32 v[24:25], v[24:25], v[224:225], v[228:229]\n"
    "v_pk_fma_f32 v[26:27], v[26:27], v[226:227], v[230:231]\n"
    "v_pk_fma_f32 v[28:29], v[28:29], v[224:225], v[228:229]\n"
    "v_pk_fma_f32 v[30:31], v[30:31], v[226:227], v[230:231]\n"
    "v_pk_fma_f32 v[32:33], v[32:33], v[224:225], v[228:229]\n"
    "v_pk_fma_f32 v[34:35], v[34:35], v[226:227], v[230:231]\n"
    "v_pk_fma_f32 v[36:37], v[36:37], v[224:225], v[228:229]\n"
    "v_pk_fma_f32 v[38:39], v[38:39], v[226:227], v[230:231]\n"
    "v_pk_fma_f32 v[40:41], v[40:41], v[224:225], v[228:229]\n"
    "v_pk_fma_f32 v[42:43], v[42:43], v[226:227], v[230:231]\n"
    "v_pk_fma_f32 v[44:45], v[44:45], v[224:225], v[228:229]\n"
    "v_pk_fma_f32 v[46:47], v[46:47], v[226:227], v[230:231]\n"
    "v_pk_fma_f32 v[48:49], v[48:49], v[224:225], v[228:229]\n"
    "v_pk_fma_f32 v[50:51], v[50:51], v[226:227], v[230:231]\n"
    "v_pk_fma_f32 v[52:53], v[52:53], v[224:225], v[228:229]\n"
    "v_pk_fma_f32 v[54:55], v[54:55], v[226:227], v[230:231]\n"
    "v_pk_fma_f32 v[56:57], v[56:57], v[224:225], v[228:229]\n"
    "v_pk_fma_f32 v[58:59], v[58:59], v[226:227], v[230:231]\n"
    "v_pk_fma_f32 v[60:61], v[60:61], v[224:225], v[228:229]\n"
    "v_pk_fma_f32 v[62:63], v[62:63], v[226:227], v[230:231]\n"
    "v_mfma_f32_16x16x32_bf16 v[168:171], a[144:147], v[144:147], 0\n"
    "s_waitcnt lgkmcnt(0)\n"
    "s_nop 0\n"
    "ds_read_u16_d16_hi v203, v203\n"
    "ds_read_b128 a[244:247], v244 offset:2048\n"
    "v_mfma_f32_16x16x32_bf16 v[168:171], a[192:195], v[148:151], v[168:171]\n"
    "v_cvt_pk_bf16_f32 v208, v64, v68\n"
    "v_cvt_pk_bf16_f32 v212, v65, v69\n"
    "v_mfma_f32_16x16x32_bf16 v[168:171], a[104:107], v[152:155], v[168:171]\n"
    "v_cvt_pk_bf16_f32 v216, v66, v70\n"
    "v_cvt_pk_bf16_f32 v220, v67, v71\n"
    "v_mfma_f32_16x16x32_bf16 v[172:175], a[148:151], v[144:147], 0\n"
    "v_cvt_pk_bf16_f32 v209, v72, v76\n"
    "v_cvt_pk_bf16_f32 v213, v73, v77\n"
    "v_cvt_pk_bf16_f32 v217, v74, v78\n"
    "v_cvt_pk_bf16_f32 v221, v75, v79\n"
    "v_cvt_pk_bf16_f32 v210, v80, v84\n"
    "v_cvt_pk_bf16_f32 v214, v81, v85\n"
    "v_cvt_pk_bf16_f32 v218, v82, v86\n"
    "v_cvt_pk_bf16_f32 v222, v83, v87\n"
    "v_cvt_pk_bf16_f32 v211, v88, v92\n"
    "v_cvt_pk_bf16_f32 v215, v89, v93\n"
    "v_cvt_pk_bf16_f32 v219, v90, v94\n"
    "v_cvt_pk_bf16_f32 v223, v91, v95\n"
    "v_cvt_pk_bf16_f32 v80, v0, v4\n"
    "v_cvt_pk_bf16_f32 v84, v1, v5\n"
    "v_cvt_pk_bf16_f32 v88, v2, v6\n"
    "v_cvt_pk_bf16_f32 v92, v3, v7\n"
    "buffer_load_dwordx4 v[0:3], v246, s[72:75], s62 offen offset:0\n"
    "v_cvt_pk_bf16_f32 v81, v8, v12\n"
    "v_cvt_pk_bf16_f32 v85, v9, v13\n"
    "v_cvt_pk_bf16_f32 v89, v10, v14\n"
    "v_cvt_pk_bf16_f32 v93, v11, v15\n"
    "v_cvt_pk_bf16_f32 v82, v16, v20\n"
    "v_cvt_pk_bf16_f32 v86, v17, v21\n"
    "v_cvt_pk_bf16_f32 v90, v18, v22\n"
    "v_cvt_pk_bf16_f32 v94, v19, v23\n"
    "v_cvt_pk_bf16_f32 v83, v24, v28\n"
    "v_cvt_pk_bf16_f32 v87, v25, v29\n"
    "v_cvt_pk_bf16_f32 v91, v26, v30\n"
    "v_cvt_pk_bf16_f32 v95, v27, v31\n"
    "v_cvt_pk_bf16_f32 v64, v32, v36\n"
    "v_cvt_pk_bf16_f32 v68, v33, v37\n"
    "v_cvt_pk_bf16_f32 v72, v34, v38\n"
    "v_cvt_pk_bf16_f32 v76, v35, v39\n"
    "v_cvt_pk_bf16_f32 v65, v40, v44\n"
    "v_cvt_pk_bf16_f32 v69, v41, v45\n"
    "v_cvt_pk_bf16_f32 v73, v42, v46\n"
    "v_cvt_pk_bf16_f32 v77, v43, v47\n"
    "v_cvt_pk_bf16_f32 v66, v48, v52\n"
    "v_cvt_pk_bf16_f32 v70, v49, v53\n"
    "v_cvt_pk_bf16_f32 v74, v50, v54\n"
    "v_cvt_pk_bf16_f32 v78, v51, v55\n"
    "v_cvt_pk_bf16_f32 v67, v56, v60\n"
    "v_cvt_pk_bf16_f32 v71, v57, v61\n"
    "v_cvt_pk_bf16_f32 v75, v58, v62\n"
    "v_cvt_pk_bf16_f32 v79, v59, v63\n"
    "v_exp_f16_e64 v235, v233\n"
    "v_mfma_f32_16x16x32_bf16 v[172:175], a[196:199], v[148:151], v[172:175]\n"
    "ds_read_u16 v197, v197\n"
    "v_lshlrev_b32_sdwa v205, 1, v179 dst_sel:DWORD dst_unused:UNUSED_PAD src0_sel:DWORD src1_sel:WORD_1\n"
    "v_lshlrev_b32_sdwa v198, 1, v180 dst_sel:DWORD dst_unused:UNUSED_PAD src0_sel:DWORD src1_sel:WORD_1\n"
    "v_lshlrev_b32_sdwa v206, 1, v181 dst_sel:DWORD dst_unused:UNUSED_PAD src0_sel:DWORD src1_sel:WORD_1\n"
    "v_lshlrev_b32_sdwa v199, 1, v182 dst_sel:DWORD dst_unused:UNUSED_PAD src0_sel:DWORD src1_sel:WORD_1\n"
    "v_exp_f16_sdwa v234, v232 dst_sel:WORD_1 dst_unused:UNUSED_PRESERVE src0_sel:WORD_1\n"
    "v_mfma_f32_16x16x32_bf16 v[172:175], a[108:111], v[152:155], v[172:175]\n"
    "s_waitcnt lgkmcnt(1)\n"
    "buffer_load_dwordx4 v[4:7], v246, s[72:75], s62 offen offset:192\n"
    "ds_read_u16_d16_hi v205, v205\n"
    "v_mfma_f32_16x16x32_bf16 v[176:179], a[144:147], v[156:159], 0\n"
    "v_or_b32_e32 v191, v191, v207\n"
    "v_lshlrev_b32_sdwa v207, 1, v183 dst_sel:DWORD dst_unused:UNUSED_PAD src0_sel:DWORD src1_sel:WORD_1\n"
    "v_exp_f16_sdwa v235, v233 dst_sel:WORD_1 dst_unused:UNUSED_PRESERVE src0_sel:WORD_1\n"
    "v_mfma_f32_16x16x32_bf16 v[176:179], a[192:195], v[160:163], v[176:179]\n"
    "ds_read_u16 v198, v198\n"
    "v_pk_fma_f16 v234, v234, s49, s49\n"
    "v_mfma_f32_16x16x32_bf16 v[176:179], a[104:107], v[164:167], v[176:179]\n"
    "ds_read_u16_d16_hi v206, v206\n"
    "v_pk_fma_f16 v235, v235, s49, s49\n"
    "v_mfma_f32_16x16x32_bf16 v[180:183], a[148:151], v[156:159], 0\n"
    "ds_read_u16 v199, v199\n"
    "v_cvt_pk_bf16_f32 v96, v96, v97\n"
    "v_mfma_f32_16x16x32_bf16 v[180:183], a[196:199], v[160:163], v[180:183]\n"
    "v_cvt_pk_bf16_f32 v97, v98, v99\n"
    "v_rcp_f16_e64 v232, v234\n"
    "v_mfma_f32_16x16x32_bf16 v[180:183], a[108:111], v[164:167], v[180:183]\n"
    "ds_read_u16_d16_hi v207, v207\n"
    "v_cvt_pk_bf16_f32 v98, v100, v101\n"
    "v_cvt_pk_bf16_f32 v99, v102, v103\n"
    "ds_write_b128 v242, v[96:99] offset:24640\n"
    "v_cvt_pk_bf16_f32 v112, v112, v113\n"
    "v_rcp_f16_e64 v233, v235\n"
    "v_cvt_pk_bf16_f32 v113, v114, v115\n"
    "v_cvt_pk_bf16_f32 v114, v116, v117\n"
    "v_cvt_pk_bf16_f32 v115, v118, v119\n"
    "ds_write_b128 v242, v[112:115] offset:26688\n"
    "buffer_load_dwordx4 v[8:11], v247, s[72:75], s62 offen offset:0\n"
    "v_rcp_f16_sdwa v232, v234 dst_sel:WORD_1 dst_unused:UNUSED_PRESERVE src0_sel:WORD_1\n"
    "v_cvt_pk_bf16_f32 v128, v128, v129\n"
    "v_cvt_pk_bf16_f32 v129, v130, v131\n"
    "v_cvt_pk_bf16_f32 v130, v132, v133\n"
    "v_or_b32_e64 v193, v193, v201\n"
    "v_cvt_pk_bf16_f32 v131, v134, v135\n"
    "ds_write_b128 v242, v[128:131] offset:28736\n"
    "v_rcp_f16_sdwa v233, v235 dst_sel:WORD_1 dst_unused:UNUSED_PRESERVE src0_sel:WORD_1\n"
    "v_cvt_pk_bf16_f32 v104, v104, v105\n"
    "v_cvt_pk_bf16_f32 v105, v106, v107\n"
    "v_cvt_pk_bf16_f32 v106, v108, v109\n"
    "v_pk_mul_f16 v184, v184, v232\n"
    "v_cvt_pk_bf16_f32 v107, v110, v111\n"
    "ds_write_b128 v242, v[104:107] offset:25664\n"
    "v_mfma_f32_16x16x32_f16 v[96:99], a[0:3], v[184:187], 0\n"
    "s_waitcnt lgkmcnt(2)\n"
    "v_or_b32_e32 v194, v194, v202\n"
    "v_cvt_pk_bf16_f32 v120, v120, v121\n"
    "v_mfma_f32_16x16x32_f16 v[100:103], a[4:7], v[184:187], 0\n"
    "v_cvt_pk_bf16_f32 v121, v122, v123\n"
    "v_pk_mul_f16 v188, v188, v233\n"
    "v_mfma_f32_16x16x32_f16 v[112:115], a[8:11], v[184:187], 0\n"
    "buffer_load_dwordx4 v[12:15], v247, s[72:75], s62 offen offset:192\n"
    "v_cvt_pk_bf16_f32 v122, v124, v125\n"
    "v_mfma_f32_16x16x32_f16 v[116:119], a[12:15], v[184:187], 0\n"
    "v_cvt_pk_bf16_f32 v123, v126, v127\n"
    "v_cvt_pk_bf16_f32 v136, v136, v137\n"
    "v_mfma_f32_16x16x32_f16 v[128:131], v[236:239], v[184:187], 0\n"
    "ds_write_b128 v242, v[120:123] offset:27712\n"
    "v_mfma_f32_16x16x32_f16 v[132:135], a[244:247], v[184:187], 0\n"
    "v_pk_fma_f16 v232, v192, v192, s48\n"
    "v_cvt_pk_bf16_f32 v137, v138, v139\n"
    "v_mfma_f32_16x16x32_f16 v[104:107], a[0:3], v[188:191], 0\n"
    "v_cvt_pk_bf16_f32 v138, v140, v141\n"
    "v_or_b32_e64 v195, v195, v203\n"
    "v_mfma_f32_16x16x32_f16 v[108:111], a[4:7], v[188:191], 0\n"
    "v_cvt_pk_bf16_f32 v139, v142, v143\n"
    "v_pk_fma_f16 v233, v196, v196, s48\n"
    "v_mfma_f32_16x16x32_f16 v[120:123], a[8:11], v[188:191], 0\n"
    "ds_write_b128 v242, v[136:139] offset:29760\n"
    "v_mfma_f32_16x16x32_f16 v[124:127], a[12:15], v[188:191], 0\n"
    "v_pk_mul_f16 v232, v192, v232\n"
    "v_or_b32_e64 v197, v197, v205\n"
    "v_mfma_f32_16x16x32_f16 v[136:139], v[236:239], v[188:191], 0\n"
    "buffer_load_dwordx4 v[16:19], v246, s[72:75], s63 offen offset:0\n"
    "v_pk_mul_f16 v233, v196, v233\n"
    "v_mfma_f32_16x16x32_f16 v[140:143], a[244:247], v[188:191], 0\n"
    "ds_read_b128 v[236:239], v244 offset:4096\n"
    "v_cvt_pk_f16_f32 v184, v168, v169\n"
    "s_waitcnt vmcnt(13)\n"
    "s_nop 0\n"
    "v_smfmac_f32_32x32x32_bf16 v[96:111], a[248:251], a[224:231], v241\n"
    "v_lshlrev_b32_sdwa v185, 1, v170 dst_sel:DWORD dst_unused:UNUSED_PAD src0_sel:DWORD src1_sel:WORD_1\n"
    "v_lshlrev_b32_sdwa v201, 1, v171 dst_sel:DWORD dst_unused:UNUSED_PAD src0_sel:DWORD src1_sel:WORD_1\n"
    "v_lshlrev_b32_sdwa v186, 1, v172 dst_sel:DWORD dst_unused:UNUSED_PAD src0_sel:DWORD src1_sel:WORD_1\n"
    "v_lshlrev_b32_sdwa v202, 1, v173 dst_sel:DWORD dst_unused:UNUSED_PAD src0_sel:DWORD src1_sel:WORD_1\n"
    "v_lshlrev_b32_sdwa v187, 1, v174 dst_sel:DWORD dst_unused:UNUSED_PAD src0_sel:DWORD src1_sel:WORD_1\n"
    "v_smfmac_f32_32x32x32_bf16 v[112:127], a[248:251], a[232:239], v241\n"
    "ds_read_u16 v185, v185\n"
    "ds_read_u16_d16_hi v201, v201\n"
    "ds_read_u16 v186, v186\n"
    "v_lshlrev_b32_sdwa v203, 1, v175 dst_sel:DWORD dst_unused:UNUSED_PAD src0_sel:DWORD src1_sel:WORD_1\n"
    "v_exp_f16_e64 v234, v232\n"
    "v_mfma_f32_16x16x32_bf16 v[168:171], a[152:155], v[144:147], 0\n"
    "s_waitcnt lgkmcnt(4)\n"
    "ds_read_u16_d16_hi v202, v202\n"
    "v_or_b32_e32 v198, v198, v206\n"
    "v_mfma_f32_16x16x32_bf16 v[168:171], a[200:203], v[148:151], v[168:171]\n"
    "s_barrier\n"
    "s_nop 0\n"
    "v_mfma_f32_16x16x32_bf16 v[168:171], a[112:115], v[152:155], v[168:171]\n"
    "buffer_load_dwordx4 v[20:23], v246, s[72:75], s63 offen offset:192\n"
    "ds_read_u16 v187, v187\n"
    "ds_write_b128 v253, v[208:211] offset:2048\n"
    "v_mfma_f32_16x16x32_bf16 v[172:175], a[156:159], v[144:147], 0\n"
    "v_cvt_pk_f16_f32 v188, v176, v177\n"
    "v_lshlrev_b32_sdwa v189, 1, v178 dst_sel:DWORD dst_unused:UNUSED_PAD src0_sel:DWORD src1_sel:WORD_1\n"
    "v_exp_f16_e64 v235, v233\n"
    "v_mfma_f32_16x16x32_bf16 v[172:175], a[204:207], v[148:151], v[172:175]\n"
    "v_lshlrev_b32_sdwa v205, 1, v179 dst_sel:DWORD dst_unused:UNUSED_PAD src0_sel:DWORD src1_sel:WORD_1\n"
    "v_lshlrev_b32_sdwa v190, 1, v180 dst_sel:DWORD dst_unused:UNUSED_PAD src0_sel:DWORD src1_sel:WORD_1\n"
    "v_lshlrev_b32_sdwa v206, 1, v181 dst_sel:DWORD dst_unused:UNUSED_PAD src0_sel:DWORD src1_sel:WORD_1\n"
    "v_lshlrev_b32_sdwa v191, 1, v182 dst_sel:DWORD dst_unused:UNUSED_PAD src0_sel:DWORD src1_sel:WORD_1\n"
    "v_exp_f16_sdwa v234, v232 dst_sel:WORD_1 dst_unused:UNUSED_PRESERVE src0_sel:WORD_1\n"
    "v_mfma_f32_16x16x32_bf16 v[172:175], a[116:119], v[152:155], v[172:175]\n"
    "ds_read_u16_d16_hi v203, v203\n"
    "ds_read_b128 a[244:247], v244 offset:6144\n"
    "ds_read_b128 a[232:235], v243 offset:24640\n"
    "v_mfma_f32_16x16x32_bf16 v[176:179], a[152:155], v[156:159], 0\n"
    "s_waitcnt lgkmcnt(7)\n"
    "ds_read_u16 v189, v189\n"
    "ds_read_b128 a[236:239], v243 offset:25664\n"
    "v_or_b32_e32 v199, v199, v207\n"
    "v_lshlrev_b32_sdwa v207, 1, v183 dst_sel:DWORD dst_unused:UNUSED_PAD src0_sel:DWORD src1_sel:WORD_1\n"
    "v_exp_f16_sdwa v235, v233 dst_sel:WORD_1 dst_unused:UNUSED_PRESERVE src0_sel:WORD_1\n"
    "v_mfma_f32_16x16x32_bf16 v[176:179], a[200:203], v[160:163], v[176:179]\n"
    "ds_read_u16_d16_hi v205, v205\n"
    "v_pk_fma_f16 v234, v234, s49, s49\n"
    "v_mfma_f32_16x16x32_bf16 v[176:179], a[112:115], v[164:167], v[176:179]\n"
    "buffer_load_dwordx4 a[224:227], v245, s[40:43], s58 offen offset:128\n"
    "ds_read_u16 v190, v190\n"
    "v_mfma_f32_16x16x32_bf16 v[180:183], a[156:159], v[156:159], 0\n"
    "s_waitcnt lgkmcnt(9)\n"
    "v_pk_fma_f16 v235, v235, s49, s49\n"
    "v_rcp_f16_e32 v232, v234\n"
    "v_mfma_f32_16x16x32_bf16 v[180:183], a[204:207], v[160:163], v[180:183]\n"
    "ds_read_u16_d16_hi v206, v206\n"
    "v_rcp_f16_e64 v233, v235\n"
    "v_mfma_f32_16x16x32_bf16 v[180:183], a[116:119], v[164:167], v[180:183]\n"
    "ds_read_u16 v191, v191\n"
    "v_rcp_f16_sdwa v232, v234 dst_sel:WORD_1 dst_unused:UNUSED_PRESERVE src0_sel:WORD_1\n"
    "v_or_b32_e64 v185, v185, v201\n"
    "v_rcp_f16_sdwa v233, v235 dst_sel:WORD_1 dst_unused:UNUSED_PRESERVE src0_sel:WORD_1\n"
    "v_pk_mul_f16 v192, v192, v232\n"
    "v_or_b32_e64 v186, v186, v202\n"
    "v_pk_mul_f16 v196, v196, v233\n"
    "v_mfma_f32_16x16x32_f16 v[96:99], a[16:19], v[192:195], v[96:99]\n"
    "s_waitcnt lgkmcnt(7)\n"
    "ds_read_u16_d16_hi v207, v207\n"
    "v_pk_fma_f16 v232, v184, v184, s48\n"
    "v_mfma_f32_16x16x32_f16 v[100:103], a[20:23], v[192:195], v[100:103]\n"
    "ds_read_b128 a[240:243], v244 offset:34816\n"
    "v_or_b32_e32 v187, v187, v203\n"
    "v_mfma_f32_16x16x32_f16 v[112:115], a[24:27], v[192:195], v[112:115]\n"
    "ds_write_b128 v253, v[212:215] offset:5120\n"
    "buffer_load_dwordx4 a[228:231], v245, s[40:43], s101 offen offset:128\n"
    "v_mfma_f32_16x16x32_f16 v[116:119], a[28:31], v[192:195], v[116:119]\n"
    "v_pk_fma_f16 v233, v188, v188, s48\n"
    "v_pk_mul_f16 v232, v184, v232\n"
    "v_mfma_f32_16x16x32_f16 v[128:131], v[236:239], v[192:195], v[128:131]\n"
    "v_pk_mul_f16 v233, v188, v233\n"
    "v_lshlrev_b32_sdwa v201, 1, v171 dst_sel:DWORD dst_unused:UNUSED_PAD src0_sel:DWORD src1_sel:WORD_1\n"
    "v_mfma_f32_16x16x32_f16 v[132:135], a[244:247], v[192:195], v[132:135]\n"
    "s_waitcnt lgkmcnt(6)\n"
    "ds_read_u16_d16_hi v201, v201\n"
    "ds_write_b128 v253, v[64:67] offset:1024\n"
    "v_mfma_f32_16x16x32_f16 v[104:107], a[16:19], v[196:199], v[104:107]\n"
    "v_or_b32_e32 v189, v189, v205\n"
    "v_cvt_pk_f16_f32 v192, v168, v169\n"
    "v_mfma_f32_16x16x32_f16 v[108:111], a[20:23], v[196:199], v[108:111]\n"
    "v_lshlrev_b32_sdwa v193, 1, v170 dst_sel:DWORD dst_unused:UNUSED_PAD src0_sel:DWORD src1_sel:WORD_1\n"
    "v_lshlrev_b32_sdwa v194, 1, v172 dst_sel:DWORD dst_unused:UNUSED_PAD src0_sel:DWORD src1_sel:WORD_1\n"
    "v_mfma_f32_16x16x32_f16 v[120:123], a[24:27], v[196:199], v[120:123]\n"
    "s_waitcnt lgkmcnt(6)\n"
    "s_nop 0\n"
    "ds_read_u16 v193, v193\n"
    "ds_read_u16 v194, v194\n"
    "v_mfma_f32_16x16x32_f16 v[124:127], a[28:31], v[196:199], v[124:127]\n"
    "ds_write_b128 v253, v[68:71] offset:4096\n"
    "v_mfma_f32_16x16x32_f16 v[136:139], v[236:239], v[196:199], v[136:139]\n"
    "buffer_load_dwordx4 v[24:27], v247, s[72:75], s63 offen offset:0\n"
    "v_lshlrev_b32_sdwa v202, 1, v173 dst_sel:DWORD dst_unused:UNUSED_PAD src0_sel:DWORD src1_sel:WORD_1\n"
    "v_smfmac_f32_32x32x32_bf16 v[96:111], a[248:251], a[232:239], v241\n"
    "s_waitcnt lgkmcnt(5)\n"
    "v_lshlrev_b32_sdwa v195, 1, v174 dst_sel:DWORD dst_unused:UNUSED_PAD src0_sel:DWORD src1_sel:WORD_1\n"
    "v_lshlrev_b32_sdwa v203, 1, v175 dst_sel:DWORD dst_unused:UNUSED_PAD src0_sel:DWORD src1_sel:WORD_1\n"
    "v_exp_f16_e32 v234, v232\n"
    "v_or_b32_e64 v190, v190, v206\n"
    "v_mfma_f32_16x16x32_f16 v[140:143], a[244:247], v[196:199], v[140:143]\n"
    "ds_read_u16_d16_hi v202, v202\n"
    "ds_read_b128 v[236:239], v244 offset:8192\n"
    "ds_write_b128 v253, v[80:83] offset:0\n"
    "v_mfma_f32_16x16x32_bf16 v[168:171], a[160:163], v[144:147], 0\n"
    "v_cvt_pk_f16_f32 v196, v176, v177\n"
    "v_lshlrev_b32_sdwa v197, 1, v178 dst_sel:DWORD dst_unused:UNUSED_PAD src0_sel:DWORD src1_sel:WORD_1\n"
    "v_mfma_f32_16x16x32_bf16 v[168:171], a[208:211], v[148:151], v[168:171]\n"
    "v_exp_f16_e64 v235, v233\n"
    "v_mfma_f32_16x16x32_bf16 v[168:171], a[120:123], v[152:155], v[168:171]\n"
    "ds_read_u16 v195, v195\n"
    "ds_read_u16_d16_hi v203, v203\n"
    "v_mfma_f32_16x16x32_bf16 v[172:175], a[164:167], v[144:147], 0\n"
    "v_lshlrev_b32_sdwa v205, 1, v179 dst_sel:DWORD dst_unused:UNUSED_PAD src0_sel:DWORD src1_sel:WORD_1\n"
    "v_lshlrev_b32_sdwa v198, 1, v180 dst_sel:DWORD dst_unused:UNUSED_PAD src0_sel:DWORD src1_sel:WORD_1\n"
    "v_mfma_f32_16x16x32_bf16 v[172:175], a[212:215], v[148:151], v[172:175]\n"
    "s_waitcnt lgkmcnt(7)\n"
    "s_nop 0\n"
    "ds_read_b128 a[244:247], v244 offset:10240\n"
    "buffer_load_dwordx4 v[28:31], v247, s[72:75], s63 offen offset:192\n"
    "ds_write_b128 v253, v[84:87] offset:3072\n"
    "v_lshlrev_b32_sdwa v206, 1, v181 dst_sel:DWORD dst_unused:UNUSED_PAD src0_sel:DWORD src1_sel:WORD_1\n"
    "v_lshlrev_b32_sdwa v199, 1, v182 dst_sel:DWORD dst_unused:UNUSED_PAD src0_sel:DWORD src1_sel:WORD_1\n"
    "v_exp_f16_sdwa v234, v232 dst_sel:WORD_1 dst_unused:UNUSED_PRESERVE src0_sel:WORD_1\n"
    "v_mfma_f32_16x16x32_bf16 v[172:175], a[240:243], v[152:155], v[172:175]\n"
    "v_or_b32_e64 v191, v191, v207\n"
    "v_lshlrev_b32_sdwa v207, 1, v183 dst_sel:DWORD dst_unused:UNUSED_PAD src0_sel:DWORD src1_sel:WORD_1\n"
    "v_mfma_f32_16x16x32_bf16 v[176:179], a[160:163], v[156:159], 0\n"
    "ds_read_b128 a[232:235], v243 offset:26688\n"
    "ds_read_u16 v197, v197\n"
    "v_exp_f16_sdwa v235, v233 dst_sel:WORD_1 dst_unused:UNUSED_PRESERVE src0_sel:WORD_1\n"
    "v_mfma_f32_16x16x32_bf16 v[176:179], a[208:211], v[160:163], v[176:179]\n"
    "s_waitcnt lgkmcnt(7)\n"
    "s_nop 0\n"
    "ds_read_b128 a[236:239], v243 offset:27712\n"
    "ds_read_u16_d16_hi v205, v205\n"
    "v_mfma_f32_16x16x32_bf16 v[176:179], a[120:123], v[164:167], v[176:179]\n"
    "v_pk_fma_f16 v234, v234, s49, s49\n"
    "v_pk_fma_f16 v235, v235, s49, s49\n"
    "v_mfma_f32_16x16x32_bf16 v[180:183], a[164:167], v[156:159], 0\n"
    "ds_read_u16 v198, v198\n"
    "v_rcp_f16_e64 v232, v234\n"
    "v_mfma_f32_16x16x32_bf16 v[180:183], a[212:215], v[160:163], v[180:183]\n"
    "v_rcp_f16_e64 v233, v235\n"
    "v_mfma_f32_16x16x32_bf16 v[180:183], a[240:243], v[164:167], v[180:183]\n"
    "buffer_load_dwordx4 v[32:35], v246, s[72:75], s62 offen offset:64\n"
    "ds_read_u16_d16_hi v206, v206\n"
    "v_rcp_f16_sdwa v232, v234 dst_sel:WORD_1 dst_unused:UNUSED_PRESERVE src0_sel:WORD_1\n"
    "v_or_b32_e64 v193, v193, v201\n"
    "v_rcp_f16_sdwa v233, v235 dst_sel:WORD_1 dst_unused:UNUSED_PRESERVE src0_sel:WORD_1\n"
    "v_pk_mul_f16 v184, v184, v232\n"
    "v_or_b32_e64 v194, v194, v202\n"
    "v_pk_mul_f16 v188, v188, v233\n"
    "v_mfma_f32_16x16x32_f16 v[96:99], a[32:35], v[184:187], v[96:99]\n"
    "s_waitcnt lgkmcnt(6)\n"
    "ds_read_u16 v199, v199\n"
    "ds_read_u16_d16_hi v207, v207\n"
    "v_mfma_f32_16x16x32_f16 v[100:103], a[36:39], v[184:187], v[100:103]\n"
    "s_barrier\n"
    "v_mfma_f32_16x16x32_f16 v[112:115], a[40:43], v[184:187], v[112:115]\n"
    "ds_read_b128 a[240:243], v244 offset:38912\n"
    "v_pk_fma_f16 v232, v192, v192, s48\n"
    "v_mfma_f32_16x16x32_f16 v[116:119], a[44:47], v[184:187], v[116:119]\n"
    "v_or_b32_e64 v195, v195, v203\n"
    "v_pk_fma_f16 v233, v196, v196, s48\n"
    "v_mfma_f32_16x16x32_f16 v[128:131], v[236:239], v[184:187], v[128:131]\n"
    "v_pk_mul_f16 v232, v192, v232\n"
    "v_pk_mul_f16 v233, v196, v233\n"
    "v_mfma_f32_16x16x32_f16 v[132:135], a[244:247], v[184:187], v[132:135]\n"
    "v_cvt_pk_f16_f32 v184, v168, v169\n"
    "v_lshlrev_b32_sdwa v185, 1, v170 dst_sel:DWORD dst_unused:UNUSED_PAD src0_sel:DWORD src1_sel:WORD_1\n"
    "v_mfma_f32_16x16x32_f16 v[104:107], a[32:35], v[188:191], v[104:107]\n"
    "s_waitcnt lgkmcnt(5)\n"
    "buffer_load_dwordx4 v[36:39], v246, s[72:75], s62 offen offset:256\n"
    "ds_read_u16 v185, v185\n"
    "v_mfma_f32_16x16x32_f16 v[108:111], a[36:39], v[188:191], v[108:111]\n"
    "v_or_b32_e32 v197, v197, v205\n"
    "v_lshlrev_b32_sdwa v201, 1, v171 dst_sel:DWORD dst_unused:UNUSED_PAD src0_sel:DWORD src1_sel:WORD_1\n"
    "v_mfma_f32_16x16x32_f16 v[120:123], a[40:43], v[188:191], v[120:123]\n"
    "ds_read_u16_d16_hi v201, v201\n"
    "v_lshlrev_b32_sdwa v186, 1, v172 dst_sel:DWORD dst_unused:UNUSED_PAD src0_sel:DWORD src1_sel:WORD_1\n"
    "v_mfma_f32_16x16x32_f16 v[124:127], a[44:47], v[188:191], v[124:127]\n"
    "ds_read_u16 v186, v186\n"
    "v_lshlrev_b32_sdwa v202, 1, v173 dst_sel:DWORD dst_unused:UNUSED_PAD src0_sel:DWORD src1_sel:WORD_1\n"
    "v_mfma_f32_16x16x32_f16 v[136:139], v[236:239], v[188:191], v[136:139]\n"
    "s_waitcnt lgkmcnt(6)\n"
    "s_nop 0\n"
    "ds_read_u16_d16_hi v202, v202\n"
    "ds_read_b128 v[236:239], v244 offset:12288\n"
    "v_smfmac_f32_32x32x32_bf16 v[112:127], a[248:251], a[232:239], v241\n"
    "v_lshlrev_b32_sdwa v187, 1, v174 dst_sel:DWORD dst_unused:UNUSED_PAD src0_sel:DWORD src1_sel:WORD_1\n"
    "v_lshlrev_b32_sdwa v203, 1, v175 dst_sel:DWORD dst_unused:UNUSED_PAD src0_sel:DWORD src1_sel:WORD_1\n"
    "v_exp_f16_e64 v234, v232\n"
    "v_or_b32_e64 v198, v198, v206\n"
    "v_mfma_f32_16x16x32_f16 v[140:143], a[244:247], v[188:191], v[140:143]\n"
    "s_waitcnt lgkmcnt(5)\n"
    "ds_read_u16 v187, v187\n"
    "ds_read_u16_d16_hi v203, v203\n"
    "v_mfma_f32_16x16x32_bf16 v[168:171], a[168:171], v[144:147], 0\n"
    "ds_read_b128 a[244:247], v244 offset:14336\n"
    "v_mov_b32_e32 v64, v216\n"
    "v_mfma_f32_16x16x32_bf16 v[168:171], a[216:219], v[148:151], v[168:171]\n"
    "ds_read_b128 a[232:235], v243 offset:28736\n"
    "v_cvt_pk_f16_f32 v188, v176, v177\n"
    "v_mfma_f32_16x16x32_bf16 v[168:171], a[124:127], v[152:155], v[168:171]\n"
    "buffer_load_dwordx4 v[40:43], v247, s[72:75], s62 offen offset:64\n"
    "v_mov_b32_e64 v65, v217\n"
    "v_mfma_f32_16x16x32_bf16 v[172:175], a[172:175], v[144:147], 0\n"
    "v_lshlrev_b32_sdwa v189, 1, v178 dst_sel:DWORD dst_unused:UNUSED_PAD src0_sel:DWORD src1_sel:WORD_1\n"
    "v_mov_b32_e64 v66, v218\n"
    "v_exp_f16_e64 v235, v233\n"
    "v_mfma_f32_16x16x32_bf16 v[172:175], a[220:223], v[148:151], v[172:175]\n"
    "s_waitcnt lgkmcnt(7)\n"
    "ds_read_u16 v189, v189\n"
    "ds_read_b128 a[236:239], v243 offset:29760\n"
    "v_lshlrev_b32_sdwa v205, 1, v179 dst_sel:DWORD dst_unused:UNUSED_PAD src0_sel:DWORD src1_sel:WORD_1\n"
    "v_mov_b32_e32 v67, v219\n"
    "v_lshlrev_b32_sdwa v190, 1, v180 dst_sel:DWORD dst_unused:UNUSED_PAD src0_sel:DWORD src1_sel:WORD_1\n"
    "v_mov_b32_e64 v68, v220\n"
    "v_lshlrev_b32_sdwa v206, 1, v181 dst_sel:DWORD dst_unused:UNUSED_PAD src0_sel:DWORD src1_sel:WORD_1\n"
    "v_mov_b32_e64 v69, v221\n"
    "v_lshlrev_b32_sdwa v191, 1, v182 dst_sel:DWORD dst_unused:UNUSED_PAD src0_sel:DWORD src1_sel:WORD_1\n"
    "v_mov_b32_e64 v70, v222\n"
    "v_mov_b32_e64 v71, v223\n"
    "v_exp_f16_sdwa v234, v232 dst_sel:WORD_1 dst_unused:UNUSED_PRESERVE src0_sel:WORD_1\n"
    "v_mfma_f32_16x16x32_bf16 v[172:175], a[240:243], v[152:155], v[172:175]\n"
    "s_waitcnt lgkmcnt(6)\n"
    "ds_read_u16_d16_hi v205, v205\n"
    "v_or_b32_e32 v199, v199, v207\n"
    "v_mfma_f32_16x16x32_bf16 v[176:179], a[168:171], v[156:159], 0\n"
    "v_lshlrev_b32_sdwa v207, 1, v183 dst_sel:DWORD dst_unused:UNUSED_PAD src0_sel:DWORD src1_sel:WORD_1\n"
    "v_exp_f16_sdwa v235, v233 dst_sel:WORD_1 dst_unused:UNUSED_PRESERVE src0_sel:WORD_1\n"
    "v_mfma_f32_16x16x32_bf16 v[176:179], a[216:219], v[160:163], v[176:179]\n"
    "ds_read_u16 v190, v190\n"
    "ds_read_b128 v[144:147], v242 offset:0\n"
    "v_mfma_f32_16x16x32_bf16 v[176:179], a[124:127], v[164:167], v[176:179]\n"
    "buffer_load_dwordx4 v[44:47], v247, s[72:75], s62 offen offset:256\n"
    "v_pk_fma_f16 v234, v234, s49, s49\n"
    "v_mfma_f32_16x16x32_bf16 v[180:183], a[172:175], v[156:159], 0\n"
    "s_waitcnt lgkmcnt(6)\n"
    "s_nop 0\n"
    "ds_read_u16_d16_hi v206, v206\n"
    "ds_read_b128 v[148:151], v242 offset:1024\n"
    "v_pk_fma_f16 v235, v235, s49, s49\n"
    "v_mfma_f32_16x16x32_bf16 v[180:183], a[220:223], v[160:163], v[180:183]\n"
    "ds_read_u16 v191, v191\n"
    "ds_read_b128 v[152:155], v242 offset:2048\n"
    "v_mfma_f32_16x16x32_bf16 v[180:183], a[240:243], v[164:167], v[180:183]\n"
    "v_rcp_f16_e64 v232, v234\n"
    "v_rcp_f16_e64 v233, v235\n"
    "v_rcp_f16_sdwa v232, v234 dst_sel:WORD_1 dst_unused:UNUSED_PRESERVE src0_sel:WORD_1\n"
    "v_or_b32_e64 v185, v185, v201\n"
    "v_rcp_f16_sdwa v233, v235 dst_sel:WORD_1 dst_unused:UNUSED_PRESERVE src0_sel:WORD_1\n"
    "v_pk_mul_f16 v192, v192, v232\n"
    "v_or_b32_e64 v186, v186, v202\n"
    "v_pk_mul_f16 v196, v196, v233\n"
    "v_mfma_f32_16x16x32_f16 v[96:99], a[48:51], v[192:195], v[96:99]\n"
    "ds_read_u16_d16_hi v207, v207\n"
    "ds_read_b128 v[156:159], v242 offset:3072\n"
    "ds_read_b128 v[160:163], v242 offset:4096\n"
    "v_mfma_f32_16x16x32_f16 v[100:103], a[52:55], v[192:195], v[100:103]\n"
    "s_waitcnt lgkmcnt(9)\n"
    "ds_read_b128 v[164:167], v242 offset:5120\n"
    "ds_read_b128 a[240:243], v244 offset:32768\n"
    "v_mfma_f32_16x16x32_f16 v[112:115], a[56:59], v[192:195], v[112:115]\n"
    "v_pk_fma_f16 v232, v184, v184, s48\n"
    "v_or_b32_e32 v187, v187, v203\n"
    "v_mfma_f32_16x16x32_f16 v[116:119], a[60:63], v[192:195], v[116:119]\n"
    "buffer_load_dwordx4 v[48:51], v246, s[72:75], s63 offen offset:64\n"
    "v_pk_fma_f16 v233, v188, v188, s48\n"
    "v_mfma_f32_16x16x32_f16 v[128:131], v[236:239], v[192:195], v[128:131]\n"
    "v_pk_mul_f16 v232, v184, v232\n"
    "v_or_b32_e64 v189, v189, v205\n"
    "v_mfma_f32_16x16x32_f16 v[132:135], a[244:247], v[192:195], v[132:135]\n"
    "s_waitcnt lgkmcnt(7)\n"
    "s_nop 0\n"
    "v_pk_mul_f16 v233, v188, v233\n"
    "v_cvt_pk_f16_f32 v192, v168, v169\n"
    "v_mfma_f32_16x16x32_f16 v[104:107], a[48:51], v[196:199], v[104:107]\n"
    "v_lshlrev_b32_sdwa v193, 1, v170 dst_sel:DWORD dst_unused:UNUSED_PAD src0_sel:DWORD src1_sel:WORD_1\n"
    "v_lshlrev_b32_sdwa v201, 1, v171 dst_sel:DWORD dst_unused:UNUSED_PAD src0_sel:DWORD src1_sel:WORD_1\n"
    "v_mfma_f32_16x16x32_f16 v[108:111], a[52:55], v[196:199], v[108:111]\n"
    "ds_read_u16 v193, v193\n"
    "ds_read_u16_d16_hi v201, v201\n"
    "v_mfma_f32_16x16x32_f16 v[120:123], a[56:59], v[196:199], v[120:123]\n"
    "v_lshlrev_b32_sdwa v194, 1, v172 dst_sel:DWORD dst_unused:UNUSED_PAD src0_sel:DWORD src1_sel:WORD_1\n"
    "v_lshlrev_b32_sdwa v202, 1, v173 dst_sel:DWORD dst_unused:UNUSED_PAD src0_sel:DWORD src1_sel:WORD_1\n"
    "v_mfma_f32_16x16x32_f16 v[124:127], a[60:63], v[196:199], v[124:127]\n"
    "ds_read_u16 v194, v194\n"
    "ds_read_u16_d16_hi v202, v202\n"
    "v_mfma_f32_16x16x32_f16 v[136:139], v[236:239], v[196:199], v[136:139]\n"
    "ds_read_b128 v[236:239], v244 offset:16384\n"
    "v_lshlrev_b32_sdwa v195, 1, v174 dst_sel:DWORD dst_unused:UNUSED_PAD src0_sel:DWORD src1_sel:WORD_1\n"
    "v_mfma_f32_16x16x32_f16 v[140:143], a[244:247], v[196:199], v[140:143]\n"
    "s_waitcnt lgkmcnt(7)\n"
    "ds_read_u16 v195, v195\n"
    "v_lshlrev_b32_sdwa v203, 1, v175 dst_sel:DWORD dst_unused:UNUSED_PAD src0_sel:DWORD src1_sel:WORD_1\n"
    "v_mfma_f32_16x16x32_bf16 v[168:171], a[128:131], v[144:147], 0\n"
    "v_exp_f16_e32 v234, v232\n"
    "v_mfma_f32_16x16x32_bf16 v[168:171], a[176:179], v[148:151], v[168:171]\n"
    "buffer_load_dwordx4 v[52:55], v246, s[72:75], s63 offen offset:256\n"
    "ds_read_u16_d16_hi v203, v203\n"
    "v_smfmac_f32_32x32x32_bf16 v[128:143], a[248:251], a[232:239], v241\n"
    "v_or_b32_e64 v190, v190, v206\n"
    "v_cvt_pk_f16_f32 v196, v176, v177\n"
    "v_lshlrev_b32_sdwa v197, 1, v178 dst_sel:DWORD dst_unused:UNUSED_PAD src0_sel:DWORD src1_sel:WORD_1\n"
    "v_exp_f16_e64 v235, v233\n"
    "v_mfma_f32_16x16x32_bf16 v[168:171], a[96:99], v[152:155], v[168:171]\n"
    "ds_read_b128 a[244:247], v244 offset:18432\n"
    "ds_read_u16 v197, v197\n"
    "v_mfma_f32_16x16x32_bf16 v[172:175], a[132:135], v[144:147], 0\n"
    "v_lshlrev_b32_sdwa v205, 1, v179 dst_sel:DWORD dst_unused:UNUSED_PAD src0_sel:DWORD src1_sel:WORD_1\n"
    "v_lshlrev_b32_sdwa v198, 1, v180 dst_sel:DWORD dst_unused:UNUSED_PAD src0_sel:DWORD src1_sel:WORD_1\n"
    "v_mfma_f32_16x16x32_bf16 v[172:175], a[180:183], v[148:151], v[172:175]\n"
    "v_lshlrev_b32_sdwa v206, 1, v181 dst_sel:DWORD dst_unused:UNUSED_PAD src0_sel:DWORD src1_sel:WORD_1\n"
    "v_lshlrev_b32_sdwa v199, 1, v182 dst_sel:DWORD dst_unused:UNUSED_PAD src0_sel:DWORD src1_sel:WORD_1\n"
    "v_exp_f16_sdwa v234, v232 dst_sel:WORD_1 dst_unused:UNUSED_PRESERVE src0_sel:WORD_1\n"
    "s_waitcnt lgkmcnt(7)\n"
    "v_mfma_f32_16x16x32_bf16 v[172:175], a[240:243], v[152:155], v[172:175]\n"
    "ds_read_u16_d16_hi v205, v205\n"
    "v_or_b32_e32 v191, v191, v207\n"
    "v_mfma_f32_16x16x32_bf16 v[176:179], a[128:131], v[156:159], 0\n"
    "v_lshlrev_b32_sdwa v207, 1, v183 dst_sel:DWORD dst_unused:UNUSED_PAD src0_sel:DWORD src1_sel:WORD_1\n"
    "v_exp_f16_sdwa v235, v233 dst_sel:WORD_1 dst_unused:UNUSED_PRESERVE src0_sel:WORD_1\n"
    "v_mfma_f32_16x16x32_bf16 v[176:179], a[176:179], v[160:163], v[176:179]\n"
    "ds_read_u16 v198, v198\n"
    "v_pk_fma_f16 v234, v234, s49, s49\n"
    "v_mfma_f32_16x16x32_bf16 v[176:179], a[96:99], v[164:167], v[176:179]\n"
    "s_waitcnt lgkmcnt(6)\n"
    "buffer_load_dwordx4 v[56:59], v247, s[72:75], s63 offen offset:64\n"
    "ds_read_u16_d16_hi v206, v206\n"
    "v_mfma_f32_16x16x32_bf16 v[180:183], a[132:135], v[156:159], 0\n"
    "v_pk_fma_f16 v235, v235, s49, s49\n"
    "v_rcp_f16_e32 v232, v234\n"
    "v_mfma_f32_16x16x32_bf16 v[180:183], a[180:183], v[160:163], v[180:183]\n"
    "ds_read_u16 v199, v199\n"
    "ds_read_u16_d16_hi v207, v207\n"
    "v_mfma_f32_16x16x32_bf16 v[180:183], a[240:243], v[164:167], v[180:183]\n"
    "v_rcp_f16_e64 v233, v235\n"
    "v_rcp_f16_sdwa v232, v234 dst_sel:WORD_1 dst_unused:UNUSED_PRESERVE src0_sel:WORD_1\n"
    "v_or_b32_e64 v193, v193, v201\n"
    "v_rcp_f16_sdwa v233, v235 dst_sel:WORD_1 dst_unused:UNUSED_PRESERVE src0_sel:WORD_1\n"
    "v_pk_mul_f16 v184, v184, v232\n"
    "v_or_b32_e64 v194, v194, v202\n"
    "v_pk_mul_f16 v188, v188, v233\n"
    "v_mfma_f32_16x16x32_f16 v[96:99], a[64:67], v[184:187], v[96:99]\n"
    "s_waitcnt lgkmcnt(6)\n"
    "v_pk_fma_f16 v232, v192, v192, s48\n"
    "v_or_b32_e32 v195, v195, v203\n"
    "v_mfma_f32_16x16x32_f16 v[100:103], a[68:71], v[184:187], v[100:103]\n"
    "s_barrier\n"
    "v_mfma_f32_16x16x32_f16 v[112:115], a[72:75], v[184:187], v[112:115]\n"
    "s_waitcnt lgkmcnt(4)\n"
    "v_pk_fma_f16 v233, v196, v196, s48\n"
    "v_pk_mul_f16 v232, v192, v232\n"
    "v_mfma_f32_16x16x32_f16 v[116:119], a[76:79], v[184:187], v[116:119]\n"
    "buffer_load_dwordx4 v[60:63], v247, s[72:75], s63 offen offset:256\n"
    "v_or_b32_e64 v197, v197, v205\n"
    "v_mfma_f32_16x16x32_f16 v[128:131], v[236:239], v[184:187], v[128:131]\n"
    "v_pk_mul_f16 v233, v196, v233\n"
    "v_exp_f16_e64 v234, v232\n"
    "v_mfma_f32_16x16x32_f16 v[132:135], a[244:247], v[184:187], v[132:135]\n"
    "s_waitcnt lgkmcnt(2)\n"
    "v_or_b32_e32 v198, v198, v206\n"
    "v_exp_f16_e64 v235, v233\n"
    "v_mfma_f32_16x16x32_f16 v[104:107], a[64:67], v[188:191], v[104:107]\n"
    "v_exp_f16_sdwa v234, v232 dst_sel:WORD_1 dst_unused:UNUSED_PRESERVE src0_sel:WORD_1\n"
    "v_mfma_f32_16x16x32_f16 v[108:111], a[68:71], v[188:191], v[108:111]\n"
    "v_exp_f16_sdwa v235, v233 dst_sel:WORD_1 dst_unused:UNUSED_PRESERVE src0_sel:WORD_1\n"
    "v_mfma_f32_16x16x32_f16 v[120:123], a[72:75], v[188:191], v[120:123]\n"
    "s_waitcnt lgkmcnt(0)\n"
    "v_or_b32_e32 v199, v199, v207\n"
    "v_pk_fma_f16 v234, v234, s49, s49\n"
    "v_mfma_f32_16x16x32_f16 v[124:127], a[76:79], v[188:191], v[124:127]\n"
    "v_pk_fma_f16 v235, v235, s49, s49\n"
    "v_rcp_f16_e64 v232, v234\n"
    "v_mfma_f32_16x16x32_f16 v[136:139], v[236:239], v[188:191], v[136:139]\n"
    "v_rcp_f16_e32 v233, v235\n"
    "s_mov_b32 s59, s58\n"
    "s_add_u32 s57, s57, 1572864\n"
    "s_add_u32 s58, s58, 1572864\n"
    "s_mov_b32 s70, s46\n"
    "s_mov_b32 s60, s62\n"
    "s_mov_b32 s61, s63\n"
    "s_nop 0\n"
    "s_add_u32 s62, s62, 1572864\n"
    "s_add_u32 s63, s63, 1572864\n"
    "s_add_u32 s100, s57, 98304\n"
    "s_add_u32 s101, s58, 98304\n"
    "s_add_u32 s39, s59, 98304\n"
    "s_add_u32 s64, s64, 1\n"
    "s_cmp_lt_u32 s64, 2\n"
    "s_cselect_b32 s74, s42, 0\n"
    "s_cmp_lg_u32 s64, 3\n"
    "s_nop 0\n"
    "s_cbranch_scc1 1b\n"
    "v_mfma_f32_16x16x32_f16 v[140:143], a[244:247], v[188:191], v[140:143]\n"
    "ds_read_b128 a[240:243], v244 offset:36864\n"
    "ds_read_b128 v[236:239], v244 offset:20480\n"
    "ds_read_b128 a[244:247], v244 offset:22528\n"
    "v_cvt_pk_f16_f32 v184, v168, v169\n"
    "v_lshlrev_b32_sdwa v185, 1, v170 dst_sel:DWORD dst_unused:UNUSED_PAD src0_sel:DWORD src1_sel:WORD_1\n"
    "v_lshlrev_b32_sdwa v201, 1, v171 dst_sel:DWORD dst_unused:UNUSED_PAD src0_sel:DWORD src1_sel:WORD_1\n"
    "v_mfma_f32_16x16x32_bf16 v[168:171], a[136:139], v[144:147], 0\n"
    "ds_read_u16 v185, v185\n"
    "ds_read_u16_d16_hi v201, v201\n"
    "v_mfma_f32_16x16x32_bf16 v[168:171], a[184:187], v[148:151], v[168:171]\n"
    "v_lshlrev_b32_sdwa v186, 1, v172 dst_sel:DWORD dst_unused:UNUSED_PAD src0_sel:DWORD src1_sel:WORD_1\n"
    "v_lshlrev_b32_sdwa v202, 1, v173 dst_sel:DWORD dst_unused:UNUSED_PAD src0_sel:DWORD src1_sel:WORD_1\n"
    "v_mfma_f32_16x16x32_bf16 v[168:171], a[100:103], v[152:155], v[168:171]\n"
    "ds_read_u16 v186, v186\n"
    "ds_read_u16_d16_hi v202, v202\n"
    "s_waitcnt vmcnt(10)\n"
    "s_nop 0\n"
    "v_smfmac_f32_32x32x32_bf16 v[128:143], a[248:251], a[224:231], v241\n"
    "v_lshlrev_b32_sdwa v187, 1, v174 dst_sel:DWORD dst_unused:UNUSED_PAD src0_sel:DWORD src1_sel:WORD_1\n"
    "v_lshlrev_b32_sdwa v203, 1, v175 dst_sel:DWORD dst_unused:UNUSED_PAD src0_sel:DWORD src1_sel:WORD_1\n"
    "v_cvt_pk_f16_f32 v188, v176, v177\n"
    "v_lshlrev_b32_sdwa v189, 1, v178 dst_sel:DWORD dst_unused:UNUSED_PAD src0_sel:DWORD src1_sel:WORD_1\n"
    "v_lshlrev_b32_sdwa v205, 1, v179 dst_sel:DWORD dst_unused:UNUSED_PAD src0_sel:DWORD src1_sel:WORD_1\n"
    "v_mfma_f32_16x16x32_bf16 v[172:175], a[140:143], v[144:147], 0\n"
    "ds_read_u16 v187, v187\n"
    "v_lshlrev_b32_sdwa v190, 1, v180 dst_sel:DWORD dst_unused:UNUSED_PAD src0_sel:DWORD src1_sel:WORD_1\n"
    "v_mfma_f32_16x16x32_bf16 v[172:175], a[188:191], v[148:151], v[172:175]\n"
    "s_waitcnt lgkmcnt(7)\n"
    "s_nop 0\n"
    "ds_read_u16_d16_hi v203, v203\n"
    "v_lshlrev_b32_sdwa v206, 1, v181 dst_sel:DWORD dst_unused:UNUSED_PAD src0_sel:DWORD src1_sel:WORD_1\n"
    "v_mfma_f32_16x16x32_bf16 v[172:175], a[240:243], v[152:155], v[172:175]\n"
    "v_lshlrev_b32_sdwa v191, 1, v182 dst_sel:DWORD dst_unused:UNUSED_PAD src0_sel:DWORD src1_sel:WORD_1\n"
    "v_lshlrev_b32_sdwa v207, 1, v183 dst_sel:DWORD dst_unused:UNUSED_PAD src0_sel:DWORD src1_sel:WORD_1\n"
    "v_mfma_f32_16x16x32_bf16 v[176:179], a[136:139], v[156:159], 0\n"
    "ds_read_u16 v189, v189\n"
    "buffer_load_dwordx4 a[232:235], v245, s[40:43], s57 offen offset:64\n"
    "v_mfma_f32_16x16x32_bf16 v[176:179], a[184:187], v[160:163], v[176:179]\n"
    "ds_read_u16_d16_hi v205, v205\n"
    "v_rcp_f16_sdwa v232, v234 dst_sel:WORD_1 dst_unused:UNUSED_PRESERVE src0_sel:WORD_1\n"
    "v_mfma_f32_16x16x32_bf16 v[176:179], a[100:103], v[164:167], v[176:179]\n"
    "v_rcp_f16_sdwa v233, v235 dst_sel:WORD_1 dst_unused:UNUSED_PRESERVE src0_sel:WORD_1\n"
    "v_mfma_f32_16x16x32_bf16 v[180:183], a[140:143], v[156:159], 0\n"
    "ds_read_u16 v190, v190\n"
    "v_pk_mul_f16 v192, v192, v232\n"
    "v_mfma_f32_16x16x32_bf16 v[180:183], a[188:191], v[160:163], v[180:183]\n"
    "s_waitcnt lgkmcnt(8)\n"
    "v_pk_mul_f16 v196, v196, v233\n"
    "s_waitcnt lgkmcnt(7)\n"
    "v_or_b32_e64 v185, v185, v201\n"
    "v_mfma_f32_16x16x32_bf16 v[180:183], a[240:243], v[164:167], v[180:183]\n"
    "ds_read_u16_d16_hi v206, v206\n"
    "v_pk_fma_f16 v232, v184, v184, s48\n"
    "v_mfma_f32_16x16x32_f16 v[96:99], a[80:83], v[192:195], v[96:99]\n"
    "ds_read_u16 v191, v191\n"
    "v_pk_fma_f16 v233, v188, v188, s48\n"
    "v_mfma_f32_16x16x32_f16 v[100:103], a[84:87], v[192:195], v[100:103]\n"
    "v_pk_mul_f16 v232, v184, v232\n"
    "v_pk_mul_f16 v233, v188, v233\n"
    "v_mfma_f32_16x16x32_f16 v[112:115], a[88:91], v[192:195], v[112:115]\n"
    "s_waitcnt lgkmcnt(7)\n"
    "ds_read_u16_d16_hi v207, v207\n"
    "v_or_b32_e32 v186, v186, v202\n"
    "v_mfma_f32_16x16x32_f16 v[116:119], a[92:95], v[192:195], v[116:119]\n"
    "v_lshlrev_b32_sdwa v201, 1, v171 dst_sel:DWORD dst_unused:UNUSED_PAD src0_sel:DWORD src1_sel:WORD_1\n"
    "v_lshlrev_b32_sdwa v202, 1, v173 dst_sel:DWORD dst_unused:UNUSED_PAD src0_sel:DWORD src1_sel:WORD_1\n"
    "v_mfma_f32_16x16x32_f16 v[128:131], v[236:239], v[192:195], v[128:131]\n"
    "ds_read_u16_d16_hi v201, v201\n"
    "v_exp_f16_e64 v234, v232\n"
    "v_mfma_f32_16x16x32_f16 v[132:135], a[244:247], v[192:195], v[132:135]\n"
    "s_waitcnt lgkmcnt(7)\n"
    "buffer_load_dwordx4 a[236:239], v245, s[40:43], s100 offen offset:64\n"
    "ds_read_u16_d16_hi v202, v202\n"
    "v_mfma_f32_16x16x32_f16 v[104:107], a[80:83], v[196:199], v[104:107]\n"
    "v_or_b32_e32 v187, v187, v203\n"
    "v_cvt_pk_f16_f32 v192, v168, v169\n"
    "v_mfma_f32_16x16x32_f16 v[108:111], a[84:87], v[196:199], v[108:111]\n"
    "v_lshlrev_b32_sdwa v193, 1, v170 dst_sel:DWORD dst_unused:UNUSED_PAD src0_sel:DWORD src1_sel:WORD_1\n"
    "v_lshlrev_b32_sdwa v194, 1, v172 dst_sel:DWORD dst_unused:UNUSED_PAD src0_sel:DWORD src1_sel:WORD_1\n"
    "v_mfma_f32_16x16x32_f16 v[120:123], a[88:91], v[196:199], v[120:123]\n"
    "ds_read_u16 v193, v193\n"
    "ds_read_u16 v194, v194\n"
    "v_mfma_f32_16x16x32_f16 v[124:127], a[92:95], v[196:199], v[124:127]\n"
    "s_waitcnt lgkmcnt(8)\n"
    "v_or_b32_e32 v189, v189, v205\n"
    "v_lshlrev_b32_sdwa v195, 1, v174 dst_sel:DWORD dst_unused:UNUSED_PAD src0_sel:DWORD src1_sel:WORD_1\n"
    "v_mfma_f32_16x16x32_f16 v[136:139], v[236:239], v[196:199], v[136:139]\n"
    "ds_read_b128 v[236:239], v244 offset:0\n"
    "v_lshlrev_b32_sdwa v203, 1, v175 dst_sel:DWORD dst_unused:UNUSED_PAD src0_sel:DWORD src1_sel:WORD_1\n"
    "v_mfma_f32_16x16x32_f16 v[140:143], a[244:247], v[196:199], v[140:143]\n"
    "ds_read_u16 v195, v195\n"
    "v_cvt_pk_f16_f32 v196, v176, v177\n"
    "v_mfma_f32_16x16x32_bf16 v[168:171], a[144:147], v[144:147], 0\n"
    "s_waitcnt lgkmcnt(8)\n"
    "v_or_b32_e32 v190, v190, v206\n"
    "v_lshlrev_b32_sdwa v197, 1, v178 dst_sel:DWORD dst_unused:UNUSED_PAD src0_sel:DWORD src1_sel:WORD_1\n"
    "v_mfma_f32_16x16x32_bf16 v[168:171], a[192:195], v[148:151], v[168:171]\n"
    "ds_read_u16_d16_hi v203, v203\n"
    "v_exp_f16_e64 v235, v233\n"
    "v_mfma_f32_16x16x32_bf16 v[168:171], a[104:107], v[152:155], v[168:171]\n"
    "v_lshlrev_b32_sdwa v205, 1, v179 dst_sel:DWORD dst_unused:UNUSED_PAD src0_sel:DWORD src1_sel:WORD_1\n"
    "v_lshlrev_b32_sdwa v198, 1, v180 dst_sel:DWORD dst_unused:UNUSED_PAD src0_sel:DWORD src1_sel:WORD_1\n"
    "v_mfma_f32_16x16x32_bf16 v[172:175], a[148:151], v[144:147], 0\n"
    "s_waitcnt lgkmcnt(7)\n"
    "s_nop 0\n"
    "ds_read_b128 a[244:247], v244 offset:2048\n"
    "buffer_load_dwordx4 a[224:227], v245, s[40:43], s57 offen offset:0\n"
    "ds_read_u16 v197, v197\n"
    "v_mfma_f32_16x16x32_bf16 v[172:175], a[196:199], v[148:151], v[172:175]\n"
    "v_lshlrev_b32_sdwa v206, 1, v181 dst_sel:DWORD dst_unused:UNUSED_PAD src0_sel:DWORD src1_sel:WORD_1\n"
    "v_lshlrev_b32_sdwa v199, 1, v182 dst_sel:DWORD dst_unused:UNUSED_PAD src0_sel:DWORD src1_sel:WORD_1\n"
    "v_exp_f16_sdwa v234, v232 dst_sel:WORD_1 dst_unused:UNUSED_PRESERVE src0_sel:WORD_1\n"
    "v_mfma_f32_16x16x32_bf16 v[172:175], a[108:111], v[152:155], v[172:175]\n"
    "ds_read_u16_d16_hi v205, v205\n"
    "v_or_b32_e64 v191, v191, v207\n"
    "v_mfma_f32_16x16x32_bf16 v[176:179], a[144:147], v[156:159], 0\n"
    "v_lshlrev_b32_sdwa v207, 1, v183 dst_sel:DWORD dst_unused:UNUSED_PAD src0_sel:DWORD src1_sel:WORD_1\n"
    "v_exp_f16_sdwa v235, v233 dst_sel:WORD_1 dst_unused:UNUSED_PRESERVE src0_sel:WORD_1\n"
    "v_mfma_f32_16x16x32_bf16 v[176:179], a[192:195], v[160:163], v[176:179]\n"
    "ds_read_u16 v198, v198\n"
    "v_pk_fma_f16 v234, v234, s49, s49\n"
    "v_mfma_f32_16x16x32_bf16 v[176:179], a[104:107], v[164:167], v[176:179]\n"
    "s_waitcnt lgkmcnt(8)\n"
    "ds_read_u16_d16_hi v206, v206\n"
    "v_pk_fma_f16 v235, v235, s49, s49\n"
    "v_mfma_f32_16x16x32_bf16 v[180:183], a[148:151], v[156:159], 0\n"
    "s_waitcnt lgkmcnt(7)\n"
    "v_cvt_pk_bf16_f32 v96, v96, v97\n"
    "v_cvt_pk_bf16_f32 v97, v98, v99\n"
    "v_mfma_f32_16x16x32_bf16 v[180:183], a[196:199], v[160:163], v[180:183]\n"
    "ds_read_u16 v199, v199\n"
    "v_rcp_f16_e64 v232, v234\n"
    "v_mfma_f32_16x16x32_bf16 v[180:183], a[108:111], v[164:167], v[180:183]\n"
    "ds_read_u16_d16_hi v207, v207\n"
    "v_cvt_pk_bf16_f32 v98, v100, v101\n"
    "v_cvt_pk_bf16_f32 v99, v102, v103\n"
    "v_cvt_pk_bf16_f32 v112, v112, v113\n"
    "v_rcp_f16_e64 v233, v235\n"
    "ds_write_b128 v242, v[96:99] offset:24640\n"
    "buffer_load_dwordx4 a[228:231], v245, s[40:43], s100 offen offset:0\n"
    "v_cvt_pk_bf16_f32 v113, v114, v115\n"
    "v_cvt_pk_bf16_f32 v114, v116, v117\n"
    "v_cvt_pk_bf16_f32 v115, v118, v119\n"
    "v_rcp_f16_sdwa v232, v234 dst_sel:WORD_1 dst_unused:UNUSED_PRESERVE src0_sel:WORD_1\n"
    "ds_write_b128 v242, v[112:115] offset:26688\n"
    "v_cvt_pk_bf16_f32 v128, v128, v129\n"
    "v_cvt_pk_bf16_f32 v129, v130, v131\n"
    "v_cvt_pk_bf16_f32 v130, v132, v133\n"
    "v_or_b32_e64 v193, v193, v201\n"
    "v_cvt_pk_bf16_f32 v131, v134, v135\n"
    "v_rcp_f16_sdwa v233, v235 dst_sel:WORD_1 dst_unused:UNUSED_PRESERVE src0_sel:WORD_1\n"
    "ds_write_b128 v242, v[128:131] offset:28736\n"
    "v_cvt_pk_bf16_f32 v104, v104, v105\n"
    "v_cvt_pk_bf16_f32 v105, v106, v107\n"
    "v_cvt_pk_bf16_f32 v106, v108, v109\n"
    "v_pk_mul_f16 v184, v184, v232\n"
    "v_cvt_pk_bf16_f32 v107, v110, v111\n"
    "v_or_b32_e64 v194, v194, v202\n"
    "v_mfma_f32_16x16x32_f16 v[96:99], a[0:3], v[184:187], 0\n"
    "s_waitcnt lgkmcnt(4)\n"
    "ds_write_b128 v242, v[104:107] offset:25664\n"
    "v_mfma_f32_16x16x32_f16 v[100:103], a[4:7], v[184:187], 0\n"
    "s_waitcnt lgkmcnt(3)\n"
    "v_cvt_pk_bf16_f32 v120, v120, v121\n"
    "v_cvt_pk_bf16_f32 v121, v122, v123\n"
    "v_mfma_f32_16x16x32_f16 v[112:115], a[8:11], v[184:187], 0\n"
    "v_pk_mul_f16 v188, v188, v233\n"
    "v_cvt_pk_bf16_f32 v122, v124, v125\n"
    "v_mfma_f32_16x16x32_f16 v[116:119], a[12:15], v[184:187], 0\n"
    "v_cvt_pk_bf16_f32 v123, v126, v127\n"
    "v_pk_fma_f16 v232, v192, v192, s48\n"
    "v_mfma_f32_16x16x32_f16 v[128:131], v[236:239], v[184:187], 0\n"
    "ds_write_b128 v242, v[120:123] offset:27712\n"
    "v_mfma_f32_16x16x32_f16 v[132:135], a[244:247], v[184:187], 0\n"
    "v_cvt_pk_bf16_f32 v136, v136, v137\n"
    "v_cvt_pk_bf16_f32 v137, v138, v139\n"
    "v_mfma_f32_16x16x32_f16 v[104:107], a[0:3], v[188:191], 0\n"
    "v_cvt_pk_bf16_f32 v138, v140, v141\n"
    "v_or_b32_e64 v195, v195, v203\n"
    "v_mfma_f32_16x16x32_f16 v[108:111], a[4:7], v[188:191], 0\n"
    "v_cvt_pk_bf16_f32 v139, v142, v143\n"
    "v_pk_fma_f16 v233, v196, v196, s48\n"
    "v_mfma_f32_16x16x32_f16 v[120:123], a[8:11], v[188:191], 0\n"
    "ds_write_b128 v242, v[136:139] offset:29760\n"
    "v_mfma_f32_16x16x32_f16 v[124:127], a[12:15], v[188:191], 0\n"
    "v_pk_mul_f16 v232, v192, v232\n"
    "v_or_b32_e64 v197, v197, v205\n"
    "v_mfma_f32_16x16x32_f16 v[136:139], v[236:239], v[188:191], 0\n"
    "v_pk_mul_f16 v233, v196, v233\n"
    "v_cvt_pk_f16_f32 v184, v168, v169\n"
    "v_mfma_f32_16x16x32_f16 v[140:143], a[244:247], v[188:191], 0\n"
    "ds_read_b128 a[252:255], v249 offset:0\n"
    "ds_read_b128 v[236:239], v244 offset:4096\n"
    "v_lshlrev_b32_sdwa v185, 1, v170 dst_sel:DWORD dst_unused:UNUSED_PAD src0_sel:DWORD src1_sel:WORD_1\n"
    "v_lshlrev_b32_sdwa v201, 1, v171 dst_sel:DWORD dst_unused:UNUSED_PAD src0_sel:DWORD src1_sel:WORD_1\n"
    "v_mfma_f32_16x16x32_bf16 v[168:171], a[152:155], v[144:147], 0\n"
    "ds_read_u16 v185, v185\n"
    "ds_read_u16_d16_hi v201, v201\n"
    "v_mfma_f32_16x16x32_bf16 v[168:171], a[200:203], v[148:151], v[168:171]\n"
    "v_lshlrev_b32_sdwa v186, 1, v172 dst_sel:DWORD dst_unused:UNUSED_PAD src0_sel:DWORD src1_sel:WORD_1\n"
    "v_lshlrev_b32_sdwa v202, 1, v173 dst_sel:DWORD dst_unused:UNUSED_PAD src0_sel:DWORD src1_sel:WORD_1\n"
    "v_lshlrev_b32_sdwa v187, 1, v174 dst_sel:DWORD dst_unused:UNUSED_PAD src0_sel:DWORD src1_sel:WORD_1\n"
    "v_lshlrev_b32_sdwa v203, 1, v175 dst_sel:DWORD dst_unused:UNUSED_PAD src0_sel:DWORD src1_sel:WORD_1\n"
    "v_exp_f16_e64 v234, v232\n"
    "v_mfma_f32_16x16x32_bf16 v[168:171], a[112:115], v[152:155], v[168:171]\n"
    "ds_read_u16 v186, v186\n"
    "ds_read_u16_d16_hi v202, v202\n"
    "ds_read_u16 v187, v187\n"
    "v_mfma_f32_16x16x32_bf16 v[172:175], a[156:159], v[144:147], 0\n"
    "v_or_b32_e64 v198, v198, v206\n"
    "v_cvt_pk_f16_f32 v188, v176, v177\n"
    "v_lshlrev_b32_sdwa v189, 1, v178 dst_sel:DWORD dst_unused:UNUSED_PAD src0_sel:DWORD src1_sel:WORD_1\n"
    "v_exp_f16_e64 v235, v233\n"
    "v_mfma_f32_16x16x32_bf16 v[172:175], a[204:207], v[148:151], v[172:175]\n"
    "ds_read_u16_d16_hi v203, v203\n"
    "v_lshlrev_b32_sdwa v205, 1, v179 dst_sel:DWORD dst_unused:UNUSED_PAD src0_sel:DWORD src1_sel:WORD_1\n"
    "v_lshlrev_b32_sdwa v190, 1, v180 dst_sel:DWORD dst_unused:UNUSED_PAD src0_sel:DWORD src1_sel:WORD_1\n"
    "v_lshlrev_b32_sdwa v206, 1, v181 dst_sel:DWORD dst_unused:UNUSED_PAD src0_sel:DWORD src1_sel:WORD_1\n"
    "v_lshlrev_b32_sdwa v191, 1, v182 dst_sel:DWORD dst_unused:UNUSED_PAD src0_sel:DWORD src1_sel:WORD_1\n"
    "v_exp_f16_sdwa v234, v232 dst_sel:WORD_1 dst_unused:UNUSED_PRESERVE src0_sel:WORD_1\n"
    "v_mfma_f32_16x16x32_bf16 v[172:175], a[116:119], v[152:155], v[172:175]\n"
    "s_waitcnt lgkmcnt(6)\n"
    "ds_read_b128 a[244:247], v244 offset:6144\n"
    "ds_read_u16 v189, v189\n"
    "buffer_store_dwordx4 a[252:255], v255, s[68:71], s59 offen offset:0 sc1\n"
    "v_mfma_f32_16x16x32_bf16 v[176:179], a[152:155], v[156:159], 0\n"
    "ds_read_u16_d16_hi v205, v205\n"
    "v_or_b32_e32 v199, v199, v207\n"
    "v_lshlrev_b32_sdwa v207, 1, v183 dst_sel:DWORD dst_unused:UNUSED_PAD src0_sel:DWORD src1_sel:WORD_1\n"
    "v_exp_f16_sdwa v235, v233 dst_sel:WORD_1 dst_unused:UNUSED_PRESERVE src0_sel:WORD_1\n"
    "v_mfma_f32_16x16x32_bf16 v[176:179], a[200:203], v[160:163], v[176:179]\n"
    "s_waitcnt lgkmcnt(7)\n"
    "s_nop 0\n"
    "ds_read_b128 a[252:255], v249 offset:2048\n"
    "ds_read_u16 v190, v190\n"
    "v_mfma_f32_16x16x32_bf16 v[176:179], a[112:115], v[164:167], v[176:179]\n"
    "v_pk_fma_f16 v234, v234, s49, s49\n"
    "v_pk_fma_f16 v235, v235, s49, s49\n"
    "v_mfma_f32_16x16x32_bf16 v[180:183], a[156:159], v[156:159], 0\n"
    "ds_read_u16_d16_hi v206, v206\n"
    "v_rcp_f16_e64 v232, v234\n"
    "v_mfma_f32_16x16x32_bf16 v[180:183], a[204:207], v[160:163], v[180:183]\n"
    "v_rcp_f16_e64 v233, v235\n"
    "v_mfma_f32_16x16x32_bf16 v[180:183], a[116:119], v[164:167], v[180:183]\n"
    "s_waitcnt lgkmcnt(8)\n"
    "ds_read_u16 v191, v191\n"
    "v_rcp_f16_sdwa v232, v234 dst_sel:WORD_1 dst_unused:UNUSED_PRESERVE src0_sel:WORD_1\n"
    "v_or_b32_e32 v185, v185, v201\n"
    "v_rcp_f16_sdwa v233, v235 dst_sel:WORD_1 dst_unused:UNUSED_PRESERVE src0_sel:WORD_1\n"
    "v_pk_mul_f16 v192, v192, v232\n"
    "v_or_b32_e64 v186, v186, v202\n"
    "v_pk_mul_f16 v196, v196, v233\n"
    "v_mfma_f32_16x16x32_f16 v[96:99], a[16:19], v[192:195], v[96:99]\n"
    "s_waitcnt lgkmcnt(6)\n"
    "ds_read_u16_d16_hi v207, v207\n"
    "ds_read_b128 a[240:243], v244 offset:34816\n"
    "v_mfma_f32_16x16x32_f16 v[100:103], a[20:23], v[192:195], v[100:103]\n"
    "v_pk_fma_f16 v232, v184, v184, s48\n"
    "v_or_b32_e32 v187, v187, v203\n"
    "v_mfma_f32_16x16x32_f16 v[112:115], a[24:27], v[192:195], v[112:115]\n"
    "v_pk_fma_f16 v233, v188, v188, s48\n"
    "v_pk_mul_f16 v232, v184, v232\n"
    "v_mfma_f32_16x16x32_f16 v[116:119], a[28:31], v[192:195], v[116:119]\n"
    "v_pk_mul_f16 v233, v188, v233\n"
    "v_lshlrev_b32_sdwa v201, 1, v171 dst_sel:DWORD dst_unused:UNUSED_PAD src0_sel:DWORD src1_sel:WORD_1\n"
    "v_mfma_f32_16x16x32_f16 v[128:131], v[236:239], v[192:195], v[128:131]\n"
    "ds_read_u16_d16_hi v201, v201\n"
    "s_waitcnt lgkmcnt(6)\n"
    "buffer_store_dwordx4 a[252:255], v255, s[68:71], s59 offen offset:64 sc1\n"
    "v_mfma_f32_16x16x32_f16 v[132:135], a[244:247], v[192:195], v[132:135]\n"
    "v_or_b32_e32 v189, v189, v205\n"
    "v_cvt_pk_f16_f32 v192, v168, v169\n"
    "v_mfma_f32_16x16x32_f16 v[104:107], a[16:19], v[196:199], v[104:107]\n"
    "ds_read_b128 a[252:255], v249 offset:4096\n"
    "v_lshlrev_b32_sdwa v193, 1, v170 dst_sel:DWORD dst_unused:UNUSED_PAD src0_sel:DWORD src1_sel:WORD_1\n"
    "v_mfma_f32_16x16x32_f16 v[108:111], a[20:23], v[196:199], v[108:111]\n"
    "ds_read_u16 v193, v193\n"
    "v_lshlrev_b32_sdwa v194, 1, v172 dst_sel:DWORD dst_unused:UNUSED_PAD src0_sel:DWORD src1_sel:WORD_1\n"
    "v_mfma_f32_16x16x32_f16 v[120:123], a[24:27], v[196:199], v[120:123]\n"
    "s_waitcnt lgkmcnt(6)\n"
    "s_nop 0\n"
    "ds_read_u16 v194, v194\n"
    "v_lshlrev_b32_sdwa v202, 1, v173 dst_sel:DWORD dst_unused:UNUSED_PAD src0_sel:DWORD src1_sel:WORD_1\n"
    "v_mfma_f32_16x16x32_f16 v[124:127], a[28:31], v[196:199], v[124:127]\n"
    "ds_read_u16_d16_hi v202, v202\n"
    "v_lshlrev_b32_sdwa v195, 1, v174 dst_sel:DWORD dst_unused:UNUSED_PAD src0_sel:DWORD src1_sel:WORD_1\n"
    "v_mfma_f32_16x16x32_f16 v[136:139], v[236:239], v[196:199], v[136:139]\n"
    "ds_read_b128 v[236:239], v244 offset:8192\n"
    "ds_read_u16 v195, v195\n"
    "v_mfma_f32_16x16x32_f16 v[140:143], a[244:247], v[196:199], v[140:143]\n"
    "v_lshlrev_b32_sdwa v203, 1, v175 dst_sel:DWORD dst_unused:UNUSED_PAD src0_sel:DWORD src1_sel:WORD_1\n"
    "v_exp_f16_e64 v234, v232\n"
    "v_mfma_f32_16x16x32_bf16 v[168:171], a[160:163], v[144:147], 0\n"
    "s_waitcnt lgkmcnt(7)\n"
    "ds_read_u16_d16_hi v203, v203\n"
    "v_or_b32_e32 v190, v190, v206\n"
    "v_mfma_f32_16x16x32_bf16 v[168:171], a[208:211], v[148:151], v[168:171]\n"
    "ds_read_b128 a[244:247], v244 offset:10240\n"
    "v_cvt_pk_f16_f32 v196, v176, v177\n"
    "v_mfma_f32_16x16x32_bf16 v[168:171], a[120:123], v[152:155], v[168:171]\n"
    "s_waitcnt lgkmcnt(7)\n"
    "buffer_store_dwordx4 a[252:255], v255, s[68:71], s59 offen offset:128 sc1\n"
    "v_mfma_f32_16x16x32_bf16 v[172:175], a[164:167], v[144:147], 0\n"
    "v_lshlrev_b32_sdwa v197, 1, v178 dst_sel:DWORD dst_unused:UNUSED_PAD src0_sel:DWORD src1_sel:WORD_1\n"
    "v_exp_f16_e32 v235, v233\n"
    "v_mfma_f32_16x16x32_bf16 v[172:175], a[212:215], v[148:151], v[172:175]\n"
    "s_waitcnt lgkmcnt(6)\n"
    "s_nop 0\n"
    "ds_read_b128 a[252:255], v249 offset:1024\n"
    "ds_read_u16 v197, v197\n"
    "v_lshlrev_b32_sdwa v205, 1, v179 dst_sel:DWORD dst_unused:UNUSED_PAD src0_sel:DWORD src1_sel:WORD_1\n"
    "v_lshlrev_b32_sdwa v198, 1, v180 dst_sel:DWORD dst_unused:UNUSED_PAD src0_sel:DWORD src1_sel:WORD_1\n"
    "v_lshlrev_b32_sdwa v206, 1, v181 dst_sel:DWORD dst_unused:UNUSED_PAD src0_sel:DWORD src1_sel:WORD_1\n"
    "v_lshlrev_b32_sdwa v199, 1, v182 dst_sel:DWORD dst_unused:UNUSED_PAD src0_sel:DWORD src1_sel:WORD_1\n"
    "v_exp_f16_sdwa v234, v232 dst_sel:WORD_1 dst_unused:UNUSED_PRESERVE src0_sel:WORD_1\n"
    "v_mfma_f32_16x16x32_bf16 v[172:175], a[240:243], v[152:155], v[172:175]\n"
    "ds_read_u16_d16_hi v205, v205\n"
    "v_or_b32_e64 v191, v191, v207\n"
    "v_mfma_f32_16x16x32_bf16 v[176:179], a[160:163], v[156:159], 0\n"
    "s_waitcnt lgkmcnt(6)\n"
    "s_nop 0\n"
    "v_lshlrev_b32_sdwa v207, 1, v183 dst_sel:DWORD dst_unused:UNUSED_PAD src0_sel:DWORD src1_sel:WORD_1\n"
    "v_exp_f16_sdwa v235, v233 dst_sel:WORD_1 dst_unused:UNUSED_PRESERVE src0_sel:WORD_1\n"
    "v_mfma_f32_16x16x32_bf16 v[176:179], a[208:211], v[160:163], v[176:179]\n"
    "ds_read_u16 v198, v198\n"
    "v_pk_fma_f16 v234, v234, s49, s49\n"
    "v_mfma_f32_16x16x32_bf16 v[176:179], a[120:123], v[164:167], v[176:179]\n"
    "ds_read_u16_d16_hi v206, v206\n"
    "v_pk_fma_f16 v235, v235, s49, s49\n"
    "v_mfma_f32_16x16x32_bf16 v[180:183], a[164:167], v[156:159], 0\n"
    "ds_read_u16 v199, v199\n"
    "v_rcp_f16_e64 v232, v234\n"
    "v_mfma_f32_16x16x32_bf16 v[180:183], a[212:215], v[160:163], v[180:183]\n"
    "s_waitcnt lgkmcnt(6)\n"
    "ds_read_u16_d16_hi v207, v207\n"
    "v_rcp_f16_e32 v233, v235\n"
    "v_mfma_f32_16x16x32_bf16 v[180:183], a[240:243], v[164:167], v[180:183]\n"
    "s_waitcnt lgkmcnt(6)\n"
    "buffer_store_dwordx4 a[252:255], v255, s[68:71], s39 offen offset:0 sc1\n"
    "v_rcp_f16_sdwa v232, v234 dst_sel:WORD_1 dst_unused:UNUSED_PRESERVE src0_sel:WORD_1\n"
    "v_or_b32_e32 v193, v193, v201\n"
    "v_rcp_f16_sdwa v233, v235 dst_sel:WORD_1 dst_unused:UNUSED_PRESERVE src0_sel:WORD_1\n"
    "v_pk_mul_f16 v184, v184, v232\n"
    "v_or_b32_e64 v194, v194, v202\n"
    "v_pk_mul_f16 v188, v188, v233\n"
    "v_mfma_f32_16x16x32_f16 v[96:99], a[32:35], v[184:187], v[96:99]\n"
    "ds_read_b128 a[252:255], v249 offset:3072\n"
    "ds_read_b128 a[240:243], v244 offset:38912\n"
    "v_mfma_f32_16x16x32_f16 v[100:103], a[36:39], v[184:187], v[100:103]\n"
    "s_waitcnt lgkmcnt(6)\n"
    "v_pk_fma_f16 v232, v192, v192, s48\n"
    "v_or_b32_e32 v195, v195, v203\n"
    "v_mfma_f32_16x16x32_f16 v[112:115], a[40:43], v[184:187], v[112:115]\n"
    "v_pk_fma_f16 v233, v196, v196, s48\n"
    "v_pk_mul_f16 v232, v192, v232\n"
    "v_mfma_f32_16x16x32_f16 v[116:119], a[44:47], v[184:187], v[116:119]\n"
    "v_or_b32_e64 v197, v197, v205\n"
    "v_pk_mul_f16 v233, v196, v233\n"
    "v_mfma_f32_16x16x32_f16 v[128:131], v[236:239], v[184:187], v[128:131]\n"
    "s_waitcnt lgkmcnt(4)\n"
    "s_nop 0\n"
    "v_lshlrev_b32_sdwa v201, 1, v171 dst_sel:DWORD dst_unused:UNUSED_PAD src0_sel:DWORD src1_sel:WORD_1\n"
    "v_lshlrev_b32_sdwa v202, 1, v173 dst_sel:DWORD dst_unused:UNUSED_PAD src0_sel:DWORD src1_sel:WORD_1\n"
    "v_mfma_f32_16x16x32_f16 v[132:135], a[244:247], v[184:187], v[132:135]\n"
    "ds_read_u16_d16_hi v201, v201\n"
    "ds_read_u16_d16_hi v202, v202\n"
    "v_mfma_f32_16x16x32_f16 v[104:107], a[32:35], v[188:191], v[104:107]\n"
    "v_cvt_pk_f16_f32 v184, v168, v169\n"
    "v_lshlrev_b32_sdwa v185, 1, v170 dst_sel:DWORD dst_unused:UNUSED_PAD src0_sel:DWORD src1_sel:WORD_1\n"
    "v_mfma_f32_16x16x32_f16 v[108:111], a[36:39], v[188:191], v[108:111]\n"
    "ds_read_u16 v185, v185\n"
    "v_lshlrev_b32_sdwa v186, 1, v172 dst_sel:DWORD dst_unused:UNUSED_PAD src0_sel:DWORD src1_sel:WORD_1\n"
    "v_mfma_f32_16x16x32_f16 v[120:123], a[40:43], v[188:191], v[120:123]\n"
    "s_waitcnt lgkmcnt(4)\n"
    "s_nop 0\n"
    "ds_read_u16 v186, v186\n"
    "buffer_store_dwordx4 a[252:255], v255, s[68:71], s39 offen offset:64 sc1\n"
    "v_mfma_f32_16x16x32_f16 v[124:127], a[44:47], v[188:191], v[124:127]\n"
    "v_lshlrev_b32_sdwa v187, 1, v174 dst_sel:DWORD dst_unused:UNUSED_PAD src0_sel:DWORD src1_sel:WORD_1\n"
    "v_lshlrev_b32_sdwa v203, 1, v175 dst_sel:DWORD dst_unused:UNUSED_PAD src0_sel:DWORD src1_sel:WORD_1\n"
    "v_mfma_f32_16x16x32_f16 v[136:139], v[236:239], v[188:191], v[136:139]\n"
    "ds_read_b128 a[252:255], v249 offset:5120\n"
    "ds_read_b128 v[236:239], v244 offset:12288\n"
    "ds_read_u16 v187, v187\n"
    "v_mfma_f32_16x16x32_f16 v[140:143], a[244:247], v[188:191], v[140:143]\n"
    "v_exp_f16_e64 v234, v232\n"
    "v_mfma_f32_16x16x32_bf16 v[168:171], a[168:171], v[144:147], 0\n"
    "ds_read_u16_d16_hi v203, v203\n"
    "ds_read_b128 a[244:247], v244 offset:14336\n"
    "v_mfma_f32_16x16x32_bf16 v[168:171], a[216:219], v[148:151], v[168:171]\n"
    "v_or_b32_e64 v198, v198, v206\n"
    "v_cvt_pk_f16_f32 v188, v176, v177\n"
    "v_mfma_f32_16x16x32_bf16 v[168:171], a[124:127], v[152:155], v[168:171]\n"
    "v_lshlrev_b32_sdwa v189, 1, v178 dst_sel:DWORD dst_unused:UNUSED_PAD src0_sel:DWORD src1_sel:WORD_1\n"
    "v_exp_f16_e64 v235, v233\n"
    "v_mfma_f32_16x16x32_bf16 v[172:175], a[172:175], v[144:147], 0\n"
    "ds_read_u16 v189, v189\n"
    "v_lshlrev_b32_sdwa v205, 1, v179 dst_sel:DWORD dst_unused:UNUSED_PAD src0_sel:DWORD src1_sel:WORD_1\n"
    "v_mfma_f32_16x16x32_bf16 v[172:175], a[220:223], v[148:151], v[172:175]\n"
    "v_lshlrev_b32_sdwa v190, 1, v180 dst_sel:DWORD dst_unused:UNUSED_PAD src0_sel:DWORD src1_sel:WORD_1\n"
    "v_lshlrev_b32_sdwa v206, 1, v181 dst_sel:DWORD dst_unused:UNUSED_PAD src0_sel:DWORD src1_sel:WORD_1\n"
    "v_lshlrev_b32_sdwa v191, 1, v182 dst_sel:DWORD dst_unused:UNUSED_PAD src0_sel:DWORD src1_sel:WORD_1\n"
    "v_exp_f16_sdwa v234, v232 dst_sel:WORD_1 dst_unused:UNUSED_PRESERVE src0_sel:WORD_1\n"
    "s_waitcnt lgkmcnt(7)\n"
    "v_mfma_f32_16x16x32_bf16 v[172:175], a[240:243], v[152:155], v[172:175]\n"
    "ds_read_u16_d16_hi v205, v205\n"
    "v_or_b32_e32 v199, v199, v207\n"
    "v_mfma_f32_16x16x32_bf16 v[176:179], a[168:171], v[156:159], 0\n"
    "s_waitcnt lgkmcnt(6)\n"
    "s_nop 0\n"
    "v_lshlrev_b32_sdwa v207, 1, v183 dst_sel:DWORD dst_unused:UNUSED_PAD src0_sel:DWORD src1_sel:WORD_1\n"
    "v_exp_f16_sdwa v235, v233 dst_sel:WORD_1 dst_unused:UNUSED_PRESERVE src0_sel:WORD_1\n"
    "v_mfma_f32_16x16x32_bf16 v[176:179], a[216:219], v[160:163], v[176:179]\n"
    "ds_read_u16 v190, v190\n"
    "ds_read_b128 v[144:147], v243 offset:0\n"
    "buffer_store_dwordx4 a[252:255], v255, s[68:71], s39 offen offset:128 sc1\n"
    "v_mfma_f32_16x16x32_bf16 v[176:179], a[124:127], v[164:167], v[176:179]\n"
    "v_pk_fma_f16 v234, v234, s49, s49\n"
    "v_pk_fma_f16 v235, v235, s49, s49\n"
    "v_mfma_f32_16x16x32_bf16 v[180:183], a[172:175], v[156:159], 0\n"
    "ds_read_u16_d16_hi v206, v206\n"
    "ds_read_b128 v[148:151], v243 offset:1024\n"
    "ds_read_u16 v191, v191\n"
    "v_mfma_f32_16x16x32_bf16 v[180:183], a[220:223], v[160:163], v[180:183]\n"
    "s_waitcnt lgkmcnt(7)\n"
    "ds_read_b128 v[152:155], v243 offset:2048\n"
    "v_rcp_f16_e32 v232, v234\n"
    "v_mfma_f32_16x16x32_bf16 v[180:183], a[240:243], v[164:167], v[180:183]\n"
    "ds_read_u16_d16_hi v207, v207\n"
    "v_rcp_f16_e64 v233, v235\n"
    "v_rcp_f16_sdwa v232, v234 dst_sel:WORD_1 dst_unused:UNUSED_PRESERVE src0_sel:WORD_1\n"
    "v_or_b32_e64 v185, v185, v201\n"
    "v_rcp_f16_sdwa v233, v235 dst_sel:WORD_1 dst_unused:UNUSED_PRESERVE src0_sel:WORD_1\n"
    "v_pk_mul_f16 v192, v192, v232\n"
    "v_or_b32_e64 v186, v186, v202\n"
    "v_pk_mul_f16 v196, v196, v233\n"
    "v_mfma_f32_16x16x32_f16 v[96:99], a[48:51], v[192:195], v[96:99]\n"
    "s_waitcnt lgkmcnt(7)\n"
    "s_nop 0\n"
    "ds_read_b128 v[156:159], v243 offset:3072\n"
    "ds_read_b128 v[160:163], v243 offset:4096\n"
    "ds_read_b128 v[164:167], v243 offset:5120\n"
    "v_mfma_f32_16x16x32_f16 v[100:103], a[52:55], v[192:195], v[100:103]\n"
    "ds_read_b128 a[240:243], v244 offset:32768\n"
    "v_pk_fma_f16 v232, v184, v184, s48\n"
    "v_mfma_f32_16x16x32_f16 v[112:115], a[56:59], v[192:195], v[112:115]\n"
    "v_or_b32_e64 v187, v187, v203\n"
    "v_pk_fma_f16 v233, v188, v188, s48\n"
    "v_mfma_f32_16x16x32_f16 v[116:119], a[60:63], v[192:195], v[116:119]\n"
    "v_pk_mul_f16 v232, v184, v232\n"
    "v_or_b32_e64 v189, v189, v205\n"
    "v_mfma_f32_16x16x32_f16 v[128:131], v[236:239], v[192:195], v[128:131]\n"
    "s_waitcnt lgkmcnt(7)\n"
    "s_nop 0\n"
    "v_pk_mul_f16 v233, v188, v233\n"
    "v_lshlrev_b32_sdwa v201, 1, v171 dst_sel:DWORD dst_unused:UNUSED_PAD src0_sel:DWORD src1_sel:WORD_1\n"
    "v_mfma_f32_16x16x32_f16 v[132:135], a[244:247], v[192:195], v[132:135]\n"
    "ds_read_u16_d16_hi v201, v201\n"
    "v_cvt_pk_f16_f32 v192, v168, v169\n"
    "v_mfma_f32_16x16x32_f16 v[104:107], a[48:51], v[196:199], v[104:107]\n"
    "v_lshlrev_b32_sdwa v193, 1, v170 dst_sel:DWORD dst_unused:UNUSED_PAD src0_sel:DWORD src1_sel:WORD_1\n"
    "v_lshlrev_b32_sdwa v194, 1, v172 dst_sel:DWORD dst_unused:UNUSED_PAD src0_sel:DWORD src1_sel:WORD_1\n"
    "v_mfma_f32_16x16x32_f16 v[108:111], a[52:55], v[196:199], v[108:111]\n"
    "ds_read_u16 v193, v193\n"
    "ds_read_u16 v194, v194\n"
    "v_mfma_f32_16x16x32_f16 v[120:123], a[56:59], v[196:199], v[120:123]\n"
    "v_lshlrev_b32_sdwa v202, 1, v173 dst_sel:DWORD dst_unused:UNUSED_PAD src0_sel:DWORD src1_sel:WORD_1\n"
    "v_lshlrev_b32_sdwa v195, 1, v174 dst_sel:DWORD dst_unused:UNUSED_PAD src0_sel:DWORD src1_sel:WORD_1\n"
    "v_mfma_f32_16x16x32_f16 v[124:127], a[60:63], v[196:199], v[124:127]\n"
    "ds_read_u16_d16_hi v202, v202\n"
    "ds_read_u16 v195, v195\n"
    "v_mfma_f32_16x16x32_f16 v[136:139], v[236:239], v[196:199], v[136:139]\n"
    "s_waitcnt lgkmcnt(8)\n"
    "ds_read_b128 v[236:239], v244 offset:16384\n"
    "v_lshlrev_b32_sdwa v203, 1, v175 dst_sel:DWORD dst_unused:UNUSED_PAD src0_sel:DWORD src1_sel:WORD_1\n"
    "v_mfma_f32_16x16x32_f16 v[140:143], a[244:247], v[196:199], v[140:143]\n"
    "v_exp_f16_e32 v234, v232\n"
    "v_mfma_f32_16x16x32_bf16 v[168:171], a[128:131], v[144:147], 0\n"
    "s_waitcnt lgkmcnt(7)\n"
    "ds_read_u16_d16_hi v203, v203\n"
    "v_or_b32_e32 v190, v190, v206\n"
    "v_mfma_f32_16x16x32_bf16 v[168:171], a[176:179], v[148:151], v[168:171]\n"
    "s_barrier\n"
    "v_mfma_f32_16x16x32_bf16 v[168:171], a[96:99], v[152:155], v[168:171]\n"
    "ds_read_b128 a[244:247], v244 offset:18432\n"
    "ds_write_b128 v253, v[64:67] offset:2048\n"
    "v_mfma_f32_16x16x32_bf16 v[172:175], a[132:135], v[144:147], 0\n"
    "s_waitcnt lgkmcnt(7)\n"
    "v_cvt_pk_f16_f32 v196, v176, v177\n"
    "v_lshlrev_b32_sdwa v197, 1, v178 dst_sel:DWORD dst_unused:UNUSED_PAD src0_sel:DWORD src1_sel:WORD_1\n"
    "v_exp_f16_e64 v235, v233\n"
    "v_mfma_f32_16x16x32_bf16 v[172:175], a[180:183], v[148:151], v[172:175]\n"
    "v_lshlrev_b32_sdwa v205, 1, v179 dst_sel:DWORD dst_unused:UNUSED_PAD src0_sel:DWORD src1_sel:WORD_1\n"
    "v_lshlrev_b32_sdwa v198, 1, v180 dst_sel:DWORD dst_unused:UNUSED_PAD src0_sel:DWORD src1_sel:WORD_1\n"
    "v_lshlrev_b32_sdwa v206, 1, v181 dst_sel:DWORD dst_unused:UNUSED_PAD src0_sel:DWORD src1_sel:WORD_1\n"
    "v_lshlrev_b32_sdwa v199, 1, v182 dst_sel:DWORD dst_unused:UNUSED_PAD src0_sel:DWORD src1_sel:WORD_1\n"
    "v_exp_f16_sdwa v234, v232 dst_sel:WORD_1 dst_unused:UNUSED_PRESERVE src0_sel:WORD_1\n"
    "v_mfma_f32_16x16x32_bf16 v[172:175], a[240:243], v[152:155], v[172:175]\n"
    "s_waitcnt lgkmcnt(5)\n"
    "ds_read_u16 v197, v197\n"
    "ds_read_u16_d16_hi v205, v205\n"
    "ds_write_b128 v253, v[68:71] offset:5120\n"
    "v_mfma_f32_16x16x32_bf16 v[176:179], a[128:131], v[156:159], 0\n"
    "v_or_b32_e32 v191, v191, v207\n"
    "v_lshlrev_b32_sdwa v207, 1, v183 dst_sel:DWORD dst_unused:UNUSED_PAD src0_sel:DWORD src1_sel:WORD_1\n"
    "v_exp_f16_sdwa v235, v233 dst_sel:WORD_1 dst_unused:UNUSED_PRESERVE src0_sel:WORD_1\n"
    "v_mfma_f32_16x16x32_bf16 v[176:179], a[176:179], v[160:163], v[176:179]\n"
    "v_pk_fma_f16 v234, v234, s49, s49\n"
    "v_pk_fma_f16 v235, v235, s49, s49\n"
    "v_mfma_f32_16x16x32_bf16 v[176:179], a[96:99], v[164:167], v[176:179]\n"
    "s_waitcnt lgkmcnt(4)\n"
    "ds_read_u16 v198, v198\n"
    "ds_read_u16_d16_hi v206, v206\n"
    "ds_write_b128 v253, v[72:75] offset:1024\n"
    "v_mfma_f32_16x16x32_bf16 v[180:183], a[132:135], v[156:159], 0\n"
    "v_rcp_f16_e32 v232, v234\n"
    "v_mfma_f32_16x16x32_bf16 v[180:183], a[180:183], v[160:163], v[180:183]\n"
    "v_rcp_f16_e64 v233, v235\n"
    "v_mfma_f32_16x16x32_bf16 v[180:183], a[240:243], v[164:167], v[180:183]\n"
    "ds_read_u16 v199, v199\n"
    "ds_read_u16_d16_hi v207, v207\n"
    "v_rcp_f16_sdwa v232, v234 dst_sel:WORD_1 dst_unused:UNUSED_PRESERVE src0_sel:WORD_1\n"
    "v_or_b32_e64 v193, v193, v201\n"
    "v_rcp_f16_sdwa v233, v235 dst_sel:WORD_1 dst_unused:UNUSED_PRESERVE src0_sel:WORD_1\n"
    "v_pk_mul_f16 v184, v184, v232\n"
    "v_or_b32_e64 v194, v194, v202\n"
    "v_pk_mul_f16 v188, v188, v233\n"
    "v_mfma_f32_16x16x32_f16 v[96:99], a[64:67], v[184:187], v[96:99]\n"
    "s_waitcnt lgkmcnt(5)\n"
    "ds_read_b128 a[240:243], v244 offset:36864\n"
    "ds_write_b128 v253, v[76:79] offset:4096\n"
    "v_mfma_f32_16x16x32_f16 v[100:103], a[68:71], v[184:187], v[100:103]\n"
    "v_pk_fma_f16 v232, v192, v192, s48\n"
    "v_or_b32_e32 v195, v195, v203\n"
    "v_mfma_f32_16x16x32_f16 v[112:115], a[72:75], v[184:187], v[112:115]\n"
    "v_pk_fma_f16 v233, v196, v196, s48\n"
    "v_pk_mul_f16 v232, v192, v232\n"
    "v_mfma_f32_16x16x32_f16 v[116:119], a[76:79], v[184:187], v[116:119]\n"
    "ds_write_b128 v253, v[88:91] offset:0\n"
    "v_mfma_f32_16x16x32_f16 v[128:131], v[236:239], v[184:187], v[128:131]\n"
    "s_waitcnt lgkmcnt(5)\n"
    "v_or_b32_e32 v197, v197, v205\n"
    "v_pk_mul_f16 v233, v196, v233\n"
    "v_mfma_f32_16x16x32_f16 v[132:135], a[244:247], v[184:187], v[132:135]\n"
    "v_cvt_pk_f16_f32 v184, v168, v169\n"
    "v_lshlrev_b32_sdwa v185, 1, v170 dst_sel:DWORD dst_unused:UNUSED_PAD src0_sel:DWORD src1_sel:WORD_1\n"
    "v_mfma_f32_16x16x32_f16 v[104:107], a[64:67], v[188:191], v[104:107]\n"
    "ds_read_u16 v185, v185\n"
    "ds_write_b128 v253, v[92:95] offset:3072\n"
    "v_mfma_f32_16x16x32_f16 v[108:111], a[68:71], v[188:191], v[108:111]\n"
    "v_lshlrev_b32_sdwa v201, 1, v171 dst_sel:DWORD dst_unused:UNUSED_PAD src0_sel:DWORD src1_sel:WORD_1\n"
    "v_lshlrev_b32_sdwa v186, 1, v172 dst_sel:DWORD dst_unused:UNUSED_PAD src0_sel:DWORD src1_sel:WORD_1\n"
    "v_mfma_f32_16x16x32_f16 v[120:123], a[72:75], v[188:191], v[120:123]\n"
    "s_waitcnt lgkmcnt(4)\n"
    "s_nop 0\n"
    "v_lshlrev_b32_sdwa v202, 1, v173 dst_sel:DWORD dst_unused:UNUSED_PAD src0_sel:DWORD src1_sel:WORD_1\n"
    "v_lshlrev_b32_sdwa v187, 1, v174 dst_sel:DWORD dst_unused:UNUSED_PAD src0_sel:DWORD src1_sel:WORD_1\n"
    "v_mfma_f32_16x16x32_f16 v[124:127], a[76:79], v[188:191], v[124:127]\n"
    "ds_read_u16_d16_hi v201, v201\n"
    "ds_read_u16 v186, v186\n"
    "ds_read_u16_d16_hi v202, v202\n"
    "v_mfma_f32_16x16x32_f16 v[136:139], v[236:239], v[188:191], v[136:139]\n"
    "ds_read_b128 v[236:239], v244 offset:20480\n"
    "v_lshlrev_b32_sdwa v203, 1, v175 dst_sel:DWORD dst_unused:UNUSED_PAD src0_sel:DWORD src1_sel:WORD_1\n"
    "v_mfma_f32_16x16x32_f16 v[140:143], a[244:247], v[188:191], v[140:143]\n"
    "ds_read_u16 v187, v187\n"
    "v_exp_f16_e64 v234, v232\n"
    "v_mfma_f32_16x16x32_bf16 v[168:171], a[136:139], v[144:147], 0\n"
    "v_or_b32_e64 v198, v198, v206\n"
    "v_cvt_pk_f16_f32 v188, v176, v177\n"
    "v_mfma_f32_16x16x32_bf16 v[168:171], a[184:187], v[148:151], v[168:171]\n"
    "ds_read_u16_d16_hi v203, v203\n"
    "v_lshlrev_b32_sdwa v189, 1, v178 dst_sel:DWORD dst_unused:UNUSED_PAD src0_sel:DWORD src1_sel:WORD_1\n"
    "v_mfma_f32_16x16x32_bf16 v[168:171], a[100:103], v[152:155], v[168:171]\n"
    "v_exp_f16_e64 v235, v233\n"
    "v_mfma_f32_16x16x32_bf16 v[172:175], a[140:143], v[144:147], 0\n"
    "ds_read_b128 a[244:247], v244 offset:22528\n"
    "ds_read_u16 v189, v189\n"
    "v_mfma_f32_16x16x32_bf16 v[172:175], a[188:191], v[148:151], v[172:175]\n"
    "v_lshlrev_b32_sdwa v205, 1, v179 dst_sel:DWORD dst_unused:UNUSED_PAD src0_sel:DWORD src1_sel:WORD_1\n"
    "v_lshlrev_b32_sdwa v190, 1, v180 dst_sel:DWORD dst_unused:UNUSED_PAD src0_sel:DWORD src1_sel:WORD_1\n"
    "v_lshlrev_b32_sdwa v206, 1, v181 dst_sel:DWORD dst_unused:UNUSED_PAD src0_sel:DWORD src1_sel:WORD_1\n"
    "v_lshlrev_b32_sdwa v191, 1, v182 dst_sel:DWORD dst_unused:UNUSED_PAD src0_sel:DWORD src1_sel:WORD_1\n"
    "v_exp_f16_sdwa v234, v232 dst_sel:WORD_1 dst_unused:UNUSED_PRESERVE src0_sel:WORD_1\n"
    "v_mfma_f32_16x16x32_bf16 v[172:175], a[240:243], v[152:155], v[172:175]\n"
    "ds_read_u16_d16_hi v205, v205\n"
    "v_or_b32_e64 v199, v199, v207\n"
    "v_mfma_f32_16x16x32_bf16 v[176:179], a[136:139], v[156:159], 0\n"
    "s_waitcnt lgkmcnt(8)\n"
    "s_nop 0\n"
    "v_lshlrev_b32_sdwa v207, 1, v183 dst_sel:DWORD dst_unused:UNUSED_PAD src0_sel:DWORD src1_sel:WORD_1\n"
    "v_exp_f16_sdwa v235, v233 dst_sel:WORD_1 dst_unused:UNUSED_PRESERVE src0_sel:WORD_1\n"
    "v_mfma_f32_16x16x32_bf16 v[176:179], a[184:187], v[160:163], v[176:179]\n"
    "ds_read_u16 v190, v190\n"
    "v_pk_fma_f16 v234, v234, s49, s49\n"
    "v_mfma_f32_16x16x32_bf16 v[176:179], a[100:103], v[164:167], v[176:179]\n"
    "ds_read_u16_d16_hi v206, v206\n"
    "v_pk_fma_f16 v235, v235, s49, s49\n"
    "v_mfma_f32_16x16x32_bf16 v[180:183], a[140:143], v[156:159], 0\n"
    "s_waitcnt lgkmcnt(7)\n"
    "ds_read_u16 v191, v191\n"
    "v_rcp_f16_e32 v232, v234\n"
    "v_mfma_f32_16x16x32_bf16 v[180:183], a[188:191], v[160:163], v[180:183]\n"
    "v_rcp_f16_e64 v233, v235\n"
    "v_mfma_f32_16x16x32_bf16 v[180:183], a[240:243], v[164:167], v[180:183]\n"
    "ds_read_u16_d16_hi v207, v207\n"
    "v_rcp_f16_sdwa v232, v234 dst_sel:WORD_1 dst_unused:UNUSED_PRESERVE src0_sel:WORD_1\n"
    "v_or_b32_e64 v185, v185, v201\n"
    "v_rcp_f16_sdwa v233, v235 dst_sel:WORD_1 dst_unused:UNUSED_PRESERVE src0_sel:WORD_1\n"
    "v_pk_mul_f16 v192, v192, v232\n"
    "v_or_b32_e64 v186, v186, v202\n"
    "v_pk_mul_f16 v196, v196, v233\n"
    "v_mfma_f32_16x16x32_f16 v[96:99], a[80:83], v[192:195], v[96:99]\n"
    "s_waitcnt lgkmcnt(6)\n"
    "v_pk_fma_f16 v232, v184, v184, s48\n"
    "v_or_b32_e32 v187, v187, v203\n"
    "v_mfma_f32_16x16x32_f16 v[100:103], a[84:87], v[192:195], v[100:103]\n"
    "v_pk_fma_f16 v233, v188, v188, s48\n"
    "v_pk_mul_f16 v232, v184, v232\n"
    "v_mfma_f32_16x16x32_f16 v[112:115], a[88:91], v[192:195], v[112:115]\n"
    "v_pk_mul_f16 v233, v188, v233\n"
    "v_lshlrev_b32_sdwa v201, 1, v171 dst_sel:DWORD dst_unused:UNUSED_PAD src0_sel:DWORD src1_sel:WORD_1\n"
    "v_mfma_f32_16x16x32_f16 v[116:119], a[92:95], v[192:195], v[116:119]\n"
    "ds_read_u16_d16_hi v201, v201\n"
    "v_lshlrev_b32_sdwa v202, 1, v173 dst_sel:DWORD dst_unused:UNUSED_PAD src0_sel:DWORD src1_sel:WORD_1\n"
    "v_mfma_f32_16x16x32_f16 v[128:131], v[236:239], v[192:195], v[128:131]\n"
    "s_waitcnt lgkmcnt(5)\n"
    "ds_read_u16_d16_hi v202, v202\n"
    "v_or_b32_e32 v189, v189, v205\n"
    "v_mfma_f32_16x16x32_f16 v[132:135], a[244:247], v[192:195], v[132:135]\n"
    "v_cvt_pk_f16_f32 v192, v168, v169\n"
    "v_lshlrev_b32_sdwa v193, 1, v170 dst_sel:DWORD dst_unused:UNUSED_PAD src0_sel:DWORD src1_sel:WORD_1\n"
    "v_mfma_f32_16x16x32_f16 v[104:107], a[80:83], v[196:199], v[104:107]\n"
    "ds_read_u16 v193, v193\n"
    "v_lshlrev_b32_sdwa v194, 1, v172 dst_sel:DWORD dst_unused:UNUSED_PAD src0_sel:DWORD src1_sel:WORD_1\n"
    "v_mfma_f32_16x16x32_f16 v[108:111], a[84:87], v[196:199], v[108:111]\n"
    "ds_read_u16 v194, v194\n"
    "v_lshlrev_b32_sdwa v195, 1, v174 dst_sel:DWORD dst_unused:UNUSED_PAD src0_sel:DWORD src1_sel:WORD_1\n"
    "v_mfma_f32_16x16x32_f16 v[120:123], a[88:91], v[196:199], v[120:123]\n"
    "s_waitcnt lgkmcnt(6)\n"
    "ds_read_u16 v195, v195\n"
    "v_lshlrev_b32_sdwa v203, 1, v175 dst_sel:DWORD dst_unused:UNUSED_PAD src0_sel:DWORD src1_sel:WORD_1\n"
    "v_mfma_f32_16x16x32_f16 v[124:127], a[92:95], v[196:199], v[124:127]\n"
    "v_exp_f16_e32 v234, v232\n"
    "v_mfma_f32_16x16x32_f16 v[136:139], v[236:239], v[196:199], v[136:139]\n"
    "ds_read_b128 v[236:239], v244 offset:0\n"
    "ds_read_u16_d16_hi v203, v203\n"
    "v_mfma_f32_16x16x32_f16 v[140:143], a[244:247], v[196:199], v[140:143]\n"
    "s_waitcnt lgkmcnt(7)\n"
    "ds_read_b128 a[244:247], v244 offset:2048\n"
    "v_or_b32_e32 v190, v190, v206\n"
    "v_mfma_f32_16x16x32_bf16 v[168:171], a[144:147], v[144:147], 0\n"
    "v_cvt_pk_f16_f32 v196, v176, v177\n"
    "v_lshlrev_b32_sdwa v197, 1, v178 dst_sel:DWORD dst_unused:UNUSED_PAD src0_sel:DWORD src1_sel:WORD_1\n"
    "v_mfma_f32_16x16x32_bf16 v[168:171], a[192:195], v[148:151], v[168:171]\n"
    "v_exp_f16_e64 v235, v233\n"
    "v_mfma_f32_16x16x32_bf16 v[168:171], a[104:107], v[152:155], v[168:171]\n"
    "ds_read_u16 v197, v197\n"
    "v_lshlrev_b32_sdwa v205, 1, v179 dst_sel:DWORD dst_unused:UNUSED_PAD src0_sel:DWORD src1_sel:WORD_1\n"
    "v_mfma_f32_16x16x32_bf16 v[172:175], a[148:151], v[144:147], 0\n"
    "v_lshlrev_b32_sdwa v198, 1, v180 dst_sel:DWORD dst_unused:UNUSED_PAD src0_sel:DWORD src1_sel:WORD_1\n"
    "v_lshlrev_b32_sdwa v206, 1, v181 dst_sel:DWORD dst_unused:UNUSED_PAD src0_sel:DWORD src1_sel:WORD_1\n"
    "v_mfma_f32_16x16x32_bf16 v[172:175], a[196:199], v[148:151], v[172:175]\n"
    "v_lshlrev_b32_sdwa v199, 1, v182 dst_sel:DWORD dst_unused:UNUSED_PAD src0_sel:DWORD src1_sel:WORD_1\n"
    "v_exp_f16_sdwa v234, v232 dst_sel:WORD_1 dst_unused:UNUSED_PRESERVE src0_sel:WORD_1\n"
    "v_mfma_f32_16x16x32_bf16 v[172:175], a[108:111], v[152:155], v[172:175]\n"
    "s_waitcnt lgkmcnt(6)\n"
    "ds_read_u16_d16_hi v205, v205\n"
    "v_or_b32_e32 v191, v191, v207\n"
    "v_mfma_f32_16x16x32_bf16 v[176:179], a[144:147], v[156:159], 0\n"
    "s_waitcnt lgkmcnt(6)\n"
    "s_nop 0\n"
    "v_lshlrev_b32_sdwa v207, 1, v183 dst_sel:DWORD dst_unused:UNUSED_PAD src0_sel:DWORD src1_sel:WORD_1\n"
    "v_exp_f16_sdwa v235, v233 dst_sel:WORD_1 dst_unused:UNUSED_PRESERVE src0_sel:WORD_1\n"
    "v_mfma_f32_16x16x32_bf16 v[176:179], a[192:195], v[160:163], v[176:179]\n"
    "ds_read_u16 v198, v198\n"
    "v_pk_fma_f16 v234, v234, s49, s49\n"
    "v_mfma_f32_16x16x32_bf16 v[176:179], a[104:107], v[164:167], v[176:179]\n"
    "ds_read_u16_d16_hi v206, v206\n"
    "v_pk_fma_f16 v235, v235, s49, s49\n"
    "v_mfma_f32_16x16x32_bf16 v[180:183], a[148:151], v[156:159], 0\n"
    "ds_read_u16 v199, v199\n"
    "v_cvt_pk_bf16_f32 v96, v96, v97\n"
    "v_mfma_f32_16x16x32_bf16 v[180:183], a[196:199], v[160:163], v[180:183]\n"
    "v_cvt_pk_bf16_f32 v97, v98, v99\n"
    "v_rcp_f16_e64 v232, v234\n"
    "v_mfma_f32_16x16x32_bf16 v[180:183], a[108:111], v[164:167], v[180:183]\n"
    "s_waitcnt lgkmcnt(5)\n"
    "s_nop 0\n"
    "ds_read_u16_d16_hi v207, v207\n"
    "v_cvt_pk_bf16_f32 v98, v100, v101\n"
    "v_cvt_pk_bf16_f32 v99, v102, v103\n"
    "ds_write_b128 v242, v[96:99] offset:24640\n"
    "v_cvt_pk_bf16_f32 v112, v112, v113\n"
    "v_rcp_f16_e64 v233, v235\n"
    "v_cvt_pk_bf16_f32 v113, v114, v115\n"
    "v_cvt_pk_bf16_f32 v114, v116, v117\n"
    "v_cvt_pk_bf16_f32 v115, v118, v119\n"
    "ds_write_b128 v242, v[112:115] offset:26688\n"
    "v_rcp_f16_sdwa v232, v234 dst_sel:WORD_1 dst_unused:UNUSED_PRESERVE src0_sel:WORD_1\n"
    "v_cvt_pk_bf16_f32 v128, v128, v129\n"
    "v_cvt_pk_bf16_f32 v129, v130, v131\n"
    "v_cvt_pk_bf16_f32 v130, v132, v133\n"
    "v_or_b32_e64 v193, v193, v201\n"
    "v_cvt_pk_bf16_f32 v131, v134, v135\n"
    "ds_write_b128 v242, v[128:131] offset:28736\n"
    "v_rcp_f16_sdwa v233, v235 dst_sel:WORD_1 dst_unused:UNUSED_PRESERVE src0_sel:WORD_1\n"
    "v_cvt_pk_bf16_f32 v104, v104, v105\n"
    "v_cvt_pk_bf16_f32 v105, v106, v107\n"
    "v_cvt_pk_bf16_f32 v106, v108, v109\n"
    "v_pk_mul_f16 v184, v184, v232\n"
    "v_cvt_pk_bf16_f32 v107, v110, v111\n"
    "ds_write_b128 v242, v[104:107] offset:25664\n"
    "v_mfma_f32_16x16x32_f16 v[96:99], a[0:3], v[184:187], 0\n"
    "s_waitcnt lgkmcnt(2)\n"
    "v_or_b32_e32 v194, v194, v202\n"
    "v_cvt_pk_bf16_f32 v120, v120, v121\n"
    "v_mfma_f32_16x16x32_f16 v[100:103], a[4:7], v[184:187], 0\n"
    "v_cvt_pk_bf16_f32 v121, v122, v123\n"
    "v_pk_mul_f16 v188, v188, v233\n"
    "v_mfma_f32_16x16x32_f16 v[112:115], a[8:11], v[184:187], 0\n"
    "v_cvt_pk_bf16_f32 v122, v124, v125\n"
    "v_cvt_pk_bf16_f32 v123, v126, v127\n"
    "v_mfma_f32_16x16x32_f16 v[116:119], a[12:15], v[184:187], 0\n"
    "ds_write_b128 v242, v[120:123] offset:27712\n"
    "v_mfma_f32_16x16x32_f16 v[128:131], v[236:239], v[184:187], 0\n"
    "v_cvt_pk_bf16_f32 v136, v136, v137\n"
    "v_pk_fma_f16 v232, v192, v192, s48\n"
    "v_mfma_f32_16x16x32_f16 v[132:135], a[244:247], v[184:187], 0\n"
    "v_cvt_pk_bf16_f32 v137, v138, v139\n"
    "v_cvt_pk_bf16_f32 v138, v140, v141\n"
    "v_mfma_f32_16x16x32_f16 v[104:107], a[0:3], v[188:191], 0\n"
    "v_or_b32_e64 v195, v195, v203\n"
    "v_cvt_pk_bf16_f32 v139, v142, v143\n"
    "v_mfma_f32_16x16x32_f16 v[108:111], a[4:7], v[188:191], 0\n"
    "ds_write_b128 v242, v[136:139] offset:29760\n"
    "v_mfma_f32_16x16x32_f16 v[120:123], a[8:11], v[188:191], 0\n"
    "v_pk_fma_f16 v233, v196, v196, s48\n"
    "v_pk_mul_f16 v232, v192, v232\n"
    "v_mfma_f32_16x16x32_f16 v[124:127], a[12:15], v[188:191], 0\n"
    "v_or_b32_e64 v197, v197, v205\n"
    "v_pk_mul_f16 v233, v196, v233\n"
    "v_mfma_f32_16x16x32_f16 v[136:139], v[236:239], v[188:191], 0\n"
    "ds_read_b128 v[236:239], v244 offset:4096\n"
    "v_cvt_pk_f16_f32 v184, v168, v169\n"
    "v_mfma_f32_16x16x32_f16 v[140:143], a[244:247], v[188:191], 0\n"
    "v_lshlrev_b32_sdwa v185, 1, v170 dst_sel:DWORD dst_unused:UNUSED_PAD src0_sel:DWORD src1_sel:WORD_1\n"
    "v_lshlrev_b32_sdwa v201, 1, v171 dst_sel:DWORD dst_unused:UNUSED_PAD src0_sel:DWORD src1_sel:WORD_1\n"
    "s_waitcnt vmcnt(6)\n"
    "s_nop 0\n"
    "v_smfmac_f32_32x32x32_bf16 v[96:111], a[248:251], a[224:231], v241\n"
    "ds_read_u16 v185, v185\n"
    "ds_read_u16_d16_hi v201, v201\n"
    "buffer_load_dwordx4 a[224:227], v245, s[40:43], s57 offen offset:128\n"
    "v_lshlrev_b32_sdwa v186, 1, v172 dst_sel:DWORD dst_unused:UNUSED_PAD src0_sel:DWORD src1_sel:WORD_1\n"
    "v_lshlrev_b32_sdwa v202, 1, v173 dst_sel:DWORD dst_unused:UNUSED_PAD src0_sel:DWORD src1_sel:WORD_1\n"
    "v_smfmac_f32_32x32x32_bf16 v[112:127], a[248:251], a[232:239], v241\n"
    "ds_read_u16 v186, v186\n"
    "ds_read_u16_d16_hi v202, v202\n"
    "v_lshlrev_b32_sdwa v187, 1, v174 dst_sel:DWORD dst_unused:UNUSED_PAD src0_sel:DWORD src1_sel:WORD_1\n"
    "v_lshlrev_b32_sdwa v203, 1, v175 dst_sel:DWORD dst_unused:UNUSED_PAD src0_sel:DWORD src1_sel:WORD_1\n"
    "v_exp_f16_e64 v234, v232\n"
    "v_mfma_f32_16x16x32_bf16 v[168:171], a[152:155], v[144:147], 0\n"
    "ds_read_u16 v187, v187\n"
    "v_or_b32_e64 v198, v198, v206\n"
    "v_mfma_f32_16x16x32_bf16 v[168:171], a[200:203], v[148:151], v[168:171]\n"
    "ds_read_u16_d16_hi v203, v203\n"
    "v_cvt_pk_f16_f32 v188, v176, v177\n"
    "v_mfma_f32_16x16x32_bf16 v[168:171], a[112:115], v[152:155], v[168:171]\n"
    "v_lshlrev_b32_sdwa v189, 1, v178 dst_sel:DWORD dst_unused:UNUSED_PAD src0_sel:DWORD src1_sel:WORD_1\n"
    "v_exp_f16_e64 v235, v233\n"
    "v_mfma_f32_16x16x32_bf16 v[172:175], a[156:159], v[144:147], 0\n"
    "ds_read_b128 a[244:247], v244 offset:6144\n"
    "ds_read_u16 v189, v189\n"
    "v_mfma_f32_16x16x32_bf16 v[172:175], a[204:207], v[148:151], v[172:175]\n"
    "v_lshlrev_b32_sdwa v205, 1, v179 dst_sel:DWORD dst_unused:UNUSED_PAD src0_sel:DWORD src1_sel:WORD_1\n"
    "v_lshlrev_b32_sdwa v190, 1, v180 dst_sel:DWORD dst_unused:UNUSED_PAD src0_sel:DWORD src1_sel:WORD_1\n"
    "v_lshlrev_b32_sdwa v206, 1, v181 dst_sel:DWORD dst_unused:UNUSED_PAD src0_sel:DWORD src1_sel:WORD_1\n"
    "v_lshlrev_b32_sdwa v191, 1, v182 dst_sel:DWORD dst_unused:UNUSED_PAD src0_sel:DWORD src1_sel:WORD_1\n"
    "v_exp_f16_sdwa v234, v232 dst_sel:WORD_1 dst_unused:UNUSED_PRESERVE src0_sel:WORD_1\n"
    "v_mfma_f32_16x16x32_bf16 v[172:175], a[116:119], v[152:155], v[172:175]\n"
    "buffer_load_dwordx4 a[228:231], v245, s[40:43], s100 offen offset:128\n"
    "ds_read_u16_d16_hi v205, v205\n"
    "v_mfma_f32_16x16x32_bf16 v[176:179], a[152:155], v[156:159], 0\n"
    "v_or_b32_e64 v199, v199, v207\n"
    "v_lshlrev_b32_sdwa v207, 1, v183 dst_sel:DWORD dst_unused:UNUSED_PAD src0_sel:DWORD src1_sel:WORD_1\n"
    "v_exp_f16_sdwa v235, v233 dst_sel:WORD_1 dst_unused:UNUSED_PRESERVE src0_sel:WORD_1\n"
    "v_mfma_f32_16x16x32_bf16 v[176:179], a[200:203], v[160:163], v[176:179]\n"
    "s_waitcnt lgkmcnt(7)\n"
    "s_nop 0\n"
    "ds_read_u16 v190, v190\n"
    "v_pk_fma_f16 v234, v234, s49, s49\n"
    "v_mfma_f32_16x16x32_bf16 v[176:179], a[112:115], v[164:167], v[176:179]\n"
    "ds_read_u16_d16_hi v206, v206\n"
    "v_pk_fma_f16 v235, v235, s49, s49\n"
    "v_mfma_f32_16x16x32_bf16 v[180:183], a[156:159], v[156:159], 0\n"
    "ds_read_u16 v191, v191\n"
    "v_rcp_f16_e64 v232, v234\n"
    "v_mfma_f32_16x16x32_bf16 v[180:183], a[204:207], v[160:163], v[180:183]\n"
    "s_waitcnt lgkmcnt(8)\n"
    "v_rcp_f16_e32 v233, v235\n"
    "v_mfma_f32_16x16x32_bf16 v[180:183], a[116:119], v[164:167], v[180:183]\n"
    "s_barrier\n"
    "v_rcp_f16_sdwa v232, v234 dst_sel:WORD_1 dst_unused:UNUSED_PRESERVE src0_sel:WORD_1\n"
    "v_or_b32_e32 v185, v185, v201\n"
    "v_rcp_f16_sdwa v233, v235 dst_sel:WORD_1 dst_unused:UNUSED_PRESERVE src0_sel:WORD_1\n"
    "v_pk_mul_f16 v192, v192, v232\n"
    "v_or_b32_e64 v186, v186, v202\n"
    "v_pk_mul_f16 v196, v196, v233\n"
    "v_mfma_f32_16x16x32_f16 v[96:99], a[16:19], v[192:195], v[96:99]\n"
    "s_waitcnt lgkmcnt(5)\n"
    "s_nop 0\n"
    "ds_read_b128 a[232:235], v243 offset:24640\n"
    "ds_read_b128 a[236:239], v243 offset:25664\n"
    "ds_read_u16_d16_hi v207, v207\n"
    "v_mfma_f32_16x16x32_f16 v[100:103], a[20:23], v[192:195], v[100:103]\n"
    "ds_read_b128 a[240:243], v244 offset:34816\n"
    "v_pk_fma_f16 v232, v184, v184, s48\n"
    "v_mfma_f32_16x16x32_f16 v[112:115], a[24:27], v[192:195], v[112:115]\n"
    "s_waitcnt lgkmcnt(7)\n"
    "v_or_b32_e32 v187, v187, v203\n"
    "v_pk_fma_f16 v233, v188, v188, s48\n"
    "v_mfma_f32_16x16x32_f16 v[116:119], a[28:31], v[192:195], v[116:119]\n"
    "v_pk_mul_f16 v232, v184, v232\n"
    "v_or_b32_e64 v189, v189, v205\n"
    "v_mfma_f32_16x16x32_f16 v[128:131], v[236:239], v[192:195], v[128:131]\n"
    "v_pk_mul_f16 v233, v188, v233\n"
    "v_lshlrev_b32_sdwa v201, 1, v171 dst_sel:DWORD dst_unused:UNUSED_PAD src0_sel:DWORD src1_sel:WORD_1\n"
    "v_mfma_f32_16x16x32_f16 v[132:135], a[244:247], v[192:195], v[132:135]\n"
    "ds_read_u16_d16_hi v201, v201\n"
    "v_cvt_pk_f16_f32 v192, v168, v169\n"
    "v_mfma_f32_16x16x32_f16 v[104:107], a[16:19], v[196:199], v[104:107]\n"
    "s_waitcnt lgkmcnt(6)\n"
    "s_nop 0\n"
    "v_lshlrev_b32_sdwa v193, 1, v170 dst_sel:DWORD dst_unused:UNUSED_PAD src0_sel:DWORD src1_sel:WORD_1\n"
    "v_lshlrev_b32_sdwa v194, 1, v172 dst_sel:DWORD dst_unused:UNUSED_PAD src0_sel:DWORD src1_sel:WORD_1\n"
    "v_mfma_f32_16x16x32_f16 v[108:111], a[20:23], v[196:199], v[108:111]\n"
    "ds_read_u16 v193, v193\n"
    "ds_read_u16 v194, v194\n"
    "v_mfma_f32_16x16x32_f16 v[120:123], a[24:27], v[196:199], v[120:123]\n"
    "v_lshlrev_b32_sdwa v202, 1, v173 dst_sel:DWORD dst_unused:UNUSED_PAD src0_sel:DWORD src1_sel:WORD_1\n"
    "v_lshlrev_b32_sdwa v195, 1, v174 dst_sel:DWORD dst_unused:UNUSED_PAD src0_sel:DWORD src1_sel:WORD_1\n"
    "v_mfma_f32_16x16x32_f16 v[124:127], a[28:31], v[196:199], v[124:127]\n"
    "s_waitcnt lgkmcnt(5)\n"
    "s_nop 0\n"
    "ds_read_u16_d16_hi v202, v202\n"
    "ds_read_u16 v195, v195\n"
    "v_mfma_f32_16x16x32_f16 v[136:139], v[236:239], v[196:199], v[136:139]\n"
    "ds_read_b128 v[236:239], v244 offset:8192\n"
    "v_lshlrev_b32_sdwa v203, 1, v175 dst_sel:DWORD dst_unused:UNUSED_PAD src0_sel:DWORD src1_sel:WORD_1\n"
    "v_smfmac_f32_32x32x32_bf16 v[96:111], a[248:251], a[232:239], v241\n"
    "v_exp_f16_e64 v234, v232\n"
    "v_or_b32_e64 v190, v190, v206\n"
    "v_exp_f16_e64 v235, v233\n"
    "v_mfma_f32_16x16x32_f16 v[140:143], a[244:247], v[196:199], v[140:143]\n"
    "s_waitcnt lgkmcnt(6)\n"
    "s_nop 0\n"
    "ds_read_u16_d16_hi v203, v203\n"
    "ds_read_b128 a[244:247], v244 offset:10240\n"
    "v_mfma_f32_16x16x32_bf16 v[168:171], a[160:163], v[144:147], 0\n"
    "ds_read_b128 a[232:235], v243 offset:26688\n"
    "v_cvt_pk_f16_f32 v196, v176, v177\n"
    "v_mfma_f32_16x16x32_bf16 v[168:171], a[208:211], v[148:151], v[168:171]\n"
    "ds_read_b128 a[236:239], v243 offset:27712\n"
    "v_lshlrev_b32_sdwa v197, 1, v178 dst_sel:DWORD dst_unused:UNUSED_PAD src0_sel:DWORD src1_sel:WORD_1\n"
    "v_mfma_f32_16x16x32_bf16 v[168:171], a[120:123], v[152:155], v[168:171]\n"
    "ds_read_u16 v197, v197\n"
    "v_lshlrev_b32_sdwa v205, 1, v179 dst_sel:DWORD dst_unused:UNUSED_PAD src0_sel:DWORD src1_sel:WORD_1\n"
    "v_mfma_f32_16x16x32_bf16 v[172:175], a[164:167], v[144:147], 0\n"
    "s_waitcnt lgkmcnt(9)\n"
    "s_nop 0\n"
    "v_lshlrev_b32_sdwa v198, 1, v180 dst_sel:DWORD dst_unused:UNUSED_PAD src0_sel:DWORD src1_sel:WORD_1\n"
    "v_lshlrev_b32_sdwa v206, 1, v181 dst_sel:DWORD dst_unused:UNUSED_PAD src0_sel:DWORD src1_sel:WORD_1\n"
    "v_mfma_f32_16x16x32_bf16 v[172:175], a[212:215], v[148:151], v[172:175]\n"
    "v_lshlrev_b32_sdwa v199, 1, v182 dst_sel:DWORD dst_unused:UNUSED_PAD src0_sel:DWORD src1_sel:WORD_1\n"
    "v_exp_f16_sdwa v234, v232 dst_sel:WORD_1 dst_unused:UNUSED_PRESERVE src0_sel:WORD_1\n"
    "v_mfma_f32_16x16x32_bf16 v[172:175], a[240:243], v[152:155], v[172:175]\n"
    "ds_read_u16_d16_hi v205, v205\n"
    "v_or_b32_e64 v191, v191, v207\n"
    "v_mfma_f32_16x16x32_bf16 v[176:179], a[160:163], v[156:159], 0\n"
    "v_lshlrev_b32_sdwa v207, 1, v183 dst_sel:DWORD dst_unused:UNUSED_PAD src0_sel:DWORD src1_sel:WORD_1\n"
    "v_exp_f16_sdwa v235, v233 dst_sel:WORD_1 dst_unused:UNUSED_PRESERVE src0_sel:WORD_1\n"
    "v_mfma_f32_16x16x32_bf16 v[176:179], a[208:211], v[160:163], v[176:179]\n"
    "s_waitcnt lgkmcnt(8)\n"
    "s_nop 0\n"
    "ds_read_u16 v198, v198\n"
    "v_pk_fma_f16 v234, v234, s49, s49\n"
    "v_mfma_f32_16x16x32_bf16 v[176:179], a[120:123], v[164:167], v[176:179]\n"
    "ds_read_u16_d16_hi v206, v206\n"
    "v_pk_fma_f16 v235, v235, s49, s49\n"
    "v_mfma_f32_16x16x32_bf16 v[180:183], a[164:167], v[156:159], 0\n"
    "ds_read_u16 v199, v199\n"
    "v_rcp_f16_e64 v232, v234\n"
    "v_mfma_f32_16x16x32_bf16 v[180:183], a[212:215], v[160:163], v[180:183]\n"
    "ds_read_u16_d16_hi v207, v207\n"
    "v_rcp_f16_e64 v233, v235\n"
    "v_mfma_f32_16x16x32_bf16 v[180:183], a[240:243], v[164:167], v[180:183]\n"
    "s_waitcnt lgkmcnt(7)\n"
    "v_rcp_f16_sdwa v232, v234 dst_sel:WORD_1 dst_unused:UNUSED_PRESERVE src0_sel:WORD_1\n"
    "v_or_b32_e32 v193, v193, v201\n"
    "v_rcp_f16_sdwa v233, v235 dst_sel:WORD_1 dst_unused:UNUSED_PRESERVE src0_sel:WORD_1\n"
    "v_pk_mul_f16 v184, v184, v232\n"
    "v_or_b32_e64 v194, v194, v202\n"
    "v_pk_mul_f16 v188, v188, v233\n"
    "v_mfma_f32_16x16x32_f16 v[96:99], a[32:35], v[184:187], v[96:99]\n"
    "ds_read_b128 a[240:243], v244 offset:38912\n"
    "v_pk_fma_f16 v232, v192, v192, s48\n"
    "v_mfma_f32_16x16x32_f16 v[100:103], a[36:39], v[184:187], v[100:103]\n"
    "v_or_b32_e64 v195, v195, v203\n"
    "v_pk_fma_f16 v233, v196, v196, s48\n"
    "v_mfma_f32_16x16x32_f16 v[112:115], a[40:43], v[184:187], v[112:115]\n"
    "v_pk_mul_f16 v232, v192, v232\n"
    "v_pk_mul_f16 v233, v196, v233\n"
    "v_mfma_f32_16x16x32_f16 v[116:119], a[44:47], v[184:187], v[116:119]\n"
    "s_waitcnt lgkmcnt(5)\n"
    "v_or_b32_e32 v197, v197, v205\n"
    "v_lshlrev_b32_sdwa v201, 1, v171 dst_sel:DWORD dst_unused:UNUSED_PAD src0_sel:DWORD src1_sel:WORD_1\n"
    "v_mfma_f32_16x16x32_f16 v[128:131], v[236:239], v[184:187], v[128:131]\n"
    "ds_read_u16_d16_hi v201, v201\n"
    "v_lshlrev_b32_sdwa v202, 1, v173 dst_sel:DWORD dst_unused:UNUSED_PAD src0_sel:DWORD src1_sel:WORD_1\n"
    "v_mfma_f32_16x16x32_f16 v[132:135], a[244:247], v[184:187], v[132:135]\n"
    "ds_read_u16_d16_hi v202, v202\n"
    "v_cvt_pk_f16_f32 v184, v168, v169\n"
    "v_mfma_f32_16x16x32_f16 v[104:107], a[32:35], v[188:191], v[104:107]\n"
    "v_lshlrev_b32_sdwa v185, 1, v170 dst_sel:DWORD dst_unused:UNUSED_PAD src0_sel:DWORD src1_sel:WORD_1\n"
    "v_lshlrev_b32_sdwa v186, 1, v172 dst_sel:DWORD dst_unused:UNUSED_PAD src0_sel:DWORD src1_sel:WORD_1\n"
    "v_mfma_f32_16x16x32_f16 v[108:111], a[36:39], v[188:191], v[108:111]\n"
    "s_waitcnt lgkmcnt(5)\n"
    "s_nop 0\n"
    "ds_read_u16 v185, v185\n"
    "ds_read_u16 v186, v186\n"
    "v_mfma_f32_16x16x32_f16 v[120:123], a[40:43], v[188:191], v[120:123]\n"
    "v_lshlrev_b32_sdwa v187, 1, v174 dst_sel:DWORD dst_unused:UNUSED_PAD src0_sel:DWORD src1_sel:WORD_1\n"
    "v_lshlrev_b32_sdwa v203, 1, v175 dst_sel:DWORD dst_unused:UNUSED_PAD src0_sel:DWORD src1_sel:WORD_1\n"
    "v_mfma_f32_16x16x32_f16 v[124:127], a[44:47], v[188:191], v[124:127]\n"
    "ds_read_u16 v187, v187\n"
    "v_exp_f16_e64 v234, v232\n"
    "v_mfma_f32_16x16x32_f16 v[136:139], v[236:239], v[188:191], v[136:139]\n"
    "s_waitcnt lgkmcnt(5)\n"
    "ds_read_b128 v[236:239], v244 offset:12288\n"
    "ds_read_u16_d16_hi v203, v203\n"
    "v_smfmac_f32_32x32x32_bf16 v[112:127], a[248:251], a[232:239], v241\n"
    "ds_read_b128 a[232:235], v243 offset:28736\n"
    "v_or_b32_e32 v198, v198, v206\n"
    "v_exp_f16_e64 v235, v233\n"
    "v_lshlrev_b32_sdwa v205, 1, v179 dst_sel:DWORD dst_unused:UNUSED_PAD src0_sel:DWORD src1_sel:WORD_1\n"
    "v_mfma_f32_16x16x32_f16 v[140:143], a[244:247], v[188:191], v[140:143]\n"
    "ds_read_b128 a[244:247], v244 offset:14336\n"
    "ds_read_b128 a[236:239], v243 offset:29760\n"
    "v_mfma_f32_16x16x32_bf16 v[168:171], a[168:171], v[144:147], 0\n"
    "v_cvt_pk_f16_f32 v188, v176, v177\n"
    "v_lshlrev_b32_sdwa v189, 1, v178 dst_sel:DWORD dst_unused:UNUSED_PAD src0_sel:DWORD src1_sel:WORD_1\n"
    "v_mfma_f32_16x16x32_bf16 v[168:171], a[216:219], v[148:151], v[168:171]\n"
    "ds_read_u16 v189, v189\n"
    "v_lshlrev_b32_sdwa v190, 1, v180 dst_sel:DWORD dst_unused:UNUSED_PAD src0_sel:DWORD src1_sel:WORD_1\n"
    "v_mfma_f32_16x16x32_bf16 v[168:171], a[124:127], v[152:155], v[168:171]\n"
    "v_lshlrev_b32_sdwa v206, 1, v181 dst_sel:DWORD dst_unused:UNUSED_PAD src0_sel:DWORD src1_sel:WORD_1\n"
    "v_lshlrev_b32_sdwa v191, 1, v182 dst_sel:DWORD dst_unused:UNUSED_PAD src0_sel:DWORD src1_sel:WORD_1\n"
    "v_mfma_f32_16x16x32_bf16 v[172:175], a[172:175], v[144:147], 0\n"
    "v_exp_f16_sdwa v234, v232 dst_sel:WORD_1 dst_unused:UNUSED_PRESERVE src0_sel:WORD_1\n"
    "v_mfma_f32_16x16x32_bf16 v[172:175], a[220:223], v[148:151], v[172:175]\n"
    "s_waitcnt lgkmcnt(8)\n"
    "ds_read_u16_d16_hi v205, v205\n"
    "v_or_b32_e32 v199, v199, v207\n"
    "v_mfma_f32_16x16x32_bf16 v[172:175], a[240:243], v[152:155], v[172:175]\n"
    "s_waitcnt lgkmcnt(8)\n"
    "s_nop 0\n"
    "v_lshlrev_b32_sdwa v207, 1, v183 dst_sel:DWORD dst_unused:UNUSED_PAD src0_sel:DWORD src1_sel:WORD_1\n"
    "v_exp_f16_sdwa v235, v233 dst_sel:WORD_1 dst_unused:UNUSED_PRESERVE src0_sel:WORD_1\n"
    "v_mfma_f32_16x16x32_bf16 v[176:179], a[168:171], v[156:159], 0\n"
    "ds_read_u16 v190, v190\n"
    "v_pk_fma_f16 v234, v234, s49, s49\n"
    "v_mfma_f32_16x16x32_bf16 v[176:179], a[216:219], v[160:163], v[176:179]\n"
    "ds_read_b128 v[144:147], v242 offset:0\n"
    "ds_read_u16_d16_hi v206, v206\n"
    "v_mfma_f32_16x16x32_bf16 v[176:179], a[124:127], v[164:167], v[176:179]\n"
    "ds_read_b128 v[148:151], v242 offset:1024\n"
    "v_pk_fma_f16 v235, v235, s49, s49\n"
    "v_mfma_f32_16x16x32_bf16 v[180:183], a[172:175], v[156:159], 0\n"
    "ds_read_u16 v191, v191\n"
    "v_rcp_f16_e64 v232, v234\n"
    "v_mfma_f32_16x16x32_bf16 v[180:183], a[220:223], v[160:163], v[180:183]\n"
    "s_waitcnt lgkmcnt(8)\n"
    "v_rcp_f16_e32 v233, v235\n"
    "v_mfma_f32_16x16x32_bf16 v[180:183], a[240:243], v[164:167], v[180:183]\n"
    "ds_read_b128 v[152:155], v242 offset:2048\n"
    "v_rcp_f16_sdwa v232, v234 dst_sel:WORD_1 dst_unused:UNUSED_PRESERVE src0_sel:WORD_1\n"
    "v_or_b32_e64 v185, v185, v201\n"
    "v_rcp_f16_sdwa v233, v235 dst_sel:WORD_1 dst_unused:UNUSED_PRESERVE src0_sel:WORD_1\n"
    "v_pk_mul_f16 v192, v192, v232\n"
    "v_or_b32_e64 v186, v186, v202\n"
    "v_pk_mul_f16 v196, v196, v233\n"
    "v_mfma_f32_16x16x32_f16 v[96:99], a[48:51], v[192:195], v[96:99]\n"
    "ds_read_u16_d16_hi v207, v207\n"
    "ds_read_b128 v[156:159], v242 offset:3072\n"
    "ds_read_b128 v[160:163], v242 offset:4096\n"
    "v_mfma_f32_16x16x32_f16 v[100:103], a[52:55], v[192:195], v[100:103]\n"
    "v_pk_fma_f16 v232, v184, v184, s48\n"
    "v_or_b32_e64 v187, v187, v203\n"
    "v_mfma_f32_16x16x32_f16 v[112:115], a[56:59], v[192:195], v[112:115]\n"
    "s_waitcnt lgkmcnt(9)\n"
    "s_nop 0\n"
    "ds_read_b128 v[164:167], v242 offset:5120\n"
    "ds_read_b128 a[240:243], v244 offset:32768\n"
    "v_mfma_f32_16x16x32_f16 v[116:119], a[60:63], v[192:195], v[116:119]\n"
    "v_pk_fma_f16 v233, v188, v188, s48\n"
    "v_pk_mul_f16 v232, v184, v232\n"
    "v_mfma_f32_16x16x32_f16 v[128:131], v[236:239], v[192:195], v[128:131]\n"
    "v_or_b32_e64 v189, v189, v205\n"
    "v_pk_mul_f16 v233, v188, v233\n"
    "v_mfma_f32_16x16x32_f16 v[132:135], a[244:247], v[192:195], v[132:135]\n"
    "s_barrier\n"
    "v_mfma_f32_16x16x32_f16 v[104:107], a[48:51], v[196:199], v[104:107]\n"
    "s_waitcnt lgkmcnt(7)\n"
    "v_cvt_pk_f16_f32 v192, v168, v169\n"
    "v_lshlrev_b32_sdwa v193, 1, v170 dst_sel:DWORD dst_unused:UNUSED_PAD src0_sel:DWORD src1_sel:WORD_1\n"
    "v_mfma_f32_16x16x32_f16 v[108:111], a[52:55], v[196:199], v[108:111]\n"
    "ds_read_u16 v193, v193\n"
    "v_lshlrev_b32_sdwa v201, 1, v171 dst_sel:DWORD dst_unused:UNUSED_PAD src0_sel:DWORD src1_sel:WORD_1\n"
    "v_mfma_f32_16x16x32_f16 v[120:123], a[56:59], v[196:199], v[120:123]\n"
    "ds_read_u16_d16_hi v201, v201\n"
    "v_lshlrev_b32_sdwa v194, 1, v172 dst_sel:DWORD dst_unused:UNUSED_PAD src0_sel:DWORD src1_sel:WORD_1\n"
    "v_mfma_f32_16x16x32_f16 v[124:127], a[60:63], v[196:199], v[124:127]\n"
    "ds_read_u16 v194, v194\n"
    "v_lshlrev_b32_sdwa v202, 1, v173 dst_sel:DWORD dst_unused:UNUSED_PAD src0_sel:DWORD src1_sel:WORD_1\n"
    "v_mfma_f32_16x16x32_f16 v[136:139], v[236:239], v[196:199], v[136:139]\n"
    "ds_read_u16_d16_hi v202, v202\n"
    "ds_read_b128 v[236:239], v244 offset:16384\n"
    "v_mfma_f32_16x16x32_f16 v[140:143], a[244:247], v[196:199], v[140:143]\n"
    "s_waitcnt lgkmcnt(8)\n"
    "v_lshlrev_b32_sdwa v195, 1, v174 dst_sel:DWORD dst_unused:UNUSED_PAD src0_sel:DWORD src1_sel:WORD_1\n"
    "v_lshlrev_b32_sdwa v203, 1, v175 dst_sel:DWORD dst_unused:UNUSED_PAD src0_sel:DWORD src1_sel:WORD_1\n"
    "v_mfma_f32_16x16x32_bf16 v[168:171], a[128:131], v[144:147], 0\n"
    "ds_read_u16 v195, v195\n"
    "v_exp_f16_e32 v234, v232\n"
    "v_smfmac_f32_32x32x32_bf16 v[128:143], a[248:251], a[232:239], v241\n"
    "ds_read_u16_d16_hi v203, v203\n"
    "v_or_b32_e64 v190, v190, v206\n"
    "v_cvt_pk_f16_f32 v196, v176, v177\n"
    "v_lshlrev_b32_sdwa v197, 1, v178 dst_sel:DWORD dst_unused:UNUSED_PAD src0_sel:DWORD src1_sel:WORD_1\n"
    "v_exp_f16_e64 v235, v233\n"
    "v_mfma_f32_16x16x32_bf16 v[168:171], a[176:179], v[148:151], v[168:171]\n"
    "ds_read_b128 a[244:247], v244 offset:18432\n"
    "ds_read_u16 v197, v197\n"
    "v_mfma_f32_16x16x32_bf16 v[168:171], a[96:99], v[152:155], v[168:171]\n"
    "v_lshlrev_b32_sdwa v205, 1, v179 dst_sel:DWORD dst_unused:UNUSED_PAD src0_sel:DWORD src1_sel:WORD_1\n"
    "v_lshlrev_b32_sdwa v198, 1, v180 dst_sel:DWORD dst_unused:UNUSED_PAD src0_sel:DWORD src1_sel:WORD_1\n"
    "v_mfma_f32_16x16x32_bf16 v[172:175], a[132:135], v[144:147], 0\n"
    "v_lshlrev_b32_sdwa v206, 1, v181 dst_sel:DWORD dst_unused:UNUSED_PAD src0_sel:DWORD src1_sel:WORD_1\n"
    "v_lshlrev_b32_sdwa v199, 1, v182 dst_sel:DWORD dst_unused:UNUSED_PAD src0_sel:DWORD src1_sel:WORD_1\n"
    "v_mfma_f32_16x16x32_bf16 v[172:175], a[180:183], v[148:151], v[172:175]\n"
    "v_exp_f16_sdwa v234, v232 dst_sel:WORD_1 dst_unused:UNUSED_PRESERVE src0_sel:WORD_1\n"
    "s_waitcnt lgkmcnt(7)\n"
    "v_mfma_f32_16x16x32_bf16 v[172:175], a[240:243], v[152:155], v[172:175]\n"
    "ds_read_u16_d16_hi v205, v205\n"
    "v_or_b32_e32 v191, v191, v207\n"
    "v_mfma_f32_16x16x32_bf16 v[176:179], a[128:131], v[156:159], 0\n"
    "v_lshlrev_b32_sdwa v207, 1, v183 dst_sel:DWORD dst_unused:UNUSED_PAD src0_sel:DWORD src1_sel:WORD_1\n"
    "v_exp_f16_sdwa v235, v233 dst_sel:WORD_1 dst_unused:UNUSED_PRESERVE src0_sel:WORD_1\n"
    "v_mfma_f32_16x16x32_bf16 v[176:179], a[176:179], v[160:163], v[176:179]\n"
    "ds_read_u16 v198, v198\n"
    "v_pk_fma_f16 v234, v234, s49, s49\n"
    "v_mfma_f32_16x16x32_bf16 v[176:179], a[96:99], v[164:167], v[176:179]\n"
    "s_waitcnt lgkmcnt(6)\n"
    "ds_read_u16_d16_hi v206, v206\n"
    "v_pk_fma_f16 v235, v235, s49, s49\n"
    "v_mfma_f32_16x16x32_bf16 v[180:183], a[132:135], v[156:159], 0\n"
    "ds_read_u16 v199, v199\n"
    "v_rcp_f16_e32 v232, v234\n"
    "v_mfma_f32_16x16x32_bf16 v[180:183], a[180:183], v[160:163], v[180:183]\n"
    "ds_read_u16_d16_hi v207, v207\n"
    "v_rcp_f16_e64 v233, v235\n"
    "v_mfma_f32_16x16x32_bf16 v[180:183], a[240:243], v[164:167], v[180:183]\n"
    "v_rcp_f16_sdwa v232, v234 dst_sel:WORD_1 dst_unused:UNUSED_PRESERVE src0_sel:WORD_1\n"
    "v_or_b32_e64 v193, v193, v201\n"
    "v_rcp_f16_sdwa v233, v235 dst_sel:WORD_1 dst_unused:UNUSED_PRESERVE src0_sel:WORD_1\n"
    "v_pk_mul_f16 v184, v184, v232\n"
    "v_or_b32_e64 v194, v194, v202\n"
    "v_pk_mul_f16 v188, v188, v233\n"
    "v_mfma_f32_16x16x32_f16 v[96:99], a[64:67], v[184:187], v[96:99]\n"
    "s_waitcnt lgkmcnt(6)\n"
    "ds_read_b128 a[240:243], v244 offset:36864\n"
    "v_pk_fma_f16 v232, v192, v192, s48\n"
    "v_mfma_f32_16x16x32_f16 v[100:103], a[68:71], v[184:187], v[100:103]\n"
    "v_or_b32_e32 v195, v195, v203\n"
    "v_pk_fma_f16 v233, v196, v196, s48\n"
    "v_mfma_f32_16x16x32_f16 v[112:115], a[72:75], v[184:187], v[112:115]\n"
    "v_pk_mul_f16 v232, v192, v232\n"
    "s_waitcnt lgkmcnt(5)\n"
    "v_or_b32_e32 v197, v197, v205\n"
    "v_mfma_f32_16x16x32_f16 v[116:119], a[76:79], v[184:187], v[116:119]\n"
    "v_pk_mul_f16 v233, v196, v233\n"
    "v_lshlrev_b32_sdwa v201, 1, v171 dst_sel:DWORD dst_unused:UNUSED_PAD src0_sel:DWORD src1_sel:WORD_1\n"
    "v_mfma_f32_16x16x32_f16 v[128:131], v[236:239], v[184:187], v[128:131]\n"
    "ds_read_u16_d16_hi v201, v201\n"
    "v_lshlrev_b32_sdwa v202, 1, v173 dst_sel:DWORD dst_unused:UNUSED_PAD src0_sel:DWORD src1_sel:WORD_1\n"
    "v_mfma_f32_16x16x32_f16 v[132:135], a[244:247], v[184:187], v[132:135]\n"
    "ds_read_u16_d16_hi v202, v202\n"
    "v_cvt_pk_f16_f32 v184, v168, v169\n"
    "v_mfma_f32_16x16x32_f16 v[104:107], a[64:67], v[188:191], v[104:107]\n"
    "s_waitcnt lgkmcnt(5)\n"
    "s_nop 0\n"
    "v_lshlrev_b32_sdwa v185, 1, v170 dst_sel:DWORD dst_unused:UNUSED_PAD src0_sel:DWORD src1_sel:WORD_1\n"
    "v_lshlrev_b32_sdwa v186, 1, v172 dst_sel:DWORD dst_unused:UNUSED_PAD src0_sel:DWORD src1_sel:WORD_1\n"
    "v_mfma_f32_16x16x32_f16 v[108:111], a[68:71], v[188:191], v[108:111]\n"
    "ds_read_u16 v185, v185\n"
    "ds_read_u16 v186, v186\n"
    "v_mfma_f32_16x16x32_f16 v[120:123], a[72:75], v[188:191], v[120:123]\n"
    "v_lshlrev_b32_sdwa v187, 1, v174 dst_sel:DWORD dst_unused:UNUSED_PAD src0_sel:DWORD src1_sel:WORD_1\n"
    "v_lshlrev_b32_sdwa v203, 1, v175 dst_sel:DWORD dst_unused:UNUSED_PAD src0_sel:DWORD src1_sel:WORD_1\n"
    "v_mfma_f32_16x16x32_f16 v[124:127], a[76:79], v[188:191], v[124:127]\n"
    "s_waitcnt lgkmcnt(4)\n"
    "ds_read_u16 v187, v187\n"
    "v_exp_f16_e32 v234, v232\n"
    "v_mfma_f32_16x16x32_f16 v[136:139], v[236:239], v[188:191], v[136:139]\n"
    "ds_read_b128 v[236:239], v244 offset:20480\n"
    "ds_read_u16_d16_hi v203, v203\n"
    "v_mfma_f32_16x16x32_f16 v[140:143], a[244:247], v[188:191], v[140:143]\n"
    "v_or_b32_e64 v198, v198, v206\n"
    "v_cvt_pk_f16_f32 v188, v176, v177\n"
    "v_mfma_f32_16x16x32_bf16 v[168:171], a[136:139], v[144:147], 0\n"
    "ds_read_b128 a[244:247], v244 offset:22528\n"
    "v_lshlrev_b32_sdwa v189, 1, v178 dst_sel:DWORD dst_unused:UNUSED_PAD src0_sel:DWORD src1_sel:WORD_1\n"
    "v_mfma_f32_16x16x32_bf16 v[168:171], a[184:187], v[148:151], v[168:171]\n"
    "v_exp_f16_e64 v235, v233\n"
    "v_mfma_f32_16x16x32_bf16 v[168:171], a[100:103], v[152:155], v[168:171]\n"
    "ds_read_u16 v189, v189\n"
    "v_lshlrev_b32_sdwa v205, 1, v179 dst_sel:DWORD dst_unused:UNUSED_PAD src0_sel:DWORD src1_sel:WORD_1\n"
    "v_mfma_f32_16x16x32_bf16 v[172:175], a[140:143], v[144:147], 0\n"
    "v_lshlrev_b32_sdwa v190, 1, v180 dst_sel:DWORD dst_unused:UNUSED_PAD src0_sel:DWORD src1_sel:WORD_1\n"
    "v_lshlrev_b32_sdwa v206, 1, v181 dst_sel:DWORD dst_unused:UNUSED_PAD src0_sel:DWORD src1_sel:WORD_1\n"
    "v_mfma_f32_16x16x32_bf16 v[172:175], a[188:191], v[148:151], v[172:175]\n"
    "v_lshlrev_b32_sdwa v191, 1, v182 dst_sel:DWORD dst_unused:UNUSED_PAD src0_sel:DWORD src1_sel:WORD_1\n"
    "v_exp_f16_sdwa v234, v232 dst_sel:WORD_1 dst_unused:UNUSED_PRESERVE src0_sel:WORD_1\n"
    "v_mfma_f32_16x16x32_bf16 v[172:175], a[240:243], v[152:155], v[172:175]\n"
    "s_waitcnt lgkmcnt(6)\n"
    "ds_read_u16_d16_hi v205, v205\n"
    "v_or_b32_e32 v199, v199, v207\n"
    "v_mfma_f32_16x16x32_bf16 v[176:179], a[136:139], v[156:159], 0\n"
    "v_lshlrev_b32_sdwa v207, 1, v183 dst_sel:DWORD dst_unused:UNUSED_PAD src0_sel:DWORD src1_sel:WORD_1\n"
    "v_exp_f16_sdwa v235, v233 dst_sel:WORD_1 dst_unused:UNUSED_PRESERVE src0_sel:WORD_1\n"
    "v_mfma_f32_16x16x32_bf16 v[176:179], a[184:187], v[160:163], v[176:179]\n"
    "s_waitcnt lgkmcnt(6)\n"
    "s_nop 0\n"
    "ds_read_u16 v190, v190\n"
    "v_pk_fma_f16 v234, v234, s49, s49\n"
    "v_mfma_f32_16x16x32_bf16 v[176:179], a[100:103], v[164:167], v[176:179]\n"
    "ds_read_u16_d16_hi v206, v206\n"
    "v_pk_fma_f16 v235, v235, s49, s49\n"
    "s_waitcnt vmcnt(0)\n"
    "v_smfmac_f32_32x32x32_bf16 v[128:143], a[248:251], a[224:231], v241\n"
    "ds_read_u16 v191, v191\n"
    "buffer_load_dwordx4 a[232:235], v245, s[40:43], s58 offen offset:64\n"
    "v_rcp_f16_e32 v232, v234\n"
    "v_rcp_f16_e64 v233, v235\n"
    "v_mfma_f32_16x16x32_bf16 v[180:183], a[140:143], v[156:159], 0\n"
    "s_waitcnt lgkmcnt(5)\n"
    "ds_read_u16_d16_hi v207, v207\n"
    "v_rcp_f16_sdwa v232, v234 dst_sel:WORD_1 dst_unused:UNUSED_PRESERVE src0_sel:WORD_1\n"
    "v_mfma_f32_16x16x32_bf16 v[180:183], a[188:191], v[160:163], v[180:183]\n"
    "v_or_b32_e32 v185, v185, v201\n"
    "v_rcp_f16_sdwa v233, v235 dst_sel:WORD_1 dst_unused:UNUSED_PRESERVE src0_sel:WORD_1\n"
    "v_mfma_f32_16x16x32_bf16 v[180:183], a[240:243], v[164:167], v[180:183]\n"
    "v_pk_mul_f16 v192, v192, v232\n"
    "v_or_b32_e64 v186, v186, v202\n"
    "v_pk_mul_f16 v196, v196, v233\n"
    "v_mfma_f32_16x16x32_f16 v[96:99], a[80:83], v[192:195], v[96:99]\n"
    "v_pk_fma_f16 v232, v184, v184, s48\n"
    "v_or_b32_e64 v187, v187, v203\n"
    "v_mfma_f32_16x16x32_f16 v[100:103], a[84:87], v[192:195], v[100:103]\n"
    "s_waitcnt lgkmcnt(4)\n"
    "v_pk_fma_f16 v233, v188, v188, s48\n"
    "v_pk_mul_f16 v232, v184, v232\n"
    "v_mfma_f32_16x16x32_f16 v[112:115], a[88:91], v[192:195], v[112:115]\n"
    "v_or_b32_e32 v189, v189, v205\n"
    "v_pk_mul_f16 v233, v188, v233\n"
    "v_mfma_f32_16x16x32_f16 v[116:119], a[92:95], v[192:195], v[116:119]\n"
    "v_lshlrev_b32_sdwa v201, 1, v171 dst_sel:DWORD dst_unused:UNUSED_PAD src0_sel:DWORD src1_sel:WORD_1\n"
    "v_lshlrev_b32_sdwa v202, 1, v173 dst_sel:DWORD dst_unused:UNUSED_PAD src0_sel:DWORD src1_sel:WORD_1\n"
    "v_mfma_f32_16x16x32_f16 v[128:131], v[236:239], v[192:195], v[128:131]\n"
    "s_waitcnt lgkmcnt(2)\n"
    "s_nop 0\n"
    "buffer_load_dwordx4 a[236:239], v245, s[40:43], s101 offen offset:64\n"
    "ds_read_u16_d16_hi v201, v201\n"
    "ds_read_u16_d16_hi v202, v202\n"
    "v_mfma_f32_16x16x32_f16 v[132:135], a[244:247], v[192:195], v[132:135]\n"
    "v_cvt_pk_f16_f32 v192, v168, v169\n"
    "v_lshlrev_b32_sdwa v193, 1, v170 dst_sel:DWORD dst_unused:UNUSED_PAD src0_sel:DWORD src1_sel:WORD_1\n"
    "v_mfma_f32_16x16x32_f16 v[104:107], a[80:83], v[196:199], v[104:107]\n"
    "ds_read_u16 v193, v193\n"
    "v_lshlrev_b32_sdwa v194, 1, v172 dst_sel:DWORD dst_unused:UNUSED_PAD src0_sel:DWORD src1_sel:WORD_1\n"
    "v_mfma_f32_16x16x32_f16 v[108:111], a[84:87], v[196:199], v[108:111]\n"
    "s_waitcnt lgkmcnt(3)\n"
    "s_nop 0\n"
    "ds_read_u16 v194, v194\n"
    "v_lshlrev_b32_sdwa v195, 1, v174 dst_sel:DWORD dst_unused:UNUSED_PAD src0_sel:DWORD src1_sel:WORD_1\n"
    "v_mfma_f32_16x16x32_f16 v[120:123], a[88:91], v[196:199], v[120:123]\n"
    "ds_read_u16 v195, v195\n"
    "v_lshlrev_b32_sdwa v203, 1, v175 dst_sel:DWORD dst_unused:UNUSED_PAD src0_sel:DWORD src1_sel:WORD_1\n"
    "v_mfma_f32_16x16x32_f16 v[124:127], a[92:95], v[196:199], v[124:127]\n"
    "v_exp_f16_e64 v234, v232\n"
    "v_mfma_f32_16x16x32_f16 v[136:139], v[236:239], v[196:199], v[136:139]\n"
    "ds_read_b128 v[236:239], v244 offset:0\n"
    "ds_read_u16_d16_hi v203, v203\n"
    "v_mfma_f32_16x16x32_f16 v[140:143], a[244:247], v[196:199], v[140:143]\n"
    "v_or_b32_e64 v190, v190, v206\n"
    "v_cvt_pk_f16_f32 v196, v176, v177\n"
    "v_mfma_f32_16x16x32_bf16 v[168:171], a[144:147], v[144:147], 0\n"
    "ds_read_b128 a[244:247], v244 offset:2048\n"
    "v_lshlrev_b32_sdwa v197, 1, v178 dst_sel:DWORD dst_unused:UNUSED_PAD src0_sel:DWORD src1_sel:WORD_1\n"
    "v_mfma_f32_16x16x32_bf16 v[168:171], a[192:195], v[148:151], v[168:171]\n"
    "v_exp_f16_e64 v235, v233\n"
    "v_mfma_f32_16x16x32_bf16 v[168:171], a[104:107], v[152:155], v[168:171]\n"
    "buffer_load_dwordx4 a[224:227], v245, s[40:43], s58 offen offset:0\n"
    "ds_read_u16 v197, v197\n"
    "v_mfma_f32_16x16x32_bf16 v[172:175], a[148:151], v[144:147], 0\n"
    "v_lshlrev_b32_sdwa v205, 1, v179 dst_sel:DWORD dst_unused:UNUSED_PAD src0_sel:DWORD src1_sel:WORD_1\n"
    "v_lshlrev_b32_sdwa v198, 1, v180 dst_sel:DWORD dst_unused:UNUSED_PAD src0_sel:DWORD src1_sel:WORD_1\n"
    "v_mfma_f32_16x16x32_bf16 v[172:175], a[196:199], v[148:151], v[172:175]\n"
    "v_lshlrev_b32_sdwa v206, 1, v181 dst_sel:DWORD dst_unused:UNUSED_PAD src0_sel:DWORD src1_sel:WORD_1\n"
    "v_lshlrev_b32_sdwa v199, 1, v182 dst_sel:DWORD dst_unused:UNUSED_PAD src0_sel:DWORD src1_sel:WORD_1\n"
    "v_exp_f16_sdwa v234, v232 dst_sel:WORD_1 dst_unused:UNUSED_PRESERVE src0_sel:WORD_1\n"
    "v_mfma_f32_16x16x32_bf16 v[172:175], a[108:111], v[152:155], v[172:175]\n"
    "s_waitcnt lgkmcnt(6)\n"
    "ds_read_u16_d16_hi v205, v205\n"
    "v_or_b32_e32 v191, v191, v207\n"
    "v_mfma_f32_16x16x32_bf16 v[176:179], a[144:147], v[156:159], 0\n"
    "v_lshlrev_b32_sdwa v207, 1, v183 dst_sel:DWORD dst_unused:UNUSED_PAD src0_sel:DWORD src1_sel:WORD_1\n"
    "v_exp_f16_sdwa v235, v233 dst_sel:WORD_1 dst_unused:UNUSED_PRESERVE src0_sel:WORD_1\n"
    "v_mfma_f32_16x16x32_bf16 v[176:179], a[192:195], v[160:163], v[176:179]\n"
    "s_waitcnt lgkmcnt(6)\n"
    "s_nop 0\n"
    "ds_read_u16 v198, v198\n"
    "v_pk_fma_f16 v234, v234, s49, s49\n"
    "v_mfma_f32_16x16x32_bf16 v[176:179], a[104:107], v[164:167], v[176:179]\n"
    "ds_read_u16_d16_hi v206, v206\n"
    "v_pk_fma_f16 v235, v235, s49, s49\n"
    "v_mfma_f32_16x16x32_bf16 v[180:183], a[148:151], v[156:159], 0\n"
    "ds_read_u16 v199, v199\n"
    "v_cvt_pk_bf16_f32 v96, v96, v97\n"
    "v_mfma_f32_16x16x32_bf16 v[180:183], a[196:199], v[160:163], v[180:183]\n"
    "v_cvt_pk_bf16_f32 v97, v98, v99\n"
    "v_rcp_f16_e64 v232, v234\n"
    "v_mfma_f32_16x16x32_bf16 v[180:183], a[108:111], v[164:167], v[180:183]\n"
    "s_waitcnt lgkmcnt(5)\n"
    "s_nop 0\n"
    "ds_read_u16_d16_hi v207, v207\n"
    "buffer_load_dwordx4 a[228:231], v245, s[40:43], s101 offen offset:0\n"
    "v_cvt_pk_bf16_f32 v98, v100, v101\n"
    "v_cvt_pk_bf16_f32 v99, v102, v103\n"
    "ds_write_b128 v242, v[96:99] offset:24640\n"
    "v_cvt_pk_bf16_f32 v112, v112, v113\n"
    "v_rcp_f16_e64 v233, v235\n"
    "v_cvt_pk_bf16_f32 v113, v114, v115\n"
    "v_cvt_pk_bf16_f32 v114, v116, v117\n"
    "v_cvt_pk_bf16_f32 v115, v118, v119\n"
    "ds_write_b128 v242, v[112:115] offset:26688\n"
    "v_rcp_f16_sdwa v232, v234 dst_sel:WORD_1 dst_unused:UNUSED_PRESERVE src0_sel:WORD_1\n"
    "v_cvt_pk_bf16_f32 v128, v128, v129\n"
    "v_cvt_pk_bf16_f32 v129, v130, v131\n"
    "v_cvt_pk_bf16_f32 v130, v132, v133\n"
    "v_or_b32_e64 v193, v193, v201\n"
    "v_cvt_pk_bf16_f32 v131, v134, v135\n"
    "ds_write_b128 v242, v[128:131] offset:28736\n"
    "v_rcp_f16_sdwa v233, v235 dst_sel:WORD_1 dst_unused:UNUSED_PRESERVE src0_sel:WORD_1\n"
    "v_cvt_pk_bf16_f32 v104, v104, v105\n"
    "v_cvt_pk_bf16_f32 v105, v106, v107\n"
    "v_cvt_pk_bf16_f32 v106, v108, v109\n"
    "v_pk_mul_f16 v184, v184, v232\n"
    "v_cvt_pk_bf16_f32 v107, v110, v111\n"
    "ds_write_b128 v242, v[104:107] offset:25664\n"
    "v_mfma_f32_16x16x32_f16 v[96:99], a[0:3], v[184:187], 0\n"
    "s_waitcnt lgkmcnt(2)\n"
    "v_or_b32_e32 v194, v194, v202\n"
    "v_cvt_pk_bf16_f32 v120, v120, v121\n"
    "v_mfma_f32_16x16x32_f16 v[100:103], a[4:7], v[184:187], 0\n"
    "v_cvt_pk_bf16_f32 v121, v122, v123\n"
    "v_pk_mul_f16 v188, v188, v233\n"
    "v_mfma_f32_16x16x32_f16 v[112:115], a[8:11], v[184:187], 0\n"
    "v_cvt_pk_bf16_f32 v122, v124, v125\n"
    "v_cvt_pk_bf16_f32 v123, v126, v127\n"
    "v_mfma_f32_16x16x32_f16 v[116:119], a[12:15], v[184:187], 0\n"
    "ds_write_b128 v242, v[120:123] offset:27712\n"
    "v_mfma_f32_16x16x32_f16 v[128:131], v[236:239], v[184:187], 0\n"
    "v_pk_fma_f16 v232, v192, v192, s48\n"
    "v_cvt_pk_bf16_f32 v136, v136, v137\n"
    "v_mfma_f32_16x16x32_f16 v[132:135], a[244:247], v[184:187], 0\n"
    "v_cvt_pk_bf16_f32 v137, v138, v139\n"
    "v_cvt_pk_bf16_f32 v138, v140, v141\n"
    "v_mfma_f32_16x16x32_f16 v[104:107], a[0:3], v[188:191], 0\n"
    "v_or_b32_e64 v195, v195, v203\n"
    "v_cvt_pk_bf16_f32 v139, v142, v143\n"
    "v_mfma_f32_16x16x32_f16 v[108:111], a[4:7], v[188:191], 0\n"
    "ds_write_b128 v242, v[136:139] offset:29760\n"
    "v_mfma_f32_16x16x32_f16 v[120:123], a[8:11], v[188:191], 0\n"
    "v_pk_fma_f16 v233, v196, v196, s48\n"
    "v_pk_mul_f16 v232, v192, v232\n"
    "v_mfma_f32_16x16x32_f16 v[124:127], a[12:15], v[188:191], 0\n"
    "v_or_b32_e64 v197, v197, v205\n"
    "v_pk_mul_f16 v233, v196, v233\n"
    "v_mfma_f32_16x16x32_f16 v[136:139], v[236:239], v[188:191], 0\n"
    "ds_read_b128 a[252:255], v249 offset:0\n"
    "ds_read_b128 v[236:239], v244 offset:4096\n"
    "v_mfma_f32_16x16x32_f16 v[140:143], a[244:247], v[188:191], 0\n"
    "v_cvt_pk_f16_f32 v184, v168, v169\n"
    "v_lshlrev_b32_sdwa v185, 1, v170 dst_sel:DWORD dst_unused:UNUSED_PAD src0_sel:DWORD src1_sel:WORD_1\n"
    "v_lshlrev_b32_sdwa v201, 1, v171 dst_sel:DWORD dst_unused:UNUSED_PAD src0_sel:DWORD src1_sel:WORD_1\n"
    "v_mfma_f32_16x16x32_bf16 v[168:171], a[152:155], v[144:147], 0\n"
    "ds_read_u16 v185, v185\n"
    "ds_read_u16_d16_hi v201, v201\n"
    "v_mfma_f32_16x16x32_bf16 v[168:171], a[200:203], v[148:151], v[168:171]\n"
    "v_lshlrev_b32_sdwa v186, 1, v172 dst_sel:DWORD dst_unused:UNUSED_PAD src0_sel:DWORD src1_sel:WORD_1\n"
    "v_lshlrev_b32_sdwa v202, 1, v173 dst_sel:DWORD dst_unused:UNUSED_PAD src0_sel:DWORD src1_sel:WORD_1\n"
    "v_lshlrev_b32_sdwa v187, 1, v174 dst_sel:DWORD dst_unused:UNUSED_PAD src0_sel:DWORD src1_sel:WORD_1\n"
    "v_lshlrev_b32_sdwa v203, 1, v175 dst_sel:DWORD dst_unused:UNUSED_PAD src0_sel:DWORD src1_sel:WORD_1\n"
    "v_exp_f16_e64 v234, v232\n"
    "v_mfma_f32_16x16x32_bf16 v[168:171], a[112:115], v[152:155], v[168:171]\n"
    "ds_read_u16 v186, v186\n"
    "ds_read_u16_d16_hi v202, v202\n"
    "ds_read_u16 v187, v187\n"
    "v_or_b32_e64 v198, v198, v206\n"
    "v_cvt_pk_f16_f32 v188, v176, v177\n"
    "v_lshlrev_b32_sdwa v189, 1, v178 dst_sel:DWORD dst_unused:UNUSED_PAD src0_sel:DWORD src1_sel:WORD_1\n"
    "v_exp_f16_e64 v235, v233\n"
    "v_mfma_f32_16x16x32_bf16 v[172:175], a[156:159], v[144:147], 0\n"
    "ds_read_u16_d16_hi v203, v203\n"
    "s_waitcnt lgkmcnt(7)\n"
    "s_nop 0\n"
    "buffer_store_dwordx4 a[252:255], v255, s[44:47], s57 offen offset:0 sc1\n"
    "ds_read_b128 a[244:247], v244 offset:6144\n"
    "v_mfma_f32_16x16x32_bf16 v[172:175], a[204:207], v[148:151], v[172:175]\n"
    "ds_read_b128 a[252:255], v249 offset:2048\n"
    "v_lshlrev_b32_sdwa v205, 1, v179 dst_sel:DWORD dst_unused:UNUSED_PAD src0_sel:DWORD src1_sel:WORD_1\n"
    "v_lshlrev_b32_sdwa v190, 1, v180 dst_sel:DWORD dst_unused:UNUSED_PAD src0_sel:DWORD src1_sel:WORD_1\n"
    "v_lshlrev_b32_sdwa v206, 1, v181 dst_sel:DWORD dst_unused:UNUSED_PAD src0_sel:DWORD src1_sel:WORD_1\n"
    "v_lshlrev_b32_sdwa v191, 1, v182 dst_sel:DWORD dst_unused:UNUSED_PAD src0_sel:DWORD src1_sel:WORD_1\n"
    "v_exp_f16_sdwa v234, v232 dst_sel:WORD_1 dst_unused:UNUSED_PRESERVE src0_sel:WORD_1\n"
    "v_mfma_f32_16x16x32_bf16 v[172:175], a[116:119], v[152:155], v[172:175]\n"
    "ds_read_u16 v189, v189\n"
    "ds_read_u16_d16_hi v205, v205\n"
    "v_mfma_f32_16x16x32_bf16 v[176:179], a[152:155], v[156:159], 0\n"
    "v_or_b32_e64 v199, v199, v207\n"
    "v_lshlrev_b32_sdwa v207, 1, v183 dst_sel:DWORD dst_unused:UNUSED_PAD src0_sel:DWORD src1_sel:WORD_1\n"
    "v_exp_f16_sdwa v235, v233 dst_sel:WORD_1 dst_unused:UNUSED_PRESERVE src0_sel:WORD_1\n"
    "v_mfma_f32_16x16x32_bf16 v[176:179], a[200:203], v[160:163], v[176:179]\n"
    "s_waitcnt lgkmcnt(8)\n"
    "s_nop 0\n"
    "ds_read_u16 v190, v190\n"
    "v_pk_fma_f16 v234, v234, s49, s49\n"
    "v_mfma_f32_16x16x32_bf16 v[176:179], a[112:115], v[164:167], v[176:179]\n"
    "ds_read_u16_d16_hi v206, v206\n"
    "v_pk_fma_f16 v235, v235, s49, s49\n"
    "v_mfma_f32_16x16x32_bf16 v[180:183], a[156:159], v[156:159], 0\n"
    "v_rcp_f16_e64 v232, v234\n"
    "v_mfma_f32_16x16x32_bf16 v[180:183], a[204:207], v[160:163], v[180:183]\n"
    "s_waitcnt lgkmcnt(8)\n"
    "ds_read_u16 v191, v191\n"
    "v_rcp_f16_e32 v233, v235\n"
    "v_mfma_f32_16x16x32_bf16 v[180:183], a[116:119], v[164:167], v[180:183]\n"
    "v_rcp_f16_sdwa v232, v234 dst_sel:WORD_1 dst_unused:UNUSED_PRESERVE src0_sel:WORD_1\n"
    "v_or_b32_e64 v185, v185, v201\n"
    "v_rcp_f16_sdwa v233, v235 dst_sel:WORD_1 dst_unused:UNUSED_PRESERVE src0_sel:WORD_1\n"
    "v_pk_mul_f16 v192, v192, v232\n"
    "v_or_b32_e64 v186, v186, v202\n"
    "v_pk_mul_f16 v196, v196, v233\n"
    "v_mfma_f32_16x16x32_f16 v[96:99], a[16:19], v[192:195], v[96:99]\n"
    "s_waitcnt lgkmcnt(5)\n"
    "ds_read_u16_d16_hi v207, v207\n"
    "ds_read_b128 a[240:243], v244 offset:34816\n"
    "buffer_store_dwordx4 a[252:255], v255, s[44:47], s57 offen offset:64 sc1\n"
    "v_mfma_f32_16x16x32_f16 v[100:103], a[20:23], v[192:195], v[100:103]\n"
    "v_pk_fma_f16 v232, v184, v184, s48\n"
    "v_or_b32_e32 v187, v187, v203\n"
    "v_mfma_f32_16x16x32_f16 v[112:115], a[24:27], v[192:195], v[112:115]\n"
    "ds_read_b128 a[252:255], v249 offset:4096\n"
    "v_pk_fma_f16 v233, v188, v188, s48\n"
    "v_mfma_f32_16x16x32_f16 v[116:119], a[28:31], v[192:195], v[116:119]\n"
    "v_pk_mul_f16 v232, v184, v232\n"
    "v_pk_mul_f16 v233, v188, v233\n"
    "v_mfma_f32_16x16x32_f16 v[128:131], v[236:239], v[192:195], v[128:131]\n"
    "s_waitcnt lgkmcnt(6)\n"
    "v_or_b32_e32 v189, v189, v205\n"
    "v_lshlrev_b32_sdwa v201, 1, v171 dst_sel:DWORD dst_unused:UNUSED_PAD src0_sel:DWORD src1_sel:WORD_1\n"
    "v_mfma_f32_16x16x32_f16 v[132:135], a[244:247], v[192:195], v[132:135]\n"
    "ds_read_u16_d16_hi v201, v201\n"
    "v_cvt_pk_f16_f32 v192, v168, v169\n"
    "v_mfma_f32_16x16x32_f16 v[104:107], a[16:19], v[196:199], v[104:107]\n"
    "v_lshlrev_b32_sdwa v193, 1, v170 dst_sel:DWORD dst_unused:UNUSED_PAD src0_sel:DWORD src1_sel:WORD_1\n"
    "v_lshlrev_b32_sdwa v194, 1, v172 dst_sel:DWORD dst_unused:UNUSED_PAD src0_sel:DWORD src1_sel:WORD_1\n"
    "v_mfma_f32_16x16x32_f16 v[108:111], a[20:23], v[196:199], v[108:111]\n"
    "s_waitcnt lgkmcnt(5)\n"
    "s_nop 0\n"
    "ds_read_u16 v193, v193\n"
    "ds_read_u16 v194, v194\n"
    "v_mfma_f32_16x16x32_f16 v[120:123], a[24:27], v[196:199], v[120:123]\n"
    "v_lshlrev_b32_sdwa v202, 1, v173 dst_sel:DWORD dst_unused:UNUSED_PAD src0_sel:DWORD src1_sel:WORD_1\n"
    "v_lshlrev_b32_sdwa v195, 1, v174 dst_sel:DWORD dst_unused:UNUSED_PAD src0_sel:DWORD src1_sel:WORD_1\n"
    "v_mfma_f32_16x16x32_f16 v[124:127], a[28:31], v[196:199], v[124:127]\n"
    "ds_read_u16_d16_hi v202, v202\n"
    "ds_read_u16 v195, v195\n"
    "v_mfma_f32_16x16x32_f16 v[136:139], v[236:239], v[196:199], v[136:139]\n"
    "ds_read_b128 v[236:239], v244 offset:8192\n"
    "v_lshlrev_b32_sdwa v203, 1, v175 dst_sel:DWORD dst_unused:UNUSED_PAD src0_sel:DWORD src1_sel:WORD_1\n"
    "v_mfma_f32_16x16x32_f16 v[140:143], a[244:247], v[196:199], v[140:143]\n"
    "s_waitcnt lgkmcnt(7)\n"
    "v_exp_f16_e32 v234, v232\n"
    "v_mfma_f32_16x16x32_bf16 v[168:171], a[160:163], v[144:147], 0\n"
    "s_waitcnt lgkmcnt(6)\n"
    "ds_read_u16_d16_hi v203, v203\n"
    "buffer_store_dwordx4 a[252:255], v255, s[44:47], s57 offen offset:128 sc1\n"
    "ds_read_b128 a[244:247], v244 offset:10240\n"
    "v_mfma_f32_16x16x32_bf16 v[168:171], a[208:211], v[148:151], v[168:171]\n"
    "ds_read_b128 a[252:255], v249 offset:1024\n"
    "v_or_b32_e32 v190, v190, v206\n"
    "v_mfma_f32_16x16x32_bf16 v[168:171], a[120:123], v[152:155], v[168:171]\n"
    "v_cvt_pk_f16_f32 v196, v176, v177\n"
    "v_lshlrev_b32_sdwa v197, 1, v178 dst_sel:DWORD dst_unused:UNUSED_PAD src0_sel:DWORD src1_sel:WORD_1\n"
    "v_exp_f16_e64 v235, v233\n"
    "v_mfma_f32_16x16x32_bf16 v[172:175], a[164:167], v[144:147], 0\n"
    "ds_read_u16 v197, v197\n"
    "v_lshlrev_b32_sdwa v205, 1, v179 dst_sel:DWORD dst_unused:UNUSED_PAD src0_sel:DWORD src1_sel:WORD_1\n"
    "v_mfma_f32_16x16x32_bf16 v[172:175], a[212:215], v[148:151], v[172:175]\n"
    "s_waitcnt lgkmcnt(8)\n"
    "s_nop 0\n"
    "v_lshlrev_b32_sdwa v198, 1, v180 dst_sel:DWORD dst_unused:UNUSED_PAD src0_sel:DWORD src1_sel:WORD_1\n"
    "v_lshlrev_b32_sdwa v206, 1, v181 dst_sel:DWORD dst_unused:UNUSED_PAD src0_sel:DWORD src1_sel:WORD_1\n"
    "v_lshlrev_b32_sdwa v199, 1, v182 dst_sel:DWORD dst_unused:UNUSED_PAD src0_sel:DWORD src1_sel:WORD_1\n"
    "v_exp_f16_sdwa v234, v232 dst_sel:WORD_1 dst_unused:UNUSED_PRESERVE src0_sel:WORD_1\n"
    "v_mfma_f32_16x16x32_bf16 v[172:175], a[240:243], v[152:155], v[172:175]\n"
    "ds_read_u16_d16_hi v205, v205\n"
    "v_or_b32_e64 v191, v191, v207\n"
    "v_mfma_f32_16x16x32_bf16 v[176:179], a[160:163], v[156:159], 0\n"
    "s_waitcnt lgkmcnt(7)\n"
    "s_nop 0\n"
    "v_lshlrev_b32_sdwa v207, 1, v183 dst_sel:DWORD dst_unused:UNUSED_PAD src0_sel:DWORD src1_sel:WORD_1\n"
    "v_exp_f16_sdwa v235, v233 dst_sel:WORD_1 dst_unused:UNUSED_PRESERVE src0_sel:WORD_1\n"
    "v_mfma_f32_16x16x32_bf16 v[176:179], a[208:211], v[160:163], v[176:179]\n"
    "ds_read_u16 v198, v198\n"
    "v_pk_fma_f16 v234, v234, s49, s49\n"
    "v_mfma_f32_16x16x32_bf16 v[176:179], a[120:123], v[164:167], v[176:179]\n"
    "ds_read_u16_d16_hi v206, v206\n"
    "v_pk_fma_f16 v235, v235, s49, s49\n"
    "v_mfma_f32_16x16x32_bf16 v[180:183], a[164:167], v[156:159], 0\n"
    "ds_read_u16 v199, v199\n"
    "v_rcp_f16_e64 v232, v234\n"
    "v_mfma_f32_16x16x32_bf16 v[180:183], a[212:215], v[160:163], v[180:183]\n"
    "s_waitcnt lgkmcnt(6)\n"
    "ds_read_u16_d16_hi v207, v207\n"
    "v_rcp_f16_e32 v233, v235\n"
    "v_mfma_f32_16x16x32_bf16 v[180:183], a[240:243], v[164:167], v[180:183]\n"
    "s_waitcnt lgkmcnt(6)\n"
    "buffer_store_dwordx4 a[252:255], v255, s[44:47], s100 offen offset:0 sc1\n"
    "v_rcp_f16_sdwa v232, v234 dst_sel:WORD_1 dst_unused:UNUSED_PRESERVE src0_sel:WORD_1\n"
    "v_or_b32_e32 v193, v193, v201\n"
    "v_rcp_f16_sdwa v233, v235 dst_sel:WORD_1 dst_unused:UNUSED_PRESERVE src0_sel:WORD_1\n"
    "v_pk_mul_f16 v184, v184, v232\n"
    "v_or_b32_e64 v194, v194, v202\n"
    "v_pk_mul_f16 v188, v188, v233\n"
    "v_mfma_f32_16x16x32_f16 v[96:99], a[32:35], v[184:187], v[96:99]\n"
    "ds_read_b128 a[252:255], v249 offset:3072\n"
    "ds_read_b128 a[240:243], v244 offset:38912\n"
    "v_mfma_f32_16x16x32_f16 v[100:103], a[36:39], v[184:187], v[100:103]\n"
    "s_waitcnt lgkmcnt(6)\n"
    "v_pk_fma_f16 v232, v192, v192, s48\n"
    "v_or_b32_e32 v195, v195, v203\n"
    "v_mfma_f32_16x16x32_f16 v[112:115], a[40:43], v[184:187], v[112:115]\n"
    "v_pk_fma_f16 v233, v196, v196, s48\n"
    "v_pk_mul_f16 v232, v192, v232\n"
    "v_mfma_f32_16x16x32_f16 v[116:119], a[44:47], v[184:187], v[116:119]\n"
    "v_or_b32_e64 v197, v197, v205\n"
    "v_pk_mul_f16 v233, v196, v233\n"
    "v_mfma_f32_16x16x32_f16 v[128:131], v[236:239], v[184:187], v[128:131]\n"
    "s_waitcnt lgkmcnt(4)\n"
    "s_nop 0\n"
    "v_lshlrev_b32_sdwa v201, 1, v171 dst_sel:DWORD dst_unused:UNUSED_PAD src0_sel:DWORD src1_sel:WORD_1\n"
    "v_lshlrev_b32_sdwa v202, 1, v173 dst_sel:DWORD dst_unused:UNUSED_PAD src0_sel:DWORD src1_sel:WORD_1\n"
    "v_mfma_f32_16x16x32_f16 v[132:135], a[244:247], v[184:187], v[132:135]\n"
    "ds_read_u16_d16_hi v201, v201\n"
    "ds_read_u16_d16_hi v202, v202\n"
    "v_mfma_f32_16x16x32_f16 v[104:107], a[32:35], v[188:191], v[104:107]\n"
    "v_cvt_pk_f16_f32 v184, v168, v169\n"
    "v_lshlrev_b32_sdwa v185, 1, v170 dst_sel:DWORD dst_unused:UNUSED_PAD src0_sel:DWORD src1_sel:WORD_1\n"
    "v_mfma_f32_16x16x32_f16 v[108:111], a[36:39], v[188:191], v[108:111]\n"
    "ds_read_u16 v185, v185\n"
    "v_lshlrev_b32_sdwa v186, 1, v172 dst_sel:DWORD dst_unused:UNUSED_PAD src0_sel:DWORD src1_sel:WORD_1\n"
    "v_mfma_f32_16x16x32_f16 v[120:123], a[40:43], v[188:191], v[120:123]\n"
    "s_waitcnt lgkmcnt(4)\n"
    "s_nop 0\n"
    "ds_read_u16 v186, v186\n"
    "buffer_store_dwordx4 a[252:255], v255, s[44:47], s100 offen offset:64 sc1\n"
    "v_mfma_f32_16x16x32_f16 v[124:127], a[44:47], v[188:191], v[124:127]\n"
    "v_lshlrev_b32_sdwa v187, 1, v174 dst_sel:DWORD dst_unused:UNUSED_PAD src0_sel:DWORD src1_sel:WORD_1\n"
    "v_lshlrev_b32_sdwa v203, 1, v175 dst_sel:DWORD dst_unused:UNUSED_PAD src0_sel:DWORD src1_sel:WORD_1\n"
    "v_mfma_f32_16x16x32_f16 v[136:139], v[236:239], v[188:191], v[136:139]\n"
    "ds_read_b128 a[252:255], v249 offset:5120\n"
    "ds_read_b128 v[236:239], v244 offset:12288\n"
    "ds_read_u16 v187, v187\n"
    "v_mfma_f32_16x16x32_f16 v[140:143], a[244:247], v[188:191], v[140:143]\n"
    "v_exp_f16_e64 v234, v232\n"
    "v_mfma_f32_16x16x32_bf16 v[168:171], a[168:171], v[144:147], 0\n"
    "ds_read_u16_d16_hi v203, v203\n"
    "ds_read_b128 a[244:247], v244 offset:14336\n"
    "v_mfma_f32_16x16x32_bf16 v[168:171], a[216:219], v[148:151], v[168:171]\n"
    "v_or_b32_e64 v198, v198, v206\n"
    "v_cvt_pk_f16_f32 v188, v176, v177\n"
    "v_mfma_f32_16x16x32_bf16 v[168:171], a[124:127], v[152:155], v[168:171]\n"
    "v_lshlrev_b32_sdwa v189, 1, v178 dst_sel:DWORD dst_unused:UNUSED_PAD src0_sel:DWORD src1_sel:WORD_1\n"
    "v_exp_f16_e64 v235, v233\n"
    "v_mfma_f32_16x16x32_bf16 v[172:175], a[172:175], v[144:147], 0\n"
    "ds_read_u16 v189, v189\n"
    "v_lshlrev_b32_sdwa v205, 1, v179 dst_sel:DWORD dst_unused:UNUSED_PAD src0_sel:DWORD src1_sel:WORD_1\n"
    "v_mfma_f32_16x16x32_bf16 v[172:175], a[220:223], v[148:151], v[172:175]\n"
    "v_lshlrev_b32_sdwa v190, 1, v180 dst_sel:DWORD dst_unused:UNUSED_PAD src0_sel:DWORD src1_sel:WORD_1\n"
    "v_lshlrev_b32_sdwa v206, 1, v181 dst_sel:DWORD dst_unused:UNUSED_PAD src0_sel:DWORD src1_sel:WORD_1\n"
    "v_lshlrev_b32_sdwa v191, 1, v182 dst_sel:DWORD dst_unused:UNUSED_PAD src0_sel:DWORD src1_sel:WORD_1\n"
    "v_exp_f16_sdwa v234, v232 dst_sel:WORD_1 dst_unused:UNUSED_PRESERVE src0_sel:WORD_1\n"
    "s_waitcnt lgkmcnt(7)\n"
    "v_mfma_f32_16x16x32_bf16 v[172:175], a[240:243], v[152:155], v[172:175]\n"
    "ds_read_u16_d16_hi v205, v205\n"
    "v_or_b32_e32 v199, v199, v207\n"
    "v_mfma_f32_16x16x32_bf16 v[176:179], a[168:171], v[156:159], 0\n"
    "s_waitcnt lgkmcnt(6)\n"
    "s_nop 0\n"
    "v_lshlrev_b32_sdwa v207, 1, v183 dst_sel:DWORD dst_unused:UNUSED_PAD src0_sel:DWORD src1_sel:WORD_1\n"
    "v_exp_f16_sdwa v235, v233 dst_sel:WORD_1 dst_unused:UNUSED_PRESERVE src0_sel:WORD_1\n"
    "v_mfma_f32_16x16x32_bf16 v[176:179], a[216:219], v[160:163], v[176:179]\n"
    "ds_read_u16 v190, v190\n"
    "ds_read_b128 v[144:147], v243 offset:0\n"
    "buffer_store_dwordx4 a[252:255], v255, s[44:47], s100 offen offset:128 sc1\n"
    "v_mfma_f32_16x16x32_bf16 v[176:179], a[124:127], v[164:167], v[176:179]\n"
    "v_pk_fma_f16 v234, v234, s49, s49\n"
    "v_pk_fma_f16 v235, v235, s49, s49\n"
    "v_mfma_f32_16x16x32_bf16 v[180:183], a[172:175], v[156:159], 0\n"
    "ds_read_u16_d16_hi v206, v206\n"
    "ds_read_b128 v[148:151], v243 offset:1024\n"
    "ds_read_u16 v191, v191\n"
    "v_mfma_f32_16x16x32_bf16 v[180:183], a[220:223], v[160:163], v[180:183]\n"
    "s_waitcnt lgkmcnt(7)\n"
    "ds_read_b128 v[152:155], v243 offset:2048\n"
    "v_rcp_f16_e32 v232, v234\n"
    "v_mfma_f32_16x16x32_bf16 v[180:183], a[240:243], v[164:167], v[180:183]\n"
    "ds_read_u16_d16_hi v207, v207\n"
    "v_rcp_f16_e64 v233, v235\n"
    "v_rcp_f16_sdwa v232, v234 dst_sel:WORD_1 dst_unused:UNUSED_PRESERVE src0_sel:WORD_1\n"
    "v_or_b32_e64 v185, v185, v201\n"
    "v_rcp_f16_sdwa v233, v235 dst_sel:WORD_1 dst_unused:UNUSED_PRESERVE src0_sel:WORD_1\n"
    "v_pk_mul_f16 v192, v192, v232\n"
    "v_or_b32_e64 v186, v186, v202\n"
    "v_pk_mul_f16 v196, v196, v233\n"
    "v_mfma_f32_16x16x32_f16 v[96:99], a[48:51], v[192:195], v[96:99]\n"
    "s_waitcnt lgkmcnt(7)\n"
    "s_nop 0\n"
    "ds_read_b128 v[156:159], v243 offset:3072\n"
    "ds_read_b128 v[160:163], v243 offset:4096\n"
    "ds_read_b128 v[164:167], v243 offset:5120\n"
    "v_mfma_f32_16x16x32_f16 v[100:103], a[52:55], v[192:195], v[100:103]\n"
    "ds_read_b128 a[240:243], v244 offset:32768\n"
    "v_pk_fma_f16 v232, v184, v184, s48\n"
    "v_mfma_f32_16x16x32_f16 v[112:115], a[56:59], v[192:195], v[112:115]\n"
    "v_or_b32_e64 v187, v187, v203\n"
    "v_pk_fma_f16 v233, v188, v188, s48\n"
    "v_mfma_f32_16x16x32_f16 v[116:119], a[60:63], v[192:195], v[116:119]\n"
    "v_pk_mul_f16 v232, v184, v232\n"
    "v_or_b32_e64 v189, v189, v205\n"
    "v_mfma_f32_16x16x32_f16 v[128:131], v[236:239], v[192:195], v[128:131]\n"
    "s_waitcnt lgkmcnt(7)\n"
    "s_nop 0\n"
    "v_pk_mul_f16 v233, v188, v233\n"
    "v_lshlrev_b32_sdwa v201, 1, v171 dst_sel:DWORD dst_unused:UNUSED_PAD src0_sel:DWORD src1_sel:WORD_1\n"
    "v_mfma_f32_16x16x32_f16 v[132:135], a[244:247], v[192:195], v[132:135]\n"
    "ds_read_u16_d16_hi v201, v201\n"
    "v_cvt_pk_f16_f32 v192, v168, v169\n"
    "v_mfma_f32_16x16x32_f16 v[104:107], a[48:51], v[196:199], v[104:107]\n"
    "v_lshlrev_b32_sdwa v193, 1, v170 dst_sel:DWORD dst_unused:UNUSED_PAD src0_sel:DWORD src1_sel:WORD_1\n"
    "v_lshlrev_b32_sdwa v194, 1, v172 dst_sel:DWORD dst_unused:UNUSED_PAD src0_sel:DWORD src1_sel:WORD_1\n"
    "v_mfma_f32_16x16x32_f16 v[108:111], a[52:55], v[196:199], v[108:111]\n"
    "ds_read_u16 v193, v193\n"
    "ds_read_u16 v194, v194\n"
    "v_mfma_f32_16x16x32_f16 v[120:123], a[56:59], v[196:199], v[120:123]\n"
    "v_lshlrev_b32_sdwa v202, 1, v173 dst_sel:DWORD dst_unused:UNUSED_PAD src0_sel:DWORD src1_sel:WORD_1\n"
    "v_lshlrev_b32_sdwa v195, 1, v174 dst_sel:DWORD dst_unused:UNUSED_PAD src0_sel:DWORD src1_sel:WORD_1\n"
    "v_mfma_f32_16x16x32_f16 v[124:127], a[60:63], v[196:199], v[124:127]\n"
    "ds_read_u16_d16_hi v202, v202\n"
    "ds_read_u16 v195, v195\n"
    "v_mfma_f32_16x16x32_f16 v[136:139], v[236:239], v[196:199], v[136:139]\n"
    "s_waitcnt lgkmcnt(8)\n"
    "ds_read_b128 v[236:239], v244 offset:16384\n"
    "v_lshlrev_b32_sdwa v203, 1, v175 dst_sel:DWORD dst_unused:UNUSED_PAD src0_sel:DWORD src1_sel:WORD_1\n"
    "v_mfma_f32_16x16x32_f16 v[140:143], a[244:247], v[196:199], v[140:143]\n"
    "v_exp_f16_e32 v234, v232\n"
    "v_mfma_f32_16x16x32_bf16 v[168:171], a[128:131], v[144:147], 0\n"
    "ds_read_u16_d16_hi v203, v203\n"
    "v_or_b32_e64 v190, v190, v206\n"
    "v_mfma_f32_16x16x32_bf16 v[168:171], a[176:179], v[148:151], v[168:171]\n"
    "ds_read_b128 a[244:247], v244 offset:18432\n"
    "v_cvt_pk_f16_f32 v196, v176, v177\n"
    "v_mfma_f32_16x16x32_bf16 v[168:171], a[96:99], v[152:155], v[168:171]\n"
    "v_lshlrev_b32_sdwa v197, 1, v178 dst_sel:DWORD dst_unused:UNUSED_PAD src0_sel:DWORD src1_sel:WORD_1\n"
    "v_exp_f16_e64 v235, v233\n"
    "v_mfma_f32_16x16x32_bf16 v[172:175], a[132:135], v[144:147], 0\n"
    "ds_read_u16 v197, v197\n"
    "v_lshlrev_b32_sdwa v205, 1, v179 dst_sel:DWORD dst_unused:UNUSED_PAD src0_sel:DWORD src1_sel:WORD_1\n"
    "v_mfma_f32_16x16x32_bf16 v[172:175], a[180:183], v[148:151], v[172:175]\n"
    "v_lshlrev_b32_sdwa v198, 1, v180 dst_sel:DWORD dst_unused:UNUSED_PAD src0_sel:DWORD src1_sel:WORD_1\n"
    "v_lshlrev_b32_sdwa v206, 1, v181 dst_sel:DWORD dst_unused:UNUSED_PAD src0_sel:DWORD src1_sel:WORD_1\n"
    "v_lshlrev_b32_sdwa v199, 1, v182 dst_sel:DWORD dst_unused:UNUSED_PAD src0_sel:DWORD src1_sel:WORD_1\n"
    "v_exp_f16_sdwa v234, v232 dst_sel:WORD_1 dst_unused:UNUSED_PRESERVE src0_sel:WORD_1\n"
    "s_waitcnt lgkmcnt(7)\n"
    "v_mfma_f32_16x16x32_bf16 v[172:175], a[240:243], v[152:155], v[172:175]\n"
    "ds_read_u16_d16_hi v205, v205\n"
    "v_or_b32_e32 v191, v191, v207\n"
    "v_mfma_f32_16x16x32_bf16 v[176:179], a[128:131], v[156:159], 0\n"
    "v_lshlrev_b32_sdwa v207, 1, v183 dst_sel:DWORD dst_unused:UNUSED_PAD src0_sel:DWORD src1_sel:WORD_1\n"
    "v_exp_f16_sdwa v235, v233 dst_sel:WORD_1 dst_unused:UNUSED_PRESERVE src0_sel:WORD_1\n"
    "v_mfma_f32_16x16x32_bf16 v[176:179], a[176:179], v[160:163], v[176:179]\n"
    "s_waitcnt lgkmcnt(6)\n"
    "s_nop 0\n"
    "ds_read_u16 v198, v198\n"
    "v_pk_fma_f16 v234, v234, s49, s49\n"
    "v_mfma_f32_16x16x32_bf16 v[176:179], a[96:99], v[164:167], v[176:179]\n"
    "ds_read_u16_d16_hi v206, v206\n"
    "v_pk_fma_f16 v235, v235, s49, s49\n"
    "v_mfma_f32_16x16x32_bf16 v[180:183], a[132:135], v[156:159], 0\n"
    "ds_read_u16 v199, v199\n"
    "v_rcp_f16_e64 v232, v234\n"
    "v_mfma_f32_16x16x32_bf16 v[180:183], a[180:183], v[160:163], v[180:183]\n"
    "ds_read_u16_d16_hi v207, v207\n"
    "v_rcp_f16_e64 v233, v235\n"
    "v_mfma_f32_16x16x32_bf16 v[180:183], a[240:243], v[164:167], v[180:183]\n"
    "s_waitcnt lgkmcnt(6)\n"
    "v_rcp_f16_sdwa v232, v234 dst_sel:WORD_1 dst_unused:UNUSED_PRESERVE src0_sel:WORD_1\n"
    "v_or_b32_e32 v193, v193, v201\n"
    "v_rcp_f16_sdwa v233, v235 dst_sel:WORD_1 dst_unused:UNUSED_PRESERVE src0_sel:WORD_1\n"
    "v_pk_mul_f16 v184, v184, v232\n"
    "v_or_b32_e64 v194, v194, v202\n"
    "v_pk_mul_f16 v188, v188, v233\n"
    "v_mfma_f32_16x16x32_f16 v[96:99], a[64:67], v[184:187], v[96:99]\n"
    "ds_read_b128 a[240:243], v244 offset:36864\n"
    "v_pk_fma_f16 v232, v192, v192, s48\n"
    "v_mfma_f32_16x16x32_f16 v[100:103], a[68:71], v[184:187], v[100:103]\n"
    "v_or_b32_e64 v195, v195, v203\n"
    "v_pk_fma_f16 v233, v196, v196, s48\n"
    "v_mfma_f32_16x16x32_f16 v[112:115], a[72:75], v[184:187], v[112:115]\n"
    "s_waitcnt lgkmcnt(5)\n"
    "v_pk_mul_f16 v232, v192, v232\n"
    "v_or_b32_e32 v197, v197, v205\n"
    "v_mfma_f32_16x16x32_f16 v[116:119], a[76:79], v[184:187], v[116:119]\n"
    "v_pk_mul_f16 v233, v196, v233\n"
    "v_lshlrev_b32_sdwa v201, 1, v171 dst_sel:DWORD dst_unused:UNUSED_PAD src0_sel:DWORD src1_sel:WORD_1\n"
    "v_mfma_f32_16x16x32_f16 v[128:131], v[236:239], v[184:187], v[128:131]\n"
    "ds_read_u16_d16_hi v201, v201\n"
    "v_lshlrev_b32_sdwa v202, 1, v173 dst_sel:DWORD dst_unused:UNUSED_PAD src0_sel:DWORD src1_sel:WORD_1\n"
    "v_mfma_f32_16x16x32_f16 v[132:135], a[244:247], v[184:187], v[132:135]\n"
    "ds_read_u16_d16_hi v202, v202\n"
    "v_cvt_pk_f16_f32 v184, v168, v169\n"
    "v_mfma_f32_16x16x32_f16 v[104:107], a[64:67], v[188:191], v[104:107]\n"
    "s_waitcnt lgkmcnt(5)\n"
    "s_nop 0\n"
    "v_lshlrev_b32_sdwa v185, 1, v170 dst_sel:DWORD dst_unused:UNUSED_PAD src0_sel:DWORD src1_sel:WORD_1\n"
    "v_lshlrev_b32_sdwa v186, 1, v172 dst_sel:DWORD dst_unused:UNUSED_PAD src0_sel:DWORD src1_sel:WORD_1\n"
    "v_mfma_f32_16x16x32_f16 v[108:111], a[68:71], v[188:191], v[108:111]\n"
    "ds_read_u16 v185, v185\n"
    "ds_read_u16 v186, v186\n"
    "v_mfma_f32_16x16x32_f16 v[120:123], a[72:75], v[188:191], v[120:123]\n"
    "v_lshlrev_b32_sdwa v187, 1, v174 dst_sel:DWORD dst_unused:UNUSED_PAD src0_sel:DWORD src1_sel:WORD_1\n"
    "v_lshlrev_b32_sdwa v203, 1, v175 dst_sel:DWORD dst_unused:UNUSED_PAD src0_sel:DWORD src1_sel:WORD_1\n"
    "v_mfma_f32_16x16x32_f16 v[124:127], a[76:79], v[188:191], v[124:127]\n"
    "s_waitcnt lgkmcnt(4)\n"
    "ds_read_u16 v187, v187\n"
    "v_exp_f16_e32 v234, v232\n"
    "v_mfma_f32_16x16x32_f16 v[136:139], v[236:239], v[188:191], v[136:139]\n"
    "ds_read_b128 v[236:239], v244 offset:20480\n"
    "ds_read_u16_d16_hi v203, v203\n"
    "v_mfma_f32_16x16x32_f16 v[140:143], a[244:247], v[188:191], v[140:143]\n"
    "v_or_b32_e64 v198, v198, v206\n"
    "v_cvt_pk_f16_f32 v188, v176, v177\n"
    "v_mfma_f32_16x16x32_bf16 v[168:171], a[136:139], v[144:147], 0\n"
    "ds_read_b128 a[244:247], v244 offset:22528\n"
    "v_lshlrev_b32_sdwa v189, 1, v178 dst_sel:DWORD dst_unused:UNUSED_PAD src0_sel:DWORD src1_sel:WORD_1\n"
    "v_mfma_f32_16x16x32_bf16 v[168:171], a[184:187], v[148:151], v[168:171]\n"
    "v_exp_f16_e64 v235, v233\n"
    "v_mfma_f32_16x16x32_bf16 v[168:171], a[100:103], v[152:155], v[168:171]\n"
    "ds_read_u16 v189, v189\n"
    "v_lshlrev_b32_sdwa v205, 1, v179 dst_sel:DWORD dst_unused:UNUSED_PAD src0_sel:DWORD src1_sel:WORD_1\n"
    "v_mfma_f32_16x16x32_bf16 v[172:175], a[140:143], v[144:147], 0\n"
    "v_lshlrev_b32_sdwa v190, 1, v180 dst_sel:DWORD dst_unused:UNUSED_PAD src0_sel:DWORD src1_sel:WORD_1\n"
    "v_lshlrev_b32_sdwa v206, 1, v181 dst_sel:DWORD dst_unused:UNUSED_PAD src0_sel:DWORD src1_sel:WORD_1\n"
    "v_mfma_f32_16x16x32_bf16 v[172:175], a[188:191], v[148:151], v[172:175]\n"
    "v_lshlrev_b32_sdwa v191, 1, v182 dst_sel:DWORD dst_unused:UNUSED_PAD src0_sel:DWORD src1_sel:WORD_1\n"
    "v_exp_f16_sdwa v234, v232 dst_sel:WORD_1 dst_unused:UNUSED_PRESERVE src0_sel:WORD_1\n"
    "v_mfma_f32_16x16x32_bf16 v[172:175], a[240:243], v[152:155], v[172:175]\n"
    "s_waitcnt lgkmcnt(6)\n"
    "ds_read_u16_d16_hi v205, v205\n"
    "v_or_b32_e32 v199, v199, v207\n"
    "v_mfma_f32_16x16x32_bf16 v[176:179], a[136:139], v[156:159], 0\n"
    "v_lshlrev_b32_sdwa v207, 1, v183 dst_sel:DWORD dst_unused:UNUSED_PAD src0_sel:DWORD src1_sel:WORD_1\n"
    "v_exp_f16_sdwa v235, v233 dst_sel:WORD_1 dst_unused:UNUSED_PRESERVE src0_sel:WORD_1\n"
    "v_mfma_f32_16x16x32_bf16 v[176:179], a[184:187], v[160:163], v[176:179]\n"
    "s_waitcnt lgkmcnt(6)\n"
    "s_nop 0\n"
    "ds_read_u16 v190, v190\n"
    "v_pk_fma_f16 v234, v234, s49, s49\n"
    "v_mfma_f32_16x16x32_bf16 v[176:179], a[100:103], v[164:167], v[176:179]\n"
    "ds_read_u16_d16_hi v206, v206\n"
    "v_pk_fma_f16 v235, v235, s49, s49\n"
    "v_mfma_f32_16x16x32_bf16 v[180:183], a[140:143], v[156:159], 0\n"
    "ds_read_u16 v191, v191\n"
    "v_rcp_f16_e64 v232, v234\n"
    "v_mfma_f32_16x16x32_bf16 v[180:183], a[188:191], v[160:163], v[180:183]\n"
    "ds_read_u16_d16_hi v207, v207\n"
    "v_rcp_f16_e64 v233, v235\n"
    "v_mfma_f32_16x16x32_bf16 v[180:183], a[240:243], v[164:167], v[180:183]\n"
    "s_waitcnt lgkmcnt(6)\n"
    "v_rcp_f16_sdwa v232, v234 dst_sel:WORD_1 dst_unused:UNUSED_PRESERVE src0_sel:WORD_1\n"
    "v_or_b32_e32 v185, v185, v201\n"
    "v_rcp_f16_sdwa v233, v235 dst_sel:WORD_1 dst_unused:UNUSED_PRESERVE src0_sel:WORD_1\n"
    "v_pk_mul_f16 v192, v192, v232\n"
    "v_or_b32_e64 v186, v186, v202\n"
    "v_pk_mul_f16 v196, v196, v233\n"
    "v_mfma_f32_16x16x32_f16 v[96:99], a[80:83], v[192:195], v[96:99]\n"
    "v_pk_fma_f16 v232, v184, v184, s48\n"
    "v_or_b32_e64 v187, v187, v203\n"
    "v_mfma_f32_16x16x32_f16 v[100:103], a[84:87], v[192:195], v[100:103]\n"
    "v_pk_fma_f16 v233, v188, v188, s48\n"
    "v_pk_mul_f16 v232, v184, v232\n"
    "v_mfma_f32_16x16x32_f16 v[112:115], a[88:91], v[192:195], v[112:115]\n"
    "s_waitcnt lgkmcnt(4)\n"
    "v_or_b32_e32 v189, v189, v205\n"
    "v_pk_mul_f16 v233, v188, v233\n"
    "v_mfma_f32_16x16x32_f16 v[116:119], a[92:95], v[192:195], v[116:119]\n"
    "v_lshlrev_b32_sdwa v201, 1, v171 dst_sel:DWORD dst_unused:UNUSED_PAD src0_sel:DWORD src1_sel:WORD_1\n"
    "v_lshlrev_b32_sdwa v202, 1, v173 dst_sel:DWORD dst_unused:UNUSED_PAD src0_sel:DWORD src1_sel:WORD_1\n"
    "v_mfma_f32_16x16x32_f16 v[128:131], v[236:239], v[192:195], v[128:131]\n"
    "ds_read_u16_d16_hi v201, v201\n"
    "ds_read_u16_d16_hi v202, v202\n"
    "v_mfma_f32_16x16x32_f16 v[132:135], a[244:247], v[192:195], v[132:135]\n"
    "s_waitcnt lgkmcnt(4)\n"
    "s_nop 0\n"
    "v_cvt_pk_f16_f32 v192, v168, v169\n"
    "v_lshlrev_b32_sdwa v193, 1, v170 dst_sel:DWORD dst_unused:UNUSED_PAD src0_sel:DWORD src1_sel:WORD_1\n"
    "v_mfma_f32_16x16x32_f16 v[104:107], a[80:83], v[196:199], v[104:107]\n"
    "ds_read_u16 v193, v193\n"
    "v_lshlrev_b32_sdwa v194, 1, v172 dst_sel:DWORD dst_unused:UNUSED_PAD src0_sel:DWORD src1_sel:WORD_1\n"
    "v_mfma_f32_16x16x32_f16 v[108:111], a[84:87], v[196:199], v[108:111]\n"
    "ds_read_u16 v194, v194\n"
    "v_lshlrev_b32_sdwa v195, 1, v174 dst_sel:DWORD dst_unused:UNUSED_PAD src0_sel:DWORD src1_sel:WORD_1\n"
    "v_mfma_f32_16x16x32_f16 v[120:123], a[88:91], v[196:199], v[120:123]\n"
    "ds_read_u16 v195, v195\n"
    "v_lshlrev_b32_sdwa v203, 1, v175 dst_sel:DWORD dst_unused:UNUSED_PAD src0_sel:DWORD src1_sel:WORD_1\n"
    "v_mfma_f32_16x16x32_f16 v[124:127], a[92:95], v[196:199], v[124:127]\n"
    "s_waitcnt lgkmcnt(5)\n"
    "v_exp_f16_e32 v234, v232\n"
    "v_mfma_f32_16x16x32_f16 v[136:139], v[236:239], v[196:199], v[136:139]\n"
    "ds_read_b128 v[236:239], v244 offset:0\n"
    "ds_read_u16_d16_hi v203, v203\n"
    "v_mfma_f32_16x16x32_f16 v[140:143], a[244:247], v[196:199], v[140:143]\n"
    "v_or_b32_e64 v190, v190, v206\n"
    "v_cvt_pk_f16_f32 v196, v176, v177\n"
    "v_mfma_f32_16x16x32_bf16 v[168:171], a[144:147], v[144:147], 0\n"
    "ds_read_b128 a[244:247], v244 offset:2048\n"
    "v_lshlrev_b32_sdwa v197, 1, v178 dst_sel:DWORD dst_unused:UNUSED_PAD src0_sel:DWORD src1_sel:WORD_1\n"
    "v_mfma_f32_16x16x32_bf16 v[168:171], a[192:195], v[148:151], v[168:171]\n"
    "v_exp_f16_e64 v235, v233\n"
    "v_mfma_f32_16x16x32_bf16 v[168:171], a[104:107], v[152:155], v[168:171]\n"
    "ds_read_u16 v197, v197\n"
    "v_lshlrev_b32_sdwa v205, 1, v179 dst_sel:DWORD dst_unused:UNUSED_PAD src0_sel:DWORD src1_sel:WORD_1\n"
    "v_mfma_f32_16x16x32_bf16 v[172:175], a[148:151], v[144:147], 0\n"
    "v_lshlrev_b32_sdwa v198, 1, v180 dst_sel:DWORD dst_unused:UNUSED_PAD src0_sel:DWORD src1_sel:WORD_1\n"
    "v_lshlrev_b32_sdwa v206, 1, v181 dst_sel:DWORD dst_unused:UNUSED_PAD src0_sel:DWORD src1_sel:WORD_1\n"
    "v_mfma_f32_16x16x32_bf16 v[172:175], a[196:199], v[148:151], v[172:175]\n"
    "v_lshlrev_b32_sdwa v199, 1, v182 dst_sel:DWORD dst_unused:UNUSED_PAD src0_sel:DWORD src1_sel:WORD_1\n"
    "v_exp_f16_sdwa v234, v232 dst_sel:WORD_1 dst_unused:UNUSED_PRESERVE src0_sel:WORD_1\n"
    "v_mfma_f32_16x16x32_bf16 v[172:175], a[108:111], v[152:155], v[172:175]\n"
    "ds_read_u16_d16_hi v205, v205\n"
    "v_or_b32_e64 v191, v191, v207\n"
    "v_mfma_f32_16x16x32_bf16 v[176:179], a[144:147], v[156:159], 0\n"
    "s_waitcnt lgkmcnt(7)\n"
    "v_lshlrev_b32_sdwa v207, 1, v183 dst_sel:DWORD dst_unused:UNUSED_PAD src0_sel:DWORD src1_sel:WORD_1\n"
    "v_exp_f16_sdwa v235, v233 dst_sel:WORD_1 dst_unused:UNUSED_PRESERVE src0_sel:WORD_1\n"
    "v_mfma_f32_16x16x32_bf16 v[176:179], a[192:195], v[160:163], v[176:179]\n"
    "s_waitcnt lgkmcnt(6)\n"
    "ds_read_u16 v198, v198\n"
    "v_pk_fma_f16 v234, v234, s49, s49\n"
    "v_mfma_f32_16x16x32_bf16 v[176:179], a[104:107], v[164:167], v[176:179]\n"
    "ds_read_u16_d16_hi v206, v206\n"
    "v_pk_fma_f16 v235, v235, s49, s49\n"
    "v_mfma_f32_16x16x32_bf16 v[180:183], a[148:151], v[156:159], 0\n"
    "ds_read_u16 v199, v199\n"
    "v_cvt_pk_bf16_f32 v96, v96, v97\n"
    "v_mfma_f32_16x16x32_bf16 v[180:183], a[196:199], v[160:163], v[180:183]\n"
    "v_cvt_pk_bf16_f32 v97, v98, v99\n"
    "v_rcp_f16_e64 v232, v234\n"
    "v_mfma_f32_16x16x32_bf16 v[180:183], a[108:111], v[164:167], v[180:183]\n"
    "s_waitcnt lgkmcnt(5)\n"
    "s_nop 0\n"
    "ds_read_u16_d16_hi v207, v207\n"
    "v_cvt_pk_bf16_f32 v98, v100, v101\n"
    "v_cvt_pk_bf16_f32 v99, v102, v103\n"
    "ds_write_b128 v242, v[96:99] offset:24640\n"
    "v_cvt_pk_bf16_f32 v112, v112, v113\n"
    "v_rcp_f16_e64 v233, v235\n"
    "v_cvt_pk_bf16_f32 v113, v114, v115\n"
    "v_cvt_pk_bf16_f32 v114, v116, v117\n"
    "v_cvt_pk_bf16_f32 v115, v118, v119\n"
    "ds_write_b128 v242, v[112:115] offset:26688\n"
    "v_rcp_f16_sdwa v232, v234 dst_sel:WORD_1 dst_unused:UNUSED_PRESERVE src0_sel:WORD_1\n"
    "v_cvt_pk_bf16_f32 v128, v128, v129\n"
    "v_cvt_pk_bf16_f32 v129, v130, v131\n"
    "v_cvt_pk_bf16_f32 v130, v132, v133\n"
    "v_or_b32_e64 v193, v193, v201\n"
    "v_cvt_pk_bf16_f32 v131, v134, v135\n"
    "ds_write_b128 v242, v[128:131] offset:28736\n"
    "v_rcp_f16_sdwa v233, v235 dst_sel:WORD_1 dst_unused:UNUSED_PRESERVE src0_sel:WORD_1\n"
    "v_cvt_pk_bf16_f32 v104, v104, v105\n"
    "v_cvt_pk_bf16_f32 v105, v106, v107\n"
    "v_cvt_pk_bf16_f32 v106, v108, v109\n"
    "v_pk_mul_f16 v184, v184, v232\n"
    "v_cvt_pk_bf16_f32 v107, v110, v111\n"
    "ds_write_b128 v242, v[104:107] offset:25664\n"
    "v_mfma_f32_16x16x32_f16 v[96:99], a[0:3], v[184:187], 0\n"
    "s_waitcnt lgkmcnt(3)\n"
    "v_or_b32_e32 v194, v194, v202\n"
    "v_cvt_pk_bf16_f32 v120, v120, v121\n"
    "v_mfma_f32_16x16x32_f16 v[100:103], a[4:7], v[184:187], 0\n"
    "v_cvt_pk_bf16_f32 v121, v122, v123\n"
    "v_pk_mul_f16 v188, v188, v233\n"
    "v_mfma_f32_16x16x32_f16 v[112:115], a[8:11], v[184:187], 0\n"
    "v_cvt_pk_bf16_f32 v122, v124, v125\n"
    "v_cvt_pk_bf16_f32 v123, v126, v127\n"
    "v_mfma_f32_16x16x32_f16 v[116:119], a[12:15], v[184:187], 0\n"
    "ds_write_b128 v242, v[120:123] offset:27712\n"
    "v_mfma_f32_16x16x32_f16 v[128:131], v[236:239], v[184:187], 0\n"
    "v_cvt_pk_bf16_f32 v136, v136, v137\n"
    "v_pk_fma_f16 v232, v192, v192, s48\n"
    "v_mfma_f32_16x16x32_f16 v[132:135], a[244:247], v[184:187], 0\n"
    "v_cvt_pk_bf16_f32 v137, v138, v139\n"
    "v_cvt_pk_bf16_f32 v138, v140, v141\n"
    "v_mfma_f32_16x16x32_f16 v[104:107], a[0:3], v[188:191], 0\n"
    "v_or_b32_e64 v195, v195, v203\n"
    "v_cvt_pk_bf16_f32 v139, v142, v143\n"
    "v_mfma_f32_16x16x32_f16 v[108:111], a[4:7], v[188:191], 0\n"
    "ds_write_b128 v242, v[136:139] offset:29760\n"
    "v_mfma_f32_16x16x32_f16 v[120:123], a[8:11], v[188:191], 0\n"
    "v_pk_fma_f16 v233, v196, v196, s48\n"
    "v_pk_mul_f16 v232, v192, v232\n"
    "v_mfma_f32_16x16x32_f16 v[124:127], a[12:15], v[188:191], 0\n"
    "v_or_b32_e64 v197, v197, v205\n"
    "v_pk_mul_f16 v233, v196, v233\n"
    "v_mfma_f32_16x16x32_f16 v[136:139], v[236:239], v[188:191], 0\n"
    "ds_read_b128 v[236:239], v244 offset:4096\n"
    "v_cvt_pk_f16_f32 v184, v168, v169\n"
    "v_mfma_f32_16x16x32_f16 v[140:143], a[244:247], v[188:191], 0\n"
    "v_lshlrev_b32_sdwa v185, 1, v170 dst_sel:DWORD dst_unused:UNUSED_PAD src0_sel:DWORD src1_sel:WORD_1\n"
    "v_lshlrev_b32_sdwa v201, 1, v171 dst_sel:DWORD dst_unused:UNUSED_PAD src0_sel:DWORD src1_sel:WORD_1\n"
    "s_waitcnt vmcnt(6)\n"
    "s_nop 0\n"
    "v_smfmac_f32_32x32x32_bf16 v[96:111], a[248:251], a[224:231], v241\n"
    "ds_read_u16 v185, v185\n"
    "ds_read_u16_d16_hi v201, v201\n"
    "buffer_load_dwordx4 a[224:227], v245, s[40:43], s58 offen offset:128\n"
    "v_lshlrev_b32_sdwa v186, 1, v172 dst_sel:DWORD dst_unused:UNUSED_PAD src0_sel:DWORD src1_sel:WORD_1\n"
    "v_lshlrev_b32_sdwa v202, 1, v173 dst_sel:DWORD dst_unused:UNUSED_PAD src0_sel:DWORD src1_sel:WORD_1\n"
    "v_smfmac_f32_32x32x32_bf16 v[112:127], a[248:251], a[232:239], v241\n"
    "s_waitcnt lgkmcnt(3)\n"
    "s_nop 0\n"
    "ds_read_u16 v186, v186\n"
    "ds_read_u16_d16_hi v202, v202\n"
    "v_lshlrev_b32_sdwa v187, 1, v174 dst_sel:DWORD dst_unused:UNUSED_PAD src0_sel:DWORD src1_sel:WORD_1\n"
    "v_lshlrev_b32_sdwa v203, 1, v175 dst_sel:DWORD dst_unused:UNUSED_PAD src0_sel:DWORD src1_sel:WORD_1\n"
    "v_exp_f16_e64 v234, v232\n"
    "v_mfma_f32_16x16x32_bf16 v[168:171], a[152:155], v[144:147], 0\n"
    "s_barrier\n"
    "v_mfma_f32_16x16x32_bf16 v[168:171], a[200:203], v[148:151], v[168:171]\n"
    "ds_read_u16 v187, v187\n"
    "ds_read_u16_d16_hi v203, v203\n"
    "ds_read_b128 a[244:247], v244 offset:6144\n"
    "v_mfma_f32_16x16x32_bf16 v[168:171], a[112:115], v[152:155], v[168:171]\n"
    "v_or_b32_e32 v198, v198, v206\n"
    "v_cvt_pk_f16_f32 v188, v176, v177\n"
    "v_mfma_f32_16x16x32_bf16 v[172:175], a[156:159], v[144:147], 0\n"
    "ds_read_b128 a[232:235], v243 offset:24640\n"
    "v_lshlrev_b32_sdwa v189, 1, v178 dst_sel:DWORD dst_unused:UNUSED_PAD src0_sel:DWORD src1_sel:WORD_1\n"
    "v_exp_f16_e64 v235, v233\n"
    "v_mfma_f32_16x16x32_bf16 v[172:175], a[204:207], v[148:151], v[172:175]\n"
    "ds_read_u16 v189, v189\n"
    "buffer_load_dwordx4 a[228:231], v245, s[40:43], s101 offen offset:128\n"
    "ds_read_b128 a[236:239], v243 offset:25664\n"
    "v_lshlrev_b32_sdwa v205, 1, v179 dst_sel:DWORD dst_unused:UNUSED_PAD src0_sel:DWORD src1_sel:WORD_1\n"
    "v_lshlrev_b32_sdwa v190, 1, v180 dst_sel:DWORD dst_unused:UNUSED_PAD src0_sel:DWORD src1_sel:WORD_1\n"
    "v_lshlrev_b32_sdwa v206, 1, v181 dst_sel:DWORD dst_unused:UNUSED_PAD src0_sel:DWORD src1_sel:WORD_1\n"
    "v_lshlrev_b32_sdwa v191, 1, v182 dst_sel:DWORD dst_unused:UNUSED_PAD src0_sel:DWORD src1_sel:WORD_1\n"
    "v_exp_f16_sdwa v234, v232 dst_sel:WORD_1 dst_unused:UNUSED_PRESERVE src0_sel:WORD_1\n"
    "v_mfma_f32_16x16x32_bf16 v[172:175], a[116:119], v[152:155], v[172:175]\n"
    "s_waitcnt lgkmcnt(8)\n"
    "ds_read_u16_d16_hi v205, v205\n"
    "v_or_b32_e32 v199, v199, v207\n"
    "v_mfma_f32_16x16x32_bf16 v[176:179], a[152:155], v[156:159], 0\n"
    "v_lshlrev_b32_sdwa v207, 1, v183 dst_sel:DWORD dst_unused:UNUSED_PAD src0_sel:DWORD src1_sel:WORD_1\n"
    "v_exp_f16_sdwa v235, v233 dst_sel:WORD_1 dst_unused:UNUSED_PRESERVE src0_sel:WORD_1\n"
    "v_mfma_f32_16x16x32_bf16 v[176:179], a[200:203], v[160:163], v[176:179]\n"
    "ds_read_u16 v190, v190\n"
    "v_pk_fma_f16 v234, v234, s49, s49\n"
    "v_mfma_f32_16x16x32_bf16 v[176:179], a[112:115], v[164:167], v[176:179]\n"
    "s_waitcnt lgkmcnt(8)\n"
    "ds_read_u16_d16_hi v206, v206\n"
    "v_pk_fma_f16 v235, v235, s49, s49\n"
    "v_mfma_f32_16x16x32_bf16 v[180:183], a[156:159], v[156:159], 0\n"
    "ds_read_u16 v191, v191\n"
    "v_rcp_f16_e32 v232, v234\n"
    "v_mfma_f32_16x16x32_bf16 v[180:183], a[204:207], v[160:163], v[180:183]\n"
    "v_rcp_f16_e64 v233, v235\n"
    "v_mfma_f32_16x16x32_bf16 v[180:183], a[116:119], v[164:167], v[180:183]\n"
    "s_waitcnt lgkmcnt(7)\n"
    "ds_read_u16_d16_hi v207, v207\n"
    "v_rcp_f16_sdwa v232, v234 dst_sel:WORD_1 dst_unused:UNUSED_PRESERVE src0_sel:WORD_1\n"
    "v_or_b32_e32 v185, v185, v201\n"
    "v_rcp_f16_sdwa v233, v235 dst_sel:WORD_1 dst_unused:UNUSED_PRESERVE src0_sel:WORD_1\n"
    "v_pk_mul_f16 v192, v192, v232\n"
    "v_or_b32_e64 v186, v186, v202\n"
    "v_pk_mul_f16 v196, v196, v233\n"
    "v_mfma_f32_16x16x32_f16 v[96:99], a[16:19], v[192:195], v[96:99]\n"
    "ds_read_b128 a[240:243], v244 offset:34816\n"
    "v_pk_fma_f16 v232, v184, v184, s48\n"
    "v_mfma_f32_16x16x32_f16 v[100:103], a[20:23], v[192:195], v[100:103]\n"
    "v_or_b32_e64 v187, v187, v203\n"
    "v_pk_fma_f16 v233, v188, v188, s48\n"
    "v_mfma_f32_16x16x32_f16 v[112:115], a[24:27], v[192:195], v[112:115]\n"
    "v_pk_mul_f16 v232, v184, v232\n"
    "v_pk_mul_f16 v233, v188, v233\n"
    "v_mfma_f32_16x16x32_f16 v[116:119], a[28:31], v[192:195], v[116:119]\n"
    "v_lshlrev_b32_sdwa v201, 1, v171 dst_sel:DWORD dst_unused:UNUSED_PAD src0_sel:DWORD src1_sel:WORD_1\n"
    "v_lshlrev_b32_sdwa v202, 1, v173 dst_sel:DWORD dst_unused:UNUSED_PAD src0_sel:DWORD src1_sel:WORD_1\n"
    "v_mfma_f32_16x16x32_f16 v[128:131], v[236:239], v[192:195], v[128:131]\n"
    "s_waitcnt lgkmcnt(5)\n"
    "ds_read_u16_d16_hi v201, v201\n"
    "ds_read_u16_d16_hi v202, v202\n"
    "v_mfma_f32_16x16x32_f16 v[132:135], a[244:247], v[192:195], v[132:135]\n"
    "v_or_b32_e32 v189, v189, v205\n"
    "v_cvt_pk_f16_f32 v192, v168, v169\n"
    "v_mfma_f32_16x16x32_f16 v[104:107], a[16:19], v[196:199], v[104:107]\n"
    "v_lshlrev_b32_sdwa v193, 1, v170 dst_sel:DWORD dst_unused:UNUSED_PAD src0_sel:DWORD src1_sel:WORD_1\n"
    "v_lshlrev_b32_sdwa v194, 1, v172 dst_sel:DWORD dst_unused:UNUSED_PAD src0_sel:DWORD src1_sel:WORD_1\n"
    "v_mfma_f32_16x16x32_f16 v[108:111], a[20:23], v[196:199], v[108:111]\n"
    "s_waitcnt lgkmcnt(5)\n"
    "s_nop 0\n"
    "ds_read_u16 v193, v193\n"
    "ds_read_u16 v194, v194\n"
    "v_mfma_f32_16x16x32_f16 v[120:123], a[24:27], v[196:199], v[120:123]\n"
    "v_lshlrev_b32_sdwa v195, 1, v174 dst_sel:DWORD dst_unused:UNUSED_PAD src0_sel:DWORD src1_sel:WORD_1\n"
    "v_lshlrev_b32_sdwa v203, 1, v175 dst_sel:DWORD dst_unused:UNUSED_PAD src0_sel:DWORD src1_sel:WORD_1\n"
    "v_mfma_f32_16x16x32_f16 v[124:127], a[28:31], v[196:199], v[124:127]\n"
    "ds_read_u16 v195, v195\n"
    "v_exp_f16_e64 v234, v232\n"
    "v_mfma_f32_16x16x32_f16 v[136:139], v[236:239], v[196:199], v[136:139]\n"
    "ds_read_b128 v[236:239], v244 offset:8192\n"
    "ds_read_u16_d16_hi v203, v203\n"
    "v_smfmac_f32_32x32x32_bf16 v[96:111], a[248:251], a[232:239], v241\n"
    "s_waitcnt lgkmcnt(7)\n"
    "ds_read_b128 a[232:235], v243 offset:26688\n"
    "v_or_b32_e32 v190, v190, v206\n"
    "v_exp_f16_e64 v235, v233\n"
    "v_lshlrev_b32_sdwa v205, 1, v179 dst_sel:DWORD dst_unused:UNUSED_PAD src0_sel:DWORD src1_sel:WORD_1\n"
    "v_mfma_f32_16x16x32_f16 v[140:143], a[244:247], v[196:199], v[140:143]\n"
    "ds_read_b128 a[244:247], v244 offset:10240\n"
    "ds_read_b128 a[236:239], v243 offset:27712\n"
    "v_mfma_f32_16x16x32_bf16 v[168:171], a[160:163], v[144:147], 0\n"
    "v_cvt_pk_f16_f32 v196, v176, v177\n"
    "v_lshlrev_b32_sdwa v197, 1, v178 dst_sel:DWORD dst_unused:UNUSED_PAD src0_sel:DWORD src1_sel:WORD_1\n"
    "v_mfma_f32_16x16x32_bf16 v[168:171], a[208:211], v[148:151], v[168:171]\n"
    "ds_read_u16 v197, v197\n"
    "v_lshlrev_b32_sdwa v198, 1, v180 dst_sel:DWORD dst_unused:UNUSED_PAD src0_sel:DWORD src1_sel:WORD_1\n"
    "v_mfma_f32_16x16x32_bf16 v[168:171], a[120:123], v[152:155], v[168:171]\n"
    "v_lshlrev_b32_sdwa v206, 1, v181 dst_sel:DWORD dst_unused:UNUSED_PAD src0_sel:DWORD src1_sel:WORD_1\n"
    "v_lshlrev_b32_sdwa v199, 1, v182 dst_sel:DWORD dst_unused:UNUSED_PAD src0_sel:DWORD src1_sel:WORD_1\n"
    "v_mfma_f32_16x16x32_bf16 v[172:175], a[164:167], v[144:147], 0\n"
    "v_exp_f16_sdwa v234, v232 dst_sel:WORD_1 dst_unused:UNUSED_PRESERVE src0_sel:WORD_1\n"
    "v_mfma_f32_16x16x32_bf16 v[172:175], a[212:215], v[148:151], v[172:175]\n"
    "s_waitcnt lgkmcnt(8)\n"
    "ds_read_u16_d16_hi v205, v205\n"
    "v_or_b32_e32 v191, v191, v207\n"
    "v_mfma_f32_16x16x32_bf16 v[172:175], a[240:243], v[152:155], v[172:175]\n"
    "s_waitcnt lgkmcnt(8)\n"
    "s_nop 0\n"
    "v_lshlrev_b32_sdwa v207, 1, v183 dst_sel:DWORD dst_unused:UNUSED_PAD src0_sel:DWORD src1_sel:WORD_1\n"
    "v_exp_f16_sdwa v235, v233 dst_sel:WORD_1 dst_unused:UNUSED_PRESERVE src0_sel:WORD_1\n"
    "v_mfma_f32_16x16x32_bf16 v[176:179], a[160:163], v[156:159], 0\n"
    "ds_read_u16 v198, v198\n"
    "v_pk_fma_f16 v234, v234, s49, s49\n"
    "v_mfma_f32_16x16x32_bf16 v[176:179], a[208:211], v[160:163], v[176:179]\n"
    "ds_read_u16_d16_hi v206, v206\n"
    "v_pk_fma_f16 v235, v235, s49, s49\n"
    "v_mfma_f32_16x16x32_bf16 v[176:179], a[120:123], v[164:167], v[176:179]\n"
    "ds_read_u16 v199, v199\n"
    "v_rcp_f16_e64 v232, v234\n"
    "v_mfma_f32_16x16x32_bf16 v[180:183], a[164:167], v[156:159], 0\n"
    "s_waitcnt lgkmcnt(7)\n"
    "ds_read_u16_d16_hi v207, v207\n"
    "v_rcp_f16_e32 v233, v235\n"
    "v_mfma_f32_16x16x32_bf16 v[180:183], a[212:215], v[160:163], v[180:183]\n"
    "v_rcp_f16_sdwa v232, v234 dst_sel:WORD_1 dst_unused:UNUSED_PRESERVE src0_sel:WORD_1\n"
    "v_mfma_f32_16x16x32_bf16 v[180:183], a[240:243], v[164:167], v[180:183]\n"
    "v_or_b32_e64 v193, v193, v201\n"
    "v_rcp_f16_sdwa v233, v235 dst_sel:WORD_1 dst_unused:UNUSED_PRESERVE src0_sel:WORD_1\n"
    "v_pk_mul_f16 v184, v184, v232\n"
    "v_or_b32_e64 v194, v194, v202\n"
    "v_pk_mul_f16 v188, v188, v233\n"
    "v_mfma_f32_16x16x32_f16 v[96:99], a[32:35], v[184:187], v[96:99]\n"
    "s_waitcnt lgkmcnt(6)\n"
    "ds_read_b128 a[240:243], v244 offset:38912\n"
    "v_pk_fma_f16 v232, v192, v192, s48\n"
    "v_mfma_f32_16x16x32_f16 v[100:103], a[36:39], v[184:187], v[100:103]\n"
    "v_or_b32_e32 v195, v195, v203\n"
    "v_pk_fma_f16 v233, v196, v196, s48\n"
    "v_mfma_f32_16x16x32_f16 v[112:115], a[40:43], v[184:187], v[112:115]\n"
    "v_pk_mul_f16 v232, v192, v232\n"
    "s_waitcnt lgkmcnt(5)\n"
    "v_or_b32_e32 v197, v197, v205\n"
    "v_mfma_f32_16x16x32_f16 v[116:119], a[44:47], v[184:187], v[116:119]\n"
    "v_pk_mul_f16 v233, v196, v233\n"
    "v_lshlrev_b32_sdwa v201, 1, v171 dst_sel:DWORD dst_unused:UNUSED_PAD src0_sel:DWORD src1_sel:WORD_1\n"
    "v_mfma_f32_16x16x32_f16 v[128:131], v[236:239], v[184:187], v[128:131]\n"
    "ds_read_u16_d16_hi v201, v201\n"
    "v_lshlrev_b32_sdwa v202, 1, v173 dst_sel:DWORD dst_unused:UNUSED_PAD src0_sel:DWORD src1_sel:WORD_1\n"
    "v_mfma_f32_16x16x32_f16 v[132:135], a[244:247], v[184:187], v[132:135]\n"
    "ds_read_u16_d16_hi v202, v202\n"
    "v_cvt_pk_f16_f32 v184, v168, v169\n"
    "v_mfma_f32_16x16x32_f16 v[104:107], a[32:35], v[188:191], v[104:107]\n"
    "s_waitcnt lgkmcnt(5)\n"
    "s_nop 0\n"
    "v_lshlrev_b32_sdwa v185, 1, v170 dst_sel:DWORD dst_unused:UNUSED_PAD src0_sel:DWORD src1_sel:WORD_1\n"
    "v_lshlrev_b32_sdwa v186, 1, v172 dst_sel:DWORD dst_unused:UNUSED_PAD src0_sel:DWORD src1_sel:WORD_1\n"
    "v_mfma_f32_16x16x32_f16 v[108:111], a[36:39], v[188:191], v[108:111]\n"
    "ds_read_u16 v185, v185\n"
    "ds_read_u16 v186, v186\n"
    "v_mfma_f32_16x16x32_f16 v[120:123], a[40:43], v[188:191], v[120:123]\n"
    "v_lshlrev_b32_sdwa v187, 1, v174 dst_sel:DWORD dst_unused:UNUSED_PAD src0_sel:DWORD src1_sel:WORD_1\n"
    "v_lshlrev_b32_sdwa v203, 1, v175 dst_sel:DWORD dst_unused:UNUSED_PAD src0_sel:DWORD src1_sel:WORD_1\n"
    "v_mfma_f32_16x16x32_f16 v[124:127], a[44:47], v[188:191], v[124:127]\n"
    "s_waitcnt lgkmcnt(4)\n"
    "ds_read_u16 v187, v187\n"
    "v_exp_f16_e32 v234, v232\n"
    "v_mfma_f32_16x16x32_f16 v[136:139], v[236:239], v[188:191], v[136:139]\n"
    "ds_read_b128 v[236:239], v244 offset:12288\n"
    "ds_read_u16_d16_hi v203, v203\n"
    "v_smfmac_f32_32x32x32_bf16 v[112:127], a[248:251], a[232:239], v241\n"
    "ds_read_b128 a[232:235], v243 offset:28736\n"
    "v_or_b32_e64 v198, v198, v206\n"
    "v_exp_f16_e64 v235, v233\n"
    "v_lshlrev_b32_sdwa v205, 1, v179 dst_sel:DWORD dst_unused:UNUSED_PAD src0_sel:DWORD src1_sel:WORD_1\n"
    "v_mfma_f32_16x16x32_f16 v[140:143], a[244:247], v[188:191], v[140:143]\n"
    "ds_read_b128 a[244:247], v244 offset:14336\n"
    "ds_read_b128 a[236:239], v243 offset:29760\n"
    "v_mfma_f32_16x16x32_bf16 v[168:171], a[168:171], v[144:147], 0\n"
    "v_cvt_pk_f16_f32 v188, v176, v177\n"
    "v_lshlrev_b32_sdwa v189, 1, v178 dst_sel:DWORD dst_unused:UNUSED_PAD src0_sel:DWORD src1_sel:WORD_1\n"
    "v_mfma_f32_16x16x32_bf16 v[168:171], a[216:219], v[148:151], v[168:171]\n"
    "ds_read_u16 v189, v189\n"
    "v_lshlrev_b32_sdwa v190, 1, v180 dst_sel:DWORD dst_unused:UNUSED_PAD src0_sel:DWORD src1_sel:WORD_1\n"
    "v_mfma_f32_16x16x32_bf16 v[168:171], a[124:127], v[152:155], v[168:171]\n"
    "v_lshlrev_b32_sdwa v206, 1, v181 dst_sel:DWORD dst_unused:UNUSED_PAD src0_sel:DWORD src1_sel:WORD_1\n"
    "v_lshlrev_b32_sdwa v191, 1, v182 dst_sel:DWORD dst_unused:UNUSED_PAD src0_sel:DWORD src1_sel:WORD_1\n"
    "v_mfma_f32_16x16x32_bf16 v[172:175], a[172:175], v[144:147], 0\n"
    "v_exp_f16_sdwa v234, v232 dst_sel:WORD_1 dst_unused:UNUSED_PRESERVE src0_sel:WORD_1\n"
    "v_mfma_f32_16x16x32_bf16 v[172:175], a[220:223], v[148:151], v[172:175]\n"
    "s_waitcnt lgkmcnt(8)\n"
    "ds_read_u16_d16_hi v205, v205\n"
    "v_or_b32_e32 v199, v199, v207\n"
    "v_mfma_f32_16x16x32_bf16 v[172:175], a[240:243], v[152:155], v[172:175]\n"
    "s_waitcnt lgkmcnt(8)\n"
    "s_nop 0\n"
    "v_lshlrev_b32_sdwa v207, 1, v183 dst_sel:DWORD dst_unused:UNUSED_PAD src0_sel:DWORD src1_sel:WORD_1\n"
    "v_exp_f16_sdwa v235, v233 dst_sel:WORD_1 dst_unused:UNUSED_PRESERVE src0_sel:WORD_1\n"
    "v_mfma_f32_16x16x32_bf16 v[176:179], a[168:171], v[156:159], 0\n"
    "ds_read_u16 v190, v190\n"
    "v_pk_fma_f16 v234, v234, s49, s49\n"
    "v_mfma_f32_16x16x32_bf16 v[176:179], a[216:219], v[160:163], v[176:179]\n"
    "ds_read_u16_d16_hi v206, v206\n"
    "v_pk_fma_f16 v235, v235, s49, s49\n"
    "v_mfma_f32_16x16x32_bf16 v[176:179], a[124:127], v[164:167], v[176:179]\n"
    "ds_read_u16 v191, v191\n"
    "v_rcp_f16_e64 v232, v234\n"
    "v_mfma_f32_16x16x32_bf16 v[180:183], a[172:175], v[156:159], 0\n"
    "s_waitcnt lgkmcnt(7)\n"
    "ds_read_u16_d16_hi v207, v207\n"
    "v_rcp_f16_e32 v233, v235\n"
    "v_mfma_f32_16x16x32_bf16 v[180:183], a[220:223], v[160:163], v[180:183]\n"
    "v_rcp_f16_sdwa v232, v234 dst_sel:WORD_1 dst_unused:UNUSED_PRESERVE src0_sel:WORD_1\n"
    "v_mfma_f32_16x16x32_bf16 v[180:183], a[240:243], v[164:167], v[180:183]\n"
    "v_or_b32_e64 v185, v185, v201\n"
    "v_rcp_f16_sdwa v233, v235 dst_sel:WORD_1 dst_unused:UNUSED_PRESERVE src0_sel:WORD_1\n"
    "v_pk_mul_f16 v192, v192, v232\n"
    "v_or_b32_e64 v186, v186, v202\n"
    "v_pk_mul_f16 v196, v196, v233\n"
    "v_mfma_f32_16x16x32_f16 v[96:99], a[48:51], v[192:195], v[96:99]\n"
    "s_waitcnt lgkmcnt(6)\n"
    "v_pk_fma_f16 v232, v184, v184, s48\n"
    "v_or_b32_e32 v187, v187, v203\n"
    "v_mfma_f32_16x16x32_f16 v[100:103], a[52:55], v[192:195], v[100:103]\n"
    "v_pk_fma_f16 v233, v188, v188, s48\n"
    "v_pk_mul_f16 v232, v184, v232\n"
    "v_mfma_f32_16x16x32_f16 v[112:115], a[56:59], v[192:195], v[112:115]\n"
    "v_pk_mul_f16 v233, v188, v233\n"
    "s_waitcnt lgkmcnt(4)\n"
    "v_or_b32_e32 v189, v189, v205\n"
    "v_mfma_f32_16x16x32_f16 v[116:119], a[60:63], v[192:195], v[116:119]\n"
    "v_lshlrev_b32_sdwa v201, 1, v171 dst_sel:DWORD dst_unused:UNUSED_PAD src0_sel:DWORD src1_sel:WORD_1\n"
    "v_lshlrev_b32_sdwa v202, 1, v173 dst_sel:DWORD dst_unused:UNUSED_PAD src0_sel:DWORD src1_sel:WORD_1\n"
    "v_mfma_f32_16x16x32_f16 v[128:131], v[236:239], v[192:195], v[128:131]\n"
    "ds_read_u16_d16_hi v201, v201\n"
    "ds_read_u16_d16_hi v202, v202\n"
    "v_mfma_f32_16x16x32_f16 v[132:135], a[244:247], v[192:195], v[132:135]\n"
    "v_cvt_pk_f16_f32 v192, v168, v169\n"
    "v_lshlrev_b32_sdwa v193, 1, v170 dst_sel:DWORD dst_unused:UNUSED_PAD src0_sel:DWORD src1_sel:WORD_1\n"
    "v_mfma_f32_16x16x32_f16 v[104:107], a[48:51], v[196:199], v[104:107]\n"
    "s_waitcnt lgkmcnt(4)\n"
    "s_nop 0\n"
    "ds_read_u16 v193, v193\n"
    "v_lshlrev_b32_sdwa v194, 1, v172 dst_sel:DWORD dst_unused:UNUSED_PAD src0_sel:DWORD src1_sel:WORD_1\n"
    "v_mfma_f32_16x16x32_f16 v[108:111], a[52:55], v[196:199], v[108:111]\n"
    "ds_read_u16 v194, v194\n"
    "v_lshlrev_b32_sdwa v195, 1, v174 dst_sel:DWORD dst_unused:UNUSED_PAD src0_sel:DWORD src1_sel:WORD_1\n"
    "v_mfma_f32_16x16x32_f16 v[120:123], a[56:59], v[196:199], v[120:123]\n"
    "ds_read_u16 v195, v195\n"
    "v_lshlrev_b32_sdwa v203, 1, v175 dst_sel:DWORD dst_unused:UNUSED_PAD src0_sel:DWORD src1_sel:WORD_1\n"
    "v_mfma_f32_16x16x32_f16 v[124:127], a[60:63], v[196:199], v[124:127]\n"
    "s_waitcnt lgkmcnt(5)\n"
    "v_exp_f16_e32 v234, v232\n"
    "v_mfma_f32_16x16x32_f16 v[136:139], v[236:239], v[196:199], v[136:139]\n"
    "ds_read_b128 v[236:239], v244 offset:16384\n"
    "ds_read_u16_d16_hi v203, v203\n"
    "v_mfma_f32_16x16x32_f16 v[140:143], a[244:247], v[196:199], v[140:143]\n"
    "v_or_b32_e64 v190, v190, v206\n"
    "v_cvt_pk_f16_f32 v196, v176, v177\n"
    "v_lshlrev_b32_sdwa v197, 1, v178 dst_sel:DWORD dst_unused:UNUSED_PAD src0_sel:DWORD src1_sel:WORD_1\n"
    "v_exp_f16_e64 v235, v233\n"
    "v_lshlrev_b32_sdwa v205, 1, v179 dst_sel:DWORD dst_unused:UNUSED_PAD src0_sel:DWORD src1_sel:WORD_1\n"
    "v_lshlrev_b32_sdwa v198, 1, v180 dst_sel:DWORD dst_unused:UNUSED_PAD src0_sel:DWORD src1_sel:WORD_1\n"
    "v_smfmac_f32_32x32x32_bf16 v[128:143], a[248:251], a[232:239], v241\n"
    "ds_read_b128 a[244:247], v244 offset:18432\n"
    "ds_read_u16 v197, v197\n"
    "v_lshlrev_b32_sdwa v206, 1, v181 dst_sel:DWORD dst_unused:UNUSED_PAD src0_sel:DWORD src1_sel:WORD_1\n"
    "v_lshlrev_b32_sdwa v199, 1, v182 dst_sel:DWORD dst_unused:UNUSED_PAD src0_sel:DWORD src1_sel:WORD_1\n"
    "v_exp_f16_sdwa v234, v232 dst_sel:WORD_1 dst_unused:UNUSED_PRESERVE src0_sel:WORD_1\n"
    "v_or_b32_e64 v191, v191, v207\n"
    "v_lshlrev_b32_sdwa v207, 1, v183 dst_sel:DWORD dst_unused:UNUSED_PAD src0_sel:DWORD src1_sel:WORD_1\n"
    "v_exp_f16_sdwa v235, v233 dst_sel:WORD_1 dst_unused:UNUSED_PRESERVE src0_sel:WORD_1\n"
    "v_pk_fma_f16 v234, v234, s49, s49\n"
    "v_pk_fma_f16 v235, v235, s49, s49\n"
    "v_rcp_f16_e64 v232, v234\n"
    "v_rcp_f16_e64 v233, v235\n"
    "v_rcp_f16_sdwa v232, v234 dst_sel:WORD_1 dst_unused:UNUSED_PRESERVE src0_sel:WORD_1\n"
    "s_waitcnt lgkmcnt(6)\n"
    "v_or_b32_e32 v193, v193, v201\n"
    "v_rcp_f16_sdwa v233, v235 dst_sel:WORD_1 dst_unused:UNUSED_PRESERVE src0_sel:WORD_1\n"
    "v_pk_mul_f16 v184, v184, v232\n"
    "s_waitcnt lgkmcnt(5)\n"
    "v_or_b32_e32 v194, v194, v202\n"
    "v_mfma_f32_16x16x32_f16 v[96:99], a[64:67], v[184:187], v[96:99]\n"
    "s_barrier\n"
    "v_mfma_f32_16x16x32_f16 v[100:103], a[68:71], v[184:187], v[100:103]\n"
    "s_waitcnt lgkmcnt(3)\n"
    "ds_read_u16_d16_hi v205, v205\n"
    "ds_read_u16 v198, v198\n"
    "ds_read_u16_d16_hi v206, v206\n"
    "v_mfma_f32_16x16x32_f16 v[112:115], a[72:75], v[184:187], v[112:115]\n"
    "s_waitcnt lgkmcnt(4)\n"
    "ds_read_u16 v199, v199\n"
    "v_pk_mul_f16 v188, v188, v233\n"
    "v_mfma_f32_16x16x32_f16 v[116:119], a[76:79], v[184:187], v[116:119]\n"
    "v_pk_fma_f16 v232, v192, v192, s48\n"
    "v_or_b32_e32 v195, v195, v203\n"
    "v_mfma_f32_16x16x32_f16 v[128:131], v[236:239], v[184:187], v[128:131]\n"
    "ds_read_u16_d16_hi v207, v207\n"
    "v_pk_fma_f16 v233, v196, v196, s48\n"
    "v_mfma_f32_16x16x32_f16 v[132:135], a[244:247], v[184:187], v[132:135]\n"
    "v_pk_mul_f16 v232, v192, v232\n"
    "v_pk_mul_f16 v233, v196, v233\n"
    "v_mfma_f32_16x16x32_f16 v[104:107], a[64:67], v[188:191], v[104:107]\n"
    "v_exp_f16_e64 v234, v232\n"
    "v_mfma_f32_16x16x32_f16 v[108:111], a[68:71], v[188:191], v[108:111]\n"
    "v_exp_f16_e64 v235, v233\n"
    "v_mfma_f32_16x16x32_f16 v[120:123], a[72:75], v[188:191], v[120:123]\n"
    "v_exp_f16_sdwa v234, v232 dst_sel:WORD_1 dst_unused:UNUSED_PRESERVE src0_sel:WORD_1\n"
    "v_mfma_f32_16x16x32_f16 v[124:127], a[76:79], v[188:191], v[124:127]\n"
    "v_exp_f16_sdwa v235, v233 dst_sel:WORD_1 dst_unused:UNUSED_PRESERVE src0_sel:WORD_1\n"
    "v_mfma_f32_16x16x32_f16 v[136:139], v[236:239], v[188:191], v[136:139]\n"
    "ds_read_b128 v[236:239], v244 offset:20480\n"
    "v_pk_fma_f16 v234, v234, s49, s49\n"
    "v_mfma_f32_16x16x32_f16 v[140:143], a[244:247], v[188:191], v[140:143]\n"
    "s_waitcnt lgkmcnt(5)\n"
    "ds_read_b128 a[244:247], v244 offset:22528\n"
    "v_or_b32_e32 v197, v197, v205\n"
    "v_pk_fma_f16 v235, v235, s49, s49\n"
    "v_rcp_f16_e64 v232, v234\n"
    "v_rcp_f16_e32 v233, v235\n"
    "s_waitcnt vmcnt(0)\n"
    "v_smfmac_f32_32x32x32_bf16 v[128:143], a[248:251], a[224:231], v241\n"
    "v_rcp_f16_sdwa v232, v234 dst_sel:WORD_1 dst_unused:UNUSED_PRESERVE src0_sel:WORD_1\n"
    "s_waitcnt lgkmcnt(4)\n"
    "v_or_b32_e32 v198, v198, v206\n"
    "v_rcp_f16_sdwa v233, v235 dst_sel:WORD_1 dst_unused:UNUSED_PRESERVE src0_sel:WORD_1\n"
    "v_pk_mul_f16 v192, v192, v232\n"
    "v_pk_mul_f16 v196, v196, v233\n"
    "s_nop 0\n"
    "v_mfma_f32_16x16x32_f16 v[96:99], a[80:83], v[192:195], v[96:99]\n"
    "v_mfma_f32_16x16x32_f16 v[100:103], a[84:87], v[192:195], v[100:103]\n"
    "v_mfma_f32_16x16x32_f16 v[112:115], a[88:91], v[192:195], v[112:115]\n"
    "s_waitcnt lgkmcnt(1)\n"
    "v_or_b32_e64 v199, v199, v207\n"
    "v_mfma_f32_16x16x32_f16 v[116:119], a[92:95], v[192:195], v[116:119]\n"
    "s_waitcnt lgkmcnt(0)\n"
    "s_nop 0\n"
    "v_mfma_f32_16x16x32_f16 v[128:131], v[236:239], v[192:195], v[128:131]\n"
    "v_mfma_f32_16x16x32_f16 v[132:135], a[244:247], v[192:195], v[132:135]\n"
    "v_cvt_pk_bf16_f32 v96, v96, v97\n"
    "v_cvt_pk_bf16_f32 v97, v98, v99\n"
    "v_mfma_f32_16x16x32_f16 v[104:107], a[80:83], v[196:199], v[104:107]\n"
    "v_cvt_pk_bf16_f32 v98, v100, v101\n"
    "v_cvt_pk_bf16_f32 v99, v102, v103\n"
    "v_mfma_f32_16x16x32_f16 v[108:111], a[84:87], v[196:199], v[108:111]\n"
    "ds_write_b128 v242, v[96:99] offset:24640\n"
    "v_mfma_f32_16x16x32_f16 v[120:123], a[88:91], v[196:199], v[120:123]\n"
    "v_cvt_pk_bf16_f32 v112, v112, v113\n"
    "v_cvt_pk_bf16_f32 v113, v114, v115\n"
    "v_mfma_f32_16x16x32_f16 v[124:127], a[92:95], v[196:199], v[124:127]\n"
    "v_cvt_pk_bf16_f32 v114, v116, v117\n"
    "v_cvt_pk_bf16_f32 v115, v118, v119\n"
    "v_mfma_f32_16x16x32_f16 v[136:139], v[236:239], v[196:199], v[136:139]\n"
    "ds_write_b128 v242, v[112:115] offset:26688\n"
    "v_mfma_f32_16x16x32_f16 v[140:143], a[244:247], v[196:199], v[140:143]\n"
    "v_cvt_pk_bf16_f32 v128, v128, v129\n"
    "v_cvt_pk_bf16_f32 v129, v130, v131\n"
    "v_cvt_pk_bf16_f32 v130, v132, v133\n"
    "v_cvt_pk_bf16_f32 v131, v134, v135\n"
    "v_cvt_pk_bf16_f32 v104, v104, v105\n"
    "v_cvt_pk_bf16_f32 v105, v106, v107\n"
    "ds_write_b128 v242, v[128:131] offset:28736\n"
    "v_cvt_pk_bf16_f32 v106, v108, v109\n"
    "v_cvt_pk_bf16_f32 v107, v110, v111\n"
    "v_cvt_pk_bf16_f32 v120, v120, v121\n"
    "v_cvt_pk_bf16_f32 v121, v122, v123\n"
    "v_cvt_pk_bf16_f32 v122, v124, v125\n"
    "v_cvt_pk_bf16_f32 v123, v126, v127\n"
    "ds_write_b128 v242, v[104:107] offset:25664\n"
    "v_cvt_pk_bf16_f32 v136, v136, v137\n"
    "v_cvt_pk_bf16_f32 v137, v138, v139\n"
    "v_cvt_pk_bf16_f32 v138, v140, v141\n"
    "v_cvt_pk_bf16_f32 v139, v142, v143\n"
    "ds_write_b128 v242, v[120:123] offset:27712\n"
    "ds_write_b128 v242, v[136:139] offset:29760\n"
    "ds_read_b128 a[224:227], v249 offset:0\n"
    "ds_read_b128 a[228:231], v249 offset:2048\n"
    "ds_read_b128 a[232:235], v249 offset:4096\n"
    "ds_read_b128 a[236:239], v249 offset:1024\n"
    "ds_read_b128 a[252:255], v249 offset:3072\n"
    "ds_read_b128 a[240:243], v249 offset:5120\n"
    "s_waitcnt lgkmcnt(5)\n"
    "buffer_store_dwordx4 a[224:227], v255, s[44:47], s58 offen offset:0 sc1\n"
    "s_waitcnt lgkmcnt(4)\n"
    "buffer_store_dwordx4 a[228:231], v255, s[44:47], s58 offen offset:64 sc1\n"
    "s_waitcnt lgkmcnt(3)\n"
    "buffer_store_dwordx4 a[232:235], v255, s[44:47], s58 offen offset:128 sc1\n"
    "s_waitcnt lgkmcnt(1)\n"
    "buffer_store_dwordx4 a[236:239], v255, s[44:47], s101 offen offset:0 sc1\n"
    "buffer_store_dwordx4 a[252:255], v255, s[44:47], s101 offen offset:64 sc1\n"
    "s_waitcnt lgkmcnt(0)\n"
    "s_nop 0\n"
    "buffer_store_dwordx4 a[240:243], v255, s[44:47], s101 offen offset:128 sc1\n"
    "98:\n"
    "s_waitcnt vmcnt(0) lgkmcnt(0)\n"
    ".Lreplk_end:\n"
    : "+{s66}"(flg), "+{v[208:215]}"(PA)
    : "{s[40:43]}"(rx), "{s[44:47]}"(srd(out, XBYTES)), "{s[48:55]}"(S), "{s56}"((u32)(L_XT - L_H - w * GS)),
      "{s67}"(0x3fffffffu | (tx == 0 ? 0x80000000u : 0u) | (tx == 31 ? 0x40000000u : 0u)), "{s[96:99]}"(T), "{s[76:79]}"(rt), "{s[80:87]}"(rw), "{s[88:95]}"(rd)
    : "memory", "scc", "vcc", "v0", "v1", "v2", "v3", "v4", "v5", "v6", "v7", "v8", "v9", "v10", "v11", "v12", "v13", "v14", "v15", "v16", "v17", "v18", "v19", "v20", "v21", "v22", "v23", "v24", "v25", "v26", "v27", "v28", "v29", "v30", "v31", "v32", "v33", "v34", "v35", "v36", "v37", "v38", "v39", "v40", "v41", "v42", "v43", "v44", "v45", "v46", "v47", "v48", "v49", "v50", "v51", "v52", "v53", "v54", "v55", "v56", "v57", "v58", "v59", "v60", "v61", "v62", "v63", "v64", "v65", "v66", "v67", "v68", "v69", "v70", "v71", "v72", "v73", "v74", "v75", "v76", "v77", "v78", "v79", "v80", "v81", "v82", "v83", "v84", "v85", "v86", "v87", "v88", "v89", "v90", "v91", "v92", "v93", "v94", "v95", "v96", "v97", "v98", "v99", "v100", "v101", "v102", "v103", "v104", "v105", "v106", "v107", "v108", "v109", "v110", "v111", "v112", "v113", "v114", "v115", "v116", "v117", "v118", "v119", "v120", "v121", "v122", "v123", "v124", "v125", "v126", "v127", "v128", "v129", "v130", "v131", "v132", "v133", "v134", "v135", "v136", "v137", "v138", "v139", "v140", "v141", "v142", "v143", "v144", "v145", "v146", "v147", "v148", "v149", "v150", "v151", "v152", "v153", "v154", "v155", "v156", "v157", "v158", "v159", "v160", "v161", "v162", "v163", "v164", "v165", "v166", "v167", "v168", "v169", "v170", "v171", "v172", "v173", "v174", "v175", "v176", "v177", "v178", "v179", "v180", "v181", "v182", "v183", "v184", "v185", "v186", "v187", "v188", "v189", "v190", "v191", "v192", "v193", "v194", "v195", "v196", "v197", "v198", "v199", "v200", "v201", "v202", "v203", "v204", "v205", "v206", "v207", "v216", "v217", "v218", "v219", "v220", "v221", "v222", "v223", "v224", "v225", "v226", "v227", "v228", "v229", "v230", "v231", "v232", "v233", "v234", "v235", "v236", "v237", "v238", "v239", "v240", "v241", "v242", "v243", "v244", "v245", "v246", "v247", "v248", "v249", "v250", "v251", "v252", "v253", "v254", "v255", "a0", "a1", "a2", "a3", "a4", "a5", "a6", "a7", "a8", "a9", "a10", "a11", "a12", "a13", "a14", "a15", "a16", "a17", "a18", "a19", "a20", "a21", "a22", "a23", "a24", "a25", "a26", "a27", "a28", "a29", "a30", "a31", "a32", "a33", "a34", "a35", "a36", "a37", "a38", "a39", "a40", "a41", "a42", "a43", "a44", "a45", "a46", "a47", "a48", "a49", "a50", "a51", "a52", "a53", "a54", "a55", "a56", "a57", "a58", "a59", "a60", "a61", "a62", "a63", "a64", "a65", "a66", "a67", "a68", "a69", "a70", "a71", "a72", "a73", "a74", "a75", "a76", "a77", "a78", "a79", "a80", "a81", "a82", "a83", "a84", "a85", "a86", "a87", "a88", "a89", "a90", "a91", "a92", "a93", "a94", "a95", "a96", "a97", "a98", "a99", "a100", "a101", "a102", "a103", "a104", "a105", "a106", "a107", "a108", "a109", "a110", "a111", "a112", "a113", "a114", "a115", "a116", "a117", "a118", "a119", "a120", "a121", "a122", "a123", "a124", "a125", "a126", "a127", "a128", "a129", "a130", "a131", "a132", "a133", "a134", "a135", "a136", "a137", "a138", "a139", "a140", "a141", "a142", "a143", "a144", "a145", "a146", "a147", "a148", "a149", "a150", "a151", "a152", "a153", "a154", "a155", "a156", "a157", "a158", "a159", "a160", "a161", "a162", "a163", "a164", "a165", "a166", "a167", "a168", "a169", "a170", "a171", "a172", "a173", "a174", "a175", "a176", "a177", "a178", "a179", "a180", "a181", "a182", "a183", "a184", "a185", "a186", "a187", "a188", "a189", "a190", "a191", "a192", "a193", "a194", "a195", "a196", "a197", "a198", "a199", "a200", "a201", "a202", "a203", "a204", "a205", "a206", "a207", "a208", "a209", "a210", "a211", "a212", "a213", "a214", "a215", "a216", "a217", "a218", "a219", "a220", "a221", "a222", "a223", "a224", "a225", "a226", "a227", "a228", "a229", "a230", "a231", "a232", "a233", "a234", "a235", "a236", "a237", "a238", "a239", "a240", "a241", "a242", "a243", "a244", "a245", "a246", "a247", "a248", "a249", "a250", "a251", "a252", "a253", "a254", "a255", "s39", "s57", "s58", "s59", "s60", "s61", "s62", "s63", "s64", "s65", "s68", "s69", "s70", "s71", "s72", "s73", "s74", "s75", "s100", "s101");
  if (flg != 6u) {
    const int l2 = __builtin_amdgcn_mbcnt_hi(~0u, __builtin_amdgcn_mbcnt_lo(~0u, 0u));
    slow_path(x, dwl, dws, w1, w2, out, (float*)smem, w * 64 + l2, tx, ty0);
  }
}

extern "C" void kernel_launch(void* const* d_in, const int* in_sizes, int n_in, void* d_out, int out_size, void* d_ws,
                              size_t ws_size, hipStream_t stream) {
  replk_kernel<<<256, 256, 0, stream>>>((const u16*)d_in[0], (const u16*)d_in[1], (const u16*)d_in[2], (const u16*)d_in[3],
                                        (const u16*)d_in[4], (u16*)d_out
                                        );
}
```

```cpp
#include <hip/hip_runtime.h>
#include <hip/hip_bf16.h>

typedef unsigned u32;
typedef unsigned short u16;
typedef unsigned long long u64;
typedef u32 u32x4 __attribute__((ext_vector_type(4)));
typedef u32 u32x8 __attribute__((ext_vector_type(8)));
typedef u32 u32x16 __attribute__((ext_vector_type(16)));
typedef _Float16 f16x2 __attribute__((ext_vector_type(2)));
typedef __bf16 bf16x2 __attribute__((ext_vector_type(2)));
typedef float f32x2 __attribute__((ext_vector_type(2)));
#define DI __device__ __forceinline__
#define LDSP(T, off) ((__attribute__((address_space(3))) T*)(smem3 + (off)))

constexpr int IMG = 512;
constexpr int CH = 96;
constexpr int HID = 384;
constexpr int PXB = 192;
constexpr int ROWB = 98304;
constexpr int TOEP_CS = 336;
constexpr int XT_RS = 320;
constexpr int XT_PP = 1360;
constexpr int XT_CG = 6752;
constexpr int GS = 6160;
constexpr int L_LUT = 0;
constexpr int L_TOEP = 8208;
constexpr int L_W = 40960;
constexpr int L_LUTN = 65536;
constexpr int L_XT = 81920;
constexpr int L_H = 108928;
constexpr int L_EX = 133568;
constexpr int XT_Q = 27024;
constexpr int L_MISC = 162992;
constexpr int L_TOTAL = 163056;
constexpr int LUTK = 106;
constexpr u32 XBYTES = IMG * IMG * PXB;
constexpr float GAMMA = -0.4684938781597355f;
constexpr float GC1 = -2.302208198144325f, GC3 = -0.1029432395800235f;
constexpr u32 MU_PK = 0x44ea44eau, CC_PK = 0xb77fb77fu;
__device__ const u32 CTAB[9 * 1024] = {
3722283144u,52428,108928,115088,3722283144u,56797,108944,115104,3722283144u,61166,108960,115120,3722283144u,52428,108976,115136,3722283144u,52428,108992,115152,3722283144u,56797,109008,115168,3722283144u,61166,109024,115184,3722283144u,52428,109040,115200,3722283144u,52428,109056,115216,3722283144u,56797,109072,115232,3722283144u,61166,109088,115248,3722283144u,52428,109104,115264,3722283144u,52428,109120,115280,3722283144u,56797,109136,115296,3722283144u,61166,109152,115312,3722283144u,52428,109168,115328,
3722283144u,52428,109376,115536,3722283144u,56797,109392,115552,3722283144u,61166,109408,115568,3722283144u,52428,109424,115584,3722283144u,52428,109312,115472,3722283144u,56797,109328,115488,3722283144u,61166,109344,115504,3722283144u,52428,109360,115520,3722283144u,52428,109248,115408,3722283144u,56797,109264,115424,3722283144u,61166,109280,115440,3722283144u,52428,109296,115456,3722283144u,52428,109184,115344,3722283144u,56797,109200,115360,3722283144u,61166,109216,115376,3722283144u,52428,109232,115392,
3722283144u,52428,109504,115664,3722283144u,56797,109520,115680,3722283144u,61166,109536,115696,3722283144u,52428,109552,115712,3722283144u,52428,109440,115600,3722283144u,56797,109456,115616,3722283144u,61166,109472,115632,3722283144u,52428,109488,115648,3722283144u,52428,109632,115792,3722283144u,56797,109648,115808,3722283144u,61166,109664,115824,3722283144u,52428,109680,115840,3722283144u,52428,109568,115728,3722283144u,56797,109584,115744,3722283144u,61166,109600,115760,3722283144u,52428,109616,115776,
3722283144u,52428,109824,115984,3722283144u,56797,109840,116000,3722283144u,61166,109856,116016,3722283144u,52428,109872,116032,3722283144u,52428,109888,116048,3722283144u,56797,109904,116064,3722283144u,61166,109920,116080,3722283144u,52428,109936,116096,3722283144u,52428,109696,115856,3722283144u,56797,109712,115872,3722283144u,61166,109728,115888,3722283144u,52428,109744,115904,3722283144u,52428,109760,115920,3722283144u,56797,109776,115936,3722283144u,61166,109792,115952,3722283144u,52428,109808,115968,
3722283144u,52428,115088,108928,3722283144u,56797,115104,108944,3722283144u,61166,115120,108960,3722283144u,52428,115136,108976,3722283144u,52428,115152,108992,3722283144u,56797,115168,109008,3722283144u,61166,115184,109024,3722283144u,52428,115200,109040,3722283144u,52428,115216,109056,3722283144u,56797,115232,109072,3722283144u,61166,115248,109088,3722283144u,52428,115264,109104,3722283144u,52428,115280,109120,3722283144u,56797,115296,109136,3722283144u,61166,115312,109152,3722283144u,52428,115328,109168,
3722283144u,52428,115536,109376,3722283144u,56797,115552,109392,3722283144u,61166,115568,109408,3722283144u,52428,115584,109424,3722283144u,52428,115472,109312,3722283144u,56797,115488,109328,3722283144u,61166,115504,109344,3722283144u,52428,115520,109360,3722283144u,52428,115408,109248,3722283144u,56797,115424,109264,3722283144u,61166,115440,109280,3722283144u,52428,115456,109296,3722283144u,52428,115344,109184,3722283144u,56797,115360,109200,3722283144u,61166,115376,109216,3722283144u,52428,115392,109232,
3722283144u,52428,115664,109504,3722283144u,56797,115680,109520,3722283144u,61166,115696,109536,3722283144u,52428,115712,109552,3722283144u,52428,115600,109440,3722283144u,56797,115616,109456,3722283144u,61166,115632,109472,3722283144u,52428,115648,109488,3722283144u,52428,115792,109632,3722283144u,56797,115808,109648,3722283144u,61166,115824,109664,3722283144u,52428,115840,109680,3722283144u,52428,115728,109568,3722283144u,56797,115744,109584,3722283144u,61166,115760,109600,3722283144u,52428,115776,109616,
3722283144u,52428,115984,109824,3722283144u,56797,116000,109840,3722283144u,61166,116016,109856,3722283144u,52428,116032,109872,3722283144u,52428,116048,109888,3722283144u,56797,116064,109904,3722283144u,61166,116080,109920,3722283144u,52428,116096,109936,3722283144u,52428,115856,109696,3722283144u,56797,115872,109712,3722283144u,61166,115888,109728,3722283144u,52428,115904,109744,3722283144u,52428,115920,109760,3722283144u,56797,115936,109776,3722283144u,61166,115952,109792,3722283144u,52428,115968,109808,
3722283144u,52428,121248,127408,3722283144u,56797,121264,127424,3722283144u,61166,121280,127440,3722283144u,52428,121296,127456,3722283144u,52428,121312,127472,3722283144u,56797,121328,127488,3722283144u,61166,121344,127504,3722283144u,52428,121360,127520,3722283144u,52428,121376,127536,3722283144u,56797,121392,127552,3722283144u,61166,121408,127568,3722283144u,52428,121424,127584,3722283144u,52428,121440,127600,3722283144u,56797,121456,127616,3722283144u,61166,121472,127632,3722283144u,52428,121488,127648,
3722283144u,52428,121696,127856,3722283144u,56797,121712,127872,3722283144u,61166,121728,127888,3722283144u,52428,121744,127904,3722283144u,52428,121632,127792,3722283144u,56797,121648,127808,3722283144u,61166,121664,127824,3722283144u,52428,121680,127840,3722283144u,52428,121568,127728,3722283144u,56797,121584,127744,3722283144u,61166,121600,127760,3722283144u,52428,121616,127776,3722283144u,52428,121504,127664,3722283144u,56797,121520,127680,3722283144u,61166,121536,127696,3722283144u,52428,121552,127712,
3722283144u,52428,121824,127984,3722283144u,56797,121840,128000,3722283144u,61166,121856,128016,3722283144u,52428,121872,128032,3722283144u,52428,121760,127920,3722283144u,56797,121776,127936,3722283144u,61166,121792,127952,3722283144u,52428,121808,127968,3722283144u,52428,121952,128112,3722283144u,56797,121968,128128,3722283144u,61166,121984,128144,3722283144u,52428,122000,128160,3722283144u,52428,121888,128048,3722283144u,56797,121904,128064,3722283144u,61166,121920,128080,3722283144u,52428,121936,128096,
3722283144u,52428,122144,128304,3722283144u,56797,122160,128320,3722283144u,61166,122176,128336,3722283144u,52428,122192,128352,3722283144u,52428,122208,128368,3722283144u,56797,122224,128384,3722283144u,61166,122240,128400,3722283144u,52428,122256,128416,3722283144u,52428,122016,128176,3722283144u,56797,122032,128192,3722283144u,61166,122048,128208,3722283144u,52428,122064,128224,3722283144u,52428,122080,128240,3722283144u,56797,122096,128256,3722283144u,61166,122112,128272,3722283144u,52428,122128,128288,
3722283144u,52428,127408,121248,3722283144u,56797,127424,121264,3722283144u,61166,127440,121280,3722283144u,52428,127456,121296,3722283144u,52428,127472,121312,3722283144u,56797,127488,121328,3722283144u,61166,127504,121344,3722283144u,52428,127520,121360,3722283144u,52428,127536,121376,3722283144u,56797,127552,121392,3722283144u,61166,127568,121408,3722283144u,52428,127584,121424,3722283144u,52428,127600,121440,3722283144u,56797,127616,121456,3722283144u,61166,127632,121472,3722283144u,52428,127648,121488,
3722283144u,52428,127856,121696,3722283144u,56797,127872,121712,3722283144u,61166,127888,121728,3722283144u,52428,127904,121744,3722283144u,52428,127792,121632,3722283144u,56797,127808,121648,3722283144u,61166,127824,121664,3722283144u,52428,127840,121680,3722283144u,52428,127728,121568,3722283144u,56797,127744,121584,3722283144u,61166,127760,121600,3722283144u,52428,127776,121616,3722283144u,52428,127664,121504,3722283144u,56797,127680,121520,3722283144u,61166,127696,121536,3722283144u,52428,127712,121552,
3722283144u,52428,127984,121824,3722283144u,56797,128000,121840,3722283144u,61166,128016,121856,3722283144u,52428,128032,121872,3722283144u,52428,127920,121760,3722283144u,56797,127936,121776,3722283144u,61166,127952,121792,3722283144u,52428,127968,121808,3722283144u,52428,128112,121952,3722283144u,56797,128128,121968,3722283144u,61166,128144,121984,3722283144u,52428,128160,122000,3722283144u,52428,128048,121888,3722283144u,56797,128064,121904,3722283144u,61166,128080,121920,3722283144u,52428,128096,121936,
3722283144u,52428,128304,122144,3722283144u,56797,128320,122160,3722283144u,61166,128336,122176,3722283144u,52428,128352,122192,3722283144u,52428,128368,122208,3722283144u,56797,128384,122224,3722283144u,61166,128400,122240,3722283144u,52428,128416,122256,3722283144u,52428,128176,122016,3722283144u,56797,128192,122032,3722283144u,61166,128208,122048,3722283144u,52428,128224,122064,3722283144u,52428,128240,122080,3722283144u,56797,128256,122096,3722283144u,61166,128272,122112,3722283144u,52428,128288,122128,
40960,393216,2147483648u,3072,40976,393408,2147483664u,3088,40992,393600,2147483680u,3104,41008,393792,2147483696u,3120,41024,393984,2147483840u,3264,41040,394176,2147483856u,3280,41056,394368,2147483872u,3296,41072,394560,2147483888u,3312,41088,394752,384,1073745280,41104,394944,400,1073745296,41120,395136,416,1073745312,41136,395328,432,1073745328,41152,395520,576,1073745472,41168,395712,592,1073745488,41184,395904,608,1073745504,41200,396096,624,1073745520,
41216,393232,768,396288,41232,393424,784,396304,41248,393616,800,396320,41264,393808,816,396336,41280,394000,960,396480,41296,394192,976,396496,41312,394384,992,396512,41328,394576,1008,396528,41344,394768,1152,1074138496,41360,394960,1168,1074138512,41376,395152,1184,1074138528,41392,395344,1200,1074138544,41408,395536,1344,1074138688,41424,395728,1360,1074138704,41440,395920,1376,1074138720,41456,396112,1392,1074138736,
41472,393248,1536,789504,41488,393440,1552,789520,41504,393632,1568,789536,41520,393824,1584,789552,41536,394016,1728,789696,41552,394208,1744,789712,41568,394400,1760,789728,41584,394592,1776,789744,41600,394784,1920,1074531712,41616,394976,1936,1074531728,41632,395168,1952,1074531744,41648,395360,1968,1074531760,41664,395552,2112,1074531904,41680,395744,2128,1074531920,41696,395936,2144,1074531936,41712,396128,2160,1074531952,
41728,393264,2304,1182720,41744,393456,2320,1182736,41760,393648,2336,1182752,41776,393840,2352,1182768,41792,394032,2496,1182912,41808,394224,2512,1182928,41824,394416,2528,1182944,41840,394608,2544,1182960,41856,394800,2688,1074924928,41872,394992,2704,1074924944,41888,395184,2720,1074924960,41904,395376,2736,1074924976,41920,395568,2880,1074925120,41936,395760,2896,1074925136,41952,395952,2912,1074925152,41968,396144,2928,1074925168,
41984,0,2147581952u,101376,42000,192,2147581968u,101392,42016,384,2147581984u,101408,42032,576,2147582000u,101424,42048,768,2147582144u,101568,42064,960,2147582160u,101584,42080,1152,2147582176u,101600,42096,1344,2147582192u,101616,42112,1536,98688,1073843584,42128,1728,98704,1073843600,42144,1920,98720,1073843616,42160,2112,98736,1073843632,42176,2304,98880,1073843776,42192,2496,98896,1073843792,42208,2688,98912,1073843808,42224,2880,98928,1073843824,
42240,16,99072,494592,42256,208,99088,494608,42272,400,99104,494624,42288,592,99120,494640,42304,784,99264,494784,42320,976,99280,494800,42336,1168,99296,494816,42352,1360,99312,494832,42368,1552,99456,1074236800,42384,1744,99472,1074236816,42400,1936,99488,1074236832,42416,2128,99504,1074236848,42432,2320,99648,1074236992,42448,2512,99664,1074237008,42464,2704,99680,1074237024,42480,2896,99696,1074237040,
42496,32,99840,887808,42512,224,99856,887824,42528,416,99872,887840,42544,608,99888,887856,42560,800,100032,888000,42576,992,100048,888016,42592,1184,100064,888032,42608,1376,100080,888048,42624,1568,100224,1074630016,42640,1760,100240,1074630032,42656,1952,100256,1074630048,42672,2144,100272,1074630064,42688,2336,100416,1074630208,42704,2528,100432,1074630224,42720,2720,100448,1074630240,42736,2912,100464,1074630256,
42752,48,100608,1281024,42768,240,100624,1281040,42784,432,100640,1281056,42800,624,100656,1281072,42816,816,100800,1281216,42832,1008,100816,1281232,42848,1200,100832,1281248,42864,1392,100848,1281264,42880,1584,100992,1075023232,42896,1776,101008,1075023248,42912,1968,101024,1075023264,42928,2160,101040,1075023280,42944,2352,101184,1075023424,42960,2544,101200,1075023440,42976,2736,101216,1075023456,42992,2928,101232,1075023472,
40960,1179648,2147680256u,199680,40976,1179840,2147680272u,199696,40992,1180032,2147680288u,199712,41008,1180224,2147680304u,199728,41024,1180416,2147680448u,199872,41040,1180608,2147680464u,199888,41056,1180800,2147680480u,199904,41072,1180992,2147680496u,199920,41088,1181184,196992,1073941888,41104,1181376,197008,1073941904,41120,1181568,197024,1073941920,41136,1181760,197040,1073941936,41152,1181952,197184,1073942080,41168,1182144,197200,1073942096,41184,1182336,197216,1073942112,41200,1182528,197232,1073942128,
41216,1179664,197376,592896,41232,1179856,197392,592912,41248,1180048,197408,592928,41264,1180240,197424,592944,41280,1180432,197568,593088,41296,1180624,197584,593104,41312,1180816,197600,593120,41328,1181008,197616,593136,41344,1181200,197760,1074335104,41360,1181392,197776,1074335120,41376,1181584,197792,1074335136,41392,1181776,197808,1074335152,41408,1181968,197952,1074335296,41424,1182160,197968,1074335312,41440,1182352,197984,1074335328,41456,1182544,198000,1074335344,
41472,1179680,198144,986112,41488,1179872,198160,986128,41504,1180064,198176,986144,41520,1180256,198192,986160,41536,1180448,198336,986304,41552,1180640,198352,986320,41568,1180832,198368,986336,41584,1181024,198384,986352,41600,1181216,198528,1074728320,41616,1181408,198544,1074728336,41632,1181600,198560,1074728352,41648,1181792,198576,1074728368,41664,1181984,198720,1074728512,41680,1182176,198736,1074728528,41696,1182368,198752,1074728544,41712,1182560,198768,1074728560,
41728,1179696,198912,1379328,41744,1179888,198928,1379344,41760,1180080,198944,1379360,41776,1180272,198960,1379376,41792,1180464,199104,1379520,41808,1180656,199120,1379536,41824,1180848,199136,1379552,41840,1181040,199152,1379568,41856,1181232,199296,1075121536,41872,1181424,199312,1075121552,41888,1181616,199328,1075121568,41904,1181808,199344,1075121584,41920,1182000,199488,1075121728,41936,1182192,199504,1075121744,41952,1182384,199520,1075121760,41968,1182576,199536,1075121776,
41984,786432,2147778560u,297984,42000,786624,2147778576u,298000,42016,786816,2147778592u,298016,42032,787008,2147778608u,298032,42048,787200,2147778752u,298176,42064,787392,2147778768u,298192,42080,787584,2147778784u,298208,42096,787776,2147778800u,298224,42112,787968,295296,1074040192,42128,788160,295312,1074040208,42144,788352,295328,1074040224,42160,788544,295344,1074040240,42176,788736,295488,1074040384,42192,788928,295504,1074040400,42208,789120,295520,1074040416,42224,789312,295536,1074040432,
42240,786448,295680,691200,42256,786640,295696,691216,42272,786832,295712,691232,42288,787024,295728,691248,42304,787216,295872,691392,42320,787408,295888,691408,42336,787600,295904,691424,42352,787792,295920,691440,42368,787984,296064,1074433408,42384,788176,296080,1074433424,42400,788368,296096,1074433440,42416,788560,296112,1074433456,42432,788752,296256,1074433600,42448,788944,296272,1074433616,42464,789136,296288,1074433632,42480,789328,296304,1074433648,
42496,786464,296448,1084416,42512,786656,296464,1084432,42528,786848,296480,1084448,42544,787040,296496,1084464,42560,787232,296640,1084608,42576,787424,296656,1084624,42592,787616,296672,1084640,42608,787808,296688,1084656,42624,788000,296832,1074826624,42640,788192,296848,1074826640,42656,788384,296864,1074826656,42672,788576,296880,1074826672,42688,788768,297024,1074826816,42704,788960,297040,1074826832,42720,789152,297056,1074826848,42736,789344,297072,1074826864,
42752,786480,297216,1477632,42768,786672,297232,1477648,42784,786864,297248,1477664,42800,787056,297264,1477680,42816,787248,297408,1477824,42832,787440,297424,1477840,42848,787632,297440,1477856,42864,787824,297456,1477872,42880,788016,297600,1075219840,42896,788208,297616,1075219856,42912,788400,297632,1075219872,42928,788592,297648,1075219888,42944,788784,297792,1075220032,42960,788976,297808,1075220048,42976,789168,297824,1075220064,42992,789360,297840,1075220080,
81920,139728,81920,8208,88672,140176,83280,8192,95424,140304,84640,8192,102176,140624,86000,8192,81924,139744,81936,8224,88676,140192,83296,8864,95428,140320,84656,8864,102180,140640,86016,8864,81928,139760,81968,8240,88680,140208,83328,8864,95432,140336,84688,8864,102184,140656,86048,8864,81932,139776,81952,8256,88684,140224,83312,8192,95436,140352,84672,8192,102188,140672,86032,8192,
81936,139792,81936,8464,88688,140112,83296,8400,95440,140240,84656,8336,102192,140688,86016,8272,81940,139808,81952,8480,88692,140128,83312,8416,95444,140256,84672,8352,102196,140704,86032,8288,81944,139824,81984,8496,88696,140144,83344,8432,95448,140272,84704,8368,102200,140720,86064,8304,81948,139840,81968,8512,88700,140160,83328,8448,95452,140288,84688,8384,102204,140736,86048,8320,
81952,139856,83280,8272,88704,140048,84640,8208,95456,140432,86000,8192,102208,140496,87360,8192,81956,139872,83296,8288,88708,140064,84656,8224,95460,140448,86016,8864,102212,140512,87376,8864,81960,139888,83328,8304,88712,140080,84688,8240,95464,140464,86048,8864,102216,140528,87408,8864,81964,139904,83312,8320,88716,140096,84672,8256,95468,140480,86032,8192,102220,140544,87392,8192,
81968,139920,83296,8864,88720,139984,84656,8464,95472,140368,86016,8400,102224,140560,87376,8336,81972,139936,83312,8192,88724,140000,84672,8480,95476,140384,86032,8416,102228,140576,87392,8352,81976,139952,83344,8192,88728,140016,84704,8496,95480,140400,86064,8432,102232,140592,87424,8368,81980,139968,83328,8864,88732,140032,84688,8512,95484,140416,86048,8448,102236,140608,87408,8384,
82240,133568,88672,10896,88992,134016,90032,10880,95744,134144,91392,10880,102496,134464,92752,10880,82244,133584,88688,10912,88996,134032,90048,11552,95748,134160,91408,11552,102500,134480,92768,11552,82248,133600,88720,10928,89000,134048,90080,11552,95752,134176,91440,11552,102504,134496,92800,11552,82252,133616,88704,10944,89004,134064,90064,10880,95756,134192,91424,10880,102508,134512,92784,10880,
82256,133632,88688,11152,89008,133952,90048,11088,95760,134080,91408,11024,102512,134528,92768,10960,82260,133648,88704,11168,89012,133968,90064,11104,95764,134096,91424,11040,102516,134544,92784,10976,82264,133664,88736,11184,89016,133984,90096,11120,95768,134112,91456,11056,102520,134560,92816,10992,82268,133680,88720,11200,89020,134000,90080,11136,95772,134128,91440,11072,102524,134576,92800,11008,
82272,133696,90032,10960,89024,133888,91392,10896,95776,134272,92752,10880,102528,134336,94112,10880,82276,133712,90048,10976,89028,133904,91408,10912,95780,134288,92768,11552,102532,134352,94128,11552,82280,133728,90080,10992,89032,133920,91440,10928,95784,134304,92800,11552,102536,134368,94160,11552,82284,133744,90064,11008,89036,133936,91424,10944,95788,134320,92784,10880,102540,134384,94144,10880,
82288,133760,90048,11552,89040,133824,91408,11152,95792,134208,92768,11088,102544,134400,94128,11024,82292,133776,90064,10880,89044,133840,91424,11168,95796,134224,92784,11104,102548,134416,94144,11040,82296,133792,90096,10880,89048,133856,91456,11184,95800,134240,92816,11120,102552,134432,94176,11056,82300,133808,90080,11552,89052,133872,91440,11200,95804,134256,92800,11136,102556,134448,94160,11072,
82560,152048,95424,13584,89312,152496,96784,13568,96064,152624,98144,13568,102816,152944,99504,13568,82564,152064,95440,13600,89316,152512,96800,14240,96068,152640,98160,14240,102820,152960,99520,14240,82568,152080,95472,13616,89320,152528,96832,14240,96072,152656,98192,14240,102824,152976,99552,14240,82572,152096,95456,13632,89324,152544,96816,13568,96076,152672,98176,13568,102828,152992,99536,13568,
82576,152112,95440,13840,89328,152432,96800,13776,96080,152560,98160,13712,102832,153008,99520,13648,82580,152128,95456,13856,89332,152448,96816,13792,96084,152576,98176,13728,102836,153024,99536,13664,82584,152144,95488,13872,89336,152464,96848,13808,96088,152592,98208,13744,102840,153040,99568,13680,82588,152160,95472,13888,89340,152480,96832,13824,96092,152608,98192,13760,102844,153056,99552,13696,
82592,152176,96784,13648,89344,152368,98144,13584,96096,152752,99504,13568,102848,152816,100864,13568,82596,152192,96800,13664,89348,152384,98160,13600,96100,152768,99520,14240,102852,152832,100880,14240,82600,152208,96832,13680,89352,152400,98192,13616,96104,152784,99552,14240,102856,152848,100912,14240,82604,152224,96816,13696,89356,152416,98176,13632,96108,152800,99536,13568,102860,152864,100896,13568,
82608,152240,96800,14240,89360,152304,98160,13840,96112,152688,99520,13776,102864,152880,100880,13712,82612,152256,96816,13568,89364,152320,98176,13856,96116,152704,99536,13792,102868,152896,100896,13728,82616,152272,96848,13568,89368,152336,98208,13872,96120,152720,99568,13808,102872,152912,100928,13744,82620,152288,96832,14240,89372,152352,98192,13888,96124,152736,99552,13824,102876,152928,100912,13760,
82880,145888,102176,16272,89632,146336,103536,16256,96384,146464,104896,16256,103136,146784,106256,16256,82884,145904,102192,16288,89636,146352,103552,16928,96388,146480,104912,16928,103140,146800,106272,16928,82888,145920,102224,16304,89640,146368,103584,16928,96392,146496,104944,16928,103144,146816,106304,16928,82892,145936,102208,16320,89644,146384,103568,16256,96396,146512,104928,16256,103148,146832,106288,16256,
82896,145952,102192,16528,89648,146272,103552,16464,96400,146400,104912,16400,103152,146848,106272,16336,82900,145968,102208,16544,89652,146288,103568,16480,96404,146416,104928,16416,103156,146864,106288,16352,82904,145984,102240,16560,89656,146304,103600,16496,96408,146432,104960,16432,103160,146880,106320,16368,82908,146000,102224,16576,89660,146320,103584,16512,96412,146448,104944,16448,103164,146896,106304,16384,
82912,146016,103536,16336,89664,146208,104896,16272,96416,146592,106256,16256,103168,146656,107616,16256,82916,146032,103552,16352,89668,146224,104912,16288,96420,146608,106272,16928,103172,146672,107632,16928,82920,146048,103584,16368,89672,146240,104944,16304,96424,146624,106304,16928,103176,146688,107664,16928,82924,146064,103568,16384,89676,146256,104928,16320,96428,146640,106288,16256,103180,146704,107648,16256,
82928,146080,103552,16928,89680,146144,104912,16528,96432,146528,106272,16464,103184,146720,107632,16400,82932,146096,103568,16256,89684,146160,104928,16544,96436,146544,106288,16480,103188,146736,107648,16416,82936,146112,103600,16256,89688,146176,104960,16560,96440,146560,106320,16496,103192,146752,107680,16432,82940,146128,103584,16928,89692,146192,104944,16576,96444,146576,106304,16512,103196,146768,107664,16448,
8336,108928,81984,393216,8272,115088,88736,393232,8208,121248,95488,393248,8864,127408,102240,393264,8352,108992,81988,393408,8288,115152,88740,393424,8224,121312,95492,393440,8528,127472,102244,393456,8368,109120,81992,393600,8304,115280,88744,393616,8240,121440,95496,393632,8528,127600,102248,393648,8384,109056,81996,393792,8320,115216,88748,393808,8256,121376,95500,393824,8864,127536,102252,393840,
8528,108944,83344,393984,8528,115104,90096,394000,8464,121264,96848,394016,8400,127424,103600,394032,8864,109008,83348,394176,8864,115168,90100,394192,8480,121328,96852,394208,8416,127488,103604,394224,8864,109136,83352,394368,8864,115296,90104,394384,8496,121456,96856,394400,8432,127616,103608,394416,8528,109072,83356,394560,8528,115232,90108,394576,8512,121392,96860,394592,8448,127552,103612,394608,
8400,108960,84704,394752,8336,115120,91456,394768,8272,121280,98208,394784,8208,127440,104960,394800,8416,109024,84708,394944,8352,115184,91460,394960,8288,121344,98212,394976,8224,127504,104964,394992,8432,109152,84712,395136,8368,115312,91464,395152,8304,121472,98216,395168,8240,127632,104968,395184,8448,109088,84716,395328,8384,115248,91468,395344,8320,121408,98220,395360,8256,127568,104972,395376,
8528,108976,86064,395520,8528,115136,92816,395536,8528,121296,99568,395552,8464,127456,106320,395568,8864,109040,86068,395712,8864,115200,92820,395728,8864,121360,99572,395744,8480,127520,106324,395760,8864,109168,86072,395904,8864,115328,92824,395920,8864,121488,99576,395936,8496,127648,106328,395952,8528,109104,86076,396096,8528,115264,92828,396112,8528,121424,99580,396128,8512,127584,106332,396144,
11024,109376,82304,0,10960,115536,89056,16,10896,121696,95808,32,11552,127856,102560,48,11040,109312,82308,192,10976,115472,89060,208,10912,121632,95812,224,11216,127792,102564,240,11056,109184,82312,384,10992,115344,89064,400,10928,121504,95816,416,11216,127664,102568,432,11072,109248,82316,576,11008,115408,89068,592,10944,121568,95820,608,11552,127728,102572,624,
11216,109392,83664,768,11216,115552,90416,784,11152,121712,97168,800,11088,127872,103920,816,11552,109328,83668,960,11552,115488,90420,976,11168,121648,97172,992,11104,127808,103924,1008,11552,109200,83672,1152,11552,115360,90424,1168,11184,121520,97176,1184,11120,127680,103928,1200,11216,109264,83676,1344,11216,115424,90428,1360,11200,121584,97180,1376,11136,127744,103932,1392,
11088,109408,85024,1536,11024,115568,91776,1552,10960,121728,98528,1568,10896,127888,105280,1584,11104,109344,85028,1728,11040,115504,91780,1744,10976,121664,98532,1760,10912,127824,105284,1776,11120,109216,85032,1920,11056,115376,91784,1936,10992,121536,98536,1952,10928,127696,105288,1968,11136,109280,85036,2112,11072,115440,91788,2128,11008,121600,98540,2144,10944,127760,105292,2160,
11216,109424,86384,2304,11216,115584,93136,2320,11216,121744,99888,2336,11152,127904,106640,2352,11552,109360,86388,2496,11552,115520,93140,2512,11552,121680,99892,2528,11168,127840,106644,2544,11552,109232,86392,2688,11552,115392,93144,2704,11552,121552,99896,2720,11184,127712,106648,2736,11216,109296,86396,2880,11216,115456,93148,2896,11216,121616,99900,2912,11200,127776,106652,2928,
13712,109504,82624,1179648,13648,115664,89376,1179664,13584,121824,96128,1179680,14240,127984,102880,1179696,13728,109440,82628,1179840,13664,115600,89380,1179856,13600,121760,96132,1179872,13904,127920,102884,1179888,13744,109568,82632,1180032,13680,115728,89384,1180048,13616,121888,96136,1180064,13904,128048,102888,1180080,13760,109632,82636,1180224,13696,115792,89388,1180240,13632,121952,96140,1180256,14240,128112,102892,1180272,
13904,109520,83984,1180416,13904,115680,90736,1180432,13840,121840,97488,1180448,13776,128000,104240,1180464,14240,109456,83988,1180608,14240,115616,90740,1180624,13856,121776,97492,1180640,13792,127936,104244,1180656,14240,109584,83992,1180800,14240,115744,90744,1180816,13872,121904,97496,1180832,13808,128064,104248,1180848,13904,109648,83996,1180992,13904,115808,90748,1181008,13888,121968,97500,1181024,13824,128128,104252,1181040,
13776,109536,85344,1181184,13712,115696,92096,1181200,13648,121856,98848,1181216,13584,128016,105600,1181232,13792,109472,85348,1181376,13728,115632,92100,1181392,13664,121792,98852,1181408,13600,127952,105604,1181424,13808,109600,85352,1181568,13744,115760,92104,1181584,13680,121920,98856,1181600,13616,128080,105608,1181616,13824,109664,85356,1181760,13760,115824,92108,1181776,13696,121984,98860,1181792,13632,128144,105612,1181808,
13904,109552,86704,1181952,13904,115712,93456,1181968,13904,121872,100208,1181984,13840,128032,106960,1182000,14240,109488,86708,1182144,14240,115648,93460,1182160,14240,121808,100212,1182176,13856,127968,106964,1182192,14240,109616,86712,1182336,14240,115776,93464,1182352,14240,121936,100216,1182368,13872,128096,106968,1182384,13904,109680,86716,1182528,13904,115840,93468,1182544,13904,122000,100220,1182560,13888,128160,106972,1182576,
16400,109824,82944,786432,16336,115984,89696,786448,16272,122144,96448,786464,16928,128304,103200,786480,16416,109888,82948,786624,16352,116048,89700,786640,16288,122208,96452,786656,16592,128368,103204,786672,16432,109760,82952,786816,16368,115920,89704,786832,16304,122080,96456,786848,16592,128240,103208,786864,16448,109696,82956,787008,16384,115856,89708,787024,16320,122016,96460,787040,16928,128176,103212,787056,
16592,109840,84304,787200,16592,116000,91056,787216,16528,122160,97808,787232,16464,128320,104560,787248,16928,109904,84308,787392,16928,116064,91060,787408,16544,122224,97812,787424,16480,128384,104564,787440,16928,109776,84312,787584,16928,115936,91064,787600,16560,122096,97816,787616,16496,128256,104568,787632,16592,109712,84316,787776,16592,115872,91068,787792,16576,122032,97820,787808,16512,128192,104572,787824,
16464,109856,85664,787968,16400,116016,92416,787984,16336,122176,99168,788000,16272,128336,105920,788016,16480,109920,85668,788160,16416,116080,92420,788176,16352,122240,99172,788192,16288,128400,105924,788208,16496,109792,85672,788352,16432,115952,92424,788368,16368,122112,99176,788384,16304,128272,105928,788400,16512,109728,85676,788544,16448,115888,92428,788560,16384,122048,99180,788576,16320,128208,105932,788592,
16592,109872,87024,788736,16592,116032,93776,788752,16592,122192,100528,788768,16528,128352,107280,788784,16928,109936,87028,788928,16928,116096,93780,788944,16928,122256,100532,788960,16544,128416,107284,788976,16928,109808,87032,789120,16928,115968,93784,789136,16928,122128,100536,789152,16560,128288,107288,789168,16592,109744,87036,789312,16592,115904,93788,789328,16592,122064,100540,789344,16576,128224,107292,789360,
81920,40960,40960,40960,88672,40976,40976,40968,95424,40992,40992,40976,102176,41008,41008,40984,108944,41024,41152,41760,115696,41040,41168,41768,122448,41056,41184,41776,129200,41072,41200,41784,135968,41088,41344,42560,142720,41104,41360,42568,149472,41120,41376,42576,156224,41136,41392,42584,81924,41152,41536,43360,88676,41168,41552,43368,95428,41184,41568,43376,102180,41200,41584,43384,
108948,41760,41728,44160,115700,41776,41744,44168,122452,41728,41760,44176,129204,41744,41776,44184,135972,41824,41920,44960,142724,41840,41936,44968,149476,41792,41952,44976,156228,41808,41968,44984,81928,41888,42112,45760,88680,41904,42128,45768,95432,41856,42144,45776,102184,41872,42160,45784,108952,41952,42304,46560,115704,41968,42320,46568,122456,41920,42336,46576,129208,41936,42352,46584,
135976,42560,42496,47104,142728,42576,42512,47112,149480,42592,42528,47120,156232,42608,42544,47128,81932,42496,42688,47904,88684,42512,42704,47912,95436,42528,42720,47920,102188,42544,42736,47928,108956,42688,42880,48704,115708,42704,42896,48712,122460,42720,42912,48720,129212,42736,42928,48728,135980,42624,43072,49504,142732,42640,43088,49512,149484,42656,43104,49520,156236,42672,43120,49528,
81936,43360,43264,50304,88688,43376,43280,50312,95440,43328,43296,50320,102192,43344,43312,50328,108960,43296,43456,51104,115712,43312,43472,51112,122464,43264,43488,51120,129216,43280,43504,51128,135984,43488,43648,51904,142736,43504,43664,51912,149488,43456,43680,51920,156240,43472,43696,51928,81940,43424,43840,52704,88692,43440,43856,52712,95444,43392,43872,52720,102196,43408,43888,52728,
108964,44160,59392,41344,115716,44176,59408,41352,122468,44192,59424,41360,129220,44208,59440,41368,135988,44224,59584,42144,142740,44240,59600,42152,149492,44256,59616,42160,156244,44272,59632,42168,81944,44032,59776,42944,88696,44048,59792,42952,95448,44064,59808,42960,102200,44080,59824,42968,108968,44096,59968,43744,115720,44112,59984,43752,122472,44128,60000,43760,129224,44144,60016,43768,
135992,44960,60160,44288,142744,44976,60176,44296,149496,44928,60192,44304,156248,44944,60208,44312,81948,45024,60352,45088,88700,45040,60368,45096,95452,44992,60384,45104,102204,45008,60400,45112,108972,44832,60544,45888,115724,44848,60560,45896,122476,44800,60576,45904,129228,44816,60592,45912,135996,44896,60736,46688,142748,44912,60752,46696,149500,44864,60768,46704,156252,44880,60784,46712,
81952,45760,60928,47488,88704,45776,60944,47496,95456,45792,60960,47504,102208,45808,60976,47512,108976,45696,61120,48288,115728,45712,61136,48296,122480,45728,61152,48304,129232,45744,61168,48312,136000,45632,61312,49088,142752,45648,61328,49096,149504,45664,61344,49104,156256,45680,61360,49112,81956,45568,61504,49888,88708,45584,61520,49896,95460,45600,61536,49904,102212,45616,61552,49912,
108980,46560,61696,50432,115732,46576,61712,50440,122484,46528,61728,50448,129236,46544,61744,50456,136004,46496,61888,51232,142756,46512,61904,51240,149508,46464,61920,51248,156260,46480,61936,51256,81960,46432,62080,52032,88712,46448,62096,52040,95464,46400,62112,52048,102216,46416,62128,52056,108984,46368,62272,52832,115736,46384,62288,52840,122488,46336,62304,52848,129240,46352,62320,52856,
136008,47104,40968,40960,142760,47120,40984,40968,149512,47136,41000,40976,156264,47152,41016,40984,81964,47168,41160,41760,88716,47184,41176,41768,95468,47200,41192,41776,102220,47216,41208,41784,108988,47232,41352,42560,115740,47248,41368,42568,122492,47264,41384,42576,129244,47280,41400,42584,136012,47296,41544,43360,142764,47312,41560,43368,149516,47328,41576,43376,156268,47344,41592,43384,
81968,47904,41736,44160,88720,47920,41752,44168,95472,47872,41768,44176,102224,47888,41784,44184,108992,47968,41928,44960,115744,47984,41944,44968,122496,47936,41960,44976,129248,47952,41976,44984,136016,48032,42120,45760,142768,48048,42136,45768,149520,48000,42152,45776,156272,48016,42168,45784,81972,48096,42312,46560,88724,48112,42328,46568,95476,48064,42344,46576,102228,48080,42360,46584,
108996,48704,42504,47104,115748,48720,42520,47112,122500,48736,42536,47120,129252,48752,42552,47128,136020,48640,42696,47904,142772,48656,42712,47912,149524,48672,42728,47920,156276,48688,42744,47928,81976,48832,42888,48704,88728,48848,42904,48712,95480,48864,42920,48720,102232,48880,42936,48728,109000,48768,43080,49504,115752,48784,43096,49512,122504,48800,43112,49520,129256,48816,43128,49528,
136024,49504,43272,50304,142776,49520,43288,50312,149528,49472,43304,50320,156280,49488,43320,50328,81980,49440,43464,51104,88732,49456,43480,51112,95484,49408,43496,51120,102236,49424,43512,51128,109004,49632,43656,51904,115756,49648,43672,51912,122508,49600,43688,51920,129260,49616,43704,51928,136028,49568,43848,52704,142780,49584,43864,52712,149532,49536,43880,52720,156284,49552,43896,52728,
81984,50304,59400,41344,88736,50320,59416,41352,95488,50336,59432,41360,102240,50352,59448,41368,109008,50368,59592,42144,115760,50384,59608,42152,122512,50400,59624,42160,129264,50416,59640,42168,136032,50176,59784,42944,142784,50192,59800,42952,149536,50208,59816,42960,156288,50224,59832,42968,81988,50240,59976,43744,88740,50256,59992,43752,95492,50272,60008,43760,102244,50288,60024,43768,
109012,51104,60168,44288,115764,51120,60184,44296,122516,51072,60200,44304,129268,51088,60216,44312,136036,51168,60360,45088,142788,51184,60376,45096,149540,51136,60392,45104,156292,51152,60408,45112,81992,50976,60552,45888,88744,50992,60568,45896,95496,50944,60584,45904,102248,50960,60600,45912,109016,51040,60744,46688,115768,51056,60760,46696,122520,51008,60776,46704,129272,51024,60792,46712,
136040,51904,60936,47488,142792,51920,60952,47496,149544,51936,60968,47504,156296,51952,60984,47512,81996,51840,61128,48288,88748,51856,61144,48296,95500,51872,61160,48304,102252,51888,61176,48312,109020,51776,61320,49088,115772,51792,61336,49096,122524,51808,61352,49104,129276,51824,61368,49112,136044,51712,61512,49888,142796,51728,61528,49896,149548,51744,61544,49904,156300,51760,61560,49912,
156300,52704,61704,50432,156300,52720,61720,50440,156300,52672,61736,50448,156300,52688,61752,50456,156300,52640,61896,51232,156300,52656,61912,51240,156300,52608,61928,51248,156300,52624,61944,51256,156300,52576,62088,52032,156300,52592,62104,52040,156300,52544,62120,52048,156300,52560,62136,52056,156300,52512,62280,52832,156300,52528,62296,52840,156300,52480,62312,52848,156300,52496,62328,52856,
40960,8208,8192,41088,40976,8880,8528,41096,40992,9552,8864,41104,41008,10224,9200,41112,41152,10896,9536,41888,41168,11568,9872,41896,41184,12240,10208,41904,41200,12912,10544,41912,41344,13584,10880,42688,41360,14256,11216,42696,41376,14928,11552,42704,41392,15600,11888,42712,41536,16272,12224,43488,41552,16944,12560,43496,41568,17616,12896,43504,41584,18288,13232,43512,
41728,18960,13568,44032,41744,19632,13904,44040,41760,20304,14240,44048,41776,20976,14576,44056,41920,21648,14912,44832,41936,22320,15248,44840,41952,22992,15584,44848,41968,23664,15920,44856,42112,24336,16256,45632,42128,25008,16592,45640,42144,25680,16928,45648,42160,26352,17264,45656,42304,27024,17600,46432,42320,27696,17936,46440,42336,28368,18272,46448,42352,29040,18608,46456,
42496,29712,18944,47232,42512,30384,19280,47240,42528,31056,19616,47248,42544,31728,19952,47256,42688,32400,20288,48032,42704,33072,20624,48040,42720,33744,20960,48048,42736,34416,21296,48056,42880,35088,21632,48832,42896,35760,21968,48840,42912,36432,22304,48848,42928,37104,22640,48856,43072,37776,22976,49632,43088,38448,23312,49640,43104,39120,23648,49648,43120,39792,23984,49656,
43264,8272,24320,50176,43280,8944,24656,50184,43296,9616,24992,50192,43312,10288,25328,50200,43456,10960,25664,50976,43472,11632,26000,50984,43488,12304,26336,50992,43504,12976,26672,51000,43648,13648,27008,51776,43664,14320,27344,51784,43680,14992,27680,51792,43696,15664,28016,51800,43840,16336,28352,52576,43856,17008,28688,52584,43872,17680,29024,52592,43888,18352,29360,52600,
59392,19024,29696,41472,59408,19696,30032,41480,59424,20368,30368,41488,59440,21040,30704,41496,59584,21712,31040,42272,59600,22384,31376,42280,59616,23056,31712,42288,59632,23728,32048,42296,59776,24400,32384,43072,59792,25072,32720,43080,59808,25744,33056,43088,59824,26416,33392,43096,59968,27088,33728,43872,59984,27760,34064,43880,60000,28432,34400,43888,60016,29104,34736,43896,
60160,29776,35072,44672,60176,30448,35408,44680,60192,31120,35744,44688,60208,31792,36080,44696,60352,32464,36416,45472,60368,33136,36752,45480,60384,33808,37088,45488,60400,34480,37424,45496,60544,35152,37760,46272,60560,35824,38096,46280,60576,36496,38432,46288,60592,37168,38768,46296,60736,37840,39104,47072,60752,38512,39440,47080,60768,39184,39776,47088,60784,39856,40112,47096,
60928,8336,40448,47616,60944,9008,40784,47624,60960,9680,162992,47632,60976,10352,162992,47640,61120,11024,162992,48416,61136,11696,162992,48424,61152,12368,162992,48432,61168,13040,162992,48440,61312,13712,162992,49216,61328,14384,162992,49224,61344,15056,162992,49232,61360,15728,162992,49240,61504,16400,162992,50016,61520,17072,162992,50024,61536,17744,162992,50032,61552,18416,162992,50040,
61696,19088,162992,50816,61712,19760,162992,50824,61728,20432,162992,50832,61744,21104,162992,50840,61888,21776,162992,51616,61904,22448,162992,51624,61920,23120,162992,51632,61936,23792,162992,51640,62080,24464,162992,52416,62096,25136,162992,52424,62112,25808,162992,52432,62128,26480,162992,52440,62272,27152,162992,53216,62288,27824,162992,53224,62304,28496,162992,53232,62320,29168,162992,53240,
40960,29840,162992,41088,40976,30512,162992,41096,40992,31184,162992,41104,41008,31856,162992,41112,41152,32528,162992,41888,41168,33200,162992,41896,41184,33872,162992,41904,41200,34544,162992,41912,41344,35216,162992,42688,41360,35888,162992,42696,41376,36560,162992,42704,41392,37232,162992,42712,41536,37904,162992,43488,41552,38576,162992,43496,41568,39248,162992,43504,41584,39920,162992,43512,
41728,8400,162992,44032,41744,9072,162992,44040,41760,9744,162992,44048,41776,10416,162992,44056,41920,11088,162992,44832,41936,11760,162992,44840,41952,12432,162992,44848,41968,13104,162992,44856,42112,13776,162992,45632,42128,14448,162992,45640,42144,15120,162992,45648,42160,15792,162992,45656,42304,16464,162992,46432,42320,17136,162992,46440,42336,17808,162992,46448,42352,18480,162992,46456,
42496,19152,162992,47232,42512,19824,162992,47240,42528,20496,162992,47248,42544,21168,162992,47256,42688,21840,162992,48032,42704,22512,162992,48040,42720,23184,162992,48048,42736,23856,162992,48056,42880,24528,162992,48832,42896,25200,162992,48840,42912,25872,162992,48848,42928,26544,162992,48856,43072,27216,162992,49632,43088,27888,162992,49640,43104,28560,162992,49648,43120,29232,162992,49656,
43264,29904,162992,50176,43280,30576,162992,50184,43296,31248,162992,50192,43312,31920,162992,50200,43456,32592,162992,50976,43472,33264,162992,50984,43488,33936,162992,50992,43504,34608,162992,51000,43648,35280,162992,51776,43664,35952,162992,51784,43680,36624,162992,51792,43696,37296,162992,51800,43840,37968,162992,52576,43856,38640,162992,52584,43872,39312,162992,52592,43888,39984,162992,52600,
59392,8464,162992,41472,59408,9136,162992,41480,59424,9808,162992,41488,59440,10480,162992,41496,59584,11152,162992,42272,59600,11824,162992,42280,59616,12496,162992,42288,59632,13168,162992,42296,59776,13840,162992,43072,59792,14512,162992,43080,59808,15184,162992,43088,59824,15856,162992,43096,59968,16528,162992,43872,59984,17200,162992,43880,60000,17872,162992,43888,60016,18544,162992,43896,
60160,19216,162992,44672,60176,19888,162992,44680,60192,20560,162992,44688,60208,21232,162992,44696,60352,21904,162992,45472,60368,22576,162992,45480,60384,23248,162992,45488,60400,23920,162992,45496,60544,24592,162992,46272,60560,25264,162992,46280,60576,25936,162992,46288,60592,26608,162992,46296,60736,27280,162992,47072,60752,27952,162992,47080,60768,28624,162992,47088,60784,29296,162992,47096,
60928,29968,162992,47616,60944,30640,162992,47624,60960,31312,162992,47632,60976,31984,162992,47640,61120,32656,162992,48416,61136,33328,162992,48424,61152,34000,162992,48432,61168,34672,162992,48440,61312,35344,162992,49216,61328,36016,162992,49224,61344,36688,162992,49232,61360,37360,162992,49240,61504,38032,162992,50016,61520,38704,162992,50024,61536,39376,162992,50032,61552,40048,162992,50040,
61696,40048,162992,50816,61712,40048,162992,50824,61728,40048,162992,50832,61744,40048,162992,50840,61888,40048,162992,51616,61904,40048,162992,51624,61920,40048,162992,51632,61936,40048,162992,51640,62080,40048,162992,52416,62096,40048,162992,52424,62112,40048,162992,52432,62128,40048,162992,52440,62272,40048,162992,53216,62288,40048,162992,53224,62304,40048,162992,53232,62320,40048,162992,53240,
16256,0,0,0,16256,0,0,0,16256,0,0,0,1065353216,0,0,0,0,0,16256,0,0,0,16256,0,0,0,16256,0,0,0,1065353216,0,0,16256,0,0,0,16256,0,0,0,16256,0,0,0,1065353216,0,0,0,0,0,16256,0,0,0,16256,0,0,0,16256,0,0,0,1065353216,
0,0,0,0,0,0,0,0,0,0,0,0,0,0,0,0,0,0,0,0,0,0,0,0,0,0,0,0,0,0,0,0,0,0,0,0,0,0,0,0,0,0,0,0,0,0,0,0,0,0,0,0,0,0,0,0,0,0,0,0,0,0,0,0,
0,0,0,0,0,0,0,0,0,0,0,0,0,0,0,0,0,0,0,0,0,0,0,0,0,0,0,0,0,0,0,0,0,0,0,0,0,0,0,0,0,0,0,0,0,0,0,0,0,0,0,0,0,0,0,0,0,0,0,0,0,0,0,0,
16256,0,0,0,16256,0,0,0,16256,0,0,0,1065353216,0,0,0,0,0,16256,0,0,0,16256,0,0,0,16256,0,0,0,1065353216,0,0,16256,0,0,0,16256,0,0,0,16256,0,0,0,1065353216,0,0,0,0,0,16256,0,0,0,16256,0,0,0,16256,0,0,0,1065353216,
16256,0,0,0,16256,0,0,0,16256,0,0,0,1065353216,0,0,0,0,0,16256,0,0,0,16256,0,0,0,16256,0,0,0,1065353216,0,0,16256,0,0,0,16256,0,0,0,16256,0,0,0,1065353216,0,0,0,0,0,16256,0,0,0,16256,0,0,0,16256,0,0,0,1065353216,
0,0,0,0,0,0,0,0,0,0,0,0,0,0,0,0,0,0,0,0,0,0,0,0,0,0,0,0,0,0,0,0,0,0,0,0,0,0,0,0,0,0,0,0,0,0,0,0,0,0,0,0,0,0,0,0,0,0,0,0,0,0,0,0,
0,0,0,0,0,0,0,0,0,0,0,0,0,0,0,0,0,0,0,0,0,0,0,0,0,0,0,0,0,0,0,0,0,0,0,0,0,0,0,0,0,0,0,0,0,0,0,0,0,0,0,0,0,0,0,0,0,0,0,0,0,0,0,0,
16256,0,0,0,16256,0,0,0,16256,0,0,0,1065353216,0,0,0,0,0,16256,0,0,0,16256,0,0,0,16256,0,0,0,1065353216,0,0,16256,0,0,0,16256,0,0,0,16256,0,0,0,1065353216,0,0,0,0,0,16256,0,0,0,16256,0,0,0,16256,0,0,0,1065353216,
16256,0,0,0,16256,0,0,0,16256,0,0,0,1065353216,0,0,0,0,0,16256,0,0,0,16256,0,0,0,16256,0,0,0,1065353216,0,0,16256,0,0,0,16256,0,0,0,16256,0,0,0,1065353216,0,0,0,0,0,16256,0,0,0,16256,0,0,0,16256,0,0,0,1065353216,
0,0,0,0,0,0,0,0,0,0,0,0,0,0,0,0,0,0,0,0,0,0,0,0,0,0,0,0,0,0,0,0,0,0,0,0,0,0,0,0,0,0,0,0,0,0,0,0,0,0,0,0,0,0,0,0,0,0,0,0,0,0,0,0,
0,0,0,0,0,0,0,0,0,0,0,0,0,0,0,0,0,0,0,0,0,0,0,0,0,0,0,0,0,0,0,0,0,0,0,0,0,0,0,0,0,0,0,0,0,0,0,0,0,0,0,0,0,0,0,0,0,0,0,0,0,0,0,0,
16256,0,0,0,16256,0,0,0,16256,0,0,0,1065353216,0,0,0,0,0,16256,0,0,0,16256,0,0,0,16256,0,0,0,1065353216,0,0,16256,0,0,0,16256,0,0,0,16256,0,0,0,1065353216,0,0,0,0,0,16256,0,0,0,16256,0,0,0,16256,0,0,0,1065353216,
16256,0,0,0,16256,0,0,0,16256,0,0,0,1065353216,0,0,0,0,0,16256,0,0,0,16256,0,0,0,16256,0,0,0,1065353216,0,0,16256,0,0,0,16256,0,0,0,16256,0,0,0,1065353216,0,0,0,0,0,16256,0,0,0,16256,0,0,0,16256,0,0,0,1065353216,
0,0,0,0,0,0,0,0,0,0,0,0,0,0,0,0,0,0,0,0,0,0,0,0,0,0,0,0,0,0,0,0,0,0,0,0,0,0,0,0,0,0,0,0,0,0,0,0,0,0,0,0,0,0,0,0,0,0,0,0,0,0,0,0,
0,0,0,0,0,0,0,0,0,0,0,0,0,0,0,0,0,0,0,0,0,0,0,0,0,0,0,0,0,0,0,0,0,0,0,0,0,0,0,0,0,0,0,0,0,0,0,0,0,0,0,0,0,0,0,0,0,0,0,0,0,0,0,0,
16256,0,0,0,16256,0,0,0,16256,0,0,0,1065353216,0,0,0,0,0,16256,0,0,0,16256,0,0,0,16256,0,0,0,1065353216,0,0,16256,0,0,0,16256,0,0,0,16256,0,0,0,1065353216,0,0,0,0,0,16256,0,0,0,16256,0,0,0,16256,0,0,0,1065353216,
0,0,0,889206016,0,0,0,889206016,0,0,0,0,0,0,0,0,0,889206016,0,889206016,0,889206016,0,889206016,0,0,0,0,0,0,0,0,0,889206016,0,0,0,889206016,0,0,0,0,0,0,0,0,0,0,0,0,0,889206016,0,0,0,889206016,0,0,0,0,0,0,0,0,
0,0,0,889206016,0,0,0,889206016,0,0,0,0,0,0,0,0,0,889206016,0,889206016,0,889206016,0,889206016,0,0,0,0,0,0,0,0,0,889206016,0,0,0,889206016,0,0,0,0,0,0,0,0,0,0,0,0,0,889206016,0,0,0,889206016,0,0,0,0,0,0,0,0,
0,0,0,889206016,0,0,0,889206016,0,0,0,0,0,0,0,0,0,889206016,0,889206016,0,889206016,0,889206016,0,0,0,0,0,0,0,0,0,889206016,0,0,0,889206016,0,0,0,0,0,0,0,0,0,0,0,0,0,889206016,0,0,0,889206016,0,0,0,0,0,0,0,0,
0,0,0,889206016,0,0,0,889206016,0,0,0,0,0,0,0,0,0,889206016,0,889206016,0,889206016,0,889206016,0,0,0,0,0,0,0,0,0,889206016,0,0,0,889206016,0,0,0,0,0,0,0,0,0,0,0,0,0,889206016,0,0,0,889206016,0,0,0,0,0,0,0,0,
0,0,0,889206016,0,0,0,889206016,0,0,0,0,0,0,0,0,0,889206016,0,889206016,0,889206016,0,889206016,0,0,0,0,0,0,0,0,0,889206016,0,0,0,889206016,0,0,0,0,0,0,0,0,0,0,0,0,0,889206016,0,0,0,889206016,0,0,0,0,0,0,0,0,
0,0,0,889206016,0,0,0,889206016,0,0,0,0,0,0,0,0,0,889206016,0,889206016,0,889206016,0,889206016,0,0,0,0,0,0,0,0,0,889206016,0,0,0,889206016,0,0,0,0,0,0,0,0,0,0,0,0,0,889206016,0,0,0,889206016,0,0,0,0,0,0,0,0,
0,0,0,889206016,0,0,0,889206016,0,0,0,0,0,0,0,0,0,889206016,0,889206016,0,889206016,0,889206016,0,0,0,0,0,0,0,0,0,889206016,0,0,0,889206016,0,0,0,0,0,0,0,0,0,0,0,0,0,889206016,0,0,0,889206016,0,0,0,0,0,0,0,0,
0,0,0,889206016,0,0,0,889206016,0,0,0,0,0,0,0,0,0,889206016,0,889206016,0,889206016,0,889206016,0,0,0,0,0,0,0,0,0,889206016,0,0,0,889206016,0,0,0,0,0,0,0,0,0,0,0,0,0,889206016,0,0,0,889206016,0,0,0,0,0,0,0,0,
0,0,0,889206016,0,0,0,889206016,0,0,0,0,0,0,0,0,0,889206016,0,889206016,0,889206016,0,889206016,0,0,0,0,0,0,0,0,0,889206016,0,0,0,889206016,0,0,0,0,0,0,0,0,0,0,0,0,0,889206016,0,0,0,889206016,0,0,0,0,0,0,0,0,
0,0,0,889206016,0,0,0,889206016,0,0,0,0,0,0,0,0,0,889206016,0,889206016,0,889206016,0,889206016,0,0,0,0,0,0,0,0,0,889206016,0,0,0,889206016,0,0,0,0,0,0,0,0,0,0,0,0,0,889206016,0,0,0,889206016,0,0,0,0,0,0,0,0,
0,0,0,889206016,0,0,0,889206016,0,0,0,0,0,0,0,0,0,889206016,0,889206016,0,889206016,0,889206016,0,0,0,0,0,0,0,0,0,889206016,0,0,0,889206016,0,0,0,0,0,0,0,0,0,0,0,0,0,889206016,0,0,0,889206016,0,0,0,0,0,0,0,0,
0,0,0,889206016,0,0,0,889206016,0,0,0,0,0,0,0,0,0,889206016,0,889206016,0,889206016,0,889206016,0,0,0,0,0,0,0,0,0,889206016,0,0,0,889206016,0,0,0,0,0,0,0,0,0,0,0,0,0,889206016,0,0,0,889206016,0,0,0,0,0,0,0,0,
0,0,0,889206016,0,0,0,889206016,0,0,0,0,0,0,0,0,0,889206016,0,889206016,0,889206016,0,889206016,0,0,0,0,0,0,0,0,0,889206016,0,0,0,889206016,0,0,0,0,0,0,0,0,0,0,0,0,0,889206016,0,0,0,889206016,0,0,0,0,0,0,0,0,
0,0,0,889206016,0,0,0,889206016,0,0,0,0,0,0,0,0,0,889206016,0,889206016,0,889206016,0,889206016,0,0,0,0,0,0,0,0,0,889206016,0,0,0,889206016,0,0,0,0,0,0,0,0,0,0,0,0,0,889206016,0,0,0,889206016,0,0,0,0,0,0,0,0,
0,0,0,889206016,0,0,0,889206016,0,0,0,0,0,0,0,0,0,889206016,0,889206016,0,889206016,0,889206016,0,0,0,0,0,0,0,0,0,889206016,0,0,0,889206016,0,0,0,0,0,0,0,0,0,0,0,0,0,889206016,0,0,0,889206016,0,0,0,0,0,0,0,0,
0,0,0,889206016,0,0,0,889206016,0,0,0,0,0,0,0,0,0,889206016,0,889206016,0,889206016,0,889206016,0,0,0,0,0,0,0,0,0,889206016,0,0,0,889206016,0,0,0,0,0,0,0,0,0,0,0,0,0,889206016,0,0,0,889206016,0,0,0,0,0,0,0,0,
0,889206016,0,40960,0,889206016,0,40976,0,0,0,40992,0,0,0,41008,0,0,0,41024,0,0,0,41040,0,0,0,41056,0,0,0,41072,0,889206016,0,41088,0,889206016,0,41104,0,0,0,41120,0,0,0,41136,0,889206016,0,41152,0,889206016,0,41168,0,0,0,41184,0,0,0,41200,
0,889206016,0,41216,0,889206016,0,41232,0,0,0,41248,0,0,0,41264,0,0,0,41280,0,0,0,41296,0,0,0,41312,0,0,0,41328,0,889206016,0,41344,0,889206016,0,41360,0,0,0,41376,0,0,0,41392,0,889206016,0,41408,0,889206016,0,41424,0,0,0,41440,0,0,0,41456,
0,889206016,0,41472,0,889206016,0,41488,0,0,0,41504,0,0,0,41520,0,0,0,41536,0,0,0,41552,0,0,0,41568,0,0,0,41584,0,889206016,0,41600,0,889206016,0,41616,0,0,0,41632,0,0,0,41648,0,889206016,0,41664,0,889206016,0,41680,0,0,0,41696,0,0,0,41712,
0,889206016,0,41728,0,889206016,0,41744,0,0,0,41760,0,0,0,41776,0,0,0,41792,0,0,0,41808,0,0,0,41824,0,0,0,41840,0,889206016,0,41856,0,889206016,0,41872,0,0,0,41888,0,0,0,41904,0,889206016,0,41920,0,889206016,0,41936,0,0,0,41952,0,0,0,41968,
0,889206016,0,41984,0,889206016,0,42000,0,0,0,42016,0,0,0,42032,0,0,0,42048,0,0,0,42064,0,0,0,42080,0,0,0,42096,0,889206016,0,42112,0,889206016,0,42128,0,0,0,42144,0,0,0,42160,0,889206016,0,42176,0,889206016,0,42192,0,0,0,42208,0,0,0,42224,
0,889206016,0,42240,0,889206016,0,42256,0,0,0,42272,0,0,0,42288,0,0,0,42304,0,0,0,42320,0,0,0,42336,0,0,0,42352,0,889206016,0,42368,0,889206016,0,42384,0,0,0,42400,0,0,0,42416,0,889206016,0,42432,0,889206016,0,42448,0,0,0,42464,0,0,0,42480,
0,889206016,0,42496,0,889206016,0,42512,0,0,0,42528,0,0,0,42544,0,0,0,42560,0,0,0,42576,0,0,0,42592,0,0,0,42608,0,889206016,0,42624,0,889206016,0,42640,0,0,0,42656,0,0,0,42672,0,889206016,0,42688,0,889206016,0,42704,0,0,0,42720,0,0,0,42736,
0,889206016,0,42752,0,889206016,0,42768,0,0,0,42784,0,0,0,42800,0,0,0,42816,0,0,0,42832,0,0,0,42848,0,0,0,42864,0,889206016,0,42880,0,889206016,0,42896,0,0,0,42912,0,0,0,42928,0,889206016,0,42944,0,889206016,0,42960,0,0,0,42976,0,0,0,42992,
0,889206016,0,43008,0,889206016,0,43024,0,0,0,43040,0,0,0,43056,0,0,0,43072,0,0,0,43088,0,0,0,43104,0,0,0,43120,0,889206016,0,43136,0,889206016,0,43152,0,0,0,43168,0,0,0,43184,0,889206016,0,43200,0,889206016,0,43216,0,0,0,43232,0,0,0,43248,
0,889206016,0,43264,0,889206016,0,43280,0,0,0,43296,0,0,0,43312,0,0,0,43328,0,0,0,43344,0,0,0,43360,0,0,0,43376,0,889206016,0,43392,0,889206016,0,43408,0,0,0,43424,0,0,0,43440,0,889206016,0,43456,0,889206016,0,43472,0,0,0,43488,0,0,0,43504,
0,889206016,0,43520,0,889206016,0,43536,0,0,0,43552,0,0,0,43568,0,0,0,43584,0,0,0,43600,0,0,0,43616,0,0,0,43632,0,889206016,0,43648,0,889206016,0,43664,0,0,0,43680,0,0,0,43696,0,889206016,0,43712,0,889206016,0,43728,0,0,0,43744,0,0,0,43760,
0,889206016,0,43776,0,889206016,0,43792,0,0,0,43808,0,0,0,43824,0,0,0,43840,0,0,0,43856,0,0,0,43872,0,0,0,43888,0,889206016,0,43904,0,889206016,0,43920,0,0,0,43936,0,0,0,43952,0,889206016,0,43968,0,889206016,0,43984,0,0,0,44000,0,0,0,44016,
0,889206016,0,44032,0,889206016,0,44048,0,0,0,44064,0,0,0,44080,0,0,0,44096,0,0,0,44112,0,0,0,44128,0,0,0,44144,0,889206016,0,44160,0,889206016,0,44176,0,0,0,44192,0,0,0,44208,0,889206016,0,44224,0,889206016,0,44240,0,0,0,44256,0,0,0,44272,
0,889206016,0,44288,0,889206016,0,44304,0,0,0,44320,0,0,0,44336,0,0,0,44352,0,0,0,44368,0,0,0,44384,0,0,0,44400,0,889206016,0,44416,0,889206016,0,44432,0,0,0,44448,0,0,0,44464,0,889206016,0,44480,0,889206016,0,44496,0,0,0,44512,0,0,0,44528,
0,889206016,0,44544,0,889206016,0,44560,0,0,0,44576,0,0,0,44592,0,0,0,44608,0,0,0,44624,0,0,0,44640,0,0,0,44656,0,889206016,0,44672,0,889206016,0,44688,0,0,0,44704,0,0,0,44720,0,889206016,0,44736,0,889206016,0,44752,0,0,0,44768,0,0,0,44784,
0,889206016,0,44800,0,889206016,0,44816,0,0,0,44832,0,0,0,44848,0,0,0,44864,0,0,0,44880,0,0,0,44896,0,0,0,44912,0,889206016,0,44928,0,889206016,0,44944,0,0,0,44960,0,0,0,44976,0,889206016,0,44992,0,889206016,0,45008,0,0,0,45024,0,0,0,45040};

DI float bf_lo(u32 v) { return __uint_as_float(v << 16); }
DI u32 pk_bf16(float a, float b) { f32x2 t = {a, b}; return __builtin_bit_cast(u32, __builtin_convertvector(t, bf16x2)); }
DI u32 pk_f16(float a, float b) { f32x2 t = {a, b}; return __builtin_bit_cast(u32, __builtin_convertvector(t, f16x2)); }
DI u32x4 srd(const void* p, u32 n) { return (u32x4){(u32)(size_t)p, (u32)((size_t)p >> 32), n, 0x00020000u}; }

DI void slow_path(const u16* __restrict__ x, const u16* __restrict__ dwl, const u16* __restrict__ dws, const u16* __restrict__ w1,
                  const u16* __restrict__ w2, u16* __restrict__ out, float* sm, int t, int tx, int ty0) {
  float* o = sm;
  float* g = sm + 8 * CH;
  float* st = g + 8 * HID;
#pragma unroll 1
  for (int b = 0; b < 128; ++b) {
    const int y = ty0 * 16 + (b >> 1), x0 = tx * 16 + (b & 1) * 8;
    __syncthreads();
#pragma unroll 1
    for (int i = t; i < 8 * CH; i += 256) {
      const int p = i / CH, c = i - p * CH;
      float acc = 0.f;
#pragma unroll 1
      for (int a = 0; a < 5; ++a)
#pragma unroll 1
        for (int e = 0; e < 5; ++e) {
          const int yy = y + a - 2, xc = x0 + p + e - 2;
          if (yy < 0 || yy >= IMG || xc < 0 || xc >= IMG) continue;
          const float xv = bf_lo(x[(size_t)(yy * IMG + xc) * CH + c]);
          acc = __builtin_fmaf(bf_lo(dws[(a * 5 + e) * CH + c]), xv, acc);
          if (a >= 1 && a <= 3 && e >= 1 && e <= 3) acc = __builtin_fmaf(bf_lo(dwl[((a - 1) * 3 + (e - 1)) * CH + c]), xv, acc);
        }
      o[i] = acc;
    }
    __syncthreads();
    if (t < 8) {
      float m = 0.f, v = 0.f;
#pragma unroll 1
      for (int c = 0; c < CH; ++c) m += o[t * CH + c];
      m *= (1.0f / CH);
#pragma unroll 1
      for (int c = 0; c < CH; ++c) { const float d = o[t * CH + c] - m; v = __builtin_fmaf(d, d, v); }
      st[2 * t] = m; st[2 * t + 1] = __builtin_amdgcn_rsqf(v * (1.0f / CH) + 1e-5f);
    }
    __syncthreads();
#pragma unroll 1
    for (int i = t; i < 8 * HID; i += 256) {
      const int p = i / HID, e = i - p * HID;
      const float m = st[2 * p], r = st[2 * p + 1];
      float u = 0.f;
#pragma unroll 1
      for (int c = 0; c < CH; ++c) u = __builtin_fmaf((o[p * CH + c] - m) * r, bf_lo(w1[c * HID + e]), u);
      const float z = u * __builtin_fmaf(u * u, GC3, GC1);
      g[i] = u * __builtin_amdgcn_rcpf(1.0f + __builtin_amdgcn_exp2f(z));
    }
    __syncthreads();
#pragma unroll 1
    for (int i = t; i < 8 * CH; i += 256) {
      const int p = i / CH, c = i - p * CH;
      float acc = 0.f;
#pragma unroll 1
      for (int e = 0; e < HID; ++e) acc = __builtin_fmaf(g[p * HID + e], bf_lo(w2[e * CH + c]), acc);
      const size_t idx = (size_t)(y * IMG + x0 + p) * CH + c;
      out[idx] = (u16)pk_bf16(bf_lo(x[idx]) + acc, 0.f);
    }
  }
}

__global__ __launch_bounds__(256, 1) void replk_kernel(const u16* __restrict__ x, const u16* __restrict__ dwl,
    const u16* __restrict__ dws, const u16* __restrict__ w1, const u16* __restrict__ w2, u16* __restrict__ out
    ) {
  __shared__ __attribute__((aligned(16))) char smem[L_TOTAL];
  __attribute__((address_space(3))) char* smem3 = (__attribute__((address_space(3))) char*)smem;
  const int tid = threadIdx.x, lane = tid & 63, w = __builtin_amdgcn_readfirstlane(tid >> 6), bid = blockIdx.x;
  const int tx = bid >> 3, ty0 = (bid & 7) * 4;
  const int Y0 = ty0 * 16, X0 = tx * 16;

  const int t240 = min(tid, 239), px = (t240 * 5462) >> 16, tr = (t240 * 1366) >> 16;
  u32x8 PA;
  PA[0] = ((tx == 0 && px < 2) || (tx == 31 && px >= 18)) ? 0x80000000u : (u32)((Y0 * IMG + X0 - 2) * PXB + t240 * 16);
  PA[1] = (u32)((8 * (tid >> 6) + ((tid >> 4) & 3)) * (HID * 2) + (tid & 15) * 16);
  PA[2] = (u32)((tid >> 7) * 36864 + ((tid >> 6) & 1) * 9216 + lane * 16);
  PA[3] = (u32)(t240 * 4 + tr * (4 * PXB));
  PA[4] = (tr >= 1 && tr <= 3) ? (u32)(t240 * 4 + tr * (2 * PXB) - 3 * PXB) : 0x80000000u;
  PA[5] = (u32)(tid * 16);
  {
    u64 pc; u32 clen;
    asm volatile("s_getpc_b64 %0\n.Lreplk_pc0:\n s_mov_b32 %1, .Lreplk_end-.Lreplk_pc0-4" : "=s"(pc), "=s"(clen));
    const u64 base = w == 0 ? (u64)w1 : w == 1 ? (u64)w2 : pc;
    const u32 line = w < 2 ? (u32)(tx * 18 + min(lane, 17)) : (u32)(((tx & 15) * 2 + (w & 1)) * 16 + (lane & 15));
    const u64 pa = base + min(line * 128u, w < 2 ? 0x20000u : clen);
    PA[6] = (u32)pa; PA[7] = (u32)(pa >> 32);
  }
  const u32x4 rx = srd(x, XBYTES);
  u32x4 rt = srd((const char*)x - 2 * ROWB, ty0 ? XBYTES : 0u);
  const u32x8 rw = {(u32)(size_t)w1, (u32)((size_t)w1 >> 32), CH * HID * 2, 0x00020000u, (u32)(size_t)w2, (u32)((size_t)w2 >> 32), CH * HID * 2, 0x00020000u};
  const u32x8 rd = {(u32)(size_t)dws, (u32)((size_t)dws >> 32), 25 * PXB, 0x00020000u, (u32)(size_t)dwl, (u32)((size_t)dwl >> 32), 9 * PXB, 0x00020000u};
  u32x8 S = {MU_PK, CC_PK, __float_as_uint(GAMMA * 0x1p-106f), (u32)(w >> 1), 0u, (u32)(((Y0 + 18 + w) * IMG + X0 - 2) * PXB - (L_W + (w & 1) * 1024) * 8), (u32)(L_XT - L_H + w * (XT_CG - GS)), (u32)(L_XT + w * 256 - (L_H + w * GS) / 4)};
  const u32x4 T = {(u32)(size_t)CTAB, (u32)((size_t)CTAB >> 32), (u32)(((Y0 + 14) * IMG + X0 - 2) * PXB), (u32)((Y0 * IMG + X0) * PXB)};
  u32 flg = (u32)(size_t)smem3 ? 1u : 0u;
  asm volatile(
    ".p2align 3\n"
    "s_add_u32 vcc_lo, s96, 16384\n"
    "s_addc_u32 vcc_hi, s97, 0\n"
    "global_load_dwordx4 v[216:219], v213, vcc\n"
    "s_add_u32 vcc_lo, s96, 20480\n"
    "s_addc_u32 vcc_hi, s97, 0\n"
    "global_load_dwordx4 v[220:223], v213, vcc\n"
    "buffer_load_dword v224, v211, s[88:91], 0 offen offset:0\n"
    "buffer_load_dword v225, v211, s[88:91], 0 offen offset:192\n"
    "buffer_load_dword v226, v211, s[88:91], 0 offen offset:384\n"
    "buffer_load_dword v227, v211, s[88:91], 0 offen offset:576\n"
    "buffer_load_dword v228, v211, s[88:91], 0 offen offset:768\n"
    "buffer_load_dword v229, v212, s[92:95], 0 offen offset:0\n"
    "buffer_load_dword v230, v212, s[92:95], 0 offen offset:192\n"
    "buffer_load_dword v231, v212, s[92:95], 0 offen offset:384\n"
    "v_mov_b32_e64 v88, 0\n"
    "v_mov_b32_e64 v89, 0\n"
    "v_mov_b32_e64 v90, 0\n"
    "v_mov_b32_e64 v91, 0\n"
    "v_mov_b32_e64 v59, 0\n"
    "v_mov_b32_e64 v60, 0\n"
    "v_mov_b32_e64 v67, 0\n"
    "v_mov_b32_e64 v68, 0\n"
    "v_mov_b32_e64 v75, 0\n"
    "v_mov_b32_e64 v76, 0\n"
    "v_mov_b32_e64 v83, 0\n"
    "v_mov_b32_e64 v84, 0\n"
    "v_mov_b32_e32 v36, 0x75089ba4\n"
    "v_mov_b32_e32 v38, 0xbdd2d3e8\n"
    "v_mov_b32_e32 v32, 0xc0135761\n"
    "v_lshlrev_b32_e64 v34, 16, v213\n"
    "v_lshlrev_b32_e64 v234, 1, v213\n"
    "v_or_b32_e32 v0, 0x8000, v34\n"
    "v_or_b32_e32 v1, 0x18000, v34\n"
    "v_or_b32_e32 v2, 0x28000, v34\n"
    "v_or_b32_e32 v3, 0x38000, v34\n"
    "v_or_b32_e32 v4, 0x48000, v34\n"
    "v_or_b32_e32 v5, 0x58000, v34\n"
    "v_or_b32_e32 v6, 0x68000, v34\n"
    "v_or_b32_e32 v7, 0x78000, v34\n"
    "v_pk_mul_f32 v[0:1], v[0:1], v[36:37] op_sel_hi:[1,0]\n"
    "v_pk_mul_f32 v[2:3], v[2:3], v[36:37] op_sel_hi:[1,0]\n"
    "v_pk_mul_f32 v[4:5], v[4:5], v[36:37] op_sel_hi:[1,0]\n"
    "v_pk_mul_f32 v[6:7], v[6:7], v[36:37] op_sel_hi:[1,0]\n"
    "v_pk_mul_f32 v[8:9], v[0:1], v[0:1]\n"
    "v_pk_mul_f32 v[10:11], v[2:3], v[2:3]\n"
    "v_pk_mul_f32 v[12:13], v[4:5], v[4:5]\n"
    "v_pk_mul_f32 v[14:15], v[6:7], v[6:7]\n"
    "v_pk_fma_f32 v[8:9], v[8:9], v[38:39], v[32:33] op_sel_hi:[1,0,0]\n"
    "v_pk_fma_f32 v[10:11], v[10:11], v[38:39], v[32:33] op_sel_hi:[1,0,0]\n"
    "v_pk_fma_f32 v[12:13], v[12:13], v[38:39], v[32:33] op_sel_hi:[1,0,0]\n"
    "v_pk_fma_f32 v[14:15], v[14:15], v[38:39], v[32:33] op_sel_hi:[1,0,0]\n"
    "v_pk_mul_f32 v[8:9], v[8:9], v[0:1]\n"
    "v_pk_mul_f32 v[10:11], v[10:11], v[2:3]\n"
    "v_pk_mul_f32 v[12:13], v[12:13], v[4:5]\n"
    "v_pk_mul_f32 v[14:15], v[14:15], v[6:7]\n"
    "v_exp_f32_e64 v8, v8\n"
    "v_exp_f32_e64 v9, v9\n"
    "v_exp_f32_e64 v10, v10\n"
    "v_exp_f32_e64 v11, v11\n"
    "v_exp_f32_e64 v12, v12\n"
    "v_exp_f32_e64 v13, v13\n"
    "v_exp_f32_e64 v14, v14\n"
    "v_exp_f32_e64 v15, v15\n"
    "v_pk_add_f32 v[16:17], v[8:9], 1.0 op_sel_hi:[1,0]\n"
    "v_pk_add_f32 v[18:19], v[10:11], 1.0 op_sel_hi:[1,0]\n"
    "v_pk_add_f32 v[20:21], v[12:13], 1.0 op_sel_hi:[1,0]\n"
    "v_pk_add_f32 v[22:23], v[14:15], 1.0 op_sel_hi:[1,0]\n"
    "v_rcp_f32_e64 v16, v16\n"
    "v_rcp_f32_e64 v17, v17\n"
    "v_rcp_f32_e64 v18, v18\n"
    "v_rcp_f32_e64 v19, v19\n"
    "v_rcp_f32_e64 v20, v20\n"
    "v_rcp_f32_e64 v21, v21\n"
    "v_rcp_f32_e64 v22, v22\n"
    "v_rcp_f32_e64 v23, v23\n"
    "v_pk_mul_f32 v[16:17], v[16:17], v[0:1]\n"
    "v_pk_mul_f32 v[18:19], v[18:19], v[2:3]\n"
    "v_pk_mul_f32 v[20:21], v[20:21], v[4:5]\n"
    "v_pk_mul_f32 v[22:23], v[22:23], v[6:7]\n"
    "v_pk_mul_f32 v[8:9], v[8:9], v[16:17]\n"
    "v_pk_mul_f32 v[10:11], v[10:11], v[18:19]\n"
    "v_pk_mul_f32 v[12:13], v[12:13], v[20:21]\n"
    "v_pk_mul_f32 v[14:15], v[14:15], v[22:23]\n"
    "v_cvt_pk_bf16_f32 v28, v16, v17\n"
    "v_cvt_pk_bf16_f32 v29, v18, v19\n"
    "v_cvt_pk_bf16_f32 v30, v20, v21\n"
    "v_cvt_pk_bf16_f32 v31, v22, v23\n"
    "v_cvt_pk_bf16_f32 v24, -v8, -v9\n"
    "v_cvt_pk_bf16_f32 v25, -v10, -v11\n"
    "v_cvt_pk_bf16_f32 v26, -v12, -v13\n"
    "v_cvt_pk_bf16_f32 v27, -v14, -v15\n"
    "v_accvgpr_write_b32 a216, v28\n"
    "v_accvgpr_write_b32 a217, v29\n"
    "s_mov_b64 s[68:69], s[44:45]\n"
    "s_mov_b32 s70, 0\n"
    "s_mov_b32 s71, s47\n"
    "s_mov_b64 s[72:73], s[40:41]\n"
    "s_mov_b64 s[74:75], s[42:43]\n"
    "s_mov_b32 s57, 0\n"
    "s_mov_b32 s58, 196608\n"
    "s_mov_b32 s60, 0\n"
    "s_mov_b32 s61, 393216\n"
    "s_mov_b32 s62, 786432\n"
    "s_mov_b32 s63, 1179648\n"
    "s_mov_b32 s52, 1572864\n"
    "s_mov_b32 s100, 98304\n"
    "s_mov_b32 s101, 294912\n"
    "s_mov_b32 s39, 0\n"
    "v_accvgpr_write_b32 a218, v30\n"
    "v_accvgpr_write_b32 a219, v31\n"
    "ds_write_b128 v234, v[24:27] offset:0\n"
    "v_or_b32_e32 v0, 0x88000, v34\n"
    "v_or_b32_e32 v1, 0x98000, v34\n"
    "v_or_b32_e32 v2, 0xa8000, v34\n"
    "v_or_b32_e32 v3, 0xb8000, v34\n"
    "v_or_b32_e32 v4, 0xc8000, v34\n"
    "v_or_b32_e32 v5, 0xd8000, v34\n"
    "v_or_b32_e32 v6, 0xe8000, v34\n"
    "v_or_b32_e32 v7, 0xf8000, v34\n"
    "v_pk_mul_f32 v[0:1], v[0:1], v[36:37] op_sel_hi:[1,0]\n"
    "v_pk_mul_f32 v[2:3], v[2:3], v[36:37] op_sel_hi:[1,0]\n"
    "v_pk_mul_f32 v[4:5], v[4:5], v[36:37] op_sel_hi:[1,0]\n"
    "v_pk_mul_f32 v[6:7], v[6:7], v[36:37] op_sel_hi:[1,0]\n"
    "v_pk_mul_f32 v[8:9], v[0:1], v[0:1]\n"
    "v_pk_mul_f32 v[10:11], v[2:3], v[2:3]\n"
    "v_pk_mul_f32 v[12:13], v[4:5], v[4:5]\n"
    "v_pk_mul_f32 v[14:15], v[6:7], v[6:7]\n"
    "v_pk_fma_f32 v[8:9], v[8:9], v[38:39], v[32:33] op_sel_hi:[1,0,0]\n"
    "v_pk_fma_f32 v[10:11], v[10:11], v[38:39], v[32:33] op_sel_hi:[1,0,0]\n"
    "v_pk_fma_f32 v[12:13], v[12:13], v[38:39], v[32:33] op_sel_hi:[1,0,0]\n"
    "v_pk_fma_f32 v[14:15], v[14:15], v[38:39], v[32:33] op_sel_hi:[1,0,0]\n"
    "v_pk_mul_f32 v[8:9], v[8:9], v[0:1]\n"
    "v_pk_mul_f32 v[10:11], v[10:11], v[2:3]\n"
    "v_pk_mul_f32 v[12:13], v[12:13], v[4:5]\n"
    "v_pk_mul_f32 v[14:15], v[14:15], v[6:7]\n"
    "v_exp_f32_e64 v8, v8\n"
    "v_exp_f32_e64 v9, v9\n"
    "v_exp_f32_e64 v10, v10\n"
    "v_exp_f32_e64 v11, v11\n"
    "v_exp_f32_e64 v12, v12\n"
    "v_exp_f32_e64 v13, v13\n"
    "v_exp_f32_e64 v14, v14\n"
    "v_exp_f32_e64 v15, v15\n"
    "v_pk_add_f32 v[16:17], v[8:9], 1.0 op_sel_hi:[1,0]\n"
    "v_pk_add_f32 v[18:19], v[10:11], 1.0 op_sel_hi:[1,0]\n"
    "v_pk_add_f32 v[20:21], v[12:13], 1.0 op_sel_hi:[1,0]\n"
    "v_pk_add_f32 v[22:23], v[14:15], 1.0 op_sel_hi:[1,0]\n"
    "v_rcp_f32_e64 v16, v16\n"
    "v_rcp_f32_e64 v17, v17\n"
    "v_rcp_f32_e64 v18, v18\n"
    "v_rcp_f32_e64 v19, v19\n"
    "v_rcp_f32_e64 v20, v20\n"
    "v_rcp_f32_e64 v21, v21\n"
    "v_rcp_f32_e64 v22, v22\n"
    "v_rcp_f32_e64 v23, v23\n"
    "v_pk_mul_f32 v[16:17], v[16:17], v[0:1]\n"
    "v_pk_mul_f32 v[18:19], v[18:19], v[2:3]\n"
    "v_pk_mul_f32 v[20:21], v[20:21], v[4:5]\n"
    "v_pk_mul_f32 v[22:23], v[22:23], v[6:7]\n"
    "v_pk_mul_f32 v[8:9], v[8:9], v[16:17]\n"
    "v_pk_mul_f32 v[10:11], v[10:11], v[18:19]\n"
    "v_pk_mul_f32 v[12:13], v[12:13], v[20:21]\n"
    "v_pk_mul_f32 v[14:15], v[14:15], v[22:23]\n"
    "v_cvt_pk_bf16_f32 v28, v16, v17\n"
    "v_cvt_pk_bf16_f32 v29, v18, v19\n"
    "v_cvt_pk_bf16_f32 v30, v20, v21\n"
    "v_cvt_pk_bf16_f32 v31, v22, v23\n"
    "v_cvt_pk_bf16_f32 v24, -v8, -v9\n"
    "v_cvt_pk_bf16_f32 v25, -v10, -v11\n"
    "v_cvt_pk_bf16_f32 v26, -v12, -v13\n"
    "v_cvt_pk_bf16_f32 v27, -v14, -v15\n"
    "v_accvgpr_write_b32 a220, v28\n"
    "v_accvgpr_write_b32 a221, v29\n"
    "v_accvgpr_write_b32 a222, v30\n"
    "v_accvgpr_write_b32 a223, v31\n"
    "ds_write_b128 v234, v[24:27] offset:16\n"
    "v_add_u32_e32 v234, 65536, v234\n"
    "global_load_dword v233, v[214:215], off\n"
    "s_waitcnt vmcnt(10)\n"
    "v_xor_b32_e32 v211, 96, v219\n"
    "s_add_u32 m0, s38, 0\n"
    "s_mov_b32 s64, 0\n"
    "buffer_load_dwordx4 v210, s[84:87], s64 offen offset:0 lds\n"
    "buffer_load_dwordx4 v210, s[84:87], s64 offen offset:1024 lds\n"
    "s_waitcnt vmcnt(11)\n"
    "ds_write_b128 v222, v[88:91]\n"
    "buffer_load_dwordx4 v210, s[84:87], s64 offen offset:2048 lds\n"
    "s_add_u32 m0, s38, 3072\n"
    "s_mov_b32 s64, 3072\n"
    "buffer_load_dwordx4 v210, s[84:87], s64 offen offset:0 lds\n"
    "buffer_load_dwordx4 v210, s[84:87], s64 offen offset:1024 lds\n"
    "buffer_load_dwordx4 v210, s[84:87], s64 offen offset:2048 lds\n"
    "s_add_u32 m0, s38, 6144\n"
    "s_mov_b32 s64, 6144\n"
    "buffer_load_dwordx4 v210, s[84:87], s64 offen offset:0 lds\n"
    "buffer_load_dwordx4 v210, s[84:87], s64 offen offset:1024 lds\n"
    "s_waitcnt vmcnt(16)\n"
    "v_lshlrev_b32_e32 v40, 16, v224\n"
    "buffer_load_dwordx4 v210, s[84:87], s64 offen offset:2048 lds\n"
    "s_waitcnt vmcnt(16)\n"
    "v_lshlrev_b32_e64 v41, 16, v225\n"
    "s_waitcnt vmcnt(15)\n"
    "v_lshlrev_b32_e32 v42, 16, v226\n"
    "s_waitcnt vmcnt(14)\n"
    "v_lshlrev_b32_e32 v43, 16, v227\n"
    "s_waitcnt vmcnt(13)\n"
    "v_lshlrev_b32_e32 v44, 16, v228\n"
    "s_waitcnt vmcnt(12)\n"
    "v_lshlrev_b32_e32 v45, 16, v229\n"
    "s_waitcnt vmcnt(11)\n"
    "v_lshlrev_b32_e32 v46, 16, v230\n"
    "s_waitcnt vmcnt(10)\n"
    "v_lshlrev_b32_e32 v47, 16, v231\n"
    "s_mov_b32 s65, 0\n"
    "buffer_load_dwordx4 a[224:227], v208, s[76:79], s65 offen\n"
    "v_add_f32_e64 v41, v41, v45\n"
    "v_add_f32_e64 v42, v42, v46\n"
    "v_add_f32_e64 v43, v43, v47\n"
    "v_cvt_pk_bf16_f32 v56, v40, v41\n"
    "v_cvt_pk_bf16_f32 v57, v42, v43\n"
    "v_cvt_pk_bf16_f32 v58, v44, 0\n"
    "v_cvt_pk_bf16_f32 v64, 0, v40\n"
    "v_cvt_pk_bf16_f32 v65, v41, v42\n"
    "v_cvt_pk_bf16_f32 v66, v43, v44\n"
    "v_cvt_pk_bf16_f32 v61, v40, v41\n"
    "v_cvt_pk_bf16_f32 v62, v42, v43\n"
    "v_cvt_pk_bf16_f32 v63, v44, 0\n"
    "v_cvt_pk_bf16_f32 v69, 0, v40\n"
    "v_cvt_pk_bf16_f32 v70, v41, v42\n"
    "v_cvt_pk_bf16_f32 v71, v43, v44\n"
    "s_mov_b32 s65, 98304\n"
    "buffer_load_dwordx4 a[228:231], v208, s[76:79], s65 offen\n"
    "ds_write_b128 v221, v[56:59] offset:0\n"
    "v_and_b32_e32 v48, 0xffff0000, v224\n"
    "v_and_b32_e32 v49, 0xffff0000, v225\n"
    "v_and_b32_e32 v50, 0xffff0000, v226\n"
    "v_and_b32_e32 v51, 0xffff0000, v227\n"
    "v_and_b32_e32 v52, 0xffff0000, v228\n"
    "v_and_b32_e32 v53, 0xffff0000, v229\n"
    "v_and_b32_e32 v54, 0xffff0000, v230\n"
    "v_and_b32_e32 v55, 0xffff0000, v231\n"
    "ds_write_b128 v221, v[64:67] offset:16\n"
    "s_mov_b32 s65, 196608\n"
    "buffer_load_dwordx4 a[232:235], v208, s[40:43], s65 offen\n"
    "v_add_f32_e64 v49, v49, v53\n"
    "v_add_f32_e64 v50, v50, v54\n"
    "v_add_f32_e64 v51, v51, v55\n"
    "v_cvt_pk_bf16_f32 v72, v48, v49\n"
    "ds_write_b128 v221, v[60:63] offset:32\n"
    "s_mov_b32 s65, 294912\n"
    "buffer_load_dwordx4 a[236:239], v208, s[40:43], s65 offen\n"
    "v_cvt_pk_bf16_f32 v73, v50, v51\n"
    "v_cvt_pk_bf16_f32 v74, v52, 0\n"
    "v_cvt_pk_bf16_f32 v80, 0, v48\n"
    "v_cvt_pk_bf16_f32 v81, v49, v50\n"
    "ds_write_b128 v221, v[68:71] offset:48\n"
    "v_cvt_pk_bf16_f32 v82, v51, v52\n"
    "v_cvt_pk_bf16_f32 v77, v48, v49\n"
    "v_cvt_pk_bf16_f32 v78, v50, v51\n"
    "v_cvt_pk_bf16_f32 v79, v52, 0\n"
    "v_cvt_pk_bf16_f32 v85, 0, v48\n"
    "s_mov_b32 s65, 589824\n"
    "buffer_load_dwordx4 a[240:243], v208, s[40:43], s65 offen\n"
    "ds_write_b128 v221, v[72:75] offset:336\n"
    "v_cvt_pk_bf16_f32 v86, v49, v50\n"
    "v_cvt_pk_bf16_f32 v87, v51, v52\n"
    "v_xor_b32_e32 v212, 0, v223\n"
    "v_xor_b32_e32 v214, 64, v223\n"
    "v_xor_b32_e32 v215, 96, v223\n"
    "ds_write_b128 v221, v[80:83] offset:352\n"
    "s_mov_b32 s65, 688128\n"
    "buffer_load_dwordx4 a[244:247], v208, s[40:43], s65 offen\n"
    "ds_write_b128 v221, v[76:79] offset:368\n"
    "s_mov_b32 s65, 983040\n"
    "buffer_load_dwordx4 a[248:251], v208, s[40:43], s65 offen\n"
    "ds_write_b128 v221, v[84:87] offset:384\n"
    "v_lshl_add_u32 v221, s51, 8, v211\n"
    "s_mov_b32 s65, 1081344\n"
    "buffer_load_dwordx4 a[252:255], v208, s[40:43], s65 offen\n"
    "s_mov_b32 s65, 1376256\n"
    "buffer_load_dwordx4 a[168:171], v208, s[40:43], s65 offen\n"
    "s_mov_b32 s65, 1474560\n"
    "buffer_load_dwordx4 a[172:175], v208, s[40:43], s65 offen\n"
    "s_waitcnt vmcnt(10)\n"
    "s_barrier\n"
    "ds_read_b64_tr_b16 a[0:1], v220 offset:0\n"
    "ds_read_b64_tr_b16 a[2:3], v220 offset:3072\n"
    "ds_read_b64_tr_b16 a[4:5], v220 offset:8\n"
    "ds_read_b64_tr_b16 a[6:7], v220 offset:3080\n"
    "ds_read_b64_tr_b16 a[8:9], v220 offset:64\n"
    "s_add_u32 vcc_lo, s96, 0\n"
    "s_addc_u32 vcc_hi, s97, 0\n"
    "global_load_dwordx4 v[240:243], v213, vcc\n"
    "ds_read_b64_tr_b16 a[10:11], v220 offset:3136\n"
    "ds_read_b64_tr_b16 a[12:13], v220 offset:72\n"
    "ds_read_b64_tr_b16 a[14:15], v220 offset:3144\n"
    "ds_read_b64_tr_b16 v[168:169], v218 offset:128\n"
    "ds_read_b64_tr_b16 v[170:171], v218 offset:3200\n"
    "ds_read_b64_tr_b16 a[16:17], v220 offset:6144\n"
    "ds_read_b64_tr_b16 a[18:19], v220 offset:9216\n"
    "ds_read_b64_tr_b16 a[20:21], v220 offset:6152\n"
    "ds_read_b64_tr_b16 a[22:23], v220 offset:9224\n"
    "s_add_u32 vcc_lo, s96, 8192\n"
    "s_nop 0\n"
    "s_addc_u32 vcc_hi, s97, 0\n"
    "global_load_dwordx4 v[248:251], v213, vcc\n"
    "ds_read_b64_tr_b16 a[24:25], v220 offset:6208\n"
    "ds_read_b64_tr_b16 a[26:27], v220 offset:9280\n"
    "ds_read_b64_tr_b16 a[28:29], v220 offset:6216\n"
    "ds_read_b64_tr_b16 a[30:31], v220 offset:9288\n"
    "ds_read_b64_tr_b16 v[172:173], v218 offset:6272\n"
    "ds_read_b64_tr_b16 v[174:175], v218 offset:9344\n"
    "ds_read_b64_tr_b16 a[32:33], v220 offset:12288\n"
    "ds_read_b64_tr_b16 a[34:35], v220 offset:15360\n"
    "ds_read_b64_tr_b16 a[36:37], v220 offset:12296\n"
    "s_add_u32 vcc_lo, s96, 12288\n"
    "s_nop 0\n"
    "s_addc_u32 vcc_hi, s97, 0\n"
    "global_load_dwordx4 v[252:255], v213, vcc\n"
    "ds_read_b64_tr_b16 a[38:39], v220 offset:15368\n"
    "ds_read_b64_tr_b16 a[40:41], v220 offset:12352\n"
    "ds_read_b64_tr_b16 a[42:43], v220 offset:15424\n"
    "ds_read_b64_tr_b16 a[44:45], v220 offset:12360\n"
    "ds_read_b64_tr_b16 a[46:47], v220 offset:15432\n"
    "ds_read_b64_tr_b16 v[176:177], v218 offset:12416\n"
    "ds_read_b64_tr_b16 v[178:179], v218 offset:15488\n"
    "s_waitcnt vmcnt(12)\n"
    "ds_write2_b32 v216, a224, a225 offset0:0 offset1:20\n"
    "s_mov_b32 s65, 0\n"
    "buffer_load_dwordx4 a[176:179], v208, s[40:43], s65 offen\n"
    "ds_write2_b32 v216, a226, a227 offset0:40 offset1:60\n"
    "s_nop 0\n"
    "s_waitcnt vmcnt(12)\n"
    "ds_write2_b32 v216, a228, a229 offset0:80 offset1:100\n"
    "s_mov_b32 s65, 98304\n"
    "buffer_load_dwordx4 a[180:183], v208, s[40:43], s65 offen\n"
    "ds_write2_b32 v216, a230, a231 offset0:120 offset1:140\n"
    "v_add_u32_e32 v216, 1360, v216\n"
    "s_waitcnt lgkmcnt(4)\n"
    "s_barrier\n"
    "s_mov_b32 s65, 393216\n"
    "buffer_load_dwordx4 a[184:187], v208, s[40:43], s65 offen\n"
    "s_waitcnt vmcnt(13)\n"
    "ds_write2_b32 v216, a232, a233 offset0:0 offset1:20\n"
    "ds_write2_b32 v216, a234, a235 offset0:40 offset1:60\n"
    "s_waitcnt vmcnt(12)\n"
    "ds_write2_b32 v216, a236, a237 offset0:80 offset1:100\n"
    "s_mov_b32 s65, 491520\n"
    "buffer_load_dwordx4 a[188:191], v208, s[40:43], s65 offen\n"
    "ds_write2_b32 v216, a238, a239 offset0:120 offset1:140\n"
    "v_add_u32_e32 v216, 1360, v216\n"
    "s_waitcnt vmcnt(12)\n"
    "ds_write2_b32 v216, a240, a241 offset0:0 offset1:20\n"
    "s_mov_b32 s65, 786432\n"
    "buffer_load_dwordx4 a[192:195], v208, s[40:43], s65 offen\n"
    "ds_write2_b32 v216, a242, a243 offset0:40 offset1:60\n"
    "s_waitcnt vmcnt(12)\n"
    "ds_write2_b32 v216, a244, a245 offset0:80 offset1:100\n"
    "s_mov_b32 s65, 884736\n"
    "buffer_load_dwordx4 a[196:199], v208, s[40:43], s65 offen\n"
    "ds_write2_b32 v216, a246, a247 offset0:120 offset1:140\n"
    "v_add_u32_e32 v216, 1360, v216\n"
    "s_waitcnt vmcnt(12)\n"
    "ds_write2_b32 v216, a248, a249 offset0:0 offset1:20\n"
    "ds_write2_b32 v216, a250, a251 offset0:40 offset1:60\n"
    "s_mov_b32 s65, 1179648\n"
    "buffer_load_dwordx4 a[200:203], v208, s[40:43], s65 offen\n"
    "s_waitcnt vmcnt(12)\n"
    "ds_write2_b32 v216, a252, a253 offset0:80 offset1:100\n"
    "ds_write2_b32 v216, a254, a255 offset0:120 offset1:140\n"
    "s_mov_b32 s65, 1277952\n"
    "buffer_load_dwordx4 a[204:207], v208, s[40:43], s65 offen\n"
    "v_add_u32_e32 v216, 1360, v216\n"
    "s_waitcnt vmcnt(12)\n"
    "ds_write2_b32 v216, a168, a169 offset0:0 offset1:20\n"
    "ds_write2_b32 v216, a170, a171 offset0:40 offset1:60\n"
    "s_mov_b32 s65, 1572864\n"
    "buffer_load_dwordx4 a[208:211], v208, s[40:43], s65 offen\n"
    "s_waitcnt vmcnt(12)\n"
    "ds_write2_b32 v216, a172, a173 offset0:80 offset1:100\n"
    "ds_write2_b32 v216, a174, a175 offset0:120 offset1:140\n"
    "v_add_u32_e32 v216, -4800, v216\n"
    "s_mov_b32 s65, 1671168\n"
    "buffer_load_dwordx4 a[212:215], v208, s[40:43], s65 offen\n"
    "s_waitcnt vmcnt(10)\n"
    "v_mov_b32_e32 v222, v254\n"
    "v_mov_b32_e32 v254, 0\n"
    "v_xor_b32_e32 v208, 0, v219\n"
    "v_add_u32_e32 v255, s99, v255\n"
    "v_mfma_f32_32x32x1_2b_f32 v[0:31], v254, v254, 0\n"
    "s_add_u32 m0, s38, 0\n"
    "s_mov_b32 s64, 18432\n"
    "buffer_load_dwordx4 v210, s[84:87], s64 offen offset:0 lds\n"
    "s_waitcnt vmcnt(10)\n"
    "ds_write2_b32 v216, a176, a177 offset0:0 offset1:20\n"
    "s_waitcnt lgkmcnt(1)\n"
    "s_barrier\n"
    "v_mfma_f32_32x32x1_2b_f32 v[32:63], v254, v254, 0\n"
    "s_nop 0\n"
    "ds_read_b128 a[224:227], v250 offset:0\n"
    "ds_read_b128 a[228:231], v250 offset:320\n"
    "buffer_load_dwordx4 v210, s[84:87], s64 offen offset:1024 lds\n"
    "ds_read_b128 a[232:235], v251 offset:0\n"
    "ds_read_b128 a[236:239], v251 offset:336\n"
    "ds_read_b128 a[240:243], v250 offset:80\n"
    "ds_read_b128 a[244:247], v250 offset:400\n"
    "ds_read_b128 a[248:251], v251 offset:672\n"
    "ds_read_b128 a[252:255], v251 offset:1008\n"
    "v_mfma_f32_32x32x1_2b_f32 v[64:95], v254, v254, 0\n"
    "ds_write2_b32 v216, a178, a179 offset0:40 offset1:60\n"
    "buffer_load_dwordx4 v210, s[84:87], s64 offen offset:2048 lds\n"
    "v_add_u32_e64 v254, s54, v242\n"
    "ds_read_b128 a[96:99], v250 offset:160\n"
    "ds_read_b128 a[100:103], v250 offset:480\n"
    "ds_read_b128 a[104:107], v251 offset:1344\n"
    "ds_read_b128 a[108:111], v251 offset:1680\n"
    "ds_read_b128 a[112:115], v250 offset:240\n"
    "ds_read_b128 a[116:119], v250 offset:560\n"
    "ds_read_b128 a[120:123], v251 offset:2016\n"
    "s_add_u32 m0, s38, 3072\n"
    "s_mov_b32 s64, 21504\n"
    "buffer_load_dwordx4 v210, s[84:87], s64 offen offset:0 lds\n"
    "s_waitcnt lgkmcnt(13)\n"
    "v_smfmac_f32_16x16x64_bf16 v[0:3], a[232:235], a[224:231], v240\n"
    "s_waitcnt lgkmcnt(12)\n"
    "ds_read_b128 a[124:127], v251 offset:2352\n"
    "v_smfmac_f32_16x16x64_bf16 v[4:7], a[236:239], a[224:231], v240 abid:1\n"
    "s_waitcnt vmcnt(12)\n"
    "ds_write2_b32 v216, a180, a181 offset0:80 offset1:100\n"
    "buffer_load_dwordx4 v210, s[84:87], s64 offen offset:1024 lds\n"
    "ds_write2_b32 v216, a182, a183 offset0:120 offset1:140\n"
    "s_waitcnt lgkmcnt(12)\n"
    "v_smfmac_f32_16x16x64_bf16 v[8:11], a[248:251], a[240:247], v240\n"
    "s_waitcnt lgkmcnt(10)\n"
    "ds_read_b128 a[224:227], v250 offset:27024\n"
    "v_smfmac_f32_16x16x64_bf16 v[12:15], a[252:255], a[240:247], v240 abid:1\n"
    "s_nop 0\n"
    "ds_read_b128 a[228:231], v250 offset:27344\n"
    "ds_read_b128 a[232:235], v251 offset:10752\n"
    "ds_read_b128 a[236:239], v251 offset:11088\n"
    "v_add_u32_e32 v216, 1360, v216\n"
    "ds_read_b128 a[240:243], v250 offset:27104\n"
    "buffer_load_dwordx4 v210, s[84:87], s64 offen offset:2048 lds\n"
    "ds_read_b128 a[244:247], v250 offset:27424\n"
    "ds_read_b128 a[248:251], v251 offset:11424\n"
    "s_waitcnt lgkmcnt(14)\n"
    "v_smfmac_f32_16x16x64_bf16 v[16:19], a[104:107], a[96:103], v240\n"
    "s_waitcnt lgkmcnt(13)\n"
    "ds_read_b128 a[252:255], v251 offset:11760\n"
    "v_smfmac_f32_16x16x64_bf16 v[20:23], a[108:111], a[96:103], v240 abid:1\n"
    "s_waitcnt vmcnt(13)\n"
    "ds_write2_b32 v216, a184, a185 offset0:0 offset1:20\n"
    "s_add_u32 m0, s38, 6144\n"
    "s_mov_b32 s64, 24576\n"
    "buffer_load_dwordx4 v210, s[84:87], s64 offen offset:0 lds\n"
    "s_waitcnt lgkmcnt(12)\n"
    "v_smfmac_f32_16x16x64_bf16 v[24:27], a[120:123], a[112:119], v240\n"
    "s_waitcnt lgkmcnt(11)\n"
    "ds_write2_b32 v216, a186, a187 offset0:40 offset1:60\n"
    "v_smfmac_f32_16x16x64_bf16 v[28:31], a[124:127], a[112:119], v240 abid:1\n"
    "s_nop 0\n"
    "ds_read_b128 a[96:99], v250 offset:27184\n"
    "ds_read_b128 a[100:103], v250 offset:27504\n"
    "ds_read_b128 a[104:107], v251 offset:12096\n"
    "buffer_load_dwordx4 v210, s[84:87], s64 offen offset:1024 lds\n"
    "ds_read_b128 a[108:111], v251 offset:12432\n"
    "ds_read_b128 a[112:115], v250 offset:27264\n"
    "ds_read_b128 a[116:119], v250 offset:27584\n"
    "ds_read_b128 a[120:123], v251 offset:12768\n"
    "s_waitcnt lgkmcnt(14)\n"
    "v_smfmac_f32_16x16x64_bf16 v[32:35], a[232:235], a[224:231], v240\n"
    "s_waitcnt lgkmcnt(13)\n"
    "ds_read_b128 a[124:127], v251 offset:13104\n"
    "v_smfmac_f32_16x16x64_bf16 v[36:39], a[236:239], a[224:231], v240 abid:1\n"
    "buffer_load_dwordx4 v210, s[84:87], s64 offen offset:2048 lds\n"
    "s_waitcnt vmcnt(15)\n"
    "ds_write2_b32 v216, a188, a189 offset0:80 offset1:100\n"
    "v_xor_b32_e32 v210, 64, v219\n"
    "s_waitcnt lgkmcnt(12)\n"
    "v_smfmac_f32_16x16x64_bf16 v[40:43], a[248:251], a[240:247], v240\n"
    "s_nop 0\n"
    "ds_write2_b32 v216, a190, a191 offset0:120 offset1:140\n"
    "s_waitcnt lgkmcnt(11)\n"
    "v_smfmac_f32_16x16x64_bf16 v[44:47], a[252:255], a[240:247], v240 abid:1\n"
    "s_add_u32 vcc_lo, s96, 28672\n"
    "s_addc_u32 vcc_hi, s97, 0\n"
    "global_load_dwordx4 v[200:203], v213, vcc\n"
    "ds_read_b128 a[224:227], v250 offset:54048\n"
    "ds_read_b128 a[228:231], v250 offset:54368\n"
    "ds_read_b128 a[232:235], v251 offset:21504\n"
    "v_add_u32_e32 v216, 1360, v216\n"
    "ds_read_b128 a[236:239], v251 offset:21840\n"
    "ds_read_b128 a[240:243], v250 offset:54128\n"
    "ds_read_b128 a[244:247], v250 offset:54448\n"
    "ds_read_b128 a[248:251], v251 offset:22176\n"
    "s_waitcnt lgkmcnt(14)\n"
    "v_smfmac_f32_16x16x64_bf16 v[48:51], a[104:107], a[96:103], v240\n"
    "s_waitcnt lgkmcnt(13)\n"
    "s_add_u32 vcc_lo, s96, 32768\n"
    "s_addc_u32 vcc_hi, s97, 0\n"
    "global_load_dwordx4 v[204:207], v213, vcc\n"
    "v_smfmac_f32_16x16x64_bf16 v[52:55], a[108:111], a[96:103], v240 abid:1\n"
    "s_nop 0\n"
    "ds_read_b128 a[252:255], v251 offset:22512\n"
    "s_waitcnt vmcnt(16)\n"
    "ds_write2_b32 v216, a192, a193 offset0:0 offset1:20\n"
    "s_waitcnt lgkmcnt(12)\n"
    "v_smfmac_f32_16x16x64_bf16 v[56:59], a[120:123], a[112:119], v240\n"
    "ds_write2_b32 v216, a194, a195 offset0:40 offset1:60\n"
    "s_mov_b32 s59, 49152\n"
    "buffer_load_dwordx4 v[96:99], v209, s[80:83], s59 offen offset:0\n"
    "s_waitcnt lgkmcnt(11)\n"
    "v_smfmac_f32_16x16x64_bf16 v[60:63], a[124:127], a[112:119], v240 abid:1\n"
    "s_nop 0\n"
    "ds_read_b128 a[96:99], v250 offset:54208\n"
    "ds_read_b128 a[100:103], v250 offset:54528\n"
    "ds_read_b128 a[104:107], v251 offset:22848\n"
    "ds_read_b128 a[108:111], v251 offset:23184\n"
    "ds_read_b128 a[112:115], v250 offset:54288\n"
    "ds_read_b128 a[116:119], v250 offset:54608\n"
    "buffer_load_dwordx4 v[100:103], v209, s[80:83], s59 offen offset:256\n"
    "ds_read_b128 a[120:123], v251 offset:23520\n"
    "s_waitcnt lgkmcnt(14)\n"
    "v_smfmac_f32_16x16x64_bf16 v[64:67], a[232:235], a[224:231], v240\n"
    "s_waitcnt lgkmcnt(13)\n"
    "ds_read_b128 a[124:127], v251 offset:23856\n"
    "v_smfmac_f32_16x16x64_bf16 v[68:71], a[236:239], a[224:231], v240 abid:1\n"
    "s_waitcnt vmcnt(17)\n"
    "ds_write2_b32 v216, a196, a197 offset0:80 offset1:100\n"
    "ds_write2_b32 v216, a198, a199 offset0:120 offset1:140\n"
    "buffer_load_dwordx4 v[104:107], v209, s[80:83], s59 offen offset:512\n"
    "s_waitcnt lgkmcnt(13)\n"
    "v_smfmac_f32_16x16x64_bf16 v[72:75], a[248:251], a[240:247], v240\n"
    "s_waitcnt lgkmcnt(11)\n"
    "v_add_u32_e32 v216, 1360, v216\n"
    "v_smfmac_f32_16x16x64_bf16 v[76:79], a[252:255], a[240:247], v240 abid:1\n"
    "s_waitcnt vmcnt(17)\n"
    "ds_write2_b32 v216, a200, a201 offset0:0 offset1:20\n"
    "s_mov_b32 s59, 52224\n"
    "buffer_load_dwordx4 v[108:111], v209, s[80:83], s59 offen offset:0\n"
    "ds_write2_b32 v216, a202, a203 offset0:40 offset1:60\n"
    "s_waitcnt lgkmcnt(9)\n"
    "v_smfmac_f32_16x16x64_bf16 v[80:83], a[104:107], a[96:103], v240\n"
    "s_waitcnt vmcnt(17)\n"
    "ds_write2_b32 v216, a204, a205 offset0:80 offset1:100\n"
    "s_waitcnt lgkmcnt(9)\n"
    "v_smfmac_f32_16x16x64_bf16 v[84:87], a[108:111], a[96:103], v240 abid:1\n"
    "s_nop 0\n"
    "ds_write2_b32 v216, a206, a207 offset0:120 offset1:140\n"
    "buffer_load_dwordx4 v[112:115], v209, s[80:83], s59 offen offset:256\n"
    "v_add_u32_e32 v216, 1360, v216\n"
    "s_waitcnt lgkmcnt(7)\n"
    "v_smfmac_f32_16x16x64_bf16 v[88:91], a[120:123], a[112:119], v240\n"
    "s_waitcnt vmcnt(17)\n"
    "ds_write2_b32 v216, a208, a209 offset0:0 offset1:20\n"
    "s_waitcnt lgkmcnt(6)\n"
    "v_smfmac_f32_16x16x64_bf16 v[92:95], a[124:127], a[112:119], v240 abid:1\n"
    "s_nop 0\n"
    "ds_write2_b32 v216, a210, a211 offset0:40 offset1:60\n"
    "buffer_load_dwordx4 v[116:119], v209, s[80:83], s59 offen offset:512\n"
    "s_waitcnt vmcnt(17)\n"
    "ds_write2_b32 v216, a212, a213 offset0:80 offset1:100\n"
    "ds_write2_b32 v216, a214, a215 offset0:120 offset1:140\n"
    "s_mov_b32 s59, 0\n"
    "buffer_load_dwordx4 v[120:123], v209, s[80:83], s59 offen offset:0\n"
    "ds_read_b128 a[232:235], v252 offset:0\n"
    "ds_read_b128 a[236:239], v252 offset:336\n"
    "ds_read_b128 a[248:251], v252 offset:672\n"
    "ds_read_b128 a[252:255], v252 offset:1008\n"
    "buffer_load_dwordx4 v[124:127], v209, s[80:83], s59 offen offset:256\n"
    "s_waitcnt vmcnt(10)\n"
    "s_barrier\n"
    "s_waitcnt lgkmcnt(4)\n"
    "s_barrier\n"
    "ds_read_b128 a[224:227], v250 offset:640\n"
    "buffer_load_dwordx4 v[128:131], v209, s[80:83], s59 offen offset:512\n"
    "ds_read_b128 a[228:231], v250 offset:960\n"
    "ds_read_b128 a[240:243], v250 offset:720\n"
    "ds_read_b128 a[244:247], v250 offset:1040\n"
    "ds_read_b64_tr_b16 a[48:49], v220 offset:0\n"
    "ds_read_b64_tr_b16 a[50:51], v220 offset:3072\n"
    "ds_read_b64_tr_b16 a[52:53], v220 offset:8\n"
    "ds_read_b64_tr_b16 a[54:55], v220 offset:3080\n"
    "ds_read_b64_tr_b16 a[56:57], v220 offset:64\n"
    "ds_read_b64_tr_b16 a[58:59], v220 offset:3136\n"
    "ds_read_b64_tr_b16 a[60:61], v220 offset:72\n"
    "s_mov_b32 s59, 3072\n"
    "buffer_load_dwordx4 v[132:135], v209, s[80:83], s59 offen offset:0\n"
    "ds_read_b64_tr_b16 a[62:63], v220 offset:3144\n"
    "ds_read_b64_tr_b16 v[188:189], v218 offset:128\n"
    "ds_read_b64_tr_b16 v[190:191], v218 offset:3200\n"
    "ds_read_b64_tr_b16 a[64:65], v220 offset:6144\n"
    "ds_read_b64_tr_b16 a[66:67], v220 offset:9216\n"
    "ds_read_b64_tr_b16 a[68:69], v220 offset:6152\n"
    "ds_read_b64_tr_b16 a[70:71], v220 offset:9224\n"
    "ds_read_b64_tr_b16 a[72:73], v220 offset:6208\n"
    "buffer_load_dwordx4 v[136:139], v209, s[80:83], s59 offen offset:256\n"
    "s_waitcnt lgkmcnt(15)\n"
    "v_smfmac_f32_16x16x64_bf16 v[0:3], a[232:235], a[224:231], v240\n"
    "s_nop 0\n"
    "ds_read_b128 a[232:235], v252 offset:1344\n"
    "v_smfmac_f32_16x16x64_bf16 v[4:7], a[236:239], a[224:231], v240 abid:1\n"
    "ds_read_b128 a[224:227], v250 offset:800\n"
    "v_smfmac_f32_16x16x64_bf16 v[8:11], a[248:251], a[240:247], v240\n"
    "ds_read_b128 a[228:231], v250 offset:1120\n"
    "v_smfmac_f32_16x16x64_bf16 v[12:15], a[252:255], a[240:247], v240 abid:1\n"
    "ds_read_b128 a[236:239], v252 offset:1680\n"
    "buffer_load_dwordx4 v[140:143], v209, s[80:83], s59 offen offset:512\n"
    "ds_read_b128 a[240:243], v250 offset:880\n"
    "ds_read_b128 a[244:247], v250 offset:1200\n"
    "v_pk_mov_b32 v[224:225], v[0:1], v[0:1] op_sel:[0,1]\n"
    "v_pk_mov_b32 v[226:227], v[2:3], v[2:3] op_sel:[0,1]\n"
    "v_pk_mul_f32 v[228:229], v[0:1], v[0:1]\n"
    "v_pk_mul_f32 v[230:231], v[2:3], v[2:3]\n"
    "v_pk_add_f32 v[224:225], v[224:225], v[4:5]\n"
    "v_pk_add_f32 v[226:227], v[226:227], v[6:7]\n"
    "v_pk_fma_f32 v[228:229], v[4:5], v[4:5], v[228:229]\n"
    "v_pk_fma_f32 v[230:231], v[6:7], v[6:7], v[230:231]\n"
    "v_pk_add_f32 v[224:225], v[224:225], v[8:9]\n"
    "v_pk_add_f32 v[226:227], v[226:227], v[10:11]\n"
    "v_pk_fma_f32 v[228:229], v[8:9], v[8:9], v[228:229]\n"
    "v_pk_fma_f32 v[230:231], v[10:11], v[10:11], v[230:231]\n"
    "v_pk_add_f32 v[224:225], v[224:225], v[12:13]\n"
    "v_pk_add_f32 v[226:227], v[226:227], v[14:15]\n"
    "s_mov_b32 s59, 24576\n"
    "buffer_load_dwordx4 v[144:147], v209, s[80:83], s59 offen offset:0\n"
    "v_pk_fma_f32 v[228:229], v[12:13], v[12:13], v[228:229]\n"
    "v_pk_fma_f32 v[230:231], v[14:15], v[14:15], v[230:231]\n"
    "ds_read_b128 a[248:251], v252 offset:2016\n"
    "ds_read_b128 a[252:255], v252 offset:2352\n"
    "ds_read_b64_tr_b16 a[74:75], v220 offset:9280\n"
    "ds_read_b64_tr_b16 a[76:77], v220 offset:6216\n"
    "s_waitcnt vmcnt(12)\n"
    "v_or3_b32 v200, v200, v96, v97\n"
    "ds_read_b64_tr_b16 a[78:79], v220 offset:9288\n"
    "s_waitcnt lgkmcnt(8)\n"
    "v_smfmac_f32_16x16x64_bf16 v[16:19], a[232:235], a[224:231], v240\n"
    "s_waitcnt lgkmcnt(7)\n"
    "buffer_load_dwordx4 v[148:151], v209, s[80:83], s59 offen offset:256\n"
    "v_smfmac_f32_16x16x64_bf16 v[20:23], a[236:239], a[224:231], v240 abid:1\n"
    "s_nop 0\n"
    "ds_read_b128 a[224:227], v250 offset:27664\n"
    "ds_read_b128 a[228:231], v250 offset:27984\n"
    "ds_read_b128 a[232:235], v252 offset:10752\n"
    "v_or3_b32 v200, v200, v98, v99\n"
    "ds_read_b128 a[236:239], v252 offset:11088\n"
    "v_pk_add_f32 v[224:225], v[224:225], v[16:17]\n"
    "v_pk_add_f32 v[226:227], v[226:227], v[18:19]\n"
    "v_pk_fma_f32 v[228:229], v[16:17], v[16:17], v[228:229]\n"
    "v_pk_fma_f32 v[230:231], v[18:19], v[18:19], v[230:231]\n"
    "v_pk_add_f32 v[224:225], v[224:225], v[20:21]\n"
    "buffer_load_dwordx4 v[152:155], v209, s[80:83], s59 offen offset:512\n"
    "v_pk_add_f32 v[226:227], v[226:227], v[22:23]\n"
    "v_pk_fma_f32 v[228:229], v[20:21], v[20:21], v[228:229]\n"
    "v_pk_fma_f32 v[230:231], v[22:23], v[22:23], v[230:231]\n"
    "s_waitcnt lgkmcnt(7)\n"
    "v_smfmac_f32_16x16x64_bf16 v[24:27], a[248:251], a[240:247], v240\n"
    "s_nop 0\n"
    "ds_read_b128 a[248:251], v252 offset:11424\n"
    "v_smfmac_f32_16x16x64_bf16 v[28:31], a[252:255], a[240:247], v240 abid:1\n"
    "ds_read_b128 a[240:243], v250 offset:27744\n"
    "ds_read_b128 a[244:247], v250 offset:28064\n"
    "ds_read_b128 a[252:255], v252 offset:11760\n"
    "v_and_b32_e32 v232, 0x7fff7fff, v96\n"
    "s_mov_b32 s59, 27648\n"
    "buffer_load_dwordx4 v[156:159], v209, s[80:83], s59 offen offset:0\n"
    "v_pk_add_f32 v[224:225], v[224:225], v[24:25]\n"
    "v_pk_add_f32 v[226:227], v[226:227], v[26:27]\n"
    "v_pk_fma_f32 v[228:229], v[24:25], v[24:25], v[228:229]\n"
    "v_pk_fma_f32 v[230:231], v[26:27], v[26:27], v[230:231]\n"
    "v_pk_add_f32 v[224:225], v[224:225], v[28:29]\n"
    "v_pk_add_f32 v[226:227], v[226:227], v[30:31]\n"
    "v_pk_fma_f32 v[228:229], v[28:29], v[28:29], v[228:229]\n"
    "v_pk_fma_f32 v[230:231], v[30:31], v[30:31], v[230:231]\n"
    "s_waitcnt lgkmcnt(5)\n"
    "v_smfmac_f32_16x16x64_bf16 v[32:35], a[232:235], a[224:231], v240\n"
    "s_waitcnt lgkmcnt(4)\n"
    "ds_read_b128 a[232:235], v252 offset:12096\n"
    "v_smfmac_f32_16x16x64_bf16 v[36:39], a[236:239], a[224:231], v240 abid:1\n"
    "buffer_load_dwordx4 v[160:163], v209, s[80:83], s59 offen offset:256\n"
    "ds_read_b128 a[224:227], v250 offset:27824\n"
    "ds_read_b128 a[228:231], v250 offset:28144\n"
    "ds_read_b128 a[236:239], v252 offset:12432\n"
    "v_add_u32_e64 v96, v201, v96\n"
    "v_pk_add_f32 v[224:225], v[224:225], v[32:33]\n"
    "v_pk_add_f32 v[226:227], v[226:227], v[34:35]\n"
    "v_pk_fma_f32 v[228:229], v[32:33], v[32:33], v[228:229]\n"
    "v_pk_fma_f32 v[230:231], v[34:35], v[34:35], v[230:231]\n"
    "v_pk_add_f32 v[224:225], v[224:225], v[36:37]\n"
    "v_pk_add_f32 v[226:227], v[226:227], v[38:39]\n"
    "v_pk_fma_f32 v[228:229], v[36:37], v[36:37], v[228:229]\n"
    "v_pk_fma_f32 v[230:231], v[38:39], v[38:39], v[230:231]\n"
    "ds_read_b64_tr_b16 v[192:193], v218 offset:6272\n"
    "s_waitcnt lgkmcnt(6)\n"
    "v_smfmac_f32_16x16x64_bf16 v[40:43], a[248:251], a[240:247], v240\n"
    "s_waitcnt lgkmcnt(5)\n"
    "buffer_load_dwordx4 v[164:167], v209, s[80:83], s59 offen offset:512\n"
    "v_smfmac_f32_16x16x64_bf16 v[44:47], a[252:255], a[240:247], v240 abid:1\n"
    "ds_read_b128 a[240:243], v250 offset:27904\n"
    "ds_read_b128 a[244:247], v250 offset:28224\n"
    "ds_read_b128 a[248:251], v252 offset:12768\n"
    "v_xor_b32_e32 v209, 32, v219\n"
    "ds_read_b128 a[252:255], v252 offset:13104\n"
    "v_pk_add_f32 v[224:225], v[224:225], v[40:41]\n"
    "v_pk_add_f32 v[226:227], v[226:227], v[42:43]\n"
    "v_pk_fma_f32 v[228:229], v[40:41], v[40:41], v[228:229]\n"
    "s_add_u32 vcc_lo, s96, 4096\n"
    "s_addc_u32 vcc_hi, s97, 0\n"
    "global_load_dwordx4 v[244:247], v213, vcc\n"
    "v_pk_fma_f32 v[230:231], v[42:43], v[42:43], v[230:231]\n"
    "v_pk_add_f32 v[224:225], v[224:225], v[44:45]\n"
    "v_pk_add_f32 v[226:227], v[226:227], v[46:47]\n"
    "v_pk_fma_f32 v[228:229], v[44:45], v[44:45], v[228:229]\n"
    "v_pk_fma_f32 v[230:231], v[46:47], v[46:47], v[230:231]\n"
    "s_waitcnt lgkmcnt(5)\n"
    "v_smfmac_f32_16x16x64_bf16 v[48:51], a[232:235], a[224:231], v240\n"
    "s_nop 0\n"
    "ds_read_b128 a[232:235], v252 offset:21504\n"
    "v_smfmac_f32_16x16x64_bf16 v[52:55], a[236:239], a[224:231], v240 abid:1\n"
    "s_nop 0\n"
    "ds_read_b128 a[224:227], v250 offset:54688\n"
    "ds_read_b128 a[228:231], v250 offset:55008\n"
    "s_add_u32 vcc_lo, s96, 24576\n"
    "s_addc_u32 vcc_hi, s97, 0\n"
    "global_load_dwordx4 v[236:239], v213, vcc\n"
    "ds_read_b128 a[236:239], v252 offset:21840\n"
    "v_pk_add_f32 v[224:225], v[224:225], v[48:49]\n"
    "v_pk_add_f32 v[226:227], v[226:227], v[50:51]\n"
    "v_pk_fma_f32 v[228:229], v[48:49], v[48:49], v[228:229]\n"
    "v_pk_fma_f32 v[230:231], v[50:51], v[50:51], v[230:231]\n"
    "v_pk_add_f32 v[224:225], v[224:225], v[52:53]\n"
    "v_pk_add_f32 v[226:227], v[226:227], v[54:55]\n"
    "v_pk_fma_f32 v[228:229], v[52:53], v[52:53], v[228:229]\n"
    "v_pk_fma_f32 v[230:231], v[54:55], v[54:55], v[230:231]\n"
    "s_waitcnt lgkmcnt(5)\n"
    "v_smfmac_f32_16x16x64_bf16 v[56:59], a[248:251], a[240:247], v240\n"
    "s_waitcnt lgkmcnt(4)\n"
    "ds_read_b128 a[248:251], v252 offset:22176\n"
    "v_smfmac_f32_16x16x64_bf16 v[60:63], a[252:255], a[240:247], v240 abid:1\n"
    "s_nop 0\n"
    "ds_read_b128 a[240:243], v250 offset:54768\n"
    "ds_read_b128 a[244:247], v250 offset:55088\n"
    "ds_read_b128 a[252:255], v252 offset:22512\n"
    "v_xor_b32_e32 v213, 32, v223\n"
    "v_lshl_add_u32 v216, s51, 8, v209\n"
    "v_pk_add_f32 v[224:225], v[224:225], v[56:57]\n"
    "v_pk_add_f32 v[226:227], v[226:227], v[58:59]\n"
    "v_pk_fma_f32 v[228:229], v[56:57], v[56:57], v[228:229]\n"
    "v_pk_fma_f32 v[230:231], v[58:59], v[58:59], v[230:231]\n"
    "v_pk_add_f32 v[224:225], v[224:225], v[60:61]\n"
    "v_pk_add_f32 v[226:227], v[226:227], v[62:63]\n"
    "v_pk_fma_f32 v[228:229], v[60:61], v[60:61], v[228:229]\n"
    "v_pk_fma_f32 v[230:231], v[62:63], v[62:63], v[230:231]\n"
    "ds_read_b64_tr_b16 v[194:195], v218 offset:9344\n"
    "s_waitcnt lgkmcnt(6)\n"
    "v_smfmac_f32_16x16x64_bf16 v[64:67], a[232:235], a[224:231], v240\n"
    "s_waitcnt lgkmcnt(5)\n"
    "ds_read_b128 a[232:235], v252 offset:22848\n"
    "v_smfmac_f32_16x16x64_bf16 v[68:71], a[236:239], a[224:231], v240 abid:1\n"
    "ds_read_b128 a[224:227], v250 offset:54848\n"
    "ds_read_b128 a[228:231], v250 offset:55168\n"
    "ds_read_b128 a[236:239], v252 offset:23184\n"
    "v_add_u32_e64 v98, v201, v98\n"
    "ds_read_b64_tr_b16 a[80:81], v220 offset:12288\n"
    "v_pk_add_f32 v[224:225], v[224:225], v[64:65]\n"
    "v_pk_add_f32 v[226:227], v[226:227], v[66:67]\n"
    "v_pk_fma_f32 v[228:229], v[64:65], v[64:65], v[228:229]\n"
    "v_pk_fma_f32 v[230:231], v[66:67], v[66:67], v[230:231]\n"
    "v_pk_add_f32 v[224:225], v[224:225], v[68:69]\n"
    "v_pk_add_f32 v[226:227], v[226:227], v[70:71]\n"
    "v_pk_fma_f32 v[228:229], v[68:69], v[68:69], v[228:229]\n"
    "v_pk_fma_f32 v[230:231], v[70:71], v[70:71], v[230:231]\n"
    "s_waitcnt lgkmcnt(7)\n"
    "v_smfmac_f32_16x16x64_bf16 v[72:75], a[248:251], a[240:247], v240\n"
    "s_waitcnt lgkmcnt(5)\n"
    "ds_read_b128 a[248:251], v252 offset:23520\n"
    "v_smfmac_f32_16x16x64_bf16 v[76:79], a[252:255], a[240:247], v240 abid:1\n"
    "s_nop 0\n"
    "ds_read_b128 a[240:243], v250 offset:54928\n"
    "ds_read_b128 a[244:247], v250 offset:55248\n"
    "ds_read_b128 a[252:255], v252 offset:23856\n"
    "s_waitcnt vmcnt(15)\n"
    "v_or3_b32 v200, v200, v100, v101\n"
    "v_pk_add_f32 v[224:225], v[224:225], v[72:73]\n"
    "v_pk_add_f32 v[226:227], v[226:227], v[74:75]\n"
    "v_pk_fma_f32 v[228:229], v[72:73], v[72:73], v[228:229]\n"
    "v_pk_fma_f32 v[230:231], v[74:75], v[74:75], v[230:231]\n"
    "v_pk_add_f32 v[224:225], v[224:225], v[76:77]\n"
    "v_pk_add_f32 v[226:227], v[226:227], v[78:79]\n"
    "v_pk_fma_f32 v[228:229], v[76:77], v[76:77], v[228:229]\n"
    "v_pk_fma_f32 v[230:231], v[78:79], v[78:79], v[230:231]\n"
    "s_waitcnt lgkmcnt(6)\n"
    "v_smfmac_f32_16x16x64_bf16 v[80:83], a[232:235], a[224:231], v240\n"
    "s_waitcnt lgkmcnt(5)\n"
    "ds_read_b64_tr_b16 a[82:83], v220 offset:15360\n"
    "v_smfmac_f32_16x16x64_bf16 v[84:87], a[236:239], a[224:231], v240 abid:1\n"
    "ds_read_b64_tr_b16 a[84:85], v220 offset:12296\n"
    "ds_read_b64_tr_b16 a[86:87], v220 offset:15368\n"
    "ds_read_b64_tr_b16 a[88:89], v220 offset:12352\n"
    "v_or3_b32 v200, v200, v102, v103\n"
    "ds_read_b64_tr_b16 a[90:91], v220 offset:15424\n"
    "v_pk_add_f32 v[224:225], v[224:225], v[80:81]\n"
    "v_pk_add_f32 v[226:227], v[226:227], v[82:83]\n"
    "v_pk_fma_f32 v[228:229], v[80:81], v[80:81], v[228:229]\n"
    "v_pk_fma_f32 v[230:231], v[82:83], v[82:83], v[230:231]\n"
    "v_pk_add_f32 v[224:225], v[224:225], v[84:85]\n"
    "v_pk_add_f32 v[226:227], v[226:227], v[86:87]\n"
    "v_pk_fma_f32 v[228:229], v[84:85], v[84:85], v[228:229]\n"
    "v_pk_fma_f32 v[230:231], v[86:87], v[86:87], v[230:231]\n"
    "s_waitcnt lgkmcnt(6)\n"
    "v_smfmac_f32_16x16x64_bf16 v[88:91], a[248:251], a[240:247], v240\n"
    "s_waitcnt lgkmcnt(5)\n"
    "ds_read_b64_tr_b16 a[92:93], v220 offset:12360\n"
    "v_smfmac_f32_16x16x64_bf16 v[92:95], a[252:255], a[240:247], v240 abid:1\n"
    "ds_read_b64_tr_b16 a[94:95], v220 offset:15432\n"
    "ds_read_b64_tr_b16 v[196:197], v218 offset:12416\n"
    "ds_read_b64_tr_b16 v[198:199], v218 offset:15488\n"
    "v_add_u32_e64 v100, v203, v100\n"
    "v_add_u32_e64 v102, v203, v102\n"
    "v_pk_add_f32 v[224:225], v[224:225], v[88:89]\n"
    "v_pk_add_f32 v[226:227], v[226:227], v[90:91]\n"
    "v_pk_fma_f32 v[228:229], v[88:89], v[88:89], v[228:229]\n"
    "v_pk_fma_f32 v[230:231], v[90:91], v[90:91], v[230:231]\n"
    "v_pk_add_f32 v[224:225], v[224:225], v[92:93]\n"
    "v_pk_add_f32 v[226:227], v[226:227], v[94:95]\n"
    "v_pk_fma_f32 v[228:229], v[92:93], v[92:93], v[228:229]\n"
    "v_pk_fma_f32 v[230:231], v[94:95], v[94:95], v[230:231]\n"
    "ds_write_b128 v254, v[224:227]\n"
    "v_or3_b32 v200, v200, v104, v105\n"
    "v_or3_b32 v200, v200, v106, v107\n"
    "v_add_u32_e64 v104, v205, v104\n"
    "v_add_u32_e64 v106, v205, v106\n"
    "v_or3_b32 v200, v200, v108, v109\n"
    "v_or3_b32 v200, v200, v110, v111\n"
    "ds_write_b128 v254, v[228:231] offset:1024\n"
    "v_lshrrev_b32_e64 v254, 2, v242\n"
    "v_add_u32_e64 v254, s55, v254\n"
    "s_waitcnt lgkmcnt(2)\n"
    "s_barrier\n"
    "ds_write_b128 v217, v[96:99] offset:24576\n"
    "s_waitcnt vmcnt(1)\n"
    "v_lshlrev_b32_e32 v235, 3, v244\n"
    "s_mov_b32 exec_hi, 0\n"
    "buffer_load_dword v233, v235, s[40:43], s53 offen sc1\n"
    "s_mov_b32 exec_hi, -1\n"
    "s_add_u32 s53, s53, 393216\n"
    "v_lshlrev_b32_e64 v235, 3, v244\n"
    "s_mov_b32 exec_hi, 0\n"
    "buffer_load_dword v233, v235, s[40:43], s53 offen sc1\n"
    "s_mov_b32 exec_hi, -1\n"
    "s_add_u32 s53, s53, 393216\n"
    "v_lshlrev_b32_e64 v235, 3, v244\n"
    "s_mov_b32 exec_hi, 0\n"
    "buffer_load_dword v233, v235, s[40:43], s53 offen sc1\n"
    "s_mov_b32 exec_hi, -1\n"
    "s_add_u32 s53, s53, 393216\n"
    "v_lshlrev_b32_e32 v235, 3, v244\n"
    "s_mov_b32 exec_hi, 0\n"
    "buffer_load_dword v233, v235, s[40:43], s53 offen sc1\n"
    "s_mov_b32 exec_hi, -1\n"
    "s_add_u32 s53, s53, 393216\n"
    "s_waitcnt vmcnt(4)\n"
    "v_accvgpr_write_b32 a248, v236\n"
    "v_accvgpr_write_b32 a249, v237\n"
    "v_accvgpr_write_b32 a250, v238\n"
    "v_accvgpr_write_b32 a251, v239\n"
    "s_waitcnt lgkmcnt(1)\n"
    "s_barrier\n"
    "ds_read_b32 v224, v254 offset:0\n"
    "ds_read_b32 v225, v254 offset:6752\n"
    "ds_read_b32 v226, v254 offset:13504\n"
    "v_add_u32_e64 v108, v201, v108\n"
    "ds_read_b32 v227, v254 offset:20256\n"
    "ds_read_b32 v228, v254 offset:1024\n"
    "ds_read_b32 v229, v254 offset:7776\n"
    "v_add_u32_e64 v110, v201, v110\n"
    "ds_read_b32 v230, v254 offset:14528\n"
    "ds_read_b32 v231, v254 offset:21280\n"
    "ds_write_b128 v217, v[100:103] offset:24832\n"
    "v_or3_b32 v200, v200, v112, v113\n"
    "v_or3_b32 v200, v200, v114, v115\n"
    "v_add_u32_e64 v112, v203, v112\n"
    "v_add_u32_e64 v114, v203, v114\n"
    "v_or3_b32 v200, v200, v116, v117\n"
    "v_or3_b32 v200, v200, v118, v119\n"
    "v_add_u32_e64 v116, v205, v116\n"
    "v_add_u32_e64 v118, v205, v118\n"
    "v_or3_b32 v200, v200, v120, v121\n"
    "v_or3_b32 v200, v200, v122, v123\n"
    "ds_write_b128 v217, v[104:107] offset:25088\n"
    "s_waitcnt lgkmcnt(6)\n"
    "v_add_f32_e32 v224, v224, v225\n"
    "v_add_f32_e64 v226, v226, v227\n"
    "v_add_f32_e32 v224, v224, v226\n"
    "s_waitcnt lgkmcnt(4)\n"
    "v_add_f32_e64 v228, v228, v229\n"
    "v_mul_f32_e32 v224, 0x3c2aaaab, v224\n"
    "ds_write_b128 v217, v[108:111] offset:0\n"
    "s_waitcnt lgkmcnt(2)\n"
    "v_add_f32_e32 v230, v230, v231\n"
    "v_add_f32_e64 v228, v228, v230\n"
    "v_mul_f32_e32 v228, 0x3c2aaaab, v228\n"
    "v_fma_f32 v228, -v224, v224, v228\n"
    "v_max_f32_e64 v228, 0, v228\n"
    "v_add_f32_e32 v228, 0x3727c5ac, v228\n"
    "v_rsq_f32_e64 v228, v228\n"
    "ds_write_b128 v217, v[112:115] offset:256\n"
    "v_mul_f32_e64 v228, s50, v228\n"
    "v_mul_f32_e64 v224, v228, -v224\n"
    "s_waitcnt lgkmcnt(2)\n"
    "s_barrier\n"
    "ds_write_b32 v254, v228\n"
    "v_add_u32_e64 v120, v201, v120\n"
    "v_add_u32_e64 v122, v201, v122\n"
    "ds_write_b32 v254, v224 offset:1024\n"
    "v_add_u32_e64 v254, s56, v242\n"
    "ds_read_b64_tr_b16 a[96:97], v208 offset:24576\n"
    "ds_read_b64_tr_b16 v[180:181], v216 offset:24576\n"
    "ds_read_b64_tr_b16 a[100:101], v210 offset:24576\n"
    "ds_read_b64_tr_b16 v[184:185], v221 offset:24576\n"
    "v_or3_b32 v200, v200, v124, v125\n"
    "ds_read_b64_tr_b16 a[104:105], v212 offset:24576\n"
    "ds_read_b64_tr_b16 a[108:109], v213 offset:24576\n"
    "ds_read_b64_tr_b16 a[112:113], v214 offset:24576\n"
    "v_or3_b32 v200, v200, v126, v127\n"
    "ds_read_b64_tr_b16 a[116:117], v215 offset:24576\n"
    "ds_read_b64_tr_b16 a[120:121], v208 offset:24832\n"
    "ds_read_b64_tr_b16 a[124:125], v210 offset:24832\n"
    "v_add_u32_e64 v124, v203, v124\n"
    "ds_write_b128 v217, v[116:119] offset:512\n"
    "s_waitcnt lgkmcnt(11)\n"
    "s_barrier\n"
    "ds_read_b128 v[224:227], v254\n"
    "ds_read_b128 v[228:231], v254 offset:1024\n"
    "ds_write_b128 v217, v[120:123] offset:12288\n"
    "v_add_u32_e64 v126, v203, v126\n"
    "v_or3_b32 v200, v200, v128, v129\n"
    "v_or3_b32 v200, v200, v130, v131\n"
    "v_add_u32_e64 v128, v205, v128\n"
    "v_add_u32_e64 v130, v205, v130\n"
    "v_or3_b32 v200, v200, v132, v133\n"
    "v_or3_b32 v200, v200, v134, v135\n"
    "v_add_u32_e64 v132, v201, v132\n"
    "v_add_u32_e64 v134, v201, v134\n"
    "v_or3_b32 v200, v200, v136, v137\n"
    "ds_write_b128 v217, v[124:127] offset:12544\n"
    "s_waitcnt lgkmcnt(4)\n"
    "s_barrier\n"
    "ds_read_b64_tr_b16 a[98:99], v208 offset:0\n"
    "ds_read_b64_tr_b16 v[182:183], v216 offset:0\n"
    "ds_read_b64_tr_b16 a[102:103], v210 offset:0\n"
    "v_or3_b32 v200, v200, v138, v139\n"
    "s_nop 0\n"
    "s_waitcnt lgkmcnt(5)\n"
    "v_pk_fma_f32 v[64:65], v[64:65], v[224:225], v[228:229]\n"
    "v_pk_fma_f32 v[66:67], v[66:67], v[226:227], v[230:231]\n"
    "v_pk_fma_f32 v[68:69], v[68:69], v[224:225], v[228:229]\n"
    "v_pk_fma_f32 v[70:71], v[70:71], v[226:227], v[230:231]\n"
    "v_pk_fma_f32 v[72:73], v[72:73], v[224:225], v[228:229]\n"
    "v_pk_fma_f32 v[74:75], v[74:75], v[226:227], v[230:231]\n"
    "v_pk_fma_f32 v[76:77], v[76:77], v[224:225], v[228:229]\n"
    "v_pk_fma_f32 v[78:79], v[78:79], v[226:227], v[230:231]\n"
    "v_pk_fma_f32 v[80:81], v[80:81], v[224:225], v[228:229]\n"
    "v_pk_fma_f32 v[82:83], v[82:83], v[226:227], v[230:231]\n"
    "v_pk_fma_f32 v[84:85], v[84:85], v[224:225], v[228:229]\n"
    "v_pk_fma_f32 v[86:87], v[86:87], v[226:227], v[230:231]\n"
    "v_pk_fma_f32 v[88:89], v[88:89], v[224:225], v[228:229]\n"
    "v_pk_fma_f32 v[90:91], v[90:91], v[226:227], v[230:231]\n"
    "v_pk_fma_f32 v[92:93], v[92:93], v[224:225], v[228:229]\n"
    "v_pk_fma_f32 v[94:95], v[94:95], v[226:227], v[230:231]\n"
    "v_cvt_pk_bf16_f32 v96, v64, v68\n"
    "v_cvt_pk_bf16_f32 v100, v65, v69\n"
    "v_cvt_pk_bf16_f32 v104, v66, v70\n"
    "v_cvt_pk_bf16_f32 v108, v67, v71\n"
    "v_cvt_pk_bf16_f32 v97, v72, v76\n"
    "v_cvt_pk_bf16_f32 v101, v73, v77\n"
    "v_cvt_pk_bf16_f32 v105, v74, v78\n"
    "v_cvt_pk_bf16_f32 v109, v75, v79\n"
    "v_cvt_pk_bf16_f32 v98, v80, v84\n"
    "v_cvt_pk_bf16_f32 v102, v81, v85\n"
    "v_cvt_pk_bf16_f32 v106, v82, v86\n"
    "v_cvt_pk_bf16_f32 v110, v83, v87\n"
    "v_cvt_pk_bf16_f32 v99, v88, v92\n"
    "ds_write_b128 v253, v[96:99] offset:2048\n"
    "v_cvt_pk_bf16_f32 v103, v89, v93\n"
    "v_cvt_pk_bf16_f32 v107, v90, v94\n"
    "v_cvt_pk_bf16_f32 v111, v91, v95\n"
    "v_pk_fma_f32 v[0:1], v[0:1], v[224:225], v[228:229]\n"
    "v_pk_fma_f32 v[2:3], v[2:3], v[226:227], v[230:231]\n"
    "v_pk_fma_f32 v[4:5], v[4:5], v[224:225], v[228:229]\n"
    "ds_write_b128 v253, v[100:103] offset:5120\n"
    "v_pk_fma_f32 v[6:7], v[6:7], v[226:227], v[230:231]\n"
    "v_pk_fma_f32 v[8:9], v[8:9], v[224:225], v[228:229]\n"
    "v_pk_fma_f32 v[10:11], v[10:11], v[226:227], v[230:231]\n"
    "v_pk_fma_f32 v[12:13], v[12:13], v[224:225], v[228:229]\n"
    "v_pk_fma_f32 v[14:15], v[14:15], v[226:227], v[230:231]\n"
    "v_pk_fma_f32 v[16:17], v[16:17], v[224:225], v[228:229]\n"
    "v_pk_fma_f32 v[18:19], v[18:19], v[226:227], v[230:231]\n"
    "v_pk_fma_f32 v[20:21], v[20:21], v[224:225], v[228:229]\n"
    "v_pk_fma_f32 v[22:23], v[22:23], v[226:227], v[230:231]\n"
    "v_pk_fma_f32 v[24:25], v[24:25], v[224:225], v[228:229]\n"
    "v_pk_fma_f32 v[26:27], v[26:27], v[226:227], v[230:231]\n"
    "v_pk_fma_f32 v[28:29], v[28:29], v[224:225], v[228:229]\n"
    "v_pk_fma_f32 v[30:31], v[30:31], v[226:227], v[230:231]\n"
    "v_cvt_pk_bf16_f32 v80, v0, v4\n"
    "v_cvt_pk_bf16_f32 v84, v1, v5\n"
    "v_cvt_pk_bf16_f32 v88, v2, v6\n"
    "v_cvt_pk_bf16_f32 v92, v3, v7\n"
    "v_cvt_pk_bf16_f32 v81, v8, v12\n"
    "v_cvt_pk_bf16_f32 v85, v9, v13\n"
    "v_cvt_pk_bf16_f32 v89, v10, v14\n"
    "v_cvt_pk_bf16_f32 v93, v11, v15\n"
    "v_cvt_pk_bf16_f32 v82, v16, v20\n"
    "v_cvt_pk_bf16_f32 v86, v17, v21\n"
    "v_cvt_pk_bf16_f32 v90, v18, v22\n"
    "v_cvt_pk_bf16_f32 v94, v19, v23\n"
    "v_cvt_pk_bf16_f32 v83, v24, v28\n"
    "ds_write_b128 v253, v[80:83] offset:0\n"
    "v_cvt_pk_bf16_f32 v87, v25, v29\n"
    "v_cvt_pk_bf16_f32 v91, v26, v30\n"
    "v_cvt_pk_bf16_f32 v95, v27, v31\n"
    "v_pk_fma_f32 v[32:33], v[32:33], v[224:225], v[228:229]\n"
    "v_pk_fma_f32 v[34:35], v[34:35], v[226:227], v[230:231]\n"
    "v_pk_fma_f32 v[36:37], v[36:37], v[224:225], v[228:229]\n"
    "ds_write_b128 v253, v[84:87] offset:3072\n"
    "v_pk_fma_f32 v[38:39], v[38:39], v[226:227], v[230:231]\n"
    "v_pk_fma_f32 v[40:41], v[40:41], v[224:225], v[228:229]\n"
    "v_pk_fma_f32 v[42:43], v[42:43], v[226:227], v[230:231]\n"
    "v_pk_fma_f32 v[44:45], v[44:45], v[224:225], v[228:229]\n"
    "v_pk_fma_f32 v[46:47], v[46:47], v[226:227], v[230:231]\n"
    "v_pk_fma_f32 v[48:49], v[48:49], v[224:225], v[228:229]\n"
    "v_pk_fma_f32 v[50:51], v[50:51], v[226:227], v[230:231]\n"
    "v_pk_fma_f32 v[52:53], v[52:53], v[224:225], v[228:229]\n"
    "v_pk_fma_f32 v[54:55], v[54:55], v[226:227], v[230:231]\n"
    "v_pk_fma_f32 v[56:57], v[56:57], v[224:225], v[228:229]\n"
    "v_pk_fma_f32 v[58:59], v[58:59], v[226:227], v[230:231]\n"
    "v_pk_fma_f32 v[60:61], v[60:61], v[224:225], v[228:229]\n"
    "v_pk_fma_f32 v[62:63], v[62:63], v[226:227], v[230:231]\n"
    "v_cvt_pk_bf16_f32 v64, v32, v36\n"
    "v_cvt_pk_bf16_f32 v68, v33, v37\n"
    "v_cvt_pk_bf16_f32 v72, v34, v38\n"
    "v_cvt_pk_bf16_f32 v76, v35, v39\n"
    "v_cvt_pk_bf16_f32 v65, v40, v44\n"
    "v_cvt_pk_bf16_f32 v69, v41, v45\n"
    "v_cvt_pk_bf16_f32 v73, v42, v46\n"
    "v_cvt_pk_bf16_f32 v77, v43, v47\n"
    "v_cvt_pk_bf16_f32 v66, v48, v52\n"
    "v_cvt_pk_bf16_f32 v70, v49, v53\n"
    "v_cvt_pk_bf16_f32 v74, v50, v54\n"
    "v_cvt_pk_bf16_f32 v78, v51, v55\n"
    "v_cvt_pk_bf16_f32 v67, v56, v60\n"
    "ds_write_b128 v253, v[64:67] offset:1024\n"
    "v_cvt_pk_bf16_f32 v71, v57, v61\n"
    "v_cvt_pk_bf16_f32 v75, v58, v62\n"
    "v_cvt_pk_bf16_f32 v79, v59, v63\n"
    "v_mov_b32_e64 v64, v104\n"
    "v_mov_b32_e64 v65, v105\n"
    "v_mov_b32_e64 v66, v106\n"
    "ds_write_b128 v253, v[68:71] offset:4096\n"
    "v_mov_b32_e64 v67, v107\n"
    "v_mov_b32_e64 v68, v108\n"
    "v_mov_b32_e64 v69, v109\n"
    "v_mov_b32_e64 v70, v110\n"
    "v_mov_b32_e64 v71, v111\n"
    "v_add_u32_e64 v136, v203, v136\n"
    "ds_read_b64_tr_b16 v[186:187], v221 offset:0\n"
    "ds_read_b64_tr_b16 a[106:107], v212 offset:0\n"
    "ds_read_b64_tr_b16 a[110:111], v213 offset:0\n"
    "ds_read_b64_tr_b16 a[114:115], v214 offset:0\n"
    "v_add_u32_e64 v138, v203, v138\n"
    "ds_read_b64_tr_b16 a[118:119], v215 offset:0\n"
    "ds_read_b64_tr_b16 a[122:123], v208 offset:256\n"
    "ds_read_b64_tr_b16 a[126:127], v210 offset:256\n"
    "v_or3_b32 v200, v200, v140, v141\n"
    "ds_write_b128 v217, v[128:131] offset:12800\n"
    "v_or3_b32 v200, v200, v142, v143\n"
    "v_add_u32_e64 v140, v205, v140\n"
    "v_add_u32_e64 v142, v205, v142\n"
    "v_or3_b32 v200, v200, v144, v145\n"
    "v_or3_b32 v200, v200, v146, v147\n"
    "v_add_u32_e64 v144, v201, v144\n"
    "v_add_u32_e64 v146, v201, v146\n"
    "s_mov_b32 s64, 0\n"
    "s_mov_b32 s59, 0\n"
    "ds_write_b128 v217, v[132:135] offset:24576\n"
    "v_or3_b32 v200, v200, v148, v149\n"
    "v_or3_b32 v200, v200, v150, v151\n"
    "v_add_u32_e64 v148, v203, v148\n"
    "v_add_u32_e64 v150, v203, v150\n"
    "v_or3_b32 v200, v200, v152, v153\n"
    "v_or3_b32 v200, v200, v154, v155\n"
    "v_add_u32_e64 v152, v205, v152\n"
    "v_add_u32_e64 v154, v205, v154\n"
    "ds_write_b128 v217, v[136:139] offset:24832\n"
    "s_waitcnt lgkmcnt(2)\n"
    "s_barrier\n"
    "ds_read_b64_tr_b16 a[128:129], v208 offset:12288\n"
    "ds_read_b64_tr_b16 a[132:133], v209 offset:12288\n"
    "ds_read_b64_tr_b16 a[136:137], v210 offset:12288\n"
    "v_or3_b32 v200, v200, v156, v157\n"
    "ds_read_b64_tr_b16 a[140:141], v211 offset:12288\n"
    "ds_read_b64_tr_b16 a[144:145], v212 offset:12288\n"
    "ds_read_b64_tr_b16 a[148:149], v213 offset:12288\n"
    "v_or3_b32 v200, v200, v158, v159\n"
    "ds_read_b64_tr_b16 a[152:153], v214 offset:12288\n"
    "ds_read_b64_tr_b16 a[156:157], v215 offset:12288\n"
    "ds_read_b64_tr_b16 a[160:161], v208 offset:12544\n"
    "v_add_u32_e64 v156, v201, v156\n"
    "ds_read_b64_tr_b16 a[164:165], v209 offset:12544\n"
    "ds_read_b64_tr_b16 a[168:169], v210 offset:12544\n"
    "ds_read_b64_tr_b16 a[172:173], v211 offset:12544\n"
    "v_add_u32_e64 v158, v201, v158\n"
    "ds_write_b128 v217, v[140:143] offset:25088\n"
    "v_or3_b32 v200, v200, v160, v161\n"
    "v_or3_b32 v200, v200, v162, v163\n"
    "v_add_u32_e64 v160, v203, v160\n"
    "v_add_u32_e64 v162, v203, v162\n"
    "v_or3_b32 v200, v200, v164, v165\n"
    "v_or3_b32 v200, v200, v166, v167\n"
    "v_add_u32_e64 v164, v205, v164\n"
    "v_add_u32_e64 v166, v205, v166\n"
    "ds_write_b128 v217, v[144:147] offset:0\n"
    "v_and_b32_e32 v200, 0x40004000, v200\n"
    "v_and_b32_e64 v246, s67, v246\n"
    "v_and_b32_e64 v247, s67, v247\n"
    "v_add_u32_e64 v246, s98, v246\n"
    "v_add_u32_e64 v247, s98, v247\n"
    "v_add_u32_e64 v245, s99, v245\n"
    "ds_write_b128 v217, v[148:151] offset:256\n"
    "s_waitcnt lgkmcnt(2)\n"
    "s_barrier\n"
    "ds_read_b64_tr_b16 a[130:131], v208 offset:24576\n"
    "ds_read_b64_tr_b16 a[134:135], v209 offset:24576\n"
    "ds_read_b64_tr_b16 a[138:139], v210 offset:24576\n"
    "v_mov_b32_e64 v235, v222\n"
    "ds_read_b64_tr_b16 a[142:143], v211 offset:24576\n"
    "ds_read_b64_tr_b16 a[146:147], v212 offset:24576\n"
    "ds_read_b64_tr_b16 a[150:151], v213 offset:24576\n"
    "ds_read_b64_tr_b16 a[154:155], v214 offset:24576\n"
    "ds_read_b64_tr_b16 a[158:159], v215 offset:24576\n"
    "ds_read_b64_tr_b16 a[162:163], v208 offset:24832\n"
    "ds_read_b64_tr_b16 a[166:167], v209 offset:24832\n"
    "ds_read_b64_tr_b16 a[170:171], v210 offset:24832\n"
    "ds_read_b64_tr_b16 a[174:175], v211 offset:24832\n"
    "ds_write_b128 v217, v[152:155] offset:512\n"
    "ds_write_b128 v217, v[156:159] offset:12288\n"
    "ds_write_b128 v207, v[184:187] offset:36864\n"
    "s_waitcnt lgkmcnt(2)\n"
    "s_barrier\n"
    "ds_write_b128 v234, a[216:219] offset:0\n"
    "ds_write_b128 v234, a[220:223] offset:16\n"
    "ds_read_b64_tr_b16 a[176:177], v208 offset:0\n"
    "ds_read_b64_tr_b16 a[180:181], v209 offset:0\n"
    "ds_read_b64_tr_b16 a[184:185], v210 offset:0\n"
    "ds_read_b64_tr_b16 a[188:189], v211 offset:0\n"
    "ds_read_b64_tr_b16 a[192:193], v212 offset:0\n"
    "ds_read_b64_tr_b16 a[196:197], v213 offset:0\n"
    "ds_read_b64_tr_b16 a[200:201], v214 offset:0\n"
    "ds_read_b64_tr_b16 a[204:205], v215 offset:0\n"
    "ds_read_b64_tr_b16 a[208:209], v208 offset:256\n"
    "ds_read_b64_tr_b16 a[212:213], v209 offset:256\n"
    "ds_read_b64_tr_b16 a[216:217], v210 offset:256\n"
    "ds_read_b64_tr_b16 a[220:221], v211 offset:256\n"
    "ds_write_b128 v207, v[180:183] offset:32768\n"
    "ds_write_b128 v217, v[160:163] offset:12544\n"
    "ds_write_b128 v217, v[164:167] offset:12800\n"
    "v_cmp_le_u32_e32 vcc, 1, v200\n"
    "s_nop 1\n"
    "s_cmp_lg_u64 vcc, 0\n"
    "s_cselect_b32 vcc_lo, 1, 0\n"
    "s_or_b32 s66, s66, vcc_lo\n"
    "v_cmp_le_u32_e32 vcc, 981467136, v232\n"
    "s_nop 1\n"
    "s_cmp_lg_u64 vcc, 0\n"
    "s_cselect_b32 vcc_lo, 2, 0\n"
    "s_or_b32 s66, s66, vcc_lo\n"
    "v_mov_b32_e32 v200, s66\n"
    "v_mov_b32_e32 v232, 162992\n"
    "s_mov_b64 exec, 1\n"
    "ds_or_b32 v232, v200\n"
    "s_mov_b64 exec, -1\n"
    "s_waitcnt lgkmcnt(0)\n"
    "s_barrier\n"
    "ds_read_b64_tr_b16 a[178:179], v208 offset:12288\n"
    "ds_read_b64_tr_b16 a[182:183], v209 offset:12288\n"
    "ds_read_b64_tr_b16 a[186:187], v210 offset:12288\n"
    "ds_read_b64_tr_b16 a[190:191], v211 offset:12288\n"
    "ds_read_b64_tr_b16 a[194:195], v212 offset:12288\n"
    "ds_read_b64_tr_b16 a[198:199], v213 offset:12288\n"
    "ds_read_b64_tr_b16 a[202:203], v214 offset:12288\n"
    "ds_read_b64_tr_b16 a[206:207], v215 offset:12288\n"
    "ds_read_b64_tr_b16 a[210:211], v208 offset:12544\n"
    "ds_read_b64_tr_b16 a[214:215], v209 offset:12544\n"
    "ds_read_b64_tr_b16 a[218:219], v210 offset:12544\n"
    "ds_read_b64_tr_b16 a[222:223], v211 offset:12544\n"
    "ds_write_b128 v207, v[168:171] offset:0\n"
    "ds_write_b128 v207, v[172:175] offset:4096\n"
    "ds_write_b128 v207, v[176:179] offset:8192\n"
    "ds_read_b32 v232, v232\n"
    "s_waitcnt lgkmcnt(1)\n"
    "s_barrier\n"
    "ds_read_b128 v[144:147], v242 offset:0\n"
    "ds_read_b128 v[148:151], v242 offset:1024\n"
    "ds_read_b128 v[152:155], v242 offset:2048\n"
    "ds_read_b128 v[156:159], v242 offset:3072\n"
    "ds_read_b128 v[160:163], v242 offset:4096\n"
    "ds_read_b128 v[164:167], v242 offset:5120\n"
    "ds_read_b128 a[240:243], v244 offset:32768\n"
    "ds_write_b128 v207, v[188:191] offset:12288\n"
    "ds_write_b128 v207, v[192:195] offset:16384\n"
    "s_waitcnt lgkmcnt(8)\n"
    "v_mfma_f32_16x16x32_bf16 v[168:171], a[128:131], v[144:147], 0\n"
    "s_waitcnt lgkmcnt(7)\n"
    "v_mfma_f32_16x16x32_bf16 v[168:171], a[176:179], v[148:151], v[168:171]\n"
    "s_waitcnt lgkmcnt(6)\n"
    "v_mfma_f32_16x16x32_bf16 v[168:171], a[96:99], v[152:155], v[168:171]\n"
    "ds_write_b128 v207, v[196:199] offset:20480\n"
    "v_mfma_f32_16x16x32_bf16 v[172:175], a[132:135], v[144:147], 0\n"
    "s_waitcnt lgkmcnt(5)\n"
    "v_mfma_f32_16x16x32_bf16 v[172:175], a[180:183], v[148:151], v[172:175]\n"
    "s_waitcnt lgkmcnt(3)\n"
    "v_mfma_f32_16x16x32_bf16 v[172:175], a[240:243], v[152:155], v[172:175]\n"
    "v_mfma_f32_16x16x32_bf16 v[176:179], a[128:131], v[156:159], 0\n"
    "v_mfma_f32_16x16x32_bf16 v[176:179], a[176:179], v[160:163], v[176:179]\n"
    "v_mfma_f32_16x16x32_bf16 v[176:179], a[96:99], v[164:167], v[176:179]\n"
    "v_mfma_f32_16x16x32_bf16 v[180:183], a[132:135], v[156:159], 0\n"
    "v_mfma_f32_16x16x32_bf16 v[180:183], a[180:183], v[160:163], v[180:183]\n"
    "s_waitcnt lgkmcnt(0)\n"
    "s_barrier\n"
    "v_mfma_f32_16x16x32_bf16 v[180:183], a[240:243], v[164:167], v[180:183]\n"
    "s_getpc_b64 s[76:77]\n"
    ".Lreplk_pc1:\n"
    "s_add_u32 s76, s76, .Lreplk_last-.Lreplk_pc1\n"
    "s_addc_u32 s77, s77, 0\n"
    "s_sub_u32 s76, s76, s36\n"
    "s_subb_u32 s77, s77, 0\n"
    "s_add_u32 s78, s36, .Lreplk_end-.Lreplk_last\n"
    "s_mov_b32 s79, s37\n"
    "v_readfirstlane_b32 s66, v232\n"
    "s_nop 1\n"
    "s_cmp_lg_u32 s66, 2\n"
    "s_cbranch_scc1 98f\n"
    ".p2align 6\n"
    "1:\n"
    "v_lshlrev_b32_sdwa v184, 1, v168 dst_sel:DWORD dst_unused:UNUSED_PAD src0_sel:DWORD src1_sel:WORD_1\n"
    "v_lshlrev_b32_sdwa v200, 1, v169 dst_sel:DWORD dst_unused:UNUSED_PAD src0_sel:DWORD src1_sel:WORD_1\n"
    "v_lshlrev_b32_sdwa v185, 1, v170 dst_sel:DWORD dst_unused:UNUSED_PAD src0_sel:DWORD src1_sel:WORD_1\n"
    "v_lshlrev_b32_sdwa v201, 1, v171 dst_sel:DWORD dst_unused:UNUSED_PAD src0_sel:DWORD src1_sel:WORD_1\n"
    "v_mfma_f32_16x16x32_bf16 v[140:143], a[244:247], v[188:191], v[140:143]\n"
    "ds_read_b128 a[240:243], v244 offset:36864\n"
    "ds_read_u16 v184, v184\n"
    "v_mfma_f32_16x16x32_bf16 v[168:171], a[136:139], v[144:147], 0\n"
    "ds_read_u16_d16_hi v200, v200\n"
    "ds_read_u16 v185, v185\n"
    "v_mfma_f32_16x16x32_bf16 v[168:171], a[184:187], v[148:151], v[168:171]\n"
    "buffer_load_dwordx4 v[0:3], v246, s[40:43], s60 offen offset:0\n"
    "buffer_load_dwordx4 v[32:35], v246, s[40:43], s60 offen offset:64\n"
    "ds_read_u16_d16_hi v201, v201\n"
    "v_mfma_f32_16x16x32_bf16 v[168:171], a[100:103], v[152:155], v[168:171]\n"
    "v_lshlrev_b32_sdwa v186, 1, v172 dst_sel:DWORD dst_unused:UNUSED_PAD src0_sel:DWORD src1_sel:WORD_1\n"
    "v_lshlrev_b32_sdwa v202, 1, v173 dst_sel:DWORD dst_unused:UNUSED_PAD src0_sel:DWORD src1_sel:WORD_1\n"
    "s_waitcnt vmcnt(4)\n"
    "v_smfmac_f32_32x32x32_bf16 v[128:143], a[248:251], a[224:231], v241\n"
    "s_nop 0\n"
    "ds_read_u16 v186, v186\n"
    "v_lshlrev_b32_sdwa v187, 1, v174 dst_sel:DWORD dst_unused:UNUSED_PAD src0_sel:DWORD src1_sel:WORD_1\n"
    "v_lshlrev_b32_sdwa v203, 1, v175 dst_sel:DWORD dst_unused:UNUSED_PAD src0_sel:DWORD src1_sel:WORD_1\n"
    "v_lshlrev_b32_sdwa v188, 1, v176 dst_sel:DWORD dst_unused:UNUSED_PAD src0_sel:DWORD src1_sel:WORD_1\n"
    "v_lshlrev_b32_sdwa v204, 1, v177 dst_sel:DWORD dst_unused:UNUSED_PAD src0_sel:DWORD src1_sel:WORD_1\n"
    "v_mfma_f32_16x16x32_bf16 v[172:175], a[140:143], v[144:147], 0\n"
    "ds_read_b128 v[236:239], v244 offset:20480\n"
    "ds_read_u16_d16_hi v202, v202\n"
    "v_mfma_f32_16x16x32_bf16 v[172:175], a[188:191], v[148:151], v[172:175]\n"
    "v_lshlrev_b32_sdwa v189, 1, v178 dst_sel:DWORD dst_unused:UNUSED_PAD src0_sel:DWORD src1_sel:WORD_1\n"
    "v_lshlrev_b32_sdwa v205, 1, v179 dst_sel:DWORD dst_unused:UNUSED_PAD src0_sel:DWORD src1_sel:WORD_1\n"
    "s_waitcnt lgkmcnt(7)\n"
    "v_mfma_f32_16x16x32_bf16 v[172:175], a[240:243], v[152:155], v[172:175]\n"
    "s_nop 0\n"
    "ds_read_u16 v187, v187\n"
    "v_lshlrev_b32_sdwa v190, 1, v180 dst_sel:DWORD dst_unused:UNUSED_PAD src0_sel:DWORD src1_sel:WORD_1\n"
    "v_mfma_f32_16x16x32_bf16 v[176:179], a[136:139], v[156:159], 0\n"
    "v_lshlrev_b32_sdwa v206, 1, v181 dst_sel:DWORD dst_unused:UNUSED_PAD src0_sel:DWORD src1_sel:WORD_1\n"
    "v_lshlrev_b32_sdwa v191, 1, v182 dst_sel:DWORD dst_unused:UNUSED_PAD src0_sel:DWORD src1_sel:WORD_1\n"
    "v_mfma_f32_16x16x32_bf16 v[176:179], a[184:187], v[160:163], v[176:179]\n"
    "ds_read_b128 a[244:247], v244 offset:22528\n"
    "v_lshlrev_b32_sdwa v207, 1, v183 dst_sel:DWORD dst_unused:UNUSED_PAD src0_sel:DWORD src1_sel:WORD_1\n"
    "v_mfma_f32_16x16x32_bf16 v[176:179], a[100:103], v[164:167], v[176:179]\n"
    "ds_read_u16_d16_hi v203, v203\n"
    "v_pk_fma_f16 v234, v234, s49, s49\n"
    "v_mfma_f32_16x16x32_bf16 v[180:183], a[140:143], v[156:159], 0\n"
    "s_waitcnt lgkmcnt(8)\n"
    "v_rcp_f16_e32 v232, v234\n"
    "v_mfma_f32_16x16x32_bf16 v[180:183], a[188:191], v[160:163], v[180:183]\n"
    "ds_read_u16 v188, v188\n"
    "v_rcp_f16_sdwa v232, v234 dst_sel:WORD_1 dst_unused:UNUSED_PRESERVE src0_sel:WORD_1\n"
    "v_mfma_f32_16x16x32_bf16 v[180:183], a[240:243], v[164:167], v[180:183]\n"
    "ds_read_u16_d16_hi v204, v204\n"
    "v_fma_mix_f32 v234, v196, v232, 0 op_sel_hi:[1,1,0]\n"
    "v_mfma_f32_16x16x32_bf16 v[96:99], a[80:83], v[192:195], v[96:99]\n"
    "v_fma_mix_f32 v232, v196, v232, 0 op_sel:[1,1,0] op_sel_hi:[1,1,0]\n"
    "v_or_b32_e32 v184, v184, v200\n"
    "v_mfma_f32_16x16x32_bf16 v[100:103], a[84:87], v[192:195], v[100:103]\n"
    "s_waitcnt lgkmcnt(8)\n"
    "ds_read_u16 v189, v189\n"
    "v_cvt_pk_bf16_f32 v196, v234, v232\n"
    "v_mfma_f32_16x16x32_bf16 v[112:115], a[88:91], v[192:195], v[112:115]\n"
    "s_waitcnt lgkmcnt(7)\n"
    "v_or_b32_e32 v185, v185, v201\n"
    "v_lshlrev_b32_sdwa v201, 1, v171 dst_sel:DWORD dst_unused:UNUSED_PAD src0_sel:DWORD src1_sel:WORD_1\n"
    "v_mfma_f32_16x16x32_bf16 v[116:119], a[92:95], v[192:195], v[116:119]\n"
    "ds_read_u16_d16_hi v205, v205\n"
    "v_cvt_pk_bf16_f32 v96, v96, v97\n"
    "v_mfma_f32_16x16x32_bf16 v[128:131], v[236:239], v[192:195], v[128:131]\n"
    "ds_read_u16 v190, v190\n"
    "buffer_load_dwordx4 v[20:23], v247, s[40:43], s60 offen offset:0\n"
    "buffer_load_dwordx4 v[52:55], v247, s[40:43], s60 offen offset:64\n"
    "v_mfma_f32_16x16x32_bf16 v[104:107], a[80:83], v[196:199], v[104:107]\n"
    "s_waitcnt lgkmcnt(8)\n"
    "v_or_b32_e64 v186, v186, v202\n"
    "v_lshlrev_b32_sdwa v202, 1, v173 dst_sel:DWORD dst_unused:UNUSED_PAD src0_sel:DWORD src1_sel:WORD_1\n"
    "v_mfma_f32_16x16x32_bf16 v[108:111], a[84:87], v[196:199], v[108:111]\n"
    "s_waitcnt lgkmcnt(6)\n"
    "ds_read_u16_d16_hi v206, v206\n"
    "v_cvt_pk_bf16_f32 v97, v98, v99\n"
    "v_mfma_f32_16x16x32_bf16 v[132:135], a[244:247], v[192:195], v[132:135]\n"
    "v_cvt_pk_f16_f32 v192, v168, v169\n"
    "v_lshlrev_b32_sdwa v193, 1, v170 dst_sel:DWORD dst_unused:UNUSED_PAD src0_sel:DWORD src1_sel:WORD_1\n"
    "v_mfma_f32_16x16x32_bf16 v[120:123], a[88:91], v[196:199], v[120:123]\n"
    "ds_read_u16 v191, v191\n"
    "v_lshlrev_b32_sdwa v194, 1, v172 dst_sel:DWORD dst_unused:UNUSED_PAD src0_sel:DWORD src1_sel:WORD_1\n"
    "v_mfma_f32_16x16x32_bf16 v[124:127], a[92:95], v[196:199], v[124:127]\n"
    "s_waitcnt lgkmcnt(7)\n"
    "ds_read_u16_d16_hi v207, v207\n"
    "v_lshlrev_b32_sdwa v195, 1, v174 dst_sel:DWORD dst_unused:UNUSED_PAD src0_sel:DWORD src1_sel:WORD_1\n"
    "v_mfma_f32_16x16x32_bf16 v[136:139], v[236:239], v[196:199], v[136:139]\n"
    "v_or_b32_e32 v187, v187, v203\n"
    "v_lshlrev_b32_sdwa v203, 1, v175 dst_sel:DWORD dst_unused:UNUSED_PAD src0_sel:DWORD src1_sel:WORD_1\n"
    "v_mfma_f32_16x16x32_bf16 v[140:143], a[244:247], v[196:199], v[140:143]\n"
    "ds_read_u16 v193, v193\n"
    "v_cvt_pk_f16_f32 v196, v176, v177\n"
    "v_mfma_f32_16x16x32_bf16 v[168:171], a[144:147], v[144:147], 0\n"
    "v_lshlrev_b32_sdwa v197, 1, v178 dst_sel:DWORD dst_unused:UNUSED_PAD src0_sel:DWORD src1_sel:WORD_1\n"
    "v_lshlrev_b32_sdwa v198, 1, v180 dst_sel:DWORD dst_unused:UNUSED_PAD src0_sel:DWORD src1_sel:WORD_1\n"
    "v_mfma_f32_16x16x32_bf16 v[168:171], a[192:195], v[148:151], v[168:171]\n"
    "s_waitcnt lgkmcnt(7)\n"
    "ds_read_u16_d16_hi v201, v201\n"
    "v_lshlrev_b32_sdwa v199, 1, v182 dst_sel:DWORD dst_unused:UNUSED_PAD src0_sel:DWORD src1_sel:WORD_1\n"
    "v_mfma_f32_16x16x32_bf16 v[168:171], a[104:107], v[152:155], v[168:171]\n"
    "v_or_b32_e32 v188, v188, v204\n"
    "v_cvt_pk_bf16_f32 v98, v100, v101\n"
    "v_mfma_f32_16x16x32_bf16 v[172:175], a[148:151], v[144:147], 0\n"
    "ds_read_u16 v194, v194\n"
    "v_cvt_pk_bf16_f32 v99, v102, v103\n"
    "v_mfma_f32_16x16x32_bf16 v[172:175], a[196:199], v[148:151], v[172:175]\n"
    "s_waitcnt lgkmcnt(7)\n"
    "ds_read_u16_d16_hi v202, v202\n"
    "v_or_b32_e32 v189, v189, v205\n"
    "v_mfma_f32_16x16x32_bf16 v[172:175], a[108:111], v[152:155], v[172:175]\n"
    "v_lshlrev_b32_sdwa v205, 1, v179 dst_sel:DWORD dst_unused:UNUSED_PAD src0_sel:DWORD src1_sel:WORD_1\n"
    "v_cvt_pk_bf16_f32 v112, v112, v113\n"
    "v_mfma_f32_16x16x32_bf16 v[176:179], a[144:147], v[156:159], 0\n"
    "ds_read_b128 v[236:239], v244 offset:0\n"
    "v_cvt_pk_bf16_f32 v113, v114, v115\n"
    "v_mfma_f32_16x16x32_bf16 v[176:179], a[192:195], v[160:163], v[176:179]\n"
    "ds_read_u16 v195, v195\n"
    "v_cvt_pk_bf16_f32 v114, v116, v117\n"
    "v_mfma_f32_16x16x32_bf16 v[176:179], a[104:107], v[164:167], v[176:179]\n"
    "s_waitcnt lgkmcnt(8)\n"
    "v_or_b32_e32 v190, v190, v206\n"
    "v_lshlrev_b32_sdwa v206, 1, v181 dst_sel:DWORD dst_unused:UNUSED_PAD src0_sel:DWORD src1_sel:WORD_1\n"
    "v_mfma_f32_16x16x32_bf16 v[100:103], a[4:7], v[184:187], 0\n"
    "ds_read_u16_d16_hi v203, v203\n"
    "v_cvt_pk_bf16_f32 v115, v118, v119\n"
    "v_cvt_pk_bf16_f32 v128, v128, v129\n"
    "v_cvt_pk_bf16_f32 v129, v130, v131\n"
    "v_cvt_pk_bf16_f32 v130, v132, v133\n"
    "ds_write_b128 v243, v[96:99] offset:24640\n"
    "buffer_load_dwordx4 v[4:7], v246, s[40:43], s61 offen offset:0\n"
    "buffer_load_dwordx4 v[36:39], v246, s[40:43], s61 offen offset:64\n"
    "v_mfma_f32_16x16x32_bf16 v[96:99], a[0:3], v[184:187], 0\n"
    "s_waitcnt lgkmcnt(7)\n"
    "v_or_b32_e32 v191, v191, v207\n"
    "v_lshlrev_b32_sdwa v207, 1, v183 dst_sel:DWORD dst_unused:UNUSED_PAD src0_sel:DWORD src1_sel:WORD_1\n"
    "v_mfma_f32_16x16x32_bf16 v[180:183], a[148:151], v[156:159], 0\n"
    "ds_read_b128 a[244:247], v244 offset:2048\n"
    "ds_write_b128 v243, v[112:115] offset:26688\n"
    "v_mfma_f32_16x16x32_bf16 v[180:183], a[196:199], v[160:163], v[180:183]\n"
    "s_waitcnt lgkmcnt(8)\n"
    "v_or_b32_e32 v193, v193, v201\n"
    "v_cvt_pk_bf16_f32 v131, v134, v135\n"
    "v_mfma_f32_16x16x32_bf16 v[180:183], a[108:111], v[164:167], v[180:183]\n"
    "v_cvt_pk_bf16_f32 v104, v104, v105\n"
    "v_cvt_pk_bf16_f32 v105, v106, v107\n"
    "v_mfma_f32_16x16x32_bf16 v[112:115], a[8:11], v[184:187], 0\n"
    "s_waitcnt lgkmcnt(5)\n"
    "ds_read_u16 v197, v197\n"
    "ds_write_b128 v243, v[128:131] offset:28736\n"
    "v_mfma_f32_16x16x32_bf16 v[116:119], a[12:15], v[184:187], 0\n"
    "s_nop 0\n"
    "v_cvt_pk_bf16_f32 v106, v108, v109\n"
    "v_cvt_pk_bf16_f32 v107, v110, v111\n"
    "v_mfma_f32_16x16x32_bf16 v[128:131], v[236:239], v[184:187], 0\n"
    "v_or_b32_e64 v194, v194, v202\n"
    "v_cvt_pk_bf16_f32 v120, v120, v121\n"
    "v_mfma_f32_16x16x32_bf16 v[108:111], a[4:7], v[188:191], 0\n"
    "ds_read_u16_d16_hi v205, v205\n"
    "ds_write_b128 v243, v[104:107] offset:25664\n"
    "v_mfma_f32_16x16x32_bf16 v[104:107], a[0:3], v[188:191], 0\n"
    "s_waitcnt lgkmcnt(6)\n"
    "v_cvt_pk_bf16_f32 v121, v122, v123\n"
    "v_cvt_pk_bf16_f32 v122, v124, v125\n"
    "s_waitcnt lgkmcnt(5)\n"
    "v_mfma_f32_16x16x32_bf16 v[132:135], a[244:247], v[184:187], 0\n"
    "v_cvt_pk_bf16_f32 v123, v126, v127\n"
    "v_pk_fma_f16 v232, v192, v192, s48\n"
    "v_mfma_f32_16x16x32_bf16 v[124:127], a[12:15], v[188:191], 0\n"
    "ds_read_u16 v198, v198\n"
    "ds_write_b128 v243, v[120:123] offset:27712\n"
    "v_mfma_f32_16x16x32_bf16 v[120:123], a[8:11], v[188:191], 0\n"
    "v_cvt_pk_bf16_f32 v136, v136, v137\n"
    "v_cvt_pk_bf16_f32 v137, v138, v139\n"
    "v_cvt_pk_bf16_f32 v138, v140, v141\n"
    "v_or_b32_e64 v195, v195, v203\n"
    "v_cvt_pk_bf16_f32 v139, v142, v143\n"
    "v_pk_mul_f16 v232, v192, v232\n"
    "v_cvt_pk_f16_f32 v184, v168, v169\n"
    "ds_write_b128 v243, v[136:139] offset:29760\n"
    "v_mfma_f32_16x16x32_bf16 v[136:139], v[236:239], v[188:191], 0\n"
    "v_exp_f16_e32 v234, v232\n"
    "v_mfma_f32_16x16x32_bf16 v[140:143], a[244:247], v[188:191], 0\n"
    "s_waitcnt lgkmcnt(3)\n"
    "ds_read_u16_d16_hi v206, v206\n"
    "ds_read_u16 v199, v199\n"
    "v_or_b32_e64 v197, v197, v205\n"
    "v_exp_f16_sdwa v234, v232 dst_sel:WORD_1 dst_unused:UNUSED_PRESERVE src0_sel:WORD_1\n"
    "v_lshlrev_b32_sdwa v185, 1, v170 dst_sel:DWORD dst_unused:UNUSED_PAD src0_sel:DWORD src1_sel:WORD_1\n"
    "v_pk_fma_f16 v234, v234, s49, s49\n"
    "v_lshlrev_b32_sdwa v201, 1, v171 dst_sel:DWORD dst_unused:UNUSED_PAD src0_sel:DWORD src1_sel:WORD_1\n"
    "v_mfma_f32_16x16x32_bf16 v[168:171], a[152:155], v[144:147], 0\n"
    "ds_read_u16_d16_hi v207, v207\n"
    "ds_read_b128 a[252:255], v249 offset:0\n"
    "v_mfma_f32_16x16x32_bf16 v[168:171], a[200:203], v[148:151], v[168:171]\n"
    "buffer_load_dwordx4 v[24:27], v247, s[40:43], s61 offen offset:0\n"
    "buffer_load_dwordx4 v[56:59], v247, s[40:43], s61 offen offset:64\n"
    "ds_read_u16 v185, v185\n"
    "v_lshlrev_b32_sdwa v186, 1, v172 dst_sel:DWORD dst_unused:UNUSED_PAD src0_sel:DWORD src1_sel:WORD_1\n"
    "v_lshlrev_b32_sdwa v202, 1, v173 dst_sel:DWORD dst_unused:UNUSED_PAD src0_sel:DWORD src1_sel:WORD_1\n"
    "v_lshlrev_b32_sdwa v187, 1, v174 dst_sel:DWORD dst_unused:UNUSED_PAD src0_sel:DWORD src1_sel:WORD_1\n"
    "v_lshlrev_b32_sdwa v203, 1, v175 dst_sel:DWORD dst_unused:UNUSED_PAD src0_sel:DWORD src1_sel:WORD_1\n"
    "v_rcp_f16_e64 v232, v234\n"
    "v_mfma_f32_16x16x32_bf16 v[168:171], a[112:115], v[152:155], v[168:171]\n"
    "ds_read_u16_d16_hi v201, v201\n"
    "ds_read_u16 v186, v186\n"
    "v_rcp_f16_sdwa v232, v234 dst_sel:WORD_1 dst_unused:UNUSED_PRESERVE src0_sel:WORD_1\n"
    "v_mfma_f32_16x16x32_bf16 v[172:175], a[156:159], v[144:147], 0\n"
    "ds_read_u16_d16_hi v202, v202\n"
    "v_cvt_pk_f16_f32 v188, v176, v177\n"
    "v_mfma_f32_16x16x32_bf16 v[172:175], a[204:207], v[148:151], v[172:175]\n"
    "v_lshlrev_b32_sdwa v189, 1, v178 dst_sel:DWORD dst_unused:UNUSED_PAD src0_sel:DWORD src1_sel:WORD_1\n"
    "v_lshlrev_b32_sdwa v205, 1, v179 dst_sel:DWORD dst_unused:UNUSED_PAD src0_sel:DWORD src1_sel:WORD_1\n"
    "v_lshlrev_b32_sdwa v190, 1, v180 dst_sel:DWORD dst_unused:UNUSED_PAD src0_sel:DWORD src1_sel:WORD_1\n"
    "v_lshlrev_b32_sdwa v191, 1, v182 dst_sel:DWORD dst_unused:UNUSED_PAD src0_sel:DWORD src1_sel:WORD_1\n"
    "v_fma_mix_f32 v234, v192, v232, 0 op_sel_hi:[1,1,0]\n"
    "v_mfma_f32_16x16x32_bf16 v[172:175], a[116:119], v[152:155], v[172:175]\n"
    "s_waitcnt lgkmcnt(7)\n"
    "ds_read_b128 v[236:239], v244 offset:4096\n"
    "ds_read_u16 v187, v187\n"
    "v_or_b32_e32 v198, v198, v206\n"
    "v_lshlrev_b32_sdwa v206, 1, v181 dst_sel:DWORD dst_unused:UNUSED_PAD src0_sel:DWORD src1_sel:WORD_1\n"
    "v_fma_mix_f32 v232, v192, v232, 0 op_sel:[1,1,0] op_sel_hi:[1,1,0]\n"
    "v_mfma_f32_16x16x32_bf16 v[176:179], a[152:155], v[156:159], 0\n"
    "ds_read_u16_d16_hi v203, v203\n"
    "v_cvt_pk_bf16_f32 v192, v234, v232\n"
    "v_mfma_f32_16x16x32_bf16 v[176:179], a[200:203], v[160:163], v[176:179]\n"
    "s_waitcnt lgkmcnt(7)\n"
    "buffer_store_dwordx4 a[252:255], v255, s[68:71], s59 offen offset:0 sc1\n"
    "ds_read_b128 a[244:247], v244 offset:6144\n"
    "v_mfma_f32_16x16x32_bf16 v[176:179], a[112:115], v[164:167], v[176:179]\n"
    "s_nop 0\n"
    "ds_read_b128 a[252:255], v249 offset:2048\n"
    "v_or_b32_e64 v199, v199, v207\n"
    "v_lshlrev_b32_sdwa v207, 1, v183 dst_sel:DWORD dst_unused:UNUSED_PAD src0_sel:DWORD src1_sel:WORD_1\n"
    "v_pk_fma_f16 v232, v196, v196, s48\n"
    "v_mfma_f32_16x16x32_bf16 v[180:183], a[156:159], v[156:159], 0\n"
    "ds_read_u16 v189, v189\n"
    "v_pk_mul_f16 v232, v196, v232\n"
    "v_mfma_f32_16x16x32_bf16 v[180:183], a[204:207], v[160:163], v[180:183]\n"
    "ds_read_u16_d16_hi v205, v205\n"
    "v_exp_f16_e32 v234, v232\n"
    "v_mfma_f32_16x16x32_bf16 v[180:183], a[116:119], v[164:167], v[180:183]\n"
    "s_waitcnt lgkmcnt(9)\n"
    "v_exp_f16_sdwa v234, v232 dst_sel:WORD_1 dst_unused:UNUSED_PRESERVE src0_sel:WORD_1\n"
    "v_mfma_f32_16x16x32_bf16 v[96:99], a[16:19], v[192:195], v[96:99]\n"
    "ds_read_u16 v190, v190\n"
    "v_pk_fma_f16 v234, v234, s49, s49\n"
    "v_mfma_f32_16x16x32_bf16 v[100:103], a[20:23], v[192:195], v[100:103]\n"
    "ds_read_u16_d16_hi v206, v206\n"
    "v_rcp_f16_e32 v232, v234\n"
    "v_mfma_f32_16x16x32_bf16 v[112:115], a[24:27], v[192:195], v[112:115]\n"
    "s_waitcnt lgkmcnt(8)\n"
    "v_rcp_f16_sdwa v232, v234 dst_sel:WORD_1 dst_unused:UNUSED_PRESERVE src0_sel:WORD_1\n"
    "v_mfma_f32_16x16x32_bf16 v[116:119], a[28:31], v[192:195], v[116:119]\n"
    "buffer_load_dwordx4 v[8:11], v246, s[40:43], s62 offen offset:0\n"
    "buffer_load_dwordx4 v[40:43], v246, s[40:43], s62 offen offset:64\n"
    "ds_read_u16 v191, v191\n"
    "v_mfma_f32_16x16x32_bf16 v[128:131], v[236:239], v[192:195], v[128:131]\n"
    "ds_read_u16_d16_hi v207, v207\n"
    "v_or_b32_e64 v185, v185, v201\n"
    "v_fma_mix_f32 v234, v196, v232, 0 op_sel_hi:[1,1,0]\n"
    "v_fma_mix_f32 v232, v196, v232, 0 op_sel:[1,1,0] op_sel_hi:[1,1,0]\n"
    "v_or_b32_e32 v186, v186, v202\n"
    "s_waitcnt lgkmcnt(7)\n"
    "v_mfma_f32_16x16x32_bf16 v[132:135], a[244:247], v[192:195], v[132:135]\n"
    "ds_read_b128 a[240:243], v244 offset:34816\n"
    "v_cvt_pk_bf16_f32 v196, v234, v232\n"
    "v_pk_fma_f16 v232, v184, v184, s48\n"
    "v_or_b32_e32 v187, v187, v203\n"
    "v_mfma_f32_16x16x32_bf16 v[104:107], a[16:19], v[196:199], v[104:107]\n"
    "s_waitcnt lgkmcnt(7)\n"
    "v_pk_mul_f16 v232, v184, v232\n"
    "v_lshlrev_b32_sdwa v192, 1, v168 dst_sel:DWORD dst_unused:UNUSED_PAD src0_sel:DWORD src1_sel:WORD_1\n"
    "v_mfma_f32_16x16x32_bf16 v[108:111], a[20:23], v[196:199], v[108:111]\n"
    "ds_read_u16 v192, v192\n"
    "v_exp_f16_e64 v234, v232\n"
    "v_mfma_f32_16x16x32_bf16 v[120:123], a[24:27], v[196:199], v[120:123]\n"
    "v_exp_f16_sdwa v234, v232 dst_sel:WORD_1 dst_unused:UNUSED_PRESERVE src0_sel:WORD_1\n"
    "v_mfma_f32_16x16x32_bf16 v[124:127], a[28:31], v[196:199], v[124:127]\n"
    "v_pk_fma_f16 v234, v234, s49, s49\n"
    "s_waitcnt lgkmcnt(6)\n"
    "v_or_b32_e32 v189, v189, v205\n"
    "v_mfma_f32_16x16x32_bf16 v[136:139], v[236:239], v[196:199], v[136:139]\n"
    "ds_read_b128 v[236:239], v244 offset:8192\n"
    "v_lshlrev_b32_sdwa v200, 1, v169 dst_sel:DWORD dst_unused:UNUSED_PAD src0_sel:DWORD src1_sel:WORD_1\n"
    "v_mfma_f32_16x16x32_bf16 v[140:143], a[244:247], v[196:199], v[140:143]\n"
    "ds_read_u16_d16_hi v200, v200\n"
    "buffer_store_dwordx4 a[252:255], v255, s[68:71], s59 offen offset:64 sc1\n"
    "v_lshlrev_b32_sdwa v193, 1, v170 dst_sel:DWORD dst_unused:UNUSED_PAD src0_sel:DWORD src1_sel:WORD_1\n"
    "v_lshlrev_b32_sdwa v201, 1, v171 dst_sel:DWORD dst_unused:UNUSED_PAD src0_sel:DWORD src1_sel:WORD_1\n"
    "v_mfma_f32_16x16x32_bf16 v[168:171], a[160:163], v[144:147], 0\n"
    "s_waitcnt lgkmcnt(6)\n"
    "ds_read_b128 a[252:255], v249 offset:4096\n"
    "ds_read_u16 v193, v193\n"
    "v_mfma_f32_16x16x32_bf16 v[168:171], a[208:211], v[148:151], v[168:171]\n"
    "s_nop 0\n"
    "ds_read_u16_d16_hi v201, v201\n"
    "v_lshlrev_b32_sdwa v194, 1, v172 dst_sel:DWORD dst_unused:UNUSED_PAD src0_sel:DWORD src1_sel:WORD_1\n"
    "v_lshlrev_b32_sdwa v202, 1, v173 dst_sel:DWORD dst_unused:UNUSED_PAD src0_sel:DWORD src1_sel:WORD_1\n"
    "v_lshlrev_b32_sdwa v195, 1, v174 dst_sel:DWORD dst_unused:UNUSED_PAD src0_sel:DWORD src1_sel:WORD_1\n"
    "v_lshlrev_b32_sdwa v203, 1, v175 dst_sel:DWORD dst_unused:UNUSED_PAD src0_sel:DWORD src1_sel:WORD_1\n"
    "v_rcp_f16_e32 v232, v234\n"
    "v_mfma_f32_16x16x32_bf16 v[168:171], a[120:123], v[152:155], v[168:171]\n"
    "s_waitcnt lgkmcnt(6)\n"
    "ds_read_u16 v194, v194\n"
    "ds_read_u16_d16_hi v202, v202\n"
    "v_or_b32_e64 v190, v190, v206\n"
    "v_rcp_f16_sdwa v232, v234 dst_sel:WORD_1 dst_unused:UNUSED_PRESERVE src0_sel:WORD_1\n"
    "v_mfma_f32_16x16x32_bf16 v[172:175], a[164:167], v[144:147], 0\n"
    "ds_read_u16 v195, v195\n"
    "v_lshlrev_b32_sdwa v196, 1, v176 dst_sel:DWORD dst_unused:UNUSED_PAD src0_sel:DWORD src1_sel:WORD_1\n"
    "v_mfma_f32_16x16x32_bf16 v[172:175], a[212:215], v[148:151], v[172:175]\n"
    "ds_read_b128 a[244:247], v244 offset:10240\n"
    "v_lshlrev_b32_sdwa v204, 1, v177 dst_sel:DWORD dst_unused:UNUSED_PAD src0_sel:DWORD src1_sel:WORD_1\n"
    "v_lshlrev_b32_sdwa v197, 1, v178 dst_sel:DWORD dst_unused:UNUSED_PAD src0_sel:DWORD src1_sel:WORD_1\n"
    "v_lshlrev_b32_sdwa v205, 1, v179 dst_sel:DWORD dst_unused:UNUSED_PAD src0_sel:DWORD src1_sel:WORD_1\n"
    "v_lshlrev_b32_sdwa v198, 1, v180 dst_sel:DWORD dst_unused:UNUSED_PAD src0_sel:DWORD src1_sel:WORD_1\n"
    "v_lshlrev_b32_sdwa v206, 1, v181 dst_sel:DWORD dst_unused:UNUSED_PAD src0_sel:DWORD src1_sel:WORD_1\n"
    "v_lshlrev_b32_sdwa v199, 1, v182 dst_sel:DWORD dst_unused:UNUSED_PAD src0_sel:DWORD src1_sel:WORD_1\n"
    "v_fma_mix_f32 v234, v184, v232, 0 op_sel_hi:[1,1,0]\n"
    "v_mfma_f32_16x16x32_bf16 v[172:175], a[240:243], v[152:155], v[172:175]\n"
    "s_waitcnt lgkmcnt(6)\n"
    "buffer_load_dwordx4 v[12:15], v246, s[40:43], s63 offen offset:0\n"
    "buffer_load_dwordx4 v[44:47], v246, s[40:43], s63 offen offset:64\n"
    "ds_read_u16_d16_hi v203, v203\n"
    "v_or_b32_e32 v191, v191, v207\n"
    "v_lshlrev_b32_sdwa v207, 1, v183 dst_sel:DWORD dst_unused:UNUSED_PAD src0_sel:DWORD src1_sel:WORD_1\n"
    "v_fma_mix_f32 v232, v184, v232, 0 op_sel:[1,1,0] op_sel_hi:[1,1,0]\n"
    "v_mfma_f32_16x16x32_bf16 v[176:179], a[160:163], v[156:159], 0\n"
    "ds_read_u16 v196, v196\n"
    "ds_read_u16_d16_hi v204, v204\n"
    "v_cvt_pk_bf16_f32 v184, v234, v232\n"
    "v_mfma_f32_16x16x32_bf16 v[176:179], a[208:211], v[160:163], v[176:179]\n"
    "v_pk_fma_f16 v232, v188, v188, s48\n"
    "v_or_b32_e32 v192, v192, v200\n"
    "v_mfma_f32_16x16x32_bf16 v[176:179], a[120:123], v[164:167], v[176:179]\n"
    "s_waitcnt lgkmcnt(7)\n"
    "ds_read_u16 v197, v197\n"
    "v_pk_mul_f16 v232, v188, v232\n"
    "v_mfma_f32_16x16x32_bf16 v[180:183], a[164:167], v[156:159], 0\n"
    "ds_read_u16_d16_hi v205, v205\n"
    "v_exp_f16_e64 v234, v232\n"
    "v_mfma_f32_16x16x32_bf16 v[180:183], a[212:215], v[160:163], v[180:183]\n"
    "v_exp_f16_sdwa v234, v232 dst_sel:WORD_1 dst_unused:UNUSED_PRESERVE src0_sel:WORD_1\n"
    "v_mfma_f32_16x16x32_bf16 v[180:183], a[240:243], v[164:167], v[180:183]\n"
    "ds_read_u16 v198, v198\n"
    "v_pk_fma_f16 v234, v234, s49, s49\n"
    "v_mfma_f32_16x16x32_bf16 v[96:99], a[32:35], v[184:187], v[96:99]\n"
    "s_waitcnt lgkmcnt(8)\n"
    "v_rcp_f16_e32 v232, v234\n"
    "v_mfma_f32_16x16x32_bf16 v[100:103], a[36:39], v[184:187], v[100:103]\n"
    "ds_read_u16_d16_hi v206, v206\n"
    "buffer_store_dwordx4 a[252:255], v255, s[68:71], s59 offen offset:128 sc1\n"
    "v_mfma_f32_16x16x32_bf16 v[112:115], a[40:43], v[184:187], v[112:115]\n"
    "s_nop 0\n"
    "ds_read_u16 v199, v199\n"
    "v_rcp_f16_sdwa v232, v234 dst_sel:WORD_1 dst_unused:UNUSED_PRESERVE src0_sel:WORD_1\n"
    "v_mfma_f32_16x16x32_bf16 v[116:119], a[44:47], v[184:187], v[116:119]\n"
    "s_waitcnt lgkmcnt(7)\n"
    "ds_read_b128 a[252:255], v249 offset:1024\n"
    "v_fma_mix_f32 v234, v188, v232, 0 op_sel_hi:[1,1,0]\n"
    "v_mfma_f32_16x16x32_bf16 v[128:131], v[236:239], v[184:187], v[128:131]\n"
    "ds_read_u16_d16_hi v207, v207\n"
    "v_fma_mix_f32 v232, v188, v232, 0 op_sel:[1,1,0] op_sel_hi:[1,1,0]\n"
    "v_mfma_f32_16x16x32_bf16 v[132:135], a[244:247], v[184:187], v[132:135]\n"
    "v_cvt_pk_bf16_f32 v188, v234, v232\n"
    "v_or_b32_e32 v193, v193, v201\n"
    "v_cvt_pk_f16_f32 v184, v168, v169\n"
    "v_mfma_f32_16x16x32_bf16 v[104:107], a[32:35], v[188:191], v[104:107]\n"
    "s_waitcnt lgkmcnt(7)\n"
    "ds_read_b128 a[240:243], v244 offset:38912\n"
    "v_lshlrev_b32_sdwa v185, 1, v170 dst_sel:DWORD dst_unused:UNUSED_PAD src0_sel:DWORD src1_sel:WORD_1\n"
    "v_mfma_f32_16x16x32_bf16 v[108:111], a[36:39], v[188:191], v[108:111]\n"
    "ds_read_u16 v185, v185\n"
    "v_lshlrev_b32_sdwa v201, 1, v171 dst_sel:DWORD dst_unused:UNUSED_PAD src0_sel:DWORD src1_sel:WORD_1\n"
    "v_mfma_f32_16x16x32_bf16 v[120:123], a[40:43], v[188:191], v[120:123]\n"
    "ds_read_u16_d16_hi v201, v201\n"
    "v_lshlrev_b32_sdwa v186, 1, v172 dst_sel:DWORD dst_unused:UNUSED_PAD src0_sel:DWORD src1_sel:WORD_1\n"
    "v_mfma_f32_16x16x32_bf16 v[124:127], a[44:47], v[188:191], v[124:127]\n"
    "v_or_b32_e32 v194, v194, v202\n"
    "v_lshlrev_b32_sdwa v202, 1, v173 dst_sel:DWORD dst_unused:UNUSED_PAD src0_sel:DWORD src1_sel:WORD_1\n"
    "v_mfma_f32_16x16x32_bf16 v[136:139], v[236:239], v[188:191], v[136:139]\n"
    "s_waitcnt lgkmcnt(8)\n"
    "buffer_load_dwordx4 v[16:19], v246, s[40:43], s52 offen offset:0\n"
    "buffer_load_dwordx4 v[48:51], v246, s[40:43], s52 offen offset:64\n"
    "ds_read_u16 v186, v186\n"
    "v_mfma_f32_16x16x32_bf16 v[140:143], a[244:247], v[188:191], v[140:143]\n"
    "ds_read_u16_d16_hi v202, v202\n"
    "v_lshlrev_b32_sdwa v187, 1, v174 dst_sel:DWORD dst_unused:UNUSED_PAD src0_sel:DWORD src1_sel:WORD_1\n"
    "v_mfma_f32_16x16x32_bf16 v[168:171], a[168:171], v[144:147], 0\n"
    "v_cvt_pk_f16_f32 v188, v176, v177\n"
    "v_lshlrev_b32_sdwa v189, 1, v178 dst_sel:DWORD dst_unused:UNUSED_PAD src0_sel:DWORD src1_sel:WORD_1\n"
    "v_mfma_f32_16x16x32_bf16 v[168:171], a[216:219], v[148:151], v[168:171]\n"
    "s_waitcnt lgkmcnt(8)\n"
    "ds_read_b128 v[236:239], v244 offset:12288\n"
    "v_lshlrev_b32_sdwa v190, 1, v180 dst_sel:DWORD dst_unused:UNUSED_PAD src0_sel:DWORD src1_sel:WORD_1\n"
    "v_mfma_f32_16x16x32_bf16 v[168:171], a[124:127], v[152:155], v[168:171]\n"
    "s_waitcnt lgkmcnt(7)\n"
    "ds_read_u16 v187, v187\n"
    "v_lshlrev_b32_sdwa v191, 1, v182 dst_sel:DWORD dst_unused:UNUSED_PAD src0_sel:DWORD src1_sel:WORD_1\n"
    "v_or_b32_e64 v195, v195, v203\n"
    "v_lshlrev_b32_sdwa v203, 1, v175 dst_sel:DWORD dst_unused:UNUSED_PAD src0_sel:DWORD src1_sel:WORD_1\n"
    "v_mfma_f32_16x16x32_bf16 v[172:175], a[172:175], v[144:147], 0\n"
    "ds_read_u16_d16_hi v203, v203\n"
    "v_or_b32_e64 v196, v196, v204\n"
    "v_mfma_f32_16x16x32_bf16 v[172:175], a[220:223], v[148:151], v[172:175]\n"
    "v_or_b32_e64 v197, v197, v205\n"
    "v_lshlrev_b32_sdwa v205, 1, v179 dst_sel:DWORD dst_unused:UNUSED_PAD src0_sel:DWORD src1_sel:WORD_1\n"
    "s_waitcnt lgkmcnt(7)\n"
    "v_mfma_f32_16x16x32_bf16 v[172:175], a[240:243], v[152:155], v[172:175]\n"
    "s_nop 0\n"
    "ds_read_b128 a[244:247], v244 offset:14336\n"
    "v_or_b32_e64 v198, v198, v206\n"
    "v_mfma_f32_16x16x32_bf16 v[176:179], a[168:171], v[156:159], 0\n"
    "ds_read_u16 v189, v189\n"
    "v_lshlrev_b32_sdwa v206, 1, v181 dst_sel:DWORD dst_unused:UNUSED_PAD src0_sel:DWORD src1_sel:WORD_1\n"
    "v_mfma_f32_16x16x32_bf16 v[176:179], a[216:219], v[160:163], v[176:179]\n"
    "buffer_store_dwordx4 a[252:255], v255, s[68:71], s39 offen offset:0 sc1\n"
    "ds_read_u16_d16_hi v205, v205\n"
    "v_mfma_f32_16x16x32_bf16 v[176:179], a[124:127], v[164:167], v[176:179]\n"
    "s_waitcnt lgkmcnt(8)\n"
    "ds_read_b128 a[252:255], v249 offset:3072\n"
    "v_or_b32_e32 v199, v199, v207\n"
    "v_mfma_f32_16x16x32_bf16 v[96:99], a[48:51], v[192:195], v[96:99]\n"
    "ds_read_u16 v190, v190\n"
    "v_lshlrev_b32_sdwa v207, 1, v183 dst_sel:DWORD dst_unused:UNUSED_PAD src0_sel:DWORD src1_sel:WORD_1\n"
    "v_mfma_f32_16x16x32_bf16 v[180:183], a[172:175], v[156:159], 0\n"
    "v_or_b32_e64 v185, v185, v201\n"
    "v_pk_fma_f16 v232, v184, v184, s48\n"
    "v_mfma_f32_16x16x32_bf16 v[180:183], a[220:223], v[160:163], v[180:183]\n"
    "s_waitcnt lgkmcnt(7)\n"
    "ds_read_b128 v[144:147], v243 offset:0\n"
    "v_or_b32_e32 v186, v186, v202\n"
    "v_mfma_f32_16x16x32_bf16 v[180:183], a[240:243], v[164:167], v[180:183]\n"
    "ds_read_u16_d16_hi v206, v206\n"
    "v_pk_mul_f16 v232, v184, v232\n"
    "v_mfma_f32_16x16x32_bf16 v[100:103], a[52:55], v[192:195], v[100:103]\n"
    "v_exp_f16_e64 v234, v232\n"
    "v_mfma_f32_16x16x32_bf16 v[112:115], a[56:59], v[192:195], v[112:115]\n"
    "ds_read_b128 v[148:151], v243 offset:1024\n"
    "v_exp_f16_sdwa v234, v232 dst_sel:WORD_1 dst_unused:UNUSED_PRESERVE src0_sel:WORD_1\n"
    "v_mfma_f32_16x16x32_bf16 v[116:119], a[60:63], v[192:195], v[116:119]\n"
    "ds_read_u16 v191, v191\n"
    "v_pk_fma_f16 v234, v234, s49, s49\n"
    "v_mfma_f32_16x16x32_bf16 v[128:131], v[236:239], v[192:195], v[128:131]\n"
    "s_waitcnt lgkmcnt(8)\n"
    "ds_read_b128 v[152:155], v243 offset:2048\n"
    "v_or_b32_e32 v187, v187, v203\n"
    "v_mfma_f32_16x16x32_bf16 v[132:135], a[244:247], v[192:195], v[132:135]\n"
    "ds_read_u16_d16_hi v207, v207\n"
    "v_cvt_pk_f16_f32 v192, v168, v169\n"
    "v_mfma_f32_16x16x32_bf16 v[104:107], a[48:51], v[196:199], v[104:107]\n"
    "v_lshlrev_b32_sdwa v193, 1, v170 dst_sel:DWORD dst_unused:UNUSED_PAD src0_sel:DWORD src1_sel:WORD_1\n"
    "v_lshlrev_b32_sdwa v201, 1, v171 dst_sel:DWORD dst_unused:UNUSED_PAD src0_sel:DWORD src1_sel:WORD_1\n"
    "v_mfma_f32_16x16x32_bf16 v[108:111], a[52:55], v[196:199], v[108:111]\n"
    "ds_read_b128 v[156:159], v243 offset:3072\n"
    "v_lshlrev_b32_sdwa v194, 1, v172 dst_sel:DWORD dst_unused:UNUSED_PAD src0_sel:DWORD src1_sel:WORD_1\n"
    "v_mfma_f32_16x16x32_bf16 v[120:123], a[56:59], v[196:199], v[120:123]\n"
    "s_waitcnt lgkmcnt(8)\n"
    "ds_read_b128 v[160:163], v243 offset:4096\n"
    "buffer_store_dwordx4 a[252:255], v255, s[68:71], s39 offen offset:64 sc1\n"
    "v_mfma_f32_16x16x32_bf16 v[124:127], a[60:63], v[196:199], v[124:127]\n"
    "s_nop 0\n"
    "ds_read_b128 v[164:167], v243 offset:5120\n"
    "ds_read_b128 a[252:255], v249 offset:5120\n"
    "v_mfma_f32_16x16x32_bf16 v[136:139], v[236:239], v[196:199], v[136:139]\n"
    "s_waitcnt lgkmcnt(9)\n"
    "ds_read_b128 a[240:243], v244 offset:32768\n"
    "v_or_b32_e32 v189, v189, v205\n"
    "v_mfma_f32_16x16x32_bf16 v[140:143], a[244:247], v[196:199], v[140:143]\n"
    "ds_read_u16 v193, v193\n"
    "v_lshlrev_b32_sdwa v202, 1, v173 dst_sel:DWORD dst_unused:UNUSED_PAD src0_sel:DWORD src1_sel:WORD_1\n"
    "v_mfma_f32_16x16x32_bf16 v[168:171], a[128:131], v[144:147], 0\n"
    "v_lshlrev_b32_sdwa v195, 1, v174 dst_sel:DWORD dst_unused:UNUSED_PAD src0_sel:DWORD src1_sel:WORD_1\n"
    "v_lshlrev_b32_sdwa v203, 1, v175 dst_sel:DWORD dst_unused:UNUSED_PAD src0_sel:DWORD src1_sel:WORD_1\n"
    "s_waitcnt lgkmcnt(9)\n"
    "v_mfma_f32_16x16x32_bf16 v[168:171], a[176:179], v[148:151], v[168:171]\n"
    "s_nop 0\n"
    "ds_read_u16_d16_hi v201, v201\n"
    "v_rcp_f16_e64 v232, v234\n"
    "v_or_b32_e32 v190, v190, v206\n"
    "v_rcp_f16_sdwa v232, v234 dst_sel:WORD_1 dst_unused:UNUSED_PRESERVE src0_sel:WORD_1\n"
    "v_mfma_f32_16x16x32_bf16 v[172:175], a[132:135], v[144:147], 0\n"
    "s_waitcnt lgkmcnt(8)\n"
    "ds_read_u16 v194, v194\n"
    "v_cvt_pk_f16_f32 v196, v176, v177\n"
    "v_mfma_f32_16x16x32_bf16 v[168:171], a[96:99], v[152:155], v[168:171]\n"
    "ds_read_u16_d16_hi v202, v202\n"
    "v_lshlrev_b32_sdwa v197, 1, v178 dst_sel:DWORD dst_unused:UNUSED_PAD src0_sel:DWORD src1_sel:WORD_1\n"
    "v_lshlrev_b32_sdwa v205, 1, v179 dst_sel:DWORD dst_unused:UNUSED_PAD src0_sel:DWORD src1_sel:WORD_1\n"
    "v_lshlrev_b32_sdwa v198, 1, v180 dst_sel:DWORD dst_unused:UNUSED_PAD src0_sel:DWORD src1_sel:WORD_1\n"
    "v_lshlrev_b32_sdwa v206, 1, v181 dst_sel:DWORD dst_unused:UNUSED_PAD src0_sel:DWORD src1_sel:WORD_1\n"
    "v_lshlrev_b32_sdwa v199, 1, v182 dst_sel:DWORD dst_unused:UNUSED_PAD src0_sel:DWORD src1_sel:WORD_1\n"
    "v_fma_mix_f32 v234, v184, v232, 0 op_sel_hi:[1,1,0]\n"
    "v_mfma_f32_16x16x32_bf16 v[172:175], a[180:183], v[148:151], v[172:175]\n"
    "s_waitcnt lgkmcnt(8)\n"
    "ds_read_b128 v[236:239], v244 offset:16384\n"
    "ds_read_u16 v195, v195\n"
    "v_or_b32_e32 v191, v191, v207\n"
    "v_lshlrev_b32_sdwa v207, 1, v183 dst_sel:DWORD dst_unused:UNUSED_PAD src0_sel:DWORD src1_sel:WORD_1\n"
    "v_fma_mix_f32 v232, v184, v232, 0 op_sel:[1,1,0] op_sel_hi:[1,1,0]\n"
    "v_mfma_f32_16x16x32_bf16 v[176:179], a[128:131], v[156:159], 0\n"
    "v_cvt_pk_bf16_f32 v184, v234, v232\n"
    "v_pk_fma_f16 v232, v188, v188, s48\n"
    "s_waitcnt lgkmcnt(7)\n"
    "v_mfma_f32_16x16x32_bf16 v[176:179], a[176:179], v[160:163], v[176:179]\n"
    "s_barrier\n"
    "s_waitcnt lgkmcnt(6)\n"
    "v_mfma_f32_16x16x32_bf16 v[172:175], a[240:243], v[152:155], v[172:175]\n"
    "s_nop 0\n"
    "ds_read_u16_d16_hi v203, v203\n"
    "buffer_store_dwordx4 a[252:255], v255, s[68:71], s39 offen offset:128 sc1\n"
    "v_mfma_f32_16x16x32_bf16 v[176:179], a[96:99], v[164:167], v[176:179]\n"
    "ds_read_b128 a[244:247], v244 offset:18432\n"
    "ds_write_b128 v253, v[64:67] offset:2048\n"
    "v_mfma_f32_16x16x32_bf16 v[180:183], a[132:135], v[156:159], 0\n"
    "s_waitcnt lgkmcnt(7)\n"
    "v_pk_mul_f16 v232, v188, v232\n"
    "v_or_b32_e32 v193, v193, v201\n"
    "v_mfma_f32_16x16x32_bf16 v[180:183], a[180:183], v[160:163], v[180:183]\n"
    "v_exp_f16_e64 v234, v232\n"
    "v_mfma_f32_16x16x32_bf16 v[180:183], a[240:243], v[164:167], v[180:183]\n"
    "ds_read_u16 v197, v197\n"
    "ds_write_b128 v253, v[68:71] offset:5120\n"
    "v_exp_f16_sdwa v234, v232 dst_sel:WORD_1 dst_unused:UNUSED_PRESERVE src0_sel:WORD_1\n"
    "v_mfma_f32_16x16x32_bf16 v[96:99], a[64:67], v[184:187], v[96:99]\n"
    "s_waitcnt lgkmcnt(6)\n"
    "v_pk_fma_f16 v234, v234, s49, s49\n"
    "v_or_b32_e32 v194, v194, v202\n"
    "v_mfma_f32_16x16x32_bf16 v[100:103], a[68:71], v[184:187], v[100:103]\n"
    "v_rcp_f16_e32 v232, v234\n"
    "v_mfma_f32_16x16x32_bf16 v[112:115], a[72:75], v[184:187], v[112:115]\n"
    "ds_read_u16_d16_hi v205, v205\n"
    "ds_write_b128 v253, v[88:91] offset:0\n"
    "v_rcp_f16_sdwa v232, v234 dst_sel:WORD_1 dst_unused:UNUSED_PRESERVE src0_sel:WORD_1\n"
    "v_mfma_f32_16x16x32_bf16 v[116:119], a[76:79], v[184:187], v[116:119]\n"
    "s_waitcnt lgkmcnt(4)\n"
    "v_fma_mix_f32 v234, v188, v232, 0 op_sel_hi:[1,1,0]\n"
    "v_fma_mix_f32 v232, v188, v232, 0 op_sel:[1,1,0] op_sel_hi:[1,1,0]\n"
    "v_mfma_f32_16x16x32_bf16 v[128:131], v[236:239], v[184:187], v[128:131]\n"
    "v_cvt_pk_bf16_f32 v188, v234, v232\n"
    "v_pk_fma_f16 v232, v192, v192, s48\n"
    "v_mfma_f32_16x16x32_bf16 v[132:135], a[244:247], v[184:187], v[132:135]\n"
    "ds_read_u16 v198, v198\n"
    "ds_write_b128 v253, v[92:95] offset:3072\n"
    "v_mfma_f32_16x16x32_bf16 v[104:107], a[64:67], v[188:191], v[104:107]\n"
    "buffer_load_dwordx4 a[232:235], v245, s[40:43], s57 offen offset:64\n"
    "buffer_load_dwordx4 a[224:227], v245, s[40:43], s57 offen offset:0\n"
    "v_mfma_f32_16x16x32_bf16 v[108:111], a[68:71], v[188:191], v[108:111]\n"
    "v_or_b32_e64 v195, v195, v203\n"
    "v_pk_mul_f16 v232, v192, v232\n"
    "v_mfma_f32_16x16x32_bf16 v[120:123], a[72:75], v[188:191], v[120:123]\n"
    "ds_read_u16_d16_hi v206, v206\n"
    "ds_read_u16 v199, v199\n"
    "v_mfma_f32_16x16x32_bf16 v[124:127], a[76:79], v[188:191], v[124:127]\n"
    "ds_read_u16_d16_hi v207, v207\n"
    "ds_write_b128 v253, v[72:75] offset:1024\n"
    "v_mfma_f32_16x16x32_bf16 v[136:139], v[236:239], v[188:191], v[136:139]\n"
    "s_waitcnt lgkmcnt(6)\n"
    "v_exp_f16_e32 v234, v232\n"
    "v_mfma_f32_16x16x32_bf16 v[140:143], a[244:247], v[188:191], v[140:143]\n"
    "v_or_b32_e64 v197, v197, v205\n"
    "v_exp_f16_sdwa v234, v232 dst_sel:WORD_1 dst_unused:UNUSED_PRESERVE src0_sel:WORD_1\n"
    "v_lshlrev_b32_sdwa v184, 1, v168 dst_sel:DWORD dst_unused:UNUSED_PAD src0_sel:DWORD src1_sel:WORD_1\n"
    "v_pk_fma_f16 v234, v234, s49, s49\n"
    "v_lshlrev_b32_sdwa v200, 1, v169 dst_sel:DWORD dst_unused:UNUSED_PAD src0_sel:DWORD src1_sel:WORD_1\n"
    "v_lshlrev_b32_sdwa v185, 1, v170 dst_sel:DWORD dst_unused:UNUSED_PAD src0_sel:DWORD src1_sel:WORD_1\n"
    "v_lshlrev_b32_sdwa v201, 1, v171 dst_sel:DWORD dst_unused:UNUSED_PAD src0_sel:DWORD src1_sel:WORD_1\n"
    "v_mfma_f32_16x16x32_bf16 v[168:171], a[136:139], v[144:147], 0\n"
    "ds_read_b128 a[240:243], v244 offset:36864\n"
    "ds_write_b128 v253, v[76:79] offset:4096\n"
    "v_mfma_f32_16x16x32_bf16 v[168:171], a[184:187], v[148:151], v[168:171]\n"
    "v_lshlrev_b32_sdwa v186, 1, v172 dst_sel:DWORD dst_unused:UNUSED_PAD src0_sel:DWORD src1_sel:WORD_1\n"
    "v_lshlrev_b32_sdwa v202, 1, v173 dst_sel:DWORD dst_unused:UNUSED_PAD src0_sel:DWORD src1_sel:WORD_1\n"
    "v_lshlrev_b32_sdwa v187, 1, v174 dst_sel:DWORD dst_unused:UNUSED_PAD src0_sel:DWORD src1_sel:WORD_1\n"
    "v_lshlrev_b32_sdwa v203, 1, v175 dst_sel:DWORD dst_unused:UNUSED_PAD src0_sel:DWORD src1_sel:WORD_1\n"
    "v_rcp_f16_e32 v232, v234\n"
    "v_mfma_f32_16x16x32_bf16 v[168:171], a[100:103], v[152:155], v[168:171]\n"
    "s_waitcnt lgkmcnt(5)\n"
    "ds_read_u16 v184, v184\n"
    "ds_read_u16_d16_hi v200, v200\n"
    "v_or_b32_e64 v198, v198, v206\n"
    "v_rcp_f16_sdwa v232, v234 dst_sel:WORD_1 dst_unused:UNUSED_PRESERVE src0_sel:WORD_1\n"
    "v_mfma_f32_16x16x32_bf16 v[172:175], a[140:143], v[144:147], 0\n"
    "ds_read_u16 v185, v185\n"
    "ds_read_u16_d16_hi v201, v201\n"
    "v_lshlrev_b32_sdwa v188, 1, v176 dst_sel:DWORD dst_unused:UNUSED_PAD src0_sel:DWORD src1_sel:WORD_1\n"
    "v_lshlrev_b32_sdwa v204, 1, v177 dst_sel:DWORD dst_unused:UNUSED_PAD src0_sel:DWORD src1_sel:WORD_1\n"
    "v_lshlrev_b32_sdwa v189, 1, v178 dst_sel:DWORD dst_unused:UNUSED_PAD src0_sel:DWORD src1_sel:WORD_1\n"
    "v_lshlrev_b32_sdwa v205, 1, v179 dst_sel:DWORD dst_unused:UNUSED_PAD src0_sel:DWORD src1_sel:WORD_1\n"
    "v_lshlrev_b32_sdwa v190, 1, v180 dst_sel:DWORD dst_unused:UNUSED_PAD src0_sel:DWORD src1_sel:WORD_1\n"
    "v_lshlrev_b32_sdwa v206, 1, v181 dst_sel:DWORD dst_unused:UNUSED_PAD src0_sel:DWORD src1_sel:WORD_1\n"
    "v_lshlrev_b32_sdwa v191, 1, v182 dst_sel:DWORD dst_unused:UNUSED_PAD src0_sel:DWORD src1_sel:WORD_1\n"
    "v_fma_mix_f32 v234, v192, v232, 0 op_sel_hi:[1,1,0]\n"
    "v_mfma_f32_16x16x32_bf16 v[172:175], a[188:191], v[148:151], v[172:175]\n"
    "s_waitcnt lgkmcnt(5)\n"
    "ds_read_u16 v186, v186\n"
    "ds_read_b128 v[236:239], v244 offset:20480\n"
    "v_mfma_f32_16x16x32_bf16 v[172:175], a[240:243], v[152:155], v[172:175]\n"
    "s_nop 0\n"
    "buffer_load_dwordx4 a[236:239], v245, s[40:43], s100 offen offset:64\n"
    "buffer_load_dwordx4 a[228:231], v245, s[40:43], s100 offen offset:0\n"
    "ds_read_u16_d16_hi v202, v202\n"
    "v_or_b32_e64 v199, v199, v207\n"
    "v_lshlrev_b32_sdwa v207, 1, v183 dst_sel:DWORD dst_unused:UNUSED_PAD src0_sel:DWORD src1_sel:WORD_1\n"
    "v_fma_mix_f32 v232, v192, v232, 0 op_sel:[1,1,0] op_sel_hi:[1,1,0]\n"
    "v_mfma_f32_16x16x32_bf16 v[176:179], a[136:139], v[156:159], 0\n"
    "ds_read_u16 v187, v187\n"
    "v_cvt_pk_bf16_f32 v192, v234, v232\n"
    "v_mfma_f32_16x16x32_bf16 v[176:179], a[184:187], v[160:163], v[176:179]\n"
    "v_pk_fma_f16 v232, v196, v196, s48\n"
    "v_mov_b32_e64 v254, 0\n"
    "v_mfma_f32_16x16x32_bf16 v[176:179], a[100:103], v[164:167], v[176:179]\n"
    "ds_read_b128 a[244:247], v244 offset:22528\n"
    "v_pk_mul_f16 v232, v196, v232\n"
    "v_mfma_f32_16x16x32_bf16 v[180:183], a[140:143], v[156:159], 0\n"
    "ds_read_u16_d16_hi v203, v203\n"
    "v_exp_f16_e32 v234, v232\n"
    "v_mfma_f32_16x16x32_bf16 v[180:183], a[188:191], v[160:163], v[180:183]\n"
    "s_waitcnt lgkmcnt(8)\n"
    "ds_read_u16 v188, v188\n"
    "v_exp_f16_sdwa v234, v232 dst_sel:WORD_1 dst_unused:UNUSED_PRESERVE src0_sel:WORD_1\n"
    "v_mfma_f32_16x16x32_bf16 v[180:183], a[240:243], v[164:167], v[180:183]\n"
    "v_pk_fma_f16 v234, v234, s49, s49\n"
    "v_or_b32_e64 v184, v184, v200\n"
    "v_mfma_f32_16x16x32_bf16 v[96:99], a[80:83], v[192:195], v[96:99]\n"
    "ds_read_u16_d16_hi v204, v204\n"
    "v_rcp_f16_e32 v232, v234\n"
    "v_mfma_f32_16x16x32_bf16 v[100:103], a[84:87], v[192:195], v[100:103]\n"
    "s_waitcnt lgkmcnt(8)\n"
    "ds_read_u16 v189, v189\n"
    "v_rcp_f16_sdwa v232, v234 dst_sel:WORD_1 dst_unused:UNUSED_PRESERVE src0_sel:WORD_1\n"
    "v_mfma_f32_16x16x32_bf16 v[112:115], a[88:91], v[192:195], v[112:115]\n"
    "v_fma_mix_f32 v234, v196, v232, 0 op_sel_hi:[1,1,0]\n"
    "v_fma_mix_f32 v232, v196, v232, 0 op_sel:[1,1,0] op_sel_hi:[1,1,0]\n"
    "v_mfma_f32_16x16x32_bf16 v[116:119], a[92:95], v[192:195], v[116:119]\n"
    "s_waitcnt lgkmcnt(7)\n"
    "ds_read_u16_d16_hi v205, v205\n"
    "v_cvt_pk_bf16_f32 v196, v234, v232\n"
    "v_mfma_f32_16x16x32_bf16 v[128:131], v[236:239], v[192:195], v[128:131]\n"
    "v_or_b32_e32 v185, v185, v201\n"
    "v_lshlrev_b32_sdwa v201, 1, v171 dst_sel:DWORD dst_unused:UNUSED_PAD src0_sel:DWORD src1_sel:WORD_1\n"
    "v_mfma_f32_16x16x32_bf16 v[104:107], a[80:83], v[196:199], v[104:107]\n"
    "s_waitcnt lgkmcnt(7)\n"
    "ds_read_u16 v190, v190\n"
    "v_or_b32_e32 v186, v186, v202\n"
    "v_mfma_f32_16x16x32_bf16 v[108:111], a[84:87], v[196:199], v[108:111]\n"
    "ds_read_u16_d16_hi v206, v206\n"
    "v_lshlrev_b32_sdwa v202, 1, v173 dst_sel:DWORD dst_unused:UNUSED_PAD src0_sel:DWORD src1_sel:WORD_1\n"
    "s_waitcnt lgkmcnt(7)\n"
    "v_mfma_f32_16x16x32_bf16 v[132:135], a[244:247], v[192:195], v[132:135]\n"
    "s_nop 0\n"
    "v_cvt_pk_f16_f32 v192, v168, v169\n"
    "v_lshlrev_b32_sdwa v193, 1, v170 dst_sel:DWORD dst_unused:UNUSED_PAD src0_sel:DWORD src1_sel:WORD_1\n"
    "v_mfma_f32_16x16x32_bf16 v[120:123], a[88:91], v[196:199], v[120:123]\n"
    "ds_read_u16 v191, v191\n"
    "v_lshlrev_b32_sdwa v194, 1, v172 dst_sel:DWORD dst_unused:UNUSED_PAD src0_sel:DWORD src1_sel:WORD_1\n"
    "v_mfma_f32_16x16x32_bf16 v[124:127], a[92:95], v[196:199], v[124:127]\n"
    "v_lshlrev_b32_sdwa v195, 1, v174 dst_sel:DWORD dst_unused:UNUSED_PAD src0_sel:DWORD src1_sel:WORD_1\n"
    "s_waitcnt lgkmcnt(7)\n"
    "v_or_b32_e32 v187, v187, v203\n"
    "v_mfma_f32_16x16x32_bf16 v[136:139], v[236:239], v[196:199], v[136:139]\n"
    "ds_read_u16_d16_hi v207, v207\n"
    "v_lshlrev_b32_sdwa v203, 1, v175 dst_sel:DWORD dst_unused:UNUSED_PAD src0_sel:DWORD src1_sel:WORD_1\n"
    "v_mfma_f32_16x16x32_bf16 v[140:143], a[244:247], v[196:199], v[140:143]\n"
    "ds_read_u16 v193, v193\n"
    "v_cvt_pk_f16_f32 v196, v176, v177\n"
    "v_mfma_f32_16x16x32_bf16 v[168:171], a[144:147], v[144:147], 0\n"
    "v_lshlrev_b32_sdwa v197, 1, v178 dst_sel:DWORD dst_unused:UNUSED_PAD src0_sel:DWORD src1_sel:WORD_1\n"
    "v_lshlrev_b32_sdwa v198, 1, v180 dst_sel:DWORD dst_unused:UNUSED_PAD src0_sel:DWORD src1_sel:WORD_1\n"
    "v_mfma_f32_16x16x32_bf16 v[168:171], a[192:195], v[148:151], v[168:171]\n"
    "s_waitcnt lgkmcnt(7)\n"
    "ds_read_u16_d16_hi v201, v201\n"
    "v_lshlrev_b32_sdwa v199, 1, v182 dst_sel:DWORD dst_unused:UNUSED_PAD src0_sel:DWORD src1_sel:WORD_1\n"
    "v_mfma_f32_16x16x32_bf16 v[168:171], a[104:107], v[152:155], v[168:171]\n"
    "v_or_b32_e32 v188, v188, v204\n"
    "v_cvt_pk_bf16_f32 v96, v96, v97\n"
    "v_mfma_f32_16x16x32_bf16 v[172:175], a[148:151], v[144:147], 0\n"
    "ds_read_u16 v194, v194\n"
    "v_cvt_pk_bf16_f32 v97, v98, v99\n"
    "v_mfma_f32_16x16x32_bf16 v[172:175], a[196:199], v[148:151], v[172:175]\n"
    "s_waitcnt lgkmcnt(7)\n"
    "ds_read_u16_d16_hi v202, v202\n"
    "v_or_b32_e32 v189, v189, v205\n"
    "v_mfma_f32_16x16x32_bf16 v[172:175], a[108:111], v[152:155], v[172:175]\n"
    "v_lshlrev_b32_sdwa v205, 1, v179 dst_sel:DWORD dst_unused:UNUSED_PAD src0_sel:DWORD src1_sel:WORD_1\n"
    "v_cvt_pk_bf16_f32 v98, v100, v101\n"
    "v_mfma_f32_16x16x32_bf16 v[176:179], a[144:147], v[156:159], 0\n"
    "ds_read_b128 v[236:239], v244 offset:0\n"
    "v_cvt_pk_bf16_f32 v99, v102, v103\n"
    "v_mfma_f32_16x16x32_bf16 v[176:179], a[192:195], v[160:163], v[176:179]\n"
    "ds_read_u16 v195, v195\n"
    "v_cvt_pk_bf16_f32 v112, v112, v113\n"
    "v_mfma_f32_16x16x32_bf16 v[176:179], a[104:107], v[164:167], v[176:179]\n"
    "s_waitcnt lgkmcnt(8)\n"
    "v_or_b32_e32 v190, v190, v206\n"
    "v_lshlrev_b32_sdwa v206, 1, v181 dst_sel:DWORD dst_unused:UNUSED_PAD src0_sel:DWORD src1_sel:WORD_1\n"
    "v_mfma_f32_16x16x32_bf16 v[100:103], a[4:7], v[184:187], 0\n"
    "ds_read_u16_d16_hi v203, v203\n"
    "v_cvt_pk_bf16_f32 v113, v114, v115\n"
    "v_cvt_pk_bf16_f32 v114, v116, v117\n"
    "v_cvt_pk_bf16_f32 v115, v118, v119\n"
    "v_cvt_pk_bf16_f32 v128, v128, v129\n"
    "s_waitcnt vmcnt(23)\n"
    "ds_write2_b32 v248, v0, v1 offset0:0 offset1:20\n"
    "v_cvt_pk_bf16_f32 v129, v130, v131\n"
    "s_waitcnt lgkmcnt(8)\n"
    "v_or_b32_e64 v191, v191, v207\n"
    "v_mfma_f32_16x16x32_bf16 v[116:119], a[12:15], v[184:187], 0\n"
    "ds_read_b128 a[244:247], v244 offset:2048\n"
    "ds_write2_b32 v248, v2, v3 offset0:40 offset1:60\n"
    "buffer_load_dwordx4 v[64:67], v246, s[40:43], s60 offen offset:128\n"
    "v_add_u32_e32 v248, 1360, v248\n"
    "v_lshlrev_b32_sdwa v207, 1, v183 dst_sel:DWORD dst_unused:UNUSED_PAD src0_sel:DWORD src1_sel:WORD_1\n"
    "v_mfma_f32_16x16x32_bf16 v[180:183], a[148:151], v[156:159], 0\n"
    "s_waitcnt lgkmcnt(8)\n"
    "ds_read_u16 v197, v197\n"
    "s_waitcnt vmcnt(22)\n"
    "ds_write2_b32 v235, v20, v21 offset0:0 offset1:20\n"
    "v_mfma_f32_16x16x32_bf16 v[180:183], a[196:199], v[160:163], v[180:183]\n"
    "v_cvt_pk_bf16_f32 v130, v132, v133\n"
    "v_or_b32_e64 v193, v193, v201\n"
    "v_mfma_f32_16x16x32_bf16 v[180:183], a[108:111], v[164:167], v[180:183]\n"
    "ds_read_u16_d16_hi v205, v205\n"
    "ds_write2_b32 v235, v22, v23 offset0:40 offset1:60\n"
    "v_add_u32_e32 v235, 1360, v235\n"
    "v_cvt_pk_bf16_f32 v131, v134, v135\n"
    "v_cvt_pk_bf16_f32 v104, v104, v105\n"
    "v_cvt_pk_bf16_f32 v105, v106, v107\n"
    "v_cvt_pk_bf16_f32 v106, v108, v109\n"
    "s_waitcnt vmcnt(20)\n"
    "ds_write2_b32 v248, v4, v5 offset0:0 offset1:20\n"
    "v_cvt_pk_bf16_f32 v107, v110, v111\n"
    "v_mfma_f32_16x16x32_bf16 v[108:111], a[4:7], v[188:191], 0\n"
    "s_waitcnt lgkmcnt(9)\n"
    "v_or_b32_e64 v194, v194, v202\n"
    "ds_write2_b32 v248, v6, v7 offset0:40 offset1:60\n"
    "v_add_u32_e32 v248, 1360, v248\n"
    "s_waitcnt lgkmcnt(7)\n"
    "v_mfma_f32_16x16x32_bf16 v[132:135], a[244:247], v[184:187], 0\n"
    "s_nop 0\n"
    "ds_read_u16 v198, v198\n"
    "ds_write_b128 v242, v[96:99] offset:24640\n"
    "v_mfma_f32_16x16x32_bf16 v[96:99], a[0:3], v[184:187], 0\n"
    "v_cvt_pk_bf16_f32 v120, v120, v121\n"
    "v_cvt_pk_bf16_f32 v121, v122, v123\n"
    "v_cvt_pk_bf16_f32 v122, v124, v125\n"
    "v_cvt_pk_bf16_f32 v123, v126, v127\n"
    "v_cvt_pk_bf16_f32 v136, v136, v137\n"
    "v_pk_fma_f16 v232, v192, v192, s48\n"
    "v_mfma_f32_16x16x32_bf16 v[124:127], a[12:15], v[188:191], 0\n"
    "s_nop 0\n"
    "ds_read_u16_d16_hi v206, v206\n"
    "s_waitcnt vmcnt(18)\n"
    "ds_write2_b32 v235, v24, v25 offset0:0 offset1:20\n"
    "v_cvt_pk_bf16_f32 v137, v138, v139\n"
    "v_cvt_pk_bf16_f32 v138, v140, v141\n"
    "v_or_b32_e64 v195, v195, v203\n"
    "v_cvt_pk_bf16_f32 v139, v142, v143\n"
    "ds_write_b128 v242, v[112:115] offset:26688\n"
    "v_mfma_f32_16x16x32_bf16 v[112:115], a[8:11], v[184:187], 0\n"
    "s_waitcnt lgkmcnt(5)\n"
    "v_pk_mul_f16 v232, v192, v232\n"
    "v_or_b32_e32 v197, v197, v205\n"
    "v_exp_f16_e64 v234, v232\n"
    "v_mfma_f32_16x16x32_bf16 v[140:143], a[244:247], v[188:191], 0\n"
    "ds_read_u16 v199, v199\n"
    "ds_write2_b32 v235, v26, v27 offset0:40 offset1:60\n"
    "v_add_u32_e32 v235, -1360, v235\n"
    "v_exp_f16_sdwa v234, v232 dst_sel:WORD_1 dst_unused:UNUSED_PRESERVE src0_sel:WORD_1\n"
    "v_lshlrev_b32_sdwa v201, 1, v171 dst_sel:DWORD dst_unused:UNUSED_PAD src0_sel:DWORD src1_sel:WORD_1\n"
    "v_pk_fma_f16 v234, v234, s49, s49\n"
    "ds_write_b128 v242, v[128:131] offset:28736\n"
    "buffer_load_dwordx4 v[68:71], v246, s[40:43], s61 offen offset:128\n"
    "v_mfma_f32_16x16x32_bf16 v[128:131], v[236:239], v[184:187], 0\n"
    "s_waitcnt lgkmcnt(4)\n"
    "v_cvt_pk_f16_f32 v184, v168, v169\n"
    "v_lshlrev_b32_sdwa v185, 1, v170 dst_sel:DWORD dst_unused:UNUSED_PAD src0_sel:DWORD src1_sel:WORD_1\n"
    "v_mfma_f32_16x16x32_bf16 v[168:171], a[152:155], v[144:147], 0\n"
    "s_nop 0\n"
    "ds_read_u16_d16_hi v207, v207\n"
    "ds_write_b128 v242, v[104:107] offset:25664\n"
    "v_mfma_f32_16x16x32_bf16 v[104:107], a[0:3], v[188:191], 0\n"
    "v_lshlrev_b32_sdwa v186, 1, v172 dst_sel:DWORD dst_unused:UNUSED_PAD src0_sel:DWORD src1_sel:WORD_1\n"
    "v_lshlrev_b32_sdwa v202, 1, v173 dst_sel:DWORD dst_unused:UNUSED_PAD src0_sel:DWORD src1_sel:WORD_1\n"
    "v_mfma_f32_16x16x32_bf16 v[168:171], a[200:203], v[148:151], v[168:171]\n"
    "v_lshlrev_b32_sdwa v187, 1, v174 dst_sel:DWORD dst_unused:UNUSED_PAD src0_sel:DWORD src1_sel:WORD_1\n"
    "v_lshlrev_b32_sdwa v203, 1, v175 dst_sel:DWORD dst_unused:UNUSED_PAD src0_sel:DWORD src1_sel:WORD_1\n"
    "v_rcp_f16_e64 v232, v234\n"
    "v_mfma_f32_16x16x32_bf16 v[168:171], a[112:115], v[152:155], v[168:171]\n"
    "ds_read_b128 v[216:219], v251 offset:0\n"
    "s_waitcnt vmcnt(16)\n"
    "ds_write2_b32 v248, v8, v9 offset0:0 offset1:20\n"
    "v_or_b32_e32 v198, v198, v206\n"
    "v_rcp_f16_sdwa v232, v234 dst_sel:WORD_1 dst_unused:UNUSED_PRESERVE src0_sel:WORD_1\n"
    "v_mfma_f32_16x16x32_bf16 v[172:175], a[156:159], v[144:147], 0\n"
    "ds_read_b128 v[220:223], v251 offset:336\n"
    "ds_write_b128 v242, v[120:123] offset:27712\n"
    "v_mfma_f32_16x16x32_bf16 v[120:123], a[8:11], v[188:191], 0\n"
    "v_lshlrev_b32_sdwa v205, 1, v179 dst_sel:DWORD dst_unused:UNUSED_PAD src0_sel:DWORD src1_sel:WORD_1\n"
    "v_lshlrev_b32_sdwa v206, 1, v181 dst_sel:DWORD dst_unused:UNUSED_PAD src0_sel:DWORD src1_sel:WORD_1\n"
    "v_mfma_f32_16x16x32_bf16 v[172:175], a[204:207], v[148:151], v[172:175]\n"
    "s_waitcnt lgkmcnt(4)\n"
    "v_fma_mix_f32 v234, v192, v232, 0 op_sel_hi:[1,1,0]\n"
    "v_or_b32_e32 v199, v199, v207\n"
    "v_mfma_f32_16x16x32_bf16 v[172:175], a[116:119], v[152:155], v[172:175]\n"
    "ds_read_u16 v185, v185\n"
    "ds_write2_b32 v248, v10, v11 offset0:40 offset1:60\n"
    "v_add_u32_e32 v248, 1360, v248\n"
    "v_lshlrev_b32_sdwa v207, 1, v183 dst_sel:DWORD dst_unused:UNUSED_PAD src0_sel:DWORD src1_sel:WORD_1\n"
    "v_fma_mix_f32 v232, v192, v232, 0 op_sel:[1,1,0] op_sel_hi:[1,1,0]\n"
    "v_lshlrev_b32_sdwa v200, 1, v169 dst_sel:DWORD dst_unused:UNUSED_PAD src0_sel:DWORD src1_sel:WORD_1\n"
    "v_cvt_pk_bf16_f32 v192, v234, v232\n"
    "ds_write_b128 v242, v[136:139] offset:29760\n"
    "v_mfma_f32_16x16x32_bf16 v[136:139], v[236:239], v[188:191], 0\n"
    "v_cvt_pk_f16_f32 v188, v176, v177\n"
    "v_lshlrev_b32_sdwa v189, 1, v178 dst_sel:DWORD dst_unused:UNUSED_PAD src0_sel:DWORD src1_sel:WORD_1\n"
    "v_mfma_f32_16x16x32_bf16 v[176:179], a[152:155], v[156:159], 0\n"
    "ds_read_u16_d16_hi v201, v201\n"
    "s_waitcnt vmcnt(13)\n"
    "ds_write2_b32 v248, v12, v13 offset0:0 offset1:20\n"
    "v_mfma_f32_16x16x32_bf16 v[176:179], a[200:203], v[160:163], v[176:179]\n"
    "s_nop 0\n"
    "v_lshlrev_b32_sdwa v190, 1, v180 dst_sel:DWORD dst_unused:UNUSED_PAD src0_sel:DWORD src1_sel:WORD_1\n"
    "v_lshlrev_b32_sdwa v191, 1, v182 dst_sel:DWORD dst_unused:UNUSED_PAD src0_sel:DWORD src1_sel:WORD_1\n"
    "v_mfma_f32_16x16x32_bf16 v[176:179], a[112:115], v[164:167], v[176:179]\n"
    "ds_read_u16 v186, v186\n"
    "ds_write2_b32 v248, v14, v15 offset0:40 offset1:60\n"
    "v_add_u32_e32 v248, 1360, v248\n"
    "v_pk_fma_f16 v232, v196, v196, s48\n"
    "v_mfma_f32_16x16x32_bf16 v[180:183], a[156:159], v[156:159], 0\n"
    "buffer_load_dwordx4 v[72:75], v246, s[40:43], s62 offen offset:128\n"
    "s_waitcnt vmcnt(11)\n"
    "ds_write2_b32 v248, v16, v17 offset0:0 offset1:20\n"
    "v_pk_mul_f16 v232, v196, v232\n"
    "v_mfma_f32_16x16x32_bf16 v[180:183], a[204:207], v[160:163], v[180:183]\n"
    "v_exp_f16_e32 v234, v232\n"
    "v_mfma_f32_16x16x32_bf16 v[180:183], a[116:119], v[164:167], v[180:183]\n"
    "ds_read_u16_d16_hi v202, v202\n"
    "ds_write2_b32 v248, v18, v19 offset0:40 offset1:60\n"
    "v_mfma_f32_32x32x1_2b_f32 v[0:31], v254, v254, 0\n"
    "v_add_u32_e32 v248, -5440, v248\n"
    "v_exp_f16_sdwa v234, v232 dst_sel:WORD_1 dst_unused:UNUSED_PRESERVE src0_sel:WORD_1\n"
    "s_waitcnt lgkmcnt(5)\n"
    "v_or_b32_e32 v185, v185, v201\n"
    "v_pk_fma_f16 v234, v234, s49, s49\n"
    "v_lshlrev_b32_sdwa v201, 1, v171 dst_sel:DWORD dst_unused:UNUSED_PAD src0_sel:DWORD src1_sel:WORD_1\n"
    "v_rcp_f16_e64 v232, v234\n"
    "v_lshlrev_b32_sdwa v204, 1, v177 dst_sel:DWORD dst_unused:UNUSED_PAD src0_sel:DWORD src1_sel:WORD_1\n"
    "v_rcp_f16_sdwa v232, v234 dst_sel:WORD_1 dst_unused:UNUSED_PRESERVE src0_sel:WORD_1\n"
    "v_mov_b32_e64 v254, 0\n"
    "v_fma_mix_f32 v234, v196, v232, 0 op_sel_hi:[1,1,0]\n"
    "v_mfma_f32_16x16x32_bf16 v[96:99], a[16:19], v[192:195], v[96:99]\n"
    "ds_read_b128 v[236:239], v244 offset:4096\n"
    "ds_read_u16 v187, v187\n"
    "v_mfma_f32_16x16x32_bf16 v[100:103], a[20:23], v[192:195], v[100:103]\n"
    "ds_read_u16_d16_hi v203, v203\n"
    "ds_read_b128 a[244:247], v244 offset:6144\n"
    "v_mfma_f32_16x16x32_bf16 v[112:115], a[24:27], v[192:195], v[112:115]\n"
    "ds_read_u16 v189, v189\n"
    "v_fma_mix_f32 v232, v196, v232, 0 op_sel:[1,1,0] op_sel_hi:[1,1,0]\n"
    "v_mfma_f32_16x16x32_bf16 v[116:119], a[28:31], v[192:195], v[116:119]\n"
    "v_cvt_pk_bf16_f32 v196, v234, v232\n"
    "v_pk_fma_f16 v232, v184, v184, s48\n"
    "s_waitcnt lgkmcnt(5)\n"
    "s_barrier\n"
    "v_mfma_f32_16x16x32_bf16 v[104:107], a[16:19], v[196:199], v[104:107]\n"
    "ds_read_b128 v[208:211], v250 offset:0\n"
    "ds_read_b128 v[212:215], v250 offset:320\n"
    "v_mfma_f32_16x16x32_bf16 v[108:111], a[20:23], v[196:199], v[108:111]\n"
    "ds_read_u16_d16_hi v205, v205\n"
    "ds_read_u16 v190, v190\n"
    "v_mfma_f32_16x16x32_bf16 v[120:123], a[24:27], v[196:199], v[120:123]\n"
    "s_waitcnt lgkmcnt(8)\n"
    "v_or_b32_e32 v186, v186, v202\n"
    "v_pk_mul_f16 v232, v184, v232\n"
    "v_mfma_f32_16x16x32_bf16 v[128:131], v[236:239], v[192:195], v[128:131]\n"
    "ds_read_u16_d16_hi v206, v206\n"
    "v_exp_f16_e64 v234, v232\n"
    "v_mfma_f32_16x16x32_bf16 v[124:127], a[28:31], v[196:199], v[124:127]\n"
    "v_exp_f16_sdwa v234, v232 dst_sel:WORD_1 dst_unused:UNUSED_PRESERVE src0_sel:WORD_1\n"
    "v_mfma_f32_16x16x32_bf16 v[136:139], v[236:239], v[196:199], v[136:139]\n"
    "ds_read_u16 v191, v191\n"
    "v_pk_fma_f16 v234, v234, s49, s49\n"
    "v_lshlrev_b32_sdwa v202, 1, v173 dst_sel:DWORD dst_unused:UNUSED_PAD src0_sel:DWORD src1_sel:WORD_1\n"
    "v_rcp_f16_e32 v232, v234\n"
    "s_waitcnt lgkmcnt(7)\n"
    "v_mfma_f32_16x16x32_bf16 v[132:135], a[244:247], v[192:195], v[132:135]\n"
    "ds_read_u16_d16_hi v207, v207\n"
    "v_or_b32_e64 v187, v187, v203\n"
    "v_mfma_f32_16x16x32_bf16 v[140:143], a[244:247], v[196:199], v[140:143]\n"
    "ds_read_b128 a[240:243], v244 offset:34816\n"
    "v_lshlrev_b32_sdwa v192, 1, v168 dst_sel:DWORD dst_unused:UNUSED_PAD src0_sel:DWORD src1_sel:WORD_1\n"
    "v_lshlrev_b32_sdwa v193, 1, v170 dst_sel:DWORD dst_unused:UNUSED_PAD src0_sel:DWORD src1_sel:WORD_1\n"
    "v_mfma_f32_16x16x32_bf16 v[168:171], a[160:163], v[144:147], 0\n"
    "s_nop 0\n"
    "buffer_load_dwordx4 v[76:79], v246, s[40:43], s63 offen offset:128\n"
    "ds_read_u16 v192, v192\n"
    "v_mfma_f32_16x16x32_bf16 v[168:171], a[208:211], v[148:151], v[168:171]\n"
    "s_waitcnt lgkmcnt(7)\n"
    "v_lshlrev_b32_sdwa v194, 1, v172 dst_sel:DWORD dst_unused:UNUSED_PAD src0_sel:DWORD src1_sel:WORD_1\n"
    "v_lshlrev_b32_sdwa v195, 1, v174 dst_sel:DWORD dst_unused:UNUSED_PAD src0_sel:DWORD src1_sel:WORD_1\n"
    "v_smfmac_f32_16x16x64_bf16 v[0:3], v[216:219], v[208:215], v240\n"
    "ds_read_b128 v[216:219], v252 offset:0\n"
    "ds_read_u16_d16_hi v200, v200\n"
    "v_smfmac_f32_16x16x64_bf16 v[4:7], v[220:223], v[208:215], v240 abid:1\n"
    "v_lshlrev_b32_sdwa v203, 1, v175 dst_sel:DWORD dst_unused:UNUSED_PAD src0_sel:DWORD src1_sel:WORD_1\n"
    "v_mfma_f32_16x16x32_bf16 v[168:171], a[120:123], v[152:155], v[168:171]\n"
    "s_waitcnt lgkmcnt(8)\n"
    "ds_read_b128 v[208:211], v250 offset:640\n"
    "v_or_b32_e32 v189, v189, v205\n"
    "v_rcp_f16_sdwa v232, v234 dst_sel:WORD_1 dst_unused:UNUSED_PRESERVE src0_sel:WORD_1\n"
    "v_mfma_f32_16x16x32_bf16 v[172:175], a[164:167], v[144:147], 0\n"
    "ds_read_b128 v[212:215], v250 offset:960\n"
    "ds_read_b128 v[220:223], v252 offset:336\n"
    "v_lshlrev_b32_sdwa v196, 1, v176 dst_sel:DWORD dst_unused:UNUSED_PAD src0_sel:DWORD src1_sel:WORD_1\n"
    "v_lshlrev_b32_sdwa v197, 1, v178 dst_sel:DWORD dst_unused:UNUSED_PAD src0_sel:DWORD src1_sel:WORD_1\n"
    "v_lshlrev_b32_sdwa v205, 1, v179 dst_sel:DWORD dst_unused:UNUSED_PAD src0_sel:DWORD src1_sel:WORD_1\n"
    "v_lshlrev_b32_sdwa v198, 1, v180 dst_sel:DWORD dst_unused:UNUSED_PAD src0_sel:DWORD src1_sel:WORD_1\n"
    "s_waitcnt lgkmcnt(9)\n"
    "v_or_b32_e32 v190, v190, v206\n"
    "v_lshlrev_b32_sdwa v206, 1, v181 dst_sel:DWORD dst_unused:UNUSED_PAD src0_sel:DWORD src1_sel:WORD_1\n"
    "v_lshlrev_b32_sdwa v199, 1, v182 dst_sel:DWORD dst_unused:UNUSED_PAD src0_sel:DWORD src1_sel:WORD_1\n"
    "v_fma_mix_f32 v234, v184, v232, 0 op_sel_hi:[1,1,0]\n"
    "v_mfma_f32_16x16x32_bf16 v[172:175], a[212:215], v[148:151], v[172:175]\n"
    "ds_read_u16 v193, v193\n"
    "ds_read_u16_d16_hi v201, v201\n"
    "v_fma_mix_f32 v232, v184, v232, 0 op_sel:[1,1,0] op_sel_hi:[1,1,0]\n"
    "v_mfma_f32_16x16x32_bf16 v[176:179], a[160:163], v[156:159], 0\n"
    "v_cvt_pk_bf16_f32 v184, v234, v232\n"
    "v_pk_fma_f16 v232, v188, v188, s48\n"
    "s_waitcnt lgkmcnt(8)\n"
    "v_mfma_f32_16x16x32_bf16 v[172:175], a[240:243], v[152:155], v[172:175]\n"
    "s_nop 0\n"
    "ds_read_u16 v194, v194\n"
    "v_or_b32_e64 v191, v191, v207\n"
    "v_mfma_f32_16x16x32_bf16 v[176:179], a[208:211], v[160:163], v[176:179]\n"
    "ds_read_b128 v[236:239], v244 offset:8192\n"
    "v_lshlrev_b32_sdwa v207, 1, v183 dst_sel:DWORD dst_unused:UNUSED_PAD src0_sel:DWORD src1_sel:WORD_1\n"
    "v_mfma_f32_16x16x32_bf16 v[176:179], a[120:123], v[164:167], v[176:179]\n"
    "ds_read_u16_d16_hi v202, v202\n"
    "v_pk_mul_f16 v232, v188, v232\n"
    "v_mfma_f32_16x16x32_bf16 v[180:183], a[164:167], v[156:159], 0\n"
    "v_exp_f16_e32 v234, v232\n"
    "v_mfma_f32_16x16x32_bf16 v[180:183], a[212:215], v[160:163], v[180:183]\n"
    "ds_read_u16 v195, v195\n"
    "v_exp_f16_sdwa v234, v232 dst_sel:WORD_1 dst_unused:UNUSED_PRESERVE src0_sel:WORD_1\n"
    "v_mfma_f32_16x16x32_bf16 v[180:183], a[240:243], v[164:167], v[180:183]\n"
    "s_waitcnt lgkmcnt(8)\n"
    "ds_read_b128 a[244:247], v244 offset:10240\n"
    "v_pk_fma_f16 v234, v234, s49, s49\n"
    "s_waitcnt lgkmcnt(8)\n"
    "v_smfmac_f32_16x16x64_bf16 v[0:3], v[216:219], v[208:215], v240\n"
    "s_waitcnt lgkmcnt(7)\n"
    "ds_read_b128 v[216:219], v251 offset:672\n"
    "v_smfmac_f32_16x16x64_bf16 v[4:7], v[220:223], v[208:215], v240 abid:1\n"
    "ds_read_b128 v[208:211], v250 offset:80\n"
    "s_waitcnt vmcnt(5)\n"
    "v_smfmac_f32_32x32x32_bf16 v[112:127], a[248:251], a[232:239], v241\n"
    "s_nop 0\n"
    "ds_read_b128 v[212:215], v250 offset:400\n"
    "ds_read_b128 v[220:223], v251 offset:1008\n"
    "v_rcp_f16_e64 v232, v234\n"
    "v_or_b32_e64 v192, v192, v200\n"
    "v_rcp_f16_sdwa v232, v234 dst_sel:WORD_1 dst_unused:UNUSED_PRESERVE src0_sel:WORD_1\n"
    "s_waitcnt vmcnt(4)\n"
    "v_smfmac_f32_32x32x32_bf16 v[96:111], a[248:251], a[224:231], v241\n"
    "s_waitcnt lgkmcnt(9)\n"
    "buffer_load_dwordx4 a[224:227], v245, s[40:43], s57 offen offset:128\n"
    "ds_read_b128 a[232:235], v243 offset:24640\n"
    "v_fma_mix_f32 v234, v188, v232, 0 op_sel_hi:[1,1,0]\n"
    "v_fma_mix_f32 v232, v188, v232, 0 op_sel:[1,1,0] op_sel_hi:[1,1,0]\n"
    "v_or_b32_e64 v193, v193, v201\n"
    "v_cvt_pk_bf16_f32 v188, v234, v232\n"
    "ds_read_b128 a[236:239], v243 offset:25664\n"
    "ds_read_u16_d16_hi v203, v203\n"
    "v_pk_mov_b32 v[224:225], v[0:1], v[0:1] op_sel:[0,1]\n"
    "v_pk_mov_b32 v[226:227], v[2:3], v[2:3] op_sel:[0,1]\n"
    "v_pk_mul_f32 v[228:229], v[0:1], v[0:1]\n"
    "v_pk_mul_f32 v[230:231], v[2:3], v[2:3]\n"
    "v_pk_add_f32 v[224:225], v[224:225], v[4:5]\n"
    "v_pk_add_f32 v[226:227], v[226:227], v[6:7]\n"
    "v_pk_fma_f32 v[228:229], v[4:5], v[4:5], v[228:229]\n"
    "v_pk_fma_f32 v[230:231], v[6:7], v[6:7], v[230:231]\n"
    "v_mfma_f32_16x16x32_bf16 v[112:115], a[40:43], v[184:187], v[112:115]\n"
    "s_waitcnt lgkmcnt(9)\n"
    "ds_read_u16 v196, v196\n"
    "ds_read_u16_d16_hi v204, v204\n"
    "v_mfma_f32_16x16x32_bf16 v[116:119], a[44:47], v[184:187], v[116:119]\n"
    "s_waitcnt lgkmcnt(9)\n"
    "ds_read_u16 v197, v197\n"
    "v_lshlrev_b32_sdwa v201, 1, v171 dst_sel:DWORD dst_unused:UNUSED_PAD src0_sel:DWORD src1_sel:WORD_1\n"
    "v_mfma_f32_16x16x32_bf16 v[128:131], v[236:239], v[184:187], v[128:131]\n"
    "v_or_b32_e64 v194, v194, v202\n"
    "v_lshlrev_b32_sdwa v202, 1, v173 dst_sel:DWORD dst_unused:UNUSED_PAD src0_sel:DWORD src1_sel:WORD_1\n"
    "v_mfma_f32_16x16x32_bf16 v[132:135], a[244:247], v[184:187], v[132:135]\n"
    "ds_read_u16_d16_hi v205, v205\n"
    "v_mfma_f32_16x16x32_bf16 v[120:123], a[40:43], v[188:191], v[120:123]\n"
    "s_waitcnt lgkmcnt(8)\n"
    "v_smfmac_f32_16x16x64_bf16 v[8:11], v[216:219], v[208:215], v240\n"
    "s_waitcnt lgkmcnt(7)\n"
    "ds_read_b128 v[216:219], v252 offset:672\n"
    "buffer_load_dwordx4 v[80:83], v246, s[40:43], s52 offen offset:128\n"
    "v_smfmac_f32_16x16x64_bf16 v[12:15], v[220:223], v[208:215], v240 abid:1\n"
    "s_waitcnt lgkmcnt(6)\n"
    "ds_read_b128 v[208:211], v250 offset:720\n"
    "ds_read_b128 v[212:215], v250 offset:1040\n"
    "v_smfmac_f32_32x32x32_bf16 v[96:111], a[248:251], a[232:239], v241\n"
    "ds_read_b128 v[220:223], v252 offset:1008\n"
    "v_mfma_f32_16x16x32_bf16 v[124:127], a[44:47], v[188:191], v[124:127]\n"
    "s_waitcnt lgkmcnt(8)\n"
    "ds_read_u16 v198, v198\n"
    "v_or_b32_e64 v195, v195, v203\n"
    "v_mfma_f32_16x16x32_bf16 v[136:139], v[236:239], v[188:191], v[136:139]\n"
    "ds_read_u16_d16_hi v206, v206\n"
    "v_lshlrev_b32_sdwa v203, 1, v175 dst_sel:DWORD dst_unused:UNUSED_PAD src0_sel:DWORD src1_sel:WORD_1\n"
    "v_mfma_f32_16x16x32_bf16 v[140:143], a[244:247], v[188:191], v[140:143]\n"
    "buffer_load_dwordx4 a[228:231], v245, s[40:43], s100 offen offset:128\n"
    "ds_read_u16 v199, v199\n"
    "v_mfma_f32_16x16x32_bf16 v[96:99], a[32:35], v[184:187], v[96:99]\n"
    "s_nop 0\n"
    "v_mfma_f32_16x16x32_bf16 v[100:103], a[36:39], v[184:187], v[100:103]\n"
    "s_waitcnt lgkmcnt(9)\n"
    "ds_read_u16_d16_hi v207, v207\n"
    "v_cvt_pk_f16_f32 v184, v168, v169\n"
    "v_mfma_f32_16x16x32_bf16 v[104:107], a[32:35], v[188:191], v[104:107]\n"
    "v_lshlrev_b32_sdwa v185, 1, v170 dst_sel:DWORD dst_unused:UNUSED_PAD src0_sel:DWORD src1_sel:WORD_1\n"
    "v_lshlrev_b32_sdwa v186, 1, v172 dst_sel:DWORD dst_unused:UNUSED_PAD src0_sel:DWORD src1_sel:WORD_1\n"
    "v_mfma_f32_16x16x32_bf16 v[108:111], a[36:39], v[188:191], v[108:111]\n"
    "s_nop 0\n"
    "ds_read_b128 a[232:235], v243 offset:26688\n"
    "v_lshlrev_b32_sdwa v187, 1, v174 dst_sel:DWORD dst_unused:UNUSED_PAD src0_sel:DWORD src1_sel:WORD_1\n"
    "v_mfma_f32_16x16x32_bf16 v[168:171], a[168:171], v[144:147], 0\n"
    "s_waitcnt lgkmcnt(8)\n"
    "ds_read_b128 a[236:239], v243 offset:27712\n"
    "v_cvt_pk_f16_f32 v188, v176, v177\n"
    "v_mfma_f32_16x16x32_bf16 v[168:171], a[216:219], v[148:151], v[168:171]\n"
    "ds_read_b128 a[240:243], v244 offset:38912\n"
    "v_lshlrev_b32_sdwa v189, 1, v178 dst_sel:DWORD dst_unused:UNUSED_PAD src0_sel:DWORD src1_sel:WORD_1\n"
    "v_mfma_f32_16x16x32_bf16 v[168:171], a[124:127], v[152:155], v[168:171]\n"
    "s_waitcnt lgkmcnt(8)\n"
    "ds_read_u16 v185, v185\n"
    "v_lshlrev_b32_sdwa v190, 1, v180 dst_sel:DWORD dst_unused:UNUSED_PAD src0_sel:DWORD src1_sel:WORD_1\n"
    "v_smfmac_f32_16x16x64_bf16 v[8:11], v[216:219], v[208:215], v240\n"
    "s_waitcnt lgkmcnt(8)\n"
    "v_lshlrev_b32_sdwa v191, 1, v182 dst_sel:DWORD dst_unused:UNUSED_PAD src0_sel:DWORD src1_sel:WORD_1\n"
    "v_smfmac_f32_16x16x64_bf16 v[12:15], v[220:223], v[208:215], v240 abid:1\n"
    "ds_read_b128 v[208:211], v250 offset:160\n"
    "ds_read_b128 v[212:215], v250 offset:480\n"
    "v_mfma_f32_16x16x32_bf16 v[172:175], a[172:175], v[144:147], 0\n"
    "buffer_load_dwordx4 v[84:87], v247, s[40:43], s60 offen offset:128\n"
    "ds_read_b128 v[216:219], v251 offset:1344\n"
    "v_or_b32_e64 v196, v196, v204\n"
    "v_or_b32_e64 v197, v197, v205\n"
    "v_lshlrev_b32_sdwa v205, 1, v179 dst_sel:DWORD dst_unused:UNUSED_PAD src0_sel:DWORD src1_sel:WORD_1\n"
    "v_pk_add_f32 v[224:225], v[224:225], v[8:9]\n"
    "v_pk_add_f32 v[226:227], v[226:227], v[10:11]\n"
    "v_pk_fma_f32 v[228:229], v[8:9], v[8:9], v[228:229]\n"
    "v_pk_fma_f32 v[230:231], v[10:11], v[10:11], v[230:231]\n"
    "v_pk_add_f32 v[224:225], v[224:225], v[12:13]\n"
    "v_pk_add_f32 v[226:227], v[226:227], v[14:15]\n"
    "v_pk_fma_f32 v[228:229], v[12:13], v[12:13], v[228:229]\n"
    "v_pk_fma_f32 v[230:231], v[14:15], v[14:15], v[230:231]\n"
    "v_mfma_f32_16x16x32_bf16 v[172:175], a[220:223], v[148:151], v[172:175]\n"
    "s_waitcnt lgkmcnt(9)\n"
    "ds_read_b128 v[220:223], v251 offset:1680\n"
    "ds_read_u16_d16_hi v201, v201\n"
    "v_mfma_f32_16x16x32_bf16 v[176:179], a[168:171], v[156:159], 0\n"
    "s_nop 0\n"
    "ds_read_u16 v186, v186\n"
    "v_or_b32_e32 v198, v198, v206\n"
    "v_mfma_f32_16x16x32_bf16 v[176:179], a[216:219], v[160:163], v[176:179]\n"
    "s_waitcnt lgkmcnt(9)\n"
    "ds_read_u16_d16_hi v202, v202\n"
    "v_lshlrev_b32_sdwa v206, 1, v181 dst_sel:DWORD dst_unused:UNUSED_PAD src0_sel:DWORD src1_sel:WORD_1\n"
    "v_mfma_f32_16x16x32_bf16 v[176:179], a[124:127], v[164:167], v[176:179]\n"
    "s_waitcnt lgkmcnt(9)\n"
    "v_or_b32_e64 v199, v199, v207\n"
    "v_lshlrev_b32_sdwa v207, 1, v183 dst_sel:DWORD dst_unused:UNUSED_PAD src0_sel:DWORD src1_sel:WORD_1\n"
    "v_smfmac_f32_32x32x32_bf16 v[112:127], a[248:251], a[232:239], v241\n"
    "s_waitcnt lgkmcnt(8)\n"
    "ds_read_b128 v[236:239], v244 offset:12288\n"
    "v_pk_fma_f16 v232, v184, v184, s48\n"
    "v_pk_mul_f16 v232, v184, v232\n"
    "v_exp_f16_e64 v234, v232\n"
    "v_mfma_f32_16x16x32_bf16 v[172:175], a[240:243], v[152:155], v[172:175]\n"
    "ds_read_u16 v187, v187\n"
    "v_exp_f16_sdwa v234, v232 dst_sel:WORD_1 dst_unused:UNUSED_PRESERVE src0_sel:WORD_1\n"
    "v_mfma_f32_16x16x32_bf16 v[180:183], a[172:175], v[156:159], 0\n"
    "ds_read_u16_d16_hi v203, v203\n"
    "v_pk_fma_f16 v234, v234, s49, s49\n"
    "v_mfma_f32_16x16x32_bf16 v[180:183], a[220:223], v[160:163], v[180:183]\n"
    "ds_read_b128 a[244:247], v244 offset:14336\n"
    "v_rcp_f16_e64 v232, v234\n"
    "s_waitcnt lgkmcnt(8)\n"
    "v_smfmac_f32_16x16x64_bf16 v[16:19], v[216:219], v[208:215], v240\n"
    "s_waitcnt lgkmcnt(7)\n"
    "ds_read_b128 v[216:219], v252 offset:1344\n"
    "ds_read_u16 v189, v189\n"
    "v_smfmac_f32_16x16x64_bf16 v[20:23], v[220:223], v[208:215], v240 abid:1\n"
    "v_rcp_f16_sdwa v232, v234 dst_sel:WORD_1 dst_unused:UNUSED_PRESERVE src0_sel:WORD_1\n"
    "v_mfma_f32_16x16x32_bf16 v[180:183], a[240:243], v[164:167], v[180:183]\n"
    "ds_read_b128 v[208:211], v250 offset:800\n"
    "s_waitcnt lgkmcnt(9)\n"
    "v_or_b32_e32 v185, v185, v201\n"
    "v_mfma_f32_16x16x32_bf16 v[96:99], a[48:51], v[192:195], v[96:99]\n"
    "ds_read_b128 v[212:215], v250 offset:1120\n"
    "v_lshlrev_b32_sdwa v201, 1, v171 dst_sel:DWORD dst_unused:UNUSED_PAD src0_sel:DWORD src1_sel:WORD_1\n"
    "v_mfma_f32_16x16x32_bf16 v[100:103], a[52:55], v[192:195], v[100:103]\n"
    "ds_read_b128 v[220:223], v252 offset:1680\n"
    "v_fma_mix_f32 v234, v184, v232, 0 op_sel_hi:[1,1,0]\n"
    "v_mfma_f32_16x16x32_bf16 v[112:115], a[56:59], v[192:195], v[112:115]\n"
    "ds_read_b128 a[232:235], v243 offset:28736\n"
    "v_fma_mix_f32 v232, v184, v232, 0 op_sel:[1,1,0] op_sel_hi:[1,1,0]\n"
    "v_mfma_f32_16x16x32_bf16 v[116:119], a[60:63], v[192:195], v[116:119]\n"
    "s_waitcnt lgkmcnt(9)\n"
    "ds_read_u16_d16_hi v205, v205\n"
    "v_or_b32_e32 v186, v186, v202\n"
    "v_mfma_f32_16x16x32_bf16 v[128:131], v[236:239], v[192:195], v[128:131]\n"
    "buffer_load_dwordx4 v[88:91], v247, s[40:43], s61 offen offset:128\n"
    "ds_read_b128 a[236:239], v243 offset:29760\n"
    "v_mfma_f32_16x16x32_bf16 v[104:107], a[48:51], v[196:199], v[104:107]\n"
    "v_lshlrev_b32_sdwa v202, 1, v173 dst_sel:DWORD dst_unused:UNUSED_PAD src0_sel:DWORD src1_sel:WORD_1\n"
    "v_cvt_pk_bf16_f32 v184, v234, v232\n"
    "v_mfma_f32_16x16x32_bf16 v[108:111], a[52:55], v[196:199], v[108:111]\n"
    "ds_read_u16 v190, v190\n"
    "v_pk_fma_f16 v232, v188, v188, s48\n"
    "v_mfma_f32_16x16x32_bf16 v[120:123], a[56:59], v[196:199], v[120:123]\n"
    "s_waitcnt lgkmcnt(9)\n"
    "ds_read_b128 v[144:147], v242 offset:0\n"
    "v_or_b32_e32 v187, v187, v203\n"
    "v_mfma_f32_16x16x32_bf16 v[132:135], a[244:247], v[192:195], v[132:135]\n"
    "ds_read_u16_d16_hi v206, v206\n"
    "v_cvt_pk_f16_f32 v192, v168, v169\n"
    "v_mfma_f32_16x16x32_bf16 v[124:127], a[60:63], v[196:199], v[124:127]\n"
    "v_lshlrev_b32_sdwa v193, 1, v170 dst_sel:DWORD dst_unused:UNUSED_PAD src0_sel:DWORD src1_sel:WORD_1\n"
    "v_lshlrev_b32_sdwa v194, 1, v172 dst_sel:DWORD dst_unused:UNUSED_PAD src0_sel:DWORD src1_sel:WORD_1\n"
    "v_mfma_f32_16x16x32_bf16 v[136:139], v[236:239], v[196:199], v[136:139]\n"
    "ds_read_b128 v[148:151], v242 offset:1024\n"
    "v_lshlrev_b32_sdwa v195, 1, v174 dst_sel:DWORD dst_unused:UNUSED_PAD src0_sel:DWORD src1_sel:WORD_1\n"
    "v_mfma_f32_16x16x32_bf16 v[140:143], a[244:247], v[196:199], v[140:143]\n"
    "ds_read_u16 v191, v191\n"
    "v_lshlrev_b32_sdwa v203, 1, v175 dst_sel:DWORD dst_unused:UNUSED_PAD src0_sel:DWORD src1_sel:WORD_1\n"
    "s_waitcnt lgkmcnt(9)\n"
    "v_smfmac_f32_16x16x64_bf16 v[16:19], v[216:219], v[208:215], v240\n"
    "s_waitcnt lgkmcnt(8)\n"
    "ds_read_b128 v[216:219], v251 offset:2016\n"
    "v_smfmac_f32_16x16x64_bf16 v[20:23], v[220:223], v[208:215], v240 abid:1\n"
    "ds_read_b128 v[208:211], v250 offset:240\n"
    "v_cvt_pk_f16_f32 v196, v176, v177\n"
    "v_lshlrev_b32_sdwa v197, 1, v178 dst_sel:DWORD dst_unused:UNUSED_PAD src0_sel:DWORD src1_sel:WORD_1\n"
    "v_lshlrev_b32_sdwa v198, 1, v180 dst_sel:DWORD dst_unused:UNUSED_PAD src0_sel:DWORD src1_sel:WORD_1\n"
    "ds_read_b128 v[212:215], v250 offset:560\n"
    "v_pk_add_f32 v[224:225], v[224:225], v[16:17]\n"
    "v_pk_add_f32 v[226:227], v[226:227], v[18:19]\n"
    "v_pk_fma_f32 v[228:229], v[16:17], v[16:17], v[228:229]\n"
    "v_pk_fma_f32 v[230:231], v[18:19], v[18:19], v[230:231]\n"
    "v_pk_add_f32 v[224:225], v[224:225], v[20:21]\n"
    "v_pk_add_f32 v[226:227], v[226:227], v[22:23]\n"
    "v_pk_fma_f32 v[228:229], v[20:21], v[20:21], v[228:229]\n"
    "v_pk_fma_f32 v[230:231], v[22:23], v[22:23], v[230:231]\n"
    "s_waitcnt lgkmcnt(8)\n"
    "v_smfmac_f32_32x32x32_bf16 v[128:143], a[248:251], a[232:239], v241\n"
    "s_nop 0\n"
    "ds_read_b128 v[220:223], v251 offset:2352\n"
    "ds_read_b128 v[152:155], v242 offset:2048\n"
    "ds_read_u16_d16_hi v207, v207\n"
    "v_or_b32_e64 v189, v189, v205\n"
    "v_lshlrev_b32_sdwa v205, 1, v179 dst_sel:DWORD dst_unused:UNUSED_PAD src0_sel:DWORD src1_sel:WORD_1\n"
    "s_waitcnt lgkmcnt(9)\n"
    "v_mfma_f32_16x16x32_bf16 v[168:171], a[128:131], v[144:147], 0\n"
    "s_nop 0\n"
    "ds_read_b128 v[156:159], v242 offset:3072\n"
    "ds_read_b128 v[160:163], v242 offset:4096\n"
    "v_mfma_f32_16x16x32_bf16 v[172:175], a[132:135], v[144:147], 0\n"
    "ds_read_b128 v[164:167], v242 offset:5120\n"
    "v_lshlrev_b32_sdwa v199, 1, v182 dst_sel:DWORD dst_unused:UNUSED_PAD src0_sel:DWORD src1_sel:WORD_1\n"
    "s_waitcnt lgkmcnt(10)\n"
    "v_mfma_f32_16x16x32_bf16 v[168:171], a[176:179], v[148:151], v[168:171]\n"
    "s_nop 0\n"
    "ds_read_b128 a[240:243], v244 offset:32768\n"
    "v_or_b32_e64 v190, v190, v206\n"
    "v_mfma_f32_16x16x32_bf16 v[172:175], a[180:183], v[148:151], v[172:175]\n"
    "ds_read_u16 v193, v193\n"
    "v_lshlrev_b32_sdwa v206, 1, v181 dst_sel:DWORD dst_unused:UNUSED_PAD src0_sel:DWORD src1_sel:WORD_1\n"
    "v_pk_mul_f16 v232, v188, v232\n"
    "v_exp_f16_e64 v234, v232\n"
    "ds_read_u16_d16_hi v201, v201\n"
    "v_exp_f16_sdwa v234, v232 dst_sel:WORD_1 dst_unused:UNUSED_PRESERVE src0_sel:WORD_1\n"
    "ds_read_u16 v194, v194\n"
    "s_waitcnt lgkmcnt(10)\n"
    "v_smfmac_f32_16x16x64_bf16 v[24:27], v[216:219], v[208:215], v240\n"
    "s_waitcnt lgkmcnt(9)\n"
    "v_pk_fma_f16 v234, v234, s49, s49\n"
    "v_smfmac_f32_16x16x64_bf16 v[28:31], v[220:223], v[208:215], v240 abid:1\n"
    "s_waitcnt lgkmcnt(8)\n"
    "ds_read_b128 v[208:211], v250 offset:880\n"
    "v_mfma_f32_16x16x32_bf16 v[168:171], a[96:99], v[152:155], v[168:171]\n"
    "s_nop 0\n"
    "ds_read_b128 v[212:215], v250 offset:1200\n"
    "v_rcp_f16_e64 v232, v234\n"
    "v_mfma_f32_16x16x32_bf16 v[96:99], a[64:67], v[184:187], v[96:99]\n"
    "ds_read_b128 v[216:219], v252 offset:2016\n"
    "v_rcp_f16_sdwa v232, v234 dst_sel:WORD_1 dst_unused:UNUSED_PRESERVE src0_sel:WORD_1\n"
    "s_waitcnt lgkmcnt(9)\n"
    "v_mfma_f32_16x16x32_bf16 v[176:179], a[128:131], v[156:159], 0\n"
    "s_waitcnt lgkmcnt(8)\n"
    "ds_read_b128 v[220:223], v252 offset:2352\n"
    "ds_read_u16_d16_hi v202, v202\n"
    "v_mfma_f32_16x16x32_bf16 v[176:179], a[176:179], v[160:163], v[176:179]\n"
    "s_waitcnt lgkmcnt(9)\n"
    "v_or_b32_e64 v191, v191, v207\n"
    "v_lshlrev_b32_sdwa v207, 1, v183 dst_sel:DWORD dst_unused:UNUSED_PAD src0_sel:DWORD src1_sel:WORD_1\n"
    "v_mfma_f32_16x16x32_bf16 v[176:179], a[96:99], v[164:167], v[176:179]\n"
    "s_waitcnt lgkmcnt(8)\n"
    "ds_read_b128 v[236:239], v244 offset:16384\n"
    "v_fma_mix_f32 v234, v188, v232, 0 op_sel_hi:[1,1,0]\n"
    "v_mfma_f32_16x16x32_bf16 v[172:175], a[240:243], v[152:155], v[172:175]\n"
    "ds_read_u16 v195, v195\n"
    "v_fma_mix_f32 v232, v188, v232, 0 op_sel:[1,1,0] op_sel_hi:[1,1,0]\n"
    "v_mfma_f32_16x16x32_bf16 v[180:183], a[132:135], v[156:159], 0\n"
    "v_cvt_pk_bf16_f32 v188, v234, v232\n"
    "v_pk_fma_f16 v232, v192, v192, s48\n"
    "v_mfma_f32_16x16x32_bf16 v[180:183], a[180:183], v[160:163], v[180:183]\n"
    "ds_read_u16_d16_hi v203, v203\n"
    "v_pk_mul_f16 v232, v192, v232\n"
    "v_mfma_f32_16x16x32_bf16 v[180:183], a[240:243], v[164:167], v[180:183]\n"
    "v_exp_f16_e32 v234, v232\n"
    "v_mfma_f32_16x16x32_bf16 v[100:103], a[68:71], v[184:187], v[100:103]\n"
    "s_waitcnt lgkmcnt(9)\n"
    "ds_read_b128 a[244:247], v244 offset:18432\n"
    "v_or_b32_e64 v193, v193, v201\n"
    "v_mfma_f32_16x16x32_bf16 v[112:115], a[72:75], v[184:187], v[112:115]\n"
    "ds_read_u16 v197, v197\n"
    "v_exp_f16_sdwa v234, v232 dst_sel:WORD_1 dst_unused:UNUSED_PRESERVE src0_sel:WORD_1\n"
    "v_mfma_f32_16x16x32_bf16 v[116:119], a[76:79], v[184:187], v[116:119]\n"
    "s_waitcnt lgkmcnt(8)\n"
    "s_barrier\n"
    "s_waitcnt lgkmcnt(7)\n"
    "v_smfmac_f32_16x16x64_bf16 v[24:27], v[216:219], v[208:215], v240\n"
    "s_waitcnt lgkmcnt(6)\n"
    "ds_read_u16_d16_hi v205, v205\n"
    "ds_write2_b32 v248, v32, v33 offset0:0 offset1:20\n"
    "v_smfmac_f32_16x16x64_bf16 v[28:31], v[220:223], v[208:215], v240 abid:1\n"
    "s_nop 0\n"
    "v_pk_fma_f16 v234, v234, s49, s49\n"
    "s_waitcnt lgkmcnt(6)\n"
    "v_mfma_f32_16x16x32_bf16 v[128:131], v[236:239], v[184:187], v[128:131]\n"
    "s_add_u32 s60, s60, 1572864\n"
    "ds_read_u16 v198, v198\n"
    "ds_write2_b32 v248, v34, v35 offset0:40 offset1:60\n"
    "v_add_u32_e32 v248, 1360, v248\n"
    "v_pk_add_f32 v[224:225], v[224:225], v[24:25]\n"
    "v_pk_add_f32 v[226:227], v[226:227], v[26:27]\n"
    "v_pk_fma_f32 v[228:229], v[24:25], v[24:25], v[228:229]\n"
    "v_pk_fma_f32 v[230:231], v[26:27], v[26:27], v[230:231]\n"
    "v_pk_add_f32 v[224:225], v[224:225], v[28:29]\n"
    "v_pk_add_f32 v[226:227], v[226:227], v[30:31]\n"
    "v_pk_fma_f32 v[228:229], v[28:29], v[28:29], v[228:229]\n"
    "v_pk_fma_f32 v[230:231], v[30:31], v[30:31], v[230:231]\n"
    "v_mfma_f32_16x16x32_bf16 v[104:107], a[64:67], v[188:191], v[104:107]\n"
    "s_waitcnt lgkmcnt(5)\n"
    "ds_read_u16_d16_hi v206, v206\n"
    "ds_write2_b32 v235, v52, v53 offset0:0 offset1:20\n"
    "v_mfma_f32_16x16x32_bf16 v[132:135], a[244:247], v[184:187], v[132:135]\n"
    "v_or_b32_e32 v194, v194, v202\n"
    "v_or_b32_e64 v195, v195, v203\n"
    "v_mfma_f32_16x16x32_bf16 v[108:111], a[68:71], v[188:191], v[108:111]\n"
    "ds_read_u16 v199, v199\n"
    "ds_write2_b32 v235, v54, v55 offset0:40 offset1:60\n"
    "v_mfma_f32_16x16x32_bf16 v[120:123], a[72:75], v[188:191], v[120:123]\n"
    "v_add_u32_e32 v235, 1360, v235\n"
    "v_lshlrev_b32_sdwa v184, 1, v168 dst_sel:DWORD dst_unused:UNUSED_PAD src0_sel:DWORD src1_sel:WORD_1\n"
    "v_mfma_f32_16x16x32_bf16 v[124:127], a[76:79], v[188:191], v[124:127]\n"
    "s_waitcnt lgkmcnt(6)\n"
    "ds_read_u16_d16_hi v207, v207\n"
    "ds_write2_b32 v248, v36, v37 offset0:0 offset1:20\n"
    "v_mfma_f32_16x16x32_bf16 v[136:139], v[236:239], v[188:191], v[136:139]\n"
    "v_or_b32_e32 v197, v197, v205\n"
    "v_lshlrev_b32_sdwa v200, 1, v169 dst_sel:DWORD dst_unused:UNUSED_PAD src0_sel:DWORD src1_sel:WORD_1\n"
    "v_mfma_f32_16x16x32_bf16 v[140:143], a[244:247], v[188:191], v[140:143]\n"
    "ds_read_b128 a[240:243], v244 offset:36864\n"
    "ds_write2_b32 v248, v38, v39 offset0:40 offset1:60\n"
    "v_add_u32_e32 v248, 1360, v248\n"
    "v_lshlrev_b32_sdwa v185, 1, v170 dst_sel:DWORD dst_unused:UNUSED_PAD src0_sel:DWORD src1_sel:WORD_1\n"
    "v_lshlrev_b32_sdwa v201, 1, v171 dst_sel:DWORD dst_unused:UNUSED_PAD src0_sel:DWORD src1_sel:WORD_1\n"
    "v_mfma_f32_16x16x32_bf16 v[168:171], a[136:139], v[144:147], 0\n"
    "s_nop 0\n"
    "ds_read_u16 v184, v184\n"
    "ds_write2_b32 v235, v56, v57 offset0:0 offset1:20\n"
    "v_mfma_f32_16x16x32_bf16 v[168:171], a[184:187], v[148:151], v[168:171]\n"
    "s_waitcnt lgkmcnt(8)\n"
    "v_lshlrev_b32_sdwa v186, 1, v172 dst_sel:DWORD dst_unused:UNUSED_PAD src0_sel:DWORD src1_sel:WORD_1\n"
    "v_lshlrev_b32_sdwa v202, 1, v173 dst_sel:DWORD dst_unused:UNUSED_PAD src0_sel:DWORD src1_sel:WORD_1\n"
    "v_lshlrev_b32_sdwa v187, 1, v174 dst_sel:DWORD dst_unused:UNUSED_PAD src0_sel:DWORD src1_sel:WORD_1\n"
    "ds_write2_b32 v235, v58, v59 offset0:40 offset1:60\n"
    "v_add_u32_e32 v235, -1360, v235\n"
    "v_lshlrev_b32_sdwa v203, 1, v175 dst_sel:DWORD dst_unused:UNUSED_PAD src0_sel:DWORD src1_sel:WORD_1\n"
    "ds_write2_b32 v248, v40, v41 offset0:0 offset1:20\n"
    "v_rcp_f16_e32 v232, v234\n"
    "v_mfma_f32_16x16x32_bf16 v[168:171], a[100:103], v[152:155], v[168:171]\n"
    "s_waitcnt lgkmcnt(6)\n"
    "ds_read_u16_d16_hi v200, v200\n"
    "ds_write2_b32 v248, v42, v43 offset0:40 offset1:60\n"
    "v_add_u32_e32 v248, 1360, v248\n"
    "v_or_b32_e64 v198, v198, v206\n"
    "v_rcp_f16_sdwa v232, v234 dst_sel:WORD_1 dst_unused:UNUSED_PRESERVE src0_sel:WORD_1\n"
    "v_mfma_f32_16x16x32_bf16 v[172:175], a[140:143], v[144:147], 0\n"
    "s_add_u32 s61, s61, 1572864\n"
    "ds_read_u16 v185, v185\n"
    "ds_write2_b32 v248, v44, v45 offset0:0 offset1:20\n"
    "v_lshlrev_b32_sdwa v188, 1, v176 dst_sel:DWORD dst_unused:UNUSED_PAD src0_sel:DWORD src1_sel:WORD_1\n"
    "v_lshlrev_b32_sdwa v204, 1, v177 dst_sel:DWORD dst_unused:UNUSED_PAD src0_sel:DWORD src1_sel:WORD_1\n"
    "v_lshlrev_b32_sdwa v189, 1, v178 dst_sel:DWORD dst_unused:UNUSED_PAD src0_sel:DWORD src1_sel:WORD_1\n"
    "v_lshlrev_b32_sdwa v205, 1, v179 dst_sel:DWORD dst_unused:UNUSED_PAD src0_sel:DWORD src1_sel:WORD_1\n"
    "v_lshlrev_b32_sdwa v190, 1, v180 dst_sel:DWORD dst_unused:UNUSED_PAD src0_sel:DWORD src1_sel:WORD_1\n"
    "v_lshlrev_b32_sdwa v206, 1, v181 dst_sel:DWORD dst_unused:UNUSED_PAD src0_sel:DWORD src1_sel:WORD_1\n"
    "v_lshlrev_b32_sdwa v191, 1, v182 dst_sel:DWORD dst_unused:UNUSED_PAD src0_sel:DWORD src1_sel:WORD_1\n"
    "v_fma_mix_f32 v234, v192, v232, 0 op_sel_hi:[1,1,0]\n"
    "v_mfma_f32_16x16x32_bf16 v[172:175], a[188:191], v[148:151], v[172:175]\n"
    "s_waitcnt lgkmcnt(4)\n"
    "ds_read_u16_d16_hi v201, v201\n"
    "ds_write2_b32 v248, v46, v47 offset0:40 offset1:60\n"
    "v_mfma_f32_16x16x32_bf16 v[172:175], a[240:243], v[152:155], v[172:175]\n"
    "s_add_u32 s62, s62, 1572864\n"
    "v_add_u32_e32 v248, 1360, v248\n"
    "v_or_b32_e32 v199, v199, v207\n"
    "v_lshlrev_b32_sdwa v207, 1, v183 dst_sel:DWORD dst_unused:UNUSED_PAD src0_sel:DWORD src1_sel:WORD_1\n"
    "v_fma_mix_f32 v232, v192, v232, 0 op_sel:[1,1,0] op_sel_hi:[1,1,0]\n"
    "v_mfma_f32_16x16x32_bf16 v[176:179], a[136:139], v[156:159], 0\n"
    "ds_read_u16 v186, v186\n"
    "ds_write2_b32 v248, v48, v49 offset0:0 offset1:20\n"
    "v_cvt_pk_bf16_f32 v192, v234, v232\n"
    "v_mfma_f32_16x16x32_bf16 v[176:179], a[184:187], v[160:163], v[176:179]\n"
    "s_waitcnt lgkmcnt(6)\n"
    "v_pk_fma_f16 v232, v196, v196, s48\n"
    "v_or_b32_e32 v184, v184, v200\n"
    "v_mfma_f32_16x16x32_bf16 v[176:179], a[100:103], v[164:167], v[176:179]\n"
    "s_add_u32 s63, s63, 1572864\n"
    "ds_read_b128 v[236:239], v244 offset:20480\n"
    "ds_write2_b32 v248, v50, v51 offset0:40 offset1:60\n"
    "v_mfma_f32_32x32x1_2b_f32 v[32:63], v254, v254, 0\n"
    "v_add_u32_e32 v248, -5440, v248\n"
    "v_pk_mul_f16 v232, v196, v232\n"
    "v_mov_b32_e64 v254, 0\n"
    "v_exp_f16_e64 v234, v232\n"
    "ds_read_u16_d16_hi v202, v202\n"
    "ds_read_b128 v[216:219], v251 offset:10752\n"
    "v_exp_f16_sdwa v234, v232 dst_sel:WORD_1 dst_unused:UNUSED_PRESERVE src0_sel:WORD_1\n"
    "s_waitcnt lgkmcnt(6)\n"
    "v_or_b32_e32 v185, v185, v201\n"
    "v_pk_fma_f16 v234, v234, s49, s49\n"
    "v_mfma_f32_16x16x32_bf16 v[180:183], a[140:143], v[156:159], 0\n"
    "s_add_u32 s52, s52, 1572864\n"
    "ds_read_b128 v[220:223], v251 offset:11088\n"
    "ds_read_u16 v187, v187\n"
    "v_mfma_f32_16x16x32_bf16 v[180:183], a[188:191], v[160:163], v[180:183]\n"
    "ds_read_b128 a[244:247], v244 offset:22528\n"
    "ds_read_u16_d16_hi v203, v203\n"
    "v_mfma_f32_16x16x32_bf16 v[180:183], a[240:243], v[164:167], v[180:183]\n"
    "ds_read_u16 v188, v188\n"
    "v_rcp_f16_e32 v232, v234\n"
    "v_mfma_f32_16x16x32_bf16 v[96:99], a[80:83], v[192:195], v[96:99]\n"
    "s_waitcnt lgkmcnt(7)\n"
    "ds_read_u16_d16_hi v204, v204\n"
    "v_rcp_f16_sdwa v232, v234 dst_sel:WORD_1 dst_unused:UNUSED_PRESERVE src0_sel:WORD_1\n"
    "s_waitcnt vmcnt(2)\n"
    "v_smfmac_f32_32x32x32_bf16 v[128:143], a[248:251], a[224:231], v241\n"
    "s_barrier\n"
    "v_mfma_f32_16x16x32_bf16 v[100:103], a[84:87], v[192:195], v[100:103]\n"
    "s_nop 0\n"
    "ds_read_b128 v[208:211], v250 offset:0\n"
    "ds_read_b128 v[212:215], v250 offset:320\n"
    "v_mfma_f32_16x16x32_bf16 v[112:115], a[88:91], v[192:195], v[112:115]\n"
    "s_waitcnt lgkmcnt(7)\n"
    "ds_read_u16 v189, v189\n"
    "v_fma_mix_f32 v234, v196, v232, 0 op_sel_hi:[1,1,0]\n"
    "v_mfma_f32_16x16x32_bf16 v[116:119], a[92:95], v[192:195], v[116:119]\n"
    "ds_read_u16_d16_hi v205, v205\n"
    "v_fma_mix_f32 v232, v196, v232, 0 op_sel:[1,1,0] op_sel_hi:[1,1,0]\n"
    "v_mfma_f32_16x16x32_bf16 v[128:131], v[236:239], v[192:195], v[128:131]\n"
    "v_cvt_pk_bf16_f32 v196, v234, v232\n"
    "v_lshlrev_b32_sdwa v201, 1, v171 dst_sel:DWORD dst_unused:UNUSED_PAD src0_sel:DWORD src1_sel:WORD_1\n"
    "v_or_b32_e32 v186, v186, v202\n"
    "s_waitcnt lgkmcnt(7)\n"
    "v_mfma_f32_16x16x32_bf16 v[132:135], a[244:247], v[192:195], v[132:135]\n"
    "ds_read_u16 v190, v190\n"
    "v_cvt_pk_f16_f32 v192, v168, v169\n"
    "v_mfma_f32_16x16x32_bf16 v[104:107], a[80:83], v[196:199], v[104:107]\n"
    "ds_read_u16_d16_hi v206, v206\n"
    "v_lshlrev_b32_sdwa v193, 1, v170 dst_sel:DWORD dst_unused:UNUSED_PAD src0_sel:DWORD src1_sel:WORD_1\n"
    "v_mfma_f32_16x16x32_bf16 v[108:111], a[84:87], v[196:199], v[108:111]\n"
    "v_lshlrev_b32_sdwa v194, 1, v172 dst_sel:DWORD dst_unused:UNUSED_PAD src0_sel:DWORD src1_sel:WORD_1\n"
    "v_lshlrev_b32_sdwa v202, 1, v173 dst_sel:DWORD dst_unused:UNUSED_PAD src0_sel:DWORD src1_sel:WORD_1\n"
    "v_mfma_f32_16x16x32_bf16 v[120:123], a[88:91], v[196:199], v[120:123]\n"
    "s_waitcnt lgkmcnt(8)\n"
    "ds_read_u16 v191, v191\n"
    "v_lshlrev_b32_sdwa v195, 1, v174 dst_sel:DWORD dst_unused:UNUSED_PAD src0_sel:DWORD src1_sel:WORD_1\n"
    "v_mfma_f32_16x16x32_bf16 v[124:127], a[92:95], v[196:199], v[124:127]\n"
    "v_or_b32_e32 v187, v187, v203\n"
    "v_lshlrev_b32_sdwa v203, 1, v175 dst_sel:DWORD dst_unused:UNUSED_PAD src0_sel:DWORD src1_sel:WORD_1\n"
    "v_mfma_f32_16x16x32_bf16 v[136:139], v[236:239], v[196:199], v[136:139]\n"
    "ds_read_u16_d16_hi v207, v207\n"
    "v_cvt_pk_bf16_f32 v96, v96, v97\n"
    "v_mfma_f32_16x16x32_bf16 v[140:143], a[244:247], v[196:199], v[140:143]\n"
    "s_waitcnt lgkmcnt(7)\n"
    "ds_read_u16 v193, v193\n"
    "v_cvt_pk_f16_f32 v196, v176, v177\n"
    "s_waitcnt lgkmcnt(7)\n"
    "v_smfmac_f32_16x16x64_bf16 v[32:35], v[216:219], v[208:215], v240\n"
    "v_lshlrev_b32_sdwa v197, 1, v178 dst_sel:DWORD dst_unused:UNUSED_PAD src0_sel:DWORD src1_sel:WORD_1\n"
    "v_smfmac_f32_16x16x64_bf16 v[36:39], v[220:223], v[208:215], v240 abid:1\n"
    "ds_read_b128 v[208:211], v250 offset:640\n"
    "ds_read_b128 v[212:215], v250 offset:960\n"
    "v_mfma_f32_16x16x32_bf16 v[168:171], a[144:147], v[144:147], 0\n"
    "v_lshlrev_b32_sdwa v198, 1, v180 dst_sel:DWORD dst_unused:UNUSED_PAD src0_sel:DWORD src1_sel:WORD_1\n"
    "v_lshlrev_b32_sdwa v199, 1, v182 dst_sel:DWORD dst_unused:UNUSED_PAD src0_sel:DWORD src1_sel:WORD_1\n"
    "v_mfma_f32_16x16x32_bf16 v[168:171], a[192:195], v[148:151], v[168:171]\n"
    "ds_read_b128 v[216:219], v252 offset:10752\n"
    "ds_read_b128 v[220:223], v252 offset:11088\n"
    "v_mfma_f32_16x16x32_bf16 v[168:171], a[104:107], v[152:155], v[168:171]\n"
    "s_waitcnt lgkmcnt(9)\n"
    "v_or_b32_e32 v188, v188, v204\n"
    "v_or_b32_e64 v189, v189, v205\n"
    "v_mfma_f32_16x16x32_bf16 v[172:175], a[148:151], v[144:147], 0\n"
    "ds_read_u16_d16_hi v201, v201\n"
    "v_lshlrev_b32_sdwa v205, 1, v179 dst_sel:DWORD dst_unused:UNUSED_PAD src0_sel:DWORD src1_sel:WORD_1\n"
    "v_mfma_f32_16x16x32_bf16 v[172:175], a[196:199], v[148:151], v[172:175]\n"
    "ds_read_u16 v194, v194\n"
    "v_cvt_pk_bf16_f32 v97, v98, v99\n"
    "v_mfma_f32_16x16x32_bf16 v[172:175], a[108:111], v[152:155], v[172:175]\n"
    "v_cvt_pk_bf16_f32 v98, v100, v101\n"
    "v_cvt_pk_bf16_f32 v99, v102, v103\n"
    "v_mfma_f32_16x16x32_bf16 v[176:179], a[144:147], v[156:159], 0\n"
    "s_waitcnt lgkmcnt(9)\n"
    "ds_read_u16_d16_hi v202, v202\n"
    "v_or_b32_e32 v190, v190, v206\n"
    "v_mfma_f32_16x16x32_bf16 v[176:179], a[192:195], v[160:163], v[176:179]\n"
    "v_lshlrev_b32_sdwa v206, 1, v181 dst_sel:DWORD dst_unused:UNUSED_PAD src0_sel:DWORD src1_sel:WORD_1\n"
    "v_cvt_pk_bf16_f32 v112, v112, v113\n"
    "v_mfma_f32_16x16x32_bf16 v[176:179], a[104:107], v[164:167], v[176:179]\n"
    "s_nop 0\n"
    "ds_read_b128 v[236:239], v244 offset:0\n"
    "v_cvt_pk_bf16_f32 v113, v114, v115\n"
    "v_mfma_f32_16x16x32_bf16 v[100:103], a[4:7], v[184:187], 0\n"
    "s_waitcnt lgkmcnt(9)\n"
    "ds_read_u16 v195, v195\n"
    "v_cvt_pk_bf16_f32 v114, v116, v117\n"
    "v_cvt_pk_bf16_f32 v115, v118, v119\n"
    "v_cvt_pk_bf16_f32 v128, v128, v129\n"
    "v_cvt_pk_bf16_f32 v129, v130, v131\n"
    "ds_write_b128 v243, v[96:99] offset:24640\n"
    "v_mfma_f32_16x16x32_bf16 v[96:99], a[0:3], v[184:187], 0\n"
    "s_waitcnt lgkmcnt(7)\n"
    "v_or_b32_e32 v191, v191, v207\n"
    "v_lshlrev_b32_sdwa v207, 1, v183 dst_sel:DWORD dst_unused:UNUSED_PAD src0_sel:DWORD src1_sel:WORD_1\n"
    "v_smfmac_f32_16x16x64_bf16 v[32:35], v[216:219], v[208:215], v240\n"
    "s_waitcnt lgkmcnt(6)\n"
    "ds_read_b128 v[216:219], v251 offset:11424\n"
    "ds_write_b128 v243, v[112:115] offset:26688\n"
    "v_smfmac_f32_16x16x64_bf16 v[36:39], v[220:223], v[208:215], v240 abid:1\n"
    "s_nop 0\n"
    "v_cvt_pk_bf16_f32 v130, v132, v133\n"
    "v_mfma_f32_16x16x32_bf16 v[180:183], a[148:151], v[156:159], 0\n"
    "s_waitcnt lgkmcnt(7)\n"
    "v_or_b32_e32 v193, v193, v201\n"
    "v_cvt_pk_bf16_f32 v131, v134, v135\n"
    "v_cvt_pk_bf16_f32 v104, v104, v105\n"
    "ds_read_b128 v[208:211], v250 offset:80\n"
    "ds_read_b128 v[212:215], v250 offset:400\n"
    "v_pk_add_f32 v[224:225], v[224:225], v[32:33]\n"
    "v_pk_add_f32 v[226:227], v[226:227], v[34:35]\n"
    "v_pk_fma_f32 v[228:229], v[32:33], v[32:33], v[228:229]\n"
    "v_pk_fma_f32 v[230:231], v[34:35], v[34:35], v[230:231]\n"
    "v_pk_add_f32 v[224:225], v[224:225], v[36:37]\n"
    "v_pk_add_f32 v[226:227], v[226:227], v[38:39]\n"
    "v_pk_fma_f32 v[228:229], v[36:37], v[36:37], v[228:229]\n"
    "v_pk_fma_f32 v[230:231], v[38:39], v[38:39], v[230:231]\n"
    "v_mfma_f32_16x16x32_bf16 v[180:183], a[196:199], v[160:163], v[180:183]\n"
    "s_waitcnt lgkmcnt(6)\n"
    "ds_read_b128 v[220:223], v251 offset:11760\n"
    "ds_write_b128 v243, v[128:131] offset:28736\n"
    "v_mfma_f32_16x16x32_bf16 v[180:183], a[108:111], v[164:167], v[180:183]\n"
    "s_nop 0\n"
    "v_cvt_pk_bf16_f32 v105, v106, v107\n"
    "v_cvt_pk_bf16_f32 v106, v108, v109\n"
    "v_mfma_f32_16x16x32_bf16 v[112:115], a[8:11], v[184:187], 0\n"
    "v_cvt_pk_bf16_f32 v107, v110, v111\n"
    "v_or_b32_e64 v194, v194, v202\n"
    "v_mfma_f32_16x16x32_bf16 v[116:119], a[12:15], v[184:187], 0\n"
    "ds_read_u16_d16_hi v203, v203\n"
    "ds_write_b128 v243, v[104:107] offset:25664\n"
    "v_mfma_f32_16x16x32_bf16 v[128:131], v[236:239], v[184:187], 0\n"
    "v_cvt_pk_bf16_f32 v120, v120, v121\n"
    "v_cvt_pk_bf16_f32 v121, v122, v123\n"
    "v_mfma_f32_16x16x32_bf16 v[104:107], a[0:3], v[188:191], 0\n"
    "v_cvt_pk_bf16_f32 v122, v124, v125\n"
    "v_cvt_pk_bf16_f32 v123, v126, v127\n"
    "s_waitcnt lgkmcnt(4)\n"
    "v_smfmac_f32_16x16x64_bf16 v[40:43], v[216:219], v[208:215], v240\n"
    "s_waitcnt lgkmcnt(3)\n"
    "ds_read_b128 v[216:219], v252 offset:11424\n"
    "ds_write_b128 v243, v[120:123] offset:27712\n"
    "v_smfmac_f32_16x16x64_bf16 v[44:47], v[220:223], v[208:215], v240 abid:1\n"
    "v_pk_fma_f16 v232, v192, v192, s48\n"
    "v_mfma_f32_16x16x32_bf16 v[108:111], a[4:7], v[188:191], 0\n"
    "v_cvt_pk_bf16_f32 v136, v136, v137\n"
    "v_cvt_pk_bf16_f32 v137, v138, v139\n"
    "v_mfma_f32_16x16x32_bf16 v[120:123], a[8:11], v[188:191], 0\n"
    "ds_read_b128 v[208:211], v250 offset:720\n"
    "ds_read_b128 v[212:215], v250 offset:1040\n"
    "v_mfma_f32_16x16x32_bf16 v[124:127], a[12:15], v[188:191], 0\n"
    "ds_read_b128 v[220:223], v252 offset:11760\n"
    "ds_read_b128 a[244:247], v244 offset:2048\n"
    "v_cvt_pk_bf16_f32 v138, v140, v141\n"
    "s_waitcnt lgkmcnt(6)\n"
    "v_or_b32_e32 v195, v195, v203\n"
    "v_cvt_pk_bf16_f32 v139, v142, v143\n"
    "ds_write_b128 v243, v[136:139] offset:29760\n"
    "v_pk_mul_f16 v232, v192, v232\n"
    "v_mfma_f32_16x16x32_bf16 v[136:139], v[236:239], v[188:191], 0\n"
    "v_exp_f16_e64 v234, v232\n"
    "v_lshlrev_b32_sdwa v201, 1, v171 dst_sel:DWORD dst_unused:UNUSED_PAD src0_sel:DWORD src1_sel:WORD_1\n"
    "v_exp_f16_sdwa v234, v232 dst_sel:WORD_1 dst_unused:UNUSED_PRESERVE src0_sel:WORD_1\n"
    "v_lshlrev_b32_sdwa v202, 1, v173 dst_sel:DWORD dst_unused:UNUSED_PAD src0_sel:DWORD src1_sel:WORD_1\n"
    "v_pk_fma_f16 v234, v234, s49, s49\n"
    "ds_read_u16 v197, v197\n"
    "ds_read_u16_d16_hi v205, v205\n"
    "v_lshlrev_b32_sdwa v203, 1, v175 dst_sel:DWORD dst_unused:UNUSED_PAD src0_sel:DWORD src1_sel:WORD_1\n"
    "v_rcp_f16_e64 v232, v234\n"
    "ds_read_u16 v198, v198\n"
    "ds_read_u16_d16_hi v206, v206\n"
    "s_waitcnt lgkmcnt(7)\n"
    "v_smfmac_f32_16x16x64_bf16 v[40:43], v[216:219], v[208:215], v240\n"
    "s_waitcnt lgkmcnt(6)\n"
    "ds_read_b128 v[216:219], v251 offset:12096\n"
    "ds_read_u16 v199, v199\n"
    "v_smfmac_f32_16x16x64_bf16 v[44:47], v[220:223], v[208:215], v240 abid:1\n"
    "s_waitcnt lgkmcnt(7)\n"
    "v_rcp_f16_sdwa v232, v234 dst_sel:WORD_1 dst_unused:UNUSED_PRESERVE src0_sel:WORD_1\n"
    "v_mfma_f32_16x16x32_bf16 v[132:135], a[244:247], v[184:187], 0\n"
    "s_nop 0\n"
    "ds_read_b128 v[208:211], v250 offset:160\n"
    "v_cvt_pk_f16_f32 v184, v168, v169\n"
    "v_lshlrev_b32_sdwa v185, 1, v170 dst_sel:DWORD dst_unused:UNUSED_PAD src0_sel:DWORD src1_sel:WORD_1\n"
    "ds_read_b128 v[212:215], v250 offset:480\n"
    "v_pk_add_f32 v[224:225], v[224:225], v[40:41]\n"
    "v_pk_add_f32 v[226:227], v[226:227], v[42:43]\n"
    "v_pk_fma_f32 v[228:229], v[40:41], v[40:41], v[228:229]\n"
    "v_pk_fma_f32 v[230:231], v[42:43], v[42:43], v[230:231]\n"
    "v_pk_add_f32 v[224:225], v[224:225], v[44:45]\n"
    "v_pk_add_f32 v[226:227], v[226:227], v[46:47]\n"
    "v_pk_fma_f32 v[228:229], v[44:45], v[44:45], v[228:229]\n"
    "v_pk_fma_f32 v[230:231], v[46:47], v[46:47], v[230:231]\n"
    "v_mfma_f32_16x16x32_bf16 v[140:143], a[244:247], v[188:191], 0\n"
    "ds_read_b128 v[220:223], v251 offset:12432\n"
    "ds_read_u16_d16_hi v207, v207\n"
    "v_mfma_f32_16x16x32_bf16 v[168:171], a[152:155], v[144:147], 0\n"
    "ds_read_b128 a[252:255], v249 offset:0\n"
    "v_lshlrev_b32_sdwa v186, 1, v172 dst_sel:DWORD dst_unused:UNUSED_PAD src0_sel:DWORD src1_sel:WORD_1\n"
    "v_mfma_f32_16x16x32_bf16 v[168:171], a[200:203], v[148:151], v[168:171]\n"
    "s_waitcnt lgkmcnt(9)\n"
    "ds_read_u16 v185, v185\n"
    "v_or_b32_e32 v197, v197, v205\n"
    "v_mfma_f32_16x16x32_bf16 v[168:171], a[112:115], v[152:155], v[168:171]\n"
    "v_lshlrev_b32_sdwa v187, 1, v174 dst_sel:DWORD dst_unused:UNUSED_PAD src0_sel:DWORD src1_sel:WORD_1\n"
    "v_cvt_pk_f16_f32 v188, v176, v177\n"
    "v_mfma_f32_16x16x32_bf16 v[172:175], a[156:159], v[144:147], 0\n"
    "ds_read_u16_d16_hi v201, v201\n"
    "v_lshlrev_b32_sdwa v189, 1, v178 dst_sel:DWORD dst_unused:UNUSED_PAD src0_sel:DWORD src1_sel:WORD_1\n"
    "v_lshlrev_b32_sdwa v205, 1, v179 dst_sel:DWORD dst_unused:UNUSED_PAD src0_sel:DWORD src1_sel:WORD_1\n"
    "v_lshlrev_b32_sdwa v190, 1, v180 dst_sel:DWORD dst_unused:UNUSED_PAD src0_sel:DWORD src1_sel:WORD_1\n"
    "v_lshlrev_b32_sdwa v191, 1, v182 dst_sel:DWORD dst_unused:UNUSED_PAD src0_sel:DWORD src1_sel:WORD_1\n"
    "s_waitcnt lgkmcnt(8)\n"
    "v_or_b32_e32 v198, v198, v206\n"
    "v_lshlrev_b32_sdwa v206, 1, v181 dst_sel:DWORD dst_unused:UNUSED_PAD src0_sel:DWORD src1_sel:WORD_1\n"
    "v_fma_mix_f32 v234, v192, v232, 0 op_sel_hi:[1,1,0]\n"
    "v_mfma_f32_16x16x32_bf16 v[172:175], a[204:207], v[148:151], v[172:175]\n"
    "ds_read_u16 v186, v186\n"
    "ds_read_u16_d16_hi v202, v202\n"
    "v_mfma_f32_16x16x32_bf16 v[172:175], a[116:119], v[152:155], v[172:175]\n"
    "v_fma_mix_f32 v232, v192, v232, 0 op_sel:[1,1,0] op_sel_hi:[1,1,0]\n"
    "v_lshlrev_b32_sdwa v200, 1, v169 dst_sel:DWORD dst_unused:UNUSED_PAD src0_sel:DWORD src1_sel:WORD_1\n"
    "v_mfma_f32_16x16x32_bf16 v[176:179], a[152:155], v[156:159], 0\n"
    "s_waitcnt lgkmcnt(7)\n"
    "ds_read_b128 v[236:239], v244 offset:4096\n"
    "v_cvt_pk_bf16_f32 v192, v234, v232\n"
    "v_smfmac_f32_16x16x64_bf16 v[48:51], v[216:219], v[208:215], v240\n"
    "s_waitcnt lgkmcnt(7)\n"
    "ds_read_b128 v[216:219], v252 offset:12096\n"
    "v_smfmac_f32_16x16x64_bf16 v[52:55], v[220:223], v[208:215], v240 abid:1\n"
    "ds_read_b128 v[208:211], v250 offset:800\n"
    "v_mfma_f32_16x16x32_bf16 v[176:179], a[200:203], v[160:163], v[176:179]\n"
    "ds_read_b128 v[212:215], v250 offset:1120\n"
    "v_pk_fma_f16 v232, v196, v196, s48\n"
    "v_mfma_f32_16x16x32_bf16 v[176:179], a[112:115], v[164:167], v[176:179]\n"
    "s_waitcnt lgkmcnt(8)\n"
    "ds_read_b128 v[220:223], v252 offset:12432\n"
    "buffer_store_dwordx4 a[252:255], v255, s[44:47], s57 offen offset:0 sc1\n"
    "v_or_b32_e32 v199, v199, v207\n"
    "v_lshlrev_b32_sdwa v207, 1, v183 dst_sel:DWORD dst_unused:UNUSED_PAD src0_sel:DWORD src1_sel:WORD_1\n"
    "v_mfma_f32_16x16x32_bf16 v[180:183], a[156:159], v[156:159], 0\n"
    "ds_read_b128 a[252:255], v249 offset:2048\n"
    "ds_read_u16 v187, v187\n"
    "v_pk_mul_f16 v232, v196, v232\n"
    "v_mfma_f32_16x16x32_bf16 v[180:183], a[204:207], v[160:163], v[180:183]\n"
    "ds_read_u16_d16_hi v203, v203\n"
    "v_exp_f16_e32 v234, v232\n"
    "v_mfma_f32_16x16x32_bf16 v[180:183], a[116:119], v[164:167], v[180:183]\n"
    "s_waitcnt lgkmcnt(10)\n"
    "v_exp_f16_sdwa v234, v232 dst_sel:WORD_1 dst_unused:UNUSED_PRESERVE src0_sel:WORD_1\n"
    "v_or_b32_e64 v185, v185, v201\n"
    "v_pk_fma_f16 v234, v234, s49, s49\n"
    "v_mfma_f32_16x16x32_bf16 v[96:99], a[16:19], v[192:195], v[96:99]\n"
    "ds_read_b128 a[244:247], v244 offset:6144\n"
    "ds_read_u16 v189, v189\n"
    "v_mfma_f32_16x16x32_bf16 v[100:103], a[20:23], v[192:195], v[100:103]\n"
    "v_rcp_f16_e32 v232, v234\n"
    "v_mfma_f32_16x16x32_bf16 v[112:115], a[24:27], v[192:195], v[112:115]\n"
    "s_waitcnt lgkmcnt(9)\n"
    "ds_read_u16_d16_hi v205, v205\n"
    "v_rcp_f16_sdwa v232, v234 dst_sel:WORD_1 dst_unused:UNUSED_PRESERVE src0_sel:WORD_1\n"
    "v_mfma_f32_16x16x32_bf16 v[116:119], a[28:31], v[192:195], v[116:119]\n"
    "ds_read_u16 v190, v190\n"
    "v_fma_mix_f32 v234, v196, v232, 0 op_sel_hi:[1,1,0]\n"
    "v_mfma_f32_16x16x32_bf16 v[128:131], v[236:239], v[192:195], v[128:131]\n"
    "s_waitcnt lgkmcnt(8)\n"
    "v_fma_mix_f32 v232, v196, v232, 0 op_sel:[1,1,0] op_sel_hi:[1,1,0]\n"
    "v_or_b32_e32 v186, v186, v202\n"
    "v_smfmac_f32_16x16x64_bf16 v[48:51], v[216:219], v[208:215], v240\n"
    "s_waitcnt lgkmcnt(7)\n"
    "ds_read_b128 v[216:219], v251 offset:12768\n"
    "v_smfmac_f32_16x16x64_bf16 v[52:55], v[220:223], v[208:215], v240 abid:1\n"
    "s_waitcnt lgkmcnt(7)\n"
    "ds_read_b128 v[208:211], v250 offset:240\n"
    "buffer_store_dwordx4 a[252:255], v255, s[44:47], s57 offen offset:64 sc1\n"
    "ds_read_b128 v[212:215], v250 offset:560\n"
    "ds_read_b128 v[220:223], v251 offset:13104\n"
    "v_pk_add_f32 v[224:225], v[224:225], v[48:49]\n"
    "v_pk_add_f32 v[226:227], v[226:227], v[50:51]\n"
    "v_pk_fma_f32 v[228:229], v[48:49], v[48:49], v[228:229]\n"
    "v_pk_fma_f32 v[230:231], v[50:51], v[50:51], v[230:231]\n"
    "v_pk_add_f32 v[224:225], v[224:225], v[52:53]\n"
    "v_pk_add_f32 v[226:227], v[226:227], v[54:55]\n"
    "v_pk_fma_f32 v[228:229], v[52:53], v[52:53], v[228:229]\n"
    "v_pk_fma_f32 v[230:231], v[54:55], v[54:55], v[230:231]\n"
    "v_cvt_pk_bf16_f32 v196, v234, v232\n"
    "s_waitcnt lgkmcnt(7)\n"
    "v_mfma_f32_16x16x32_bf16 v[132:135], a[244:247], v[192:195], v[132:135]\n"
    "s_nop 0\n"
    "ds_read_u16_d16_hi v206, v206\n"
    "ds_read_u16 v191, v191\n"
    "v_mfma_f32_16x16x32_bf16 v[104:107], a[16:19], v[196:199], v[104:107]\n"
    "ds_read_u16_d16_hi v207, v207\n"
    "v_pk_fma_f16 v232, v184, v184, s48\n"
    "v_mfma_f32_16x16x32_bf16 v[108:111], a[20:23], v[196:199], v[108:111]\n"
    "v_or_b32_e64 v187, v187, v203\n"
    "v_pk_mul_f16 v232, v184, v232\n"
    "v_mfma_f32_16x16x32_bf16 v[120:123], a[24:27], v[196:199], v[120:123]\n"
    "ds_read_b128 a[252:255], v249 offset:4096\n"
    "v_exp_f16_e32 v234, v232\n"
    "v_mfma_f32_16x16x32_bf16 v[124:127], a[28:31], v[196:199], v[124:127]\n"
    "s_waitcnt lgkmcnt(9)\n"
    "ds_read_b128 a[240:243], v244 offset:34816\n"
    "v_or_b32_e64 v189, v189, v205\n"
    "v_mfma_f32_16x16x32_bf16 v[136:139], v[236:239], v[196:199], v[136:139]\n"
    "ds_read_u16_d16_hi v200, v200\n"
    "v_exp_f16_sdwa v234, v232 dst_sel:WORD_1 dst_unused:UNUSED_PRESERVE src0_sel:WORD_1\n"
    "v_mfma_f32_16x16x32_bf16 v[140:143], a[244:247], v[196:199], v[140:143]\n"
    "v_pk_fma_f16 v234, v234, s49, s49\n"
    "v_lshlrev_b32_sdwa v192, 1, v168 dst_sel:DWORD dst_unused:UNUSED_PAD src0_sel:DWORD src1_sel:WORD_1\n"
    "v_lshlrev_b32_sdwa v193, 1, v170 dst_sel:DWORD dst_unused:UNUSED_PAD src0_sel:DWORD src1_sel:WORD_1\n"
    "v_lshlrev_b32_sdwa v201, 1, v171 dst_sel:DWORD dst_unused:UNUSED_PAD src0_sel:DWORD src1_sel:WORD_1\n"
    "s_waitcnt lgkmcnt(7)\n"
    "v_smfmac_f32_16x16x64_bf16 v[56:59], v[216:219], v[208:215], v240\n"
    "s_waitcnt lgkmcnt(6)\n"
    "ds_read_b128 v[216:219], v252 offset:12768\n"
    "ds_read_u16 v192, v192\n"
    "v_smfmac_f32_16x16x64_bf16 v[60:63], v[220:223], v[208:215], v240 abid:1\n"
    "ds_read_b128 v[208:211], v250 offset:880\n"
    "v_mfma_f32_16x16x32_bf16 v[168:171], a[160:163], v[144:147], 0\n"
    "s_nop 0\n"
    "ds_read_b128 v[212:215], v250 offset:1200\n"
    "v_lshlrev_b32_sdwa v194, 1, v172 dst_sel:DWORD dst_unused:UNUSED_PAD src0_sel:DWORD src1_sel:WORD_1\n"
    "v_mfma_f32_16x16x32_bf16 v[168:171], a[208:211], v[148:151], v[168:171]\n"
    "s_waitcnt lgkmcnt(9)\n"
    "ds_read_b128 v[220:223], v252 offset:13104\n"
    "v_lshlrev_b32_sdwa v202, 1, v173 dst_sel:DWORD dst_unused:UNUSED_PAD src0_sel:DWORD src1_sel:WORD_1\n"
    "v_lshlrev_b32_sdwa v195, 1, v174 dst_sel:DWORD dst_unused:UNUSED_PAD src0_sel:DWORD src1_sel:WORD_1\n"
    "v_lshlrev_b32_sdwa v203, 1, v175 dst_sel:DWORD dst_unused:UNUSED_PAD src0_sel:DWORD src1_sel:WORD_1\n"
    "v_rcp_f16_e64 v232, v234\n"
    "v_mfma_f32_16x16x32_bf16 v[168:171], a[120:123], v[152:155], v[168:171]\n"
    "ds_read_u16 v193, v193\n"
    "v_or_b32_e32 v190, v190, v206\n"
    "v_rcp_f16_sdwa v232, v234 dst_sel:WORD_1 dst_unused:UNUSED_PRESERVE src0_sel:WORD_1\n"
    "v_mfma_f32_16x16x32_bf16 v[172:175], a[164:167], v[144:147], 0\n"
    "s_waitcnt lgkmcnt(8)\n"
    "ds_read_u16_d16_hi v201, v201\n"
    "buffer_store_dwordx4 a[252:255], v255, s[44:47], s57 offen offset:128 sc1\n"
    "v_lshlrev_b32_sdwa v196, 1, v176 dst_sel:DWORD dst_unused:UNUSED_PAD src0_sel:DWORD src1_sel:WORD_1\n"
    "v_lshlrev_b32_sdwa v204, 1, v177 dst_sel:DWORD dst_unused:UNUSED_PAD src0_sel:DWORD src1_sel:WORD_1\n"
    "v_lshlrev_b32_sdwa v197, 1, v178 dst_sel:DWORD dst_unused:UNUSED_PAD src0_sel:DWORD src1_sel:WORD_1\n"
    "v_lshlrev_b32_sdwa v205, 1, v179 dst_sel:DWORD dst_unused:UNUSED_PAD src0_sel:DWORD src1_sel:WORD_1\n"
    "v_lshlrev_b32_sdwa v198, 1, v180 dst_sel:DWORD dst_unused:UNUSED_PAD src0_sel:DWORD src1_sel:WORD_1\n"
    "v_lshlrev_b32_sdwa v206, 1, v181 dst_sel:DWORD dst_unused:UNUSED_PAD src0_sel:DWORD src1_sel:WORD_1\n"
    "v_lshlrev_b32_sdwa v199, 1, v182 dst_sel:DWORD dst_unused:UNUSED_PAD src0_sel:DWORD src1_sel:WORD_1\n"
    "v_fma_mix_f32 v234, v184, v232, 0 op_sel_hi:[1,1,0]\n"
    "v_mfma_f32_16x16x32_bf16 v[172:175], a[212:215], v[148:151], v[172:175]\n"
    "s_waitcnt lgkmcnt(6)\n"
    "ds_read_b128 a[252:255], v249 offset:1024\n"
    "ds_read_u16 v194, v194\n"
    "v_mfma_f32_16x16x32_bf16 v[172:175], a[240:243], v[152:155], v[172:175]\n"
    "s_nop 0\n"
    "ds_read_b128 v[236:239], v244 offset:8192\n"
    "ds_read_u16_d16_hi v202, v202\n"
    "v_or_b32_e32 v191, v191, v207\n"
    "v_lshlrev_b32_sdwa v207, 1, v183 dst_sel:DWORD dst_unused:UNUSED_PAD src0_sel:DWORD src1_sel:WORD_1\n"
    "v_fma_mix_f32 v232, v184, v232, 0 op_sel:[1,1,0] op_sel_hi:[1,1,0]\n"
    "v_mfma_f32_16x16x32_bf16 v[176:179], a[160:163], v[156:159], 0\n"
    "s_waitcnt lgkmcnt(8)\n"
    "ds_read_u16 v195, v195\n"
    "v_cvt_pk_bf16_f32 v184, v234, v232\n"
    "s_waitcnt lgkmcnt(8)\n"
    "v_smfmac_f32_16x16x64_bf16 v[56:59], v[216:219], v[208:215], v240\n"
    "s_waitcnt lgkmcnt(7)\n"
    "ds_read_b128 a[244:247], v244 offset:10240\n"
    "v_smfmac_f32_16x16x64_bf16 v[60:63], v[220:223], v[208:215], v240 abid:1\n"
    "s_barrier\n"
    "v_mfma_f32_16x16x32_bf16 v[176:179], a[208:211], v[160:163], v[176:179]\n"
    "ds_read_u16_d16_hi v203, v203\n"
    "ds_write2_b32 v248, v64, v65 offset0:0 offset1:20\n"
    "v_mfma_f32_16x16x32_bf16 v[176:179], a[120:123], v[164:167], v[176:179]\n"
    "s_waitcnt lgkmcnt(7)\n"
    "buffer_store_dwordx4 a[252:255], v255, s[44:47], s100 offen offset:0 sc1\n"
    "ds_write2_b32 v248, v66, v67 offset0:40 offset1:60\n"
    "v_pk_add_f32 v[224:225], v[224:225], v[56:57]\n"
    "v_pk_add_f32 v[226:227], v[226:227], v[58:59]\n"
    "v_pk_fma_f32 v[228:229], v[56:57], v[56:57], v[228:229]\n"
    "v_pk_fma_f32 v[230:231], v[58:59], v[58:59], v[230:231]\n"
    "v_pk_add_f32 v[224:225], v[224:225], v[60:61]\n"
    "v_pk_add_f32 v[226:227], v[226:227], v[62:63]\n"
    "v_pk_fma_f32 v[228:229], v[60:61], v[60:61], v[228:229]\n"
    "v_pk_fma_f32 v[230:231], v[62:63], v[62:63], v[230:231]\n"
    "v_add_u32_e32 v248, 1360, v248\n"
    "v_pk_fma_f16 v232, v188, v188, s48\n"
    "v_mfma_f32_16x16x32_bf16 v[180:183], a[164:167], v[156:159], 0\n"
    "ds_read_u16 v196, v196\n"
    "s_waitcnt vmcnt(5)\n"
    "ds_write2_b32 v235, v84, v85 offset0:0 offset1:20\n"
    "v_pk_mul_f16 v232, v188, v232\n"
    "v_mfma_f32_16x16x32_bf16 v[180:183], a[212:215], v[160:163], v[180:183]\n"
    "v_exp_f16_e64 v234, v232\n"
    "v_mfma_f32_16x16x32_bf16 v[180:183], a[240:243], v[164:167], v[180:183]\n"
    "s_waitcnt lgkmcnt(5)\n"
    "ds_read_u16_d16_hi v204, v204\n"
    "ds_write2_b32 v235, v86, v87 offset0:40 offset1:60\n"
    "v_add_u32_e32 v235, 1360, v235\n"
    "v_exp_f16_sdwa v234, v232 dst_sel:WORD_1 dst_unused:UNUSED_PRESERVE src0_sel:WORD_1\n"
    "v_or_b32_e32 v192, v192, v200\n"
    "v_pk_fma_f16 v234, v234, s49, s49\n"
    "v_mfma_f32_16x16x32_bf16 v[96:99], a[32:35], v[184:187], v[96:99]\n"
    "s_waitcnt lgkmcnt(4)\n"
    "ds_read_u16 v197, v197\n"
    "ds_write2_b32 v248, v68, v69 offset0:0 offset1:20\n"
    "v_mfma_f32_16x16x32_bf16 v[100:103], a[36:39], v[184:187], v[100:103]\n"
    "v_rcp_f16_e64 v232, v234\n"
    "v_mfma_f32_16x16x32_bf16 v[112:115], a[40:43], v[184:187], v[112:115]\n"
    "ds_read_u16_d16_hi v205, v205\n"
    "ds_write2_b32 v248, v70, v71 offset0:40 offset1:60\n"
    "v_add_u32_e32 v248, 1360, v248\n"
    "v_rcp_f16_sdwa v232, v234 dst_sel:WORD_1 dst_unused:UNUSED_PRESERVE src0_sel:WORD_1\n"
    "v_mfma_f32_16x16x32_bf16 v[116:119], a[44:47], v[184:187], v[116:119]\n"
    "ds_read_u16 v198, v198\n"
    "ds_read_u16_d16_hi v206, v206\n"
    "v_fma_mix_f32 v234, v188, v232, 0 op_sel_hi:[1,1,0]\n"
    "v_mfma_f32_16x16x32_bf16 v[128:131], v[236:239], v[184:187], v[128:131]\n"
    "ds_read_u16 v199, v199\n"
    "v_fma_mix_f32 v232, v188, v232, 0 op_sel:[1,1,0] op_sel_hi:[1,1,0]\n"
    "v_mfma_f32_16x16x32_bf16 v[132:135], a[244:247], v[184:187], v[132:135]\n"
    "ds_read_b128 a[252:255], v249 offset:3072\n"
    "v_cvt_pk_bf16_f32 v188, v234, v232\n"
    "v_or_b32_e32 v193, v193, v201\n"
    "v_cvt_pk_f16_f32 v184, v168, v169\n"
    "v_mfma_f32_16x16x32_bf16 v[104:107], a[32:35], v[188:191], v[104:107]\n"
    "s_waitcnt lgkmcnt(8)\n"
    "ds_read_u16_d16_hi v207, v207\n"
    "v_lshlrev_b32_sdwa v185, 1, v170 dst_sel:DWORD dst_unused:UNUSED_PAD src0_sel:DWORD src1_sel:WORD_1\n"
    "v_mfma_f32_16x16x32_bf16 v[108:111], a[36:39], v[188:191], v[108:111]\n"
    "ds_read_b128 a[240:243], v244 offset:38912\n"
    "v_lshlrev_b32_sdwa v201, 1, v171 dst_sel:DWORD dst_unused:UNUSED_PAD src0_sel:DWORD src1_sel:WORD_1\n"
    "v_mfma_f32_16x16x32_bf16 v[120:123], a[40:43], v[188:191], v[120:123]\n"
    "ds_read_u16 v185, v185\n"
    "v_lshlrev_b32_sdwa v186, 1, v172 dst_sel:DWORD dst_unused:UNUSED_PAD src0_sel:DWORD src1_sel:WORD_1\n"
    "v_mfma_f32_16x16x32_bf16 v[124:127], a[44:47], v[188:191], v[124:127]\n"
    "v_or_b32_e64 v194, v194, v202\n"
    "v_lshlrev_b32_sdwa v202, 1, v173 dst_sel:DWORD dst_unused:UNUSED_PAD src0_sel:DWORD src1_sel:WORD_1\n"
    "v_mfma_f32_16x16x32_bf16 v[136:139], v[236:239], v[188:191], v[136:139]\n"
    "s_nop 0\n"
    "ds_read_u16_d16_hi v201, v201\n"
    "v_lshlrev_b32_sdwa v187, 1, v174 dst_sel:DWORD dst_unused:UNUSED_PAD src0_sel:DWORD src1_sel:WORD_1\n"
    "v_mfma_f32_16x16x32_bf16 v[140:143], a[244:247], v[188:191], v[140:143]\n"
    "s_waitcnt lgkmcnt(8)\n"
    "v_cvt_pk_f16_f32 v188, v176, v177\n"
    "v_lshlrev_b32_sdwa v189, 1, v178 dst_sel:DWORD dst_unused:UNUSED_PAD src0_sel:DWORD src1_sel:WORD_1\n"
    "v_mfma_f32_16x16x32_bf16 v[168:171], a[168:171], v[144:147], 0\n"
    "ds_read_u16 v186, v186\n"
    "v_lshlrev_b32_sdwa v190, 1, v180 dst_sel:DWORD dst_unused:UNUSED_PAD src0_sel:DWORD src1_sel:WORD_1\n"
    "v_mfma_f32_16x16x32_bf16 v[168:171], a[216:219], v[148:151], v[168:171]\n"
    "v_lshlrev_b32_sdwa v191, 1, v182 dst_sel:DWORD dst_unused:UNUSED_PAD src0_sel:DWORD src1_sel:WORD_1\n"
    "v_or_b32_e64 v195, v195, v203\n"
    "v_mfma_f32_16x16x32_bf16 v[168:171], a[124:127], v[152:155], v[168:171]\n"
    "ds_read_u16_d16_hi v202, v202\n"
    "v_lshlrev_b32_sdwa v203, 1, v175 dst_sel:DWORD dst_unused:UNUSED_PAD src0_sel:DWORD src1_sel:WORD_1\n"
    "v_mfma_f32_16x16x32_bf16 v[172:175], a[172:175], v[144:147], 0\n"
    "s_waitcnt lgkmcnt(8)\n"
    "ds_read_b128 v[236:239], v244 offset:12288\n"
    "v_or_b32_e32 v196, v196, v204\n"
    "v_mfma_f32_16x16x32_bf16 v[172:175], a[220:223], v[148:151], v[172:175]\n"
    "ds_read_u16 v187, v187\n"
    "s_waitcnt lgkmcnt(8)\n"
    "buffer_store_dwordx4 a[252:255], v255, s[44:47], s100 offen offset:64 sc1\n"
    "s_waitcnt vmcnt(5)\n"
    "ds_write2_b32 v235, v88, v89 offset0:0 offset1:20\n"
    "v_or_b32_e32 v197, v197, v205\n"
    "s_waitcnt lgkmcnt(7)\n"
    "v_mfma_f32_16x16x32_bf16 v[172:175], a[240:243], v[152:155], v[172:175]\n"
    "ds_read_b128 a[252:255], v249 offset:5120\n"
    "ds_write2_b32 v235, v90, v91 offset0:40 offset1:60\n"
    "v_add_u32_e32 v235, -1360, v235\n"
    "v_lshlrev_b32_sdwa v205, 1, v179 dst_sel:DWORD dst_unused:UNUSED_PAD src0_sel:DWORD src1_sel:WORD_1\n"
    "v_mfma_f32_16x16x32_bf16 v[176:179], a[168:171], v[156:159], 0\n"
    "v_or_b32_e64 v198, v198, v206\n"
    "v_lshlrev_b32_sdwa v206, 1, v181 dst_sel:DWORD dst_unused:UNUSED_PAD src0_sel:DWORD src1_sel:WORD_1\n"
    "v_mfma_f32_16x16x32_bf16 v[176:179], a[216:219], v[160:163], v[176:179]\n"
    "ds_read_u16_d16_hi v203, v203\n"
    "ds_write2_b32 v248, v72, v73 offset0:0 offset1:20\n"
    "v_mfma_f32_16x16x32_bf16 v[176:179], a[124:127], v[164:167], v[176:179]\n"
    "s_waitcnt lgkmcnt(9)\n"
    "v_or_b32_e32 v199, v199, v207\n"
    "v_lshlrev_b32_sdwa v207, 1, v183 dst_sel:DWORD dst_unused:UNUSED_PAD src0_sel:DWORD src1_sel:WORD_1\n"
    "v_mfma_f32_16x16x32_bf16 v[180:183], a[172:175], v[156:159], 0\n"
    "ds_read_b128 a[244:247], v244 offset:14336\n"
    "ds_write2_b32 v248, v74, v75 offset0:40 offset1:60\n"
    "v_mfma_f32_16x16x32_bf16 v[180:183], a[220:223], v[160:163], v[180:183]\n"
    "v_add_u32_e32 v248, 1360, v248\n"
    "v_or_b32_e32 v185, v185, v201\n"
    "v_mfma_f32_16x16x32_bf16 v[180:183], a[240:243], v[164:167], v[180:183]\n"
    "s_waitcnt lgkmcnt(8)\n"
    "ds_read_u16 v189, v189\n"
    "ds_write2_b32 v248, v76, v77 offset0:0 offset1:20\n"
    "v_mfma_f32_16x16x32_bf16 v[96:99], a[48:51], v[192:195], v[96:99]\n"
    "v_or_b32_e64 v186, v186, v202\n"
    "v_pk_fma_f16 v232, v184, v184, s48\n"
    "v_mfma_f32_16x16x32_bf16 v[100:103], a[52:55], v[192:195], v[100:103]\n"
    "ds_read_u16_d16_hi v205, v205\n"
    "ds_write2_b32 v248, v78, v79 offset0:40 offset1:60\n"
    "v_mfma_f32_16x16x32_bf16 v[112:115], a[56:59], v[192:195], v[112:115]\n"
    "s_waitcnt lgkmcnt(8)\n"
    "buffer_store_dwordx4 a[252:255], v255, s[44:47], s100 offen offset:128 sc1\n"
    "ds_read_u16 v190, v190\n"
    "v_mfma_f32_16x16x32_bf16 v[116:119], a[60:63], v[192:195], v[116:119]\n"
    "ds_read_b128 v[144:147], v243 offset:0\n"
    "ds_read_u16_d16_hi v206, v206\n"
    "v_mfma_f32_16x16x32_bf16 v[128:131], v[236:239], v[192:195], v[128:131]\n"
    "s_waitcnt lgkmcnt(9)\n"
    "v_add_u32_e32 v248, 1360, v248\n"
    "v_or_b32_e32 v187, v187, v203\n"
    "v_mfma_f32_16x16x32_bf16 v[104:107], a[48:51], v[196:199], v[104:107]\n"
    "s_waitcnt lgkmcnt(7)\n"
    "ds_read_b128 v[148:151], v243 offset:1024\n"
    "v_pk_mul_f16 v232, v184, v232\n"
    "v_mfma_f32_16x16x32_bf16 v[132:135], a[244:247], v[192:195], v[132:135]\n"
    "ds_read_u16 v191, v191\n"
    "v_exp_f16_e64 v234, v232\n"
    "v_mfma_f32_16x16x32_bf16 v[108:111], a[52:55], v[196:199], v[108:111]\n"
    "v_exp_f16_sdwa v234, v232 dst_sel:WORD_1 dst_unused:UNUSED_PRESERVE src0_sel:WORD_1\n"
    "v_mfma_f32_16x16x32_bf16 v[120:123], a[56:59], v[196:199], v[120:123]\n"
    "ds_read_b128 v[152:155], v243 offset:2048\n"
    "ds_write2_b32 v248, v80, v81 offset0:0 offset1:20\n"
    "v_mfma_f32_16x16x32_bf16 v[124:127], a[60:63], v[196:199], v[124:127]\n"
    "v_pk_fma_f16 v234, v234, s49, s49\n"
    "v_cvt_pk_f16_f32 v192, v168, v169\n"
    "v_mfma_f32_16x16x32_bf16 v[136:139], v[236:239], v[196:199], v[136:139]\n"
    "ds_read_u16_d16_hi v207, v207\n"
    "v_lshlrev_b32_sdwa v193, 1, v170 dst_sel:DWORD dst_unused:UNUSED_PAD src0_sel:DWORD src1_sel:WORD_1\n"
    "v_mfma_f32_16x16x32_bf16 v[140:143], a[244:247], v[196:199], v[140:143]\n"
    "s_waitcnt lgkmcnt(8)\n"
    "ds_read_b128 v[156:159], v243 offset:3072\n"
    "v_or_b32_e32 v189, v189, v205\n"
    "v_lshlrev_b32_sdwa v201, 1, v171 dst_sel:DWORD dst_unused:UNUSED_PAD src0_sel:DWORD src1_sel:WORD_1\n"
    "v_lshlrev_b32_sdwa v194, 1, v172 dst_sel:DWORD dst_unused:UNUSED_PAD src0_sel:DWORD src1_sel:WORD_1\n"
    "v_lshlrev_b32_sdwa v202, 1, v173 dst_sel:DWORD dst_unused:UNUSED_PAD src0_sel:DWORD src1_sel:WORD_1\n"
    "v_lshlrev_b32_sdwa v195, 1, v174 dst_sel:DWORD dst_unused:UNUSED_PAD src0_sel:DWORD src1_sel:WORD_1\n"
    "v_lshlrev_b32_sdwa v203, 1, v175 dst_sel:DWORD dst_unused:UNUSED_PAD src0_sel:DWORD src1_sel:WORD_1\n"
    "v_rcp_f16_e32 v232, v234\n"
    "s_waitcnt lgkmcnt(7)\n"
    "v_mfma_f32_16x16x32_bf16 v[168:171], a[128:131], v[144:147], 0\n"
    "ds_read_b128 v[160:163], v243 offset:4096\n"
    "ds_write2_b32 v248, v82, v83 offset0:40 offset1:60\n"
    "v_mfma_f32_32x32x1_2b_f32 v[64:95], v254, v254, 0\n"
    "buffer_load_dwordx4 a[232:235], v245, s[40:43], s58 offen offset:64\n"
    "buffer_load_dwordx4 a[224:227], v245, s[40:43], s58 offen offset:0\n"
    "v_add_u32_e32 v248, -5440, v248\n"
    "s_waitcnt lgkmcnt(7)\n"
    "v_or_b32_e32 v190, v190, v206\n"
    "v_rcp_f16_sdwa v232, v234 dst_sel:WORD_1 dst_unused:UNUSED_PRESERVE src0_sel:WORD_1\n"
    "v_cvt_pk_f16_f32 v196, v176, v177\n"
    "v_lshlrev_b32_sdwa v197, 1, v178 dst_sel:DWORD dst_unused:UNUSED_PAD src0_sel:DWORD src1_sel:WORD_1\n"
    "v_lshlrev_b32_sdwa v205, 1, v179 dst_sel:DWORD dst_unused:UNUSED_PAD src0_sel:DWORD src1_sel:WORD_1\n"
    "v_lshlrev_b32_sdwa v198, 1, v180 dst_sel:DWORD dst_unused:UNUSED_PAD src0_sel:DWORD src1_sel:WORD_1\n"
    "v_lshlrev_b32_sdwa v206, 1, v181 dst_sel:DWORD dst_unused:UNUSED_PAD src0_sel:DWORD src1_sel:WORD_1\n"
    "v_lshlrev_b32_sdwa v199, 1, v182 dst_sel:DWORD dst_unused:UNUSED_PAD src0_sel:DWORD src1_sel:WORD_1\n"
    "v_fma_mix_f32 v234, v184, v232, 0 op_sel_hi:[1,1,0]\n"
    "v_mfma_f32_16x16x32_bf16 v[168:171], a[176:179], v[148:151], v[168:171]\n"
    "s_waitcnt lgkmcnt(4)\n"
    "ds_read_b128 v[164:167], v243 offset:5120\n"
    "ds_read_b128 a[240:243], v244 offset:32768\n"
    "v_mfma_f32_16x16x32_bf16 v[168:171], a[96:99], v[152:155], v[168:171]\n"
    "s_nop 0\n"
    "ds_read_u16 v193, v193\n"
    "ds_read_u16_d16_hi v201, v201\n"
    "v_mfma_f32_16x16x32_bf16 v[172:175], a[132:135], v[144:147], 0\n"
    "ds_read_u16 v194, v194\n"
    "v_fma_mix_f32 v232, v184, v232, 0 op_sel:[1,1,0] op_sel_hi:[1,1,0]\n"
    "v_mfma_f32_16x16x32_bf16 v[172:175], a[180:183], v[148:151], v[172:175]\n"
    "s_waitcnt lgkmcnt(7)\n"
    "v_or_b32_e32 v191, v191, v207\n"
    "v_lshlrev_b32_sdwa v207, 1, v183 dst_sel:DWORD dst_unused:UNUSED_PAD src0_sel:DWORD src1_sel:WORD_1\n"
    "v_mfma_f32_16x16x32_bf16 v[176:179], a[128:131], v[156:159], 0\n"
    "ds_read_u16_d16_hi v202, v202\n"
    "v_cvt_pk_bf16_f32 v184, v234, v232\n"
    "s_waitcnt lgkmcnt(6)\n"
    "v_mfma_f32_16x16x32_bf16 v[176:179], a[176:179], v[160:163], v[176:179]\n"
    "s_nop 0\n"
    "ds_read_b128 v[236:239], v244 offset:16384\n"
    "v_pk_fma_f16 v232, v188, v188, s48\n"
    "v_mfma_f32_16x16x32_bf16 v[180:183], a[132:135], v[156:159], 0\n"
    "ds_read_u16 v195, v195\n"
    "v_pk_mul_f16 v232, v188, v232\n"
    "v_mfma_f32_16x16x32_bf16 v[180:183], a[180:183], v[160:163], v[180:183]\n"
    "v_exp_f16_e64 v234, v232\n"
    "v_lshlrev_b32_sdwa v200, 1, v169 dst_sel:DWORD dst_unused:UNUSED_PAD src0_sel:DWORD src1_sel:WORD_1\n"
    "s_waitcnt lgkmcnt(7)\n"
    "v_mfma_f32_16x16x32_bf16 v[176:179], a[96:99], v[164:167], v[176:179]\n"
    "s_barrier\n"
    "s_waitcnt lgkmcnt(6)\n"
    "v_mfma_f32_16x16x32_bf16 v[172:175], a[240:243], v[152:155], v[172:175]\n"
    "s_nop 0\n"
    "ds_read_b128 v[208:211], v250 offset:0\n"
    "ds_read_b128 v[212:215], v250 offset:320\n"
    "v_mfma_f32_16x16x32_bf16 v[180:183], a[240:243], v[164:167], v[180:183]\n"
    "ds_read_b128 v[216:219], v251 offset:21504\n"
    "ds_read_b128 v[220:223], v251 offset:21840\n"
    "v_exp_f16_sdwa v234, v232 dst_sel:WORD_1 dst_unused:UNUSED_PRESERVE src0_sel:WORD_1\n"
    "v_mfma_f32_16x16x32_bf16 v[96:99], a[64:67], v[184:187], v[96:99]\n"
    "ds_read_u16_d16_hi v203, v203\n"
    "ds_read_b128 a[244:247], v244 offset:18432\n"
    "v_pk_fma_f16 v234, v234, s49, s49\n"
    "v_mfma_f32_16x16x32_bf16 v[100:103], a[68:71], v[184:187], v[100:103]\n"
    "s_waitcnt lgkmcnt(10)\n"
    "ds_read_u16 v197, v197\n"
    "buffer_load_dwordx4 a[236:239], v245, s[40:43], s101 offen offset:64\n"
    "buffer_load_dwordx4 a[228:231], v245, s[40:43], s101 offen offset:0\n"
    "v_rcp_f16_e32 v232, v234\n"
    "v_mfma_f32_16x16x32_bf16 v[112:115], a[72:75], v[184:187], v[112:115]\n"
    "ds_read_u16_d16_hi v205, v205\n"
    "v_rcp_f16_sdwa v232, v234 dst_sel:WORD_1 dst_unused:UNUSED_PRESERVE src0_sel:WORD_1\n"
    "v_mfma_f32_16x16x32_bf16 v[116:119], a[76:79], v[184:187], v[116:119]\n"
    "s_waitcnt lgkmcnt(9)\n"
    "v_or_b32_e32 v193, v193, v201\n"
    "v_fma_mix_f32 v234, v188, v232, 0 op_sel_hi:[1,1,0]\n"
    "v_mfma_f32_16x16x32_bf16 v[128:131], v[236:239], v[184:187], v[128:131]\n"
    "ds_read_u16 v198, v198\n"
    "v_fma_mix_f32 v232, v188, v232, 0 op_sel:[1,1,0] op_sel_hi:[1,1,0]\n"
    "v_or_b32_e64 v194, v194, v202\n"
    "v_cvt_pk_bf16_f32 v188, v234, v232\n"
    "v_pk_fma_f16 v232, v192, v192, s48\n"
    "v_lshlrev_b32_e64 v254, 3, v244\n"
    "v_mfma_f32_16x16x32_bf16 v[104:107], a[64:67], v[188:191], v[104:107]\n"
    "ds_read_u16_d16_hi v206, v206\n"
    "v_pk_mul_f16 v232, v192, v232\n"
    "v_mfma_f32_16x16x32_bf16 v[108:111], a[68:71], v[188:191], v[108:111]\n"
    "s_waitcnt lgkmcnt(7)\n"
    "ds_read_u16 v199, v199\n"
    "v_exp_f16_e64 v234, v232\n"
    "v_smfmac_f32_16x16x64_bf16 v[64:67], v[216:219], v[208:215], v240\n"
    "s_waitcnt lgkmcnt(7)\n"
    "ds_read_b128 v[216:219], v252 offset:21504\n"
    "v_smfmac_f32_16x16x64_bf16 v[68:71], v[220:223], v[208:215], v240 abid:1\n"
    "s_nop 0\n"
    "ds_read_b128 v[208:211], v250 offset:640\n"
    "v_mfma_f32_16x16x32_bf16 v[120:123], a[72:75], v[188:191], v[120:123]\n"
    "s_mov_b32 exec_hi, 0\n"
    "buffer_load_dword v233, v254, s[72:75], s53 offen sc1\n"
    "s_mov_b32 exec_hi, -1\n"
    "s_waitcnt lgkmcnt(7)\n"
    "v_mfma_f32_16x16x32_bf16 v[132:135], a[244:247], v[184:187], v[132:135]\n"
    "s_add_u32 s53, s53, 393216\n"
    "ds_read_b128 v[212:215], v250 offset:960\n"
    "ds_read_b128 v[220:223], v252 offset:21840\n"
    "v_mfma_f32_16x16x32_bf16 v[124:127], a[76:79], v[188:191], v[124:127]\n"
    "ds_read_u16_d16_hi v207, v207\n"
    "v_or_b32_e64 v195, v195, v203\n"
    "v_mfma_f32_16x16x32_bf16 v[136:139], v[236:239], v[188:191], v[136:139]\n"
    "v_exp_f16_sdwa v234, v232 dst_sel:WORD_1 dst_unused:UNUSED_PRESERVE src0_sel:WORD_1\n"
    "v_mfma_f32_16x16x32_bf16 v[140:143], a[244:247], v[188:191], v[140:143]\n"
    "ds_read_b128 a[240:243], v244 offset:36864\n"
    "v_pk_fma_f16 v234, v234, s49, s49\n"
    "v_lshlrev_b32_sdwa v184, 1, v168 dst_sel:DWORD dst_unused:UNUSED_PAD src0_sel:DWORD src1_sel:WORD_1\n"
    "v_lshlrev_b32_sdwa v185, 1, v170 dst_sel:DWORD dst_unused:UNUSED_PAD src0_sel:DWORD src1_sel:WORD_1\n"
    "s_waitcnt lgkmcnt(9)\n"
    "v_or_b32_e32 v197, v197, v205\n"
    "v_lshlrev_b32_sdwa v201, 1, v171 dst_sel:DWORD dst_unused:UNUSED_PAD src0_sel:DWORD src1_sel:WORD_1\n"
    "v_mfma_f32_16x16x32_bf16 v[168:171], a[136:139], v[144:147], 0\n"
    "ds_read_u16 v184, v184\n"
    "ds_read_u16_d16_hi v200, v200\n"
    "v_mfma_f32_16x16x32_bf16 v[168:171], a[184:187], v[148:151], v[168:171]\n"
    "v_lshlrev_b32_sdwa v186, 1, v172 dst_sel:DWORD dst_unused:UNUSED_PAD src0_sel:DWORD src1_sel:WORD_1\n"
    "v_lshlrev_b32_sdwa v202, 1, v173 dst_sel:DWORD dst_unused:UNUSED_PAD src0_sel:DWORD src1_sel:WORD_1\n"
    "v_lshlrev_b32_sdwa v187, 1, v174 dst_sel:DWORD dst_unused:UNUSED_PAD src0_sel:DWORD src1_sel:WORD_1\n"
    "v_lshlrev_b32_sdwa v203, 1, v175 dst_sel:DWORD dst_unused:UNUSED_PAD src0_sel:DWORD src1_sel:WORD_1\n"
    "v_rcp_f16_e32 v232, v234\n"
    "v_mfma_f32_16x16x32_bf16 v[168:171], a[100:103], v[152:155], v[168:171]\n"
    "s_waitcnt lgkmcnt(9)\n"
    "ds_read_u16 v185, v185\n"
    "v_or_b32_e64 v198, v198, v206\n"
    "v_rcp_f16_sdwa v232, v234 dst_sel:WORD_1 dst_unused:UNUSED_PRESERVE src0_sel:WORD_1\n"
    "v_mfma_f32_16x16x32_bf16 v[172:175], a[140:143], v[144:147], 0\n"
    "ds_read_u16_d16_hi v201, v201\n"
    "v_lshlrev_b32_sdwa v188, 1, v176 dst_sel:DWORD dst_unused:UNUSED_PAD src0_sel:DWORD src1_sel:WORD_1\n"
    "v_mfma_f32_16x16x32_bf16 v[172:175], a[188:191], v[148:151], v[172:175]\n"
    "ds_read_u16 v186, v186\n"
    "v_lshlrev_b32_sdwa v204, 1, v177 dst_sel:DWORD dst_unused:UNUSED_PAD src0_sel:DWORD src1_sel:WORD_1\n"
    "s_waitcnt lgkmcnt(8)\n"
    "v_smfmac_f32_16x16x64_bf16 v[64:67], v[216:219], v[208:215], v240\n"
    "s_waitcnt lgkmcnt(7)\n"
    "v_lshlrev_b32_sdwa v189, 1, v178 dst_sel:DWORD dst_unused:UNUSED_PAD src0_sel:DWORD src1_sel:WORD_1\n"
    "v_smfmac_f32_16x16x64_bf16 v[68:71], v[220:223], v[208:215], v240 abid:1\n"
    "ds_read_b128 v[208:211], v250 offset:80\n"
    "ds_read_b128 v[212:215], v250 offset:400\n"
    "v_lshlrev_b32_sdwa v205, 1, v179 dst_sel:DWORD dst_unused:UNUSED_PAD src0_sel:DWORD src1_sel:WORD_1\n"
    "v_lshlrev_b32_sdwa v190, 1, v180 dst_sel:DWORD dst_unused:UNUSED_PAD src0_sel:DWORD src1_sel:WORD_1\n"
    "v_lshlrev_b32_sdwa v206, 1, v181 dst_sel:DWORD dst_unused:UNUSED_PAD src0_sel:DWORD src1_sel:WORD_1\n"
    "v_pk_add_f32 v[224:225], v[224:225], v[64:65]\n"
    "v_pk_add_f32 v[226:227], v[226:227], v[66:67]\n"
    "v_pk_fma_f32 v[228:229], v[64:65], v[64:65], v[228:229]\n"
    "v_pk_fma_f32 v[230:231], v[66:67], v[66:67], v[230:231]\n"
    "v_pk_add_f32 v[224:225], v[224:225], v[68:69]\n"
    "v_pk_add_f32 v[226:227], v[226:227], v[70:71]\n"
    "v_pk_fma_f32 v[228:229], v[68:69], v[68:69], v[228:229]\n"
    "v_pk_fma_f32 v[230:231], v[70:71], v[70:71], v[230:231]\n"
    "v_lshlrev_b32_sdwa v191, 1, v182 dst_sel:DWORD dst_unused:UNUSED_PAD src0_sel:DWORD src1_sel:WORD_1\n"
    "v_fma_mix_f32 v234, v192, v232, 0 op_sel_hi:[1,1,0]\n"
    "s_waitcnt lgkmcnt(7)\n"
    "v_mfma_f32_16x16x32_bf16 v[172:175], a[240:243], v[152:155], v[172:175]\n"
    "s_nop 0\n"
    "ds_read_b128 v[216:219], v251 offset:22176\n"
    "ds_read_b128 v[220:223], v251 offset:22512\n"
    "v_or_b32_e32 v199, v199, v207\n"
    "v_lshlrev_b32_sdwa v207, 1, v183 dst_sel:DWORD dst_unused:UNUSED_PAD src0_sel:DWORD src1_sel:WORD_1\n"
    "v_fma_mix_f32 v232, v192, v232, 0 op_sel:[1,1,0] op_sel_hi:[1,1,0]\n"
    "v_mfma_f32_16x16x32_bf16 v[176:179], a[136:139], v[156:159], 0\n"
    "s_waitcnt lgkmcnt(7)\n"
    "ds_read_b128 v[236:239], v244 offset:20480\n"
    "ds_read_u16_d16_hi v202, v202\n"
    "v_cvt_pk_bf16_f32 v192, v234, v232\n"
    "v_mfma_f32_16x16x32_bf16 v[176:179], a[184:187], v[160:163], v[176:179]\n"
    "ds_read_u16 v187, v187\n"
    "ds_read_b128 a[244:247], v244 offset:22528\n"
    "v_mfma_f32_16x16x32_bf16 v[176:179], a[100:103], v[164:167], v[176:179]\n"
    "v_pk_fma_f16 v232, v196, v196, s48\n"
    "v_or_b32_e64 v184, v184, v200\n"
    "v_mfma_f32_16x16x32_bf16 v[180:183], a[140:143], v[156:159], 0\n"
    "ds_read_u16_d16_hi v203, v203\n"
    "v_pk_mul_f16 v232, v196, v232\n"
    "v_mfma_f32_16x16x32_bf16 v[180:183], a[188:191], v[160:163], v[180:183]\n"
    "s_waitcnt lgkmcnt(10)\n"
    "ds_read_u16 v188, v188\n"
    "v_exp_f16_e32 v234, v232\n"
    "v_mfma_f32_16x16x32_bf16 v[180:183], a[240:243], v[164:167], v[180:183]\n"
    "v_exp_f16_sdwa v234, v232 dst_sel:WORD_1 dst_unused:UNUSED_PRESERVE src0_sel:WORD_1\n"
    "v_mfma_f32_16x16x32_bf16 v[96:99], a[80:83], v[192:195], v[96:99]\n"
    "ds_read_u16_d16_hi v204, v204\n"
    "v_pk_fma_f16 v234, v234, s49, s49\n"
    "s_waitcnt lgkmcnt(8)\n"
    "v_smfmac_f32_16x16x64_bf16 v[72:75], v[216:219], v[208:215], v240\n"
    "s_waitcnt lgkmcnt(7)\n"
    "ds_read_b128 v[216:219], v252 offset:22176\n"
    "v_smfmac_f32_16x16x64_bf16 v[76:79], v[220:223], v[208:215], v240 abid:1\n"
    "ds_read_b128 v[208:211], v250 offset:720\n"
    "v_mfma_f32_16x16x32_bf16 v[100:103], a[84:87], v[192:195], v[100:103]\n"
    "ds_read_b128 v[212:215], v250 offset:1040\n"
    "v_rcp_f16_e32 v232, v234\n"
    "v_mfma_f32_16x16x32_bf16 v[112:115], a[88:91], v[192:195], v[112:115]\n"
    "ds_read_b128 v[220:223], v252 offset:22512\n"
    "v_rcp_f16_sdwa v232, v234 dst_sel:WORD_1 dst_unused:UNUSED_PRESERVE src0_sel:WORD_1\n"
    "v_mfma_f32_16x16x32_bf16 v[116:119], a[92:95], v[192:195], v[116:119]\n"
    "s_waitcnt lgkmcnt(9)\n"
    "ds_read_u16 v189, v189\n"
    "v_fma_mix_f32 v234, v196, v232, 0 op_sel_hi:[1,1,0]\n"
    "v_mfma_f32_16x16x32_bf16 v[128:131], v[236:239], v[192:195], v[128:131]\n"
    "v_fma_mix_f32 v232, v196, v232, 0 op_sel:[1,1,0] op_sel_hi:[1,1,0]\n"
    "v_or_b32_e32 v185, v185, v201\n"
    "s_waitcnt lgkmcnt(8)\n"
    "v_mfma_f32_16x16x32_bf16 v[132:135], a[244:247], v[192:195], v[132:135]\n"
    "ds_read_u16_d16_hi v205, v205\n"
    "v_cvt_pk_bf16_f32 v196, v234, v232\n"
    "v_cvt_pk_f16_f32 v192, v168, v169\n"
    "v_lshlrev_b32_sdwa v193, 1, v170 dst_sel:DWORD dst_unused:UNUSED_PAD src0_sel:DWORD src1_sel:WORD_1\n"
    "v_mfma_f32_16x16x32_bf16 v[104:107], a[80:83], v[196:199], v[104:107]\n"
    "ds_read_u16 v190, v190\n"
    "v_lshlrev_b32_sdwa v201, 1, v171 dst_sel:DWORD dst_unused:UNUSED_PAD src0_sel:DWORD src1_sel:WORD_1\n"
    "v_mfma_f32_16x16x32_bf16 v[108:111], a[84:87], v[196:199], v[108:111]\n"
    "s_waitcnt lgkmcnt(9)\n"
    "ds_read_u16_d16_hi v206, v206\n"
    "v_lshlrev_b32_sdwa v194, 1, v172 dst_sel:DWORD dst_unused:UNUSED_PAD src0_sel:DWORD src1_sel:WORD_1\n"
    "v_mfma_f32_16x16x32_bf16 v[120:123], a[88:91], v[196:199], v[120:123]\n"
    "v_or_b32_e32 v186, v186, v202\n"
    "v_lshlrev_b32_sdwa v202, 1, v173 dst_sel:DWORD dst_unused:UNUSED_PAD src0_sel:DWORD src1_sel:WORD_1\n"
    "v_mfma_f32_16x16x32_bf16 v[124:127], a[92:95], v[196:199], v[124:127]\n"
    "ds_read_u16 v191, v191\n"
    "v_lshlrev_b32_sdwa v195, 1, v174 dst_sel:DWORD dst_unused:UNUSED_PAD src0_sel:DWORD src1_sel:WORD_1\n"
    "v_mfma_f32_16x16x32_bf16 v[136:139], v[236:239], v[196:199], v[136:139]\n"
    "s_waitcnt lgkmcnt(8)\n"
    "v_or_b32_e32 v187, v187, v203\n"
    "v_lshlrev_b32_sdwa v203, 1, v175 dst_sel:DWORD dst_unused:UNUSED_PAD src0_sel:DWORD src1_sel:WORD_1\n"
    "v_mfma_f32_16x16x32_bf16 v[140:143], a[244:247], v[196:199], v[140:143]\n"
    "ds_read_u16_d16_hi v207, v207\n"
    "v_cvt_pk_f16_f32 v196, v176, v177\n"
    "v_mfma_f32_16x16x32_bf16 v[168:171], a[144:147], v[144:147], 0\n"
    "s_waitcnt lgkmcnt(7)\n"
    "v_lshlrev_b32_sdwa v197, 1, v178 dst_sel:DWORD dst_unused:UNUSED_PAD src0_sel:DWORD src1_sel:WORD_1\n"
    "v_lshlrev_b32_sdwa v198, 1, v180 dst_sel:DWORD dst_unused:UNUSED_PAD src0_sel:DWORD src1_sel:WORD_1\n"
    "v_smfmac_f32_16x16x64_bf16 v[72:75], v[216:219], v[208:215], v240\n"
    "s_waitcnt lgkmcnt(6)\n"
    "ds_read_b128 v[216:219], v251 offset:22848\n"
    "ds_read_u16 v193, v193\n"
    "v_smfmac_f32_16x16x64_bf16 v[76:79], v[220:223], v[208:215], v240 abid:1\n"
    "v_lshlrev_b32_sdwa v199, 1, v182 dst_sel:DWORD dst_unused:UNUSED_PAD src0_sel:DWORD src1_sel:WORD_1\n"
    "v_mfma_f32_16x16x32_bf16 v[168:171], a[192:195], v[148:151], v[168:171]\n"
    "ds_read_b128 v[208:211], v250 offset:160\n"
    "v_or_b32_e64 v188, v188, v204\n"
    "v_lshlrev_b32_e64 v254, 3, v244\n"
    "ds_read_b128 v[212:215], v250 offset:480\n"
    "v_pk_add_f32 v[224:225], v[224:225], v[72:73]\n"
    "v_pk_add_f32 v[226:227], v[226:227], v[74:75]\n"
    "v_pk_fma_f32 v[228:229], v[72:73], v[72:73], v[228:229]\n"
    "v_pk_fma_f32 v[230:231], v[74:75], v[74:75], v[230:231]\n"
    "v_pk_add_f32 v[224:225], v[224:225], v[76:77]\n"
    "v_pk_add_f32 v[226:227], v[226:227], v[78:79]\n"
    "v_pk_fma_f32 v[228:229], v[76:77], v[76:77], v[228:229]\n"
    "v_pk_fma_f32 v[230:231], v[78:79], v[78:79], v[230:231]\n"
    "v_mfma_f32_16x16x32_bf16 v[168:171], a[104:107], v[152:155], v[168:171]\n"
    "s_waitcnt lgkmcnt(7)\n"
    "ds_read_b128 v[220:223], v251 offset:23184\n"
    "s_mov_b32 exec_hi, 0\n"
    "buffer_load_dword v233, v254, s[72:75], s53 offen sc1\n"
    "s_mov_b32 exec_hi, -1\n"
    "v_mfma_f32_16x16x32_bf16 v[172:175], a[148:151], v[144:147], 0\n"
    "s_add_u32 s53, s53, 393216\n"
    "ds_read_u16_d16_hi v201, v201\n"
    "ds_read_u16 v194, v194\n"
    "v_mfma_f32_16x16x32_bf16 v[172:175], a[196:199], v[148:151], v[172:175]\n"
    "s_waitcnt lgkmcnt(9)\n"
    "ds_read_u16_d16_hi v202, v202\n"
    "v_or_b32_e64 v189, v189, v205\n"
    "v_mfma_f32_16x16x32_bf16 v[172:175], a[108:111], v[152:155], v[172:175]\n"
    "v_lshlrev_b32_sdwa v205, 1, v179 dst_sel:DWORD dst_unused:UNUSED_PAD src0_sel:DWORD src1_sel:WORD_1\n"
    "v_cvt_pk_bf16_f32 v96, v96, v97\n"
    "v_mfma_f32_16x16x32_bf16 v[176:179], a[144:147], v[156:159], 0\n"
    "s_nop 0\n"
    "ds_read_b128 v[236:239], v244 offset:0\n"
    "v_cvt_pk_bf16_f32 v97, v98, v99\n"
    "v_mfma_f32_16x16x32_bf16 v[176:179], a[192:195], v[160:163], v[176:179]\n"
    "s_waitcnt lgkmcnt(8)\n"
    "ds_read_u16 v195, v195\n"
    "v_cvt_pk_bf16_f32 v98, v100, v101\n"
    "v_mfma_f32_16x16x32_bf16 v[176:179], a[104:107], v[164:167], v[176:179]\n"
    "v_cvt_pk_bf16_f32 v99, v102, v103\n"
    "ds_write_b128 v242, v[96:99] offset:24640\n"
    "v_or_b32_e64 v190, v190, v206\n"
    "s_waitcnt lgkmcnt(7)\n"
    "v_smfmac_f32_16x16x64_bf16 v[80:83], v[216:219], v[208:215], v240\n"
    "s_waitcnt lgkmcnt(6)\n"
    "v_lshlrev_b32_sdwa v206, 1, v181 dst_sel:DWORD dst_unused:UNUSED_PAD src0_sel:DWORD src1_sel:WORD_1\n"
    "v_smfmac_f32_16x16x64_bf16 v[84:87], v[220:223], v[208:215], v240 abid:1\n"
    "ds_read_b128 v[208:211], v250 offset:800\n"
    "ds_read_b128 v[212:215], v250 offset:1120\n"
    "v_mfma_f32_16x16x32_bf16 v[96:99], a[0:3], v[184:187], 0\n"
    "s_nop 0\n"
    "ds_read_b128 v[216:219], v252 offset:22848\n"
    "ds_read_b128 v[220:223], v252 offset:23184\n"
    "v_mfma_f32_16x16x32_bf16 v[100:103], a[4:7], v[184:187], 0\n"
    "s_waitcnt lgkmcnt(9)\n"
    "ds_read_u16_d16_hi v203, v203\n"
    "v_cvt_pk_bf16_f32 v112, v112, v113\n"
    "v_cvt_pk_bf16_f32 v113, v114, v115\n"
    "v_cvt_pk_bf16_f32 v114, v116, v117\n"
    "v_cvt_pk_bf16_f32 v115, v118, v119\n"
    "v_cvt_pk_bf16_f32 v128, v128, v129\n"
    "v_cvt_pk_bf16_f32 v129, v130, v131\n"
    "ds_write_b128 v242, v[112:115] offset:26688\n"
    "v_mfma_f32_16x16x32_bf16 v[112:115], a[8:11], v[184:187], 0\n"
    "s_waitcnt lgkmcnt(8)\n"
    "v_or_b32_e32 v191, v191, v207\n"
    "v_lshlrev_b32_sdwa v207, 1, v183 dst_sel:DWORD dst_unused:UNUSED_PAD src0_sel:DWORD src1_sel:WORD_1\n"
    "v_mfma_f32_16x16x32_bf16 v[180:183], a[148:151], v[156:159], 0\n"
    "ds_read_b128 a[244:247], v244 offset:2048\n"
    "ds_read_u16 v197, v197\n"
    "v_mfma_f32_16x16x32_bf16 v[180:183], a[196:199], v[160:163], v[180:183]\n"
    "v_cvt_pk_bf16_f32 v130, v132, v133\n"
    "v_or_b32_e64 v193, v193, v201\n"
    "v_mfma_f32_16x16x32_bf16 v[180:183], a[108:111], v[164:167], v[180:183]\n"
    "ds_read_u16_d16_hi v205, v205\n"
    "v_cvt_pk_bf16_f32 v131, v134, v135\n"
    "v_mfma_f32_16x16x32_bf16 v[116:119], a[12:15], v[184:187], 0\n"
    "v_cvt_pk_bf16_f32 v104, v104, v105\n"
    "ds_write_b128 v242, v[128:131] offset:28736\n"
    "s_waitcnt lgkmcnt(7)\n"
    "v_smfmac_f32_16x16x64_bf16 v[80:83], v[216:219], v[208:215], v240\n"
    "s_waitcnt lgkmcnt(6)\n"
    "v_cvt_pk_bf16_f32 v105, v106, v107\n"
    "v_smfmac_f32_16x16x64_bf16 v[84:87], v[220:223], v[208:215], v240 abid:1\n"
    "ds_read_b128 v[208:211], v250 offset:240\n"
    "ds_read_b128 v[212:215], v250 offset:560\n"
    "v_mfma_f32_16x16x32_bf16 v[128:131], v[236:239], v[184:187], 0\n"
    "ds_read_b128 v[216:219], v251 offset:23520\n"
    "ds_read_b128 v[220:223], v251 offset:23856\n"
    "v_cvt_pk_bf16_f32 v106, v108, v109\n"
    "v_cvt_pk_bf16_f32 v107, v110, v111\n"
    "v_or_b32_e64 v194, v194, v202\n"
    "v_pk_add_f32 v[224:225], v[224:225], v[80:81]\n"
    "v_pk_add_f32 v[226:227], v[226:227], v[82:83]\n"
    "v_pk_fma_f32 v[228:229], v[80:81], v[80:81], v[228:229]\n"
    "v_pk_fma_f32 v[230:231], v[82:83], v[82:83], v[230:231]\n"
    "v_pk_add_f32 v[224:225], v[224:225], v[84:85]\n"
    "v_pk_add_f32 v[226:227], v[226:227], v[86:87]\n"
    "v_pk_fma_f32 v[228:229], v[84:85], v[84:85], v[228:229]\n"
    "v_pk_fma_f32 v[230:231], v[86:87], v[86:87], v[230:231]\n"
    "s_waitcnt lgkmcnt(7)\n"
    "v_mfma_f32_16x16x32_bf16 v[132:135], a[244:247], v[184:187], 0\n"
    "s_nop 0\n"
    "ds_read_u16 v198, v198\n"
    "ds_write_b128 v242, v[104:107] offset:25664\n"
    "v_mfma_f32_16x16x32_bf16 v[104:107], a[0:3], v[188:191], 0\n"
    "v_cvt_pk_bf16_f32 v120, v120, v121\n"
    "v_cvt_pk_bf16_f32 v121, v122, v123\n"
    "v_mfma_f32_16x16x32_bf16 v[108:111], a[4:7], v[188:191], 0\n"
    "v_cvt_pk_bf16_f32 v122, v124, v125\n"
    "v_cvt_pk_bf16_f32 v123, v126, v127\n"
    "v_add_u32_e32 v254, s54, v242\n"
    "ds_write_b128 v242, v[120:123] offset:27712\n"
    "v_mfma_f32_16x16x32_bf16 v[120:123], a[8:11], v[188:191], 0\n"
    "s_waitcnt lgkmcnt(6)\n"
    "v_cvt_pk_bf16_f32 v136, v136, v137\n"
    "v_pk_fma_f16 v232, v192, v192, s48\n"
    "v_mfma_f32_16x16x32_bf16 v[124:127], a[12:15], v[188:191], 0\n"
    "ds_read_u16_d16_hi v206, v206\n"
    "ds_read_u16 v199, v199\n"
    "s_waitcnt lgkmcnt(6)\n"
    "v_smfmac_f32_16x16x64_bf16 v[88:91], v[216:219], v[208:215], v240\n"
    "s_waitcnt lgkmcnt(5)\n"
    "ds_read_b128 v[216:219], v252 offset:23520\n"
    "ds_read_u16_d16_hi v207, v207\n"
    "v_smfmac_f32_16x16x64_bf16 v[92:95], v[220:223], v[208:215], v240 abid:1\n"
    "ds_read_b128 v[208:211], v250 offset:880\n"
    "v_cvt_pk_bf16_f32 v137, v138, v139\n"
    "ds_read_b128 v[212:215], v250 offset:1200\n"
    "v_cvt_pk_bf16_f32 v138, v140, v141\n"
    "ds_read_b128 v[220:223], v252 offset:23856\n"
    "v_or_b32_e64 v195, v195, v203\n"
    "v_cvt_pk_bf16_f32 v139, v142, v143\n"
    "ds_write_b128 v242, v[136:139] offset:29760\n"
    "v_pk_mul_f16 v232, v192, v232\n"
    "v_mfma_f32_16x16x32_bf16 v[136:139], v[236:239], v[188:191], 0\n"
    "v_exp_f16_e64 v234, v232\n"
    "v_mfma_f32_16x16x32_bf16 v[140:143], a[244:247], v[188:191], 0\n"
    "v_exp_f16_sdwa v234, v232 dst_sel:WORD_1 dst_unused:UNUSED_PRESERVE src0_sel:WORD_1\n"
    "v_or_b32_e64 v197, v197, v205\n"
    "v_pk_fma_f16 v234, v234, s49, s49\n"
    "v_cvt_pk_f16_f32 v184, v168, v169\n"
    "v_lshlrev_b32_sdwa v185, 1, v170 dst_sel:DWORD dst_unused:UNUSED_PAD src0_sel:DWORD src1_sel:WORD_1\n"
    "v_lshlrev_b32_sdwa v201, 1, v171 dst_sel:DWORD dst_unused:UNUSED_PAD src0_sel:DWORD src1_sel:WORD_1\n"
    "v_mfma_f32_16x16x32_bf16 v[168:171], a[152:155], v[144:147], 0\n"
    "s_waitcnt lgkmcnt(7)\n"
    "ds_read_u16 v185, v185\n"
    "ds_read_u16_d16_hi v201, v201\n"
    "v_mfma_f32_16x16x32_bf16 v[168:171], a[200:203], v[148:151], v[168:171]\n"
    "s_nop 0\n"
    "ds_read_b128 v[236:239], v244 offset:4096\n"
    "v_lshlrev_b32_sdwa v186, 1, v172 dst_sel:DWORD dst_unused:UNUSED_PAD src0_sel:DWORD src1_sel:WORD_1\n"
    "v_lshlrev_b32_sdwa v202, 1, v173 dst_sel:DWORD dst_unused:UNUSED_PAD src0_sel:DWORD src1_sel:WORD_1\n"
    "v_lshlrev_b32_sdwa v187, 1, v174 dst_sel:DWORD dst_unused:UNUSED_PAD src0_sel:DWORD src1_sel:WORD_1\n"
    "v_lshlrev_b32_sdwa v203, 1, v175 dst_sel:DWORD dst_unused:UNUSED_PAD src0_sel:DWORD src1_sel:WORD_1\n"
    "v_rcp_f16_e32 v232, v234\n"
    "v_mfma_f32_16x16x32_bf16 v[168:171], a[112:115], v[152:155], v[168:171]\n"
    "s_waitcnt lgkmcnt(6)\n"
    "ds_read_u16 v186, v186\n"
    "ds_read_u16_d16_hi v202, v202\n"
    "v_or_b32_e64 v198, v198, v206\n"
    "s_waitcnt lgkmcnt(7)\n"
    "v_smfmac_f32_16x16x64_bf16 v[88:91], v[216:219], v[208:215], v240\n"
    "s_waitcnt lgkmcnt(6)\n"
    "ds_read_u16 v187, v187\n"
    "v_smfmac_f32_16x16x64_bf16 v[92:95], v[220:223], v[208:215], v240 abid:1\n"
    "ds_read_u16_d16_hi v203, v203\n"
    "v_rcp_f16_sdwa v232, v234 dst_sel:WORD_1 dst_unused:UNUSED_PRESERVE src0_sel:WORD_1\n"
    "v_mfma_f32_16x16x32_bf16 v[172:175], a[156:159], v[144:147], 0\n"
    "v_cvt_pk_f16_f32 v188, v176, v177\n"
    "v_lshlrev_b32_sdwa v189, 1, v178 dst_sel:DWORD dst_unused:UNUSED_PAD src0_sel:DWORD src1_sel:WORD_1\n"
    "v_lshlrev_b32_sdwa v205, 1, v179 dst_sel:DWORD dst_unused:UNUSED_PAD src0_sel:DWORD src1_sel:WORD_1\n"
    "v_lshlrev_b32_sdwa v190, 1, v180 dst_sel:DWORD dst_unused:UNUSED_PAD src0_sel:DWORD src1_sel:WORD_1\n"
    "v_lshlrev_b32_sdwa v206, 1, v181 dst_sel:DWORD dst_unused:UNUSED_PAD src0_sel:DWORD src1_sel:WORD_1\n"
    "v_pk_add_f32 v[224:225], v[224:225], v[88:89]\n"
    "v_pk_add_f32 v[226:227], v[226:227], v[90:91]\n"
    "v_pk_fma_f32 v[228:229], v[88:89], v[88:89], v[228:229]\n"
    "v_pk_fma_f32 v[230:231], v[90:91], v[90:91], v[230:231]\n"
    "v_pk_add_f32 v[224:225], v[224:225], v[92:93]\n"
    "v_pk_add_f32 v[226:227], v[226:227], v[94:95]\n"
    "v_pk_fma_f32 v[228:229], v[92:93], v[92:93], v[228:229]\n"
    "v_pk_fma_f32 v[230:231], v[94:95], v[94:95], v[230:231]\n"
    "v_mfma_f32_16x16x32_bf16 v[172:175], a[204:207], v[148:151], v[172:175]\n"
    "ds_read_b128 a[244:247], v244 offset:6144\n"
    "ds_write_b128 v254, v[224:227]\n"
    "v_lshlrev_b32_sdwa v191, 1, v182 dst_sel:DWORD dst_unused:UNUSED_PAD src0_sel:DWORD src1_sel:WORD_1\n"
    "v_fma_mix_f32 v234, v192, v232, 0 op_sel_hi:[1,1,0]\n"
    "v_mfma_f32_16x16x32_bf16 v[172:175], a[116:119], v[152:155], v[172:175]\n"
    "s_waitcnt lgkmcnt(6)\n"
    "v_or_b32_e32 v199, v199, v207\n"
    "v_lshlrev_b32_sdwa v207, 1, v183 dst_sel:DWORD dst_unused:UNUSED_PAD src0_sel:DWORD src1_sel:WORD_1\n"
    "v_fma_mix_f32 v232, v192, v232, 0 op_sel:[1,1,0] op_sel_hi:[1,1,0]\n"
    "v_mfma_f32_16x16x32_bf16 v[176:179], a[152:155], v[156:159], 0\n"
    "ds_read_u16 v189, v189\n"
    "ds_write_b128 v254, v[228:231] offset:1024\n"
    "v_lshrrev_b32_e64 v254, 2, v242\n"
    "v_cvt_pk_bf16_f32 v192, v234, v232\n"
    "v_mfma_f32_16x16x32_bf16 v[176:179], a[200:203], v[160:163], v[176:179]\n"
    "s_waitcnt lgkmcnt(6)\n"
    "v_add_u32_e32 v254, s55, v254\n"
    "v_pk_fma_f16 v232, v196, v196, s48\n"
    "v_mfma_f32_16x16x32_bf16 v[176:179], a[112:115], v[164:167], v[176:179]\n"
    "v_pk_mul_f16 v232, v196, v232\n"
    "v_or_b32_e64 v185, v185, v201\n"
    "v_mfma_f32_16x16x32_bf16 v[180:183], a[156:159], v[156:159], 0\n"
    "ds_read_u16_d16_hi v205, v205\n"
    "ds_read_u16 v190, v190\n"
    "v_mfma_f32_16x16x32_bf16 v[180:183], a[204:207], v[160:163], v[180:183]\n"
    "s_waitcnt lgkmcnt(5)\n"
    "ds_read_u16_d16_hi v206, v206\n"
    "ds_read_u16 v191, v191\n"
    "v_exp_f16_e32 v234, v232\n"
    "v_mfma_f32_16x16x32_bf16 v[180:183], a[116:119], v[164:167], v[180:183]\n"
    "v_exp_f16_sdwa v234, v232 dst_sel:WORD_1 dst_unused:UNUSED_PRESERVE src0_sel:WORD_1\n"
    "v_mfma_f32_16x16x32_bf16 v[96:99], a[16:19], v[192:195], v[96:99]\n"
    "ds_read_u16_d16_hi v207, v207\n"
    "v_pk_fma_f16 v234, v234, s49, s49\n"
    "v_mfma_f32_16x16x32_bf16 v[100:103], a[20:23], v[192:195], v[100:103]\n"
    "v_rcp_f16_e64 v232, v234\n"
    "v_mfma_f32_16x16x32_bf16 v[112:115], a[24:27], v[192:195], v[112:115]\n"
    "s_waitcnt lgkmcnt(5)\n"
    "s_barrier\n"
    "v_rcp_f16_sdwa v232, v234 dst_sel:WORD_1 dst_unused:UNUSED_PRESERVE src0_sel:WORD_1\n"
    "v_mfma_f32_16x16x32_bf16 v[116:119], a[28:31], v[192:195], v[116:119]\n"
    "ds_read_b32 v224, v254 offset:0\n"
    "ds_read_b32 v225, v254 offset:6752\n"
    "v_mfma_f32_16x16x32_bf16 v[128:131], v[236:239], v[192:195], v[128:131]\n"
    "ds_read_b32 v226, v254 offset:13504\n"
    "ds_read_b32 v227, v254 offset:20256\n"
    "ds_read_b32 v228, v254 offset:1024\n"
    "v_fma_mix_f32 v234, v196, v232, 0 op_sel_hi:[1,1,0]\n"
    "v_mfma_f32_16x16x32_bf16 v[132:135], a[244:247], v[192:195], v[132:135]\n"
    "s_nop 0\n"
    "s_waitcnt lgkmcnt(9)\n"
    "ds_read_b32 v229, v254 offset:7776\n"
    "ds_read_b32 v230, v254 offset:14528\n"
    "ds_read_b32 v231, v254 offset:21280\n"
    "v_fma_mix_f32 v232, v196, v232, 0 op_sel:[1,1,0] op_sel_hi:[1,1,0]\n"
    "v_or_b32_e64 v186, v186, v202\n"
    "v_cvt_pk_bf16_f32 v196, v234, v232\n"
    "v_pk_fma_f16 v232, v184, v184, s48\n"
    "v_or_b32_e64 v187, v187, v203\n"
    "v_mfma_f32_16x16x32_bf16 v[104:107], a[16:19], v[196:199], v[104:107]\n"
    "ds_read_b128 a[240:243], v244 offset:34816\n"
    "v_pk_mul_f16 v232, v184, v232\n"
    "v_mfma_f32_16x16x32_bf16 v[108:111], a[20:23], v[196:199], v[108:111]\n"
    "s_waitcnt lgkmcnt(11)\n"
    "v_exp_f16_e32 v234, v232\n"
    "v_mfma_f32_16x16x32_bf16 v[120:123], a[24:27], v[196:199], v[120:123]\n"
    "v_exp_f16_sdwa v234, v232 dst_sel:WORD_1 dst_unused:UNUSED_PRESERVE src0_sel:WORD_1\n"
    "v_mfma_f32_16x16x32_bf16 v[124:127], a[28:31], v[196:199], v[124:127]\n"
    "v_or_b32_e64 v189, v189, v205\n"
    "v_pk_fma_f16 v234, v234, s49, s49\n"
    "v_mfma_f32_16x16x32_bf16 v[136:139], v[236:239], v[196:199], v[136:139]\n"
    "s_waitcnt lgkmcnt(6)\n"
    "ds_read_b128 v[236:239], v244 offset:8192\n"
    "v_add_f32_e32 v224, v224, v225\n"
    "v_mfma_f32_16x16x32_bf16 v[140:143], a[244:247], v[196:199], v[140:143]\n"
    "s_waitcnt lgkmcnt(5)\n"
    "v_add_f32_e32 v226, v226, v227\n"
    "v_lshlrev_b32_sdwa v192, 1, v168 dst_sel:DWORD dst_unused:UNUSED_PAD src0_sel:DWORD src1_sel:WORD_1\n"
    "s_waitcnt vmcnt(3)\n"
    "v_smfmac_f32_32x32x32_bf16 v[112:127], a[248:251], a[232:239], v241\n"
    "s_waitcnt lgkmcnt(2)\n"
    "ds_read_u16 v192, v192\n"
    "ds_read_b128 a[232:235], v243 offset:24640\n"
    "v_add_f32_e64 v228, v228, v229\n"
    "v_add_f32_e64 v230, v230, v231\n"
    "v_add_f32_e32 v224, v224, v226\n"
    "s_waitcnt vmcnt(2)\n"
    "v_smfmac_f32_32x32x32_bf16 v[96:111], a[248:251], a[224:231], v241\n"
    "buffer_load_dwordx4 a[224:227], v245, s[40:43], s58 offen offset:128\n"
    "ds_read_b128 a[236:239], v243 offset:25664\n"
    "v_add_f32_e64 v228, v228, v230\n"
    "v_mul_f32_e32 v224, 0x3c2aaaab, v224\n"
    "v_mul_f32_e32 v228, 0x3c2aaaab, v228\n"
    "v_lshlrev_b32_sdwa v200, 1, v169 dst_sel:DWORD dst_unused:UNUSED_PAD src0_sel:DWORD src1_sel:WORD_1\n"
    "v_fma_f32 v228, -v224, v224, v228\n"
    "v_lshlrev_b32_sdwa v193, 1, v170 dst_sel:DWORD dst_unused:UNUSED_PAD src0_sel:DWORD src1_sel:WORD_1\n"
    "v_max_f32_e64 v228, 0, v228\n"
    "v_lshlrev_b32_sdwa v201, 1, v171 dst_sel:DWORD dst_unused:UNUSED_PAD src0_sel:DWORD src1_sel:WORD_1\n"
    "v_mfma_f32_16x16x32_bf16 v[168:171], a[160:163], v[144:147], 0\n"
    "ds_read_u16_d16_hi v200, v200\n"
    "ds_read_u16 v193, v193\n"
    "v_mfma_f32_16x16x32_bf16 v[168:171], a[208:211], v[148:151], v[168:171]\n"
    "ds_read_u16_d16_hi v201, v201\n"
    "v_add_f32_e32 v228, 0x3727c5ac, v228\n"
    "v_lshlrev_b32_sdwa v194, 1, v172 dst_sel:DWORD dst_unused:UNUSED_PAD src0_sel:DWORD src1_sel:WORD_1\n"
    "v_rsq_f32_e64 v228, v228\n"
    "v_lshlrev_b32_sdwa v202, 1, v173 dst_sel:DWORD dst_unused:UNUSED_PAD src0_sel:DWORD src1_sel:WORD_1\n"
    "v_mul_f32_e64 v228, s50, v228\n"
    "ds_write_b32 v254, v228\n"
    "ds_read_u16 v194, v194\n"
    "v_mul_f32_e64 v224, v228, -v224\n"
    "ds_write_b32 v254, v224 offset:1024\n"
    "ds_read_u16_d16_hi v202, v202\n"
    "v_add_u32_e64 v254, s56, v242\n"
    "v_lshlrev_b32_sdwa v195, 1, v174 dst_sel:DWORD dst_unused:UNUSED_PAD src0_sel:DWORD src1_sel:WORD_1\n"
    "v_lshlrev_b32_sdwa v203, 1, v175 dst_sel:DWORD dst_unused:UNUSED_PAD src0_sel:DWORD src1_sel:WORD_1\n"
    "v_rcp_f16_e64 v232, v234\n"
    "v_mfma_f32_16x16x32_bf16 v[168:171], a[120:123], v[152:155], v[168:171]\n"
    "ds_read_u16 v195, v195\n"
    "ds_read_b128 a[244:247], v244 offset:10240\n"
    "v_or_b32_e64 v190, v190, v206\n"
    "v_rcp_f16_sdwa v232, v234 dst_sel:WORD_1 dst_unused:UNUSED_PRESERVE src0_sel:WORD_1\n"
    "v_mfma_f32_16x16x32_bf16 v[172:175], a[164:167], v[144:147], 0\n"
    "buffer_load_dwordx4 a[228:231], v245, s[40:43], s101 offen offset:128\n"
    "ds_read_u16_d16_hi v203, v203\n"
    "v_mfma_f32_16x16x32_bf16 v[172:175], a[212:215], v[148:151], v[172:175]\n"
    "v_lshlrev_b32_sdwa v196, 1, v176 dst_sel:DWORD dst_unused:UNUSED_PAD src0_sel:DWORD src1_sel:WORD_1\n"
    "v_lshlrev_b32_sdwa v204, 1, v177 dst_sel:DWORD dst_unused:UNUSED_PAD src0_sel:DWORD src1_sel:WORD_1\n"
    "v_lshlrev_b32_sdwa v197, 1, v178 dst_sel:DWORD dst_unused:UNUSED_PAD src0_sel:DWORD src1_sel:WORD_1\n"
    "v_lshlrev_b32_sdwa v205, 1, v179 dst_sel:DWORD dst_unused:UNUSED_PAD src0_sel:DWORD src1_sel:WORD_1\n"
    "v_lshlrev_b32_sdwa v198, 1, v180 dst_sel:DWORD dst_unused:UNUSED_PAD src0_sel:DWORD src1_sel:WORD_1\n"
    "v_lshlrev_b32_sdwa v206, 1, v181 dst_sel:DWORD dst_unused:UNUSED_PAD src0_sel:DWORD src1_sel:WORD_1\n"
    "v_lshlrev_b32_sdwa v199, 1, v182 dst_sel:DWORD dst_unused:UNUSED_PAD src0_sel:DWORD src1_sel:WORD_1\n"
    "v_fma_mix_f32 v234, v184, v232, 0 op_sel_hi:[1,1,0]\n"
    "s_waitcnt lgkmcnt(8)\n"
    "v_mfma_f32_16x16x32_bf16 v[172:175], a[240:243], v[152:155], v[172:175]\n"
    "s_nop 0\n"
    "ds_read_u16 v196, v196\n"
    "ds_read_u16_d16_hi v204, v204\n"
    "v_or_b32_e64 v191, v191, v207\n"
    "v_lshlrev_b32_sdwa v207, 1, v183 dst_sel:DWORD dst_unused:UNUSED_PAD src0_sel:DWORD src1_sel:WORD_1\n"
    "v_fma_mix_f32 v232, v184, v232, 0 op_sel:[1,1,0] op_sel_hi:[1,1,0]\n"
    "v_mfma_f32_16x16x32_bf16 v[176:179], a[160:163], v[156:159], 0\n"
    "s_waitcnt lgkmcnt(6)\n"
    "ds_read_u16 v197, v197\n"
    "v_cvt_pk_bf16_f32 v184, v234, v232\n"
    "s_barrier\n"
    "v_mfma_f32_16x16x32_bf16 v[176:179], a[208:211], v[160:163], v[176:179]\n"
    "ds_read_b128 v[224:227], v254\n"
    "ds_read_b128 v[228:231], v254 offset:1024\n"
    "v_smfmac_f32_32x32x32_bf16 v[96:111], a[248:251], a[232:239], v241\n"
    "ds_read_u16_d16_hi v205, v205\n"
    "v_pk_fma_f16 v232, v188, v188, s48\n"
    "v_lshlrev_b32_e64 v254, 3, v244\n"
    "v_pk_mul_f16 v232, v188, v232\n"
    "v_or_b32_e64 v192, v192, v200\n"
    "v_mfma_f32_16x16x32_bf16 v[176:179], a[120:123], v[164:167], v[176:179]\n"
    "s_mov_b32 exec_hi, 0\n"
    "buffer_load_dword v233, v254, s[72:75], s53 offen sc1\n"
    "s_mov_b32 exec_hi, -1\n"
    "v_mfma_f32_16x16x32_bf16 v[180:183], a[164:167], v[156:159], 0\n"
    "s_add_u32 s53, s53, 393216\n"
    "ds_read_u16 v198, v198\n"
    "ds_read_u16_d16_hi v206, v206\n"
    "v_mfma_f32_16x16x32_bf16 v[180:183], a[212:215], v[160:163], v[180:183]\n"
    "v_exp_f16_e32 v234, v232\n"
    "v_mfma_f32_16x16x32_bf16 v[180:183], a[240:243], v[164:167], v[180:183]\n"
    "s_waitcnt lgkmcnt(8)\n"
    "ds_read_u16 v199, v199\n"
    "v_exp_f16_sdwa v234, v232 dst_sel:WORD_1 dst_unused:UNUSED_PRESERVE src0_sel:WORD_1\n"
    "v_mfma_f32_16x16x32_bf16 v[96:99], a[32:35], v[184:187], v[96:99]\n"
    "ds_read_u16_d16_hi v207, v207\n"
    "v_pk_fma_f16 v234, v234, s49, s49\n"
    "v_mfma_f32_16x16x32_bf16 v[100:103], a[36:39], v[184:187], v[100:103]\n"
    "v_rcp_f16_e64 v232, v234\n"
    "v_mfma_f32_16x16x32_bf16 v[112:115], a[40:43], v[184:187], v[112:115]\n"
    "ds_read_b128 a[232:235], v243 offset:26688\n"
    "v_rcp_f16_sdwa v232, v234 dst_sel:WORD_1 dst_unused:UNUSED_PRESERVE src0_sel:WORD_1\n"
    "v_mfma_f32_16x16x32_bf16 v[116:119], a[44:47], v[184:187], v[116:119]\n"
    "s_waitcnt lgkmcnt(7)\n"
    "ds_read_b128 a[236:239], v243 offset:27712\n"
    "v_fma_mix_f32 v234, v188, v232, 0 op_sel_hi:[1,1,0]\n"
    "v_mfma_f32_16x16x32_bf16 v[128:131], v[236:239], v[184:187], v[128:131]\n"
    "s_waitcnt lgkmcnt(7)\n"
    "ds_read_b128 a[240:243], v244 offset:38912\n"
    "v_fma_mix_f32 v232, v188, v232, 0 op_sel:[1,1,0] op_sel_hi:[1,1,0]\n"
    "v_or_b32_e64 v193, v193, v201\n"
    "v_cvt_pk_bf16_f32 v188, v234, v232\n"
    "v_lshlrev_b32_sdwa v201, 1, v171 dst_sel:DWORD dst_unused:UNUSED_PAD src0_sel:DWORD src1_sel:WORD_1\n"
    "v_pk_fma_f32 v[64:65], v[64:65], v[224:225], v[228:229]\n"
    "v_pk_fma_f32 v[66:67], v[66:67], v[226:227], v[230:231]\n"
    "v_pk_fma_f32 v[68:69], v[68:69], v[224:225], v[228:229]\n"
    "v_pk_fma_f32 v[70:71], v[70:71], v[226:227], v[230:231]\n"
    "v_pk_fma_f32 v[72:73], v[72:73], v[224:225], v[228:229]\n"
    "v_pk_fma_f32 v[74:75], v[74:75], v[226:227], v[230:231]\n"
    "v_pk_fma_f32 v[76:77], v[76:77], v[224:225], v[228:229]\n"
    "v_pk_fma_f32 v[78:79], v[78:79], v[226:227], v[230:231]\n"
    "v_pk_fma_f32 v[80:81], v[80:81], v[224:225], v[228:229]\n"
    "v_pk_fma_f32 v[82:83], v[82:83], v[226:227], v[230:231]\n"
    "v_pk_fma_f32 v[84:85], v[84:85], v[224:225], v[228:229]\n"
    "v_pk_fma_f32 v[86:87], v[86:87], v[226:227], v[230:231]\n"
    "v_pk_fma_f32 v[88:89], v[88:89], v[224:225], v[228:229]\n"
    "v_pk_fma_f32 v[90:91], v[90:91], v[226:227], v[230:231]\n"
    "v_pk_fma_f32 v[92:93], v[92:93], v[224:225], v[228:229]\n"
    "v_pk_fma_f32 v[94:95], v[94:95], v[226:227], v[230:231]\n"
    "v_pk_fma_f32 v[0:1], v[0:1], v[224:225], v[228:229]\n"
    "v_pk_fma_f32 v[2:3], v[2:3], v[226:227], v[230:231]\n"
    "v_pk_fma_f32 v[4:5], v[4:5], v[224:225], v[228:229]\n"
    "v_pk_fma_f32 v[6:7], v[6:7], v[226:227], v[230:231]\n"
    "v_pk_fma_f32 v[8:9], v[8:9], v[224:225], v[228:229]\n"
    "v_pk_fma_f32 v[10:11], v[10:11], v[226:227], v[230:231]\n"
    "v_pk_fma_f32 v[12:13], v[12:13], v[224:225], v[228:229]\n"
    "v_pk_fma_f32 v[14:15], v[14:15], v[226:227], v[230:231]\n"
    "v_pk_fma_f32 v[16:17], v[16:17], v[224:225], v[228:229]\n"
    "v_pk_fma_f32 v[18:19], v[18:19], v[226:227], v[230:231]\n"
    "v_pk_fma_f32 v[20:21], v[20:21], v[224:225], v[228:229]\n"
    "v_pk_fma_f32 v[22:23], v[22:23], v[226:227], v[230:231]\n"
    "v_pk_fma_f32 v[24:25], v[24:25], v[224:225], v[228:229]\n"
    "v_pk_fma_f32 v[26:27], v[26:27], v[226:227], v[230:231]\n"
    "v_pk_fma_f32 v[28:29], v[28:29], v[224:225], v[228:229]\n"
    "v_pk_fma_f32 v[30:31], v[30:31], v[226:227], v[230:231]\n"
    "v_pk_fma_f32 v[32:33], v[32:33], v[224:225], v[228:229]\n"
    "v_pk_fma_f32 v[34:35], v[34:35], v[226:227], v[230:231]\n"
    "v_pk_fma_f32 v[36:37], v[36:37], v[224:225], v[228:229]\n"
    "v_pk_fma_f32 v[38:39], v[38:39], v[226:227], v[230:231]\n"
    "v_pk_fma_f32 v[40:41], v[40:41], v[224:225], v[228:229]\n"
    "v_pk_fma_f32 v[42:43], v[42:43], v[226:227], v[230:231]\n"
    "v_pk_fma_f32 v[44:45], v[44:45], v[224:225], v[228:229]\n"
    "v_pk_fma_f32 v[46:47], v[46:47], v[226:227], v[230:231]\n"
    "v_pk_fma_f32 v[48:49], v[48:49], v[224:225], v[228:229]\n"
    "v_pk_fma_f32 v[50:51], v[50:51], v[226:227], v[230:231]\n"
    "v_pk_fma_f32 v[52:53], v[52:53], v[224:225], v[228:229]\n"
    "v_pk_fma_f32 v[54:55], v[54:55], v[226:227], v[230:231]\n"
    "v_pk_fma_f32 v[56:57], v[56:57], v[224:225], v[228:229]\n"
    "v_pk_fma_f32 v[58:59], v[58:59], v[226:227], v[230:231]\n"
    "v_pk_fma_f32 v[60:61], v[60:61], v[224:225], v[228:229]\n"
    "v_pk_fma_f32 v[62:63], v[62:63], v[226:227], v[230:231]\n"
    "v_mfma_f32_16x16x32_bf16 v[132:135], a[244:247], v[184:187], v[132:135]\n"
    "s_waitcnt lgkmcnt(0)\n"
    "ds_read_u16_d16_hi v201, v201\n"
    "v_cvt_pk_bf16_f32 v208, v64, v68\n"
    "v_mfma_f32_16x16x32_bf16 v[104:107], a[32:35], v[188:191], v[104:107]\n"
    "s_nop 0\n"
    "v_cvt_pk_bf16_f32 v212, v65, v69\n"
    "v_cvt_pk_bf16_f32 v216, v66, v70\n"
    "v_mfma_f32_16x16x32_bf16 v[108:111], a[36:39], v[188:191], v[108:111]\n"
    "v_cvt_pk_bf16_f32 v220, v67, v71\n"
    "v_cvt_pk_bf16_f32 v209, v72, v76\n"
    "v_mfma_f32_16x16x32_bf16 v[120:123], a[40:43], v[188:191], v[120:123]\n"
    "v_cvt_pk_bf16_f32 v213, v73, v77\n"
    "v_cvt_pk_bf16_f32 v217, v74, v78\n"
    "v_mfma_f32_16x16x32_bf16 v[124:127], a[44:47], v[188:191], v[124:127]\n"
    "v_cvt_pk_bf16_f32 v221, v75, v79\n"
    "v_cvt_pk_bf16_f32 v210, v80, v84\n"
    "v_mfma_f32_16x16x32_bf16 v[136:139], v[236:239], v[188:191], v[136:139]\n"
    "ds_read_b128 v[236:239], v244 offset:12288\n"
    "v_cvt_pk_bf16_f32 v214, v81, v85\n"
    "v_mfma_f32_16x16x32_bf16 v[140:143], a[244:247], v[188:191], v[140:143]\n"
    "v_cvt_pk_bf16_f32 v218, v82, v86\n"
    "v_cvt_pk_bf16_f32 v222, v83, v87\n"
    "v_smfmac_f32_32x32x32_bf16 v[112:127], a[248:251], a[232:239], v241\n"
    "v_cvt_pk_bf16_f32 v211, v88, v92\n"
    "ds_write_b128 v253, v[208:211] offset:2048\n"
    "v_cvt_pk_bf16_f32 v215, v89, v93\n"
    "v_cvt_pk_bf16_f32 v219, v90, v94\n"
    "v_cvt_pk_bf16_f32 v223, v91, v95\n"
    "v_cvt_pk_bf16_f32 v80, v0, v4\n"
    "v_cvt_pk_bf16_f32 v84, v1, v5\n"
    "v_cvt_pk_bf16_f32 v88, v2, v6\n"
    "v_cvt_pk_bf16_f32 v92, v3, v7\n"
    "v_cvt_pk_bf16_f32 v81, v8, v12\n"
    "v_cvt_pk_bf16_f32 v85, v9, v13\n"
    "v_cvt_pk_bf16_f32 v89, v10, v14\n"
    "v_cvt_pk_bf16_f32 v93, v11, v15\n"
    "v_cvt_pk_bf16_f32 v82, v16, v20\n"
    "v_cvt_pk_bf16_f32 v86, v17, v21\n"
    "v_cvt_pk_bf16_f32 v90, v18, v22\n"
    "v_cvt_pk_bf16_f32 v94, v19, v23\n"
    "v_cvt_pk_bf16_f32 v83, v24, v28\n"
    "v_cvt_pk_f16_f32 v184, v168, v169\n"
    "v_cvt_pk_bf16_f32 v87, v25, v29\n"
    "v_lshlrev_b32_sdwa v185, 1, v170 dst_sel:DWORD dst_unused:UNUSED_PAD src0_sel:DWORD src1_sel:WORD_1\n"
    "v_mfma_f32_16x16x32_bf16 v[168:171], a[168:171], v[144:147], 0\n"
    "ds_read_u16 v185, v185\n"
    "ds_write_b128 v253, v[212:215] offset:5120\n"
    "v_lshlrev_b32_sdwa v186, 1, v172 dst_sel:DWORD dst_unused:UNUSED_PAD src0_sel:DWORD src1_sel:WORD_1\n"
    "v_cvt_pk_bf16_f32 v91, v26, v30\n"
    "v_or_b32_e64 v194, v194, v202\n"
    "v_mfma_f32_16x16x32_bf16 v[168:171], a[216:219], v[148:151], v[168:171]\n"
    "v_lshlrev_b32_sdwa v202, 1, v173 dst_sel:DWORD dst_unused:UNUSED_PAD src0_sel:DWORD src1_sel:WORD_1\n"
    "v_lshlrev_b32_sdwa v187, 1, v174 dst_sel:DWORD dst_unused:UNUSED_PAD src0_sel:DWORD src1_sel:WORD_1\n"
    "v_mfma_f32_16x16x32_bf16 v[168:171], a[124:127], v[152:155], v[168:171]\n"
    "ds_read_u16 v186, v186\n"
    "ds_write_b128 v253, v[80:83] offset:0\n"
    "v_cvt_pk_bf16_f32 v95, v27, v31\n"
    "v_cvt_pk_f16_f32 v188, v176, v177\n"
    "v_lshlrev_b32_sdwa v189, 1, v178 dst_sel:DWORD dst_unused:UNUSED_PAD src0_sel:DWORD src1_sel:WORD_1\n"
    "v_lshlrev_b32_sdwa v190, 1, v180 dst_sel:DWORD dst_unused:UNUSED_PAD src0_sel:DWORD src1_sel:WORD_1\n"
    "v_lshlrev_b32_sdwa v191, 1, v182 dst_sel:DWORD dst_unused:UNUSED_PAD src0_sel:DWORD src1_sel:WORD_1\n"
    "v_or_b32_e64 v195, v195, v203\n"
    "v_lshlrev_b32_sdwa v203, 1, v175 dst_sel:DWORD dst_unused:UNUSED_PAD src0_sel:DWORD src1_sel:WORD_1\n"
    "v_mfma_f32_16x16x32_bf16 v[172:175], a[172:175], v[144:147], 0\n"
    "ds_read_u16_d16_hi v202, v202\n"
    "ds_write_b128 v253, v[84:87] offset:3072\n"
    "v_mfma_f32_16x16x32_bf16 v[172:175], a[220:223], v[148:151], v[172:175]\n"
    "s_nop 0\n"
    "v_cvt_pk_bf16_f32 v64, v32, v36\n"
    "v_cvt_pk_bf16_f32 v68, v33, v37\n"
    "v_mfma_f32_16x16x32_bf16 v[172:175], a[240:243], v[152:155], v[172:175]\n"
    "s_waitcnt lgkmcnt(4)\n"
    "v_cvt_pk_bf16_f32 v72, v34, v38\n"
    "v_cvt_pk_bf16_f32 v76, v35, v39\n"
    "v_cvt_pk_bf16_f32 v65, v40, v44\n"
    "v_or_b32_e64 v196, v196, v204\n"
    "v_cvt_pk_bf16_f32 v69, v41, v45\n"
    "v_cvt_pk_bf16_f32 v73, v42, v46\n"
    "v_cvt_pk_bf16_f32 v77, v43, v47\n"
    "v_cvt_pk_bf16_f32 v66, v48, v52\n"
    "v_cvt_pk_bf16_f32 v70, v49, v53\n"
    "v_or_b32_e64 v197, v197, v205\n"
    "v_lshlrev_b32_sdwa v205, 1, v179 dst_sel:DWORD dst_unused:UNUSED_PAD src0_sel:DWORD src1_sel:WORD_1\n"
    "v_mfma_f32_16x16x32_bf16 v[176:179], a[168:171], v[156:159], 0\n"
    "ds_read_u16 v187, v187\n"
    "ds_read_u16_d16_hi v203, v203\n"
    "v_mfma_f32_16x16x32_bf16 v[176:179], a[216:219], v[160:163], v[176:179]\n"
    "ds_read_b128 a[244:247], v244 offset:14336\n"
    "ds_read_u16 v189, v189\n"
    "v_mfma_f32_16x16x32_bf16 v[176:179], a[124:127], v[164:167], v[176:179]\n"
    "v_cvt_pk_bf16_f32 v74, v50, v54\n"
    "v_cvt_pk_bf16_f32 v78, v51, v55\n"
    "v_cvt_pk_bf16_f32 v67, v56, v60\n"
    "v_cvt_pk_bf16_f32 v71, v57, v61\n"
    "v_cvt_pk_bf16_f32 v75, v58, v62\n"
    "v_or_b32_e32 v198, v198, v206\n"
    "v_mfma_f32_16x16x32_bf16 v[96:99], a[48:51], v[192:195], v[96:99]\n"
    "s_waitcnt lgkmcnt(4)\n"
    "ds_read_b128 a[232:235], v243 offset:28736\n"
    "ds_write_b128 v253, v[64:67] offset:1024\n"
    "v_mfma_f32_16x16x32_bf16 v[100:103], a[52:55], v[192:195], v[100:103]\n"
    "v_lshlrev_b32_sdwa v206, 1, v181 dst_sel:DWORD dst_unused:UNUSED_PAD src0_sel:DWORD src1_sel:WORD_1\n"
    "v_cvt_pk_bf16_f32 v79, v59, v63\n"
    "v_mfma_f32_16x16x32_bf16 v[112:115], a[56:59], v[192:195], v[112:115]\n"
    "v_or_b32_e64 v199, v199, v207\n"
    "v_lshlrev_b32_sdwa v207, 1, v183 dst_sel:DWORD dst_unused:UNUSED_PAD src0_sel:DWORD src1_sel:WORD_1\n"
    "v_mfma_f32_16x16x32_bf16 v[180:183], a[172:175], v[156:159], 0\n"
    "ds_read_u16_d16_hi v205, v205\n"
    "ds_write_b128 v253, v[68:71] offset:4096\n"
    "v_mfma_f32_16x16x32_bf16 v[180:183], a[220:223], v[160:163], v[180:183]\n"
    "v_or_b32_e64 v185, v185, v201\n"
    "v_or_b32_e64 v186, v186, v202\n"
    "v_mfma_f32_16x16x32_bf16 v[180:183], a[240:243], v[164:167], v[180:183]\n"
    "v_pk_fma_f16 v232, v184, v184, s48\n"
    "v_lshlrev_b32_e32 v254, 3, v244\n"
    "v_mfma_f32_16x16x32_bf16 v[116:119], a[60:63], v[192:195], v[116:119]\n"
    "s_waitcnt lgkmcnt(5)\n"
    "ds_read_b128 a[236:239], v243 offset:29760\n"
    "ds_read_u16 v190, v190\n"
    "v_mfma_f32_16x16x32_bf16 v[128:131], v[236:239], v[192:195], v[128:131]\n"
    "ds_read_u16_d16_hi v206, v206\n"
    "s_mov_b32 exec_hi, 0\n"
    "buffer_load_dword v233, v254, s[72:75], s53 offen sc1\n"
    "s_mov_b32 exec_hi, -1\n"
    "v_mfma_f32_16x16x32_bf16 v[132:135], a[244:247], v[192:195], v[132:135]\n"
    "s_add_u32 s53, s53, 393216\n"
    "ds_read_u16 v191, v191\n"
    "v_or_b32_e64 v187, v187, v203\n"
    "v_mfma_f32_16x16x32_bf16 v[104:107], a[48:51], v[196:199], v[104:107]\n"
    "ds_read_u16_d16_hi v207, v207\n"
    "v_pk_mul_f16 v232, v184, v232\n"
    "v_mfma_f32_16x16x32_bf16 v[108:111], a[52:55], v[196:199], v[108:111]\n"
    "v_exp_f16_e64 v234, v232\n"
    "v_mfma_f32_16x16x32_bf16 v[120:123], a[56:59], v[196:199], v[120:123]\n"
    "ds_read_b128 a[240:243], v244 offset:32768\n"
    "v_exp_f16_sdwa v234, v232 dst_sel:WORD_1 dst_unused:UNUSED_PRESERVE src0_sel:WORD_1\n"
    "v_mfma_f32_16x16x32_bf16 v[124:127], a[60:63], v[196:199], v[124:127]\n"
    "v_pk_fma_f16 v234, v234, s49, s49\n"
    "v_cvt_pk_f16_f32 v192, v168, v169\n"
    "v_mfma_f32_16x16x32_bf16 v[136:139], v[236:239], v[196:199], v[136:139]\n"
    "s_waitcnt lgkmcnt(6)\n"
    "ds_read_b128 v[236:239], v244 offset:16384\n"
    "v_or_b32_e64 v189, v189, v205\n"
    "v_mfma_f32_16x16x32_bf16 v[140:143], a[244:247], v[196:199], v[140:143]\n"
    "s_barrier\n"
    "v_lshlrev_b32_sdwa v193, 1, v170 dst_sel:DWORD dst_unused:UNUSED_PAD src0_sel:DWORD src1_sel:WORD_1\n"
    "v_lshlrev_b32_sdwa v201, 1, v171 dst_sel:DWORD dst_unused:UNUSED_PAD src0_sel:DWORD src1_sel:WORD_1\n"
    "v_lshlrev_b32_sdwa v194, 1, v172 dst_sel:DWORD dst_unused:UNUSED_PAD src0_sel:DWORD src1_sel:WORD_1\n"
    "v_lshlrev_b32_sdwa v202, 1, v173 dst_sel:DWORD dst_unused:UNUSED_PAD src0_sel:DWORD src1_sel:WORD_1\n"
    "v_lshlrev_b32_sdwa v195, 1, v174 dst_sel:DWORD dst_unused:UNUSED_PAD src0_sel:DWORD src1_sel:WORD_1\n"
    "s_waitcnt lgkmcnt(6)\n"
    "v_smfmac_f32_32x32x32_bf16 v[128:143], a[248:251], a[232:239], v241\n"
    "s_nop 0\n"
    "ds_read_b128 v[144:147], v242 offset:0\n"
    "ds_read_b128 v[148:151], v242 offset:1024\n"
    "v_lshlrev_b32_sdwa v203, 1, v175 dst_sel:DWORD dst_unused:UNUSED_PAD src0_sel:DWORD src1_sel:WORD_1\n"
    "v_rcp_f16_e32 v232, v234\n"
    "s_waitcnt lgkmcnt(6)\n"
    "v_or_b32_e64 v190, v190, v206\n"
    "v_mov_b32_e64 v64, v216\n"
    "v_rcp_f16_sdwa v232, v234 dst_sel:WORD_1 dst_unused:UNUSED_PRESERVE src0_sel:WORD_1\n"
    "v_cvt_pk_f16_f32 v196, v176, v177\n"
    "v_mov_b32_e64 v65, v217\n"
    "v_lshlrev_b32_sdwa v197, 1, v178 dst_sel:DWORD dst_unused:UNUSED_PAD src0_sel:DWORD src1_sel:WORD_1\n"
    "v_mov_b32_e64 v66, v218\n"
    "v_lshlrev_b32_sdwa v205, 1, v179 dst_sel:DWORD dst_unused:UNUSED_PAD src0_sel:DWORD src1_sel:WORD_1\n"
    "v_mov_b32_e64 v67, v219\n"
    "v_lshlrev_b32_sdwa v198, 1, v180 dst_sel:DWORD dst_unused:UNUSED_PAD src0_sel:DWORD src1_sel:WORD_1\n"
    "v_mov_b32_e64 v68, v220\n"
    "v_lshlrev_b32_sdwa v206, 1, v181 dst_sel:DWORD dst_unused:UNUSED_PAD src0_sel:DWORD src1_sel:WORD_1\n"
    "v_mov_b32_e64 v69, v221\n"
    "v_lshlrev_b32_sdwa v199, 1, v182 dst_sel:DWORD dst_unused:UNUSED_PAD src0_sel:DWORD src1_sel:WORD_1\n"
    "v_mov_b32_e64 v70, v222\n"
    "v_mov_b32_e64 v71, v223\n"
    "v_fma_mix_f32 v234, v184, v232, 0 op_sel_hi:[1,1,0]\n"
    "s_waitcnt lgkmcnt(2)\n"
    "v_or_b32_e32 v191, v191, v207\n"
    "v_lshlrev_b32_sdwa v207, 1, v183 dst_sel:DWORD dst_unused:UNUSED_PAD src0_sel:DWORD src1_sel:WORD_1\n"
    "v_fma_mix_f32 v232, v184, v232, 0 op_sel:[1,1,0] op_sel_hi:[1,1,0]\n"
    "v_cvt_pk_bf16_f32 v184, v234, v232\n"
    "ds_read_b128 v[152:155], v242 offset:2048\n"
    "ds_read_b128 v[156:159], v242 offset:3072\n"
    "s_waitcnt lgkmcnt(3)\n"
    "v_mfma_f32_16x16x32_bf16 v[168:171], a[128:131], v[144:147], 0\n"
    "s_waitcnt lgkmcnt(2)\n"
    "ds_read_b128 v[160:163], v242 offset:4096\n"
    "ds_read_b128 v[164:167], v242 offset:5120\n"
    "v_mfma_f32_16x16x32_bf16 v[168:171], a[176:179], v[148:151], v[168:171]\n"
    "ds_read_u16 v193, v193\n"
    "ds_read_u16_d16_hi v201, v201\n"
    "v_mfma_f32_16x16x32_bf16 v[172:175], a[132:135], v[144:147], 0\n"
    "ds_read_u16 v194, v194\n"
    "v_pk_fma_f16 v232, v188, v188, s48\n"
    "v_mfma_f32_16x16x32_bf16 v[172:175], a[180:183], v[148:151], v[172:175]\n"
    "v_pk_mul_f16 v232, v188, v232\n"
    "v_exp_f16_e64 v234, v232\n"
    "ds_read_u16_d16_hi v202, v202\n"
    "v_exp_f16_sdwa v234, v232 dst_sel:WORD_1 dst_unused:UNUSED_PRESERVE src0_sel:WORD_1\n"
    "v_mfma_f32_16x16x32_bf16 v[96:99], a[64:67], v[184:187], v[96:99]\n"
    "ds_read_u16 v195, v195\n"
    "v_pk_fma_f16 v234, v234, s49, s49\n"
    "v_mfma_f32_16x16x32_bf16 v[100:103], a[68:71], v[184:187], v[100:103]\n"
    "s_waitcnt lgkmcnt(8)\n"
    "v_rcp_f16_e64 v232, v234\n"
    "v_mfma_f32_16x16x32_bf16 v[168:171], a[96:99], v[152:155], v[168:171]\n"
    "s_waitcnt lgkmcnt(7)\n"
    "ds_read_u16_d16_hi v203, v203\n"
    "v_rcp_f16_sdwa v232, v234 dst_sel:WORD_1 dst_unused:UNUSED_PRESERVE src0_sel:WORD_1\n"
    "v_mfma_f32_16x16x32_bf16 v[172:175], a[240:243], v[152:155], v[172:175]\n"
    "s_nop 0\n"
    "ds_read_b128 a[244:247], v244 offset:18432\n"
    "v_fma_mix_f32 v234, v188, v232, 0 op_sel_hi:[1,1,0]\n"
    "v_mfma_f32_16x16x32_bf16 v[176:179], a[128:131], v[156:159], 0\n"
    "s_waitcnt lgkmcnt(7)\n"
    "ds_read_u16 v197, v197\n"
    "v_fma_mix_f32 v232, v188, v232, 0 op_sel:[1,1,0] op_sel_hi:[1,1,0]\n"
    "v_mfma_f32_16x16x32_bf16 v[176:179], a[176:179], v[160:163], v[176:179]\n"
    "v_cvt_pk_bf16_f32 v188, v234, v232\n"
    "v_pk_fma_f16 v232, v192, v192, s48\n"
    "v_mfma_f32_16x16x32_bf16 v[176:179], a[96:99], v[164:167], v[176:179]\n"
    "ds_read_u16_d16_hi v205, v205\n"
    "v_pk_mul_f16 v232, v192, v232\n"
    "v_mfma_f32_16x16x32_bf16 v[180:183], a[132:135], v[156:159], 0\n"
    "v_exp_f16_e64 v234, v232\n"
    "v_mfma_f32_16x16x32_bf16 v[180:183], a[180:183], v[160:163], v[180:183]\n"
    "ds_read_u16 v198, v198\n"
    "s_waitcnt lgkmcnt(8)\n"
    "v_or_b32_e32 v193, v193, v201\n"
    "v_mfma_f32_16x16x32_bf16 v[180:183], a[240:243], v[164:167], v[180:183]\n"
    "ds_read_u16_d16_hi v206, v206\n"
    "v_exp_f16_sdwa v234, v232 dst_sel:WORD_1 dst_unused:UNUSED_PRESERVE src0_sel:WORD_1\n"
    "v_mfma_f32_16x16x32_bf16 v[112:115], a[72:75], v[184:187], v[112:115]\n"
    "v_pk_fma_f16 v234, v234, s49, s49\n"
    "v_rcp_f16_e32 v232, v234\n"
    "v_mfma_f32_16x16x32_bf16 v[116:119], a[76:79], v[184:187], v[116:119]\n"
    "s_waitcnt lgkmcnt(7)\n"
    "ds_read_u16 v199, v199\n"
    "v_or_b32_e64 v194, v194, v202\n"
    "v_mfma_f32_16x16x32_bf16 v[128:131], v[236:239], v[184:187], v[128:131]\n"
    "ds_read_u16_d16_hi v207, v207\n"
    "v_rcp_f16_sdwa v232, v234 dst_sel:WORD_1 dst_unused:UNUSED_PRESERVE src0_sel:WORD_1\n"
    "v_mfma_f32_16x16x32_bf16 v[104:107], a[64:67], v[188:191], v[104:107]\n"
    "v_fma_mix_f32 v234, v192, v232, 0 op_sel_hi:[1,1,0]\n"
    "v_fma_mix_f32 v232, v192, v232, 0 op_sel:[1,1,0] op_sel_hi:[1,1,0]\n"
    "v_mfma_f32_16x16x32_bf16 v[108:111], a[68:71], v[188:191], v[108:111]\n"
    "s_waitcnt lgkmcnt(6)\n"
    "v_or_b32_e32 v195, v195, v203\n"
    "v_cvt_pk_bf16_f32 v192, v234, v232\n"
    "v_mfma_f32_16x16x32_bf16 v[132:135], a[244:247], v[184:187], v[132:135]\n"
    "v_pk_fma_f16 v232, v196, v196, s48\n"
    "v_pk_mul_f16 v232, v196, v232\n"
    "v_mfma_f32_16x16x32_bf16 v[120:123], a[72:75], v[188:191], v[120:123]\n"
    "v_exp_f16_e32 v234, v232\n"
    "v_mfma_f32_16x16x32_bf16 v[124:127], a[76:79], v[188:191], v[124:127]\n"
    "v_exp_f16_sdwa v234, v232 dst_sel:WORD_1 dst_unused:UNUSED_PRESERVE src0_sel:WORD_1\n"
    "v_mfma_f32_16x16x32_bf16 v[136:139], v[236:239], v[188:191], v[136:139]\n"
    "s_waitcnt lgkmcnt(4)\n"
    "v_or_b32_e32 v197, v197, v205\n"
    "s_waitcnt lgkmcnt(2)\n"
    "v_or_b32_e32 v198, v198, v206\n"
    "s_waitcnt lgkmcnt(0)\n"
    "v_or_b32_e32 v199, v199, v207\n"
    "s_mov_b32 s59, s58\n"
    "s_add_u32 s57, s57, 1572864\n"
    "s_add_u32 s58, s58, 1572864\n"
    "s_mov_b32 s70, s46\n"
    "s_add_u32 s100, s57, 98304\n"
    "s_add_u32 s101, s58, 98304\n"
    "s_add_u32 s39, s59, 98304\n"
    "s_add_u32 s64, s64, 1\n"
    "s_cmp_lt_u32 s64, 2\n"
    "s_cselect_b64 s[72:73], s[72:73], s[76:77]\n"
    "s_cselect_b32 s74, s74, s78\n"
    "s_cselect_b32 s53, s53, s79\n"
    "s_cmp_lg_u32 s64, 3\n"
    "s_cbranch_scc1 1b\n"
    ".Lreplk_last:\n"
    "v_lshlrev_b32_sdwa v184, 1, v168 dst_sel:DWORD dst_unused:UNUSED_PAD src0_sel:DWORD src1_sel:WORD_1\n"
    "v_lshlrev_b32_sdwa v200, 1, v169 dst_sel:DWORD dst_unused:UNUSED_PAD src0_sel:DWORD src1_sel:WORD_1\n"
    "v_lshlrev_b32_sdwa v185, 1, v170 dst_sel:DWORD dst_unused:UNUSED_PAD src0_sel:DWORD src1_sel:WORD_1\n"
    "v_lshlrev_b32_sdwa v201, 1, v171 dst_sel:DWORD dst_unused:UNUSED_PAD src0_sel:DWORD src1_sel:WORD_1\n"
    "v_mfma_f32_16x16x32_bf16 v[140:143], a[244:247], v[188:191], v[140:143]\n"
    "ds_read_b128 a[240:243], v244 offset:36864\n"
    "ds_read_u16 v184, v184\n"
    "v_mfma_f32_16x16x32_bf16 v[168:171], a[136:139], v[144:147], 0\n"
    "ds_read_u16_d16_hi v200, v200\n"
    "ds_read_u16 v185, v185\n"
    "v_mfma_f32_16x16x32_bf16 v[168:171], a[184:187], v[148:151], v[168:171]\n"
    "v_lshlrev_b32_sdwa v186, 1, v172 dst_sel:DWORD dst_unused:UNUSED_PAD src0_sel:DWORD src1_sel:WORD_1\n"
    "v_lshlrev_b32_sdwa v202, 1, v173 dst_sel:DWORD dst_unused:UNUSED_PAD src0_sel:DWORD src1_sel:WORD_1\n"
    "v_mfma_f32_16x16x32_bf16 v[168:171], a[100:103], v[152:155], v[168:171]\n"
    "ds_read_u16_d16_hi v201, v201\n"
    "v_lshlrev_b32_sdwa v187, 1, v174 dst_sel:DWORD dst_unused:UNUSED_PAD src0_sel:DWORD src1_sel:WORD_1\n"
    "s_waitcnt vmcnt(2)\n"
    "v_smfmac_f32_32x32x32_bf16 v[128:143], a[248:251], a[224:231], v241\n"
    "s_nop 0\n"
    "ds_read_u16 v186, v186\n"
    "v_lshlrev_b32_sdwa v203, 1, v175 dst_sel:DWORD dst_unused:UNUSED_PAD src0_sel:DWORD src1_sel:WORD_1\n"
    "v_lshlrev_b32_sdwa v188, 1, v176 dst_sel:DWORD dst_unused:UNUSED_PAD src0_sel:DWORD src1_sel:WORD_1\n"
    "v_lshlrev_b32_sdwa v204, 1, v177 dst_sel:DWORD dst_unused:UNUSED_PAD src0_sel:DWORD src1_sel:WORD_1\n"
    "v_lshlrev_b32_sdwa v189, 1, v178 dst_sel:DWORD dst_unused:UNUSED_PAD src0_sel:DWORD src1_sel:WORD_1\n"
    "v_mfma_f32_16x16x32_bf16 v[172:175], a[140:143], v[144:147], 0\n"
    "ds_read_b128 v[236:239], v244 offset:20480\n"
    "v_lshlrev_b32_sdwa v205, 1, v179 dst_sel:DWORD dst_unused:UNUSED_PAD src0_sel:DWORD src1_sel:WORD_1\n"
    "v_mfma_f32_16x16x32_bf16 v[172:175], a[188:191], v[148:151], v[172:175]\n"
    "s_nop 0\n"
    "ds_read_u16_d16_hi v202, v202\n"
    "v_lshlrev_b32_sdwa v190, 1, v180 dst_sel:DWORD dst_unused:UNUSED_PAD src0_sel:DWORD src1_sel:WORD_1\n"
    "v_mfma_f32_16x16x32_bf16 v[176:179], a[136:139], v[156:159], 0\n"
    "s_waitcnt lgkmcnt(7)\n"
    "v_lshlrev_b32_sdwa v206, 1, v181 dst_sel:DWORD dst_unused:UNUSED_PAD src0_sel:DWORD src1_sel:WORD_1\n"
    "v_lshlrev_b32_sdwa v191, 1, v182 dst_sel:DWORD dst_unused:UNUSED_PAD src0_sel:DWORD src1_sel:WORD_1\n"
    "v_mfma_f32_16x16x32_bf16 v[172:175], a[240:243], v[152:155], v[172:175]\n"
    "ds_read_u16 v187, v187\n"
    "v_lshlrev_b32_sdwa v207, 1, v183 dst_sel:DWORD dst_unused:UNUSED_PAD src0_sel:DWORD src1_sel:WORD_1\n"
    "v_mfma_f32_16x16x32_bf16 v[176:179], a[184:187], v[160:163], v[176:179]\n"
    "ds_read_b128 a[244:247], v244 offset:22528\n"
    "v_pk_fma_f16 v234, v234, s49, s49\n"
    "v_mfma_f32_16x16x32_bf16 v[176:179], a[100:103], v[164:167], v[176:179]\n"
    "ds_read_u16_d16_hi v203, v203\n"
    "v_rcp_f16_e32 v232, v234\n"
    "v_mfma_f32_16x16x32_bf16 v[180:183], a[140:143], v[156:159], 0\n"
    "v_rcp_f16_sdwa v232, v234 dst_sel:WORD_1 dst_unused:UNUSED_PRESERVE src0_sel:WORD_1\n"
    "v_mfma_f32_16x16x32_bf16 v[180:183], a[188:191], v[160:163], v[180:183]\n"
    "s_waitcnt lgkmcnt(8)\n"
    "ds_read_u16 v188, v188\n"
    "v_fma_mix_f32 v234, v196, v232, 0 op_sel_hi:[1,1,0]\n"
    "v_mfma_f32_16x16x32_bf16 v[180:183], a[240:243], v[164:167], v[180:183]\n"
    "v_fma_mix_f32 v232, v196, v232, 0 op_sel:[1,1,0] op_sel_hi:[1,1,0]\n"
    "v_or_b32_e64 v184, v184, v200\n"
    "v_mfma_f32_16x16x32_bf16 v[96:99], a[80:83], v[192:195], v[96:99]\n"
    "ds_read_u16_d16_hi v204, v204\n"
    "v_cvt_pk_bf16_f32 v196, v234, v232\n"
    "v_mfma_f32_16x16x32_bf16 v[100:103], a[84:87], v[192:195], v[100:103]\n"
    "s_waitcnt lgkmcnt(8)\n"
    "ds_read_u16 v189, v189\n"
    "v_or_b32_e32 v185, v185, v201\n"
    "v_mfma_f32_16x16x32_bf16 v[112:115], a[88:91], v[192:195], v[112:115]\n"
    "s_nop 0\n"
    "v_lshlrev_b32_sdwa v201, 1, v171 dst_sel:DWORD dst_unused:UNUSED_PAD src0_sel:DWORD src1_sel:WORD_1\n"
    "v_cvt_pk_bf16_f32 v96, v96, v97\n"
    "v_mfma_f32_16x16x32_bf16 v[116:119], a[92:95], v[192:195], v[116:119]\n"
    "s_waitcnt lgkmcnt(7)\n"
    "ds_read_u16_d16_hi v205, v205\n"
    "v_cvt_pk_bf16_f32 v97, v98, v99\n"
    "v_mfma_f32_16x16x32_bf16 v[128:131], v[236:239], v[192:195], v[128:131]\n"
    "v_cvt_pk_bf16_f32 v98, v100, v101\n"
    "v_cvt_pk_bf16_f32 v99, v102, v103\n"
    "v_mfma_f32_16x16x32_bf16 v[104:107], a[80:83], v[196:199], v[104:107]\n"
    "ds_read_u16 v190, v190\n"
    "s_waitcnt lgkmcnt(8)\n"
    "v_or_b32_e32 v186, v186, v202\n"
    "v_mfma_f32_16x16x32_bf16 v[108:111], a[84:87], v[196:199], v[108:111]\n"
    "ds_read_u16_d16_hi v206, v206\n"
    "v_lshlrev_b32_sdwa v202, 1, v173 dst_sel:DWORD dst_unused:UNUSED_PAD src0_sel:DWORD src1_sel:WORD_1\n"
    "s_waitcnt lgkmcnt(7)\n"
    "v_mfma_f32_16x16x32_bf16 v[132:135], a[244:247], v[192:195], v[132:135]\n"
    "s_nop 0\n"
    "v_cvt_pk_f16_f32 v192, v168, v169\n"
    "v_lshlrev_b32_sdwa v193, 1, v170 dst_sel:DWORD dst_unused:UNUSED_PAD src0_sel:DWORD src1_sel:WORD_1\n"
    "v_mfma_f32_16x16x32_bf16 v[120:123], a[88:91], v[196:199], v[120:123]\n"
    "ds_read_u16 v191, v191\n"
    "v_lshlrev_b32_sdwa v194, 1, v172 dst_sel:DWORD dst_unused:UNUSED_PAD src0_sel:DWORD src1_sel:WORD_1\n"
    "v_mfma_f32_16x16x32_bf16 v[124:127], a[92:95], v[196:199], v[124:127]\n"
    "v_lshlrev_b32_sdwa v195, 1, v174 dst_sel:DWORD dst_unused:UNUSED_PAD src0_sel:DWORD src1_sel:WORD_1\n"
    "s_waitcnt lgkmcnt(7)\n"
    "v_or_b32_e32 v187, v187, v203\n"
    "v_mfma_f32_16x16x32_bf16 v[136:139], v[236:239], v[196:199], v[136:139]\n"
    "ds_read_u16_d16_hi v207, v207\n"
    "v_lshlrev_b32_sdwa v203, 1, v175 dst_sel:DWORD dst_unused:UNUSED_PAD src0_sel:DWORD src1_sel:WORD_1\n"
    "v_mfma_f32_16x16x32_bf16 v[140:143], a[244:247], v[196:199], v[140:143]\n"
    "ds_read_u16 v193, v193\n"
    "v_cvt_pk_f16_f32 v196, v176, v177\n"
    "v_mfma_f32_16x16x32_bf16 v[168:171], a[144:147], v[144:147], 0\n"
    "v_lshlrev_b32_sdwa v197, 1, v178 dst_sel:DWORD dst_unused:UNUSED_PAD src0_sel:DWORD src1_sel:WORD_1\n"
    "v_lshlrev_b32_sdwa v198, 1, v180 dst_sel:DWORD dst_unused:UNUSED_PAD src0_sel:DWORD src1_sel:WORD_1\n"
    "v_mfma_f32_16x16x32_bf16 v[168:171], a[192:195], v[148:151], v[168:171]\n"
    "s_waitcnt lgkmcnt(7)\n"
    "ds_read_u16_d16_hi v201, v201\n"
    "v_lshlrev_b32_sdwa v199, 1, v182 dst_sel:DWORD dst_unused:UNUSED_PAD src0_sel:DWORD src1_sel:WORD_1\n"
    "v_mfma_f32_16x16x32_bf16 v[168:171], a[104:107], v[152:155], v[168:171]\n"
    "v_or_b32_e32 v188, v188, v204\n"
    "v_cvt_pk_bf16_f32 v112, v112, v113\n"
    "v_mfma_f32_16x16x32_bf16 v[172:175], a[148:151], v[144:147], 0\n"
    "ds_read_u16 v194, v194\n"
    "v_cvt_pk_bf16_f32 v113, v114, v115\n"
    "v_mfma_f32_16x16x32_bf16 v[172:175], a[196:199], v[148:151], v[172:175]\n"
    "s_waitcnt lgkmcnt(7)\n"
    "ds_read_u16_d16_hi v202, v202\n"
    "v_or_b32_e32 v189, v189, v205\n"
    "v_mfma_f32_16x16x32_bf16 v[172:175], a[108:111], v[152:155], v[172:175]\n"
    "v_lshlrev_b32_sdwa v205, 1, v179 dst_sel:DWORD dst_unused:UNUSED_PAD src0_sel:DWORD src1_sel:WORD_1\n"
    "v_cvt_pk_bf16_f32 v114, v116, v117\n"
    "v_mfma_f32_16x16x32_bf16 v[176:179], a[144:147], v[156:159], 0\n"
    "ds_read_b128 v[236:239], v244 offset:0\n"
    "v_cvt_pk_bf16_f32 v115, v118, v119\n"
    "v_mfma_f32_16x16x32_bf16 v[176:179], a[192:195], v[160:163], v[176:179]\n"
    "ds_read_u16 v195, v195\n"
    "v_cvt_pk_bf16_f32 v128, v128, v129\n"
    "v_mfma_f32_16x16x32_bf16 v[176:179], a[104:107], v[164:167], v[176:179]\n"
    "s_waitcnt lgkmcnt(8)\n"
    "v_or_b32_e32 v190, v190, v206\n"
    "v_lshlrev_b32_sdwa v206, 1, v181 dst_sel:DWORD dst_unused:UNUSED_PAD src0_sel:DWORD src1_sel:WORD_1\n"
    "v_mfma_f32_16x16x32_bf16 v[100:103], a[4:7], v[184:187], 0\n"
    "ds_read_u16_d16_hi v203, v203\n"
    "v_cvt_pk_bf16_f32 v129, v130, v131\n"
    "v_cvt_pk_bf16_f32 v130, v132, v133\n"
    "v_cvt_pk_bf16_f32 v131, v134, v135\n"
    "v_cvt_pk_bf16_f32 v104, v104, v105\n"
    "ds_write_b128 v243, v[96:99] offset:24640\n"
    "v_mfma_f32_16x16x32_bf16 v[96:99], a[0:3], v[184:187], 0\n"
    "s_waitcnt lgkmcnt(8)\n"
    "v_or_b32_e32 v191, v191, v207\n"
    "v_lshlrev_b32_sdwa v207, 1, v183 dst_sel:DWORD dst_unused:UNUSED_PAD src0_sel:DWORD src1_sel:WORD_1\n"
    "v_mfma_f32_16x16x32_bf16 v[180:183], a[148:151], v[156:159], 0\n"
    "ds_read_b128 a[244:247], v244 offset:2048\n"
    "ds_write_b128 v243, v[112:115] offset:26688\n"
    "v_mfma_f32_16x16x32_bf16 v[180:183], a[196:199], v[160:163], v[180:183]\n"
    "s_waitcnt lgkmcnt(8)\n"
    "v_or_b32_e32 v193, v193, v201\n"
    "v_cvt_pk_bf16_f32 v105, v106, v107\n"
    "v_mfma_f32_16x16x32_bf16 v[180:183], a[108:111], v[164:167], v[180:183]\n"
    "v_cvt_pk_bf16_f32 v106, v108, v109\n"
    "v_cvt_pk_bf16_f32 v107, v110, v111\n"
    "v_mfma_f32_16x16x32_bf16 v[112:115], a[8:11], v[184:187], 0\n"
    "ds_read_u16 v197, v197\n"
    "ds_write_b128 v243, v[128:131] offset:28736\n"
    "v_mfma_f32_16x16x32_bf16 v[116:119], a[12:15], v[184:187], 0\n"
    "s_waitcnt lgkmcnt(7)\n"
    "v_or_b32_e32 v194, v194, v202\n"
    "v_cvt_pk_bf16_f32 v120, v120, v121\n"
    "v_mfma_f32_16x16x32_bf16 v[128:131], v[236:239], v[184:187], 0\n"
    "v_cvt_pk_bf16_f32 v121, v122, v123\n"
    "v_cvt_pk_bf16_f32 v122, v124, v125\n"
    "v_mfma_f32_16x16x32_bf16 v[108:111], a[4:7], v[188:191], 0\n"
    "ds_read_u16_d16_hi v205, v205\n"
    "ds_write_b128 v243, v[104:107] offset:25664\n"
    "v_mfma_f32_16x16x32_bf16 v[104:107], a[0:3], v[188:191], 0\n"
    "s_waitcnt lgkmcnt(6)\n"
    "v_cvt_pk_bf16_f32 v123, v126, v127\n"
    "v_pk_fma_f16 v232, v192, v192, s48\n"
    "v_mfma_f32_16x16x32_bf16 v[124:127], a[12:15], v[188:191], 0\n"
    "s_waitcnt lgkmcnt(5)\n"
    "v_cvt_pk_bf16_f32 v136, v136, v137\n"
    "v_cvt_pk_bf16_f32 v137, v138, v139\n"
    "v_mfma_f32_16x16x32_bf16 v[132:135], a[244:247], v[184:187], 0\n"
    "ds_read_u16 v198, v198\n"
    "ds_write_b128 v243, v[120:123] offset:27712\n"
    "v_mfma_f32_16x16x32_bf16 v[120:123], a[8:11], v[188:191], 0\n"
    "v_cvt_pk_bf16_f32 v138, v140, v141\n"
    "v_or_b32_e64 v195, v195, v203\n"
    "v_cvt_pk_bf16_f32 v139, v142, v143\n"
    "v_pk_mul_f16 v232, v192, v232\n"
    "v_cvt_pk_f16_f32 v184, v168, v169\n"
    "v_exp_f16_e64 v234, v232\n"
    "v_mfma_f32_16x16x32_bf16 v[140:143], a[244:247], v[188:191], 0\n"
    "ds_read_u16_d16_hi v206, v206\n"
    "ds_write_b128 v243, v[136:139] offset:29760\n"
    "v_mfma_f32_16x16x32_bf16 v[136:139], v[236:239], v[188:191], 0\n"
    "v_exp_f16_sdwa v234, v232 dst_sel:WORD_1 dst_unused:UNUSED_PRESERVE src0_sel:WORD_1\n"
    "v_lshlrev_b32_sdwa v185, 1, v170 dst_sel:DWORD dst_unused:UNUSED_PAD src0_sel:DWORD src1_sel:WORD_1\n"
    "v_pk_fma_f16 v234, v234, s49, s49\n"
    "v_lshlrev_b32_sdwa v201, 1, v171 dst_sel:DWORD dst_unused:UNUSED_PAD src0_sel:DWORD src1_sel:WORD_1\n"
    "v_mfma_f32_16x16x32_bf16 v[168:171], a[152:155], v[144:147], 0\n"
    "s_waitcnt lgkmcnt(4)\n"
    "ds_read_u16 v199, v199\n"
    "ds_read_u16_d16_hi v207, v207\n"
    "v_mfma_f32_16x16x32_bf16 v[168:171], a[200:203], v[148:151], v[168:171]\n"
    "s_nop 0\n"
    "ds_read_b128 a[252:255], v249 offset:0\n"
    "ds_read_u16 v185, v185\n"
    "v_or_b32_e64 v197, v197, v205\n"
    "v_lshlrev_b32_sdwa v186, 1, v172 dst_sel:DWORD dst_unused:UNUSED_PAD src0_sel:DWORD src1_sel:WORD_1\n"
    "v_lshlrev_b32_sdwa v202, 1, v173 dst_sel:DWORD dst_unused:UNUSED_PAD src0_sel:DWORD src1_sel:WORD_1\n"
    "v_lshlrev_b32_sdwa v187, 1, v174 dst_sel:DWORD dst_unused:UNUSED_PAD src0_sel:DWORD src1_sel:WORD_1\n"
    "v_lshlrev_b32_sdwa v203, 1, v175 dst_sel:DWORD dst_unused:UNUSED_PAD src0_sel:DWORD src1_sel:WORD_1\n"
    "v_rcp_f16_e64 v232, v234\n"
    "v_mfma_f32_16x16x32_bf16 v[168:171], a[112:115], v[152:155], v[168:171]\n"
    "ds_read_u16_d16_hi v201, v201\n"
    "v_rcp_f16_sdwa v232, v234 dst_sel:WORD_1 dst_unused:UNUSED_PRESERVE src0_sel:WORD_1\n"
    "v_mfma_f32_16x16x32_bf16 v[172:175], a[156:159], v[144:147], 0\n"
    "ds_read_u16 v186, v186\n"
    "v_cvt_pk_f16_f32 v188, v176, v177\n"
    "v_mfma_f32_16x16x32_bf16 v[172:175], a[204:207], v[148:151], v[172:175]\n"
    "v_lshlrev_b32_sdwa v189, 1, v178 dst_sel:DWORD dst_unused:UNUSED_PAD src0_sel:DWORD src1_sel:WORD_1\n"
    "v_lshlrev_b32_sdwa v205, 1, v179 dst_sel:DWORD dst_unused:UNUSED_PAD src0_sel:DWORD src1_sel:WORD_1\n"
    "v_lshlrev_b32_sdwa v190, 1, v180 dst_sel:DWORD dst_unused:UNUSED_PAD src0_sel:DWORD src1_sel:WORD_1\n"
    "v_lshlrev_b32_sdwa v191, 1, v182 dst_sel:DWORD dst_unused:UNUSED_PAD src0_sel:DWORD src1_sel:WORD_1\n"
    "s_waitcnt lgkmcnt(6)\n"
    "v_or_b32_e32 v198, v198, v206\n"
    "v_lshlrev_b32_sdwa v206, 1, v181 dst_sel:DWORD dst_unused:UNUSED_PAD src0_sel:DWORD src1_sel:WORD_1\n"
    "v_fma_mix_f32 v234, v192, v232, 0 op_sel_hi:[1,1,0]\n"
    "v_mfma_f32_16x16x32_bf16 v[172:175], a[116:119], v[152:155], v[172:175]\n"
    "ds_read_u16_d16_hi v202, v202\n"
    "ds_read_b128 v[236:239], v244 offset:4096\n"
    "v_fma_mix_f32 v232, v192, v232, 0 op_sel:[1,1,0] op_sel_hi:[1,1,0]\n"
    "v_mfma_f32_16x16x32_bf16 v[176:179], a[152:155], v[156:159], 0\n"
    "ds_read_u16 v187, v187\n"
    "v_cvt_pk_bf16_f32 v192, v234, v232\n"
    "v_mfma_f32_16x16x32_bf16 v[176:179], a[200:203], v[160:163], v[176:179]\n"
    "v_pk_fma_f16 v232, v196, v196, s48\n"
    "v_lshlrev_b32_sdwa v200, 1, v169 dst_sel:DWORD dst_unused:UNUSED_PAD src0_sel:DWORD src1_sel:WORD_1\n"
    "v_mfma_f32_16x16x32_bf16 v[176:179], a[112:115], v[164:167], v[176:179]\n"
    "ds_read_u16_d16_hi v203, v203\n"
    "v_pk_mul_f16 v232, v196, v232\n"
    "v_exp_f16_e32 v234, v232\n"
    "s_waitcnt lgkmcnt(7)\n"
    "v_or_b32_e64 v199, v199, v207\n"
    "v_lshlrev_b32_sdwa v207, 1, v183 dst_sel:DWORD dst_unused:UNUSED_PAD src0_sel:DWORD src1_sel:WORD_1\n"
    "v_mfma_f32_16x16x32_bf16 v[180:183], a[156:159], v[156:159], 0\n"
    "ds_read_b128 a[244:247], v244 offset:6144\n"
    "buffer_store_dwordx4 a[252:255], v255, s[68:71], s59 offen offset:0 sc1\n"
    "v_mfma_f32_16x16x32_bf16 v[180:183], a[204:207], v[160:163], v[180:183]\n"
    "ds_read_u16 v189, v189\n"
    "ds_read_b128 a[252:255], v249 offset:2048\n"
    "v_mfma_f32_16x16x32_bf16 v[180:183], a[116:119], v[164:167], v[180:183]\n"
    "s_waitcnt lgkmcnt(8)\n"
    "ds_read_u16_d16_hi v205, v205\n"
    "v_exp_f16_sdwa v234, v232 dst_sel:WORD_1 dst_unused:UNUSED_PRESERVE src0_sel:WORD_1\n"
    "v_mfma_f32_16x16x32_bf16 v[96:99], a[16:19], v[192:195], v[96:99]\n"
    "s_nop 0\n"
    "ds_read_u16 v190, v190\n"
    "v_pk_fma_f16 v234, v234, s49, s49\n"
    "v_mfma_f32_16x16x32_bf16 v[100:103], a[20:23], v[192:195], v[100:103]\n"
    "v_rcp_f16_e64 v232, v234\n"
    "v_mfma_f32_16x16x32_bf16 v[112:115], a[24:27], v[192:195], v[112:115]\n"
    "ds_read_u16_d16_hi v206, v206\n"
    "v_rcp_f16_sdwa v232, v234 dst_sel:WORD_1 dst_unused:UNUSED_PRESERVE src0_sel:WORD_1\n"
    "v_mfma_f32_16x16x32_bf16 v[116:119], a[28:31], v[192:195], v[116:119]\n"
    "s_waitcnt lgkmcnt(8)\n"
    "v_or_b32_e32 v185, v185, v201\n"
    "v_fma_mix_f32 v234, v196, v232, 0 op_sel_hi:[1,1,0]\n"
    "v_mfma_f32_16x16x32_bf16 v[128:131], v[236:239], v[192:195], v[128:131]\n"
    "ds_read_u16 v191, v191\n"
    "v_fma_mix_f32 v232, v196, v232, 0 op_sel:[1,1,0] op_sel_hi:[1,1,0]\n"
    "v_or_b32_e64 v186, v186, v202\n"
    "v_cvt_pk_bf16_f32 v196, v234, v232\n"
    "v_pk_fma_f16 v232, v184, v184, s48\n"
    "v_lshlrev_b32_sdwa v201, 1, v171 dst_sel:DWORD dst_unused:UNUSED_PAD src0_sel:DWORD src1_sel:WORD_1\n"
    "v_mfma_f32_16x16x32_bf16 v[104:107], a[16:19], v[196:199], v[104:107]\n"
    "ds_read_u16_d16_hi v207, v207\n"
    "v_pk_mul_f16 v232, v184, v232\n"
    "s_waitcnt lgkmcnt(7)\n"
    "v_mfma_f32_16x16x32_bf16 v[132:135], a[244:247], v[192:195], v[132:135]\n"
    "s_nop 0\n"
    "ds_read_b128 a[240:243], v244 offset:34816\n"
    "v_or_b32_e64 v187, v187, v203\n"
    "v_mfma_f32_16x16x32_bf16 v[108:111], a[20:23], v[196:199], v[108:111]\n"
    "ds_read_u16_d16_hi v200, v200\n"
    "v_exp_f16_e32 v234, v232\n"
    "v_mfma_f32_16x16x32_bf16 v[120:123], a[24:27], v[196:199], v[120:123]\n"
    "s_waitcnt lgkmcnt(7)\n"
    "ds_read_u16_d16_hi v201, v201\n"
    "buffer_store_dwordx4 a[252:255], v255, s[68:71], s59 offen offset:64 sc1\n"
    "v_mfma_f32_16x16x32_bf16 v[124:127], a[28:31], v[196:199], v[124:127]\n"
    "s_nop 0\n"
    "v_exp_f16_sdwa v234, v232 dst_sel:WORD_1 dst_unused:UNUSED_PRESERVE src0_sel:WORD_1\n"
    "v_mfma_f32_16x16x32_bf16 v[136:139], v[236:239], v[196:199], v[136:139]\n"
    "s_waitcnt lgkmcnt(7)\n"
    "ds_read_b128 a[252:255], v249 offset:4096\n"
    "ds_read_b128 v[236:239], v244 offset:8192\n"
    "v_mfma_f32_16x16x32_bf16 v[140:143], a[244:247], v[196:199], v[140:143]\n"
    "v_or_b32_e64 v189, v189, v205\n"
    "v_lshlrev_b32_sdwa v192, 1, v168 dst_sel:DWORD dst_unused:UNUSED_PAD src0_sel:DWORD src1_sel:WORD_1\n"
    "v_pk_fma_f16 v234, v234, s49, s49\n"
    "v_lshlrev_b32_sdwa v193, 1, v170 dst_sel:DWORD dst_unused:UNUSED_PAD src0_sel:DWORD src1_sel:WORD_1\n"
    "v_mfma_f32_16x16x32_bf16 v[168:171], a[160:163], v[144:147], 0\n"
    "s_nop 0\n"
    "ds_read_u16 v192, v192\n"
    "ds_read_u16 v193, v193\n"
    "v_mfma_f32_16x16x32_bf16 v[168:171], a[208:211], v[148:151], v[168:171]\n"
    "s_waitcnt lgkmcnt(9)\n"
    "v_lshlrev_b32_sdwa v194, 1, v172 dst_sel:DWORD dst_unused:UNUSED_PAD src0_sel:DWORD src1_sel:WORD_1\n"
    "v_lshlrev_b32_sdwa v202, 1, v173 dst_sel:DWORD dst_unused:UNUSED_PAD src0_sel:DWORD src1_sel:WORD_1\n"
    "v_lshlrev_b32_sdwa v195, 1, v174 dst_sel:DWORD dst_unused:UNUSED_PAD src0_sel:DWORD src1_sel:WORD_1\n"
    "v_lshlrev_b32_sdwa v203, 1, v175 dst_sel:DWORD dst_unused:UNUSED_PAD src0_sel:DWORD src1_sel:WORD_1\n"
    "v_rcp_f16_e64 v232, v234\n"
    "v_mfma_f32_16x16x32_bf16 v[168:171], a[120:123], v[152:155], v[168:171]\n"
    "ds_read_u16 v194, v194\n"
    "v_or_b32_e32 v190, v190, v206\n"
    "v_rcp_f16_sdwa v232, v234 dst_sel:WORD_1 dst_unused:UNUSED_PRESERVE src0_sel:WORD_1\n"
    "v_mfma_f32_16x16x32_bf16 v[172:175], a[164:167], v[144:147], 0\n"
    "s_waitcnt lgkmcnt(7)\n"
    "ds_read_u16_d16_hi v202, v202\n"
    "v_lshlrev_b32_sdwa v196, 1, v176 dst_sel:DWORD dst_unused:UNUSED_PAD src0_sel:DWORD src1_sel:WORD_1\n"
    "v_mfma_f32_16x16x32_bf16 v[172:175], a[212:215], v[148:151], v[172:175]\n"
    "ds_read_u16 v195, v195\n"
    "v_lshlrev_b32_sdwa v204, 1, v177 dst_sel:DWORD dst_unused:UNUSED_PAD src0_sel:DWORD src1_sel:WORD_1\n"
    "v_lshlrev_b32_sdwa v197, 1, v178 dst_sel:DWORD dst_unused:UNUSED_PAD src0_sel:DWORD src1_sel:WORD_1\n"
    "v_lshlrev_b32_sdwa v205, 1, v179 dst_sel:DWORD dst_unused:UNUSED_PAD src0_sel:DWORD src1_sel:WORD_1\n"
    "v_lshlrev_b32_sdwa v198, 1, v180 dst_sel:DWORD dst_unused:UNUSED_PAD src0_sel:DWORD src1_sel:WORD_1\n"
    "v_lshlrev_b32_sdwa v206, 1, v181 dst_sel:DWORD dst_unused:UNUSED_PAD src0_sel:DWORD src1_sel:WORD_1\n"
    "v_lshlrev_b32_sdwa v199, 1, v182 dst_sel:DWORD dst_unused:UNUSED_PAD src0_sel:DWORD src1_sel:WORD_1\n"
    "v_fma_mix_f32 v234, v184, v232, 0 op_sel_hi:[1,1,0]\n"
    "v_mfma_f32_16x16x32_bf16 v[172:175], a[240:243], v[152:155], v[172:175]\n"
    "ds_read_b128 a[244:247], v244 offset:10240\n"
    "s_waitcnt lgkmcnt(7)\n"
    "buffer_store_dwordx4 a[252:255], v255, s[68:71], s59 offen offset:128 sc1\n"
    "v_or_b32_e32 v191, v191, v207\n"
    "v_lshlrev_b32_sdwa v207, 1, v183 dst_sel:DWORD dst_unused:UNUSED_PAD src0_sel:DWORD src1_sel:WORD_1\n"
    "v_fma_mix_f32 v232, v184, v232, 0 op_sel:[1,1,0] op_sel_hi:[1,1,0]\n"
    "v_mfma_f32_16x16x32_bf16 v[176:179], a[160:163], v[156:159], 0\n"
    "ds_read_b128 a[252:255], v249 offset:1024\n"
    "ds_read_u16_d16_hi v203, v203\n"
    "v_cvt_pk_bf16_f32 v184, v234, v232\n"
    "v_mfma_f32_16x16x32_bf16 v[176:179], a[208:211], v[160:163], v[176:179]\n"
    "s_waitcnt lgkmcnt(7)\n"
    "ds_read_u16 v196, v196\n"
    "ds_read_u16_d16_hi v204, v204\n"
    "v_mfma_f32_16x16x32_bf16 v[176:179], a[120:123], v[164:167], v[176:179]\n"
    "s_nop 0\n"
    "v_pk_fma_f16 v232, v188, v188, s48\n"
    "v_or_b32_e32 v192, v192, v200\n"
    "v_mfma_f32_16x16x32_bf16 v[180:183], a[164:167], v[156:159], 0\n"
    "s_waitcnt lgkmcnt(8)\n"
    "ds_read_u16 v197, v197\n"
    "v_pk_mul_f16 v232, v188, v232\n"
    "v_mfma_f32_16x16x32_bf16 v[180:183], a[212:215], v[160:163], v[180:183]\n"
    "v_exp_f16_e64 v234, v232\n"
    "v_mfma_f32_16x16x32_bf16 v[180:183], a[240:243], v[164:167], v[180:183]\n"
    "ds_read_u16_d16_hi v205, v205\n"
    "v_exp_f16_sdwa v234, v232 dst_sel:WORD_1 dst_unused:UNUSED_PRESERVE src0_sel:WORD_1\n"
    "v_mfma_f32_16x16x32_bf16 v[96:99], a[32:35], v[184:187], v[96:99]\n"
    "s_waitcnt lgkmcnt(8)\n"
    "v_pk_fma_f16 v234, v234, s49, s49\n"
    "v_or_b32_e32 v193, v193, v201\n"
    "v_mfma_f32_16x16x32_bf16 v[100:103], a[36:39], v[184:187], v[100:103]\n"
    "ds_read_u16 v198, v198\n"
    "v_rcp_f16_e64 v232, v234\n"
    "v_mfma_f32_16x16x32_bf16 v[112:115], a[40:43], v[184:187], v[112:115]\n"
    "ds_read_u16_d16_hi v206, v206\n"
    "s_waitcnt lgkmcnt(7)\n"
    "buffer_store_dwordx4 a[252:255], v255, s[68:71], s39 offen offset:0 sc1\n"
    "v_rcp_f16_sdwa v232, v234 dst_sel:WORD_1 dst_unused:UNUSED_PRESERVE src0_sel:WORD_1\n"
    "v_mfma_f32_16x16x32_bf16 v[116:119], a[44:47], v[184:187], v[116:119]\n"
    "ds_read_u16 v199, v199\n"
    "v_fma_mix_f32 v234, v188, v232, 0 op_sel_hi:[1,1,0]\n"
    "v_mfma_f32_16x16x32_bf16 v[128:131], v[236:239], v[184:187], v[128:131]\n"
    "s_waitcnt lgkmcnt(7)\n"
    "ds_read_b128 a[252:255], v249 offset:3072\n"
    "v_fma_mix_f32 v232, v188, v232, 0 op_sel:[1,1,0] op_sel_hi:[1,1,0]\n"
    "v_mfma_f32_16x16x32_bf16 v[132:135], a[244:247], v[184:187], v[132:135]\n"
    "ds_read_u16_d16_hi v207, v207\n"
    "v_cvt_pk_bf16_f32 v188, v234, v232\n"
    "v_cvt_pk_f16_f32 v184, v168, v169\n"
    "v_lshlrev_b32_sdwa v185, 1, v170 dst_sel:DWORD dst_unused:UNUSED_PAD src0_sel:DWORD src1_sel:WORD_1\n"
    "v_mfma_f32_16x16x32_bf16 v[104:107], a[32:35], v[188:191], v[104:107]\n"
    "ds_read_b128 a[240:243], v244 offset:38912\n"
    "v_lshlrev_b32_sdwa v201, 1, v171 dst_sel:DWORD dst_unused:UNUSED_PAD src0_sel:DWORD src1_sel:WORD_1\n"
    "v_mfma_f32_16x16x32_bf16 v[108:111], a[36:39], v[188:191], v[108:111]\n"
    "s_waitcnt lgkmcnt(8)\n"
    "ds_read_u16 v185, v185\n"
    "v_lshlrev_b32_sdwa v186, 1, v172 dst_sel:DWORD dst_unused:UNUSED_PAD src0_sel:DWORD src1_sel:WORD_1\n"
    "v_mfma_f32_16x16x32_bf16 v[120:123], a[40:43], v[188:191], v[120:123]\n"
    "v_or_b32_e32 v194, v194, v202\n"
    "v_lshlrev_b32_sdwa v202, 1, v173 dst_sel:DWORD dst_unused:UNUSED_PAD src0_sel:DWORD src1_sel:WORD_1\n"
    "v_mfma_f32_16x16x32_bf16 v[124:127], a[44:47], v[188:191], v[124:127]\n"
    "ds_read_u16_d16_hi v201, v201\n"
    "v_lshlrev_b32_sdwa v187, 1, v174 dst_sel:DWORD dst_unused:UNUSED_PAD src0_sel:DWORD src1_sel:WORD_1\n"
    "v_mfma_f32_16x16x32_bf16 v[136:139], v[236:239], v[188:191], v[136:139]\n"
    "s_waitcnt lgkmcnt(8)\n"
    "v_or_b32_e32 v195, v195, v203\n"
    "v_lshlrev_b32_sdwa v203, 1, v175 dst_sel:DWORD dst_unused:UNUSED_PAD src0_sel:DWORD src1_sel:WORD_1\n"
    "v_mfma_f32_16x16x32_bf16 v[140:143], a[244:247], v[188:191], v[140:143]\n"
    "ds_read_u16 v186, v186\n"
    "v_cvt_pk_f16_f32 v188, v176, v177\n"
    "v_mfma_f32_16x16x32_bf16 v[168:171], a[168:171], v[144:147], 0\n"
    "ds_read_u16_d16_hi v202, v202\n"
    "v_lshlrev_b32_sdwa v189, 1, v178 dst_sel:DWORD dst_unused:UNUSED_PAD src0_sel:DWORD src1_sel:WORD_1\n"
    "v_mfma_f32_16x16x32_bf16 v[168:171], a[216:219], v[148:151], v[168:171]\n"
    "v_lshlrev_b32_sdwa v190, 1, v180 dst_sel:DWORD dst_unused:UNUSED_PAD src0_sel:DWORD src1_sel:WORD_1\n"
    "v_lshlrev_b32_sdwa v191, 1, v182 dst_sel:DWORD dst_unused:UNUSED_PAD src0_sel:DWORD src1_sel:WORD_1\n"
    "v_mfma_f32_16x16x32_bf16 v[168:171], a[124:127], v[152:155], v[168:171]\n"
    "s_waitcnt lgkmcnt(8)\n"
    "ds_read_b128 v[236:239], v244 offset:12288\n"
    "v_or_b32_e64 v196, v196, v204\n"
    "v_mfma_f32_16x16x32_bf16 v[172:175], a[172:175], v[144:147], 0\n"
    "s_waitcnt lgkmcnt(7)\n"
    "ds_read_u16 v187, v187\n"
    "buffer_store_dwordx4 a[252:255], v255, s[68:71], s39 offen offset:64 sc1\n"
    "v_mfma_f32_16x16x32_bf16 v[172:175], a[220:223], v[148:151], v[172:175]\n"
    "ds_read_u16_d16_hi v203, v203\n"
    "v_or_b32_e64 v197, v197, v205\n"
    "v_lshlrev_b32_sdwa v205, 1, v179 dst_sel:DWORD dst_unused:UNUSED_PAD src0_sel:DWORD src1_sel:WORD_1\n"
    "s_waitcnt lgkmcnt(7)\n"
    "v_mfma_f32_16x16x32_bf16 v[172:175], a[240:243], v[152:155], v[172:175]\n"
    "s_nop 0\n"
    "ds_read_b128 a[252:255], v249 offset:5120\n"
    "v_or_b32_e64 v198, v198, v206\n"
    "v_mfma_f32_16x16x32_bf16 v[176:179], a[168:171], v[156:159], 0\n"
    "ds_read_b128 a[244:247], v244 offset:14336\n"
    "v_lshlrev_b32_sdwa v206, 1, v181 dst_sel:DWORD dst_unused:UNUSED_PAD src0_sel:DWORD src1_sel:WORD_1\n"
    "v_mfma_f32_16x16x32_bf16 v[176:179], a[216:219], v[160:163], v[176:179]\n"
    "ds_read_u16 v189, v189\n"
    "v_or_b32_e64 v199, v199, v207\n"
    "v_mfma_f32_16x16x32_bf16 v[176:179], a[124:127], v[164:167], v[176:179]\n"
    "v_lshlrev_b32_sdwa v207, 1, v183 dst_sel:DWORD dst_unused:UNUSED_PAD src0_sel:DWORD src1_sel:WORD_1\n"
    "v_pk_fma_f16 v232, v184, v184, s48\n"
    "v_mfma_f32_16x16x32_bf16 v[180:183], a[172:175], v[156:159], 0\n"
    "s_waitcnt lgkmcnt(8)\n"
    "ds_read_u16_d16_hi v205, v205\n"
    "v_or_b32_e32 v185, v185, v201\n"
    "v_mfma_f32_16x16x32_bf16 v[180:183], a[220:223], v[160:163], v[180:183]\n"
    "v_pk_mul_f16 v232, v184, v232\n"
    "v_lshlrev_b32_sdwa v201, 1, v171 dst_sel:DWORD dst_unused:UNUSED_PAD src0_sel:DWORD src1_sel:WORD_1\n"
    "v_mfma_f32_16x16x32_bf16 v[180:183], a[240:243], v[164:167], v[180:183]\n"
    "ds_read_u16 v190, v190\n"
    "v_exp_f16_e32 v234, v232\n"
    "v_mfma_f32_16x16x32_bf16 v[96:99], a[48:51], v[192:195], v[96:99]\n"
    "s_waitcnt lgkmcnt(7)\n"
    "ds_read_b128 v[144:147], v243 offset:0\n"
    "v_or_b32_e64 v186, v186, v202\n"
    "v_mfma_f32_16x16x32_bf16 v[100:103], a[52:55], v[192:195], v[100:103]\n"
    "ds_read_u16_d16_hi v206, v206\n"
    "v_exp_f16_sdwa v234, v232 dst_sel:WORD_1 dst_unused:UNUSED_PRESERVE src0_sel:WORD_1\n"
    "v_mfma_f32_16x16x32_bf16 v[112:115], a[56:59], v[192:195], v[112:115]\n"
    "v_pk_fma_f16 v234, v234, s49, s49\n"
    "v_lshlrev_b32_sdwa v202, 1, v173 dst_sel:DWORD dst_unused:UNUSED_PAD src0_sel:DWORD src1_sel:WORD_1\n"
    "v_mfma_f32_16x16x32_bf16 v[116:119], a[60:63], v[192:195], v[116:119]\n"
    "ds_read_b128 v[148:151], v243 offset:1024\n"
    "ds_read_u16 v191, v191\n"
    "v_mfma_f32_16x16x32_bf16 v[128:131], v[236:239], v[192:195], v[128:131]\n"
    "buffer_load_dwordx4 a[232:235], v245, s[40:43], s57 offen offset:64\n"
    "buffer_load_dwordx4 a[224:227], v245, s[40:43], s57 offen offset:0\n"
    "v_mfma_f32_16x16x32_bf16 v[104:107], a[48:51], v[196:199], v[104:107]\n"
    "s_waitcnt lgkmcnt(8)\n"
    "ds_read_b128 v[152:155], v243 offset:2048\n"
    "v_or_b32_e32 v187, v187, v203\n"
    "s_waitcnt lgkmcnt(8)\n"
    "v_mfma_f32_16x16x32_bf16 v[132:135], a[244:247], v[192:195], v[132:135]\n"
    "s_nop 0\n"
    "ds_read_u16_d16_hi v207, v207\n"
    "v_cvt_pk_f16_f32 v192, v168, v169\n"
    "v_mfma_f32_16x16x32_bf16 v[108:111], a[52:55], v[196:199], v[108:111]\n"
    "v_lshlrev_b32_sdwa v193, 1, v170 dst_sel:DWORD dst_unused:UNUSED_PAD src0_sel:DWORD src1_sel:WORD_1\n"
    "v_lshlrev_b32_sdwa v194, 1, v172 dst_sel:DWORD dst_unused:UNUSED_PAD src0_sel:DWORD src1_sel:WORD_1\n"
    "v_mfma_f32_16x16x32_bf16 v[120:123], a[56:59], v[196:199], v[120:123]\n"
    "ds_read_b128 v[156:159], v243 offset:3072\n"
    "v_lshlrev_b32_sdwa v195, 1, v174 dst_sel:DWORD dst_unused:UNUSED_PAD src0_sel:DWORD src1_sel:WORD_1\n"
    "v_mfma_f32_16x16x32_bf16 v[124:127], a[60:63], v[196:199], v[124:127]\n"
    "ds_read_b128 v[160:163], v243 offset:4096\n"
    "v_lshlrev_b32_sdwa v203, 1, v175 dst_sel:DWORD dst_unused:UNUSED_PAD src0_sel:DWORD src1_sel:WORD_1\n"
    "v_mfma_f32_16x16x32_bf16 v[136:139], v[236:239], v[196:199], v[136:139]\n"
    "s_waitcnt lgkmcnt(9)\n"
    "ds_read_b128 v[164:167], v243 offset:5120\n"
    "v_or_b32_e32 v189, v189, v205\n"
    "v_mfma_f32_16x16x32_bf16 v[140:143], a[244:247], v[196:199], v[140:143]\n"
    "ds_read_b128 a[240:243], v244 offset:32768\n"
    "v_rcp_f16_e32 v232, v234\n"
    "s_waitcnt lgkmcnt(9)\n"
    "v_mfma_f32_16x16x32_bf16 v[168:171], a[128:131], v[144:147], 0\n"
    "ds_read_u16 v193, v193\n"
    "v_rcp_f16_sdwa v232, v234 dst_sel:WORD_1 dst_unused:UNUSED_PRESERVE src0_sel:WORD_1\n"
    "v_mfma_f32_16x16x32_bf16 v[172:175], a[132:135], v[144:147], 0\n"
    "ds_read_u16_d16_hi v201, v201\n"
    "buffer_store_dwordx4 a[252:255], v255, s[68:71], s39 offen offset:128 sc1\n"
    "s_waitcnt lgkmcnt(9)\n"
    "v_mfma_f32_16x16x32_bf16 v[168:171], a[176:179], v[148:151], v[168:171]\n"
    "s_nop 0\n"
    "ds_read_u16 v194, v194\n"
    "v_or_b32_e64 v190, v190, v206\n"
    "v_cvt_pk_f16_f32 v196, v176, v177\n"
    "s_waitcnt lgkmcnt(8)\n"
    "v_mfma_f32_16x16x32_bf16 v[168:171], a[96:99], v[152:155], v[168:171]\n"
    "s_nop 0\n"
    "ds_read_u16_d16_hi v202, v202\n"
    "v_lshlrev_b32_sdwa v197, 1, v178 dst_sel:DWORD dst_unused:UNUSED_PAD src0_sel:DWORD src1_sel:WORD_1\n"
    "v_lshlrev_b32_sdwa v205, 1, v179 dst_sel:DWORD dst_unused:UNUSED_PAD src0_sel:DWORD src1_sel:WORD_1\n"
    "v_lshlrev_b32_sdwa v198, 1, v180 dst_sel:DWORD dst_unused:UNUSED_PAD src0_sel:DWORD src1_sel:WORD_1\n"
    "v_lshlrev_b32_sdwa v206, 1, v181 dst_sel:DWORD dst_unused:UNUSED_PAD src0_sel:DWORD src1_sel:WORD_1\n"
    "v_lshlrev_b32_sdwa v199, 1, v182 dst_sel:DWORD dst_unused:UNUSED_PAD src0_sel:DWORD src1_sel:WORD_1\n"
    "v_fma_mix_f32 v234, v184, v232, 0 op_sel_hi:[1,1,0]\n"
    "v_mfma_f32_16x16x32_bf16 v[172:175], a[180:183], v[148:151], v[172:175]\n"
    "s_waitcnt lgkmcnt(7)\n"
    "ds_read_b128 v[236:239], v244 offset:16384\n"
    "ds_read_u16 v195, v195\n"
    "v_or_b32_e32 v191, v191, v207\n"
    "v_lshlrev_b32_sdwa v207, 1, v183 dst_sel:DWORD dst_unused:UNUSED_PAD src0_sel:DWORD src1_sel:WORD_1\n"
    "v_fma_mix_f32 v232, v184, v232, 0 op_sel:[1,1,0] op_sel_hi:[1,1,0]\n"
    "v_mfma_f32_16x16x32_bf16 v[176:179], a[128:131], v[156:159], 0\n"
    "s_waitcnt lgkmcnt(7)\n"
    "ds_read_u16_d16_hi v203, v203\n"
    "v_cvt_pk_bf16_f32 v184, v234, v232\n"
    "v_mfma_f32_16x16x32_bf16 v[176:179], a[176:179], v[160:163], v[176:179]\n"
    "s_waitcnt lgkmcnt(7)\n"
    "v_pk_fma_f16 v232, v188, v188, s48\n"
    "v_lshlrev_b32_sdwa v200, 1, v169 dst_sel:DWORD dst_unused:UNUSED_PAD src0_sel:DWORD src1_sel:WORD_1\n"
    "v_mfma_f32_16x16x32_bf16 v[172:175], a[240:243], v[152:155], v[172:175]\n"
    "s_barrier\n"
    "v_mfma_f32_16x16x32_bf16 v[176:179], a[96:99], v[164:167], v[176:179]\n"
    "ds_read_b128 a[244:247], v244 offset:18432\n"
    "ds_write_b128 v253, v[64:67] offset:2048\n"
    "v_mfma_f32_16x16x32_bf16 v[180:183], a[132:135], v[156:159], 0\n"
    "s_waitcnt lgkmcnt(7)\n"
    "buffer_load_dwordx4 a[236:239], v245, s[40:43], s100 offen offset:64\n"
    "buffer_load_dwordx4 a[228:231], v245, s[40:43], s100 offen offset:0\n"
    "v_pk_mul_f16 v232, v188, v232\n"
    "v_mfma_f32_16x16x32_bf16 v[180:183], a[180:183], v[160:163], v[180:183]\n"
    "v_exp_f16_e64 v234, v232\n"
    "v_mfma_f32_16x16x32_bf16 v[180:183], a[240:243], v[164:167], v[180:183]\n"
    "ds_read_u16 v197, v197\n"
    "ds_write_b128 v253, v[68:71] offset:5120\n"
    "v_exp_f16_sdwa v234, v232 dst_sel:WORD_1 dst_unused:UNUSED_PRESERVE src0_sel:WORD_1\n"
    "v_mfma_f32_16x16x32_bf16 v[96:99], a[64:67], v[184:187], v[96:99]\n"
    "s_waitcnt lgkmcnt(6)\n"
    "v_pk_fma_f16 v234, v234, s49, s49\n"
    "v_or_b32_e32 v193, v193, v201\n"
    "v_mfma_f32_16x16x32_bf16 v[100:103], a[68:71], v[184:187], v[100:103]\n"
    "v_rcp_f16_e32 v232, v234\n"
    "v_mfma_f32_16x16x32_bf16 v[112:115], a[72:75], v[184:187], v[112:115]\n"
    "ds_read_u16_d16_hi v205, v205\n"
    "ds_write_b128 v253, v[88:91] offset:0\n"
    "v_rcp_f16_sdwa v232, v234 dst_sel:WORD_1 dst_unused:UNUSED_PRESERVE src0_sel:WORD_1\n"
    "v_mfma_f32_16x16x32_bf16 v[116:119], a[76:79], v[184:187], v[116:119]\n"
    "s_waitcnt lgkmcnt(4)\n"
    "v_fma_mix_f32 v234, v188, v232, 0 op_sel_hi:[1,1,0]\n"
    "v_fma_mix_f32 v232, v188, v232, 0 op_sel:[1,1,0] op_sel_hi:[1,1,0]\n"
    "v_mfma_f32_16x16x32_bf16 v[128:131], v[236:239], v[184:187], v[128:131]\n"
    "v_or_b32_e64 v194, v194, v202\n"
    "v_cvt_pk_bf16_f32 v188, v234, v232\n"
    "v_mfma_f32_16x16x32_bf16 v[132:135], a[244:247], v[184:187], v[132:135]\n"
    "ds_read_u16 v198, v198\n"
    "ds_write_b128 v253, v[92:95] offset:3072\n"
    "v_mfma_f32_16x16x32_bf16 v[104:107], a[64:67], v[188:191], v[104:107]\n"
    "v_pk_fma_f16 v232, v192, v192, s48\n"
    "v_or_b32_e64 v195, v195, v203\n"
    "v_mfma_f32_16x16x32_bf16 v[108:111], a[68:71], v[188:191], v[108:111]\n"
    "v_pk_mul_f16 v232, v192, v232\n"
    "v_lshlrev_b32_sdwa v184, 1, v168 dst_sel:DWORD dst_unused:UNUSED_PAD src0_sel:DWORD src1_sel:WORD_1\n"
    "v_mfma_f32_16x16x32_bf16 v[120:123], a[72:75], v[188:191], v[120:123]\n"
    "ds_read_u16_d16_hi v206, v206\n"
    "ds_read_u16 v199, v199\n"
    "v_mfma_f32_16x16x32_bf16 v[124:127], a[76:79], v[188:191], v[124:127]\n"
    "ds_read_u16_d16_hi v207, v207\n"
    "ds_write_b128 v253, v[72:75] offset:1024\n"
    "v_mfma_f32_16x16x32_bf16 v[136:139], v[236:239], v[188:191], v[136:139]\n"
    "s_waitcnt lgkmcnt(6)\n"
    "v_exp_f16_e32 v234, v232\n"
    "v_mfma_f32_16x16x32_bf16 v[140:143], a[244:247], v[188:191], v[140:143]\n"
    "v_or_b32_e64 v197, v197, v205\n"
    "v_exp_f16_sdwa v234, v232 dst_sel:WORD_1 dst_unused:UNUSED_PRESERVE src0_sel:WORD_1\n"
    "v_lshlrev_b32_sdwa v185, 1, v170 dst_sel:DWORD dst_unused:UNUSED_PAD src0_sel:DWORD src1_sel:WORD_1\n"
    "v_pk_fma_f16 v234, v234, s49, s49\n"
    "v_lshlrev_b32_sdwa v201, 1, v171 dst_sel:DWORD dst_unused:UNUSED_PAD src0_sel:DWORD src1_sel:WORD_1\n"
    "v_mfma_f32_16x16x32_bf16 v[168:171], a[136:139], v[144:147], 0\n"
    "ds_read_b128 a[240:243], v244 offset:36864\n"
    "ds_write_b128 v253, v[76:79] offset:4096\n"
    "v_mfma_f32_16x16x32_bf16 v[168:171], a[184:187], v[148:151], v[168:171]\n"
    "v_lshlrev_b32_sdwa v186, 1, v172 dst_sel:DWORD dst_unused:UNUSED_PAD src0_sel:DWORD src1_sel:WORD_1\n"
    "v_lshlrev_b32_sdwa v202, 1, v173 dst_sel:DWORD dst_unused:UNUSED_PAD src0_sel:DWORD src1_sel:WORD_1\n"
    "v_lshlrev_b32_sdwa v187, 1, v174 dst_sel:DWORD dst_unused:UNUSED_PAD src0_sel:DWORD src1_sel:WORD_1\n"
    "v_lshlrev_b32_sdwa v203, 1, v175 dst_sel:DWORD dst_unused:UNUSED_PAD src0_sel:DWORD src1_sel:WORD_1\n"
    "v_rcp_f16_e32 v232, v234\n"
    "v_mfma_f32_16x16x32_bf16 v[168:171], a[100:103], v[152:155], v[168:171]\n"
    "s_waitcnt lgkmcnt(5)\n"
    "ds_read_u16 v184, v184\n"
    "ds_read_u16_d16_hi v200, v200\n"
    "v_or_b32_e64 v198, v198, v206\n"
    "v_rcp_f16_sdwa v232, v234 dst_sel:WORD_1 dst_unused:UNUSED_PRESERVE src0_sel:WORD_1\n"
    "v_mfma_f32_16x16x32_bf16 v[172:175], a[140:143], v[144:147], 0\n"
    "ds_read_u16 v185, v185\n"
    "ds_read_u16_d16_hi v201, v201\n"
    "v_lshlrev_b32_sdwa v188, 1, v176 dst_sel:DWORD dst_unused:UNUSED_PAD src0_sel:DWORD src1_sel:WORD_1\n"
    "v_lshlrev_b32_sdwa v204, 1, v177 dst_sel:DWORD dst_unused:UNUSED_PAD src0_sel:DWORD src1_sel:WORD_1\n"
    "v_lshlrev_b32_sdwa v189, 1, v178 dst_sel:DWORD dst_unused:UNUSED_PAD src0_sel:DWORD src1_sel:WORD_1\n"
    "v_lshlrev_b32_sdwa v205, 1, v179 dst_sel:DWORD dst_unused:UNUSED_PAD src0_sel:DWORD src1_sel:WORD_1\n"
    "v_lshlrev_b32_sdwa v190, 1, v180 dst_sel:DWORD dst_unused:UNUSED_PAD src0_sel:DWORD src1_sel:WORD_1\n"
    "v_lshlrev_b32_sdwa v206, 1, v181 dst_sel:DWORD dst_unused:UNUSED_PAD src0_sel:DWORD src1_sel:WORD_1\n"
    "v_lshlrev_b32_sdwa v191, 1, v182 dst_sel:DWORD dst_unused:UNUSED_PAD src0_sel:DWORD src1_sel:WORD_1\n"
    "v_fma_mix_f32 v234, v192, v232, 0 op_sel_hi:[1,1,0]\n"
    "v_mfma_f32_16x16x32_bf16 v[172:175], a[188:191], v[148:151], v[172:175]\n"
    "s_waitcnt lgkmcnt(6)\n"
    "ds_read_u16 v186, v186\n"
    "ds_read_b128 v[236:239], v244 offset:20480\n"
    "s_waitcnt lgkmcnt(7)\n"
    "v_mfma_f32_16x16x32_bf16 v[172:175], a[240:243], v[152:155], v[172:175]\n"
    "ds_read_u16_d16_hi v202, v202\n"
    "v_or_b32_e64 v199, v199, v207\n"
    "v_lshlrev_b32_sdwa v207, 1, v183 dst_sel:DWORD dst_unused:UNUSED_PAD src0_sel:DWORD src1_sel:WORD_1\n"
    "v_fma_mix_f32 v232, v192, v232, 0 op_sel:[1,1,0] op_sel_hi:[1,1,0]\n"
    "v_mfma_f32_16x16x32_bf16 v[176:179], a[136:139], v[156:159], 0\n"
    "v_cvt_pk_bf16_f32 v192, v234, v232\n"
    "v_pk_fma_f16 v232, v196, v196, s48\n"
    "v_mfma_f32_16x16x32_bf16 v[176:179], a[184:187], v[160:163], v[176:179]\n"
    "ds_read_u16 v187, v187\n"
    "v_pk_mul_f16 v232, v196, v232\n"
    "v_mfma_f32_16x16x32_bf16 v[176:179], a[100:103], v[164:167], v[176:179]\n"
    "ds_read_b128 a[244:247], v244 offset:22528\n"
    "v_exp_f16_e64 v234, v232\n"
    "v_mfma_f32_16x16x32_bf16 v[180:183], a[140:143], v[156:159], 0\n"
    "ds_read_u16_d16_hi v203, v203\n"
    "v_exp_f16_sdwa v234, v232 dst_sel:WORD_1 dst_unused:UNUSED_PRESERVE src0_sel:WORD_1\n"
    "v_mfma_f32_16x16x32_bf16 v[180:183], a[188:191], v[160:163], v[180:183]\n"
    "s_waitcnt lgkmcnt(8)\n"
    "v_pk_fma_f16 v234, v234, s49, s49\n"
    "v_or_b32_e32 v184, v184, v200\n"
    "v_mfma_f32_16x16x32_bf16 v[180:183], a[240:243], v[164:167], v[180:183]\n"
    "ds_read_u16 v188, v188\n"
    "v_rcp_f16_e32 v232, v234\n"
    "v_mfma_f32_16x16x32_bf16 v[96:99], a[80:83], v[192:195], v[96:99]\n"
    "ds_read_u16_d16_hi v204, v204\n"
    "v_rcp_f16_sdwa v232, v234 dst_sel:WORD_1 dst_unused:UNUSED_PRESERVE src0_sel:WORD_1\n"
    "v_mfma_f32_16x16x32_bf16 v[100:103], a[84:87], v[192:195], v[100:103]\n"
    "s_waitcnt lgkmcnt(8)\n"
    "v_fma_mix_f32 v234, v196, v232, 0 op_sel_hi:[1,1,0]\n"
    "v_fma_mix_f32 v232, v196, v232, 0 op_sel:[1,1,0] op_sel_hi:[1,1,0]\n"
    "v_mfma_f32_16x16x32_bf16 v[112:115], a[88:91], v[192:195], v[112:115]\n"
    "ds_read_u16 v189, v189\n"
    "v_cvt_pk_bf16_f32 v196, v234, v232\n"
    "v_mfma_f32_16x16x32_bf16 v[116:119], a[92:95], v[192:195], v[116:119]\n"
    "s_waitcnt lgkmcnt(7)\n"
    "ds_read_u16_d16_hi v205, v205\n"
    "v_or_b32_e32 v185, v185, v201\n"
    "v_mfma_f32_16x16x32_bf16 v[128:131], v[236:239], v[192:195], v[128:131]\n"
    "v_lshlrev_b32_sdwa v201, 1, v171 dst_sel:DWORD dst_unused:UNUSED_PAD src0_sel:DWORD src1_sel:WORD_1\n"
    "v_cvt_pk_bf16_f32 v96, v96, v97\n"
    "v_mfma_f32_16x16x32_bf16 v[104:107], a[80:83], v[196:199], v[104:107]\n"
    "ds_read_u16 v190, v190\n"
    "v_cvt_pk_bf16_f32 v97, v98, v99\n"
    "v_mfma_f32_16x16x32_bf16 v[108:111], a[84:87], v[196:199], v[108:111]\n"
    "s_waitcnt lgkmcnt(8)\n"
    "v_or_b32_e64 v186, v186, v202\n"
    "v_lshlrev_b32_sdwa v202, 1, v173 dst_sel:DWORD dst_unused:UNUSED_PAD src0_sel:DWORD src1_sel:WORD_1\n"
    "v_mfma_f32_16x16x32_bf16 v[120:123], a[88:91], v[196:199], v[120:123]\n"
    "s_waitcnt lgkmcnt(6)\n"
    "ds_read_u16_d16_hi v206, v206\n"
    "v_cvt_pk_bf16_f32 v98, v100, v101\n"
    "v_mfma_f32_16x16x32_bf16 v[132:135], a[244:247], v[192:195], v[132:135]\n"
    "v_cvt_pk_f16_f32 v192, v168, v169\n"
    "v_lshlrev_b32_sdwa v193, 1, v170 dst_sel:DWORD dst_unused:UNUSED_PAD src0_sel:DWORD src1_sel:WORD_1\n"
    "v_mfma_f32_16x16x32_bf16 v[124:127], a[92:95], v[196:199], v[124:127]\n"
    "s_nop 0\n"
    "ds_read_u16 v191, v191\n"
    "v_lshlrev_b32_sdwa v194, 1, v172 dst_sel:DWORD dst_unused:UNUSED_PAD src0_sel:DWORD src1_sel:WORD_1\n"
    "v_mfma_f32_16x16x32_bf16 v[136:139], v[236:239], v[196:199], v[136:139]\n"
    "s_waitcnt lgkmcnt(7)\n"
    "ds_read_u16_d16_hi v207, v207\n"
    "v_lshlrev_b32_sdwa v195, 1, v174 dst_sel:DWORD dst_unused:UNUSED_PAD src0_sel:DWORD src1_sel:WORD_1\n"
    "v_mfma_f32_16x16x32_bf16 v[140:143], a[244:247], v[196:199], v[140:143]\n"
    "v_cvt_pk_f16_f32 v196, v176, v177\n"
    "v_lshlrev_b32_sdwa v197, 1, v178 dst_sel:DWORD dst_unused:UNUSED_PAD src0_sel:DWORD src1_sel:WORD_1\n"
    "v_mfma_f32_16x16x32_bf16 v[168:171], a[144:147], v[144:147], 0\n"
    "ds_read_u16 v193, v193\n"
    "v_lshlrev_b32_sdwa v198, 1, v180 dst_sel:DWORD dst_unused:UNUSED_PAD src0_sel:DWORD src1_sel:WORD_1\n"
    "v_mfma_f32_16x16x32_bf16 v[168:171], a[192:195], v[148:151], v[168:171]\n"
    "v_lshlrev_b32_sdwa v199, 1, v182 dst_sel:DWORD dst_unused:UNUSED_PAD src0_sel:DWORD src1_sel:WORD_1\n"
    "v_or_b32_e32 v187, v187, v203\n"
    "v_mfma_f32_16x16x32_bf16 v[168:171], a[104:107], v[152:155], v[168:171]\n"
    "s_waitcnt lgkmcnt(7)\n"
    "ds_read_u16_d16_hi v201, v201\n"
    "v_lshlrev_b32_sdwa v203, 1, v175 dst_sel:DWORD dst_unused:UNUSED_PAD src0_sel:DWORD src1_sel:WORD_1\n"
    "v_mfma_f32_16x16x32_bf16 v[172:175], a[148:151], v[144:147], 0\n"
    "v_or_b32_e64 v188, v188, v204\n"
    "v_cvt_pk_bf16_f32 v99, v102, v103\n"
    "v_mfma_f32_16x16x32_bf16 v[172:175], a[196:199], v[148:151], v[172:175]\n"
    "ds_read_u16 v194, v194\n"
    "v_cvt_pk_bf16_f32 v112, v112, v113\n"
    "v_mfma_f32_16x16x32_bf16 v[172:175], a[108:111], v[152:155], v[172:175]\n"
    "s_waitcnt lgkmcnt(7)\n"
    "ds_read_u16_d16_hi v202, v202\n"
    "v_or_b32_e32 v189, v189, v205\n"
    "v_lshlrev_b32_sdwa v205, 1, v179 dst_sel:DWORD dst_unused:UNUSED_PAD src0_sel:DWORD src1_sel:WORD_1\n"
    "v_mfma_f32_16x16x32_bf16 v[176:179], a[144:147], v[156:159], 0\n"
    "v_cvt_pk_bf16_f32 v113, v114, v115\n"
    "v_cvt_pk_bf16_f32 v114, v116, v117\n"
    "v_mfma_f32_16x16x32_bf16 v[176:179], a[192:195], v[160:163], v[176:179]\n"
    "ds_read_b128 v[236:239], v244 offset:0\n"
    "v_cvt_pk_bf16_f32 v115, v118, v119\n"
    "v_mfma_f32_16x16x32_bf16 v[176:179], a[104:107], v[164:167], v[176:179]\n"
    "ds_read_u16 v195, v195\n"
    "s_waitcnt lgkmcnt(8)\n"
    "v_or_b32_e32 v190, v190, v206\n"
    "v_mfma_f32_16x16x32_bf16 v[100:103], a[4:7], v[184:187], 0\n"
    "ds_read_u16_d16_hi v203, v203\n"
    "v_lshlrev_b32_sdwa v206, 1, v181 dst_sel:DWORD dst_unused:UNUSED_PAD src0_sel:DWORD src1_sel:WORD_1\n"
    "v_cvt_pk_bf16_f32 v128, v128, v129\n"
    "v_cvt_pk_bf16_f32 v129, v130, v131\n"
    "v_cvt_pk_bf16_f32 v130, v132, v133\n"
    "v_cvt_pk_bf16_f32 v131, v134, v135\n"
    "v_cvt_pk_bf16_f32 v104, v104, v105\n"
    "ds_write_b128 v242, v[96:99] offset:24640\n"
    "v_mfma_f32_16x16x32_bf16 v[96:99], a[0:3], v[184:187], 0\n"
    "s_waitcnt lgkmcnt(8)\n"
    "v_or_b32_e32 v191, v191, v207\n"
    "v_lshlrev_b32_sdwa v207, 1, v183 dst_sel:DWORD dst_unused:UNUSED_PAD src0_sel:DWORD src1_sel:WORD_1\n"
    "v_mfma_f32_16x16x32_bf16 v[180:183], a[148:151], v[156:159], 0\n"
    "ds_read_b128 a[244:247], v244 offset:2048\n"
    "ds_write_b128 v242, v[112:115] offset:26688\n"
    "v_mfma_f32_16x16x32_bf16 v[180:183], a[196:199], v[160:163], v[180:183]\n"
    "s_waitcnt lgkmcnt(8)\n"
    "v_or_b32_e32 v193, v193, v201\n"
    "v_cvt_pk_bf16_f32 v105, v106, v107\n"
    "v_mfma_f32_16x16x32_bf16 v[180:183], a[108:111], v[164:167], v[180:183]\n"
    "v_cvt_pk_bf16_f32 v106, v108, v109\n"
    "v_cvt_pk_bf16_f32 v107, v110, v111\n"
    "v_mfma_f32_16x16x32_bf16 v[112:115], a[8:11], v[184:187], 0\n"
    "ds_read_u16 v197, v197\n"
    "ds_write_b128 v242, v[128:131] offset:28736\n"
    "v_mfma_f32_16x16x32_bf16 v[116:119], a[12:15], v[184:187], 0\n"
    "s_waitcnt lgkmcnt(7)\n"
    "v_or_b32_e32 v194, v194, v202\n"
    "v_cvt_pk_bf16_f32 v120, v120, v121\n"
    "v_mfma_f32_16x16x32_bf16 v[128:131], v[236:239], v[184:187], 0\n"
    "v_cvt_pk_bf16_f32 v121, v122, v123\n"
    "v_cvt_pk_bf16_f32 v122, v124, v125\n"
    "v_mfma_f32_16x16x32_bf16 v[108:111], a[4:7], v[188:191], 0\n"
    "s_nop 0\n"
    "ds_read_u16_d16_hi v205, v205\n"
    "ds_write_b128 v242, v[104:107] offset:25664\n"
    "v_mfma_f32_16x16x32_bf16 v[104:107], a[0:3], v[188:191], 0\n"
    "s_waitcnt lgkmcnt(6)\n"
    "v_cvt_pk_bf16_f32 v123, v126, v127\n"
    "v_cvt_pk_bf16_f32 v136, v136, v137\n"
    "v_pk_fma_f16 v232, v192, v192, s48\n"
    "s_waitcnt lgkmcnt(5)\n"
    "v_mfma_f32_16x16x32_bf16 v[132:135], a[244:247], v[184:187], 0\n"
    "s_nop 0\n"
    "v_cvt_pk_bf16_f32 v137, v138, v139\n"
    "v_cvt_pk_bf16_f32 v138, v140, v141\n"
    "v_mfma_f32_16x16x32_bf16 v[124:127], a[12:15], v[188:191], 0\n"
    "ds_read_u16 v198, v198\n"
    "ds_write_b128 v242, v[120:123] offset:27712\n"
    "v_mfma_f32_16x16x32_bf16 v[120:123], a[8:11], v[188:191], 0\n"
    "v_or_b32_e64 v195, v195, v203\n"
    "v_cvt_pk_bf16_f32 v139, v142, v143\n"
    "v_pk_mul_f16 v232, v192, v232\n"
    "v_cvt_pk_f16_f32 v184, v168, v169\n"
    "v_exp_f16_e64 v234, v232\n"
    "v_mfma_f32_16x16x32_bf16 v[140:143], a[244:247], v[188:191], 0\n"
    "ds_read_u16_d16_hi v206, v206\n"
    "ds_write_b128 v242, v[136:139] offset:29760\n"
    "v_mfma_f32_16x16x32_bf16 v[136:139], v[236:239], v[188:191], 0\n"
    "v_exp_f16_sdwa v234, v232 dst_sel:WORD_1 dst_unused:UNUSED_PRESERVE src0_sel:WORD_1\n"
    "v_lshlrev_b32_sdwa v185, 1, v170 dst_sel:DWORD dst_unused:UNUSED_PAD src0_sel:DWORD src1_sel:WORD_1\n"
    "v_pk_fma_f16 v234, v234, s49, s49\n"
    "s_waitcnt lgkmcnt(4)\n"
    "v_or_b32_e32 v197, v197, v205\n"
    "v_lshlrev_b32_sdwa v201, 1, v171 dst_sel:DWORD dst_unused:UNUSED_PAD src0_sel:DWORD src1_sel:WORD_1\n"
    "v_mfma_f32_16x16x32_bf16 v[168:171], a[152:155], v[144:147], 0\n"
    "ds_read_u16 v199, v199\n"
    "ds_read_u16_d16_hi v207, v207\n"
    "v_mfma_f32_16x16x32_bf16 v[168:171], a[200:203], v[148:151], v[168:171]\n"
    "ds_read_u16 v185, v185\n"
    "ds_read_u16_d16_hi v201, v201\n"
    "v_lshlrev_b32_sdwa v186, 1, v172 dst_sel:DWORD dst_unused:UNUSED_PAD src0_sel:DWORD src1_sel:WORD_1\n"
    "v_lshlrev_b32_sdwa v202, 1, v173 dst_sel:DWORD dst_unused:UNUSED_PAD src0_sel:DWORD src1_sel:WORD_1\n"
    "v_lshlrev_b32_sdwa v187, 1, v174 dst_sel:DWORD dst_unused:UNUSED_PAD src0_sel:DWORD src1_sel:WORD_1\n"
    "v_lshlrev_b32_sdwa v203, 1, v175 dst_sel:DWORD dst_unused:UNUSED_PAD src0_sel:DWORD src1_sel:WORD_1\n"
    "v_rcp_f16_e64 v232, v234\n"
    "v_mfma_f32_16x16x32_bf16 v[168:171], a[112:115], v[152:155], v[168:171]\n"
    "ds_read_u16 v186, v186\n"
    "v_rcp_f16_sdwa v232, v234 dst_sel:WORD_1 dst_unused:UNUSED_PRESERVE src0_sel:WORD_1\n"
    "v_mfma_f32_16x16x32_bf16 v[172:175], a[156:159], v[144:147], 0\n"
    "ds_read_u16_d16_hi v202, v202\n"
    "v_cvt_pk_f16_f32 v188, v176, v177\n"
    "v_mfma_f32_16x16x32_bf16 v[172:175], a[204:207], v[148:151], v[172:175]\n"
    "v_lshlrev_b32_sdwa v189, 1, v178 dst_sel:DWORD dst_unused:UNUSED_PAD src0_sel:DWORD src1_sel:WORD_1\n"
    "v_lshlrev_b32_sdwa v205, 1, v179 dst_sel:DWORD dst_unused:UNUSED_PAD src0_sel:DWORD src1_sel:WORD_1\n"
    "s_waitcnt lgkmcnt(6)\n"
    "v_or_b32_e32 v198, v198, v206\n"
    "v_lshlrev_b32_sdwa v190, 1, v180 dst_sel:DWORD dst_unused:UNUSED_PAD src0_sel:DWORD src1_sel:WORD_1\n"
    "v_lshlrev_b32_sdwa v206, 1, v181 dst_sel:DWORD dst_unused:UNUSED_PAD src0_sel:DWORD src1_sel:WORD_1\n"
    "v_lshlrev_b32_sdwa v191, 1, v182 dst_sel:DWORD dst_unused:UNUSED_PAD src0_sel:DWORD src1_sel:WORD_1\n"
    "v_fma_mix_f32 v234, v192, v232, 0 op_sel_hi:[1,1,0]\n"
    "v_mfma_f32_16x16x32_bf16 v[172:175], a[116:119], v[152:155], v[172:175]\n"
    "s_barrier\n"
    "v_fma_mix_f32 v232, v192, v232, 0 op_sel:[1,1,0] op_sel_hi:[1,1,0]\n"
    "v_mfma_f32_16x16x32_bf16 v[176:179], a[152:155], v[156:159], 0\n"
    "ds_read_b128 v[236:239], v244 offset:4096\n"
    "ds_read_u16 v187, v187\n"
    "v_cvt_pk_bf16_f32 v192, v234, v232\n"
    "v_mfma_f32_16x16x32_bf16 v[176:179], a[200:203], v[160:163], v[176:179]\n"
    "s_waitcnt lgkmcnt(6)\n"
    "ds_read_u16_d16_hi v203, v203\n"
    "ds_read_b128 a[244:247], v244 offset:6144\n"
    "v_mfma_f32_16x16x32_bf16 v[176:179], a[112:115], v[164:167], v[176:179]\n"
    "ds_read_u16 v189, v189\n"
    "v_or_b32_e64 v199, v199, v207\n"
    "v_lshlrev_b32_sdwa v207, 1, v183 dst_sel:DWORD dst_unused:UNUSED_PAD src0_sel:DWORD src1_sel:WORD_1\n"
    "v_pk_fma_f16 v232, v196, v196, s48\n"
    "v_mfma_f32_16x16x32_bf16 v[180:183], a[156:159], v[156:159], 0\n"
    "ds_read_u16_d16_hi v205, v205\n"
    "v_pk_mul_f16 v232, v196, v232\n"
    "v_mfma_f32_16x16x32_bf16 v[180:183], a[204:207], v[160:163], v[180:183]\n"
    "s_waitcnt lgkmcnt(8)\n"
    "v_exp_f16_e32 v234, v232\n"
    "v_mfma_f32_16x16x32_bf16 v[180:183], a[116:119], v[164:167], v[180:183]\n"
    "ds_read_u16 v190, v190\n"
    "v_exp_f16_sdwa v234, v232 dst_sel:WORD_1 dst_unused:UNUSED_PRESERVE src0_sel:WORD_1\n"
    "v_mfma_f32_16x16x32_bf16 v[96:99], a[16:19], v[192:195], v[96:99]\n"
    "ds_read_u16_d16_hi v206, v206\n"
    "v_pk_fma_f16 v234, v234, s49, s49\n"
    "v_mfma_f32_16x16x32_bf16 v[100:103], a[20:23], v[192:195], v[100:103]\n"
    "v_rcp_f16_e32 v232, v234\n"
    "v_mfma_f32_16x16x32_bf16 v[112:115], a[24:27], v[192:195], v[112:115]\n"
    "s_waitcnt lgkmcnt(7)\n"
    "ds_read_u16 v191, v191\n"
    "v_rcp_f16_sdwa v232, v234 dst_sel:WORD_1 dst_unused:UNUSED_PRESERVE src0_sel:WORD_1\n"
    "v_mfma_f32_16x16x32_bf16 v[116:119], a[28:31], v[192:195], v[116:119]\n"
    "ds_read_u16_d16_hi v207, v207\n"
    "v_or_b32_e64 v185, v185, v201\n"
    "v_fma_mix_f32 v234, v196, v232, 0 op_sel_hi:[1,1,0]\n"
    "v_mfma_f32_16x16x32_bf16 v[128:131], v[236:239], v[192:195], v[128:131]\n"
    "v_fma_mix_f32 v232, v196, v232, 0 op_sel:[1,1,0] op_sel_hi:[1,1,0]\n"
    "v_or_b32_e64 v186, v186, v202\n"
    "v_cvt_pk_bf16_f32 v196, v234, v232\n"
    "v_pk_fma_f16 v232, v184, v184, s48\n"
    "s_waitcnt lgkmcnt(6)\n"
    "v_mfma_f32_16x16x32_bf16 v[132:135], a[244:247], v[192:195], v[132:135]\n"
    "s_nop 0\n"
    "ds_read_b128 a[240:243], v244 offset:34816\n"
    "v_or_b32_e64 v187, v187, v203\n"
    "v_mfma_f32_16x16x32_bf16 v[104:107], a[16:19], v[196:199], v[104:107]\n"
    "v_pk_mul_f16 v232, v184, v232\n"
    "v_lshlrev_b32_sdwa v192, 1, v168 dst_sel:DWORD dst_unused:UNUSED_PAD src0_sel:DWORD src1_sel:WORD_1\n"
    "v_mfma_f32_16x16x32_bf16 v[108:111], a[20:23], v[196:199], v[108:111]\n"
    "ds_read_u16 v192, v192\n"
    "v_exp_f16_e64 v234, v232\n"
    "v_mfma_f32_16x16x32_bf16 v[120:123], a[24:27], v[196:199], v[120:123]\n"
    "v_exp_f16_sdwa v234, v232 dst_sel:WORD_1 dst_unused:UNUSED_PRESERVE src0_sel:WORD_1\n"
    "v_mfma_f32_16x16x32_bf16 v[124:127], a[28:31], v[196:199], v[124:127]\n"
    "s_waitcnt lgkmcnt(6)\n"
    "v_or_b32_e32 v189, v189, v205\n"
    "v_pk_fma_f16 v234, v234, s49, s49\n"
    "v_mfma_f32_16x16x32_bf16 v[136:139], v[236:239], v[196:199], v[136:139]\n"
    "ds_read_b128 v[236:239], v244 offset:8192\n"
    "v_lshlrev_b32_sdwa v200, 1, v169 dst_sel:DWORD dst_unused:UNUSED_PAD src0_sel:DWORD src1_sel:WORD_1\n"
    "v_mfma_f32_16x16x32_bf16 v[140:143], a[244:247], v[196:199], v[140:143]\n"
    "ds_read_u16_d16_hi v200, v200\n"
    "v_lshlrev_b32_sdwa v193, 1, v170 dst_sel:DWORD dst_unused:UNUSED_PAD src0_sel:DWORD src1_sel:WORD_1\n"
    "v_lshlrev_b32_sdwa v201, 1, v171 dst_sel:DWORD dst_unused:UNUSED_PAD src0_sel:DWORD src1_sel:WORD_1\n"
    "v_mfma_f32_16x16x32_bf16 v[168:171], a[160:163], v[144:147], 0\n"
    "s_waitcnt lgkmcnt(6)\n"
    "ds_read_u16 v193, v193\n"
    "ds_read_u16_d16_hi v201, v201\n"
    "v_mfma_f32_16x16x32_bf16 v[168:171], a[208:211], v[148:151], v[168:171]\n"
    "s_nop 0\n"
    "v_lshlrev_b32_sdwa v194, 1, v172 dst_sel:DWORD dst_unused:UNUSED_PAD src0_sel:DWORD src1_sel:WORD_1\n"
    "v_lshlrev_b32_sdwa v202, 1, v173 dst_sel:DWORD dst_unused:UNUSED_PAD src0_sel:DWORD src1_sel:WORD_1\n"
    "v_lshlrev_b32_sdwa v195, 1, v174 dst_sel:DWORD dst_unused:UNUSED_PAD src0_sel:DWORD src1_sel:WORD_1\n"
    "v_lshlrev_b32_sdwa v203, 1, v175 dst_sel:DWORD dst_unused:UNUSED_PAD src0_sel:DWORD src1_sel:WORD_1\n"
    "v_rcp_f16_e64 v232, v234\n"
    "v_mfma_f32_16x16x32_bf16 v[168:171], a[120:123], v[152:155], v[168:171]\n"
    "ds_read_u16 v194, v194\n"
    "ds_read_u16_d16_hi v202, v202\n"
    "v_or_b32_e32 v190, v190, v206\n"
    "v_rcp_f16_sdwa v232, v234 dst_sel:WORD_1 dst_unused:UNUSED_PRESERVE src0_sel:WORD_1\n"
    "v_mfma_f32_16x16x32_bf16 v[172:175], a[164:167], v[144:147], 0\n"
    "s_waitcnt lgkmcnt(7)\n"
    "ds_read_u16 v195, v195\n"
    "v_lshlrev_b32_sdwa v196, 1, v176 dst_sel:DWORD dst_unused:UNUSED_PAD src0_sel:DWORD src1_sel:WORD_1\n"
    "v_lshlrev_b32_sdwa v204, 1, v177 dst_sel:DWORD dst_unused:UNUSED_PAD src0_sel:DWORD src1_sel:WORD_1\n"
    "v_lshlrev_b32_sdwa v197, 1, v178 dst_sel:DWORD dst_unused:UNUSED_PAD src0_sel:DWORD src1_sel:WORD_1\n"
    "v_lshlrev_b32_sdwa v205, 1, v179 dst_sel:DWORD dst_unused:UNUSED_PAD src0_sel:DWORD src1_sel:WORD_1\n"
    "v_lshlrev_b32_sdwa v198, 1, v180 dst_sel:DWORD dst_unused:UNUSED_PAD src0_sel:DWORD src1_sel:WORD_1\n"
    "v_lshlrev_b32_sdwa v206, 1, v181 dst_sel:DWORD dst_unused:UNUSED_PAD src0_sel:DWORD src1_sel:WORD_1\n"
    "v_lshlrev_b32_sdwa v199, 1, v182 dst_sel:DWORD dst_unused:UNUSED_PAD src0_sel:DWORD src1_sel:WORD_1\n"
    "s_waitcnt vmcnt(1)\n"
    "v_smfmac_f32_32x32x32_bf16 v[112:127], a[248:251], a[232:239], v241\n"
    "s_nop 0\n"
    "ds_read_b128 a[232:235], v243 offset:24640\n"
    "ds_read_b128 a[236:239], v243 offset:25664\n"
    "v_fma_mix_f32 v234, v184, v232, 0 op_sel_hi:[1,1,0]\n"
    "v_or_b32_e64 v191, v191, v207\n"
    "v_lshlrev_b32_sdwa v207, 1, v183 dst_sel:DWORD dst_unused:UNUSED_PAD src0_sel:DWORD src1_sel:WORD_1\n"
    "s_waitcnt vmcnt(0)\n"
    "v_smfmac_f32_32x32x32_bf16 v[96:111], a[248:251], a[224:231], v241\n"
    "s_waitcnt lgkmcnt(7)\n"
    "buffer_load_dwordx4 a[224:227], v245, s[40:43], s57 offen offset:128\n"
    "ds_read_b128 a[244:247], v244 offset:10240\n"
    "v_fma_mix_f32 v232, v184, v232, 0 op_sel:[1,1,0] op_sel_hi:[1,1,0]\n"
    "v_or_b32_e64 v192, v192, v200\n"
    "v_cvt_pk_bf16_f32 v184, v234, v232\n"
    "v_mfma_f32_16x16x32_bf16 v[172:175], a[212:215], v[148:151], v[172:175]\n"
    "ds_read_u16_d16_hi v203, v203\n"
    "ds_read_u16 v196, v196\n"
    "v_mfma_f32_16x16x32_bf16 v[172:175], a[240:243], v[152:155], v[172:175]\n"
    "ds_read_u16_d16_hi v204, v204\n"
    "v_pk_fma_f16 v232, v188, v188, s48\n"
    "v_mfma_f32_16x16x32_bf16 v[176:179], a[160:163], v[156:159], 0\n"
    "s_waitcnt lgkmcnt(9)\n"
    "ds_read_u16 v197, v197\n"
    "v_pk_mul_f16 v232, v188, v232\n"
    "v_mfma_f32_16x16x32_bf16 v[176:179], a[208:211], v[160:163], v[176:179]\n"
    "v_exp_f16_e32 v234, v232\n"
    "v_mfma_f32_16x16x32_bf16 v[176:179], a[120:123], v[164:167], v[176:179]\n"
    "ds_read_u16_d16_hi v205, v205\n"
    "v_exp_f16_sdwa v234, v232 dst_sel:WORD_1 dst_unused:UNUSED_PRESERVE src0_sel:WORD_1\n"
    "v_mfma_f32_16x16x32_bf16 v[180:183], a[164:167], v[156:159], 0\n"
    "s_waitcnt lgkmcnt(9)\n"
    "v_pk_fma_f16 v234, v234, s49, s49\n"
    "v_or_b32_e32 v193, v193, v201\n"
    "v_mfma_f32_16x16x32_bf16 v[180:183], a[212:215], v[160:163], v[180:183]\n"
    "ds_read_u16 v198, v198\n"
    "v_rcp_f16_e32 v232, v234\n"
    "s_waitcnt lgkmcnt(7)\n"
    "v_smfmac_f32_32x32x32_bf16 v[96:111], a[248:251], a[232:239], v241\n"
    "buffer_load_dwordx4 a[228:231], v245, s[40:43], s100 offen offset:128\n"
    "ds_read_u16_d16_hi v206, v206\n"
    "v_rcp_f16_sdwa v232, v234 dst_sel:WORD_1 dst_unused:UNUSED_PRESERVE src0_sel:WORD_1\n"
    "v_lshlrev_b32_sdwa v201, 1, v171 dst_sel:DWORD dst_unused:UNUSED_PAD src0_sel:DWORD src1_sel:WORD_1\n"
    "v_mfma_f32_16x16x32_bf16 v[180:183], a[240:243], v[164:167], v[180:183]\n"
    "s_nop 0\n"
    "ds_read_u16 v199, v199\n"
    "v_fma_mix_f32 v234, v188, v232, 0 op_sel_hi:[1,1,0]\n"
    "v_mfma_f32_16x16x32_bf16 v[112:115], a[40:43], v[184:187], v[112:115]\n"
    "s_waitcnt lgkmcnt(7)\n"
    "ds_read_u16_d16_hi v207, v207\n"
    "v_fma_mix_f32 v232, v188, v232, 0 op_sel:[1,1,0] op_sel_hi:[1,1,0]\n"
    "v_mfma_f32_16x16x32_bf16 v[96:99], a[32:35], v[184:187], v[96:99]\n"
    "v_cvt_pk_bf16_f32 v188, v234, v232\n"
    "v_or_b32_e64 v194, v194, v202\n"
    "v_mfma_f32_16x16x32_bf16 v[100:103], a[36:39], v[184:187], v[100:103]\n"
    "ds_read_b128 a[232:235], v243 offset:26688\n"
    "v_lshlrev_b32_sdwa v202, 1, v173 dst_sel:DWORD dst_unused:UNUSED_PAD src0_sel:DWORD src1_sel:WORD_1\n"
    "v_mfma_f32_16x16x32_bf16 v[116:119], a[44:47], v[184:187], v[116:119]\n"
    "ds_read_b128 a[236:239], v243 offset:27712\n"
    "v_or_b32_e32 v195, v195, v203\n"
    "v_mfma_f32_16x16x32_bf16 v[128:131], v[236:239], v[184:187], v[128:131]\n"
    "s_waitcnt lgkmcnt(8)\n"
    "ds_read_b128 a[240:243], v244 offset:38912\n"
    "v_lshlrev_b32_sdwa v203, 1, v175 dst_sel:DWORD dst_unused:UNUSED_PAD src0_sel:DWORD src1_sel:WORD_1\n"
    "v_mfma_f32_16x16x32_bf16 v[132:135], a[244:247], v[184:187], v[132:135]\n"
    "ds_read_u16_d16_hi v201, v201\n"
    "v_cvt_pk_f16_f32 v184, v168, v169\n"
    "v_mfma_f32_16x16x32_bf16 v[104:107], a[32:35], v[188:191], v[104:107]\n"
    "v_lshlrev_b32_sdwa v185, 1, v170 dst_sel:DWORD dst_unused:UNUSED_PAD src0_sel:DWORD src1_sel:WORD_1\n"
    "v_lshlrev_b32_sdwa v186, 1, v172 dst_sel:DWORD dst_unused:UNUSED_PAD src0_sel:DWORD src1_sel:WORD_1\n"
    "v_mfma_f32_16x16x32_bf16 v[108:111], a[36:39], v[188:191], v[108:111]\n"
    "s_waitcnt lgkmcnt(8)\n"
    "ds_read_u16 v185, v185\n"
    "v_lshlrev_b32_sdwa v187, 1, v174 dst_sel:DWORD dst_unused:UNUSED_PAD src0_sel:DWORD src1_sel:WORD_1\n"
    "v_mfma_f32_16x16x32_bf16 v[120:123], a[40:43], v[188:191], v[120:123]\n"
    "v_or_b32_e32 v196, v196, v204\n"
    "v_or_b32_e64 v197, v197, v205\n"
    "v_mfma_f32_16x16x32_bf16 v[124:127], a[44:47], v[188:191], v[124:127]\n"
    "ds_read_u16 v186, v186\n"
    "v_lshlrev_b32_sdwa v205, 1, v179 dst_sel:DWORD dst_unused:UNUSED_PAD src0_sel:DWORD src1_sel:WORD_1\n"
    "v_mfma_f32_16x16x32_bf16 v[136:139], v[236:239], v[188:191], v[136:139]\n"
    "s_nop 0\n"
    "ds_read_u16_d16_hi v202, v202\n"
    "v_pk_fma_f16 v232, v184, v184, s48\n"
    "v_mfma_f32_16x16x32_bf16 v[140:143], a[244:247], v[188:191], v[140:143]\n"
    "s_waitcnt lgkmcnt(9)\n"
    "v_cvt_pk_f16_f32 v188, v176, v177\n"
    "v_lshlrev_b32_sdwa v189, 1, v178 dst_sel:DWORD dst_unused:UNUSED_PAD src0_sel:DWORD src1_sel:WORD_1\n"
    "v_mfma_f32_16x16x32_bf16 v[168:171], a[168:171], v[144:147], 0\n"
    "ds_read_b128 v[236:239], v244 offset:12288\n"
    "v_lshlrev_b32_sdwa v190, 1, v180 dst_sel:DWORD dst_unused:UNUSED_PAD src0_sel:DWORD src1_sel:WORD_1\n"
    "v_mfma_f32_16x16x32_bf16 v[168:171], a[216:219], v[148:151], v[168:171]\n"
    "ds_read_u16 v187, v187\n"
    "v_lshlrev_b32_sdwa v191, 1, v182 dst_sel:DWORD dst_unused:UNUSED_PAD src0_sel:DWORD src1_sel:WORD_1\n"
    "v_mfma_f32_16x16x32_bf16 v[168:171], a[124:127], v[152:155], v[168:171]\n"
    "s_waitcnt lgkmcnt(8)\n"
    "v_or_b32_e32 v198, v198, v206\n"
    "v_lshlrev_b32_sdwa v206, 1, v181 dst_sel:DWORD dst_unused:UNUSED_PAD src0_sel:DWORD src1_sel:WORD_1\n"
    "v_mfma_f32_16x16x32_bf16 v[172:175], a[172:175], v[144:147], 0\n"
    "s_waitcnt lgkmcnt(7)\n"
    "ds_read_u16_d16_hi v203, v203\n"
    "v_or_b32_e32 v199, v199, v207\n"
    "v_smfmac_f32_32x32x32_bf16 v[112:127], a[248:251], a[232:239], v241\n"
    "ds_read_b128 a[244:247], v244 offset:14336\n"
    "v_lshlrev_b32_sdwa v207, 1, v183 dst_sel:DWORD dst_unused:UNUSED_PAD src0_sel:DWORD src1_sel:WORD_1\n"
    "v_pk_mul_f16 v232, v184, v232\n"
    "v_exp_f16_e32 v234, v232\n"
    "v_mfma_f32_16x16x32_bf16 v[172:175], a[220:223], v[148:151], v[172:175]\n"
    "s_waitcnt lgkmcnt(8)\n"
    "ds_read_u16 v189, v189\n"
    "v_exp_f16_sdwa v234, v232 dst_sel:WORD_1 dst_unused:UNUSED_PRESERVE src0_sel:WORD_1\n"
    "v_mfma_f32_16x16x32_bf16 v[172:175], a[240:243], v[152:155], v[172:175]\n"
    "ds_read_b128 a[232:235], v243 offset:28736\n"
    "v_pk_fma_f16 v234, v234, s49, s49\n"
    "v_mfma_f32_16x16x32_bf16 v[176:179], a[168:171], v[156:159], 0\n"
    "ds_read_u16_d16_hi v205, v205\n"
    "s_waitcnt lgkmcnt(9)\n"
    "v_or_b32_e32 v185, v185, v201\n"
    "v_mfma_f32_16x16x32_bf16 v[176:179], a[216:219], v[160:163], v[176:179]\n"
    "v_lshlrev_b32_sdwa v201, 1, v171 dst_sel:DWORD dst_unused:UNUSED_PAD src0_sel:DWORD src1_sel:WORD_1\n"
    "v_rcp_f16_e64 v232, v234\n"
    "v_mfma_f32_16x16x32_bf16 v[176:179], a[124:127], v[164:167], v[176:179]\n"
    "ds_read_b128 a[236:239], v243 offset:29760\n"
    "ds_read_u16 v190, v190\n"
    "v_mfma_f32_16x16x32_bf16 v[180:183], a[172:175], v[156:159], 0\n"
    "s_waitcnt lgkmcnt(8)\n"
    "v_or_b32_e32 v186, v186, v202\n"
    "v_lshlrev_b32_sdwa v202, 1, v173 dst_sel:DWORD dst_unused:UNUSED_PAD src0_sel:DWORD src1_sel:WORD_1\n"
    "v_mfma_f32_16x16x32_bf16 v[180:183], a[220:223], v[160:163], v[180:183]\n"
    "ds_read_b128 v[144:147], v242 offset:0\n"
    "v_rcp_f16_sdwa v232, v234 dst_sel:WORD_1 dst_unused:UNUSED_PRESERVE src0_sel:WORD_1\n"
    "v_mfma_f32_16x16x32_bf16 v[180:183], a[240:243], v[164:167], v[180:183]\n"
    "ds_read_u16_d16_hi v206, v206\n"
    "v_fma_mix_f32 v234, v184, v232, 0 op_sel_hi:[1,1,0]\n"
    "v_mfma_f32_16x16x32_bf16 v[96:99], a[48:51], v[192:195], v[96:99]\n"
    "v_fma_mix_f32 v232, v184, v232, 0 op_sel:[1,1,0] op_sel_hi:[1,1,0]\n"
    "v_cvt_pk_bf16_f32 v184, v234, v232\n"
    "v_mfma_f32_16x16x32_bf16 v[100:103], a[52:55], v[192:195], v[100:103]\n"
    "ds_read_b128 v[148:151], v242 offset:1024\n"
    "ds_read_u16 v191, v191\n"
    "v_mfma_f32_16x16x32_bf16 v[112:115], a[56:59], v[192:195], v[112:115]\n"
    "s_waitcnt lgkmcnt(9)\n"
    "v_or_b32_e32 v187, v187, v203\n"
    "v_lshlrev_b32_sdwa v203, 1, v175 dst_sel:DWORD dst_unused:UNUSED_PAD src0_sel:DWORD src1_sel:WORD_1\n"
    "v_mfma_f32_16x16x32_bf16 v[116:119], a[60:63], v[192:195], v[116:119]\n"
    "ds_read_b128 v[152:155], v242 offset:2048\n"
    "v_pk_fma_f16 v232, v188, v188, s48\n"
    "v_mfma_f32_16x16x32_bf16 v[128:131], v[236:239], v[192:195], v[128:131]\n"
    "ds_read_u16_d16_hi v207, v207\n"
    "v_pk_mul_f16 v232, v188, v232\n"
    "v_mfma_f32_16x16x32_bf16 v[132:135], a[244:247], v[192:195], v[132:135]\n"
    "v_cvt_pk_f16_f32 v192, v168, v169\n"
    "v_lshlrev_b32_sdwa v193, 1, v170 dst_sel:DWORD dst_unused:UNUSED_PAD src0_sel:DWORD src1_sel:WORD_1\n"
    "v_mfma_f32_16x16x32_bf16 v[104:107], a[48:51], v[196:199], v[104:107]\n"
    "ds_read_b128 v[156:159], v242 offset:3072\n"
    "v_lshlrev_b32_sdwa v194, 1, v172 dst_sel:DWORD dst_unused:UNUSED_PAD src0_sel:DWORD src1_sel:WORD_1\n"
    "v_mfma_f32_16x16x32_bf16 v[108:111], a[52:55], v[196:199], v[108:111]\n"
    "s_waitcnt lgkmcnt(8)\n"
    "ds_read_b128 v[160:163], v242 offset:4096\n"
    "v_or_b32_e32 v189, v189, v205\n"
    "v_mfma_f32_16x16x32_bf16 v[120:123], a[56:59], v[196:199], v[120:123]\n"
    "ds_read_b128 v[164:167], v242 offset:5120\n"
    "v_lshlrev_b32_sdwa v195, 1, v174 dst_sel:DWORD dst_unused:UNUSED_PAD src0_sel:DWORD src1_sel:WORD_1\n"
    "v_mfma_f32_16x16x32_bf16 v[124:127], a[60:63], v[196:199], v[124:127]\n"
    "ds_read_b128 a[240:243], v244 offset:32768\n"
    "v_lshlrev_b32_sdwa v205, 1, v179 dst_sel:DWORD dst_unused:UNUSED_PAD src0_sel:DWORD src1_sel:WORD_1\n"
    "v_mfma_f32_16x16x32_bf16 v[136:139], v[236:239], v[196:199], v[136:139]\n"
    "s_waitcnt lgkmcnt(9)\n"
    "ds_read_u16 v193, v193\n"
    "v_exp_f16_e32 v234, v232\n"
    "v_mfma_f32_16x16x32_bf16 v[140:143], a[244:247], v[196:199], v[140:143]\n"
    "v_cvt_pk_f16_f32 v196, v176, v177\n"
    "v_lshlrev_b32_sdwa v197, 1, v178 dst_sel:DWORD dst_unused:UNUSED_PAD src0_sel:DWORD src1_sel:WORD_1\n"
    "v_mfma_f32_16x16x32_bf16 v[168:171], a[128:131], v[144:147], 0\n"
    "s_waitcnt lgkmcnt(8)\n"
    "ds_read_u16_d16_hi v201, v201\n"
    "v_or_b32_e32 v190, v190, v206\n"
    "v_smfmac_f32_32x32x32_bf16 v[128:143], a[248:251], a[232:239], v241\n"
    "ds_read_u16 v194, v194\n"
    "v_lshlrev_b32_sdwa v198, 1, v180 dst_sel:DWORD dst_unused:UNUSED_PAD src0_sel:DWORD src1_sel:WORD_1\n"
    "v_lshlrev_b32_sdwa v206, 1, v181 dst_sel:DWORD dst_unused:UNUSED_PAD src0_sel:DWORD src1_sel:WORD_1\n"
    "v_lshlrev_b32_sdwa v199, 1, v182 dst_sel:DWORD dst_unused:UNUSED_PAD src0_sel:DWORD src1_sel:WORD_1\n"
    "v_exp_f16_sdwa v234, v232 dst_sel:WORD_1 dst_unused:UNUSED_PRESERVE src0_sel:WORD_1\n"
    "v_mfma_f32_16x16x32_bf16 v[168:171], a[176:179], v[148:151], v[168:171]\n"
    "s_waitcnt lgkmcnt(8)\n"
    "ds_read_u16_d16_hi v202, v202\n"
    "v_pk_fma_f16 v234, v234, s49, s49\n"
    "v_mfma_f32_16x16x32_bf16 v[168:171], a[96:99], v[152:155], v[168:171]\n"
    "v_rcp_f16_e32 v232, v234\n"
    "v_mfma_f32_16x16x32_bf16 v[172:175], a[132:135], v[144:147], 0\n"
    "ds_read_b128 v[236:239], v244 offset:16384\n"
    "ds_read_u16 v195, v195\n"
    "v_mfma_f32_16x16x32_bf16 v[172:175], a[180:183], v[148:151], v[172:175]\n"
    "s_waitcnt lgkmcnt(9)\n"
    "v_or_b32_e32 v191, v191, v207\n"
    "v_lshlrev_b32_sdwa v207, 1, v183 dst_sel:DWORD dst_unused:UNUSED_PAD src0_sel:DWORD src1_sel:WORD_1\n"
    "v_mfma_f32_16x16x32_bf16 v[176:179], a[128:131], v[156:159], 0\n"
    "s_waitcnt lgkmcnt(8)\n"
    "ds_read_u16_d16_hi v203, v203\n"
    "v_rcp_f16_sdwa v232, v234 dst_sel:WORD_1 dst_unused:UNUSED_PRESERVE src0_sel:WORD_1\n"
    "v_mfma_f32_16x16x32_bf16 v[176:179], a[176:179], v[160:163], v[176:179]\n"
    "s_waitcnt lgkmcnt(7)\n"
    "v_fma_mix_f32 v234, v188, v232, 0 op_sel_hi:[1,1,0]\n"
    "v_fma_mix_f32 v232, v188, v232, 0 op_sel:[1,1,0] op_sel_hi:[1,1,0]\n"
    "v_mfma_f32_16x16x32_bf16 v[172:175], a[240:243], v[152:155], v[172:175]\n"
    "ds_read_b128 a[244:247], v244 offset:18432\n"
    "v_cvt_pk_bf16_f32 v188, v234, v232\n"
    "v_mfma_f32_16x16x32_bf16 v[176:179], a[96:99], v[164:167], v[176:179]\n"
    "ds_read_u16 v197, v197\n"
    "v_pk_fma_f16 v232, v192, v192, s48\n"
    "v_mfma_f32_16x16x32_bf16 v[180:183], a[132:135], v[156:159], 0\n"
    "v_pk_mul_f16 v232, v192, v232\n"
    "v_lshlrev_b32_sdwa v200, 1, v169 dst_sel:DWORD dst_unused:UNUSED_PAD src0_sel:DWORD src1_sel:WORD_1\n"
    "v_mfma_f32_16x16x32_bf16 v[180:183], a[180:183], v[160:163], v[180:183]\n"
    "ds_read_u16_d16_hi v205, v205\n"
    "v_exp_f16_e32 v234, v232\n"
    "v_mfma_f32_16x16x32_bf16 v[180:183], a[240:243], v[164:167], v[180:183]\n"
    "s_waitcnt lgkmcnt(8)\n"
    "ds_read_u16 v198, v198\n"
    "v_or_b32_e64 v193, v193, v201\n"
    "v_mfma_f32_16x16x32_bf16 v[96:99], a[64:67], v[184:187], v[96:99]\n"
    "v_exp_f16_sdwa v234, v232 dst_sel:WORD_1 dst_unused:UNUSED_PRESERVE src0_sel:WORD_1\n"
    "v_mfma_f32_16x16x32_bf16 v[100:103], a[68:71], v[184:187], v[100:103]\n"
    "ds_read_u16_d16_hi v206, v206\n"
    "v_pk_fma_f16 v234, v234, s49, s49\n"
    "v_mfma_f32_16x16x32_bf16 v[112:115], a[72:75], v[184:187], v[112:115]\n"
    "s_waitcnt lgkmcnt(7)\n"
    "v_or_b32_e32 v194, v194, v202\n"
    "v_lshlrev_b32_sdwa v201, 1, v171 dst_sel:DWORD dst_unused:UNUSED_PAD src0_sel:DWORD src1_sel:WORD_1\n"
    "v_mfma_f32_16x16x32_bf16 v[116:119], a[76:79], v[184:187], v[116:119]\n"
    "ds_read_u16 v199, v199\n"
    "v_lshlrev_b32_sdwa v202, 1, v173 dst_sel:DWORD dst_unused:UNUSED_PAD src0_sel:DWORD src1_sel:WORD_1\n"
    "v_mfma_f32_16x16x32_bf16 v[128:131], v[236:239], v[184:187], v[128:131]\n"
    "ds_read_u16_d16_hi v207, v207\n"
    "v_rcp_f16_e32 v232, v234\n"
    "v_mfma_f32_16x16x32_bf16 v[104:107], a[64:67], v[188:191], v[104:107]\n"
    "v_rcp_f16_sdwa v232, v234 dst_sel:WORD_1 dst_unused:UNUSED_PRESERVE src0_sel:WORD_1\n"
    "v_mfma_f32_16x16x32_bf16 v[108:111], a[68:71], v[188:191], v[108:111]\n"
    "s_waitcnt lgkmcnt(6)\n"
    "ds_read_b128 a[240:243], v244 offset:36864\n"
    "v_or_b32_e64 v195, v195, v203\n"
    "v_mfma_f32_16x16x32_bf16 v[132:135], a[244:247], v[184:187], v[132:135]\n"
    "ds_read_u16_d16_hi v200, v200\n"
    "v_lshlrev_b32_sdwa v184, 1, v168 dst_sel:DWORD dst_unused:UNUSED_PAD src0_sel:DWORD src1_sel:WORD_1\n"
    "v_mfma_f32_16x16x32_bf16 v[120:123], a[72:75], v[188:191], v[120:123]\n"
    "v_lshlrev_b32_sdwa v185, 1, v170 dst_sel:DWORD dst_unused:UNUSED_PAD src0_sel:DWORD src1_sel:WORD_1\n"
    "v_lshlrev_b32_sdwa v186, 1, v172 dst_sel:DWORD dst_unused:UNUSED_PAD src0_sel:DWORD src1_sel:WORD_1\n"
    "v_mfma_f32_16x16x32_bf16 v[124:127], a[76:79], v[188:191], v[124:127]\n"
    "ds_read_u16 v184, v184\n"
    "v_lshlrev_b32_sdwa v187, 1, v174 dst_sel:DWORD dst_unused:UNUSED_PAD src0_sel:DWORD src1_sel:WORD_1\n"
    "v_mfma_f32_16x16x32_bf16 v[136:139], v[236:239], v[188:191], v[136:139]\n"
    "ds_read_u16 v185, v185\n"
    "v_lshlrev_b32_sdwa v203, 1, v175 dst_sel:DWORD dst_unused:UNUSED_PAD src0_sel:DWORD src1_sel:WORD_1\n"
    "v_mfma_f32_16x16x32_bf16 v[140:143], a[244:247], v[188:191], v[140:143]\n"
    "s_waitcnt lgkmcnt(8)\n"
    "v_or_b32_e32 v197, v197, v205\n"
    "v_lshlrev_b32_sdwa v188, 1, v176 dst_sel:DWORD dst_unused:UNUSED_PAD src0_sel:DWORD src1_sel:WORD_1\n"
    "v_mfma_f32_16x16x32_bf16 v[168:171], a[136:139], v[144:147], 0\n"
    "ds_read_u16_d16_hi v201, v201\n"
    "v_lshlrev_b32_sdwa v204, 1, v177 dst_sel:DWORD dst_unused:UNUSED_PAD src0_sel:DWORD src1_sel:WORD_1\n"
    "v_mfma_f32_16x16x32_bf16 v[168:171], a[184:187], v[148:151], v[168:171]\n"
    "v_lshlrev_b32_sdwa v189, 1, v178 dst_sel:DWORD dst_unused:UNUSED_PAD src0_sel:DWORD src1_sel:WORD_1\n"
    "v_lshlrev_b32_sdwa v205, 1, v179 dst_sel:DWORD dst_unused:UNUSED_PAD src0_sel:DWORD src1_sel:WORD_1\n"
    "v_mfma_f32_16x16x32_bf16 v[168:171], a[100:103], v[152:155], v[168:171]\n"
    "ds_read_u16 v186, v186\n"
    "s_waitcnt lgkmcnt(8)\n"
    "v_or_b32_e32 v198, v198, v206\n"
    "v_mfma_f32_16x16x32_bf16 v[172:175], a[140:143], v[144:147], 0\n"
    "ds_read_b128 v[236:239], v244 offset:20480\n"
    "v_lshlrev_b32_sdwa v190, 1, v180 dst_sel:DWORD dst_unused:UNUSED_PAD src0_sel:DWORD src1_sel:WORD_1\n"
    "v_lshlrev_b32_sdwa v206, 1, v181 dst_sel:DWORD dst_unused:UNUSED_PAD src0_sel:DWORD src1_sel:WORD_1\n"
    "v_lshlrev_b32_sdwa v191, 1, v182 dst_sel:DWORD dst_unused:UNUSED_PAD src0_sel:DWORD src1_sel:WORD_1\n"
    "v_fma_mix_f32 v234, v192, v232, 0 op_sel_hi:[1,1,0]\n"
    "v_mfma_f32_16x16x32_bf16 v[172:175], a[188:191], v[148:151], v[172:175]\n"
    "ds_read_u16_d16_hi v202, v202\n"
    "v_fma_mix_f32 v232, v192, v232, 0 op_sel:[1,1,0] op_sel_hi:[1,1,0]\n"
    "s_waitcnt lgkmcnt(7)\n"
    "v_mfma_f32_16x16x32_bf16 v[172:175], a[240:243], v[152:155], v[172:175]\n"
    "s_nop 0\n"
    "ds_read_u16 v187, v187\n"
    "v_or_b32_e64 v199, v199, v207\n"
    "v_mfma_f32_16x16x32_bf16 v[176:179], a[136:139], v[156:159], 0\n"
    "v_lshlrev_b32_sdwa v207, 1, v183 dst_sel:DWORD dst_unused:UNUSED_PAD src0_sel:DWORD src1_sel:WORD_1\n"
    "v_cvt_pk_bf16_f32 v192, v234, v232\n"
    "v_mfma_f32_16x16x32_bf16 v[176:179], a[184:187], v[160:163], v[176:179]\n"
    "ds_read_b128 a[244:247], v244 offset:22528\n"
    "v_pk_fma_f16 v232, v196, v196, s48\n"
    "v_mfma_f32_16x16x32_bf16 v[176:179], a[100:103], v[164:167], v[176:179]\n"
    "ds_read_u16_d16_hi v203, v203\n"
    "v_pk_mul_f16 v232, v196, v232\n"
    "v_mfma_f32_16x16x32_bf16 v[180:183], a[140:143], v[156:159], 0\n"
    "s_waitcnt lgkmcnt(8)\n"
    "v_exp_f16_e32 v234, v232\n"
    "v_mfma_f32_16x16x32_bf16 v[180:183], a[188:191], v[160:163], v[180:183]\n"
    "ds_read_u16 v188, v188\n"
    "v_exp_f16_sdwa v234, v232 dst_sel:WORD_1 dst_unused:UNUSED_PRESERVE src0_sel:WORD_1\n"
    "v_mfma_f32_16x16x32_bf16 v[180:183], a[240:243], v[164:167], v[180:183]\n"
    "ds_read_u16_d16_hi v204, v204\n"
    "v_pk_fma_f16 v234, v234, s49, s49\n"
    "v_mfma_f32_16x16x32_bf16 v[96:99], a[80:83], v[192:195], v[96:99]\n"
    "v_rcp_f16_e32 v232, v234\n"
    "v_mfma_f32_16x16x32_bf16 v[100:103], a[84:87], v[192:195], v[100:103]\n"
    "s_waitcnt lgkmcnt(8)\n"
    "ds_read_u16 v189, v189\n"
    "v_rcp_f16_sdwa v232, v234 dst_sel:WORD_1 dst_unused:UNUSED_PRESERVE src0_sel:WORD_1\n"
    "v_mfma_f32_16x16x32_bf16 v[112:115], a[88:91], v[192:195], v[112:115]\n"
    "ds_read_u16_d16_hi v205, v205\n"
    "v_fma_mix_f32 v234, v196, v232, 0 op_sel_hi:[1,1,0]\n"
    "v_mfma_f32_16x16x32_bf16 v[116:119], a[92:95], v[192:195], v[116:119]\n"
    "v_fma_mix_f32 v232, v196, v232, 0 op_sel:[1,1,0] op_sel_hi:[1,1,0]\n"
    "v_or_b32_e64 v184, v184, v200\n"
    "v_cvt_pk_bf16_f32 v196, v234, v232\n"
    "v_or_b32_e64 v185, v185, v201\n"
    "v_lshlrev_b32_sdwa v201, 1, v171 dst_sel:DWORD dst_unused:UNUSED_PAD src0_sel:DWORD src1_sel:WORD_1\n"
    "v_mfma_f32_16x16x32_bf16 v[104:107], a[80:83], v[196:199], v[104:107]\n"
    "s_waitcnt lgkmcnt(7)\n"
    "ds_read_u16 v190, v190\n"
    "v_or_b32_e32 v186, v186, v202\n"
    "v_mfma_f32_16x16x32_bf16 v[108:111], a[84:87], v[196:199], v[108:111]\n"
    "ds_read_u16_d16_hi v206, v206\n"
    "v_lshlrev_b32_sdwa v202, 1, v173 dst_sel:DWORD dst_unused:UNUSED_PAD src0_sel:DWORD src1_sel:WORD_1\n"
    "v_mfma_f32_16x16x32_bf16 v[120:123], a[88:91], v[196:199], v[120:123]\n"
    "v_cvt_pk_bf16_f32 v96, v96, v97\n"
    "v_cvt_pk_bf16_f32 v97, v98, v99\n"
    "v_mfma_f32_16x16x32_bf16 v[124:127], a[92:95], v[196:199], v[124:127]\n"
    "ds_read_u16 v191, v191\n"
    "v_cvt_pk_bf16_f32 v98, v100, v101\n"
    "v_cvt_pk_bf16_f32 v99, v102, v103\n"
    "v_cvt_pk_bf16_f32 v112, v112, v113\n"
    "v_cvt_pk_bf16_f32 v113, v114, v115\n"
    "s_waitcnt lgkmcnt(7)\n"
    "v_or_b32_e32 v187, v187, v203\n"
    "v_lshlrev_b32_sdwa v203, 1, v175 dst_sel:DWORD dst_unused:UNUSED_PAD src0_sel:DWORD src1_sel:WORD_1\n"
    "v_cvt_pk_bf16_f32 v114, v116, v117\n"
    "v_cvt_pk_bf16_f32 v115, v118, v119\n"
    "v_cvt_pk_bf16_f32 v104, v104, v105\n"
    "v_cvt_pk_bf16_f32 v105, v106, v107\n"
    "v_cvt_pk_bf16_f32 v106, v108, v109\n"
    "s_waitcnt vmcnt(0)\n"
    "v_smfmac_f32_32x32x32_bf16 v[128:143], a[248:251], a[224:231], v241\n"
    "s_nop 0\n"
    "ds_read_u16_d16_hi v207, v207\n"
    "ds_write_b128 v243, v[96:99] offset:24640\n"
    "s_waitcnt lgkmcnt(7)\n"
    "v_or_b32_e32 v188, v188, v204\n"
    "v_cvt_pk_bf16_f32 v107, v110, v111\n"
    "v_cvt_pk_bf16_f32 v120, v120, v121\n"
    "v_cvt_pk_bf16_f32 v121, v122, v123\n"
    "v_cvt_pk_bf16_f32 v122, v124, v125\n"
    "v_cvt_pk_bf16_f32 v123, v126, v127\n"
    "ds_write_b128 v243, v[112:115] offset:26688\n"
    "v_mfma_f32_16x16x32_bf16 v[128:131], v[236:239], v[192:195], v[128:131]\n"
    "s_waitcnt lgkmcnt(5)\n"
    "v_or_b32_e32 v189, v189, v205\n"
    "v_lshlrev_b32_sdwa v205, 1, v179 dst_sel:DWORD dst_unused:UNUSED_PAD src0_sel:DWORD src1_sel:WORD_1\n"
    "v_mfma_f32_16x16x32_bf16 v[132:135], a[244:247], v[192:195], v[132:135]\n"
    "s_nop 0\n"
    "ds_read_u16_d16_hi v201, v201\n"
    "ds_write_b128 v243, v[104:107] offset:25664\n"
    "v_mfma_f32_16x16x32_bf16 v[136:139], v[236:239], v[196:199], v[136:139]\n"
    "s_waitcnt lgkmcnt(6)\n"
    "v_cvt_pk_f16_f32 v192, v168, v169\n"
    "v_lshlrev_b32_sdwa v193, 1, v170 dst_sel:DWORD dst_unused:UNUSED_PAD src0_sel:DWORD src1_sel:WORD_1\n"
    "v_mfma_f32_16x16x32_bf16 v[140:143], a[244:247], v[196:199], v[140:143]\n"
    "v_lshlrev_b32_sdwa v194, 1, v172 dst_sel:DWORD dst_unused:UNUSED_PAD src0_sel:DWORD src1_sel:WORD_1\n"
    "v_lshlrev_b32_sdwa v195, 1, v174 dst_sel:DWORD dst_unused:UNUSED_PAD src0_sel:DWORD src1_sel:WORD_1\n"
    "v_mfma_f32_16x16x32_bf16 v[168:171], a[144:147], v[144:147], 0\n"
    "s_waitcnt lgkmcnt(3)\n"
    "ds_read_u16 v193, v193\n"
    "ds_write_b128 v243, v[120:123] offset:27712\n"
    "v_mfma_f32_16x16x32_bf16 v[168:171], a[192:195], v[148:151], v[168:171]\n"
    "s_nop 0\n"
    "v_cvt_pk_f16_f32 v196, v176, v177\n"
    "v_lshlrev_b32_sdwa v197, 1, v178 dst_sel:DWORD dst_unused:UNUSED_PAD src0_sel:DWORD src1_sel:WORD_1\n"
    "v_mfma_f32_16x16x32_bf16 v[168:171], a[104:107], v[152:155], v[168:171]\n"
    "v_lshlrev_b32_sdwa v198, 1, v180 dst_sel:DWORD dst_unused:UNUSED_PAD src0_sel:DWORD src1_sel:WORD_1\n"
    "v_lshlrev_b32_sdwa v199, 1, v182 dst_sel:DWORD dst_unused:UNUSED_PAD src0_sel:DWORD src1_sel:WORD_1\n"
    "v_mfma_f32_16x16x32_bf16 v[172:175], a[148:151], v[144:147], 0\n"
    "ds_read_u16 v194, v194\n"
    "ds_read_u16_d16_hi v202, v202\n"
    "v_mfma_f32_16x16x32_bf16 v[172:175], a[196:199], v[148:151], v[172:175]\n"
    "ds_read_b128 v[236:239], v244 offset:0\n"
    "ds_read_u16 v195, v195\n"
    "v_mfma_f32_16x16x32_bf16 v[172:175], a[108:111], v[152:155], v[172:175]\n"
    "v_or_b32_e64 v190, v190, v206\n"
    "v_lshlrev_b32_sdwa v206, 1, v181 dst_sel:DWORD dst_unused:UNUSED_PAD src0_sel:DWORD src1_sel:WORD_1\n"
    "v_mfma_f32_16x16x32_bf16 v[176:179], a[144:147], v[156:159], 0\n"
    "ds_read_u16_d16_hi v203, v203\n"
    "v_cvt_pk_bf16_f32 v128, v128, v129\n"
    "v_mfma_f32_16x16x32_bf16 v[176:179], a[192:195], v[160:163], v[176:179]\n"
    "ds_read_b128 a[244:247], v244 offset:2048\n"
    "v_cvt_pk_bf16_f32 v129, v130, v131\n"
    "v_mfma_f32_16x16x32_bf16 v[176:179], a[104:107], v[164:167], v[176:179]\n"
    "ds_read_u16 v197, v197\n"
    "v_or_b32_e32 v191, v191, v207\n"
    "v_mfma_f32_16x16x32_bf16 v[96:99], a[0:3], v[184:187], 0\n"
    "s_waitcnt lgkmcnt(7)\n"
    "v_lshlrev_b32_sdwa v207, 1, v183 dst_sel:DWORD dst_unused:UNUSED_PAD src0_sel:DWORD src1_sel:WORD_1\n"
    "v_cvt_pk_bf16_f32 v130, v132, v133\n"
    "v_mfma_f32_16x16x32_bf16 v[180:183], a[148:151], v[156:159], 0\n"
    "ds_read_u16_d16_hi v205, v205\n"
    "v_or_b32_e64 v193, v193, v201\n"
    "v_mfma_f32_16x16x32_bf16 v[180:183], a[196:199], v[160:163], v[180:183]\n"
    "v_cvt_pk_bf16_f32 v131, v134, v135\n"
    "v_pk_fma_f16 v232, v192, v192, s48\n"
    "v_mfma_f32_16x16x32_bf16 v[180:183], a[108:111], v[164:167], v[180:183]\n"
    "ds_read_u16 v198, v198\n"
    "v_cvt_pk_bf16_f32 v136, v136, v137\n"
    "v_mfma_f32_16x16x32_bf16 v[100:103], a[4:7], v[184:187], 0\n"
    "v_cvt_pk_bf16_f32 v137, v138, v139\n"
    "v_cvt_pk_bf16_f32 v138, v140, v141\n"
    "v_mfma_f32_16x16x32_bf16 v[112:115], a[8:11], v[184:187], 0\n"
    "ds_read_u16_d16_hi v206, v206\n"
    "v_cvt_pk_bf16_f32 v139, v142, v143\n"
    "v_mfma_f32_16x16x32_bf16 v[116:119], a[12:15], v[184:187], 0\n"
    "s_waitcnt lgkmcnt(7)\n"
    "ds_read_u16 v199, v199\n"
    "v_or_b32_e32 v194, v194, v202\n"
    "v_mfma_f32_16x16x32_bf16 v[104:107], a[0:3], v[188:191], 0\n"
    "v_pk_mul_f16 v232, v192, v232\n"
    "v_lshlrev_b32_sdwa v201, 1, v171 dst_sel:DWORD dst_unused:UNUSED_PAD src0_sel:DWORD src1_sel:WORD_1\n"
    "v_mfma_f32_16x16x32_bf16 v[108:111], a[4:7], v[188:191], 0\n"
    "ds_read_u16_d16_hi v207, v207\n"
    "v_exp_f16_e32 v234, v232\n"
    "v_mfma_f32_16x16x32_bf16 v[120:123], a[8:11], v[188:191], 0\n"
    "v_exp_f16_sdwa v234, v232 dst_sel:WORD_1 dst_unused:UNUSED_PRESERVE src0_sel:WORD_1\n"
    "v_mfma_f32_16x16x32_bf16 v[124:127], a[12:15], v[188:191], 0\n"
    "s_waitcnt lgkmcnt(6)\n"
    "ds_read_u16_d16_hi v201, v201\n"
    "v_or_b32_e64 v195, v195, v203\n"
    "v_mfma_f32_16x16x32_bf16 v[132:135], a[244:247], v[184:187], 0\n"
    "ds_write_b128 v243, v[128:131] offset:28736\n"
    "v_mfma_f32_16x16x32_bf16 v[128:131], v[236:239], v[184:187], 0\n"
    "v_pk_fma_f16 v234, v234, s49, s49\n"
    "v_cvt_pk_f16_f32 v184, v168, v169\n"
    "v_mfma_f32_16x16x32_bf16 v[140:143], a[244:247], v[188:191], 0\n"
    "v_lshlrev_b32_sdwa v185, 1, v170 dst_sel:DWORD dst_unused:UNUSED_PAD src0_sel:DWORD src1_sel:WORD_1\n"
    "v_lshlrev_b32_sdwa v186, 1, v172 dst_sel:DWORD dst_unused:UNUSED_PAD src0_sel:DWORD src1_sel:WORD_1\n"
    "v_mfma_f32_16x16x32_bf16 v[168:171], a[152:155], v[144:147], 0\n"
    "s_waitcnt lgkmcnt(6)\n"
    "ds_read_u16 v185, v185\n"
    "ds_write_b128 v243, v[136:139] offset:29760\n"
    "v_mfma_f32_16x16x32_bf16 v[136:139], v[236:239], v[188:191], 0\n"
    "v_or_b32_e32 v197, v197, v205\n"
    "v_lshlrev_b32_sdwa v202, 1, v173 dst_sel:DWORD dst_unused:UNUSED_PAD src0_sel:DWORD src1_sel:WORD_1\n"
    "v_mfma_f32_16x16x32_bf16 v[168:171], a[200:203], v[148:151], v[168:171]\n"
    "v_lshlrev_b32_sdwa v187, 1, v174 dst_sel:DWORD dst_unused:UNUSED_PAD src0_sel:DWORD src1_sel:WORD_1\n"
    "v_lshlrev_b32_sdwa v203, 1, v175 dst_sel:DWORD dst_unused:UNUSED_PAD src0_sel:DWORD src1_sel:WORD_1\n"
    "v_rcp_f16_e32 v232, v234\n"
    "v_mfma_f32_16x16x32_bf16 v[168:171], a[112:115], v[152:155], v[168:171]\n"
    "s_waitcnt lgkmcnt(6)\n"
    "ds_read_b128 a[252:255], v249 offset:0\n"
    "ds_read_u16 v186, v186\n"
    "v_or_b32_e64 v198, v198, v206\n"
    "v_rcp_f16_sdwa v232, v234 dst_sel:WORD_1 dst_unused:UNUSED_PRESERVE src0_sel:WORD_1\n"
    "v_mfma_f32_16x16x32_bf16 v[172:175], a[156:159], v[144:147], 0\n"
    "ds_read_u16_d16_hi v202, v202\n"
    "ds_read_b128 v[236:239], v244 offset:4096\n"
    "v_cvt_pk_f16_f32 v188, v176, v177\n"
    "v_lshlrev_b32_sdwa v189, 1, v178 dst_sel:DWORD dst_unused:UNUSED_PAD src0_sel:DWORD src1_sel:WORD_1\n"
    "v_lshlrev_b32_sdwa v205, 1, v179 dst_sel:DWORD dst_unused:UNUSED_PAD src0_sel:DWORD src1_sel:WORD_1\n"
    "v_lshlrev_b32_sdwa v190, 1, v180 dst_sel:DWORD dst_unused:UNUSED_PAD src0_sel:DWORD src1_sel:WORD_1\n"
    "v_lshlrev_b32_sdwa v206, 1, v181 dst_sel:DWORD dst_unused:UNUSED_PAD src0_sel:DWORD src1_sel:WORD_1\n"
    "v_lshlrev_b32_sdwa v191, 1, v182 dst_sel:DWORD dst_unused:UNUSED_PAD src0_sel:DWORD src1_sel:WORD_1\n"
    "v_fma_mix_f32 v234, v192, v232, 0 op_sel_hi:[1,1,0]\n"
    "v_mfma_f32_16x16x32_bf16 v[172:175], a[204:207], v[148:151], v[172:175]\n"
    "s_waitcnt lgkmcnt(6)\n"
    "ds_read_u16 v187, v187\n"
    "ds_read_u16_d16_hi v203, v203\n"
    "v_mfma_f32_16x16x32_bf16 v[172:175], a[116:119], v[152:155], v[172:175]\n"
    "v_or_b32_e32 v199, v199, v207\n"
    "v_lshlrev_b32_sdwa v207, 1, v183 dst_sel:DWORD dst_unused:UNUSED_PAD src0_sel:DWORD src1_sel:WORD_1\n"
    "v_fma_mix_f32 v232, v192, v232, 0 op_sel:[1,1,0] op_sel_hi:[1,1,0]\n"
    "v_mfma_f32_16x16x32_bf16 v[176:179], a[152:155], v[156:159], 0\n"
    "s_nop 0\n"
    "ds_read_b128 a[244:247], v244 offset:6144\n"
    "v_cvt_pk_bf16_f32 v192, v234, v232\n"
    "v_mfma_f32_16x16x32_bf16 v[176:179], a[200:203], v[160:163], v[176:179]\n"
    "s_waitcnt lgkmcnt(7)\n"
    "ds_read_u16 v189, v189\n"
    "v_pk_fma_f16 v232, v196, v196, s48\n"
    "v_mfma_f32_16x16x32_bf16 v[176:179], a[112:115], v[164:167], v[176:179]\n"
    "v_pk_mul_f16 v232, v196, v232\n"
    "v_or_b32_e32 v185, v185, v201\n"
    "v_mfma_f32_16x16x32_bf16 v[180:183], a[156:159], v[156:159], 0\n"
    "s_waitcnt lgkmcnt(7)\n"
    "ds_read_u16_d16_hi v205, v205\n"
    "buffer_store_dwordx4 a[252:255], v255, s[44:47], s57 offen offset:0 sc1\n"
    "v_mfma_f32_16x16x32_bf16 v[180:183], a[204:207], v[160:163], v[180:183]\n"
    "ds_read_u16 v190, v190\n"
    "v_exp_f16_e64 v234, v232\n"
    "v_mfma_f32_16x16x32_bf16 v[180:183], a[116:119], v[164:167], v[180:183]\n"
    "ds_read_b128 a[252:255], v249 offset:2048\n"
    "v_exp_f16_sdwa v234, v232 dst_sel:WORD_1 dst_unused:UNUSED_PRESERVE src0_sel:WORD_1\n"
    "v_lshlrev_b32_sdwa v200, 1, v169 dst_sel:DWORD dst_unused:UNUSED_PAD src0_sel:DWORD src1_sel:WORD_1\n"
    "v_pk_fma_f16 v234, v234, s49, s49\n"
    "v_mfma_f32_16x16x32_bf16 v[96:99], a[16:19], v[192:195], v[96:99]\n"
    "s_waitcnt lgkmcnt(7)\n"
    "ds_read_u16_d16_hi v206, v206\n"
    "v_rcp_f16_e32 v232, v234\n"
    "v_mfma_f32_16x16x32_bf16 v[100:103], a[20:23], v[192:195], v[100:103]\n"
    "ds_read_u16 v191, v191\n"
    "v_rcp_f16_sdwa v232, v234 dst_sel:WORD_1 dst_unused:UNUSED_PRESERVE src0_sel:WORD_1\n"
    "v_mfma_f32_16x16x32_bf16 v[112:115], a[24:27], v[192:195], v[112:115]\n"
    "ds_read_u16_d16_hi v207, v207\n"
    "v_fma_mix_f32 v234, v196, v232, 0 op_sel_hi:[1,1,0]\n"
    "v_mfma_f32_16x16x32_bf16 v[116:119], a[28:31], v[192:195], v[116:119]\n"
    "v_fma_mix_f32 v232, v196, v232, 0 op_sel:[1,1,0] op_sel_hi:[1,1,0]\n"
    "v_or_b32_e32 v186, v186, v202\n"
    "v_mfma_f32_16x16x32_bf16 v[128:131], v[236:239], v[192:195], v[128:131]\n"
    "s_waitcnt lgkmcnt(7)\n"
    "ds_read_b128 a[240:243], v244 offset:34816\n"
    "v_cvt_pk_bf16_f32 v196, v234, v232\n"
    "v_mfma_f32_16x16x32_bf16 v[132:135], a[244:247], v[192:195], v[132:135]\n"
    "v_pk_fma_f16 v232, v184, v184, s48\n"
    "v_or_b32_e64 v187, v187, v203\n"
    "v_mfma_f32_16x16x32_bf16 v[104:107], a[16:19], v[196:199], v[104:107]\n"
    "ds_read_u16_d16_hi v200, v200\n"
    "v_pk_mul_f16 v232, v184, v232\n"
    "v_mfma_f32_16x16x32_bf16 v[108:111], a[20:23], v[196:199], v[108:111]\n"
    "v_exp_f16_e64 v234, v232\n"
    "v_mfma_f32_16x16x32_bf16 v[120:123], a[24:27], v[196:199], v[120:123]\n"
    "s_waitcnt lgkmcnt(7)\n"
    "v_or_b32_e32 v189, v189, v205\n"
    "v_exp_f16_sdwa v234, v232 dst_sel:WORD_1 dst_unused:UNUSED_PRESERVE src0_sel:WORD_1\n"
    "v_mfma_f32_16x16x32_bf16 v[124:127], a[28:31], v[196:199], v[124:127]\n"
    "v_pk_fma_f16 v234, v234, s49, s49\n"
    "v_lshlrev_b32_sdwa v192, 1, v168 dst_sel:DWORD dst_unused:UNUSED_PAD src0_sel:DWORD src1_sel:WORD_1\n"
    "v_mfma_f32_16x16x32_bf16 v[136:139], v[236:239], v[196:199], v[136:139]\n"
    "s_waitcnt lgkmcnt(5)\n"
    "ds_read_u16 v192, v192\n"
    "buffer_store_dwordx4 a[252:255], v255, s[44:47], s57 offen offset:64 sc1\n"
    "v_mfma_f32_16x16x32_bf16 v[140:143], a[244:247], v[196:199], v[140:143]\n"
    "s_nop 0\n"
    "ds_read_b128 v[236:239], v244 offset:8192\n"
    "ds_read_b128 a[252:255], v249 offset:4096\n"
    "v_lshlrev_b32_sdwa v193, 1, v170 dst_sel:DWORD dst_unused:UNUSED_PAD src0_sel:DWORD src1_sel:WORD_1\n"
    "v_lshlrev_b32_sdwa v201, 1, v171 dst_sel:DWORD dst_unused:UNUSED_PAD src0_sel:DWORD src1_sel:WORD_1\n"
    "v_mfma_f32_16x16x32_bf16 v[168:171], a[160:163], v[144:147], 0\n"
    "s_waitcnt lgkmcnt(7)\n"
    "ds_read_u16 v193, v193\n"
    "ds_read_u16_d16_hi v201, v201\n"
    "v_mfma_f32_16x16x32_bf16 v[168:171], a[208:211], v[148:151], v[168:171]\n"
    "s_nop 0\n"
    "v_lshlrev_b32_sdwa v194, 1, v172 dst_sel:DWORD dst_unused:UNUSED_PAD src0_sel:DWORD src1_sel:WORD_1\n"
    "v_lshlrev_b32_sdwa v202, 1, v173 dst_sel:DWORD dst_unused:UNUSED_PAD src0_sel:DWORD src1_sel:WORD_1\n"
    "v_lshlrev_b32_sdwa v195, 1, v174 dst_sel:DWORD dst_unused:UNUSED_PAD src0_sel:DWORD src1_sel:WORD_1\n"
    "v_lshlrev_b32_sdwa v203, 1, v175 dst_sel:DWORD dst_unused:UNUSED_PAD src0_sel:DWORD src1_sel:WORD_1\n"
    "v_rcp_f16_e32 v232, v234\n"
    "v_mfma_f32_16x16x32_bf16 v[168:171], a[120:123], v[152:155], v[168:171]\n"
    "s_waitcnt lgkmcnt(6)\n"
    "ds_read_u16 v194, v194\n"
    "ds_read_u16_d16_hi v202, v202\n"
    "v_or_b32_e64 v190, v190, v206\n"
    "v_rcp_f16_sdwa v232, v234 dst_sel:WORD_1 dst_unused:UNUSED_PRESERVE src0_sel:WORD_1\n"
    "v_mfma_f32_16x16x32_bf16 v[172:175], a[164:167], v[144:147], 0\n"
    "ds_read_u16 v195, v195\n"
    "v_lshlrev_b32_sdwa v196, 1, v176 dst_sel:DWORD dst_unused:UNUSED_PAD src0_sel:DWORD src1_sel:WORD_1\n"
    "v_lshlrev_b32_sdwa v204, 1, v177 dst_sel:DWORD dst_unused:UNUSED_PAD src0_sel:DWORD src1_sel:WORD_1\n"
    "v_lshlrev_b32_sdwa v197, 1, v178 dst_sel:DWORD dst_unused:UNUSED_PAD src0_sel:DWORD src1_sel:WORD_1\n"
    "v_lshlrev_b32_sdwa v205, 1, v179 dst_sel:DWORD dst_unused:UNUSED_PAD src0_sel:DWORD src1_sel:WORD_1\n"
    "v_lshlrev_b32_sdwa v198, 1, v180 dst_sel:DWORD dst_unused:UNUSED_PAD src0_sel:DWORD src1_sel:WORD_1\n"
    "v_lshlrev_b32_sdwa v206, 1, v181 dst_sel:DWORD dst_unused:UNUSED_PAD src0_sel:DWORD src1_sel:WORD_1\n"
    "v_lshlrev_b32_sdwa v199, 1, v182 dst_sel:DWORD dst_unused:UNUSED_PAD src0_sel:DWORD src1_sel:WORD_1\n"
    "v_fma_mix_f32 v234, v184, v232, 0 op_sel_hi:[1,1,0]\n"
    "v_mfma_f32_16x16x32_bf16 v[172:175], a[212:215], v[148:151], v[172:175]\n"
    "s_waitcnt lgkmcnt(5)\n"
    "ds_read_b128 a[244:247], v244 offset:10240\n"
    "buffer_store_dwordx4 a[252:255], v255, s[44:47], s57 offen offset:128 sc1\n"
    "v_mfma_f32_16x16x32_bf16 v[172:175], a[240:243], v[152:155], v[172:175]\n"
    "s_nop 0\n"
    "ds_read_u16_d16_hi v203, v203\n"
    "ds_read_b128 a[252:255], v249 offset:1024\n"
    "v_or_b32_e64 v191, v191, v207\n"
    "v_lshlrev_b32_sdwa v207, 1, v183 dst_sel:DWORD dst_unused:UNUSED_PAD src0_sel:DWORD src1_sel:WORD_1\n"
    "v_fma_mix_f32 v232, v184, v232, 0 op_sel:[1,1,0] op_sel_hi:[1,1,0]\n"
    "v_mfma_f32_16x16x32_bf16 v[176:179], a[160:163], v[156:159], 0\n"
    "ds_read_u16 v196, v196\n"
    "ds_read_u16_d16_hi v204, v204\n"
    "v_cvt_pk_bf16_f32 v184, v234, v232\n"
    "v_mfma_f32_16x16x32_bf16 v[176:179], a[208:211], v[160:163], v[176:179]\n"
    "ds_read_u16 v197, v197\n"
    "v_pk_fma_f16 v232, v188, v188, s48\n"
    "v_mfma_f32_16x16x32_bf16 v[176:179], a[120:123], v[164:167], v[176:179]\n"
    "s_waitcnt lgkmcnt(9)\n"
    "v_pk_mul_f16 v232, v188, v232\n"
    "v_or_b32_e32 v192, v192, v200\n"
    "v_mfma_f32_16x16x32_bf16 v[180:183], a[164:167], v[156:159], 0\n"
    "ds_read_u16_d16_hi v205, v205\n"
    "v_exp_f16_e32 v234, v232\n"
    "v_mfma_f32_16x16x32_bf16 v[180:183], a[212:215], v[160:163], v[180:183]\n"
    "s_waitcnt lgkmcnt(8)\n"
    "v_exp_f16_sdwa v234, v232 dst_sel:WORD_1 dst_unused:UNUSED_PRESERVE src0_sel:WORD_1\n"
    "v_mfma_f32_16x16x32_bf16 v[180:183], a[240:243], v[164:167], v[180:183]\n"
    "ds_read_u16 v198, v198\n"
    "v_pk_fma_f16 v234, v234, s49, s49\n"
    "v_mfma_f32_16x16x32_bf16 v[96:99], a[32:35], v[184:187], v[96:99]\n"
    "ds_read_u16_d16_hi v206, v206\n"
    "v_rcp_f16_e32 v232, v234\n"
    "v_mfma_f32_16x16x32_bf16 v[100:103], a[36:39], v[184:187], v[100:103]\n"
    "v_rcp_f16_sdwa v232, v234 dst_sel:WORD_1 dst_unused:UNUSED_PRESERVE src0_sel:WORD_1\n"
    "v_mfma_f32_16x16x32_bf16 v[112:115], a[40:43], v[184:187], v[112:115]\n"
    "s_waitcnt lgkmcnt(6)\n"
    "ds_read_u16 v199, v199\n"
    "buffer_store_dwordx4 a[252:255], v255, s[44:47], s100 offen offset:0 sc1\n"
    "v_mfma_f32_16x16x32_bf16 v[116:119], a[44:47], v[184:187], v[116:119]\n"
    "ds_read_u16_d16_hi v207, v207\n"
    "v_fma_mix_f32 v234, v188, v232, 0 op_sel_hi:[1,1,0]\n"
    "v_mfma_f32_16x16x32_bf16 v[128:131], v[236:239], v[184:187], v[128:131]\n"
    "ds_read_b128 a[252:255], v249 offset:3072\n"
    "v_fma_mix_f32 v232, v188, v232, 0 op_sel:[1,1,0] op_sel_hi:[1,1,0]\n"
    "v_mfma_f32_16x16x32_bf16 v[132:135], a[244:247], v[184:187], v[132:135]\n"
    "ds_read_b128 a[240:243], v244 offset:38912\n"
    "v_cvt_pk_bf16_f32 v188, v234, v232\n"
    "v_or_b32_e32 v193, v193, v201\n"
    "v_cvt_pk_f16_f32 v184, v168, v169\n"
    "v_mfma_f32_16x16x32_bf16 v[104:107], a[32:35], v[188:191], v[104:107]\n"
    "s_waitcnt lgkmcnt(8)\n"
    "v_lshlrev_b32_sdwa v185, 1, v170 dst_sel:DWORD dst_unused:UNUSED_PAD src0_sel:DWORD src1_sel:WORD_1\n"
    "v_lshlrev_b32_sdwa v201, 1, v171 dst_sel:DWORD dst_unused:UNUSED_PAD src0_sel:DWORD src1_sel:WORD_1\n"
    "v_mfma_f32_16x16x32_bf16 v[108:111], a[36:39], v[188:191], v[108:111]\n"
    "ds_read_u16 v185, v185\n"
    "ds_read_u16_d16_hi v201, v201\n"
    "v_mfma_f32_16x16x32_bf16 v[120:123], a[40:43], v[188:191], v[120:123]\n"
    "v_lshlrev_b32_sdwa v186, 1, v172 dst_sel:DWORD dst_unused:UNUSED_PAD src0_sel:DWORD src1_sel:WORD_1\n"
    "v_or_b32_e32 v194, v194, v202\n"
    "v_mfma_f32_16x16x32_bf16 v[124:127], a[44:47], v[188:191], v[124:127]\n"
    "s_waitcnt lgkmcnt(8)\n"
    "ds_read_u16 v186, v186\n"
    "v_lshlrev_b32_sdwa v202, 1, v173 dst_sel:DWORD dst_unused:UNUSED_PAD src0_sel:DWORD src1_sel:WORD_1\n"
    "v_mfma_f32_16x16x32_bf16 v[136:139], v[236:239], v[188:191], v[136:139]\n"
    "v_lshlrev_b32_sdwa v187, 1, v174 dst_sel:DWORD dst_unused:UNUSED_PAD src0_sel:DWORD src1_sel:WORD_1\n"
    "v_or_b32_e64 v195, v195, v203\n"
    "v_mfma_f32_16x16x32_bf16 v[140:143], a[244:247], v[188:191], v[140:143]\n"
    "ds_read_u16_d16_hi v202, v202\n"
    "v_lshlrev_b32_sdwa v203, 1, v175 dst_sel:DWORD dst_unused:UNUSED_PAD src0_sel:DWORD src1_sel:WORD_1\n"
    "v_mfma_f32_16x16x32_bf16 v[168:171], a[168:171], v[144:147], 0\n"
    "s_nop 0\n"
    "v_cvt_pk_f16_f32 v188, v176, v177\n"
    "v_lshlrev_b32_sdwa v189, 1, v178 dst_sel:DWORD dst_unused:UNUSED_PAD src0_sel:DWORD src1_sel:WORD_1\n"
    "v_mfma_f32_16x16x32_bf16 v[168:171], a[216:219], v[148:151], v[168:171]\n"
    "s_waitcnt lgkmcnt(8)\n"
    "ds_read_b128 v[236:239], v244 offset:12288\n"
    "v_lshlrev_b32_sdwa v190, 1, v180 dst_sel:DWORD dst_unused:UNUSED_PAD src0_sel:DWORD src1_sel:WORD_1\n"
    "v_mfma_f32_16x16x32_bf16 v[168:171], a[124:127], v[152:155], v[168:171]\n"
    "ds_read_u16 v187, v187\n"
    "v_lshlrev_b32_sdwa v191, 1, v182 dst_sel:DWORD dst_unused:UNUSED_PAD src0_sel:DWORD src1_sel:WORD_1\n"
    "v_mfma_f32_16x16x32_bf16 v[172:175], a[172:175], v[144:147], 0\n"
    "v_or_b32_e64 v196, v196, v204\n"
    "v_or_b32_e32 v197, v197, v205\n"
    "v_mfma_f32_16x16x32_bf16 v[172:175], a[220:223], v[148:151], v[172:175]\n"
    "s_waitcnt lgkmcnt(7)\n"
    "ds_read_u16_d16_hi v203, v203\n"
    "buffer_store_dwordx4 a[252:255], v255, s[44:47], s100 offen offset:64 sc1\n"
    "s_waitcnt lgkmcnt(7)\n"
    "v_mfma_f32_16x16x32_bf16 v[172:175], a[240:243], v[152:155], v[172:175]\n"
    "s_nop 0\n"
    "ds_read_b128 a[252:255], v249 offset:5120\n"
    "ds_read_b128 a[244:247], v244 offset:14336\n"
    "v_lshlrev_b32_sdwa v205, 1, v179 dst_sel:DWORD dst_unused:UNUSED_PAD src0_sel:DWORD src1_sel:WORD_1\n"
    "v_mfma_f32_16x16x32_bf16 v[176:179], a[168:171], v[156:159], 0\n"
    "ds_read_u16 v189, v189\n"
    "v_or_b32_e64 v198, v198, v206\n"
    "v_mfma_f32_16x16x32_bf16 v[176:179], a[216:219], v[160:163], v[176:179]\n"
    "v_lshlrev_b32_sdwa v206, 1, v181 dst_sel:DWORD dst_unused:UNUSED_PAD src0_sel:DWORD src1_sel:WORD_1\n"
    "v_or_b32_e32 v199, v199, v207\n"
    "v_mfma_f32_16x16x32_bf16 v[176:179], a[124:127], v[164:167], v[176:179]\n"
    "s_waitcnt lgkmcnt(8)\n"
    "ds_read_u16_d16_hi v205, v205\n"
    "v_lshlrev_b32_sdwa v207, 1, v183 dst_sel:DWORD dst_unused:UNUSED_PAD src0_sel:DWORD src1_sel:WORD_1\n"
    "v_mfma_f32_16x16x32_bf16 v[180:183], a[172:175], v[156:159], 0\n"
    "ds_read_u16 v190, v190\n"
    "v_or_b32_e64 v185, v185, v201\n"
    "v_mfma_f32_16x16x32_bf16 v[180:183], a[220:223], v[160:163], v[180:183]\n"
    "v_pk_fma_f16 v232, v184, v184, s48\n"
    "v_lshlrev_b32_sdwa v201, 1, v171 dst_sel:DWORD dst_unused:UNUSED_PAD src0_sel:DWORD src1_sel:WORD_1\n"
    "v_mfma_f32_16x16x32_bf16 v[180:183], a[240:243], v[164:167], v[180:183]\n"
    "s_waitcnt lgkmcnt(7)\n"
    "ds_read_b128 v[144:147], v243 offset:0\n"
    "v_or_b32_e32 v186, v186, v202\n"
    "v_mfma_f32_16x16x32_bf16 v[96:99], a[48:51], v[192:195], v[96:99]\n"
    "ds_read_u16_d16_hi v206, v206\n"
    "buffer_load_dwordx4 a[232:235], v245, s[40:43], s58 offen offset:64\n"
    "buffer_load_dwordx4 a[224:227], v245, s[40:43], s58 offen offset:0\n"
    "v_mfma_f32_16x16x32_bf16 v[100:103], a[52:55], v[192:195], v[100:103]\n"
    "v_pk_mul_f16 v232, v184, v232\n"
    "v_lshlrev_b32_sdwa v202, 1, v173 dst_sel:DWORD dst_unused:UNUSED_PAD src0_sel:DWORD src1_sel:WORD_1\n"
    "v_mfma_f32_16x16x32_bf16 v[112:115], a[56:59], v[192:195], v[112:115]\n"
    "ds_read_b128 v[148:151], v243 offset:1024\n"
    "ds_read_u16 v191, v191\n"
    "v_mfma_f32_16x16x32_bf16 v[116:119], a[60:63], v[192:195], v[116:119]\n"
    "v_exp_f16_e32 v234, v232\n"
    "v_mfma_f32_16x16x32_bf16 v[128:131], v[236:239], v[192:195], v[128:131]\n"
    "s_waitcnt lgkmcnt(8)\n"
    "ds_read_b128 v[152:155], v243 offset:2048\n"
    "v_or_b32_e32 v187, v187, v203\n"
    "s_waitcnt lgkmcnt(8)\n"
    "v_mfma_f32_16x16x32_bf16 v[132:135], a[244:247], v[192:195], v[132:135]\n"
    "ds_read_u16_d16_hi v207, v207\n"
    "v_exp_f16_sdwa v234, v232 dst_sel:WORD_1 dst_unused:UNUSED_PRESERVE src0_sel:WORD_1\n"
    "v_mfma_f32_16x16x32_bf16 v[104:107], a[48:51], v[196:199], v[104:107]\n"
    "v_pk_fma_f16 v234, v234, s49, s49\n"
    "v_cvt_pk_f16_f32 v192, v168, v169\n"
    "v_mfma_f32_16x16x32_bf16 v[108:111], a[52:55], v[196:199], v[108:111]\n"
    "ds_read_b128 v[156:159], v243 offset:3072\n"
    "ds_read_b128 v[160:163], v243 offset:4096\n"
    "v_mfma_f32_16x16x32_bf16 v[120:123], a[56:59], v[196:199], v[120:123]\n"
    "v_lshlrev_b32_sdwa v193, 1, v170 dst_sel:DWORD dst_unused:UNUSED_PAD src0_sel:DWORD src1_sel:WORD_1\n"
    "v_lshlrev_b32_sdwa v194, 1, v172 dst_sel:DWORD dst_unused:UNUSED_PAD src0_sel:DWORD src1_sel:WORD_1\n"
    "v_mfma_f32_16x16x32_bf16 v[124:127], a[60:63], v[196:199], v[124:127]\n"
    "s_waitcnt lgkmcnt(9)\n"
    "ds_read_b128 v[164:167], v243 offset:5120\n"
    "buffer_store_dwordx4 a[252:255], v255, s[44:47], s100 offen offset:128 sc1\n"
    "v_mfma_f32_16x16x32_bf16 v[136:139], v[236:239], v[196:199], v[136:139]\n"
    "s_waitcnt lgkmcnt(8)\n"
    "ds_read_b128 a[240:243], v244 offset:32768\n"
    "ds_read_u16 v193, v193\n"
    "v_mfma_f32_16x16x32_bf16 v[140:143], a[244:247], v[196:199], v[140:143]\n"
    "ds_read_u16_d16_hi v201, v201\n"
    "v_or_b32_e32 v189, v189, v205\n"
    "v_mfma_f32_16x16x32_bf16 v[168:171], a[128:131], v[144:147], 0\n"
    "s_waitcnt lgkmcnt(9)\n"
    "v_lshlrev_b32_sdwa v195, 1, v174 dst_sel:DWORD dst_unused:UNUSED_PAD src0_sel:DWORD src1_sel:WORD_1\n"
    "v_lshlrev_b32_sdwa v203, 1, v175 dst_sel:DWORD dst_unused:UNUSED_PAD src0_sel:DWORD src1_sel:WORD_1\n"
    "v_mfma_f32_16x16x32_bf16 v[168:171], a[176:179], v[148:151], v[168:171]\n"
    "ds_read_u16 v194, v194\n"
    "v_rcp_f16_e64 v232, v234\n"
    "v_or_b32_e32 v190, v190, v206\n"
    "v_rcp_f16_sdwa v232, v234 dst_sel:WORD_1 dst_unused:UNUSED_PRESERVE src0_sel:WORD_1\n"
    "s_waitcnt lgkmcnt(8)\n"
    "v_mfma_f32_16x16x32_bf16 v[168:171], a[96:99], v[152:155], v[168:171]\n"
    "ds_read_u16_d16_hi v202, v202\n"
    "v_cvt_pk_f16_f32 v196, v176, v177\n"
    "v_mfma_f32_16x16x32_bf16 v[172:175], a[132:135], v[144:147], 0\n"
    "ds_read_b128 v[236:239], v244 offset:16384\n"
    "v_lshlrev_b32_sdwa v197, 1, v178 dst_sel:DWORD dst_unused:UNUSED_PAD src0_sel:DWORD src1_sel:WORD_1\n"
    "v_mfma_f32_16x16x32_bf16 v[172:175], a[180:183], v[148:151], v[172:175]\n"
    "ds_read_u16 v195, v195\n"
    "v_lshlrev_b32_sdwa v205, 1, v179 dst_sel:DWORD dst_unused:UNUSED_PAD src0_sel:DWORD src1_sel:WORD_1\n"
    "v_lshlrev_b32_sdwa v198, 1, v180 dst_sel:DWORD dst_unused:UNUSED_PAD src0_sel:DWORD src1_sel:WORD_1\n"
    "v_lshlrev_b32_sdwa v206, 1, v181 dst_sel:DWORD dst_unused:UNUSED_PAD src0_sel:DWORD src1_sel:WORD_1\n"
    "v_lshlrev_b32_sdwa v199, 1, v182 dst_sel:DWORD dst_unused:UNUSED_PAD src0_sel:DWORD src1_sel:WORD_1\n"
    "v_fma_mix_f32 v234, v184, v232, 0 op_sel_hi:[1,1,0]\n"
    "s_waitcnt lgkmcnt(8)\n"
    "v_or_b32_e32 v191, v191, v207\n"
    "v_lshlrev_b32_sdwa v207, 1, v183 dst_sel:DWORD dst_unused:UNUSED_PAD src0_sel:DWORD src1_sel:WORD_1\n"
    "v_fma_mix_f32 v232, v184, v232, 0 op_sel:[1,1,0] op_sel_hi:[1,1,0]\n"
    "v_mfma_f32_16x16x32_bf16 v[176:179], a[128:131], v[156:159], 0\n"
    "s_waitcnt lgkmcnt(6)\n"
    "ds_read_u16_d16_hi v203, v203\n"
    "v_cvt_pk_bf16_f32 v184, v234, v232\n"
    "v_mfma_f32_16x16x32_bf16 v[172:175], a[240:243], v[152:155], v[172:175]\n"
    "s_barrier\n"
    "v_mfma_f32_16x16x32_bf16 v[176:179], a[176:179], v[160:163], v[176:179]\n"
    "ds_read_b128 a[244:247], v244 offset:18432\n"
    "ds_read_u16 v197, v197\n"
    "v_mfma_f32_16x16x32_bf16 v[176:179], a[96:99], v[164:167], v[176:179]\n"
    "ds_read_u16_d16_hi v205, v205\n"
    "buffer_load_dwordx4 a[236:239], v245, s[40:43], s101 offen offset:64\n"
    "buffer_load_dwordx4 a[228:231], v245, s[40:43], s101 offen offset:0\n"
    "v_pk_fma_f16 v232, v188, v188, s48\n"
    "v_mfma_f32_16x16x32_bf16 v[180:183], a[132:135], v[156:159], 0\n"
    "s_waitcnt lgkmcnt(8)\n"
    "ds_read_u16 v198, v198\n"
    "v_pk_mul_f16 v232, v188, v232\n"
    "v_mfma_f32_16x16x32_bf16 v[180:183], a[180:183], v[160:163], v[180:183]\n"
    "v_exp_f16_e64 v234, v232\n"
    "v_mfma_f32_16x16x32_bf16 v[180:183], a[240:243], v[164:167], v[180:183]\n"
    "ds_read_u16_d16_hi v206, v206\n"
    "v_exp_f16_sdwa v234, v232 dst_sel:WORD_1 dst_unused:UNUSED_PRESERVE src0_sel:WORD_1\n"
    "v_mfma_f32_16x16x32_bf16 v[96:99], a[64:67], v[184:187], v[96:99]\n"
    "s_waitcnt lgkmcnt(7)\n"
    "ds_read_u16 v199, v199\n"
    "v_pk_fma_f16 v234, v234, s49, s49\n"
    "v_mfma_f32_16x16x32_bf16 v[100:103], a[68:71], v[184:187], v[100:103]\n"
    "v_rcp_f16_e64 v232, v234\n"
    "v_mfma_f32_16x16x32_bf16 v[112:115], a[72:75], v[184:187], v[112:115]\n"
    "ds_read_u16_d16_hi v207, v207\n"
    "v_rcp_f16_sdwa v232, v234 dst_sel:WORD_1 dst_unused:UNUSED_PRESERVE src0_sel:WORD_1\n"
    "v_mfma_f32_16x16x32_bf16 v[116:119], a[76:79], v[184:187], v[116:119]\n"
    "v_or_b32_e32 v193, v193, v201\n"
    "v_fma_mix_f32 v234, v188, v232, 0 op_sel_hi:[1,1,0]\n"
    "v_mfma_f32_16x16x32_bf16 v[128:131], v[236:239], v[184:187], v[128:131]\n"
    "s_waitcnt lgkmcnt(6)\n"
    "ds_read_b128 a[240:243], v244 offset:36864\n"
    "v_fma_mix_f32 v232, v188, v232, 0 op_sel:[1,1,0] op_sel_hi:[1,1,0]\n"
    "v_mfma_f32_16x16x32_bf16 v[132:135], a[244:247], v[184:187], v[132:135]\n"
    "v_or_b32_e64 v194, v194, v202\n"
    "v_cvt_pk_bf16_f32 v188, v234, v232\n"
    "v_pk_fma_f16 v232, v192, v192, s48\n"
    "v_or_b32_e64 v195, v195, v203\n"
    "v_mfma_f32_16x16x32_bf16 v[104:107], a[64:67], v[188:191], v[104:107]\n"
    "v_pk_mul_f16 v232, v192, v232\n"
    "v_lshlrev_b32_sdwa v184, 1, v168 dst_sel:DWORD dst_unused:UNUSED_PAD src0_sel:DWORD src1_sel:WORD_1\n"
    "v_mfma_f32_16x16x32_bf16 v[108:111], a[68:71], v[188:191], v[108:111]\n"
    "ds_read_u16 v184, v184\n"
    "v_exp_f16_e32 v234, v232\n"
    "v_mfma_f32_16x16x32_bf16 v[120:123], a[72:75], v[188:191], v[120:123]\n"
    "s_waitcnt lgkmcnt(6)\n"
    "v_exp_f16_sdwa v234, v232 dst_sel:WORD_1 dst_unused:UNUSED_PRESERVE src0_sel:WORD_1\n"
    "v_mfma_f32_16x16x32_bf16 v[124:127], a[76:79], v[188:191], v[124:127]\n"
    "v_or_b32_e64 v197, v197, v205\n"
    "v_pk_fma_f16 v234, v234, s49, s49\n"
    "v_mfma_f32_16x16x32_bf16 v[136:139], v[236:239], v[188:191], v[136:139]\n"
    "s_nop 0\n"
    "ds_read_b128 v[236:239], v244 offset:20480\n"
    "v_lshlrev_b32_sdwa v200, 1, v169 dst_sel:DWORD dst_unused:UNUSED_PAD src0_sel:DWORD src1_sel:WORD_1\n"
    "v_mfma_f32_16x16x32_bf16 v[140:143], a[244:247], v[188:191], v[140:143]\n"
    "s_waitcnt lgkmcnt(5)\n"
    "ds_read_u16_d16_hi v200, v200\n"
    "v_lshlrev_b32_sdwa v185, 1, v170 dst_sel:DWORD dst_unused:UNUSED_PAD src0_sel:DWORD src1_sel:WORD_1\n"
    "v_lshlrev_b32_sdwa v201, 1, v171 dst_sel:DWORD dst_unused:UNUSED_PAD src0_sel:DWORD src1_sel:WORD_1\n"
    "v_mfma_f32_16x16x32_bf16 v[168:171], a[136:139], v[144:147], 0\n"
    "ds_read_u16 v185, v185\n"
    "ds_read_u16_d16_hi v201, v201\n"
    "v_mfma_f32_16x16x32_bf16 v[168:171], a[184:187], v[148:151], v[168:171]\n"
    "v_lshlrev_b32_sdwa v186, 1, v172 dst_sel:DWORD dst_unused:UNUSED_PAD src0_sel:DWORD src1_sel:WORD_1\n"
    "v_lshlrev_b32_sdwa v202, 1, v173 dst_sel:DWORD dst_unused:UNUSED_PAD src0_sel:DWORD src1_sel:WORD_1\n"
    "v_lshlrev_b32_sdwa v187, 1, v174 dst_sel:DWORD dst_unused:UNUSED_PAD src0_sel:DWORD src1_sel:WORD_1\n"
    "v_lshlrev_b32_sdwa v203, 1, v175 dst_sel:DWORD dst_unused:UNUSED_PAD src0_sel:DWORD src1_sel:WORD_1\n"
    "v_rcp_f16_e32 v232, v234\n"
    "v_mfma_f32_16x16x32_bf16 v[168:171], a[100:103], v[152:155], v[168:171]\n"
    "s_waitcnt lgkmcnt(5)\n"
    "ds_read_u16 v186, v186\n"
    "ds_read_u16_d16_hi v202, v202\n"
    "v_or_b32_e64 v198, v198, v206\n"
    "v_rcp_f16_sdwa v232, v234 dst_sel:WORD_1 dst_unused:UNUSED_PRESERVE src0_sel:WORD_1\n"
    "v_mfma_f32_16x16x32_bf16 v[172:175], a[140:143], v[144:147], 0\n"
    "ds_read_u16 v187, v187\n"
    "v_lshlrev_b32_sdwa v188, 1, v176 dst_sel:DWORD dst_unused:UNUSED_PAD src0_sel:DWORD src1_sel:WORD_1\n"
    "v_mfma_f32_16x16x32_bf16 v[172:175], a[188:191], v[148:151], v[172:175]\n"
    "ds_read_b128 a[244:247], v244 offset:22528\n"
    "v_lshlrev_b32_sdwa v204, 1, v177 dst_sel:DWORD dst_unused:UNUSED_PAD src0_sel:DWORD src1_sel:WORD_1\n"
    "v_lshlrev_b32_sdwa v189, 1, v178 dst_sel:DWORD dst_unused:UNUSED_PAD src0_sel:DWORD src1_sel:WORD_1\n"
    "v_lshlrev_b32_sdwa v205, 1, v179 dst_sel:DWORD dst_unused:UNUSED_PAD src0_sel:DWORD src1_sel:WORD_1\n"
    "v_lshlrev_b32_sdwa v190, 1, v180 dst_sel:DWORD dst_unused:UNUSED_PAD src0_sel:DWORD src1_sel:WORD_1\n"
    "v_lshlrev_b32_sdwa v206, 1, v181 dst_sel:DWORD dst_unused:UNUSED_PAD src0_sel:DWORD src1_sel:WORD_1\n"
    "v_lshlrev_b32_sdwa v191, 1, v182 dst_sel:DWORD dst_unused:UNUSED_PAD src0_sel:DWORD src1_sel:WORD_1\n"
    "v_fma_mix_f32 v234, v192, v232, 0 op_sel_hi:[1,1,0]\n"
    "v_mfma_f32_16x16x32_bf16 v[172:175], a[240:243], v[152:155], v[172:175]\n"
    "ds_read_u16_d16_hi v203, v203\n"
    "ds_read_u16 v188, v188\n"
    "v_or_b32_e32 v199, v199, v207\n"
    "v_lshlrev_b32_sdwa v207, 1, v183 dst_sel:DWORD dst_unused:UNUSED_PAD src0_sel:DWORD src1_sel:WORD_1\n"
    "v_fma_mix_f32 v232, v192, v232, 0 op_sel:[1,1,0] op_sel_hi:[1,1,0]\n"
    "v_mfma_f32_16x16x32_bf16 v[176:179], a[136:139], v[156:159], 0\n"
    "s_waitcnt lgkmcnt(8)\n"
    "ds_read_u16_d16_hi v204, v204\n"
    "v_cvt_pk_bf16_f32 v192, v234, v232\n"
    "v_mfma_f32_16x16x32_bf16 v[176:179], a[184:187], v[160:163], v[176:179]\n"
    "v_pk_fma_f16 v232, v196, v196, s48\n"
    "v_or_b32_e64 v184, v184, v200\n"
    "v_mfma_f32_16x16x32_bf16 v[176:179], a[100:103], v[164:167], v[176:179]\n"
    "ds_read_u16 v189, v189\n"
    "v_pk_mul_f16 v232, v196, v232\n"
    "v_mfma_f32_16x16x32_bf16 v[180:183], a[140:143], v[156:159], 0\n"
    "s_waitcnt lgkmcnt(8)\n"
    "v_exp_f16_e32 v234, v232\n"
    "v_mfma_f32_16x16x32_bf16 v[180:183], a[188:191], v[160:163], v[180:183]\n"
    "ds_read_u16_d16_hi v205, v205\n"
    "v_exp_f16_sdwa v234, v232 dst_sel:WORD_1 dst_unused:UNUSED_PRESERVE src0_sel:WORD_1\n"
    "v_mfma_f32_16x16x32_bf16 v[180:183], a[240:243], v[164:167], v[180:183]\n"
    "ds_read_u16 v190, v190\n"
    "v_pk_fma_f16 v234, v234, s49, s49\n"
    "v_mfma_f32_16x16x32_bf16 v[96:99], a[80:83], v[192:195], v[96:99]\n"
    "s_waitcnt lgkmcnt(8)\n"
    "v_rcp_f16_e32 v232, v234\n"
    "v_mfma_f32_16x16x32_bf16 v[100:103], a[84:87], v[192:195], v[100:103]\n"
    "ds_read_u16_d16_hi v206, v206\n"
    "v_rcp_f16_sdwa v232, v234 dst_sel:WORD_1 dst_unused:UNUSED_PRESERVE src0_sel:WORD_1\n"
    "v_mfma_f32_16x16x32_bf16 v[112:115], a[88:91], v[192:195], v[112:115]\n"
    "ds_read_u16 v191, v191\n"
    "v_fma_mix_f32 v234, v196, v232, 0 op_sel_hi:[1,1,0]\n"
    "v_mfma_f32_16x16x32_bf16 v[116:119], a[92:95], v[192:195], v[116:119]\n"
    "s_waitcnt lgkmcnt(8)\n"
    "v_fma_mix_f32 v232, v196, v232, 0 op_sel:[1,1,0] op_sel_hi:[1,1,0]\n"
    "v_or_b32_e64 v185, v185, v201\n"
    "v_mfma_f32_16x16x32_bf16 v[128:131], v[236:239], v[192:195], v[128:131]\n"
    "s_waitcnt lgkmcnt(7)\n"
    "ds_read_u16_d16_hi v207, v207\n"
    "v_cvt_pk_bf16_f32 v196, v234, v232\n"
    "v_mfma_f32_16x16x32_bf16 v[132:135], a[244:247], v[192:195], v[132:135]\n"
    "v_cvt_pk_f16_f32 v192, v168, v169\n"
    "v_lshlrev_b32_sdwa v193, 1, v170 dst_sel:DWORD dst_unused:UNUSED_PAD src0_sel:DWORD src1_sel:WORD_1\n"
    "v_mfma_f32_16x16x32_bf16 v[104:107], a[80:83], v[196:199], v[104:107]\n"
    "ds_read_u16 v193, v193\n"
    "v_lshlrev_b32_sdwa v201, 1, v171 dst_sel:DWORD dst_unused:UNUSED_PAD src0_sel:DWORD src1_sel:WORD_1\n"
    "v_mfma_f32_16x16x32_bf16 v[108:111], a[84:87], v[196:199], v[108:111]\n"
    "v_lshlrev_b32_sdwa v194, 1, v172 dst_sel:DWORD dst_unused:UNUSED_PAD src0_sel:DWORD src1_sel:WORD_1\n"
    "v_or_b32_e32 v186, v186, v202\n"
    "v_mfma_f32_16x16x32_bf16 v[120:123], a[88:91], v[196:199], v[120:123]\n"
    "s_waitcnt lgkmcnt(7)\n"
    "ds_read_u16_d16_hi v201, v201\n"
    "v_lshlrev_b32_sdwa v202, 1, v173 dst_sel:DWORD dst_unused:UNUSED_PAD src0_sel:DWORD src1_sel:WORD_1\n"
    "v_mfma_f32_16x16x32_bf16 v[124:127], a[92:95], v[196:199], v[124:127]\n"
    "ds_read_u16 v194, v194\n"
    "v_lshlrev_b32_sdwa v195, 1, v174 dst_sel:DWORD dst_unused:UNUSED_PAD src0_sel:DWORD src1_sel:WORD_1\n"
    "v_mfma_f32_16x16x32_bf16 v[136:139], v[236:239], v[196:199], v[136:139]\n"
    "v_or_b32_e32 v187, v187, v203\n"
    "v_lshlrev_b32_sdwa v203, 1, v175 dst_sel:DWORD dst_unused:UNUSED_PAD src0_sel:DWORD src1_sel:WORD_1\n"
    "v_mfma_f32_16x16x32_bf16 v[140:143], a[244:247], v[196:199], v[140:143]\n"
    "s_waitcnt lgkmcnt(7)\n"
    "ds_read_u16_d16_hi v202, v202\n"
    "v_cvt_pk_f16_f32 v196, v176, v177\n"
    "v_mfma_f32_16x16x32_bf16 v[168:171], a[144:147], v[144:147], 0\n"
    "v_lshlrev_b32_sdwa v197, 1, v178 dst_sel:DWORD dst_unused:UNUSED_PAD src0_sel:DWORD src1_sel:WORD_1\n"
    "v_lshlrev_b32_sdwa v198, 1, v180 dst_sel:DWORD dst_unused:UNUSED_PAD src0_sel:DWORD src1_sel:WORD_1\n"
    "v_mfma_f32_16x16x32_bf16 v[168:171], a[192:195], v[148:151], v[168:171]\n"
    "ds_read_b128 v[236:239], v244 offset:0\n"
    "v_lshlrev_b32_sdwa v199, 1, v182 dst_sel:DWORD dst_unused:UNUSED_PAD src0_sel:DWORD src1_sel:WORD_1\n"
    "v_mfma_f32_16x16x32_bf16 v[168:171], a[104:107], v[152:155], v[168:171]\n"
    "ds_read_u16 v195, v195\n"
    "v_or_b32_e64 v188, v188, v204\n"
    "v_mfma_f32_16x16x32_bf16 v[172:175], a[148:151], v[144:147], 0\n"
    "s_waitcnt lgkmcnt(8)\n"
    "v_or_b32_e32 v189, v189, v205\n"
    "v_lshlrev_b32_sdwa v205, 1, v179 dst_sel:DWORD dst_unused:UNUSED_PAD src0_sel:DWORD src1_sel:WORD_1\n"
    "v_mfma_f32_16x16x32_bf16 v[172:175], a[196:199], v[148:151], v[172:175]\n"
    "ds_read_u16_d16_hi v203, v203\n"
    "v_cvt_pk_bf16_f32 v96, v96, v97\n"
    "v_mfma_f32_16x16x32_bf16 v[172:175], a[108:111], v[152:155], v[172:175]\n"
    "s_nop 0\n"
    "ds_read_b128 a[244:247], v244 offset:2048\n"
    "v_cvt_pk_bf16_f32 v97, v98, v99\n"
    "v_mfma_f32_16x16x32_bf16 v[176:179], a[144:147], v[156:159], 0\n"
    "s_waitcnt lgkmcnt(8)\n"
    "ds_read_u16 v197, v197\n"
    "v_cvt_pk_bf16_f32 v98, v100, v101\n"
    "v_mfma_f32_16x16x32_bf16 v[176:179], a[192:195], v[160:163], v[176:179]\n"
    "v_cvt_pk_bf16_f32 v99, v102, v103\n"
    "v_or_b32_e64 v190, v190, v206\n"
    "v_mfma_f32_16x16x32_bf16 v[176:179], a[104:107], v[164:167], v[176:179]\n"
    "ds_read_u16_d16_hi v205, v205\n"
    "v_lshlrev_b32_sdwa v206, 1, v181 dst_sel:DWORD dst_unused:UNUSED_PAD src0_sel:DWORD src1_sel:WORD_1\n"
    "v_mfma_f32_16x16x32_bf16 v[100:103], a[4:7], v[184:187], 0\n"
    "s_nop 0\n"
    "v_cvt_pk_bf16_f32 v112, v112, v113\n"
    "v_cvt_pk_bf16_f32 v113, v114, v115\n"
    "ds_write_b128 v242, v[96:99] offset:24640\n"
    "v_mfma_f32_16x16x32_bf16 v[96:99], a[0:3], v[184:187], 0\n"
    "s_waitcnt lgkmcnt(8)\n"
    "v_cvt_pk_bf16_f32 v114, v116, v117\n"
    "v_cvt_pk_bf16_f32 v115, v118, v119\n"
    "v_cvt_pk_bf16_f32 v128, v128, v129\n"
    "v_cvt_pk_bf16_f32 v129, v130, v131\n"
    "ds_write_b128 v242, v[112:115] offset:26688\n"
    "v_mfma_f32_16x16x32_bf16 v[112:115], a[8:11], v[184:187], 0\n"
    "s_waitcnt lgkmcnt(7)\n"
    "v_or_b32_e32 v191, v191, v207\n"
    "v_lshlrev_b32_sdwa v207, 1, v183 dst_sel:DWORD dst_unused:UNUSED_PAD src0_sel:DWORD src1_sel:WORD_1\n"
    "v_mfma_f32_16x16x32_bf16 v[180:183], a[148:151], v[156:159], 0\n"
    "ds_read_u16 v198, v198\n"
    "ds_read_u16_d16_hi v206, v206\n"
    "v_mfma_f32_16x16x32_bf16 v[180:183], a[196:199], v[160:163], v[180:183]\n"
    "ds_read_u16 v199, v199\n"
    "v_cvt_pk_bf16_f32 v130, v132, v133\n"
    "v_mfma_f32_16x16x32_bf16 v[180:183], a[108:111], v[164:167], v[180:183]\n"
    "s_waitcnt lgkmcnt(7)\n"
    "v_or_b32_e32 v193, v193, v201\n"
    "v_cvt_pk_bf16_f32 v131, v134, v135\n"
    "v_mfma_f32_16x16x32_bf16 v[116:119], a[12:15], v[184:187], 0\n"
    "ds_read_u16_d16_hi v207, v207\n"
    "v_cvt_pk_bf16_f32 v104, v104, v105\n"
    "v_mfma_f32_16x16x32_bf16 v[132:135], a[244:247], v[184:187], 0\n"
    "v_cvt_pk_bf16_f32 v105, v106, v107\n"
    "v_cvt_pk_bf16_f32 v106, v108, v109\n"
    "ds_write_b128 v242, v[128:131] offset:28736\n"
    "v_mfma_f32_16x16x32_bf16 v[128:131], v[236:239], v[184:187], 0\n"
    "s_waitcnt lgkmcnt(6)\n"
    "v_cvt_pk_bf16_f32 v107, v110, v111\n"
    "v_or_b32_e32 v194, v194, v202\n"
    "v_mfma_f32_16x16x32_bf16 v[108:111], a[4:7], v[188:191], 0\n"
    "ds_write_b128 v242, v[104:107] offset:25664\n"
    "v_mfma_f32_16x16x32_bf16 v[104:107], a[0:3], v[188:191], 0\n"
    "v_cvt_pk_bf16_f32 v120, v120, v121\n"
    "v_cvt_pk_bf16_f32 v121, v122, v123\n"
    "v_cvt_pk_bf16_f32 v122, v124, v125\n"
    "v_cvt_pk_bf16_f32 v123, v126, v127\n"
    "v_cvt_pk_bf16_f32 v136, v136, v137\n"
    "v_pk_fma_f16 v232, v192, v192, s48\n"
    "v_mfma_f32_16x16x32_bf16 v[124:127], a[12:15], v[188:191], 0\n"
    "s_nop 0\n"
    "ds_write_b128 v242, v[120:123] offset:27712\n"
    "v_mfma_f32_16x16x32_bf16 v[120:123], a[8:11], v[188:191], 0\n"
    "s_waitcnt lgkmcnt(5)\n"
    "v_cvt_pk_bf16_f32 v137, v138, v139\n"
    "v_cvt_pk_bf16_f32 v138, v140, v141\n"
    "v_or_b32_e64 v195, v195, v203\n"
    "v_cvt_pk_bf16_f32 v139, v142, v143\n"
    "v_pk_mul_f16 v232, v192, v232\n"
    "v_or_b32_e64 v197, v197, v205\n"
    "ds_write_b128 v242, v[136:139] offset:29760\n"
    "v_mfma_f32_16x16x32_bf16 v[136:139], v[236:239], v[188:191], 0\n"
    "s_waitcnt lgkmcnt(3)\n"
    "v_exp_f16_e32 v234, v232\n"
    "v_mfma_f32_16x16x32_bf16 v[140:143], a[244:247], v[188:191], 0\n"
    "v_exp_f16_sdwa v234, v232 dst_sel:WORD_1 dst_unused:UNUSED_PRESERVE src0_sel:WORD_1\n"
    "v_cvt_pk_f16_f32 v184, v168, v169\n"
    "v_pk_fma_f16 v234, v234, s49, s49\n"
    "v_lshlrev_b32_sdwa v185, 1, v170 dst_sel:DWORD dst_unused:UNUSED_PAD src0_sel:DWORD src1_sel:WORD_1\n"
    "v_lshlrev_b32_sdwa v201, 1, v171 dst_sel:DWORD dst_unused:UNUSED_PAD src0_sel:DWORD src1_sel:WORD_1\n"
    "v_mfma_f32_16x16x32_bf16 v[168:171], a[152:155], v[144:147], 0\n"
    "ds_read_u16 v185, v185\n"
    "ds_read_u16_d16_hi v201, v201\n"
    "v_mfma_f32_16x16x32_bf16 v[168:171], a[200:203], v[148:151], v[168:171]\n"
    "ds_read_b128 v[236:239], v244 offset:4096\n"
    "v_lshlrev_b32_sdwa v186, 1, v172 dst_sel:DWORD dst_unused:UNUSED_PAD src0_sel:DWORD src1_sel:WORD_1\n"
    "v_lshlrev_b32_sdwa v202, 1, v173 dst_sel:DWORD dst_unused:UNUSED_PAD src0_sel:DWORD src1_sel:WORD_1\n"
    "v_lshlrev_b32_sdwa v187, 1, v174 dst_sel:DWORD dst_unused:UNUSED_PAD src0_sel:DWORD src1_sel:WORD_1\n"
    "v_lshlrev_b32_sdwa v203, 1, v175 dst_sel:DWORD dst_unused:UNUSED_PAD src0_sel:DWORD src1_sel:WORD_1\n"
    "v_rcp_f16_e32 v232, v234\n"
    "v_mfma_f32_16x16x32_bf16 v[168:171], a[112:115], v[152:155], v[168:171]\n"
    "s_waitcnt lgkmcnt(3)\n"
    "ds_read_u16 v186, v186\n"
    "ds_read_u16_d16_hi v202, v202\n"
    "v_or_b32_e32 v198, v198, v206\n"
    "v_rcp_f16_sdwa v232, v234 dst_sel:WORD_1 dst_unused:UNUSED_PRESERVE src0_sel:WORD_1\n"
    "v_mfma_f32_16x16x32_bf16 v[172:175], a[156:159], v[144:147], 0\n"
    "s_barrier\n"
    "v_mfma_f32_16x16x32_bf16 v[172:175], a[204:207], v[148:151], v[172:175]\n"
    "ds_read_u16 v187, v187\n"
    "ds_read_u16_d16_hi v203, v203\n"
    "v_cvt_pk_f16_f32 v188, v176, v177\n"
    "v_lshlrev_b32_sdwa v189, 1, v178 dst_sel:DWORD dst_unused:UNUSED_PAD src0_sel:DWORD src1_sel:WORD_1\n"
    "v_lshlrev_b32_sdwa v205, 1, v179 dst_sel:DWORD dst_unused:UNUSED_PAD src0_sel:DWORD src1_sel:WORD_1\n"
    "v_lshlrev_b32_sdwa v190, 1, v180 dst_sel:DWORD dst_unused:UNUSED_PAD src0_sel:DWORD src1_sel:WORD_1\n"
    "v_lshlrev_b32_sdwa v206, 1, v181 dst_sel:DWORD dst_unused:UNUSED_PAD src0_sel:DWORD src1_sel:WORD_1\n"
    "v_lshlrev_b32_sdwa v191, 1, v182 dst_sel:DWORD dst_unused:UNUSED_PAD src0_sel:DWORD src1_sel:WORD_1\n"
    "v_fma_mix_f32 v234, v192, v232, 0 op_sel_hi:[1,1,0]\n"
    "v_mfma_f32_16x16x32_bf16 v[172:175], a[116:119], v[152:155], v[172:175]\n"
    "ds_read_b128 a[244:247], v244 offset:6144\n"
    "ds_read_u16 v189, v189\n"
    "v_or_b32_e64 v199, v199, v207\n"
    "v_lshlrev_b32_sdwa v207, 1, v183 dst_sel:DWORD dst_unused:UNUSED_PAD src0_sel:DWORD src1_sel:WORD_1\n"
    "v_fma_mix_f32 v232, v192, v232, 0 op_sel:[1,1,0] op_sel_hi:[1,1,0]\n"
    "v_mfma_f32_16x16x32_bf16 v[176:179], a[152:155], v[156:159], 0\n"
    "s_nop 0\n"
    "ds_read_u16_d16_hi v205, v205\n"
    "v_cvt_pk_bf16_f32 v192, v234, v232\n"
    "v_mfma_f32_16x16x32_bf16 v[176:179], a[200:203], v[160:163], v[176:179]\n"
    "s_waitcnt lgkmcnt(7)\n"
    "ds_read_u16 v190, v190\n"
    "v_pk_fma_f16 v232, v196, v196, s48\n"
    "v_mfma_f32_16x16x32_bf16 v[176:179], a[112:115], v[164:167], v[176:179]\n"
    "v_pk_mul_f16 v232, v196, v232\n"
    "v_or_b32_e64 v185, v185, v201\n"
    "v_mfma_f32_16x16x32_bf16 v[180:183], a[156:159], v[156:159], 0\n"
    "ds_read_u16_d16_hi v206, v206\n"
    "v_exp_f16_e64 v234, v232\n"
    "v_mfma_f32_16x16x32_bf16 v[180:183], a[204:207], v[160:163], v[180:183]\n"
    "ds_read_u16 v191, v191\n"
    "v_exp_f16_sdwa v234, v232 dst_sel:WORD_1 dst_unused:UNUSED_PRESERVE src0_sel:WORD_1\n"
    "v_mfma_f32_16x16x32_bf16 v[180:183], a[116:119], v[164:167], v[180:183]\n"
    "s_waitcnt lgkmcnt(8)\n"
    "v_pk_fma_f16 v234, v234, s49, s49\n"
    "v_or_b32_e32 v186, v186, v202\n"
    "v_mfma_f32_16x16x32_bf16 v[96:99], a[16:19], v[192:195], v[96:99]\n"
    "ds_read_u16_d16_hi v207, v207\n"
    "v_rcp_f16_e32 v232, v234\n"
    "v_mfma_f32_16x16x32_bf16 v[100:103], a[20:23], v[192:195], v[100:103]\n"
    "v_rcp_f16_sdwa v232, v234 dst_sel:WORD_1 dst_unused:UNUSED_PRESERVE src0_sel:WORD_1\n"
    "v_mfma_f32_16x16x32_bf16 v[112:115], a[24:27], v[192:195], v[112:115]\n"
    "s_waitcnt lgkmcnt(6)\n"
    "v_fma_mix_f32 v234, v196, v232, 0 op_sel_hi:[1,1,0]\n"
    "v_fma_mix_f32 v232, v196, v232, 0 op_sel:[1,1,0] op_sel_hi:[1,1,0]\n"
    "v_mfma_f32_16x16x32_bf16 v[116:119], a[28:31], v[192:195], v[116:119]\n"
    "ds_read_b128 a[240:243], v244 offset:34816\n"
    "v_cvt_pk_bf16_f32 v196, v234, v232\n"
    "v_mfma_f32_16x16x32_bf16 v[128:131], v[236:239], v[192:195], v[128:131]\n"
    "v_pk_fma_f16 v232, v184, v184, s48\n"
    "v_or_b32_e64 v187, v187, v203\n"
    "v_mfma_f32_16x16x32_bf16 v[132:135], a[244:247], v[192:195], v[132:135]\n"
    "v_pk_mul_f16 v232, v184, v232\n"
    "v_lshlrev_b32_sdwa v192, 1, v168 dst_sel:DWORD dst_unused:UNUSED_PAD src0_sel:DWORD src1_sel:WORD_1\n"
    "v_mfma_f32_16x16x32_bf16 v[104:107], a[16:19], v[196:199], v[104:107]\n"
    "s_waitcnt lgkmcnt(5)\n"
    "ds_read_u16 v192, v192\n"
    "v_exp_f16_e32 v234, v232\n"
    "v_mfma_f32_16x16x32_bf16 v[108:111], a[20:23], v[196:199], v[108:111]\n"
    "v_exp_f16_sdwa v234, v232 dst_sel:WORD_1 dst_unused:UNUSED_PRESERVE src0_sel:WORD_1\n"
    "v_mfma_f32_16x16x32_bf16 v[120:123], a[24:27], v[196:199], v[120:123]\n"
    "v_or_b32_e64 v189, v189, v205\n"
    "v_pk_fma_f16 v234, v234, s49, s49\n"
    "v_mfma_f32_16x16x32_bf16 v[124:127], a[28:31], v[196:199], v[124:127]\n"
    "s_nop 0\n"
    "v_lshlrev_b32_sdwa v200, 1, v169 dst_sel:DWORD dst_unused:UNUSED_PAD src0_sel:DWORD src1_sel:WORD_1\n"
    "v_lshlrev_b32_sdwa v193, 1, v170 dst_sel:DWORD dst_unused:UNUSED_PAD src0_sel:DWORD src1_sel:WORD_1\n"
    "v_mfma_f32_16x16x32_bf16 v[136:139], v[236:239], v[196:199], v[136:139]\n"
    "s_waitcnt lgkmcnt(4)\n"
    "ds_read_u16_d16_hi v200, v200\n"
    "ds_read_u16 v193, v193\n"
    "v_mfma_f32_16x16x32_bf16 v[140:143], a[244:247], v[196:199], v[140:143]\n"
    "ds_read_b128 v[236:239], v244 offset:8192\n"
    "v_lshlrev_b32_sdwa v201, 1, v171 dst_sel:DWORD dst_unused:UNUSED_PAD src0_sel:DWORD src1_sel:WORD_1\n"
    "v_mfma_f32_16x16x32_bf16 v[168:171], a[160:163], v[144:147], 0\n"
    "s_nop 0\n"
    "ds_read_u16_d16_hi v201, v201\n"
    "v_lshlrev_b32_sdwa v194, 1, v172 dst_sel:DWORD dst_unused:UNUSED_PAD src0_sel:DWORD src1_sel:WORD_1\n"
    "v_mfma_f32_16x16x32_bf16 v[168:171], a[208:211], v[148:151], v[168:171]\n"
    "s_waitcnt lgkmcnt(5)\n"
    "ds_read_u16 v194, v194\n"
    "v_lshlrev_b32_sdwa v202, 1, v173 dst_sel:DWORD dst_unused:UNUSED_PAD src0_sel:DWORD src1_sel:WORD_1\n"
    "v_lshlrev_b32_sdwa v195, 1, v174 dst_sel:DWORD dst_unused:UNUSED_PAD src0_sel:DWORD src1_sel:WORD_1\n"
    "v_lshlrev_b32_sdwa v203, 1, v175 dst_sel:DWORD dst_unused:UNUSED_PAD src0_sel:DWORD src1_sel:WORD_1\n"
    "v_rcp_f16_e64 v232, v234\n"
    "v_mfma_f32_16x16x32_bf16 v[168:171], a[120:123], v[152:155], v[168:171]\n"
    "ds_read_u16_d16_hi v202, v202\n"
    "v_or_b32_e64 v190, v190, v206\n"
    "v_rcp_f16_sdwa v232, v234 dst_sel:WORD_1 dst_unused:UNUSED_PRESERVE src0_sel:WORD_1\n"
    "v_mfma_f32_16x16x32_bf16 v[172:175], a[164:167], v[144:147], 0\n"
    "ds_read_u16 v195, v195\n"
    "v_lshlrev_b32_sdwa v196, 1, v176 dst_sel:DWORD dst_unused:UNUSED_PAD src0_sel:DWORD src1_sel:WORD_1\n"
    "v_mfma_f32_16x16x32_bf16 v[172:175], a[212:215], v[148:151], v[172:175]\n"
    "ds_read_b128 a[244:247], v244 offset:10240\n"
    "v_lshlrev_b32_sdwa v204, 1, v177 dst_sel:DWORD dst_unused:UNUSED_PAD src0_sel:DWORD src1_sel:WORD_1\n"
    "v_lshlrev_b32_sdwa v197, 1, v178 dst_sel:DWORD dst_unused:UNUSED_PAD src0_sel:DWORD src1_sel:WORD_1\n"
    "v_lshlrev_b32_sdwa v205, 1, v179 dst_sel:DWORD dst_unused:UNUSED_PAD src0_sel:DWORD src1_sel:WORD_1\n"
    "v_lshlrev_b32_sdwa v198, 1, v180 dst_sel:DWORD dst_unused:UNUSED_PAD src0_sel:DWORD src1_sel:WORD_1\n"
    "v_lshlrev_b32_sdwa v206, 1, v181 dst_sel:DWORD dst_unused:UNUSED_PAD src0_sel:DWORD src1_sel:WORD_1\n"
    "v_lshlrev_b32_sdwa v199, 1, v182 dst_sel:DWORD dst_unused:UNUSED_PAD src0_sel:DWORD src1_sel:WORD_1\n"
    "v_fma_mix_f32 v234, v184, v232, 0 op_sel_hi:[1,1,0]\n"
    "v_mfma_f32_16x16x32_bf16 v[172:175], a[240:243], v[152:155], v[172:175]\n"
    "s_waitcnt lgkmcnt(7)\n"
    "ds_read_u16_d16_hi v203, v203\n"
    "ds_read_u16 v196, v196\n"
    "v_or_b32_e32 v191, v191, v207\n"
    "v_lshlrev_b32_sdwa v207, 1, v183 dst_sel:DWORD dst_unused:UNUSED_PAD src0_sel:DWORD src1_sel:WORD_1\n"
    "v_fma_mix_f32 v232, v184, v232, 0 op_sel:[1,1,0] op_sel_hi:[1,1,0]\n"
    "v_mfma_f32_16x16x32_bf16 v[176:179], a[160:163], v[156:159], 0\n"
    "v_cvt_pk_bf16_f32 v184, v234, v232\n"
    "v_pk_fma_f16 v232, v188, v188, s48\n"
    "v_mfma_f32_16x16x32_bf16 v[176:179], a[208:211], v[160:163], v[176:179]\n"
    "ds_read_u16_d16_hi v204, v204\n"
    "v_pk_mul_f16 v232, v188, v232\n"
    "v_mfma_f32_16x16x32_bf16 v[176:179], a[120:123], v[164:167], v[176:179]\n"
    "ds_read_u16 v197, v197\n"
    "v_exp_f16_e64 v234, v232\n"
    "s_waitcnt vmcnt(1)\n"
    "v_smfmac_f32_32x32x32_bf16 v[112:127], a[248:251], a[232:239], v241\n"
    "s_waitcnt lgkmcnt(8)\n"
    "ds_read_b128 a[232:235], v243 offset:24640\n"
    "v_exp_f16_sdwa v234, v232 dst_sel:WORD_1 dst_unused:UNUSED_PRESERVE src0_sel:WORD_1\n"
    "v_or_b32_e64 v192, v192, v200\n"
    "v_pk_fma_f16 v234, v234, s49, s49\n"
    "s_waitcnt vmcnt(0)\n"
    "v_smfmac_f32_32x32x32_bf16 v[96:111], a[248:251], a[224:231], v241\n"
    "s_nop 0\n"
    "buffer_load_dwordx4 a[224:227], v245, s[40:43], s58 offen offset:128\n"
    "ds_read_b128 a[236:239], v243 offset:25664\n"
    "v_rcp_f16_e64 v232, v234\n"
    "v_or_b32_e32 v193, v193, v201\n"
    "v_mfma_f32_16x16x32_bf16 v[180:183], a[164:167], v[156:159], 0\n"
    "s_waitcnt lgkmcnt(8)\n"
    "ds_read_u16_d16_hi v205, v205\n"
    "ds_read_u16 v198, v198\n"
    "v_mfma_f32_16x16x32_bf16 v[180:183], a[212:215], v[160:163], v[180:183]\n"
    "v_rcp_f16_sdwa v232, v234 dst_sel:WORD_1 dst_unused:UNUSED_PRESERVE src0_sel:WORD_1\n"
    "v_mfma_f32_16x16x32_bf16 v[180:183], a[240:243], v[164:167], v[180:183]\n"
    "s_nop 0\n"
    "ds_read_u16_d16_hi v206, v206\n"
    "v_fma_mix_f32 v234, v188, v232, 0 op_sel_hi:[1,1,0]\n"
    "v_mfma_f32_16x16x32_bf16 v[112:115], a[40:43], v[184:187], v[112:115]\n"
    "s_waitcnt lgkmcnt(8)\n"
    "v_fma_mix_f32 v232, v188, v232, 0 op_sel:[1,1,0] op_sel_hi:[1,1,0]\n"
    "v_lshlrev_b32_sdwa v201, 1, v171 dst_sel:DWORD dst_unused:UNUSED_PAD src0_sel:DWORD src1_sel:WORD_1\n"
    "v_mfma_f32_16x16x32_bf16 v[116:119], a[44:47], v[184:187], v[116:119]\n"
    "ds_read_u16 v199, v199\n"
    "v_cvt_pk_bf16_f32 v188, v234, v232\n"
    "v_mfma_f32_16x16x32_bf16 v[128:131], v[236:239], v[184:187], v[128:131]\n"
    "ds_read_u16_d16_hi v207, v207\n"
    "v_or_b32_e64 v194, v194, v202\n"
    "v_mfma_f32_16x16x32_bf16 v[132:135], a[244:247], v[184:187], v[132:135]\n"
    "v_lshlrev_b32_sdwa v202, 1, v173 dst_sel:DWORD dst_unused:UNUSED_PAD src0_sel:DWORD src1_sel:WORD_1\n"
    "v_or_b32_e32 v195, v195, v203\n"
    "v_mfma_f32_16x16x32_bf16 v[120:123], a[40:43], v[188:191], v[120:123]\n"
    "s_waitcnt lgkmcnt(8)\n"
    "ds_read_b128 a[240:243], v244 offset:38912\n"
    "v_lshlrev_b32_sdwa v203, 1, v175 dst_sel:DWORD dst_unused:UNUSED_PAD src0_sel:DWORD src1_sel:WORD_1\n"
    "v_mfma_f32_16x16x32_bf16 v[124:127], a[44:47], v[188:191], v[124:127]\n"
    "buffer_load_dwordx4 a[228:231], v245, s[40:43], s101 offen offset:128\n"
    "ds_read_u16_d16_hi v201, v201\n"
    "v_mfma_f32_16x16x32_bf16 v[136:139], v[236:239], v[188:191], v[136:139]\n"
    "s_waitcnt lgkmcnt(7)\n"
    "v_or_b32_e32 v196, v196, v204\n"
    "v_smfmac_f32_32x32x32_bf16 v[96:111], a[248:251], a[232:239], v241\n"
    "ds_read_b128 a[232:235], v243 offset:26688\n"
    "ds_read_b128 a[236:239], v243 offset:27712\n"
    "v_mfma_f32_16x16x32_bf16 v[140:143], a[244:247], v[188:191], v[140:143]\n"
    "s_waitcnt lgkmcnt(8)\n"
    "ds_read_u16_d16_hi v202, v202\n"
    "v_or_b32_e32 v197, v197, v205\n"
    "v_lshlrev_b32_sdwa v205, 1, v179 dst_sel:DWORD dst_unused:UNUSED_PAD src0_sel:DWORD src1_sel:WORD_1\n"
    "ds_read_b128 v[236:239], v244 offset:12288\n"
    "ds_read_u16_d16_hi v203, v203\n"
    "ds_read_b128 a[244:247], v244 offset:14336\n"
    "v_mfma_f32_16x16x32_bf16 v[96:99], a[32:35], v[184:187], v[96:99]\n"
    "s_nop 0\n"
    "v_mfma_f32_16x16x32_bf16 v[100:103], a[36:39], v[184:187], v[100:103]\n"
    "s_waitcnt lgkmcnt(10)\n"
    "v_cvt_pk_f16_f32 v184, v168, v169\n"
    "v_lshlrev_b32_sdwa v185, 1, v170 dst_sel:DWORD dst_unused:UNUSED_PAD src0_sel:DWORD src1_sel:WORD_1\n"
    "v_mfma_f32_16x16x32_bf16 v[104:107], a[32:35], v[188:191], v[104:107]\n"
    "v_lshlrev_b32_sdwa v186, 1, v172 dst_sel:DWORD dst_unused:UNUSED_PAD src0_sel:DWORD src1_sel:WORD_1\n"
    "v_lshlrev_b32_sdwa v187, 1, v174 dst_sel:DWORD dst_unused:UNUSED_PAD src0_sel:DWORD src1_sel:WORD_1\n"
    "v_mfma_f32_16x16x32_bf16 v[108:111], a[36:39], v[188:191], v[108:111]\n"
    "ds_read_u16 v185, v185\n"
    "v_cvt_pk_f16_f32 v188, v176, v177\n"
    "v_mfma_f32_16x16x32_bf16 v[168:171], a[168:171], v[144:147], 0\n"
    "s_nop 0\n"
    "v_lshlrev_b32_sdwa v189, 1, v178 dst_sel:DWORD dst_unused:UNUSED_PAD src0_sel:DWORD src1_sel:WORD_1\n"
    "v_lshlrev_b32_sdwa v190, 1, v180 dst_sel:DWORD dst_unused:UNUSED_PAD src0_sel:DWORD src1_sel:WORD_1\n"
    "v_mfma_f32_16x16x32_bf16 v[168:171], a[216:219], v[148:151], v[168:171]\n"
    "s_waitcnt lgkmcnt(8)\n"
    "ds_read_u16 v186, v186\n"
    "v_lshlrev_b32_sdwa v191, 1, v182 dst_sel:DWORD dst_unused:UNUSED_PAD src0_sel:DWORD src1_sel:WORD_1\n"
    "v_mfma_f32_16x16x32_bf16 v[168:171], a[124:127], v[152:155], v[168:171]\n"
    "ds_read_u16 v187, v187\n"
    "v_or_b32_e64 v198, v198, v206\n"
    "v_mfma_f32_16x16x32_bf16 v[172:175], a[172:175], v[144:147], 0\n"
    "v_lshlrev_b32_sdwa v206, 1, v181 dst_sel:DWORD dst_unused:UNUSED_PAD src0_sel:DWORD src1_sel:WORD_1\n"
    "v_or_b32_e32 v199, v199, v207\n"
    "v_mfma_f32_16x16x32_bf16 v[172:175], a[220:223], v[148:151], v[172:175]\n"
    "s_waitcnt lgkmcnt(7)\n"
    "ds_read_u16 v189, v189\n"
    "v_lshlrev_b32_sdwa v207, 1, v183 dst_sel:DWORD dst_unused:UNUSED_PAD src0_sel:DWORD src1_sel:WORD_1\n"
    "v_smfmac_f32_32x32x32_bf16 v[112:127], a[248:251], a[232:239], v241\n"
    "v_pk_fma_f16 v232, v184, v184, s48\n"
    "v_pk_mul_f16 v232, v184, v232\n"
    "v_exp_f16_e64 v234, v232\n"
    "ds_read_b128 a[232:235], v243 offset:28736\n"
    "ds_read_u16_d16_hi v205, v205\n"
    "v_mfma_f32_16x16x32_bf16 v[172:175], a[240:243], v[152:155], v[172:175]\n"
    "s_waitcnt lgkmcnt(8)\n"
    "v_exp_f16_sdwa v234, v232 dst_sel:WORD_1 dst_unused:UNUSED_PRESERVE src0_sel:WORD_1\n"
    "v_mfma_f32_16x16x32_bf16 v[176:179], a[168:171], v[156:159], 0\n"
    "s_nop 0\n"
    "ds_read_b128 a[236:239], v243 offset:29760\n"
    "v_pk_fma_f16 v234, v234, s49, s49\n"
    "v_mfma_f32_16x16x32_bf16 v[176:179], a[216:219], v[160:163], v[176:179]\n"
    "s_waitcnt lgkmcnt(7)\n"
    "ds_read_u16 v190, v190\n"
    "v_rcp_f16_e32 v232, v234\n"
    "v_mfma_f32_16x16x32_bf16 v[176:179], a[124:127], v[164:167], v[176:179]\n"
    "ds_read_u16_d16_hi v206, v206\n"
    "v_rcp_f16_sdwa v232, v234 dst_sel:WORD_1 dst_unused:UNUSED_PRESERVE src0_sel:WORD_1\n"
    "v_mfma_f32_16x16x32_bf16 v[180:183], a[172:175], v[156:159], 0\n"
    "v_fma_mix_f32 v234, v184, v232, 0 op_sel_hi:[1,1,0]\n"
    "s_waitcnt lgkmcnt(8)\n"
    "v_or_b32_e32 v185, v185, v201\n"
    "v_mfma_f32_16x16x32_bf16 v[180:183], a[220:223], v[160:163], v[180:183]\n"
    "ds_read_u16 v191, v191\n"
    "v_lshlrev_b32_sdwa v201, 1, v171 dst_sel:DWORD dst_unused:UNUSED_PAD src0_sel:DWORD src1_sel:WORD_1\n"
    "v_mfma_f32_16x16x32_bf16 v[180:183], a[240:243], v[164:167], v[180:183]\n"
    "s_waitcnt lgkmcnt(8)\n"
    "ds_read_u16_d16_hi v207, v207\n"
    "v_or_b32_e32 v186, v186, v202\n"
    "v_mfma_f32_16x16x32_bf16 v[96:99], a[48:51], v[192:195], v[96:99]\n"
    "v_lshlrev_b32_sdwa v202, 1, v173 dst_sel:DWORD dst_unused:UNUSED_PAD src0_sel:DWORD src1_sel:WORD_1\n"
    "v_fma_mix_f32 v232, v184, v232, 0 op_sel:[1,1,0] op_sel_hi:[1,1,0]\n"
    "v_mfma_f32_16x16x32_bf16 v[100:103], a[52:55], v[192:195], v[100:103]\n"
    "s_waitcnt lgkmcnt(8)\n"
    "ds_read_u16_d16_hi v201, v201\n"
    "v_or_b32_e32 v187, v187, v203\n"
    "v_mfma_f32_16x16x32_bf16 v[112:115], a[56:59], v[192:195], v[112:115]\n"
    "v_lshlrev_b32_sdwa v203, 1, v175 dst_sel:DWORD dst_unused:UNUSED_PAD src0_sel:DWORD src1_sel:WORD_1\n"
    "v_cvt_pk_bf16_f32 v184, v234, v232\n"
    "v_mfma_f32_16x16x32_bf16 v[116:119], a[60:63], v[192:195], v[116:119]\n"
    "ds_read_u16_d16_hi v202, v202\n"
    "v_pk_fma_f16 v232, v188, v188, s48\n"
    "v_mfma_f32_16x16x32_bf16 v[128:131], v[236:239], v[192:195], v[128:131]\n"
    "v_pk_mul_f16 v232, v188, v232\n"
    "v_exp_f16_e32 v234, v232\n"
    "v_mfma_f32_16x16x32_bf16 v[132:135], a[244:247], v[192:195], v[132:135]\n"
    "s_waitcnt lgkmcnt(6)\n"
    "ds_read_u16_d16_hi v203, v203\n"
    "v_or_b32_e64 v189, v189, v205\n"
    "v_mfma_f32_16x16x32_bf16 v[104:107], a[48:51], v[196:199], v[104:107]\n"
    "v_cvt_pk_f16_f32 v192, v168, v169\n"
    "v_lshlrev_b32_sdwa v193, 1, v170 dst_sel:DWORD dst_unused:UNUSED_PAD src0_sel:DWORD src1_sel:WORD_1\n"
    "v_mfma_f32_16x16x32_bf16 v[108:111], a[52:55], v[196:199], v[108:111]\n"
    "ds_read_u16 v193, v193\n"
    "v_lshlrev_b32_sdwa v194, 1, v172 dst_sel:DWORD dst_unused:UNUSED_PAD src0_sel:DWORD src1_sel:WORD_1\n"
    "v_mfma_f32_16x16x32_bf16 v[120:123], a[56:59], v[196:199], v[120:123]\n"
    "ds_read_u16 v194, v194\n"
    "v_lshlrev_b32_sdwa v195, 1, v174 dst_sel:DWORD dst_unused:UNUSED_PAD src0_sel:DWORD src1_sel:WORD_1\n"
    "v_mfma_f32_16x16x32_bf16 v[124:127], a[60:63], v[196:199], v[124:127]\n"
    "v_lshlrev_b32_sdwa v205, 1, v179 dst_sel:DWORD dst_unused:UNUSED_PAD src0_sel:DWORD src1_sel:WORD_1\n"
    "s_waitcnt lgkmcnt(7)\n"
    "v_or_b32_e32 v190, v190, v206\n"
    "v_mfma_f32_16x16x32_bf16 v[136:139], v[236:239], v[196:199], v[136:139]\n"
    "ds_read_b128 v[236:239], v244 offset:16384\n"
    "ds_read_u16 v195, v195\n"
    "v_mfma_f32_16x16x32_bf16 v[140:143], a[244:247], v[196:199], v[140:143]\n"
    "v_cvt_pk_f16_f32 v196, v176, v177\n"
    "v_lshlrev_b32_sdwa v197, 1, v178 dst_sel:DWORD dst_unused:UNUSED_PAD src0_sel:DWORD src1_sel:WORD_1\n"
    "v_lshlrev_b32_sdwa v198, 1, v180 dst_sel:DWORD dst_unused:UNUSED_PAD src0_sel:DWORD src1_sel:WORD_1\n"
    "v_lshlrev_b32_sdwa v206, 1, v181 dst_sel:DWORD dst_unused:UNUSED_PAD src0_sel:DWORD src1_sel:WORD_1\n"
    "v_lshlrev_b32_sdwa v199, 1, v182 dst_sel:DWORD dst_unused:UNUSED_PAD src0_sel:DWORD src1_sel:WORD_1\n"
    "v_exp_f16_sdwa v234, v232 dst_sel:WORD_1 dst_unused:UNUSED_PRESERVE src0_sel:WORD_1\n"
    "v_smfmac_f32_32x32x32_bf16 v[128:143], a[248:251], a[232:239], v241\n"
    "s_waitcnt lgkmcnt(7)\n"
    "ds_read_b128 a[244:247], v244 offset:18432\n"
    "ds_read_u16 v197, v197\n"
    "v_or_b32_e32 v191, v191, v207\n"
    "v_lshlrev_b32_sdwa v207, 1, v183 dst_sel:DWORD dst_unused:UNUSED_PAD src0_sel:DWORD src1_sel:WORD_1\n"
    "v_pk_fma_f16 v234, v234, s49, s49\n"
    "v_mfma_f32_16x16x32_bf16 v[96:99], a[64:67], v[184:187], v[96:99]\n"
    "ds_read_u16_d16_hi v205, v205\n"
    "v_rcp_f16_e64 v232, v234\n"
    "v_mfma_f32_16x16x32_bf16 v[100:103], a[68:71], v[184:187], v[100:103]\n"
    "ds_read_u16 v198, v198\n"
    "v_rcp_f16_sdwa v232, v234 dst_sel:WORD_1 dst_unused:UNUSED_PRESERVE src0_sel:WORD_1\n"
    "v_mfma_f32_16x16x32_bf16 v[112:115], a[72:75], v[184:187], v[112:115]\n"
    "v_fma_mix_f32 v234, v188, v232, 0 op_sel_hi:[1,1,0]\n"
    "v_fma_mix_f32 v232, v188, v232, 0 op_sel:[1,1,0] op_sel_hi:[1,1,0]\n"
    "v_mfma_f32_16x16x32_bf16 v[116:119], a[76:79], v[184:187], v[116:119]\n"
    "ds_read_u16_d16_hi v206, v206\n"
    "v_cvt_pk_bf16_f32 v188, v234, v232\n"
    "v_pk_fma_f16 v232, v192, v192, s48\n"
    "s_waitcnt lgkmcnt(8)\n"
    "v_or_b32_e32 v193, v193, v201\n"
    "v_mfma_f32_16x16x32_bf16 v[104:107], a[64:67], v[188:191], v[104:107]\n"
    "ds_read_u16 v199, v199\n"
    "v_pk_mul_f16 v232, v192, v232\n"
    "s_waitcnt lgkmcnt(7)\n"
    "v_mfma_f32_16x16x32_bf16 v[128:131], v[236:239], v[184:187], v[128:131]\n"
    "s_nop 0\n"
    "ds_read_u16_d16_hi v207, v207\n"
    "v_or_b32_e64 v194, v194, v202\n"
    "v_mfma_f32_16x16x32_bf16 v[108:111], a[68:71], v[188:191], v[108:111]\n"
    "v_exp_f16_e64 v234, v232\n"
    "v_mfma_f32_16x16x32_bf16 v[120:123], a[72:75], v[188:191], v[120:123]\n"
    "s_waitcnt lgkmcnt(6)\n"
    "v_or_b32_e32 v195, v195, v203\n"
    "v_exp_f16_sdwa v234, v232 dst_sel:WORD_1 dst_unused:UNUSED_PRESERVE src0_sel:WORD_1\n"
    "v_mfma_f32_16x16x32_bf16 v[132:135], a[244:247], v[184:187], v[132:135]\n"
    "v_pk_fma_f16 v234, v234, s49, s49\n"
    "v_rcp_f16_e64 v232, v234\n"
    "v_mfma_f32_16x16x32_bf16 v[124:127], a[76:79], v[188:191], v[124:127]\n"
    "v_rcp_f16_sdwa v232, v234 dst_sel:WORD_1 dst_unused:UNUSED_PRESERVE src0_sel:WORD_1\n"
    "v_mfma_f32_16x16x32_bf16 v[136:139], v[236:239], v[188:191], v[136:139]\n"
    "ds_read_b128 v[236:239], v244 offset:20480\n"
    "v_fma_mix_f32 v234, v192, v232, 0 op_sel_hi:[1,1,0]\n"
    "v_mfma_f32_16x16x32_bf16 v[140:143], a[244:247], v[188:191], v[140:143]\n"
    "ds_read_b128 a[244:247], v244 offset:22528\n"
    "s_waitcnt lgkmcnt(6)\n"
    "v_or_b32_e32 v197, v197, v205\n"
    "v_fma_mix_f32 v232, v192, v232, 0 op_sel:[1,1,0] op_sel_hi:[1,1,0]\n"
    "v_cvt_pk_bf16_f32 v192, v234, v232\n"
    "v_pk_fma_f16 v232, v196, v196, s48\n"
    "v_pk_mul_f16 v232, v196, v232\n"
    "v_exp_f16_e64 v234, v232\n"
    "v_mfma_f32_16x16x32_bf16 v[96:99], a[80:83], v[192:195], v[96:99]\n"
    "v_exp_f16_sdwa v234, v232 dst_sel:WORD_1 dst_unused:UNUSED_PRESERVE src0_sel:WORD_1\n"
    "v_mfma_f32_16x16x32_bf16 v[100:103], a[84:87], v[192:195], v[100:103]\n"
    "s_waitcnt lgkmcnt(4)\n"
    "v_or_b32_e32 v198, v198, v206\n"
    "v_pk_fma_f16 v234, v234, s49, s49\n"
    "v_rcp_f16_e64 v232, v234\n"
    "v_mfma_f32_16x16x32_bf16 v[112:115], a[88:91], v[192:195], v[112:115]\n"
    "v_rcp_f16_sdwa v232, v234 dst_sel:WORD_1 dst_unused:UNUSED_PRESERVE src0_sel:WORD_1\n"
    "v_mfma_f32_16x16x32_bf16 v[116:119], a[92:95], v[192:195], v[116:119]\n"
    "v_fma_mix_f32 v234, v196, v232, 0 op_sel_hi:[1,1,0]\n"
    "v_fma_mix_f32 v232, v196, v232, 0 op_sel:[1,1,0] op_sel_hi:[1,1,0]\n"
    "v_cvt_pk_bf16_f32 v96, v96, v97\n"
    "v_cvt_pk_bf16_f32 v196, v234, v232\n"
    "s_waitcnt lgkmcnt(1)\n"
    "v_or_b32_e32 v199, v199, v207\n"
    "v_cvt_pk_bf16_f32 v97, v98, v99\n"
    "v_cvt_pk_bf16_f32 v98, v100, v101\n"
    "v_mfma_f32_16x16x32_bf16 v[104:107], a[80:83], v[196:199], v[104:107]\n"
    "v_cvt_pk_bf16_f32 v99, v102, v103\n"
    "v_cvt_pk_bf16_f32 v112, v112, v113\n"
    "v_mfma_f32_16x16x32_bf16 v[108:111], a[84:87], v[196:199], v[108:111]\n"
    "ds_write_b128 v243, v[96:99] offset:24640\n"
    "v_mfma_f32_16x16x32_bf16 v[120:123], a[88:91], v[196:199], v[120:123]\n"
    "v_cvt_pk_bf16_f32 v113, v114, v115\n"
    "v_cvt_pk_bf16_f32 v114, v116, v117\n"
    "v_mfma_f32_16x16x32_bf16 v[124:127], a[92:95], v[196:199], v[124:127]\n"
    "v_cvt_pk_bf16_f32 v115, v118, v119\n"
    "v_cvt_pk_bf16_f32 v104, v104, v105\n"
    "ds_write_b128 v243, v[112:115] offset:26688\n"
    "v_cvt_pk_bf16_f32 v105, v106, v107\n"
    "v_cvt_pk_bf16_f32 v106, v108, v109\n"
    "v_cvt_pk_bf16_f32 v107, v110, v111\n"
    "v_cvt_pk_bf16_f32 v120, v120, v121\n"
    "v_cvt_pk_bf16_f32 v121, v122, v123\n"
    "v_cvt_pk_bf16_f32 v122, v124, v125\n"
    "ds_write_b128 v243, v[104:107] offset:25664\n"
    "v_cvt_pk_bf16_f32 v123, v126, v127\n"
    "ds_write_b128 v243, v[120:123] offset:27712\n"
    "s_waitcnt vmcnt(0)\n"
    "v_smfmac_f32_32x32x32_bf16 v[128:143], a[248:251], a[224:231], v241\n"
    "s_waitcnt lgkmcnt(0)\n"
    "s_nop 8\n"
    "v_mfma_f32_16x16x32_bf16 v[128:131], v[236:239], v[192:195], v[128:131]\n"
    "v_mfma_f32_16x16x32_bf16 v[132:135], a[244:247], v[192:195], v[132:135]\n"
    "v_mfma_f32_16x16x32_bf16 v[136:139], v[236:239], v[196:199], v[136:139]\n"
    "v_mfma_f32_16x16x32_bf16 v[140:143], a[244:247], v[196:199], v[140:143]\n"
    "s_nop 4\n"
    "v_cvt_pk_bf16_f32 v128, v128, v129\n"
    "v_cvt_pk_bf16_f32 v129, v130, v131\n"
    "v_cvt_pk_bf16_f32 v130, v132, v133\n"
    "v_cvt_pk_bf16_f32 v131, v134, v135\n"
    "ds_write_b128 v243, v[128:131] offset:28736\n"
    "v_cvt_pk_bf16_f32 v136, v136, v137\n"
    "v_cvt_pk_bf16_f32 v137, v138, v139\n"
    "v_cvt_pk_bf16_f32 v138, v140, v141\n"
    "v_cvt_pk_bf16_f32 v139, v142, v143\n"
    "ds_write_b128 v243, v[136:139] offset:29760\n"
    "ds_read_b128 a[224:227], v249 offset:0\n"
    "ds_read_b128 a[228:231], v249 offset:2048\n"
    "ds_read_b128 a[232:235], v249 offset:4096\n"
    "ds_read_b128 a[236:239], v249 offset:1024\n"
    "ds_read_b128 a[252:255], v249 offset:3072\n"
    "ds_read_b128 a[240:243], v249 offset:5120\n"
    "s_waitcnt lgkmcnt(5)\n"
    "buffer_store_dwordx4 a[224:227], v255, s[44:47], s58 offen offset:0 sc1\n"
    "s_waitcnt lgkmcnt(4)\n"
    "buffer_store_dwordx4 a[228:231], v255, s[44:47], s58 offen offset:64 sc1\n"
    "s_waitcnt lgkmcnt(3)\n"
    "buffer_store_dwordx4 a[232:235], v255, s[44:47], s58 offen offset:128 sc1\n"
    "s_waitcnt lgkmcnt(1)\n"
    "buffer_store_dwordx4 a[236:239], v255, s[44:47], s101 offen offset:0 sc1\n"
    "buffer_store_dwordx4 a[252:255], v255, s[44:47], s101 offen offset:64 sc1\n"
    "s_waitcnt lgkmcnt(0)\n"
    "buffer_store_dwordx4 a[240:243], v255, s[44:47], s101 offen offset:128 sc1\n"
    "97:\n"
    "98:\n"
    "s_waitcnt vmcnt(0) lgkmcnt(0)\n"
    ".Lreplk_end:\n"
    : "+{s66}"(flg), "+{s[76:79]}"(rt), "+{s[48:55]}"(S), "+{v[208:215]}"(PA)
    : "{s[40:43]}"(rx), "{s[44:47]}"(srd(out, XBYTES)), "{s56}"((u32)(L_XT - L_H - w * GS)), "{s38}"((u32)(L_W + w * 9216)),
      "{s36}"((u32)((L_W + (w & 1) * 1024) * 8)), "{s37}"((u32)(w * 4096 + (tx & 1) * 16384)),
      "{s67}"(0x3fffffffu | (tx == 0 ? 0x80000000u : 0u) | (tx == 31 ? 0x40000000u : 0u)), "{s[96:99]}"(T), "{s[80:87]}"(rw), "{s[88:95]}"(rd)
    : "memory", "scc", "vcc", "m0", "v0", "v1", "v2", "v3", "v4", "v5", "v6", "v7", "v8", "v9", "v10", "v11", "v12", "v13", "v14", "v15", "v16", "v17", "v18", "v19", "v20", "v21", "v22", "v23", "v24", "v25", "v26", "v27", "v28", "v29", "v30", "v31", "v32", "v33", "v34", "v35", "v36", "v37", "v38", "v39", "v40", "v41", "v42", "v43", "v44", "v45", "v46", "v47", "v48", "v49", "v50", "v51", "v52", "v53", "v54", "v55", "v56", "v57", "v58", "v59", "v60", "v61", "v62", "v63", "v64", "v65", "v66", "v67", "v68", "v69", "v70", "v71", "v72", "v73", "v74", "v75", "v76", "v77", "v78", "v79", "v80", "v81", "v82", "v83", "v84", "v85", "v86", "v87", "v88", "v89", "v90", "v91", "v92", "v93", "v94", "v95", "v96", "v97", "v98", "v99", "v100", "v101", "v102", "v103", "v104", "v105", "v106", "v107", "v108", "v109", "v110", "v111", "v112", "v113", "v114", "v115", "v116", "v117", "v118", "v119", "v120", "v121", "v122", "v123", "v124", "v125", "v126", "v127", "v128", "v129", "v130", "v131", "v132", "v133", "v134", "v135", "v136", "v137", "v138", "v139", "v140", "v141", "v142", "v143", "v144", "v145", "v146", "v147", "v148", "v149", "v150", "v151", "v152", "v153", "v154", "v155", "v156", "v157", "v158", "v159", "v160", "v161", "v162", "v163", "v164", "v165", "v166", "v167", "v168", "v169", "v170", "v171", "v172", "v173", "v174", "v175", "v176", "v177", "v178", "v179", "v180", "v181", "v182", "v183", "v184", "v185", "v186", "v187", "v188", "v189", "v190", "v191", "v192", "v193", "v194", "v195", "v196", "v197", "v198", "v199", "v200", "v201", "v202", "v203", "v204", "v205", "v206", "v207", "v216", "v217", "v218", "v219", "v220", "v221", "v222", "v223", "v224", "v225", "v226", "v227", "v228", "v229", "v230", "v231", "v232", "v233", "v234", "v235", "v236", "v237", "v238", "v239", "v240", "v241", "v242", "v243", "v244", "v245", "v246", "v247", "v248", "v249", "v250", "v251", "v252", "v253", "v254", "v255", "a0", "a1", "a2", "a3", "a4", "a5", "a6", "a7", "a8", "a9", "a10", "a11", "a12", "a13", "a14", "a15", "a16", "a17", "a18", "a19", "a20", "a21", "a22", "a23", "a24", "a25", "a26", "a27", "a28", "a29", "a30", "a31", "a32", "a33", "a34", "a35", "a36", "a37", "a38", "a39", "a40", "a41", "a42", "a43", "a44", "a45", "a46", "a47", "a48", "a49", "a50", "a51", "a52", "a53", "a54", "a55", "a56", "a57", "a58", "a59", "a60", "a61", "a62", "a63", "a64", "a65", "a66", "a67", "a68", "a69", "a70", "a71", "a72", "a73", "a74", "a75", "a76", "a77", "a78", "a79", "a80", "a81", "a82", "a83", "a84", "a85", "a86", "a87", "a88", "a89", "a90", "a91", "a92", "a93", "a94", "a95", "a96", "a97", "a98", "a99", "a100", "a101", "a102", "a103", "a104", "a105", "a106", "a107", "a108", "a109", "a110", "a111", "a112", "a113", "a114", "a115", "a116", "a117", "a118", "a119", "a120", "a121", "a122", "a123", "a124", "a125", "a126", "a127", "a128", "a129", "a130", "a131", "a132", "a133", "a134", "a135", "a136", "a137", "a138", "a139", "a140", "a141", "a142", "a143", "a144", "a145", "a146", "a147", "a148", "a149", "a150", "a151", "a152", "a153", "a154", "a155", "a156", "a157", "a158", "a159", "a160", "a161", "a162", "a163", "a164", "a165", "a166", "a167", "a168", "a169", "a170", "a171", "a172", "a173", "a174", "a175", "a176", "a177", "a178", "a179", "a180", "a181", "a182", "a183", "a184", "a185", "a186", "a187", "a188", "a189", "a190", "a191", "a192", "a193", "a194", "a195", "a196", "a197", "a198", "a199", "a200", "a201", "a202", "a203", "a204", "a205", "a206", "a207", "a208", "a209", "a210", "a211", "a212", "a213", "a214", "a215", "a216", "a217", "a218", "a219", "a220", "a221", "a222", "a223", "a224", "a225", "a226", "a227", "a228", "a229", "a230", "a231", "a232", "a233", "a234", "a235", "a236", "a237", "a238", "a239", "a240", "a241", "a242", "a243", "a244", "a245", "a246", "a247", "a248", "a249", "a250", "a251", "a252", "a253", "a254", "a255", "s39", "s57", "s58", "s59", "s60", "s61", "s62", "s63", "s64", "s65", "s68", "s69", "s70", "s71", "s72", "s73", "s74", "s75", "s100", "s101");
  if (flg != 2u) {
    const int l2 = __builtin_amdgcn_mbcnt_hi(~0u, __builtin_amdgcn_mbcnt_lo(~0u, 0u));
    slow_path(x, dwl, dws, w1, w2, out, (float*)smem, w * 64 + l2, tx, ty0);
  }
}

extern "C" void kernel_launch(void* const* d_in, const int* in_sizes, int n_in, void* d_out, int out_size, void* d_ws,
                              size_t ws_size, hipStream_t stream) {
  replk_kernel<<<256, 256, 0, stream>>>((const u16*)d_in[0], (const u16*)d_in[1], (const u16*)d_in[2], (const u16*)d_in[3],
                                        (const u16*)d_in[4], (u16*)d_out
                                        );
}
```

```cpp
#include <hip/hip_runtime.h>
#include <hip/hip_bf16.h>

typedef unsigned u32;
typedef unsigned short u16;
typedef unsigned long long u64;
typedef u32 u32x4 __attribute__((ext_vector_type(4)));
typedef u32 u32x8 __attribute__((ext_vector_type(8)));
typedef u32 u32x16 __attribute__((ext_vector_type(16)));
typedef _Float16 f16x2 __attribute__((ext_vector_type(2)));
typedef __bf16 bf16x2 __attribute__((ext_vector_type(2)));
typedef float f32x2 __attribute__((ext_vector_type(2)));
#define DI __device__ __forceinline__
#define LDSP(T, off) ((__attribute__((address_space(3))) T*)(smem3 + (off)))

constexpr int IMG = 512;
constexpr int CH = 96;
constexpr int HID = 384;
constexpr int PXB = 192;
constexpr int ROWB = 98304;
constexpr int TOEP_CS = 336;
constexpr int XT_RS = 320;
constexpr int XT_PP = 1360;
constexpr int XT_CG = 6752;
constexpr int GS = 6160;
constexpr int L_LUT = 0;
constexpr int L_TOEP = 8208;
constexpr int L_W = 40960;
constexpr int L_LUTN = 65536;
constexpr int L_XT = 81920;
constexpr int L_H = 108928;
constexpr int L_EX = 133568;
constexpr int XT_Q = 27024;
constexpr int L_MISC = 162992;
constexpr int L_TOTAL = 163056;
constexpr int LUTK = 106;
constexpr u32 XBYTES = IMG * IMG * PXB;
constexpr float GAMMA = -0.4684938781597355f;
constexpr float GC1 = -2.302208198144325f, GC3 = -0.1029432395800235f;
constexpr u32 MU_PK = 0x44ea44eau, CC_PK = 0xb77fb77fu;
__device__ const u32 CTAB[9 * 1024] = {
3722283144u,52428,108928,115088,3722283144u,56797,108944,115104,3722283144u,61166,108960,115120,3722283144u,52428,108976,115136,3722283144u,52428,108992,115152,3722283144u,56797,109008,115168,3722283144u,61166,109024,115184,3722283144u,52428,109040,115200,3722283144u,52428,109056,115216,3722283144u,56797,109072,115232,3722283144u,61166,109088,115248,3722283144u,52428,109104,115264,3722283144u,52428,109120,115280,3722283144u,56797,109136,115296,3722283144u,61166,109152,115312,3722283144u,52428,109168,115328,
3722283144u,52428,109376,115536,3722283144u,56797,109392,115552,3722283144u,61166,109408,115568,3722283144u,52428,109424,115584,3722283144u,52428,109312,115472,3722283144u,56797,109328,115488,3722283144u,61166,109344,115504,3722283144u,52428,109360,115520,3722283144u,52428,109248,115408,3722283144u,56797,109264,115424,3722283144u,61166,109280,115440,3722283144u,52428,109296,115456,3722283144u,52428,109184,115344,3722283144u,56797,109200,115360,3722283144u,61166,109216,115376,3722283144u,52428,109232,115392,
3722283144u,52428,109504,115664,3722283144u,56797,109520,115680,3722283144u,61166,109536,115696,3722283144u,52428,109552,115712,3722283144u,52428,109440,115600,3722283144u,56797,109456,115616,3722283144u,61166,109472,115632,3722283144u,52428,109488,115648,3722283144u,52428,109632,115792,3722283144u,56797,109648,115808,3722283144u,61166,109664,115824,3722283144u,52428,109680,115840,3722283144u,52428,109568,115728,3722283144u,56797,109584,115744,3722283144u,61166,109600,115760,3722283144u,52428,109616,115776,
3722283144u,52428,109824,115984,3722283144u,56797,109840,116000,3722283144u,61166,109856,116016,3722283144u,52428,109872,116032,3722283144u,52428,109888,116048,3722283144u,56797,109904,116064,3722283144u,61166,109920,116080,3722283144u,52428,109936,116096,3722283144u,52428,109696,115856,3722283144u,56797,109712,115872,3722283144u,61166,109728,115888,3722283144u,52428,109744,115904,3722283144u,52428,109760,115920,3722283144u,56797,109776,115936,3722283144u,61166,109792,115952,3722283144u,52428,109808,115968,
3722283144u,52428,115088,108928,3722283144u,56797,115104,108944,3722283144u,61166,115120,108960,3722283144u,52428,115136,108976,3722283144u,52428,115152,108992,3722283144u,56797,115168,109008,3722283144u,61166,115184,109024,3722283144u,52428,115200,109040,3722283144u,52428,115216,109056,3722283144u,56797,115232,109072,3722283144u,61166,115248,109088,3722283144u,52428,115264,109104,3722283144u,52428,115280,109120,3722283144u,56797,115296,109136,3722283144u,61166,115312,109152,3722283144u,52428,115328,109168,
3722283144u,52428,115536,109376,3722283144u,56797,115552,109392,3722283144u,61166,115568,109408,3722283144u,52428,115584,109424,3722283144u,52428,115472,109312,3722283144u,56797,115488,109328,3722283144u,61166,115504,109344,3722283144u,52428,115520,109360,3722283144u,52428,115408,109248,3722283144u,56797,115424,109264,3722283144u,61166,115440,109280,3722283144u,52428,115456,109296,3722283144u,52428,115344,109184,3722283144u,56797,115360,109200,3722283144u,61166,115376,109216,3722283144u,52428,115392,109232,
3722283144u,52428,115664,109504,3722283144u,56797,115680,109520,3722283144u,61166,115696,109536,3722283144u,52428,115712,109552,3722283144u,52428,115600,109440,3722283144u,56797,115616,109456,3722283144u,61166,115632,109472,3722283144u,52428,115648,109488,3722283144u,52428,115792,109632,3722283144u,56797,115808,109648,3722283144u,61166,115824,109664,3722283144u,52428,115840,109680,3722283144u,52428,115728,109568,3722283144u,56797,115744,109584,3722283144u,61166,115760,109600,3722283144u,52428,115776,109616,
3722283144u,52428,115984,109824,3722283144u,56797,116000,109840,3722283144u,61166,116016,109856,3722283144u,52428,116032,109872,3722283144u,52428,116048,109888,3722283144u,56797,116064,109904,3722283144u,61166,116080,109920,3722283144u,52428,116096,109936,3722283144u,52428,115856,109696,3722283144u,56797,115872,109712,3722283144u,61166,115888,109728,3722283144u,52428,115904,109744,3722283144u,52428,115920,109760,3722283144u,56797,115936,109776,3722283144u,61166,115952,109792,3722283144u,52428,115968,109808,
3722283144u,52428,121248,127408,3722283144u,56797,121264,127424,3722283144u,61166,121280,127440,3722283144u,52428,121296,127456,3722283144u,52428,121312,127472,3722283144u,56797,121328,127488,3722283144u,61166,121344,127504,3722283144u,52428,121360,127520,3722283144u,52428,121376,127536,3722283144u,56797,121392,127552,3722283144u,61166,121408,127568,3722283144u,52428,121424,127584,3722283144u,52428,121440,127600,3722283144u,56797,121456,127616,3722283144u,61166,121472,127632,3722283144u,52428,121488,127648,
3722283144u,52428,121696,127856,3722283144u,56797,121712,127872,3722283144u,61166,121728,127888,3722283144u,52428,121744,127904,3722283144u,52428,121632,127792,3722283144u,56797,121648,127808,3722283144u,61166,121664,127824,3722283144u,52428,121680,127840,3722283144u,52428,121568,127728,3722283144u,56797,121584,127744,3722283144u,61166,121600,127760,3722283144u,52428,121616,127776,3722283144u,52428,121504,127664,3722283144u,56797,121520,127680,3722283144u,61166,121536,127696,3722283144u,52428,121552,127712,
3722283144u,52428,121824,127984,3722283144u,56797,121840,128000,3722283144u,61166,121856,128016,3722283144u,52428,121872,128032,3722283144u,52428,121760,127920,3722283144u,56797,121776,127936,3722283144u,61166,121792,127952,3722283144u,52428,121808,127968,3722283144u,52428,121952,128112,3722283144u,56797,121968,128128,3722283144u,61166,121984,128144,3722283144u,52428,122000,128160,3722283144u,52428,121888,128048,3722283144u,56797,121904,128064,3722283144u,61166,121920,128080,3722283144u,52428,121936,128096,
3722283144u,52428,122144,128304,3722283144u,56797,122160,128320,3722283144u,61166,122176,128336,3722283144u,52428,122192,128352,3722283144u,52428,122208,128368,3722283144u,56797,122224,128384,3722283144u,61166,122240,128400,3722283144u,52428,122256,128416,3722283144u,52428,122016,128176,3722283144u,56797,122032,128192,3722283144u,61166,122048,128208,3722283144u,52428,122064,128224,3722283144u,52428,122080,128240,3722283144u,56797,122096,128256,3722283144u,61166,122112,128272,3722283144u,52428,122128,128288,
3722283144u,52428,127408,121248,3722283144u,56797,127424,121264,3722283144u,61166,127440,121280,3722283144u,52428,127456,121296,3722283144u,52428,127472,121312,3722283144u,56797,127488,121328,3722283144u,61166,127504,121344,3722283144u,52428,127520,121360,3722283144u,52428,127536,121376,3722283144u,56797,127552,121392,3722283144u,61166,127568,121408,3722283144u,52428,127584,121424,3722283144u,52428,127600,121440,3722283144u,56797,127616,121456,3722283144u,61166,127632,121472,3722283144u,52428,127648,121488,
3722283144u,52428,127856,121696,3722283144u,56797,127872,121712,3722283144u,61166,127888,121728,3722283144u,52428,127904,121744,3722283144u,52428,127792,121632,3722283144u,56797,127808,121648,3722283144u,61166,127824,121664,3722283144u,52428,127840,121680,3722283144u,52428,127728,121568,3722283144u,56797,127744,121584,3722283144u,61166,127760,121600,3722283144u,52428,127776,121616,3722283144u,52428,127664,121504,3722283144u,56797,127680,121520,3722283144u,61166,127696,121536,3722283144u,52428,127712,121552,
3722283144u,52428,127984,121824,3722283144u,56797,128000,121840,3722283144u,61166,128016,121856,3722283144u,52428,128032,121872,3722283144u,52428,127920,121760,3722283144u,56797,127936,121776,3722283144u,61166,127952,121792,3722283144u,52428,127968,121808,3722283144u,52428,128112,121952,3722283144u,56797,128128,121968,3722283144u,61166,128144,121984,3722283144u,52428,128160,122000,3722283144u,52428,128048,121888,3722283144u,56797,128064,121904,3722283144u,61166,128080,121920,3722283144u,52428,128096,121936,
3722283144u,52428,128304,122144,3722283144u,56797,128320,122160,3722283144u,61166,128336,122176,3722283144u,52428,128352,122192,3722283144u,52428,128368,122208,3722283144u,56797,128384,122224,3722283144u,61166,128400,122240,3722283144u,52428,128416,122256,3722283144u,52428,128176,122016,3722283144u,56797,128192,122032,3722283144u,61166,128208,122048,3722283144u,52428,128224,122064,3722283144u,52428,128240,122080,3722283144u,56797,128256,122096,3722283144u,61166,128272,122112,3722283144u,52428,128288,122128,
40960,393216,2147483648u,3072,40976,393408,2147483664u,3088,40992,393600,2147483680u,3104,41008,393792,2147483696u,3120,41024,393984,2147483840u,3264,41040,394176,2147483856u,3280,41056,394368,2147483872u,3296,41072,394560,2147483888u,3312,41088,394752,384,1073745280,41104,394944,400,1073745296,41120,395136,416,1073745312,41136,395328,432,1073745328,41152,395520,576,1073745472,41168,395712,592,1073745488,41184,395904,608,1073745504,41200,396096,624,1073745520,
41216,393232,768,396288,41232,393424,784,396304,41248,393616,800,396320,41264,393808,816,396336,41280,394000,960,396480,41296,394192,976,396496,41312,394384,992,396512,41328,394576,1008,396528,41344,394768,1152,1074138496,41360,394960,1168,1074138512,41376,395152,1184,1074138528,41392,395344,1200,1074138544,41408,395536,1344,1074138688,41424,395728,1360,1074138704,41440,395920,1376,1074138720,41456,396112,1392,1074138736,
41472,393248,1536,789504,41488,393440,1552,789520,41504,393632,1568,789536,41520,393824,1584,789552,41536,394016,1728,789696,41552,394208,1744,789712,41568,394400,1760,789728,41584,394592,1776,789744,41600,394784,1920,1074531712,41616,394976,1936,1074531728,41632,395168,1952,1074531744,41648,395360,1968,1074531760,41664,395552,2112,1074531904,41680,395744,2128,1074531920,41696,395936,2144,1074531936,41712,396128,2160,1074531952,
41728,393264,2304,1182720,41744,393456,2320,1182736,41760,393648,2336,1182752,41776,393840,2352,1182768,41792,394032,2496,1182912,41808,394224,2512,1182928,41824,394416,2528,1182944,41840,394608,2544,1182960,41856,394800,2688,1074924928,41872,394992,2704,1074924944,41888,395184,2720,1074924960,41904,395376,2736,1074924976,41920,395568,2880,1074925120,41936,395760,2896,1074925136,41952,395952,2912,1074925152,41968,396144,2928,1074925168,
41984,0,2147581952u,101376,42000,192,2147581968u,101392,42016,384,2147581984u,101408,42032,576,2147582000u,101424,42048,768,2147582144u,101568,42064,960,2147582160u,101584,42080,1152,2147582176u,101600,42096,1344,2147582192u,101616,42112,1536,98688,1073843584,42128,1728,98704,1073843600,42144,1920,98720,1073843616,42160,2112,98736,1073843632,42176,2304,98880,1073843776,42192,2496,98896,1073843792,42208,2688,98912,1073843808,42224,2880,98928,1073843824,
42240,16,99072,494592,42256,208,99088,494608,42272,400,99104,494624,42288,592,99120,494640,42304,784,99264,494784,42320,976,99280,494800,42336,1168,99296,494816,42352,1360,99312,494832,42368,1552,99456,1074236800,42384,1744,99472,1074236816,42400,1936,99488,1074236832,42416,2128,99504,1074236848,42432,2320,99648,1074236992,42448,2512,99664,1074237008,42464,2704,99680,1074237024,42480,2896,99696,1074237040,
42496,32,99840,887808,42512,224,99856,887824,42528,416,99872,887840,42544,608,99888,887856,42560,800,100032,888000,42576,992,100048,888016,42592,1184,100064,888032,42608,1376,100080,888048,42624,1568,100224,1074630016,42640,1760,100240,1074630032,42656,1952,100256,1074630048,42672,2144,100272,1074630064,42688,2336,100416,1074630208,42704,2528,100432,1074630224,42720,2720,100448,1074630240,42736,2912,100464,1074630256,
42752,48,100608,1281024,42768,240,100624,1281040,42784,432,100640,1281056,42800,624,100656,1281072,42816,816,100800,1281216,42832,1008,100816,1281232,42848,1200,100832,1281248,42864,1392,100848,1281264,42880,1584,100992,1075023232,42896,1776,101008,1075023248,42912,1968,101024,1075023264,42928,2160,101040,1075023280,42944,2352,101184,1075023424,42960,2544,101200,1075023440,42976,2736,101216,1075023456,42992,2928,101232,1075023472,
40960,1179648,2147680256u,199680,40976,1179840,2147680272u,199696,40992,1180032,2147680288u,199712,41008,1180224,2147680304u,199728,41024,1180416,2147680448u,199872,41040,1180608,2147680464u,199888,41056,1180800,2147680480u,199904,41072,1180992,2147680496u,199920,41088,1181184,196992,1073941888,41104,1181376,197008,1073941904,41120,1181568,197024,1073941920,41136,1181760,197040,1073941936,41152,1181952,197184,1073942080,41168,1182144,197200,1073942096,41184,1182336,197216,1073942112,41200,1182528,197232,1073942128,
41216,1179664,197376,592896,41232,1179856,197392,592912,41248,1180048,197408,592928,41264,1180240,197424,592944,41280,1180432,197568,593088,41296,1180624,197584,593104,41312,1180816,197600,593120,41328,1181008,197616,593136,41344,1181200,197760,1074335104,41360,1181392,197776,1074335120,41376,1181584,197792,1074335136,41392,1181776,197808,1074335152,41408,1181968,197952,1074335296,41424,1182160,197968,1074335312,41440,1182352,197984,1074335328,41456,1182544,198000,1074335344,
41472,1179680,198144,986112,41488,1179872,198160,986128,41504,1180064,198176,986144,41520,1180256,198192,986160,41536,1180448,198336,986304,41552,1180640,198352,986320,41568,1180832,198368,986336,41584,1181024,198384,986352,41600,1181216,198528,1074728320,41616,1181408,198544,1074728336,41632,1181600,198560,1074728352,41648,1181792,198576,1074728368,41664,1181984,198720,1074728512,41680,1182176,198736,1074728528,41696,1182368,198752,1074728544,41712,1182560,198768,1074728560,
41728,1179696,198912,1379328,41744,1179888,198928,1379344,41760,1180080,198944,1379360,41776,1180272,198960,1379376,41792,1180464,199104,1379520,41808,1180656,199120,1379536,41824,1180848,199136,1379552,41840,1181040,199152,1379568,41856,1181232,199296,1075121536,41872,1181424,199312,1075121552,41888,1181616,199328,1075121568,41904,1181808,199344,1075121584,41920,1182000,199488,1075121728,41936,1182192,199504,1075121744,41952,1182384,199520,1075121760,41968,1182576,199536,1075121776,
41984,786432,2147778560u,297984,42000,786624,2147778576u,298000,42016,786816,2147778592u,298016,42032,787008,2147778608u,298032,42048,787200,2147778752u,298176,42064,787392,2147778768u,298192,42080,787584,2147778784u,298208,42096,787776,2147778800u,298224,42112,787968,295296,1074040192,42128,788160,295312,1074040208,42144,788352,295328,1074040224,42160,788544,295344,1074040240,42176,788736,295488,1074040384,42192,788928,295504,1074040400,42208,789120,295520,1074040416,42224,789312,295536,1074040432,
42240,786448,295680,691200,42256,786640,295696,691216,42272,786832,295712,691232,42288,787024,295728,691248,42304,787216,295872,691392,42320,787408,295888,691408,42336,787600,295904,691424,42352,787792,295920,691440,42368,787984,296064,1074433408,42384,788176,296080,1074433424,42400,788368,296096,1074433440,42416,788560,296112,1074433456,42432,788752,296256,1074433600,42448,788944,296272,1074433616,42464,789136,296288,1074433632,42480,789328,296304,1074433648,
42496,786464,296448,1084416,42512,786656,296464,1084432,42528,786848,296480,1084448,42544,787040,296496,1084464,42560,787232,296640,1084608,42576,787424,296656,1084624,42592,787616,296672,1084640,42608,787808,296688,1084656,42624,788000,296832,1074826624,42640,788192,296848,1074826640,42656,788384,296864,1074826656,42672,788576,296880,1074826672,42688,788768,297024,1074826816,42704,788960,297040,1074826832,42720,789152,297056,1074826848,42736,789344,297072,1074826864,
42752,786480,297216,1477632,42768,786672,297232,1477648,42784,786864,297248,1477664,42800,787056,297264,1477680,42816,787248,297408,1477824,42832,787440,297424,1477840,42848,787632,297440,1477856,42864,787824,297456,1477872,42880,788016,297600,1075219840,42896,788208,297616,1075219856,42912,788400,297632,1075219872,42928,788592,297648,1075219888,42944,788784,297792,1075220032,42960,788976,297808,1075220048,42976,789168,297824,1075220064,42992,789360,297840,1075220080,
81920,139728,81920,8208,88672,140176,83280,8192,95424,140304,84640,8192,102176,140624,86000,8192,81924,139744,81936,8224,88676,140192,83296,8864,95428,140320,84656,8864,102180,140640,86016,8864,81928,139760,81968,8240,88680,140208,83328,8864,95432,140336,84688,8864,102184,140656,86048,8864,81932,139776,81952,8256,88684,140224,83312,8192,95436,140352,84672,8192,102188,140672,86032,8192,
81936,139792,81936,8464,88688,140112,83296,8400,95440,140240,84656,8336,102192,140688,86016,8272,81940,139808,81952,8480,88692,140128,83312,8416,95444,140256,84672,8352,102196,140704,86032,8288,81944,139824,81984,8496,88696,140144,83344,8432,95448,140272,84704,8368,102200,140720,86064,8304,81948,139840,81968,8512,88700,140160,83328,8448,95452,140288,84688,8384,102204,140736,86048,8320,
81952,139856,83280,8272,88704,140048,84640,8208,95456,140432,86000,8192,102208,140496,87360,8192,81956,139872,83296,8288,88708,140064,84656,8224,95460,140448,86016,8864,102212,140512,87376,8864,81960,139888,83328,8304,88712,140080,84688,8240,95464,140464,86048,8864,102216,140528,87408,8864,81964,139904,83312,8320,88716,140096,84672,8256,95468,140480,86032,8192,102220,140544,87392,8192,
81968,139920,83296,8864,88720,139984,84656,8464,95472,140368,86016,8400,102224,140560,87376,8336,81972,139936,83312,8192,88724,140000,84672,8480,95476,140384,86032,8416,102228,140576,87392,8352,81976,139952,83344,8192,88728,140016,84704,8496,95480,140400,86064,8432,102232,140592,87424,8368,81980,139968,83328,8864,88732,140032,84688,8512,95484,140416,86048,8448,102236,140608,87408,8384,
82240,133568,88672,10896,88992,134016,90032,10880,95744,134144,91392,10880,102496,134464,92752,10880,82244,133584,88688,10912,88996,134032,90048,11552,95748,134160,91408,11552,102500,134480,92768,11552,82248,133600,88720,10928,89000,134048,90080,11552,95752,134176,91440,11552,102504,134496,92800,11552,82252,133616,88704,10944,89004,134064,90064,10880,95756,134192,91424,10880,102508,134512,92784,10880,
82256,133632,88688,11152,89008,133952,90048,11088,95760,134080,91408,11024,102512,134528,92768,10960,82260,133648,88704,11168,89012,133968,90064,11104,95764,134096,91424,11040,102516,134544,92784,10976,82264,133664,88736,11184,89016,133984,90096,11120,95768,134112,91456,11056,102520,134560,92816,10992,82268,133680,88720,11200,89020,134000,90080,11136,95772,134128,91440,11072,102524,134576,92800,11008,
82272,133696,90032,10960,89024,133888,91392,10896,95776,134272,92752,10880,102528,134336,94112,10880,82276,133712,90048,10976,89028,133904,91408,10912,95780,134288,92768,11552,102532,134352,94128,11552,82280,133728,90080,10992,89032,133920,91440,10928,95784,134304,92800,11552,102536,134368,94160,11552,82284,133744,90064,11008,89036,133936,91424,10944,95788,134320,92784,10880,102540,134384,94144,10880,
82288,133760,90048,11552,89040,133824,91408,11152,95792,134208,92768,11088,102544,134400,94128,11024,82292,133776,90064,10880,89044,133840,91424,11168,95796,134224,92784,11104,102548,134416,94144,11040,82296,133792,90096,10880,89048,133856,91456,11184,95800,134240,92816,11120,102552,134432,94176,11056,82300,133808,90080,11552,89052,133872,91440,11200,95804,134256,92800,11136,102556,134448,94160,11072,
82560,152048,95424,13584,89312,152496,96784,13568,96064,152624,98144,13568,102816,152944,99504,13568,82564,152064,95440,13600,89316,152512,96800,14240,96068,152640,98160,14240,102820,152960,99520,14240,82568,152080,95472,13616,89320,152528,96832,14240,96072,152656,98192,14240,102824,152976,99552,14240,82572,152096,95456,13632,89324,152544,96816,13568,96076,152672,98176,13568,102828,152992,99536,13568,
82576,152112,95440,13840,89328,152432,96800,13776,96080,152560,98160,13712,102832,153008,99520,13648,82580,152128,95456,13856,89332,152448,96816,13792,96084,152576,98176,13728,102836,153024,99536,13664,82584,152144,95488,13872,89336,152464,96848,13808,96088,152592,98208,13744,102840,153040,99568,13680,82588,152160,95472,13888,89340,152480,96832,13824,96092,152608,98192,13760,102844,153056,99552,13696,
82592,152176,96784,13648,89344,152368,98144,13584,96096,152752,99504,13568,102848,152816,100864,13568,82596,152192,96800,13664,89348,152384,98160,13600,96100,152768,99520,14240,102852,152832,100880,14240,82600,152208,96832,13680,89352,152400,98192,13616,96104,152784,99552,14240,102856,152848,100912,14240,82604,152224,96816,13696,89356,152416,98176,13632,96108,152800,99536,13568,102860,152864,100896,13568,
82608,152240,96800,14240,89360,152304,98160,13840,96112,152688,99520,13776,102864,152880,100880,13712,82612,152256,96816,13568,89364,152320,98176,13856,96116,152704,99536,13792,102868,152896,100896,13728,82616,152272,96848,13568,89368,152336,98208,13872,96120,152720,99568,13808,102872,152912,100928,13744,82620,152288,96832,14240,89372,152352,98192,13888,96124,152736,99552,13824,102876,152928,100912,13760,
82880,145888,102176,16272,89632,146336,103536,16256,96384,146464,104896,16256,103136,146784,106256,16256,82884,145904,102192,16288,89636,146352,103552,16928,96388,146480,104912,16928,103140,146800,106272,16928,82888,145920,102224,16304,89640,146368,103584,16928,96392,146496,104944,16928,103144,146816,106304,16928,82892,145936,102208,16320,89644,146384,103568,16256,96396,146512,104928,16256,103148,146832,106288,16256,
82896,145952,102192,16528,89648,146272,103552,16464,96400,146400,104912,16400,103152,146848,106272,16336,82900,145968,102208,16544,89652,146288,103568,16480,96404,146416,104928,16416,103156,146864,106288,16352,82904,145984,102240,16560,89656,146304,103600,16496,96408,146432,104960,16432,103160,146880,106320,16368,82908,146000,102224,16576,89660,146320,103584,16512,96412,146448,104944,16448,103164,146896,106304,16384,
82912,146016,103536,16336,89664,146208,104896,16272,96416,146592,106256,16256,103168,146656,107616,16256,82916,146032,103552,16352,89668,146224,104912,16288,96420,146608,106272,16928,103172,146672,107632,16928,82920,146048,103584,16368,89672,146240,104944,16304,96424,146624,106304,16928,103176,146688,107664,16928,82924,146064,103568,16384,89676,146256,104928,16320,96428,146640,106288,16256,103180,146704,107648,16256,
82928,146080,103552,16928,89680,146144,104912,16528,96432,146528,106272,16464,103184,146720,107632,16400,82932,146096,103568,16256,89684,146160,104928,16544,96436,146544,106288,16480,103188,146736,107648,16416,82936,146112,103600,16256,89688,146176,104960,16560,96440,146560,106320,16496,103192,146752,107680,16432,82940,146128,103584,16928,89692,146192,104944,16576,96444,146576,106304,16512,103196,146768,107664,16448,
8336,108928,81984,393216,8272,115088,88736,393232,8208,121248,95488,393248,8864,127408,102240,393264,8352,108992,81988,393408,8288,115152,88740,393424,8224,121312,95492,393440,8528,127472,102244,393456,8368,109120,81992,393600,8304,115280,88744,393616,8240,121440,95496,393632,8528,127600,102248,393648,8384,109056,81996,393792,8320,115216,88748,393808,8256,121376,95500,393824,8864,127536,102252,393840,
8528,108944,83344,393984,8528,115104,90096,394000,8464,121264,96848,394016,8400,127424,103600,394032,8864,109008,83348,394176,8864,115168,90100,394192,8480,121328,96852,394208,8416,127488,103604,394224,8864,109136,83352,394368,8864,115296,90104,394384,8496,121456,96856,394400,8432,127616,103608,394416,8528,109072,83356,394560,8528,115232,90108,394576,8512,121392,96860,394592,8448,127552,103612,394608,
8400,108960,84704,394752,8336,115120,91456,394768,8272,121280,98208,394784,8208,127440,104960,394800,8416,109024,84708,394944,8352,115184,91460,394960,8288,121344,98212,394976,8224,127504,104964,394992,8432,109152,84712,395136,8368,115312,91464,395152,8304,121472,98216,395168,8240,127632,104968,395184,8448,109088,84716,395328,8384,115248,91468,395344,8320,121408,98220,395360,8256,127568,104972,395376,
8528,108976,86064,395520,8528,115136,92816,395536,8528,121296,99568,395552,8464,127456,106320,395568,8864,109040,86068,395712,8864,115200,92820,395728,8864,121360,99572,395744,8480,127520,106324,395760,8864,109168,86072,395904,8864,115328,92824,395920,8864,121488,99576,395936,8496,127648,106328,395952,8528,109104,86076,396096,8528,115264,92828,396112,8528,121424,99580,396128,8512,127584,106332,396144,
11024,109376,82304,0,10960,115536,89056,16,10896,121696,95808,32,11552,127856,102560,48,11040,109312,82308,192,10976,115472,89060,208,10912,121632,95812,224,11216,127792,102564,240,11056,109184,82312,384,10992,115344,89064,400,10928,121504,95816,416,11216,127664,102568,432,11072,109248,82316,576,11008,115408,89068,592,10944,121568,95820,608,11552,127728,102572,624,
11216,109392,83664,768,11216,115552,90416,784,11152,121712,97168,800,11088,127872,103920,816,11552,109328,83668,960,11552,115488,90420,976,11168,121648,97172,992,11104,127808,103924,1008,11552,109200,83672,1152,11552,115360,90424,1168,11184,121520,97176,1184,11120,127680,103928,1200,11216,109264,83676,1344,11216,115424,90428,1360,11200,121584,97180,1376,11136,127744,103932,1392,
11088,109408,85024,1536,11024,115568,91776,1552,10960,121728,98528,1568,10896,127888,105280,1584,11104,109344,85028,1728,11040,115504,91780,1744,10976,121664,98532,1760,10912,127824,105284,1776,11120,109216,85032,1920,11056,115376,91784,1936,10992,121536,98536,1952,10928,127696,105288,1968,11136,109280,85036,2112,11072,115440,91788,2128,11008,121600,98540,2144,10944,127760,105292,2160,
11216,109424,86384,2304,11216,115584,93136,2320,11216,121744,99888,2336,11152,127904,106640,2352,11552,109360,86388,2496,11552,115520,93140,2512,11552,121680,99892,2528,11168,127840,106644,2544,11552,109232,86392,2688,11552,115392,93144,2704,11552,121552,99896,2720,11184,127712,106648,2736,11216,109296,86396,2880,11216,115456,93148,2896,11216,121616,99900,2912,11200,127776,106652,2928,
13712,109504,82624,1179648,13648,115664,89376,1179664,13584,121824,96128,1179680,14240,127984,102880,1179696,13728,109440,82628,1179840,13664,115600,89380,1179856,13600,121760,96132,1179872,13904,127920,102884,1179888,13744,109568,82632,1180032,13680,115728,89384,1180048,13616,121888,96136,1180064,13904,128048,102888,1180080,13760,109632,82636,1180224,13696,115792,89388,1180240,13632,121952,96140,1180256,14240,128112,102892,1180272,
13904,109520,83984,1180416,13904,115680,90736,1180432,13840,121840,97488,1180448,13776,128000,104240,1180464,14240,109456,83988,1180608,14240,115616,90740,1180624,13856,121776,97492,1180640,13792,127936,104244,1180656,14240,109584,83992,1180800,14240,115744,90744,1180816,13872,121904,97496,1180832,13808,128064,104248,1180848,13904,109648,83996,1180992,13904,115808,90748,1181008,13888,121968,97500,1181024,13824,128128,104252,1181040,
13776,109536,85344,1181184,13712,115696,92096,1181200,13648,121856,98848,1181216,13584,128016,105600,1181232,13792,109472,85348,1181376,13728,115632,92100,1181392,13664,121792,98852,1181408,13600,127952,105604,1181424,13808,109600,85352,1181568,13744,115760,92104,1181584,13680,121920,98856,1181600,13616,128080,105608,1181616,13824,109664,85356,1181760,13760,115824,92108,1181776,13696,121984,98860,1181792,13632,128144,105612,1181808,
13904,109552,86704,1181952,13904,115712,93456,1181968,13904,121872,100208,1181984,13840,128032,106960,1182000,14240,109488,86708,1182144,14240,115648,93460,1182160,14240,121808,100212,1182176,13856,127968,106964,1182192,14240,109616,86712,1182336,14240,115776,93464,1182352,14240,121936,100216,1182368,13872,128096,106968,1182384,13904,109680,86716,1182528,13904,115840,93468,1182544,13904,122000,100220,1182560,13888,128160,106972,1182576,
16400,109824,82944,786432,16336,115984,89696,786448,16272,122144,96448,786464,16928,128304,103200,786480,16416,109888,82948,786624,16352,116048,89700,786640,16288,122208,96452,786656,16592,128368,103204,786672,16432,109760,82952,786816,16368,115920,89704,786832,16304,122080,96456,786848,16592,128240,103208,786864,16448,109696,82956,787008,16384,115856,89708,787024,16320,122016,96460,787040,16928,128176,103212,787056,
16592,109840,84304,787200,16592,116000,91056,787216,16528,122160,97808,787232,16464,128320,104560,787248,16928,109904,84308,787392,16928,116064,91060,787408,16544,122224,97812,787424,16480,128384,104564,787440,16928,109776,84312,787584,16928,115936,91064,787600,16560,122096,97816,787616,16496,128256,104568,787632,16592,109712,84316,787776,16592,115872,91068,787792,16576,122032,97820,787808,16512,128192,104572,787824,
16464,109856,85664,787968,16400,116016,92416,787984,16336,122176,99168,788000,16272,128336,105920,788016,16480,109920,85668,788160,16416,116080,92420,788176,16352,122240,99172,788192,16288,128400,105924,788208,16496,109792,85672,788352,16432,115952,92424,788368,16368,122112,99176,788384,16304,128272,105928,788400,16512,109728,85676,788544,16448,115888,92428,788560,16384,122048,99180,788576,16320,128208,105932,788592,
16592,109872,87024,788736,16592,116032,93776,788752,16592,122192,100528,788768,16528,128352,107280,788784,16928,109936,87028,788928,16928,116096,93780,788944,16928,122256,100532,788960,16544,128416,107284,788976,16928,109808,87032,789120,16928,115968,93784,789136,16928,122128,100536,789152,16560,128288,107288,789168,16592,109744,87036,789312,16592,115904,93788,789328,16592,122064,100540,789344,16576,128224,107292,789360,
81920,40960,40960,40960,88672,40976,40976,40968,95424,40992,40992,40976,102176,41008,41008,40984,108944,41024,41152,41760,115696,41040,41168,41768,122448,41056,41184,41776,129200,41072,41200,41784,135968,41088,41344,42560,142720,41104,41360,42568,149472,41120,41376,42576,156224,41136,41392,42584,81924,41152,41536,43360,88676,41168,41552,43368,95428,41184,41568,43376,102180,41200,41584,43384,
108948,41760,41728,44160,115700,41776,41744,44168,122452,41728,41760,44176,129204,41744,41776,44184,135972,41824,41920,44960,142724,41840,41936,44968,149476,41792,41952,44976,156228,41808,41968,44984,81928,41888,42112,45760,88680,41904,42128,45768,95432,41856,42144,45776,102184,41872,42160,45784,108952,41952,42304,46560,115704,41968,42320,46568,122456,41920,42336,46576,129208,41936,42352,46584,
135976,42560,42496,47104,142728,42576,42512,47112,149480,42592,42528,47120,156232,42608,42544,47128,81932,42496,42688,47904,88684,42512,42704,47912,95436,42528,42720,47920,102188,42544,42736,47928,108956,42688,42880,48704,115708,42704,42896,48712,122460,42720,42912,48720,129212,42736,42928,48728,135980,42624,43072,49504,142732,42640,43088,49512,149484,42656,43104,49520,156236,42672,43120,49528,
81936,43360,43264,50304,88688,43376,43280,50312,95440,43328,43296,50320,102192,43344,43312,50328,108960,43296,43456,51104,115712,43312,43472,51112,122464,43264,43488,51120,129216,43280,43504,51128,135984,43488,43648,51904,142736,43504,43664,51912,149488,43456,43680,51920,156240,43472,43696,51928,81940,43424,43840,52704,88692,43440,43856,52712,95444,43392,43872,52720,102196,43408,43888,52728,
108964,44160,59392,41344,115716,44176,59408,41352,122468,44192,59424,41360,129220,44208,59440,41368,135988,44224,59584,42144,142740,44240,59600,42152,149492,44256,59616,42160,156244,44272,59632,42168,81944,44032,59776,42944,88696,44048,59792,42952,95448,44064,59808,42960,102200,44080,59824,42968,108968,44096,59968,43744,115720,44112,59984,43752,122472,44128,60000,43760,129224,44144,60016,43768,
135992,44960,60160,44288,142744,44976,60176,44296,149496,44928,60192,44304,156248,44944,60208,44312,81948,45024,60352,45088,88700,45040,60368,45096,95452,44992,60384,45104,102204,45008,60400,45112,108972,44832,60544,45888,115724,44848,60560,45896,122476,44800,60576,45904,129228,44816,60592,45912,135996,44896,60736,46688,142748,44912,60752,46696,149500,44864,60768,46704,156252,44880,60784,46712,
81952,45760,60928,47488,88704,45776,60944,47496,95456,45792,60960,47504,102208,45808,60976,47512,108976,45696,61120,48288,115728,45712,61136,48296,122480,45728,61152,48304,129232,45744,61168,48312,136000,45632,61312,49088,142752,45648,61328,49096,149504,45664,61344,49104,156256,45680,61360,49112,81956,45568,61504,49888,88708,45584,61520,49896,95460,45600,61536,49904,102212,45616,61552,49912,
108980,46560,61696,50432,115732,46576,61712,50440,122484,46528,61728,50448,129236,46544,61744,50456,136004,46496,61888,51232,142756,46512,61904,51240,149508,46464,61920,51248,156260,46480,61936,51256,81960,46432,62080,52032,88712,46448,62096,52040,95464,46400,62112,52048,102216,46416,62128,52056,108984,46368,62272,52832,115736,46384,62288,52840,122488,46336,62304,52848,129240,46352,62320,52856,
136008,47104,40968,40960,142760,47120,40984,40968,149512,47136,41000,40976,156264,47152,41016,40984,81964,47168,41160,41760,88716,47184,41176,41768,95468,47200,41192,41776,102220,47216,41208,41784,108988,47232,41352,42560,115740,47248,41368,42568,122492,47264,41384,42576,129244,47280,41400,42584,136012,47296,41544,43360,142764,47312,41560,43368,149516,47328,41576,43376,156268,47344,41592,43384,
81968,47904,41736,44160,88720,47920,41752,44168,95472,47872,41768,44176,102224,47888,41784,44184,108992,47968,41928,44960,115744,47984,41944,44968,122496,47936,41960,44976,129248,47952,41976,44984,136016,48032,42120,45760,142768,48048,42136,45768,149520,48000,42152,45776,156272,48016,42168,45784,81972,48096,42312,46560,88724,48112,42328,46568,95476,48064,42344,46576,102228,48080,42360,46584,
108996,48704,42504,47104,115748,48720,42520,47112,122500,48736,42536,47120,129252,48752,42552,47128,136020,48640,42696,47904,142772,48656,42712,47912,149524,48672,42728,47920,156276,48688,42744,47928,81976,48832,42888,48704,88728,48848,42904,48712,95480,48864,42920,48720,102232,48880,42936,48728,109000,48768,43080,49504,115752,48784,43096,49512,122504,48800,43112,49520,129256,48816,43128,49528,
136024,49504,43272,50304,142776,49520,43288,50312,149528,49472,43304,50320,156280,49488,43320,50328,81980,49440,43464,51104,88732,49456,43480,51112,95484,49408,43496,51120,102236,49424,43512,51128,109004,49632,43656,51904,115756,49648,43672,51912,122508,49600,43688,51920,129260,49616,43704,51928,136028,49568,43848,52704,142780,49584,43864,52712,149532,49536,43880,52720,156284,49552,43896,52728,
81984,50304,59400,41344,88736,50320,59416,41352,95488,50336,59432,41360,102240,50352,59448,41368,109008,50368,59592,42144,115760,50384,59608,42152,122512,50400,59624,42160,129264,50416,59640,42168,136032,50176,59784,42944,142784,50192,59800,42952,149536,50208,59816,42960,156288,50224,59832,42968,81988,50240,59976,43744,88740,50256,59992,43752,95492,50272,60008,43760,102244,50288,60024,43768,
109012,51104,60168,44288,115764,51120,60184,44296,122516,51072,60200,44304,129268,51088,60216,44312,136036,51168,60360,45088,142788,51184,60376,45096,149540,51136,60392,45104,156292,51152,60408,45112,81992,50976,60552,45888,88744,50992,60568,45896,95496,50944,60584,45904,102248,50960,60600,45912,109016,51040,60744,46688,115768,51056,60760,46696,122520,51008,60776,46704,129272,51024,60792,46712,
136040,51904,60936,47488,142792,51920,60952,47496,149544,51936,60968,47504,156296,51952,60984,47512,81996,51840,61128,48288,88748,51856,61144,48296,95500,51872,61160,48304,102252,51888,61176,48312,109020,51776,61320,49088,115772,51792,61336,49096,122524,51808,61352,49104,129276,51824,61368,49112,136044,51712,61512,49888,142796,51728,61528,49896,149548,51744,61544,49904,156300,51760,61560,49912,
156300,52704,61704,50432,156300,52720,61720,50440,156300,52672,61736,50448,156300,52688,61752,50456,156300,52640,61896,51232,156300,52656,61912,51240,156300,52608,61928,51248,156300,52624,61944,51256,156300,52576,62088,52032,156300,52592,62104,52040,156300,52544,62120,52048,156300,52560,62136,52056,156300,52512,62280,52832,156300,52528,62296,52840,156300,52480,62312,52848,156300,52496,62328,52856,
40960,8208,8192,41088,40976,8880,8528,41096,40992,9552,8864,41104,41008,10224,9200,41112,41152,10896,9536,41888,41168,11568,9872,41896,41184,12240,10208,41904,41200,12912,10544,41912,41344,13584,10880,42688,41360,14256,11216,42696,41376,14928,11552,42704,41392,15600,11888,42712,41536,16272,12224,43488,41552,16944,12560,43496,41568,17616,12896,43504,41584,18288,13232,43512,
41728,18960,13568,44032,41744,19632,13904,44040,41760,20304,14240,44048,41776,20976,14576,44056,41920,21648,14912,44832,41936,22320,15248,44840,41952,22992,15584,44848,41968,23664,15920,44856,42112,24336,16256,45632,42128,25008,16592,45640,42144,25680,16928,45648,42160,26352,17264,45656,42304,27024,17600,46432,42320,27696,17936,46440,42336,28368,18272,46448,42352,29040,18608,46456,
42496,29712,18944,47232,42512,30384,19280,47240,42528,31056,19616,47248,42544,31728,19952,47256,42688,32400,20288,48032,42704,33072,20624,48040,42720,33744,20960,48048,42736,34416,21296,48056,42880,35088,21632,48832,42896,35760,21968,48840,42912,36432,22304,48848,42928,37104,22640,48856,43072,37776,22976,49632,43088,38448,23312,49640,43104,39120,23648,49648,43120,39792,23984,49656,
43264,8272,24320,50176,43280,8944,24656,50184,43296,9616,24992,50192,43312,10288,25328,50200,43456,10960,25664,50976,43472,11632,26000,50984,43488,12304,26336,50992,43504,12976,26672,51000,43648,13648,27008,51776,43664,14320,27344,51784,43680,14992,27680,51792,43696,15664,28016,51800,43840,16336,28352,52576,43856,17008,28688,52584,43872,17680,29024,52592,43888,18352,29360,52600,
59392,19024,29696,41472,59408,19696,30032,41480,59424,20368,30368,41488,59440,21040,30704,41496,59584,21712,31040,42272,59600,22384,31376,42280,59616,23056,31712,42288,59632,23728,32048,42296,59776,24400,32384,43072,59792,25072,32720,43080,59808,25744,33056,43088,59824,26416,33392,43096,59968,27088,33728,43872,59984,27760,34064,43880,60000,28432,34400,43888,60016,29104,34736,43896,
60160,29776,35072,44672,60176,30448,35408,44680,60192,31120,35744,44688,60208,31792,36080,44696,60352,32464,36416,45472,60368,33136,36752,45480,60384,33808,37088,45488,60400,34480,37424,45496,60544,35152,37760,46272,60560,35824,38096,46280,60576,36496,38432,46288,60592,37168,38768,46296,60736,37840,39104,47072,60752,38512,39440,47080,60768,39184,39776,47088,60784,39856,40112,47096,
60928,8336,40448,47616,60944,9008,40784,47624,60960,9680,162992,47632,60976,10352,162992,47640,61120,11024,162992,48416,61136,11696,162992,48424,61152,12368,162992,48432,61168,13040,162992,48440,61312,13712,162992,49216,61328,14384,162992,49224,61344,15056,162992,49232,61360,15728,162992,49240,61504,16400,162992,50016,61520,17072,162992,50024,61536,17744,162992,50032,61552,18416,162992,50040,
61696,19088,162992,50816,61712,19760,162992,50824,61728,20432,162992,50832,61744,21104,162992,50840,61888,21776,162992,51616,61904,22448,162992,51624,61920,23120,162992,51632,61936,23792,162992,51640,62080,24464,162992,52416,62096,25136,162992,52424,62112,25808,162992,52432,62128,26480,162992,52440,62272,27152,162992,53216,62288,27824,162992,53224,62304,28496,162992,53232,62320,29168,162992,53240,
40960,29840,162992,41088,40976,30512,162992,41096,40992,31184,162992,41104,41008,31856,162992,41112,41152,32528,162992,41888,41168,33200,162992,41896,41184,33872,162992,41904,41200,34544,162992,41912,41344,35216,162992,42688,41360,35888,162992,42696,41376,36560,162992,42704,41392,37232,162992,42712,41536,37904,162992,43488,41552,38576,162992,43496,41568,39248,162992,43504,41584,39920,162992,43512,
41728,8400,162992,44032,41744,9072,162992,44040,41760,9744,162992,44048,41776,10416,162992,44056,41920,11088,162992,44832,41936,11760,162992,44840,41952,12432,162992,44848,41968,13104,162992,44856,42112,13776,162992,45632,42128,14448,162992,45640,42144,15120,162992,45648,42160,15792,162992,45656,42304,16464,162992,46432,42320,17136,162992,46440,42336,17808,162992,46448,42352,18480,162992,46456,
42496,19152,162992,47232,42512,19824,162992,47240,42528,20496,162992,47248,42544,21168,162992,47256,42688,21840,162992,48032,42704,22512,162992,48040,42720,23184,162992,48048,42736,23856,162992,48056,42880,24528,162992,48832,42896,25200,162992,48840,42912,25872,162992,48848,42928,26544,162992,48856,43072,27216,162992,49632,43088,27888,162992,49640,43104,28560,162992,49648,43120,29232,162992,49656,
43264,29904,162992,50176,43280,30576,162992,50184,43296,31248,162992,50192,43312,31920,162992,50200,43456,32592,162992,50976,43472,33264,162992,50984,43488,33936,162992,50992,43504,34608,162992,51000,43648,35280,162992,51776,43664,35952,162992,51784,43680,36624,162992,51792,43696,37296,162992,51800,43840,37968,162992,52576,43856,38640,162992,52584,43872,39312,162992,52592,43888,39984,162992,52600,
59392,8464,162992,41472,59408,9136,162992,41480,59424,9808,162992,41488,59440,10480,162992,41496,59584,11152,162992,42272,59600,11824,162992,42280,59616,12496,162992,42288,59632,13168,162992,42296,59776,13840,162992,43072,59792,14512,162992,43080,59808,15184,162992,43088,59824,15856,162992,43096,59968,16528,162992,43872,59984,17200,162992,43880,60000,17872,162992,43888,60016,18544,162992,43896,
60160,19216,162992,44672,60176,19888,162992,44680,60192,20560,162992,44688,60208,21232,162992,44696,60352,21904,162992,45472,60368,22576,162992,45480,60384,23248,162992,45488,60400,23920,162992,45496,60544,24592,162992,46272,60560,25264,162992,46280,60576,25936,162992,46288,60592,26608,162992,46296,60736,27280,162992,47072,60752,27952,162992,47080,60768,28624,162992,47088,60784,29296,162992,47096,
60928,29968,162992,47616,60944,30640,162992,47624,60960,31312,162992,47632,60976,31984,162992,47640,61120,32656,162992,48416,61136,33328,162992,48424,61152,34000,162992,48432,61168,34672,162992,48440,61312,35344,162992,49216,61328,36016,162992,49224,61344,36688,162992,49232,61360,37360,162992,49240,61504,38032,162992,50016,61520,38704,162992,50024,61536,39376,162992,50032,61552,40048,162992,50040,
61696,40048,162992,50816,61712,40048,162992,50824,61728,40048,162992,50832,61744,40048,162992,50840,61888,40048,162992,51616,61904,40048,162992,51624,61920,40048,162992,51632,61936,40048,162992,51640,62080,40048,162992,52416,62096,40048,162992,52424,62112,40048,162992,52432,62128,40048,162992,52440,62272,40048,162992,53216,62288,40048,162992,53224,62304,40048,162992,53232,62320,40048,162992,53240,
16256,0,0,0,16256,0,0,0,16256,0,0,0,1065353216,0,0,0,0,0,16256,0,0,0,16256,0,0,0,16256,0,0,0,1065353216,0,0,16256,0,0,0,16256,0,0,0,16256,0,0,0,1065353216,0,0,0,0,0,16256,0,0,0,16256,0,0,0,16256,0,0,0,1065353216,
0,0,0,0,0,0,0,0,0,0,0,0,0,0,0,0,0,0,0,0,0,0,0,0,0,0,0,0,0,0,0,0,0,0,0,0,0,0,0,0,0,0,0,0,0,0,0,0,0,0,0,0,0,0,0,0,0,0,0,0,0,0,0,0,
0,0,0,0,0,0,0,0,0,0,0,0,0,0,0,0,0,0,0,0,0,0,0,0,0,0,0,0,0,0,0,0,0,0,0,0,0,0,0,0,0,0,0,0,0,0,0,0,0,0,0,0,0,0,0,0,0,0,0,0,0,0,0,0,
16256,0,0,0,16256,0,0,0,16256,0,0,0,1065353216,0,0,0,0,0,16256,0,0,0,16256,0,0,0,16256,0,0,0,1065353216,0,0,16256,0,0,0,16256,0,0,0,16256,0,0,0,1065353216,0,0,0,0,0,16256,0,0,0,16256,0,0,0,16256,0,0,0,1065353216,
16256,0,0,0,16256,0,0,0,16256,0,0,0,1065353216,0,0,0,0,0,16256,0,0,0,16256,0,0,0,16256,0,0,0,1065353216,0,0,16256,0,0,0,16256,0,0,0,16256,0,0,0,1065353216,0,0,0,0,0,16256,0,0,0,16256,0,0,0,16256,0,0,0,1065353216,
0,0,0,0,0,0,0,0,0,0,0,0,0,0,0,0,0,0,0,0,0,0,0,0,0,0,0,0,0,0,0,0,0,0,0,0,0,0,0,0,0,0,0,0,0,0,0,0,0,0,0,0,0,0,0,0,0,0,0,0,0,0,0,0,
0,0,0,0,0,0,0,0,0,0,0,0,0,0,0,0,0,0,0,0,0,0,0,0,0,0,0,0,0,0,0,0,0,0,0,0,0,0,0,0,0,0,0,0,0,0,0,0,0,0,0,0,0,0,0,0,0,0,0,0,0,0,0,0,
16256,0,0,0,16256,0,0,0,16256,0,0,0,1065353216,0,0,0,0,0,16256,0,0,0,16256,0,0,0,16256,0,0,0,1065353216,0,0,16256,0,0,0,16256,0,0,0,16256,0,0,0,1065353216,0,0,0,0,0,16256,0,0,0,16256,0,0,0,16256,0,0,0,1065353216,
16256,0,0,0,16256,0,0,0,16256,0,0,0,1065353216,0,0,0,0,0,16256,0,0,0,16256,0,0,0,16256,0,0,0,1065353216,0,0,16256,0,0,0,16256,0,0,0,16256,0,0,0,1065353216,0,0,0,0,0,16256,0,0,0,16256,0,0,0,16256,0,0,0,1065353216,
0,0,0,0,0,0,0,0,0,0,0,0,0,0,0,0,0,0,0,0,0,0,0,0,0,0,0,0,0,0,0,0,0,0,0,0,0,0,0,0,0,0,0,0,0,0,0,0,0,0,0,0,0,0,0,0,0,0,0,0,0,0,0,0,
0,0,0,0,0,0,0,0,0,0,0,0,0,0,0,0,0,0,0,0,0,0,0,0,0,0,0,0,0,0,0,0,0,0,0,0,0,0,0,0,0,0,0,0,0,0,0,0,0,0,0,0,0,0,0,0,0,0,0,0,0,0,0,0,
16256,0,0,0,16256,0,0,0,16256,0,0,0,1065353216,0,0,0,0,0,16256,0,0,0,16256,0,0,0,16256,0,0,0,1065353216,0,0,16256,0,0,0,16256,0,0,0,16256,0,0,0,1065353216,0,0,0,0,0,16256,0,0,0,16256,0,0,0,16256,0,0,0,1065353216,
16256,0,0,0,16256,0,0,0,16256,0,0,0,1065353216,0,0,0,0,0,16256,0,0,0,16256,0,0,0,16256,0,0,0,1065353216,0,0,16256,0,0,0,16256,0,0,0,16256,0,0,0,1065353216,0,0,0,0,0,16256,0,0,0,16256,0,0,0,16256,0,0,0,1065353216,
0,0,0,0,0,0,0,0,0,0,0,0,0,0,0,0,0,0,0,0,0,0,0,0,0,0,0,0,0,0,0,0,0,0,0,0,0,0,0,0,0,0,0,0,0,0,0,0,0,0,0,0,0,0,0,0,0,0,0,0,0,0,0,0,
0,0,0,0,0,0,0,0,0,0,0,0,0,0,0,0,0,0,0,0,0,0,0,0,0,0,0,0,0,0,0,0,0,0,0,0,0,0,0,0,0,0,0,0,0,0,0,0,0,0,0,0,0,0,0,0,0,0,0,0,0,0,0,0,
16256,0,0,0,16256,0,0,0,16256,0,0,0,1065353216,0,0,0,0,0,16256,0,0,0,16256,0,0,0,16256,0,0,0,1065353216,0,0,16256,0,0,0,16256,0,0,0,16256,0,0,0,1065353216,0,0,0,0,0,16256,0,0,0,16256,0,0,0,16256,0,0,0,1065353216,
0,0,0,889206016,0,0,0,889206016,0,0,0,0,0,0,0,0,0,889206016,0,889206016,0,889206016,0,889206016,0,0,0,0,0,0,0,0,0,889206016,0,0,0,889206016,0,0,0,0,0,0,0,0,0,0,0,0,0,889206016,0,0,0,889206016,0,0,0,0,0,0,0,0,
0,0,0,889206016,0,0,0,889206016,0,0,0,0,0,0,0,0,0,889206016,0,889206016,0,889206016,0,889206016,0,0,0,0,0,0,0,0,0,889206016,0,0,0,889206016,0,0,0,0,0,0,0,0,0,0,0,0,0,889206016,0,0,0,889206016,0,0,0,0,0,0,0,0,
0,0,0,889206016,0,0,0,889206016,0,0,0,0,0,0,0,0,0,889206016,0,889206016,0,889206016,0,889206016,0,0,0,0,0,0,0,0,0,889206016,0,0,0,889206016,0,0,0,0,0,0,0,0,0,0,0,0,0,889206016,0,0,0,889206016,0,0,0,0,0,0,0,0,
0,0,0,889206016,0,0,0,889206016,0,0,0,0,0,0,0,0,0,889206016,0,889206016,0,889206016,0,889206016,0,0,0,0,0,0,0,0,0,889206016,0,0,0,889206016,0,0,0,0,0,0,0,0,0,0,0,0,0,889206016,0,0,0,889206016,0,0,0,0,0,0,0,0,
0,0,0,889206016,0,0,0,889206016,0,0,0,0,0,0,0,0,0,889206016,0,889206016,0,889206016,0,889206016,0,0,0,0,0,0,0,0,0,889206016,0,0,0,889206016,0,0,0,0,0,0,0,0,0,0,0,0,0,889206016,0,0,0,889206016,0,0,0,0,0,0,0,0,
0,0,0,889206016,0,0,0,889206016,0,0,0,0,0,0,0,0,0,889206016,0,889206016,0,889206016,0,889206016,0,0,0,0,0,0,0,0,0,889206016,0,0,0,889206016,0,0,0,0,0,0,0,0,0,0,0,0,0,889206016,0,0,0,889206016,0,0,0,0,0,0,0,0,
0,0,0,889206016,0,0,0,889206016,0,0,0,0,0,0,0,0,0,889206016,0,889206016,0,889206016,0,889206016,0,0,0,0,0,0,0,0,0,889206016,0,0,0,889206016,0,0,0,0,0,0,0,0,0,0,0,0,0,889206016,0,0,0,889206016,0,0,0,0,0,0,0,0,
0,0,0,889206016,0,0,0,889206016,0,0,0,0,0,0,0,0,0,889206016,0,889206016,0,889206016,0,889206016,0,0,0,0,0,0,0,0,0,889206016,0,0,0,889206016,0,0,0,0,0,0,0,0,0,0,0,0,0,889206016,0,0,0,889206016,0,0,0,0,0,0,0,0,
0,0,0,889206016,0,0,0,889206016,0,0,0,0,0,0,0,0,0,889206016,0,889206016,0,889206016,0,889206016,0,0,0,0,0,0,0,0,0,889206016,0,0,0,889206016,0,0,0,0,0,0,0,0,0,0,0,0,0,889206016,0,0,0,889206016,0,0,0,0,0,0,0,0,
0,0,0,889206016,0,0,0,889206016,0,0,0,0,0,0,0,0,0,889206016,0,889206016,0,889206016,0,889206016,0,0,0,0,0,0,0,0,0,889206016,0,0,0,889206016,0,0,0,0,0,0,0,0,0,0,0,0,0,889206016,0,0,0,889206016,0,0,0,0,0,0,0,0,
0,0,0,889206016,0,0,0,889206016,0,0,0,0,0,0,0,0,0,889206016,0,889206016,0,889206016,0,889206016,0,0,0,0,0,0,0,0,0,889206016,0,0,0,889206016,0,0,0,0,0,0,0,0,0,0,0,0,0,889206016,0,0,0,889206016,0,0,0,0,0,0,0,0,
0,0,0,889206016,0,0,0,889206016,0,0,0,0,0,0,0,0,0,889206016,0,889206016,0,889206016,0,889206016,0,0,0,0,0,0,0,0,0,889206016,0,0,0,889206016,0,0,0,0,0,0,0,0,0,0,0,0,0,889206016,0,0,0,889206016,0,0,0,0,0,0,0,0,
0,0,0,889206016,0,0,0,889206016,0,0,0,0,0,0,0,0,0,889206016,0,889206016,0,889206016,0,889206016,0,0,0,0,0,0,0,0,0,889206016,0,0,0,889206016,0,0,0,0,0,0,0,0,0,0,0,0,0,889206016,0,0,0,889206016,0,0,0,0,0,0,0,0,
0,0,0,889206016,0,0,0,889206016,0,0,0,0,0,0,0,0,0,889206016,0,889206016,0,889206016,0,889206016,0,0,0,0,0,0,0,0,0,889206016,0,0,0,889206016,0,0,0,0,0,0,0,0,0,0,0,0,0,889206016,0,0,0,889206016,0,0,0,0,0,0,0,0,
0,0,0,889206016,0,0,0,889206016,0,0,0,0,0,0,0,0,0,889206016,0,889206016,0,889206016,0,889206016,0,0,0,0,0,0,0,0,0,889206016,0,0,0,889206016,0,0,0,0,0,0,0,0,0,0,0,0,0,889206016,0,0,0,889206016,0,0,0,0,0,0,0,0,
0,0,0,889206016,0,0,0,889206016,0,0,0,0,0,0,0,0,0,889206016,0,889206016,0,889206016,0,889206016,0,0,0,0,0,0,0,0,0,889206016,0,0,0,889206016,0,0,0,0,0,0,0,0,0,0,0,0,0,889206016,0,0,0,889206016,0,0,0,0,0,0,0,0,
0,889206016,0,40960,0,889206016,0,40976,0,0,0,40992,0,0,0,41008,0,0,0,41024,0,0,0,41040,0,0,0,41056,0,0,0,41072,0,889206016,0,41088,0,889206016,0,41104,0,0,0,41120,0,0,0,41136,0,889206016,0,41152,0,889206016,0,41168,0,0,0,41184,0,0,0,41200,
0,889206016,0,41216,0,889206016,0,41232,0,0,0,41248,0,0,0,41264,0,0,0,41280,0,0,0,41296,0,0,0,41312,0,0,0,41328,0,889206016,0,41344,0,889206016,0,41360,0,0,0,41376,0,0,0,41392,0,889206016,0,41408,0,889206016,0,41424,0,0,0,41440,0,0,0,41456,
0,889206016,0,41472,0,889206016,0,41488,0,0,0,41504,0,0,0,41520,0,0,0,41536,0,0,0,41552,0,0,0,41568,0,0,0,41584,0,889206016,0,41600,0,889206016,0,41616,0,0,0,41632,0,0,0,41648,0,889206016,0,41664,0,889206016,0,41680,0,0,0,41696,0,0,0,41712,
0,889206016,0,41728,0,889206016,0,41744,0,0,0,41760,0,0,0,41776,0,0,0,41792,0,0,0,41808,0,0,0,41824,0,0,0,41840,0,889206016,0,41856,0,889206016,0,41872,0,0,0,41888,0,0,0,41904,0,889206016,0,41920,0,889206016,0,41936,0,0,0,41952,0,0,0,41968,
0,889206016,0,41984,0,889206016,0,42000,0,0,0,42016,0,0,0,42032,0,0,0,42048,0,0,0,42064,0,0,0,42080,0,0,0,42096,0,889206016,0,42112,0,889206016,0,42128,0,0,0,42144,0,0,0,42160,0,889206016,0,42176,0,889206016,0,42192,0,0,0,42208,0,0,0,42224,
0,889206016,0,42240,0,889206016,0,42256,0,0,0,42272,0,0,0,42288,0,0,0,42304,0,0,0,42320,0,0,0,42336,0,0,0,42352,0,889206016,0,42368,0,889206016,0,42384,0,0,0,42400,0,0,0,42416,0,889206016,0,42432,0,889206016,0,42448,0,0,0,42464,0,0,0,42480,
0,889206016,0,42496,0,889206016,0,42512,0,0,0,42528,0,0,0,42544,0,0,0,42560,0,0,0,42576,0,0,0,42592,0,0,0,42608,0,889206016,0,42624,0,889206016,0,42640,0,0,0,42656,0,0,0,42672,0,889206016,0,42688,0,889206016,0,42704,0,0,0,42720,0,0,0,42736,
0,889206016,0,42752,0,889206016,0,42768,0,0,0,42784,0,0,0,42800,0,0,0,42816,0,0,0,42832,0,0,0,42848,0,0,0,42864,0,889206016,0,42880,0,889206016,0,42896,0,0,0,42912,0,0,0,42928,0,889206016,0,42944,0,889206016,0,42960,0,0,0,42976,0,0,0,42992,
0,889206016,0,43008,0,889206016,0,43024,0,0,0,43040,0,0,0,43056,0,0,0,43072,0,0,0,43088,0,0,0,43104,0,0,0,43120,0,889206016,0,43136,0,889206016,0,43152,0,0,0,43168,0,0,0,43184,0,889206016,0,43200,0,889206016,0,43216,0,0,0,43232,0,0,0,43248,
0,889206016,0,43264,0,889206016,0,43280,0,0,0,43296,0,0,0,43312,0,0,0,43328,0,0,0,43344,0,0,0,43360,0,0,0,43376,0,889206016,0,43392,0,889206016,0,43408,0,0,0,43424,0,0,0,43440,0,889206016,0,43456,0,889206016,0,43472,0,0,0,43488,0,0,0,43504,
0,889206016,0,43520,0,889206016,0,43536,0,0,0,43552,0,0,0,43568,0,0,0,43584,0,0,0,43600,0,0,0,43616,0,0,0,43632,0,889206016,0,43648,0,889206016,0,43664,0,0,0,43680,0,0,0,43696,0,889206016,0,43712,0,889206016,0,43728,0,0,0,43744,0,0,0,43760,
0,889206016,0,43776,0,889206016,0,43792,0,0,0,43808,0,0,0,43824,0,0,0,43840,0,0,0,43856,0,0,0,43872,0,0,0,43888,0,889206016,0,43904,0,889206016,0,43920,0,0,0,43936,0,0,0,43952,0,889206016,0,43968,0,889206016,0,43984,0,0,0,44000,0,0,0,44016,
0,889206016,0,44032,0,889206016,0,44048,0,0,0,44064,0,0,0,44080,0,0,0,44096,0,0,0,44112,0,0,0,44128,0,0,0,44144,0,889206016,0,44160,0,889206016,0,44176,0,0,0,44192,0,0,0,44208,0,889206016,0,44224,0,889206016,0,44240,0,0,0,44256,0,0,0,44272,
0,889206016,0,44288,0,889206016,0,44304,0,0,0,44320,0,0,0,44336,0,0,0,44352,0,0,0,44368,0,0,0,44384,0,0,0,44400,0,889206016,0,44416,0,889206016,0,44432,0,0,0,44448,0,0,0,44464,0,889206016,0,44480,0,889206016,0,44496,0,0,0,44512,0,0,0,44528,
0,889206016,0,44544,0,889206016,0,44560,0,0,0,44576,0,0,0,44592,0,0,0,44608,0,0,0,44624,0,0,0,44640,0,0,0,44656,0,889206016,0,44672,0,889206016,0,44688,0,0,0,44704,0,0,0,44720,0,889206016,0,44736,0,889206016,0,44752,0,0,0,44768,0,0,0,44784,
0,889206016,0,44800,0,889206016,0,44816,0,0,0,44832,0,0,0,44848,0,0,0,44864,0,0,0,44880,0,0,0,44896,0,0,0,44912,0,889206016,0,44928,0,889206016,0,44944,0,0,0,44960,0,0,0,44976,0,889206016,0,44992,0,889206016,0,45008,0,0,0,45024,0,0,0,45040};

DI float bf_lo(u32 v) { return __uint_as_float(v << 16); }
DI u32 pk_bf16(float a, float b) { f32x2 t = {a, b}; return __builtin_bit_cast(u32, __builtin_convertvector(t, bf16x2)); }
DI u32 pk_f16(float a, float b) { f32x2 t = {a, b}; return __builtin_bit_cast(u32, __builtin_convertvector(t, f16x2)); }
DI u32x4 srd(const void* p, u32 n) { return (u32x4){(u32)(size_t)p, (u32)((size_t)p >> 32), n, 0x00020000u}; }

DI void slow_path(const u16* __restrict__ x, const u16* __restrict__ dwl, const u16* __restrict__ dws, const u16* __restrict__ w1,
                  const u16* __restrict__ w2, u16* __restrict__ out, float* sm, int t, int tx, int ty0) {
  float* o = sm;
  float* g = sm + 8 * CH;
  float* st = g + 8 * HID;
#pragma unroll 1
  for (int b = 0; b < 128; ++b) {
    const int y = ty0 * 16 + (b >> 1), x0 = tx * 16 + (b & 1) * 8;
    __syncthreads();
#pragma unroll 1
    for (int i = t; i < 8 * CH; i += 256) {
      const int p = i / CH, c = i - p * CH;
      float acc = 0.f;
#pragma unroll 1
      for (int a = 0; a < 5; ++a)
#pragma unroll 1
        for (int e = 0; e < 5; ++e) {
          const int yy = y + a - 2, xc = x0 + p + e - 2;
          if (yy < 0 || yy >= IMG || xc < 0 || xc >= IMG) continue;
          const float xv = bf_lo(x[(size_t)(yy * IMG + xc) * CH + c]);
          acc = __builtin_fmaf(bf_lo(dws[(a * 5 + e) * CH + c]), xv, acc);
          if (a >= 1 && a <= 3 && e >= 1 && e <= 3) acc = __builtin_fmaf(bf_lo(dwl[((a - 1) * 3 + (e - 1)) * CH + c]), xv, acc);
        }
      o[i] = acc;
    }
    __syncthreads();
    if (t < 8) {
      float m = 0.f, v = 0.f;
#pragma unroll 1
      for (int c = 0; c < CH; ++c) m += o[t * CH + c];
      m *= (1.0f / CH);
#pragma unroll 1
      for (int c = 0; c < CH; ++c) { const float d = o[t * CH + c] - m; v = __builtin_fmaf(d, d, v); }
      st[2 * t] = m; st[2 * t + 1] = __builtin_amdgcn_rsqf(v * (1.0f / CH) + 1e-5f);
    }
    __syncthreads();
#pragma unroll 1
    for (int i = t; i < 8 * HID; i += 256) {
      const int p = i / HID, e = i - p * HID;
      const float m = st[2 * p], r = st[2 * p + 1];
      float u = 0.f;
#pragma unroll 1
      for (int c = 0; c < CH; ++c) u = __builtin_fmaf((o[p * CH + c] - m) * r, bf_lo(w1[c * HID + e]), u);
      const float z = u * __builtin_fmaf(u * u, GC3, GC1);
      g[i] = u * __builtin_amdgcn_rcpf(1.0f + __builtin_amdgcn_exp2f(z));
    }
    __syncthreads();
#pragma unroll 1
    for (int i = t; i < 8 * CH; i += 256) {
      const int p = i / CH, c = i - p * CH;
      float acc = 0.f;
#pragma unroll 1
      for (int e = 0; e < HID; ++e) acc = __builtin_fmaf(g[p * HID + e], bf_lo(w2[e * CH + c]), acc);
      const size_t idx = (size_t)(y * IMG + x0 + p) * CH + c;
      out[idx] = (u16)pk_bf16(bf_lo(x[idx]) + acc, 0.f);
    }
  }
}

__global__ __launch_bounds__(256, 1) void replk_kernel(const u16* __restrict__ x, const u16* __restrict__ dwl,
    const u16* __restrict__ dws, const u16* __restrict__ w1, const u16* __restrict__ w2, u16* __restrict__ out
    ) {
  __shared__ __attribute__((aligned(16))) char smem[L_TOTAL];
  __attribute__((address_space(3))) char* smem3 = (__attribute__((address_space(3))) char*)smem;
  const int tid = threadIdx.x, lane = tid & 63, w = __builtin_amdgcn_readfirstlane(tid >> 6), bid = blockIdx.x;
  const int tx = bid >> 3, ty0 = (bid & 7) * 4;
  const int Y0 = ty0 * 16, X0 = tx * 16;

  const int t240 = min(tid, 239), px = (t240 * 5462) >> 16, tr = (t240 * 1366) >> 16;
  u32x8 PA;
  PA[0] = ((tx == 0 && px < 2) || (tx == 31 && px >= 18)) ? 0x80000000u : (u32)((Y0 * IMG + X0 - 2) * PXB + t240 * 16);
  PA[1] = (u32)((8 * (tid >> 6) + ((tid >> 4) & 3)) * (HID * 2) + (tid & 15) * 16);
  PA[2] = (u32)((tid >> 7) * 36864 + ((tid >> 6) & 1) * 9216 + lane * 16);
  PA[3] = (u32)(t240 * 4 + tr * (4 * PXB));
  PA[4] = (tr >= 1 && tr <= 3) ? (u32)(t240 * 4 + tr * (2 * PXB) - 3 * PXB) : 0x80000000u;
  PA[5] = (u32)(tid * 16);
  {
    u64 pc; u32 clen;
    asm volatile("s_getpc_b64 %0\n.Lreplk_pc0:\n s_mov_b32 %1, .Lreplk_end-.Lreplk_pc0-4" : "=s"(pc), "=s"(clen));
    const u64 base = w == 0 ? (u64)w1 : w == 1 ? (u64)w2 : pc;
    const u32 line = w < 2 ? (u32)(tx * 18 + min(lane, 17)) : (u32)(((tx & 15) * 2 + (w & 1)) * 16 + (lane & 15));
    const u64 pa = base + min(line * 128u, w < 2 ? 0x20000u : clen);
    PA[6] = (u32)pa; PA[7] = (u32)(pa >> 32);
  }
  const u32x4 rx = srd(x, XBYTES);
  u32x4 rt = srd((const char*)x - 2 * ROWB, ty0 ? XBYTES : 0u);
  const u32x8 rw = {(u32)(size_t)w1, (u32)((size_t)w1 >> 32), CH * HID * 2, 0x00020000u, (u32)(size_t)w2, (u32)((size_t)w2 >> 32), CH * HID * 2, 0x00020000u};
  const u32x8 rd = {(u32)(size_t)dws, (u32)((size_t)dws >> 32), 25 * PXB, 0x00020000u, (u32)(size_t)dwl, (u32)((size_t)dwl >> 32), 9 * PXB, 0x00020000u};
  u32x8 S = {MU_PK, CC_PK, __float_as_uint(GAMMA * 0x1p-106f), (u32)(w >> 1), 0u, (u32)(((Y0 + 18 + w) * IMG + X0 - 2) * PXB - (L_W + (w & 1) * 1024) * 8), (u32)(L_XT - L_H + w * (XT_CG - GS)), (u32)(L_XT + w * 256 - (L_H + w * GS) / 4)};
  const u32x4 T = {(u32)(size_t)CTAB, (u32)((size_t)CTAB >> 32), (u32)(((Y0 + 14) * IMG + X0 - 2) * PXB), (u32)((Y0 * IMG + X0) * PXB)};
  u32 flg = (u32)(size_t)smem3 ? 1u : 0u;
  asm volatile(
    ".p2align 3\n"
    "s_add_u32 vcc_lo, s96, 16384\n"
    "s_addc_u32 vcc_hi, s97, 0\n"
    "global_load_dwordx4 v[216:219], v213, vcc\n"
    "s_add_u32 vcc_lo, s96, 20480\n"
    "s_addc_u32 vcc_hi, s97, 0\n"
    "global_load_dwordx4 v[220:223], v213, vcc\n"
    "buffer_load_dword v224, v211, s[88:91], 0 offen offset:0\n"
    "buffer_load_dword v225, v211, s[88:91], 0 offen offset:192\n"
    "buffer_load_dword v226, v211, s[88:91], 0 offen offset:384\n"
    "buffer_load_dword v227, v211, s[88:91], 0 offen offset:576\n"
    "buffer_load_dword v228, v211, s[88:91], 0 offen offset:768\n"
    "buffer_load_dword v229, v212, s[92:95], 0 offen offset:0\n"
    "buffer_load_dword v230, v212, s[92:95], 0 offen offset:192\n"
    "buffer_load_dword v231, v212, s[92:95], 0 offen offset:384\n"
    "global_load_dword v233, v[214:215], off\n"
    "s_add_u32 m0, s38, 0\n"
    "s_mov_b32 s64, 0\n"
    "buffer_load_dwordx4 v210, s[84:87], s64 offen offset:0 lds\n"
    "v_mov_b32_e64 v88, 0\n"
    "v_mov_b32_e64 v89, 0\n"
    "v_mov_b32_e64 v90, 0\n"
    "v_mov_b32_e64 v91, 0\n"
    "v_mov_b32_e64 v59, 0\n"
    "v_mov_b32_e64 v60, 0\n"
    "v_mov_b32_e64 v67, 0\n"
    "v_mov_b32_e64 v68, 0\n"
    "v_mov_b32_e64 v75, 0\n"
    "v_mov_b32_e64 v76, 0\n"
    "v_mov_b32_e64 v83, 0\n"
    "v_mov_b32_e64 v84, 0\n"
    "v_mov_b32_e32 v36, 0x75089ba4\n"
    "v_mov_b32_e32 v38, 0xbdd2d3e8\n"
    "v_mov_b32_e32 v32, 0xc0135761\n"
    "v_lshlrev_b32_e64 v34, 16, v213\n"
    "v_lshlrev_b32_e64 v234, 1, v213\n"
    "v_or_b32_e32 v0, 0x8000, v34\n"
    "v_or_b32_e32 v1, 0x18000, v34\n"
    "v_or_b32_e32 v2, 0x28000, v34\n"
    "v_or_b32_e32 v3, 0x38000, v34\n"
    "v_or_b32_e32 v4, 0x48000, v34\n"
    "v_or_b32_e32 v5, 0x58000, v34\n"
    "v_or_b32_e32 v6, 0x68000, v34\n"
    "v_or_b32_e32 v7, 0x78000, v34\n"
    "v_pk_mul_f32 v[0:1], v[0:1], v[36:37] op_sel_hi:[1,0]\n"
    "v_pk_mul_f32 v[2:3], v[2:3], v[36:37] op_sel_hi:[1,0]\n"
    "v_pk_mul_f32 v[4:5], v[4:5], v[36:37] op_sel_hi:[1,0]\n"
    "v_pk_mul_f32 v[6:7], v[6:7], v[36:37] op_sel_hi:[1,0]\n"
    "v_pk_mul_f32 v[8:9], v[0:1], v[0:1]\n"
    "v_pk_mul_f32 v[10:11], v[2:3], v[2:3]\n"
    "v_pk_mul_f32 v[12:13], v[4:5], v[4:5]\n"
    "v_pk_mul_f32 v[14:15], v[6:7], v[6:7]\n"
    "v_pk_fma_f32 v[8:9], v[8:9], v[38:39], v[32:33] op_sel_hi:[1,0,0]\n"
    "v_pk_fma_f32 v[10:11], v[10:11], v[38:39], v[32:33] op_sel_hi:[1,0,0]\n"
    "v_pk_fma_f32 v[12:13], v[12:13], v[38:39], v[32:33] op_sel_hi:[1,0,0]\n"
    "v_pk_fma_f32 v[14:15], v[14:15], v[38:39], v[32:33] op_sel_hi:[1,0,0]\n"
    "v_pk_mul_f32 v[8:9], v[8:9], v[0:1]\n"
    "v_pk_mul_f32 v[10:11], v[10:11], v[2:3]\n"
    "v_pk_mul_f32 v[12:13], v[12:13], v[4:5]\n"
    "v_pk_mul_f32 v[14:15], v[14:15], v[6:7]\n"
    "v_exp_f32_e64 v8, v8\n"
    "v_exp_f32_e64 v9, v9\n"
    "v_exp_f32_e64 v10, v10\n"
    "v_exp_f32_e64 v11, v11\n"
    "v_exp_f32_e64 v12, v12\n"
    "v_exp_f32_e64 v13, v13\n"
    "v_exp_f32_e64 v14, v14\n"
    "v_exp_f32_e64 v15, v15\n"
    "v_pk_add_f32 v[16:17], v[8:9], 1.0 op_sel_hi:[1,0]\n"
    "v_pk_add_f32 v[18:19], v[10:11], 1.0 op_sel_hi:[1,0]\n"
    "v_pk_add_f32 v[20:21], v[12:13], 1.0 op_sel_hi:[1,0]\n"
    "v_pk_add_f32 v[22:23], v[14:15], 1.0 op_sel_hi:[1,0]\n"
    "v_rcp_f32_e64 v16, v16\n"
    "v_rcp_f32_e64 v17, v17\n"
    "v_rcp_f32_e64 v18, v18\n"
    "v_rcp_f32_e64 v19, v19\n"
    "v_rcp_f32_e64 v20, v20\n"
    "v_rcp_f32_e64 v21, v21\n"
    "v_rcp_f32_e64 v22, v22\n"
    "v_rcp_f32_e64 v23, v23\n"
    "v_pk_mul_f32 v[16:17], v[16:17], v[0:1]\n"
    "v_pk_mul_f32 v[18:19], v[18:19], v[2:3]\n"
    "v_pk_mul_f32 v[20:21], v[20:21], v[4:5]\n"
    "v_pk_mul_f32 v[22:23], v[22:23], v[6:7]\n"
    "v_pk_mul_f32 v[8:9], v[8:9], v[16:17]\n"
    "v_pk_mul_f32 v[10:11], v[10:11], v[18:19]\n"
    "v_pk_mul_f32 v[12:13], v[12:13], v[20:21]\n"
    "v_pk_mul_f32 v[14:15], v[14:15], v[22:23]\n"
    "v_cvt_pk_bf16_f32 v28, v16, v17\n"
    "v_cvt_pk_bf16_f32 v29, v18, v19\n"
    "v_cvt_pk_bf16_f32 v30, v20, v21\n"
    "v_cvt_pk_bf16_f32 v31, v22, v23\n"
    "v_cvt_pk_bf16_f32 v24, -v8, -v9\n"
    "v_cvt_pk_bf16_f32 v25, -v10, -v11\n"
    "v_cvt_pk_bf16_f32 v26, -v12, -v13\n"
    "v_cvt_pk_bf16_f32 v27, -v14, -v15\n"
    "v_accvgpr_write_b32 a216, v28\n"
    "v_accvgpr_write_b32 a217, v29\n"
    "s_mov_b64 s[68:69], s[44:45]\n"
    "s_mov_b32 s70, 0\n"
    "s_mov_b32 s71, s47\n"
    "s_mov_b64 s[72:73], s[40:41]\n"
    "s_mov_b64 s[74:75], s[42:43]\n"
    "s_mov_b32 s57, 0\n"
    "s_mov_b32 s58, 196608\n"
    "s_mov_b32 s60, 0\n"
    "s_mov_b32 s61, 393216\n"
    "s_mov_b32 s62, 786432\n"
    "s_mov_b32 s63, 1179648\n"
    "s_mov_b32 s52, 1572864\n"
    "s_mov_b32 s100, 98304\n"
    "s_mov_b32 s101, 294912\n"
    "s_mov_b32 s39, 0\n"
    "v_accvgpr_write_b32 a218, v30\n"
    "v_accvgpr_write_b32 a219, v31\n"
    "ds_write_b128 v234, v[24:27] offset:0\n"
    "v_or_b32_e32 v0, 0x88000, v34\n"
    "v_or_b32_e32 v1, 0x98000, v34\n"
    "v_or_b32_e32 v2, 0xa8000, v34\n"
    "v_or_b32_e32 v3, 0xb8000, v34\n"
    "v_or_b32_e32 v4, 0xc8000, v34\n"
    "v_or_b32_e32 v5, 0xd8000, v34\n"
    "v_or_b32_e32 v6, 0xe8000, v34\n"
    "v_or_b32_e32 v7, 0xf8000, v34\n"
    "v_pk_mul_f32 v[0:1], v[0:1], v[36:37] op_sel_hi:[1,0]\n"
    "v_pk_mul_f32 v[2:3], v[2:3], v[36:37] op_sel_hi:[1,0]\n"
    "v_pk_mul_f32 v[4:5], v[4:5], v[36:37] op_sel_hi:[1,0]\n"
    "v_pk_mul_f32 v[6:7], v[6:7], v[36:37] op_sel_hi:[1,0]\n"
    "v_pk_mul_f32 v[8:9], v[0:1], v[0:1]\n"
    "v_pk_mul_f32 v[10:11], v[2:3], v[2:3]\n"
    "v_pk_mul_f32 v[12:13], v[4:5], v[4:5]\n"
    "v_pk_mul_f32 v[14:15], v[6:7], v[6:7]\n"
    "v_pk_fma_f32 v[8:9], v[8:9], v[38:39], v[32:33] op_sel_hi:[1,0,0]\n"
    "v_pk_fma_f32 v[10:11], v[10:11], v[38:39], v[32:33] op_sel_hi:[1,0,0]\n"
    "v_pk_fma_f32 v[12:13], v[12:13], v[38:39], v[32:33] op_sel_hi:[1,0,0]\n"
    "v_pk_fma_f32 v[14:15], v[14:15], v[38:39], v[32:33] op_sel_hi:[1,0,0]\n"
    "v_pk_mul_f32 v[8:9], v[8:9], v[0:1]\n"
    "v_pk_mul_f32 v[10:11], v[10:11], v[2:3]\n"
    "v_pk_mul_f32 v[12:13], v[12:13], v[4:5]\n"
    "v_pk_mul_f32 v[14:15], v[14:15], v[6:7]\n"
    "v_exp_f32_e64 v8, v8\n"
    "v_exp_f32_e64 v9, v9\n"
    "v_exp_f32_e64 v10, v10\n"
    "v_exp_f32_e64 v11, v11\n"
    "v_exp_f32_e64 v12, v12\n"
    "v_exp_f32_e64 v13, v13\n"
    "v_exp_f32_e64 v14, v14\n"
    "v_exp_f32_e64 v15, v15\n"
    "v_pk_add_f32 v[16:17], v[8:9], 1.0 op_sel_hi:[1,0]\n"
    "v_pk_add_f32 v[18:19], v[10:11], 1.0 op_sel_hi:[1,0]\n"
    "v_pk_add_f32 v[20:21], v[12:13], 1.0 op_sel_hi:[1,0]\n"
    "v_pk_add_f32 v[22:23], v[14:15], 1.0 op_sel_hi:[1,0]\n"
    "v_rcp_f32_e64 v16, v16\n"
    "v_rcp_f32_e64 v17, v17\n"
    "v_rcp_f32_e64 v18, v18\n"
    "v_rcp_f32_e64 v19, v19\n"
    "v_rcp_f32_e64 v20, v20\n"
    "v_rcp_f32_e64 v21, v21\n"
    "v_rcp_f32_e64 v22, v22\n"
    "v_rcp_f32_e64 v23, v23\n"
    "v_pk_mul_f32 v[16:17], v[16:17], v[0:1]\n"
    "v_pk_mul_f32 v[18:19], v[18:19], v[2:3]\n"
    "v_pk_mul_f32 v[20:21], v[20:21], v[4:5]\n"
    "v_pk_mul_f32 v[22:23], v[22:23], v[6:7]\n"
    "v_pk_mul_f32 v[8:9], v[8:9], v[16:17]\n"
    "v_pk_mul_f32 v[10:11], v[10:11], v[18:19]\n"
    "v_pk_mul_f32 v[12:13], v[12:13], v[20:21]\n"
    "v_pk_mul_f32 v[14:15], v[14:15], v[22:23]\n"
    "v_cvt_pk_bf16_f32 v28, v16, v17\n"
    "v_cvt_pk_bf16_f32 v29, v18, v19\n"
    "v_cvt_pk_bf16_f32 v30, v20, v21\n"
    "v_cvt_pk_bf16_f32 v31, v22, v23\n"
    "v_cvt_pk_bf16_f32 v24, -v8, -v9\n"
    "v_cvt_pk_bf16_f32 v25, -v10, -v11\n"
    "v_cvt_pk_bf16_f32 v26, -v12, -v13\n"
    "v_cvt_pk_bf16_f32 v27, -v14, -v15\n"
    "v_accvgpr_write_b32 a220, v28\n"
    "v_accvgpr_write_b32 a221, v29\n"
    "v_accvgpr_write_b32 a222, v30\n"
    "v_accvgpr_write_b32 a223, v31\n"
    "ds_write_b128 v234, v[24:27] offset:16\n"
    "v_add_u32_e32 v234, 65536, v234\n"
    "buffer_load_dwordx4 v210, s[84:87], s64 offen offset:1024 lds\n"
    "s_waitcnt vmcnt(12)\n"
    "v_xor_b32_e32 v211, 96, v219\n"
    "buffer_load_dwordx4 v210, s[84:87], s64 offen offset:2048 lds\n"
    "s_waitcnt vmcnt(12)\n"
    "ds_write_b128 v222, v[88:91]\n"
    "s_add_u32 m0, s38, 3072\n"
    "s_mov_b32 s64, 3072\n"
    "buffer_load_dwordx4 v210, s[84:87], s64 offen offset:0 lds\n"
    "buffer_load_dwordx4 v210, s[84:87], s64 offen offset:1024 lds\n"
    "buffer_load_dwordx4 v210, s[84:87], s64 offen offset:2048 lds\n"
    "s_add_u32 m0, s38, 6144\n"
    "s_mov_b32 s64, 6144\n"
    "buffer_load_dwordx4 v210, s[84:87], s64 offen offset:0 lds\n"
    "buffer_load_dwordx4 v210, s[84:87], s64 offen offset:1024 lds\n"
    "s_waitcnt vmcnt(16)\n"
    "v_lshlrev_b32_e32 v40, 16, v224\n"
    "buffer_load_dwordx4 v210, s[84:87], s64 offen offset:2048 lds\n"
    "s_mov_b32 s65, 0\n"
    "buffer_load_dwordx4 a[224:227], v208, s[76:79], s65 offen\n"
    "s_waitcnt vmcnt(17)\n"
    "v_lshlrev_b32_e64 v41, 16, v225\n"
    "s_mov_b32 s65, 98304\n"
    "buffer_load_dwordx4 a[228:231], v208, s[76:79], s65 offen\n"
    "s_mov_b32 s65, 196608\n"
    "buffer_load_dwordx4 a[232:235], v208, s[40:43], s65 offen\n"
    "s_waitcnt vmcnt(18)\n"
    "v_lshlrev_b32_e32 v42, 16, v226\n"
    "s_waitcnt vmcnt(17)\n"
    "v_lshlrev_b32_e32 v43, 16, v227\n"
    "s_waitcnt vmcnt(16)\n"
    "v_lshlrev_b32_e32 v44, 16, v228\n"
    "s_waitcnt vmcnt(15)\n"
    "v_lshlrev_b32_e32 v45, 16, v229\n"
    "s_waitcnt vmcnt(14)\n"
    "v_lshlrev_b32_e32 v46, 16, v230\n"
    "s_waitcnt vmcnt(13)\n"
    "v_lshlrev_b32_e32 v47, 16, v231\n"
    "v_add_f32_e64 v41, v41, v45\n"
    "v_add_f32_e64 v42, v42, v46\n"
    "v_add_f32_e64 v43, v43, v47\n"
    "v_cvt_pk_bf16_f32 v56, v40, v41\n"
    "v_cvt_pk_bf16_f32 v57, v42, v43\n"
    "v_cvt_pk_bf16_f32 v58, v44, 0\n"
    "v_cvt_pk_bf16_f32 v64, 0, v40\n"
    "s_mov_b32 s65, 294912\n"
    "buffer_load_dwordx4 a[236:239], v208, s[40:43], s65 offen\n"
    "ds_write_b128 v221, v[56:59] offset:0\n"
    "v_cvt_pk_bf16_f32 v65, v41, v42\n"
    "v_cvt_pk_bf16_f32 v66, v43, v44\n"
    "v_cvt_pk_bf16_f32 v61, v40, v41\n"
    "v_cvt_pk_bf16_f32 v62, v42, v43\n"
    "v_cvt_pk_bf16_f32 v63, v44, 0\n"
    "v_cvt_pk_bf16_f32 v69, 0, v40\n"
    "v_cvt_pk_bf16_f32 v70, v41, v42\n"
    "v_cvt_pk_bf16_f32 v71, v43, v44\n"
    "ds_write_b128 v221, v[64:67] offset:16\n"
    "s_mov_b32 s65, 589824\n"
    "buffer_load_dwordx4 a[240:243], v208, s[40:43], s65 offen\n"
    "v_and_b32_e32 v48, 0xffff0000, v224\n"
    "v_and_b32_e32 v49, 0xffff0000, v225\n"
    "v_and_b32_e32 v50, 0xffff0000, v226\n"
    "v_and_b32_e32 v51, 0xffff0000, v227\n"
    "ds_write_b128 v221, v[60:63] offset:32\n"
    "v_and_b32_e32 v52, 0xffff0000, v228\n"
    "v_and_b32_e32 v53, 0xffff0000, v229\n"
    "v_and_b32_e32 v54, 0xffff0000, v230\n"
    "v_and_b32_e32 v55, 0xffff0000, v231\n"
    "v_add_f32_e64 v49, v49, v53\n"
    "v_add_f32_e64 v50, v50, v54\n"
    "ds_write_b128 v221, v[68:71] offset:48\n"
    "s_mov_b32 s65, 688128\n"
    "buffer_load_dwordx4 a[244:247], v208, s[40:43], s65 offen\n"
    "v_add_f32_e64 v51, v51, v55\n"
    "v_cvt_pk_bf16_f32 v72, v48, v49\n"
    "v_cvt_pk_bf16_f32 v73, v50, v51\n"
    "v_cvt_pk_bf16_f32 v74, v52, 0\n"
    "v_cvt_pk_bf16_f32 v80, 0, v48\n"
    "v_cvt_pk_bf16_f32 v81, v49, v50\n"
    "v_cvt_pk_bf16_f32 v82, v51, v52\n"
    "v_cvt_pk_bf16_f32 v77, v48, v49\n"
    "v_cvt_pk_bf16_f32 v78, v50, v51\n"
    "v_cvt_pk_bf16_f32 v79, v52, 0\n"
    "v_cvt_pk_bf16_f32 v85, 0, v48\n"
    "v_cvt_pk_bf16_f32 v86, v49, v50\n"
    "v_cvt_pk_bf16_f32 v87, v51, v52\n"
    "ds_write_b128 v221, v[72:75] offset:336\n"
    "s_mov_b32 s65, 983040\n"
    "buffer_load_dwordx4 a[248:251], v208, s[40:43], s65 offen\n"
    "v_xor_b32_e32 v212, 0, v223\n"
    "v_xor_b32_e32 v214, 64, v223\n"
    "v_xor_b32_e32 v215, 96, v223\n"
    "ds_write_b128 v221, v[80:83] offset:352\n"
    "s_mov_b32 s65, 1081344\n"
    "buffer_load_dwordx4 a[252:255], v208, s[40:43], s65 offen\n"
    "ds_write_b128 v221, v[76:79] offset:368\n"
    "ds_write_b128 v221, v[84:87] offset:384\n"
    "s_mov_b32 s65, 1376256\n"
    "buffer_load_dwordx4 a[168:171], v208, s[40:43], s65 offen\n"
    "v_lshl_add_u32 v221, s51, 8, v211\n"
    "s_mov_b32 s65, 1474560\n"
    "buffer_load_dwordx4 a[172:175], v208, s[40:43], s65 offen\n"
    "s_add_u32 vcc_lo, s96, 0\n"
    "s_addc_u32 vcc_hi, s97, 0\n"
    "global_load_dwordx4 v[240:243], v213, vcc\n"
    "s_add_u32 vcc_lo, s96, 8192\n"
    "s_nop 0\n"
    "s_addc_u32 vcc_hi, s97, 0\n"
    "global_load_dwordx4 v[248:251], v213, vcc\n"
    "s_waitcnt vmcnt(12)\n"
    "s_barrier\n"
    "ds_read_b64_tr_b16 a[0:1], v220 offset:0\n"
    "ds_read_b64_tr_b16 a[2:3], v220 offset:3072\n"
    "ds_read_b64_tr_b16 a[4:5], v220 offset:8\n"
    "ds_read_b64_tr_b16 a[6:7], v220 offset:3080\n"
    "ds_read_b64_tr_b16 a[8:9], v220 offset:64\n"
    "ds_read_b64_tr_b16 a[10:11], v220 offset:3136\n"
    "ds_read_b64_tr_b16 a[12:13], v220 offset:72\n"
    "ds_read_b64_tr_b16 a[14:15], v220 offset:3144\n"
    "ds_read_b64_tr_b16 v[168:169], v218 offset:128\n"
    "ds_read_b64_tr_b16 v[170:171], v218 offset:3200\n"
    "ds_read_b64_tr_b16 a[16:17], v220 offset:6144\n"
    "ds_read_b64_tr_b16 a[18:19], v220 offset:9216\n"
    "ds_read_b64_tr_b16 a[20:21], v220 offset:6152\n"
    "ds_read_b64_tr_b16 a[22:23], v220 offset:9224\n"
    "ds_read_b64_tr_b16 a[24:25], v220 offset:6208\n"
    "ds_read_b64_tr_b16 a[26:27], v220 offset:9280\n"
    "ds_read_b64_tr_b16 a[28:29], v220 offset:6216\n"
    "ds_read_b64_tr_b16 a[30:31], v220 offset:9288\n"
    "ds_read_b64_tr_b16 v[172:173], v218 offset:6272\n"
    "ds_read_b64_tr_b16 v[174:175], v218 offset:9344\n"
    "ds_read_b64_tr_b16 a[32:33], v220 offset:12288\n"
    "ds_read_b64_tr_b16 a[34:35], v220 offset:15360\n"
    "s_add_u32 vcc_lo, s96, 12288\n"
    "s_nop 0\n"
    "s_addc_u32 vcc_hi, s97, 0\n"
    "global_load_dwordx4 v[252:255], v213, vcc\n"
    "ds_read_b64_tr_b16 a[36:37], v220 offset:12296\n"
    "ds_read_b64_tr_b16 a[38:39], v220 offset:15368\n"
    "ds_read_b64_tr_b16 a[40:41], v220 offset:12352\n"
    "ds_read_b64_tr_b16 a[42:43], v220 offset:15424\n"
    "ds_read_b64_tr_b16 a[44:45], v220 offset:12360\n"
    "ds_read_b64_tr_b16 a[46:47], v220 offset:15432\n"
    "ds_read_b64_tr_b16 v[176:177], v218 offset:12416\n"
    "ds_read_b64_tr_b16 v[178:179], v218 offset:15488\n"
    "s_waitcnt vmcnt(12)\n"
    "ds_write2_b32 v216, a224, a225 offset0:0 offset1:20\n"
    "ds_write2_b32 v216, a226, a227 offset0:40 offset1:60\n"
    "s_waitcnt lgkmcnt(2)\n"
    "s_barrier\n"
    "s_mov_b32 s65, 0\n"
    "buffer_load_dwordx4 a[176:179], v208, s[40:43], s65 offen\n"
    "s_nop 0\n"
    "s_waitcnt vmcnt(12)\n"
    "ds_write2_b32 v216, a228, a229 offset0:80 offset1:100\n"
    "ds_write2_b32 v216, a230, a231 offset0:120 offset1:140\n"
    "v_add_u32_e32 v216, 1360, v216\n"
    "s_mov_b32 s65, 98304\n"
    "buffer_load_dwordx4 a[180:183], v208, s[40:43], s65 offen\n"
    "s_waitcnt vmcnt(12)\n"
    "ds_write2_b32 v216, a232, a233 offset0:0 offset1:20\n"
    "ds_write2_b32 v216, a234, a235 offset0:40 offset1:60\n"
    "s_mov_b32 s65, 393216\n"
    "buffer_load_dwordx4 a[184:187], v208, s[40:43], s65 offen\n"
    "s_waitcnt vmcnt(12)\n"
    "ds_write2_b32 v216, a236, a237 offset0:80 offset1:100\n"
    "ds_write2_b32 v216, a238, a239 offset0:120 offset1:140\n"
    "v_add_u32_e32 v216, 1360, v216\n"
    "s_mov_b32 s65, 491520\n"
    "buffer_load_dwordx4 a[188:191], v208, s[40:43], s65 offen\n"
    "s_waitcnt vmcnt(12)\n"
    "ds_write2_b32 v216, a240, a241 offset0:0 offset1:20\n"
    "ds_write2_b32 v216, a242, a243 offset0:40 offset1:60\n"
    "s_mov_b32 s65, 786432\n"
    "buffer_load_dwordx4 a[192:195], v208, s[40:43], s65 offen\n"
    "s_waitcnt vmcnt(12)\n"
    "ds_write2_b32 v216, a244, a245 offset0:80 offset1:100\n"
    "ds_write2_b32 v216, a246, a247 offset0:120 offset1:140\n"
    "v_add_u32_e32 v216, 1360, v216\n"
    "s_mov_b32 s65, 884736\n"
    "buffer_load_dwordx4 a[196:199], v208, s[40:43], s65 offen\n"
    "s_waitcnt vmcnt(12)\n"
    "ds_write2_b32 v216, a248, a249 offset0:0 offset1:20\n"
    "ds_write2_b32 v216, a250, a251 offset0:40 offset1:60\n"
    "s_mov_b32 s65, 1179648\n"
    "buffer_load_dwordx4 a[200:203], v208, s[40:43], s65 offen\n"
    "s_waitcnt vmcnt(12)\n"
    "ds_write2_b32 v216, a252, a253 offset0:80 offset1:100\n"
    "ds_write2_b32 v216, a254, a255 offset0:120 offset1:140\n"
    "v_add_u32_e32 v216, 1360, v216\n"
    "s_mov_b32 s65, 1277952\n"
    "buffer_load_dwordx4 a[204:207], v208, s[40:43], s65 offen\n"
    "s_waitcnt vmcnt(12)\n"
    "ds_write2_b32 v216, a168, a169 offset0:0 offset1:20\n"
    "ds_write2_b32 v216, a170, a171 offset0:40 offset1:60\n"
    "s_mov_b32 s65, 1572864\n"
    "buffer_load_dwordx4 a[208:211], v208, s[40:43], s65 offen\n"
    "s_waitcnt vmcnt(12)\n"
    "ds_write2_b32 v216, a172, a173 offset0:80 offset1:100\n"
    "ds_write2_b32 v216, a174, a175 offset0:120 offset1:140\n"
    "v_add_u32_e32 v216, -4800, v216\n"
    "s_mov_b32 s65, 1671168\n"
    "buffer_load_dwordx4 a[212:215], v208, s[40:43], s65 offen\n"
    "v_xor_b32_e32 v208, 0, v219\n"
    "s_waitcnt lgkmcnt(0)\n"
    "s_barrier\n"
    "s_add_u32 m0, s38, 0\n"
    "s_mov_b32 s64, 18432\n"
    "buffer_load_dwordx4 v210, s[84:87], s64 offen offset:0 lds\n"
    "s_nop 0\n"
    "s_waitcnt vmcnt(12)\n"
    "ds_read_b128 a[224:227], v250 offset:0\n"
    "ds_read_b128 a[228:231], v250 offset:320\n"
    "ds_read_b128 a[232:235], v251 offset:0\n"
    "ds_read_b128 a[236:239], v251 offset:336\n"
    "ds_read_b128 a[240:243], v250 offset:80\n"
    "ds_read_b128 a[244:247], v250 offset:400\n"
    "ds_read_b128 a[248:251], v251 offset:672\n"
    "ds_read_b128 a[252:255], v251 offset:1008\n"
    "buffer_load_dwordx4 v210, s[84:87], s64 offen offset:1024 lds\n"
    "s_waitcnt vmcnt(12)\n"
    "v_mov_b32_e32 v222, v254\n"
    "v_mov_b32_e64 v254, 0\n"
    "ds_read_b128 a[96:99], v250 offset:160\n"
    "ds_read_b128 a[100:103], v250 offset:480\n"
    "v_mfma_f32_32x32x1_2b_f32 v[0:31], v254, v254, 0\n"
    "ds_read_b128 a[104:107], v251 offset:1344\n"
    "ds_read_b128 a[108:111], v251 offset:1680\n"
    "ds_read_b128 a[112:115], v250 offset:240\n"
    "v_add_u32_e64 v255, s99, v255\n"
    "ds_read_b128 a[116:119], v250 offset:560\n"
    "ds_read_b128 a[120:123], v251 offset:2016\n"
    "ds_read_b128 a[124:127], v251 offset:2352\n"
    "v_mfma_f32_32x32x1_2b_f32 v[32:63], v254, v254, 0\n"
    "s_waitcnt lgkmcnt(13)\n"
    "s_nop 8\n"
    "v_smfmac_f32_16x16x64_bf16 v[0:3], a[232:235], a[224:231], v240\n"
    "s_waitcnt lgkmcnt(9)\n"
    "ds_read_b128 a[232:235], v251 offset:10752\n"
    "v_mfma_f32_32x32x1_2b_f32 v[64:95], v254, v254, 0\n"
    "s_waitcnt lgkmcnt(9)\n"
    "v_add_u32_e64 v254, s54, v242\n"
    "buffer_load_dwordx4 v210, s[84:87], s64 offen offset:2048 lds\n"
    "s_waitcnt vmcnt(12)\n"
    "ds_write2_b32 v216, a176, a177 offset0:0 offset1:20\n"
    "v_smfmac_f32_16x16x64_bf16 v[4:7], a[236:239], a[224:231], v240 abid:1\n"
    "s_waitcnt lgkmcnt(6)\n"
    "ds_read_b128 a[224:227], v250 offset:27024\n"
    "v_smfmac_f32_16x16x64_bf16 v[8:11], a[248:251], a[240:247], v240\n"
    "ds_read_b128 a[228:231], v250 offset:27344\n"
    "v_smfmac_f32_16x16x64_bf16 v[12:15], a[252:255], a[240:247], v240 abid:1\n"
    "ds_read_b128 a[236:239], v251 offset:11088\n"
    "v_smfmac_f32_16x16x64_bf16 v[16:19], a[104:107], a[96:103], v240\n"
    "s_waitcnt lgkmcnt(6)\n"
    "ds_write2_b32 v216, a178, a179 offset0:40 offset1:60\n"
    "v_smfmac_f32_16x16x64_bf16 v[20:23], a[108:111], a[96:103], v240 abid:1\n"
    "s_waitcnt lgkmcnt(6)\n"
    "v_smfmac_f32_16x16x64_bf16 v[24:27], a[120:123], a[112:119], v240\n"
    "ds_read_b128 a[240:243], v250 offset:27104\n"
    "v_smfmac_f32_16x16x64_bf16 v[28:31], a[124:127], a[112:119], v240 abid:1\n"
    "ds_read_b128 a[244:247], v250 offset:27424\n"
    "ds_read_b128 a[248:251], v251 offset:11424\n"
    "ds_read_b128 a[252:255], v251 offset:11760\n"
    "ds_read_b128 a[96:99], v250 offset:27184\n"
    "ds_read_b128 a[100:103], v250 offset:27504\n"
    "ds_read_b128 a[104:107], v251 offset:12096\n"
    "s_waitcnt lgkmcnt(9)\n"
    "v_smfmac_f32_16x16x64_bf16 v[32:35], a[232:235], a[224:231], v240\n"
    "s_add_u32 m0, s38, 3072\n"
    "s_mov_b32 s64, 21504\n"
    "buffer_load_dwordx4 v210, s[84:87], s64 offen offset:0 lds\n"
    "s_waitcnt lgkmcnt(7)\n"
    "v_smfmac_f32_16x16x64_bf16 v[36:39], a[236:239], a[224:231], v240 abid:1\n"
    "ds_read_b128 a[108:111], v251 offset:12432\n"
    "ds_read_b128 a[112:115], v250 offset:27264\n"
    "ds_read_b128 a[116:119], v250 offset:27584\n"
    "ds_read_b128 a[120:123], v251 offset:12768\n"
    "ds_read_b128 a[124:127], v251 offset:13104\n"
    "ds_read_b128 a[224:227], v250 offset:54048\n"
    "ds_read_b128 a[228:231], v250 offset:54368\n"
    "ds_read_b128 a[232:235], v251 offset:21504\n"
    "ds_read_b128 a[236:239], v251 offset:21840\n"
    "s_waitcnt vmcnt(12)\n"
    "ds_write2_b32 v216, a180, a181 offset0:80 offset1:100\n"
    "s_waitcnt lgkmcnt(14)\n"
    "v_smfmac_f32_16x16x64_bf16 v[40:43], a[248:251], a[240:247], v240\n"
    "s_waitcnt lgkmcnt(13)\n"
    "v_smfmac_f32_16x16x64_bf16 v[44:47], a[252:255], a[240:247], v240 abid:1\n"
    "s_nop 0\n"
    "ds_read_b128 a[240:243], v250 offset:54128\n"
    "ds_read_b128 a[244:247], v250 offset:54448\n"
    "ds_read_b128 a[248:251], v251 offset:22176\n"
    "ds_read_b128 a[252:255], v251 offset:22512\n"
    "s_waitcnt lgkmcnt(14)\n"
    "v_smfmac_f32_16x16x64_bf16 v[48:51], a[104:107], a[96:103], v240\n"
    "s_waitcnt lgkmcnt(13)\n"
    "ds_write2_b32 v216, a182, a183 offset0:120 offset1:140\n"
    "v_smfmac_f32_16x16x64_bf16 v[52:55], a[108:111], a[96:103], v240 abid:1\n"
    "v_add_u32_e32 v216, 1360, v216\n"
    "ds_read_b128 a[96:99], v250 offset:54208\n"
    "buffer_load_dwordx4 v210, s[84:87], s64 offen offset:1024 lds\n"
    "ds_read_b128 a[100:103], v250 offset:54528\n"
    "s_waitcnt lgkmcnt(13)\n"
    "v_smfmac_f32_16x16x64_bf16 v[56:59], a[120:123], a[112:119], v240\n"
    "s_waitcnt lgkmcnt(12)\n"
    "ds_read_b128 a[104:107], v251 offset:22848\n"
    "v_smfmac_f32_16x16x64_bf16 v[60:63], a[124:127], a[112:119], v240 abid:1\n"
    "ds_read_b128 a[108:111], v251 offset:23184\n"
    "ds_read_b128 a[112:115], v250 offset:54288\n"
    "ds_read_b128 a[116:119], v250 offset:54608\n"
    "ds_read_b128 a[120:123], v251 offset:23520\n"
    "s_waitcnt lgkmcnt(14)\n"
    "v_smfmac_f32_16x16x64_bf16 v[64:67], a[232:235], a[224:231], v240\n"
    "s_waitcnt lgkmcnt(13)\n"
    "ds_read_b128 a[124:127], v251 offset:23856\n"
    "v_smfmac_f32_16x16x64_bf16 v[68:71], a[236:239], a[224:231], v240 abid:1\n"
    "s_waitcnt vmcnt(12)\n"
    "ds_write2_b32 v216, a184, a185 offset0:0 offset1:20\n"
    "ds_write2_b32 v216, a186, a187 offset0:40 offset1:60\n"
    "s_waitcnt lgkmcnt(12)\n"
    "v_smfmac_f32_16x16x64_bf16 v[72:75], a[248:251], a[240:247], v240\n"
    "ds_read_b128 a[232:235], v252 offset:0\n"
    "s_waitcnt lgkmcnt(12)\n"
    "v_smfmac_f32_16x16x64_bf16 v[76:79], a[252:255], a[240:247], v240 abid:1\n"
    "s_nop 0\n"
    "ds_read_b128 a[236:239], v252 offset:336\n"
    "ds_read_b128 a[248:251], v252 offset:672\n"
    "ds_read_b128 a[252:255], v252 offset:1008\n"
    "buffer_load_dwordx4 v210, s[84:87], s64 offen offset:2048 lds\n"
    "s_waitcnt vmcnt(12)\n"
    "ds_write2_b32 v216, a188, a189 offset0:80 offset1:100\n"
    "s_waitcnt lgkmcnt(12)\n"
    "v_smfmac_f32_16x16x64_bf16 v[80:83], a[104:107], a[96:103], v240\n"
    "s_nop 0\n"
    "ds_write2_b32 v216, a190, a191 offset0:120 offset1:140\n"
    "s_waitcnt lgkmcnt(12)\n"
    "v_smfmac_f32_16x16x64_bf16 v[84:87], a[108:111], a[96:103], v240 abid:1\n"
    "v_add_u32_e32 v216, 1360, v216\n"
    "s_waitcnt lgkmcnt(9)\n"
    "v_smfmac_f32_16x16x64_bf16 v[88:91], a[120:123], a[112:119], v240\n"
    "s_waitcnt lgkmcnt(8)\n"
    "v_smfmac_f32_16x16x64_bf16 v[92:95], a[124:127], a[112:119], v240 abid:1\n"
    "s_add_u32 m0, s38, 6144\n"
    "s_mov_b32 s64, 24576\n"
    "buffer_load_dwordx4 v210, s[84:87], s64 offen offset:0 lds\n"
    "s_waitcnt vmcnt(12)\n"
    "ds_write2_b32 v216, a192, a193 offset0:0 offset1:20\n"
    "ds_write2_b32 v216, a194, a195 offset0:40 offset1:60\n"
    "buffer_load_dwordx4 v210, s[84:87], s64 offen offset:1024 lds\n"
    "s_waitcnt vmcnt(12)\n"
    "ds_write2_b32 v216, a196, a197 offset0:80 offset1:100\n"
    "ds_write2_b32 v216, a198, a199 offset0:120 offset1:140\n"
    "v_add_u32_e32 v216, 1360, v216\n"
    "buffer_load_dwordx4 v210, s[84:87], s64 offen offset:2048 lds\n"
    "s_waitcnt vmcnt(12)\n"
    "ds_write2_b32 v216, a200, a201 offset0:0 offset1:20\n"
    "v_xor_b32_e32 v210, 64, v219\n"
    "ds_write2_b32 v216, a202, a203 offset0:40 offset1:60\n"
    "s_add_u32 vcc_lo, s96, 28672\n"
    "s_addc_u32 vcc_hi, s97, 0\n"
    "global_load_dwordx4 v[200:203], v213, vcc\n"
    "s_waitcnt vmcnt(12)\n"
    "ds_write2_b32 v216, a204, a205 offset0:80 offset1:100\n"
    "ds_write2_b32 v216, a206, a207 offset0:120 offset1:140\n"
    "v_add_u32_e32 v216, 1360, v216\n"
    "s_add_u32 vcc_lo, s96, 32768\n"
    "s_addc_u32 vcc_hi, s97, 0\n"
    "global_load_dwordx4 v[204:207], v213, vcc\n"
    "s_waitcnt vmcnt(12)\n"
    "ds_write2_b32 v216, a208, a209 offset0:0 offset1:20\n"
    "ds_write2_b32 v216, a210, a211 offset0:40 offset1:60\n"
    "s_mov_b32 s59, 49152\n"
    "buffer_load_dwordx4 v[96:99], v209, s[80:83], s59 offen offset:0\n"
    "s_nop 0\n"
    "s_waitcnt vmcnt(12)\n"
    "ds_write2_b32 v216, a212, a213 offset0:80 offset1:100\n"
    "ds_write2_b32 v216, a214, a215 offset0:120 offset1:140\n"
    "buffer_load_dwordx4 v[100:103], v209, s[80:83], s59 offen offset:256\n"
    "s_waitcnt lgkmcnt(0)\n"
    "s_barrier\n"
    "buffer_load_dwordx4 v[104:107], v209, s[80:83], s59 offen offset:512\n"
    "ds_read_b128 a[224:227], v250 offset:640\n"
    "ds_read_b128 a[228:231], v250 offset:960\n"
    "ds_read_b128 a[240:243], v250 offset:720\n"
    "ds_read_b128 a[244:247], v250 offset:1040\n"
    "s_mov_b32 s59, 52224\n"
    "buffer_load_dwordx4 v[108:111], v209, s[80:83], s59 offen offset:0\n"
    "s_waitcnt lgkmcnt(2)\n"
    "v_smfmac_f32_16x16x64_bf16 v[0:3], a[232:235], a[224:231], v240\n"
    "s_nop 0\n"
    "buffer_load_dwordx4 v[112:115], v209, s[80:83], s59 offen offset:256\n"
    "v_smfmac_f32_16x16x64_bf16 v[4:7], a[236:239], a[224:231], v240 abid:1\n"
    "ds_read_b128 a[224:227], v250 offset:800\n"
    "s_waitcnt lgkmcnt(1)\n"
    "v_smfmac_f32_16x16x64_bf16 v[8:11], a[248:251], a[240:247], v240\n"
    "s_nop 0\n"
    "ds_read_b128 a[228:231], v250 offset:1120\n"
    "v_smfmac_f32_16x16x64_bf16 v[12:15], a[252:255], a[240:247], v240 abid:1\n"
    "ds_read_b128 a[232:235], v252 offset:1344\n"
    "ds_read_b128 a[236:239], v252 offset:1680\n"
    "ds_read_b128 a[240:243], v250 offset:880\n"
    "v_pk_mov_b32 v[224:225], v[0:1], v[0:1] op_sel:[0,1]\n"
    "v_pk_mov_b32 v[226:227], v[2:3], v[2:3] op_sel:[0,1]\n"
    "v_pk_mul_f32 v[228:229], v[0:1], v[0:1]\n"
    "v_pk_mul_f32 v[230:231], v[2:3], v[2:3]\n"
    "buffer_load_dwordx4 v[116:119], v209, s[80:83], s59 offen offset:512\n"
    "v_pk_add_f32 v[224:225], v[224:225], v[4:5]\n"
    "v_pk_add_f32 v[226:227], v[226:227], v[6:7]\n"
    "v_pk_fma_f32 v[228:229], v[4:5], v[4:5], v[228:229]\n"
    "v_pk_fma_f32 v[230:231], v[6:7], v[6:7], v[230:231]\n"
    "v_pk_add_f32 v[224:225], v[224:225], v[8:9]\n"
    "v_pk_add_f32 v[226:227], v[226:227], v[10:11]\n"
    "v_pk_fma_f32 v[228:229], v[8:9], v[8:9], v[228:229]\n"
    "v_pk_fma_f32 v[230:231], v[10:11], v[10:11], v[230:231]\n"
    "v_pk_add_f32 v[224:225], v[224:225], v[12:13]\n"
    "v_pk_add_f32 v[226:227], v[226:227], v[14:15]\n"
    "v_pk_fma_f32 v[228:229], v[12:13], v[12:13], v[228:229]\n"
    "v_pk_fma_f32 v[230:231], v[14:15], v[14:15], v[230:231]\n"
    "ds_read_b128 a[244:247], v250 offset:1200\n"
    "ds_read_b128 a[248:251], v252 offset:2016\n"
    "ds_read_b128 a[252:255], v252 offset:2352\n"
    "s_mov_b32 s59, 0\n"
    "buffer_load_dwordx4 v[120:123], v209, s[80:83], s59 offen offset:0\n"
    "s_waitcnt lgkmcnt(5)\n"
    "v_smfmac_f32_16x16x64_bf16 v[16:19], a[232:235], a[224:231], v240\n"
    "ds_read_b128 a[232:235], v252 offset:10752\n"
    "s_waitcnt lgkmcnt(5)\n"
    "v_smfmac_f32_16x16x64_bf16 v[20:23], a[236:239], a[224:231], v240 abid:1\n"
    "ds_read_b128 a[224:227], v250 offset:27664\n"
    "ds_read_b128 a[228:231], v250 offset:27984\n"
    "ds_read_b128 a[236:239], v252 offset:11088\n"
    "s_nop 1\n"
    "v_pk_add_f32 v[224:225], v[224:225], v[16:17]\n"
    "v_pk_add_f32 v[226:227], v[226:227], v[18:19]\n"
    "v_pk_fma_f32 v[228:229], v[16:17], v[16:17], v[228:229]\n"
    "v_pk_fma_f32 v[230:231], v[18:19], v[18:19], v[230:231]\n"
    "v_pk_add_f32 v[224:225], v[224:225], v[20:21]\n"
    "v_pk_add_f32 v[226:227], v[226:227], v[22:23]\n"
    "buffer_load_dwordx4 v[124:127], v209, s[80:83], s59 offen offset:256\n"
    "v_pk_fma_f32 v[228:229], v[20:21], v[20:21], v[228:229]\n"
    "v_pk_fma_f32 v[230:231], v[22:23], v[22:23], v[230:231]\n"
    "s_waitcnt lgkmcnt(5)\n"
    "v_smfmac_f32_16x16x64_bf16 v[24:27], a[248:251], a[240:247], v240\n"
    "s_nop 0\n"
    "ds_read_b128 a[248:251], v252 offset:11424\n"
    "s_waitcnt lgkmcnt(5)\n"
    "v_smfmac_f32_16x16x64_bf16 v[28:31], a[252:255], a[240:247], v240 abid:1\n"
    "ds_read_b128 a[240:243], v250 offset:27744\n"
    "ds_read_b128 a[244:247], v250 offset:28064\n"
    "ds_read_b128 a[252:255], v252 offset:11760\n"
    "buffer_load_dwordx4 v[128:131], v209, s[80:83], s59 offen offset:512\n"
    "s_nop 0\n"
    "v_pk_add_f32 v[224:225], v[224:225], v[24:25]\n"
    "v_pk_add_f32 v[226:227], v[226:227], v[26:27]\n"
    "v_pk_fma_f32 v[228:229], v[24:25], v[24:25], v[228:229]\n"
    "v_pk_fma_f32 v[230:231], v[26:27], v[26:27], v[230:231]\n"
    "v_pk_add_f32 v[224:225], v[224:225], v[28:29]\n"
    "v_pk_add_f32 v[226:227], v[226:227], v[30:31]\n"
    "v_pk_fma_f32 v[228:229], v[28:29], v[28:29], v[228:229]\n"
    "v_pk_fma_f32 v[230:231], v[30:31], v[30:31], v[230:231]\n"
    "s_waitcnt lgkmcnt(4)\n"
    "v_smfmac_f32_16x16x64_bf16 v[32:35], a[232:235], a[224:231], v240\n"
    "s_nop 0\n"
    "ds_read_b128 a[232:235], v252 offset:12096\n"
    "v_smfmac_f32_16x16x64_bf16 v[36:39], a[236:239], a[224:231], v240 abid:1\n"
    "ds_read_b128 a[224:227], v250 offset:27824\n"
    "ds_read_b128 a[228:231], v250 offset:28144\n"
    "ds_read_b128 a[236:239], v252 offset:12432\n"
    "s_mov_b32 s59, 3072\n"
    "buffer_load_dwordx4 v[132:135], v209, s[80:83], s59 offen offset:0\n"
    "s_waitcnt vmcnt(12)\n"
    "s_barrier\n"
    "v_pk_add_f32 v[224:225], v[224:225], v[32:33]\n"
    "v_pk_add_f32 v[226:227], v[226:227], v[34:35]\n"
    "v_pk_fma_f32 v[228:229], v[32:33], v[32:33], v[228:229]\n"
    "v_pk_fma_f32 v[230:231], v[34:35], v[34:35], v[230:231]\n"
    "v_pk_add_f32 v[224:225], v[224:225], v[36:37]\n"
    "v_pk_add_f32 v[226:227], v[226:227], v[38:39]\n"
    "v_pk_fma_f32 v[228:229], v[36:37], v[36:37], v[228:229]\n"
    "v_pk_fma_f32 v[230:231], v[38:39], v[38:39], v[230:231]\n"
    "s_waitcnt lgkmcnt(4)\n"
    "v_smfmac_f32_16x16x64_bf16 v[40:43], a[248:251], a[240:247], v240\n"
    "s_nop 0\n"
    "buffer_load_dwordx4 v[136:139], v209, s[80:83], s59 offen offset:256\n"
    "v_smfmac_f32_16x16x64_bf16 v[44:47], a[252:255], a[240:247], v240 abid:1\n"
    "ds_read_b128 a[240:243], v250 offset:27904\n"
    "ds_read_b128 a[244:247], v250 offset:28224\n"
    "ds_read_b128 a[248:251], v252 offset:12768\n"
    "ds_read_b128 a[252:255], v252 offset:13104\n"
    "ds_read_b64_tr_b16 a[48:49], v220 offset:0\n"
    "s_waitcnt lgkmcnt(6)\n"
    "v_smfmac_f32_16x16x64_bf16 v[48:51], a[232:235], a[224:231], v240\n"
    "s_waitcnt lgkmcnt(5)\n"
    "ds_read_b128 a[232:235], v252 offset:21504\n"
    "v_smfmac_f32_16x16x64_bf16 v[52:55], a[236:239], a[224:231], v240 abid:1\n"
    "ds_read_b128 a[224:227], v250 offset:54688\n"
    "ds_read_b128 a[228:231], v250 offset:55008\n"
    "buffer_load_dwordx4 v[140:143], v209, s[80:83], s59 offen offset:512\n"
    "ds_read_b128 a[236:239], v252 offset:21840\n"
    "v_pk_add_f32 v[224:225], v[224:225], v[40:41]\n"
    "v_pk_add_f32 v[226:227], v[226:227], v[42:43]\n"
    "v_pk_fma_f32 v[228:229], v[40:41], v[40:41], v[228:229]\n"
    "v_pk_fma_f32 v[230:231], v[42:43], v[42:43], v[230:231]\n"
    "v_pk_add_f32 v[224:225], v[224:225], v[44:45]\n"
    "v_pk_add_f32 v[226:227], v[226:227], v[46:47]\n"
    "v_pk_fma_f32 v[228:229], v[44:45], v[44:45], v[228:229]\n"
    "v_pk_fma_f32 v[230:231], v[46:47], v[46:47], v[230:231]\n"
    "v_pk_add_f32 v[224:225], v[224:225], v[48:49]\n"
    "v_pk_add_f32 v[226:227], v[226:227], v[50:51]\n"
    "v_pk_fma_f32 v[228:229], v[48:49], v[48:49], v[228:229]\n"
    "v_pk_fma_f32 v[230:231], v[50:51], v[50:51], v[230:231]\n"
    "v_pk_add_f32 v[224:225], v[224:225], v[52:53]\n"
    "v_pk_add_f32 v[226:227], v[226:227], v[54:55]\n"
    "v_pk_fma_f32 v[228:229], v[52:53], v[52:53], v[228:229]\n"
    "v_pk_fma_f32 v[230:231], v[54:55], v[54:55], v[230:231]\n"
    "ds_read_b64_tr_b16 a[50:51], v220 offset:3072\n"
    "s_mov_b32 s59, 24576\n"
    "buffer_load_dwordx4 v[144:147], v209, s[80:83], s59 offen offset:0\n"
    "s_waitcnt lgkmcnt(7)\n"
    "v_smfmac_f32_16x16x64_bf16 v[56:59], a[248:251], a[240:247], v240\n"
    "s_waitcnt lgkmcnt(6)\n"
    "ds_read_b128 a[248:251], v252 offset:22176\n"
    "v_smfmac_f32_16x16x64_bf16 v[60:63], a[252:255], a[240:247], v240 abid:1\n"
    "s_nop 0\n"
    "ds_read_b128 a[240:243], v250 offset:54768\n"
    "ds_read_b128 a[244:247], v250 offset:55088\n"
    "ds_read_b128 a[252:255], v252 offset:22512\n"
    "s_waitcnt vmcnt(12)\n"
    "v_or3_b32 v200, v200, v96, v97\n"
    "v_pk_add_f32 v[224:225], v[224:225], v[56:57]\n"
    "v_pk_add_f32 v[226:227], v[226:227], v[58:59]\n"
    "v_pk_fma_f32 v[228:229], v[56:57], v[56:57], v[228:229]\n"
    "v_pk_fma_f32 v[230:231], v[58:59], v[58:59], v[230:231]\n"
    "v_pk_add_f32 v[224:225], v[224:225], v[60:61]\n"
    "v_pk_add_f32 v[226:227], v[226:227], v[62:63]\n"
    "buffer_load_dwordx4 v[148:151], v209, s[80:83], s59 offen offset:256\n"
    "v_pk_fma_f32 v[228:229], v[60:61], v[60:61], v[228:229]\n"
    "v_pk_fma_f32 v[230:231], v[62:63], v[62:63], v[230:231]\n"
    "s_waitcnt lgkmcnt(5)\n"
    "v_smfmac_f32_16x16x64_bf16 v[64:67], a[232:235], a[224:231], v240\n"
    "s_nop 0\n"
    "ds_read_b128 a[232:235], v252 offset:22848\n"
    "v_smfmac_f32_16x16x64_bf16 v[68:71], a[236:239], a[224:231], v240 abid:1\n"
    "ds_read_b128 a[224:227], v250 offset:54848\n"
    "ds_read_b128 a[228:231], v250 offset:55168\n"
    "ds_read_b128 a[236:239], v252 offset:23184\n"
    "v_or3_b32 v200, v200, v98, v99\n"
    "ds_read_b64_tr_b16 a[52:53], v220 offset:8\n"
    "ds_read_b64_tr_b16 a[54:55], v220 offset:3080\n"
    "v_pk_add_f32 v[224:225], v[224:225], v[64:65]\n"
    "buffer_load_dwordx4 v[152:155], v209, s[80:83], s59 offen offset:512\n"
    "v_pk_add_f32 v[226:227], v[226:227], v[66:67]\n"
    "v_pk_fma_f32 v[228:229], v[64:65], v[64:65], v[228:229]\n"
    "v_pk_fma_f32 v[230:231], v[66:67], v[66:67], v[230:231]\n"
    "v_pk_add_f32 v[224:225], v[224:225], v[68:69]\n"
    "v_pk_add_f32 v[226:227], v[226:227], v[70:71]\n"
    "v_pk_fma_f32 v[228:229], v[68:69], v[68:69], v[228:229]\n"
    "v_pk_fma_f32 v[230:231], v[70:71], v[70:71], v[230:231]\n"
    "s_waitcnt lgkmcnt(6)\n"
    "v_smfmac_f32_16x16x64_bf16 v[72:75], a[248:251], a[240:247], v240\n"
    "s_nop 0\n"
    "ds_read_b128 a[248:251], v252 offset:23520\n"
    "v_smfmac_f32_16x16x64_bf16 v[76:79], a[252:255], a[240:247], v240 abid:1\n"
    "ds_read_b128 a[240:243], v250 offset:54928\n"
    "ds_read_b128 a[244:247], v250 offset:55248\n"
    "ds_read_b128 a[252:255], v252 offset:23856\n"
    "s_mov_b32 s59, 27648\n"
    "buffer_load_dwordx4 v[156:159], v209, s[80:83], s59 offen offset:0\n"
    "ds_read_b64_tr_b16 a[56:57], v220 offset:64\n"
    "v_pk_add_f32 v[224:225], v[224:225], v[72:73]\n"
    "v_pk_add_f32 v[226:227], v[226:227], v[74:75]\n"
    "v_pk_fma_f32 v[228:229], v[72:73], v[72:73], v[228:229]\n"
    "v_pk_fma_f32 v[230:231], v[74:75], v[74:75], v[230:231]\n"
    "v_pk_add_f32 v[224:225], v[224:225], v[76:77]\n"
    "v_pk_add_f32 v[226:227], v[226:227], v[78:79]\n"
    "v_pk_fma_f32 v[228:229], v[76:77], v[76:77], v[228:229]\n"
    "v_pk_fma_f32 v[230:231], v[78:79], v[78:79], v[230:231]\n"
    "s_waitcnt lgkmcnt(7)\n"
    "v_smfmac_f32_16x16x64_bf16 v[80:83], a[232:235], a[224:231], v240\n"
    "s_nop 0\n"
    "ds_read_b64_tr_b16 a[58:59], v220 offset:3136\n"
    "v_smfmac_f32_16x16x64_bf16 v[84:87], a[236:239], a[224:231], v240 abid:1\n"
    "ds_read_b64_tr_b16 a[60:61], v220 offset:72\n"
    "ds_read_b64_tr_b16 a[62:63], v220 offset:3144\n"
    "buffer_load_dwordx4 v[160:163], v209, s[80:83], s59 offen offset:256\n"
    "ds_read_b64_tr_b16 v[188:189], v218 offset:128\n"
    "ds_read_b64_tr_b16 v[190:191], v218 offset:3200\n"
    "v_and_b32_e32 v232, 0x7fff7fff, v96\n"
    "v_pk_add_f32 v[224:225], v[224:225], v[80:81]\n"
    "v_pk_add_f32 v[226:227], v[226:227], v[82:83]\n"
    "v_pk_fma_f32 v[228:229], v[80:81], v[80:81], v[228:229]\n"
    "v_pk_fma_f32 v[230:231], v[82:83], v[82:83], v[230:231]\n"
    "v_pk_add_f32 v[224:225], v[224:225], v[84:85]\n"
    "v_pk_add_f32 v[226:227], v[226:227], v[86:87]\n"
    "v_pk_fma_f32 v[228:229], v[84:85], v[84:85], v[228:229]\n"
    "v_pk_fma_f32 v[230:231], v[86:87], v[86:87], v[230:231]\n"
    "s_waitcnt lgkmcnt(7)\n"
    "v_smfmac_f32_16x16x64_bf16 v[88:91], a[248:251], a[240:247], v240\n"
    "s_waitcnt lgkmcnt(6)\n"
    "ds_read_b64_tr_b16 a[64:65], v220 offset:6144\n"
    "v_smfmac_f32_16x16x64_bf16 v[92:95], a[252:255], a[240:247], v240 abid:1\n"
    "ds_read_b64_tr_b16 a[66:67], v220 offset:9216\n"
    "buffer_load_dwordx4 v[164:167], v209, s[80:83], s59 offen offset:512\n"
    "ds_read_b64_tr_b16 a[68:69], v220 offset:6152\n"
    "ds_read_b64_tr_b16 a[70:71], v220 offset:9224\n"
    "ds_read_b64_tr_b16 a[72:73], v220 offset:6208\n"
    "v_pk_add_f32 v[224:225], v[224:225], v[88:89]\n"
    "v_pk_add_f32 v[226:227], v[226:227], v[90:91]\n"
    "v_pk_fma_f32 v[228:229], v[88:89], v[88:89], v[228:229]\n"
    "v_pk_fma_f32 v[230:231], v[90:91], v[90:91], v[230:231]\n"
    "v_pk_add_f32 v[224:225], v[224:225], v[92:93]\n"
    "v_pk_add_f32 v[226:227], v[226:227], v[94:95]\n"
    "v_pk_fma_f32 v[228:229], v[92:93], v[92:93], v[228:229]\n"
    "v_pk_fma_f32 v[230:231], v[94:95], v[94:95], v[230:231]\n"
    "ds_write_b128 v254, v[224:227]\n"
    "v_xor_b32_e32 v209, 32, v219\n"
    "v_lshl_add_u32 v216, s51, 8, v209\n"
    "v_add_u32_e32 v96, v201, v96\n"
    "s_add_u32 vcc_lo, s96, 4096\n"
    "s_addc_u32 vcc_hi, s97, 0\n"
    "global_load_dwordx4 v[244:247], v213, vcc\n"
    "ds_write_b128 v254, v[228:231] offset:1024\n"
    "v_lshrrev_b32_e32 v254, 2, v242\n"
    "v_add_u32_e64 v254, s55, v254\n"
    "v_add_u32_e32 v98, v201, v98\n"
    "s_waitcnt vmcnt(17)\n"
    "v_or3_b32 v200, v200, v100, v101\n"
    "v_or3_b32 v200, v200, v102, v103\n"
    "v_add_u32_e64 v100, v203, v100\n"
    "v_add_u32_e64 v102, v203, v102\n"
    "ds_read_b64_tr_b16 a[74:75], v220 offset:9280\n"
    "ds_read_b64_tr_b16 a[76:77], v220 offset:6216\n"
    "ds_read_b64_tr_b16 a[78:79], v220 offset:9288\n"
    "s_add_u32 vcc_lo, s96, 24576\n"
    "s_nop 0\n"
    "s_addc_u32 vcc_hi, s97, 0\n"
    "global_load_dwordx4 v[236:239], v213, vcc\n"
    "ds_read_b64_tr_b16 v[192:193], v218 offset:6272\n"
    "ds_read_b64_tr_b16 v[194:195], v218 offset:9344\n"
    "ds_read_b64_tr_b16 a[80:81], v220 offset:12288\n"
    "v_xor_b32_e32 v213, 32, v223\n"
    "ds_read_b64_tr_b16 a[82:83], v220 offset:15360\n"
    "ds_read_b64_tr_b16 a[84:85], v220 offset:12296\n"
    "ds_read_b64_tr_b16 a[86:87], v220 offset:15368\n"
    "s_waitcnt vmcnt(17)\n"
    "v_or3_b32 v200, v200, v104, v105\n"
    "ds_read_b64_tr_b16 a[88:89], v220 offset:12352\n"
    "s_waitcnt lgkmcnt(10)\n"
    "s_barrier\n"
    "ds_read_b32 v224, v254 offset:0\n"
    "ds_read_b32 v225, v254 offset:6752\n"
    "ds_read_b32 v226, v254 offset:13504\n"
    "v_or3_b32 v200, v200, v106, v107\n"
    "ds_read_b32 v227, v254 offset:20256\n"
    "ds_read_b32 v228, v254 offset:1024\n"
    "ds_read_b32 v229, v254 offset:7776\n"
    "v_add_u32_e64 v104, v205, v104\n"
    "ds_read_b32 v230, v254 offset:14528\n"
    "ds_read_b32 v231, v254 offset:21280\n"
    "ds_read_b64_tr_b16 a[90:91], v220 offset:15424\n"
    "v_add_u32_e32 v106, v205, v106\n"
    "ds_read_b64_tr_b16 a[92:93], v220 offset:12360\n"
    "ds_read_b64_tr_b16 a[94:95], v220 offset:15432\n"
    "ds_read_b64_tr_b16 v[196:197], v218 offset:12416\n"
    "s_waitcnt vmcnt(16)\n"
    "v_or3_b32 v200, v200, v108, v109\n"
    "ds_read_b64_tr_b16 v[198:199], v218 offset:15488\n"
    "v_or3_b32 v200, v200, v110, v111\n"
    "v_add_u32_e64 v108, v201, v108\n"
    "v_add_u32_e32 v110, v201, v110\n"
    "s_waitcnt vmcnt(15)\n"
    "v_or3_b32 v200, v200, v112, v113\n"
    "v_or3_b32 v200, v200, v114, v115\n"
    "s_waitcnt lgkmcnt(11)\n"
    "v_add_f32_e32 v224, v224, v225\n"
    "v_add_u32_e64 v112, v203, v112\n"
    "v_add_u32_e32 v114, v203, v114\n"
    "s_waitcnt lgkmcnt(9)\n"
    "v_add_f32_e64 v226, v226, v227\n"
    "v_add_f32_e64 v224, v224, v226\n"
    "v_mul_f32_e32 v224, 0x3c2aaaab, v224\n"
    "s_waitcnt lgkmcnt(7)\n"
    "v_add_f32_e32 v228, v228, v229\n"
    "s_waitcnt vmcnt(14)\n"
    "v_or3_b32 v200, v200, v116, v117\n"
    "s_waitcnt lgkmcnt(5)\n"
    "v_add_f32_e64 v230, v230, v231\n"
    "v_add_f32_e64 v228, v228, v230\n"
    "v_mul_f32_e32 v228, 0x3c2aaaab, v228\n"
    "v_fma_f32 v228, -v224, v224, v228\n"
    "v_max_f32_e64 v228, 0, v228\n"
    "v_add_f32_e32 v228, 0x3727c5ac, v228\n"
    "v_rsq_f32_e64 v228, v228\n"
    "v_or3_b32 v200, v200, v118, v119\n"
    "v_mul_f32_e64 v228, s50, v228\n"
    "v_mul_f32_e64 v224, v228, -v224\n"
    "ds_write_b32 v254, v228\n"
    "ds_write_b32 v254, v224 offset:1024\n"
    "v_add_u32_e64 v254, s56, v242\n"
    "s_waitcnt lgkmcnt(2)\n"
    "s_barrier\n"
    "ds_write_b128 v217, v[96:99] offset:24576\n"
    "v_add_u32_e64 v116, v205, v116\n"
    "v_add_u32_e32 v118, v205, v118\n"
    "s_waitcnt vmcnt(13)\n"
    "v_or3_b32 v200, v200, v120, v121\n"
    "v_or3_b32 v200, v200, v122, v123\n"
    "v_add_u32_e64 v120, v201, v120\n"
    "v_add_u32_e32 v122, v201, v122\n"
    "s_waitcnt vmcnt(12)\n"
    "v_or3_b32 v200, v200, v124, v125\n"
    "ds_write_b128 v217, v[100:103] offset:24832\n"
    "s_waitcnt lgkmcnt(2)\n"
    "s_barrier\n"
    "ds_read_b128 v[224:227], v254\n"
    "ds_read_b128 v[228:231], v254 offset:1024\n"
    "ds_write_b128 v217, v[104:107] offset:25088\n"
    "v_or3_b32 v200, v200, v126, v127\n"
    "v_add_u32_e64 v124, v203, v124\n"
    "v_add_u32_e32 v126, v203, v126\n"
    "s_waitcnt vmcnt(11)\n"
    "v_or3_b32 v200, v200, v128, v129\n"
    "v_or3_b32 v200, v200, v130, v131\n"
    "v_add_u32_e64 v128, v205, v128\n"
    "v_add_u32_e32 v130, v205, v130\n"
    "s_waitcnt vmcnt(10)\n"
    "v_or3_b32 v200, v200, v132, v133\n"
    "ds_write_b128 v217, v[108:111] offset:0\n"
    "v_or3_b32 v200, v200, v134, v135\n"
    "v_add_u32_e64 v132, v201, v132\n"
    "v_add_u32_e64 v134, v201, v134\n"
    "s_waitcnt vmcnt(9)\n"
    "v_or3_b32 v200, v200, v136, v137\n"
    "v_or3_b32 v200, v200, v138, v139\n"
    "ds_write_b128 v217, v[112:115] offset:256\n"
    "s_waitcnt lgkmcnt(3)\n"
    "v_pk_fma_f32 v[64:65], v[64:65], v[224:225], v[228:229]\n"
    "v_pk_fma_f32 v[66:67], v[66:67], v[226:227], v[230:231]\n"
    "v_pk_fma_f32 v[68:69], v[68:69], v[224:225], v[228:229]\n"
    "v_pk_fma_f32 v[70:71], v[70:71], v[226:227], v[230:231]\n"
    "v_pk_fma_f32 v[72:73], v[72:73], v[224:225], v[228:229]\n"
    "v_pk_fma_f32 v[74:75], v[74:75], v[226:227], v[230:231]\n"
    "v_pk_fma_f32 v[76:77], v[76:77], v[224:225], v[228:229]\n"
    "v_pk_fma_f32 v[78:79], v[78:79], v[226:227], v[230:231]\n"
    "v_pk_fma_f32 v[80:81], v[80:81], v[224:225], v[228:229]\n"
    "v_pk_fma_f32 v[82:83], v[82:83], v[226:227], v[230:231]\n"
    "v_pk_fma_f32 v[84:85], v[84:85], v[224:225], v[228:229]\n"
    "v_pk_fma_f32 v[86:87], v[86:87], v[226:227], v[230:231]\n"
    "v_pk_fma_f32 v[88:89], v[88:89], v[224:225], v[228:229]\n"
    "v_pk_fma_f32 v[90:91], v[90:91], v[226:227], v[230:231]\n"
    "v_pk_fma_f32 v[92:93], v[92:93], v[224:225], v[228:229]\n"
    "v_pk_fma_f32 v[94:95], v[94:95], v[226:227], v[230:231]\n"
    "v_cvt_pk_bf16_f32 v96, v64, v68\n"
    "v_cvt_pk_bf16_f32 v100, v65, v69\n"
    "v_cvt_pk_bf16_f32 v104, v66, v70\n"
    "v_cvt_pk_bf16_f32 v108, v67, v71\n"
    "v_cvt_pk_bf16_f32 v97, v72, v76\n"
    "v_cvt_pk_bf16_f32 v101, v73, v77\n"
    "v_cvt_pk_bf16_f32 v105, v74, v78\n"
    "v_cvt_pk_bf16_f32 v109, v75, v79\n"
    "v_cvt_pk_bf16_f32 v98, v80, v84\n"
    "v_cvt_pk_bf16_f32 v102, v81, v85\n"
    "v_cvt_pk_bf16_f32 v106, v82, v86\n"
    "v_cvt_pk_bf16_f32 v110, v83, v87\n"
    "v_cvt_pk_bf16_f32 v99, v88, v92\n"
    "ds_write_b128 v253, v[96:99] offset:2048\n"
    "v_cvt_pk_bf16_f32 v103, v89, v93\n"
    "v_cvt_pk_bf16_f32 v107, v90, v94\n"
    "v_cvt_pk_bf16_f32 v111, v91, v95\n"
    "v_pk_fma_f32 v[0:1], v[0:1], v[224:225], v[228:229]\n"
    "v_pk_fma_f32 v[2:3], v[2:3], v[226:227], v[230:231]\n"
    "v_pk_fma_f32 v[4:5], v[4:5], v[224:225], v[228:229]\n"
    "ds_write_b128 v253, v[100:103] offset:5120\n"
    "v_pk_fma_f32 v[6:7], v[6:7], v[226:227], v[230:231]\n"
    "v_pk_fma_f32 v[8:9], v[8:9], v[224:225], v[228:229]\n"
    "v_pk_fma_f32 v[10:11], v[10:11], v[226:227], v[230:231]\n"
    "v_pk_fma_f32 v[12:13], v[12:13], v[224:225], v[228:229]\n"
    "v_pk_fma_f32 v[14:15], v[14:15], v[226:227], v[230:231]\n"
    "v_pk_fma_f32 v[16:17], v[16:17], v[224:225], v[228:229]\n"
    "v_pk_fma_f32 v[18:19], v[18:19], v[226:227], v[230:231]\n"
    "v_pk_fma_f32 v[20:21], v[20:21], v[224:225], v[228:229]\n"
    "v_pk_fma_f32 v[22:23], v[22:23], v[226:227], v[230:231]\n"
    "v_pk_fma_f32 v[24:25], v[24:25], v[224:225], v[228:229]\n"
    "v_pk_fma_f32 v[26:27], v[26:27], v[226:227], v[230:231]\n"
    "v_pk_fma_f32 v[28:29], v[28:29], v[224:225], v[228:229]\n"
    "v_pk_fma_f32 v[30:31], v[30:31], v[226:227], v[230:231]\n"
    "v_cvt_pk_bf16_f32 v80, v0, v4\n"
    "v_cvt_pk_bf16_f32 v84, v1, v5\n"
    "v_cvt_pk_bf16_f32 v88, v2, v6\n"
    "v_cvt_pk_bf16_f32 v92, v3, v7\n"
    "v_cvt_pk_bf16_f32 v81, v8, v12\n"
    "v_cvt_pk_bf16_f32 v85, v9, v13\n"
    "v_cvt_pk_bf16_f32 v89, v10, v14\n"
    "v_cvt_pk_bf16_f32 v93, v11, v15\n"
    "v_cvt_pk_bf16_f32 v82, v16, v20\n"
    "s_waitcnt vmcnt(1)\n"
    "v_lshlrev_b32_e32 v235, 3, v244\n"
    "s_mov_b32 exec_hi, 0\n"
    "buffer_load_dword v233, v235, s[40:43], s53 offen sc1\n"
    "s_mov_b32 exec_hi, -1\n"
    "s_add_u32 s53, s53, 393216\n"
    "v_lshlrev_b32_e64 v235, 3, v244\n"
    "s_mov_b32 exec_hi, 0\n"
    "buffer_load_dword v233, v235, s[40:43], s53 offen sc1\n"
    "s_mov_b32 exec_hi, -1\n"
    "s_add_u32 s53, s53, 393216\n"
    "v_lshlrev_b32_e64 v235, 3, v244\n"
    "s_mov_b32 exec_hi, 0\n"
    "buffer_load_dword v233, v235, s[40:43], s53 offen sc1\n"
    "s_mov_b32 exec_hi, -1\n"
    "s_add_u32 s53, s53, 393216\n"
    "v_lshlrev_b32_e32 v235, 3, v244\n"
    "s_mov_b32 exec_hi, 0\n"
    "buffer_load_dword v233, v235, s[40:43], s53 offen sc1\n"
    "s_mov_b32 exec_hi, -1\n"
    "s_add_u32 s53, s53, 393216\n"
    "s_waitcnt vmcnt(4)\n"
    "v_accvgpr_write_b32 a248, v236\n"
    "v_accvgpr_write_b32 a249, v237\n"
    "v_accvgpr_write_b32 a250, v238\n"
    "v_accvgpr_write_b32 a251, v239\n"
    "v_cvt_pk_bf16_f32 v86, v17, v21\n"
    "v_cvt_pk_bf16_f32 v90, v18, v22\n"
    "v_cvt_pk_bf16_f32 v94, v19, v23\n"
    "v_cvt_pk_bf16_f32 v83, v24, v28\n"
    "ds_write_b128 v253, v[80:83] offset:0\n"
    "v_cvt_pk_bf16_f32 v87, v25, v29\n"
    "v_cvt_pk_bf16_f32 v91, v26, v30\n"
    "v_cvt_pk_bf16_f32 v95, v27, v31\n"
    "v_pk_fma_f32 v[32:33], v[32:33], v[224:225], v[228:229]\n"
    "v_pk_fma_f32 v[34:35], v[34:35], v[226:227], v[230:231]\n"
    "v_pk_fma_f32 v[36:37], v[36:37], v[224:225], v[228:229]\n"
    "ds_write_b128 v253, v[84:87] offset:3072\n"
    "v_pk_fma_f32 v[38:39], v[38:39], v[226:227], v[230:231]\n"
    "v_pk_fma_f32 v[40:41], v[40:41], v[224:225], v[228:229]\n"
    "v_pk_fma_f32 v[42:43], v[42:43], v[226:227], v[230:231]\n"
    "v_pk_fma_f32 v[44:45], v[44:45], v[224:225], v[228:229]\n"
    "v_pk_fma_f32 v[46:47], v[46:47], v[226:227], v[230:231]\n"
    "v_pk_fma_f32 v[48:49], v[48:49], v[224:225], v[228:229]\n"
    "v_pk_fma_f32 v[50:51], v[50:51], v[226:227], v[230:231]\n"
    "v_pk_fma_f32 v[52:53], v[52:53], v[224:225], v[228:229]\n"
    "v_pk_fma_f32 v[54:55], v[54:55], v[226:227], v[230:231]\n"
    "v_pk_fma_f32 v[56:57], v[56:57], v[224:225], v[228:229]\n"
    "v_pk_fma_f32 v[58:59], v[58:59], v[226:227], v[230:231]\n"
    "v_pk_fma_f32 v[60:61], v[60:61], v[224:225], v[228:229]\n"
    "v_pk_fma_f32 v[62:63], v[62:63], v[226:227], v[230:231]\n"
    "v_cvt_pk_bf16_f32 v64, v32, v36\n"
    "v_cvt_pk_bf16_f32 v68, v33, v37\n"
    "v_cvt_pk_bf16_f32 v72, v34, v38\n"
    "v_cvt_pk_bf16_f32 v76, v35, v39\n"
    "v_cvt_pk_bf16_f32 v65, v40, v44\n"
    "v_cvt_pk_bf16_f32 v69, v41, v45\n"
    "v_cvt_pk_bf16_f32 v73, v42, v46\n"
    "v_cvt_pk_bf16_f32 v77, v43, v47\n"
    "v_cvt_pk_bf16_f32 v66, v48, v52\n"
    "v_cvt_pk_bf16_f32 v70, v49, v53\n"
    "v_cvt_pk_bf16_f32 v74, v50, v54\n"
    "v_cvt_pk_bf16_f32 v78, v51, v55\n"
    "v_cvt_pk_bf16_f32 v67, v56, v60\n"
    "ds_write_b128 v253, v[64:67] offset:1024\n"
    "v_cvt_pk_bf16_f32 v71, v57, v61\n"
    "v_cvt_pk_bf16_f32 v75, v58, v62\n"
    "v_cvt_pk_bf16_f32 v79, v59, v63\n"
    "v_mov_b32_e64 v64, v104\n"
    "v_mov_b32_e64 v65, v105\n"
    "v_mov_b32_e64 v66, v106\n"
    "ds_write_b128 v253, v[68:71] offset:4096\n"
    "v_mov_b32_e64 v67, v107\n"
    "v_mov_b32_e64 v68, v108\n"
    "v_mov_b32_e64 v69, v109\n"
    "v_mov_b32_e64 v70, v110\n"
    "v_mov_b32_e64 v71, v111\n"
    "s_waitcnt lgkmcnt(2)\n"
    "s_barrier\n"
    "ds_read_b64_tr_b16 a[96:97], v208 offset:24576\n"
    "ds_read_b64_tr_b16 v[180:181], v216 offset:24576\n"
    "ds_read_b64_tr_b16 a[100:101], v210 offset:24576\n"
    "v_add_u32_e64 v136, v203, v136\n"
    "ds_read_b64_tr_b16 v[184:185], v221 offset:24576\n"
    "ds_read_b64_tr_b16 a[104:105], v212 offset:24576\n"
    "ds_read_b64_tr_b16 a[108:109], v213 offset:24576\n"
    "v_add_u32_e64 v138, v203, v138\n"
    "ds_read_b64_tr_b16 a[112:113], v214 offset:24576\n"
    "ds_read_b64_tr_b16 a[116:117], v215 offset:24576\n"
    "ds_read_b64_tr_b16 a[120:121], v208 offset:24832\n"
    "s_mov_b32 s64, 0\n"
    "s_mov_b32 s59, 0\n"
    "ds_read_b64_tr_b16 a[124:125], v210 offset:24832\n"
    "ds_write_b128 v217, v[116:119] offset:512\n"
    "v_or3_b32 v200, v200, v140, v141\n"
    "v_or3_b32 v200, v200, v142, v143\n"
    "v_add_u32_e64 v140, v205, v140\n"
    "v_add_u32_e64 v142, v205, v142\n"
    "v_or3_b32 v200, v200, v144, v145\n"
    "v_or3_b32 v200, v200, v146, v147\n"
    "v_add_u32_e64 v144, v201, v144\n"
    "v_add_u32_e64 v146, v201, v146\n"
    "v_or3_b32 v200, v200, v148, v149\n"
    "v_or3_b32 v200, v200, v150, v151\n"
    "ds_write_b128 v217, v[120:123] offset:12288\n"
    "v_add_u32_e64 v148, v203, v148\n"
    "v_add_u32_e64 v150, v203, v150\n"
    "v_or3_b32 v200, v200, v152, v153\n"
    "v_or3_b32 v200, v200, v154, v155\n"
    "v_add_u32_e64 v152, v205, v152\n"
    "v_add_u32_e64 v154, v205, v154\n"
    "ds_write_b128 v217, v[124:127] offset:12544\n"
    "s_waitcnt lgkmcnt(2)\n"
    "s_barrier\n"
    "ds_read_b64_tr_b16 a[98:99], v208 offset:0\n"
    "ds_read_b64_tr_b16 v[182:183], v216 offset:0\n"
    "ds_read_b64_tr_b16 a[102:103], v210 offset:0\n"
    "v_or3_b32 v200, v200, v156, v157\n"
    "ds_read_b64_tr_b16 v[186:187], v221 offset:0\n"
    "ds_read_b64_tr_b16 a[106:107], v212 offset:0\n"
    "ds_read_b64_tr_b16 a[110:111], v213 offset:0\n"
    "v_or3_b32 v200, v200, v158, v159\n"
    "ds_read_b64_tr_b16 a[114:115], v214 offset:0\n"
    "ds_read_b64_tr_b16 a[118:119], v215 offset:0\n"
    "ds_read_b64_tr_b16 a[122:123], v208 offset:256\n"
    "v_add_u32_e64 v156, v201, v156\n"
    "ds_read_b64_tr_b16 a[126:127], v210 offset:256\n"
    "ds_write_b128 v217, v[128:131] offset:12800\n"
    "v_add_u32_e64 v158, v201, v158\n"
    "v_or3_b32 v200, v200, v160, v161\n"
    "v_or3_b32 v200, v200, v162, v163\n"
    "v_add_u32_e64 v160, v203, v160\n"
    "v_add_u32_e64 v162, v203, v162\n"
    "v_or3_b32 v200, v200, v164, v165\n"
    "v_or3_b32 v200, v200, v166, v167\n"
    "v_add_u32_e64 v164, v205, v164\n"
    "v_add_u32_e64 v166, v205, v166\n"
    "ds_write_b128 v217, v[132:135] offset:24576\n"
    "v_and_b32_e32 v200, 0x40004000, v200\n"
    "v_and_b32_e64 v246, s67, v246\n"
    "v_and_b32_e64 v247, s67, v247\n"
    "v_add_u32_e64 v246, s98, v246\n"
    "v_add_u32_e64 v247, s98, v247\n"
    "v_add_u32_e64 v245, s99, v245\n"
    "ds_write_b128 v217, v[136:139] offset:24832\n"
    "s_waitcnt lgkmcnt(2)\n"
    "s_barrier\n"
    "ds_read_b64_tr_b16 a[128:129], v208 offset:12288\n"
    "ds_read_b64_tr_b16 a[132:133], v209 offset:12288\n"
    "ds_read_b64_tr_b16 a[136:137], v210 offset:12288\n"
    "v_mov_b32_e64 v235, v222\n"
    "ds_read_b64_tr_b16 a[140:141], v211 offset:12288\n"
    "ds_read_b64_tr_b16 a[144:145], v212 offset:12288\n"
    "ds_read_b64_tr_b16 a[148:149], v213 offset:12288\n"
    "ds_read_b64_tr_b16 a[152:153], v214 offset:12288\n"
    "ds_read_b64_tr_b16 a[156:157], v215 offset:12288\n"
    "ds_read_b64_tr_b16 a[160:161], v208 offset:12544\n"
    "ds_read_b64_tr_b16 a[164:165], v209 offset:12544\n"
    "ds_read_b64_tr_b16 a[168:169], v210 offset:12544\n"
    "ds_read_b64_tr_b16 a[172:173], v211 offset:12544\n"
    "ds_write_b128 v217, v[140:143] offset:25088\n"
    "ds_write_b128 v217, v[144:147] offset:0\n"
    "ds_write_b128 v217, v[148:151] offset:256\n"
    "s_waitcnt lgkmcnt(2)\n"
    "s_barrier\n"
    "ds_read_b64_tr_b16 a[130:131], v208 offset:24576\n"
    "ds_read_b64_tr_b16 a[134:135], v209 offset:24576\n"
    "ds_read_b64_tr_b16 a[138:139], v210 offset:24576\n"
    "ds_read_b64_tr_b16 a[142:143], v211 offset:24576\n"
    "ds_read_b64_tr_b16 a[146:147], v212 offset:24576\n"
    "ds_read_b64_tr_b16 a[150:151], v213 offset:24576\n"
    "ds_read_b64_tr_b16 a[154:155], v214 offset:24576\n"
    "ds_read_b64_tr_b16 a[158:159], v215 offset:24576\n"
    "ds_read_b64_tr_b16 a[162:163], v208 offset:24832\n"
    "ds_read_b64_tr_b16 a[166:167], v209 offset:24832\n"
    "ds_read_b64_tr_b16 a[170:171], v210 offset:24832\n"
    "ds_read_b64_tr_b16 a[174:175], v211 offset:24832\n"
    "ds_write_b128 v217, v[152:155] offset:512\n"
    "ds_write_b128 v217, v[156:159] offset:12288\n"
    "ds_write_b128 v207, v[184:187] offset:36864\n"
    "s_waitcnt lgkmcnt(2)\n"
    "s_barrier\n"
    "ds_write_b128 v234, a[216:219] offset:0\n"
    "ds_write_b128 v234, a[220:223] offset:16\n"
    "ds_read_b64_tr_b16 a[176:177], v208 offset:0\n"
    "ds_read_b64_tr_b16 a[180:181], v209 offset:0\n"
    "ds_read_b64_tr_b16 a[184:185], v210 offset:0\n"
    "ds_read_b64_tr_b16 a[188:189], v211 offset:0\n"
    "ds_read_b64_tr_b16 a[192:193], v212 offset:0\n"
    "ds_read_b64_tr_b16 a[196:197], v213 offset:0\n"
    "ds_read_b64_tr_b16 a[200:201], v214 offset:0\n"
    "ds_read_b64_tr_b16 a[204:205], v215 offset:0\n"
    "ds_read_b64_tr_b16 a[208:209], v208 offset:256\n"
    "ds_read_b64_tr_b16 a[212:213], v209 offset:256\n"
    "ds_read_b64_tr_b16 a[216:217], v210 offset:256\n"
    "ds_read_b64_tr_b16 a[220:221], v211 offset:256\n"
    "ds_write_b128 v207, v[180:183] offset:32768\n"
    "ds_write_b128 v217, v[160:163] offset:12544\n"
    "ds_write_b128 v217, v[164:167] offset:12800\n"
    "v_cmp_le_u32_e32 vcc, 1, v200\n"
    "s_nop 1\n"
    "s_cmp_lg_u64 vcc, 0\n"
    "s_cselect_b32 vcc_lo, 1, 0\n"
    "s_or_b32 s66, s66, vcc_lo\n"
    "v_cmp_le_u32_e32 vcc, 981467136, v232\n"
    "s_nop 1\n"
    "s_cmp_lg_u64 vcc, 0\n"
    "s_cselect_b32 vcc_lo, 2, 0\n"
    "s_or_b32 s66, s66, vcc_lo\n"
    "v_mov_b32_e32 v200, s66\n"
    "v_mov_b32_e32 v232, 162992\n"
    "s_mov_b64 exec, 1\n"
    "ds_or_b32 v232, v200\n"
    "s_mov_b64 exec, -1\n"
    "s_waitcnt lgkmcnt(0)\n"
    "s_barrier\n"
    "ds_read_b64_tr_b16 a[178:179], v208 offset:12288\n"
    "ds_read_b64_tr_b16 a[182:183], v209 offset:12288\n"
    "ds_read_b64_tr_b16 a[186:187], v210 offset:12288\n"
    "ds_read_b64_tr_b16 a[190:191], v211 offset:12288\n"
    "ds_read_b64_tr_b16 a[194:195], v212 offset:12288\n"
    "ds_read_b64_tr_b16 a[198:199], v213 offset:12288\n"
    "ds_read_b64_tr_b16 a[202:203], v214 offset:12288\n"
    "ds_read_b64_tr_b16 a[206:207], v215 offset:12288\n"
    "ds_read_b64_tr_b16 a[210:211], v208 offset:12544\n"
    "ds_read_b64_tr_b16 a[214:215], v209 offset:12544\n"
    "ds_read_b64_tr_b16 a[218:219], v210 offset:12544\n"
    "ds_read_b64_tr_b16 a[222:223], v211 offset:12544\n"
    "ds_write_b128 v207, v[168:171] offset:0\n"
    "ds_write_b128 v207, v[172:175] offset:4096\n"
    "ds_write_b128 v207, v[176:179] offset:8192\n"
    "ds_read_b32 v232, v232\n"
    "s_waitcnt lgkmcnt(1)\n"
    "s_barrier\n"
    "ds_read_b128 v[144:147], v242 offset:0\n"
    "ds_read_b128 v[148:151], v242 offset:1024\n"
    "ds_read_b128 v[152:155], v242 offset:2048\n"
    "ds_read_b128 v[156:159], v242 offset:3072\n"
    "ds_read_b128 v[160:163], v242 offset:4096\n"
    "ds_read_b128 v[164:167], v242 offset:5120\n"
    "ds_read_b128 a[240:243], v244 offset:32768\n"
    "ds_write_b128 v207, v[188:191] offset:12288\n"
    "ds_write_b128 v207, v[192:195] offset:16384\n"
    "s_waitcnt lgkmcnt(8)\n"
    "v_mfma_f32_16x16x32_bf16 v[168:171], a[128:131], v[144:147], 0\n"
    "s_waitcnt lgkmcnt(7)\n"
    "v_mfma_f32_16x16x32_bf16 v[168:171], a[176:179], v[148:151], v[168:171]\n"
    "s_waitcnt lgkmcnt(6)\n"
    "v_mfma_f32_16x16x32_bf16 v[168:171], a[96:99], v[152:155], v[168:171]\n"
    "ds_write_b128 v207, v[196:199] offset:20480\n"
    "v_mfma_f32_16x16x32_bf16 v[172:175], a[132:135], v[144:147], 0\n"
    "s_waitcnt lgkmcnt(5)\n"
    "v_mfma_f32_16x16x32_bf16 v[172:175], a[180:183], v[148:151], v[172:175]\n"
    "s_waitcnt lgkmcnt(3)\n"
    "v_mfma_f32_16x16x32_bf16 v[172:175], a[240:243], v[152:155], v[172:175]\n"
    "v_mfma_f32_16x16x32_bf16 v[176:179], a[128:131], v[156:159], 0\n"
    "v_mfma_f32_16x16x32_bf16 v[176:179], a[176:179], v[160:163], v[176:179]\n"
    "v_mfma_f32_16x16x32_bf16 v[176:179], a[96:99], v[164:167], v[176:179]\n"
    "v_mfma_f32_16x16x32_bf16 v[180:183], a[132:135], v[156:159], 0\n"
    "v_mfma_f32_16x16x32_bf16 v[180:183], a[180:183], v[160:163], v[180:183]\n"
    "s_waitcnt lgkmcnt(0)\n"
    "s_barrier\n"
    "v_mfma_f32_16x16x32_bf16 v[180:183], a[240:243], v[164:167], v[180:183]\n"
    "s_getpc_b64 s[76:77]\n"
    ".Lreplk_pc1:\n"
    "s_add_u32 s76, s76, .Lreplk_last-.Lreplk_pc1\n"
    "s_addc_u32 s77, s77, 0\n"
    "s_sub_u32 s76, s76, s36\n"
    "s_subb_u32 s77, s77, 0\n"
    "s_add_u32 s78, s36, .Lreplk_end-.Lreplk_last\n"
    "s_mov_b32 s79, s37\n"
    "v_readfirstlane_b32 s66, v232\n"
    "s_nop 1\n"
    "s_cmp_lg_u32 s66, 2\n"
    "s_cbranch_scc1 98f\n"
    ".p2align 6\n"
    "1:\n"
    "v_lshlrev_b32_sdwa v184, 1, v168 dst_sel:DWORD dst_unused:UNUSED_PAD src0_sel:DWORD src1_sel:WORD_1\n"
    "v_lshlrev_b32_sdwa v200, 1, v169 dst_sel:DWORD dst_unused:UNUSED_PAD src0_sel:DWORD src1_sel:WORD_1\n"
    "v_lshlrev_b32_sdwa v185, 1, v170 dst_sel:DWORD dst_unused:UNUSED_PAD src0_sel:DWORD src1_sel:WORD_1\n"
    "v_lshlrev_b32_sdwa v201, 1, v171 dst_sel:DWORD dst_unused:UNUSED_PAD src0_sel:DWORD src1_sel:WORD_1\n"
    "v_mfma_f32_16x16x32_bf16 v[140:143], a[244:247], v[188:191], v[140:143]\n"
    "ds_read_b128 a[240:243], v244 offset:36864\n"
    "ds_read_u16 v184, v184\n"
    "v_mfma_f32_16x16x32_bf16 v[168:171], a[136:139], v[144:147], 0\n"
    "ds_read_u16_d16_hi v200, v200\n"
    "ds_read_u16 v185, v185\n"
    "v_mfma_f32_16x16x32_bf16 v[168:171], a[184:187], v[148:151], v[168:171]\n"
    "buffer_load_dwordx4 v[0:3], v246, s[40:43], s60 offen offset:0\n"
    "buffer_load_dwordx4 v[32:35], v246, s[40:43], s60 offen offset:64\n"
    "ds_read_u16_d16_hi v201, v201\n"
    "v_mfma_f32_16x16x32_bf16 v[168:171], a[100:103], v[152:155], v[168:171]\n"
    "v_lshlrev_b32_sdwa v186, 1, v172 dst_sel:DWORD dst_unused:UNUSED_PAD src0_sel:DWORD src1_sel:WORD_1\n"
    "v_lshlrev_b32_sdwa v202, 1, v173 dst_sel:DWORD dst_unused:UNUSED_PAD src0_sel:DWORD src1_sel:WORD_1\n"
    "s_waitcnt vmcnt(4)\n"
    "v_smfmac_f32_32x32x32_bf16 v[128:143], a[248:251], a[224:231], v241\n"
    "s_nop 0\n"
    "ds_read_u16 v186, v186\n"
    "v_lshlrev_b32_sdwa v187, 1, v174 dst_sel:DWORD dst_unused:UNUSED_PAD src0_sel:DWORD src1_sel:WORD_1\n"
    "v_lshlrev_b32_sdwa v203, 1, v175 dst_sel:DWORD dst_unused:UNUSED_PAD src0_sel:DWORD src1_sel:WORD_1\n"
    "v_lshlrev_b32_sdwa v188, 1, v176 dst_sel:DWORD dst_unused:UNUSED_PAD src0_sel:DWORD src1_sel:WORD_1\n"
    "v_lshlrev_b32_sdwa v204, 1, v177 dst_sel:DWORD dst_unused:UNUSED_PAD src0_sel:DWORD src1_sel:WORD_1\n"
    "v_mfma_f32_16x16x32_bf16 v[172:175], a[140:143], v[144:147], 0\n"
    "ds_read_b128 v[236:239], v244 offset:20480\n"
    "ds_read_u16_d16_hi v202, v202\n"
    "v_mfma_f32_16x16x32_bf16 v[172:175], a[188:191], v[148:151], v[172:175]\n"
    "v_lshlrev_b32_sdwa v189, 1, v178 dst_sel:DWORD dst_unused:UNUSED_PAD src0_sel:DWORD src1_sel:WORD_1\n"
    "v_lshlrev_b32_sdwa v205, 1, v179 dst_sel:DWORD dst_unused:UNUSED_PAD src0_sel:DWORD src1_sel:WORD_1\n"
    "s_waitcnt lgkmcnt(7)\n"
    "v_mfma_f32_16x16x32_bf16 v[172:175], a[240:243], v[152:155], v[172:175]\n"
    "s_nop 0\n"
    "ds_read_u16 v187, v187\n"
    "v_lshlrev_b32_sdwa v190, 1, v180 dst_sel:DWORD dst_unused:UNUSED_PAD src0_sel:DWORD src1_sel:WORD_1\n"
    "v_mfma_f32_16x16x32_bf16 v[176:179], a[136:139], v[156:159], 0\n"
    "v_lshlrev_b32_sdwa v206, 1, v181 dst_sel:DWORD dst_unused:UNUSED_PAD src0_sel:DWORD src1_sel:WORD_1\n"
    "v_lshlrev_b32_sdwa v191, 1, v182 dst_sel:DWORD dst_unused:UNUSED_PAD src0_sel:DWORD src1_sel:WORD_1\n"
    "v_mfma_f32_16x16x32_bf16 v[176:179], a[184:187], v[160:163], v[176:179]\n"
    "ds_read_b128 a[244:247], v244 offset:22528\n"
    "v_lshlrev_b32_sdwa v207, 1, v183 dst_sel:DWORD dst_unused:UNUSED_PAD src0_sel:DWORD src1_sel:WORD_1\n"
    "v_mfma_f32_16x16x32_bf16 v[176:179], a[100:103], v[164:167], v[176:179]\n"
    "ds_read_u16_d16_hi v203, v203\n"
    "v_pk_fma_f16 v234, v234, s49, s49\n"
    "v_mfma_f32_16x16x32_bf16 v[180:183], a[140:143], v[156:159], 0\n"
    "s_waitcnt lgkmcnt(8)\n"
    "v_rcp_f16_e32 v232, v234\n"
    "v_mfma_f32_16x16x32_bf16 v[180:183], a[188:191], v[160:163], v[180:183]\n"
    "ds_read_u16 v188, v188\n"
    "v_rcp_f16_sdwa v232, v234 dst_sel:WORD_1 dst_unused:UNUSED_PRESERVE src0_sel:WORD_1\n"
    "v_mfma_f32_16x16x32_bf16 v[180:183], a[240:243], v[164:167], v[180:183]\n"
    "ds_read_u16_d16_hi v204, v204\n"
    "v_fma_mix_f32 v234, v196, v232, 0 op_sel_hi:[1,1,0]\n"
    "v_mfma_f32_16x16x32_bf16 v[96:99], a[80:83], v[192:195], v[96:99]\n"
    "v_fma_mix_f32 v232, v196, v232, 0 op_sel:[1,1,0] op_sel_hi:[1,1,0]\n"
    "v_or_b32_e32 v184, v184, v200\n"
    "v_mfma_f32_16x16x32_bf16 v[100:103], a[84:87], v[192:195], v[100:103]\n"
    "s_waitcnt lgkmcnt(8)\n"
    "ds_read_u16 v189, v189\n"
    "v_cvt_pk_bf16_f32 v196, v234, v232\n"
    "v_mfma_f32_16x16x32_bf16 v[112:115], a[88:91], v[192:195], v[112:115]\n"
    "s_waitcnt lgkmcnt(7)\n"
    "v_or_b32_e32 v185, v185, v201\n"
    "v_lshlrev_b32_sdwa v201, 1, v171 dst_sel:DWORD dst_unused:UNUSED_PAD src0_sel:DWORD src1_sel:WORD_1\n"
    "v_mfma_f32_16x16x32_bf16 v[116:119], a[92:95], v[192:195], v[116:119]\n"
    "ds_read_u16_d16_hi v205, v205\n"
    "v_cvt_pk_bf16_f32 v96, v96, v97\n"
    "v_mfma_f32_16x16x32_bf16 v[128:131], v[236:239], v[192:195], v[128:131]\n"
    "ds_read_u16 v190, v190\n"
    "buffer_load_dwordx4 v[20:23], v247, s[40:43], s60 offen offset:0\n"
    "buffer_load_dwordx4 v[52:55], v247, s[40:43], s60 offen offset:64\n"
    "v_mfma_f32_16x16x32_bf16 v[104:107], a[80:83], v[196:199], v[104:107]\n"
    "s_waitcnt lgkmcnt(8)\n"
    "v_or_b32_e64 v186, v186, v202\n"
    "v_lshlrev_b32_sdwa v202, 1, v173 dst_sel:DWORD dst_unused:UNUSED_PAD src0_sel:DWORD src1_sel:WORD_1\n"
    "v_mfma_f32_16x16x32_bf16 v[108:111], a[84:87], v[196:199], v[108:111]\n"
    "s_waitcnt lgkmcnt(6)\n"
    "ds_read_u16_d16_hi v206, v206\n"
    "v_cvt_pk_bf16_f32 v97, v98, v99\n"
    "v_mfma_f32_16x16x32_bf16 v[132:135], a[244:247], v[192:195], v[132:135]\n"
    "v_cvt_pk_f16_f32 v192, v168, v169\n"
    "v_lshlrev_b32_sdwa v193, 1, v170 dst_sel:DWORD dst_unused:UNUSED_PAD src0_sel:DWORD src1_sel:WORD_1\n"
    "v_mfma_f32_16x16x32_bf16 v[120:123], a[88:91], v[196:199], v[120:123]\n"
    "ds_read_u16 v191, v191\n"
    "v_lshlrev_b32_sdwa v194, 1, v172 dst_sel:DWORD dst_unused:UNUSED_PAD src0_sel:DWORD src1_sel:WORD_1\n"
    "v_mfma_f32_16x16x32_bf16 v[124:127], a[92:95], v[196:199], v[124:127]\n"
    "s_waitcnt lgkmcnt(7)\n"
    "ds_read_u16_d16_hi v207, v207\n"
    "v_lshlrev_b32_sdwa v195, 1, v174 dst_sel:DWORD dst_unused:UNUSED_PAD src0_sel:DWORD src1_sel:WORD_1\n"
    "v_mfma_f32_16x16x32_bf16 v[136:139], v[236:239], v[196:199], v[136:139]\n"
    "v_or_b32_e32 v187, v187, v203\n"
    "v_lshlrev_b32_sdwa v203, 1, v175 dst_sel:DWORD dst_unused:UNUSED_PAD src0_sel:DWORD src1_sel:WORD_1\n"
    "v_mfma_f32_16x16x32_bf16 v[140:143], a[244:247], v[196:199], v[140:143]\n"
    "ds_read_u16 v193, v193\n"
    "v_cvt_pk_f16_f32 v196, v176, v177\n"
    "v_mfma_f32_16x16x32_bf16 v[168:171], a[144:147], v[144:147], 0\n"
    "v_lshlrev_b32_sdwa v197, 1, v178 dst_sel:DWORD dst_unused:UNUSED_PAD src0_sel:DWORD src1_sel:WORD_1\n"
    "v_lshlrev_b32_sdwa v198, 1, v180 dst_sel:DWORD dst_unused:UNUSED_PAD src0_sel:DWORD src1_sel:WORD_1\n"
    "v_mfma_f32_16x16x32_bf16 v[168:171], a[192:195], v[148:151], v[168:171]\n"
    "s_waitcnt lgkmcnt(7)\n"
    "ds_read_u16_d16_hi v201, v201\n"
    "v_lshlrev_b32_sdwa v199, 1, v182 dst_sel:DWORD dst_unused:UNUSED_PAD src0_sel:DWORD src1_sel:WORD_1\n"
    "v_mfma_f32_16x16x32_bf16 v[168:171], a[104:107], v[152:155], v[168:171]\n"
    "v_or_b32_e32 v188, v188, v204\n"
    "v_cvt_pk_bf16_f32 v98, v100, v101\n"
    "v_mfma_f32_16x16x32_bf16 v[172:175], a[148:151], v[144:147], 0\n"
    "ds_read_u16 v194, v194\n"
    "v_cvt_pk_bf16_f32 v99, v102, v103\n"
    "v_mfma_f32_16x16x32_bf16 v[172:175], a[196:199], v[148:151], v[172:175]\n"
    "s_waitcnt lgkmcnt(7)\n"
    "ds_read_u16_d16_hi v202, v202\n"
    "v_or_b32_e32 v189, v189, v205\n"
    "v_mfma_f32_16x16x32_bf16 v[172:175], a[108:111], v[152:155], v[172:175]\n"
    "v_lshlrev_b32_sdwa v205, 1, v179 dst_sel:DWORD dst_unused:UNUSED_PAD src0_sel:DWORD src1_sel:WORD_1\n"
    "v_cvt_pk_bf16_f32 v112, v112, v113\n"
    "v_mfma_f32_16x16x32_bf16 v[176:179], a[144:147], v[156:159], 0\n"
    "ds_read_b128 v[236:239], v244 offset:0\n"
    "v_cvt_pk_bf16_f32 v113, v114, v115\n"
    "v_mfma_f32_16x16x32_bf16 v[176:179], a[192:195], v[160:163], v[176:179]\n"
    "ds_read_u16 v195, v195\n"
    "v_cvt_pk_bf16_f32 v114, v116, v117\n"
    "v_mfma_f32_16x16x32_bf16 v[176:179], a[104:107], v[164:167], v[176:179]\n"
    "s_waitcnt lgkmcnt(8)\n"
    "v_or_b32_e32 v190, v190, v206\n"
    "v_lshlrev_b32_sdwa v206, 1, v181 dst_sel:DWORD dst_unused:UNUSED_PAD src0_sel:DWORD src1_sel:WORD_1\n"
    "v_mfma_f32_16x16x32_bf16 v[100:103], a[4:7], v[184:187], 0\n"
    "ds_read_u16_d16_hi v203, v203\n"
    "v_cvt_pk_bf16_f32 v115, v118, v119\n"
    "v_cvt_pk_bf16_f32 v128, v128, v129\n"
    "v_cvt_pk_bf16_f32 v129, v130, v131\n"
    "v_cvt_pk_bf16_f32 v130, v132, v133\n"
    "ds_write_b128 v243, v[96:99] offset:24640\n"
    "buffer_load_dwordx4 v[4:7], v246, s[40:43], s61 offen offset:0\n"
    "buffer_load_dwordx4 v[36:39], v246, s[40:43], s61 offen offset:64\n"
    "v_mfma_f32_16x16x32_bf16 v[96:99], a[0:3], v[184:187], 0\n"
    "s_waitcnt lgkmcnt(7)\n"
    "v_or_b32_e32 v191, v191, v207\n"
    "v_lshlrev_b32_sdwa v207, 1, v183 dst_sel:DWORD dst_unused:UNUSED_PAD src0_sel:DWORD src1_sel:WORD_1\n"
    "v_mfma_f32_16x16x32_bf16 v[180:183], a[148:151], v[156:159], 0\n"
    "ds_read_b128 a[244:247], v244 offset:2048\n"
    "ds_write_b128 v243, v[112:115] offset:26688\n"
    "v_mfma_f32_16x16x32_bf16 v[180:183], a[196:199], v[160:163], v[180:183]\n"
    "s_waitcnt lgkmcnt(8)\n"
    "v_or_b32_e32 v193, v193, v201\n"
    "v_cvt_pk_bf16_f32 v131, v134, v135\n"
    "v_mfma_f32_16x16x32_bf16 v[180:183], a[108:111], v[164:167], v[180:183]\n"
    "v_cvt_pk_bf16_f32 v104, v104, v105\n"
    "v_cvt_pk_bf16_f32 v105, v106, v107\n"
    "v_mfma_f32_16x16x32_bf16 v[112:115], a[8:11], v[184:187], 0\n"
    "s_waitcnt lgkmcnt(5)\n"
    "ds_read_u16 v197, v197\n"
    "ds_write_b128 v243, v[128:131] offset:28736\n"
    "v_mfma_f32_16x16x32_bf16 v[116:119], a[12:15], v[184:187], 0\n"
    "s_nop 0\n"
    "v_cvt_pk_bf16_f32 v106, v108, v109\n"
    "v_cvt_pk_bf16_f32 v107, v110, v111\n"
    "v_mfma_f32_16x16x32_bf16 v[128:131], v[236:239], v[184:187], 0\n"
    "v_or_b32_e64 v194, v194, v202\n"
    "v_cvt_pk_bf16_f32 v120, v120, v121\n"
    "v_mfma_f32_16x16x32_bf16 v[108:111], a[4:7], v[188:191], 0\n"
    "ds_read_u16_d16_hi v205, v205\n"
    "ds_write_b128 v243, v[104:107] offset:25664\n"
    "v_mfma_f32_16x16x32_bf16 v[104:107], a[0:3], v[188:191], 0\n"
    "s_waitcnt lgkmcnt(6)\n"
    "v_cvt_pk_bf16_f32 v121, v122, v123\n"
    "v_cvt_pk_bf16_f32 v122, v124, v125\n"
    "s_waitcnt lgkmcnt(5)\n"
    "v_mfma_f32_16x16x32_bf16 v[132:135], a[244:247], v[184:187], 0\n"
    "v_cvt_pk_bf16_f32 v123, v126, v127\n"
    "v_pk_fma_f16 v232, v192, v192, s48\n"
    "v_mfma_f32_16x16x32_bf16 v[124:127], a[12:15], v[188:191], 0\n"
    "ds_read_u16 v198, v198\n"
    "ds_write_b128 v243, v[120:123] offset:27712\n"
    "v_mfma_f32_16x16x32_bf16 v[120:123], a[8:11], v[188:191], 0\n"
    "v_cvt_pk_bf16_f32 v136, v136, v137\n"
    "v_cvt_pk_bf16_f32 v137, v138, v139\n"
    "v_cvt_pk_bf16_f32 v138, v140, v141\n"
    "v_or_b32_e64 v195, v195, v203\n"
    "v_cvt_pk_bf16_f32 v139, v142, v143\n"
    "v_pk_mul_f16 v232, v192, v232\n"
    "v_cvt_pk_f16_f32 v184, v168, v169\n"
    "ds_write_b128 v243, v[136:139] offset:29760\n"
    "v_mfma_f32_16x16x32_bf16 v[136:139], v[236:239], v[188:191], 0\n"
    "v_exp_f16_e32 v234, v232\n"
    "v_mfma_f32_16x16x32_bf16 v[140:143], a[244:247], v[188:191], 0\n"
    "s_waitcnt lgkmcnt(3)\n"
    "ds_read_u16_d16_hi v206, v206\n"
    "ds_read_u16 v199, v199\n"
    "v_or_b32_e64 v197, v197, v205\n"
    "v_exp_f16_sdwa v234, v232 dst_sel:WORD_1 dst_unused:UNUSED_PRESERVE src0_sel:WORD_1\n"
    "v_lshlrev_b32_sdwa v185, 1, v170 dst_sel:DWORD dst_unused:UNUSED_PAD src0_sel:DWORD src1_sel:WORD_1\n"
    "v_pk_fma_f16 v234, v234, s49, s49\n"
    "v_lshlrev_b32_sdwa v201, 1, v171 dst_sel:DWORD dst_unused:UNUSED_PAD src0_sel:DWORD src1_sel:WORD_1\n"
    "v_mfma_f32_16x16x32_bf16 v[168:171], a[152:155], v[144:147], 0\n"
    "ds_read_u16_d16_hi v207, v207\n"
    "ds_read_b128 a[252:255], v249 offset:0\n"
    "v_mfma_f32_16x16x32_bf16 v[168:171], a[200:203], v[148:151], v[168:171]\n"
    "buffer_load_dwordx4 v[24:27], v247, s[40:43], s61 offen offset:0\n"
    "buffer_load_dwordx4 v[56:59], v247, s[40:43], s61 offen offset:64\n"
    "ds_read_u16 v185, v185\n"
    "v_lshlrev_b32_sdwa v186, 1, v172 dst_sel:DWORD dst_unused:UNUSED_PAD src0_sel:DWORD src1_sel:WORD_1\n"
    "v_lshlrev_b32_sdwa v202, 1, v173 dst_sel:DWORD dst_unused:UNUSED_PAD src0_sel:DWORD src1_sel:WORD_1\n"
    "v_lshlrev_b32_sdwa v187, 1, v174 dst_sel:DWORD dst_unused:UNUSED_PAD src0_sel:DWORD src1_sel:WORD_1\n"
    "v_lshlrev_b32_sdwa v203, 1, v175 dst_sel:DWORD dst_unused:UNUSED_PAD src0_sel:DWORD src1_sel:WORD_1\n"
    "v_rcp_f16_e64 v232, v234\n"
    "v_mfma_f32_16x16x32_bf16 v[168:171], a[112:115], v[152:155], v[168:171]\n"
    "ds_read_u16_d16_hi v201, v201\n"
    "ds_read_u16 v186, v186\n"
    "v_rcp_f16_sdwa v232, v234 dst_sel:WORD_1 dst_unused:UNUSED_PRESERVE src0_sel:WORD_1\n"
    "v_mfma_f32_16x16x32_bf16 v[172:175], a[156:159], v[144:147], 0\n"
    "ds_read_u16_d16_hi v202, v202\n"
    "v_cvt_pk_f16_f32 v188, v176, v177\n"
    "v_mfma_f32_16x16x32_bf16 v[172:175], a[204:207], v[148:151], v[172:175]\n"
    "v_lshlrev_b32_sdwa v189, 1, v178 dst_sel:DWORD dst_unused:UNUSED_PAD src0_sel:DWORD src1_sel:WORD_1\n"
    "v_lshlrev_b32_sdwa v205, 1, v179 dst_sel:DWORD dst_unused:UNUSED_PAD src0_sel:DWORD src1_sel:WORD_1\n"
    "v_lshlrev_b32_sdwa v190, 1, v180 dst_sel:DWORD dst_unused:UNUSED_PAD src0_sel:DWORD src1_sel:WORD_1\n"
    "v_lshlrev_b32_sdwa v191, 1, v182 dst_sel:DWORD dst_unused:UNUSED_PAD src0_sel:DWORD src1_sel:WORD_1\n"
    "v_fma_mix_f32 v234, v192, v232, 0 op_sel_hi:[1,1,0]\n"
    "v_mfma_f32_16x16x32_bf16 v[172:175], a[116:119], v[152:155], v[172:175]\n"
    "s_waitcnt lgkmcnt(7)\n"
    "ds_read_b128 v[236:239], v244 offset:4096\n"
    "ds_read_u16 v187, v187\n"
    "v_or_b32_e32 v198, v198, v206\n"
    "v_lshlrev_b32_sdwa v206, 1, v181 dst_sel:DWORD dst_unused:UNUSED_PAD src0_sel:DWORD src1_sel:WORD_1\n"
    "v_fma_mix_f32 v232, v192, v232, 0 op_sel:[1,1,0] op_sel_hi:[1,1,0]\n"
    "v_mfma_f32_16x16x32_bf16 v[176:179], a[152:155], v[156:159], 0\n"
    "ds_read_u16_d16_hi v203, v203\n"
    "v_cvt_pk_bf16_f32 v192, v234, v232\n"
    "v_mfma_f32_16x16x32_bf16 v[176:179], a[200:203], v[160:163], v[176:179]\n"
    "s_waitcnt lgkmcnt(7)\n"
    "buffer_store_dwordx4 a[252:255], v255, s[68:71], s59 offen offset:0 sc1\n"
    "ds_read_b128 a[244:247], v244 offset:6144\n"
    "v_mfma_f32_16x16x32_bf16 v[176:179], a[112:115], v[164:167], v[176:179]\n"
    "s_nop 0\n"
    "ds_read_b128 a[252:255], v249 offset:2048\n"
    "v_or_b32_e64 v199, v199, v207\n"
    "v_lshlrev_b32_sdwa v207, 1, v183 dst_sel:DWORD dst_unused:UNUSED_PAD src0_sel:DWORD src1_sel:WORD_1\n"
    "v_pk_fma_f16 v232, v196, v196, s48\n"
    "v_mfma_f32_16x16x32_bf16 v[180:183], a[156:159], v[156:159], 0\n"
    "ds_read_u16 v189, v189\n"
    "v_pk_mul_f16 v232, v196, v232\n"
    "v_mfma_f32_16x16x32_bf16 v[180:183], a[204:207], v[160:163], v[180:183]\n"
    "ds_read_u16_d16_hi v205, v205\n"
    "v_exp_f16_e32 v234, v232\n"
    "v_mfma_f32_16x16x32_bf16 v[180:183], a[116:119], v[164:167], v[180:183]\n"
    "s_waitcnt lgkmcnt(9)\n"
    "v_exp_f16_sdwa v234, v232 dst_sel:WORD_1 dst_unused:UNUSED_PRESERVE src0_sel:WORD_1\n"
    "v_mfma_f32_16x16x32_bf16 v[96:99], a[16:19], v[192:195], v[96:99]\n"
    "ds_read_u16 v190, v190\n"
    "v_pk_fma_f16 v234, v234, s49, s49\n"
    "v_mfma_f32_16x16x32_bf16 v[100:103], a[20:23], v[192:195], v[100:103]\n"
    "ds_read_u16_d16_hi v206, v206\n"
    "v_rcp_f16_e32 v232, v234\n"
    "v_mfma_f32_16x16x32_bf16 v[112:115], a[24:27], v[192:195], v[112:115]\n"
    "s_waitcnt lgkmcnt(8)\n"
    "v_rcp_f16_sdwa v232, v234 dst_sel:WORD_1 dst_unused:UNUSED_PRESERVE src0_sel:WORD_1\n"
    "v_mfma_f32_16x16x32_bf16 v[116:119], a[28:31], v[192:195], v[116:119]\n"
    "buffer_load_dwordx4 v[8:11], v246, s[40:43], s62 offen offset:0\n"
    "buffer_load_dwordx4 v[40:43], v246, s[40:43], s62 offen offset:64\n"
    "ds_read_u16 v191, v191\n"
    "v_mfma_f32_16x16x32_bf16 v[128:131], v[236:239], v[192:195], v[128:131]\n"
    "ds_read_u16_d16_hi v207, v207\n"
    "v_or_b32_e64 v185, v185, v201\n"
    "v_fma_mix_f32 v234, v196, v232, 0 op_sel_hi:[1,1,0]\n"
    "v_fma_mix_f32 v232, v196, v232, 0 op_sel:[1,1,0] op_sel_hi:[1,1,0]\n"
    "v_or_b32_e32 v186, v186, v202\n"
    "s_waitcnt lgkmcnt(7)\n"
    "v_mfma_f32_16x16x32_bf16 v[132:135], a[244:247], v[192:195], v[132:135]\n"
    "ds_read_b128 a[240:243], v244 offset:34816\n"
    "v_cvt_pk_bf16_f32 v196, v234, v232\n"
    "v_pk_fma_f16 v232, v184, v184, s48\n"
    "v_or_b32_e32 v187, v187, v203\n"
    "v_mfma_f32_16x16x32_bf16 v[104:107], a[16:19], v[196:199], v[104:107]\n"
    "s_waitcnt lgkmcnt(7)\n"
    "v_pk_mul_f16 v232, v184, v232\n"
    "v_lshlrev_b32_sdwa v192, 1, v168 dst_sel:DWORD dst_unused:UNUSED_PAD src0_sel:DWORD src1_sel:WORD_1\n"
    "v_mfma_f32_16x16x32_bf16 v[108:111], a[20:23], v[196:199], v[108:111]\n"
    "ds_read_u16 v192, v192\n"
    "v_exp_f16_e64 v234, v232\n"
    "v_mfma_f32_16x16x32_bf16 v[120:123], a[24:27], v[196:199], v[120:123]\n"
    "v_exp_f16_sdwa v234, v232 dst_sel:WORD_1 dst_unused:UNUSED_PRESERVE src0_sel:WORD_1\n"
    "v_mfma_f32_16x16x32_bf16 v[124:127], a[28:31], v[196:199], v[124:127]\n"
    "v_pk_fma_f16 v234, v234, s49, s49\n"
    "s_waitcnt lgkmcnt(6)\n"
    "v_or_b32_e32 v189, v189, v205\n"
    "v_mfma_f32_16x16x32_bf16 v[136:139], v[236:239], v[196:199], v[136:139]\n"
    "ds_read_b128 v[236:239], v244 offset:8192\n"
    "v_lshlrev_b32_sdwa v200, 1, v169 dst_sel:DWORD dst_unused:UNUSED_PAD src0_sel:DWORD src1_sel:WORD_1\n"
    "v_mfma_f32_16x16x32_bf16 v[140:143], a[244:247], v[196:199], v[140:143]\n"
    "ds_read_u16_d16_hi v200, v200\n"
    "buffer_store_dwordx4 a[252:255], v255, s[68:71], s59 offen offset:64 sc1\n"
    "v_lshlrev_b32_sdwa v193, 1, v170 dst_sel:DWORD dst_unused:UNUSED_PAD src0_sel:DWORD src1_sel:WORD_1\n"
    "v_lshlrev_b32_sdwa v201, 1, v171 dst_sel:DWORD dst_unused:UNUSED_PAD src0_sel:DWORD src1_sel:WORD_1\n"
    "v_mfma_f32_16x16x32_bf16 v[168:171], a[160:163], v[144:147], 0\n"
    "s_waitcnt lgkmcnt(6)\n"
    "ds_read_b128 a[252:255], v249 offset:4096\n"
    "ds_read_u16 v193, v193\n"
    "v_mfma_f32_16x16x32_bf16 v[168:171], a[208:211], v[148:151], v[168:171]\n"
    "s_nop 0\n"
    "ds_read_u16_d16_hi v201, v201\n"
    "v_lshlrev_b32_sdwa v194, 1, v172 dst_sel:DWORD dst_unused:UNUSED_PAD src0_sel:DWORD src1_sel:WORD_1\n"
    "v_lshlrev_b32_sdwa v202, 1, v173 dst_sel:DWORD dst_unused:UNUSED_PAD src0_sel:DWORD src1_sel:WORD_1\n"
    "v_lshlrev_b32_sdwa v195, 1, v174 dst_sel:DWORD dst_unused:UNUSED_PAD src0_sel:DWORD src1_sel:WORD_1\n"
    "v_lshlrev_b32_sdwa v203, 1, v175 dst_sel:DWORD dst_unused:UNUSED_PAD src0_sel:DWORD src1_sel:WORD_1\n"
    "v_rcp_f16_e32 v232, v234\n"
    "v_mfma_f32_16x16x32_bf16 v[168:171], a[120:123], v[152:155], v[168:171]\n"
    "s_waitcnt lgkmcnt(6)\n"
    "ds_read_u16 v194, v194\n"
    "ds_read_u16_d16_hi v202, v202\n"
    "v_or_b32_e64 v190, v190, v206\n"
    "v_rcp_f16_sdwa v232, v234 dst_sel:WORD_1 dst_unused:UNUSED_PRESERVE src0_sel:WORD_1\n"
    "v_mfma_f32_16x16x32_bf16 v[172:175], a[164:167], v[144:147], 0\n"
    "ds_read_u16 v195, v195\n"
    "v_lshlrev_b32_sdwa v196, 1, v176 dst_sel:DWORD dst_unused:UNUSED_PAD src0_sel:DWORD src1_sel:WORD_1\n"
    "v_mfma_f32_16x16x32_bf16 v[172:175], a[212:215], v[148:151], v[172:175]\n"
    "ds_read_b128 a[244:247], v244 offset:10240\n"
    "v_lshlrev_b32_sdwa v204, 1, v177 dst_sel:DWORD dst_unused:UNUSED_PAD src0_sel:DWORD src1_sel:WORD_1\n"
    "v_lshlrev_b32_sdwa v197, 1, v178 dst_sel:DWORD dst_unused:UNUSED_PAD src0_sel:DWORD src1_sel:WORD_1\n"
    "v_lshlrev_b32_sdwa v205, 1, v179 dst_sel:DWORD dst_unused:UNUSED_PAD src0_sel:DWORD src1_sel:WORD_1\n"
    "v_lshlrev_b32_sdwa v198, 1, v180 dst_sel:DWORD dst_unused:UNUSED_PAD src0_sel:DWORD src1_sel:WORD_1\n"
    "v_lshlrev_b32_sdwa v206, 1, v181 dst_sel:DWORD dst_unused:UNUSED_PAD src0_sel:DWORD src1_sel:WORD_1\n"
    "v_lshlrev_b32_sdwa v199, 1, v182 dst_sel:DWORD dst_unused:UNUSED_PAD src0_sel:DWORD src1_sel:WORD_1\n"
    "v_fma_mix_f32 v234, v184, v232, 0 op_sel_hi:[1,1,0]\n"
    "v_mfma_f32_16x16x32_bf16 v[172:175], a[240:243], v[152:155], v[172:175]\n"
    "s_waitcnt lgkmcnt(6)\n"
    "buffer_load_dwordx4 v[12:15], v246, s[40:43], s63 offen offset:0\n"
    "buffer_load_dwordx4 v[44:47], v246, s[40:43], s63 offen offset:64\n"
    "ds_read_u16_d16_hi v203, v203\n"
    "v_or_b32_e32 v191, v191, v207\n"
    "v_lshlrev_b32_sdwa v207, 1, v183 dst_sel:DWORD dst_unused:UNUSED_PAD src0_sel:DWORD src1_sel:WORD_1\n"
    "v_fma_mix_f32 v232, v184, v232, 0 op_sel:[1,1,0] op_sel_hi:[1,1,0]\n"
    "v_mfma_f32_16x16x32_bf16 v[176:179], a[160:163], v[156:159], 0\n"
    "ds_read_u16 v196, v196\n"
    "ds_read_u16_d16_hi v204, v204\n"
    "v_cvt_pk_bf16_f32 v184, v234, v232\n"
    "v_mfma_f32_16x16x32_bf16 v[176:179], a[208:211], v[160:163], v[176:179]\n"
    "v_pk_fma_f16 v232, v188, v188, s48\n"
    "v_or_b32_e32 v192, v192, v200\n"
    "v_mfma_f32_16x16x32_bf16 v[176:179], a[120:123], v[164:167], v[176:179]\n"
    "s_waitcnt lgkmcnt(7)\n"
    "ds_read_u16 v197, v197\n"
    "v_pk_mul_f16 v232, v188, v232\n"
    "v_mfma_f32_16x16x32_bf16 v[180:183], a[164:167], v[156:159], 0\n"
    "ds_read_u16_d16_hi v205, v205\n"
    "v_exp_f16_e64 v234, v232\n"
    "v_mfma_f32_16x16x32_bf16 v[180:183], a[212:215], v[160:163], v[180:183]\n"
    "v_exp_f16_sdwa v234, v232 dst_sel:WORD_1 dst_unused:UNUSED_PRESERVE src0_sel:WORD_1\n"
    "v_mfma_f32_16x16x32_bf16 v[180:183], a[240:243], v[164:167], v[180:183]\n"
    "ds_read_u16 v198, v198\n"
    "v_pk_fma_f16 v234, v234, s49, s49\n"
    "v_mfma_f32_16x16x32_bf16 v[96:99], a[32:35], v[184:187], v[96:99]\n"
    "s_waitcnt lgkmcnt(8)\n"
    "v_rcp_f16_e32 v232, v234\n"
    "v_mfma_f32_16x16x32_bf16 v[100:103], a[36:39], v[184:187], v[100:103]\n"
    "ds_read_u16_d16_hi v206, v206\n"
    "buffer_store_dwordx4 a[252:255], v255, s[68:71], s59 offen offset:128 sc1\n"
    "v_mfma_f32_16x16x32_bf16 v[112:115], a[40:43], v[184:187], v[112:115]\n"
    "s_nop 0\n"
    "ds_read_u16 v199, v199\n"
    "v_rcp_f16_sdwa v232, v234 dst_sel:WORD_1 dst_unused:UNUSED_PRESERVE src0_sel:WORD_1\n"
    "v_mfma_f32_16x16x32_bf16 v[116:119], a[44:47], v[184:187], v[116:119]\n"
    "s_waitcnt lgkmcnt(7)\n"
    "ds_read_b128 a[252:255], v249 offset:1024\n"
    "v_fma_mix_f32 v234, v188, v232, 0 op_sel_hi:[1,1,0]\n"
    "v_mfma_f32_16x16x32_bf16 v[128:131], v[236:239], v[184:187], v[128:131]\n"
    "ds_read_u16_d16_hi v207, v207\n"
    "v_fma_mix_f32 v232, v188, v232, 0 op_sel:[1,1,0] op_sel_hi:[1,1,0]\n"
    "v_mfma_f32_16x16x32_bf16 v[132:135], a[244:247], v[184:187], v[132:135]\n"
    "v_cvt_pk_bf16_f32 v188, v234, v232\n"
    "v_or_b32_e32 v193, v193, v201\n"
    "v_cvt_pk_f16_f32 v184, v168, v169\n"
    "v_mfma_f32_16x16x32_bf16 v[104:107], a[32:35], v[188:191], v[104:107]\n"
    "s_waitcnt lgkmcnt(7)\n"
    "ds_read_b128 a[240:243], v244 offset:38912\n"
    "v_lshlrev_b32_sdwa v185, 1, v170 dst_sel:DWORD dst_unused:UNUSED_PAD src0_sel:DWORD src1_sel:WORD_1\n"
    "v_mfma_f32_16x16x32_bf16 v[108:111], a[36:39], v[188:191], v[108:111]\n"
    "ds_read_u16 v185, v185\n"
    "v_lshlrev_b32_sdwa v201, 1, v171 dst_sel:DWORD dst_unused:UNUSED_PAD src0_sel:DWORD src1_sel:WORD_1\n"
    "v_mfma_f32_16x16x32_bf16 v[120:123], a[40:43], v[188:191], v[120:123]\n"
    "ds_read_u16_d16_hi v201, v201\n"
    "v_lshlrev_b32_sdwa v186, 1, v172 dst_sel:DWORD dst_unused:UNUSED_PAD src0_sel:DWORD src1_sel:WORD_1\n"
    "v_mfma_f32_16x16x32_bf16 v[124:127], a[44:47], v[188:191], v[124:127]\n"
    "v_or_b32_e32 v194, v194, v202\n"
    "v_lshlrev_b32_sdwa v202, 1, v173 dst_sel:DWORD dst_unused:UNUSED_PAD src0_sel:DWORD src1_sel:WORD_1\n"
    "v_mfma_f32_16x16x32_bf16 v[136:139], v[236:239], v[188:191], v[136:139]\n"
    "s_waitcnt lgkmcnt(8)\n"
    "buffer_load_dwordx4 v[16:19], v246, s[40:43], s52 offen offset:0\n"
    "buffer_load_dwordx4 v[48:51], v246, s[40:43], s52 offen offset:64\n"
    "ds_read_u16 v186, v186\n"
    "v_mfma_f32_16x16x32_bf16 v[140:143], a[244:247], v[188:191], v[140:143]\n"
    "ds_read_u16_d16_hi v202, v202\n"
    "v_lshlrev_b32_sdwa v187, 1, v174 dst_sel:DWORD dst_unused:UNUSED_PAD src0_sel:DWORD src1_sel:WORD_1\n"
    "v_mfma_f32_16x16x32_bf16 v[168:171], a[168:171], v[144:147], 0\n"
    "v_cvt_pk_f16_f32 v188, v176, v177\n"
    "v_lshlrev_b32_sdwa v189, 1, v178 dst_sel:DWORD dst_unused:UNUSED_PAD src0_sel:DWORD src1_sel:WORD_1\n"
    "v_mfma_f32_16x16x32_bf16 v[168:171], a[216:219], v[148:151], v[168:171]\n"
    "s_waitcnt lgkmcnt(8)\n"
    "ds_read_b128 v[236:239], v244 offset:12288\n"
    "v_lshlrev_b32_sdwa v190, 1, v180 dst_sel:DWORD dst_unused:UNUSED_PAD src0_sel:DWORD src1_sel:WORD_1\n"
    "v_mfma_f32_16x16x32_bf16 v[168:171], a[124:127], v[152:155], v[168:171]\n"
    "s_waitcnt lgkmcnt(7)\n"
    "ds_read_u16 v187, v187\n"
    "v_lshlrev_b32_sdwa v191, 1, v182 dst_sel:DWORD dst_unused:UNUSED_PAD src0_sel:DWORD src1_sel:WORD_1\n"
    "v_or_b32_e64 v195, v195, v203\n"
    "v_lshlrev_b32_sdwa v203, 1, v175 dst_sel:DWORD dst_unused:UNUSED_PAD src0_sel:DWORD src1_sel:WORD_1\n"
    "v_mfma_f32_16x16x32_bf16 v[172:175], a[172:175], v[144:147], 0\n"
    "ds_read_u16_d16_hi v203, v203\n"
    "v_or_b32_e64 v196, v196, v204\n"
    "v_mfma_f32_16x16x32_bf16 v[172:175], a[220:223], v[148:151], v[172:175]\n"
    "v_or_b32_e64 v197, v197, v205\n"
    "v_lshlrev_b32_sdwa v205, 1, v179 dst_sel:DWORD dst_unused:UNUSED_PAD src0_sel:DWORD src1_sel:WORD_1\n"
    "s_waitcnt lgkmcnt(7)\n"
    "v_mfma_f32_16x16x32_bf16 v[172:175], a[240:243], v[152:155], v[172:175]\n"
    "s_nop 0\n"
    "ds_read_b128 a[244:247], v244 offset:14336\n"
    "v_or_b32_e64 v198, v198, v206\n"
    "v_mfma_f32_16x16x32_bf16 v[176:179], a[168:171], v[156:159], 0\n"
    "ds_read_u16 v189, v189\n"
    "v_lshlrev_b32_sdwa v206, 1, v181 dst_sel:DWORD dst_unused:UNUSED_PAD src0_sel:DWORD src1_sel:WORD_1\n"
    "v_mfma_f32_16x16x32_bf16 v[176:179], a[216:219], v[160:163], v[176:179]\n"
    "buffer_store_dwordx4 a[252:255], v255, s[68:71], s39 offen offset:0 sc1\n"
    "ds_read_u16_d16_hi v205, v205\n"
    "v_mfma_f32_16x16x32_bf16 v[176:179], a[124:127], v[164:167], v[176:179]\n"
    "s_waitcnt lgkmcnt(8)\n"
    "ds_read_b128 a[252:255], v249 offset:3072\n"
    "v_or_b32_e32 v199, v199, v207\n"
    "v_mfma_f32_16x16x32_bf16 v[96:99], a[48:51], v[192:195], v[96:99]\n"
    "ds_read_u16 v190, v190\n"
    "v_lshlrev_b32_sdwa v207, 1, v183 dst_sel:DWORD dst_unused:UNUSED_PAD src0_sel:DWORD src1_sel:WORD_1\n"
    "v_mfma_f32_16x16x32_bf16 v[180:183], a[172:175], v[156:159], 0\n"
    "v_or_b32_e64 v185, v185, v201\n"
    "v_pk_fma_f16 v232, v184, v184, s48\n"
    "v_mfma_f32_16x16x32_bf16 v[180:183], a[220:223], v[160:163], v[180:183]\n"
    "s_waitcnt lgkmcnt(7)\n"
    "ds_read_b128 v[144:147], v243 offset:0\n"
    "v_or_b32_e32 v186, v186, v202\n"
    "v_mfma_f32_16x16x32_bf16 v[180:183], a[240:243], v[164:167], v[180:183]\n"
    "ds_read_u16_d16_hi v206, v206\n"
    "v_pk_mul_f16 v232, v184, v232\n"
    "v_mfma_f32_16x16x32_bf16 v[100:103], a[52:55], v[192:195], v[100:103]\n"
    "v_exp_f16_e64 v234, v232\n"
    "v_mfma_f32_16x16x32_bf16 v[112:115], a[56:59], v[192:195], v[112:115]\n"
    "ds_read_b128 v[148:151], v243 offset:1024\n"
    "v_exp_f16_sdwa v234, v232 dst_sel:WORD_1 dst_unused:UNUSED_PRESERVE src0_sel:WORD_1\n"
    "v_mfma_f32_16x16x32_bf16 v[116:119], a[60:63], v[192:195], v[116:119]\n"
    "ds_read_u16 v191, v191\n"
    "v_pk_fma_f16 v234, v234, s49, s49\n"
    "v_mfma_f32_16x16x32_bf16 v[128:131], v[236:239], v[192:195], v[128:131]\n"
    "s_waitcnt lgkmcnt(8)\n"
    "ds_read_b128 v[152:155], v243 offset:2048\n"
    "v_or_b32_e32 v187, v187, v203\n"
    "v_mfma_f32_16x16x32_bf16 v[132:135], a[244:247], v[192:195], v[132:135]\n"
    "ds_read_u16_d16_hi v207, v207\n"
    "v_cvt_pk_f16_f32 v192, v168, v169\n"
    "v_mfma_f32_16x16x32_bf16 v[104:107], a[48:51], v[196:199], v[104:107]\n"
    "v_lshlrev_b32_sdwa v193, 1, v170 dst_sel:DWORD dst_unused:UNUSED_PAD src0_sel:DWORD src1_sel:WORD_1\n"
    "v_lshlrev_b32_sdwa v201, 1, v171 dst_sel:DWORD dst_unused:UNUSED_PAD src0_sel:DWORD src1_sel:WORD_1\n"
    "v_mfma_f32_16x16x32_bf16 v[108:111], a[52:55], v[196:199], v[108:111]\n"
    "ds_read_b128 v[156:159], v243 offset:3072\n"
    "v_lshlrev_b32_sdwa v194, 1, v172 dst_sel:DWORD dst_unused:UNUSED_PAD src0_sel:DWORD src1_sel:WORD_1\n"
    "v_mfma_f32_16x16x32_bf16 v[120:123], a[56:59], v[196:199], v[120:123]\n"
    "s_waitcnt lgkmcnt(8)\n"
    "ds_read_b128 v[160:163], v243 offset:4096\n"
    "buffer_store_dwordx4 a[252:255], v255, s[68:71], s39 offen offset:64 sc1\n"
    "v_mfma_f32_16x16x32_bf16 v[124:127], a[60:63], v[196:199], v[124:127]\n"
    "s_nop 0\n"
    "ds_read_b128 v[164:167], v243 offset:5120\n"
    "ds_read_b128 a[252:255], v249 offset:5120\n"
    "v_mfma_f32_16x16x32_bf16 v[136:139], v[236:239], v[196:199], v[136:139]\n"
    "s_waitcnt lgkmcnt(9)\n"
    "ds_read_b128 a[240:243], v244 offset:32768\n"
    "v_or_b32_e32 v189, v189, v205\n"
    "v_mfma_f32_16x16x32_bf16 v[140:143], a[244:247], v[196:199], v[140:143]\n"
    "ds_read_u16 v193, v193\n"
    "v_lshlrev_b32_sdwa v202, 1, v173 dst_sel:DWORD dst_unused:UNUSED_PAD src0_sel:DWORD src1_sel:WORD_1\n"
    "v_mfma_f32_16x16x32_bf16 v[168:171], a[128:131], v[144:147], 0\n"
    "v_lshlrev_b32_sdwa v195, 1, v174 dst_sel:DWORD dst_unused:UNUSED_PAD src0_sel:DWORD src1_sel:WORD_1\n"
    "v_lshlrev_b32_sdwa v203, 1, v175 dst_sel:DWORD dst_unused:UNUSED_PAD src0_sel:DWORD src1_sel:WORD_1\n"
    "s_waitcnt lgkmcnt(9)\n"
    "v_mfma_f32_16x16x32_bf16 v[168:171], a[176:179], v[148:151], v[168:171]\n"
    "s_nop 0\n"
    "ds_read_u16_d16_hi v201, v201\n"
    "v_rcp_f16_e64 v232, v234\n"
    "v_or_b32_e32 v190, v190, v206\n"
    "v_rcp_f16_sdwa v232, v234 dst_sel:WORD_1 dst_unused:UNUSED_PRESERVE src0_sel:WORD_1\n"
    "v_mfma_f32_16x16x32_bf16 v[172:175], a[132:135], v[144:147], 0\n"
    "s_waitcnt lgkmcnt(8)\n"
    "ds_read_u16 v194, v194\n"
    "v_cvt_pk_f16_f32 v196, v176, v177\n"
    "v_mfma_f32_16x16x32_bf16 v[168:171], a[96:99], v[152:155], v[168:171]\n"
    "ds_read_u16_d16_hi v202, v202\n"
    "v_lshlrev_b32_sdwa v197, 1, v178 dst_sel:DWORD dst_unused:UNUSED_PAD src0_sel:DWORD src1_sel:WORD_1\n"
    "v_lshlrev_b32_sdwa v205, 1, v179 dst_sel:DWORD dst_unused:UNUSED_PAD src0_sel:DWORD src1_sel:WORD_1\n"
    "v_lshlrev_b32_sdwa v198, 1, v180 dst_sel:DWORD dst_unused:UNUSED_PAD src0_sel:DWORD src1_sel:WORD_1\n"
    "v_lshlrev_b32_sdwa v206, 1, v181 dst_sel:DWORD dst_unused:UNUSED_PAD src0_sel:DWORD src1_sel:WORD_1\n"
    "v_lshlrev_b32_sdwa v199, 1, v182 dst_sel:DWORD dst_unused:UNUSED_PAD src0_sel:DWORD src1_sel:WORD_1\n"
    "v_fma_mix_f32 v234, v184, v232, 0 op_sel_hi:[1,1,0]\n"
    "v_mfma_f32_16x16x32_bf16 v[172:175], a[180:183], v[148:151], v[172:175]\n"
    "s_waitcnt lgkmcnt(8)\n"
    "ds_read_b128 v[236:239], v244 offset:16384\n"
    "ds_read_u16 v195, v195\n"
    "v_or_b32_e32 v191, v191, v207\n"
    "v_lshlrev_b32_sdwa v207, 1, v183 dst_sel:DWORD dst_unused:UNUSED_PAD src0_sel:DWORD src1_sel:WORD_1\n"
    "v_fma_mix_f32 v232, v184, v232, 0 op_sel:[1,1,0] op_sel_hi:[1,1,0]\n"
    "v_mfma_f32_16x16x32_bf16 v[176:179], a[128:131], v[156:159], 0\n"
    "v_cvt_pk_bf16_f32 v184, v234, v232\n"
    "v_pk_fma_f16 v232, v188, v188, s48\n"
    "s_waitcnt lgkmcnt(7)\n"
    "v_mfma_f32_16x16x32_bf16 v[176:179], a[176:179], v[160:163], v[176:179]\n"
    "s_barrier\n"
    "s_waitcnt lgkmcnt(6)\n"
    "v_mfma_f32_16x16x32_bf16 v[172:175], a[240:243], v[152:155], v[172:175]\n"
    "s_nop 0\n"
    "ds_read_u16_d16_hi v203, v203\n"
    "buffer_store_dwordx4 a[252:255], v255, s[68:71], s39 offen offset:128 sc1\n"
    "v_mfma_f32_16x16x32_bf16 v[176:179], a[96:99], v[164:167], v[176:179]\n"
    "ds_read_b128 a[244:247], v244 offset:18432\n"
    "ds_write_b128 v253, v[64:67] offset:2048\n"
    "v_mfma_f32_16x16x32_bf16 v[180:183], a[132:135], v[156:159], 0\n"
    "s_waitcnt lgkmcnt(7)\n"
    "v_pk_mul_f16 v232, v188, v232\n"
    "v_or_b32_e32 v193, v193, v201\n"
    "v_mfma_f32_16x16x32_bf16 v[180:183], a[180:183], v[160:163], v[180:183]\n"
    "v_exp_f16_e64 v234, v232\n"
    "v_mfma_f32_16x16x32_bf16 v[180:183], a[240:243], v[164:167], v[180:183]\n"
    "ds_read_u16 v197, v197\n"
    "ds_write_b128 v253, v[68:71] offset:5120\n"
    "v_exp_f16_sdwa v234, v232 dst_sel:WORD_1 dst_unused:UNUSED_PRESERVE src0_sel:WORD_1\n"
    "v_mfma_f32_16x16x32_bf16 v[96:99], a[64:67], v[184:187], v[96:99]\n"
    "s_waitcnt lgkmcnt(6)\n"
    "v_pk_fma_f16 v234, v234, s49, s49\n"
    "v_or_b32_e32 v194, v194, v202\n"
    "v_mfma_f32_16x16x32_bf16 v[100:103], a[68:71], v[184:187], v[100:103]\n"
    "v_rcp_f16_e32 v232, v234\n"
    "v_mfma_f32_16x16x32_bf16 v[112:115], a[72:75], v[184:187], v[112:115]\n"
    "ds_read_u16_d16_hi v205, v205\n"
    "ds_write_b128 v253, v[88:91] offset:0\n"
    "v_rcp_f16_sdwa v232, v234 dst_sel:WORD_1 dst_unused:UNUSED_PRESERVE src0_sel:WORD_1\n"
    "v_mfma_f32_16x16x32_bf16 v[116:119], a[76:79], v[184:187], v[116:119]\n"
    "s_waitcnt lgkmcnt(4)\n"
    "v_fma_mix_f32 v234, v188, v232, 0 op_sel_hi:[1,1,0]\n"
    "v_fma_mix_f32 v232, v188, v232, 0 op_sel:[1,1,0] op_sel_hi:[1,1,0]\n"
    "v_mfma_f32_16x16x32_bf16 v[128:131], v[236:239], v[184:187], v[128:131]\n"
    "v_cvt_pk_bf16_f32 v188, v234, v232\n"
    "v_pk_fma_f16 v232, v192, v192, s48\n"
    "v_mfma_f32_16x16x32_bf16 v[132:135], a[244:247], v[184:187], v[132:135]\n"
    "ds_read_u16 v198, v198\n"
    "ds_write_b128 v253, v[92:95] offset:3072\n"
    "v_mfma_f32_16x16x32_bf16 v[104:107], a[64:67], v[188:191], v[104:107]\n"
    "buffer_load_dwordx4 a[232:235], v245, s[40:43], s57 offen offset:64\n"
    "buffer_load_dwordx4 a[224:227], v245, s[40:43], s57 offen offset:0\n"
    "v_mfma_f32_16x16x32_bf16 v[108:111], a[68:71], v[188:191], v[108:111]\n"
    "v_or_b32_e64 v195, v195, v203\n"
    "v_pk_mul_f16 v232, v192, v232\n"
    "v_mfma_f32_16x16x32_bf16 v[120:123], a[72:75], v[188:191], v[120:123]\n"
    "ds_read_u16_d16_hi v206, v206\n"
    "ds_read_u16 v199, v199\n"
    "v_mfma_f32_16x16x32_bf16 v[124:127], a[76:79], v[188:191], v[124:127]\n"
    "ds_read_u16_d16_hi v207, v207\n"
    "ds_write_b128 v253, v[72:75] offset:1024\n"
    "v_mfma_f32_16x16x32_bf16 v[136:139], v[236:239], v[188:191], v[136:139]\n"
    "s_waitcnt lgkmcnt(6)\n"
    "v_exp_f16_e32 v234, v232\n"
    "v_mfma_f32_16x16x32_bf16 v[140:143], a[244:247], v[188:191], v[140:143]\n"
    "v_or_b32_e64 v197, v197, v205\n"
    "v_exp_f16_sdwa v234, v232 dst_sel:WORD_1 dst_unused:UNUSED_PRESERVE src0_sel:WORD_1\n"
    "v_lshlrev_b32_sdwa v184, 1, v168 dst_sel:DWORD dst_unused:UNUSED_PAD src0_sel:DWORD src1_sel:WORD_1\n"
    "v_pk_fma_f16 v234, v234, s49, s49\n"
    "v_lshlrev_b32_sdwa v200, 1, v169 dst_sel:DWORD dst_unused:UNUSED_PAD src0_sel:DWORD src1_sel:WORD_1\n"
    "v_lshlrev_b32_sdwa v185, 1, v170 dst_sel:DWORD dst_unused:UNUSED_PAD src0_sel:DWORD src1_sel:WORD_1\n"
    "v_lshlrev_b32_sdwa v201, 1, v171 dst_sel:DWORD dst_unused:UNUSED_PAD src0_sel:DWORD src1_sel:WORD_1\n"
    "v_mfma_f32_16x16x32_bf16 v[168:171], a[136:139], v[144:147], 0\n"
    "ds_read_b128 a[240:243], v244 offset:36864\n"
    "ds_write_b128 v253, v[76:79] offset:4096\n"
    "v_mfma_f32_16x16x32_bf16 v[168:171], a[184:187], v[148:151], v[168:171]\n"
    "v_lshlrev_b32_sdwa v186, 1, v172 dst_sel:DWORD dst_unused:UNUSED_PAD src0_sel:DWORD src1_sel:WORD_1\n"
    "v_lshlrev_b32_sdwa v202, 1, v173 dst_sel:DWORD dst_unused:UNUSED_PAD src0_sel:DWORD src1_sel:WORD_1\n"
    "v_lshlrev_b32_sdwa v187, 1, v174 dst_sel:DWORD dst_unused:UNUSED_PAD src0_sel:DWORD src1_sel:WORD_1\n"
    "v_lshlrev_b32_sdwa v203, 1, v175 dst_sel:DWORD dst_unused:UNUSED_PAD src0_sel:DWORD src1_sel:WORD_1\n"
    "v_rcp_f16_e32 v232, v234\n"
    "v_mfma_f32_16x16x32_bf16 v[168:171], a[100:103], v[152:155], v[168:171]\n"
    "s_waitcnt lgkmcnt(5)\n"
    "ds_read_u16 v184, v184\n"
    "ds_read_u16_d16_hi v200, v200\n"
    "v_or_b32_e64 v198, v198, v206\n"
    "v_rcp_f16_sdwa v232, v234 dst_sel:WORD_1 dst_unused:UNUSED_PRESERVE src0_sel:WORD_1\n"
    "v_mfma_f32_16x16x32_bf16 v[172:175], a[140:143], v[144:147], 0\n"
    "ds_read_u16 v185, v185\n"
    "ds_read_u16_d16_hi v201, v201\n"
    "v_lshlrev_b32_sdwa v188, 1, v176 dst_sel:DWORD dst_unused:UNUSED_PAD src0_sel:DWORD src1_sel:WORD_1\n"
    "v_lshlrev_b32_sdwa v204, 1, v177 dst_sel:DWORD dst_unused:UNUSED_PAD src0_sel:DWORD src1_sel:WORD_1\n"
    "v_lshlrev_b32_sdwa v189, 1, v178 dst_sel:DWORD dst_unused:UNUSED_PAD src0_sel:DWORD src1_sel:WORD_1\n"
    "v_lshlrev_b32_sdwa v205, 1, v179 dst_sel:DWORD dst_unused:UNUSED_PAD src0_sel:DWORD src1_sel:WORD_1\n"
    "v_lshlrev_b32_sdwa v190, 1, v180 dst_sel:DWORD dst_unused:UNUSED_PAD src0_sel:DWORD src1_sel:WORD_1\n"
    "v_lshlrev_b32_sdwa v206, 1, v181 dst_sel:DWORD dst_unused:UNUSED_PAD src0_sel:DWORD src1_sel:WORD_1\n"
    "v_lshlrev_b32_sdwa v191, 1, v182 dst_sel:DWORD dst_unused:UNUSED_PAD src0_sel:DWORD src1_sel:WORD_1\n"
    "v_fma_mix_f32 v234, v192, v232, 0 op_sel_hi:[1,1,0]\n"
    "v_mfma_f32_16x16x32_bf16 v[172:175], a[188:191], v[148:151], v[172:175]\n"
    "s_waitcnt lgkmcnt(5)\n"
    "ds_read_u16 v186, v186\n"
    "ds_read_b128 v[236:239], v244 offset:20480\n"
    "v_mfma_f32_16x16x32_bf16 v[172:175], a[240:243], v[152:155], v[172:175]\n"
    "s_nop 0\n"
    "buffer_load_dwordx4 a[236:239], v245, s[40:43], s100 offen offset:64\n"
    "buffer_load_dwordx4 a[228:231], v245, s[40:43], s100 offen offset:0\n"
    "ds_read_u16_d16_hi v202, v202\n"
    "v_or_b32_e64 v199, v199, v207\n"
    "v_lshlrev_b32_sdwa v207, 1, v183 dst_sel:DWORD dst_unused:UNUSED_PAD src0_sel:DWORD src1_sel:WORD_1\n"
    "v_fma_mix_f32 v232, v192, v232, 0 op_sel:[1,1,0] op_sel_hi:[1,1,0]\n"
    "v_mfma_f32_16x16x32_bf16 v[176:179], a[136:139], v[156:159], 0\n"
    "ds_read_u16 v187, v187\n"
    "v_cvt_pk_bf16_f32 v192, v234, v232\n"
    "v_mfma_f32_16x16x32_bf16 v[176:179], a[184:187], v[160:163], v[176:179]\n"
    "v_pk_fma_f16 v232, v196, v196, s48\n"
    "v_mov_b32_e64 v254, 0\n"
    "v_mfma_f32_16x16x32_bf16 v[176:179], a[100:103], v[164:167], v[176:179]\n"
    "ds_read_b128 a[244:247], v244 offset:22528\n"
    "v_pk_mul_f16 v232, v196, v232\n"
    "v_mfma_f32_16x16x32_bf16 v[180:183], a[140:143], v[156:159], 0\n"
    "ds_read_u16_d16_hi v203, v203\n"
    "v_exp_f16_e32 v234, v232\n"
    "v_mfma_f32_16x16x32_bf16 v[180:183], a[188:191], v[160:163], v[180:183]\n"
    "s_waitcnt lgkmcnt(8)\n"
    "ds_read_u16 v188, v188\n"
    "v_exp_f16_sdwa v234, v232 dst_sel:WORD_1 dst_unused:UNUSED_PRESERVE src0_sel:WORD_1\n"
    "v_mfma_f32_16x16x32_bf16 v[180:183], a[240:243], v[164:167], v[180:183]\n"
    "v_pk_fma_f16 v234, v234, s49, s49\n"
    "v_or_b32_e64 v184, v184, v200\n"
    "v_mfma_f32_16x16x32_bf16 v[96:99], a[80:83], v[192:195], v[96:99]\n"
    "ds_read_u16_d16_hi v204, v204\n"
    "v_rcp_f16_e32 v232, v234\n"
    "v_mfma_f32_16x16x32_bf16 v[100:103], a[84:87], v[192:195], v[100:103]\n"
    "s_waitcnt lgkmcnt(8)\n"
    "ds_read_u16 v189, v189\n"
    "v_rcp_f16_sdwa v232, v234 dst_sel:WORD_1 dst_unused:UNUSED_PRESERVE src0_sel:WORD_1\n"
    "v_mfma_f32_16x16x32_bf16 v[112:115], a[88:91], v[192:195], v[112:115]\n"
    "v_fma_mix_f32 v234, v196, v232, 0 op_sel_hi:[1,1,0]\n"
    "v_fma_mix_f32 v232, v196, v232, 0 op_sel:[1,1,0] op_sel_hi:[1,1,0]\n"
    "v_mfma_f32_16x16x32_bf16 v[116:119], a[92:95], v[192:195], v[116:119]\n"
    "s_waitcnt lgkmcnt(7)\n"
    "ds_read_u16_d16_hi v205, v205\n"
    "v_cvt_pk_bf16_f32 v196, v234, v232\n"
    "v_mfma_f32_16x16x32_bf16 v[128:131], v[236:239], v[192:195], v[128:131]\n"
    "v_or_b32_e32 v185, v185, v201\n"
    "v_lshlrev_b32_sdwa v201, 1, v171 dst_sel:DWORD dst_unused:UNUSED_PAD src0_sel:DWORD src1_sel:WORD_1\n"
    "v_mfma_f32_16x16x32_bf16 v[104:107], a[80:83], v[196:199], v[104:107]\n"
    "s_waitcnt lgkmcnt(7)\n"
    "ds_read_u16 v190, v190\n"
    "v_or_b32_e32 v186, v186, v202\n"
    "v_mfma_f32_16x16x32_bf16 v[108:111], a[84:87], v[196:199], v[108:111]\n"
    "ds_read_u16_d16_hi v206, v206\n"
    "v_lshlrev_b32_sdwa v202, 1, v173 dst_sel:DWORD dst_unused:UNUSED_PAD src0_sel:DWORD src1_sel:WORD_1\n"
    "s_waitcnt lgkmcnt(7)\n"
    "v_mfma_f32_16x16x32_bf16 v[132:135], a[244:247], v[192:195], v[132:135]\n"
    "s_nop 0\n"
    "v_cvt_pk_f16_f32 v192, v168, v169\n"
    "v_lshlrev_b32_sdwa v193, 1, v170 dst_sel:DWORD dst_unused:UNUSED_PAD src0_sel:DWORD src1_sel:WORD_1\n"
    "v_mfma_f32_16x16x32_bf16 v[120:123], a[88:91], v[196:199], v[120:123]\n"
    "ds_read_u16 v191, v191\n"
    "v_lshlrev_b32_sdwa v194, 1, v172 dst_sel:DWORD dst_unused:UNUSED_PAD src0_sel:DWORD src1_sel:WORD_1\n"
    "v_mfma_f32_16x16x32_bf16 v[124:127], a[92:95], v[196:199], v[124:127]\n"
    "v_lshlrev_b32_sdwa v195, 1, v174 dst_sel:DWORD dst_unused:UNUSED_PAD src0_sel:DWORD src1_sel:WORD_1\n"
    "s_waitcnt lgkmcnt(7)\n"
    "v_or_b32_e32 v187, v187, v203\n"
    "v_mfma_f32_16x16x32_bf16 v[136:139], v[236:239], v[196:199], v[136:139]\n"
    "ds_read_u16_d16_hi v207, v207\n"
    "v_lshlrev_b32_sdwa v203, 1, v175 dst_sel:DWORD dst_unused:UNUSED_PAD src0_sel:DWORD src1_sel:WORD_1\n"
    "v_mfma_f32_16x16x32_bf16 v[140:143], a[244:247], v[196:199], v[140:143]\n"
    "ds_read_u16 v193, v193\n"
    "v_cvt_pk_f16_f32 v196, v176, v177\n"
    "v_mfma_f32_16x16x32_bf16 v[168:171], a[144:147], v[144:147], 0\n"
    "v_lshlrev_b32_sdwa v197, 1, v178 dst_sel:DWORD dst_unused:UNUSED_PAD src0_sel:DWORD src1_sel:WORD_1\n"
    "v_lshlrev_b32_sdwa v198, 1, v180 dst_sel:DWORD dst_unused:UNUSED_PAD src0_sel:DWORD src1_sel:WORD_1\n"
    "v_mfma_f32_16x16x32_bf16 v[168:171], a[192:195], v[148:151], v[168:171]\n"
    "s_waitcnt lgkmcnt(7)\n"
    "ds_read_u16_d16_hi v201, v201\n"
    "v_lshlrev_b32_sdwa v199, 1, v182 dst_sel:DWORD dst_unused:UNUSED_PAD src0_sel:DWORD src1_sel:WORD_1\n"
    "v_mfma_f32_16x16x32_bf16 v[168:171], a[104:107], v[152:155], v[168:171]\n"
    "v_or_b32_e32 v188, v188, v204\n"
    "v_cvt_pk_bf16_f32 v96, v96, v97\n"
    "v_mfma_f32_16x16x32_bf16 v[172:175], a[148:151], v[144:147], 0\n"
    "ds_read_u16 v194, v194\n"
    "v_cvt_pk_bf16_f32 v97, v98, v99\n"
    "v_mfma_f32_16x16x32_bf16 v[172:175], a[196:199], v[148:151], v[172:175]\n"
    "s_waitcnt lgkmcnt(7)\n"
    "ds_read_u16_d16_hi v202, v202\n"
    "v_or_b32_e32 v189, v189, v205\n"
    "v_mfma_f32_16x16x32_bf16 v[172:175], a[108:111], v[152:155], v[172:175]\n"
    "v_lshlrev_b32_sdwa v205, 1, v179 dst_sel:DWORD dst_unused:UNUSED_PAD src0_sel:DWORD src1_sel:WORD_1\n"
    "v_cvt_pk_bf16_f32 v98, v100, v101\n"
    "v_mfma_f32_16x16x32_bf16 v[176:179], a[144:147], v[156:159], 0\n"
    "ds_read_b128 v[236:239], v244 offset:0\n"
    "v_cvt_pk_bf16_f32 v99, v102, v103\n"
    "v_mfma_f32_16x16x32_bf16 v[176:179], a[192:195], v[160:163], v[176:179]\n"
    "ds_read_u16 v195, v195\n"
    "v_cvt_pk_bf16_f32 v112, v112, v113\n"
    "v_mfma_f32_16x16x32_bf16 v[176:179], a[104:107], v[164:167], v[176:179]\n"
    "s_waitcnt lgkmcnt(8)\n"
    "v_or_b32_e32 v190, v190, v206\n"
    "v_lshlrev_b32_sdwa v206, 1, v181 dst_sel:DWORD dst_unused:UNUSED_PAD src0_sel:DWORD src1_sel:WORD_1\n"
    "v_mfma_f32_16x16x32_bf16 v[100:103], a[4:7], v[184:187], 0\n"
    "ds_read_u16_d16_hi v203, v203\n"
    "v_cvt_pk_bf16_f32 v113, v114, v115\n"
    "v_cvt_pk_bf16_f32 v114, v116, v117\n"
    "v_cvt_pk_bf16_f32 v115, v118, v119\n"
    "v_cvt_pk_bf16_f32 v128, v128, v129\n"
    "s_waitcnt vmcnt(23)\n"
    "ds_write2_b32 v248, v0, v1 offset0:0 offset1:20\n"
    "v_cvt_pk_bf16_f32 v129, v130, v131\n"
    "s_waitcnt lgkmcnt(8)\n"
    "v_or_b32_e64 v191, v191, v207\n"
    "v_mfma_f32_16x16x32_bf16 v[116:119], a[12:15], v[184:187], 0\n"
    "ds_read_b128 a[244:247], v244 offset:2048\n"
    "ds_write2_b32 v248, v2, v3 offset0:40 offset1:60\n"
    "buffer_load_dwordx4 v[64:67], v246, s[40:43], s60 offen offset:128\n"
    "v_add_u32_e32 v248, 1360, v248\n"
    "v_lshlrev_b32_sdwa v207, 1, v183 dst_sel:DWORD dst_unused:UNUSED_PAD src0_sel:DWORD src1_sel:WORD_1\n"
    "v_mfma_f32_16x16x32_bf16 v[180:183], a[148:151], v[156:159], 0\n"
    "s_waitcnt lgkmcnt(8)\n"
    "ds_read_u16 v197, v197\n"
    "s_waitcnt vmcnt(22)\n"
    "ds_write2_b32 v235, v20, v21 offset0:0 offset1:20\n"
    "v_mfma_f32_16x16x32_bf16 v[180:183], a[196:199], v[160:163], v[180:183]\n"
    "v_cvt_pk_bf16_f32 v130, v132, v133\n"
    "v_or_b32_e64 v193, v193, v201\n"
    "v_mfma_f32_16x16x32_bf16 v[180:183], a[108:111], v[164:167], v[180:183]\n"
    "ds_read_u16_d16_hi v205, v205\n"
    "ds_write2_b32 v235, v22, v23 offset0:40 offset1:60\n"
    "v_add_u32_e32 v235, 1360, v235\n"
    "v_cvt_pk_bf16_f32 v131, v134, v135\n"
    "v_cvt_pk_bf16_f32 v104, v104, v105\n"
    "v_cvt_pk_bf16_f32 v105, v106, v107\n"
    "v_cvt_pk_bf16_f32 v106, v108, v109\n"
    "s_waitcnt vmcnt(20)\n"
    "ds_write2_b32 v248, v4, v5 offset0:0 offset1:20\n"
    "v_cvt_pk_bf16_f32 v107, v110, v111\n"
    "v_mfma_f32_16x16x32_bf16 v[108:111], a[4:7], v[188:191], 0\n"
    "s_waitcnt lgkmcnt(9)\n"
    "v_or_b32_e64 v194, v194, v202\n"
    "ds_write2_b32 v248, v6, v7 offset0:40 offset1:60\n"
    "v_add_u32_e32 v248, 1360, v248\n"
    "s_waitcnt lgkmcnt(7)\n"
    "v_mfma_f32_16x16x32_bf16 v[132:135], a[244:247], v[184:187], 0\n"
    "s_nop 0\n"
    "ds_read_u16 v198, v198\n"
    "ds_write_b128 v242, v[96:99] offset:24640\n"
    "v_mfma_f32_16x16x32_bf16 v[96:99], a[0:3], v[184:187], 0\n"
    "v_cvt_pk_bf16_f32 v120, v120, v121\n"
    "v_cvt_pk_bf16_f32 v121, v122, v123\n"
    "v_cvt_pk_bf16_f32 v122, v124, v125\n"
    "v_cvt_pk_bf16_f32 v123, v126, v127\n"
    "v_cvt_pk_bf16_f32 v136, v136, v137\n"
    "v_pk_fma_f16 v232, v192, v192, s48\n"
    "v_mfma_f32_16x16x32_bf16 v[124:127], a[12:15], v[188:191], 0\n"
    "s_nop 0\n"
    "ds_read_u16_d16_hi v206, v206\n"
    "s_waitcnt vmcnt(18)\n"
    "ds_write2_b32 v235, v24, v25 offset0:0 offset1:20\n"
    "v_cvt_pk_bf16_f32 v137, v138, v139\n"
    "v_cvt_pk_bf16_f32 v138, v140, v141\n"
    "v_or_b32_e64 v195, v195, v203\n"
    "v_cvt_pk_bf16_f32 v139, v142, v143\n"
    "ds_write_b128 v242, v[112:115] offset:26688\n"
    "v_mfma_f32_16x16x32_bf16 v[112:115], a[8:11], v[184:187], 0\n"
    "s_waitcnt lgkmcnt(5)\n"
    "v_pk_mul_f16 v232, v192, v232\n"
    "v_or_b32_e32 v197, v197, v205\n"
    "v_exp_f16_e64 v234, v232\n"
    "v_mfma_f32_16x16x32_bf16 v[140:143], a[244:247], v[188:191], 0\n"
    "ds_read_u16 v199, v199\n"
    "ds_write2_b32 v235, v26, v27 offset0:40 offset1:60\n"
    "v_add_u32_e32 v235, -1360, v235\n"
    "v_exp_f16_sdwa v234, v232 dst_sel:WORD_1 dst_unused:UNUSED_PRESERVE src0_sel:WORD_1\n"
    "v_lshlrev_b32_sdwa v201, 1, v171 dst_sel:DWORD dst_unused:UNUSED_PAD src0_sel:DWORD src1_sel:WORD_1\n"
    "v_pk_fma_f16 v234, v234, s49, s49\n"
    "ds_write_b128 v242, v[128:131] offset:28736\n"
    "buffer_load_dwordx4 v[68:71], v246, s[40:43], s61 offen offset:128\n"
    "v_mfma_f32_16x16x32_bf16 v[128:131], v[236:239], v[184:187], 0\n"
    "s_waitcnt lgkmcnt(4)\n"
    "v_cvt_pk_f16_f32 v184, v168, v169\n"
    "v_lshlrev_b32_sdwa v185, 1, v170 dst_sel:DWORD dst_unused:UNUSED_PAD src0_sel:DWORD src1_sel:WORD_1\n"
    "v_mfma_f32_16x16x32_bf16 v[168:171], a[152:155], v[144:147], 0\n"
    "s_nop 0\n"
    "ds_read_u16_d16_hi v207, v207\n"
    "ds_write_b128 v242, v[104:107] offset:25664\n"
    "v_mfma_f32_16x16x32_bf16 v[104:107], a[0:3], v[188:191], 0\n"
    "v_lshlrev_b32_sdwa v186, 1, v172 dst_sel:DWORD dst_unused:UNUSED_PAD src0_sel:DWORD src1_sel:WORD_1\n"
    "v_lshlrev_b32_sdwa v202, 1, v173 dst_sel:DWORD dst_unused:UNUSED_PAD src0_sel:DWORD src1_sel:WORD_1\n"
    "v_mfma_f32_16x16x32_bf16 v[168:171], a[200:203], v[148:151], v[168:171]\n"
    "v_lshlrev_b32_sdwa v187, 1, v174 dst_sel:DWORD dst_unused:UNUSED_PAD src0_sel:DWORD src1_sel:WORD_1\n"
    "v_lshlrev_b32_sdwa v203, 1, v175 dst_sel:DWORD dst_unused:UNUSED_PAD src0_sel:DWORD src1_sel:WORD_1\n"
    "v_rcp_f16_e64 v232, v234\n"
    "v_mfma_f32_16x16x32_bf16 v[168:171], a[112:115], v[152:155], v[168:171]\n"
    "ds_read_b128 v[216:219], v251 offset:0\n"
    "s_waitcnt vmcnt(16)\n"
    "ds_write2_b32 v248, v8, v9 offset0:0 offset1:20\n"
    "v_or_b32_e32 v198, v198, v206\n"
    "v_rcp_f16_sdwa v232, v234 dst_sel:WORD_1 dst_unused:UNUSED_PRESERVE src0_sel:WORD_1\n"
    "v_mfma_f32_16x16x32_bf16 v[172:175], a[156:159], v[144:147], 0\n"
    "ds_read_b128 v[220:223], v251 offset:336\n"
    "ds_write_b128 v242, v[120:123] offset:27712\n"
    "v_mfma_f32_16x16x32_bf16 v[120:123], a[8:11], v[188:191], 0\n"
    "v_lshlrev_b32_sdwa v205, 1, v179 dst_sel:DWORD dst_unused:UNUSED_PAD src0_sel:DWORD src1_sel:WORD_1\n"
    "v_lshlrev_b32_sdwa v206, 1, v181 dst_sel:DWORD dst_unused:UNUSED_PAD src0_sel:DWORD src1_sel:WORD_1\n"
    "v_mfma_f32_16x16x32_bf16 v[172:175], a[204:207], v[148:151], v[172:175]\n"
    "s_waitcnt lgkmcnt(4)\n"
    "v_fma_mix_f32 v234, v192, v232, 0 op_sel_hi:[1,1,0]\n"
    "v_or_b32_e32 v199, v199, v207\n"
    "v_mfma_f32_16x16x32_bf16 v[172:175], a[116:119], v[152:155], v[172:175]\n"
    "ds_read_u16 v185, v185\n"
    "ds_write2_b32 v248, v10, v11 offset0:40 offset1:60\n"
    "v_add_u32_e32 v248, 1360, v248\n"
    "v_lshlrev_b32_sdwa v207, 1, v183 dst_sel:DWORD dst_unused:UNUSED_PAD src0_sel:DWORD src1_sel:WORD_1\n"
    "v_fma_mix_f32 v232, v192, v232, 0 op_sel:[1,1,0] op_sel_hi:[1,1,0]\n"
    "v_lshlrev_b32_sdwa v200, 1, v169 dst_sel:DWORD dst_unused:UNUSED_PAD src0_sel:DWORD src1_sel:WORD_1\n"
    "v_cvt_pk_bf16_f32 v192, v234, v232\n"
    "ds_write_b128 v242, v[136:139] offset:29760\n"
    "v_mfma_f32_16x16x32_bf16 v[136:139], v[236:239], v[188:191], 0\n"
    "v_cvt_pk_f16_f32 v188, v176, v177\n"
    "v_lshlrev_b32_sdwa v189, 1, v178 dst_sel:DWORD dst_unused:UNUSED_PAD src0_sel:DWORD src1_sel:WORD_1\n"
    "v_mfma_f32_16x16x32_bf16 v[176:179], a[152:155], v[156:159], 0\n"
    "ds_read_u16_d16_hi v201, v201\n"
    "s_waitcnt vmcnt(13)\n"
    "ds_write2_b32 v248, v12, v13 offset0:0 offset1:20\n"
    "v_mfma_f32_16x16x32_bf16 v[176:179], a[200:203], v[160:163], v[176:179]\n"
    "s_nop 0\n"
    "v_lshlrev_b32_sdwa v190, 1, v180 dst_sel:DWORD dst_unused:UNUSED_PAD src0_sel:DWORD src1_sel:WORD_1\n"
    "v_lshlrev_b32_sdwa v191, 1, v182 dst_sel:DWORD dst_unused:UNUSED_PAD src0_sel:DWORD src1_sel:WORD_1\n"
    "v_mfma_f32_16x16x32_bf16 v[176:179], a[112:115], v[164:167], v[176:179]\n"
    "ds_read_u16 v186, v186\n"
    "ds_write2_b32 v248, v14, v15 offset0:40 offset1:60\n"
    "v_add_u32_e32 v248, 1360, v248\n"
    "v_pk_fma_f16 v232, v196, v196, s48\n"
    "v_mfma_f32_16x16x32_bf16 v[180:183], a[156:159], v[156:159], 0\n"
    "buffer_load_dwordx4 v[72:75], v246, s[40:43], s62 offen offset:128\n"
    "s_waitcnt vmcnt(11)\n"
    "ds_write2_b32 v248, v16, v17 offset0:0 offset1:20\n"
    "v_pk_mul_f16 v232, v196, v232\n"
    "v_mfma_f32_16x16x32_bf16 v[180:183], a[204:207], v[160:163], v[180:183]\n"
    "v_exp_f16_e32 v234, v232\n"
    "v_mfma_f32_16x16x32_bf16 v[180:183], a[116:119], v[164:167], v[180:183]\n"
    "ds_read_u16_d16_hi v202, v202\n"
    "ds_write2_b32 v248, v18, v19 offset0:40 offset1:60\n"
    "v_mfma_f32_32x32x1_2b_f32 v[0:31], v254, v254, 0\n"
    "v_add_u32_e32 v248, -5440, v248\n"
    "v_exp_f16_sdwa v234, v232 dst_sel:WORD_1 dst_unused:UNUSED_PRESERVE src0_sel:WORD_1\n"
    "s_waitcnt lgkmcnt(5)\n"
    "v_or_b32_e32 v185, v185, v201\n"
    "v_pk_fma_f16 v234, v234, s49, s49\n"
    "v_lshlrev_b32_sdwa v201, 1, v171 dst_sel:DWORD dst_unused:UNUSED_PAD src0_sel:DWORD src1_sel:WORD_1\n"
    "v_rcp_f16_e64 v232, v234\n"
    "v_lshlrev_b32_sdwa v204, 1, v177 dst_sel:DWORD dst_unused:UNUSED_PAD src0_sel:DWORD src1_sel:WORD_1\n"
    "v_rcp_f16_sdwa v232, v234 dst_sel:WORD_1 dst_unused:UNUSED_PRESERVE src0_sel:WORD_1\n"
    "v_mov_b32_e64 v254, 0\n"
    "v_fma_mix_f32 v234, v196, v232, 0 op_sel_hi:[1,1,0]\n"
    "v_mfma_f32_16x16x32_bf16 v[96:99], a[16:19], v[192:195], v[96:99]\n"
    "ds_read_b128 v[236:239], v244 offset:4096\n"
    "ds_read_u16 v187, v187\n"
    "v_mfma_f32_16x16x32_bf16 v[100:103], a[20:23], v[192:195], v[100:103]\n"
    "ds_read_u16_d16_hi v203, v203\n"
    "ds_read_b128 a[244:247], v244 offset:6144\n"
    "v_mfma_f32_16x16x32_bf16 v[112:115], a[24:27], v[192:195], v[112:115]\n"
    "ds_read_u16 v189, v189\n"
    "v_fma_mix_f32 v232, v196, v232, 0 op_sel:[1,1,0] op_sel_hi:[1,1,0]\n"
    "v_mfma_f32_16x16x32_bf16 v[116:119], a[28:31], v[192:195], v[116:119]\n"
    "v_cvt_pk_bf16_f32 v196, v234, v232\n"
    "v_pk_fma_f16 v232, v184, v184, s48\n"
    "s_waitcnt lgkmcnt(5)\n"
    "s_barrier\n"
    "v_mfma_f32_16x16x32_bf16 v[104:107], a[16:19], v[196:199], v[104:107]\n"
    "ds_read_b128 v[208:211], v250 offset:0\n"
    "ds_read_b128 v[212:215], v250 offset:320\n"
    "v_mfma_f32_16x16x32_bf16 v[108:111], a[20:23], v[196:199], v[108:111]\n"
    "ds_read_u16_d16_hi v205, v205\n"
    "ds_read_u16 v190, v190\n"
    "v_mfma_f32_16x16x32_bf16 v[120:123], a[24:27], v[196:199], v[120:123]\n"
    "s_waitcnt lgkmcnt(8)\n"
    "v_or_b32_e32 v186, v186, v202\n"
    "v_pk_mul_f16 v232, v184, v232\n"
    "v_mfma_f32_16x16x32_bf16 v[128:131], v[236:239], v[192:195], v[128:131]\n"
    "ds_read_u16_d16_hi v206, v206\n"
    "v_exp_f16_e64 v234, v232\n"
    "v_mfma_f32_16x16x32_bf16 v[124:127], a[28:31], v[196:199], v[124:127]\n"
    "v_exp_f16_sdwa v234, v232 dst_sel:WORD_1 dst_unused:UNUSED_PRESERVE src0_sel:WORD_1\n"
    "v_mfma_f32_16x16x32_bf16 v[136:139], v[236:239], v[196:199], v[136:139]\n"
    "ds_read_u16 v191, v191\n"
    "v_pk_fma_f16 v234, v234, s49, s49\n"
    "v_lshlrev_b32_sdwa v202, 1, v173 dst_sel:DWORD dst_unused:UNUSED_PAD src0_sel:DWORD src1_sel:WORD_1\n"
    "v_rcp_f16_e32 v232, v234\n"
    "s_waitcnt lgkmcnt(7)\n"
    "v_mfma_f32_16x16x32_bf16 v[132:135], a[244:247], v[192:195], v[132:135]\n"
    "ds_read_u16_d16_hi v207, v207\n"
    "v_or_b32_e64 v187, v187, v203\n"
    "v_mfma_f32_16x16x32_bf16 v[140:143], a[244:247], v[196:199], v[140:143]\n"
    "ds_read_b128 a[240:243], v244 offset:34816\n"
    "v_lshlrev_b32_sdwa v192, 1, v168 dst_sel:DWORD dst_unused:UNUSED_PAD src0_sel:DWORD src1_sel:WORD_1\n"
    "v_lshlrev_b32_sdwa v193, 1, v170 dst_sel:DWORD dst_unused:UNUSED_PAD src0_sel:DWORD src1_sel:WORD_1\n"
    "v_mfma_f32_16x16x32_bf16 v[168:171], a[160:163], v[144:147], 0\n"
    "s_nop 0\n"
    "buffer_load_dwordx4 v[76:79], v246, s[40:43], s63 offen offset:128\n"
    "ds_read_u16 v192, v192\n"
    "v_mfma_f32_16x16x32_bf16 v[168:171], a[208:211], v[148:151], v[168:171]\n"
    "s_waitcnt lgkmcnt(7)\n"
    "v_lshlrev_b32_sdwa v194, 1, v172 dst_sel:DWORD dst_unused:UNUSED_PAD src0_sel:DWORD src1_sel:WORD_1\n"
    "v_lshlrev_b32_sdwa v195, 1, v174 dst_sel:DWORD dst_unused:UNUSED_PAD src0_sel:DWORD src1_sel:WORD_1\n"
    "v_smfmac_f32_16x16x64_bf16 v[0:3], v[216:219], v[208:215], v240\n"
    "ds_read_b128 v[216:219], v252 offset:0\n"
    "ds_read_u16_d16_hi v200, v200\n"
    "v_smfmac_f32_16x16x64_bf16 v[4:7], v[220:223], v[208:215], v240 abid:1\n"
    "v_lshlrev_b32_sdwa v203, 1, v175 dst_sel:DWORD dst_unused:UNUSED_PAD src0_sel:DWORD src1_sel:WORD_1\n"
    "v_mfma_f32_16x16x32_bf16 v[168:171], a[120:123], v[152:155], v[168:171]\n"
    "s_waitcnt lgkmcnt(8)\n"
    "ds_read_b128 v[208:211], v250 offset:640\n"
    "v_or_b32_e32 v189, v189, v205\n"
    "v_rcp_f16_sdwa v232, v234 dst_sel:WORD_1 dst_unused:UNUSED_PRESERVE src0_sel:WORD_1\n"
    "v_mfma_f32_16x16x32_bf16 v[172:175], a[164:167], v[144:147], 0\n"
    "ds_read_b128 v[212:215], v250 offset:960\n"
    "ds_read_b128 v[220:223], v252 offset:336\n"
    "v_lshlrev_b32_sdwa v196, 1, v176 dst_sel:DWORD dst_unused:UNUSED_PAD src0_sel:DWORD src1_sel:WORD_1\n"
    "v_lshlrev_b32_sdwa v197, 1, v178 dst_sel:DWORD dst_unused:UNUSED_PAD src0_sel:DWORD src1_sel:WORD_1\n"
    "v_lshlrev_b32_sdwa v205, 1, v179 dst_sel:DWORD dst_unused:UNUSED_PAD src0_sel:DWORD src1_sel:WORD_1\n"
    "v_lshlrev_b32_sdwa v198, 1, v180 dst_sel:DWORD dst_unused:UNUSED_PAD src0_sel:DWORD src1_sel:WORD_1\n"
    "s_waitcnt lgkmcnt(9)\n"
    "v_or_b32_e32 v190, v190, v206\n"
    "v_lshlrev_b32_sdwa v206, 1, v181 dst_sel:DWORD dst_unused:UNUSED_PAD src0_sel:DWORD src1_sel:WORD_1\n"
    "v_lshlrev_b32_sdwa v199, 1, v182 dst_sel:DWORD dst_unused:UNUSED_PAD src0_sel:DWORD src1_sel:WORD_1\n"
    "v_fma_mix_f32 v234, v184, v232, 0 op_sel_hi:[1,1,0]\n"
    "v_mfma_f32_16x16x32_bf16 v[172:175], a[212:215], v[148:151], v[172:175]\n"
    "ds_read_u16 v193, v193\n"
    "ds_read_u16_d16_hi v201, v201\n"
    "v_fma_mix_f32 v232, v184, v232, 0 op_sel:[1,1,0] op_sel_hi:[1,1,0]\n"
    "v_mfma_f32_16x16x32_bf16 v[176:179], a[160:163], v[156:159], 0\n"
    "v_cvt_pk_bf16_f32 v184, v234, v232\n"
    "v_pk_fma_f16 v232, v188, v188, s48\n"
    "s_waitcnt lgkmcnt(8)\n"
    "v_mfma_f32_16x16x32_bf16 v[172:175], a[240:243], v[152:155], v[172:175]\n"
    "s_nop 0\n"
    "ds_read_u16 v194, v194\n"
    "v_or_b32_e64 v191, v191, v207\n"
    "v_mfma_f32_16x16x32_bf16 v[176:179], a[208:211], v[160:163], v[176:179]\n"
    "ds_read_b128 v[236:239], v244 offset:8192\n"
    "v_lshlrev_b32_sdwa v207, 1, v183 dst_sel:DWORD dst_unused:UNUSED_PAD src0_sel:DWORD src1_sel:WORD_1\n"
    "v_mfma_f32_16x16x32_bf16 v[176:179], a[120:123], v[164:167], v[176:179]\n"
    "ds_read_u16_d16_hi v202, v202\n"
    "v_pk_mul_f16 v232, v188, v232\n"
    "v_mfma_f32_16x16x32_bf16 v[180:183], a[164:167], v[156:159], 0\n"
    "v_exp_f16_e32 v234, v232\n"
    "v_mfma_f32_16x16x32_bf16 v[180:183], a[212:215], v[160:163], v[180:183]\n"
    "ds_read_u16 v195, v195\n"
    "v_exp_f16_sdwa v234, v232 dst_sel:WORD_1 dst_unused:UNUSED_PRESERVE src0_sel:WORD_1\n"
    "v_mfma_f32_16x16x32_bf16 v[180:183], a[240:243], v[164:167], v[180:183]\n"
    "s_waitcnt lgkmcnt(8)\n"
    "ds_read_b128 a[244:247], v244 offset:10240\n"
    "v_pk_fma_f16 v234, v234, s49, s49\n"
    "s_waitcnt lgkmcnt(8)\n"
    "v_smfmac_f32_16x16x64_bf16 v[0:3], v[216:219], v[208:215], v240\n"
    "s_waitcnt lgkmcnt(7)\n"
    "ds_read_b128 v[216:219], v251 offset:672\n"
    "v_smfmac_f32_16x16x64_bf16 v[4:7], v[220:223], v[208:215], v240 abid:1\n"
    "ds_read_b128 v[208:211], v250 offset:80\n"
    "s_waitcnt vmcnt(5)\n"
    "v_smfmac_f32_32x32x32_bf16 v[112:127], a[248:251], a[232:239], v241\n"
    "s_nop 0\n"
    "ds_read_b128 v[212:215], v250 offset:400\n"
    "ds_read_b128 v[220:223], v251 offset:1008\n"
    "v_rcp_f16_e64 v232, v234\n"
    "v_or_b32_e64 v192, v192, v200\n"
    "v_rcp_f16_sdwa v232, v234 dst_sel:WORD_1 dst_unused:UNUSED_PRESERVE src0_sel:WORD_1\n"
    "s_waitcnt vmcnt(4)\n"
    "v_smfmac_f32_32x32x32_bf16 v[96:111], a[248:251], a[224:231], v241\n"
    "s_waitcnt lgkmcnt(9)\n"
    "buffer_load_dwordx4 a[224:227], v245, s[40:43], s57 offen offset:128\n"
    "ds_read_b128 a[232:235], v243 offset:24640\n"
    "v_fma_mix_f32 v234, v188, v232, 0 op_sel_hi:[1,1,0]\n"
    "v_fma_mix_f32 v232, v188, v232, 0 op_sel:[1,1,0] op_sel_hi:[1,1,0]\n"
    "v_or_b32_e64 v193, v193, v201\n"
    "v_cvt_pk_bf16_f32 v188, v234, v232\n"
    "ds_read_b128 a[236:239], v243 offset:25664\n"
    "ds_read_u16_d16_hi v203, v203\n"
    "v_pk_mov_b32 v[224:225], v[0:1], v[0:1] op_sel:[0,1]\n"
    "v_pk_mov_b32 v[226:227], v[2:3], v[2:3] op_sel:[0,1]\n"
    "v_pk_mul_f32 v[228:229], v[0:1], v[0:1]\n"
    "v_pk_mul_f32 v[230:231], v[2:3], v[2:3]\n"
    "v_pk_add_f32 v[224:225], v[224:225], v[4:5]\n"
    "v_pk_add_f32 v[226:227], v[226:227], v[6:7]\n"
    "v_pk_fma_f32 v[228:229], v[4:5], v[4:5], v[228:229]\n"
    "v_pk_fma_f32 v[230:231], v[6:7], v[6:7], v[230:231]\n"
    "v_mfma_f32_16x16x32_bf16 v[112:115], a[40:43], v[184:187], v[112:115]\n"
    "s_waitcnt lgkmcnt(9)\n"
    "ds_read_u16 v196, v196\n"
    "ds_read_u16_d16_hi v204, v204\n"
    "v_mfma_f32_16x16x32_bf16 v[116:119], a[44:47], v[184:187], v[116:119]\n"
    "s_waitcnt lgkmcnt(9)\n"
    "ds_read_u16 v197, v197\n"
    "v_lshlrev_b32_sdwa v201, 1, v171 dst_sel:DWORD dst_unused:UNUSED_PAD src0_sel:DWORD src1_sel:WORD_1\n"
    "v_mfma_f32_16x16x32_bf16 v[128:131], v[236:239], v[184:187], v[128:131]\n"
    "v_or_b32_e64 v194, v194, v202\n"
    "v_lshlrev_b32_sdwa v202, 1, v173 dst_sel:DWORD dst_unused:UNUSED_PAD src0_sel:DWORD src1_sel:WORD_1\n"
    "v_mfma_f32_16x16x32_bf16 v[132:135], a[244:247], v[184:187], v[132:135]\n"
    "ds_read_u16_d16_hi v205, v205\n"
    "v_mfma_f32_16x16x32_bf16 v[120:123], a[40:43], v[188:191], v[120:123]\n"
    "s_waitcnt lgkmcnt(8)\n"
    "v_smfmac_f32_16x16x64_bf16 v[8:11], v[216:219], v[208:215], v240\n"
    "s_waitcnt lgkmcnt(7)\n"
    "ds_read_b128 v[216:219], v252 offset:672\n"
    "buffer_load_dwordx4 v[80:83], v246, s[40:43], s52 offen offset:128\n"
    "v_smfmac_f32_16x16x64_bf16 v[12:15], v[220:223], v[208:215], v240 abid:1\n"
    "s_waitcnt lgkmcnt(6)\n"
    "ds_read_b128 v[208:211], v250 offset:720\n"
    "ds_read_b128 v[212:215], v250 offset:1040\n"
    "v_smfmac_f32_32x32x32_bf16 v[96:111], a[248:251], a[232:239], v241\n"
    "ds_read_b128 v[220:223], v252 offset:1008\n"
    "v_mfma_f32_16x16x32_bf16 v[124:127], a[44:47], v[188:191], v[124:127]\n"
    "s_waitcnt lgkmcnt(8)\n"
    "ds_read_u16 v198, v198\n"
    "v_or_b32_e64 v195, v195, v203\n"
    "v_mfma_f32_16x16x32_bf16 v[136:139], v[236:239], v[188:191], v[136:139]\n"
    "ds_read_u16_d16_hi v206, v206\n"
    "v_lshlrev_b32_sdwa v203, 1, v175 dst_sel:DWORD dst_unused:UNUSED_PAD src0_sel:DWORD src1_sel:WORD_1\n"
    "v_mfma_f32_16x16x32_bf16 v[140:143], a[244:247], v[188:191], v[140:143]\n"
    "buffer_load_dwordx4 a[228:231], v245, s[40:43], s100 offen offset:128\n"
    "ds_read_u16 v199, v199\n"
    "v_mfma_f32_16x16x32_bf16 v[96:99], a[32:35], v[184:187], v[96:99]\n"
    "s_nop 0\n"
    "v_mfma_f32_16x16x32_bf16 v[100:103], a[36:39], v[184:187], v[100:103]\n"
    "s_waitcnt lgkmcnt(9)\n"
    "ds_read_u16_d16_hi v207, v207\n"
    "v_cvt_pk_f16_f32 v184, v168, v169\n"
    "v_mfma_f32_16x16x32_bf16 v[104:107], a[32:35], v[188:191], v[104:107]\n"
    "v_lshlrev_b32_sdwa v185, 1, v170 dst_sel:DWORD dst_unused:UNUSED_PAD src0_sel:DWORD src1_sel:WORD_1\n"
    "v_lshlrev_b32_sdwa v186, 1, v172 dst_sel:DWORD dst_unused:UNUSED_PAD src0_sel:DWORD src1_sel:WORD_1\n"
    "v_mfma_f32_16x16x32_bf16 v[108:111], a[36:39], v[188:191], v[108:111]\n"
    "s_nop 0\n"
    "ds_read_b128 a[232:235], v243 offset:26688\n"
    "v_lshlrev_b32_sdwa v187, 1, v174 dst_sel:DWORD dst_unused:UNUSED_PAD src0_sel:DWORD src1_sel:WORD_1\n"
    "v_mfma_f32_16x16x32_bf16 v[168:171], a[168:171], v[144:147], 0\n"
    "s_waitcnt lgkmcnt(8)\n"
    "ds_read_b128 a[236:239], v243 offset:27712\n"
    "v_cvt_pk_f16_f32 v188, v176, v177\n"
    "v_mfma_f32_16x16x32_bf16 v[168:171], a[216:219], v[148:151], v[168:171]\n"
    "ds_read_b128 a[240:243], v244 offset:38912\n"
    "v_lshlrev_b32_sdwa v189, 1, v178 dst_sel:DWORD dst_unused:UNUSED_PAD src0_sel:DWORD src1_sel:WORD_1\n"
    "v_mfma_f32_16x16x32_bf16 v[168:171], a[124:127], v[152:155], v[168:171]\n"
    "s_waitcnt lgkmcnt(8)\n"
    "ds_read_u16 v185, v185\n"
    "v_lshlrev_b32_sdwa v190, 1, v180 dst_sel:DWORD dst_unused:UNUSED_PAD src0_sel:DWORD src1_sel:WORD_1\n"
    "v_smfmac_f32_16x16x64_bf16 v[8:11], v[216:219], v[208:215], v240\n"
    "s_waitcnt lgkmcnt(8)\n"
    "v_lshlrev_b32_sdwa v191, 1, v182 dst_sel:DWORD dst_unused:UNUSED_PAD src0_sel:DWORD src1_sel:WORD_1\n"
    "v_smfmac_f32_16x16x64_bf16 v[12:15], v[220:223], v[208:215], v240 abid:1\n"
    "ds_read_b128 v[208:211], v250 offset:160\n"
    "ds_read_b128 v[212:215], v250 offset:480\n"
    "v_mfma_f32_16x16x32_bf16 v[172:175], a[172:175], v[144:147], 0\n"
    "buffer_load_dwordx4 v[84:87], v247, s[40:43], s60 offen offset:128\n"
    "ds_read_b128 v[216:219], v251 offset:1344\n"
    "v_or_b32_e64 v196, v196, v204\n"
    "v_or_b32_e64 v197, v197, v205\n"
    "v_lshlrev_b32_sdwa v205, 1, v179 dst_sel:DWORD dst_unused:UNUSED_PAD src0_sel:DWORD src1_sel:WORD_1\n"
    "v_pk_add_f32 v[224:225], v[224:225], v[8:9]\n"
    "v_pk_add_f32 v[226:227], v[226:227], v[10:11]\n"
    "v_pk_fma_f32 v[228:229], v[8:9], v[8:9], v[228:229]\n"
    "v_pk_fma_f32 v[230:231], v[10:11], v[10:11], v[230:231]\n"
    "v_pk_add_f32 v[224:225], v[224:225], v[12:13]\n"
    "v_pk_add_f32 v[226:227], v[226:227], v[14:15]\n"
    "v_pk_fma_f32 v[228:229], v[12:13], v[12:13], v[228:229]\n"
    "v_pk_fma_f32 v[230:231], v[14:15], v[14:15], v[230:231]\n"
    "v_mfma_f32_16x16x32_bf16 v[172:175], a[220:223], v[148:151], v[172:175]\n"
    "s_waitcnt lgkmcnt(9)\n"
    "ds_read_b128 v[220:223], v251 offset:1680\n"
    "ds_read_u16_d16_hi v201, v201\n"
    "v_mfma_f32_16x16x32_bf16 v[176:179], a[168:171], v[156:159], 0\n"
    "s_nop 0\n"
    "ds_read_u16 v186, v186\n"
    "v_or_b32_e32 v198, v198, v206\n"
    "v_mfma_f32_16x16x32_bf16 v[176:179], a[216:219], v[160:163], v[176:179]\n"
    "s_waitcnt lgkmcnt(9)\n"
    "ds_read_u16_d16_hi v202, v202\n"
    "v_lshlrev_b32_sdwa v206, 1, v181 dst_sel:DWORD dst_unused:UNUSED_PAD src0_sel:DWORD src1_sel:WORD_1\n"
    "v_mfma_f32_16x16x32_bf16 v[176:179], a[124:127], v[164:167], v[176:179]\n"
    "s_waitcnt lgkmcnt(9)\n"
    "v_or_b32_e64 v199, v199, v207\n"
    "v_lshlrev_b32_sdwa v207, 1, v183 dst_sel:DWORD dst_unused:UNUSED_PAD src0_sel:DWORD src1_sel:WORD_1\n"
    "v_smfmac_f32_32x32x32_bf16 v[112:127], a[248:251], a[232:239], v241\n"
    "s_waitcnt lgkmcnt(8)\n"
    "ds_read_b128 v[236:239], v244 offset:12288\n"
    "v_pk_fma_f16 v232, v184, v184, s48\n"
    "v_pk_mul_f16 v232, v184, v232\n"
    "v_exp_f16_e64 v234, v232\n"
    "v_mfma_f32_16x16x32_bf16 v[172:175], a[240:243], v[152:155], v[172:175]\n"
    "ds_read_u16 v187, v187\n"
    "v_exp_f16_sdwa v234, v232 dst_sel:WORD_1 dst_unused:UNUSED_PRESERVE src0_sel:WORD_1\n"
    "v_mfma_f32_16x16x32_bf16 v[180:183], a[172:175], v[156:159], 0\n"
    "ds_read_u16_d16_hi v203, v203\n"
    "v_pk_fma_f16 v234, v234, s49, s49\n"
    "v_mfma_f32_16x16x32_bf16 v[180:183], a[220:223], v[160:163], v[180:183]\n"
    "ds_read_b128 a[244:247], v244 offset:14336\n"
    "v_rcp_f16_e64 v232, v234\n"
    "s_waitcnt lgkmcnt(8)\n"
    "v_smfmac_f32_16x16x64_bf16 v[16:19], v[216:219], v[208:215], v240\n"
    "s_waitcnt lgkmcnt(7)\n"
    "ds_read_b128 v[216:219], v252 offset:1344\n"
    "ds_read_u16 v189, v189\n"
    "v_smfmac_f32_16x16x64_bf16 v[20:23], v[220:223], v[208:215], v240 abid:1\n"
    "v_rcp_f16_sdwa v232, v234 dst_sel:WORD_1 dst_unused:UNUSED_PRESERVE src0_sel:WORD_1\n"
    "v_mfma_f32_16x16x32_bf16 v[180:183], a[240:243], v[164:167], v[180:183]\n"
    "ds_read_b128 v[208:211], v250 offset:800\n"
    "s_waitcnt lgkmcnt(9)\n"
    "v_or_b32_e32 v185, v185, v201\n"
    "v_mfma_f32_16x16x32_bf16 v[96:99], a[48:51], v[192:195], v[96:99]\n"
    "ds_read_b128 v[212:215], v250 offset:1120\n"
    "v_lshlrev_b32_sdwa v201, 1, v171 dst_sel:DWORD dst_unused:UNUSED_PAD src0_sel:DWORD src1_sel:WORD_1\n"
    "v_mfma_f32_16x16x32_bf16 v[100:103], a[52:55], v[192:195], v[100:103]\n"
    "ds_read_b128 v[220:223], v252 offset:1680\n"
    "v_fma_mix_f32 v234, v184, v232, 0 op_sel_hi:[1,1,0]\n"
    "v_mfma_f32_16x16x32_bf16 v[112:115], a[56:59], v[192:195], v[112:115]\n"
    "ds_read_b128 a[232:235], v243 offset:28736\n"
    "v_fma_mix_f32 v232, v184, v232, 0 op_sel:[1,1,0] op_sel_hi:[1,1,0]\n"
    "v_mfma_f32_16x16x32_bf16 v[116:119], a[60:63], v[192:195], v[116:119]\n"
    "s_waitcnt lgkmcnt(9)\n"
    "ds_read_u16_d16_hi v205, v205\n"
    "v_or_b32_e32 v186, v186, v202\n"
    "v_mfma_f32_16x16x32_bf16 v[128:131], v[236:239], v[192:195], v[128:131]\n"
    "buffer_load_dwordx4 v[88:91], v247, s[40:43], s61 offen offset:128\n"
    "ds_read_b128 a[236:239], v243 offset:29760\n"
    "v_mfma_f32_16x16x32_bf16 v[104:107], a[48:51], v[196:199], v[104:107]\n"
    "v_lshlrev_b32_sdwa v202, 1, v173 dst_sel:DWORD dst_unused:UNUSED_PAD src0_sel:DWORD src1_sel:WORD_1\n"
    "v_cvt_pk_bf16_f32 v184, v234, v232\n"
    "v_mfma_f32_16x16x32_bf16 v[108:111], a[52:55], v[196:199], v[108:111]\n"
    "ds_read_u16 v190, v190\n"
    "v_pk_fma_f16 v232, v188, v188, s48\n"
    "v_mfma_f32_16x16x32_bf16 v[120:123], a[56:59], v[196:199], v[120:123]\n"
    "s_waitcnt lgkmcnt(9)\n"
    "ds_read_b128 v[144:147], v242 offset:0\n"
    "v_or_b32_e32 v187, v187, v203\n"
    "v_mfma_f32_16x16x32_bf16 v[132:135], a[244:247], v[192:195], v[132:135]\n"
    "ds_read_u16_d16_hi v206, v206\n"
    "v_cvt_pk_f16_f32 v192, v168, v169\n"
    "v_mfma_f32_16x16x32_bf16 v[124:127], a[60:63], v[196:199], v[124:127]\n"
    "v_lshlrev_b32_sdwa v193, 1, v170 dst_sel:DWORD dst_unused:UNUSED_PAD src0_sel:DWORD src1_sel:WORD_1\n"
    "v_lshlrev_b32_sdwa v194, 1, v172 dst_sel:DWORD dst_unused:UNUSED_PAD src0_sel:DWORD src1_sel:WORD_1\n"
    "v_mfma_f32_16x16x32_bf16 v[136:139], v[236:239], v[196:199], v[136:139]\n"
    "ds_read_b128 v[148:151], v242 offset:1024\n"
    "v_lshlrev_b32_sdwa v195, 1, v174 dst_sel:DWORD dst_unused:UNUSED_PAD src0_sel:DWORD src1_sel:WORD_1\n"
    "v_mfma_f32_16x16x32_bf16 v[140:143], a[244:247], v[196:199], v[140:143]\n"
    "ds_read_u16 v191, v191\n"
    "v_lshlrev_b32_sdwa v203, 1, v175 dst_sel:DWORD dst_unused:UNUSED_PAD src0_sel:DWORD src1_sel:WORD_1\n"
    "s_waitcnt lgkmcnt(9)\n"
    "v_smfmac_f32_16x16x64_bf16 v[16:19], v[216:219], v[208:215], v240\n"
    "s_waitcnt lgkmcnt(8)\n"
    "ds_read_b128 v[216:219], v251 offset:2016\n"
    "v_smfmac_f32_16x16x64_bf16 v[20:23], v[220:223], v[208:215], v240 abid:1\n"
    "ds_read_b128 v[208:211], v250 offset:240\n"
    "v_cvt_pk_f16_f32 v196, v176, v177\n"
    "v_lshlrev_b32_sdwa v197, 1, v178 dst_sel:DWORD dst_unused:UNUSED_PAD src0_sel:DWORD src1_sel:WORD_1\n"
    "v_lshlrev_b32_sdwa v198, 1, v180 dst_sel:DWORD dst_unused:UNUSED_PAD src0_sel:DWORD src1_sel:WORD_1\n"
    "ds_read_b128 v[212:215], v250 offset:560\n"
    "v_pk_add_f32 v[224:225], v[224:225], v[16:17]\n"
    "v_pk_add_f32 v[226:227], v[226:227], v[18:19]\n"
    "v_pk_fma_f32 v[228:229], v[16:17], v[16:17], v[228:229]\n"
    "v_pk_fma_f32 v[230:231], v[18:19], v[18:19], v[230:231]\n"
    "v_pk_add_f32 v[224:225], v[224:225], v[20:21]\n"
    "v_pk_add_f32 v[226:227], v[226:227], v[22:23]\n"
    "v_pk_fma_f32 v[228:229], v[20:21], v[20:21], v[228:229]\n"
    "v_pk_fma_f32 v[230:231], v[22:23], v[22:23], v[230:231]\n"
    "s_waitcnt lgkmcnt(8)\n"
    "v_smfmac_f32_32x32x32_bf16 v[128:143], a[248:251], a[232:239], v241\n"
    "s_nop 0\n"
    "ds_read_b128 v[220:223], v251 offset:2352\n"
    "ds_read_b128 v[152:155], v242 offset:2048\n"
    "ds_read_u16_d16_hi v207, v207\n"
    "v_or_b32_e64 v189, v189, v205\n"
    "v_lshlrev_b32_sdwa v205, 1, v179 dst_sel:DWORD dst_unused:UNUSED_PAD src0_sel:DWORD src1_sel:WORD_1\n"
    "s_waitcnt lgkmcnt(9)\n"
    "v_mfma_f32_16x16x32_bf16 v[168:171], a[128:131], v[144:147], 0\n"
    "s_nop 0\n"
    "ds_read_b128 v[156:159], v242 offset:3072\n"
    "ds_read_b128 v[160:163], v242 offset:4096\n"
    "v_mfma_f32_16x16x32_bf16 v[172:175], a[132:135], v[144:147], 0\n"
    "ds_read_b128 v[164:167], v242 offset:5120\n"
    "v_lshlrev_b32_sdwa v199, 1, v182 dst_sel:DWORD dst_unused:UNUSED_PAD src0_sel:DWORD src1_sel:WORD_1\n"
    "s_waitcnt lgkmcnt(10)\n"
    "v_mfma_f32_16x16x32_bf16 v[168:171], a[176:179], v[148:151], v[168:171]\n"
    "s_nop 0\n"
    "ds_read_b128 a[240:243], v244 offset:32768\n"
    "v_or_b32_e64 v190, v190, v206\n"
    "v_mfma_f32_16x16x32_bf16 v[172:175], a[180:183], v[148:151], v[172:175]\n"
    "ds_read_u16 v193, v193\n"
    "v_lshlrev_b32_sdwa v206, 1, v181 dst_sel:DWORD dst_unused:UNUSED_PAD src0_sel:DWORD src1_sel:WORD_1\n"
    "v_pk_mul_f16 v232, v188, v232\n"
    "v_exp_f16_e64 v234, v232\n"
    "ds_read_u16_d16_hi v201, v201\n"
    "v_exp_f16_sdwa v234, v232 dst_sel:WORD_1 dst_unused:UNUSED_PRESERVE src0_sel:WORD_1\n"
    "ds_read_u16 v194, v194\n"
    "s_waitcnt lgkmcnt(10)\n"
    "v_smfmac_f32_16x16x64_bf16 v[24:27], v[216:219], v[208:215], v240\n"
    "s_waitcnt lgkmcnt(9)\n"
    "v_pk_fma_f16 v234, v234, s49, s49\n"
    "v_smfmac_f32_16x16x64_bf16 v[28:31], v[220:223], v[208:215], v240 abid:1\n"
    "s_waitcnt lgkmcnt(8)\n"
    "ds_read_b128 v[208:211], v250 offset:880\n"
    "v_mfma_f32_16x16x32_bf16 v[168:171], a[96:99], v[152:155], v[168:171]\n"
    "s_nop 0\n"
    "ds_read_b128 v[212:215], v250 offset:1200\n"
    "v_rcp_f16_e64 v232, v234\n"
    "v_mfma_f32_16x16x32_bf16 v[96:99], a[64:67], v[184:187], v[96:99]\n"
    "ds_read_b128 v[216:219], v252 offset:2016\n"
    "v_rcp_f16_sdwa v232, v234 dst_sel:WORD_1 dst_unused:UNUSED_PRESERVE src0_sel:WORD_1\n"
    "s_waitcnt lgkmcnt(9)\n"
    "v_mfma_f32_16x16x32_bf16 v[176:179], a[128:131], v[156:159], 0\n"
    "s_waitcnt lgkmcnt(8)\n"
    "ds_read_b128 v[220:223], v252 offset:2352\n"
    "ds_read_u16_d16_hi v202, v202\n"
    "v_mfma_f32_16x16x32_bf16 v[176:179], a[176:179], v[160:163], v[176:179]\n"
    "s_waitcnt lgkmcnt(9)\n"
    "v_or_b32_e64 v191, v191, v207\n"
    "v_lshlrev_b32_sdwa v207, 1, v183 dst_sel:DWORD dst_unused:UNUSED_PAD src0_sel:DWORD src1_sel:WORD_1\n"
    "v_mfma_f32_16x16x32_bf16 v[176:179], a[96:99], v[164:167], v[176:179]\n"
    "s_waitcnt lgkmcnt(8)\n"
    "ds_read_b128 v[236:239], v244 offset:16384\n"
    "v_fma_mix_f32 v234, v188, v232, 0 op_sel_hi:[1,1,0]\n"
    "v_mfma_f32_16x16x32_bf16 v[172:175], a[240:243], v[152:155], v[172:175]\n"
    "ds_read_u16 v195, v195\n"
    "v_fma_mix_f32 v232, v188, v232, 0 op_sel:[1,1,0] op_sel_hi:[1,1,0]\n"
    "v_mfma_f32_16x16x32_bf16 v[180:183], a[132:135], v[156:159], 0\n"
    "v_cvt_pk_bf16_f32 v188, v234, v232\n"
    "v_pk_fma_f16 v232, v192, v192, s48\n"
    "v_mfma_f32_16x16x32_bf16 v[180:183], a[180:183], v[160:163], v[180:183]\n"
    "ds_read_u16_d16_hi v203, v203\n"
    "v_pk_mul_f16 v232, v192, v232\n"
    "v_mfma_f32_16x16x32_bf16 v[180:183], a[240:243], v[164:167], v[180:183]\n"
    "v_exp_f16_e32 v234, v232\n"
    "v_mfma_f32_16x16x32_bf16 v[100:103], a[68:71], v[184:187], v[100:103]\n"
    "s_waitcnt lgkmcnt(9)\n"
    "ds_read_b128 a[244:247], v244 offset:18432\n"
    "v_or_b32_e64 v193, v193, v201\n"
    "v_mfma_f32_16x16x32_bf16 v[112:115], a[72:75], v[184:187], v[112:115]\n"
    "ds_read_u16 v197, v197\n"
    "v_exp_f16_sdwa v234, v232 dst_sel:WORD_1 dst_unused:UNUSED_PRESERVE src0_sel:WORD_1\n"
    "v_mfma_f32_16x16x32_bf16 v[116:119], a[76:79], v[184:187], v[116:119]\n"
    "s_waitcnt lgkmcnt(8)\n"
    "s_barrier\n"
    "s_waitcnt lgkmcnt(7)\n"
    "v_smfmac_f32_16x16x64_bf16 v[24:27], v[216:219], v[208:215], v240\n"
    "s_waitcnt lgkmcnt(6)\n"
    "ds_read_u16_d16_hi v205, v205\n"
    "ds_write2_b32 v248, v32, v33 offset0:0 offset1:20\n"
    "v_smfmac_f32_16x16x64_bf16 v[28:31], v[220:223], v[208:215], v240 abid:1\n"
    "s_nop 0\n"
    "v_pk_fma_f16 v234, v234, s49, s49\n"
    "s_waitcnt lgkmcnt(6)\n"
    "v_mfma_f32_16x16x32_bf16 v[128:131], v[236:239], v[184:187], v[128:131]\n"
    "s_add_u32 s60, s60, 1572864\n"
    "ds_read_u16 v198, v198\n"
    "ds_write2_b32 v248, v34, v35 offset0:40 offset1:60\n"
    "v_add_u32_e32 v248, 1360, v248\n"
    "v_pk_add_f32 v[224:225], v[224:225], v[24:25]\n"
    "v_pk_add_f32 v[226:227], v[226:227], v[26:27]\n"
    "v_pk_fma_f32 v[228:229], v[24:25], v[24:25], v[228:229]\n"
    "v_pk_fma_f32 v[230:231], v[26:27], v[26:27], v[230:231]\n"
    "v_pk_add_f32 v[224:225], v[224:225], v[28:29]\n"
    "v_pk_add_f32 v[226:227], v[226:227], v[30:31]\n"
    "v_pk_fma_f32 v[228:229], v[28:29], v[28:29], v[228:229]\n"
    "v_pk_fma_f32 v[230:231], v[30:31], v[30:31], v[230:231]\n"
    "v_mfma_f32_16x16x32_bf16 v[104:107], a[64:67], v[188:191], v[104:107]\n"
    "s_waitcnt lgkmcnt(5)\n"
    "ds_read_u16_d16_hi v206, v206\n"
    "ds_write2_b32 v235, v52, v53 offset0:0 offset1:20\n"
    "v_mfma_f32_16x16x32_bf16 v[132:135], a[244:247], v[184:187], v[132:135]\n"
    "v_or_b32_e32 v194, v194, v202\n"
    "v_or_b32_e64 v195, v195, v203\n"
    "v_mfma_f32_16x16x32_bf16 v[108:111], a[68:71], v[188:191], v[108:111]\n"
    "ds_read_u16 v199, v199\n"
    "ds_write2_b32 v235, v54, v55 offset0:40 offset1:60\n"
    "v_mfma_f32_16x16x32_bf16 v[120:123], a[72:75], v[188:191], v[120:123]\n"
    "v_add_u32_e32 v235, 1360, v235\n"
    "v_lshlrev_b32_sdwa v184, 1, v168 dst_sel:DWORD dst_unused:UNUSED_PAD src0_sel:DWORD src1_sel:WORD_1\n"
    "v_mfma_f32_16x16x32_bf16 v[124:127], a[76:79], v[188:191], v[124:127]\n"
    "s_waitcnt lgkmcnt(6)\n"
    "ds_read_u16_d16_hi v207, v207\n"
    "ds_write2_b32 v248, v36, v37 offset0:0 offset1:20\n"
    "v_mfma_f32_16x16x32_bf16 v[136:139], v[236:239], v[188:191], v[136:139]\n"
    "v_or_b32_e32 v197, v197, v205\n"
    "v_lshlrev_b32_sdwa v200, 1, v169 dst_sel:DWORD dst_unused:UNUSED_PAD src0_sel:DWORD src1_sel:WORD_1\n"
    "v_mfma_f32_16x16x32_bf16 v[140:143], a[244:247], v[188:191], v[140:143]\n"
    "ds_read_b128 a[240:243], v244 offset:36864\n"
    "ds_write2_b32 v248, v38, v39 offset0:40 offset1:60\n"
    "v_add_u32_e32 v248, 1360, v248\n"
    "v_lshlrev_b32_sdwa v185, 1, v170 dst_sel:DWORD dst_unused:UNUSED_PAD src0_sel:DWORD src1_sel:WORD_1\n"
    "v_lshlrev_b32_sdwa v201, 1, v171 dst_sel:DWORD dst_unused:UNUSED_PAD src0_sel:DWORD src1_sel:WORD_1\n"
    "v_mfma_f32_16x16x32_bf16 v[168:171], a[136:139], v[144:147], 0\n"
    "s_nop 0\n"
    "ds_read_u16 v184, v184\n"
    "ds_write2_b32 v235, v56, v57 offset0:0 offset1:20\n"
    "v_mfma_f32_16x16x32_bf16 v[168:171], a[184:187], v[148:151], v[168:171]\n"
    "s_waitcnt lgkmcnt(8)\n"
    "v_lshlrev_b32_sdwa v186, 1, v172 dst_sel:DWORD dst_unused:UNUSED_PAD src0_sel:DWORD src1_sel:WORD_1\n"
    "v_lshlrev_b32_sdwa v202, 1, v173 dst_sel:DWORD dst_unused:UNUSED_PAD src0_sel:DWORD src1_sel:WORD_1\n"
    "v_lshlrev_b32_sdwa v187, 1, v174 dst_sel:DWORD dst_unused:UNUSED_PAD src0_sel:DWORD src1_sel:WORD_1\n"
    "ds_write2_b32 v235, v58, v59 offset0:40 offset1:60\n"
    "v_add_u32_e32 v235, -1360, v235\n"
    "v_lshlrev_b32_sdwa v203, 1, v175 dst_sel:DWORD dst_unused:UNUSED_PAD src0_sel:DWORD src1_sel:WORD_1\n"
    "ds_write2_b32 v248, v40, v41 offset0:0 offset1:20\n"
    "v_rcp_f16_e32 v232, v234\n"
    "v_mfma_f32_16x16x32_bf16 v[168:171], a[100:103], v[152:155], v[168:171]\n"
    "s_waitcnt lgkmcnt(6)\n"
    "ds_read_u16_d16_hi v200, v200\n"
    "ds_write2_b32 v248, v42, v43 offset0:40 offset1:60\n"
    "v_add_u32_e32 v248, 1360, v248\n"
    "v_or_b32_e64 v198, v198, v206\n"
    "v_rcp_f16_sdwa v232, v234 dst_sel:WORD_1 dst_unused:UNUSED_PRESERVE src0_sel:WORD_1\n"
    "v_mfma_f32_16x16x32_bf16 v[172:175], a[140:143], v[144:147], 0\n"
    "s_add_u32 s61, s61, 1572864\n"
    "ds_read_u16 v185, v185\n"
    "ds_write2_b32 v248, v44, v45 offset0:0 offset1:20\n"
    "v_lshlrev_b32_sdwa v188, 1, v176 dst_sel:DWORD dst_unused:UNUSED_PAD src0_sel:DWORD src1_sel:WORD_1\n"
    "v_lshlrev_b32_sdwa v204, 1, v177 dst_sel:DWORD dst_unused:UNUSED_PAD src0_sel:DWORD src1_sel:WORD_1\n"
    "v_lshlrev_b32_sdwa v189, 1, v178 dst_sel:DWORD dst_unused:UNUSED_PAD src0_sel:DWORD src1_sel:WORD_1\n"
    "v_lshlrev_b32_sdwa v205, 1, v179 dst_sel:DWORD dst_unused:UNUSED_PAD src0_sel:DWORD src1_sel:WORD_1\n"
    "v_lshlrev_b32_sdwa v190, 1, v180 dst_sel:DWORD dst_unused:UNUSED_PAD src0_sel:DWORD src1_sel:WORD_1\n"
    "v_lshlrev_b32_sdwa v206, 1, v181 dst_sel:DWORD dst_unused:UNUSED_PAD src0_sel:DWORD src1_sel:WORD_1\n"
    "v_lshlrev_b32_sdwa v191, 1, v182 dst_sel:DWORD dst_unused:UNUSED_PAD src0_sel:DWORD src1_sel:WORD_1\n"
    "v_fma_mix_f32 v234, v192, v232, 0 op_sel_hi:[1,1,0]\n"
    "v_mfma_f32_16x16x32_bf16 v[172:175], a[188:191], v[148:151], v[172:175]\n"
    "s_waitcnt lgkmcnt(4)\n"
    "ds_read_u16_d16_hi v201, v201\n"
    "ds_write2_b32 v248, v46, v47 offset0:40 offset1:60\n"
    "v_mfma_f32_16x16x32_bf16 v[172:175], a[240:243], v[152:155], v[172:175]\n"
    "s_add_u32 s62, s62, 1572864\n"
    "v_add_u32_e32 v248, 1360, v248\n"
    "v_or_b32_e32 v199, v199, v207\n"
    "v_lshlrev_b32_sdwa v207, 1, v183 dst_sel:DWORD dst_unused:UNUSED_PAD src0_sel:DWORD src1_sel:WORD_1\n"
    "v_fma_mix_f32 v232, v192, v232, 0 op_sel:[1,1,0] op_sel_hi:[1,1,0]\n"
    "v_mfma_f32_16x16x32_bf16 v[176:179], a[136:139], v[156:159], 0\n"
    "ds_read_u16 v186, v186\n"
    "ds_write2_b32 v248, v48, v49 offset0:0 offset1:20\n"
    "v_cvt_pk_bf16_f32 v192, v234, v232\n"
    "v_mfma_f32_16x16x32_bf16 v[176:179], a[184:187], v[160:163], v[176:179]\n"
    "s_waitcnt lgkmcnt(6)\n"
    "v_pk_fma_f16 v232, v196, v196, s48\n"
    "v_or_b32_e32 v184, v184, v200\n"
    "v_mfma_f32_16x16x32_bf16 v[176:179], a[100:103], v[164:167], v[176:179]\n"
    "s_add_u32 s63, s63, 1572864\n"
    "ds_read_b128 v[236:239], v244 offset:20480\n"
    "ds_write2_b32 v248, v50, v51 offset0:40 offset1:60\n"
    "v_mfma_f32_32x32x1_2b_f32 v[32:63], v254, v254, 0\n"
    "v_add_u32_e32 v248, -5440, v248\n"
    "v_pk_mul_f16 v232, v196, v232\n"
    "v_mov_b32_e64 v254, 0\n"
    "v_exp_f16_e64 v234, v232\n"
    "ds_read_u16_d16_hi v202, v202\n"
    "ds_read_b128 v[216:219], v251 offset:10752\n"
    "v_exp_f16_sdwa v234, v232 dst_sel:WORD_1 dst_unused:UNUSED_PRESERVE src0_sel:WORD_1\n"
    "s_waitcnt lgkmcnt(6)\n"
    "v_or_b32_e32 v185, v185, v201\n"
    "v_pk_fma_f16 v234, v234, s49, s49\n"
    "v_mfma_f32_16x16x32_bf16 v[180:183], a[140:143], v[156:159], 0\n"
    "s_add_u32 s52, s52, 1572864\n"
    "ds_read_b128 v[220:223], v251 offset:11088\n"
    "ds_read_u16 v187, v187\n"
    "v_mfma_f32_16x16x32_bf16 v[180:183], a[188:191], v[160:163], v[180:183]\n"
    "ds_read_b128 a[244:247], v244 offset:22528\n"
    "ds_read_u16_d16_hi v203, v203\n"
    "v_mfma_f32_16x16x32_bf16 v[180:183], a[240:243], v[164:167], v[180:183]\n"
    "ds_read_u16 v188, v188\n"
    "v_rcp_f16_e32 v232, v234\n"
    "v_mfma_f32_16x16x32_bf16 v[96:99], a[80:83], v[192:195], v[96:99]\n"
    "s_waitcnt lgkmcnt(7)\n"
    "ds_read_u16_d16_hi v204, v204\n"
    "v_rcp_f16_sdwa v232, v234 dst_sel:WORD_1 dst_unused:UNUSED_PRESERVE src0_sel:WORD_1\n"
    "s_waitcnt vmcnt(2)\n"
    "v_smfmac_f32_32x32x32_bf16 v[128:143], a[248:251], a[224:231], v241\n"
    "s_barrier\n"
    "v_mfma_f32_16x16x32_bf16 v[100:103], a[84:87], v[192:195], v[100:103]\n"
    "s_nop 0\n"
    "ds_read_b128 v[208:211], v250 offset:0\n"
    "ds_read_b128 v[212:215], v250 offset:320\n"
    "v_mfma_f32_16x16x32_bf16 v[112:115], a[88:91], v[192:195], v[112:115]\n"
    "s_waitcnt lgkmcnt(7)\n"
    "ds_read_u16 v189, v189\n"
    "v_fma_mix_f32 v234, v196, v232, 0 op_sel_hi:[1,1,0]\n"
    "v_mfma_f32_16x16x32_bf16 v[116:119], a[92:95], v[192:195], v[116:119]\n"
    "ds_read_u16_d16_hi v205, v205\n"
    "v_fma_mix_f32 v232, v196, v232, 0 op_sel:[1,1,0] op_sel_hi:[1,1,0]\n"
    "v_mfma_f32_16x16x32_bf16 v[128:131], v[236:239], v[192:195], v[128:131]\n"
    "v_cvt_pk_bf16_f32 v196, v234, v232\n"
    "v_lshlrev_b32_sdwa v201, 1, v171 dst_sel:DWORD dst_unused:UNUSED_PAD src0_sel:DWORD src1_sel:WORD_1\n"
    "v_or_b32_e32 v186, v186, v202\n"
    "s_waitcnt lgkmcnt(7)\n"
    "v_mfma_f32_16x16x32_bf16 v[132:135], a[244:247], v[192:195], v[132:135]\n"
    "ds_read_u16 v190, v190\n"
    "v_cvt_pk_f16_f32 v192, v168, v169\n"
    "v_mfma_f32_16x16x32_bf16 v[104:107], a[80:83], v[196:199], v[104:107]\n"
    "ds_read_u16_d16_hi v206, v206\n"
    "v_lshlrev_b32_sdwa v193, 1, v170 dst_sel:DWORD dst_unused:UNUSED_PAD src0_sel:DWORD src1_sel:WORD_1\n"
    "v_mfma_f32_16x16x32_bf16 v[108:111], a[84:87], v[196:199], v[108:111]\n"
    "v_lshlrev_b32_sdwa v194, 1, v172 dst_sel:DWORD dst_unused:UNUSED_PAD src0_sel:DWORD src1_sel:WORD_1\n"
    "v_lshlrev_b32_sdwa v202, 1, v173 dst_sel:DWORD dst_unused:UNUSED_PAD src0_sel:DWORD src1_sel:WORD_1\n"
    "v_mfma_f32_16x16x32_bf16 v[120:123], a[88:91], v[196:199], v[120:123]\n"
    "s_waitcnt lgkmcnt(8)\n"
    "ds_read_u16 v191, v191\n"
    "v_lshlrev_b32_sdwa v195, 1, v174 dst_sel:DWORD dst_unused:UNUSED_PAD src0_sel:DWORD src1_sel:WORD_1\n"
    "v_mfma_f32_16x16x32_bf16 v[124:127], a[92:95], v[196:199], v[124:127]\n"
    "v_or_b32_e32 v187, v187, v203\n"
    "v_lshlrev_b32_sdwa v203, 1, v175 dst_sel:DWORD dst_unused:UNUSED_PAD src0_sel:DWORD src1_sel:WORD_1\n"
    "v_mfma_f32_16x16x32_bf16 v[136:139], v[236:239], v[196:199], v[136:139]\n"
    "ds_read_u16_d16_hi v207, v207\n"
    "v_cvt_pk_bf16_f32 v96, v96, v97\n"
    "v_mfma_f32_16x16x32_bf16 v[140:143], a[244:247], v[196:199], v[140:143]\n"
    "s_waitcnt lgkmcnt(7)\n"
    "ds_read_u16 v193, v193\n"
    "v_cvt_pk_f16_f32 v196, v176, v177\n"
    "s_waitcnt lgkmcnt(7)\n"
    "v_smfmac_f32_16x16x64_bf16 v[32:35], v[216:219], v[208:215], v240\n"
    "v_lshlrev_b32_sdwa v197, 1, v178 dst_sel:DWORD dst_unused:UNUSED_PAD src0_sel:DWORD src1_sel:WORD_1\n"
    "v_smfmac_f32_16x16x64_bf16 v[36:39], v[220:223], v[208:215], v240 abid:1\n"
    "ds_read_b128 v[208:211], v250 offset:640\n"
    "ds_read_b128 v[212:215], v250 offset:960\n"
    "v_mfma_f32_16x16x32_bf16 v[168:171], a[144:147], v[144:147], 0\n"
    "v_lshlrev_b32_sdwa v198, 1, v180 dst_sel:DWORD dst_unused:UNUSED_PAD src0_sel:DWORD src1_sel:WORD_1\n"
    "v_lshlrev_b32_sdwa v199, 1, v182 dst_sel:DWORD dst_unused:UNUSED_PAD src0_sel:DWORD src1_sel:WORD_1\n"
    "v_mfma_f32_16x16x32_bf16 v[168:171], a[192:195], v[148:151], v[168:171]\n"
    "ds_read_b128 v[216:219], v252 offset:10752\n"
    "ds_read_b128 v[220:223], v252 offset:11088\n"
    "v_mfma_f32_16x16x32_bf16 v[168:171], a[104:107], v[152:155], v[168:171]\n"
    "s_waitcnt lgkmcnt(9)\n"
    "v_or_b32_e32 v188, v188, v204\n"
    "v_or_b32_e64 v189, v189, v205\n"
    "v_mfma_f32_16x16x32_bf16 v[172:175], a[148:151], v[144:147], 0\n"
    "ds_read_u16_d16_hi v201, v201\n"
    "v_lshlrev_b32_sdwa v205, 1, v179 dst_sel:DWORD dst_unused:UNUSED_PAD src0_sel:DWORD src1_sel:WORD_1\n"
    "v_mfma_f32_16x16x32_bf16 v[172:175], a[196:199], v[148:151], v[172:175]\n"
    "ds_read_u16 v194, v194\n"
    "v_cvt_pk_bf16_f32 v97, v98, v99\n"
    "v_mfma_f32_16x16x32_bf16 v[172:175], a[108:111], v[152:155], v[172:175]\n"
    "v_cvt_pk_bf16_f32 v98, v100, v101\n"
    "v_cvt_pk_bf16_f32 v99, v102, v103\n"
    "v_mfma_f32_16x16x32_bf16 v[176:179], a[144:147], v[156:159], 0\n"
    "s_waitcnt lgkmcnt(9)\n"
    "ds_read_u16_d16_hi v202, v202\n"
    "v_or_b32_e32 v190, v190, v206\n"
    "v_mfma_f32_16x16x32_bf16 v[176:179], a[192:195], v[160:163], v[176:179]\n"
    "v_lshlrev_b32_sdwa v206, 1, v181 dst_sel:DWORD dst_unused:UNUSED_PAD src0_sel:DWORD src1_sel:WORD_1\n"
    "v_cvt_pk_bf16_f32 v112, v112, v113\n"
    "v_mfma_f32_16x16x32_bf16 v[176:179], a[104:107], v[164:167], v[176:179]\n"
    "s_nop 0\n"
    "ds_read_b128 v[236:239], v244 offset:0\n"
    "v_cvt_pk_bf16_f32 v113, v114, v115\n"
    "v_mfma_f32_16x16x32_bf16 v[100:103], a[4:7], v[184:187], 0\n"
    "s_waitcnt lgkmcnt(9)\n"
    "ds_read_u16 v195, v195\n"
    "v_cvt_pk_bf16_f32 v114, v116, v117\n"
    "v_cvt_pk_bf16_f32 v115, v118, v119\n"
    "v_cvt_pk_bf16_f32 v128, v128, v129\n"
    "v_cvt_pk_bf16_f32 v129, v130, v131\n"
    "ds_write_b128 v243, v[96:99] offset:24640\n"
    "v_mfma_f32_16x16x32_bf16 v[96:99], a[0:3], v[184:187], 0\n"
    "s_waitcnt lgkmcnt(7)\n"
    "v_or_b32_e32 v191, v191, v207\n"
    "v_lshlrev_b32_sdwa v207, 1, v183 dst_sel:DWORD dst_unused:UNUSED_PAD src0_sel:DWORD src1_sel:WORD_1\n"
    "v_smfmac_f32_16x16x64_bf16 v[32:35], v[216:219], v[208:215], v240\n"
    "s_waitcnt lgkmcnt(6)\n"
    "ds_read_b128 v[216:219], v251 offset:11424\n"
    "ds_write_b128 v243, v[112:115] offset:26688\n"
    "v_smfmac_f32_16x16x64_bf16 v[36:39], v[220:223], v[208:215], v240 abid:1\n"
    "s_nop 0\n"
    "v_cvt_pk_bf16_f32 v130, v132, v133\n"
    "v_mfma_f32_16x16x32_bf16 v[180:183], a[148:151], v[156:159], 0\n"
    "s_waitcnt lgkmcnt(7)\n"
    "v_or_b32_e32 v193, v193, v201\n"
    "v_cvt_pk_bf16_f32 v131, v134, v135\n"
    "v_cvt_pk_bf16_f32 v104, v104, v105\n"
    "ds_read_b128 v[208:211], v250 offset:80\n"
    "ds_read_b128 v[212:215], v250 offset:400\n"
    "v_pk_add_f32 v[224:225], v[224:225], v[32:33]\n"
    "v_pk_add_f32 v[226:227], v[226:227], v[34:35]\n"
    "v_pk_fma_f32 v[228:229], v[32:33], v[32:33], v[228:229]\n"
    "v_pk_fma_f32 v[230:231], v[34:35], v[34:35], v[230:231]\n"
    "v_pk_add_f32 v[224:225], v[224:225], v[36:37]\n"
    "v_pk_add_f32 v[226:227], v[226:227], v[38:39]\n"
    "v_pk_fma_f32 v[228:229], v[36:37], v[36:37], v[228:229]\n"
    "v_pk_fma_f32 v[230:231], v[38:39], v[38:39], v[230:231]\n"
    "v_mfma_f32_16x16x32_bf16 v[180:183], a[196:199], v[160:163], v[180:183]\n"
    "s_waitcnt lgkmcnt(6)\n"
    "ds_read_b128 v[220:223], v251 offset:11760\n"
    "ds_write_b128 v243, v[128:131] offset:28736\n"
    "v_mfma_f32_16x16x32_bf16 v[180:183], a[108:111], v[164:167], v[180:183]\n"
    "s_nop 0\n"
    "v_cvt_pk_bf16_f32 v105, v106, v107\n"
    "v_cvt_pk_bf16_f32 v106, v108, v109\n"
    "v_mfma_f32_16x16x32_bf16 v[112:115], a[8:11], v[184:187], 0\n"
    "v_cvt_pk_bf16_f32 v107, v110, v111\n"
    "v_or_b32_e64 v194, v194, v202\n"
    "v_mfma_f32_16x16x32_bf16 v[116:119], a[12:15], v[184:187], 0\n"
    "ds_read_u16_d16_hi v203, v203\n"
    "ds_write_b128 v243, v[104:107] offset:25664\n"
    "v_mfma_f32_16x16x32_bf16 v[128:131], v[236:239], v[184:187], 0\n"
    "v_cvt_pk_bf16_f32 v120, v120, v121\n"
    "v_cvt_pk_bf16_f32 v121, v122, v123\n"
    "v_mfma_f32_16x16x32_bf16 v[104:107], a[0:3], v[188:191], 0\n"
    "v_cvt_pk_bf16_f32 v122, v124, v125\n"
    "v_cvt_pk_bf16_f32 v123, v126, v127\n"
    "s_waitcnt lgkmcnt(4)\n"
    "v_smfmac_f32_16x16x64_bf16 v[40:43], v[216:219], v[208:215], v240\n"
    "s_waitcnt lgkmcnt(3)\n"
    "ds_read_b128 v[216:219], v252 offset:11424\n"
    "ds_write_b128 v243, v[120:123] offset:27712\n"
    "v_smfmac_f32_16x16x64_bf16 v[44:47], v[220:223], v[208:215], v240 abid:1\n"
    "v_pk_fma_f16 v232, v192, v192, s48\n"
    "v_mfma_f32_16x16x32_bf16 v[108:111], a[4:7], v[188:191], 0\n"
    "v_cvt_pk_bf16_f32 v136, v136, v137\n"
    "v_cvt_pk_bf16_f32 v137, v138, v139\n"
    "v_mfma_f32_16x16x32_bf16 v[120:123], a[8:11], v[188:191], 0\n"
    "ds_read_b128 v[208:211], v250 offset:720\n"
    "ds_read_b128 v[212:215], v250 offset:1040\n"
    "v_mfma_f32_16x16x32_bf16 v[124:127], a[12:15], v[188:191], 0\n"
    "ds_read_b128 v[220:223], v252 offset:11760\n"
    "ds_read_b128 a[244:247], v244 offset:2048\n"
    "v_cvt_pk_bf16_f32 v138, v140, v141\n"
    "s_waitcnt lgkmcnt(6)\n"
    "v_or_b32_e32 v195, v195, v203\n"
    "v_cvt_pk_bf16_f32 v139, v142, v143\n"
    "ds_write_b128 v243, v[136:139] offset:29760\n"
    "v_pk_mul_f16 v232, v192, v232\n"
    "v_mfma_f32_16x16x32_bf16 v[136:139], v[236:239], v[188:191], 0\n"
    "v_exp_f16_e64 v234, v232\n"
    "v_lshlrev_b32_sdwa v201, 1, v171 dst_sel:DWORD dst_unused:UNUSED_PAD src0_sel:DWORD src1_sel:WORD_1\n"
    "v_exp_f16_sdwa v234, v232 dst_sel:WORD_1 dst_unused:UNUSED_PRESERVE src0_sel:WORD_1\n"
    "v_lshlrev_b32_sdwa v202, 1, v173 dst_sel:DWORD dst_unused:UNUSED_PAD src0_sel:DWORD src1_sel:WORD_1\n"
    "v_pk_fma_f16 v234, v234, s49, s49\n"
    "ds_read_u16 v197, v197\n"
    "ds_read_u16_d16_hi v205, v205\n"
    "v_lshlrev_b32_sdwa v203, 1, v175 dst_sel:DWORD dst_unused:UNUSED_PAD src0_sel:DWORD src1_sel:WORD_1\n"
    "v_rcp_f16_e64 v232, v234\n"
    "ds_read_u16 v198, v198\n"
    "ds_read_u16_d16_hi v206, v206\n"
    "s_waitcnt lgkmcnt(7)\n"
    "v_smfmac_f32_16x16x64_bf16 v[40:43], v[216:219], v[208:215], v240\n"
    "s_waitcnt lgkmcnt(6)\n"
    "ds_read_b128 v[216:219], v251 offset:12096\n"
    "ds_read_u16 v199, v199\n"
    "v_smfmac_f32_16x16x64_bf16 v[44:47], v[220:223], v[208:215], v240 abid:1\n"
    "s_waitcnt lgkmcnt(7)\n"
    "v_rcp_f16_sdwa v232, v234 dst_sel:WORD_1 dst_unused:UNUSED_PRESERVE src0_sel:WORD_1\n"
    "v_mfma_f32_16x16x32_bf16 v[132:135], a[244:247], v[184:187], 0\n"
    "s_nop 0\n"
    "ds_read_b128 v[208:211], v250 offset:160\n"
    "v_cvt_pk_f16_f32 v184, v168, v169\n"
    "v_lshlrev_b32_sdwa v185, 1, v170 dst_sel:DWORD dst_unused:UNUSED_PAD src0_sel:DWORD src1_sel:WORD_1\n"
    "ds_read_b128 v[212:215], v250 offset:480\n"
    "v_pk_add_f32 v[224:225], v[224:225], v[40:41]\n"
    "v_pk_add_f32 v[226:227], v[226:227], v[42:43]\n"
    "v_pk_fma_f32 v[228:229], v[40:41], v[40:41], v[228:229]\n"
    "v_pk_fma_f32 v[230:231], v[42:43], v[42:43], v[230:231]\n"
    "v_pk_add_f32 v[224:225], v[224:225], v[44:45]\n"
    "v_pk_add_f32 v[226:227], v[226:227], v[46:47]\n"
    "v_pk_fma_f32 v[228:229], v[44:45], v[44:45], v[228:229]\n"
    "v_pk_fma_f32 v[230:231], v[46:47], v[46:47], v[230:231]\n"
    "v_mfma_f32_16x16x32_bf16 v[140:143], a[244:247], v[188:191], 0\n"
    "ds_read_b128 v[220:223], v251 offset:12432\n"
    "ds_read_u16_d16_hi v207, v207\n"
    "v_mfma_f32_16x16x32_bf16 v[168:171], a[152:155], v[144:147], 0\n"
    "ds_read_b128 a[252:255], v249 offset:0\n"
    "v_lshlrev_b32_sdwa v186, 1, v172 dst_sel:DWORD dst_unused:UNUSED_PAD src0_sel:DWORD src1_sel:WORD_1\n"
    "v_mfma_f32_16x16x32_bf16 v[168:171], a[200:203], v[148:151], v[168:171]\n"
    "s_waitcnt lgkmcnt(9)\n"
    "ds_read_u16 v185, v185\n"
    "v_or_b32_e32 v197, v197, v205\n"
    "v_mfma_f32_16x16x32_bf16 v[168:171], a[112:115], v[152:155], v[168:171]\n"
    "v_lshlrev_b32_sdwa v187, 1, v174 dst_sel:DWORD dst_unused:UNUSED_PAD src0_sel:DWORD src1_sel:WORD_1\n"
    "v_cvt_pk_f16_f32 v188, v176, v177\n"
    "v_mfma_f32_16x16x32_bf16 v[172:175], a[156:159], v[144:147], 0\n"
    "ds_read_u16_d16_hi v201, v201\n"
    "v_lshlrev_b32_sdwa v189, 1, v178 dst_sel:DWORD dst_unused:UNUSED_PAD src0_sel:DWORD src1_sel:WORD_1\n"
    "v_lshlrev_b32_sdwa v205, 1, v179 dst_sel:DWORD dst_unused:UNUSED_PAD src0_sel:DWORD src1_sel:WORD_1\n"
    "v_lshlrev_b32_sdwa v190, 1, v180 dst_sel:DWORD dst_unused:UNUSED_PAD src0_sel:DWORD src1_sel:WORD_1\n"
    "v_lshlrev_b32_sdwa v191, 1, v182 dst_sel:DWORD dst_unused:UNUSED_PAD src0_sel:DWORD src1_sel:WORD_1\n"
    "s_waitcnt lgkmcnt(8)\n"
    "v_or_b32_e32 v198, v198, v206\n"
    "v_lshlrev_b32_sdwa v206, 1, v181 dst_sel:DWORD dst_unused:UNUSED_PAD src0_sel:DWORD src1_sel:WORD_1\n"
    "v_fma_mix_f32 v234, v192, v232, 0 op_sel_hi:[1,1,0]\n"
    "v_mfma_f32_16x16x32_bf16 v[172:175], a[204:207], v[148:151], v[172:175]\n"
    "ds_read_u16 v186, v186\n"
    "ds_read_u16_d16_hi v202, v202\n"
    "v_mfma_f32_16x16x32_bf16 v[172:175], a[116:119], v[152:155], v[172:175]\n"
    "v_fma_mix_f32 v232, v192, v232, 0 op_sel:[1,1,0] op_sel_hi:[1,1,0]\n"
    "v_lshlrev_b32_sdwa v200, 1, v169 dst_sel:DWORD dst_unused:UNUSED_PAD src0_sel:DWORD src1_sel:WORD_1\n"
    "v_mfma_f32_16x16x32_bf16 v[176:179], a[152:155], v[156:159], 0\n"
    "s_waitcnt lgkmcnt(7)\n"
    "ds_read_b128 v[236:239], v244 offset:4096\n"
    "v_cvt_pk_bf16_f32 v192, v234, v232\n"
    "v_smfmac_f32_16x16x64_bf16 v[48:51], v[216:219], v[208:215], v240\n"
    "s_waitcnt lgkmcnt(7)\n"
    "ds_read_b128 v[216:219], v252 offset:12096\n"
    "v_smfmac_f32_16x16x64_bf16 v[52:55], v[220:223], v[208:215], v240 abid:1\n"
    "ds_read_b128 v[208:211], v250 offset:800\n"
    "v_mfma_f32_16x16x32_bf16 v[176:179], a[200:203], v[160:163], v[176:179]\n"
    "ds_read_b128 v[212:215], v250 offset:1120\n"
    "v_pk_fma_f16 v232, v196, v196, s48\n"
    "v_mfma_f32_16x16x32_bf16 v[176:179], a[112:115], v[164:167], v[176:179]\n"
    "s_waitcnt lgkmcnt(8)\n"
    "ds_read_b128 v[220:223], v252 offset:12432\n"
    "buffer_store_dwordx4 a[252:255], v255, s[44:47], s57 offen offset:0 sc1\n"
    "v_or_b32_e32 v199, v199, v207\n"
    "v_lshlrev_b32_sdwa v207, 1, v183 dst_sel:DWORD dst_unused:UNUSED_PAD src0_sel:DWORD src1_sel:WORD_1\n"
    "v_mfma_f32_16x16x32_bf16 v[180:183], a[156:159], v[156:159], 0\n"
    "ds_read_b128 a[252:255], v249 offset:2048\n"
    "ds_read_u16 v187, v187\n"
    "v_pk_mul_f16 v232, v196, v232\n"
    "v_mfma_f32_16x16x32_bf16 v[180:183], a[204:207], v[160:163], v[180:183]\n"
    "ds_read_u16_d16_hi v203, v203\n"
    "v_exp_f16_e32 v234, v232\n"
    "v_mfma_f32_16x16x32_bf16 v[180:183], a[116:119], v[164:167], v[180:183]\n"
    "s_waitcnt lgkmcnt(10)\n"
    "v_exp_f16_sdwa v234, v232 dst_sel:WORD_1 dst_unused:UNUSED_PRESERVE src0_sel:WORD_1\n"
    "v_or_b32_e64 v185, v185, v201\n"
    "v_pk_fma_f16 v234, v234, s49, s49\n"
    "v_mfma_f32_16x16x32_bf16 v[96:99], a[16:19], v[192:195], v[96:99]\n"
    "ds_read_b128 a[244:247], v244 offset:6144\n"
    "ds_read_u16 v189, v189\n"
    "v_mfma_f32_16x16x32_bf16 v[100:103], a[20:23], v[192:195], v[100:103]\n"
    "v_rcp_f16_e32 v232, v234\n"
    "v_mfma_f32_16x16x32_bf16 v[112:115], a[24:27], v[192:195], v[112:115]\n"
    "s_waitcnt lgkmcnt(9)\n"
    "ds_read_u16_d16_hi v205, v205\n"
    "v_rcp_f16_sdwa v232, v234 dst_sel:WORD_1 dst_unused:UNUSED_PRESERVE src0_sel:WORD_1\n"
    "v_mfma_f32_16x16x32_bf16 v[116:119], a[28:31], v[192:195], v[116:119]\n"
    "ds_read_u16 v190, v190\n"
    "v_fma_mix_f32 v234, v196, v232, 0 op_sel_hi:[1,1,0]\n"
    "v_mfma_f32_16x16x32_bf16 v[128:131], v[236:239], v[192:195], v[128:131]\n"
    "s_waitcnt lgkmcnt(8)\n"
    "v_fma_mix_f32 v232, v196, v232, 0 op_sel:[1,1,0] op_sel_hi:[1,1,0]\n"
    "v_or_b32_e32 v186, v186, v202\n"
    "v_smfmac_f32_16x16x64_bf16 v[48:51], v[216:219], v[208:215], v240\n"
    "s_waitcnt lgkmcnt(7)\n"
    "ds_read_b128 v[216:219], v251 offset:12768\n"
    "v_smfmac_f32_16x16x64_bf16 v[52:55], v[220:223], v[208:215], v240 abid:1\n"
    "s_waitcnt lgkmcnt(7)\n"
    "ds_read_b128 v[208:211], v250 offset:240\n"
    "buffer_store_dwordx4 a[252:255], v255, s[44:47], s57 offen offset:64 sc1\n"
    "ds_read_b128 v[212:215], v250 offset:560\n"
    "ds_read_b128 v[220:223], v251 offset:13104\n"
    "v_pk_add_f32 v[224:225], v[224:225], v[48:49]\n"
    "v_pk_add_f32 v[226:227], v[226:227], v[50:51]\n"
    "v_pk_fma_f32 v[228:229], v[48:49], v[48:49], v[228:229]\n"
    "v_pk_fma_f32 v[230:231], v[50:51], v[50:51], v[230:231]\n"
    "v_pk_add_f32 v[224:225], v[224:225], v[52:53]\n"
    "v_pk_add_f32 v[226:227], v[226:227], v[54:55]\n"
    "v_pk_fma_f32 v[228:229], v[52:53], v[52:53], v[228:229]\n"
    "v_pk_fma_f32 v[230:231], v[54:55], v[54:55], v[230:231]\n"
    "v_cvt_pk_bf16_f32 v196, v234, v232\n"
    "s_waitcnt lgkmcnt(7)\n"
    "v_mfma_f32_16x16x32_bf16 v[132:135], a[244:247], v[192:195], v[132:135]\n"
    "s_nop 0\n"
    "ds_read_u16_d16_hi v206, v206\n"
    "ds_read_u16 v191, v191\n"
    "v_mfma_f32_16x16x32_bf16 v[104:107], a[16:19], v[196:199], v[104:107]\n"
    "ds_read_u16_d16_hi v207, v207\n"
    "v_pk_fma_f16 v232, v184, v184, s48\n"
    "v_mfma_f32_16x16x32_bf16 v[108:111], a[20:23], v[196:199], v[108:111]\n"
    "v_or_b32_e64 v187, v187, v203\n"
    "v_pk_mul_f16 v232, v184, v232\n"
    "v_mfma_f32_16x16x32_bf16 v[120:123], a[24:27], v[196:199], v[120:123]\n"
    "ds_read_b128 a[252:255], v249 offset:4096\n"
    "v_exp_f16_e32 v234, v232\n"
    "v_mfma_f32_16x16x32_bf16 v[124:127], a[28:31], v[196:199], v[124:127]\n"
    "s_waitcnt lgkmcnt(9)\n"
    "ds_read_b128 a[240:243], v244 offset:34816\n"
    "v_or_b32_e64 v189, v189, v205\n"
    "v_mfma_f32_16x16x32_bf16 v[136:139], v[236:239], v[196:199], v[136:139]\n"
    "ds_read_u16_d16_hi v200, v200\n"
    "v_exp_f16_sdwa v234, v232 dst_sel:WORD_1 dst_unused:UNUSED_PRESERVE src0_sel:WORD_1\n"
    "v_mfma_f32_16x16x32_bf16 v[140:143], a[244:247], v[196:199], v[140:143]\n"
    "v_pk_fma_f16 v234, v234, s49, s49\n"
    "v_lshlrev_b32_sdwa v192, 1, v168 dst_sel:DWORD dst_unused:UNUSED_PAD src0_sel:DWORD src1_sel:WORD_1\n"
    "v_lshlrev_b32_sdwa v193, 1, v170 dst_sel:DWORD dst_unused:UNUSED_PAD src0_sel:DWORD src1_sel:WORD_1\n"
    "v_lshlrev_b32_sdwa v201, 1, v171 dst_sel:DWORD dst_unused:UNUSED_PAD src0_sel:DWORD src1_sel:WORD_1\n"
    "s_waitcnt lgkmcnt(7)\n"
    "v_smfmac_f32_16x16x64_bf16 v[56:59], v[216:219], v[208:215], v240\n"
    "s_waitcnt lgkmcnt(6)\n"
    "ds_read_b128 v[216:219], v252 offset:12768\n"
    "ds_read_u16 v192, v192\n"
    "v_smfmac_f32_16x16x64_bf16 v[60:63], v[220:223], v[208:215], v240 abid:1\n"
    "ds_read_b128 v[208:211], v250 offset:880\n"
    "v_mfma_f32_16x16x32_bf16 v[168:171], a[160:163], v[144:147], 0\n"
    "s_nop 0\n"
    "ds_read_b128 v[212:215], v250 offset:1200\n"
    "v_lshlrev_b32_sdwa v194, 1, v172 dst_sel:DWORD dst_unused:UNUSED_PAD src0_sel:DWORD src1_sel:WORD_1\n"
    "v_mfma_f32_16x16x32_bf16 v[168:171], a[208:211], v[148:151], v[168:171]\n"
    "s_waitcnt lgkmcnt(9)\n"
    "ds_read_b128 v[220:223], v252 offset:13104\n"
    "v_lshlrev_b32_sdwa v202, 1, v173 dst_sel:DWORD dst_unused:UNUSED_PAD src0_sel:DWORD src1_sel:WORD_1\n"
    "v_lshlrev_b32_sdwa v195, 1, v174 dst_sel:DWORD dst_unused:UNUSED_PAD src0_sel:DWORD src1_sel:WORD_1\n"
    "v_lshlrev_b32_sdwa v203, 1, v175 dst_sel:DWORD dst_unused:UNUSED_PAD src0_sel:DWORD src1_sel:WORD_1\n"
    "v_rcp_f16_e64 v232, v234\n"
    "v_mfma_f32_16x16x32_bf16 v[168:171], a[120:123], v[152:155], v[168:171]\n"
    "ds_read_u16 v193, v193\n"
    "v_or_b32_e32 v190, v190, v206\n"
    "v_rcp_f16_sdwa v232, v234 dst_sel:WORD_1 dst_unused:UNUSED_PRESERVE src0_sel:WORD_1\n"
    "v_mfma_f32_16x16x32_bf16 v[172:175], a[164:167], v[144:147], 0\n"
    "s_waitcnt lgkmcnt(8)\n"
    "ds_read_u16_d16_hi v201, v201\n"
    "buffer_store_dwordx4 a[252:255], v255, s[44:47], s57 offen offset:128 sc1\n"
    "v_lshlrev_b32_sdwa v196, 1, v176 dst_sel:DWORD dst_unused:UNUSED_PAD src0_sel:DWORD src1_sel:WORD_1\n"
    "v_lshlrev_b32_sdwa v204, 1, v177 dst_sel:DWORD dst_unused:UNUSED_PAD src0_sel:DWORD src1_sel:WORD_1\n"
    "v_lshlrev_b32_sdwa v197, 1, v178 dst_sel:DWORD dst_unused:UNUSED_PAD src0_sel:DWORD src1_sel:WORD_1\n"
    "v_lshlrev_b32_sdwa v205, 1, v179 dst_sel:DWORD dst_unused:UNUSED_PAD src0_sel:DWORD src1_sel:WORD_1\n"
    "v_lshlrev_b32_sdwa v198, 1, v180 dst_sel:DWORD dst_unused:UNUSED_PAD src0_sel:DWORD src1_sel:WORD_1\n"
    "v_lshlrev_b32_sdwa v206, 1, v181 dst_sel:DWORD dst_unused:UNUSED_PAD src0_sel:DWORD src1_sel:WORD_1\n"
    "v_lshlrev_b32_sdwa v199, 1, v182 dst_sel:DWORD dst_unused:UNUSED_PAD src0_sel:DWORD src1_sel:WORD_1\n"
    "v_fma_mix_f32 v234, v184, v232, 0 op_sel_hi:[1,1,0]\n"
    "v_mfma_f32_16x16x32_bf16 v[172:175], a[212:215], v[148:151], v[172:175]\n"
    "s_waitcnt lgkmcnt(6)\n"
    "ds_read_b128 a[252:255], v249 offset:1024\n"
    "ds_read_u16 v194, v194\n"
    "v_mfma_f32_16x16x32_bf16 v[172:175], a[240:243], v[152:155], v[172:175]\n"
    "s_nop 0\n"
    "ds_read_b128 v[236:239], v244 offset:8192\n"
    "ds_read_u16_d16_hi v202, v202\n"
    "v_or_b32_e32 v191, v191, v207\n"
    "v_lshlrev_b32_sdwa v207, 1, v183 dst_sel:DWORD dst_unused:UNUSED_PAD src0_sel:DWORD src1_sel:WORD_1\n"
    "v_fma_mix_f32 v232, v184, v232, 0 op_sel:[1,1,0] op_sel_hi:[1,1,0]\n"
    "v_mfma_f32_16x16x32_bf16 v[176:179], a[160:163], v[156:159], 0\n"
    "s_waitcnt lgkmcnt(8)\n"
    "ds_read_u16 v195, v195\n"
    "v_cvt_pk_bf16_f32 v184, v234, v232\n"
    "s_waitcnt lgkmcnt(8)\n"
    "v_smfmac_f32_16x16x64_bf16 v[56:59], v[216:219], v[208:215], v240\n"
    "s_waitcnt lgkmcnt(7)\n"
    "ds_read_b128 a[244:247], v244 offset:10240\n"
    "v_smfmac_f32_16x16x64_bf16 v[60:63], v[220:223], v[208:215], v240 abid:1\n"
    "s_barrier\n"
    "v_mfma_f32_16x16x32_bf16 v[176:179], a[208:211], v[160:163], v[176:179]\n"
    "ds_read_u16_d16_hi v203, v203\n"
    "ds_write2_b32 v248, v64, v65 offset0:0 offset1:20\n"
    "v_mfma_f32_16x16x32_bf16 v[176:179], a[120:123], v[164:167], v[176:179]\n"
    "s_waitcnt lgkmcnt(7)\n"
    "buffer_store_dwordx4 a[252:255], v255, s[44:47], s100 offen offset:0 sc1\n"
    "ds_write2_b32 v248, v66, v67 offset0:40 offset1:60\n"
    "v_pk_add_f32 v[224:225], v[224:225], v[56:57]\n"
    "v_pk_add_f32 v[226:227], v[226:227], v[58:59]\n"
    "v_pk_fma_f32 v[228:229], v[56:57], v[56:57], v[228:229]\n"
    "v_pk_fma_f32 v[230:231], v[58:59], v[58:59], v[230:231]\n"
    "v_pk_add_f32 v[224:225], v[224:225], v[60:61]\n"
    "v_pk_add_f32 v[226:227], v[226:227], v[62:63]\n"
    "v_pk_fma_f32 v[228:229], v[60:61], v[60:61], v[228:229]\n"
    "v_pk_fma_f32 v[230:231], v[62:63], v[62:63], v[230:231]\n"
    "v_add_u32_e32 v248, 1360, v248\n"
    "v_pk_fma_f16 v232, v188, v188, s48\n"
    "v_mfma_f32_16x16x32_bf16 v[180:183], a[164:167], v[156:159], 0\n"
    "ds_read_u16 v196, v196\n"
    "s_waitcnt vmcnt(5)\n"
    "ds_write2_b32 v235, v84, v85 offset0:0 offset1:20\n"
    "v_pk_mul_f16 v232, v188, v232\n"
    "v_mfma_f32_16x16x32_bf16 v[180:183], a[212:215], v[160:163], v[180:183]\n"
    "v_exp_f16_e64 v234, v232\n"
    "v_mfma_f32_16x16x32_bf16 v[180:183], a[240:243], v[164:167], v[180:183]\n"
    "s_waitcnt lgkmcnt(5)\n"
    "ds_read_u16_d16_hi v204, v204\n"
    "ds_write2_b32 v235, v86, v87 offset0:40 offset1:60\n"
    "v_add_u32_e32 v235, 1360, v235\n"
    "v_exp_f16_sdwa v234, v232 dst_sel:WORD_1 dst_unused:UNUSED_PRESERVE src0_sel:WORD_1\n"
    "v_or_b32_e32 v192, v192, v200\n"
    "v_pk_fma_f16 v234, v234, s49, s49\n"
    "v_mfma_f32_16x16x32_bf16 v[96:99], a[32:35], v[184:187], v[96:99]\n"
    "s_waitcnt lgkmcnt(4)\n"
    "ds_read_u16 v197, v197\n"
    "ds_write2_b32 v248, v68, v69 offset0:0 offset1:20\n"
    "v_mfma_f32_16x16x32_bf16 v[100:103], a[36:39], v[184:187], v[100:103]\n"
    "v_rcp_f16_e64 v232, v234\n"
    "v_mfma_f32_16x16x32_bf16 v[112:115], a[40:43], v[184:187], v[112:115]\n"
    "ds_read_u16_d16_hi v205, v205\n"
    "ds_write2_b32 v248, v70, v71 offset0:40 offset1:60\n"
    "v_add_u32_e32 v248, 1360, v248\n"
    "v_rcp_f16_sdwa v232, v234 dst_sel:WORD_1 dst_unused:UNUSED_PRESERVE src0_sel:WORD_1\n"
    "v_mfma_f32_16x16x32_bf16 v[116:119], a[44:47], v[184:187], v[116:119]\n"
    "ds_read_u16 v198, v198\n"
    "ds_read_u16_d16_hi v206, v206\n"
    "v_fma_mix_f32 v234, v188, v232, 0 op_sel_hi:[1,1,0]\n"
    "v_mfma_f32_16x16x32_bf16 v[128:131], v[236:239], v[184:187], v[128:131]\n"
    "ds_read_u16 v199, v199\n"
    "v_fma_mix_f32 v232, v188, v232, 0 op_sel:[1,1,0] op_sel_hi:[1,1,0]\n"
    "v_mfma_f32_16x16x32_bf16 v[132:135], a[244:247], v[184:187], v[132:135]\n"
    "ds_read_b128 a[252:255], v249 offset:3072\n"
    "v_cvt_pk_bf16_f32 v188, v234, v232\n"
    "v_or_b32_e32 v193, v193, v201\n"
    "v_cvt_pk_f16_f32 v184, v168, v169\n"
    "v_mfma_f32_16x16x32_bf16 v[104:107], a[32:35], v[188:191], v[104:107]\n"
    "s_waitcnt lgkmcnt(8)\n"
    "ds_read_u16_d16_hi v207, v207\n"
    "v_lshlrev_b32_sdwa v185, 1, v170 dst_sel:DWORD dst_unused:UNUSED_PAD src0_sel:DWORD src1_sel:WORD_1\n"
    "v_mfma_f32_16x16x32_bf16 v[108:111], a[36:39], v[188:191], v[108:111]\n"
    "ds_read_b128 a[240:243], v244 offset:38912\n"
    "v_lshlrev_b32_sdwa v201, 1, v171 dst_sel:DWORD dst_unused:UNUSED_PAD src0_sel:DWORD src1_sel:WORD_1\n"
    "v_mfma_f32_16x16x32_bf16 v[120:123], a[40:43], v[188:191], v[120:123]\n"
    "ds_read_u16 v185, v185\n"
    "v_lshlrev_b32_sdwa v186, 1, v172 dst_sel:DWORD dst_unused:UNUSED_PAD src0_sel:DWORD src1_sel:WORD_1\n"
    "v_mfma_f32_16x16x32_bf16 v[124:127], a[44:47], v[188:191], v[124:127]\n"
    "v_or_b32_e64 v194, v194, v202\n"
    "v_lshlrev_b32_sdwa v202, 1, v173 dst_sel:DWORD dst_unused:UNUSED_PAD src0_sel:DWORD src1_sel:WORD_1\n"
    "v_mfma_f32_16x16x32_bf16 v[136:139], v[236:239], v[188:191], v[136:139]\n"
    "s_nop 0\n"
    "ds_read_u16_d16_hi v201, v201\n"
    "v_lshlrev_b32_sdwa v187, 1, v174 dst_sel:DWORD dst_unused:UNUSED_PAD src0_sel:DWORD src1_sel:WORD_1\n"
    "v_mfma_f32_16x16x32_bf16 v[140:143], a[244:247], v[188:191], v[140:143]\n"
    "s_waitcnt lgkmcnt(8)\n"
    "v_cvt_pk_f16_f32 v188, v176, v177\n"
    "v_lshlrev_b32_sdwa v189, 1, v178 dst_sel:DWORD dst_unused:UNUSED_PAD src0_sel:DWORD src1_sel:WORD_1\n"
    "v_mfma_f32_16x16x32_bf16 v[168:171], a[168:171], v[144:147], 0\n"
    "ds_read_u16 v186, v186\n"
    "v_lshlrev_b32_sdwa v190, 1, v180 dst_sel:DWORD dst_unused:UNUSED_PAD src0_sel:DWORD src1_sel:WORD_1\n"
    "v_mfma_f32_16x16x32_bf16 v[168:171], a[216:219], v[148:151], v[168:171]\n"
    "v_lshlrev_b32_sdwa v191, 1, v182 dst_sel:DWORD dst_unused:UNUSED_PAD src0_sel:DWORD src1_sel:WORD_1\n"
    "v_or_b32_e64 v195, v195, v203\n"
    "v_mfma_f32_16x16x32_bf16 v[168:171], a[124:127], v[152:155], v[168:171]\n"
    "ds_read_u16_d16_hi v202, v202\n"
    "v_lshlrev_b32_sdwa v203, 1, v175 dst_sel:DWORD dst_unused:UNUSED_PAD src0_sel:DWORD src1_sel:WORD_1\n"
    "v_mfma_f32_16x16x32_bf16 v[172:175], a[172:175], v[144:147], 0\n"
    "s_waitcnt lgkmcnt(8)\n"
    "ds_read_b128 v[236:239], v244 offset:12288\n"
    "v_or_b32_e32 v196, v196, v204\n"
    "v_mfma_f32_16x16x32_bf16 v[172:175], a[220:223], v[148:151], v[172:175]\n"
    "ds_read_u16 v187, v187\n"
    "s_waitcnt lgkmcnt(8)\n"
    "buffer_store_dwordx4 a[252:255], v255, s[44:47], s100 offen offset:64 sc1\n"
    "s_waitcnt vmcnt(5)\n"
    "ds_write2_b32 v235, v88, v89 offset0:0 offset1:20\n"
    "v_or_b32_e32 v197, v197, v205\n"
    "s_waitcnt lgkmcnt(7)\n"
    "v_mfma_f32_16x16x32_bf16 v[172:175], a[240:243], v[152:155], v[172:175]\n"
    "ds_read_b128 a[252:255], v249 offset:5120\n"
    "ds_write2_b32 v235, v90, v91 offset0:40 offset1:60\n"
    "v_add_u32_e32 v235, -1360, v235\n"
    "v_lshlrev_b32_sdwa v205, 1, v179 dst_sel:DWORD dst_unused:UNUSED_PAD src0_sel:DWORD src1_sel:WORD_1\n"
    "v_mfma_f32_16x16x32_bf16 v[176:179], a[168:171], v[156:159], 0\n"
    "v_or_b32_e64 v198, v198, v206\n"
    "v_lshlrev_b32_sdwa v206, 1, v181 dst_sel:DWORD dst_unused:UNUSED_PAD src0_sel:DWORD src1_sel:WORD_1\n"
    "v_mfma_f32_16x16x32_bf16 v[176:179], a[216:219], v[160:163], v[176:179]\n"
    "ds_read_u16_d16_hi v203, v203\n"
    "ds_write2_b32 v248, v72, v73 offset0:0 offset1:20\n"
    "v_mfma_f32_16x16x32_bf16 v[176:179], a[124:127], v[164:167], v[176:179]\n"
    "s_waitcnt lgkmcnt(9)\n"
    "v_or_b32_e32 v199, v199, v207\n"
    "v_lshlrev_b32_sdwa v207, 1, v183 dst_sel:DWORD dst_unused:UNUSED_PAD src0_sel:DWORD src1_sel:WORD_1\n"
    "v_mfma_f32_16x16x32_bf16 v[180:183], a[172:175], v[156:159], 0\n"
    "ds_read_b128 a[244:247], v244 offset:14336\n"
    "ds_write2_b32 v248, v74, v75 offset0:40 offset1:60\n"
    "v_mfma_f32_16x16x32_bf16 v[180:183], a[220:223], v[160:163], v[180:183]\n"
    "v_add_u32_e32 v248, 1360, v248\n"
    "v_or_b32_e32 v185, v185, v201\n"
    "v_mfma_f32_16x16x32_bf16 v[180:183], a[240:243], v[164:167], v[180:183]\n"
    "s_waitcnt lgkmcnt(8)\n"
    "ds_read_u16 v189, v189\n"
    "ds_write2_b32 v248, v76, v77 offset0:0 offset1:20\n"
    "v_mfma_f32_16x16x32_bf16 v[96:99], a[48:51], v[192:195], v[96:99]\n"
    "v_or_b32_e64 v186, v186, v202\n"
    "v_pk_fma_f16 v232, v184, v184, s48\n"
    "v_mfma_f32_16x16x32_bf16 v[100:103], a[52:55], v[192:195], v[100:103]\n"
    "ds_read_u16_d16_hi v205, v205\n"
    "ds_write2_b32 v248, v78, v79 offset0:40 offset1:60\n"
    "v_mfma_f32_16x16x32_bf16 v[112:115], a[56:59], v[192:195], v[112:115]\n"
    "s_waitcnt lgkmcnt(8)\n"
    "buffer_store_dwordx4 a[252:255], v255, s[44:47], s100 offen offset:128 sc1\n"
    "ds_read_u16 v190, v190\n"
    "v_mfma_f32_16x16x32_bf16 v[116:119], a[60:63], v[192:195], v[116:119]\n"
    "ds_read_b128 v[144:147], v243 offset:0\n"
    "ds_read_u16_d16_hi v206, v206\n"
    "v_mfma_f32_16x16x32_bf16 v[128:131], v[236:239], v[192:195], v[128:131]\n"
    "s_waitcnt lgkmcnt(9)\n"
    "v_add_u32_e32 v248, 1360, v248\n"
    "v_or_b32_e32 v187, v187, v203\n"
    "v_mfma_f32_16x16x32_bf16 v[104:107], a[48:51], v[196:199], v[104:107]\n"
    "s_waitcnt lgkmcnt(7)\n"
    "ds_read_b128 v[148:151], v243 offset:1024\n"
    "v_pk_mul_f16 v232, v184, v232\n"
    "v_mfma_f32_16x16x32_bf16 v[132:135], a[244:247], v[192:195], v[132:135]\n"
    "ds_read_u16 v191, v191\n"
    "v_exp_f16_e64 v234, v232\n"
    "v_mfma_f32_16x16x32_bf16 v[108:111], a[52:55], v[196:199], v[108:111]\n"
    "v_exp_f16_sdwa v234, v232 dst_sel:WORD_1 dst_unused:UNUSED_PRESERVE src0_sel:WORD_1\n"
    "v_mfma_f32_16x16x32_bf16 v[120:123], a[56:59], v[196:199], v[120:123]\n"
    "ds_read_b128 v[152:155], v243 offset:2048\n"
    "ds_write2_b32 v248, v80, v81 offset0:0 offset1:20\n"
    "v_mfma_f32_16x16x32_bf16 v[124:127], a[60:63], v[196:199], v[124:127]\n"
    "v_pk_fma_f16 v234, v234, s49, s49\n"
    "v_cvt_pk_f16_f32 v192, v168, v169\n"
    "v_mfma_f32_16x16x32_bf16 v[136:139], v[236:239], v[196:199], v[136:139]\n"
    "ds_read_u16_d16_hi v207, v207\n"
    "v_lshlrev_b32_sdwa v193, 1, v170 dst_sel:DWORD dst_unused:UNUSED_PAD src0_sel:DWORD src1_sel:WORD_1\n"
    "v_mfma_f32_16x16x32_bf16 v[140:143], a[244:247], v[196:199], v[140:143]\n"
    "s_waitcnt lgkmcnt(8)\n"
    "ds_read_b128 v[156:159], v243 offset:3072\n"
    "v_or_b32_e32 v189, v189, v205\n"
    "v_lshlrev_b32_sdwa v201, 1, v171 dst_sel:DWORD dst_unused:UNUSED_PAD src0_sel:DWORD src1_sel:WORD_1\n"
    "v_lshlrev_b32_sdwa v194, 1, v172 dst_sel:DWORD dst_unused:UNUSED_PAD src0_sel:DWORD src1_sel:WORD_1\n"
    "v_lshlrev_b32_sdwa v202, 1, v173 dst_sel:DWORD dst_unused:UNUSED_PAD src0_sel:DWORD src1_sel:WORD_1\n"
    "v_lshlrev_b32_sdwa v195, 1, v174 dst_sel:DWORD dst_unused:UNUSED_PAD src0_sel:DWORD src1_sel:WORD_1\n"
    "v_lshlrev_b32_sdwa v203, 1, v175 dst_sel:DWORD dst_unused:UNUSED_PAD src0_sel:DWORD src1_sel:WORD_1\n"
    "v_rcp_f16_e32 v232, v234\n"
    "s_waitcnt lgkmcnt(7)\n"
    "v_mfma_f32_16x16x32_bf16 v[168:171], a[128:131], v[144:147], 0\n"
    "ds_read_b128 v[160:163], v243 offset:4096\n"
    "ds_write2_b32 v248, v82, v83 offset0:40 offset1:60\n"
    "v_mfma_f32_32x32x1_2b_f32 v[64:95], v254, v254, 0\n"
    "buffer_load_dwordx4 a[232:235], v245, s[40:43], s58 offen offset:64\n"
    "buffer_load_dwordx4 a[224:227], v245, s[40:43], s58 offen offset:0\n"
    "v_add_u32_e32 v248, -5440, v248\n"
    "s_waitcnt lgkmcnt(7)\n"
    "v_or_b32_e32 v190, v190, v206\n"
    "v_rcp_f16_sdwa v232, v234 dst_sel:WORD_1 dst_unused:UNUSED_PRESERVE src0_sel:WORD_1\n"
    "v_cvt_pk_f16_f32 v196, v176, v177\n"
    "v_lshlrev_b32_sdwa v197, 1, v178 dst_sel:DWORD dst_unused:UNUSED_PAD src0_sel:DWORD src1_sel:WORD_1\n"
    "v_lshlrev_b32_sdwa v205, 1, v179 dst_sel:DWORD dst_unused:UNUSED_PAD src0_sel:DWORD src1_sel:WORD_1\n"
    "v_lshlrev_b32_sdwa v198, 1, v180 dst_sel:DWORD dst_unused:UNUSED_PAD src0_sel:DWORD src1_sel:WORD_1\n"
    "v_lshlrev_b32_sdwa v206, 1, v181 dst_sel:DWORD dst_unused:UNUSED_PAD src0_sel:DWORD src1_sel:WORD_1\n"
    "v_lshlrev_b32_sdwa v199, 1, v182 dst_sel:DWORD dst_unused:UNUSED_PAD src0_sel:DWORD src1_sel:WORD_1\n"
    "v_fma_mix_f32 v234, v184, v232, 0 op_sel_hi:[1,1,0]\n"
    "v_mfma_f32_16x16x32_bf16 v[168:171], a[176:179], v[148:151], v[168:171]\n"
    "s_waitcnt lgkmcnt(4)\n"
    "ds_read_b128 v[164:167], v243 offset:5120\n"
    "ds_read_b128 a[240:243], v244 offset:32768\n"
    "v_mfma_f32_16x16x32_bf16 v[168:171], a[96:99], v[152:155], v[168:171]\n"
    "s_nop 0\n"
    "ds_read_u16 v193, v193\n"
    "ds_read_u16_d16_hi v201, v201\n"
    "v_mfma_f32_16x16x32_bf16 v[172:175], a[132:135], v[144:147], 0\n"
    "ds_read_u16 v194, v194\n"
    "v_fma_mix_f32 v232, v184, v232, 0 op_sel:[1,1,0] op_sel_hi:[1,1,0]\n"
    "v_mfma_f32_16x16x32_bf16 v[172:175], a[180:183], v[148:151], v[172:175]\n"
    "s_waitcnt lgkmcnt(7)\n"
    "v_or_b32_e32 v191, v191, v207\n"
    "v_lshlrev_b32_sdwa v207, 1, v183 dst_sel:DWORD dst_unused:UNUSED_PAD src0_sel:DWORD src1_sel:WORD_1\n"
    "v_mfma_f32_16x16x32_bf16 v[176:179], a[128:131], v[156:159], 0\n"
    "ds_read_u16_d16_hi v202, v202\n"
    "v_cvt_pk_bf16_f32 v184, v234, v232\n"
    "s_waitcnt lgkmcnt(6)\n"
    "v_mfma_f32_16x16x32_bf16 v[176:179], a[176:179], v[160:163], v[176:179]\n"
    "s_nop 0\n"
    "ds_read_b128 v[236:239], v244 offset:16384\n"
    "v_pk_fma_f16 v232, v188, v188, s48\n"
    "v_mfma_f32_16x16x32_bf16 v[180:183], a[132:135], v[156:159], 0\n"
    "ds_read_u16 v195, v195\n"
    "v_pk_mul_f16 v232, v188, v232\n"
    "v_mfma_f32_16x16x32_bf16 v[180:183], a[180:183], v[160:163], v[180:183]\n"
    "v_exp_f16_e64 v234, v232\n"
    "v_lshlrev_b32_sdwa v200, 1, v169 dst_sel:DWORD dst_unused:UNUSED_PAD src0_sel:DWORD src1_sel:WORD_1\n"
    "s_waitcnt lgkmcnt(7)\n"
    "v_mfma_f32_16x16x32_bf16 v[176:179], a[96:99], v[164:167], v[176:179]\n"
    "s_barrier\n"
    "s_waitcnt lgkmcnt(6)\n"
    "v_mfma_f32_16x16x32_bf16 v[172:175], a[240:243], v[152:155], v[172:175]\n"
    "s_nop 0\n"
    "ds_read_b128 v[208:211], v250 offset:0\n"
    "ds_read_b128 v[212:215], v250 offset:320\n"
    "v_mfma_f32_16x16x32_bf16 v[180:183], a[240:243], v[164:167], v[180:183]\n"
    "ds_read_b128 v[216:219], v251 offset:21504\n"
    "ds_read_b128 v[220:223], v251 offset:21840\n"
    "v_exp_f16_sdwa v234, v232 dst_sel:WORD_1 dst_unused:UNUSED_PRESERVE src0_sel:WORD_1\n"
    "v_mfma_f32_16x16x32_bf16 v[96:99], a[64:67], v[184:187], v[96:99]\n"
    "ds_read_u16_d16_hi v203, v203\n"
    "ds_read_b128 a[244:247], v244 offset:18432\n"
    "v_pk_fma_f16 v234, v234, s49, s49\n"
    "v_mfma_f32_16x16x32_bf16 v[100:103], a[68:71], v[184:187], v[100:103]\n"
    "s_waitcnt lgkmcnt(10)\n"
    "ds_read_u16 v197, v197\n"
    "buffer_load_dwordx4 a[236:239], v245, s[40:43], s101 offen offset:64\n"
    "buffer_load_dwordx4 a[228:231], v245, s[40:43], s101 offen offset:0\n"
    "v_rcp_f16_e32 v232, v234\n"
    "v_mfma_f32_16x16x32_bf16 v[112:115], a[72:75], v[184:187], v[112:115]\n"
    "ds_read_u16_d16_hi v205, v205\n"
    "v_rcp_f16_sdwa v232, v234 dst_sel:WORD_1 dst_unused:UNUSED_PRESERVE src0_sel:WORD_1\n"
    "v_mfma_f32_16x16x32_bf16 v[116:119], a[76:79], v[184:187], v[116:119]\n"
    "s_waitcnt lgkmcnt(9)\n"
    "v_or_b32_e32 v193, v193, v201\n"
    "v_fma_mix_f32 v234, v188, v232, 0 op_sel_hi:[1,1,0]\n"
    "v_mfma_f32_16x16x32_bf16 v[128:131], v[236:239], v[184:187], v[128:131]\n"
    "ds_read_u16 v198, v198\n"
    "v_fma_mix_f32 v232, v188, v232, 0 op_sel:[1,1,0] op_sel_hi:[1,1,0]\n"
    "v_or_b32_e64 v194, v194, v202\n"
    "v_cvt_pk_bf16_f32 v188, v234, v232\n"
    "v_pk_fma_f16 v232, v192, v192, s48\n"
    "v_lshlrev_b32_e64 v254, 3, v244\n"
    "v_mfma_f32_16x16x32_bf16 v[104:107], a[64:67], v[188:191], v[104:107]\n"
    "ds_read_u16_d16_hi v206, v206\n"
    "v_pk_mul_f16 v232, v192, v232\n"
    "v_mfma_f32_16x16x32_bf16 v[108:111], a[68:71], v[188:191], v[108:111]\n"
    "s_waitcnt lgkmcnt(7)\n"
    "ds_read_u16 v199, v199\n"
    "v_exp_f16_e64 v234, v232\n"
    "v_smfmac_f32_16x16x64_bf16 v[64:67], v[216:219], v[208:215], v240\n"
    "s_waitcnt lgkmcnt(7)\n"
    "ds_read_b128 v[216:219], v252 offset:21504\n"
    "v_smfmac_f32_16x16x64_bf16 v[68:71], v[220:223], v[208:215], v240 abid:1\n"
    "s_nop 0\n"
    "ds_read_b128 v[208:211], v250 offset:640\n"
    "v_mfma_f32_16x16x32_bf16 v[120:123], a[72:75], v[188:191], v[120:123]\n"
    "s_mov_b32 exec_hi, 0\n"
    "buffer_load_dword v233, v254, s[72:75], s53 offen sc1\n"
    "s_mov_b32 exec_hi, -1\n"
    "s_waitcnt lgkmcnt(7)\n"
    "v_mfma_f32_16x16x32_bf16 v[132:135], a[244:247], v[184:187], v[132:135]\n"
    "s_add_u32 s53, s53, 393216\n"
    "ds_read_b128 v[212:215], v250 offset:960\n"
    "ds_read_b128 v[220:223], v252 offset:21840\n"
    "v_mfma_f32_16x16x32_bf16 v[124:127], a[76:79], v[188:191], v[124:127]\n"
    "ds_read_u16_d16_hi v207, v207\n"
    "v_or_b32_e64 v195, v195, v203\n"
    "v_mfma_f32_16x16x32_bf16 v[136:139], v[236:239], v[188:191], v[136:139]\n"
    "v_exp_f16_sdwa v234, v232 dst_sel:WORD_1 dst_unused:UNUSED_PRESERVE src0_sel:WORD_1\n"
    "v_mfma_f32_16x16x32_bf16 v[140:143], a[244:247], v[188:191], v[140:143]\n"
    "ds_read_b128 a[240:243], v244 offset:36864\n"
    "v_pk_fma_f16 v234, v234, s49, s49\n"
    "v_lshlrev_b32_sdwa v184, 1, v168 dst_sel:DWORD dst_unused:UNUSED_PAD src0_sel:DWORD src1_sel:WORD_1\n"
    "v_lshlrev_b32_sdwa v185, 1, v170 dst_sel:DWORD dst_unused:UNUSED_PAD src0_sel:DWORD src1_sel:WORD_1\n"
    "s_waitcnt lgkmcnt(9)\n"
    "v_or_b32_e32 v197, v197, v205\n"
    "v_lshlrev_b32_sdwa v201, 1, v171 dst_sel:DWORD dst_unused:UNUSED_PAD src0_sel:DWORD src1_sel:WORD_1\n"
    "v_mfma_f32_16x16x32_bf16 v[168:171], a[136:139], v[144:147], 0\n"
    "ds_read_u16 v184, v184\n"
    "ds_read_u16_d16_hi v200, v200\n"
    "v_mfma_f32_16x16x32_bf16 v[168:171], a[184:187], v[148:151], v[168:171]\n"
    "v_lshlrev_b32_sdwa v186, 1, v172 dst_sel:DWORD dst_unused:UNUSED_PAD src0_sel:DWORD src1_sel:WORD_1\n"
    "v_lshlrev_b32_sdwa v202, 1, v173 dst_sel:DWORD dst_unused:UNUSED_PAD src0_sel:DWORD src1_sel:WORD_1\n"
    "v_lshlrev_b32_sdwa v187, 1, v174 dst_sel:DWORD dst_unused:UNUSED_PAD src0_sel:DWORD src1_sel:WORD_1\n"
    "v_lshlrev_b32_sdwa v203, 1, v175 dst_sel:DWORD dst_unused:UNUSED_PAD src0_sel:DWORD src1_sel:WORD_1\n"
    "v_rcp_f16_e32 v232, v234\n"
    "v_mfma_f32_16x16x32_bf16 v[168:171], a[100:103], v[152:155], v[168:171]\n"
    "s_waitcnt lgkmcnt(9)\n"
    "ds_read_u16 v185, v185\n"
    "v_or_b32_e64 v198, v198, v206\n"
    "v_rcp_f16_sdwa v232, v234 dst_sel:WORD_1 dst_unused:UNUSED_PRESERVE src0_sel:WORD_1\n"
    "v_mfma_f32_16x16x32_bf16 v[172:175], a[140:143], v[144:147], 0\n"
    "ds_read_u16_d16_hi v201, v201\n"
    "v_lshlrev_b32_sdwa v188, 1, v176 dst_sel:DWORD dst_unused:UNUSED_PAD src0_sel:DWORD src1_sel:WORD_1\n"
    "v_mfma_f32_16x16x32_bf16 v[172:175], a[188:191], v[148:151], v[172:175]\n"
    "ds_read_u16 v186, v186\n"
    "v_lshlrev_b32_sdwa v204, 1, v177 dst_sel:DWORD dst_unused:UNUSED_PAD src0_sel:DWORD src1_sel:WORD_1\n"
    "s_waitcnt lgkmcnt(8)\n"
    "v_smfmac_f32_16x16x64_bf16 v[64:67], v[216:219], v[208:215], v240\n"
    "s_waitcnt lgkmcnt(7)\n"
    "v_lshlrev_b32_sdwa v189, 1, v178 dst_sel:DWORD dst_unused:UNUSED_PAD src0_sel:DWORD src1_sel:WORD_1\n"
    "v_smfmac_f32_16x16x64_bf16 v[68:71], v[220:223], v[208:215], v240 abid:1\n"
    "ds_read_b128 v[208:211], v250 offset:80\n"
    "ds_read_b128 v[212:215], v250 offset:400\n"
    "v_lshlrev_b32_sdwa v205, 1, v179 dst_sel:DWORD dst_unused:UNUSED_PAD src0_sel:DWORD src1_sel:WORD_1\n"
    "v_lshlrev_b32_sdwa v190, 1, v180 dst_sel:DWORD dst_unused:UNUSED_PAD src0_sel:DWORD src1_sel:WORD_1\n"
    "v_lshlrev_b32_sdwa v206, 1, v181 dst_sel:DWORD dst_unused:UNUSED_PAD src0_sel:DWORD src1_sel:WORD_1\n"
    "v_pk_add_f32 v[224:225], v[224:225], v[64:65]\n"
    "v_pk_add_f32 v[226:227], v[226:227], v[66:67]\n"
    "v_pk_fma_f32 v[228:229], v[64:65], v[64:65], v[228:229]\n"
    "v_pk_fma_f32 v[230:231], v[66:67], v[66:67], v[230:231]\n"
    "v_pk_add_f32 v[224:225], v[224:225], v[68:69]\n"
    "v_pk_add_f32 v[226:227], v[226:227], v[70:71]\n"
    "v_pk_fma_f32 v[228:229], v[68:69], v[68:69], v[228:229]\n"
    "v_pk_fma_f32 v[230:231], v[70:71], v[70:71], v[230:231]\n"
    "v_lshlrev_b32_sdwa v191, 1, v182 dst_sel:DWORD dst_unused:UNUSED_PAD src0_sel:DWORD src1_sel:WORD_1\n"
    "v_fma_mix_f32 v234, v192, v232, 0 op_sel_hi:[1,1,0]\n"
    "s_waitcnt lgkmcnt(7)\n"
    "v_mfma_f32_16x16x32_bf16 v[172:175], a[240:243], v[152:155], v[172:175]\n"
    "s_nop 0\n"
    "ds_read_b128 v[216:219], v251 offset:22176\n"
    "ds_read_b128 v[220:223], v251 offset:22512\n"
    "v_or_b32_e32 v199, v199, v207\n"
    "v_lshlrev_b32_sdwa v207, 1, v183 dst_sel:DWORD dst_unused:UNUSED_PAD src0_sel:DWORD src1_sel:WORD_1\n"
    "v_fma_mix_f32 v232, v192, v232, 0 op_sel:[1,1,0] op_sel_hi:[1,1,0]\n"
    "v_mfma_f32_16x16x32_bf16 v[176:179], a[136:139], v[156:159], 0\n"
    "s_waitcnt lgkmcnt(7)\n"
    "ds_read_b128 v[236:239], v244 offset:20480\n"
    "ds_read_u16_d16_hi v202, v202\n"
    "v_cvt_pk_bf16_f32 v192, v234, v232\n"
    "v_mfma_f32_16x16x32_bf16 v[176:179], a[184:187], v[160:163], v[176:179]\n"
    "ds_read_u16 v187, v187\n"
    "ds_read_b128 a[244:247], v244 offset:22528\n"
    "v_mfma_f32_16x16x32_bf16 v[176:179], a[100:103], v[164:167], v[176:179]\n"
    "v_pk_fma_f16 v232, v196, v196, s48\n"
    "v_or_b32_e64 v184, v184, v200\n"
    "v_mfma_f32_16x16x32_bf16 v[180:183], a[140:143], v[156:159], 0\n"
    "ds_read_u16_d16_hi v203, v203\n"
    "v_pk_mul_f16 v232, v196, v232\n"
    "v_mfma_f32_16x16x32_bf16 v[180:183], a[188:191], v[160:163], v[180:183]\n"
    "s_waitcnt lgkmcnt(10)\n"
    "ds_read_u16 v188, v188\n"
    "v_exp_f16_e32 v234, v232\n"
    "v_mfma_f32_16x16x32_bf16 v[180:183], a[240:243], v[164:167], v[180:183]\n"
    "v_exp_f16_sdwa v234, v232 dst_sel:WORD_1 dst_unused:UNUSED_PRESERVE src0_sel:WORD_1\n"
    "v_mfma_f32_16x16x32_bf16 v[96:99], a[80:83], v[192:195], v[96:99]\n"
    "ds_read_u16_d16_hi v204, v204\n"
    "v_pk_fma_f16 v234, v234, s49, s49\n"
    "s_waitcnt lgkmcnt(8)\n"
    "v_smfmac_f32_16x16x64_bf16 v[72:75], v[216:219], v[208:215], v240\n"
    "s_waitcnt lgkmcnt(7)\n"
    "ds_read_b128 v[216:219], v252 offset:22176\n"
    "v_smfmac_f32_16x16x64_bf16 v[76:79], v[220:223], v[208:215], v240 abid:1\n"
    "ds_read_b128 v[208:211], v250 offset:720\n"
    "v_mfma_f32_16x16x32_bf16 v[100:103], a[84:87], v[192:195], v[100:103]\n"
    "ds_read_b128 v[212:215], v250 offset:1040\n"
    "v_rcp_f16_e32 v232, v234\n"
    "v_mfma_f32_16x16x32_bf16 v[112:115], a[88:91], v[192:195], v[112:115]\n"
    "ds_read_b128 v[220:223], v252 offset:22512\n"
    "v_rcp_f16_sdwa v232, v234 dst_sel:WORD_1 dst_unused:UNUSED_PRESERVE src0_sel:WORD_1\n"
    "v_mfma_f32_16x16x32_bf16 v[116:119], a[92:95], v[192:195], v[116:119]\n"
    "s_waitcnt lgkmcnt(9)\n"
    "ds_read_u16 v189, v189\n"
    "v_fma_mix_f32 v234, v196, v232, 0 op_sel_hi:[1,1,0]\n"
    "v_mfma_f32_16x16x32_bf16 v[128:131], v[236:239], v[192:195], v[128:131]\n"
    "v_fma_mix_f32 v232, v196, v232, 0 op_sel:[1,1,0] op_sel_hi:[1,1,0]\n"
    "v_or_b32_e32 v185, v185, v201\n"
    "s_waitcnt lgkmcnt(8)\n"
    "v_mfma_f32_16x16x32_bf16 v[132:135], a[244:247], v[192:195], v[132:135]\n"
    "ds_read_u16_d16_hi v205, v205\n"
    "v_cvt_pk_bf16_f32 v196, v234, v232\n"
    "v_cvt_pk_f16_f32 v192, v168, v169\n"
    "v_lshlrev_b32_sdwa v193, 1, v170 dst_sel:DWORD dst_unused:UNUSED_PAD src0_sel:DWORD src1_sel:WORD_1\n"
    "v_mfma_f32_16x16x32_bf16 v[104:107], a[80:83], v[196:199], v[104:107]\n"
    "ds_read_u16 v190, v190\n"
    "v_lshlrev_b32_sdwa v201, 1, v171 dst_sel:DWORD dst_unused:UNUSED_PAD src0_sel:DWORD src1_sel:WORD_1\n"
    "v_mfma_f32_16x16x32_bf16 v[108:111], a[84:87], v[196:199], v[108:111]\n"
    "s_waitcnt lgkmcnt(9)\n"
    "ds_read_u16_d16_hi v206, v206\n"
    "v_lshlrev_b32_sdwa v194, 1, v172 dst_sel:DWORD dst_unused:UNUSED_PAD src0_sel:DWORD src1_sel:WORD_1\n"
    "v_mfma_f32_16x16x32_bf16 v[120:123], a[88:91], v[196:199], v[120:123]\n"
    "v_or_b32_e32 v186, v186, v202\n"
    "v_lshlrev_b32_sdwa v202, 1, v173 dst_sel:DWORD dst_unused:UNUSED_PAD src0_sel:DWORD src1_sel:WORD_1\n"
    "v_mfma_f32_16x16x32_bf16 v[124:127], a[92:95], v[196:199], v[124:127]\n"
    "ds_read_u16 v191, v191\n"
    "v_lshlrev_b32_sdwa v195, 1, v174 dst_sel:DWORD dst_unused:UNUSED_PAD src0_sel:DWORD src1_sel:WORD_1\n"
    "v_mfma_f32_16x16x32_bf16 v[136:139], v[236:239], v[196:199], v[136:139]\n"
    "s_waitcnt lgkmcnt(8)\n"
    "v_or_b32_e32 v187, v187, v203\n"
    "v_lshlrev_b32_sdwa v203, 1, v175 dst_sel:DWORD dst_unused:UNUSED_PAD src0_sel:DWORD src1_sel:WORD_1\n"
    "v_mfma_f32_16x16x32_bf16 v[140:143], a[244:247], v[196:199], v[140:143]\n"
    "ds_read_u16_d16_hi v207, v207\n"
    "v_cvt_pk_f16_f32 v196, v176, v177\n"
    "v_mfma_f32_16x16x32_bf16 v[168:171], a[144:147], v[144:147], 0\n"
    "s_waitcnt lgkmcnt(7)\n"
    "v_lshlrev_b32_sdwa v197, 1, v178 dst_sel:DWORD dst_unused:UNUSED_PAD src0_sel:DWORD src1_sel:WORD_1\n"
    "v_lshlrev_b32_sdwa v198, 1, v180 dst_sel:DWORD dst_unused:UNUSED_PAD src0_sel:DWORD src1_sel:WORD_1\n"
    "v_smfmac_f32_16x16x64_bf16 v[72:75], v[216:219], v[208:215], v240\n"
    "s_waitcnt lgkmcnt(6)\n"
    "ds_read_b128 v[216:219], v251 offset:22848\n"
    "ds_read_u16 v193, v193\n"
    "v_smfmac_f32_16x16x64_bf16 v[76:79], v[220:223], v[208:215], v240 abid:1\n"
    "v_lshlrev_b32_sdwa v199, 1, v182 dst_sel:DWORD dst_unused:UNUSED_PAD src0_sel:DWORD src1_sel:WORD_1\n"
    "v_mfma_f32_16x16x32_bf16 v[168:171], a[192:195], v[148:151], v[168:171]\n"
    "ds_read_b128 v[208:211], v250 offset:160\n"
    "v_or_b32_e64 v188, v188, v204\n"
    "v_lshlrev_b32_e64 v254, 3, v244\n"
    "ds_read_b128 v[212:215], v250 offset:480\n"
    "v_pk_add_f32 v[224:225], v[224:225], v[72:73]\n"
    "v_pk_add_f32 v[226:227], v[226:227], v[74:75]\n"
    "v_pk_fma_f32 v[228:229], v[72:73], v[72:73], v[228:229]\n"
    "v_pk_fma_f32 v[230:231], v[74:75], v[74:75], v[230:231]\n"
    "v_pk_add_f32 v[224:225], v[224:225], v[76:77]\n"
    "v_pk_add_f32 v[226:227], v[226:227], v[78:79]\n"
    "v_pk_fma_f32 v[228:229], v[76:77], v[76:77], v[228:229]\n"
    "v_pk_fma_f32 v[230:231], v[78:79], v[78:79], v[230:231]\n"
    "v_mfma_f32_16x16x32_bf16 v[168:171], a[104:107], v[152:155], v[168:171]\n"
    "s_waitcnt lgkmcnt(7)\n"
    "ds_read_b128 v[220:223], v251 offset:23184\n"
    "s_mov_b32 exec_hi, 0\n"
    "buffer_load_dword v233, v254, s[72:75], s53 offen sc1\n"
    "s_mov_b32 exec_hi, -1\n"
    "v_mfma_f32_16x16x32_bf16 v[172:175], a[148:151], v[144:147], 0\n"
    "s_add_u32 s53, s53, 393216\n"
    "ds_read_u16_d16_hi v201, v201\n"
    "ds_read_u16 v194, v194\n"
    "v_mfma_f32_16x16x32_bf16 v[172:175], a[196:199], v[148:151], v[172:175]\n"
    "s_waitcnt lgkmcnt(9)\n"
    "ds_read_u16_d16_hi v202, v202\n"
    "v_or_b32_e64 v189, v189, v205\n"
    "v_mfma_f32_16x16x32_bf16 v[172:175], a[108:111], v[152:155], v[172:175]\n"
    "v_lshlrev_b32_sdwa v205, 1, v179 dst_sel:DWORD dst_unused:UNUSED_PAD src0_sel:DWORD src1_sel:WORD_1\n"
    "v_cvt_pk_bf16_f32 v96, v96, v97\n"
    "v_mfma_f32_16x16x32_bf16 v[176:179], a[144:147], v[156:159], 0\n"
    "s_nop 0\n"
    "ds_read_b128 v[236:239], v244 offset:0\n"
    "v_cvt_pk_bf16_f32 v97, v98, v99\n"
    "v_mfma_f32_16x16x32_bf16 v[176:179], a[192:195], v[160:163], v[176:179]\n"
    "s_waitcnt lgkmcnt(8)\n"
    "ds_read_u16 v195, v195\n"
    "v_cvt_pk_bf16_f32 v98, v100, v101\n"
    "v_mfma_f32_16x16x32_bf16 v[176:179], a[104:107], v[164:167], v[176:179]\n"
    "v_cvt_pk_bf16_f32 v99, v102, v103\n"
    "ds_write_b128 v242, v[96:99] offset:24640\n"
    "v_or_b32_e64 v190, v190, v206\n"
    "s_waitcnt lgkmcnt(7)\n"
    "v_smfmac_f32_16x16x64_bf16 v[80:83], v[216:219], v[208:215], v240\n"
    "s_waitcnt lgkmcnt(6)\n"
    "v_lshlrev_b32_sdwa v206, 1, v181 dst_sel:DWORD dst_unused:UNUSED_PAD src0_sel:DWORD src1_sel:WORD_1\n"
    "v_smfmac_f32_16x16x64_bf16 v[84:87], v[220:223], v[208:215], v240 abid:1\n"
    "ds_read_b128 v[208:211], v250 offset:800\n"
    "ds_read_b128 v[212:215], v250 offset:1120\n"
    "v_mfma_f32_16x16x32_bf16 v[96:99], a[0:3], v[184:187], 0\n"
    "s_nop 0\n"
    "ds_read_b128 v[216:219], v252 offset:22848\n"
    "ds_read_b128 v[220:223], v252 offset:23184\n"
    "v_mfma_f32_16x16x32_bf16 v[100:103], a[4:7], v[184:187], 0\n"
    "s_waitcnt lgkmcnt(9)\n"
    "ds_read_u16_d16_hi v203, v203\n"
    "v_cvt_pk_bf16_f32 v112, v112, v113\n"
    "v_cvt_pk_bf16_f32 v113, v114, v115\n"
    "v_cvt_pk_bf16_f32 v114, v116, v117\n"
    "v_cvt_pk_bf16_f32 v115, v118, v119\n"
    "v_cvt_pk_bf16_f32 v128, v128, v129\n"
    "v_cvt_pk_bf16_f32 v129, v130, v131\n"
    "ds_write_b128 v242, v[112:115] offset:26688\n"
    "v_mfma_f32_16x16x32_bf16 v[112:115], a[8:11], v[184:187], 0\n"
    "s_waitcnt lgkmcnt(8)\n"
    "v_or_b32_e32 v191, v191, v207\n"
    "v_lshlrev_b32_sdwa v207, 1, v183 dst_sel:DWORD dst_unused:UNUSED_PAD src0_sel:DWORD src1_sel:WORD_1\n"
    "v_mfma_f32_16x16x32_bf16 v[180:183], a[148:151], v[156:159], 0\n"
    "ds_read_b128 a[244:247], v244 offset:2048\n"
    "ds_read_u16 v197, v197\n"
    "v_mfma_f32_16x16x32_bf16 v[180:183], a[196:199], v[160:163], v[180:183]\n"
    "v_cvt_pk_bf16_f32 v130, v132, v133\n"
    "v_or_b32_e64 v193, v193, v201\n"
    "v_mfma_f32_16x16x32_bf16 v[180:183], a[108:111], v[164:167], v[180:183]\n"
    "ds_read_u16_d16_hi v205, v205\n"
    "v_cvt_pk_bf16_f32 v131, v134, v135\n"
    "v_mfma_f32_16x16x32_bf16 v[116:119], a[12:15], v[184:187], 0\n"
    "v_cvt_pk_bf16_f32 v104, v104, v105\n"
    "ds_write_b128 v242, v[128:131] offset:28736\n"
    "s_waitcnt lgkmcnt(7)\n"
    "v_smfmac_f32_16x16x64_bf16 v[80:83], v[216:219], v[208:215], v240\n"
    "s_waitcnt lgkmcnt(6)\n"
    "v_cvt_pk_bf16_f32 v105, v106, v107\n"
    "v_smfmac_f32_16x16x64_bf16 v[84:87], v[220:223], v[208:215], v240 abid:1\n"
    "ds_read_b128 v[208:211], v250 offset:240\n"
    "ds_read_b128 v[212:215], v250 offset:560\n"
    "v_mfma_f32_16x16x32_bf16 v[128:131], v[236:239], v[184:187], 0\n"
    "ds_read_b128 v[216:219], v251 offset:23520\n"
    "ds_read_b128 v[220:223], v251 offset:23856\n"
    "v_cvt_pk_bf16_f32 v106, v108, v109\n"
    "v_cvt_pk_bf16_f32 v107, v110, v111\n"
    "v_or_b32_e64 v194, v194, v202\n"
    "v_pk_add_f32 v[224:225], v[224:225], v[80:81]\n"
    "v_pk_add_f32 v[226:227], v[226:227], v[82:83]\n"
    "v_pk_fma_f32 v[228:229], v[80:81], v[80:81], v[228:229]\n"
    "v_pk_fma_f32 v[230:231], v[82:83], v[82:83], v[230:231]\n"
    "v_pk_add_f32 v[224:225], v[224:225], v[84:85]\n"
    "v_pk_add_f32 v[226:227], v[226:227], v[86:87]\n"
    "v_pk_fma_f32 v[228:229], v[84:85], v[84:85], v[228:229]\n"
    "v_pk_fma_f32 v[230:231], v[86:87], v[86:87], v[230:231]\n"
    "s_waitcnt lgkmcnt(7)\n"
    "v_mfma_f32_16x16x32_bf16 v[132:135], a[244:247], v[184:187], 0\n"
    "s_nop 0\n"
    "ds_read_u16 v198, v198\n"
    "ds_write_b128 v242, v[104:107] offset:25664\n"
    "v_mfma_f32_16x16x32_bf16 v[104:107], a[0:3], v[188:191], 0\n"
    "v_cvt_pk_bf16_f32 v120, v120, v121\n"
    "v_cvt_pk_bf16_f32 v121, v122, v123\n"
    "v_mfma_f32_16x16x32_bf16 v[108:111], a[4:7], v[188:191], 0\n"
    "v_cvt_pk_bf16_f32 v122, v124, v125\n"
    "v_cvt_pk_bf16_f32 v123, v126, v127\n"
    "v_add_u32_e32 v254, s54, v242\n"
    "ds_write_b128 v242, v[120:123] offset:27712\n"
    "v_mfma_f32_16x16x32_bf16 v[120:123], a[8:11], v[188:191], 0\n"
    "s_waitcnt lgkmcnt(6)\n"
    "v_cvt_pk_bf16_f32 v136, v136, v137\n"
    "v_pk_fma_f16 v232, v192, v192, s48\n"
    "v_mfma_f32_16x16x32_bf16 v[124:127], a[12:15], v[188:191], 0\n"
    "ds_read_u16_d16_hi v206, v206\n"
    "ds_read_u16 v199, v199\n"
    "s_waitcnt lgkmcnt(6)\n"
    "v_smfmac_f32_16x16x64_bf16 v[88:91], v[216:219], v[208:215], v240\n"
    "s_waitcnt lgkmcnt(5)\n"
    "ds_read_b128 v[216:219], v252 offset:23520\n"
    "ds_read_u16_d16_hi v207, v207\n"
    "v_smfmac_f32_16x16x64_bf16 v[92:95], v[220:223], v[208:215], v240 abid:1\n"
    "ds_read_b128 v[208:211], v250 offset:880\n"
    "v_cvt_pk_bf16_f32 v137, v138, v139\n"
    "ds_read_b128 v[212:215], v250 offset:1200\n"
    "v_cvt_pk_bf16_f32 v138, v140, v141\n"
    "ds_read_b128 v[220:223], v252 offset:23856\n"
    "v_or_b32_e64 v195, v195, v203\n"
    "v_cvt_pk_bf16_f32 v139, v142, v143\n"
    "ds_write_b128 v242, v[136:139] offset:29760\n"
    "v_pk_mul_f16 v232, v192, v232\n"
    "v_mfma_f32_16x16x32_bf16 v[136:139], v[236:239], v[188:191], 0\n"
    "v_exp_f16_e64 v234, v232\n"
    "v_mfma_f32_16x16x32_bf16 v[140:143], a[244:247], v[188:191], 0\n"
    "v_exp_f16_sdwa v234, v232 dst_sel:WORD_1 dst_unused:UNUSED_PRESERVE src0_sel:WORD_1\n"
    "v_or_b32_e64 v197, v197, v205\n"
    "v_pk_fma_f16 v234, v234, s49, s49\n"
    "v_cvt_pk_f16_f32 v184, v168, v169\n"
    "v_lshlrev_b32_sdwa v185, 1, v170 dst_sel:DWORD dst_unused:UNUSED_PAD src0_sel:DWORD src1_sel:WORD_1\n"
    "v_lshlrev_b32_sdwa v201, 1, v171 dst_sel:DWORD dst_unused:UNUSED_PAD src0_sel:DWORD src1_sel:WORD_1\n"
    "v_mfma_f32_16x16x32_bf16 v[168:171], a[152:155], v[144:147], 0\n"
    "s_waitcnt lgkmcnt(7)\n"
    "ds_read_u16 v185, v185\n"
    "ds_read_u16_d16_hi v201, v201\n"
    "v_mfma_f32_16x16x32_bf16 v[168:171], a[200:203], v[148:151], v[168:171]\n"
    "s_nop 0\n"
    "ds_read_b128 v[236:239], v244 offset:4096\n"
    "v_lshlrev_b32_sdwa v186, 1, v172 dst_sel:DWORD dst_unused:UNUSED_PAD src0_sel:DWORD src1_sel:WORD_1\n"
    "v_lshlrev_b32_sdwa v202, 1, v173 dst_sel:DWORD dst_unused:UNUSED_PAD src0_sel:DWORD src1_sel:WORD_1\n"
    "v_lshlrev_b32_sdwa v187, 1, v174 dst_sel:DWORD dst_unused:UNUSED_PAD src0_sel:DWORD src1_sel:WORD_1\n"
    "v_lshlrev_b32_sdwa v203, 1, v175 dst_sel:DWORD dst_unused:UNUSED_PAD src0_sel:DWORD src1_sel:WORD_1\n"
    "v_rcp_f16_e32 v232, v234\n"
    "v_mfma_f32_16x16x32_bf16 v[168:171], a[112:115], v[152:155], v[168:171]\n"
    "s_waitcnt lgkmcnt(6)\n"
    "ds_read_u16 v186, v186\n"
    "ds_read_u16_d16_hi v202, v202\n"
    "v_or_b32_e64 v198, v198, v206\n"
    "s_waitcnt lgkmcnt(7)\n"
    "v_smfmac_f32_16x16x64_bf16 v[88:91], v[216:219], v[208:215], v240\n"
    "s_waitcnt lgkmcnt(6)\n"
    "ds_read_u16 v187, v187\n"
    "v_smfmac_f32_16x16x64_bf16 v[92:95], v[220:223], v[208:215], v240 abid:1\n"
    "ds_read_u16_d16_hi v203, v203\n"
    "v_rcp_f16_sdwa v232, v234 dst_sel:WORD_1 dst_unused:UNUSED_PRESERVE src0_sel:WORD_1\n"
    "v_mfma_f32_16x16x32_bf16 v[172:175], a[156:159], v[144:147], 0\n"
    "v_cvt_pk_f16_f32 v188, v176, v177\n"
    "v_lshlrev_b32_sdwa v189, 1, v178 dst_sel:DWORD dst_unused:UNUSED_PAD src0_sel:DWORD src1_sel:WORD_1\n"
    "v_lshlrev_b32_sdwa v205, 1, v179 dst_sel:DWORD dst_unused:UNUSED_PAD src0_sel:DWORD src1_sel:WORD_1\n"
    "v_lshlrev_b32_sdwa v190, 1, v180 dst_sel:DWORD dst_unused:UNUSED_PAD src0_sel:DWORD src1_sel:WORD_1\n"
    "v_lshlrev_b32_sdwa v206, 1, v181 dst_sel:DWORD dst_unused:UNUSED_PAD src0_sel:DWORD src1_sel:WORD_1\n"
    "v_pk_add_f32 v[224:225], v[224:225], v[88:89]\n"
    "v_pk_add_f32 v[226:227], v[226:227], v[90:91]\n"
    "v_pk_fma_f32 v[228:229], v[88:89], v[88:89], v[228:229]\n"
    "v_pk_fma_f32 v[230:231], v[90:91], v[90:91], v[230:231]\n"
    "v_pk_add_f32 v[224:225], v[224:225], v[92:93]\n"
    "v_pk_add_f32 v[226:227], v[226:227], v[94:95]\n"
    "v_pk_fma_f32 v[228:229], v[92:93], v[92:93], v[228:229]\n"
    "v_pk_fma_f32 v[230:231], v[94:95], v[94:95], v[230:231]\n"
    "v_mfma_f32_16x16x32_bf16 v[172:175], a[204:207], v[148:151], v[172:175]\n"
    "ds_read_b128 a[244:247], v244 offset:6144\n"
    "ds_write_b128 v254, v[224:227]\n"
    "v_lshlrev_b32_sdwa v191, 1, v182 dst_sel:DWORD dst_unused:UNUSED_PAD src0_sel:DWORD src1_sel:WORD_1\n"
    "v_fma_mix_f32 v234, v192, v232, 0 op_sel_hi:[1,1,0]\n"
    "v_mfma_f32_16x16x32_bf16 v[172:175], a[116:119], v[152:155], v[172:175]\n"
    "s_waitcnt lgkmcnt(6)\n"
    "v_or_b32_e32 v199, v199, v207\n"
    "v_lshlrev_b32_sdwa v207, 1, v183 dst_sel:DWORD dst_unused:UNUSED_PAD src0_sel:DWORD src1_sel:WORD_1\n"
    "v_fma_mix_f32 v232, v192, v232, 0 op_sel:[1,1,0] op_sel_hi:[1,1,0]\n"
    "v_mfma_f32_16x16x32_bf16 v[176:179], a[152:155], v[156:159], 0\n"
    "ds_read_u16 v189, v189\n"
    "ds_write_b128 v254, v[228:231] offset:1024\n"
    "v_lshrrev_b32_e64 v254, 2, v242\n"
    "v_cvt_pk_bf16_f32 v192, v234, v232\n"
    "v_mfma_f32_16x16x32_bf16 v[176:179], a[200:203], v[160:163], v[176:179]\n"
    "s_waitcnt lgkmcnt(6)\n"
    "v_add_u32_e32 v254, s55, v254\n"
    "v_pk_fma_f16 v232, v196, v196, s48\n"
    "v_mfma_f32_16x16x32_bf16 v[176:179], a[112:115], v[164:167], v[176:179]\n"
    "v_pk_mul_f16 v232, v196, v232\n"
    "v_or_b32_e64 v185, v185, v201\n"
    "v_mfma_f32_16x16x32_bf16 v[180:183], a[156:159], v[156:159], 0\n"
    "ds_read_u16_d16_hi v205, v205\n"
    "ds_read_u16 v190, v190\n"
    "v_mfma_f32_16x16x32_bf16 v[180:183], a[204:207], v[160:163], v[180:183]\n"
    "s_waitcnt lgkmcnt(5)\n"
    "ds_read_u16_d16_hi v206, v206\n"
    "ds_read_u16 v191, v191\n"
    "v_exp_f16_e32 v234, v232\n"
    "v_mfma_f32_16x16x32_bf16 v[180:183], a[116:119], v[164:167], v[180:183]\n"
    "v_exp_f16_sdwa v234, v232 dst_sel:WORD_1 dst_unused:UNUSED_PRESERVE src0_sel:WORD_1\n"
    "v_mfma_f32_16x16x32_bf16 v[96:99], a[16:19], v[192:195], v[96:99]\n"
    "ds_read_u16_d16_hi v207, v207\n"
    "v_pk_fma_f16 v234, v234, s49, s49\n"
    "v_mfma_f32_16x16x32_bf16 v[100:103], a[20:23], v[192:195], v[100:103]\n"
    "v_rcp_f16_e64 v232, v234\n"
    "v_mfma_f32_16x16x32_bf16 v[112:115], a[24:27], v[192:195], v[112:115]\n"
    "s_waitcnt lgkmcnt(5)\n"
    "s_barrier\n"
    "v_rcp_f16_sdwa v232, v234 dst_sel:WORD_1 dst_unused:UNUSED_PRESERVE src0_sel:WORD_1\n"
    "v_mfma_f32_16x16x32_bf16 v[116:119], a[28:31], v[192:195], v[116:119]\n"
    "ds_read_b32 v224, v254 offset:0\n"
    "ds_read_b32 v225, v254 offset:6752\n"
    "v_mfma_f32_16x16x32_bf16 v[128:131], v[236:239], v[192:195], v[128:131]\n"
    "ds_read_b32 v226, v254 offset:13504\n"
    "ds_read_b32 v227, v254 offset:20256\n"
    "ds_read_b32 v228, v254 offset:1024\n"
    "v_fma_mix_f32 v234, v196, v232, 0 op_sel_hi:[1,1,0]\n"
    "v_mfma_f32_16x16x32_bf16 v[132:135], a[244:247], v[192:195], v[132:135]\n"
    "s_nop 0\n"
    "s_waitcnt lgkmcnt(9)\n"
    "ds_read_b32 v229, v254 offset:7776\n"
    "ds_read_b32 v230, v254 offset:14528\n"
    "ds_read_b32 v231, v254 offset:21280\n"
    "v_fma_mix_f32 v232, v196, v232, 0 op_sel:[1,1,0] op_sel_hi:[1,1,0]\n"
    "v_or_b32_e64 v186, v186, v202\n"
    "v_cvt_pk_bf16_f32 v196, v234, v232\n"
    "v_pk_fma_f16 v232, v184, v184, s48\n"
    "v_or_b32_e64 v187, v187, v203\n"
    "v_mfma_f32_16x16x32_bf16 v[104:107], a[16:19], v[196:199], v[104:107]\n"
    "ds_read_b128 a[240:243], v244 offset:34816\n"
    "v_pk_mul_f16 v232, v184, v232\n"
    "v_mfma_f32_16x16x32_bf16 v[108:111], a[20:23], v[196:199], v[108:111]\n"
    "s_waitcnt lgkmcnt(11)\n"
    "v_exp_f16_e32 v234, v232\n"
    "v_mfma_f32_16x16x32_bf16 v[120:123], a[24:27], v[196:199], v[120:123]\n"
    "v_exp_f16_sdwa v234, v232 dst_sel:WORD_1 dst_unused:UNUSED_PRESERVE src0_sel:WORD_1\n"
    "v_mfma_f32_16x16x32_bf16 v[124:127], a[28:31], v[196:199], v[124:127]\n"
    "v_or_b32_e64 v189, v189, v205\n"
    "v_pk_fma_f16 v234, v234, s49, s49\n"
    "v_mfma_f32_16x16x32_bf16 v[136:139], v[236:239], v[196:199], v[136:139]\n"
    "s_waitcnt lgkmcnt(6)\n"
    "ds_read_b128 v[236:239], v244 offset:8192\n"
    "v_add_f32_e32 v224, v224, v225\n"
    "v_mfma_f32_16x16x32_bf16 v[140:143], a[244:247], v[196:199], v[140:143]\n"
    "s_waitcnt lgkmcnt(5)\n"
    "v_add_f32_e32 v226, v226, v227\n"
    "v_lshlrev_b32_sdwa v192, 1, v168 dst_sel:DWORD dst_unused:UNUSED_PAD src0_sel:DWORD src1_sel:WORD_1\n"
    "s_waitcnt vmcnt(3)\n"
    "v_smfmac_f32_32x32x32_bf16 v[112:127], a[248:251], a[232:239], v241\n"
    "s_waitcnt lgkmcnt(2)\n"
    "ds_read_u16 v192, v192\n"
    "ds_read_b128 a[232:235], v243 offset:24640\n"
    "v_add_f32_e64 v228, v228, v229\n"
    "v_add_f32_e64 v230, v230, v231\n"
    "v_add_f32_e32 v224, v224, v226\n"
    "s_waitcnt vmcnt(2)\n"
    "v_smfmac_f32_32x32x32_bf16 v[96:111], a[248:251], a[224:231], v241\n"
    "buffer_load_dwordx4 a[224:227], v245, s[40:43], s58 offen offset:128\n"
    "ds_read_b128 a[236:239], v243 offset:25664\n"
    "v_add_f32_e64 v228, v228, v230\n"
    "v_mul_f32_e32 v224, 0x3c2aaaab, v224\n"
    "v_mul_f32_e32 v228, 0x3c2aaaab, v228\n"
    "v_lshlrev_b32_sdwa v200, 1, v169 dst_sel:DWORD dst_unused:UNUSED_PAD src0_sel:DWORD src1_sel:WORD_1\n"
    "v_fma_f32 v228, -v224, v224, v228\n"
    "v_lshlrev_b32_sdwa v193, 1, v170 dst_sel:DWORD dst_unused:UNUSED_PAD src0_sel:DWORD src1_sel:WORD_1\n"
    "v_max_f32_e64 v228, 0, v228\n"
    "v_lshlrev_b32_sdwa v201, 1, v171 dst_sel:DWORD dst_unused:UNUSED_PAD src0_sel:DWORD src1_sel:WORD_1\n"
    "v_mfma_f32_16x16x32_bf16 v[168:171], a[160:163], v[144:147], 0\n"
    "ds_read_u16_d16_hi v200, v200\n"
    "ds_read_u16 v193, v193\n"
    "v_mfma_f32_16x16x32_bf16 v[168:171], a[208:211], v[148:151], v[168:171]\n"
    "ds_read_u16_d16_hi v201, v201\n"
    "v_add_f32_e32 v228, 0x3727c5ac, v228\n"
    "v_lshlrev_b32_sdwa v194, 1, v172 dst_sel:DWORD dst_unused:UNUSED_PAD src0_sel:DWORD src1_sel:WORD_1\n"
    "v_rsq_f32_e64 v228, v228\n"
    "v_lshlrev_b32_sdwa v202, 1, v173 dst_sel:DWORD dst_unused:UNUSED_PAD src0_sel:DWORD src1_sel:WORD_1\n"
    "v_mul_f32_e64 v228, s50, v228\n"
    "ds_write_b32 v254, v228\n"
    "ds_read_u16 v194, v194\n"
    "v_mul_f32_e64 v224, v228, -v224\n"
    "ds_write_b32 v254, v224 offset:1024\n"
    "ds_read_u16_d16_hi v202, v202\n"
    "v_add_u32_e64 v254, s56, v242\n"
    "v_lshlrev_b32_sdwa v195, 1, v174 dst_sel:DWORD dst_unused:UNUSED_PAD src0_sel:DWORD src1_sel:WORD_1\n"
    "v_lshlrev_b32_sdwa v203, 1, v175 dst_sel:DWORD dst_unused:UNUSED_PAD src0_sel:DWORD src1_sel:WORD_1\n"
    "v_rcp_f16_e64 v232, v234\n"
    "v_mfma_f32_16x16x32_bf16 v[168:171], a[120:123], v[152:155], v[168:171]\n"
    "ds_read_u16 v195, v195\n"
    "ds_read_b128 a[244:247], v244 offset:10240\n"
    "v_or_b32_e64 v190, v190, v206\n"
    "v_rcp_f16_sdwa v232, v234 dst_sel:WORD_1 dst_unused:UNUSED_PRESERVE src0_sel:WORD_1\n"
    "v_mfma_f32_16x16x32_bf16 v[172:175], a[164:167], v[144:147], 0\n"
    "buffer_load_dwordx4 a[228:231], v245, s[40:43], s101 offen offset:128\n"
    "ds_read_u16_d16_hi v203, v203\n"
    "v_mfma_f32_16x16x32_bf16 v[172:175], a[212:215], v[148:151], v[172:175]\n"
    "v_lshlrev_b32_sdwa v196, 1, v176 dst_sel:DWORD dst_unused:UNUSED_PAD src0_sel:DWORD src1_sel:WORD_1\n"
    "v_lshlrev_b32_sdwa v204, 1, v177 dst_sel:DWORD dst_unused:UNUSED_PAD src0_sel:DWORD src1_sel:WORD_1\n"
    "v_lshlrev_b32_sdwa v197, 1, v178 dst_sel:DWORD dst_unused:UNUSED_PAD src0_sel:DWORD src1_sel:WORD_1\n"
    "v_lshlrev_b32_sdwa v205, 1, v179 dst_sel:DWORD dst_unused:UNUSED_PAD src0_sel:DWORD src1_sel:WORD_1\n"
    "v_lshlrev_b32_sdwa v198, 1, v180 dst_sel:DWORD dst_unused:UNUSED_PAD src0_sel:DWORD src1_sel:WORD_1\n"
    "v_lshlrev_b32_sdwa v206, 1, v181 dst_sel:DWORD dst_unused:UNUSED_PAD src0_sel:DWORD src1_sel:WORD_1\n"
    "v_lshlrev_b32_sdwa v199, 1, v182 dst_sel:DWORD dst_unused:UNUSED_PAD src0_sel:DWORD src1_sel:WORD_1\n"
    "v_fma_mix_f32 v234, v184, v232, 0 op_sel_hi:[1,1,0]\n"
    "s_waitcnt lgkmcnt(8)\n"
    "v_mfma_f32_16x16x32_bf16 v[172:175], a[240:243], v[152:155], v[172:175]\n"
    "s_nop 0\n"
    "ds_read_u16 v196, v196\n"
    "ds_read_u16_d16_hi v204, v204\n"
    "v_or_b32_e64 v191, v191, v207\n"
    "v_lshlrev_b32_sdwa v207, 1, v183 dst_sel:DWORD dst_unused:UNUSED_PAD src0_sel:DWORD src1_sel:WORD_1\n"
    "v_fma_mix_f32 v232, v184, v232, 0 op_sel:[1,1,0] op_sel_hi:[1,1,0]\n"
    "v_mfma_f32_16x16x32_bf16 v[176:179], a[160:163], v[156:159], 0\n"
    "s_waitcnt lgkmcnt(6)\n"
    "ds_read_u16 v197, v197\n"
    "v_cvt_pk_bf16_f32 v184, v234, v232\n"
    "s_barrier\n"
    "v_mfma_f32_16x16x32_bf16 v[176:179], a[208:211], v[160:163], v[176:179]\n"
    "ds_read_b128 v[224:227], v254\n"
    "ds_read_b128 v[228:231], v254 offset:1024\n"
    "v_smfmac_f32_32x32x32_bf16 v[96:111], a[248:251], a[232:239], v241\n"
    "ds_read_u16_d16_hi v205, v205\n"
    "v_pk_fma_f16 v232, v188, v188, s48\n"
    "v_lshlrev_b32_e64 v254, 3, v244\n"
    "v_pk_mul_f16 v232, v188, v232\n"
    "v_or_b32_e64 v192, v192, v200\n"
    "v_mfma_f32_16x16x32_bf16 v[176:179], a[120:123], v[164:167], v[176:179]\n"
    "s_mov_b32 exec_hi, 0\n"
    "buffer_load_dword v233, v254, s[72:75], s53 offen sc1\n"
    "s_mov_b32 exec_hi, -1\n"
    "v_mfma_f32_16x16x32_bf16 v[180:183], a[164:167], v[156:159], 0\n"
    "s_add_u32 s53, s53, 393216\n"
    "ds_read_u16 v198, v198\n"
    "ds_read_u16_d16_hi v206, v206\n"
    "v_mfma_f32_16x16x32_bf16 v[180:183], a[212:215], v[160:163], v[180:183]\n"
    "v_exp_f16_e32 v234, v232\n"
    "v_mfma_f32_16x16x32_bf16 v[180:183], a[240:243], v[164:167], v[180:183]\n"
    "s_waitcnt lgkmcnt(8)\n"
    "ds_read_u16 v199, v199\n"
    "v_exp_f16_sdwa v234, v232 dst_sel:WORD_1 dst_unused:UNUSED_PRESERVE src0_sel:WORD_1\n"
    "v_mfma_f32_16x16x32_bf16 v[96:99], a[32:35], v[184:187], v[96:99]\n"
    "ds_read_u16_d16_hi v207, v207\n"
    "v_pk_fma_f16 v234, v234, s49, s49\n"
    "v_mfma_f32_16x16x32_bf16 v[100:103], a[36:39], v[184:187], v[100:103]\n"
    "v_rcp_f16_e64 v232, v234\n"
    "v_mfma_f32_16x16x32_bf16 v[112:115], a[40:43], v[184:187], v[112:115]\n"
    "ds_read_b128 a[232:235], v243 offset:26688\n"
    "v_rcp_f16_sdwa v232, v234 dst_sel:WORD_1 dst_unused:UNUSED_PRESERVE src0_sel:WORD_1\n"
    "v_mfma_f32_16x16x32_bf16 v[116:119], a[44:47], v[184:187], v[116:119]\n"
    "s_waitcnt lgkmcnt(7)\n"
    "ds_read_b128 a[236:239], v243 offset:27712\n"
    "v_fma_mix_f32 v234, v188, v232, 0 op_sel_hi:[1,1,0]\n"
    "v_mfma_f32_16x16x32_bf16 v[128:131], v[236:239], v[184:187], v[128:131]\n"
    "s_waitcnt lgkmcnt(7)\n"
    "ds_read_b128 a[240:243], v244 offset:38912\n"
    "v_fma_mix_f32 v232, v188, v232, 0 op_sel:[1,1,0] op_sel_hi:[1,1,0]\n"
    "v_or_b32_e64 v193, v193, v201\n"
    "v_cvt_pk_bf16_f32 v188, v234, v232\n"
    "v_lshlrev_b32_sdwa v201, 1, v171 dst_sel:DWORD dst_unused:UNUSED_PAD src0_sel:DWORD src1_sel:WORD_1\n"
    "v_pk_fma_f32 v[64:65], v[64:65], v[224:225], v[228:229]\n"
    "v_pk_fma_f32 v[66:67], v[66:67], v[226:227], v[230:231]\n"
    "v_pk_fma_f32 v[68:69], v[68:69], v[224:225], v[228:229]\n"
    "v_pk_fma_f32 v[70:71], v[70:71], v[226:227], v[230:231]\n"
    "v_pk_fma_f32 v[72:73], v[72:73], v[224:225], v[228:229]\n"
    "v_pk_fma_f32 v[74:75], v[74:75], v[226:227], v[230:231]\n"
    "v_pk_fma_f32 v[76:77], v[76:77], v[224:225], v[228:229]\n"
    "v_pk_fma_f32 v[78:79], v[78:79], v[226:227], v[230:231]\n"
    "v_pk_fma_f32 v[80:81], v[80:81], v[224:225], v[228:229]\n"
    "v_pk_fma_f32 v[82:83], v[82:83], v[226:227], v[230:231]\n"
    "v_pk_fma_f32 v[84:85], v[84:85], v[224:225], v[228:229]\n"
    "v_pk_fma_f32 v[86:87], v[86:87], v[226:227], v[230:231]\n"
    "v_pk_fma_f32 v[88:89], v[88:89], v[224:225], v[228:229]\n"
    "v_pk_fma_f32 v[90:91], v[90:91], v[226:227], v[230:231]\n"
    "v_pk_fma_f32 v[92:93], v[92:93], v[224:225], v[228:229]\n"
    "v_pk_fma_f32 v[94:95], v[94:95], v[226:227], v[230:231]\n"
    "v_pk_fma_f32 v[0:1], v[0:1], v[224:225], v[228:229]\n"
    "v_pk_fma_f32 v[2:3], v[2:3], v[226:227], v[230:231]\n"
    "v_pk_fma_f32 v[4:5], v[4:5], v[224:225], v[228:229]\n"
    "v_pk_fma_f32 v[6:7], v[6:7], v[226:227], v[230:231]\n"
    "v_pk_fma_f32 v[8:9], v[8:9], v[224:225], v[228:229]\n"
    "v_pk_fma_f32 v[10:11], v[10:11], v[226:227], v[230:231]\n"
    "v_pk_fma_f32 v[12:13], v[12:13], v[224:225], v[228:229]\n"
    "v_pk_fma_f32 v[14:15], v[14:15], v[226:227], v[230:231]\n"
    "v_pk_fma_f32 v[16:17], v[16:17], v[224:225], v[228:229]\n"
    "v_pk_fma_f32 v[18:19], v[18:19], v[226:227], v[230:231]\n"
    "v_pk_fma_f32 v[20:21], v[20:21], v[224:225], v[228:229]\n"
    "v_pk_fma_f32 v[22:23], v[22:23], v[226:227], v[230:231]\n"
    "v_pk_fma_f32 v[24:25], v[24:25], v[224:225], v[228:229]\n"
    "v_pk_fma_f32 v[26:27], v[26:27], v[226:227], v[230:231]\n"
    "v_pk_fma_f32 v[28:29], v[28:29], v[224:225], v[228:229]\n"
    "v_pk_fma_f32 v[30:31], v[30:31], v[226:227], v[230:231]\n"
    "v_pk_fma_f32 v[32:33], v[32:33], v[224:225], v[228:229]\n"
    "v_pk_fma_f32 v[34:35], v[34:35], v[226:227], v[230:231]\n"
    "v_pk_fma_f32 v[36:37], v[36:37], v[224:225], v[228:229]\n"
    "v_pk_fma_f32 v[38:39], v[38:39], v[226:227], v[230:231]\n"
    "v_pk_fma_f32 v[40:41], v[40:41], v[224:225], v[228:229]\n"
    "v_pk_fma_f32 v[42:43], v[42:43], v[226:227], v[230:231]\n"
    "v_pk_fma_f32 v[44:45], v[44:45], v[224:225], v[228:229]\n"
    "v_pk_fma_f32 v[46:47], v[46:47], v[226:227], v[230:231]\n"
    "v_pk_fma_f32 v[48:49], v[48:49], v[224:225], v[228:229]\n"
    "v_pk_fma_f32 v[50:51], v[50:51], v[226:227], v[230:231]\n"
    "v_pk_fma_f32 v[52:53], v[52:53], v[224:225], v[228:229]\n"
    "v_pk_fma_f32 v[54:55], v[54:55], v[226:227], v[230:231]\n"
    "v_pk_fma_f32 v[56:57], v[56:57], v[224:225], v[228:229]\n"
    "v_pk_fma_f32 v[58:59], v[58:59], v[226:227], v[230:231]\n"
    "v_pk_fma_f32 v[60:61], v[60:61], v[224:225], v[228:229]\n"
    "v_pk_fma_f32 v[62:63], v[62:63], v[226:227], v[230:231]\n"
    "v_mfma_f32_16x16x32_bf16 v[132:135], a[244:247], v[184:187], v[132:135]\n"
    "s_waitcnt lgkmcnt(0)\n"
    "ds_read_u16_d16_hi v201, v201\n"
    "v_cvt_pk_bf16_f32 v208, v64, v68\n"
    "v_mfma_f32_16x16x32_bf16 v[104:107], a[32:35], v[188:191], v[104:107]\n"
    "s_nop 0\n"
    "v_cvt_pk_bf16_f32 v212, v65, v69\n"
    "v_cvt_pk_bf16_f32 v216, v66, v70\n"
    "v_mfma_f32_16x16x32_bf16 v[108:111], a[36:39], v[188:191], v[108:111]\n"
    "v_cvt_pk_bf16_f32 v220, v67, v71\n"
    "v_cvt_pk_bf16_f32 v209, v72, v76\n"
    "v_mfma_f32_16x16x32_bf16 v[120:123], a[40:43], v[188:191], v[120:123]\n"
    "v_cvt_pk_bf16_f32 v213, v73, v77\n"
    "v_cvt_pk_bf16_f32 v217, v74, v78\n"
    "v_mfma_f32_16x16x32_bf16 v[124:127], a[44:47], v[188:191], v[124:127]\n"
    "v_cvt_pk_bf16_f32 v221, v75, v79\n"
    "v_cvt_pk_bf16_f32 v210, v80, v84\n"
    "v_mfma_f32_16x16x32_bf16 v[136:139], v[236:239], v[188:191], v[136:139]\n"
    "ds_read_b128 v[236:239], v244 offset:12288\n"
    "v_cvt_pk_bf16_f32 v214, v81, v85\n"
    "v_mfma_f32_16x16x32_bf16 v[140:143], a[244:247], v[188:191], v[140:143]\n"
    "v_cvt_pk_bf16_f32 v218, v82, v86\n"
    "v_cvt_pk_bf16_f32 v222, v83, v87\n"
    "v_smfmac_f32_32x32x32_bf16 v[112:127], a[248:251], a[232:239], v241\n"
    "v_cvt_pk_bf16_f32 v211, v88, v92\n"
    "ds_write_b128 v253, v[208:211] offset:2048\n"
    "v_cvt_pk_bf16_f32 v215, v89, v93\n"
    "v_cvt_pk_bf16_f32 v219, v90, v94\n"
    "v_cvt_pk_bf16_f32 v223, v91, v95\n"
    "v_cvt_pk_bf16_f32 v80, v0, v4\n"
    "v_cvt_pk_bf16_f32 v84, v1, v5\n"
    "v_cvt_pk_bf16_f32 v88, v2, v6\n"
    "v_cvt_pk_bf16_f32 v92, v3, v7\n"
    "v_cvt_pk_bf16_f32 v81, v8, v12\n"
    "v_cvt_pk_bf16_f32 v85, v9, v13\n"
    "v_cvt_pk_bf16_f32 v89, v10, v14\n"
    "v_cvt_pk_bf16_f32 v93, v11, v15\n"
    "v_cvt_pk_bf16_f32 v82, v16, v20\n"
    "v_cvt_pk_bf16_f32 v86, v17, v21\n"
    "v_cvt_pk_bf16_f32 v90, v18, v22\n"
    "v_cvt_pk_bf16_f32 v94, v19, v23\n"
    "v_cvt_pk_bf16_f32 v83, v24, v28\n"
    "v_cvt_pk_f16_f32 v184, v168, v169\n"
    "v_cvt_pk_bf16_f32 v87, v25, v29\n"
    "v_lshlrev_b32_sdwa v185, 1, v170 dst_sel:DWORD dst_unused:UNUSED_PAD src0_sel:DWORD src1_sel:WORD_1\n"
    "v_mfma_f32_16x16x32_bf16 v[168:171], a[168:171], v[144:147], 0\n"
    "ds_read_u16 v185, v185\n"
    "ds_write_b128 v253, v[212:215] offset:5120\n"
    "v_lshlrev_b32_sdwa v186, 1, v172 dst_sel:DWORD dst_unused:UNUSED_PAD src0_sel:DWORD src1_sel:WORD_1\n"
    "v_cvt_pk_bf16_f32 v91, v26, v30\n"
    "v_or_b32_e64 v194, v194, v202\n"
    "v_mfma_f32_16x16x32_bf16 v[168:171], a[216:219], v[148:151], v[168:171]\n"
    "v_lshlrev_b32_sdwa v202, 1, v173 dst_sel:DWORD dst_unused:UNUSED_PAD src0_sel:DWORD src1_sel:WORD_1\n"
    "v_lshlrev_b32_sdwa v187, 1, v174 dst_sel:DWORD dst_unused:UNUSED_PAD src0_sel:DWORD src1_sel:WORD_1\n"
    "v_mfma_f32_16x16x32_bf16 v[168:171], a[124:127], v[152:155], v[168:171]\n"
    "ds_read_u16 v186, v186\n"
    "ds_write_b128 v253, v[80:83] offset:0\n"
    "v_cvt_pk_bf16_f32 v95, v27, v31\n"
    "v_cvt_pk_f16_f32 v188, v176, v177\n"
    "v_lshlrev_b32_sdwa v189, 1, v178 dst_sel:DWORD dst_unused:UNUSED_PAD src0_sel:DWORD src1_sel:WORD_1\n"
    "v_lshlrev_b32_sdwa v190, 1, v180 dst_sel:DWORD dst_unused:UNUSED_PAD src0_sel:DWORD src1_sel:WORD_1\n"
    "v_lshlrev_b32_sdwa v191, 1, v182 dst_sel:DWORD dst_unused:UNUSED_PAD src0_sel:DWORD src1_sel:WORD_1\n"
    "v_or_b32_e64 v195, v195, v203\n"
    "v_lshlrev_b32_sdwa v203, 1, v175 dst_sel:DWORD dst_unused:UNUSED_PAD src0_sel:DWORD src1_sel:WORD_1\n"
    "v_mfma_f32_16x16x32_bf16 v[172:175], a[172:175], v[144:147], 0\n"
    "ds_read_u16_d16_hi v202, v202\n"
    "ds_write_b128 v253, v[84:87] offset:3072\n"
    "v_mfma_f32_16x16x32_bf16 v[172:175], a[220:223], v[148:151], v[172:175]\n"
    "s_nop 0\n"
    "v_cvt_pk_bf16_f32 v64, v32, v36\n"
    "v_cvt_pk_bf16_f32 v68, v33, v37\n"
    "v_mfma_f32_16x16x32_bf16 v[172:175], a[240:243], v[152:155], v[172:175]\n"
    "s_waitcnt lgkmcnt(4)\n"
    "v_cvt_pk_bf16_f32 v72, v34, v38\n"
    "v_cvt_pk_bf16_f32 v76, v35, v39\n"
    "v_cvt_pk_bf16_f32 v65, v40, v44\n"
    "v_or_b32_e64 v196, v196, v204\n"
    "v_cvt_pk_bf16_f32 v69, v41, v45\n"
    "v_cvt_pk_bf16_f32 v73, v42, v46\n"
    "v_cvt_pk_bf16_f32 v77, v43, v47\n"
    "v_cvt_pk_bf16_f32 v66, v48, v52\n"
    "v_cvt_pk_bf16_f32 v70, v49, v53\n"
    "v_or_b32_e64 v197, v197, v205\n"
    "v_lshlrev_b32_sdwa v205, 1, v179 dst_sel:DWORD dst_unused:UNUSED_PAD src0_sel:DWORD src1_sel:WORD_1\n"
    "v_mfma_f32_16x16x32_bf16 v[176:179], a[168:171], v[156:159], 0\n"
    "ds_read_u16 v187, v187\n"
    "ds_read_u16_d16_hi v203, v203\n"
    "v_mfma_f32_16x16x32_bf16 v[176:179], a[216:219], v[160:163], v[176:179]\n"
    "ds_read_b128 a[244:247], v244 offset:14336\n"
    "ds_read_u16 v189, v189\n"
    "v_mfma_f32_16x16x32_bf16 v[176:179], a[124:127], v[164:167], v[176:179]\n"
    "v_cvt_pk_bf16_f32 v74, v50, v54\n"
    "v_cvt_pk_bf16_f32 v78, v51, v55\n"
    "v_cvt_pk_bf16_f32 v67, v56, v60\n"
    "v_cvt_pk_bf16_f32 v71, v57, v61\n"
    "v_cvt_pk_bf16_f32 v75, v58, v62\n"
    "v_or_b32_e32 v198, v198, v206\n"
    "v_mfma_f32_16x16x32_bf16 v[96:99], a[48:51], v[192:195], v[96:99]\n"
    "s_waitcnt lgkmcnt(4)\n"
    "ds_read_b128 a[232:235], v243 offset:28736\n"
    "ds_write_b128 v253, v[64:67] offset:1024\n"
    "v_mfma_f32_16x16x32_bf16 v[100:103], a[52:55], v[192:195], v[100:103]\n"
    "v_lshlrev_b32_sdwa v206, 1, v181 dst_sel:DWORD dst_unused:UNUSED_PAD src0_sel:DWORD src1_sel:WORD_1\n"
    "v_cvt_pk_bf16_f32 v79, v59, v63\n"
    "v_mfma_f32_16x16x32_bf16 v[112:115], a[56:59], v[192:195], v[112:115]\n"
    "v_or_b32_e64 v199, v199, v207\n"
    "v_lshlrev_b32_sdwa v207, 1, v183 dst_sel:DWORD dst_unused:UNUSED_PAD src0_sel:DWORD src1_sel:WORD_1\n"
    "v_mfma_f32_16x16x32_bf16 v[180:183], a[172:175], v[156:159], 0\n"
    "ds_read_u16_d16_hi v205, v205\n"
    "ds_write_b128 v253, v[68:71] offset:4096\n"
    "v_mfma_f32_16x16x32_bf16 v[180:183], a[220:223], v[160:163], v[180:183]\n"
    "v_or_b32_e64 v185, v185, v201\n"
    "v_or_b32_e64 v186, v186, v202\n"
    "v_mfma_f32_16x16x32_bf16 v[180:183], a[240:243], v[164:167], v[180:183]\n"
    "v_pk_fma_f16 v232, v184, v184, s48\n"
    "v_lshlrev_b32_e32 v254, 3, v244\n"
    "v_mfma_f32_16x16x32_bf16 v[116:119], a[60:63], v[192:195], v[116:119]\n"
    "s_waitcnt lgkmcnt(5)\n"
    "ds_read_b128 a[236:239], v243 offset:29760\n"
    "ds_read_u16 v190, v190\n"
    "v_mfma_f32_16x16x32_bf16 v[128:131], v[236:239], v[192:195], v[128:131]\n"
    "ds_read_u16_d16_hi v206, v206\n"
    "s_mov_b32 exec_hi, 0\n"
    "buffer_load_dword v233, v254, s[72:75], s53 offen sc1\n"
    "s_mov_b32 exec_hi, -1\n"
    "v_mfma_f32_16x16x32_bf16 v[132:135], a[244:247], v[192:195], v[132:135]\n"
    "s_add_u32 s53, s53, 393216\n"
    "ds_read_u16 v191, v191\n"
    "v_or_b32_e64 v187, v187, v203\n"
    "v_mfma_f32_16x16x32_bf16 v[104:107], a[48:51], v[196:199], v[104:107]\n"
    "ds_read_u16_d16_hi v207, v207\n"
    "v_pk_mul_f16 v232, v184, v232\n"
    "v_mfma_f32_16x16x32_bf16 v[108:111], a[52:55], v[196:199], v[108:111]\n"
    "v_exp_f16_e64 v234, v232\n"
    "v_mfma_f32_16x16x32_bf16 v[120:123], a[56:59], v[196:199], v[120:123]\n"
    "ds_read_b128 a[240:243], v244 offset:32768\n"
    "v_exp_f16_sdwa v234, v232 dst_sel:WORD_1 dst_unused:UNUSED_PRESERVE src0_sel:WORD_1\n"
    "v_mfma_f32_16x16x32_bf16 v[124:127], a[60:63], v[196:199], v[124:127]\n"
    "v_pk_fma_f16 v234, v234, s49, s49\n"
    "v_cvt_pk_f16_f32 v192, v168, v169\n"
    "v_mfma_f32_16x16x32_bf16 v[136:139], v[236:239], v[196:199], v[136:139]\n"
    "s_waitcnt lgkmcnt(6)\n"
    "ds_read_b128 v[236:239], v244 offset:16384\n"
    "v_or_b32_e64 v189, v189, v205\n"
    "v_mfma_f32_16x16x32_bf16 v[140:143], a[244:247], v[196:199], v[140:143]\n"
    "s_barrier\n"
    "v_lshlrev_b32_sdwa v193, 1, v170 dst_sel:DWORD dst_unused:UNUSED_PAD src0_sel:DWORD src1_sel:WORD_1\n"
    "v_lshlrev_b32_sdwa v201, 1, v171 dst_sel:DWORD dst_unused:UNUSED_PAD src0_sel:DWORD src1_sel:WORD_1\n"
    "v_lshlrev_b32_sdwa v194, 1, v172 dst_sel:DWORD dst_unused:UNUSED_PAD src0_sel:DWORD src1_sel:WORD_1\n"
    "v_lshlrev_b32_sdwa v202, 1, v173 dst_sel:DWORD dst_unused:UNUSED_PAD src0_sel:DWORD src1_sel:WORD_1\n"
    "v_lshlrev_b32_sdwa v195, 1, v174 dst_sel:DWORD dst_unused:UNUSED_PAD src0_sel:DWORD src1_sel:WORD_1\n"
    "s_waitcnt lgkmcnt(6)\n"
    "v_smfmac_f32_32x32x32_bf16 v[128:143], a[248:251], a[232:239], v241\n"
    "s_nop 0\n"
    "ds_read_b128 v[144:147], v242 offset:0\n"
    "ds_read_b128 v[148:151], v242 offset:1024\n"
    "v_lshlrev_b32_sdwa v203, 1, v175 dst_sel:DWORD dst_unused:UNUSED_PAD src0_sel:DWORD src1_sel:WORD_1\n"
    "v_rcp_f16_e32 v232, v234\n"
    "s_waitcnt lgkmcnt(6)\n"
    "v_or_b32_e64 v190, v190, v206\n"
    "v_mov_b32_e64 v64, v216\n"
    "v_rcp_f16_sdwa v232, v234 dst_sel:WORD_1 dst_unused:UNUSED_PRESERVE src0_sel:WORD_1\n"
    "v_cvt_pk_f16_f32 v196, v176, v177\n"
    "v_mov_b32_e64 v65, v217\n"
    "v_lshlrev_b32_sdwa v197, 1, v178 dst_sel:DWORD dst_unused:UNUSED_PAD src0_sel:DWORD src1_sel:WORD_1\n"
    "v_mov_b32_e64 v66, v218\n"
    "v_lshlrev_b32_sdwa v205, 1, v179 dst_sel:DWORD dst_unused:UNUSED_PAD src0_sel:DWORD src1_sel:WORD_1\n"
    "v_mov_b32_e64 v67, v219\n"
    "v_lshlrev_b32_sdwa v198, 1, v180 dst_sel:DWORD dst_unused:UNUSED_PAD src0_sel:DWORD src1_sel:WORD_1\n"
    "v_mov_b32_e64 v68, v220\n"
    "v_lshlrev_b32_sdwa v206, 1, v181 dst_sel:DWORD dst_unused:UNUSED_PAD src0_sel:DWORD src1_sel:WORD_1\n"
    "v_mov_b32_e64 v69, v221\n"
    "v_lshlrev_b32_sdwa v199, 1, v182 dst_sel:DWORD dst_unused:UNUSED_PAD src0_sel:DWORD src1_sel:WORD_1\n"
    "v_mov_b32_e64 v70, v222\n"
    "v_mov_b32_e64 v71, v223\n"
    "v_fma_mix_f32 v234, v184, v232, 0 op_sel_hi:[1,1,0]\n"
    "s_waitcnt lgkmcnt(2)\n"
    "v_or_b32_e32 v191, v191, v207\n"
    "v_lshlrev_b32_sdwa v207, 1, v183 dst_sel:DWORD dst_unused:UNUSED_PAD src0_sel:DWORD src1_sel:WORD_1\n"
    "v_fma_mix_f32 v232, v184, v232, 0 op_sel:[1,1,0] op_sel_hi:[1,1,0]\n"
    "v_cvt_pk_bf16_f32 v184, v234, v232\n"
    "ds_read_b128 v[152:155], v242 offset:2048\n"
    "ds_read_b128 v[156:159], v242 offset:3072\n"
    "s_waitcnt lgkmcnt(3)\n"
    "v_mfma_f32_16x16x32_bf16 v[168:171], a[128:131], v[144:147], 0\n"
    "s_waitcnt lgkmcnt(2)\n"
    "ds_read_b128 v[160:163], v242 offset:4096\n"
    "ds_read_b128 v[164:167], v242 offset:5120\n"
    "v_mfma_f32_16x16x32_bf16 v[168:171], a[176:179], v[148:151], v[168:171]\n"
    "ds_read_u16 v193, v193\n"
    "ds_read_u16_d16_hi v201, v201\n"
    "v_mfma_f32_16x16x32_bf16 v[172:175], a[132:135], v[144:147], 0\n"
    "ds_read_u16 v194, v194\n"
    "v_pk_fma_f16 v232, v188, v188, s48\n"
    "v_mfma_f32_16x16x32_bf16 v[172:175], a[180:183], v[148:151], v[172:175]\n"
    "v_pk_mul_f16 v232, v188, v232\n"
    "v_exp_f16_e64 v234, v232\n"
    "ds_read_u16_d16_hi v202, v202\n"
    "v_exp_f16_sdwa v234, v232 dst_sel:WORD_1 dst_unused:UNUSED_PRESERVE src0_sel:WORD_1\n"
    "v_mfma_f32_16x16x32_bf16 v[96:99], a[64:67], v[184:187], v[96:99]\n"
    "ds_read_u16 v195, v195\n"
    "v_pk_fma_f16 v234, v234, s49, s49\n"
    "v_mfma_f32_16x16x32_bf16 v[100:103], a[68:71], v[184:187], v[100:103]\n"
    "s_waitcnt lgkmcnt(8)\n"
    "v_rcp_f16_e64 v232, v234\n"
    "v_mfma_f32_16x16x32_bf16 v[168:171], a[96:99], v[152:155], v[168:171]\n"
    "s_waitcnt lgkmcnt(7)\n"
    "ds_read_u16_d16_hi v203, v203\n"
    "v_rcp_f16_sdwa v232, v234 dst_sel:WORD_1 dst_unused:UNUSED_PRESERVE src0_sel:WORD_1\n"
    "v_mfma_f32_16x16x32_bf16 v[172:175], a[240:243], v[152:155], v[172:175]\n"
    "s_nop 0\n"
    "ds_read_b128 a[244:247], v244 offset:18432\n"
    "v_fma_mix_f32 v234, v188, v232, 0 op_sel_hi:[1,1,0]\n"
    "v_mfma_f32_16x16x32_bf16 v[176:179], a[128:131], v[156:159], 0\n"
    "s_waitcnt lgkmcnt(7)\n"
    "ds_read_u16 v197, v197\n"
    "v_fma_mix_f32 v232, v188, v232, 0 op_sel:[1,1,0] op_sel_hi:[1,1,0]\n"
    "v_mfma_f32_16x16x32_bf16 v[176:179], a[176:179], v[160:163], v[176:179]\n"
    "v_cvt_pk_bf16_f32 v188, v234, v232\n"
    "v_pk_fma_f16 v232, v192, v192, s48\n"
    "v_mfma_f32_16x16x32_bf16 v[176:179], a[96:99], v[164:167], v[176:179]\n"
    "ds_read_u16_d16_hi v205, v205\n"
    "v_pk_mul_f16 v232, v192, v232\n"
    "v_mfma_f32_16x16x32_bf16 v[180:183], a[132:135], v[156:159], 0\n"
    "v_exp_f16_e64 v234, v232\n"
    "v_mfma_f32_16x16x32_bf16 v[180:183], a[180:183], v[160:163], v[180:183]\n"
    "ds_read_u16 v198, v198\n"
    "s_waitcnt lgkmcnt(8)\n"
    "v_or_b32_e32 v193, v193, v201\n"
    "v_mfma_f32_16x16x32_bf16 v[180:183], a[240:243], v[164:167], v[180:183]\n"
    "ds_read_u16_d16_hi v206, v206\n"
    "v_exp_f16_sdwa v234, v232 dst_sel:WORD_1 dst_unused:UNUSED_PRESERVE src0_sel:WORD_1\n"
    "v_mfma_f32_16x16x32_bf16 v[112:115], a[72:75], v[184:187], v[112:115]\n"
    "v_pk_fma_f16 v234, v234, s49, s49\n"
    "v_rcp_f16_e32 v232, v234\n"
    "v_mfma_f32_16x16x32_bf16 v[116:119], a[76:79], v[184:187], v[116:119]\n"
    "s_waitcnt lgkmcnt(7)\n"
    "ds_read_u16 v199, v199\n"
    "v_or_b32_e64 v194, v194, v202\n"
    "v_mfma_f32_16x16x32_bf16 v[128:131], v[236:239], v[184:187], v[128:131]\n"
    "ds_read_u16_d16_hi v207, v207\n"
    "v_rcp_f16_sdwa v232, v234 dst_sel:WORD_1 dst_unused:UNUSED_PRESERVE src0_sel:WORD_1\n"
    "v_mfma_f32_16x16x32_bf16 v[104:107], a[64:67], v[188:191], v[104:107]\n"
    "v_fma_mix_f32 v234, v192, v232, 0 op_sel_hi:[1,1,0]\n"
    "v_fma_mix_f32 v232, v192, v232, 0 op_sel:[1,1,0] op_sel_hi:[1,1,0]\n"
    "v_mfma_f32_16x16x32_bf16 v[108:111], a[68:71], v[188:191], v[108:111]\n"
    "s_waitcnt lgkmcnt(6)\n"
    "v_or_b32_e32 v195, v195, v203\n"
    "v_cvt_pk_bf16_f32 v192, v234, v232\n"
    "v_mfma_f32_16x16x32_bf16 v[132:135], a[244:247], v[184:187], v[132:135]\n"
    "v_pk_fma_f16 v232, v196, v196, s48\n"
    "v_pk_mul_f16 v232, v196, v232\n"
    "v_mfma_f32_16x16x32_bf16 v[120:123], a[72:75], v[188:191], v[120:123]\n"
    "v_exp_f16_e32 v234, v232\n"
    "v_mfma_f32_16x16x32_bf16 v[124:127], a[76:79], v[188:191], v[124:127]\n"
    "v_exp_f16_sdwa v234, v232 dst_sel:WORD_1 dst_unused:UNUSED_PRESERVE src0_sel:WORD_1\n"
    "v_mfma_f32_16x16x32_bf16 v[136:139], v[236:239], v[188:191], v[136:139]\n"
    "s_waitcnt lgkmcnt(4)\n"
    "v_or_b32_e32 v197, v197, v205\n"
    "s_waitcnt lgkmcnt(2)\n"
    "v_or_b32_e32 v198, v198, v206\n"
    "s_waitcnt lgkmcnt(0)\n"
    "v_or_b32_e32 v199, v199, v207\n"
    "s_mov_b32 s59, s58\n"
    "s_add_u32 s57, s57, 1572864\n"
    "s_add_u32 s58, s58, 1572864\n"
    "s_mov_b32 s70, s46\n"
    "s_add_u32 s100, s57, 98304\n"
    "s_add_u32 s101, s58, 98304\n"
    "s_add_u32 s39, s59, 98304\n"
    "s_add_u32 s64, s64, 1\n"
    "s_cmp_lt_u32 s64, 2\n"
    "s_cselect_b64 s[72:73], s[72:73], s[76:77]\n"
    "s_cselect_b32 s74, s74, s78\n"
    "s_cselect_b32 s53, s53, s79\n"
    "s_cmp_lg_u32 s64, 3\n"
    "s_cbranch_scc1 1b\n"
    ".Lreplk_last:\n"
    "v_lshlrev_b32_sdwa v184, 1, v168 dst_sel:DWORD dst_unused:UNUSED_PAD src0_sel:DWORD src1_sel:WORD_1\n"
    "v_lshlrev_b32_sdwa v200, 1, v169 dst_sel:DWORD dst_unused:UNUSED_PAD src0_sel:DWORD src1_sel:WORD_1\n"
    "v_lshlrev_b32_sdwa v185, 1, v170 dst_sel:DWORD dst_unused:UNUSED_PAD src0_sel:DWORD src1_sel:WORD_1\n"
    "v_lshlrev_b32_sdwa v201, 1, v171 dst_sel:DWORD dst_unused:UNUSED_PAD src0_sel:DWORD src1_sel:WORD_1\n"
    "v_mfma_f32_16x16x32_bf16 v[140:143], a[244:247], v[188:191], v[140:143]\n"
    "ds_read_b128 a[240:243], v244 offset:36864\n"
    "ds_read_u16 v184, v184\n"
    "v_mfma_f32_16x16x32_bf16 v[168:171], a[136:139], v[144:147], 0\n"
    "ds_read_u16_d16_hi v200, v200\n"
    "ds_read_u16 v185, v185\n"
    "v_mfma_f32_16x16x32_bf16 v[168:171], a[184:187], v[148:151], v[168:171]\n"
    "v_lshlrev_b32_sdwa v186, 1, v172 dst_sel:DWORD dst_unused:UNUSED_PAD src0_sel:DWORD src1_sel:WORD_1\n"
    "v_lshlrev_b32_sdwa v202, 1, v173 dst_sel:DWORD dst_unused:UNUSED_PAD src0_sel:DWORD src1_sel:WORD_1\n"
    "v_mfma_f32_16x16x32_bf16 v[168:171], a[100:103], v[152:155], v[168:171]\n"
    "ds_read_u16_d16_hi v201, v201\n"
    "v_lshlrev_b32_sdwa v187, 1, v174 dst_sel:DWORD dst_unused:UNUSED_PAD src0_sel:DWORD src1_sel:WORD_1\n"
    "s_waitcnt vmcnt(2)\n"
    "v_smfmac_f32_32x32x32_bf16 v[128:143], a[248:251], a[224:231], v241\n"
    "s_nop 0\n"
    "ds_read_u16 v186, v186\n"
    "v_lshlrev_b32_sdwa v203, 1, v175 dst_sel:DWORD dst_unused:UNUSED_PAD src0_sel:DWORD src1_sel:WORD_1\n"
    "v_lshlrev_b32_sdwa v188, 1, v176 dst_sel:DWORD dst_unused:UNUSED_PAD src0_sel:DWORD src1_sel:WORD_1\n"
    "v_lshlrev_b32_sdwa v204, 1, v177 dst_sel:DWORD dst_unused:UNUSED_PAD src0_sel:DWORD src1_sel:WORD_1\n"
    "v_lshlrev_b32_sdwa v189, 1, v178 dst_sel:DWORD dst_unused:UNUSED_PAD src0_sel:DWORD src1_sel:WORD_1\n"
    "v_mfma_f32_16x16x32_bf16 v[172:175], a[140:143], v[144:147], 0\n"
    "ds_read_b128 v[236:239], v244 offset:20480\n"
    "v_lshlrev_b32_sdwa v205, 1, v179 dst_sel:DWORD dst_unused:UNUSED_PAD src0_sel:DWORD src1_sel:WORD_1\n"
    "v_mfma_f32_16x16x32_bf16 v[172:175], a[188:191], v[148:151], v[172:175]\n"
    "s_nop 0\n"
    "ds_read_u16_d16_hi v202, v202\n"
    "v_lshlrev_b32_sdwa v190, 1, v180 dst_sel:DWORD dst_unused:UNUSED_PAD src0_sel:DWORD src1_sel:WORD_1\n"
    "v_mfma_f32_16x16x32_bf16 v[176:179], a[136:139], v[156:159], 0\n"
    "s_waitcnt lgkmcnt(7)\n"
    "v_lshlrev_b32_sdwa v206, 1, v181 dst_sel:DWORD dst_unused:UNUSED_PAD src0_sel:DWORD src1_sel:WORD_1\n"
    "v_lshlrev_b32_sdwa v191, 1, v182 dst_sel:DWORD dst_unused:UNUSED_PAD src0_sel:DWORD src1_sel:WORD_1\n"
    "v_mfma_f32_16x16x32_bf16 v[172:175], a[240:243], v[152:155], v[172:175]\n"
    "ds_read_u16 v187, v187\n"
    "v_lshlrev_b32_sdwa v207, 1, v183 dst_sel:DWORD dst_unused:UNUSED_PAD src0_sel:DWORD src1_sel:WORD_1\n"
    "v_mfma_f32_16x16x32_bf16 v[176:179], a[184:187], v[160:163], v[176:179]\n"
    "ds_read_b128 a[244:247], v244 offset:22528\n"
    "v_pk_fma_f16 v234, v234, s49, s49\n"
    "v_mfma_f32_16x16x32_bf16 v[176:179], a[100:103], v[164:167], v[176:179]\n"
    "ds_read_u16_d16_hi v203, v203\n"
    "v_rcp_f16_e32 v232, v234\n"
    "v_mfma_f32_16x16x32_bf16 v[180:183], a[140:143], v[156:159], 0\n"
    "v_rcp_f16_sdwa v232, v234 dst_sel:WORD_1 dst_unused:UNUSED_PRESERVE src0_sel:WORD_1\n"
    "v_mfma_f32_16x16x32_bf16 v[180:183], a[188:191], v[160:163], v[180:183]\n"
    "s_waitcnt lgkmcnt(8)\n"
    "ds_read_u16 v188, v188\n"
    "v_fma_mix_f32 v234, v196, v232, 0 op_sel_hi:[1,1,0]\n"
    "v_mfma_f32_16x16x32_bf16 v[180:183], a[240:243], v[164:167], v[180:183]\n"
    "v_fma_mix_f32 v232, v196, v232, 0 op_sel:[1,1,0] op_sel_hi:[1,1,0]\n"
    "v_or_b32_e64 v184, v184, v200\n"
    "v_mfma_f32_16x16x32_bf16 v[96:99], a[80:83], v[192:195], v[96:99]\n"
    "ds_read_u16_d16_hi v204, v204\n"
    "v_cvt_pk_bf16_f32 v196, v234, v232\n"
    "v_mfma_f32_16x16x32_bf16 v[100:103], a[84:87], v[192:195], v[100:103]\n"
    "s_waitcnt lgkmcnt(8)\n"
    "ds_read_u16 v189, v189\n"
    "v_or_b32_e32 v185, v185, v201\n"
    "v_mfma_f32_16x16x32_bf16 v[112:115], a[88:91], v[192:195], v[112:115]\n"
    "s_nop 0\n"
    "v_lshlrev_b32_sdwa v201, 1, v171 dst_sel:DWORD dst_unused:UNUSED_PAD src0_sel:DWORD src1_sel:WORD_1\n"
    "v_cvt_pk_bf16_f32 v96, v96, v97\n"
    "v_mfma_f32_16x16x32_bf16 v[116:119], a[92:95], v[192:195], v[116:119]\n"
    "s_waitcnt lgkmcnt(7)\n"
    "ds_read_u16_d16_hi v205, v205\n"
    "v_cvt_pk_bf16_f32 v97, v98, v99\n"
    "v_mfma_f32_16x16x32_bf16 v[128:131], v[236:239], v[192:195], v[128:131]\n"
    "v_cvt_pk_bf16_f32 v98, v100, v101\n"
    "v_cvt_pk_bf16_f32 v99, v102, v103\n"
    "v_mfma_f32_16x16x32_bf16 v[104:107], a[80:83], v[196:199], v[104:107]\n"
    "ds_read_u16 v190, v190\n"
    "s_waitcnt lgkmcnt(8)\n"
    "v_or_b32_e32 v186, v186, v202\n"
    "v_mfma_f32_16x16x32_bf16 v[108:111], a[84:87], v[196:199], v[108:111]\n"
    "ds_read_u16_d16_hi v206, v206\n"
    "v_lshlrev_b32_sdwa v202, 1, v173 dst_sel:DWORD dst_unused:UNUSED_PAD src0_sel:DWORD src1_sel:WORD_1\n"
    "s_waitcnt lgkmcnt(7)\n"
    "v_mfma_f32_16x16x32_bf16 v[132:135], a[244:247], v[192:195], v[132:135]\n"
    "s_nop 0\n"
    "v_cvt_pk_f16_f32 v192, v168, v169\n"
    "v_lshlrev_b32_sdwa v193, 1, v170 dst_sel:DWORD dst_unused:UNUSED_PAD src0_sel:DWORD src1_sel:WORD_1\n"
    "v_mfma_f32_16x16x32_bf16 v[120:123], a[88:91], v[196:199], v[120:123]\n"
    "ds_read_u16 v191, v191\n"
    "v_lshlrev_b32_sdwa v194, 1, v172 dst_sel:DWORD dst_unused:UNUSED_PAD src0_sel:DWORD src1_sel:WORD_1\n"
    "v_mfma_f32_16x16x32_bf16 v[124:127], a[92:95], v[196:199], v[124:127]\n"
    "v_lshlrev_b32_sdwa v195, 1, v174 dst_sel:DWORD dst_unused:UNUSED_PAD src0_sel:DWORD src1_sel:WORD_1\n"
    "s_waitcnt lgkmcnt(7)\n"
    "v_or_b32_e32 v187, v187, v203\n"
    "v_mfma_f32_16x16x32_bf16 v[136:139], v[236:239], v[196:199], v[136:139]\n"
    "ds_read_u16_d16_hi v207, v207\n"
    "v_lshlrev_b32_sdwa v203, 1, v175 dst_sel:DWORD dst_unused:UNUSED_PAD src0_sel:DWORD src1_sel:WORD_1\n"
    "v_mfma_f32_16x16x32_bf16 v[140:143], a[244:247], v[196:199], v[140:143]\n"
    "ds_read_u16 v193, v193\n"
    "v_cvt_pk_f16_f32 v196, v176, v177\n"
    "v_mfma_f32_16x16x32_bf16 v[168:171], a[144:147], v[144:147], 0\n"
    "v_lshlrev_b32_sdwa v197, 1, v178 dst_sel:DWORD dst_unused:UNUSED_PAD src0_sel:DWORD src1_sel:WORD_1\n"
    "v_lshlrev_b32_sdwa v198, 1, v180 dst_sel:DWORD dst_unused:UNUSED_PAD src0_sel:DWORD src1_sel:WORD_1\n"
    "v_mfma_f32_16x16x32_bf16 v[168:171], a[192:195], v[148:151], v[168:171]\n"
    "s_waitcnt lgkmcnt(7)\n"
    "ds_read_u16_d16_hi v201, v201\n"
    "v_lshlrev_b32_sdwa v199, 1, v182 dst_sel:DWORD dst_unused:UNUSED_PAD src0_sel:DWORD src1_sel:WORD_1\n"
    "v_mfma_f32_16x16x32_bf16 v[168:171], a[104:107], v[152:155], v[168:171]\n"
    "v_or_b32_e32 v188, v188, v204\n"
    "v_cvt_pk_bf16_f32 v112, v112, v113\n"
    "v_mfma_f32_16x16x32_bf16 v[172:175], a[148:151], v[144:147], 0\n"
    "ds_read_u16 v194, v194\n"
    "v_cvt_pk_bf16_f32 v113, v114, v115\n"
    "v_mfma_f32_16x16x32_bf16 v[172:175], a[196:199], v[148:151], v[172:175]\n"
    "s_waitcnt lgkmcnt(7)\n"
    "ds_read_u16_d16_hi v202, v202\n"
    "v_or_b32_e32 v189, v189, v205\n"
    "v_mfma_f32_16x16x32_bf16 v[172:175], a[108:111], v[152:155], v[172:175]\n"
    "v_lshlrev_b32_sdwa v205, 1, v179 dst_sel:DWORD dst_unused:UNUSED_PAD src0_sel:DWORD src1_sel:WORD_1\n"
    "v_cvt_pk_bf16_f32 v114, v116, v117\n"
    "v_mfma_f32_16x16x32_bf16 v[176:179], a[144:147], v[156:159], 0\n"
    "ds_read_b128 v[236:239], v244 offset:0\n"
    "v_cvt_pk_bf16_f32 v115, v118, v119\n"
    "v_mfma_f32_16x16x32_bf16 v[176:179], a[192:195], v[160:163], v[176:179]\n"
    "ds_read_u16 v195, v195\n"
    "v_cvt_pk_bf16_f32 v128, v128, v129\n"
    "v_mfma_f32_16x16x32_bf16 v[176:179], a[104:107], v[164:167], v[176:179]\n"
    "s_waitcnt lgkmcnt(8)\n"
    "v_or_b32_e32 v190, v190, v206\n"
    "v_lshlrev_b32_sdwa v206, 1, v181 dst_sel:DWORD dst_unused:UNUSED_PAD src0_sel:DWORD src1_sel:WORD_1\n"
    "v_mfma_f32_16x16x32_bf16 v[100:103], a[4:7], v[184:187], 0\n"
    "ds_read_u16_d16_hi v203, v203\n"
    "v_cvt_pk_bf16_f32 v129, v130, v131\n"
    "v_cvt_pk_bf16_f32 v130, v132, v133\n"
    "v_cvt_pk_bf16_f32 v131, v134, v135\n"
    "v_cvt_pk_bf16_f32 v104, v104, v105\n"
    "ds_write_b128 v243, v[96:99] offset:24640\n"
    "v_mfma_f32_16x16x32_bf16 v[96:99], a[0:3], v[184:187], 0\n"
    "s_waitcnt lgkmcnt(8)\n"
    "v_or_b32_e32 v191, v191, v207\n"
    "v_lshlrev_b32_sdwa v207, 1, v183 dst_sel:DWORD dst_unused:UNUSED_PAD src0_sel:DWORD src1_sel:WORD_1\n"
    "v_mfma_f32_16x16x32_bf16 v[180:183], a[148:151], v[156:159], 0\n"
    "ds_read_b128 a[244:247], v244 offset:2048\n"
    "ds_write_b128 v243, v[112:115] offset:26688\n"
    "v_mfma_f32_16x16x32_bf16 v[180:183], a[196:199], v[160:163], v[180:183]\n"
    "s_waitcnt lgkmcnt(8)\n"
    "v_or_b32_e32 v193, v193, v201\n"
    "v_cvt_pk_bf16_f32 v105, v106, v107\n"
    "v_mfma_f32_16x16x32_bf16 v[180:183], a[108:111], v[164:167], v[180:183]\n"
    "v_cvt_pk_bf16_f32 v106, v108, v109\n"
    "v_cvt_pk_bf16_f32 v107, v110, v111\n"
    "v_mfma_f32_16x16x32_bf16 v[112:115], a[8:11], v[184:187], 0\n"
    "ds_read_u16 v197, v197\n"
    "ds_write_b128 v243, v[128:131] offset:28736\n"
    "v_mfma_f32_16x16x32_bf16 v[116:119], a[12:15], v[184:187], 0\n"
    "s_waitcnt lgkmcnt(7)\n"
    "v_or_b32_e32 v194, v194, v202\n"
    "v_cvt_pk_bf16_f32 v120, v120, v121\n"
    "v_mfma_f32_16x16x32_bf16 v[128:131], v[236:239], v[184:187], 0\n"
    "v_cvt_pk_bf16_f32 v121, v122, v123\n"
    "v_cvt_pk_bf16_f32 v122, v124, v125\n"
    "v_mfma_f32_16x16x32_bf16 v[108:111], a[4:7], v[188:191], 0\n"
    "ds_read_u16_d16_hi v205, v205\n"
    "ds_write_b128 v243, v[104:107] offset:25664\n"
    "v_mfma_f32_16x16x32_bf16 v[104:107], a[0:3], v[188:191], 0\n"
    "s_waitcnt lgkmcnt(6)\n"
    "v_cvt_pk_bf16_f32 v123, v126, v127\n"
    "v_pk_fma_f16 v232, v192, v192, s48\n"
    "v_mfma_f32_16x16x32_bf16 v[124:127], a[12:15], v[188:191], 0\n"
    "s_waitcnt lgkmcnt(5)\n"
    "v_cvt_pk_bf16_f32 v136, v136, v137\n"
    "v_cvt_pk_bf16_f32 v137, v138, v139\n"
    "v_mfma_f32_16x16x32_bf16 v[132:135], a[244:247], v[184:187], 0\n"
    "ds_read_u16 v198, v198\n"
    "ds_write_b128 v243, v[120:123] offset:27712\n"
    "v_mfma_f32_16x16x32_bf16 v[120:123], a[8:11], v[188:191], 0\n"
    "v_cvt_pk_bf16_f32 v138, v140, v141\n"
    "v_or_b32_e64 v195, v195, v203\n"
    "v_cvt_pk_bf16_f32 v139, v142, v143\n"
    "v_pk_mul_f16 v232, v192, v232\n"
    "v_cvt_pk_f16_f32 v184, v168, v169\n"
    "v_exp_f16_e64 v234, v232\n"
    "v_mfma_f32_16x16x32_bf16 v[140:143], a[244:247], v[188:191], 0\n"
    "ds_read_u16_d16_hi v206, v206\n"
    "ds_write_b128 v243, v[136:139] offset:29760\n"
    "v_mfma_f32_16x16x32_bf16 v[136:139], v[236:239], v[188:191], 0\n"
    "v_exp_f16_sdwa v234, v232 dst_sel:WORD_1 dst_unused:UNUSED_PRESERVE src0_sel:WORD_1\n"
    "v_lshlrev_b32_sdwa v185, 1, v170 dst_sel:DWORD dst_unused:UNUSED_PAD src0_sel:DWORD src1_sel:WORD_1\n"
    "v_pk_fma_f16 v234, v234, s49, s49\n"
    "v_lshlrev_b32_sdwa v201, 1, v171 dst_sel:DWORD dst_unused:UNUSED_PAD src0_sel:DWORD src1_sel:WORD_1\n"
    "v_mfma_f32_16x16x32_bf16 v[168:171], a[152:155], v[144:147], 0\n"
    "s_waitcnt lgkmcnt(4)\n"
    "ds_read_u16 v199, v199\n"
    "ds_read_u16_d16_hi v207, v207\n"
    "v_mfma_f32_16x16x32_bf16 v[168:171], a[200:203], v[148:151], v[168:171]\n"
    "s_nop 0\n"
    "ds_read_b128 a[252:255], v249 offset:0\n"
    "ds_read_u16 v185, v185\n"
    "v_or_b32_e64 v197, v197, v205\n"
    "v_lshlrev_b32_sdwa v186, 1, v172 dst_sel:DWORD dst_unused:UNUSED_PAD src0_sel:DWORD src1_sel:WORD_1\n"
    "v_lshlrev_b32_sdwa v202, 1, v173 dst_sel:DWORD dst_unused:UNUSED_PAD src0_sel:DWORD src1_sel:WORD_1\n"
    "v_lshlrev_b32_sdwa v187, 1, v174 dst_sel:DWORD dst_unused:UNUSED_PAD src0_sel:DWORD src1_sel:WORD_1\n"
    "v_lshlrev_b32_sdwa v203, 1, v175 dst_sel:DWORD dst_unused:UNUSED_PAD src0_sel:DWORD src1_sel:WORD_1\n"
    "v_rcp_f16_e64 v232, v234\n"
    "v_mfma_f32_16x16x32_bf16 v[168:171], a[112:115], v[152:155], v[168:171]\n"
    "ds_read_u16_d16_hi v201, v201\n"
    "v_rcp_f16_sdwa v232, v234 dst_sel:WORD_1 dst_unused:UNUSED_PRESERVE src0_sel:WORD_1\n"
    "v_mfma_f32_16x16x32_bf16 v[172:175], a[156:159], v[144:147], 0\n"
    "ds_read_u16 v186, v186\n"
    "v_cvt_pk_f16_f32 v188, v176, v177\n"
    "v_mfma_f32_16x16x32_bf16 v[172:175], a[204:207], v[148:151], v[172:175]\n"
    "v_lshlrev_b32_sdwa v189, 1, v178 dst_sel:DWORD dst_unused:UNUSED_PAD src0_sel:DWORD src1_sel:WORD_1\n"
    "v_lshlrev_b32_sdwa v205, 1, v179 dst_sel:DWORD dst_unused:UNUSED_PAD src0_sel:DWORD src1_sel:WORD_1\n"
    "v_lshlrev_b32_sdwa v190, 1, v180 dst_sel:DWORD dst_unused:UNUSED_PAD src0_sel:DWORD src1_sel:WORD_1\n"
    "v_lshlrev_b32_sdwa v191, 1, v182 dst_sel:DWORD dst_unused:UNUSED_PAD src0_sel:DWORD src1_sel:WORD_1\n"
    "s_waitcnt lgkmcnt(6)\n"
    "v_or_b32_e32 v198, v198, v206\n"
    "v_lshlrev_b32_sdwa v206, 1, v181 dst_sel:DWORD dst_unused:UNUSED_PAD src0_sel:DWORD src1_sel:WORD_1\n"
    "v_fma_mix_f32 v234, v192, v232, 0 op_sel_hi:[1,1,0]\n"
    "v_mfma_f32_16x16x32_bf16 v[172:175], a[116:119], v[152:155], v[172:175]\n"
    "ds_read_u16_d16_hi v202, v202\n"
    "ds_read_b128 v[236:239], v244 offset:4096\n"
    "v_fma_mix_f32 v232, v192, v232, 0 op_sel:[1,1,0] op_sel_hi:[1,1,0]\n"
    "v_mfma_f32_16x16x32_bf16 v[176:179], a[152:155], v[156:159], 0\n"
    "ds_read_u16 v187, v187\n"
    "v_cvt_pk_bf16_f32 v192, v234, v232\n"
    "v_mfma_f32_16x16x32_bf16 v[176:179], a[200:203], v[160:163], v[176:179]\n"
    "v_pk_fma_f16 v232, v196, v196, s48\n"
    "v_lshlrev_b32_sdwa v200, 1, v169 dst_sel:DWORD dst_unused:UNUSED_PAD src0_sel:DWORD src1_sel:WORD_1\n"
    "v_mfma_f32_16x16x32_bf16 v[176:179], a[112:115], v[164:167], v[176:179]\n"
    "ds_read_u16_d16_hi v203, v203\n"
    "v_pk_mul_f16 v232, v196, v232\n"
    "v_exp_f16_e32 v234, v232\n"
    "s_waitcnt lgkmcnt(7)\n"
    "v_or_b32_e64 v199, v199, v207\n"
    "v_lshlrev_b32_sdwa v207, 1, v183 dst_sel:DWORD dst_unused:UNUSED_PAD src0_sel:DWORD src1_sel:WORD_1\n"
    "v_mfma_f32_16x16x32_bf16 v[180:183], a[156:159], v[156:159], 0\n"
    "ds_read_b128 a[244:247], v244 offset:6144\n"
    "buffer_store_dwordx4 a[252:255], v255, s[68:71], s59 offen offset:0 sc1\n"
    "v_mfma_f32_16x16x32_bf16 v[180:183], a[204:207], v[160:163], v[180:183]\n"
    "ds_read_u16 v189, v189\n"
    "ds_read_b128 a[252:255], v249 offset:2048\n"
    "v_mfma_f32_16x16x32_bf16 v[180:183], a[116:119], v[164:167], v[180:183]\n"
    "s_waitcnt lgkmcnt(8)\n"
    "ds_read_u16_d16_hi v205, v205\n"
    "v_exp_f16_sdwa v234, v232 dst_sel:WORD_1 dst_unused:UNUSED_PRESERVE src0_sel:WORD_1\n"
    "v_mfma_f32_16x16x32_bf16 v[96:99], a[16:19], v[192:195], v[96:99]\n"
    "s_nop 0\n"
    "ds_read_u16 v190, v190\n"
    "v_pk_fma_f16 v234, v234, s49, s49\n"
    "v_mfma_f32_16x16x32_bf16 v[100:103], a[20:23], v[192:195], v[100:103]\n"
    "v_rcp_f16_e64 v232, v234\n"
    "v_mfma_f32_16x16x32_bf16 v[112:115], a[24:27], v[192:195], v[112:115]\n"
    "ds_read_u16_d16_hi v206, v206\n"
    "v_rcp_f16_sdwa v232, v234 dst_sel:WORD_1 dst_unused:UNUSED_PRESERVE src0_sel:WORD_1\n"
    "v_mfma_f32_16x16x32_bf16 v[116:119], a[28:31], v[192:195], v[116:119]\n"
    "s_waitcnt lgkmcnt(8)\n"
    "v_or_b32_e32 v185, v185, v201\n"
    "v_fma_mix_f32 v234, v196, v232, 0 op_sel_hi:[1,1,0]\n"
    "v_mfma_f32_16x16x32_bf16 v[128:131], v[236:239], v[192:195], v[128:131]\n"
    "ds_read_u16 v191, v191\n"
    "v_fma_mix_f32 v232, v196, v232, 0 op_sel:[1,1,0] op_sel_hi:[1,1,0]\n"
    "v_or_b32_e64 v186, v186, v202\n"
    "v_cvt_pk_bf16_f32 v196, v234, v232\n"
    "v_pk_fma_f16 v232, v184, v184, s48\n"
    "v_lshlrev_b32_sdwa v201, 1, v171 dst_sel:DWORD dst_unused:UNUSED_PAD src0_sel:DWORD src1_sel:WORD_1\n"
    "v_mfma_f32_16x16x32_bf16 v[104:107], a[16:19], v[196:199], v[104:107]\n"
    "ds_read_u16_d16_hi v207, v207\n"
    "v_pk_mul_f16 v232, v184, v232\n"
    "s_waitcnt lgkmcnt(7)\n"
    "v_mfma_f32_16x16x32_bf16 v[132:135], a[244:247], v[192:195], v[132:135]\n"
    "s_nop 0\n"
    "ds_read_b128 a[240:243], v244 offset:34816\n"
    "v_or_b32_e64 v187, v187, v203\n"
    "v_mfma_f32_16x16x32_bf16 v[108:111], a[20:23], v[196:199], v[108:111]\n"
    "ds_read_u16_d16_hi v200, v200\n"
    "v_exp_f16_e32 v234, v232\n"
    "v_mfma_f32_16x16x32_bf16 v[120:123], a[24:27], v[196:199], v[120:123]\n"
    "s_waitcnt lgkmcnt(7)\n"
    "ds_read_u16_d16_hi v201, v201\n"
    "buffer_store_dwordx4 a[252:255], v255, s[68:71], s59 offen offset:64 sc1\n"
    "v_mfma_f32_16x16x32_bf16 v[124:127], a[28:31], v[196:199], v[124:127]\n"
    "s_nop 0\n"
    "v_exp_f16_sdwa v234, v232 dst_sel:WORD_1 dst_unused:UNUSED_PRESERVE src0_sel:WORD_1\n"
    "v_mfma_f32_16x16x32_bf16 v[136:139], v[236:239], v[196:199], v[136:139]\n"
    "s_waitcnt lgkmcnt(7)\n"
    "ds_read_b128 a[252:255], v249 offset:4096\n"
    "ds_read_b128 v[236:239], v244 offset:8192\n"
    "v_mfma_f32_16x16x32_bf16 v[140:143], a[244:247], v[196:199], v[140:143]\n"
    "v_or_b32_e64 v189, v189, v205\n"
    "v_lshlrev_b32_sdwa v192, 1, v168 dst_sel:DWORD dst_unused:UNUSED_PAD src0_sel:DWORD src1_sel:WORD_1\n"
    "v_pk_fma_f16 v234, v234, s49, s49\n"
    "v_lshlrev_b32_sdwa v193, 1, v170 dst_sel:DWORD dst_unused:UNUSED_PAD src0_sel:DWORD src1_sel:WORD_1\n"
    "v_mfma_f32_16x16x32_bf16 v[168:171], a[160:163], v[144:147], 0\n"
    "s_nop 0\n"
    "ds_read_u16 v192, v192\n"
    "ds_read_u16 v193, v193\n"
    "v_mfma_f32_16x16x32_bf16 v[168:171], a[208:211], v[148:151], v[168:171]\n"
    "s_waitcnt lgkmcnt(9)\n"
    "v_lshlrev_b32_sdwa v194, 1, v172 dst_sel:DWORD dst_unused:UNUSED_PAD src0_sel:DWORD src1_sel:WORD_1\n"
    "v_lshlrev_b32_sdwa v202, 1, v173 dst_sel:DWORD dst_unused:UNUSED_PAD src0_sel:DWORD src1_sel:WORD_1\n"
    "v_lshlrev_b32_sdwa v195, 1, v174 dst_sel:DWORD dst_unused:UNUSED_PAD src0_sel:DWORD src1_sel:WORD_1\n"
    "v_lshlrev_b32_sdwa v203, 1, v175 dst_sel:DWORD dst_unused:UNUSED_PAD src0_sel:DWORD src1_sel:WORD_1\n"
    "v_rcp_f16_e64 v232, v234\n"
    "v_mfma_f32_16x16x32_bf16 v[168:171], a[120:123], v[152:155], v[168:171]\n"
    "ds_read_u16 v194, v194\n"
    "v_or_b32_e32 v190, v190, v206\n"
    "v_rcp_f16_sdwa v232, v234 dst_sel:WORD_1 dst_unused:UNUSED_PRESERVE src0_sel:WORD_1\n"
    "v_mfma_f32_16x16x32_bf16 v[172:175], a[164:167], v[144:147], 0\n"
    "s_waitcnt lgkmcnt(7)\n"
    "ds_read_u16_d16_hi v202, v202\n"
    "v_lshlrev_b32_sdwa v196, 1, v176 dst_sel:DWORD dst_unused:UNUSED_PAD src0_sel:DWORD src1_sel:WORD_1\n"
    "v_mfma_f32_16x16x32_bf16 v[172:175], a[212:215], v[148:151], v[172:175]\n"
    "ds_read_u16 v195, v195\n"
    "v_lshlrev_b32_sdwa v204, 1, v177 dst_sel:DWORD dst_unused:UNUSED_PAD src0_sel:DWORD src1_sel:WORD_1\n"
    "v_lshlrev_b32_sdwa v197, 1, v178 dst_sel:DWORD dst_unused:UNUSED_PAD src0_sel:DWORD src1_sel:WORD_1\n"
    "v_lshlrev_b32_sdwa v205, 1, v179 dst_sel:DWORD dst_unused:UNUSED_PAD src0_sel:DWORD src1_sel:WORD_1\n"
    "v_lshlrev_b32_sdwa v198, 1, v180 dst_sel:DWORD dst_unused:UNUSED_PAD src0_sel:DWORD src1_sel:WORD_1\n"
    "v_lshlrev_b32_sdwa v206, 1, v181 dst_sel:DWORD dst_unused:UNUSED_PAD src0_sel:DWORD src1_sel:WORD_1\n"
    "v_lshlrev_b32_sdwa v199, 1, v182 dst_sel:DWORD dst_unused:UNUSED_PAD src0_sel:DWORD src1_sel:WORD_1\n"
    "v_fma_mix_f32 v234, v184, v232, 0 op_sel_hi:[1,1,0]\n"
    "v_mfma_f32_16x16x32_bf16 v[172:175], a[240:243], v[152:155], v[172:175]\n"
    "ds_read_b128 a[244:247], v244 offset:10240\n"
    "s_waitcnt lgkmcnt(7)\n"
    "buffer_store_dwordx4 a[252:255], v255, s[68:71], s59 offen offset:128 sc1\n"
    "v_or_b32_e32 v191, v191, v207\n"
    "v_lshlrev_b32_sdwa v207, 1, v183 dst_sel:DWORD dst_unused:UNUSED_PAD src0_sel:DWORD src1_sel:WORD_1\n"
    "v_fma_mix_f32 v232, v184, v232, 0 op_sel:[1,1,0] op_sel_hi:[1,1,0]\n"
    "v_mfma_f32_16x16x32_bf16 v[176:179], a[160:163], v[156:159], 0\n"
    "ds_read_b128 a[252:255], v249 offset:1024\n"
    "ds_read_u16_d16_hi v203, v203\n"
    "v_cvt_pk_bf16_f32 v184, v234, v232\n"
    "v_mfma_f32_16x16x32_bf16 v[176:179], a[208:211], v[160:163], v[176:179]\n"
    "s_waitcnt lgkmcnt(7)\n"
    "ds_read_u16 v196, v196\n"
    "ds_read_u16_d16_hi v204, v204\n"
    "v_mfma_f32_16x16x32_bf16 v[176:179], a[120:123], v[164:167], v[176:179]\n"
    "s_nop 0\n"
    "v_pk_fma_f16 v232, v188, v188, s48\n"
    "v_or_b32_e32 v192, v192, v200\n"
    "v_mfma_f32_16x16x32_bf16 v[180:183], a[164:167], v[156:159], 0\n"
    "s_waitcnt lgkmcnt(8)\n"
    "ds_read_u16 v197, v197\n"
    "v_pk_mul_f16 v232, v188, v232\n"
    "v_mfma_f32_16x16x32_bf16 v[180:183], a[212:215], v[160:163], v[180:183]\n"
    "v_exp_f16_e64 v234, v232\n"
    "v_mfma_f32_16x16x32_bf16 v[180:183], a[240:243], v[164:167], v[180:183]\n"
    "ds_read_u16_d16_hi v205, v205\n"
    "v_exp_f16_sdwa v234, v232 dst_sel:WORD_1 dst_unused:UNUSED_PRESERVE src0_sel:WORD_1\n"
    "v_mfma_f32_16x16x32_bf16 v[96:99], a[32:35], v[184:187], v[96:99]\n"
    "s_waitcnt lgkmcnt(8)\n"
    "v_pk_fma_f16 v234, v234, s49, s49\n"
    "v_or_b32_e32 v193, v193, v201\n"
    "v_mfma_f32_16x16x32_bf16 v[100:103], a[36:39], v[184:187], v[100:103]\n"
    "ds_read_u16 v198, v198\n"
    "v_rcp_f16_e64 v232, v234\n"
    "v_mfma_f32_16x16x32_bf16 v[112:115], a[40:43], v[184:187], v[112:115]\n"
    "ds_read_u16_d16_hi v206, v206\n"
    "s_waitcnt lgkmcnt(7)\n"
    "buffer_store_dwordx4 a[252:255], v255, s[68:71], s39 offen offset:0 sc1\n"
    "v_rcp_f16_sdwa v232, v234 dst_sel:WORD_1 dst_unused:UNUSED_PRESERVE src0_sel:WORD_1\n"
    "v_mfma_f32_16x16x32_bf16 v[116:119], a[44:47], v[184:187], v[116:119]\n"
    "ds_read_u16 v199, v199\n"
    "v_fma_mix_f32 v234, v188, v232, 0 op_sel_hi:[1,1,0]\n"
    "v_mfma_f32_16x16x32_bf16 v[128:131], v[236:239], v[184:187], v[128:131]\n"
    "s_waitcnt lgkmcnt(7)\n"
    "ds_read_b128 a[252:255], v249 offset:3072\n"
    "v_fma_mix_f32 v232, v188, v232, 0 op_sel:[1,1,0] op_sel_hi:[1,1,0]\n"
    "v_mfma_f32_16x16x32_bf16 v[132:135], a[244:247], v[184:187], v[132:135]\n"
    "ds_read_u16_d16_hi v207, v207\n"
    "v_cvt_pk_bf16_f32 v188, v234, v232\n"
    "v_cvt_pk_f16_f32 v184, v168, v169\n"
    "v_lshlrev_b32_sdwa v185, 1, v170 dst_sel:DWORD dst_unused:UNUSED_PAD src0_sel:DWORD src1_sel:WORD_1\n"
    "v_mfma_f32_16x16x32_bf16 v[104:107], a[32:35], v[188:191], v[104:107]\n"
    "ds_read_b128 a[240:243], v244 offset:38912\n"
    "v_lshlrev_b32_sdwa v201, 1, v171 dst_sel:DWORD dst_unused:UNUSED_PAD src0_sel:DWORD src1_sel:WORD_1\n"
    "v_mfma_f32_16x16x32_bf16 v[108:111], a[36:39], v[188:191], v[108:111]\n"
    "s_waitcnt lgkmcnt(8)\n"
    "ds_read_u16 v185, v185\n"
    "v_lshlrev_b32_sdwa v186, 1, v172 dst_sel:DWORD dst_unused:UNUSED_PAD src0_sel:DWORD src1_sel:WORD_1\n"
    "v_mfma_f32_16x16x32_bf16 v[120:123], a[40:43], v[188:191], v[120:123]\n"
    "v_or_b32_e32 v194, v194, v202\n"
    "v_lshlrev_b32_sdwa v202, 1, v173 dst_sel:DWORD dst_unused:UNUSED_PAD src0_sel:DWORD src1_sel:WORD_1\n"
    "v_mfma_f32_16x16x32_bf16 v[124:127], a[44:47], v[188:191], v[124:127]\n"
    "ds_read_u16_d16_hi v201, v201\n"
    "v_lshlrev_b32_sdwa v187, 1, v174 dst_sel:DWORD dst_unused:UNUSED_PAD src0_sel:DWORD src1_sel:WORD_1\n"
    "v_mfma_f32_16x16x32_bf16 v[136:139], v[236:239], v[188:191], v[136:139]\n"
    "s_waitcnt lgkmcnt(8)\n"
    "v_or_b32_e32 v195, v195, v203\n"
    "v_lshlrev_b32_sdwa v203, 1, v175 dst_sel:DWORD dst_unused:UNUSED_PAD src0_sel:DWORD src1_sel:WORD_1\n"
    "v_mfma_f32_16x16x32_bf16 v[140:143], a[244:247], v[188:191], v[140:143]\n"
    "ds_read_u16 v186, v186\n"
    "v_cvt_pk_f16_f32 v188, v176, v177\n"
    "v_mfma_f32_16x16x32_bf16 v[168:171], a[168:171], v[144:147], 0\n"
    "ds_read_u16_d16_hi v202, v202\n"
    "v_lshlrev_b32_sdwa v189, 1, v178 dst_sel:DWORD dst_unused:UNUSED_PAD src0_sel:DWORD src1_sel:WORD_1\n"
    "v_mfma_f32_16x16x32_bf16 v[168:171], a[216:219], v[148:151], v[168:171]\n"
    "v_lshlrev_b32_sdwa v190, 1, v180 dst_sel:DWORD dst_unused:UNUSED_PAD src0_sel:DWORD src1_sel:WORD_1\n"
    "v_lshlrev_b32_sdwa v191, 1, v182 dst_sel:DWORD dst_unused:UNUSED_PAD src0_sel:DWORD src1_sel:WORD_1\n"
    "v_mfma_f32_16x16x32_bf16 v[168:171], a[124:127], v[152:155], v[168:171]\n"
    "s_waitcnt lgkmcnt(8)\n"
    "ds_read_b128 v[236:239], v244 offset:12288\n"
    "v_or_b32_e64 v196, v196, v204\n"
    "v_mfma_f32_16x16x32_bf16 v[172:175], a[172:175], v[144:147], 0\n"
    "s_waitcnt lgkmcnt(7)\n"
    "ds_read_u16 v187, v187\n"
    "buffer_store_dwordx4 a[252:255], v255, s[68:71], s39 offen offset:64 sc1\n"
    "v_mfma_f32_16x16x32_bf16 v[172:175], a[220:223], v[148:151], v[172:175]\n"
    "ds_read_u16_d16_hi v203, v203\n"
    "v_or_b32_e64 v197, v197, v205\n"
    "v_lshlrev_b32_sdwa v205, 1, v179 dst_sel:DWORD dst_unused:UNUSED_PAD src0_sel:DWORD src1_sel:WORD_1\n"
    "s_waitcnt lgkmcnt(7)\n"
    "v_mfma_f32_16x16x32_bf16 v[172:175], a[240:243], v[152:155], v[172:175]\n"
    "s_nop 0\n"
    "ds_read_b128 a[252:255], v249 offset:5120\n"
    "v_or_b32_e64 v198, v198, v206\n"
    "v_mfma_f32_16x16x32_bf16 v[176:179], a[168:171], v[156:159], 0\n"
    "ds_read_b128 a[244:247], v244 offset:14336\n"
    "v_lshlrev_b32_sdwa v206, 1, v181 dst_sel:DWORD dst_unused:UNUSED_PAD src0_sel:DWORD src1_sel:WORD_1\n"
    "v_mfma_f32_16x16x32_bf16 v[176:179], a[216:219], v[160:163], v[176:179]\n"
    "ds_read_u16 v189, v189\n"
    "v_or_b32_e64 v199, v199, v207\n"
    "v_mfma_f32_16x16x32_bf16 v[176:179], a[124:127], v[164:167], v[176:179]\n"
    "v_lshlrev_b32_sdwa v207, 1, v183 dst_sel:DWORD dst_unused:UNUSED_PAD src0_sel:DWORD src1_sel:WORD_1\n"
    "v_pk_fma_f16 v232, v184, v184, s48\n"
    "v_mfma_f32_16x16x32_bf16 v[180:183], a[172:175], v[156:159], 0\n"
    "s_waitcnt lgkmcnt(8)\n"
    "ds_read_u16_d16_hi v205, v205\n"
    "v_or_b32_e32 v185, v185, v201\n"
    "v_mfma_f32_16x16x32_bf16 v[180:183], a[220:223], v[160:163], v[180:183]\n"
    "v_pk_mul_f16 v232, v184, v232\n"
    "v_lshlrev_b32_sdwa v201, 1, v171 dst_sel:DWORD dst_unused:UNUSED_PAD src0_sel:DWORD src1_sel:WORD_1\n"
    "v_mfma_f32_16x16x32_bf16 v[180:183], a[240:243], v[164:167], v[180:183]\n"
    "ds_read_u16 v190, v190\n"
    "v_exp_f16_e32 v234, v232\n"
    "v_mfma_f32_16x16x32_bf16 v[96:99], a[48:51], v[192:195], v[96:99]\n"
    "s_waitcnt lgkmcnt(7)\n"
    "ds_read_b128 v[144:147], v243 offset:0\n"
    "v_or_b32_e64 v186, v186, v202\n"
    "v_mfma_f32_16x16x32_bf16 v[100:103], a[52:55], v[192:195], v[100:103]\n"
    "ds_read_u16_d16_hi v206, v206\n"
    "v_exp_f16_sdwa v234, v232 dst_sel:WORD_1 dst_unused:UNUSED_PRESERVE src0_sel:WORD_1\n"
    "v_mfma_f32_16x16x32_bf16 v[112:115], a[56:59], v[192:195], v[112:115]\n"
    "v_pk_fma_f16 v234, v234, s49, s49\n"
    "v_lshlrev_b32_sdwa v202, 1, v173 dst_sel:DWORD dst_unused:UNUSED_PAD src0_sel:DWORD src1_sel:WORD_1\n"
    "v_mfma_f32_16x16x32_bf16 v[116:119], a[60:63], v[192:195], v[116:119]\n"
    "ds_read_b128 v[148:151], v243 offset:1024\n"
    "ds_read_u16 v191, v191\n"
    "v_mfma_f32_16x16x32_bf16 v[128:131], v[236:239], v[192:195], v[128:131]\n"
    "buffer_load_dwordx4 a[232:235], v245, s[40:43], s57 offen offset:64\n"
    "buffer_load_dwordx4 a[224:227], v245, s[40:43], s57 offen offset:0\n"
    "v_mfma_f32_16x16x32_bf16 v[104:107], a[48:51], v[196:199], v[104:107]\n"
    "s_waitcnt lgkmcnt(8)\n"
    "ds_read_b128 v[152:155], v243 offset:2048\n"
    "v_or_b32_e32 v187, v187, v203\n"
    "s_waitcnt lgkmcnt(8)\n"
    "v_mfma_f32_16x16x32_bf16 v[132:135], a[244:247], v[192:195], v[132:135]\n"
    "s_nop 0\n"
    "ds_read_u16_d16_hi v207, v207\n"
    "v_cvt_pk_f16_f32 v192, v168, v169\n"
    "v_mfma_f32_16x16x32_bf16 v[108:111], a[52:55], v[196:199], v[108:111]\n"
    "v_lshlrev_b32_sdwa v193, 1, v170 dst_sel:DWORD dst_unused:UNUSED_PAD src0_sel:DWORD src1_sel:WORD_1\n"
    "v_lshlrev_b32_sdwa v194, 1, v172 dst_sel:DWORD dst_unused:UNUSED_PAD src0_sel:DWORD src1_sel:WORD_1\n"
    "v_mfma_f32_16x16x32_bf16 v[120:123], a[56:59], v[196:199], v[120:123]\n"
    "ds_read_b128 v[156:159], v243 offset:3072\n"
    "v_lshlrev_b32_sdwa v195, 1, v174 dst_sel:DWORD dst_unused:UNUSED_PAD src0_sel:DWORD src1_sel:WORD_1\n"
    "v_mfma_f32_16x16x32_bf16 v[124:127], a[60:63], v[196:199], v[124:127]\n"
    "ds_read_b128 v[160:163], v243 offset:4096\n"
    "v_lshlrev_b32_sdwa v203, 1, v175 dst_sel:DWORD dst_unused:UNUSED_PAD src0_sel:DWORD src1_sel:WORD_1\n"
    "v_mfma_f32_16x16x32_bf16 v[136:139], v[236:239], v[196:199], v[136:139]\n"
    "s_waitcnt lgkmcnt(9)\n"
    "ds_read_b128 v[164:167], v243 offset:5120\n"
    "v_or_b32_e32 v189, v189, v205\n"
    "v_mfma_f32_16x16x32_bf16 v[140:143], a[244:247], v[196:199], v[140:143]\n"
    "ds_read_b128 a[240:243], v244 offset:32768\n"
    "v_rcp_f16_e32 v232, v234\n"
    "s_waitcnt lgkmcnt(9)\n"
    "v_mfma_f32_16x16x32_bf16 v[168:171], a[128:131], v[144:147], 0\n"
    "ds_read_u16 v193, v193\n"
    "v_rcp_f16_sdwa v232, v234 dst_sel:WORD_1 dst_unused:UNUSED_PRESERVE src0_sel:WORD_1\n"
    "v_mfma_f32_16x16x32_bf16 v[172:175], a[132:135], v[144:147], 0\n"
    "ds_read_u16_d16_hi v201, v201\n"
    "buffer_store_dwordx4 a[252:255], v255, s[68:71], s39 offen offset:128 sc1\n"
    "s_waitcnt lgkmcnt(9)\n"
    "v_mfma_f32_16x16x32_bf16 v[168:171], a[176:179], v[148:151], v[168:171]\n"
    "s_nop 0\n"
    "ds_read_u16 v194, v194\n"
    "v_or_b32_e64 v190, v190, v206\n"
    "v_cvt_pk_f16_f32 v196, v176, v177\n"
    "s_waitcnt lgkmcnt(8)\n"
    "v_mfma_f32_16x16x32_bf16 v[168:171], a[96:99], v[152:155], v[168:171]\n"
    "s_nop 0\n"
    "ds_read_u16_d16_hi v202, v202\n"
    "v_lshlrev_b32_sdwa v197, 1, v178 dst_sel:DWORD dst_unused:UNUSED_PAD src0_sel:DWORD src1_sel:WORD_1\n"
    "v_lshlrev_b32_sdwa v205, 1, v179 dst_sel:DWORD dst_unused:UNUSED_PAD src0_sel:DWORD src1_sel:WORD_1\n"
    "v_lshlrev_b32_sdwa v198, 1, v180 dst_sel:DWORD dst_unused:UNUSED_PAD src0_sel:DWORD src1_sel:WORD_1\n"
    "v_lshlrev_b32_sdwa v206, 1, v181 dst_sel:DWORD dst_unused:UNUSED_PAD src0_sel:DWORD src1_sel:WORD_1\n"
    "v_lshlrev_b32_sdwa v199, 1, v182 dst_sel:DWORD dst_unused:UNUSED_PAD src0_sel:DWORD src1_sel:WORD_1\n"
    "v_fma_mix_f32 v234, v184, v232, 0 op_sel_hi:[1,1,0]\n"
    "v_mfma_f32_16x16x32_bf16 v[172:175], a[180:183], v[148:151], v[172:175]\n"
    "s_waitcnt lgkmcnt(7)\n"
    "ds_read_b128 v[236:239], v244 offset:16384\n"
    "ds_read_u16 v195, v195\n"
    "v_or_b32_e32 v191, v191, v207\n"
    "v_lshlrev_b32_sdwa v207, 1, v183 dst_sel:DWORD dst_unused:UNUSED_PAD src0_sel:DWORD src1_sel:WORD_1\n"
    "v_fma_mix_f32 v232, v184, v232, 0 op_sel:[1,1,0] op_sel_hi:[1,1,0]\n"
    "v_mfma_f32_16x16x32_bf16 v[176:179], a[128:131], v[156:159], 0\n"
    "s_waitcnt lgkmcnt(7)\n"
    "ds_read_u16_d16_hi v203, v203\n"
    "v_cvt_pk_bf16_f32 v184, v234, v232\n"
    "v_mfma_f32_16x16x32_bf16 v[176:179], a[176:179], v[160:163], v[176:179]\n"
    "s_waitcnt lgkmcnt(7)\n"
    "v_pk_fma_f16 v232, v188, v188, s48\n"
    "v_lshlrev_b32_sdwa v200, 1, v169 dst_sel:DWORD dst_unused:UNUSED_PAD src0_sel:DWORD src1_sel:WORD_1\n"
    "v_mfma_f32_16x16x32_bf16 v[172:175], a[240:243], v[152:155], v[172:175]\n"
    "s_barrier\n"
    "v_mfma_f32_16x16x32_bf16 v[176:179], a[96:99], v[164:167], v[176:179]\n"
    "ds_read_b128 a[244:247], v244 offset:18432\n"
    "ds_write_b128 v253, v[64:67] offset:2048\n"
    "v_mfma_f32_16x16x32_bf16 v[180:183], a[132:135], v[156:159], 0\n"
    "s_waitcnt lgkmcnt(7)\n"
    "buffer_load_dwordx4 a[236:239], v245, s[40:43], s100 offen offset:64\n"
    "buffer_load_dwordx4 a[228:231], v245, s[40:43], s100 offen offset:0\n"
    "v_pk_mul_f16 v232, v188, v232\n"
    "v_mfma_f32_16x16x32_bf16 v[180:183], a[180:183], v[160:163], v[180:183]\n"
    "v_exp_f16_e64 v234, v232\n"
    "v_mfma_f32_16x16x32_bf16 v[180:183], a[240:243], v[164:167], v[180:183]\n"
    "ds_read_u16 v197, v197\n"
    "ds_write_b128 v253, v[68:71] offset:5120\n"
    "v_exp_f16_sdwa v234, v232 dst_sel:WORD_1 dst_unused:UNUSED_PRESERVE src0_sel:WORD_1\n"
    "v_mfma_f32_16x16x32_bf16 v[96:99], a[64:67], v[184:187], v[96:99]\n"
    "s_waitcnt lgkmcnt(6)\n"
    "v_pk_fma_f16 v234, v234, s49, s49\n"
    "v_or_b32_e32 v193, v193, v201\n"
    "v_mfma_f32_16x16x32_bf16 v[100:103], a[68:71], v[184:187], v[100:103]\n"
    "v_rcp_f16_e32 v232, v234\n"
    "v_mfma_f32_16x16x32_bf16 v[112:115], a[72:75], v[184:187], v[112:115]\n"
    "ds_read_u16_d16_hi v205, v205\n"
    "ds_write_b128 v253, v[88:91] offset:0\n"
    "v_rcp_f16_sdwa v232, v234 dst_sel:WORD_1 dst_unused:UNUSED_PRESERVE src0_sel:WORD_1\n"
    "v_mfma_f32_16x16x32_bf16 v[116:119], a[76:79], v[184:187], v[116:119]\n"
    "s_waitcnt lgkmcnt(4)\n"
    "v_fma_mix_f32 v234, v188, v232, 0 op_sel_hi:[1,1,0]\n"
    "v_fma_mix_f32 v232, v188, v232, 0 op_sel:[1,1,0] op_sel_hi:[1,1,0]\n"
    "v_mfma_f32_16x16x32_bf16 v[128:131], v[236:239], v[184:187], v[128:131]\n"
    "v_or_b32_e64 v194, v194, v202\n"
    "v_cvt_pk_bf16_f32 v188, v234, v232\n"
    "v_mfma_f32_16x16x32_bf16 v[132:135], a[244:247], v[184:187], v[132:135]\n"
    "ds_read_u16 v198, v198\n"
    "ds_write_b128 v253, v[92:95] offset:3072\n"
    "v_mfma_f32_16x16x32_bf16 v[104:107], a[64:67], v[188:191], v[104:107]\n"
    "v_pk_fma_f16 v232, v192, v192, s48\n"
    "v_or_b32_e64 v195, v195, v203\n"
    "v_mfma_f32_16x16x32_bf16 v[108:111], a[68:71], v[188:191], v[108:111]\n"
    "v_pk_mul_f16 v232, v192, v232\n"
    "v_lshlrev_b32_sdwa v184, 1, v168 dst_sel:DWORD dst_unused:UNUSED_PAD src0_sel:DWORD src1_sel:WORD_1\n"
    "v_mfma_f32_16x16x32_bf16 v[120:123], a[72:75], v[188:191], v[120:123]\n"
    "ds_read_u16_d16_hi v206, v206\n"
    "ds_read_u16 v199, v199\n"
    "v_mfma_f32_16x16x32_bf16 v[124:127], a[76:79], v[188:191], v[124:127]\n"
    "ds_read_u16_d16_hi v207, v207\n"
    "ds_write_b128 v253, v[72:75] offset:1024\n"
    "v_mfma_f32_16x16x32_bf16 v[136:139], v[236:239], v[188:191], v[136:139]\n"
    "s_waitcnt lgkmcnt(6)\n"
    "v_exp_f16_e32 v234, v232\n"
    "v_mfma_f32_16x16x32_bf16 v[140:143], a[244:247], v[188:191], v[140:143]\n"
    "v_or_b32_e64 v197, v197, v205\n"
    "v_exp_f16_sdwa v234, v232 dst_sel:WORD_1 dst_unused:UNUSED_PRESERVE src0_sel:WORD_1\n"
    "v_lshlrev_b32_sdwa v185, 1, v170 dst_sel:DWORD dst_unused:UNUSED_PAD src0_sel:DWORD src1_sel:WORD_1\n"
    "v_pk_fma_f16 v234, v234, s49, s49\n"
    "v_lshlrev_b32_sdwa v201, 1, v171 dst_sel:DWORD dst_unused:UNUSED_PAD src0_sel:DWORD src1_sel:WORD_1\n"
    "v_mfma_f32_16x16x32_bf16 v[168:171], a[136:139], v[144:147], 0\n"
    "ds_read_b128 a[240:243], v244 offset:36864\n"
    "ds_write_b128 v253, v[76:79] offset:4096\n"
    "v_mfma_f32_16x16x32_bf16 v[168:171], a[184:187], v[148:151], v[168:171]\n"
    "v_lshlrev_b32_sdwa v186, 1, v172 dst_sel:DWORD dst_unused:UNUSED_PAD src0_sel:DWORD src1_sel:WORD_1\n"
    "v_lshlrev_b32_sdwa v202, 1, v173 dst_sel:DWORD dst_unused:UNUSED_PAD src0_sel:DWORD src1_sel:WORD_1\n"
    "v_lshlrev_b32_sdwa v187, 1, v174 dst_sel:DWORD dst_unused:UNUSED_PAD src0_sel:DWORD src1_sel:WORD_1\n"
    "v_lshlrev_b32_sdwa v203, 1, v175 dst_sel:DWORD dst_unused:UNUSED_PAD src0_sel:DWORD src1_sel:WORD_1\n"
    "v_rcp_f16_e32 v232, v234\n"
    "v_mfma_f32_16x16x32_bf16 v[168:171], a[100:103], v[152:155], v[168:171]\n"
    "s_waitcnt lgkmcnt(5)\n"
    "ds_read_u16 v184, v184\n"
    "ds_read_u16_d16_hi v200, v200\n"
    "v_or_b32_e64 v198, v198, v206\n"
    "v_rcp_f16_sdwa v232, v234 dst_sel:WORD_1 dst_unused:UNUSED_PRESERVE src0_sel:WORD_1\n"
    "v_mfma_f32_16x16x32_bf16 v[172:175], a[140:143], v[144:147], 0\n"
    "ds_read_u16 v185, v185\n"
    "ds_read_u16_d16_hi v201, v201\n"
    "v_lshlrev_b32_sdwa v188, 1, v176 dst_sel:DWORD dst_unused:UNUSED_PAD src0_sel:DWORD src1_sel:WORD_1\n"
    "v_lshlrev_b32_sdwa v204, 1, v177 dst_sel:DWORD dst_unused:UNUSED_PAD src0_sel:DWORD src1_sel:WORD_1\n"
    "v_lshlrev_b32_sdwa v189, 1, v178 dst_sel:DWORD dst_unused:UNUSED_PAD src0_sel:DWORD src1_sel:WORD_1\n"
    "v_lshlrev_b32_sdwa v205, 1, v179 dst_sel:DWORD dst_unused:UNUSED_PAD src0_sel:DWORD src1_sel:WORD_1\n"
    "v_lshlrev_b32_sdwa v190, 1, v180 dst_sel:DWORD dst_unused:UNUSED_PAD src0_sel:DWORD src1_sel:WORD_1\n"
    "v_lshlrev_b32_sdwa v206, 1, v181 dst_sel:DWORD dst_unused:UNUSED_PAD src0_sel:DWORD src1_sel:WORD_1\n"
    "v_lshlrev_b32_sdwa v191, 1, v182 dst_sel:DWORD dst_unused:UNUSED_PAD src0_sel:DWORD src1_sel:WORD_1\n"
    "v_fma_mix_f32 v234, v192, v232, 0 op_sel_hi:[1,1,0]\n"
    "v_mfma_f32_16x16x32_bf16 v[172:175], a[188:191], v[148:151], v[172:175]\n"
    "s_waitcnt lgkmcnt(6)\n"
    "ds_read_u16 v186, v186\n"
    "ds_read_b128 v[236:239], v244 offset:20480\n"
    "s_waitcnt lgkmcnt(7)\n"
    "v_mfma_f32_16x16x32_bf16 v[172:175], a[240:243], v[152:155], v[172:175]\n"
    "ds_read_u16_d16_hi v202, v202\n"
    "v_or_b32_e64 v199, v199, v207\n"
    "v_lshlrev_b32_sdwa v207, 1, v183 dst_sel:DWORD dst_unused:UNUSED_PAD src0_sel:DWORD src1_sel:WORD_1\n"
    "v_fma_mix_f32 v232, v192, v232, 0 op_sel:[1,1,0] op_sel_hi:[1,1,0]\n"
    "v_mfma_f32_16x16x32_bf16 v[176:179], a[136:139], v[156:159], 0\n"
    "v_cvt_pk_bf16_f32 v192, v234, v232\n"
    "v_pk_fma_f16 v232, v196, v196, s48\n"
    "v_mfma_f32_16x16x32_bf16 v[176:179], a[184:187], v[160:163], v[176:179]\n"
    "ds_read_u16 v187, v187\n"
    "v_pk_mul_f16 v232, v196, v232\n"
    "v_mfma_f32_16x16x32_bf16 v[176:179], a[100:103], v[164:167], v[176:179]\n"
    "ds_read_b128 a[244:247], v244 offset:22528\n"
    "v_exp_f16_e64 v234, v232\n"
    "v_mfma_f32_16x16x32_bf16 v[180:183], a[140:143], v[156:159], 0\n"
    "ds_read_u16_d16_hi v203, v203\n"
    "v_exp_f16_sdwa v234, v232 dst_sel:WORD_1 dst_unused:UNUSED_PRESERVE src0_sel:WORD_1\n"
    "v_mfma_f32_16x16x32_bf16 v[180:183], a[188:191], v[160:163], v[180:183]\n"
    "s_waitcnt lgkmcnt(8)\n"
    "v_pk_fma_f16 v234, v234, s49, s49\n"
    "v_or_b32_e32 v184, v184, v200\n"
    "v_mfma_f32_16x16x32_bf16 v[180:183], a[240:243], v[164:167], v[180:183]\n"
    "ds_read_u16 v188, v188\n"
    "v_rcp_f16_e32 v232, v234\n"
    "v_mfma_f32_16x16x32_bf16 v[96:99], a[80:83], v[192:195], v[96:99]\n"
    "ds_read_u16_d16_hi v204, v204\n"
    "v_rcp_f16_sdwa v232, v234 dst_sel:WORD_1 dst_unused:UNUSED_PRESERVE src0_sel:WORD_1\n"
    "v_mfma_f32_16x16x32_bf16 v[100:103], a[84:87], v[192:195], v[100:103]\n"
    "s_waitcnt lgkmcnt(8)\n"
    "v_fma_mix_f32 v234, v196, v232, 0 op_sel_hi:[1,1,0]\n"
    "v_fma_mix_f32 v232, v196, v232, 0 op_sel:[1,1,0] op_sel_hi:[1,1,0]\n"
    "v_mfma_f32_16x16x32_bf16 v[112:115], a[88:91], v[192:195], v[112:115]\n"
    "ds_read_u16 v189, v189\n"
    "v_cvt_pk_bf16_f32 v196, v234, v232\n"
    "v_mfma_f32_16x16x32_bf16 v[116:119], a[92:95], v[192:195], v[116:119]\n"
    "s_waitcnt lgkmcnt(7)\n"
    "ds_read_u16_d16_hi v205, v205\n"
    "v_or_b32_e32 v185, v185, v201\n"
    "v_mfma_f32_16x16x32_bf16 v[128:131], v[236:239], v[192:195], v[128:131]\n"
    "v_lshlrev_b32_sdwa v201, 1, v171 dst_sel:DWORD dst_unused:UNUSED_PAD src0_sel:DWORD src1_sel:WORD_1\n"
    "v_cvt_pk_bf16_f32 v96, v96, v97\n"
    "v_mfma_f32_16x16x32_bf16 v[104:107], a[80:83], v[196:199], v[104:107]\n"
    "ds_read_u16 v190, v190\n"
    "v_cvt_pk_bf16_f32 v97, v98, v99\n"
    "v_mfma_f32_16x16x32_bf16 v[108:111], a[84:87], v[196:199], v[108:111]\n"
    "s_waitcnt lgkmcnt(8)\n"
    "v_or_b32_e64 v186, v186, v202\n"
    "v_lshlrev_b32_sdwa v202, 1, v173 dst_sel:DWORD dst_unused:UNUSED_PAD src0_sel:DWORD src1_sel:WORD_1\n"
    "v_mfma_f32_16x16x32_bf16 v[120:123], a[88:91], v[196:199], v[120:123]\n"
    "s_waitcnt lgkmcnt(6)\n"
    "ds_read_u16_d16_hi v206, v206\n"
    "v_cvt_pk_bf16_f32 v98, v100, v101\n"
    "v_mfma_f32_16x16x32_bf16 v[132:135], a[244:247], v[192:195], v[132:135]\n"
    "v_cvt_pk_f16_f32 v192, v168, v169\n"
    "v_lshlrev_b32_sdwa v193, 1, v170 dst_sel:DWORD dst_unused:UNUSED_PAD src0_sel:DWORD src1_sel:WORD_1\n"
    "v_mfma_f32_16x16x32_bf16 v[124:127], a[92:95], v[196:199], v[124:127]\n"
    "s_nop 0\n"
    "ds_read_u16 v191, v191\n"
    "v_lshlrev_b32_sdwa v194, 1, v172 dst_sel:DWORD dst_unused:UNUSED_PAD src0_sel:DWORD src1_sel:WORD_1\n"
    "v_mfma_f32_16x16x32_bf16 v[136:139], v[236:239], v[196:199], v[136:139]\n"
    "s_waitcnt lgkmcnt(7)\n"
    "ds_read_u16_d16_hi v207, v207\n"
    "v_lshlrev_b32_sdwa v195, 1, v174 dst_sel:DWORD dst_unused:UNUSED_PAD src0_sel:DWORD src1_sel:WORD_1\n"
    "v_mfma_f32_16x16x32_bf16 v[140:143], a[244:247], v[196:199], v[140:143]\n"
    "v_cvt_pk_f16_f32 v196, v176, v177\n"
    "v_lshlrev_b32_sdwa v197, 1, v178 dst_sel:DWORD dst_unused:UNUSED_PAD src0_sel:DWORD src1_sel:WORD_1\n"
    "v_mfma_f32_16x16x32_bf16 v[168:171], a[144:147], v[144:147], 0\n"
    "ds_read_u16 v193, v193\n"
    "v_lshlrev_b32_sdwa v198, 1, v180 dst_sel:DWORD dst_unused:UNUSED_PAD src0_sel:DWORD src1_sel:WORD_1\n"
    "v_mfma_f32_16x16x32_bf16 v[168:171], a[192:195], v[148:151], v[168:171]\n"
    "v_lshlrev_b32_sdwa v199, 1, v182 dst_sel:DWORD dst_unused:UNUSED_PAD src0_sel:DWORD src1_sel:WORD_1\n"
    "v_or_b32_e32 v187, v187, v203\n"
    "v_mfma_f32_16x16x32_bf16 v[168:171], a[104:107], v[152:155], v[168:171]\n"
    "s_waitcnt lgkmcnt(7)\n"
    "ds_read_u16_d16_hi v201, v201\n"
    "v_lshlrev_b32_sdwa v203, 1, v175 dst_sel:DWORD dst_unused:UNUSED_PAD src0_sel:DWORD src1_sel:WORD_1\n"
    "v_mfma_f32_16x16x32_bf16 v[172:175], a[148:151], v[144:147], 0\n"
    "v_or_b32_e64 v188, v188, v204\n"
    "v_cvt_pk_bf16_f32 v99, v102, v103\n"
    "v_mfma_f32_16x16x32_bf16 v[172:175], a[196:199], v[148:151], v[172:175]\n"
    "ds_read_u16 v194, v194\n"
    "v_cvt_pk_bf16_f32 v112, v112, v113\n"
    "v_mfma_f32_16x16x32_bf16 v[172:175], a[108:111], v[152:155], v[172:175]\n"
    "s_waitcnt lgkmcnt(7)\n"
    "ds_read_u16_d16_hi v202, v202\n"
    "v_or_b32_e32 v189, v189, v205\n"
    "v_lshlrev_b32_sdwa v205, 1, v179 dst_sel:DWORD dst_unused:UNUSED_PAD src0_sel:DWORD src1_sel:WORD_1\n"
    "v_mfma_f32_16x16x32_bf16 v[176:179], a[144:147], v[156:159], 0\n"
    "v_cvt_pk_bf16_f32 v113, v114, v115\n"
    "v_cvt_pk_bf16_f32 v114, v116, v117\n"
    "v_mfma_f32_16x16x32_bf16 v[176:179], a[192:195], v[160:163], v[176:179]\n"
    "ds_read_b128 v[236:239], v244 offset:0\n"
    "v_cvt_pk_bf16_f32 v115, v118, v119\n"
    "v_mfma_f32_16x16x32_bf16 v[176:179], a[104:107], v[164:167], v[176:179]\n"
    "ds_read_u16 v195, v195\n"
    "s_waitcnt lgkmcnt(8)\n"
    "v_or_b32_e32 v190, v190, v206\n"
    "v_mfma_f32_16x16x32_bf16 v[100:103], a[4:7], v[184:187], 0\n"
    "ds_read_u16_d16_hi v203, v203\n"
    "v_lshlrev_b32_sdwa v206, 1, v181 dst_sel:DWORD dst_unused:UNUSED_PAD src0_sel:DWORD src1_sel:WORD_1\n"
    "v_cvt_pk_bf16_f32 v128, v128, v129\n"
    "v_cvt_pk_bf16_f32 v129, v130, v131\n"
    "v_cvt_pk_bf16_f32 v130, v132, v133\n"
    "v_cvt_pk_bf16_f32 v131, v134, v135\n"
    "v_cvt_pk_bf16_f32 v104, v104, v105\n"
    "ds_write_b128 v242, v[96:99] offset:24640\n"
    "v_mfma_f32_16x16x32_bf16 v[96:99], a[0:3], v[184:187], 0\n"
    "s_waitcnt lgkmcnt(8)\n"
    "v_or_b32_e32 v191, v191, v207\n"
    "v_lshlrev_b32_sdwa v207, 1, v183 dst_sel:DWORD dst_unused:UNUSED_PAD src0_sel:DWORD src1_sel:WORD_1\n"
    "v_mfma_f32_16x16x32_bf16 v[180:183], a[148:151], v[156:159], 0\n"
    "ds_read_b128 a[244:247], v244 offset:2048\n"
    "ds_write_b128 v242, v[112:115] offset:26688\n"
    "v_mfma_f32_16x16x32_bf16 v[180:183], a[196:199], v[160:163], v[180:183]\n"
    "s_waitcnt lgkmcnt(8)\n"
    "v_or_b32_e32 v193, v193, v201\n"
    "v_cvt_pk_bf16_f32 v105, v106, v107\n"
    "v_mfma_f32_16x16x32_bf16 v[180:183], a[108:111], v[164:167], v[180:183]\n"
    "v_cvt_pk_bf16_f32 v106, v108, v109\n"
    "v_cvt_pk_bf16_f32 v107, v110, v111\n"
    "v_mfma_f32_16x16x32_bf16 v[112:115], a[8:11], v[184:187], 0\n"
    "ds_read_u16 v197, v197\n"
    "ds_write_b128 v242, v[128:131] offset:28736\n"
    "v_mfma_f32_16x16x32_bf16 v[116:119], a[12:15], v[184:187], 0\n"
    "s_waitcnt lgkmcnt(7)\n"
    "v_or_b32_e32 v194, v194, v202\n"
    "v_cvt_pk_bf16_f32 v120, v120, v121\n"
    "v_mfma_f32_16x16x32_bf16 v[128:131], v[236:239], v[184:187], 0\n"
    "v_cvt_pk_bf16_f32 v121, v122, v123\n"
    "v_cvt_pk_bf16_f32 v122, v124, v125\n"
    "v_mfma_f32_16x16x32_bf16 v[108:111], a[4:7], v[188:191], 0\n"
    "s_nop 0\n"
    "ds_read_u16_d16_hi v205, v205\n"
    "ds_write_b128 v242, v[104:107] offset:25664\n"
    "v_mfma_f32_16x16x32_bf16 v[104:107], a[0:3], v[188:191], 0\n"
    "s_waitcnt lgkmcnt(6)\n"
    "v_cvt_pk_bf16_f32 v123, v126, v127\n"
    "v_cvt_pk_bf16_f32 v136, v136, v137\n"
    "v_pk_fma_f16 v232, v192, v192, s48\n"
    "s_waitcnt lgkmcnt(5)\n"
    "v_mfma_f32_16x16x32_bf16 v[132:135], a[244:247], v[184:187], 0\n"
    "s_nop 0\n"
    "v_cvt_pk_bf16_f32 v137, v138, v139\n"
    "v_cvt_pk_bf16_f32 v138, v140, v141\n"
    "v_mfma_f32_16x16x32_bf16 v[124:127], a[12:15], v[188:191], 0\n"
    "ds_read_u16 v198, v198\n"
    "ds_write_b128 v242, v[120:123] offset:27712\n"
    "v_mfma_f32_16x16x32_bf16 v[120:123], a[8:11], v[188:191], 0\n"
    "v_or_b32_e64 v195, v195, v203\n"
    "v_cvt_pk_bf16_f32 v139, v142, v143\n"
    "v_pk_mul_f16 v232, v192, v232\n"
    "v_cvt_pk_f16_f32 v184, v168, v169\n"
    "v_exp_f16_e64 v234, v232\n"
    "v_mfma_f32_16x16x32_bf16 v[140:143], a[244:247], v[188:191], 0\n"
    "ds_read_u16_d16_hi v206, v206\n"
    "ds_write_b128 v242, v[136:139] offset:29760\n"
    "v_mfma_f32_16x16x32_bf16 v[136:139], v[236:239], v[188:191], 0\n"
    "v_exp_f16_sdwa v234, v232 dst_sel:WORD_1 dst_unused:UNUSED_PRESERVE src0_sel:WORD_1\n"
    "v_lshlrev_b32_sdwa v185, 1, v170 dst_sel:DWORD dst_unused:UNUSED_PAD src0_sel:DWORD src1_sel:WORD_1\n"
    "v_pk_fma_f16 v234, v234, s49, s49\n"
    "s_waitcnt lgkmcnt(4)\n"
    "v_or_b32_e32 v197, v197, v205\n"
    "v_lshlrev_b32_sdwa v201, 1, v171 dst_sel:DWORD dst_unused:UNUSED_PAD src0_sel:DWORD src1_sel:WORD_1\n"
    "v_mfma_f32_16x16x32_bf16 v[168:171], a[152:155], v[144:147], 0\n"
    "ds_read_u16 v199, v199\n"
    "ds_read_u16_d16_hi v207, v207\n"
    "v_mfma_f32_16x16x32_bf16 v[168:171], a[200:203], v[148:151], v[168:171]\n"
    "ds_read_u16 v185, v185\n"
    "ds_read_u16_d16_hi v201, v201\n"
    "v_lshlrev_b32_sdwa v186, 1, v172 dst_sel:DWORD dst_unused:UNUSED_PAD src0_sel:DWORD src1_sel:WORD_1\n"
    "v_lshlrev_b32_sdwa v202, 1, v173 dst_sel:DWORD dst_unused:UNUSED_PAD src0_sel:DWORD src1_sel:WORD_1\n"
    "v_lshlrev_b32_sdwa v187, 1, v174 dst_sel:DWORD dst_unused:UNUSED_PAD src0_sel:DWORD src1_sel:WORD_1\n"
    "v_lshlrev_b32_sdwa v203, 1, v175 dst_sel:DWORD dst_unused:UNUSED_PAD src0_sel:DWORD src1_sel:WORD_1\n"
    "v_rcp_f16_e64 v232, v234\n"
    "v_mfma_f32_16x16x32_bf16 v[168:171], a[112:115], v[152:155], v[168:171]\n"
    "ds_read_u16 v186, v186\n"
    "v_rcp_f16_sdwa v232, v234 dst_sel:WORD_1 dst_unused:UNUSED_PRESERVE src0_sel:WORD_1\n"
    "v_mfma_f32_16x16x32_bf16 v[172:175], a[156:159], v[144:147], 0\n"
    "ds_read_u16_d16_hi v202, v202\n"
    "v_cvt_pk_f16_f32 v188, v176, v177\n"
    "v_mfma_f32_16x16x32_bf16 v[172:175], a[204:207], v[148:151], v[172:175]\n"
    "v_lshlrev_b32_sdwa v189, 1, v178 dst_sel:DWORD dst_unused:UNUSED_PAD src0_sel:DWORD src1_sel:WORD_1\n"
    "v_lshlrev_b32_sdwa v205, 1, v179 dst_sel:DWORD dst_unused:UNUSED_PAD src0_sel:DWORD src1_sel:WORD_1\n"
    "s_waitcnt lgkmcnt(6)\n"
    "v_or_b32_e32 v198, v198, v206\n"
    "v_lshlrev_b32_sdwa v190, 1, v180 dst_sel:DWORD dst_unused:UNUSED_PAD src0_sel:DWORD src1_sel:WORD_1\n"
    "v_lshlrev_b32_sdwa v206, 1, v181 dst_sel:DWORD dst_unused:UNUSED_PAD src0_sel:DWORD src1_sel:WORD_1\n"
    "v_lshlrev_b32_sdwa v191, 1, v182 dst_sel:DWORD dst_unused:UNUSED_PAD src0_sel:DWORD src1_sel:WORD_1\n"
    "v_fma_mix_f32 v234, v192, v232, 0 op_sel_hi:[1,1,0]\n"
    "v_mfma_f32_16x16x32_bf16 v[172:175], a[116:119], v[152:155], v[172:175]\n"
    "s_barrier\n"
    "v_fma_mix_f32 v232, v192, v232, 0 op_sel:[1,1,0] op_sel_hi:[1,1,0]\n"
    "v_mfma_f32_16x16x32_bf16 v[176:179], a[152:155], v[156:159], 0\n"
    "ds_read_b128 v[236:239], v244 offset:4096\n"
    "ds_read_u16 v187, v187\n"
    "v_cvt_pk_bf16_f32 v192, v234, v232\n"
    "v_mfma_f32_16x16x32_bf16 v[176:179], a[200:203], v[160:163], v[176:179]\n"
    "s_waitcnt lgkmcnt(6)\n"
    "ds_read_u16_d16_hi v203, v203\n"
    "ds_read_b128 a[244:247], v244 offset:6144\n"
    "v_mfma_f32_16x16x32_bf16 v[176:179], a[112:115], v[164:167], v[176:179]\n"
    "ds_read_u16 v189, v189\n"
    "v_or_b32_e64 v199, v199, v207\n"
    "v_lshlrev_b32_sdwa v207, 1, v183 dst_sel:DWORD dst_unused:UNUSED_PAD src0_sel:DWORD src1_sel:WORD_1\n"
    "v_pk_fma_f16 v232, v196, v196, s48\n"
    "v_mfma_f32_16x16x32_bf16 v[180:183], a[156:159], v[156:159], 0\n"
    "ds_read_u16_d16_hi v205, v205\n"
    "v_pk_mul_f16 v232, v196, v232\n"
    "v_mfma_f32_16x16x32_bf16 v[180:183], a[204:207], v[160:163], v[180:183]\n"
    "s_waitcnt lgkmcnt(8)\n"
    "v_exp_f16_e32 v234, v232\n"
    "v_mfma_f32_16x16x32_bf16 v[180:183], a[116:119], v[164:167], v[180:183]\n"
    "ds_read_u16 v190, v190\n"
    "v_exp_f16_sdwa v234, v232 dst_sel:WORD_1 dst_unused:UNUSED_PRESERVE src0_sel:WORD_1\n"
    "v_mfma_f32_16x16x32_bf16 v[96:99], a[16:19], v[192:195], v[96:99]\n"
    "ds_read_u16_d16_hi v206, v206\n"
    "v_pk_fma_f16 v234, v234, s49, s49\n"
    "v_mfma_f32_16x16x32_bf16 v[100:103], a[20:23], v[192:195], v[100:103]\n"
    "v_rcp_f16_e32 v232, v234\n"
    "v_mfma_f32_16x16x32_bf16 v[112:115], a[24:27], v[192:195], v[112:115]\n"
    "s_waitcnt lgkmcnt(7)\n"
    "ds_read_u16 v191, v191\n"
    "v_rcp_f16_sdwa v232, v234 dst_sel:WORD_1 dst_unused:UNUSED_PRESERVE src0_sel:WORD_1\n"
    "v_mfma_f32_16x16x32_bf16 v[116:119], a[28:31], v[192:195], v[116:119]\n"
    "ds_read_u16_d16_hi v207, v207\n"
    "v_or_b32_e64 v185, v185, v201\n"
    "v_fma_mix_f32 v234, v196, v232, 0 op_sel_hi:[1,1,0]\n"
    "v_mfma_f32_16x16x32_bf16 v[128:131], v[236:239], v[192:195], v[128:131]\n"
    "v_fma_mix_f32 v232, v196, v232, 0 op_sel:[1,1,0] op_sel_hi:[1,1,0]\n"
    "v_or_b32_e64 v186, v186, v202\n"
    "v_cvt_pk_bf16_f32 v196, v234, v232\n"
    "v_pk_fma_f16 v232, v184, v184, s48\n"
    "s_waitcnt lgkmcnt(6)\n"
    "v_mfma_f32_16x16x32_bf16 v[132:135], a[244:247], v[192:195], v[132:135]\n"
    "s_nop 0\n"
    "ds_read_b128 a[240:243], v244 offset:34816\n"
    "v_or_b32_e64 v187, v187, v203\n"
    "v_mfma_f32_16x16x32_bf16 v[104:107], a[16:19], v[196:199], v[104:107]\n"
    "v_pk_mul_f16 v232, v184, v232\n"
    "v_lshlrev_b32_sdwa v192, 1, v168 dst_sel:DWORD dst_unused:UNUSED_PAD src0_sel:DWORD src1_sel:WORD_1\n"
    "v_mfma_f32_16x16x32_bf16 v[108:111], a[20:23], v[196:199], v[108:111]\n"
    "ds_read_u16 v192, v192\n"
    "v_exp_f16_e64 v234, v232\n"
    "v_mfma_f32_16x16x32_bf16 v[120:123], a[24:27], v[196:199], v[120:123]\n"
    "v_exp_f16_sdwa v234, v232 dst_sel:WORD_1 dst_unused:UNUSED_PRESERVE src0_sel:WORD_1\n"
    "v_mfma_f32_16x16x32_bf16 v[124:127], a[28:31], v[196:199], v[124:127]\n"
    "s_waitcnt lgkmcnt(6)\n"
    "v_or_b32_e32 v189, v189, v205\n"
    "v_pk_fma_f16 v234, v234, s49, s49\n"
    "v_mfma_f32_16x16x32_bf16 v[136:139], v[236:239], v[196:199], v[136:139]\n"
    "ds_read_b128 v[236:239], v244 offset:8192\n"
    "v_lshlrev_b32_sdwa v200, 1, v169 dst_sel:DWORD dst_unused:UNUSED_PAD src0_sel:DWORD src1_sel:WORD_1\n"
    "v_mfma_f32_16x16x32_bf16 v[140:143], a[244:247], v[196:199], v[140:143]\n"
    "ds_read_u16_d16_hi v200, v200\n"
    "v_lshlrev_b32_sdwa v193, 1, v170 dst_sel:DWORD dst_unused:UNUSED_PAD src0_sel:DWORD src1_sel:WORD_1\n"
    "v_lshlrev_b32_sdwa v201, 1, v171 dst_sel:DWORD dst_unused:UNUSED_PAD src0_sel:DWORD src1_sel:WORD_1\n"
    "v_mfma_f32_16x16x32_bf16 v[168:171], a[160:163], v[144:147], 0\n"
    "s_waitcnt lgkmcnt(6)\n"
    "ds_read_u16 v193, v193\n"
    "ds_read_u16_d16_hi v201, v201\n"
    "v_mfma_f32_16x16x32_bf16 v[168:171], a[208:211], v[148:151], v[168:171]\n"
    "s_nop 0\n"
    "v_lshlrev_b32_sdwa v194, 1, v172 dst_sel:DWORD dst_unused:UNUSED_PAD src0_sel:DWORD src1_sel:WORD_1\n"
    "v_lshlrev_b32_sdwa v202, 1, v173 dst_sel:DWORD dst_unused:UNUSED_PAD src0_sel:DWORD src1_sel:WORD_1\n"
    "v_lshlrev_b32_sdwa v195, 1, v174 dst_sel:DWORD dst_unused:UNUSED_PAD src0_sel:DWORD src1_sel:WORD_1\n"
    "v_lshlrev_b32_sdwa v203, 1, v175 dst_sel:DWORD dst_unused:UNUSED_PAD src0_sel:DWORD src1_sel:WORD_1\n"
    "v_rcp_f16_e64 v232, v234\n"
    "v_mfma_f32_16x16x32_bf16 v[168:171], a[120:123], v[152:155], v[168:171]\n"
    "ds_read_u16 v194, v194\n"
    "ds_read_u16_d16_hi v202, v202\n"
    "v_or_b32_e32 v190, v190, v206\n"
    "v_rcp_f16_sdwa v232, v234 dst_sel:WORD_1 dst_unused:UNUSED_PRESERVE src0_sel:WORD_1\n"
    "v_mfma_f32_16x16x32_bf16 v[172:175], a[164:167], v[144:147], 0\n"
    "s_waitcnt lgkmcnt(7)\n"
    "ds_read_u16 v195, v195\n"
    "v_lshlrev_b32_sdwa v196, 1, v176 dst_sel:DWORD dst_unused:UNUSED_PAD src0_sel:DWORD src1_sel:WORD_1\n"
    "v_lshlrev_b32_sdwa v204, 1, v177 dst_sel:DWORD dst_unused:UNUSED_PAD src0_sel:DWORD src1_sel:WORD_1\n"
    "v_lshlrev_b32_sdwa v197, 1, v178 dst_sel:DWORD dst_unused:UNUSED_PAD src0_sel:DWORD src1_sel:WORD_1\n"
    "v_lshlrev_b32_sdwa v205, 1, v179 dst_sel:DWORD dst_unused:UNUSED_PAD src0_sel:DWORD src1_sel:WORD_1\n"
    "v_lshlrev_b32_sdwa v198, 1, v180 dst_sel:DWORD dst_unused:UNUSED_PAD src0_sel:DWORD src1_sel:WORD_1\n"
    "v_lshlrev_b32_sdwa v206, 1, v181 dst_sel:DWORD dst_unused:UNUSED_PAD src0_sel:DWORD src1_sel:WORD_1\n"
    "v_lshlrev_b32_sdwa v199, 1, v182 dst_sel:DWORD dst_unused:UNUSED_PAD src0_sel:DWORD src1_sel:WORD_1\n"
    "s_waitcnt vmcnt(1)\n"
    "v_smfmac_f32_32x32x32_bf16 v[112:127], a[248:251], a[232:239], v241\n"
    "s_nop 0\n"
    "ds_read_b128 a[232:235], v243 offset:24640\n"
    "ds_read_b128 a[236:239], v243 offset:25664\n"
    "v_fma_mix_f32 v234, v184, v232, 0 op_sel_hi:[1,1,0]\n"
    "v_or_b32_e64 v191, v191, v207\n"
    "v_lshlrev_b32_sdwa v207, 1, v183 dst_sel:DWORD dst_unused:UNUSED_PAD src0_sel:DWORD src1_sel:WORD_1\n"
    "s_waitcnt vmcnt(0)\n"
    "v_smfmac_f32_32x32x32_bf16 v[96:111], a[248:251], a[224:231], v241\n"
    "s_waitcnt lgkmcnt(7)\n"
    "buffer_load_dwordx4 a[224:227], v245, s[40:43], s57 offen offset:128\n"
    "ds_read_b128 a[244:247], v244 offset:10240\n"
    "v_fma_mix_f32 v232, v184, v232, 0 op_sel:[1,1,0] op_sel_hi:[1,1,0]\n"
    "v_or_b32_e64 v192, v192, v200\n"
    "v_cvt_pk_bf16_f32 v184, v234, v232\n"
    "v_mfma_f32_16x16x32_bf16 v[172:175], a[212:215], v[148:151], v[172:175]\n"
    "ds_read_u16_d16_hi v203, v203\n"
    "ds_read_u16 v196, v196\n"
    "v_mfma_f32_16x16x32_bf16 v[172:175], a[240:243], v[152:155], v[172:175]\n"
    "ds_read_u16_d16_hi v204, v204\n"
    "v_pk_fma_f16 v232, v188, v188, s48\n"
    "v_mfma_f32_16x16x32_bf16 v[176:179], a[160:163], v[156:159], 0\n"
    "s_waitcnt lgkmcnt(9)\n"
    "ds_read_u16 v197, v197\n"
    "v_pk_mul_f16 v232, v188, v232\n"
    "v_mfma_f32_16x16x32_bf16 v[176:179], a[208:211], v[160:163], v[176:179]\n"
    "v_exp_f16_e32 v234, v232\n"
    "v_mfma_f32_16x16x32_bf16 v[176:179], a[120:123], v[164:167], v[176:179]\n"
    "ds_read_u16_d16_hi v205, v205\n"
    "v_exp_f16_sdwa v234, v232 dst_sel:WORD_1 dst_unused:UNUSED_PRESERVE src0_sel:WORD_1\n"
    "v_mfma_f32_16x16x32_bf16 v[180:183], a[164:167], v[156:159], 0\n"
    "s_waitcnt lgkmcnt(9)\n"
    "v_pk_fma_f16 v234, v234, s49, s49\n"
    "v_or_b32_e32 v193, v193, v201\n"
    "v_mfma_f32_16x16x32_bf16 v[180:183], a[212:215], v[160:163], v[180:183]\n"
    "ds_read_u16 v198, v198\n"
    "v_rcp_f16_e32 v232, v234\n"
    "s_waitcnt lgkmcnt(7)\n"
    "v_smfmac_f32_32x32x32_bf16 v[96:111], a[248:251], a[232:239], v241\n"
    "buffer_load_dwordx4 a[228:231], v245, s[40:43], s100 offen offset:128\n"
    "ds_read_u16_d16_hi v206, v206\n"
    "v_rcp_f16_sdwa v232, v234 dst_sel:WORD_1 dst_unused:UNUSED_PRESERVE src0_sel:WORD_1\n"
    "v_lshlrev_b32_sdwa v201, 1, v171 dst_sel:DWORD dst_unused:UNUSED_PAD src0_sel:DWORD src1_sel:WORD_1\n"
    "v_mfma_f32_16x16x32_bf16 v[180:183], a[240:243], v[164:167], v[180:183]\n"
    "s_nop 0\n"
    "ds_read_u16 v199, v199\n"
    "v_fma_mix_f32 v234, v188, v232, 0 op_sel_hi:[1,1,0]\n"
    "v_mfma_f32_16x16x32_bf16 v[112:115], a[40:43], v[184:187], v[112:115]\n"
    "s_waitcnt lgkmcnt(7)\n"
    "ds_read_u16_d16_hi v207, v207\n"
    "v_fma_mix_f32 v232, v188, v232, 0 op_sel:[1,1,0] op_sel_hi:[1,1,0]\n"
    "v_mfma_f32_16x16x32_bf16 v[96:99], a[32:35], v[184:187], v[96:99]\n"
    "v_cvt_pk_bf16_f32 v188, v234, v232\n"
    "v_or_b32_e64 v194, v194, v202\n"
    "v_mfma_f32_16x16x32_bf16 v[100:103], a[36:39], v[184:187], v[100:103]\n"
    "ds_read_b128 a[232:235], v243 offset:26688\n"
    "v_lshlrev_b32_sdwa v202, 1, v173 dst_sel:DWORD dst_unused:UNUSED_PAD src0_sel:DWORD src1_sel:WORD_1\n"
    "v_mfma_f32_16x16x32_bf16 v[116:119], a[44:47], v[184:187], v[116:119]\n"
    "ds_read_b128 a[236:239], v243 offset:27712\n"
    "v_or_b32_e32 v195, v195, v203\n"
    "v_mfma_f32_16x16x32_bf16 v[128:131], v[236:239], v[184:187], v[128:131]\n"
    "s_waitcnt lgkmcnt(8)\n"
    "ds_read_b128 a[240:243], v244 offset:38912\n"
    "v_lshlrev_b32_sdwa v203, 1, v175 dst_sel:DWORD dst_unused:UNUSED_PAD src0_sel:DWORD src1_sel:WORD_1\n"
    "v_mfma_f32_16x16x32_bf16 v[132:135], a[244:247], v[184:187], v[132:135]\n"
    "ds_read_u16_d16_hi v201, v201\n"
    "v_cvt_pk_f16_f32 v184, v168, v169\n"
    "v_mfma_f32_16x16x32_bf16 v[104:107], a[32:35], v[188:191], v[104:107]\n"
    "v_lshlrev_b32_sdwa v185, 1, v170 dst_sel:DWORD dst_unused:UNUSED_PAD src0_sel:DWORD src1_sel:WORD_1\n"
    "v_lshlrev_b32_sdwa v186, 1, v172 dst_sel:DWORD dst_unused:UNUSED_PAD src0_sel:DWORD src1_sel:WORD_1\n"
    "v_mfma_f32_16x16x32_bf16 v[108:111], a[36:39], v[188:191], v[108:111]\n"
    "s_waitcnt lgkmcnt(8)\n"
    "ds_read_u16 v185, v185\n"
    "v_lshlrev_b32_sdwa v187, 1, v174 dst_sel:DWORD dst_unused:UNUSED_PAD src0_sel:DWORD src1_sel:WORD_1\n"
    "v_mfma_f32_16x16x32_bf16 v[120:123], a[40:43], v[188:191], v[120:123]\n"
    "v_or_b32_e32 v196, v196, v204\n"
    "v_or_b32_e64 v197, v197, v205\n"
    "v_mfma_f32_16x16x32_bf16 v[124:127], a[44:47], v[188:191], v[124:127]\n"
    "ds_read_u16 v186, v186\n"
    "v_lshlrev_b32_sdwa v205, 1, v179 dst_sel:DWORD dst_unused:UNUSED_PAD src0_sel:DWORD src1_sel:WORD_1\n"
    "v_mfma_f32_16x16x32_bf16 v[136:139], v[236:239], v[188:191], v[136:139]\n"
    "s_nop 0\n"
    "ds_read_u16_d16_hi v202, v202\n"
    "v_pk_fma_f16 v232, v184, v184, s48\n"
    "v_mfma_f32_16x16x32_bf16 v[140:143], a[244:247], v[188:191], v[140:143]\n"
    "s_waitcnt lgkmcnt(9)\n"
    "v_cvt_pk_f16_f32 v188, v176, v177\n"
    "v_lshlrev_b32_sdwa v189, 1, v178 dst_sel:DWORD dst_unused:UNUSED_PAD src0_sel:DWORD src1_sel:WORD_1\n"
    "v_mfma_f32_16x16x32_bf16 v[168:171], a[168:171], v[144:147], 0\n"
    "ds_read_b128 v[236:239], v244 offset:12288\n"
    "v_lshlrev_b32_sdwa v190, 1, v180 dst_sel:DWORD dst_unused:UNUSED_PAD src0_sel:DWORD src1_sel:WORD_1\n"
    "v_mfma_f32_16x16x32_bf16 v[168:171], a[216:219], v[148:151], v[168:171]\n"
    "ds_read_u16 v187, v187\n"
    "v_lshlrev_b32_sdwa v191, 1, v182 dst_sel:DWORD dst_unused:UNUSED_PAD src0_sel:DWORD src1_sel:WORD_1\n"
    "v_mfma_f32_16x16x32_bf16 v[168:171], a[124:127], v[152:155], v[168:171]\n"
    "s_waitcnt lgkmcnt(8)\n"
    "v_or_b32_e32 v198, v198, v206\n"
    "v_lshlrev_b32_sdwa v206, 1, v181 dst_sel:DWORD dst_unused:UNUSED_PAD src0_sel:DWORD src1_sel:WORD_1\n"
    "v_mfma_f32_16x16x32_bf16 v[172:175], a[172:175], v[144:147], 0\n"
    "s_waitcnt lgkmcnt(7)\n"
    "ds_read_u16_d16_hi v203, v203\n"
    "v_or_b32_e32 v199, v199, v207\n"
    "v_smfmac_f32_32x32x32_bf16 v[112:127], a[248:251], a[232:239], v241\n"
    "ds_read_b128 a[244:247], v244 offset:14336\n"
    "v_lshlrev_b32_sdwa v207, 1, v183 dst_sel:DWORD dst_unused:UNUSED_PAD src0_sel:DWORD src1_sel:WORD_1\n"
    "v_pk_mul_f16 v232, v184, v232\n"
    "v_exp_f16_e32 v234, v232\n"
    "v_mfma_f32_16x16x32_bf16 v[172:175], a[220:223], v[148:151], v[172:175]\n"
    "s_waitcnt lgkmcnt(8)\n"
    "ds_read_u16 v189, v189\n"
    "v_exp_f16_sdwa v234, v232 dst_sel:WORD_1 dst_unused:UNUSED_PRESERVE src0_sel:WORD_1\n"
    "v_mfma_f32_16x16x32_bf16 v[172:175], a[240:243], v[152:155], v[172:175]\n"
    "ds_read_b128 a[232:235], v243 offset:28736\n"
    "v_pk_fma_f16 v234, v234, s49, s49\n"
    "v_mfma_f32_16x16x32_bf16 v[176:179], a[168:171], v[156:159], 0\n"
    "ds_read_u16_d16_hi v205, v205\n"
    "s_waitcnt lgkmcnt(9)\n"
    "v_or_b32_e32 v185, v185, v201\n"
    "v_mfma_f32_16x16x32_bf16 v[176:179], a[216:219], v[160:163], v[176:179]\n"
    "v_lshlrev_b32_sdwa v201, 1, v171 dst_sel:DWORD dst_unused:UNUSED_PAD src0_sel:DWORD src1_sel:WORD_1\n"
    "v_rcp_f16_e64 v232, v234\n"
    "v_mfma_f32_16x16x32_bf16 v[176:179], a[124:127], v[164:167], v[176:179]\n"
    "ds_read_b128 a[236:239], v243 offset:29760\n"
    "ds_read_u16 v190, v190\n"
    "v_mfma_f32_16x16x32_bf16 v[180:183], a[172:175], v[156:159], 0\n"
    "s_waitcnt lgkmcnt(8)\n"
    "v_or_b32_e32 v186, v186, v202\n"
    "v_lshlrev_b32_sdwa v202, 1, v173 dst_sel:DWORD dst_unused:UNUSED_PAD src0_sel:DWORD src1_sel:WORD_1\n"
    "v_mfma_f32_16x16x32_bf16 v[180:183], a[220:223], v[160:163], v[180:183]\n"
    "ds_read_b128 v[144:147], v242 offset:0\n"
    "v_rcp_f16_sdwa v232, v234 dst_sel:WORD_1 dst_unused:UNUSED_PRESERVE src0_sel:WORD_1\n"
    "v_mfma_f32_16x16x32_bf16 v[180:183], a[240:243], v[164:167], v[180:183]\n"
    "ds_read_u16_d16_hi v206, v206\n"
    "v_fma_mix_f32 v234, v184, v232, 0 op_sel_hi:[1,1,0]\n"
    "v_mfma_f32_16x16x32_bf16 v[96:99], a[48:51], v[192:195], v[96:99]\n"
    "v_fma_mix_f32 v232, v184, v232, 0 op_sel:[1,1,0] op_sel_hi:[1,1,0]\n"
    "v_cvt_pk_bf16_f32 v184, v234, v232\n"
    "v_mfma_f32_16x16x32_bf16 v[100:103], a[52:55], v[192:195], v[100:103]\n"
    "ds_read_b128 v[148:151], v242 offset:1024\n"
    "ds_read_u16 v191, v191\n"
    "v_mfma_f32_16x16x32_bf16 v[112:115], a[56:59], v[192:195], v[112:115]\n"
    "s_waitcnt lgkmcnt(9)\n"
    "v_or_b32_e32 v187, v187, v203\n"
    "v_lshlrev_b32_sdwa v203, 1, v175 dst_sel:DWORD dst_unused:UNUSED_PAD src0_sel:DWORD src1_sel:WORD_1\n"
    "v_mfma_f32_16x16x32_bf16 v[116:119], a[60:63], v[192:195], v[116:119]\n"
    "ds_read_b128 v[152:155], v242 offset:2048\n"
    "v_pk_fma_f16 v232, v188, v188, s48\n"
    "v_mfma_f32_16x16x32_bf16 v[128:131], v[236:239], v[192:195], v[128:131]\n"
    "ds_read_u16_d16_hi v207, v207\n"
    "v_pk_mul_f16 v232, v188, v232\n"
    "v_mfma_f32_16x16x32_bf16 v[132:135], a[244:247], v[192:195], v[132:135]\n"
    "v_cvt_pk_f16_f32 v192, v168, v169\n"
    "v_lshlrev_b32_sdwa v193, 1, v170 dst_sel:DWORD dst_unused:UNUSED_PAD src0_sel:DWORD src1_sel:WORD_1\n"
    "v_mfma_f32_16x16x32_bf16 v[104:107], a[48:51], v[196:199], v[104:107]\n"
    "ds_read_b128 v[156:159], v242 offset:3072\n"
    "v_lshlrev_b32_sdwa v194, 1, v172 dst_sel:DWORD dst_unused:UNUSED_PAD src0_sel:DWORD src1_sel:WORD_1\n"
    "v_mfma_f32_16x16x32_bf16 v[108:111], a[52:55], v[196:199], v[108:111]\n"
    "s_waitcnt lgkmcnt(8)\n"
    "ds_read_b128 v[160:163], v242 offset:4096\n"
    "v_or_b32_e32 v189, v189, v205\n"
    "v_mfma_f32_16x16x32_bf16 v[120:123], a[56:59], v[196:199], v[120:123]\n"
    "ds_read_b128 v[164:167], v242 offset:5120\n"
    "v_lshlrev_b32_sdwa v195, 1, v174 dst_sel:DWORD dst_unused:UNUSED_PAD src0_sel:DWORD src1_sel:WORD_1\n"
    "v_mfma_f32_16x16x32_bf16 v[124:127], a[60:63], v[196:199], v[124:127]\n"
    "ds_read_b128 a[240:243], v244 offset:32768\n"
    "v_lshlrev_b32_sdwa v205, 1, v179 dst_sel:DWORD dst_unused:UNUSED_PAD src0_sel:DWORD src1_sel:WORD_1\n"
    "v_mfma_f32_16x16x32_bf16 v[136:139], v[236:239], v[196:199], v[136:139]\n"
    "s_waitcnt lgkmcnt(9)\n"
    "ds_read_u16 v193, v193\n"
    "v_exp_f16_e32 v234, v232\n"
    "v_mfma_f32_16x16x32_bf16 v[140:143], a[244:247], v[196:199], v[140:143]\n"
    "v_cvt_pk_f16_f32 v196, v176, v177\n"
    "v_lshlrev_b32_sdwa v197, 1, v178 dst_sel:DWORD dst_unused:UNUSED_PAD src0_sel:DWORD src1_sel:WORD_1\n"
    "v_mfma_f32_16x16x32_bf16 v[168:171], a[128:131], v[144:147], 0\n"
    "s_waitcnt lgkmcnt(8)\n"
    "ds_read_u16_d16_hi v201, v201\n"
    "v_or_b32_e32 v190, v190, v206\n"
    "v_smfmac_f32_32x32x32_bf16 v[128:143], a[248:251], a[232:239], v241\n"
    "ds_read_u16 v194, v194\n"
    "v_lshlrev_b32_sdwa v198, 1, v180 dst_sel:DWORD dst_unused:UNUSED_PAD src0_sel:DWORD src1_sel:WORD_1\n"
    "v_lshlrev_b32_sdwa v206, 1, v181 dst_sel:DWORD dst_unused:UNUSED_PAD src0_sel:DWORD src1_sel:WORD_1\n"
    "v_lshlrev_b32_sdwa v199, 1, v182 dst_sel:DWORD dst_unused:UNUSED_PAD src0_sel:DWORD src1_sel:WORD_1\n"
    "v_exp_f16_sdwa v234, v232 dst_sel:WORD_1 dst_unused:UNUSED_PRESERVE src0_sel:WORD_1\n"
    "v_mfma_f32_16x16x32_bf16 v[168:171], a[176:179], v[148:151], v[168:171]\n"
    "s_waitcnt lgkmcnt(8)\n"
    "ds_read_u16_d16_hi v202, v202\n"
    "v_pk_fma_f16 v234, v234, s49, s49\n"
    "v_mfma_f32_16x16x32_bf16 v[168:171], a[96:99], v[152:155], v[168:171]\n"
    "v_rcp_f16_e32 v232, v234\n"
    "v_mfma_f32_16x16x32_bf16 v[172:175], a[132:135], v[144:147], 0\n"
    "ds_read_b128 v[236:239], v244 offset:16384\n"
    "ds_read_u16 v195, v195\n"
    "v_mfma_f32_16x16x32_bf16 v[172:175], a[180:183], v[148:151], v[172:175]\n"
    "s_waitcnt lgkmcnt(9)\n"
    "v_or_b32_e32 v191, v191, v207\n"
    "v_lshlrev_b32_sdwa v207, 1, v183 dst_sel:DWORD dst_unused:UNUSED_PAD src0_sel:DWORD src1_sel:WORD_1\n"
    "v_mfma_f32_16x16x32_bf16 v[176:179], a[128:131], v[156:159], 0\n"
    "s_waitcnt lgkmcnt(8)\n"
    "ds_read_u16_d16_hi v203, v203\n"
    "v_rcp_f16_sdwa v232, v234 dst_sel:WORD_1 dst_unused:UNUSED_PRESERVE src0_sel:WORD_1\n"
    "v_mfma_f32_16x16x32_bf16 v[176:179], a[176:179], v[160:163], v[176:179]\n"
    "s_waitcnt lgkmcnt(7)\n"
    "v_fma_mix_f32 v234, v188, v232, 0 op_sel_hi:[1,1,0]\n"
    "v_fma_mix_f32 v232, v188, v232, 0 op_sel:[1,1,0] op_sel_hi:[1,1,0]\n"
    "v_mfma_f32_16x16x32_bf16 v[172:175], a[240:243], v[152:155], v[172:175]\n"
    "ds_read_b128 a[244:247], v244 offset:18432\n"
    "v_cvt_pk_bf16_f32 v188, v234, v232\n"
    "v_mfma_f32_16x16x32_bf16 v[176:179], a[96:99], v[164:167], v[176:179]\n"
    "ds_read_u16 v197, v197\n"
    "v_pk_fma_f16 v232, v192, v192, s48\n"
    "v_mfma_f32_16x16x32_bf16 v[180:183], a[132:135], v[156:159], 0\n"
    "v_pk_mul_f16 v232, v192, v232\n"
    "v_lshlrev_b32_sdwa v200, 1, v169 dst_sel:DWORD dst_unused:UNUSED_PAD src0_sel:DWORD src1_sel:WORD_1\n"
    "v_mfma_f32_16x16x32_bf16 v[180:183], a[180:183], v[160:163], v[180:183]\n"
    "ds_read_u16_d16_hi v205, v205\n"
    "v_exp_f16_e32 v234, v232\n"
    "v_mfma_f32_16x16x32_bf16 v[180:183], a[240:243], v[164:167], v[180:183]\n"
    "s_waitcnt lgkmcnt(8)\n"
    "ds_read_u16 v198, v198\n"
    "v_or_b32_e64 v193, v193, v201\n"
    "v_mfma_f32_16x16x32_bf16 v[96:99], a[64:67], v[184:187], v[96:99]\n"
    "v_exp_f16_sdwa v234, v232 dst_sel:WORD_1 dst_unused:UNUSED_PRESERVE src0_sel:WORD_1\n"
    "v_mfma_f32_16x16x32_bf16 v[100:103], a[68:71], v[184:187], v[100:103]\n"
    "ds_read_u16_d16_hi v206, v206\n"
    "v_pk_fma_f16 v234, v234, s49, s49\n"
    "v_mfma_f32_16x16x32_bf16 v[112:115], a[72:75], v[184:187], v[112:115]\n"
    "s_waitcnt lgkmcnt(7)\n"
    "v_or_b32_e32 v194, v194, v202\n"
    "v_lshlrev_b32_sdwa v201, 1, v171 dst_sel:DWORD dst_unused:UNUSED_PAD src0_sel:DWORD src1_sel:WORD_1\n"
    "v_mfma_f32_16x16x32_bf16 v[116:119], a[76:79], v[184:187], v[116:119]\n"
    "ds_read_u16 v199, v199\n"
    "v_lshlrev_b32_sdwa v202, 1, v173 dst_sel:DWORD dst_unused:UNUSED_PAD src0_sel:DWORD src1_sel:WORD_1\n"
    "v_mfma_f32_16x16x32_bf16 v[128:131], v[236:239], v[184:187], v[128:131]\n"
    "ds_read_u16_d16_hi v207, v207\n"
    "v_rcp_f16_e32 v232, v234\n"
    "v_mfma_f32_16x16x32_bf16 v[104:107], a[64:67], v[188:191], v[104:107]\n"
    "v_rcp_f16_sdwa v232, v234 dst_sel:WORD_1 dst_unused:UNUSED_PRESERVE src0_sel:WORD_1\n"
    "v_mfma_f32_16x16x32_bf16 v[108:111], a[68:71], v[188:191], v[108:111]\n"
    "s_waitcnt lgkmcnt(6)\n"
    "ds_read_b128 a[240:243], v244 offset:36864\n"
    "v_or_b32_e64 v195, v195, v203\n"
    "v_mfma_f32_16x16x32_bf16 v[132:135], a[244:247], v[184:187], v[132:135]\n"
    "ds_read_u16_d16_hi v200, v200\n"
    "v_lshlrev_b32_sdwa v184, 1, v168 dst_sel:DWORD dst_unused:UNUSED_PAD src0_sel:DWORD src1_sel:WORD_1\n"
    "v_mfma_f32_16x16x32_bf16 v[120:123], a[72:75], v[188:191], v[120:123]\n"
    "v_lshlrev_b32_sdwa v185, 1, v170 dst_sel:DWORD dst_unused:UNUSED_PAD src0_sel:DWORD src1_sel:WORD_1\n"
    "v_lshlrev_b32_sdwa v186, 1, v172 dst_sel:DWORD dst_unused:UNUSED_PAD src0_sel:DWORD src1_sel:WORD_1\n"
    "v_mfma_f32_16x16x32_bf16 v[124:127], a[76:79], v[188:191], v[124:127]\n"
    "ds_read_u16 v184, v184\n"
    "v_lshlrev_b32_sdwa v187, 1, v174 dst_sel:DWORD dst_unused:UNUSED_PAD src0_sel:DWORD src1_sel:WORD_1\n"
    "v_mfma_f32_16x16x32_bf16 v[136:139], v[236:239], v[188:191], v[136:139]\n"
    "ds_read_u16 v185, v185\n"
    "v_lshlrev_b32_sdwa v203, 1, v175 dst_sel:DWORD dst_unused:UNUSED_PAD src0_sel:DWORD src1_sel:WORD_1\n"
    "v_mfma_f32_16x16x32_bf16 v[140:143], a[244:247], v[188:191], v[140:143]\n"
    "s_waitcnt lgkmcnt(8)\n"
    "v_or_b32_e32 v197, v197, v205\n"
    "v_lshlrev_b32_sdwa v188, 1, v176 dst_sel:DWORD dst_unused:UNUSED_PAD src0_sel:DWORD src1_sel:WORD_1\n"
    "v_mfma_f32_16x16x32_bf16 v[168:171], a[136:139], v[144:147], 0\n"
    "ds_read_u16_d16_hi v201, v201\n"
    "v_lshlrev_b32_sdwa v204, 1, v177 dst_sel:DWORD dst_unused:UNUSED_PAD src0_sel:DWORD src1_sel:WORD_1\n"
    "v_mfma_f32_16x16x32_bf16 v[168:171], a[184:187], v[148:151], v[168:171]\n"
    "v_lshlrev_b32_sdwa v189, 1, v178 dst_sel:DWORD dst_unused:UNUSED_PAD src0_sel:DWORD src1_sel:WORD_1\n"
    "v_lshlrev_b32_sdwa v205, 1, v179 dst_sel:DWORD dst_unused:UNUSED_PAD src0_sel:DWORD src1_sel:WORD_1\n"
    "v_mfma_f32_16x16x32_bf16 v[168:171], a[100:103], v[152:155], v[168:171]\n"
    "ds_read_u16 v186, v186\n"
    "s_waitcnt lgkmcnt(8)\n"
    "v_or_b32_e32 v198, v198, v206\n"
    "v_mfma_f32_16x16x32_bf16 v[172:175], a[140:143], v[144:147], 0\n"
    "ds_read_b128 v[236:239], v244 offset:20480\n"
    "v_lshlrev_b32_sdwa v190, 1, v180 dst_sel:DWORD dst_unused:UNUSED_PAD src0_sel:DWORD src1_sel:WORD_1\n"
    "v_lshlrev_b32_sdwa v206, 1, v181 dst_sel:DWORD dst_unused:UNUSED_PAD src0_sel:DWORD src1_sel:WORD_1\n"
    "v_lshlrev_b32_sdwa v191, 1, v182 dst_sel:DWORD dst_unused:UNUSED_PAD src0_sel:DWORD src1_sel:WORD_1\n"
    "v_fma_mix_f32 v234, v192, v232, 0 op_sel_hi:[1,1,0]\n"
    "v_mfma_f32_16x16x32_bf16 v[172:175], a[188:191], v[148:151], v[172:175]\n"
    "ds_read_u16_d16_hi v202, v202\n"
    "v_fma_mix_f32 v232, v192, v232, 0 op_sel:[1,1,0] op_sel_hi:[1,1,0]\n"
    "s_waitcnt lgkmcnt(7)\n"
    "v_mfma_f32_16x16x32_bf16 v[172:175], a[240:243], v[152:155], v[172:175]\n"
    "s_nop 0\n"
    "ds_read_u16 v187, v187\n"
    "v_or_b32_e64 v199, v199, v207\n"
    "v_mfma_f32_16x16x32_bf16 v[176:179], a[136:139], v[156:159], 0\n"
    "v_lshlrev_b32_sdwa v207, 1, v183 dst_sel:DWORD dst_unused:UNUSED_PAD src0_sel:DWORD src1_sel:WORD_1\n"
    "v_cvt_pk_bf16_f32 v192, v234, v232\n"
    "v_mfma_f32_16x16x32_bf16 v[176:179], a[184:187], v[160:163], v[176:179]\n"
    "ds_read_b128 a[244:247], v244 offset:22528\n"
    "v_pk_fma_f16 v232, v196, v196, s48\n"
    "v_mfma_f32_16x16x32_bf16 v[176:179], a[100:103], v[164:167], v[176:179]\n"
    "ds_read_u16_d16_hi v203, v203\n"
    "v_pk_mul_f16 v232, v196, v232\n"
    "v_mfma_f32_16x16x32_bf16 v[180:183], a[140:143], v[156:159], 0\n"
    "s_waitcnt lgkmcnt(8)\n"
    "v_exp_f16_e32 v234, v232\n"
    "v_mfma_f32_16x16x32_bf16 v[180:183], a[188:191], v[160:163], v[180:183]\n"
    "ds_read_u16 v188, v188\n"
    "v_exp_f16_sdwa v234, v232 dst_sel:WORD_1 dst_unused:UNUSED_PRESERVE src0_sel:WORD_1\n"
    "v_mfma_f32_16x16x32_bf16 v[180:183], a[240:243], v[164:167], v[180:183]\n"
    "ds_read_u16_d16_hi v204, v204\n"
    "v_pk_fma_f16 v234, v234, s49, s49\n"
    "v_mfma_f32_16x16x32_bf16 v[96:99], a[80:83], v[192:195], v[96:99]\n"
    "v_rcp_f16_e32 v232, v234\n"
    "v_mfma_f32_16x16x32_bf16 v[100:103], a[84:87], v[192:195], v[100:103]\n"
    "s_waitcnt lgkmcnt(8)\n"
    "ds_read_u16 v189, v189\n"
    "v_rcp_f16_sdwa v232, v234 dst_sel:WORD_1 dst_unused:UNUSED_PRESERVE src0_sel:WORD_1\n"
    "v_mfma_f32_16x16x32_bf16 v[112:115], a[88:91], v[192:195], v[112:115]\n"
    "ds_read_u16_d16_hi v205, v205\n"
    "v_fma_mix_f32 v234, v196, v232, 0 op_sel_hi:[1,1,0]\n"
    "v_mfma_f32_16x16x32_bf16 v[116:119], a[92:95], v[192:195], v[116:119]\n"
    "v_fma_mix_f32 v232, v196, v232, 0 op_sel:[1,1,0] op_sel_hi:[1,1,0]\n"
    "v_or_b32_e64 v184, v184, v200\n"
    "v_cvt_pk_bf16_f32 v196, v234, v232\n"
    "v_or_b32_e64 v185, v185, v201\n"
    "v_lshlrev_b32_sdwa v201, 1, v171 dst_sel:DWORD dst_unused:UNUSED_PAD src0_sel:DWORD src1_sel:WORD_1\n"
    "v_mfma_f32_16x16x32_bf16 v[104:107], a[80:83], v[196:199], v[104:107]\n"
    "s_waitcnt lgkmcnt(7)\n"
    "ds_read_u16 v190, v190\n"
    "v_or_b32_e32 v186, v186, v202\n"
    "v_mfma_f32_16x16x32_bf16 v[108:111], a[84:87], v[196:199], v[108:111]\n"
    "ds_read_u16_d16_hi v206, v206\n"
    "v_lshlrev_b32_sdwa v202, 1, v173 dst_sel:DWORD dst_unused:UNUSED_PAD src0_sel:DWORD src1_sel:WORD_1\n"
    "v_mfma_f32_16x16x32_bf16 v[120:123], a[88:91], v[196:199], v[120:123]\n"
    "v_cvt_pk_bf16_f32 v96, v96, v97\n"
    "v_cvt_pk_bf16_f32 v97, v98, v99\n"
    "v_mfma_f32_16x16x32_bf16 v[124:127], a[92:95], v[196:199], v[124:127]\n"
    "ds_read_u16 v191, v191\n"
    "v_cvt_pk_bf16_f32 v98, v100, v101\n"
    "v_cvt_pk_bf16_f32 v99, v102, v103\n"
    "v_cvt_pk_bf16_f32 v112, v112, v113\n"
    "v_cvt_pk_bf16_f32 v113, v114, v115\n"
    "s_waitcnt lgkmcnt(7)\n"
    "v_or_b32_e32 v187, v187, v203\n"
    "v_lshlrev_b32_sdwa v203, 1, v175 dst_sel:DWORD dst_unused:UNUSED_PAD src0_sel:DWORD src1_sel:WORD_1\n"
    "v_cvt_pk_bf16_f32 v114, v116, v117\n"
    "v_cvt_pk_bf16_f32 v115, v118, v119\n"
    "v_cvt_pk_bf16_f32 v104, v104, v105\n"
    "v_cvt_pk_bf16_f32 v105, v106, v107\n"
    "v_cvt_pk_bf16_f32 v106, v108, v109\n"
    "s_waitcnt vmcnt(0)\n"
    "v_smfmac_f32_32x32x32_bf16 v[128:143], a[248:251], a[224:231], v241\n"
    "s_nop 0\n"
    "ds_read_u16_d16_hi v207, v207\n"
    "ds_write_b128 v243, v[96:99] offset:24640\n"
    "s_waitcnt lgkmcnt(7)\n"
    "v_or_b32_e32 v188, v188, v204\n"
    "v_cvt_pk_bf16_f32 v107, v110, v111\n"
    "v_cvt_pk_bf16_f32 v120, v120, v121\n"
    "v_cvt_pk_bf16_f32 v121, v122, v123\n"
    "v_cvt_pk_bf16_f32 v122, v124, v125\n"
    "v_cvt_pk_bf16_f32 v123, v126, v127\n"
    "ds_write_b128 v243, v[112:115] offset:26688\n"
    "v_mfma_f32_16x16x32_bf16 v[128:131], v[236:239], v[192:195], v[128:131]\n"
    "s_waitcnt lgkmcnt(5)\n"
    "v_or_b32_e32 v189, v189, v205\n"
    "v_lshlrev_b32_sdwa v205, 1, v179 dst_sel:DWORD dst_unused:UNUSED_PAD src0_sel:DWORD src1_sel:WORD_1\n"
    "v_mfma_f32_16x16x32_bf16 v[132:135], a[244:247], v[192:195], v[132:135]\n"
    "s_nop 0\n"
    "ds_read_u16_d16_hi v201, v201\n"
    "ds_write_b128 v243, v[104:107] offset:25664\n"
    "v_mfma_f32_16x16x32_bf16 v[136:139], v[236:239], v[196:199], v[136:139]\n"
    "s_waitcnt lgkmcnt(6)\n"
    "v_cvt_pk_f16_f32 v192, v168, v169\n"
    "v_lshlrev_b32_sdwa v193, 1, v170 dst_sel:DWORD dst_unused:UNUSED_PAD src0_sel:DWORD src1_sel:WORD_1\n"
    "v_mfma_f32_16x16x32_bf16 v[140:143], a[244:247], v[196:199], v[140:143]\n"
    "v_lshlrev_b32_sdwa v194, 1, v172 dst_sel:DWORD dst_unused:UNUSED_PAD src0_sel:DWORD src1_sel:WORD_1\n"
    "v_lshlrev_b32_sdwa v195, 1, v174 dst_sel:DWORD dst_unused:UNUSED_PAD src0_sel:DWORD src1_sel:WORD_1\n"
    "v_mfma_f32_16x16x32_bf16 v[168:171], a[144:147], v[144:147], 0\n"
    "s_waitcnt lgkmcnt(3)\n"
    "ds_read_u16 v193, v193\n"
    "ds_write_b128 v243, v[120:123] offset:27712\n"
    "v_mfma_f32_16x16x32_bf16 v[168:171], a[192:195], v[148:151], v[168:171]\n"
    "s_nop 0\n"
    "v_cvt_pk_f16_f32 v196, v176, v177\n"
    "v_lshlrev_b32_sdwa v197, 1, v178 dst_sel:DWORD dst_unused:UNUSED_PAD src0_sel:DWORD src1_sel:WORD_1\n"
    "v_mfma_f32_16x16x32_bf16 v[168:171], a[104:107], v[152:155], v[168:171]\n"
    "v_lshlrev_b32_sdwa v198, 1, v180 dst_sel:DWORD dst_unused:UNUSED_PAD src0_sel:DWORD src1_sel:WORD_1\n"
    "v_lshlrev_b32_sdwa v199, 1, v182 dst_sel:DWORD dst_unused:UNUSED_PAD src0_sel:DWORD src1_sel:WORD_1\n"
    "v_mfma_f32_16x16x32_bf16 v[172:175], a[148:151], v[144:147], 0\n"
    "ds_read_u16 v194, v194\n"
    "ds_read_u16_d16_hi v202, v202\n"
    "v_mfma_f32_16x16x32_bf16 v[172:175], a[196:199], v[148:151], v[172:175]\n"
    "ds_read_b128 v[236:239], v244 offset:0\n"
    "ds_read_u16 v195, v195\n"
    "v_mfma_f32_16x16x32_bf16 v[172:175], a[108:111], v[152:155], v[172:175]\n"
    "v_or_b32_e64 v190, v190, v206\n"
    "v_lshlrev_b32_sdwa v206, 1, v181 dst_sel:DWORD dst_unused:UNUSED_PAD src0_sel:DWORD src1_sel:WORD_1\n"
    "v_mfma_f32_16x16x32_bf16 v[176:179], a[144:147], v[156:159], 0\n"
    "ds_read_u16_d16_hi v203, v203\n"
    "v_cvt_pk_bf16_f32 v128, v128, v129\n"
    "v_mfma_f32_16x16x32_bf16 v[176:179], a[192:195], v[160:163], v[176:179]\n"
    "ds_read_b128 a[244:247], v244 offset:2048\n"
    "v_cvt_pk_bf16_f32 v129, v130, v131\n"
    "v_mfma_f32_16x16x32_bf16 v[176:179], a[104:107], v[164:167], v[176:179]\n"
    "ds_read_u16 v197, v197\n"
    "v_or_b32_e32 v191, v191, v207\n"
    "v_mfma_f32_16x16x32_bf16 v[96:99], a[0:3], v[184:187], 0\n"
    "s_waitcnt lgkmcnt(7)\n"
    "v_lshlrev_b32_sdwa v207, 1, v183 dst_sel:DWORD dst_unused:UNUSED_PAD src0_sel:DWORD src1_sel:WORD_1\n"
    "v_cvt_pk_bf16_f32 v130, v132, v133\n"
    "v_mfma_f32_16x16x32_bf16 v[180:183], a[148:151], v[156:159], 0\n"
    "ds_read_u16_d16_hi v205, v205\n"
    "v_or_b32_e64 v193, v193, v201\n"
    "v_mfma_f32_16x16x32_bf16 v[180:183], a[196:199], v[160:163], v[180:183]\n"
    "v_cvt_pk_bf16_f32 v131, v134, v135\n"
    "v_pk_fma_f16 v232, v192, v192, s48\n"
    "v_mfma_f32_16x16x32_bf16 v[180:183], a[108:111], v[164:167], v[180:183]\n"
    "ds_read_u16 v198, v198\n"
    "v_cvt_pk_bf16_f32 v136, v136, v137\n"
    "v_mfma_f32_16x16x32_bf16 v[100:103], a[4:7], v[184:187], 0\n"
    "v_cvt_pk_bf16_f32 v137, v138, v139\n"
    "v_cvt_pk_bf16_f32 v138, v140, v141\n"
    "v_mfma_f32_16x16x32_bf16 v[112:115], a[8:11], v[184:187], 0\n"
    "ds_read_u16_d16_hi v206, v206\n"
    "v_cvt_pk_bf16_f32 v139, v142, v143\n"
    "v_mfma_f32_16x16x32_bf16 v[116:119], a[12:15], v[184:187], 0\n"
    "s_waitcnt lgkmcnt(7)\n"
    "ds_read_u16 v199, v199\n"
    "v_or_b32_e32 v194, v194, v202\n"
    "v_mfma_f32_16x16x32_bf16 v[104:107], a[0:3], v[188:191], 0\n"
    "v_pk_mul_f16 v232, v192, v232\n"
    "v_lshlrev_b32_sdwa v201, 1, v171 dst_sel:DWORD dst_unused:UNUSED_PAD src0_sel:DWORD src1_sel:WORD_1\n"
    "v_mfma_f32_16x16x32_bf16 v[108:111], a[4:7], v[188:191], 0\n"
    "ds_read_u16_d16_hi v207, v207\n"
    "v_exp_f16_e32 v234, v232\n"
    "v_mfma_f32_16x16x32_bf16 v[120:123], a[8:11], v[188:191], 0\n"
    "v_exp_f16_sdwa v234, v232 dst_sel:WORD_1 dst_unused:UNUSED_PRESERVE src0_sel:WORD_1\n"
    "v_mfma_f32_16x16x32_bf16 v[124:127], a[12:15], v[188:191], 0\n"
    "s_waitcnt lgkmcnt(6)\n"
    "ds_read_u16_d16_hi v201, v201\n"
    "v_or_b32_e64 v195, v195, v203\n"
    "v_mfma_f32_16x16x32_bf16 v[132:135], a[244:247], v[184:187], 0\n"
    "ds_write_b128 v243, v[128:131] offset:28736\n"
    "v_mfma_f32_16x16x32_bf16 v[128:131], v[236:239], v[184:187], 0\n"
    "v_pk_fma_f16 v234, v234, s49, s49\n"
    "v_cvt_pk_f16_f32 v184, v168, v169\n"
    "v_mfma_f32_16x16x32_bf16 v[140:143], a[244:247], v[188:191], 0\n"
    "v_lshlrev_b32_sdwa v185, 1, v170 dst_sel:DWORD dst_unused:UNUSED_PAD src0_sel:DWORD src1_sel:WORD_1\n"
    "v_lshlrev_b32_sdwa v186, 1, v172 dst_sel:DWORD dst_unused:UNUSED_PAD src0_sel:DWORD src1_sel:WORD_1\n"
    "v_mfma_f32_16x16x32_bf16 v[168:171], a[152:155], v[144:147], 0\n"
    "s_waitcnt lgkmcnt(6)\n"
    "ds_read_u16 v185, v185\n"
    "ds_write_b128 v243, v[136:139] offset:29760\n"
    "v_mfma_f32_16x16x32_bf16 v[136:139], v[236:239], v[188:191], 0\n"
    "v_or_b32_e32 v197, v197, v205\n"
    "v_lshlrev_b32_sdwa v202, 1, v173 dst_sel:DWORD dst_unused:UNUSED_PAD src0_sel:DWORD src1_sel:WORD_1\n"
    "v_mfma_f32_16x16x32_bf16 v[168:171], a[200:203], v[148:151], v[168:171]\n"
    "v_lshlrev_b32_sdwa v187, 1, v174 dst_sel:DWORD dst_unused:UNUSED_PAD src0_sel:DWORD src1_sel:WORD_1\n"
    "v_lshlrev_b32_sdwa v203, 1, v175 dst_sel:DWORD dst_unused:UNUSED_PAD src0_sel:DWORD src1_sel:WORD_1\n"
    "v_rcp_f16_e32 v232, v234\n"
    "v_mfma_f32_16x16x32_bf16 v[168:171], a[112:115], v[152:155], v[168:171]\n"
    "s_waitcnt lgkmcnt(6)\n"
    "ds_read_b128 a[252:255], v249 offset:0\n"
    "ds_read_u16 v186, v186\n"
    "v_or_b32_e64 v198, v198, v206\n"
    "v_rcp_f16_sdwa v232, v234 dst_sel:WORD_1 dst_unused:UNUSED_PRESERVE src0_sel:WORD_1\n"
    "v_mfma_f32_16x16x32_bf16 v[172:175], a[156:159], v[144:147], 0\n"
    "ds_read_u16_d16_hi v202, v202\n"
    "ds_read_b128 v[236:239], v244 offset:4096\n"
    "v_cvt_pk_f16_f32 v188, v176, v177\n"
    "v_lshlrev_b32_sdwa v189, 1, v178 dst_sel:DWORD dst_unused:UNUSED_PAD src0_sel:DWORD src1_sel:WORD_1\n"
    "v_lshlrev_b32_sdwa v205, 1, v179 dst_sel:DWORD dst_unused:UNUSED_PAD src0_sel:DWORD src1_sel:WORD_1\n"
    "v_lshlrev_b32_sdwa v190, 1, v180 dst_sel:DWORD dst_unused:UNUSED_PAD src0_sel:DWORD src1_sel:WORD_1\n"
    "v_lshlrev_b32_sdwa v206, 1, v181 dst_sel:DWORD dst_unused:UNUSED_PAD src0_sel:DWORD src1_sel:WORD_1\n"
    "v_lshlrev_b32_sdwa v191, 1, v182 dst_sel:DWORD dst_unused:UNUSED_PAD src0_sel:DWORD src1_sel:WORD_1\n"
    "v_fma_mix_f32 v234, v192, v232, 0 op_sel_hi:[1,1,0]\n"
    "v_mfma_f32_16x16x32_bf16 v[172:175], a[204:207], v[148:151], v[172:175]\n"
    "s_waitcnt lgkmcnt(6)\n"
    "ds_read_u16 v187, v187\n"
    "ds_read_u16_d16_hi v203, v203\n"
    "v_mfma_f32_16x16x32_bf16 v[172:175], a[116:119], v[152:155], v[172:175]\n"
    "v_or_b32_e32 v199, v199, v207\n"
    "v_lshlrev_b32_sdwa v207, 1, v183 dst_sel:DWORD dst_unused:UNUSED_PAD src0_sel:DWORD src1_sel:WORD_1\n"
    "v_fma_mix_f32 v232, v192, v232, 0 op_sel:[1,1,0] op_sel_hi:[1,1,0]\n"
    "v_mfma_f32_16x16x32_bf16 v[176:179], a[152:155], v[156:159], 0\n"
    "s_nop 0\n"
    "ds_read_b128 a[244:247], v244 offset:6144\n"
    "v_cvt_pk_bf16_f32 v192, v234, v232\n"
    "v_mfma_f32_16x16x32_bf16 v[176:179], a[200:203], v[160:163], v[176:179]\n"
    "s_waitcnt lgkmcnt(7)\n"
    "ds_read_u16 v189, v189\n"
    "v_pk_fma_f16 v232, v196, v196, s48\n"
    "v_mfma_f32_16x16x32_bf16 v[176:179], a[112:115], v[164:167], v[176:179]\n"
    "v_pk_mul_f16 v232, v196, v232\n"
    "v_or_b32_e32 v185, v185, v201\n"
    "v_mfma_f32_16x16x32_bf16 v[180:183], a[156:159], v[156:159], 0\n"
    "s_waitcnt lgkmcnt(7)\n"
    "ds_read_u16_d16_hi v205, v205\n"
    "buffer_store_dwordx4 a[252:255], v255, s[44:47], s57 offen offset:0 sc1\n"
    "v_mfma_f32_16x16x32_bf16 v[180:183], a[204:207], v[160:163], v[180:183]\n"
    "ds_read_u16 v190, v190\n"
    "v_exp_f16_e64 v234, v232\n"
    "v_mfma_f32_16x16x32_bf16 v[180:183], a[116:119], v[164:167], v[180:183]\n"
    "ds_read_b128 a[252:255], v249 offset:2048\n"
    "v_exp_f16_sdwa v234, v232 dst_sel:WORD_1 dst_unused:UNUSED_PRESERVE src0_sel:WORD_1\n"
    "v_lshlrev_b32_sdwa v200, 1, v169 dst_sel:DWORD dst_unused:UNUSED_PAD src0_sel:DWORD src1_sel:WORD_1\n"
    "v_pk_fma_f16 v234, v234, s49, s49\n"
    "v_mfma_f32_16x16x32_bf16 v[96:99], a[16:19], v[192:195], v[96:99]\n"
    "s_waitcnt lgkmcnt(7)\n"
    "ds_read_u16_d16_hi v206, v206\n"
    "v_rcp_f16_e32 v232, v234\n"
    "v_mfma_f32_16x16x32_bf16 v[100:103], a[20:23], v[192:195], v[100:103]\n"
    "ds_read_u16 v191, v191\n"
    "v_rcp_f16_sdwa v232, v234 dst_sel:WORD_1 dst_unused:UNUSED_PRESERVE src0_sel:WORD_1\n"
    "v_mfma_f32_16x16x32_bf16 v[112:115], a[24:27], v[192:195], v[112:115]\n"
    "ds_read_u16_d16_hi v207, v207\n"
    "v_fma_mix_f32 v234, v196, v232, 0 op_sel_hi:[1,1,0]\n"
    "v_mfma_f32_16x16x32_bf16 v[116:119], a[28:31], v[192:195], v[116:119]\n"
    "v_fma_mix_f32 v232, v196, v232, 0 op_sel:[1,1,0] op_sel_hi:[1,1,0]\n"
    "v_or_b32_e32 v186, v186, v202\n"
    "v_mfma_f32_16x16x32_bf16 v[128:131], v[236:239], v[192:195], v[128:131]\n"
    "s_waitcnt lgkmcnt(7)\n"
    "ds_read_b128 a[240:243], v244 offset:34816\n"
    "v_cvt_pk_bf16_f32 v196, v234, v232\n"
    "v_mfma_f32_16x16x32_bf16 v[132:135], a[244:247], v[192:195], v[132:135]\n"
    "v_pk_fma_f16 v232, v184, v184, s48\n"
    "v_or_b32_e64 v187, v187, v203\n"
    "v_mfma_f32_16x16x32_bf16 v[104:107], a[16:19], v[196:199], v[104:107]\n"
    "ds_read_u16_d16_hi v200, v200\n"
    "v_pk_mul_f16 v232, v184, v232\n"
    "v_mfma_f32_16x16x32_bf16 v[108:111], a[20:23], v[196:199], v[108:111]\n"
    "v_exp_f16_e64 v234, v232\n"
    "v_mfma_f32_16x16x32_bf16 v[120:123], a[24:27], v[196:199], v[120:123]\n"
    "s_waitcnt lgkmcnt(7)\n"
    "v_or_b32_e32 v189, v189, v205\n"
    "v_exp_f16_sdwa v234, v232 dst_sel:WORD_1 dst_unused:UNUSED_PRESERVE src0_sel:WORD_1\n"
    "v_mfma_f32_16x16x32_bf16 v[124:127], a[28:31], v[196:199], v[124:127]\n"
    "v_pk_fma_f16 v234, v234, s49, s49\n"
    "v_lshlrev_b32_sdwa v192, 1, v168 dst_sel:DWORD dst_unused:UNUSED_PAD src0_sel:DWORD src1_sel:WORD_1\n"
    "v_mfma_f32_16x16x32_bf16 v[136:139], v[236:239], v[196:199], v[136:139]\n"
    "s_waitcnt lgkmcnt(5)\n"
    "ds_read_u16 v192, v192\n"
    "buffer_store_dwordx4 a[252:255], v255, s[44:47], s57 offen offset:64 sc1\n"
    "v_mfma_f32_16x16x32_bf16 v[140:143], a[244:247], v[196:199], v[140:143]\n"
    "s_nop 0\n"
    "ds_read_b128 v[236:239], v244 offset:8192\n"
    "ds_read_b128 a[252:255], v249 offset:4096\n"
    "v_lshlrev_b32_sdwa v193, 1, v170 dst_sel:DWORD dst_unused:UNUSED_PAD src0_sel:DWORD src1_sel:WORD_1\n"
    "v_lshlrev_b32_sdwa v201, 1, v171 dst_sel:DWORD dst_unused:UNUSED_PAD src0_sel:DWORD src1_sel:WORD_1\n"
    "v_mfma_f32_16x16x32_bf16 v[168:171], a[160:163], v[144:147], 0\n"
    "s_waitcnt lgkmcnt(7)\n"
    "ds_read_u16 v193, v193\n"
    "ds_read_u16_d16_hi v201, v201\n"
    "v_mfma_f32_16x16x32_bf16 v[168:171], a[208:211], v[148:151], v[168:171]\n"
    "s_nop 0\n"
    "v_lshlrev_b32_sdwa v194, 1, v172 dst_sel:DWORD dst_unused:UNUSED_PAD src0_sel:DWORD src1_sel:WORD_1\n"
    "v_lshlrev_b32_sdwa v202, 1, v173 dst_sel:DWORD dst_unused:UNUSED_PAD src0_sel:DWORD src1_sel:WORD_1\n"
    "v_lshlrev_b32_sdwa v195, 1, v174 dst_sel:DWORD dst_unused:UNUSED_PAD src0_sel:DWORD src1_sel:WORD_1\n"
    "v_lshlrev_b32_sdwa v203, 1, v175 dst_sel:DWORD dst_unused:UNUSED_PAD src0_sel:DWORD src1_sel:WORD_1\n"
    "v_rcp_f16_e32 v232, v234\n"
    "v_mfma_f32_16x16x32_bf16 v[168:171], a[120:123], v[152:155], v[168:171]\n"
    "s_waitcnt lgkmcnt(6)\n"
    "ds_read_u16 v194, v194\n"
    "ds_read_u16_d16_hi v202, v202\n"
    "v_or_b32_e64 v190, v190, v206\n"
    "v_rcp_f16_sdwa v232, v234 dst_sel:WORD_1 dst_unused:UNUSED_PRESERVE src0_sel:WORD_1\n"
    "v_mfma_f32_16x16x32_bf16 v[172:175], a[164:167], v[144:147], 0\n"
    "ds_read_u16 v195, v195\n"
    "v_lshlrev_b32_sdwa v196, 1, v176 dst_sel:DWORD dst_unused:UNUSED_PAD src0_sel:DWORD src1_sel:WORD_1\n"
    "v_lshlrev_b32_sdwa v204, 1, v177 dst_sel:DWORD dst_unused:UNUSED_PAD src0_sel:DWORD src1_sel:WORD_1\n"
    "v_lshlrev_b32_sdwa v197, 1, v178 dst_sel:DWORD dst_unused:UNUSED_PAD src0_sel:DWORD src1_sel:WORD_1\n"
    "v_lshlrev_b32_sdwa v205, 1, v179 dst_sel:DWORD dst_unused:UNUSED_PAD src0_sel:DWORD src1_sel:WORD_1\n"
    "v_lshlrev_b32_sdwa v198, 1, v180 dst_sel:DWORD dst_unused:UNUSED_PAD src0_sel:DWORD src1_sel:WORD_1\n"
    "v_lshlrev_b32_sdwa v206, 1, v181 dst_sel:DWORD dst_unused:UNUSED_PAD src0_sel:DWORD src1_sel:WORD_1\n"
    "v_lshlrev_b32_sdwa v199, 1, v182 dst_sel:DWORD dst_unused:UNUSED_PAD src0_sel:DWORD src1_sel:WORD_1\n"
    "v_fma_mix_f32 v234, v184, v232, 0 op_sel_hi:[1,1,0]\n"
    "v_mfma_f32_16x16x32_bf16 v[172:175], a[212:215], v[148:151], v[172:175]\n"
    "s_waitcnt lgkmcnt(5)\n"
    "ds_read_b128 a[244:247], v244 offset:10240\n"
    "buffer_store_dwordx4 a[252:255], v255, s[44:47], s57 offen offset:128 sc1\n"
    "v_mfma_f32_16x16x32_bf16 v[172:175], a[240:243], v[152:155], v[172:175]\n"
    "s_nop 0\n"
    "ds_read_u16_d16_hi v203, v203\n"
    "ds_read_b128 a[252:255], v249 offset:1024\n"
    "v_or_b32_e64 v191, v191, v207\n"
    "v_lshlrev_b32_sdwa v207, 1, v183 dst_sel:DWORD dst_unused:UNUSED_PAD src0_sel:DWORD src1_sel:WORD_1\n"
    "v_fma_mix_f32 v232, v184, v232, 0 op_sel:[1,1,0] op_sel_hi:[1,1,0]\n"
    "v_mfma_f32_16x16x32_bf16 v[176:179], a[160:163], v[156:159], 0\n"
    "ds_read_u16 v196, v196\n"
    "ds_read_u16_d16_hi v204, v204\n"
    "v_cvt_pk_bf16_f32 v184, v234, v232\n"
    "v_mfma_f32_16x16x32_bf16 v[176:179], a[208:211], v[160:163], v[176:179]\n"
    "ds_read_u16 v197, v197\n"
    "v_pk_fma_f16 v232, v188, v188, s48\n"
    "v_mfma_f32_16x16x32_bf16 v[176:179], a[120:123], v[164:167], v[176:179]\n"
    "s_waitcnt lgkmcnt(9)\n"
    "v_pk_mul_f16 v232, v188, v232\n"
    "v_or_b32_e32 v192, v192, v200\n"
    "v_mfma_f32_16x16x32_bf16 v[180:183], a[164:167], v[156:159], 0\n"
    "ds_read_u16_d16_hi v205, v205\n"
    "v_exp_f16_e32 v234, v232\n"
    "v_mfma_f32_16x16x32_bf16 v[180:183], a[212:215], v[160:163], v[180:183]\n"
    "s_waitcnt lgkmcnt(8)\n"
    "v_exp_f16_sdwa v234, v232 dst_sel:WORD_1 dst_unused:UNUSED_PRESERVE src0_sel:WORD_1\n"
    "v_mfma_f32_16x16x32_bf16 v[180:183], a[240:243], v[164:167], v[180:183]\n"
    "ds_read_u16 v198, v198\n"
    "v_pk_fma_f16 v234, v234, s49, s49\n"
    "v_mfma_f32_16x16x32_bf16 v[96:99], a[32:35], v[184:187], v[96:99]\n"
    "ds_read_u16_d16_hi v206, v206\n"
    "v_rcp_f16_e32 v232, v234\n"
    "v_mfma_f32_16x16x32_bf16 v[100:103], a[36:39], v[184:187], v[100:103]\n"
    "v_rcp_f16_sdwa v232, v234 dst_sel:WORD_1 dst_unused:UNUSED_PRESERVE src0_sel:WORD_1\n"
    "v_mfma_f32_16x16x32_bf16 v[112:115], a[40:43], v[184:187], v[112:115]\n"
    "s_waitcnt lgkmcnt(6)\n"
    "ds_read_u16 v199, v199\n"
    "buffer_store_dwordx4 a[252:255], v255, s[44:47], s100 offen offset:0 sc1\n"
    "v_mfma_f32_16x16x32_bf16 v[116:119], a[44:47], v[184:187], v[116:119]\n"
    "ds_read_u16_d16_hi v207, v207\n"
    "v_fma_mix_f32 v234, v188, v232, 0 op_sel_hi:[1,1,0]\n"
    "v_mfma_f32_16x16x32_bf16 v[128:131], v[236:239], v[184:187], v[128:131]\n"
    "ds_read_b128 a[252:255], v249 offset:3072\n"
    "v_fma_mix_f32 v232, v188, v232, 0 op_sel:[1,1,0] op_sel_hi:[1,1,0]\n"
    "v_mfma_f32_16x16x32_bf16 v[132:135], a[244:247], v[184:187], v[132:135]\n"
    "ds_read_b128 a[240:243], v244 offset:38912\n"
    "v_cvt_pk_bf16_f32 v188, v234, v232\n"
    "v_or_b32_e32 v193, v193, v201\n"
    "v_cvt_pk_f16_f32 v184, v168, v169\n"
    "v_mfma_f32_16x16x32_bf16 v[104:107], a[32:35], v[188:191], v[104:107]\n"
    "s_waitcnt lgkmcnt(8)\n"
    "v_lshlrev_b32_sdwa v185, 1, v170 dst_sel:DWORD dst_unused:UNUSED_PAD src0_sel:DWORD src1_sel:WORD_1\n"
    "v_lshlrev_b32_sdwa v201, 1, v171 dst_sel:DWORD dst_unused:UNUSED_PAD src0_sel:DWORD src1_sel:WORD_1\n"
    "v_mfma_f32_16x16x32_bf16 v[108:111], a[36:39], v[188:191], v[108:111]\n"
    "ds_read_u16 v185, v185\n"
    "ds_read_u16_d16_hi v201, v201\n"
    "v_mfma_f32_16x16x32_bf16 v[120:123], a[40:43], v[188:191], v[120:123]\n"
    "v_lshlrev_b32_sdwa v186, 1, v172 dst_sel:DWORD dst_unused:UNUSED_PAD src0_sel:DWORD src1_sel:WORD_1\n"
    "v_or_b32_e32 v194, v194, v202\n"
    "v_mfma_f32_16x16x32_bf16 v[124:127], a[44:47], v[188:191], v[124:127]\n"
    "s_waitcnt lgkmcnt(8)\n"
    "ds_read_u16 v186, v186\n"
    "v_lshlrev_b32_sdwa v202, 1, v173 dst_sel:DWORD dst_unused:UNUSED_PAD src0_sel:DWORD src1_sel:WORD_1\n"
    "v_mfma_f32_16x16x32_bf16 v[136:139], v[236:239], v[188:191], v[136:139]\n"
    "v_lshlrev_b32_sdwa v187, 1, v174 dst_sel:DWORD dst_unused:UNUSED_PAD src0_sel:DWORD src1_sel:WORD_1\n"
    "v_or_b32_e64 v195, v195, v203\n"
    "v_mfma_f32_16x16x32_bf16 v[140:143], a[244:247], v[188:191], v[140:143]\n"
    "ds_read_u16_d16_hi v202, v202\n"
    "v_lshlrev_b32_sdwa v203, 1, v175 dst_sel:DWORD dst_unused:UNUSED_PAD src0_sel:DWORD src1_sel:WORD_1\n"
    "v_mfma_f32_16x16x32_bf16 v[168:171], a[168:171], v[144:147], 0\n"
    "s_nop 0\n"
    "v_cvt_pk_f16_f32 v188, v176, v177\n"
    "v_lshlrev_b32_sdwa v189, 1, v178 dst_sel:DWORD dst_unused:UNUSED_PAD src0_sel:DWORD src1_sel:WORD_1\n"
    "v_mfma_f32_16x16x32_bf16 v[168:171], a[216:219], v[148:151], v[168:171]\n"
    "s_waitcnt lgkmcnt(8)\n"
    "ds_read_b128 v[236:239], v244 offset:12288\n"
    "v_lshlrev_b32_sdwa v190, 1, v180 dst_sel:DWORD dst_unused:UNUSED_PAD src0_sel:DWORD src1_sel:WORD_1\n"
    "v_mfma_f32_16x16x32_bf16 v[168:171], a[124:127], v[152:155], v[168:171]\n"
    "ds_read_u16 v187, v187\n"
    "v_lshlrev_b32_sdwa v191, 1, v182 dst_sel:DWORD dst_unused:UNUSED_PAD src0_sel:DWORD src1_sel:WORD_1\n"
    "v_mfma_f32_16x16x32_bf16 v[172:175], a[172:175], v[144:147], 0\n"
    "v_or_b32_e64 v196, v196, v204\n"
    "v_or_b32_e32 v197, v197, v205\n"
    "v_mfma_f32_16x16x32_bf16 v[172:175], a[220:223], v[148:151], v[172:175]\n"
    "s_waitcnt lgkmcnt(7)\n"
    "ds_read_u16_d16_hi v203, v203\n"
    "buffer_store_dwordx4 a[252:255], v255, s[44:47], s100 offen offset:64 sc1\n"
    "s_waitcnt lgkmcnt(7)\n"
    "v_mfma_f32_16x16x32_bf16 v[172:175], a[240:243], v[152:155], v[172:175]\n"
    "s_nop 0\n"
    "ds_read_b128 a[252:255], v249 offset:5120\n"
    "ds_read_b128 a[244:247], v244 offset:14336\n"
    "v_lshlrev_b32_sdwa v205, 1, v179 dst_sel:DWORD dst_unused:UNUSED_PAD src0_sel:DWORD src1_sel:WORD_1\n"
    "v_mfma_f32_16x16x32_bf16 v[176:179], a[168:171], v[156:159], 0\n"
    "ds_read_u16 v189, v189\n"
    "v_or_b32_e64 v198, v198, v206\n"
    "v_mfma_f32_16x16x32_bf16 v[176:179], a[216:219], v[160:163], v[176:179]\n"
    "v_lshlrev_b32_sdwa v206, 1, v181 dst_sel:DWORD dst_unused:UNUSED_PAD src0_sel:DWORD src1_sel:WORD_1\n"
    "v_or_b32_e32 v199, v199, v207\n"
    "v_mfma_f32_16x16x32_bf16 v[176:179], a[124:127], v[164:167], v[176:179]\n"
    "s_waitcnt lgkmcnt(8)\n"
    "ds_read_u16_d16_hi v205, v205\n"
    "v_lshlrev_b32_sdwa v207, 1, v183 dst_sel:DWORD dst_unused:UNUSED_PAD src0_sel:DWORD src1_sel:WORD_1\n"
    "v_mfma_f32_16x16x32_bf16 v[180:183], a[172:175], v[156:159], 0\n"
    "ds_read_u16 v190, v190\n"
    "v_or_b32_e64 v185, v185, v201\n"
    "v_mfma_f32_16x16x32_bf16 v[180:183], a[220:223], v[160:163], v[180:183]\n"
    "v_pk_fma_f16 v232, v184, v184, s48\n"
    "v_lshlrev_b32_sdwa v201, 1, v171 dst_sel:DWORD dst_unused:UNUSED_PAD src0_sel:DWORD src1_sel:WORD_1\n"
    "v_mfma_f32_16x16x32_bf16 v[180:183], a[240:243], v[164:167], v[180:183]\n"
    "s_waitcnt lgkmcnt(7)\n"
    "ds_read_b128 v[144:147], v243 offset:0\n"
    "v_or_b32_e32 v186, v186, v202\n"
    "v_mfma_f32_16x16x32_bf16 v[96:99], a[48:51], v[192:195], v[96:99]\n"
    "ds_read_u16_d16_hi v206, v206\n"
    "buffer_load_dwordx4 a[232:235], v245, s[40:43], s58 offen offset:64\n"
    "buffer_load_dwordx4 a[224:227], v245, s[40:43], s58 offen offset:0\n"
    "v_mfma_f32_16x16x32_bf16 v[100:103], a[52:55], v[192:195], v[100:103]\n"
    "v_pk_mul_f16 v232, v184, v232\n"
    "v_lshlrev_b32_sdwa v202, 1, v173 dst_sel:DWORD dst_unused:UNUSED_PAD src0_sel:DWORD src1_sel:WORD_1\n"
    "v_mfma_f32_16x16x32_bf16 v[112:115], a[56:59], v[192:195], v[112:115]\n"
    "ds_read_b128 v[148:151], v243 offset:1024\n"
    "ds_read_u16 v191, v191\n"
    "v_mfma_f32_16x16x32_bf16 v[116:119], a[60:63], v[192:195], v[116:119]\n"
    "v_exp_f16_e32 v234, v232\n"
    "v_mfma_f32_16x16x32_bf16 v[128:131], v[236:239], v[192:195], v[128:131]\n"
    "s_waitcnt lgkmcnt(8)\n"
    "ds_read_b128 v[152:155], v243 offset:2048\n"
    "v_or_b32_e32 v187, v187, v203\n"
    "s_waitcnt lgkmcnt(8)\n"
    "v_mfma_f32_16x16x32_bf16 v[132:135], a[244:247], v[192:195], v[132:135]\n"
    "ds_read_u16_d16_hi v207, v207\n"
    "v_exp_f16_sdwa v234, v232 dst_sel:WORD_1 dst_unused:UNUSED_PRESERVE src0_sel:WORD_1\n"
    "v_mfma_f32_16x16x32_bf16 v[104:107], a[48:51], v[196:199], v[104:107]\n"
    "v_pk_fma_f16 v234, v234, s49, s49\n"
    "v_cvt_pk_f16_f32 v192, v168, v169\n"
    "v_mfma_f32_16x16x32_bf16 v[108:111], a[52:55], v[196:199], v[108:111]\n"
    "ds_read_b128 v[156:159], v243 offset:3072\n"
    "ds_read_b128 v[160:163], v243 offset:4096\n"
    "v_mfma_f32_16x16x32_bf16 v[120:123], a[56:59], v[196:199], v[120:123]\n"
    "v_lshlrev_b32_sdwa v193, 1, v170 dst_sel:DWORD dst_unused:UNUSED_PAD src0_sel:DWORD src1_sel:WORD_1\n"
    "v_lshlrev_b32_sdwa v194, 1, v172 dst_sel:DWORD dst_unused:UNUSED_PAD src0_sel:DWORD src1_sel:WORD_1\n"
    "v_mfma_f32_16x16x32_bf16 v[124:127], a[60:63], v[196:199], v[124:127]\n"
    "s_waitcnt lgkmcnt(9)\n"
    "ds_read_b128 v[164:167], v243 offset:5120\n"
    "buffer_store_dwordx4 a[252:255], v255, s[44:47], s100 offen offset:128 sc1\n"
    "v_mfma_f32_16x16x32_bf16 v[136:139], v[236:239], v[196:199], v[136:139]\n"
    "s_waitcnt lgkmcnt(8)\n"
    "ds_read_b128 a[240:243], v244 offset:32768\n"
    "ds_read_u16 v193, v193\n"
    "v_mfma_f32_16x16x32_bf16 v[140:143], a[244:247], v[196:199], v[140:143]\n"
    "ds_read_u16_d16_hi v201, v201\n"
    "v_or_b32_e32 v189, v189, v205\n"
    "v_mfma_f32_16x16x32_bf16 v[168:171], a[128:131], v[144:147], 0\n"
    "s_waitcnt lgkmcnt(9)\n"
    "v_lshlrev_b32_sdwa v195, 1, v174 dst_sel:DWORD dst_unused:UNUSED_PAD src0_sel:DWORD src1_sel:WORD_1\n"
    "v_lshlrev_b32_sdwa v203, 1, v175 dst_sel:DWORD dst_unused:UNUSED_PAD src0_sel:DWORD src1_sel:WORD_1\n"
    "v_mfma_f32_16x16x32_bf16 v[168:171], a[176:179], v[148:151], v[168:171]\n"
    "ds_read_u16 v194, v194\n"
    "v_rcp_f16_e64 v232, v234\n"
    "v_or_b32_e32 v190, v190, v206\n"
    "v_rcp_f16_sdwa v232, v234 dst_sel:WORD_1 dst_unused:UNUSED_PRESERVE src0_sel:WORD_1\n"
    "s_waitcnt lgkmcnt(8)\n"
    "v_mfma_f32_16x16x32_bf16 v[168:171], a[96:99], v[152:155], v[168:171]\n"
    "ds_read_u16_d16_hi v202, v202\n"
    "v_cvt_pk_f16_f32 v196, v176, v177\n"
    "v_mfma_f32_16x16x32_bf16 v[172:175], a[132:135], v[144:147], 0\n"
    "ds_read_b128 v[236:239], v244 offset:16384\n"
    "v_lshlrev_b32_sdwa v197, 1, v178 dst_sel:DWORD dst_unused:UNUSED_PAD src0_sel:DWORD src1_sel:WORD_1\n"
    "v_mfma_f32_16x16x32_bf16 v[172:175], a[180:183], v[148:151], v[172:175]\n"
    "ds_read_u16 v195, v195\n"
    "v_lshlrev_b32_sdwa v205, 1, v179 dst_sel:DWORD dst_unused:UNUSED_PAD src0_sel:DWORD src1_sel:WORD_1\n"
    "v_lshlrev_b32_sdwa v198, 1, v180 dst_sel:DWORD dst_unused:UNUSED_PAD src0_sel:DWORD src1_sel:WORD_1\n"
    "v_lshlrev_b32_sdwa v206, 1, v181 dst_sel:DWORD dst_unused:UNUSED_PAD src0_sel:DWORD src1_sel:WORD_1\n"
    "v_lshlrev_b32_sdwa v199, 1, v182 dst_sel:DWORD dst_unused:UNUSED_PAD src0_sel:DWORD src1_sel:WORD_1\n"
    "v_fma_mix_f32 v234, v184, v232, 0 op_sel_hi:[1,1,0]\n"
    "s_waitcnt lgkmcnt(8)\n"
    "v_or_b32_e32 v191, v191, v207\n"
    "v_lshlrev_b32_sdwa v207, 1, v183 dst_sel:DWORD dst_unused:UNUSED_PAD src0_sel:DWORD src1_sel:WORD_1\n"
    "v_fma_mix_f32 v232, v184, v232, 0 op_sel:[1,1,0] op_sel_hi:[1,1,0]\n"
    "v_mfma_f32_16x16x32_bf16 v[176:179], a[128:131], v[156:159], 0\n"
    "s_waitcnt lgkmcnt(6)\n"
    "ds_read_u16_d16_hi v203, v203\n"
    "v_cvt_pk_bf16_f32 v184, v234, v232\n"
    "v_mfma_f32_16x16x32_bf16 v[172:175], a[240:243], v[152:155], v[172:175]\n"
    "s_barrier\n"
    "v_mfma_f32_16x16x32_bf16 v[176:179], a[176:179], v[160:163], v[176:179]\n"
    "ds_read_b128 a[244:247], v244 offset:18432\n"
    "ds_read_u16 v197, v197\n"
    "v_mfma_f32_16x16x32_bf16 v[176:179], a[96:99], v[164:167], v[176:179]\n"
    "ds_read_u16_d16_hi v205, v205\n"
    "buffer_load_dwordx4 a[236:239], v245, s[40:43], s101 offen offset:64\n"
    "buffer_load_dwordx4 a[228:231], v245, s[40:43], s101 offen offset:0\n"
    "v_pk_fma_f16 v232, v188, v188, s48\n"
    "v_mfma_f32_16x16x32_bf16 v[180:183], a[132:135], v[156:159], 0\n"
    "s_waitcnt lgkmcnt(8)\n"
    "ds_read_u16 v198, v198\n"
    "v_pk_mul_f16 v232, v188, v232\n"
    "v_mfma_f32_16x16x32_bf16 v[180:183], a[180:183], v[160:163], v[180:183]\n"
    "v_exp_f16_e64 v234, v232\n"
    "v_mfma_f32_16x16x32_bf16 v[180:183], a[240:243], v[164:167], v[180:183]\n"
    "ds_read_u16_d16_hi v206, v206\n"
    "v_exp_f16_sdwa v234, v232 dst_sel:WORD_1 dst_unused:UNUSED_PRESERVE src0_sel:WORD_1\n"
    "v_mfma_f32_16x16x32_bf16 v[96:99], a[64:67], v[184:187], v[96:99]\n"
    "s_waitcnt lgkmcnt(7)\n"
    "ds_read_u16 v199, v199\n"
    "v_pk_fma_f16 v234, v234, s49, s49\n"
    "v_mfma_f32_16x16x32_bf16 v[100:103], a[68:71], v[184:187], v[100:103]\n"
    "v_rcp_f16_e64 v232, v234\n"
    "v_mfma_f32_16x16x32_bf16 v[112:115], a[72:75], v[184:187], v[112:115]\n"
    "ds_read_u16_d16_hi v207, v207\n"
    "v_rcp_f16_sdwa v232, v234 dst_sel:WORD_1 dst_unused:UNUSED_PRESERVE src0_sel:WORD_1\n"
    "v_mfma_f32_16x16x32_bf16 v[116:119], a[76:79], v[184:187], v[116:119]\n"
    "v_or_b32_e32 v193, v193, v201\n"
    "v_fma_mix_f32 v234, v188, v232, 0 op_sel_hi:[1,1,0]\n"
    "v_mfma_f32_16x16x32_bf16 v[128:131], v[236:239], v[184:187], v[128:131]\n"
    "s_waitcnt lgkmcnt(6)\n"
    "ds_read_b128 a[240:243], v244 offset:36864\n"
    "v_fma_mix_f32 v232, v188, v232, 0 op_sel:[1,1,0] op_sel_hi:[1,1,0]\n"
    "v_mfma_f32_16x16x32_bf16 v[132:135], a[244:247], v[184:187], v[132:135]\n"
    "v_or_b32_e64 v194, v194, v202\n"
    "v_cvt_pk_bf16_f32 v188, v234, v232\n"
    "v_pk_fma_f16 v232, v192, v192, s48\n"
    "v_or_b32_e64 v195, v195, v203\n"
    "v_mfma_f32_16x16x32_bf16 v[104:107], a[64:67], v[188:191], v[104:107]\n"
    "v_pk_mul_f16 v232, v192, v232\n"
    "v_lshlrev_b32_sdwa v184, 1, v168 dst_sel:DWORD dst_unused:UNUSED_PAD src0_sel:DWORD src1_sel:WORD_1\n"
    "v_mfma_f32_16x16x32_bf16 v[108:111], a[68:71], v[188:191], v[108:111]\n"
    "ds_read_u16 v184, v184\n"
    "v_exp_f16_e32 v234, v232\n"
    "v_mfma_f32_16x16x32_bf16 v[120:123], a[72:75], v[188:191], v[120:123]\n"
    "s_waitcnt lgkmcnt(6)\n"
    "v_exp_f16_sdwa v234, v232 dst_sel:WORD_1 dst_unused:UNUSED_PRESERVE src0_sel:WORD_1\n"
    "v_mfma_f32_16x16x32_bf16 v[124:127], a[76:79], v[188:191], v[124:127]\n"
    "v_or_b32_e64 v197, v197, v205\n"
    "v_pk_fma_f16 v234, v234, s49, s49\n"
    "v_mfma_f32_16x16x32_bf16 v[136:139], v[236:239], v[188:191], v[136:139]\n"
    "s_nop 0\n"
    "ds_read_b128 v[236:239], v244 offset:20480\n"
    "v_lshlrev_b32_sdwa v200, 1, v169 dst_sel:DWORD dst_unused:UNUSED_PAD src0_sel:DWORD src1_sel:WORD_1\n"
    "v_mfma_f32_16x16x32_bf16 v[140:143], a[244:247], v[188:191], v[140:143]\n"
    "s_waitcnt lgkmcnt(5)\n"
    "ds_read_u16_d16_hi v200, v200\n"
    "v_lshlrev_b32_sdwa v185, 1, v170 dst_sel:DWORD dst_unused:UNUSED_PAD src0_sel:DWORD src1_sel:WORD_1\n"
    "v_lshlrev_b32_sdwa v201, 1, v171 dst_sel:DWORD dst_unused:UNUSED_PAD src0_sel:DWORD src1_sel:WORD_1\n"
    "v_mfma_f32_16x16x32_bf16 v[168:171], a[136:139], v[144:147], 0\n"
    "ds_read_u16 v185, v185\n"
    "ds_read_u16_d16_hi v201, v201\n"
    "v_mfma_f32_16x16x32_bf16 v[168:171], a[184:187], v[148:151], v[168:171]\n"
    "v_lshlrev_b32_sdwa v186, 1, v172 dst_sel:DWORD dst_unused:UNUSED_PAD src0_sel:DWORD src1_sel:WORD_1\n"
    "v_lshlrev_b32_sdwa v202, 1, v173 dst_sel:DWORD dst_unused:UNUSED_PAD src0_sel:DWORD src1_sel:WORD_1\n"
    "v_lshlrev_b32_sdwa v187, 1, v174 dst_sel:DWORD dst_unused:UNUSED_PAD src0_sel:DWORD src1_sel:WORD_1\n"
    "v_lshlrev_b32_sdwa v203, 1, v175 dst_sel:DWORD dst_unused:UNUSED_PAD src0_sel:DWORD src1_sel:WORD_1\n"
    "v_rcp_f16_e32 v232, v234\n"
    "v_mfma_f32_16x16x32_bf16 v[168:171], a[100:103], v[152:155], v[168:171]\n"
    "s_waitcnt lgkmcnt(5)\n"
    "ds_read_u16 v186, v186\n"
    "ds_read_u16_d16_hi v202, v202\n"
    "v_or_b32_e64 v198, v198, v206\n"
    "v_rcp_f16_sdwa v232, v234 dst_sel:WORD_1 dst_unused:UNUSED_PRESERVE src0_sel:WORD_1\n"
    "v_mfma_f32_16x16x32_bf16 v[172:175], a[140:143], v[144:147], 0\n"
    "ds_read_u16 v187, v187\n"
    "v_lshlrev_b32_sdwa v188, 1, v176 dst_sel:DWORD dst_unused:UNUSED_PAD src0_sel:DWORD src1_sel:WORD_1\n"
    "v_mfma_f32_16x16x32_bf16 v[172:175], a[188:191], v[148:151], v[172:175]\n"
    "ds_read_b128 a[244:247], v244 offset:22528\n"
    "v_lshlrev_b32_sdwa v204, 1, v177 dst_sel:DWORD dst_unused:UNUSED_PAD src0_sel:DWORD src1_sel:WORD_1\n"
    "v_lshlrev_b32_sdwa v189, 1, v178 dst_sel:DWORD dst_unused:UNUSED_PAD src0_sel:DWORD src1_sel:WORD_1\n"
    "v_lshlrev_b32_sdwa v205, 1, v179 dst_sel:DWORD dst_unused:UNUSED_PAD src0_sel:DWORD src1_sel:WORD_1\n"
    "v_lshlrev_b32_sdwa v190, 1, v180 dst_sel:DWORD dst_unused:UNUSED_PAD src0_sel:DWORD src1_sel:WORD_1\n"
    "v_lshlrev_b32_sdwa v206, 1, v181 dst_sel:DWORD dst_unused:UNUSED_PAD src0_sel:DWORD src1_sel:WORD_1\n"
    "v_lshlrev_b32_sdwa v191, 1, v182 dst_sel:DWORD dst_unused:UNUSED_PAD src0_sel:DWORD src1_sel:WORD_1\n"
    "v_fma_mix_f32 v234, v192, v232, 0 op_sel_hi:[1,1,0]\n"
    "v_mfma_f32_16x16x32_bf16 v[172:175], a[240:243], v[152:155], v[172:175]\n"
    "ds_read_u16_d16_hi v203, v203\n"
    "ds_read_u16 v188, v188\n"
    "v_or_b32_e32 v199, v199, v207\n"
    "v_lshlrev_b32_sdwa v207, 1, v183 dst_sel:DWORD dst_unused:UNUSED_PAD src0_sel:DWORD src1_sel:WORD_1\n"
    "v_fma_mix_f32 v232, v192, v232, 0 op_sel:[1,1,0] op_sel_hi:[1,1,0]\n"
    "v_mfma_f32_16x16x32_bf16 v[176:179], a[136:139], v[156:159], 0\n"
    "s_waitcnt lgkmcnt(8)\n"
    "ds_read_u16_d16_hi v204, v204\n"
    "v_cvt_pk_bf16_f32 v192, v234, v232\n"
    "v_mfma_f32_16x16x32_bf16 v[176:179], a[184:187], v[160:163], v[176:179]\n"
    "v_pk_fma_f16 v232, v196, v196, s48\n"
    "v_or_b32_e64 v184, v184, v200\n"
    "v_mfma_f32_16x16x32_bf16 v[176:179], a[100:103], v[164:167], v[176:179]\n"
    "ds_read_u16 v189, v189\n"
    "v_pk_mul_f16 v232, v196, v232\n"
    "v_mfma_f32_16x16x32_bf16 v[180:183], a[140:143], v[156:159], 0\n"
    "s_waitcnt lgkmcnt(8)\n"
    "v_exp_f16_e32 v234, v232\n"
    "v_mfma_f32_16x16x32_bf16 v[180:183], a[188:191], v[160:163], v[180:183]\n"
    "ds_read_u16_d16_hi v205, v205\n"
    "v_exp_f16_sdwa v234, v232 dst_sel:WORD_1 dst_unused:UNUSED_PRESERVE src0_sel:WORD_1\n"
    "v_mfma_f32_16x16x32_bf16 v[180:183], a[240:243], v[164:167], v[180:183]\n"
    "ds_read_u16 v190, v190\n"
    "v_pk_fma_f16 v234, v234, s49, s49\n"
    "v_mfma_f32_16x16x32_bf16 v[96:99], a[80:83], v[192:195], v[96:99]\n"
    "s_waitcnt lgkmcnt(8)\n"
    "v_rcp_f16_e32 v232, v234\n"
    "v_mfma_f32_16x16x32_bf16 v[100:103], a[84:87], v[192:195], v[100:103]\n"
    "ds_read_u16_d16_hi v206, v206\n"
    "v_rcp_f16_sdwa v232, v234 dst_sel:WORD_1 dst_unused:UNUSED_PRESERVE src0_sel:WORD_1\n"
    "v_mfma_f32_16x16x32_bf16 v[112:115], a[88:91], v[192:195], v[112:115]\n"
    "ds_read_u16 v191, v191\n"
    "v_fma_mix_f32 v234, v196, v232, 0 op_sel_hi:[1,1,0]\n"
    "v_mfma_f32_16x16x32_bf16 v[116:119], a[92:95], v[192:195], v[116:119]\n"
    "s_waitcnt lgkmcnt(8)\n"
    "v_fma_mix_f32 v232, v196, v232, 0 op_sel:[1,1,0] op_sel_hi:[1,1,0]\n"
    "v_or_b32_e64 v185, v185, v201\n"
    "v_mfma_f32_16x16x32_bf16 v[128:131], v[236:239], v[192:195], v[128:131]\n"
    "s_waitcnt lgkmcnt(7)\n"
    "ds_read_u16_d16_hi v207, v207\n"
    "v_cvt_pk_bf16_f32 v196, v234, v232\n"
    "v_mfma_f32_16x16x32_bf16 v[132:135], a[244:247], v[192:195], v[132:135]\n"
    "v_cvt_pk_f16_f32 v192, v168, v169\n"
    "v_lshlrev_b32_sdwa v193, 1, v170 dst_sel:DWORD dst_unused:UNUSED_PAD src0_sel:DWORD src1_sel:WORD_1\n"
    "v_mfma_f32_16x16x32_bf16 v[104:107], a[80:83], v[196:199], v[104:107]\n"
    "ds_read_u16 v193, v193\n"
    "v_lshlrev_b32_sdwa v201, 1, v171 dst_sel:DWORD dst_unused:UNUSED_PAD src0_sel:DWORD src1_sel:WORD_1\n"
    "v_mfma_f32_16x16x32_bf16 v[108:111], a[84:87], v[196:199], v[108:111]\n"
    "v_lshlrev_b32_sdwa v194, 1, v172 dst_sel:DWORD dst_unused:UNUSED_PAD src0_sel:DWORD src1_sel:WORD_1\n"
    "v_or_b32_e32 v186, v186, v202\n"
    "v_mfma_f32_16x16x32_bf16 v[120:123], a[88:91], v[196:199], v[120:123]\n"
    "s_waitcnt lgkmcnt(7)\n"
    "ds_read_u16_d16_hi v201, v201\n"
    "v_lshlrev_b32_sdwa v202, 1, v173 dst_sel:DWORD dst_unused:UNUSED_PAD src0_sel:DWORD src1_sel:WORD_1\n"
    "v_mfma_f32_16x16x32_bf16 v[124:127], a[92:95], v[196:199], v[124:127]\n"
    "ds_read_u16 v194, v194\n"
    "v_lshlrev_b32_sdwa v195, 1, v174 dst_sel:DWORD dst_unused:UNUSED_PAD src0_sel:DWORD src1_sel:WORD_1\n"
    "v_mfma_f32_16x16x32_bf16 v[136:139], v[236:239], v[196:199], v[136:139]\n"
    "v_or_b32_e32 v187, v187, v203\n"
    "v_lshlrev_b32_sdwa v203, 1, v175 dst_sel:DWORD dst_unused:UNUSED_PAD src0_sel:DWORD src1_sel:WORD_1\n"
    "v_mfma_f32_16x16x32_bf16 v[140:143], a[244:247], v[196:199], v[140:143]\n"
    "s_waitcnt lgkmcnt(7)\n"
    "ds_read_u16_d16_hi v202, v202\n"
    "v_cvt_pk_f16_f32 v196, v176, v177\n"
    "v_mfma_f32_16x16x32_bf16 v[168:171], a[144:147], v[144:147], 0\n"
    "v_lshlrev_b32_sdwa v197, 1, v178 dst_sel:DWORD dst_unused:UNUSED_PAD src0_sel:DWORD src1_sel:WORD_1\n"
    "v_lshlrev_b32_sdwa v198, 1, v180 dst_sel:DWORD dst_unused:UNUSED_PAD src0_sel:DWORD src1_sel:WORD_1\n"
    "v_mfma_f32_16x16x32_bf16 v[168:171], a[192:195], v[148:151], v[168:171]\n"
    "ds_read_b128 v[236:239], v244 offset:0\n"
    "v_lshlrev_b32_sdwa v199, 1, v182 dst_sel:DWORD dst_unused:UNUSED_PAD src0_sel:DWORD src1_sel:WORD_1\n"
    "v_mfma_f32_16x16x32_bf16 v[168:171], a[104:107], v[152:155], v[168:171]\n"
    "ds_read_u16 v195, v195\n"
    "v_or_b32_e64 v188, v188, v204\n"
    "v_mfma_f32_16x16x32_bf16 v[172:175], a[148:151], v[144:147], 0\n"
    "s_waitcnt lgkmcnt(8)\n"
    "v_or_b32_e32 v189, v189, v205\n"
    "v_lshlrev_b32_sdwa v205, 1, v179 dst_sel:DWORD dst_unused:UNUSED_PAD src0_sel:DWORD src1_sel:WORD_1\n"
    "v_mfma_f32_16x16x32_bf16 v[172:175], a[196:199], v[148:151], v[172:175]\n"
    "ds_read_u16_d16_hi v203, v203\n"
    "v_cvt_pk_bf16_f32 v96, v96, v97\n"
    "v_mfma_f32_16x16x32_bf16 v[172:175], a[108:111], v[152:155], v[172:175]\n"
    "s_nop 0\n"
    "ds_read_b128 a[244:247], v244 offset:2048\n"
    "v_cvt_pk_bf16_f32 v97, v98, v99\n"
    "v_mfma_f32_16x16x32_bf16 v[176:179], a[144:147], v[156:159], 0\n"
    "s_waitcnt lgkmcnt(8)\n"
    "ds_read_u16 v197, v197\n"
    "v_cvt_pk_bf16_f32 v98, v100, v101\n"
    "v_mfma_f32_16x16x32_bf16 v[176:179], a[192:195], v[160:163], v[176:179]\n"
    "v_cvt_pk_bf16_f32 v99, v102, v103\n"
    "v_or_b32_e64 v190, v190, v206\n"
    "v_mfma_f32_16x16x32_bf16 v[176:179], a[104:107], v[164:167], v[176:179]\n"
    "ds_read_u16_d16_hi v205, v205\n"
    "v_lshlrev_b32_sdwa v206, 1, v181 dst_sel:DWORD dst_unused:UNUSED_PAD src0_sel:DWORD src1_sel:WORD_1\n"
    "v_mfma_f32_16x16x32_bf16 v[100:103], a[4:7], v[184:187], 0\n"
    "s_nop 0\n"
    "v_cvt_pk_bf16_f32 v112, v112, v113\n"
    "v_cvt_pk_bf16_f32 v113, v114, v115\n"
    "ds_write_b128 v242, v[96:99] offset:24640\n"
    "v_mfma_f32_16x16x32_bf16 v[96:99], a[0:3], v[184:187], 0\n"
    "s_waitcnt lgkmcnt(8)\n"
    "v_cvt_pk_bf16_f32 v114, v116, v117\n"
    "v_cvt_pk_bf16_f32 v115, v118, v119\n"
    "v_cvt_pk_bf16_f32 v128, v128, v129\n"
    "v_cvt_pk_bf16_f32 v129, v130, v131\n"
    "ds_write_b128 v242, v[112:115] offset:26688\n"
    "v_mfma_f32_16x16x32_bf16 v[112:115], a[8:11], v[184:187], 0\n"
    "s_waitcnt lgkmcnt(7)\n"
    "v_or_b32_e32 v191, v191, v207\n"
    "v_lshlrev_b32_sdwa v207, 1, v183 dst_sel:DWORD dst_unused:UNUSED_PAD src0_sel:DWORD src1_sel:WORD_1\n"
    "v_mfma_f32_16x16x32_bf16 v[180:183], a[148:151], v[156:159], 0\n"
    "ds_read_u16 v198, v198\n"
    "ds_read_u16_d16_hi v206, v206\n"
    "v_mfma_f32_16x16x32_bf16 v[180:183], a[196:199], v[160:163], v[180:183]\n"
    "ds_read_u16 v199, v199\n"
    "v_cvt_pk_bf16_f32 v130, v132, v133\n"
    "v_mfma_f32_16x16x32_bf16 v[180:183], a[108:111], v[164:167], v[180:183]\n"
    "s_waitcnt lgkmcnt(7)\n"
    "v_or_b32_e32 v193, v193, v201\n"
    "v_cvt_pk_bf16_f32 v131, v134, v135\n"
    "v_mfma_f32_16x16x32_bf16 v[116:119], a[12:15], v[184:187], 0\n"
    "ds_read_u16_d16_hi v207, v207\n"
    "v_cvt_pk_bf16_f32 v104, v104, v105\n"
    "v_mfma_f32_16x16x32_bf16 v[132:135], a[244:247], v[184:187], 0\n"
    "v_cvt_pk_bf16_f32 v105, v106, v107\n"
    "v_cvt_pk_bf16_f32 v106, v108, v109\n"
    "ds_write_b128 v242, v[128:131] offset:28736\n"
    "v_mfma_f32_16x16x32_bf16 v[128:131], v[236:239], v[184:187], 0\n"
    "s_waitcnt lgkmcnt(6)\n"
    "v_cvt_pk_bf16_f32 v107, v110, v111\n"
    "v_or_b32_e32 v194, v194, v202\n"
    "v_mfma_f32_16x16x32_bf16 v[108:111], a[4:7], v[188:191], 0\n"
    "ds_write_b128 v242, v[104:107] offset:25664\n"
    "v_mfma_f32_16x16x32_bf16 v[104:107], a[0:3], v[188:191], 0\n"
    "v_cvt_pk_bf16_f32 v120, v120, v121\n"
    "v_cvt_pk_bf16_f32 v121, v122, v123\n"
    "v_cvt_pk_bf16_f32 v122, v124, v125\n"
    "v_cvt_pk_bf16_f32 v123, v126, v127\n"
    "v_cvt_pk_bf16_f32 v136, v136, v137\n"
    "v_pk_fma_f16 v232, v192, v192, s48\n"
    "v_mfma_f32_16x16x32_bf16 v[124:127], a[12:15], v[188:191], 0\n"
    "s_nop 0\n"
    "ds_write_b128 v242, v[120:123] offset:27712\n"
    "v_mfma_f32_16x16x32_bf16 v[120:123], a[8:11], v[188:191], 0\n"
    "s_waitcnt lgkmcnt(5)\n"
    "v_cvt_pk_bf16_f32 v137, v138, v139\n"
    "v_cvt_pk_bf16_f32 v138, v140, v141\n"
    "v_or_b32_e64 v195, v195, v203\n"
    "v_cvt_pk_bf16_f32 v139, v142, v143\n"
    "v_pk_mul_f16 v232, v192, v232\n"
    "v_or_b32_e64 v197, v197, v205\n"
    "ds_write_b128 v242, v[136:139] offset:29760\n"
    "v_mfma_f32_16x16x32_bf16 v[136:139], v[236:239], v[188:191], 0\n"
    "s_waitcnt lgkmcnt(3)\n"
    "v_exp_f16_e32 v234, v232\n"
    "v_mfma_f32_16x16x32_bf16 v[140:143], a[244:247], v[188:191], 0\n"
    "v_exp_f16_sdwa v234, v232 dst_sel:WORD_1 dst_unused:UNUSED_PRESERVE src0_sel:WORD_1\n"
    "v_cvt_pk_f16_f32 v184, v168, v169\n"
    "v_pk_fma_f16 v234, v234, s49, s49\n"
    "v_lshlrev_b32_sdwa v185, 1, v170 dst_sel:DWORD dst_unused:UNUSED_PAD src0_sel:DWORD src1_sel:WORD_1\n"
    "v_lshlrev_b32_sdwa v201, 1, v171 dst_sel:DWORD dst_unused:UNUSED_PAD src0_sel:DWORD src1_sel:WORD_1\n"
    "v_mfma_f32_16x16x32_bf16 v[168:171], a[152:155], v[144:147], 0\n"
    "ds_read_u16 v185, v185\n"
    "ds_read_u16_d16_hi v201, v201\n"
    "v_mfma_f32_16x16x32_bf16 v[168:171], a[200:203], v[148:151], v[168:171]\n"
    "ds_read_b128 v[236:239], v244 offset:4096\n"
    "v_lshlrev_b32_sdwa v186, 1, v172 dst_sel:DWORD dst_unused:UNUSED_PAD src0_sel:DWORD src1_sel:WORD_1\n"
    "v_lshlrev_b32_sdwa v202, 1, v173 dst_sel:DWORD dst_unused:UNUSED_PAD src0_sel:DWORD src1_sel:WORD_1\n"
    "v_lshlrev_b32_sdwa v187, 1, v174 dst_sel:DWORD dst_unused:UNUSED_PAD src0_sel:DWORD src1_sel:WORD_1\n"
    "v_lshlrev_b32_sdwa v203, 1, v175 dst_sel:DWORD dst_unused:UNUSED_PAD src0_sel:DWORD src1_sel:WORD_1\n"
    "v_rcp_f16_e32 v232, v234\n"
    "v_mfma_f32_16x16x32_bf16 v[168:171], a[112:115], v[152:155], v[168:171]\n"
    "s_waitcnt lgkmcnt(3)\n"
    "ds_read_u16 v186, v186\n"
    "ds_read_u16_d16_hi v202, v202\n"
    "v_or_b32_e32 v198, v198, v206\n"
    "v_rcp_f16_sdwa v232, v234 dst_sel:WORD_1 dst_unused:UNUSED_PRESERVE src0_sel:WORD_1\n"
    "v_mfma_f32_16x16x32_bf16 v[172:175], a[156:159], v[144:147], 0\n"
    "s_barrier\n"
    "v_mfma_f32_16x16x32_bf16 v[172:175], a[204:207], v[148:151], v[172:175]\n"
    "ds_read_u16 v187, v187\n"
    "ds_read_u16_d16_hi v203, v203\n"
    "v_cvt_pk_f16_f32 v188, v176, v177\n"
    "v_lshlrev_b32_sdwa v189, 1, v178 dst_sel:DWORD dst_unused:UNUSED_PAD src0_sel:DWORD src1_sel:WORD_1\n"
    "v_lshlrev_b32_sdwa v205, 1, v179 dst_sel:DWORD dst_unused:UNUSED_PAD src0_sel:DWORD src1_sel:WORD_1\n"
    "v_lshlrev_b32_sdwa v190, 1, v180 dst_sel:DWORD dst_unused:UNUSED_PAD src0_sel:DWORD src1_sel:WORD_1\n"
    "v_lshlrev_b32_sdwa v206, 1, v181 dst_sel:DWORD dst_unused:UNUSED_PAD src0_sel:DWORD src1_sel:WORD_1\n"
    "v_lshlrev_b32_sdwa v191, 1, v182 dst_sel:DWORD dst_unused:UNUSED_PAD src0_sel:DWORD src1_sel:WORD_1\n"
    "v_fma_mix_f32 v234, v192, v232, 0 op_sel_hi:[1,1,0]\n"
    "v_mfma_f32_16x16x32_bf16 v[172:175], a[116:119], v[152:155], v[172:175]\n"
    "ds_read_b128 a[244:247], v244 offset:6144\n"
    "ds_read_u16 v189, v189\n"
    "v_or_b32_e64 v199, v199, v207\n"
    "v_lshlrev_b32_sdwa v207, 1, v183 dst_sel:DWORD dst_unused:UNUSED_PAD src0_sel:DWORD src1_sel:WORD_1\n"
    "v_fma_mix_f32 v232, v192, v232, 0 op_sel:[1,1,0] op_sel_hi:[1,1,0]\n"
    "v_mfma_f32_16x16x32_bf16 v[176:179], a[152:155], v[156:159], 0\n"
    "s_nop 0\n"
    "ds_read_u16_d16_hi v205, v205\n"
    "v_cvt_pk_bf16_f32 v192, v234, v232\n"
    "v_mfma_f32_16x16x32_bf16 v[176:179], a[200:203], v[160:163], v[176:179]\n"
    "s_waitcnt lgkmcnt(7)\n"
    "ds_read_u16 v190, v190\n"
    "v_pk_fma_f16 v232, v196, v196, s48\n"
    "v_mfma_f32_16x16x32_bf16 v[176:179], a[112:115], v[164:167], v[176:179]\n"
    "v_pk_mul_f16 v232, v196, v232\n"
    "v_or_b32_e64 v185, v185, v201\n"
    "v_mfma_f32_16x16x32_bf16 v[180:183], a[156:159], v[156:159], 0\n"
    "ds_read_u16_d16_hi v206, v206\n"
    "v_exp_f16_e64 v234, v232\n"
    "v_mfma_f32_16x16x32_bf16 v[180:183], a[204:207], v[160:163], v[180:183]\n"
    "ds_read_u16 v191, v191\n"
    "v_exp_f16_sdwa v234, v232 dst_sel:WORD_1 dst_unused:UNUSED_PRESERVE src0_sel:WORD_1\n"
    "v_mfma_f32_16x16x32_bf16 v[180:183], a[116:119], v[164:167], v[180:183]\n"
    "s_waitcnt lgkmcnt(8)\n"
    "v_pk_fma_f16 v234, v234, s49, s49\n"
    "v_or_b32_e32 v186, v186, v202\n"
    "v_mfma_f32_16x16x32_bf16 v[96:99], a[16:19], v[192:195], v[96:99]\n"
    "ds_read_u16_d16_hi v207, v207\n"
    "v_rcp_f16_e32 v232, v234\n"
    "v_mfma_f32_16x16x32_bf16 v[100:103], a[20:23], v[192:195], v[100:103]\n"
    "v_rcp_f16_sdwa v232, v234 dst_sel:WORD_1 dst_unused:UNUSED_PRESERVE src0_sel:WORD_1\n"
    "v_mfma_f32_16x16x32_bf16 v[112:115], a[24:27], v[192:195], v[112:115]\n"
    "s_waitcnt lgkmcnt(6)\n"
    "v_fma_mix_f32 v234, v196, v232, 0 op_sel_hi:[1,1,0]\n"
    "v_fma_mix_f32 v232, v196, v232, 0 op_sel:[1,1,0] op_sel_hi:[1,1,0]\n"
    "v_mfma_f32_16x16x32_bf16 v[116:119], a[28:31], v[192:195], v[116:119]\n"
    "ds_read_b128 a[240:243], v244 offset:34816\n"
    "v_cvt_pk_bf16_f32 v196, v234, v232\n"
    "v_mfma_f32_16x16x32_bf16 v[128:131], v[236:239], v[192:195], v[128:131]\n"
    "v_pk_fma_f16 v232, v184, v184, s48\n"
    "v_or_b32_e64 v187, v187, v203\n"
    "v_mfma_f32_16x16x32_bf16 v[132:135], a[244:247], v[192:195], v[132:135]\n"
    "v_pk_mul_f16 v232, v184, v232\n"
    "v_lshlrev_b32_sdwa v192, 1, v168 dst_sel:DWORD dst_unused:UNUSED_PAD src0_sel:DWORD src1_sel:WORD_1\n"
    "v_mfma_f32_16x16x32_bf16 v[104:107], a[16:19], v[196:199], v[104:107]\n"
    "s_waitcnt lgkmcnt(5)\n"
    "ds_read_u16 v192, v192\n"
    "v_exp_f16_e32 v234, v232\n"
    "v_mfma_f32_16x16x32_bf16 v[108:111], a[20:23], v[196:199], v[108:111]\n"
    "v_exp_f16_sdwa v234, v232 dst_sel:WORD_1 dst_unused:UNUSED_PRESERVE src0_sel:WORD_1\n"
    "v_mfma_f32_16x16x32_bf16 v[120:123], a[24:27], v[196:199], v[120:123]\n"
    "v_or_b32_e64 v189, v189, v205\n"
    "v_pk_fma_f16 v234, v234, s49, s49\n"
    "v_mfma_f32_16x16x32_bf16 v[124:127], a[28:31], v[196:199], v[124:127]\n"
    "s_nop 0\n"
    "v_lshlrev_b32_sdwa v200, 1, v169 dst_sel:DWORD dst_unused:UNUSED_PAD src0_sel:DWORD src1_sel:WORD_1\n"
    "v_lshlrev_b32_sdwa v193, 1, v170 dst_sel:DWORD dst_unused:UNUSED_PAD src0_sel:DWORD src1_sel:WORD_1\n"
    "v_mfma_f32_16x16x32_bf16 v[136:139], v[236:239], v[196:199], v[136:139]\n"
    "s_waitcnt lgkmcnt(4)\n"
    "ds_read_u16_d16_hi v200, v200\n"
    "ds_read_u16 v193, v193\n"
    "v_mfma_f32_16x16x32_bf16 v[140:143], a[244:247], v[196:199], v[140:143]\n"
    "ds_read_b128 v[236:239], v244 offset:8192\n"
    "v_lshlrev_b32_sdwa v201, 1, v171 dst_sel:DWORD dst_unused:UNUSED_PAD src0_sel:DWORD src1_sel:WORD_1\n"
    "v_mfma_f32_16x16x32_bf16 v[168:171], a[160:163], v[144:147], 0\n"
    "s_nop 0\n"
    "ds_read_u16_d16_hi v201, v201\n"
    "v_lshlrev_b32_sdwa v194, 1, v172 dst_sel:DWORD dst_unused:UNUSED_PAD src0_sel:DWORD src1_sel:WORD_1\n"
    "v_mfma_f32_16x16x32_bf16 v[168:171], a[208:211], v[148:151], v[168:171]\n"
    "s_waitcnt lgkmcnt(5)\n"
    "ds_read_u16 v194, v194\n"
    "v_lshlrev_b32_sdwa v202, 1, v173 dst_sel:DWORD dst_unused:UNUSED_PAD src0_sel:DWORD src1_sel:WORD_1\n"
    "v_lshlrev_b32_sdwa v195, 1, v174 dst_sel:DWORD dst_unused:UNUSED_PAD src0_sel:DWORD src1_sel:WORD_1\n"
    "v_lshlrev_b32_sdwa v203, 1, v175 dst_sel:DWORD dst_unused:UNUSED_PAD src0_sel:DWORD src1_sel:WORD_1\n"
    "v_rcp_f16_e64 v232, v234\n"
    "v_mfma_f32_16x16x32_bf16 v[168:171], a[120:123], v[152:155], v[168:171]\n"
    "ds_read_u16_d16_hi v202, v202\n"
    "v_or_b32_e64 v190, v190, v206\n"
    "v_rcp_f16_sdwa v232, v234 dst_sel:WORD_1 dst_unused:UNUSED_PRESERVE src0_sel:WORD_1\n"
    "v_mfma_f32_16x16x32_bf16 v[172:175], a[164:167], v[144:147], 0\n"
    "ds_read_u16 v195, v195\n"
    "v_lshlrev_b32_sdwa v196, 1, v176 dst_sel:DWORD dst_unused:UNUSED_PAD src0_sel:DWORD src1_sel:WORD_1\n"
    "v_mfma_f32_16x16x32_bf16 v[172:175], a[212:215], v[148:151], v[172:175]\n"
    "ds_read_b128 a[244:247], v244 offset:10240\n"
    "v_lshlrev_b32_sdwa v204, 1, v177 dst_sel:DWORD dst_unused:UNUSED_PAD src0_sel:DWORD src1_sel:WORD_1\n"
    "v_lshlrev_b32_sdwa v197, 1, v178 dst_sel:DWORD dst_unused:UNUSED_PAD src0_sel:DWORD src1_sel:WORD_1\n"
    "v_lshlrev_b32_sdwa v205, 1, v179 dst_sel:DWORD dst_unused:UNUSED_PAD src0_sel:DWORD src1_sel:WORD_1\n"
    "v_lshlrev_b32_sdwa v198, 1, v180 dst_sel:DWORD dst_unused:UNUSED_PAD src0_sel:DWORD src1_sel:WORD_1\n"
    "v_lshlrev_b32_sdwa v206, 1, v181 dst_sel:DWORD dst_unused:UNUSED_PAD src0_sel:DWORD src1_sel:WORD_1\n"
    "v_lshlrev_b32_sdwa v199, 1, v182 dst_sel:DWORD dst_unused:UNUSED_PAD src0_sel:DWORD src1_sel:WORD_1\n"
    "v_fma_mix_f32 v234, v184, v232, 0 op_sel_hi:[1,1,0]\n"
    "v_mfma_f32_16x16x32_bf16 v[172:175], a[240:243], v[152:155], v[172:175]\n"
    "s_waitcnt lgkmcnt(7)\n"
    "ds_read_u16_d16_hi v203, v203\n"
    "ds_read_u16 v196, v196\n"
    "v_or_b32_e32 v191, v191, v207\n"
    "v_lshlrev_b32_sdwa v207, 1, v183 dst_sel:DWORD dst_unused:UNUSED_PAD src0_sel:DWORD src1_sel:WORD_1\n"
    "v_fma_mix_f32 v232, v184, v232, 0 op_sel:[1,1,0] op_sel_hi:[1,1,0]\n"
    "v_mfma_f32_16x16x32_bf16 v[176:179], a[160:163], v[156:159], 0\n"
    "v_cvt_pk_bf16_f32 v184, v234, v232\n"
    "v_pk_fma_f16 v232, v188, v188, s48\n"
    "v_mfma_f32_16x16x32_bf16 v[176:179], a[208:211], v[160:163], v[176:179]\n"
    "ds_read_u16_d16_hi v204, v204\n"
    "v_pk_mul_f16 v232, v188, v232\n"
    "v_mfma_f32_16x16x32_bf16 v[176:179], a[120:123], v[164:167], v[176:179]\n"
    "ds_read_u16 v197, v197\n"
    "v_exp_f16_e64 v234, v232\n"
    "s_waitcnt vmcnt(1)\n"
    "v_smfmac_f32_32x32x32_bf16 v[112:127], a[248:251], a[232:239], v241\n"
    "s_waitcnt lgkmcnt(8)\n"
    "ds_read_b128 a[232:235], v243 offset:24640\n"
    "v_exp_f16_sdwa v234, v232 dst_sel:WORD_1 dst_unused:UNUSED_PRESERVE src0_sel:WORD_1\n"
    "v_or_b32_e64 v192, v192, v200\n"
    "v_pk_fma_f16 v234, v234, s49, s49\n"
    "s_waitcnt vmcnt(0)\n"
    "v_smfmac_f32_32x32x32_bf16 v[96:111], a[248:251], a[224:231], v241\n"
    "s_nop 0\n"
    "buffer_load_dwordx4 a[224:227], v245, s[40:43], s58 offen offset:128\n"
    "ds_read_b128 a[236:239], v243 offset:25664\n"
    "v_rcp_f16_e64 v232, v234\n"
    "v_or_b32_e32 v193, v193, v201\n"
    "v_mfma_f32_16x16x32_bf16 v[180:183], a[164:167], v[156:159], 0\n"
    "s_waitcnt lgkmcnt(8)\n"
    "ds_read_u16_d16_hi v205, v205\n"
    "ds_read_u16 v198, v198\n"
    "v_mfma_f32_16x16x32_bf16 v[180:183], a[212:215], v[160:163], v[180:183]\n"
    "v_rcp_f16_sdwa v232, v234 dst_sel:WORD_1 dst_unused:UNUSED_PRESERVE src0_sel:WORD_1\n"
    "v_mfma_f32_16x16x32_bf16 v[180:183], a[240:243], v[164:167], v[180:183]\n"
    "s_nop 0\n"
    "ds_read_u16_d16_hi v206, v206\n"
    "v_fma_mix_f32 v234, v188, v232, 0 op_sel_hi:[1,1,0]\n"
    "v_mfma_f32_16x16x32_bf16 v[112:115], a[40:43], v[184:187], v[112:115]\n"
    "s_waitcnt lgkmcnt(8)\n"
    "v_fma_mix_f32 v232, v188, v232, 0 op_sel:[1,1,0] op_sel_hi:[1,1,0]\n"
    "v_lshlrev_b32_sdwa v201, 1, v171 dst_sel:DWORD dst_unused:UNUSED_PAD src0_sel:DWORD src1_sel:WORD_1\n"
    "v_mfma_f32_16x16x32_bf16 v[116:119], a[44:47], v[184:187], v[116:119]\n"
    "ds_read_u16 v199, v199\n"
    "v_cvt_pk_bf16_f32 v188, v234, v232\n"
    "v_mfma_f32_16x16x32_bf16 v[128:131], v[236:239], v[184:187], v[128:131]\n"
    "ds_read_u16_d16_hi v207, v207\n"
    "v_or_b32_e64 v194, v194, v202\n"
    "v_mfma_f32_16x16x32_bf16 v[132:135], a[244:247], v[184:187], v[132:135]\n"
    "v_lshlrev_b32_sdwa v202, 1, v173 dst_sel:DWORD dst_unused:UNUSED_PAD src0_sel:DWORD src1_sel:WORD_1\n"
    "v_or_b32_e32 v195, v195, v203\n"
    "v_mfma_f32_16x16x32_bf16 v[120:123], a[40:43], v[188:191], v[120:123]\n"
    "s_waitcnt lgkmcnt(8)\n"
    "ds_read_b128 a[240:243], v244 offset:38912\n"
    "v_lshlrev_b32_sdwa v203, 1, v175 dst_sel:DWORD dst_unused:UNUSED_PAD src0_sel:DWORD src1_sel:WORD_1\n"
    "v_mfma_f32_16x16x32_bf16 v[124:127], a[44:47], v[188:191], v[124:127]\n"
    "buffer_load_dwordx4 a[228:231], v245, s[40:43], s101 offen offset:128\n"
    "ds_read_u16_d16_hi v201, v201\n"
    "v_mfma_f32_16x16x32_bf16 v[136:139], v[236:239], v[188:191], v[136:139]\n"
    "s_waitcnt lgkmcnt(7)\n"
    "v_or_b32_e32 v196, v196, v204\n"
    "v_smfmac_f32_32x32x32_bf16 v[96:111], a[248:251], a[232:239], v241\n"
    "ds_read_b128 a[232:235], v243 offset:26688\n"
    "ds_read_b128 a[236:239], v243 offset:27712\n"
    "v_mfma_f32_16x16x32_bf16 v[140:143], a[244:247], v[188:191], v[140:143]\n"
    "s_waitcnt lgkmcnt(8)\n"
    "ds_read_u16_d16_hi v202, v202\n"
    "v_or_b32_e32 v197, v197, v205\n"
    "v_lshlrev_b32_sdwa v205, 1, v179 dst_sel:DWORD dst_unused:UNUSED_PAD src0_sel:DWORD src1_sel:WORD_1\n"
    "ds_read_b128 v[236:239], v244 offset:12288\n"
    "ds_read_u16_d16_hi v203, v203\n"
    "ds_read_b128 a[244:247], v244 offset:14336\n"
    "v_mfma_f32_16x16x32_bf16 v[96:99], a[32:35], v[184:187], v[96:99]\n"
    "s_nop 0\n"
    "v_mfma_f32_16x16x32_bf16 v[100:103], a[36:39], v[184:187], v[100:103]\n"
    "s_waitcnt lgkmcnt(10)\n"
    "v_cvt_pk_f16_f32 v184, v168, v169\n"
    "v_lshlrev_b32_sdwa v185, 1, v170 dst_sel:DWORD dst_unused:UNUSED_PAD src0_sel:DWORD src1_sel:WORD_1\n"
    "v_mfma_f32_16x16x32_bf16 v[104:107], a[32:35], v[188:191], v[104:107]\n"
    "v_lshlrev_b32_sdwa v186, 1, v172 dst_sel:DWORD dst_unused:UNUSED_PAD src0_sel:DWORD src1_sel:WORD_1\n"
    "v_lshlrev_b32_sdwa v187, 1, v174 dst_sel:DWORD dst_unused:UNUSED_PAD src0_sel:DWORD src1_sel:WORD_1\n"
    "v_mfma_f32_16x16x32_bf16 v[108:111], a[36:39], v[188:191], v[108:111]\n"
    "ds_read_u16 v185, v185\n"
    "v_cvt_pk_f16_f32 v188, v176, v177\n"
    "v_mfma_f32_16x16x32_bf16 v[168:171], a[168:171], v[144:147], 0\n"
    "s_nop 0\n"
    "v_lshlrev_b32_sdwa v189, 1, v178 dst_sel:DWORD dst_unused:UNUSED_PAD src0_sel:DWORD src1_sel:WORD_1\n"
    "v_lshlrev_b32_sdwa v190, 1, v180 dst_sel:DWORD dst_unused:UNUSED_PAD src0_sel:DWORD src1_sel:WORD_1\n"
    "v_mfma_f32_16x16x32_bf16 v[168:171], a[216:219], v[148:151], v[168:171]\n"
    "s_waitcnt lgkmcnt(8)\n"
    "ds_read_u16 v186, v186\n"
    "v_lshlrev_b32_sdwa v191, 1, v182 dst_sel:DWORD dst_unused:UNUSED_PAD src0_sel:DWORD src1_sel:WORD_1\n"
    "v_mfma_f32_16x16x32_bf16 v[168:171], a[124:127], v[152:155], v[168:171]\n"
    "ds_read_u16 v187, v187\n"
    "v_or_b32_e64 v198, v198, v206\n"
    "v_mfma_f32_16x16x32_bf16 v[172:175], a[172:175], v[144:147], 0\n"
    "v_lshlrev_b32_sdwa v206, 1, v181 dst_sel:DWORD dst_unused:UNUSED_PAD src0_sel:DWORD src1_sel:WORD_1\n"
    "v_or_b32_e32 v199, v199, v207\n"
    "v_mfma_f32_16x16x32_bf16 v[172:175], a[220:223], v[148:151], v[172:175]\n"
    "s_waitcnt lgkmcnt(7)\n"
    "ds_read_u16 v189, v189\n"
    "v_lshlrev_b32_sdwa v207, 1, v183 dst_sel:DWORD dst_unused:UNUSED_PAD src0_sel:DWORD src1_sel:WORD_1\n"
    "v_smfmac_f32_32x32x32_bf16 v[112:127], a[248:251], a[232:239], v241\n"
    "v_pk_fma_f16 v232, v184, v184, s48\n"
    "v_pk_mul_f16 v232, v184, v232\n"
    "v_exp_f16_e64 v234, v232\n"
    "ds_read_b128 a[232:235], v243 offset:28736\n"
    "ds_read_u16_d16_hi v205, v205\n"
    "v_mfma_f32_16x16x32_bf16 v[172:175], a[240:243], v[152:155], v[172:175]\n"
    "s_waitcnt lgkmcnt(8)\n"
    "v_exp_f16_sdwa v234, v232 dst_sel:WORD_1 dst_unused:UNUSED_PRESERVE src0_sel:WORD_1\n"
    "v_mfma_f32_16x16x32_bf16 v[176:179], a[168:171], v[156:159], 0\n"
    "s_nop 0\n"
    "ds_read_b128 a[236:239], v243 offset:29760\n"
    "v_pk_fma_f16 v234, v234, s49, s49\n"
    "v_mfma_f32_16x16x32_bf16 v[176:179], a[216:219], v[160:163], v[176:179]\n"
    "s_waitcnt lgkmcnt(7)\n"
    "ds_read_u16 v190, v190\n"
    "v_rcp_f16_e32 v232, v234\n"
    "v_mfma_f32_16x16x32_bf16 v[176:179], a[124:127], v[164:167], v[176:179]\n"
    "ds_read_u16_d16_hi v206, v206\n"
    "v_rcp_f16_sdwa v232, v234 dst_sel:WORD_1 dst_unused:UNUSED_PRESERVE src0_sel:WORD_1\n"
    "v_mfma_f32_16x16x32_bf16 v[180:183], a[172:175], v[156:159], 0\n"
    "v_fma_mix_f32 v234, v184, v232, 0 op_sel_hi:[1,1,0]\n"
    "s_waitcnt lgkmcnt(8)\n"
    "v_or_b32_e32 v185, v185, v201\n"
    "v_mfma_f32_16x16x32_bf16 v[180:183], a[220:223], v[160:163], v[180:183]\n"
    "ds_read_u16 v191, v191\n"
    "v_lshlrev_b32_sdwa v201, 1, v171 dst_sel:DWORD dst_unused:UNUSED_PAD src0_sel:DWORD src1_sel:WORD_1\n"
    "v_mfma_f32_16x16x32_bf16 v[180:183], a[240:243], v[164:167], v[180:183]\n"
    "s_waitcnt lgkmcnt(8)\n"
    "ds_read_u16_d16_hi v207, v207\n"
    "v_or_b32_e32 v186, v186, v202\n"
    "v_mfma_f32_16x16x32_bf16 v[96:99], a[48:51], v[192:195], v[96:99]\n"
    "v_lshlrev_b32_sdwa v202, 1, v173 dst_sel:DWORD dst_unused:UNUSED_PAD src0_sel:DWORD src1_sel:WORD_1\n"
    "v_fma_mix_f32 v232, v184, v232, 0 op_sel:[1,1,0] op_sel_hi:[1,1,0]\n"
    "v_mfma_f32_16x16x32_bf16 v[100:103], a[52:55], v[192:195], v[100:103]\n"
    "s_waitcnt lgkmcnt(8)\n"
    "ds_read_u16_d16_hi v201, v201\n"
    "v_or_b32_e32 v187, v187, v203\n"
    "v_mfma_f32_16x16x32_bf16 v[112:115], a[56:59], v[192:195], v[112:115]\n"
    "v_lshlrev_b32_sdwa v203, 1, v175 dst_sel:DWORD dst_unused:UNUSED_PAD src0_sel:DWORD src1_sel:WORD_1\n"
    "v_cvt_pk_bf16_f32 v184, v234, v232\n"
    "v_mfma_f32_16x16x32_bf16 v[116:119], a[60:63], v[192:195], v[116:119]\n"
    "ds_read_u16_d16_hi v202, v202\n"
    "v_pk_fma_f16 v232, v188, v188, s48\n"
    "v_mfma_f32_16x16x32_bf16 v[128:131], v[236:239], v[192:195], v[128:131]\n"
    "v_pk_mul_f16 v232, v188, v232\n"
    "v_exp_f16_e32 v234, v232\n"
    "v_mfma_f32_16x16x32_bf16 v[132:135], a[244:247], v[192:195], v[132:135]\n"
    "s_waitcnt lgkmcnt(6)\n"
    "ds_read_u16_d16_hi v203, v203\n"
    "v_or_b32_e64 v189, v189, v205\n"
    "v_mfma_f32_16x16x32_bf16 v[104:107], a[48:51], v[196:199], v[104:107]\n"
    "v_cvt_pk_f16_f32 v192, v168, v169\n"
    "v_lshlrev_b32_sdwa v193, 1, v170 dst_sel:DWORD dst_unused:UNUSED_PAD src0_sel:DWORD src1_sel:WORD_1\n"
    "v_mfma_f32_16x16x32_bf16 v[108:111], a[52:55], v[196:199], v[108:111]\n"
    "ds_read_u16 v193, v193\n"
    "v_lshlrev_b32_sdwa v194, 1, v172 dst_sel:DWORD dst_unused:UNUSED_PAD src0_sel:DWORD src1_sel:WORD_1\n"
    "v_mfma_f32_16x16x32_bf16 v[120:123], a[56:59], v[196:199], v[120:123]\n"
    "ds_read_u16 v194, v194\n"
    "v_lshlrev_b32_sdwa v195, 1, v174 dst_sel:DWORD dst_unused:UNUSED_PAD src0_sel:DWORD src1_sel:WORD_1\n"
    "v_mfma_f32_16x16x32_bf16 v[124:127], a[60:63], v[196:199], v[124:127]\n"
    "v_lshlrev_b32_sdwa v205, 1, v179 dst_sel:DWORD dst_unused:UNUSED_PAD src0_sel:DWORD src1_sel:WORD_1\n"
    "s_waitcnt lgkmcnt(7)\n"
    "v_or_b32_e32 v190, v190, v206\n"
    "v_mfma_f32_16x16x32_bf16 v[136:139], v[236:239], v[196:199], v[136:139]\n"
    "ds_read_b128 v[236:239], v244 offset:16384\n"
    "ds_read_u16 v195, v195\n"
    "v_mfma_f32_16x16x32_bf16 v[140:143], a[244:247], v[196:199], v[140:143]\n"
    "v_cvt_pk_f16_f32 v196, v176, v177\n"
    "v_lshlrev_b32_sdwa v197, 1, v178 dst_sel:DWORD dst_unused:UNUSED_PAD src0_sel:DWORD src1_sel:WORD_1\n"
    "v_lshlrev_b32_sdwa v198, 1, v180 dst_sel:DWORD dst_unused:UNUSED_PAD src0_sel:DWORD src1_sel:WORD_1\n"
    "v_lshlrev_b32_sdwa v206, 1, v181 dst_sel:DWORD dst_unused:UNUSED_PAD src0_sel:DWORD src1_sel:WORD_1\n"
    "v_lshlrev_b32_sdwa v199, 1, v182 dst_sel:DWORD dst_unused:UNUSED_PAD src0_sel:DWORD src1_sel:WORD_1\n"
    "v_exp_f16_sdwa v234, v232 dst_sel:WORD_1 dst_unused:UNUSED_PRESERVE src0_sel:WORD_1\n"
    "v_smfmac_f32_32x32x32_bf16 v[128:143], a[248:251], a[232:239], v241\n"
    "s_waitcnt lgkmcnt(7)\n"
    "ds_read_b128 a[244:247], v244 offset:18432\n"
    "ds_read_u16 v197, v197\n"
    "v_or_b32_e32 v191, v191, v207\n"
    "v_lshlrev_b32_sdwa v207, 1, v183 dst_sel:DWORD dst_unused:UNUSED_PAD src0_sel:DWORD src1_sel:WORD_1\n"
    "v_pk_fma_f16 v234, v234, s49, s49\n"
    "v_mfma_f32_16x16x32_bf16 v[96:99], a[64:67], v[184:187], v[96:99]\n"
    "ds_read_u16_d16_hi v205, v205\n"
    "v_rcp_f16_e64 v232, v234\n"
    "v_mfma_f32_16x16x32_bf16 v[100:103], a[68:71], v[184:187], v[100:103]\n"
    "ds_read_u16 v198, v198\n"
    "v_rcp_f16_sdwa v232, v234 dst_sel:WORD_1 dst_unused:UNUSED_PRESERVE src0_sel:WORD_1\n"
    "v_mfma_f32_16x16x32_bf16 v[112:115], a[72:75], v[184:187], v[112:115]\n"
    "v_fma_mix_f32 v234, v188, v232, 0 op_sel_hi:[1,1,0]\n"
    "v_fma_mix_f32 v232, v188, v232, 0 op_sel:[1,1,0] op_sel_hi:[1,1,0]\n"
    "v_mfma_f32_16x16x32_bf16 v[116:119], a[76:79], v[184:187], v[116:119]\n"
    "ds_read_u16_d16_hi v206, v206\n"
    "v_cvt_pk_bf16_f32 v188, v234, v232\n"
    "v_pk_fma_f16 v232, v192, v192, s48\n"
    "s_waitcnt lgkmcnt(8)\n"
    "v_or_b32_e32 v193, v193, v201\n"
    "v_mfma_f32_16x16x32_bf16 v[104:107], a[64:67], v[188:191], v[104:107]\n"
    "ds_read_u16 v199, v199\n"
    "v_pk_mul_f16 v232, v192, v232\n"
    "s_waitcnt lgkmcnt(7)\n"
    "v_mfma_f32_16x16x32_bf16 v[128:131], v[236:239], v[184:187], v[128:131]\n"
    "s_nop 0\n"
    "ds_read_u16_d16_hi v207, v207\n"
    "v_or_b32_e64 v194, v194, v202\n"
    "v_mfma_f32_16x16x32_bf16 v[108:111], a[68:71], v[188:191], v[108:111]\n"
    "v_exp_f16_e64 v234, v232\n"
    "v_mfma_f32_16x16x32_bf16 v[120:123], a[72:75], v[188:191], v[120:123]\n"
    "s_waitcnt lgkmcnt(6)\n"
    "v_or_b32_e32 v195, v195, v203\n"
    "v_exp_f16_sdwa v234, v232 dst_sel:WORD_1 dst_unused:UNUSED_PRESERVE src0_sel:WORD_1\n"
    "v_mfma_f32_16x16x32_bf16 v[132:135], a[244:247], v[184:187], v[132:135]\n"
    "v_pk_fma_f16 v234, v234, s49, s49\n"
    "v_rcp_f16_e64 v232, v234\n"
    "v_mfma_f32_16x16x32_bf16 v[124:127], a[76:79], v[188:191], v[124:127]\n"
    "v_rcp_f16_sdwa v232, v234 dst_sel:WORD_1 dst_unused:UNUSED_PRESERVE src0_sel:WORD_1\n"
    "v_mfma_f32_16x16x32_bf16 v[136:139], v[236:239], v[188:191], v[136:139]\n"
    "ds_read_b128 v[236:239], v244 offset:20480\n"
    "v_fma_mix_f32 v234, v192, v232, 0 op_sel_hi:[1,1,0]\n"
    "v_mfma_f32_16x16x32_bf16 v[140:143], a[244:247], v[188:191], v[140:143]\n"
    "ds_read_b128 a[244:247], v244 offset:22528\n"
    "s_waitcnt lgkmcnt(6)\n"
    "v_or_b32_e32 v197, v197, v205\n"
    "v_fma_mix_f32 v232, v192, v232, 0 op_sel:[1,1,0] op_sel_hi:[1,1,0]\n"
    "v_cvt_pk_bf16_f32 v192, v234, v232\n"
    "v_pk_fma_f16 v232, v196, v196, s48\n"
    "v_pk_mul_f16 v232, v196, v232\n"
    "v_exp_f16_e64 v234, v232\n"
    "v_mfma_f32_16x16x32_bf16 v[96:99], a[80:83], v[192:195], v[96:99]\n"
    "v_exp_f16_sdwa v234, v232 dst_sel:WORD_1 dst_unused:UNUSED_PRESERVE src0_sel:WORD_1\n"
    "v_mfma_f32_16x16x32_bf16 v[100:103], a[84:87], v[192:195], v[100:103]\n"
    "s_waitcnt lgkmcnt(4)\n"
    "v_or_b32_e32 v198, v198, v206\n"
    "v_pk_fma_f16 v234, v234, s49, s49\n"
    "v_rcp_f16_e64 v232, v234\n"
    "v_mfma_f32_16x16x32_bf16 v[112:115], a[88:91], v[192:195], v[112:115]\n"
    "v_rcp_f16_sdwa v232, v234 dst_sel:WORD_1 dst_unused:UNUSED_PRESERVE src0_sel:WORD_1\n"
    "v_mfma_f32_16x16x32_bf16 v[116:119], a[92:95], v[192:195], v[116:119]\n"
    "v_fma_mix_f32 v234, v196, v232, 0 op_sel_hi:[1,1,0]\n"
    "v_fma_mix_f32 v232, v196, v232, 0 op_sel:[1,1,0] op_sel_hi:[1,1,0]\n"
    "v_cvt_pk_bf16_f32 v96, v96, v97\n"
    "v_cvt_pk_bf16_f32 v196, v234, v232\n"
    "s_waitcnt lgkmcnt(1)\n"
    "v_or_b32_e32 v199, v199, v207\n"
    "v_cvt_pk_bf16_f32 v97, v98, v99\n"
    "v_cvt_pk_bf16_f32 v98, v100, v101\n"
    "v_mfma_f32_16x16x32_bf16 v[104:107], a[80:83], v[196:199], v[104:107]\n"
    "v_cvt_pk_bf16_f32 v99, v102, v103\n"
    "v_cvt_pk_bf16_f32 v112, v112, v113\n"
    "v_mfma_f32_16x16x32_bf16 v[108:111], a[84:87], v[196:199], v[108:111]\n"
    "ds_write_b128 v243, v[96:99] offset:24640\n"
    "v_mfma_f32_16x16x32_bf16 v[120:123], a[88:91], v[196:199], v[120:123]\n"
    "v_cvt_pk_bf16_f32 v113, v114, v115\n"
    "v_cvt_pk_bf16_f32 v114, v116, v117\n"
    "v_mfma_f32_16x16x32_bf16 v[124:127], a[92:95], v[196:199], v[124:127]\n"
    "v_cvt_pk_bf16_f32 v115, v118, v119\n"
    "v_cvt_pk_bf16_f32 v104, v104, v105\n"
    "ds_write_b128 v243, v[112:115] offset:26688\n"
    "v_cvt_pk_bf16_f32 v105, v106, v107\n"
    "v_cvt_pk_bf16_f32 v106, v108, v109\n"
    "v_cvt_pk_bf16_f32 v107, v110, v111\n"
    "v_cvt_pk_bf16_f32 v120, v120, v121\n"
    "v_cvt_pk_bf16_f32 v121, v122, v123\n"
    "v_cvt_pk_bf16_f32 v122, v124, v125\n"
    "ds_write_b128 v243, v[104:107] offset:25664\n"
    "v_cvt_pk_bf16_f32 v123, v126, v127\n"
    "ds_write_b128 v243, v[120:123] offset:27712\n"
    "s_waitcnt vmcnt(0)\n"
    "v_smfmac_f32_32x32x32_bf16 v[128:143], a[248:251], a[224:231], v241\n"
    "s_waitcnt lgkmcnt(0)\n"
    "s_nop 8\n"
    "v_mfma_f32_16x16x32_bf16 v[128:131], v[236:239], v[192:195], v[128:131]\n"
    "v_mfma_f32_16x16x32_bf16 v[132:135], a[244:247], v[192:195], v[132:135]\n"
    "v_mfma_f32_16x16x32_bf16 v[136:139], v[236:239], v[196:199], v[136:139]\n"
    "v_mfma_f32_16x16x32_bf16 v[140:143], a[244:247], v[196:199], v[140:143]\n"
    "s_nop 4\n"
    "v_cvt_pk_bf16_f32 v128, v128, v129\n"
    "v_cvt_pk_bf16_f32 v129, v130, v131\n"
    "v_cvt_pk_bf16_f32 v130, v132, v133\n"
    "v_cvt_pk_bf16_f32 v131, v134, v135\n"
    "ds_write_b128 v243, v[128:131] offset:28736\n"
    "v_cvt_pk_bf16_f32 v136, v136, v137\n"
    "v_cvt_pk_bf16_f32 v137, v138, v139\n"
    "v_cvt_pk_bf16_f32 v138, v140, v141\n"
    "v_cvt_pk_bf16_f32 v139, v142, v143\n"
    "ds_write_b128 v243, v[136:139] offset:29760\n"
    "ds_read_b128 a[224:227], v249 offset:0\n"
    "ds_read_b128 a[228:231], v249 offset:2048\n"
    "ds_read_b128 a[232:235], v249 offset:4096\n"
    "ds_read_b128 a[236:239], v249 offset:1024\n"
    "ds_read_b128 a[252:255], v249 offset:3072\n"
    "ds_read_b128 a[240:243], v249 offset:5120\n"
    "s_waitcnt lgkmcnt(5)\n"
    "buffer_store_dwordx4 a[224:227], v255, s[44:47], s58 offen offset:0 sc1\n"
    "s_waitcnt lgkmcnt(4)\n"
    "buffer_store_dwordx4 a[228:231], v255, s[44:47], s58 offen offset:64 sc1\n"
    "s_waitcnt lgkmcnt(3)\n"
    "buffer_store_dwordx4 a[232:235], v255, s[44:47], s58 offen offset:128 sc1\n"
    "s_waitcnt lgkmcnt(1)\n"
    "buffer_store_dwordx4 a[236:239], v255, s[44:47], s101 offen offset:0 sc1\n"
    "buffer_store_dwordx4 a[252:255], v255, s[44:47], s101 offen offset:64 sc1\n"
    "s_waitcnt lgkmcnt(0)\n"
    "buffer_store_dwordx4 a[240:243], v255, s[44:47], s101 offen offset:128 sc1\n"
    "97:\n"
    "98:\n"
    "s_waitcnt vmcnt(0) lgkmcnt(0)\n"
    ".Lreplk_end:\n"
    : "+{s66}"(flg), "+{s[76:79]}"(rt), "+{s[48:55]}"(S), "+{v[208:215]}"(PA)
    : "{s[40:43]}"(rx), "{s[44:47]}"(srd(out, XBYTES)), "{s56}"((u32)(L_XT - L_H - w * GS)), "{s38}"((u32)(L_W + w * 9216)),
      "{s36}"((u32)((L_W + (w & 1) * 1024) * 8)), "{s37}"((u32)(w * 4096 + (tx & 1) * 16384)),
      "{s67}"(0x3fffffffu | (tx == 0 ? 0x80000000u : 0u) | (tx == 31 ? 0x40000000u : 0u)), "{s[96:99]}"(T), "{s[80:87]}"(rw), "{s[88:95]}"(rd)
    : "memory", "scc", "vcc", "m0", "v0", "v1", "v2", "v3", "v4", "v5", "v6", "v7", "v8", "v9", "v10", "v11", "v12", "v13", "v14", "v15", "v16", "v17", "v18", "v19", "v20", "v21", "v22", "v23", "v24", "v25", "v26", "v27", "v28", "v29", "v30", "v31", "v32", "v33", "v34", "v35", "v36", "v37", "v38", "v39", "v40", "v41", "v42", "v43", "v44", "v45", "v46", "v47", "v48", "v49", "v50", "v51", "v52", "v53", "v54", "v55", "v56", "v57", "v58", "v59", "v60", "v61", "v62", "v63", "v64", "v65", "v66", "v67", "v68", "v69", "v70", "v71", "v72", "v73", "v74", "v75", "v76", "v77", "v78", "v79", "v80", "v81", "v82", "v83", "v84", "v85", "v86", "v87", "v88", "v89", "v90", "v91", "v92", "v93", "v94", "v95", "v96", "v97", "v98", "v99", "v100", "v101", "v102", "v103", "v104", "v105", "v106", "v107", "v108", "v109", "v110", "v111", "v112", "v113", "v114", "v115", "v116", "v117", "v118", "v119", "v120", "v121", "v122", "v123", "v124", "v125", "v126", "v127", "v128", "v129", "v130", "v131", "v132", "v133", "v134", "v135", "v136", "v137", "v138", "v139", "v140", "v141", "v142", "v143", "v144", "v145", "v146", "v147", "v148", "v149", "v150", "v151", "v152", "v153", "v154", "v155", "v156", "v157", "v158", "v159", "v160", "v161", "v162", "v163", "v164", "v165", "v166", "v167", "v168", "v169", "v170", "v171", "v172", "v173", "v174", "v175", "v176", "v177", "v178", "v179", "v180", "v181", "v182", "v183", "v184", "v185", "v186", "v187", "v188", "v189", "v190", "v191", "v192", "v193", "v194", "v195", "v196", "v197", "v198", "v199", "v200", "v201", "v202", "v203", "v204", "v205", "v206", "v207", "v216", "v217", "v218", "v219", "v220", "v221", "v222", "v223", "v224", "v225", "v226", "v227", "v228", "v229", "v230", "v231", "v232", "v233", "v234", "v235", "v236", "v237", "v238", "v239", "v240", "v241", "v242", "v243", "v244", "v245", "v246", "v247", "v248", "v249", "v250", "v251", "v252", "v253", "v254", "v255", "a0", "a1", "a2", "a3", "a4", "a5", "a6", "a7", "a8", "a9", "a10", "a11", "a12", "a13", "a14", "a15", "a16", "a17", "a18", "a19", "a20", "a21", "a22", "a23", "a24", "a25", "a26", "a27", "a28", "a29", "a30", "a31", "a32", "a33", "a34", "a35", "a36", "a37", "a38", "a39", "a40", "a41", "a42", "a43", "a44", "a45", "a46", "a47", "a48", "a49", "a50", "a51", "a52", "a53", "a54", "a55", "a56", "a57", "a58", "a59", "a60", "a61", "a62", "a63", "a64", "a65", "a66", "a67", "a68", "a69", "a70", "a71", "a72", "a73", "a74", "a75", "a76", "a77", "a78", "a79", "a80", "a81", "a82", "a83", "a84", "a85", "a86", "a87", "a88", "a89", "a90", "a91", "a92", "a93", "a94", "a95", "a96", "a97", "a98", "a99", "a100", "a101", "a102", "a103", "a104", "a105", "a106", "a107", "a108", "a109", "a110", "a111", "a112", "a113", "a114", "a115", "a116", "a117", "a118", "a119", "a120", "a121", "a122", "a123", "a124", "a125", "a126", "a127", "a128", "a129", "a130", "a131", "a132", "a133", "a134", "a135", "a136", "a137", "a138", "a139", "a140", "a141", "a142", "a143", "a144", "a145", "a146", "a147", "a148", "a149", "a150", "a151", "a152", "a153", "a154", "a155", "a156", "a157", "a158", "a159", "a160", "a161", "a162", "a163", "a164", "a165", "a166", "a167", "a168", "a169", "a170", "a171", "a172", "a173", "a174", "a175", "a176", "a177", "a178", "a179", "a180", "a181", "a182", "a183", "a184", "a185", "a186", "a187", "a188", "a189", "a190", "a191", "a192", "a193", "a194", "a195", "a196", "a197", "a198", "a199", "a200", "a201", "a202", "a203", "a204", "a205", "a206", "a207", "a208", "a209", "a210", "a211", "a212", "a213", "a214", "a215", "a216", "a217", "a218", "a219", "a220", "a221", "a222", "a223", "a224", "a225", "a226", "a227", "a228", "a229", "a230", "a231", "a232", "a233", "a234", "a235", "a236", "a237", "a238", "a239", "a240", "a241", "a242", "a243", "a244", "a245", "a246", "a247", "a248", "a249", "a250", "a251", "a252", "a253", "a254", "a255", "s39", "s57", "s58", "s59", "s60", "s61", "s62", "s63", "s64", "s65", "s68", "s69", "s70", "s71", "s72", "s73", "s74", "s75", "s100", "s101");
  if (flg != 2u) {
    const int l2 = __builtin_amdgcn_mbcnt_hi(~0u, __builtin_amdgcn_mbcnt_lo(~0u, 0u));
    slow_path(x, dwl, dws, w1, w2, out, (float*)smem, w * 64 + l2, tx, ty0);
  }
}

extern "C" void kernel_launch(void* const* d_in, const int* in_sizes, int n_in, void* d_out, int out_size, void* d_ws,
                              size_t ws_size, hipStream_t stream) {
  replk_kernel<<<256, 256, 0, stream>>>((const u16*)d_in[0], (const u16*)d_in[1], (const u16*)d_in[2], (const u16*)d_in[3],
                                        (const u16*)d_in[4], (u16*)d_out
                                        );
}
```

```cpp
#include <hip/hip_runtime.h>
#include <hip/hip_bf16.h>

typedef unsigned u32;
typedef unsigned short u16;
typedef unsigned long long u64;
typedef u32 u32x4 __attribute__((ext_vector_type(4)));
typedef u32 u32x8 __attribute__((ext_vector_type(8)));
typedef u32 u32x16 __attribute__((ext_vector_type(16)));
typedef _Float16 f16x2 __attribute__((ext_vector_type(2)));
typedef __bf16 bf16x2 __attribute__((ext_vector_type(2)));
typedef float f32x2 __attribute__((ext_vector_type(2)));
#define DI __device__ __forceinline__
#define LDSP(T, off) ((__attribute__((address_space(3))) T*)(smem3 + (off)))

constexpr int IMG = 512;
constexpr int CH = 96;
constexpr int HID = 384;
constexpr int PXB = 192;
constexpr int ROWB = 98304;
constexpr int TOEP_CS = 336;
constexpr int XT_RS = 320;
constexpr int XT_PP = 1360;
constexpr int XT_CG = 6752;
constexpr int GS = 6160;
constexpr int L_LUT = 0;
constexpr int L_TOEP = 8208;
constexpr int L_W = 40960;
constexpr int L_LUTN = 65536;
constexpr int L_XT = 81920;
constexpr int L_H = 108928;
constexpr int L_EX = 133568;
constexpr int XT_Q = 27024;
constexpr int L_MISC = 162992;
constexpr int L_TOTAL = 163056;
constexpr int LUTK = 106;
constexpr u32 XBYTES = IMG * IMG * PXB;
constexpr float GAMMA = -0.4684938781597355f;
constexpr float GC1 = -2.302208198144325f, GC3 = -0.1029432395800235f;
constexpr u32 MU_PK = 0x44ea44eau, CC_PK = 0xb77fb77fu;
__device__ const u32 CTAB[9 * 1024] = {
3722283144u,52428,108928,115088,3722283144u,56797,108944,115104,3722283144u,61166,108960,115120,3722283144u,52428,108976,115136,3722283144u,52428,108992,115152,3722283144u,56797,109008,115168,3722283144u,61166,109024,115184,3722283144u,52428,109040,115200,3722283144u,52428,109056,115216,3722283144u,56797,109072,115232,3722283144u,61166,109088,115248,3722283144u,52428,109104,115264,3722283144u,52428,109120,115280,3722283144u,56797,109136,115296,3722283144u,61166,109152,115312,3722283144u,52428,109168,115328,
3722283144u,52428,109376,115536,3722283144u,56797,109392,115552,3722283144u,61166,109408,115568,3722283144u,52428,109424,115584,3722283144u,52428,109312,115472,3722283144u,56797,109328,115488,3722283144u,61166,109344,115504,3722283144u,52428,109360,115520,3722283144u,52428,109248,115408,3722283144u,56797,109264,115424,3722283144u,61166,109280,115440,3722283144u,52428,109296,115456,3722283144u,52428,109184,115344,3722283144u,56797,109200,115360,3722283144u,61166,109216,115376,3722283144u,52428,109232,115392,
3722283144u,52428,109504,115664,3722283144u,56797,109520,115680,3722283144u,61166,109536,115696,3722283144u,52428,109552,115712,3722283144u,52428,109440,115600,3722283144u,56797,109456,115616,3722283144u,61166,109472,115632,3722283144u,52428,109488,115648,3722283144u,52428,109632,115792,3722283144u,56797,109648,115808,3722283144u,61166,109664,115824,3722283144u,52428,109680,115840,3722283144u,52428,109568,115728,3722283144u,56797,109584,115744,3722283144u,61166,109600,115760,3722283144u,52428,109616,115776,
3722283144u,52428,109824,115984,3722283144u,56797,109840,116000,3722283144u,61166,109856,116016,3722283144u,52428,109872,116032,3722283144u,52428,109888,116048,3722283144u,56797,109904,116064,3722283144u,61166,109920,116080,3722283144u,52428,109936,116096,3722283144u,52428,109696,115856,3722283144u,56797,109712,115872,3722283144u,61166,109728,115888,3722283144u,52428,109744,115904,3722283144u,52428,109760,115920,3722283144u,56797,109776,115936,3722283144u,61166,109792,115952,3722283144u,52428,109808,115968,
3722283144u,52428,115088,108928,3722283144u,56797,115104,108944,3722283144u,61166,115120,108960,3722283144u,52428,115136,108976,3722283144u,52428,115152,108992,3722283144u,56797,115168,109008,3722283144u,61166,115184,109024,3722283144u,52428,115200,109040,3722283144u,52428,115216,109056,3722283144u,56797,115232,109072,3722283144u,61166,115248,109088,3722283144u,52428,115264,109104,3722283144u,52428,115280,109120,3722283144u,56797,115296,109136,3722283144u,61166,115312,109152,3722283144u,52428,115328,109168,
3722283144u,52428,115536,109376,3722283144u,56797,115552,109392,3722283144u,61166,115568,109408,3722283144u,52428,115584,109424,3722283144u,52428,115472,109312,3722283144u,56797,115488,109328,3722283144u,61166,115504,109344,3722283144u,52428,115520,109360,3722283144u,52428,115408,109248,3722283144u,56797,115424,109264,3722283144u,61166,115440,109280,3722283144u,52428,115456,109296,3722283144u,52428,115344,109184,3722283144u,56797,115360,109200,3722283144u,61166,115376,109216,3722283144u,52428,115392,109232,
3722283144u,52428,115664,109504,3722283144u,56797,115680,109520,3722283144u,61166,115696,109536,3722283144u,52428,115712,109552,3722283144u,52428,115600,109440,3722283144u,56797,115616,109456,3722283144u,61166,115632,109472,3722283144u,52428,115648,109488,3722283144u,52428,115792,109632,3722283144u,56797,115808,109648,3722283144u,61166,115824,109664,3722283144u,52428,115840,109680,3722283144u,52428,115728,109568,3722283144u,56797,115744,109584,3722283144u,61166,115760,109600,3722283144u,52428,115776,109616,
3722283144u,52428,115984,109824,3722283144u,56797,116000,109840,3722283144u,61166,116016,109856,3722283144u,52428,116032,109872,3722283144u,52428,116048,109888,3722283144u,56797,116064,109904,3722283144u,61166,116080,109920,3722283144u,52428,116096,109936,3722283144u,52428,115856,109696,3722283144u,56797,115872,109712,3722283144u,61166,115888,109728,3722283144u,52428,115904,109744,3722283144u,52428,115920,109760,3722283144u,56797,115936,109776,3722283144u,61166,115952,109792,3722283144u,52428,115968,109808,
3722283144u,52428,121248,127408,3722283144u,56797,121264,127424,3722283144u,61166,121280,127440,3722283144u,52428,121296,127456,3722283144u,52428,121312,127472,3722283144u,56797,121328,127488,3722283144u,61166,121344,127504,3722283144u,52428,121360,127520,3722283144u,52428,121376,127536,3722283144u,56797,121392,127552,3722283144u,61166,121408,127568,3722283144u,52428,121424,127584,3722283144u,52428,121440,127600,3722283144u,56797,121456,127616,3722283144u,61166,121472,127632,3722283144u,52428,121488,127648,
3722283144u,52428,121696,127856,3722283144u,56797,121712,127872,3722283144u,61166,121728,127888,3722283144u,52428,121744,127904,3722283144u,52428,121632,127792,3722283144u,56797,121648,127808,3722283144u,61166,121664,127824,3722283144u,52428,121680,127840,3722283144u,52428,121568,127728,3722283144u,56797,121584,127744,3722283144u,61166,121600,127760,3722283144u,52428,121616,127776,3722283144u,52428,121504,127664,3722283144u,56797,121520,127680,3722283144u,61166,121536,127696,3722283144u,52428,121552,127712,
3722283144u,52428,121824,127984,3722283144u,56797,121840,128000,3722283144u,61166,121856,128016,3722283144u,52428,121872,128032,3722283144u,52428,121760,127920,3722283144u,56797,121776,127936,3722283144u,61166,121792,127952,3722283144u,52428,121808,127968,3722283144u,52428,121952,128112,3722283144u,56797,121968,128128,3722283144u,61166,121984,128144,3722283144u,52428,122000,128160,3722283144u,52428,121888,128048,3722283144u,56797,121904,128064,3722283144u,61166,121920,128080,3722283144u,52428,121936,128096,
3722283144u,52428,122144,128304,3722283144u,56797,122160,128320,3722283144u,61166,122176,128336,3722283144u,52428,122192,128352,3722283144u,52428,122208,128368,3722283144u,56797,122224,128384,3722283144u,61166,122240,128400,3722283144u,52428,122256,128416,3722283144u,52428,122016,128176,3722283144u,56797,122032,128192,3722283144u,61166,122048,128208,3722283144u,52428,122064,128224,3722283144u,52428,122080,128240,3722283144u,56797,122096,128256,3722283144u,61166,122112,128272,3722283144u,52428,122128,128288,
3722283144u,52428,127408,121248,3722283144u,56797,127424,121264,3722283144u,61166,127440,121280,3722283144u,52428,127456,121296,3722283144u,52428,127472,121312,3722283144u,56797,127488,121328,3722283144u,61166,127504,121344,3722283144u,52428,127520,121360,3722283144u,52428,127536,121376,3722283144u,56797,127552,121392,3722283144u,61166,127568,121408,3722283144u,52428,127584,121424,3722283144u,52428,127600,121440,3722283144u,56797,127616,121456,3722283144u,61166,127632,121472,3722283144u,52428,127648,121488,
3722283144u,52428,127856,121696,3722283144u,56797,127872,121712,3722283144u,61166,127888,121728,3722283144u,52428,127904,121744,3722283144u,52428,127792,121632,3722283144u,56797,127808,121648,3722283144u,61166,127824,121664,3722283144u,52428,127840,121680,3722283144u,52428,127728,121568,3722283144u,56797,127744,121584,3722283144u,61166,127760,121600,3722283144u,52428,127776,121616,3722283144u,52428,127664,121504,3722283144u,56797,127680,121520,3722283144u,61166,127696,121536,3722283144u,52428,127712,121552,
3722283144u,52428,127984,121824,3722283144u,56797,128000,121840,3722283144u,61166,128016,121856,3722283144u,52428,128032,121872,3722283144u,52428,127920,121760,3722283144u,56797,127936,121776,3722283144u,61166,127952,121792,3722283144u,52428,127968,121808,3722283144u,52428,128112,121952,3722283144u,56797,128128,121968,3722283144u,61166,128144,121984,3722283144u,52428,128160,122000,3722283144u,52428,128048,121888,3722283144u,56797,128064,121904,3722283144u,61166,128080,121920,3722283144u,52428,128096,121936,
3722283144u,52428,128304,122144,3722283144u,56797,128320,122160,3722283144u,61166,128336,122176,3722283144u,52428,128352,122192,3722283144u,52428,128368,122208,3722283144u,56797,128384,122224,3722283144u,61166,128400,122240,3722283144u,52428,128416,122256,3722283144u,52428,128176,122016,3722283144u,56797,128192,122032,3722283144u,61166,128208,122048,3722283144u,52428,128224,122064,3722283144u,52428,128240,122080,3722283144u,56797,128256,122096,3722283144u,61166,128272,122112,3722283144u,52428,128288,122128,
40960,393216,2147483648u,3072,40976,393408,2147483664u,3088,40992,393600,2147483680u,3104,41008,393792,2147483696u,3120,41024,393984,2147483840u,3264,41040,394176,2147483856u,3280,41056,394368,2147483872u,3296,41072,394560,2147483888u,3312,41088,394752,384,1073745280,41104,394944,400,1073745296,41120,395136,416,1073745312,41136,395328,432,1073745328,41152,395520,576,1073745472,41168,395712,592,1073745488,41184,395904,608,1073745504,41200,396096,624,1073745520,
41216,393232,768,396288,41232,393424,784,396304,41248,393616,800,396320,41264,393808,816,396336,41280,394000,960,396480,41296,394192,976,396496,41312,394384,992,396512,41328,394576,1008,396528,41344,394768,1152,1074138496,41360,394960,1168,1074138512,41376,395152,1184,1074138528,41392,395344,1200,1074138544,41408,395536,1344,1074138688,41424,395728,1360,1074138704,41440,395920,1376,1074138720,41456,396112,1392,1074138736,
41472,393248,1536,789504,41488,393440,1552,789520,41504,393632,1568,789536,41520,393824,1584,789552,41536,394016,1728,789696,41552,394208,1744,789712,41568,394400,1760,789728,41584,394592,1776,789744,41600,394784,1920,1074531712,41616,394976,1936,1074531728,41632,395168,1952,1074531744,41648,395360,1968,1074531760,41664,395552,2112,1074531904,41680,395744,2128,1074531920,41696,395936,2144,1074531936,41712,396128,2160,1074531952,
41728,393264,2304,1182720,41744,393456,2320,1182736,41760,393648,2336,1182752,41776,393840,2352,1182768,41792,394032,2496,1182912,41808,394224,2512,1182928,41824,394416,2528,1182944,41840,394608,2544,1182960,41856,394800,2688,1074924928,41872,394992,2704,1074924944,41888,395184,2720,1074924960,41904,395376,2736,1074924976,41920,395568,2880,1074925120,41936,395760,2896,1074925136,41952,395952,2912,1074925152,41968,396144,2928,1074925168,
41984,0,2147581952u,101376,42000,192,2147581968u,101392,42016,384,2147581984u,101408,42032,576,2147582000u,101424,42048,768,2147582144u,101568,42064,960,2147582160u,101584,42080,1152,2147582176u,101600,42096,1344,2147582192u,101616,42112,1536,98688,1073843584,42128,1728,98704,1073843600,42144,1920,98720,1073843616,42160,2112,98736,1073843632,42176,2304,98880,1073843776,42192,2496,98896,1073843792,42208,2688,98912,1073843808,42224,2880,98928,1073843824,
42240,16,99072,494592,42256,208,99088,494608,42272,400,99104,494624,42288,592,99120,494640,42304,784,99264,494784,42320,976,99280,494800,42336,1168,99296,494816,42352,1360,99312,494832,42368,1552,99456,1074236800,42384,1744,99472,1074236816,42400,1936,99488,1074236832,42416,2128,99504,1074236848,42432,2320,99648,1074236992,42448,2512,99664,1074237008,42464,2704,99680,1074237024,42480,2896,99696,1074237040,
42496,32,99840,887808,42512,224,99856,887824,42528,416,99872,887840,42544,608,99888,887856,42560,800,100032,888000,42576,992,100048,888016,42592,1184,100064,888032,42608,1376,100080,888048,42624,1568,100224,1074630016,42640,1760,100240,1074630032,42656,1952,100256,1074630048,42672,2144,100272,1074630064,42688,2336,100416,1074630208,42704,2528,100432,1074630224,42720,2720,100448,1074630240,42736,2912,100464,1074630256,
42752,48,100608,1281024,42768,240,100624,1281040,42784,432,100640,1281056,42800,624,100656,1281072,42816,816,100800,1281216,42832,1008,100816,1281232,42848,1200,100832,1281248,42864,1392,100848,1281264,42880,1584,100992,1075023232,42896,1776,101008,1075023248,42912,1968,101024,1075023264,42928,2160,101040,1075023280,42944,2352,101184,1075023424,42960,2544,101200,1075023440,42976,2736,101216,1075023456,42992,2928,101232,1075023472,
40960,1179648,2147680256u,199680,40976,1179840,2147680272u,199696,40992,1180032,2147680288u,199712,41008,1180224,2147680304u,199728,41024,1180416,2147680448u,199872,41040,1180608,2147680464u,199888,41056,1180800,2147680480u,199904,41072,1180992,2147680496u,199920,41088,1181184,196992,1073941888,41104,1181376,197008,1073941904,41120,1181568,197024,1073941920,41136,1181760,197040,1073941936,41152,1181952,197184,1073942080,41168,1182144,197200,1073942096,41184,1182336,197216,1073942112,41200,1182528,197232,1073942128,
41216,1179664,197376,592896,41232,1179856,197392,592912,41248,1180048,197408,592928,41264,1180240,197424,592944,41280,1180432,197568,593088,41296,1180624,197584,593104,41312,1180816,197600,593120,41328,1181008,197616,593136,41344,1181200,197760,1074335104,41360,1181392,197776,1074335120,41376,1181584,197792,1074335136,41392,1181776,197808,1074335152,41408,1181968,197952,1074335296,41424,1182160,197968,1074335312,41440,1182352,197984,1074335328,41456,1182544,198000,1074335344,
41472,1179680,198144,986112,41488,1179872,198160,986128,41504,1180064,198176,986144,41520,1180256,198192,986160,41536,1180448,198336,986304,41552,1180640,198352,986320,41568,1180832,198368,986336,41584,1181024,198384,986352,41600,1181216,198528,1074728320,41616,1181408,198544,1074728336,41632,1181600,198560,1074728352,41648,1181792,198576,1074728368,41664,1181984,198720,1074728512,41680,1182176,198736,1074728528,41696,1182368,198752,1074728544,41712,1182560,198768,1074728560,
41728,1179696,198912,1379328,41744,1179888,198928,1379344,41760,1180080,198944,1379360,41776,1180272,198960,1379376,41792,1180464,199104,1379520,41808,1180656,199120,1379536,41824,1180848,199136,1379552,41840,1181040,199152,1379568,41856,1181232,199296,1075121536,41872,1181424,199312,1075121552,41888,1181616,199328,1075121568,41904,1181808,199344,1075121584,41920,1182000,199488,1075121728,41936,1182192,199504,1075121744,41952,1182384,199520,1075121760,41968,1182576,199536,1075121776,
41984,786432,2147778560u,297984,42000,786624,2147778576u,298000,42016,786816,2147778592u,298016,42032,787008,2147778608u,298032,42048,787200,2147778752u,298176,42064,787392,2147778768u,298192,42080,787584,2147778784u,298208,42096,787776,2147778800u,298224,42112,787968,295296,1074040192,42128,788160,295312,1074040208,42144,788352,295328,1074040224,42160,788544,295344,1074040240,42176,788736,295488,1074040384,42192,788928,295504,1074040400,42208,789120,295520,1074040416,42224,789312,295536,1074040432,
42240,786448,295680,691200,42256,786640,295696,691216,42272,786832,295712,691232,42288,787024,295728,691248,42304,787216,295872,691392,42320,787408,295888,691408,42336,787600,295904,691424,42352,787792,295920,691440,42368,787984,296064,1074433408,42384,788176,296080,1074433424,42400,788368,296096,1074433440,42416,788560,296112,1074433456,42432,788752,296256,1074433600,42448,788944,296272,1074433616,42464,789136,296288,1074433632,42480,789328,296304,1074433648,
42496,786464,296448,1084416,42512,786656,296464,1084432,42528,786848,296480,1084448,42544,787040,296496,1084464,42560,787232,296640,1084608,42576,787424,296656,1084624,42592,787616,296672,1084640,42608,787808,296688,1084656,42624,788000,296832,1074826624,42640,788192,296848,1074826640,42656,788384,296864,1074826656,42672,788576,296880,1074826672,42688,788768,297024,1074826816,42704,788960,297040,1074826832,42720,789152,297056,1074826848,42736,789344,297072,1074826864,
42752,786480,297216,1477632,42768,786672,297232,1477648,42784,786864,297248,1477664,42800,787056,297264,1477680,42816,787248,297408,1477824,42832,787440,297424,1477840,42848,787632,297440,1477856,42864,787824,297456,1477872,42880,788016,297600,1075219840,42896,788208,297616,1075219856,42912,788400,297632,1075219872,42928,788592,297648,1075219888,42944,788784,297792,1075220032,42960,788976,297808,1075220048,42976,789168,297824,1075220064,42992,789360,297840,1075220080,
81920,139728,81920,8208,88672,140176,83280,8192,95424,140304,84640,8192,102176,140624,86000,8192,81924,139744,81936,8224,88676,140192,83296,8864,95428,140320,84656,8864,102180,140640,86016,8864,81928,139760,81968,8240,88680,140208,83328,8864,95432,140336,84688,8864,102184,140656,86048,8864,81932,139776,81952,8256,88684,140224,83312,8192,95436,140352,84672,8192,102188,140672,86032,8192,
81936,139792,81936,8464,88688,140112,83296,8400,95440,140240,84656,8336,102192,140688,86016,8272,81940,139808,81952,8480,88692,140128,83312,8416,95444,140256,84672,8352,102196,140704,86032,8288,81944,139824,81984,8496,88696,140144,83344,8432,95448,140272,84704,8368,102200,140720,86064,8304,81948,139840,81968,8512,88700,140160,83328,8448,95452,140288,84688,8384,102204,140736,86048,8320,
81952,139856,83280,8272,88704,140048,84640,8208,95456,140432,86000,8192,102208,140496,87360,8192,81956,139872,83296,8288,88708,140064,84656,8224,95460,140448,86016,8864,102212,140512,87376,8864,81960,139888,83328,8304,88712,140080,84688,8240,95464,140464,86048,8864,102216,140528,87408,8864,81964,139904,83312,8320,88716,140096,84672,8256,95468,140480,86032,8192,102220,140544,87392,8192,
81968,139920,83296,8864,88720,139984,84656,8464,95472,140368,86016,8400,102224,140560,87376,8336,81972,139936,83312,8192,88724,140000,84672,8480,95476,140384,86032,8416,102228,140576,87392,8352,81976,139952,83344,8192,88728,140016,84704,8496,95480,140400,86064,8432,102232,140592,87424,8368,81980,139968,83328,8864,88732,140032,84688,8512,95484,140416,86048,8448,102236,140608,87408,8384,
82240,133568,88672,10896,88992,134016,90032,10880,95744,134144,91392,10880,102496,134464,92752,10880,82244,133584,88688,10912,88996,134032,90048,11552,95748,134160,91408,11552,102500,134480,92768,11552,82248,133600,88720,10928,89000,134048,90080,11552,95752,134176,91440,11552,102504,134496,92800,11552,82252,133616,88704,10944,89004,134064,90064,10880,95756,134192,91424,10880,102508,134512,92784,10880,
82256,133632,88688,11152,89008,133952,90048,11088,95760,134080,91408,11024,102512,134528,92768,10960,82260,133648,88704,11168,89012,133968,90064,11104,95764,134096,91424,11040,102516,134544,92784,10976,82264,133664,88736,11184,89016,133984,90096,11120,95768,134112,91456,11056,102520,134560,92816,10992,82268,133680,88720,11200,89020,134000,90080,11136,95772,134128,91440,11072,102524,134576,92800,11008,
82272,133696,90032,10960,89024,133888,91392,10896,95776,134272,92752,10880,102528,134336,94112,10880,82276,133712,90048,10976,89028,133904,91408,10912,95780,134288,92768,11552,102532,134352,94128,11552,82280,133728,90080,10992,89032,133920,91440,10928,95784,134304,92800,11552,102536,134368,94160,11552,82284,133744,90064,11008,89036,133936,91424,10944,95788,134320,92784,10880,102540,134384,94144,10880,
82288,133760,90048,11552,89040,133824,91408,11152,95792,134208,92768,11088,102544,134400,94128,11024,82292,133776,90064,10880,89044,133840,91424,11168,95796,134224,92784,11104,102548,134416,94144,11040,82296,133792,90096,10880,89048,133856,91456,11184,95800,134240,92816,11120,102552,134432,94176,11056,82300,133808,90080,11552,89052,133872,91440,11200,95804,134256,92800,11136,102556,134448,94160,11072,
82560,152048,95424,13584,89312,152496,96784,13568,96064,152624,98144,13568,102816,152944,99504,13568,82564,152064,95440,13600,89316,152512,96800,14240,96068,152640,98160,14240,102820,152960,99520,14240,82568,152080,95472,13616,89320,152528,96832,14240,96072,152656,98192,14240,102824,152976,99552,14240,82572,152096,95456,13632,89324,152544,96816,13568,96076,152672,98176,13568,102828,152992,99536,13568,
82576,152112,95440,13840,89328,152432,96800,13776,96080,152560,98160,13712,102832,153008,99520,13648,82580,152128,95456,13856,89332,152448,96816,13792,96084,152576,98176,13728,102836,153024,99536,13664,82584,152144,95488,13872,89336,152464,96848,13808,96088,152592,98208,13744,102840,153040,99568,13680,82588,152160,95472,13888,89340,152480,96832,13824,96092,152608,98192,13760,102844,153056,99552,13696,
82592,152176,96784,13648,89344,152368,98144,13584,96096,152752,99504,13568,102848,152816,100864,13568,82596,152192,96800,13664,89348,152384,98160,13600,96100,152768,99520,14240,102852,152832,100880,14240,82600,152208,96832,13680,89352,152400,98192,13616,96104,152784,99552,14240,102856,152848,100912,14240,82604,152224,96816,13696,89356,152416,98176,13632,96108,152800,99536,13568,102860,152864,100896,13568,
82608,152240,96800,14240,89360,152304,98160,13840,96112,152688,99520,13776,102864,152880,100880,13712,82612,152256,96816,13568,89364,152320,98176,13856,96116,152704,99536,13792,102868,152896,100896,13728,82616,152272,96848,13568,89368,152336,98208,13872,96120,152720,99568,13808,102872,152912,100928,13744,82620,152288,96832,14240,89372,152352,98192,13888,96124,152736,99552,13824,102876,152928,100912,13760,
82880,145888,102176,16272,89632,146336,103536,16256,96384,146464,104896,16256,103136,146784,106256,16256,82884,145904,102192,16288,89636,146352,103552,16928,96388,146480,104912,16928,103140,146800,106272,16928,82888,145920,102224,16304,89640,146368,103584,16928,96392,146496,104944,16928,103144,146816,106304,16928,82892,145936,102208,16320,89644,146384,103568,16256,96396,146512,104928,16256,103148,146832,106288,16256,
82896,145952,102192,16528,89648,146272,103552,16464,96400,146400,104912,16400,103152,146848,106272,16336,82900,145968,102208,16544,89652,146288,103568,16480,96404,146416,104928,16416,103156,146864,106288,16352,82904,145984,102240,16560,89656,146304,103600,16496,96408,146432,104960,16432,103160,146880,106320,16368,82908,146000,102224,16576,89660,146320,103584,16512,96412,146448,104944,16448,103164,146896,106304,16384,
82912,146016,103536,16336,89664,146208,104896,16272,96416,146592,106256,16256,103168,146656,107616,16256,82916,146032,103552,16352,89668,146224,104912,16288,96420,146608,106272,16928,103172,146672,107632,16928,82920,146048,103584,16368,89672,146240,104944,16304,96424,146624,106304,16928,103176,146688,107664,16928,82924,146064,103568,16384,89676,146256,104928,16320,96428,146640,106288,16256,103180,146704,107648,16256,
82928,146080,103552,16928,89680,146144,104912,16528,96432,146528,106272,16464,103184,146720,107632,16400,82932,146096,103568,16256,89684,146160,104928,16544,96436,146544,106288,16480,103188,146736,107648,16416,82936,146112,103600,16256,89688,146176,104960,16560,96440,146560,106320,16496,103192,146752,107680,16432,82940,146128,103584,16928,89692,146192,104944,16576,96444,146576,106304,16512,103196,146768,107664,16448,
8336,108928,81984,393216,8272,115088,88736,393232,8208,121248,95488,393248,8864,127408,102240,393264,8352,108992,81988,393408,8288,115152,88740,393424,8224,121312,95492,393440,8528,127472,102244,393456,8368,109120,81992,393600,8304,115280,88744,393616,8240,121440,95496,393632,8528,127600,102248,393648,8384,109056,81996,393792,8320,115216,88748,393808,8256,121376,95500,393824,8864,127536,102252,393840,
8528,108944,83344,393984,8528,115104,90096,394000,8464,121264,96848,394016,8400,127424,103600,394032,8864,109008,83348,394176,8864,115168,90100,394192,8480,121328,96852,394208,8416,127488,103604,394224,8864,109136,83352,394368,8864,115296,90104,394384,8496,121456,96856,394400,8432,127616,103608,394416,8528,109072,83356,394560,8528,115232,90108,394576,8512,121392,96860,394592,8448,127552,103612,394608,
8400,108960,84704,394752,8336,115120,91456,394768,8272,121280,98208,394784,8208,127440,104960,394800,8416,109024,84708,394944,8352,115184,91460,394960,8288,121344,98212,394976,8224,127504,104964,394992,8432,109152,84712,395136,8368,115312,91464,395152,8304,121472,98216,395168,8240,127632,104968,395184,8448,109088,84716,395328,8384,115248,91468,395344,8320,121408,98220,395360,8256,127568,104972,395376,
8528,108976,86064,395520,8528,115136,92816,395536,8528,121296,99568,395552,8464,127456,106320,395568,8864,109040,86068,395712,8864,115200,92820,395728,8864,121360,99572,395744,8480,127520,106324,395760,8864,109168,86072,395904,8864,115328,92824,395920,8864,121488,99576,395936,8496,127648,106328,395952,8528,109104,86076,396096,8528,115264,92828,396112,8528,121424,99580,396128,8512,127584,106332,396144,
11024,109376,82304,0,10960,115536,89056,16,10896,121696,95808,32,11552,127856,102560,48,11040,109312,82308,192,10976,115472,89060,208,10912,121632,95812,224,11216,127792,102564,240,11056,109184,82312,384,10992,115344,89064,400,10928,121504,95816,416,11216,127664,102568,432,11072,109248,82316,576,11008,115408,89068,592,10944,121568,95820,608,11552,127728,102572,624,
11216,109392,83664,768,11216,115552,90416,784,11152,121712,97168,800,11088,127872,103920,816,11552,109328,83668,960,11552,115488,90420,976,11168,121648,97172,992,11104,127808,103924,1008,11552,109200,83672,1152,11552,115360,90424,1168,11184,121520,97176,1184,11120,127680,103928,1200,11216,109264,83676,1344,11216,115424,90428,1360,11200,121584,97180,1376,11136,127744,103932,1392,
11088,109408,85024,1536,11024,115568,91776,1552,10960,121728,98528,1568,10896,127888,105280,1584,11104,109344,85028,1728,11040,115504,91780,1744,10976,121664,98532,1760,10912,127824,105284,1776,11120,109216,85032,1920,11056,115376,91784,1936,10992,121536,98536,1952,10928,127696,105288,1968,11136,109280,85036,2112,11072,115440,91788,2128,11008,121600,98540,2144,10944,127760,105292,2160,
11216,109424,86384,2304,11216,115584,93136,2320,11216,121744,99888,2336,11152,127904,106640,2352,11552,109360,86388,2496,11552,115520,93140,2512,11552,121680,99892,2528,11168,127840,106644,2544,11552,109232,86392,2688,11552,115392,93144,2704,11552,121552,99896,2720,11184,127712,106648,2736,11216,109296,86396,2880,11216,115456,93148,2896,11216,121616,99900,2912,11200,127776,106652,2928,
13712,109504,82624,1179648,13648,115664,89376,1179664,13584,121824,96128,1179680,14240,127984,102880,1179696,13728,109440,82628,1179840,13664,115600,89380,1179856,13600,121760,96132,1179872,13904,127920,102884,1179888,13744,109568,82632,1180032,13680,115728,89384,1180048,13616,121888,96136,1180064,13904,128048,102888,1180080,13760,109632,82636,1180224,13696,115792,89388,1180240,13632,121952,96140,1180256,14240,128112,102892,1180272,
13904,109520,83984,1180416,13904,115680,90736,1180432,13840,121840,97488,1180448,13776,128000,104240,1180464,14240,109456,83988,1180608,14240,115616,90740,1180624,13856,121776,97492,1180640,13792,127936,104244,1180656,14240,109584,83992,1180800,14240,115744,90744,1180816,13872,121904,97496,1180832,13808,128064,104248,1180848,13904,109648,83996,1180992,13904,115808,90748,1181008,13888,121968,97500,1181024,13824,128128,104252,1181040,
13776,109536,85344,1181184,13712,115696,92096,1181200,13648,121856,98848,1181216,13584,128016,105600,1181232,13792,109472,85348,1181376,13728,115632,92100,1181392,13664,121792,98852,1181408,13600,127952,105604,1181424,13808,109600,85352,1181568,13744,115760,92104,1181584,13680,121920,98856,1181600,13616,128080,105608,1181616,13824,109664,85356,1181760,13760,115824,92108,1181776,13696,121984,98860,1181792,13632,128144,105612,1181808,
13904,109552,86704,1181952,13904,115712,93456,1181968,13904,121872,100208,1181984,13840,128032,106960,1182000,14240,109488,86708,1182144,14240,115648,93460,1182160,14240,121808,100212,1182176,13856,127968,106964,1182192,14240,109616,86712,1182336,14240,115776,93464,1182352,14240,121936,100216,1182368,13872,128096,106968,1182384,13904,109680,86716,1182528,13904,115840,93468,1182544,13904,122000,100220,1182560,13888,128160,106972,1182576,
16400,109824,82944,786432,16336,115984,89696,786448,16272,122144,96448,786464,16928,128304,103200,786480,16416,109888,82948,786624,16352,116048,89700,786640,16288,122208,96452,786656,16592,128368,103204,786672,16432,109760,82952,786816,16368,115920,89704,786832,16304,122080,96456,786848,16592,128240,103208,786864,16448,109696,82956,787008,16384,115856,89708,787024,16320,122016,96460,787040,16928,128176,103212,787056,
16592,109840,84304,787200,16592,116000,91056,787216,16528,122160,97808,787232,16464,128320,104560,787248,16928,109904,84308,787392,16928,116064,91060,787408,16544,122224,97812,787424,16480,128384,104564,787440,16928,109776,84312,787584,16928,115936,91064,787600,16560,122096,97816,787616,16496,128256,104568,787632,16592,109712,84316,787776,16592,115872,91068,787792,16576,122032,97820,787808,16512,128192,104572,787824,
16464,109856,85664,787968,16400,116016,92416,787984,16336,122176,99168,788000,16272,128336,105920,788016,16480,109920,85668,788160,16416,116080,92420,788176,16352,122240,99172,788192,16288,128400,105924,788208,16496,109792,85672,788352,16432,115952,92424,788368,16368,122112,99176,788384,16304,128272,105928,788400,16512,109728,85676,788544,16448,115888,92428,788560,16384,122048,99180,788576,16320,128208,105932,788592,
16592,109872,87024,788736,16592,116032,93776,788752,16592,122192,100528,788768,16528,128352,107280,788784,16928,109936,87028,788928,16928,116096,93780,788944,16928,122256,100532,788960,16544,128416,107284,788976,16928,109808,87032,789120,16928,115968,93784,789136,16928,122128,100536,789152,16560,128288,107288,789168,16592,109744,87036,789312,16592,115904,93788,789328,16592,122064,100540,789344,16576,128224,107292,789360,
81920,40960,40960,40960,88672,40976,40976,40968,95424,40992,40992,40976,102176,41008,41008,40984,108944,41024,41152,41760,115696,41040,41168,41768,122448,41056,41184,41776,129200,41072,41200,41784,135968,41088,41344,42560,142720,41104,41360,42568,149472,41120,41376,42576,156224,41136,41392,42584,81924,41152,41536,43360,88676,41168,41552,43368,95428,41184,41568,43376,102180,41200,41584,43384,
108948,41760,41728,44160,115700,41776,41744,44168,122452,41728,41760,44176,129204,41744,41776,44184,135972,41824,41920,44960,142724,41840,41936,44968,149476,41792,41952,44976,156228,41808,41968,44984,81928,41888,42112,45760,88680,41904,42128,45768,95432,41856,42144,45776,102184,41872,42160,45784,108952,41952,42304,46560,115704,41968,42320,46568,122456,41920,42336,46576,129208,41936,42352,46584,
135976,42560,42496,47104,142728,42576,42512,47112,149480,42592,42528,47120,156232,42608,42544,47128,81932,42496,42688,47904,88684,42512,42704,47912,95436,42528,42720,47920,102188,42544,42736,47928,108956,42688,42880,48704,115708,42704,42896,48712,122460,42720,42912,48720,129212,42736,42928,48728,135980,42624,43072,49504,142732,42640,43088,49512,149484,42656,43104,49520,156236,42672,43120,49528,
81936,43360,43264,50304,88688,43376,43280,50312,95440,43328,43296,50320,102192,43344,43312,50328,108960,43296,43456,51104,115712,43312,43472,51112,122464,43264,43488,51120,129216,43280,43504,51128,135984,43488,43648,51904,142736,43504,43664,51912,149488,43456,43680,51920,156240,43472,43696,51928,81940,43424,43840,52704,88692,43440,43856,52712,95444,43392,43872,52720,102196,43408,43888,52728,
108964,44160,59392,41344,115716,44176,59408,41352,122468,44192,59424,41360,129220,44208,59440,41368,135988,44224,59584,42144,142740,44240,59600,42152,149492,44256,59616,42160,156244,44272,59632,42168,81944,44032,59776,42944,88696,44048,59792,42952,95448,44064,59808,42960,102200,44080,59824,42968,108968,44096,59968,43744,115720,44112,59984,43752,122472,44128,60000,43760,129224,44144,60016,43768,
135992,44960,60160,44288,142744,44976,60176,44296,149496,44928,60192,44304,156248,44944,60208,44312,81948,45024,60352,45088,88700,45040,60368,45096,95452,44992,60384,45104,102204,45008,60400,45112,108972,44832,60544,45888,115724,44848,60560,45896,122476,44800,60576,45904,129228,44816,60592,45912,135996,44896,60736,46688,142748,44912,60752,46696,149500,44864,60768,46704,156252,44880,60784,46712,
81952,45760,60928,47488,88704,45776,60944,47496,95456,45792,60960,47504,102208,45808,60976,47512,108976,45696,61120,48288,115728,45712,61136,48296,122480,45728,61152,48304,129232,45744,61168,48312,136000,45632,61312,49088,142752,45648,61328,49096,149504,45664,61344,49104,156256,45680,61360,49112,81956,45568,61504,49888,88708,45584,61520,49896,95460,45600,61536,49904,102212,45616,61552,49912,
108980,46560,61696,50432,115732,46576,61712,50440,122484,46528,61728,50448,129236,46544,61744,50456,136004,46496,61888,51232,142756,46512,61904,51240,149508,46464,61920,51248,156260,46480,61936,51256,81960,46432,62080,52032,88712,46448,62096,52040,95464,46400,62112,52048,102216,46416,62128,52056,108984,46368,62272,52832,115736,46384,62288,52840,122488,46336,62304,52848,129240,46352,62320,52856,
136008,47104,40968,40960,142760,47120,40984,40968,149512,47136,41000,40976,156264,47152,41016,40984,81964,47168,41160,41760,88716,47184,41176,41768,95468,47200,41192,41776,102220,47216,41208,41784,108988,47232,41352,42560,115740,47248,41368,42568,122492,47264,41384,42576,129244,47280,41400,42584,136012,47296,41544,43360,142764,47312,41560,43368,149516,47328,41576,43376,156268,47344,41592,43384,
81968,47904,41736,44160,88720,47920,41752,44168,95472,47872,41768,44176,102224,47888,41784,44184,108992,47968,41928,44960,115744,47984,41944,44968,122496,47936,41960,44976,129248,47952,41976,44984,136016,48032,42120,45760,142768,48048,42136,45768,149520,48000,42152,45776,156272,48016,42168,45784,81972,48096,42312,46560,88724,48112,42328,46568,95476,48064,42344,46576,102228,48080,42360,46584,
108996,48704,42504,47104,115748,48720,42520,47112,122500,48736,42536,47120,129252,48752,42552,47128,136020,48640,42696,47904,142772,48656,42712,47912,149524,48672,42728,47920,156276,48688,42744,47928,81976,48832,42888,48704,88728,48848,42904,48712,95480,48864,42920,48720,102232,48880,42936,48728,109000,48768,43080,49504,115752,48784,43096,49512,122504,48800,43112,49520,129256,48816,43128,49528,
136024,49504,43272,50304,142776,49520,43288,50312,149528,49472,43304,50320,156280,49488,43320,50328,81980,49440,43464,51104,88732,49456,43480,51112,95484,49408,43496,51120,102236,49424,43512,51128,109004,49632,43656,51904,115756,49648,43672,51912,122508,49600,43688,51920,129260,49616,43704,51928,136028,49568,43848,52704,142780,49584,43864,52712,149532,49536,43880,52720,156284,49552,43896,52728,
81984,50304,59400,41344,88736,50320,59416,41352,95488,50336,59432,41360,102240,50352,59448,41368,109008,50368,59592,42144,115760,50384,59608,42152,122512,50400,59624,42160,129264,50416,59640,42168,136032,50176,59784,42944,142784,50192,59800,42952,149536,50208,59816,42960,156288,50224,59832,42968,81988,50240,59976,43744,88740,50256,59992,43752,95492,50272,60008,43760,102244,50288,60024,43768,
109012,51104,60168,44288,115764,51120,60184,44296,122516,51072,60200,44304,129268,51088,60216,44312,136036,51168,60360,45088,142788,51184,60376,45096,149540,51136,60392,45104,156292,51152,60408,45112,81992,50976,60552,45888,88744,50992,60568,45896,95496,50944,60584,45904,102248,50960,60600,45912,109016,51040,60744,46688,115768,51056,60760,46696,122520,51008,60776,46704,129272,51024,60792,46712,
136040,51904,60936,47488,142792,51920,60952,47496,149544,51936,60968,47504,156296,51952,60984,47512,81996,51840,61128,48288,88748,51856,61144,48296,95500,51872,61160,48304,102252,51888,61176,48312,109020,51776,61320,49088,115772,51792,61336,49096,122524,51808,61352,49104,129276,51824,61368,49112,136044,51712,61512,49888,142796,51728,61528,49896,149548,51744,61544,49904,156300,51760,61560,49912,
156300,52704,61704,50432,156300,52720,61720,50440,156300,52672,61736,50448,156300,52688,61752,50456,156300,52640,61896,51232,156300,52656,61912,51240,156300,52608,61928,51248,156300,52624,61944,51256,156300,52576,62088,52032,156300,52592,62104,52040,156300,52544,62120,52048,156300,52560,62136,52056,156300,52512,62280,52832,156300,52528,62296,52840,156300,52480,62312,52848,156300,52496,62328,52856,
40960,8208,8192,41088,40976,8880,8528,41096,40992,9552,8864,41104,41008,10224,9200,41112,41152,10896,9536,41888,41168,11568,9872,41896,41184,12240,10208,41904,41200,12912,10544,41912,41344,13584,10880,42688,41360,14256,11216,42696,41376,14928,11552,42704,41392,15600,11888,42712,41536,16272,12224,43488,41552,16944,12560,43496,41568,17616,12896,43504,41584,18288,13232,43512,
41728,18960,13568,44032,41744,19632,13904,44040,41760,20304,14240,44048,41776,20976,14576,44056,41920,21648,14912,44832,41936,22320,15248,44840,41952,22992,15584,44848,41968,23664,15920,44856,42112,24336,16256,45632,42128,25008,16592,45640,42144,25680,16928,45648,42160,26352,17264,45656,42304,27024,17600,46432,42320,27696,17936,46440,42336,28368,18272,46448,42352,29040,18608,46456,
42496,29712,18944,47232,42512,30384,19280,47240,42528,31056,19616,47248,42544,31728,19952,47256,42688,32400,20288,48032,42704,33072,20624,48040,42720,33744,20960,48048,42736,34416,21296,48056,42880,35088,21632,48832,42896,35760,21968,48840,42912,36432,22304,48848,42928,37104,22640,48856,43072,37776,22976,49632,43088,38448,23312,49640,43104,39120,23648,49648,43120,39792,23984,49656,
43264,8272,24320,50176,43280,8944,24656,50184,43296,9616,24992,50192,43312,10288,25328,50200,43456,10960,25664,50976,43472,11632,26000,50984,43488,12304,26336,50992,43504,12976,26672,51000,43648,13648,27008,51776,43664,14320,27344,51784,43680,14992,27680,51792,43696,15664,28016,51800,43840,16336,28352,52576,43856,17008,28688,52584,43872,17680,29024,52592,43888,18352,29360,52600,
59392,19024,29696,41472,59408,19696,30032,41480,59424,20368,30368,41488,59440,21040,30704,41496,59584,21712,31040,42272,59600,22384,31376,42280,59616,23056,31712,42288,59632,23728,32048,42296,59776,24400,32384,43072,59792,25072,32720,43080,59808,25744,33056,43088,59824,26416,33392,43096,59968,27088,33728,43872,59984,27760,34064,43880,60000,28432,34400,43888,60016,29104,34736,43896,
60160,29776,35072,44672,60176,30448,35408,44680,60192,31120,35744,44688,60208,31792,36080,44696,60352,32464,36416,45472,60368,33136,36752,45480,60384,33808,37088,45488,60400,34480,37424,45496,60544,35152,37760,46272,60560,35824,38096,46280,60576,36496,38432,46288,60592,37168,38768,46296,60736,37840,39104,47072,60752,38512,39440,47080,60768,39184,39776,47088,60784,39856,40112,47096,
60928,8336,40448,47616,60944,9008,40784,47624,60960,9680,162992,47632,60976,10352,162992,47640,61120,11024,162992,48416,61136,11696,162992,48424,61152,12368,162992,48432,61168,13040,162992,48440,61312,13712,162992,49216,61328,14384,162992,49224,61344,15056,162992,49232,61360,15728,162992,49240,61504,16400,162992,50016,61520,17072,162992,50024,61536,17744,162992,50032,61552,18416,162992,50040,
61696,19088,162992,50816,61712,19760,162992,50824,61728,20432,162992,50832,61744,21104,162992,50840,61888,21776,162992,51616,61904,22448,162992,51624,61920,23120,162992,51632,61936,23792,162992,51640,62080,24464,162992,52416,62096,25136,162992,52424,62112,25808,162992,52432,62128,26480,162992,52440,62272,27152,162992,53216,62288,27824,162992,53224,62304,28496,162992,53232,62320,29168,162992,53240,
40960,29840,162992,41088,40976,30512,162992,41096,40992,31184,162992,41104,41008,31856,162992,41112,41152,32528,162992,41888,41168,33200,162992,41896,41184,33872,162992,41904,41200,34544,162992,41912,41344,35216,162992,42688,41360,35888,162992,42696,41376,36560,162992,42704,41392,37232,162992,42712,41536,37904,162992,43488,41552,38576,162992,43496,41568,39248,162992,43504,41584,39920,162992,43512,
41728,8400,162992,44032,41744,9072,162992,44040,41760,9744,162992,44048,41776,10416,162992,44056,41920,11088,162992,44832,41936,11760,162992,44840,41952,12432,162992,44848,41968,13104,162992,44856,42112,13776,162992,45632,42128,14448,162992,45640,42144,15120,162992,45648,42160,15792,162992,45656,42304,16464,162992,46432,42320,17136,162992,46440,42336,17808,162992,46448,42352,18480,162992,46456,
42496,19152,162992,47232,42512,19824,162992,47240,42528,20496,162992,47248,42544,21168,162992,47256,42688,21840,162992,48032,42704,22512,162992,48040,42720,23184,162992,48048,42736,23856,162992,48056,42880,24528,162992,48832,42896,25200,162992,48840,42912,25872,162992,48848,42928,26544,162992,48856,43072,27216,162992,49632,43088,27888,162992,49640,43104,28560,162992,49648,43120,29232,162992,49656,
43264,29904,162992,50176,43280,30576,162992,50184,43296,31248,162992,50192,43312,31920,162992,50200,43456,32592,162992,50976,43472,33264,162992,50984,43488,33936,162992,50992,43504,34608,162992,51000,43648,35280,162992,51776,43664,35952,162992,51784,43680,36624,162992,51792,43696,37296,162992,51800,43840,37968,162992,52576,43856,38640,162992,52584,43872,39312,162992,52592,43888,39984,162992,52600,
59392,8464,162992,41472,59408,9136,162992,41480,59424,9808,162992,41488,59440,10480,162992,41496,59584,11152,162992,42272,59600,11824,162992,42280,59616,12496,162992,42288,59632,13168,162992,42296,59776,13840,162992,43072,59792,14512,162992,43080,59808,15184,162992,43088,59824,15856,162992,43096,59968,16528,162992,43872,59984,17200,162992,43880,60000,17872,162992,43888,60016,18544,162992,43896,
60160,19216,162992,44672,60176,19888,162992,44680,60192,20560,162992,44688,60208,21232,162992,44696,60352,21904,162992,45472,60368,22576,162992,45480,60384,23248,162992,45488,60400,23920,162992,45496,60544,24592,162992,46272,60560,25264,162992,46280,60576,25936,162992,46288,60592,26608,162992,46296,60736,27280,162992,47072,60752,27952,162992,47080,60768,28624,162992,47088,60784,29296,162992,47096,
60928,29968,162992,47616,60944,30640,162992,47624,60960,31312,162992,47632,60976,31984,162992,47640,61120,32656,162992,48416,61136,33328,162992,48424,61152,34000,162992,48432,61168,34672,162992,48440,61312,35344,162992,49216,61328,36016,162992,49224,61344,36688,162992,49232,61360,37360,162992,49240,61504,38032,162992,50016,61520,38704,162992,50024,61536,39376,162992,50032,61552,40048,162992,50040,
61696,40048,162992,50816,61712,40048,162992,50824,61728,40048,162992,50832,61744,40048,162992,50840,61888,40048,162992,51616,61904,40048,162992,51624,61920,40048,162992,51632,61936,40048,162992,51640,62080,40048,162992,52416,62096,40048,162992,52424,62112,40048,162992,52432,62128,40048,162992,52440,62272,40048,162992,53216,62288,40048,162992,53224,62304,40048,162992,53232,62320,40048,162992,53240,
16256,0,0,0,16256,0,0,0,16256,0,0,0,1065353216,0,0,0,0,0,16256,0,0,0,16256,0,0,0,16256,0,0,0,1065353216,0,0,16256,0,0,0,16256,0,0,0,16256,0,0,0,1065353216,0,0,0,0,0,16256,0,0,0,16256,0,0,0,16256,0,0,0,1065353216,
0,0,0,0,0,0,0,0,0,0,0,0,0,0,0,0,0,0,0,0,0,0,0,0,0,0,0,0,0,0,0,0,0,0,0,0,0,0,0,0,0,0,0,0,0,0,0,0,0,0,0,0,0,0,0,0,0,0,0,0,0,0,0,0,
0,0,0,0,0,0,0,0,0,0,0,0,0,0,0,0,0,0,0,0,0,0,0,0,0,0,0,0,0,0,0,0,0,0,0,0,0,0,0,0,0,0,0,0,0,0,0,0,0,0,0,0,0,0,0,0,0,0,0,0,0,0,0,0,
16256,0,0,0,16256,0,0,0,16256,0,0,0,1065353216,0,0,0,0,0,16256,0,0,0,16256,0,0,0,16256,0,0,0,1065353216,0,0,16256,0,0,0,16256,0,0,0,16256,0,0,0,1065353216,0,0,0,0,0,16256,0,0,0,16256,0,0,0,16256,0,0,0,1065353216,
16256,0,0,0,16256,0,0,0,16256,0,0,0,1065353216,0,0,0,0,0,16256,0,0,0,16256,0,0,0,16256,0,0,0,1065353216,0,0,16256,0,0,0,16256,0,0,0,16256,0,0,0,1065353216,0,0,0,0,0,16256,0,0,0,16256,0,0,0,16256,0,0,0,1065353216,
0,0,0,0,0,0,0,0,0,0,0,0,0,0,0,0,0,0,0,0,0,0,0,0,0,0,0,0,0,0,0,0,0,0,0,0,0,0,0,0,0,0,0,0,0,0,0,0,0,0,0,0,0,0,0,0,0,0,0,0,0,0,0,0,
0,0,0,0,0,0,0,0,0,0,0,0,0,0,0,0,0,0,0,0,0,0,0,0,0,0,0,0,0,0,0,0,0,0,0,0,0,0,0,0,0,0,0,0,0,0,0,0,0,0,0,0,0,0,0,0,0,0,0,0,0,0,0,0,
16256,0,0,0,16256,0,0,0,16256,0,0,0,1065353216,0,0,0,0,0,16256,0,0,0,16256,0,0,0,16256,0,0,0,1065353216,0,0,16256,0,0,0,16256,0,0,0,16256,0,0,0,1065353216,0,0,0,0,0,16256,0,0,0,16256,0,0,0,16256,0,0,0,1065353216,
16256,0,0,0,16256,0,0,0,16256,0,0,0,1065353216,0,0,0,0,0,16256,0,0,0,16256,0,0,0,16256,0,0,0,1065353216,0,0,16256,0,0,0,16256,0,0,0,16256,0,0,0,1065353216,0,0,0,0,0,16256,0,0,0,16256,0,0,0,16256,0,0,0,1065353216,
0,0,0,0,0,0,0,0,0,0,0,0,0,0,0,0,0,0,0,0,0,0,0,0,0,0,0,0,0,0,0,0,0,0,0,0,0,0,0,0,0,0,0,0,0,0,0,0,0,0,0,0,0,0,0,0,0,0,0,0,0,0,0,0,
0,0,0,0,0,0,0,0,0,0,0,0,0,0,0,0,0,0,0,0,0,0,0,0,0,0,0,0,0,0,0,0,0,0,0,0,0,0,0,0,0,0,0,0,0,0,0,0,0,0,0,0,0,0,0,0,0,0,0,0,0,0,0,0,
16256,0,0,0,16256,0,0,0,16256,0,0,0,1065353216,0,0,0,0,0,16256,0,0,0,16256,0,0,0,16256,0,0,0,1065353216,0,0,16256,0,0,0,16256,0,0,0,16256,0,0,0,1065353216,0,0,0,0,0,16256,0,0,0,16256,0,0,0,16256,0,0,0,1065353216,
16256,0,0,0,16256,0,0,0,16256,0,0,0,1065353216,0,0,0,0,0,16256,0,0,0,16256,0,0,0,16256,0,0,0,1065353216,0,0,16256,0,0,0,16256,0,0,0,16256,0,0,0,1065353216,0,0,0,0,0,16256,0,0,0,16256,0,0,0,16256,0,0,0,1065353216,
0,0,0,0,0,0,0,0,0,0,0,0,0,0,0,0,0,0,0,0,0,0,0,0,0,0,0,0,0,0,0,0,0,0,0,0,0,0,0,0,0,0,0,0,0,0,0,0,0,0,0,0,0,0,0,0,0,0,0,0,0,0,0,0,
0,0,0,0,0,0,0,0,0,0,0,0,0,0,0,0,0,0,0,0,0,0,0,0,0,0,0,0,0,0,0,0,0,0,0,0,0,0,0,0,0,0,0,0,0,0,0,0,0,0,0,0,0,0,0,0,0,0,0,0,0,0,0,0,
16256,0,0,0,16256,0,0,0,16256,0,0,0,1065353216,0,0,0,0,0,16256,0,0,0,16256,0,0,0,16256,0,0,0,1065353216,0,0,16256,0,0,0,16256,0,0,0,16256,0,0,0,1065353216,0,0,0,0,0,16256,0,0,0,16256,0,0,0,16256,0,0,0,1065353216,
0,0,0,889206016,0,0,0,889206016,0,0,0,0,0,0,0,0,0,889206016,0,889206016,0,889206016,0,889206016,0,0,0,0,0,0,0,0,0,889206016,0,0,0,889206016,0,0,0,0,0,0,0,0,0,0,0,0,0,889206016,0,0,0,889206016,0,0,0,0,0,0,0,0,
0,0,0,889206016,0,0,0,889206016,0,0,0,0,0,0,0,0,0,889206016,0,889206016,0,889206016,0,889206016,0,0,0,0,0,0,0,0,0,889206016,0,0,0,889206016,0,0,0,0,0,0,0,0,0,0,0,0,0,889206016,0,0,0,889206016,0,0,0,0,0,0,0,0,
0,0,0,889206016,0,0,0,889206016,0,0,0,0,0,0,0,0,0,889206016,0,889206016,0,889206016,0,889206016,0,0,0,0,0,0,0,0,0,889206016,0,0,0,889206016,0,0,0,0,0,0,0,0,0,0,0,0,0,889206016,0,0,0,889206016,0,0,0,0,0,0,0,0,
0,0,0,889206016,0,0,0,889206016,0,0,0,0,0,0,0,0,0,889206016,0,889206016,0,889206016,0,889206016,0,0,0,0,0,0,0,0,0,889206016,0,0,0,889206016,0,0,0,0,0,0,0,0,0,0,0,0,0,889206016,0,0,0,889206016,0,0,0,0,0,0,0,0,
0,0,0,889206016,0,0,0,889206016,0,0,0,0,0,0,0,0,0,889206016,0,889206016,0,889206016,0,889206016,0,0,0,0,0,0,0,0,0,889206016,0,0,0,889206016,0,0,0,0,0,0,0,0,0,0,0,0,0,889206016,0,0,0,889206016,0,0,0,0,0,0,0,0,
0,0,0,889206016,0,0,0,889206016,0,0,0,0,0,0,0,0,0,889206016,0,889206016,0,889206016,0,889206016,0,0,0,0,0,0,0,0,0,889206016,0,0,0,889206016,0,0,0,0,0,0,0,0,0,0,0,0,0,889206016,0,0,0,889206016,0,0,0,0,0,0,0,0,
0,0,0,889206016,0,0,0,889206016,0,0,0,0,0,0,0,0,0,889206016,0,889206016,0,889206016,0,889206016,0,0,0,0,0,0,0,0,0,889206016,0,0,0,889206016,0,0,0,0,0,0,0,0,0,0,0,0,0,889206016,0,0,0,889206016,0,0,0,0,0,0,0,0,
0,0,0,889206016,0,0,0,889206016,0,0,0,0,0,0,0,0,0,889206016,0,889206016,0,889206016,0,889206016,0,0,0,0,0,0,0,0,0,889206016,0,0,0,889206016,0,0,0,0,0,0,0,0,0,0,0,0,0,889206016,0,0,0,889206016,0,0,0,0,0,0,0,0,
0,0,0,889206016,0,0,0,889206016,0,0,0,0,0,0,0,0,0,889206016,0,889206016,0,889206016,0,889206016,0,0,0,0,0,0,0,0,0,889206016,0,0,0,889206016,0,0,0,0,0,0,0,0,0,0,0,0,0,889206016,0,0,0,889206016,0,0,0,0,0,0,0,0,
0,0,0,889206016,0,0,0,889206016,0,0,0,0,0,0,0,0,0,889206016,0,889206016,0,889206016,0,889206016,0,0,0,0,0,0,0,0,0,889206016,0,0,0,889206016,0,0,0,0,0,0,0,0,0,0,0,0,0,889206016,0,0,0,889206016,0,0,0,0,0,0,0,0,
0,0,0,889206016,0,0,0,889206016,0,0,0,0,0,0,0,0,0,889206016,0,889206016,0,889206016,0,889206016,0,0,0,0,0,0,0,0,0,889206016,0,0,0,889206016,0,0,0,0,0,0,0,0,0,0,0,0,0,889206016,0,0,0,889206016,0,0,0,0,0,0,0,0,
0,0,0,889206016,0,0,0,889206016,0,0,0,0,0,0,0,0,0,889206016,0,889206016,0,889206016,0,889206016,0,0,0,0,0,0,0,0,0,889206016,0,0,0,889206016,0,0,0,0,0,0,0,0,0,0,0,0,0,889206016,0,0,0,889206016,0,0,0,0,0,0,0,0,
0,0,0,889206016,0,0,0,889206016,0,0,0,0,0,0,0,0,0,889206016,0,889206016,0,889206016,0,889206016,0,0,0,0,0,0,0,0,0,889206016,0,0,0,889206016,0,0,0,0,0,0,0,0,0,0,0,0,0,889206016,0,0,0,889206016,0,0,0,0,0,0,0,0,
0,0,0,889206016,0,0,0,889206016,0,0,0,0,0,0,0,0,0,889206016,0,889206016,0,889206016,0,889206016,0,0,0,0,0,0,0,0,0,889206016,0,0,0,889206016,0,0,0,0,0,0,0,0,0,0,0,0,0,889206016,0,0,0,889206016,0,0,0,0,0,0,0,0,
0,0,0,889206016,0,0,0,889206016,0,0,0,0,0,0,0,0,0,889206016,0,889206016,0,889206016,0,889206016,0,0,0,0,0,0,0,0,0,889206016,0,0,0,889206016,0,0,0,0,0,0,0,0,0,0,0,0,0,889206016,0,0,0,889206016,0,0,0,0,0,0,0,0,
0,0,0,889206016,0,0,0,889206016,0,0,0,0,0,0,0,0,0,889206016,0,889206016,0,889206016,0,889206016,0,0,0,0,0,0,0,0,0,889206016,0,0,0,889206016,0,0,0,0,0,0,0,0,0,0,0,0,0,889206016,0,0,0,889206016,0,0,0,0,0,0,0,0,
0,889206016,0,40960,0,889206016,0,40976,0,0,0,40992,0,0,0,41008,0,0,0,41024,0,0,0,41040,0,0,0,41056,0,0,0,41072,0,889206016,0,41088,0,889206016,0,41104,0,0,0,41120,0,0,0,41136,0,889206016,0,41152,0,889206016,0,41168,0,0,0,41184,0,0,0,41200,
0,889206016,0,41216,0,889206016,0,41232,0,0,0,41248,0,0,0,41264,0,0,0,41280,0,0,0,41296,0,0,0,41312,0,0,0,41328,0,889206016,0,41344,0,889206016,0,41360,0,0,0,41376,0,0,0,41392,0,889206016,0,41408,0,889206016,0,41424,0,0,0,41440,0,0,0,41456,
0,889206016,0,41472,0,889206016,0,41488,0,0,0,41504,0,0,0,41520,0,0,0,41536,0,0,0,41552,0,0,0,41568,0,0,0,41584,0,889206016,0,41600,0,889206016,0,41616,0,0,0,41632,0,0,0,41648,0,889206016,0,41664,0,889206016,0,41680,0,0,0,41696,0,0,0,41712,
0,889206016,0,41728,0,889206016,0,41744,0,0,0,41760,0,0,0,41776,0,0,0,41792,0,0,0,41808,0,0,0,41824,0,0,0,41840,0,889206016,0,41856,0,889206016,0,41872,0,0,0,41888,0,0,0,41904,0,889206016,0,41920,0,889206016,0,41936,0,0,0,41952,0,0,0,41968,
0,889206016,0,41984,0,889206016,0,42000,0,0,0,42016,0,0,0,42032,0,0,0,42048,0,0,0,42064,0,0,0,42080,0,0,0,42096,0,889206016,0,42112,0,889206016,0,42128,0,0,0,42144,0,0,0,42160,0,889206016,0,42176,0,889206016,0,42192,0,0,0,42208,0,0,0,42224,
0,889206016,0,42240,0,889206016,0,42256,0,0,0,42272,0,0,0,42288,0,0,0,42304,0,0,0,42320,0,0,0,42336,0,0,0,42352,0,889206016,0,42368,0,889206016,0,42384,0,0,0,42400,0,0,0,42416,0,889206016,0,42432,0,889206016,0,42448,0,0,0,42464,0,0,0,42480,
0,889206016,0,42496,0,889206016,0,42512,0,0,0,42528,0,0,0,42544,0,0,0,42560,0,0,0,42576,0,0,0,42592,0,0,0,42608,0,889206016,0,42624,0,889206016,0,42640,0,0,0,42656,0,0,0,42672,0,889206016,0,42688,0,889206016,0,42704,0,0,0,42720,0,0,0,42736,
0,889206016,0,42752,0,889206016,0,42768,0,0,0,42784,0,0,0,42800,0,0,0,42816,0,0,0,42832,0,0,0,42848,0,0,0,42864,0,889206016,0,42880,0,889206016,0,42896,0,0,0,42912,0,0,0,42928,0,889206016,0,42944,0,889206016,0,42960,0,0,0,42976,0,0,0,42992,
0,889206016,0,43008,0,889206016,0,43024,0,0,0,43040,0,0,0,43056,0,0,0,43072,0,0,0,43088,0,0,0,43104,0,0,0,43120,0,889206016,0,43136,0,889206016,0,43152,0,0,0,43168,0,0,0,43184,0,889206016,0,43200,0,889206016,0,43216,0,0,0,43232,0,0,0,43248,
0,889206016,0,43264,0,889206016,0,43280,0,0,0,43296,0,0,0,43312,0,0,0,43328,0,0,0,43344,0,0,0,43360,0,0,0,43376,0,889206016,0,43392,0,889206016,0,43408,0,0,0,43424,0,0,0,43440,0,889206016,0,43456,0,889206016,0,43472,0,0,0,43488,0,0,0,43504,
0,889206016,0,43520,0,889206016,0,43536,0,0,0,43552,0,0,0,43568,0,0,0,43584,0,0,0,43600,0,0,0,43616,0,0,0,43632,0,889206016,0,43648,0,889206016,0,43664,0,0,0,43680,0,0,0,43696,0,889206016,0,43712,0,889206016,0,43728,0,0,0,43744,0,0,0,43760,
0,889206016,0,43776,0,889206016,0,43792,0,0,0,43808,0,0,0,43824,0,0,0,43840,0,0,0,43856,0,0,0,43872,0,0,0,43888,0,889206016,0,43904,0,889206016,0,43920,0,0,0,43936,0,0,0,43952,0,889206016,0,43968,0,889206016,0,43984,0,0,0,44000,0,0,0,44016,
0,889206016,0,44032,0,889206016,0,44048,0,0,0,44064,0,0,0,44080,0,0,0,44096,0,0,0,44112,0,0,0,44128,0,0,0,44144,0,889206016,0,44160,0,889206016,0,44176,0,0,0,44192,0,0,0,44208,0,889206016,0,44224,0,889206016,0,44240,0,0,0,44256,0,0,0,44272,
0,889206016,0,44288,0,889206016,0,44304,0,0,0,44320,0,0,0,44336,0,0,0,44352,0,0,0,44368,0,0,0,44384,0,0,0,44400,0,889206016,0,44416,0,889206016,0,44432,0,0,0,44448,0,0,0,44464,0,889206016,0,44480,0,889206016,0,44496,0,0,0,44512,0,0,0,44528,
0,889206016,0,44544,0,889206016,0,44560,0,0,0,44576,0,0,0,44592,0,0,0,44608,0,0,0,44624,0,0,0,44640,0,0,0,44656,0,889206016,0,44672,0,889206016,0,44688,0,0,0,44704,0,0,0,44720,0,889206016,0,44736,0,889206016,0,44752,0,0,0,44768,0,0,0,44784,
0,889206016,0,44800,0,889206016,0,44816,0,0,0,44832,0,0,0,44848,0,0,0,44864,0,0,0,44880,0,0,0,44896,0,0,0,44912,0,889206016,0,44928,0,889206016,0,44944,0,0,0,44960,0,0,0,44976,0,889206016,0,44992,0,889206016,0,45008,0,0,0,45024,0,0,0,45040};

DI float bf_lo(u32 v) { return __uint_as_float(v << 16); }
DI u32 pk_bf16(float a, float b) { f32x2 t = {a, b}; return __builtin_bit_cast(u32, __builtin_convertvector(t, bf16x2)); }

DI void slow_path(const u16* __restrict__ x, const u16* __restrict__ dwl, const u16* __restrict__ dws, const u16* __restrict__ w1,
                  const u16* __restrict__ w2, u16* __restrict__ out, float* sm, int t, int tx, int ty0) {
  float* o = sm;
  float* g = sm + 8 * CH;
  float* st = g + 8 * HID;
#pragma unroll 1
  for (int b = 0; b < 128; ++b) {
    const int y = ty0 * 16 + (b >> 1), x0 = tx * 16 + (b & 1) * 8;
    __syncthreads();
#pragma unroll 1
    for (int i = t; i < 8 * CH; i += 256) {
      const int p = i / CH, c = i - p * CH;
      float acc = 0.f;
#pragma unroll 1
      for (int a = 0; a < 5; ++a)
#pragma unroll 1
        for (int e = 0; e < 5; ++e) {
          const int yy = y + a - 2, xc = x0 + p + e - 2;
          if (yy < 0 || yy >= IMG || xc < 0 || xc >= IMG) continue;
          const float xv = bf_lo(x[(size_t)(yy * IMG + xc) * CH + c]);
          acc = __builtin_fmaf(bf_lo(dws[(a * 5 + e) * CH + c]), xv, acc);
          if (a >= 1 && a <= 3 && e >= 1 && e <= 3) acc = __builtin_fmaf(bf_lo(dwl[((a - 1) * 3 + (e - 1)) * CH + c]), xv, acc);
        }
      o[i] = acc;
    }
    __syncthreads();
    if (t < 8) {
      float m = 0.f, v = 0.f;
#pragma unroll 1
      for (int c = 0; c < CH; ++c) m += o[t * CH + c];
      m *= (1.0f / CH);
#pragma unroll 1
      for (int c = 0; c < CH; ++c) { const float d = o[t * CH + c] - m; v = __builtin_fmaf(d, d, v); }
      st[2 * t] = m; st[2 * t + 1] = __builtin_amdgcn_rsqf(v * (1.0f / CH) + 1e-5f);
    }
    __syncthreads();
#pragma unroll 1
    for (int i = t; i < 8 * HID; i += 256) {
      const int p = i / HID, e = i - p * HID;
      const float m = st[2 * p], r = st[2 * p + 1];
      float u = 0.f;
#pragma unroll 1
      for (int c = 0; c < CH; ++c) u = __builtin_fmaf((o[p * CH + c] - m) * r, bf_lo(w1[c * HID + e]), u);
      const float z = u * __builtin_fmaf(u * u, GC3, GC1);
      g[i] = u * __builtin_amdgcn_rcpf(1.0f + __builtin_amdgcn_exp2f(z));
    }
    __syncthreads();
#pragma unroll 1
    for (int i = t; i < 8 * CH; i += 256) {
      const int p = i / CH, c = i - p * CH;
      float acc = 0.f;
#pragma unroll 1
      for (int e = 0; e < HID; ++e) acc = __builtin_fmaf(g[p * HID + e], bf_lo(w2[e * CH + c]), acc);
      const size_t idx = (size_t)(y * IMG + x0 + p) * CH + c;
      out[idx] = (u16)pk_bf16(bf_lo(x[idx]) + acc, 0.f);
    }
  }
}

__global__ __launch_bounds__(256, 1) void replk_kernel(const u16* __restrict__ x, const u16* __restrict__ dwl,
    const u16* __restrict__ dws, const u16* __restrict__ w1, const u16* __restrict__ w2, u16* __restrict__ out
    ) {
  __shared__ __attribute__((aligned(16))) char smem[L_TOTAL];
  __attribute__((address_space(3))) char* smem3 = (__attribute__((address_space(3))) char*)smem;
  const int w = __builtin_amdgcn_readfirstlane(threadIdx.x >> 6);
  u32 tid = threadIdx.x, blk = blockIdx.x;
  u32 flg = (u32)(size_t)smem3 ? 1u : 0u;
  u64 ka = (u64)__builtin_amdgcn_kernarg_segment_ptr();
  asm volatile(
    ".p2align 3\n"
    "s_load_dwordx2 s[40:41], s[98:99], 0x0\n"
    "s_load_dwordx2 s[88:89], s[98:99], 0x10\n"
    "s_load_dwordx2 s[92:93], s[98:99], 0x8\n"
    "s_load_dwordx2 s[84:85], s[98:99], 0x20\n"
    "s_load_dwordx2 s[80:81], s[98:99], 0x18\n"
    "s_load_dwordx2 s[44:45], s[98:99], 0x28\n"
    "v_lshlrev_b32_e32 v213, 4, v208\n"
    "v_lshrrev_b32_e32 v0, 6, v208\n"
    "s_add_u32 vcc_lo, s96, 16384\n"
    "s_nop 0\n"
    "s_addc_u32 vcc_hi, s97, 0\n"
    "global_load_dwordx4 v[216:219], v213, vcc\n"
    "s_add_u32 vcc_lo, s96, 20480\n"
    "s_addc_u32 vcc_hi, s97, 0\n"
    "global_load_dwordx4 v[220:223], v213, vcc\n"
    "v_readfirstlane_b32 s60, v0\n"
    "v_min_u32_e32 v1, 239, v208\n"
    "v_mul_u32_u24_e32 v2, 1366, v1\n"
    "s_lshr_b32 s61, s65, 3\n"
    "s_and_b32 s62, s65, 7\n"
    "v_lshrrev_b32_e32 v2, 16, v2\n"
    "v_lshlrev_b32_e32 v3, 2, v1\n"
    "s_lshl_b32 s62, s62, 6\n"
    "s_movk_i32 s63, 768\n"
    "v_mad_u32_u24 v211, v2, s63, v3\n"
    "v_mul_u32_u24_e32 v4, 384, v2\n"
    "v_add_u32_e32 v2, -1, v2\n"
    "s_movk_i32 s90, 4800\n"
    "s_mov_b32 s91, 0x20000\n"
    "v_add_u32_e32 v3, -576, v3\n"
    "v_cmp_gt_u32_e32 vcc, 3, v2\n"
    "v_add_u32_e32 v3, v3, v4\n"
    "v_bfrev_b32_e32 v4, 1\n"
    "s_movk_i32 s94, 1728\n"
    "s_mov_b32 s95, 0x20000\n"
    "v_cndmask_b32_e32 v212, v4, v3, vcc\n"
    "s_lshl_b32 s99, s62, 5\n"
    "s_add_u32 s99, s99, s61\n"
    "s_mulk_i32 s99, 3072\n"
    "s_lshr_b32 s51, s60, 1\n"
    "s_mul_i32 s63, s51, 18432\n"
    "s_lshl_b32 s52, s60, 13\n"
    "s_add_u32 s63, s63, s52\n"
    "v_add_u32_e32 v210, s63, v213\n"
    "s_mul_i32 s38, s60, 9216\n"
    "s_add_u32 s38, s38, 40960\n"
    "s_mov_b32 s86, 73728\n"
    "s_mov_b32 s87, 0x20000\n"
    "v_and_b32_e32 v5, 63, v208\n"
    "s_cmp_lt_u32 s60, 2\n"
    "s_cselect_b32 s63, 17, 15\n"
    "v_min_u32_e32 v5, s63, v5\n"
    "s_and_b32 s52, s61, 15\n"
    "s_lshl_b32 s52, s52, 1\n"
    "s_add_u32 s52, s52, s60\n"
    "s_sub_u32 s52, s52, 2\n"
    "s_lshl_b32 s52, s52, 4\n"
    "s_mul_i32 s53, s61, 18\n"
    "s_getpc_b64 s[76:77]\n"
    ".Lreplk_pc0:\n"
    "s_cmp_lt_u32 s60, 2\n"
    "s_cselect_b32 s52, s53, s52\n"
    "s_mov_b32 s53, .Lreplk_end-.Lreplk_pc0-4\n"
    "s_cselect_b32 s53, 0x20000, s53\n"
    "v_add_lshl_u32 v5, s52, v5, 7\n"
    "v_min_u32_e32 v214, s53, v5\n"
    "s_cmp_eq_u32 s61, 0\n"
    "s_cselect_b32 s52, 24, 0\n"
    "s_cmp_eq_u32 s61, 31\n"
    "s_cselect_b32 s53, 40, s52\n"
    "s_sub_u32 s53, 256, s53\n"
    "v_subrev_u32_e32 v6, s52, v208\n"
    "s_sub_u32 s63, s99, 384\n"
    "v_cmp_gt_u32_e32 vcc, s53, v6\n"
    "v_lshl_add_u32 v7, v1, 4, s63\n"
    "v_bfe_u32 v8, v208, 4, 2\n"
    "v_lshl_or_b32 v8, v0, 3, v8\n"
    "v_and_b32_e32 v9, 0xf0, v213\n"
    "v_cndmask_b32_e32 v208, v4, v7, vcc\n"
    "v_mul_u32_u24_e32 v8, 768, v8\n"
    "s_mov_b32 s42, 50331648\n"
    "s_mov_b32 s43, 0x20000\n"
    "v_or_b32_e32 v209, v8, v9\n"
    "s_mov_b32 s82, 73728\n"
    "s_mov_b32 s83, 0x20000\n"
    "s_cmp_eq_u32 s62, 0\n"
    "s_cselect_b32 s78, 0, s42\n"
    "s_mov_b32 s79, 0x20000\n"
    "s_waitcnt lgkmcnt(0)\n"
    "s_cmp_eq_u32 s60, 0\n"
    "s_cselect_b64 s[70:71], s[80:81], s[76:77]\n"
    "s_cmp_eq_u32 s60, 1\n"
    "s_cselect_b64 s[70:71], s[84:85], s[70:71]\n"
    "s_sub_u32 s76, s40, 196608\n"
    "s_subb_u32 s77, s41, 0\n"
    "buffer_load_dword v224, v211, s[88:91], 0 offen offset:0\n"
    "buffer_load_dword v225, v211, s[88:91], 0 offen offset:192\n"
    "buffer_load_dword v226, v211, s[88:91], 0 offen offset:384\n"
    "buffer_load_dword v227, v211, s[88:91], 0 offen offset:576\n"
    "buffer_load_dword v228, v211, s[88:91], 0 offen offset:768\n"
    "buffer_load_dword v229, v212, s[92:95], 0 offen offset:0\n"
    "buffer_load_dword v230, v212, s[92:95], 0 offen offset:192\n"
    "buffer_load_dword v231, v212, s[92:95], 0 offen offset:384\n"
    "global_load_dword v233, v214, s[70:71]\n"
    "s_add_u32 m0, s38, 0\n"
    "s_mov_b32 s64, 0\n"
    "buffer_load_dwordx4 v210, s[84:87], s64 offen offset:0 lds\n"
    "v_mov_b32_e64 v88, 0\n"
    "v_mov_b32_e64 v89, 0\n"
    "v_mov_b32_e64 v90, 0\n"
    "v_mov_b32_e64 v91, 0\n"
    "v_mov_b32_e64 v59, 0\n"
    "v_mov_b32_e64 v60, 0\n"
    "v_mov_b32_e64 v67, 0\n"
    "v_mov_b32_e64 v68, 0\n"
    "v_mov_b32_e64 v75, 0\n"
    "v_mov_b32_e64 v76, 0\n"
    "v_mov_b32_e64 v83, 0\n"
    "v_mov_b32_e64 v84, 0\n"
    "v_mov_b32_e32 v36, 0x75089ba4\n"
    "v_mov_b32_e32 v38, 0xbdd2d3e8\n"
    "v_mov_b32_e32 v32, 0xc0135761\n"
    "v_lshlrev_b32_e64 v34, 16, v213\n"
    "v_lshlrev_b32_e64 v234, 1, v213\n"
    "v_or_b32_e32 v0, 0x8000, v34\n"
    "v_or_b32_e32 v1, 0x18000, v34\n"
    "v_or_b32_e32 v2, 0x28000, v34\n"
    "v_or_b32_e32 v3, 0x38000, v34\n"
    "v_or_b32_e32 v4, 0x48000, v34\n"
    "v_or_b32_e32 v5, 0x58000, v34\n"
    "v_or_b32_e32 v6, 0x68000, v34\n"
    "v_or_b32_e32 v7, 0x78000, v34\n"
    "v_cmp_ne_u32_e64 s[72:73], 0, v34\n"
    "s_nop 0\n"
    "s_nop 1\n"
    "v_cndmask_b32_e64 v0, 0, v0, s[72:73]\n"
    "v_pk_mul_f32 v[0:1], v[0:1], v[36:37] op_sel_hi:[1,0]\n"
    "v_pk_mul_f32 v[2:3], v[2:3], v[36:37] op_sel_hi:[1,0]\n"
    "v_pk_mul_f32 v[4:5], v[4:5], v[36:37] op_sel_hi:[1,0]\n"
    "v_pk_mul_f32 v[6:7], v[6:7], v[36:37] op_sel_hi:[1,0]\n"
    "v_pk_mul_f32 v[8:9], v[0:1], v[0:1]\n"
    "v_pk_mul_f32 v[10:11], v[2:3], v[2:3]\n"
    "v_pk_mul_f32 v[12:13], v[4:5], v[4:5]\n"
    "v_pk_mul_f32 v[14:15], v[6:7], v[6:7]\n"
    "v_pk_fma_f32 v[8:9], v[8:9], v[38:39], v[32:33] op_sel_hi:[1,0,0]\n"
    "v_pk_fma_f32 v[10:11], v[10:11], v[38:39], v[32:33] op_sel_hi:[1,0,0]\n"
    "v_pk_fma_f32 v[12:13], v[12:13], v[38:39], v[32:33] op_sel_hi:[1,0,0]\n"
    "v_pk_fma_f32 v[14:15], v[14:15], v[38:39], v[32:33] op_sel_hi:[1,0,0]\n"
    "v_pk_mul_f32 v[8:9], v[8:9], v[0:1]\n"
    "v_pk_mul_f32 v[10:11], v[10:11], v[2:3]\n"
    "v_pk_mul_f32 v[12:13], v[12:13], v[4:5]\n"
    "v_pk_mul_f32 v[14:15], v[14:15], v[6:7]\n"
    "v_exp_f32_e64 v8, v8\n"
    "v_exp_f32_e64 v9, v9\n"
    "v_exp_f32_e64 v10, v10\n"
    "v_exp_f32_e64 v11, v11\n"
    "v_exp_f32_e64 v12, v12\n"
    "v_exp_f32_e64 v13, v13\n"
    "v_exp_f32_e64 v14, v14\n"
    "v_exp_f32_e64 v15, v15\n"
    "v_pk_add_f32 v[16:17], v[8:9], 1.0 op_sel_hi:[1,0]\n"
    "v_pk_add_f32 v[18:19], v[10:11], 1.0 op_sel_hi:[1,0]\n"
    "v_pk_add_f32 v[20:21], v[12:13], 1.0 op_sel_hi:[1,0]\n"
    "v_pk_add_f32 v[22:23], v[14:15], 1.0 op_sel_hi:[1,0]\n"
    "v_rcp_f32_e64 v16, v16\n"
    "v_rcp_f32_e64 v17, v17\n"
    "v_rcp_f32_e64 v18, v18\n"
    "v_rcp_f32_e64 v19, v19\n"
    "v_rcp_f32_e64 v20, v20\n"
    "v_rcp_f32_e64 v21, v21\n"
    "v_rcp_f32_e64 v22, v22\n"
    "v_rcp_f32_e64 v23, v23\n"
    "v_pk_mul_f32 v[16:17], v[16:17], v[0:1]\n"
    "v_pk_mul_f32 v[18:19], v[18:19], v[2:3]\n"
    "v_pk_mul_f32 v[20:21], v[20:21], v[4:5]\n"
    "v_pk_mul_f32 v[22:23], v[22:23], v[6:7]\n"
    "v_pk_mul_f32 v[8:9], v[8:9], v[16:17]\n"
    "v_pk_mul_f32 v[10:11], v[10:11], v[18:19]\n"
    "v_pk_mul_f32 v[12:13], v[12:13], v[20:21]\n"
    "v_pk_mul_f32 v[14:15], v[14:15], v[22:23]\n"
    "v_cvt_pk_bf16_f32 v28, v16, v17\n"
    "v_cvt_pk_bf16_f32 v29, v18, v19\n"
    "v_cvt_pk_bf16_f32 v30, v20, v21\n"
    "v_cvt_pk_bf16_f32 v31, v22, v23\n"
    "v_cvt_pk_bf16_f32 v24, -v8, -v9\n"
    "v_cvt_pk_bf16_f32 v25, -v10, -v11\n"
    "v_cvt_pk_bf16_f32 v26, -v12, -v13\n"
    "v_cvt_pk_bf16_f32 v27, -v14, -v15\n"
    "v_accvgpr_write_b32 a216, v28\n"
    "v_accvgpr_write_b32 a217, v29\n"
    "v_accvgpr_write_b32 a218, v30\n"
    "v_accvgpr_write_b32 a219, v31\n"
    "ds_write_b128 v234, v[24:27] offset:0\n"
    "v_or_b32_e32 v0, 0x88000, v34\n"
    "v_or_b32_e32 v1, 0x98000, v34\n"
    "v_or_b32_e32 v2, 0xa8000, v34\n"
    "v_or_b32_e32 v3, 0xb8000, v34\n"
    "v_or_b32_e32 v4, 0xc8000, v34\n"
    "v_or_b32_e32 v5, 0xd8000, v34\n"
    "v_or_b32_e32 v6, 0xe8000, v34\n"
    "v_or_b32_e32 v7, 0xf8000, v34\n"
    "v_pk_mul_f32 v[0:1], v[0:1], v[36:37] op_sel_hi:[1,0]\n"
    "v_pk_mul_f32 v[2:3], v[2:3], v[36:37] op_sel_hi:[1,0]\n"
    "v_pk_mul_f32 v[4:5], v[4:5], v[36:37] op_sel_hi:[1,0]\n"
    "v_pk_mul_f32 v[6:7], v[6:7], v[36:37] op_sel_hi:[1,0]\n"
    "v_pk_mul_f32 v[8:9], v[0:1], v[0:1]\n"
    "v_pk_mul_f32 v[10:11], v[2:3], v[2:3]\n"
    "v_pk_mul_f32 v[12:13], v[4:5], v[4:5]\n"
    "v_pk_mul_f32 v[14:15], v[6:7], v[6:7]\n"
    "v_pk_fma_f32 v[8:9], v[8:9], v[38:39], v[32:33] op_sel_hi:[1,0,0]\n"
    "v_pk_fma_f32 v[10:11], v[10:11], v[38:39], v[32:33] op_sel_hi:[1,0,0]\n"
    "v_pk_fma_f32 v[12:13], v[12:13], v[38:39], v[32:33] op_sel_hi:[1,0,0]\n"
    "v_pk_fma_f32 v[14:15], v[14:15], v[38:39], v[32:33] op_sel_hi:[1,0,0]\n"
    "v_pk_mul_f32 v[8:9], v[8:9], v[0:1]\n"
    "v_pk_mul_f32 v[10:11], v[10:11], v[2:3]\n"
    "v_pk_mul_f32 v[12:13], v[12:13], v[4:5]\n"
    "v_pk_mul_f32 v[14:15], v[14:15], v[6:7]\n"
    "v_exp_f32_e64 v8, v8\n"
    "v_exp_f32_e64 v9, v9\n"
    "v_exp_f32_e64 v10, v10\n"
    "v_exp_f32_e64 v11, v11\n"
    "v_exp_f32_e64 v12, v12\n"
    "v_exp_f32_e64 v13, v13\n"
    "v_exp_f32_e64 v14, v14\n"
    "v_exp_f32_e64 v15, v15\n"
    "v_pk_add_f32 v[16:17], v[8:9], 1.0 op_sel_hi:[1,0]\n"
    "v_pk_add_f32 v[18:19], v[10:11], 1.0 op_sel_hi:[1,0]\n"
    "v_pk_add_f32 v[20:21], v[12:13], 1.0 op_sel_hi:[1,0]\n"
    "v_pk_add_f32 v[22:23], v[14:15], 1.0 op_sel_hi:[1,0]\n"
    "v_rcp_f32_e64 v16, v16\n"
    "v_rcp_f32_e64 v17, v17\n"
    "v_rcp_f32_e64 v18, v18\n"
    "v_rcp_f32_e64 v19, v19\n"
    "v_rcp_f32_e64 v20, v20\n"
    "v_rcp_f32_e64 v21, v21\n"
    "v_rcp_f32_e64 v22, v22\n"
    "v_rcp_f32_e64 v23, v23\n"
    "v_pk_mul_f32 v[16:17], v[16:17], v[0:1]\n"
    "v_pk_mul_f32 v[18:19], v[18:19], v[2:3]\n"
    "v_pk_mul_f32 v[20:21], v[20:21], v[4:5]\n"
    "v_pk_mul_f32 v[22:23], v[22:23], v[6:7]\n"
    "v_pk_mul_f32 v[8:9], v[8:9], v[16:17]\n"
    "v_pk_mul_f32 v[10:11], v[10:11], v[18:19]\n"
    "v_pk_mul_f32 v[12:13], v[12:13], v[20:21]\n"
    "v_pk_mul_f32 v[14:15], v[14:15], v[22:23]\n"
    "v_cvt_pk_bf16_f32 v28, v16, v17\n"
    "v_cvt_pk_bf16_f32 v29, v18, v19\n"
    "v_cvt_pk_bf16_f32 v30, v20, v21\n"
    "v_cvt_pk_bf16_f32 v31, v22, v23\n"
    "v_cvt_pk_bf16_f32 v24, -v8, -v9\n"
    "v_cvt_pk_bf16_f32 v25, -v10, -v11\n"
    "v_cvt_pk_bf16_f32 v26, -v12, -v13\n"
    "v_cvt_pk_bf16_f32 v27, -v14, -v15\n"
    "v_accvgpr_write_b32 a220, v28\n"
    "v_accvgpr_write_b32 a221, v29\n"
    "v_accvgpr_write_b32 a222, v30\n"
    "v_accvgpr_write_b32 a223, v31\n"
    "ds_write_b128 v234, v[24:27] offset:16\n"
    "v_add_u32_e32 v234, 65536, v234\n"
    "buffer_load_dwordx4 v210, s[84:87], s64 offen offset:1024 lds\n"
    "s_waitcnt vmcnt(12)\n"
    "v_xor_b32_e32 v211, 96, v219\n"
    "buffer_load_dwordx4 v210, s[84:87], s64 offen offset:2048 lds\n"
    "s_waitcnt vmcnt(12)\n"
    "ds_write_b128 v222, v[88:91]\n"
    "s_add_u32 m0, s38, 3072\n"
    "s_mov_b32 s64, 3072\n"
    "buffer_load_dwordx4 v210, s[84:87], s64 offen offset:0 lds\n"
    "buffer_load_dwordx4 v210, s[84:87], s64 offen offset:1024 lds\n"
    "buffer_load_dwordx4 v210, s[84:87], s64 offen offset:2048 lds\n"
    "s_add_u32 m0, s38, 6144\n"
    "s_mov_b32 s64, 6144\n"
    "buffer_load_dwordx4 v210, s[84:87], s64 offen offset:0 lds\n"
    "buffer_load_dwordx4 v210, s[84:87], s64 offen offset:1024 lds\n"
    "s_waitcnt vmcnt(16)\n"
    "v_lshlrev_b32_e32 v40, 16, v224\n"
    "buffer_load_dwordx4 v210, s[84:87], s64 offen offset:2048 lds\n"
    "s_mov_b32 s65, 0\n"
    "buffer_load_dwordx4 a[224:227], v208, s[76:79], s65 offen\n"
    "s_waitcnt vmcnt(17)\n"
    "v_lshlrev_b32_e64 v41, 16, v225\n"
    "s_mov_b32 s65, 98304\n"
    "buffer_load_dwordx4 a[228:231], v208, s[76:79], s65 offen\n"
    "s_mov_b32 s65, 196608\n"
    "buffer_load_dwordx4 a[232:235], v208, s[40:43], s65 offen\n"
    "s_mov_b32 s48, 0x44ea44ea\n"
    "s_mov_b32 s49, 0xb77fb77f\n"
    "s_mov_b32 s50, 0x89efde6e\n"
    "s_mov_b32 s46, s42\n"
    "s_mov_b32 s47, s43\n"
    "s_mul_i32 s54, s60, 592\n"
    "s_add_u32 s54, s54, -27008\n"
    "s_mul_i32 s55, s60, -1284\n"
    "s_add_u32 s55, s55, 54688\n"
    "s_mul_i32 s56, s60, -6160\n"
    "s_add_u32 s56, s56, -27008\n"
    "s_add_u32 s98, s99, 1375872\n"
    "s_and_b32 s36, s60, 1\n"
    "s_lshl_b32 s36, s36, 13\n"
    "s_add_u32 s36, s36, 327680\n"
    "s_mul_i32 s53, s60, 98304\n"
    "s_add_u32 s53, s53, s98\n"
    "s_add_u32 s53, s53, 393216\n"
    "s_sub_u32 s53, s53, s36\n"
    "s_and_b32 s37, s61, 1\n"
    "s_lshl_b32 s37, s37, 2\n"
    "s_add_u32 s37, s37, s60\n"
    "s_lshl_b32 s37, s37, 12\n"
    "s_cmp_eq_u32 s61, 0\n"
    "s_cselect_b32 s67, 2, 0\n"
    "s_cmp_eq_u32 s61, 31\n"
    "s_cselect_b32 s67, 1, s67\n"
    "s_lshl_b32 s67, s67, 30\n"
    "s_or_b32 s67, s67, 0x3fffffff\n"
    "s_mov_b64 s[68:69], s[44:45]\n"
    "s_mov_b32 s70, 0\n"
    "s_mov_b32 s71, s47\n"
    "s_mov_b64 s[72:73], s[40:41]\n"
    "s_mov_b64 s[74:75], s[42:43]\n"
    "s_mov_b32 s57, 0\n"
    "s_mov_b32 s58, 196608\n"
    "s_mov_b32 s60, 0\n"
    "s_mov_b32 s61, 393216\n"
    "s_mov_b32 s62, 786432\n"
    "s_mov_b32 s63, 1179648\n"
    "s_mov_b32 s52, 1572864\n"
    "s_mov_b32 s88, 98304\n"
    "s_mov_b32 s89, 294912\n"
    "s_mov_b32 s39, 0\n"
    "s_getpc_b64 s[76:77]\n"
    ".Lreplk_pc1:\n"
    "s_add_u32 s76, s76, .Lreplk_last-.Lreplk_pc1\n"
    "s_addc_u32 s77, s77, 0\n"
    "s_sub_u32 s76, s76, s36\n"
    "s_subb_u32 s77, s77, 0\n"
    "s_add_u32 s78, s36, .Lreplk_end-.Lreplk_last\n"
    "s_mov_b32 s79, s37\n"
    "s_mov_b32 s65, 294912\n"
    "buffer_load_dwordx4 a[236:239], v208, s[40:43], s65 offen\n"
    "s_mov_b32 s65, 589824\n"
    "buffer_load_dwordx4 a[240:243], v208, s[40:43], s65 offen\n"
    "s_waitcnt vmcnt(20)\n"
    "v_lshlrev_b32_e32 v42, 16, v226\n"
    "s_waitcnt vmcnt(19)\n"
    "v_lshlrev_b32_e32 v43, 16, v227\n"
    "s_waitcnt vmcnt(18)\n"
    "v_lshlrev_b32_e32 v44, 16, v228\n"
    "s_waitcnt vmcnt(17)\n"
    "v_lshlrev_b32_e32 v45, 16, v229\n"
    "s_mov_b32 s65, 688128\n"
    "buffer_load_dwordx4 a[244:247], v208, s[40:43], s65 offen\n"
    "s_waitcnt vmcnt(17)\n"
    "v_lshlrev_b32_e32 v46, 16, v230\n"
    "s_waitcnt vmcnt(16)\n"
    "v_lshlrev_b32_e32 v47, 16, v231\n"
    "v_add_f32_e64 v41, v41, v45\n"
    "v_add_f32_e64 v42, v42, v46\n"
    "v_add_f32_e64 v43, v43, v47\n"
    "v_cvt_pk_bf16_f32 v56, v40, v41\n"
    "v_cvt_pk_bf16_f32 v57, v42, v43\n"
    "v_cvt_pk_bf16_f32 v58, v44, 0\n"
    "v_cvt_pk_bf16_f32 v64, 0, v40\n"
    "v_cvt_pk_bf16_f32 v65, v41, v42\n"
    "v_cvt_pk_bf16_f32 v66, v43, v44\n"
    "v_cvt_pk_bf16_f32 v61, v40, v41\n"
    "v_cvt_pk_bf16_f32 v62, v42, v43\n"
    "v_cvt_pk_bf16_f32 v63, v44, 0\n"
    "v_cvt_pk_bf16_f32 v69, 0, v40\n"
    "v_cvt_pk_bf16_f32 v70, v41, v42\n"
    "s_mov_b32 s65, 983040\n"
    "buffer_load_dwordx4 a[248:251], v208, s[40:43], s65 offen\n"
    "ds_write_b128 v221, v[56:59] offset:0\n"
    "v_cvt_pk_bf16_f32 v71, v43, v44\n"
    "v_and_b32_e32 v48, 0xffff0000, v224\n"
    "v_and_b32_e32 v49, 0xffff0000, v225\n"
    "v_and_b32_e32 v50, 0xffff0000, v226\n"
    "v_and_b32_e32 v51, 0xffff0000, v227\n"
    "v_and_b32_e32 v52, 0xffff0000, v228\n"
    "v_and_b32_e32 v53, 0xffff0000, v229\n"
    "v_and_b32_e32 v54, 0xffff0000, v230\n"
    "ds_write_b128 v221, v[64:67] offset:16\n"
    "s_mov_b32 s65, 1081344\n"
    "buffer_load_dwordx4 a[252:255], v208, s[40:43], s65 offen\n"
    "v_and_b32_e32 v55, 0xffff0000, v231\n"
    "v_add_f32_e64 v49, v49, v53\n"
    "v_add_f32_e64 v50, v50, v54\n"
    "v_add_f32_e64 v51, v51, v55\n"
    "ds_write_b128 v221, v[60:63] offset:32\n"
    "v_cvt_pk_bf16_f32 v72, v48, v49\n"
    "v_cvt_pk_bf16_f32 v73, v50, v51\n"
    "v_cvt_pk_bf16_f32 v74, v52, 0\n"
    "v_cvt_pk_bf16_f32 v80, 0, v48\n"
    "v_cvt_pk_bf16_f32 v81, v49, v50\n"
    "v_cvt_pk_bf16_f32 v82, v51, v52\n"
    "ds_write_b128 v221, v[68:71] offset:48\n"
    "s_mov_b32 s65, 1376256\n"
    "buffer_load_dwordx4 a[168:171], v208, s[40:43], s65 offen\n"
    "v_cvt_pk_bf16_f32 v77, v48, v49\n"
    "v_cvt_pk_bf16_f32 v78, v50, v51\n"
    "v_cvt_pk_bf16_f32 v79, v52, 0\n"
    "v_cvt_pk_bf16_f32 v85, 0, v48\n"
    "v_cvt_pk_bf16_f32 v86, v49, v50\n"
    "v_cvt_pk_bf16_f32 v87, v51, v52\n"
    "ds_write_b128 v221, v[72:75] offset:336\n"
    "v_xor_b32_e32 v212, 0, v223\n"
    "s_mov_b32 s65, 1474560\n"
    "buffer_load_dwordx4 a[172:175], v208, s[40:43], s65 offen\n"
    "v_xor_b32_e32 v214, 64, v223\n"
    "v_xor_b32_e32 v215, 96, v223\n"
    "ds_write_b128 v221, v[80:83] offset:352\n"
    "ds_write_b128 v221, v[76:79] offset:368\n"
    "s_add_u32 vcc_lo, s96, 0\n"
    "s_addc_u32 vcc_hi, s97, 0\n"
    "global_load_dwordx4 v[240:243], v213, vcc\n"
    "ds_write_b128 v221, v[84:87] offset:384\n"
    "v_lshl_add_u32 v221, s51, 8, v211\n"
    "s_add_u32 vcc_lo, s96, 8192\n"
    "s_nop 0\n"
    "s_addc_u32 vcc_hi, s97, 0\n"
    "global_load_dwordx4 v[248:251], v213, vcc\n"
    "s_waitcnt vmcnt(12)\n"
    "s_barrier\n"
    "ds_read_b64_tr_b16 a[0:1], v220 offset:0\n"
    "ds_read_b64_tr_b16 a[2:3], v220 offset:3072\n"
    "ds_read_b64_tr_b16 a[4:5], v220 offset:8\n"
    "ds_read_b64_tr_b16 a[6:7], v220 offset:3080\n"
    "ds_read_b64_tr_b16 a[8:9], v220 offset:64\n"
    "ds_read_b64_tr_b16 a[10:11], v220 offset:3136\n"
    "ds_read_b64_tr_b16 a[12:13], v220 offset:72\n"
    "ds_read_b64_tr_b16 a[14:15], v220 offset:3144\n"
    "ds_read_b64_tr_b16 v[168:169], v218 offset:128\n"
    "ds_read_b64_tr_b16 v[170:171], v218 offset:3200\n"
    "ds_read_b64_tr_b16 a[16:17], v220 offset:6144\n"
    "ds_read_b64_tr_b16 a[18:19], v220 offset:9216\n"
    "ds_read_b64_tr_b16 a[20:21], v220 offset:6152\n"
    "ds_read_b64_tr_b16 a[22:23], v220 offset:9224\n"
    "ds_read_b64_tr_b16 a[24:25], v220 offset:6208\n"
    "ds_read_b64_tr_b16 a[26:27], v220 offset:9280\n"
    "ds_read_b64_tr_b16 a[28:29], v220 offset:6216\n"
    "ds_read_b64_tr_b16 a[30:31], v220 offset:9288\n"
    "ds_read_b64_tr_b16 v[172:173], v218 offset:6272\n"
    "ds_read_b64_tr_b16 v[174:175], v218 offset:9344\n"
    "ds_read_b64_tr_b16 a[32:33], v220 offset:12288\n"
    "ds_read_b64_tr_b16 a[34:35], v220 offset:15360\n"
    "s_add_u32 vcc_lo, s96, 12288\n"
    "s_nop 0\n"
    "s_addc_u32 vcc_hi, s97, 0\n"
    "global_load_dwordx4 v[252:255], v213, vcc\n"
    "ds_read_b64_tr_b16 a[36:37], v220 offset:12296\n"
    "ds_read_b64_tr_b16 a[38:39], v220 offset:15368\n"
    "ds_read_b64_tr_b16 a[40:41], v220 offset:12352\n"
    "ds_read_b64_tr_b16 a[42:43], v220 offset:15424\n"
    "ds_read_b64_tr_b16 a[44:45], v220 offset:12360\n"
    "ds_read_b64_tr_b16 a[46:47], v220 offset:15432\n"
    "ds_read_b64_tr_b16 v[176:177], v218 offset:12416\n"
    "ds_read_b64_tr_b16 v[178:179], v218 offset:15488\n"
    "s_waitcnt vmcnt(12)\n"
    "ds_write2_b32 v216, a224, a225 offset0:0 offset1:20\n"
    "ds_write2_b32 v216, a226, a227 offset0:40 offset1:60\n"
    "s_waitcnt lgkmcnt(2)\n"
    "s_barrier\n"
    "s_mov_b32 s65, 0\n"
    "buffer_load_dwordx4 a[176:179], v208, s[40:43], s65 offen\n"
    "s_nop 0\n"
    "s_waitcnt vmcnt(12)\n"
    "ds_write2_b32 v216, a228, a229 offset0:80 offset1:100\n"
    "ds_write2_b32 v216, a230, a231 offset0:120 offset1:140\n"
    "v_add_u32_e32 v216, 1360, v216\n"
    "s_mov_b32 s65, 98304\n"
    "buffer_load_dwordx4 a[180:183], v208, s[40:43], s65 offen\n"
    "s_waitcnt vmcnt(12)\n"
    "ds_write2_b32 v216, a232, a233 offset0:0 offset1:20\n"
    "ds_write2_b32 v216, a234, a235 offset0:40 offset1:60\n"
    "s_mov_b32 s65, 393216\n"
    "buffer_load_dwordx4 a[184:187], v208, s[40:43], s65 offen\n"
    "s_waitcnt vmcnt(12)\n"
    "ds_write2_b32 v216, a236, a237 offset0:80 offset1:100\n"
    "ds_write2_b32 v216, a238, a239 offset0:120 offset1:140\n"
    "v_add_u32_e32 v216, 1360, v216\n"
    "s_mov_b32 s65, 491520\n"
    "buffer_load_dwordx4 a[188:191], v208, s[40:43], s65 offen\n"
    "s_waitcnt vmcnt(12)\n"
    "ds_write2_b32 v216, a240, a241 offset0:0 offset1:20\n"
    "ds_write2_b32 v216, a242, a243 offset0:40 offset1:60\n"
    "s_mov_b32 s65, 786432\n"
    "buffer_load_dwordx4 a[192:195], v208, s[40:43], s65 offen\n"
    "s_waitcnt vmcnt(12)\n"
    "ds_write2_b32 v216, a244, a245 offset0:80 offset1:100\n"
    "ds_write2_b32 v216, a246, a247 offset0:120 offset1:140\n"
    "v_add_u32_e32 v216, 1360, v216\n"
    "s_mov_b32 s65, 884736\n"
    "buffer_load_dwordx4 a[196:199], v208, s[40:43], s65 offen\n"
    "s_waitcnt vmcnt(12)\n"
    "ds_write2_b32 v216, a248, a249 offset0:0 offset1:20\n"
    "ds_write2_b32 v216, a250, a251 offset0:40 offset1:60\n"
    "s_mov_b32 s65, 1179648\n"
    "buffer_load_dwordx4 a[200:203], v208, s[40:43], s65 offen\n"
    "s_waitcnt vmcnt(12)\n"
    "ds_write2_b32 v216, a252, a253 offset0:80 offset1:100\n"
    "ds_write2_b32 v216, a254, a255 offset0:120 offset1:140\n"
    "v_add_u32_e32 v216, 1360, v216\n"
    "s_mov_b32 s65, 1277952\n"
    "buffer_load_dwordx4 a[204:207], v208, s[40:43], s65 offen\n"
    "s_waitcnt vmcnt(12)\n"
    "ds_write2_b32 v216, a168, a169 offset0:0 offset1:20\n"
    "ds_write2_b32 v216, a170, a171 offset0:40 offset1:60\n"
    "s_mov_b32 s65, 1572864\n"
    "buffer_load_dwordx4 a[208:211], v208, s[40:43], s65 offen\n"
    "s_waitcnt vmcnt(12)\n"
    "ds_write2_b32 v216, a172, a173 offset0:80 offset1:100\n"
    "ds_write2_b32 v216, a174, a175 offset0:120 offset1:140\n"
    "v_add_u32_e32 v216, -4800, v216\n"
    "s_mov_b32 s65, 1671168\n"
    "buffer_load_dwordx4 a[212:215], v208, s[40:43], s65 offen\n"
    "v_xor_b32_e32 v208, 0, v219\n"
    "s_waitcnt lgkmcnt(0)\n"
    "s_barrier\n"
    "s_add_u32 m0, s38, 0\n"
    "s_mov_b32 s64, 18432\n"
    "buffer_load_dwordx4 v210, s[84:87], s64 offen offset:0 lds\n"
    "s_nop 0\n"
    "s_waitcnt vmcnt(12)\n"
    "ds_read_b128 a[224:227], v250 offset:0\n"
    "ds_read_b128 a[228:231], v250 offset:320\n"
    "ds_read_b128 a[232:235], v251 offset:0\n"
    "ds_read_b128 a[236:239], v251 offset:336\n"
    "ds_read_b128 a[240:243], v250 offset:80\n"
    "ds_read_b128 a[244:247], v250 offset:400\n"
    "ds_read_b128 a[248:251], v251 offset:672\n"
    "ds_read_b128 a[252:255], v251 offset:1008\n"
    "buffer_load_dwordx4 v210, s[84:87], s64 offen offset:1024 lds\n"
    "s_waitcnt vmcnt(12)\n"
    "v_mov_b32_e32 v222, v254\n"
    "v_mov_b32_e64 v254, 0\n"
    "ds_read_b128 a[96:99], v250 offset:160\n"
    "ds_read_b128 a[100:103], v250 offset:480\n"
    "v_mfma_f32_32x32x1_2b_f32 v[0:31], v254, v254, 0\n"
    "ds_read_b128 a[104:107], v251 offset:1344\n"
    "ds_read_b128 a[108:111], v251 offset:1680\n"
    "ds_read_b128 a[112:115], v250 offset:240\n"
    "v_add_u32_e64 v255, s99, v255\n"
    "ds_read_b128 a[116:119], v250 offset:560\n"
    "ds_read_b128 a[120:123], v251 offset:2016\n"
    "ds_read_b128 a[124:127], v251 offset:2352\n"
    "v_mfma_f32_32x32x1_2b_f32 v[32:63], v254, v254, 0\n"
    "s_waitcnt lgkmcnt(13)\n"
    "s_nop 8\n"
    "v_smfmac_f32_16x16x64_bf16 v[0:3], a[232:235], a[224:231], v240\n"
    "s_waitcnt lgkmcnt(9)\n"
    "ds_read_b128 a[232:235], v251 offset:10752\n"
    "v_mfma_f32_32x32x1_2b_f32 v[64:95], v254, v254, 0\n"
    "s_waitcnt lgkmcnt(9)\n"
    "v_add_u32_e64 v254, s54, v242\n"
    "buffer_load_dwordx4 v210, s[84:87], s64 offen offset:2048 lds\n"
    "s_waitcnt vmcnt(12)\n"
    "ds_write2_b32 v216, a176, a177 offset0:0 offset1:20\n"
    "v_smfmac_f32_16x16x64_bf16 v[4:7], a[236:239], a[224:231], v240 abid:1\n"
    "s_waitcnt lgkmcnt(6)\n"
    "ds_read_b128 a[224:227], v250 offset:27024\n"
    "v_smfmac_f32_16x16x64_bf16 v[8:11], a[248:251], a[240:247], v240\n"
    "ds_read_b128 a[228:231], v250 offset:27344\n"
    "v_smfmac_f32_16x16x64_bf16 v[12:15], a[252:255], a[240:247], v240 abid:1\n"
    "ds_read_b128 a[236:239], v251 offset:11088\n"
    "v_smfmac_f32_16x16x64_bf16 v[16:19], a[104:107], a[96:103], v240\n"
    "s_waitcnt lgkmcnt(6)\n"
    "ds_write2_b32 v216, a178, a179 offset0:40 offset1:60\n"
    "v_smfmac_f32_16x16x64_bf16 v[20:23], a[108:111], a[96:103], v240 abid:1\n"
    "s_waitcnt lgkmcnt(6)\n"
    "v_smfmac_f32_16x16x64_bf16 v[24:27], a[120:123], a[112:119], v240\n"
    "ds_read_b128 a[240:243], v250 offset:27104\n"
    "v_smfmac_f32_16x16x64_bf16 v[28:31], a[124:127], a[112:119], v240 abid:1\n"
    "ds_read_b128 a[244:247], v250 offset:27424\n"
    "ds_read_b128 a[248:251], v251 offset:11424\n"
    "ds_read_b128 a[252:255], v251 offset:11760\n"
    "ds_read_b128 a[96:99], v250 offset:27184\n"
    "ds_read_b128 a[100:103], v250 offset:27504\n"
    "ds_read_b128 a[104:107], v251 offset:12096\n"
    "s_waitcnt lgkmcnt(9)\n"
    "v_smfmac_f32_16x16x64_bf16 v[32:35], a[232:235], a[224:231], v240\n"
    "s_add_u32 m0, s38, 3072\n"
    "s_mov_b32 s64, 21504\n"
    "buffer_load_dwordx4 v210, s[84:87], s64 offen offset:0 lds\n"
    "s_waitcnt lgkmcnt(7)\n"
    "v_smfmac_f32_16x16x64_bf16 v[36:39], a[236:239], a[224:231], v240 abid:1\n"
    "ds_read_b128 a[108:111], v251 offset:12432\n"
    "ds_read_b128 a[112:115], v250 offset:27264\n"
    "ds_read_b128 a[116:119], v250 offset:27584\n"
    "ds_read_b128 a[120:123], v251 offset:12768\n"
    "ds_read_b128 a[124:127], v251 offset:13104\n"
    "ds_read_b128 a[224:227], v250 offset:54048\n"
    "ds_read_b128 a[228:231], v250 offset:54368\n"
    "ds_read_b128 a[232:235], v251 offset:21504\n"
    "ds_read_b128 a[236:239], v251 offset:21840\n"
    "s_waitcnt vmcnt(12)\n"
    "ds_write2_b32 v216, a180, a181 offset0:80 offset1:100\n"
    "s_waitcnt lgkmcnt(14)\n"
    "v_smfmac_f32_16x16x64_bf16 v[40:43], a[248:251], a[240:247], v240\n"
    "s_waitcnt lgkmcnt(13)\n"
    "v_smfmac_f32_16x16x64_bf16 v[44:47], a[252:255], a[240:247], v240 abid:1\n"
    "s_nop 0\n"
    "ds_read_b128 a[240:243], v250 offset:54128\n"
    "ds_read_b128 a[244:247], v250 offset:54448\n"
    "ds_read_b128 a[248:251], v251 offset:22176\n"
    "ds_read_b128 a[252:255], v251 offset:22512\n"
    "s_waitcnt lgkmcnt(14)\n"
    "v_smfmac_f32_16x16x64_bf16 v[48:51], a[104:107], a[96:103], v240\n"
    "s_waitcnt lgkmcnt(13)\n"
    "ds_write2_b32 v216, a182, a183 offset0:120 offset1:140\n"
    "v_smfmac_f32_16x16x64_bf16 v[52:55], a[108:111], a[96:103], v240 abid:1\n"
    "v_add_u32_e32 v216, 1360, v216\n"
    "ds_read_b128 a[96:99], v250 offset:54208\n"
    "buffer_load_dwordx4 v210, s[84:87], s64 offen offset:1024 lds\n"
    "ds_read_b128 a[100:103], v250 offset:54528\n"
    "s_waitcnt lgkmcnt(13)\n"
    "v_smfmac_f32_16x16x64_bf16 v[56:59], a[120:123], a[112:119], v240\n"
    "s_waitcnt lgkmcnt(12)\n"
    "ds_read_b128 a[104:107], v251 offset:22848\n"
    "v_smfmac_f32_16x16x64_bf16 v[60:63], a[124:127], a[112:119], v240 abid:1\n"
    "ds_read_b128 a[108:111], v251 offset:23184\n"
    "ds_read_b128 a[112:115], v250 offset:54288\n"
    "ds_read_b128 a[116:119], v250 offset:54608\n"
    "ds_read_b128 a[120:123], v251 offset:23520\n"
    "s_waitcnt lgkmcnt(14)\n"
    "v_smfmac_f32_16x16x64_bf16 v[64:67], a[232:235], a[224:231], v240\n"
    "s_waitcnt lgkmcnt(13)\n"
    "ds_read_b128 a[124:127], v251 offset:23856\n"
    "v_smfmac_f32_16x16x64_bf16 v[68:71], a[236:239], a[224:231], v240 abid:1\n"
    "s_waitcnt vmcnt(12)\n"
    "ds_write2_b32 v216, a184, a185 offset0:0 offset1:20\n"
    "ds_write2_b32 v216, a186, a187 offset0:40 offset1:60\n"
    "s_waitcnt lgkmcnt(12)\n"
    "v_smfmac_f32_16x16x64_bf16 v[72:75], a[248:251], a[240:247], v240\n"
    "ds_read_b128 a[232:235], v252 offset:0\n"
    "s_waitcnt lgkmcnt(12)\n"
    "v_smfmac_f32_16x16x64_bf16 v[76:79], a[252:255], a[240:247], v240 abid:1\n"
    "s_nop 0\n"
    "ds_read_b128 a[236:239], v252 offset:336\n"
    "ds_read_b128 a[248:251], v252 offset:672\n"
    "ds_read_b128 a[252:255], v252 offset:1008\n"
    "buffer_load_dwordx4 v210, s[84:87], s64 offen offset:2048 lds\n"
    "s_waitcnt vmcnt(12)\n"
    "ds_write2_b32 v216, a188, a189 offset0:80 offset1:100\n"
    "s_waitcnt lgkmcnt(12)\n"
    "v_smfmac_f32_16x16x64_bf16 v[80:83], a[104:107], a[96:103], v240\n"
    "s_nop 0\n"
    "ds_write2_b32 v216, a190, a191 offset0:120 offset1:140\n"
    "s_waitcnt lgkmcnt(12)\n"
    "v_smfmac_f32_16x16x64_bf16 v[84:87], a[108:111], a[96:103], v240 abid:1\n"
    "v_add_u32_e32 v216, 1360, v216\n"
    "s_waitcnt lgkmcnt(9)\n"
    "v_smfmac_f32_16x16x64_bf16 v[88:91], a[120:123], a[112:119], v240\n"
    "s_waitcnt lgkmcnt(8)\n"
    "v_smfmac_f32_16x16x64_bf16 v[92:95], a[124:127], a[112:119], v240 abid:1\n"
    "s_add_u32 m0, s38, 6144\n"
    "s_mov_b32 s64, 24576\n"
    "buffer_load_dwordx4 v210, s[84:87], s64 offen offset:0 lds\n"
    "s_waitcnt vmcnt(12)\n"
    "ds_write2_b32 v216, a192, a193 offset0:0 offset1:20\n"
    "ds_write2_b32 v216, a194, a195 offset0:40 offset1:60\n"
    "buffer_load_dwordx4 v210, s[84:87], s64 offen offset:1024 lds\n"
    "s_waitcnt vmcnt(12)\n"
    "ds_write2_b32 v216, a196, a197 offset0:80 offset1:100\n"
    "ds_write2_b32 v216, a198, a199 offset0:120 offset1:140\n"
    "v_add_u32_e32 v216, 1360, v216\n"
    "buffer_load_dwordx4 v210, s[84:87], s64 offen offset:2048 lds\n"
    "s_waitcnt vmcnt(12)\n"
    "ds_write2_b32 v216, a200, a201 offset0:0 offset1:20\n"
    "v_xor_b32_e32 v210, 64, v219\n"
    "ds_write2_b32 v216, a202, a203 offset0:40 offset1:60\n"
    "s_add_u32 vcc_lo, s96, 28672\n"
    "s_addc_u32 vcc_hi, s97, 0\n"
    "global_load_dwordx4 v[200:203], v213, vcc\n"
    "s_waitcnt vmcnt(12)\n"
    "ds_write2_b32 v216, a204, a205 offset0:80 offset1:100\n"
    "ds_write2_b32 v216, a206, a207 offset0:120 offset1:140\n"
    "v_add_u32_e32 v216, 1360, v216\n"
    "s_add_u32 vcc_lo, s96, 32768\n"
    "s_addc_u32 vcc_hi, s97, 0\n"
    "global_load_dwordx4 v[204:207], v213, vcc\n"
    "s_waitcnt vmcnt(12)\n"
    "ds_write2_b32 v216, a208, a209 offset0:0 offset1:20\n"
    "ds_write2_b32 v216, a210, a211 offset0:40 offset1:60\n"
    "s_mov_b32 s59, 49152\n"
    "buffer_load_dwordx4 v[96:99], v209, s[80:83], s59 offen offset:0\n"
    "s_nop 0\n"
    "s_waitcnt vmcnt(12)\n"
    "ds_write2_b32 v216, a212, a213 offset0:80 offset1:100\n"
    "ds_write2_b32 v216, a214, a215 offset0:120 offset1:140\n"
    "buffer_load_dwordx4 v[100:103], v209, s[80:83], s59 offen offset:256\n"
    "s_waitcnt lgkmcnt(0)\n"
    "s_barrier\n"
    "buffer_load_dwordx4 v[104:107], v209, s[80:83], s59 offen offset:512\n"
    "ds_read_b128 a[224:227], v250 offset:640\n"
    "ds_read_b128 a[228:231], v250 offset:960\n"
    "ds_read_b128 a[240:243], v250 offset:720\n"
    "ds_read_b128 a[244:247], v250 offset:1040\n"
    "s_mov_b32 s59, 52224\n"
    "buffer_load_dwordx4 v[108:111], v209, s[80:83], s59 offen offset:0\n"
    "s_waitcnt lgkmcnt(2)\n"
    "v_smfmac_f32_16x16x64_bf16 v[0:3], a[232:235], a[224:231], v240\n"
    "s_nop 0\n"
    "buffer_load_dwordx4 v[112:115], v209, s[80:83], s59 offen offset:256\n"
    "v_smfmac_f32_16x16x64_bf16 v[4:7], a[236:239], a[224:231], v240 abid:1\n"
    "ds_read_b128 a[224:227], v250 offset:800\n"
    "s_waitcnt lgkmcnt(1)\n"
    "v_smfmac_f32_16x16x64_bf16 v[8:11], a[248:251], a[240:247], v240\n"
    "s_nop 0\n"
    "ds_read_b128 a[228:231], v250 offset:1120\n"
    "v_smfmac_f32_16x16x64_bf16 v[12:15], a[252:255], a[240:247], v240 abid:1\n"
    "ds_read_b128 a[232:235], v252 offset:1344\n"
    "ds_read_b128 a[236:239], v252 offset:1680\n"
    "ds_read_b128 a[240:243], v250 offset:880\n"
    "v_pk_mov_b32 v[224:225], v[0:1], v[0:1] op_sel:[0,1]\n"
    "v_pk_mov_b32 v[226:227], v[2:3], v[2:3] op_sel:[0,1]\n"
    "v_pk_mul_f32 v[228:229], v[0:1], v[0:1]\n"
    "v_pk_mul_f32 v[230:231], v[2:3], v[2:3]\n"
    "buffer_load_dwordx4 v[116:119], v209, s[80:83], s59 offen offset:512\n"
    "v_pk_add_f32 v[224:225], v[224:225], v[4:5]\n"
    "v_pk_add_f32 v[226:227], v[226:227], v[6:7]\n"
    "v_pk_fma_f32 v[228:229], v[4:5], v[4:5], v[228:229]\n"
    "v_pk_fma_f32 v[230:231], v[6:7], v[6:7], v[230:231]\n"
    "v_pk_add_f32 v[224:225], v[224:225], v[8:9]\n"
    "v_pk_add_f32 v[226:227], v[226:227], v[10:11]\n"
    "v_pk_fma_f32 v[228:229], v[8:9], v[8:9], v[228:229]\n"
    "v_pk_fma_f32 v[230:231], v[10:11], v[10:11], v[230:231]\n"
    "v_pk_add_f32 v[224:225], v[224:225], v[12:13]\n"
    "v_pk_add_f32 v[226:227], v[226:227], v[14:15]\n"
    "v_pk_fma_f32 v[228:229], v[12:13], v[12:13], v[228:229]\n"
    "v_pk_fma_f32 v[230:231], v[14:15], v[14:15], v[230:231]\n"
    "ds_read_b128 a[244:247], v250 offset:1200\n"
    "ds_read_b128 a[248:251], v252 offset:2016\n"
    "ds_read_b128 a[252:255], v252 offset:2352\n"
    "s_mov_b32 s59, 0\n"
    "buffer_load_dwordx4 v[120:123], v209, s[80:83], s59 offen offset:0\n"
    "s_waitcnt lgkmcnt(5)\n"
    "v_smfmac_f32_16x16x64_bf16 v[16:19], a[232:235], a[224:231], v240\n"
    "ds_read_b128 a[232:235], v252 offset:10752\n"
    "s_waitcnt lgkmcnt(5)\n"
    "v_smfmac_f32_16x16x64_bf16 v[20:23], a[236:239], a[224:231], v240 abid:1\n"
    "ds_read_b128 a[224:227], v250 offset:27664\n"
    "ds_read_b128 a[228:231], v250 offset:27984\n"
    "ds_read_b128 a[236:239], v252 offset:11088\n"
    "s_nop 1\n"
    "v_pk_add_f32 v[224:225], v[224:225], v[16:17]\n"
    "v_pk_add_f32 v[226:227], v[226:227], v[18:19]\n"
    "v_pk_fma_f32 v[228:229], v[16:17], v[16:17], v[228:229]\n"
    "v_pk_fma_f32 v[230:231], v[18:19], v[18:19], v[230:231]\n"
    "v_pk_add_f32 v[224:225], v[224:225], v[20:21]\n"
    "v_pk_add_f32 v[226:227], v[226:227], v[22:23]\n"
    "buffer_load_dwordx4 v[124:127], v209, s[80:83], s59 offen offset:256\n"
    "v_pk_fma_f32 v[228:229], v[20:21], v[20:21], v[228:229]\n"
    "v_pk_fma_f32 v[230:231], v[22:23], v[22:23], v[230:231]\n"
    "s_waitcnt lgkmcnt(5)\n"
    "v_smfmac_f32_16x16x64_bf16 v[24:27], a[248:251], a[240:247], v240\n"
    "s_nop 0\n"
    "ds_read_b128 a[248:251], v252 offset:11424\n"
    "s_waitcnt lgkmcnt(5)\n"
    "v_smfmac_f32_16x16x64_bf16 v[28:31], a[252:255], a[240:247], v240 abid:1\n"
    "ds_read_b128 a[240:243], v250 offset:27744\n"
    "ds_read_b128 a[244:247], v250 offset:28064\n"
    "ds_read_b128 a[252:255], v252 offset:11760\n"
    "buffer_load_dwordx4 v[128:131], v209, s[80:83], s59 offen offset:512\n"
    "s_nop 0\n"
    "v_pk_add_f32 v[224:225], v[224:225], v[24:25]\n"
    "v_pk_add_f32 v[226:227], v[226:227], v[26:27]\n"
    "v_pk_fma_f32 v[228:229], v[24:25], v[24:25], v[228:229]\n"
    "v_pk_fma_f32 v[230:231], v[26:27], v[26:27], v[230:231]\n"
    "v_pk_add_f32 v[224:225], v[224:225], v[28:29]\n"
    "v_pk_add_f32 v[226:227], v[226:227], v[30:31]\n"
    "v_pk_fma_f32 v[228:229], v[28:29], v[28:29], v[228:229]\n"
    "v_pk_fma_f32 v[230:231], v[30:31], v[30:31], v[230:231]\n"
    "s_waitcnt lgkmcnt(4)\n"
    "v_smfmac_f32_16x16x64_bf16 v[32:35], a[232:235], a[224:231], v240\n"
    "s_nop 0\n"
    "ds_read_b128 a[232:235], v252 offset:12096\n"
    "v_smfmac_f32_16x16x64_bf16 v[36:39], a[236:239], a[224:231], v240 abid:1\n"
    "ds_read_b128 a[224:227], v250 offset:27824\n"
    "ds_read_b128 a[228:231], v250 offset:28144\n"
    "ds_read_b128 a[236:239], v252 offset:12432\n"
    "s_mov_b32 s59, 3072\n"
    "buffer_load_dwordx4 v[132:135], v209, s[80:83], s59 offen offset:0\n"
    "s_waitcnt vmcnt(12)\n"
    "s_barrier\n"
    "v_pk_add_f32 v[224:225], v[224:225], v[32:33]\n"
    "v_pk_add_f32 v[226:227], v[226:227], v[34:35]\n"
    "v_pk_fma_f32 v[228:229], v[32:33], v[32:33], v[228:229]\n"
    "v_pk_fma_f32 v[230:231], v[34:35], v[34:35], v[230:231]\n"
    "v_pk_add_f32 v[224:225], v[224:225], v[36:37]\n"
    "v_pk_add_f32 v[226:227], v[226:227], v[38:39]\n"
    "v_pk_fma_f32 v[228:229], v[36:37], v[36:37], v[228:229]\n"
    "v_pk_fma_f32 v[230:231], v[38:39], v[38:39], v[230:231]\n"
    "s_waitcnt lgkmcnt(4)\n"
    "v_smfmac_f32_16x16x64_bf16 v[40:43], a[248:251], a[240:247], v240\n"
    "s_nop 0\n"
    "buffer_load_dwordx4 v[136:139], v209, s[80:83], s59 offen offset:256\n"
    "v_smfmac_f32_16x16x64_bf16 v[44:47], a[252:255], a[240:247], v240 abid:1\n"
    "ds_read_b128 a[240:243], v250 offset:27904\n"
    "ds_read_b128 a[244:247], v250 offset:28224\n"
    "ds_read_b128 a[248:251], v252 offset:12768\n"
    "ds_read_b128 a[252:255], v252 offset:13104\n"
    "ds_read_b64_tr_b16 a[48:49], v220 offset:0\n"
    "s_waitcnt lgkmcnt(6)\n"
    "v_smfmac_f32_16x16x64_bf16 v[48:51], a[232:235], a[224:231], v240\n"
    "s_waitcnt lgkmcnt(5)\n"
    "ds_read_b128 a[232:235], v252 offset:21504\n"
    "v_smfmac_f32_16x16x64_bf16 v[52:55], a[236:239], a[224:231], v240 abid:1\n"
    "ds_read_b128 a[224:227], v250 offset:54688\n"
    "ds_read_b128 a[228:231], v250 offset:55008\n"
    "buffer_load_dwordx4 v[140:143], v209, s[80:83], s59 offen offset:512\n"
    "ds_read_b128 a[236:239], v252 offset:21840\n"
    "v_pk_add_f32 v[224:225], v[224:225], v[40:41]\n"
    "v_pk_add_f32 v[226:227], v[226:227], v[42:43]\n"
    "v_pk_fma_f32 v[228:229], v[40:41], v[40:41], v[228:229]\n"
    "v_pk_fma_f32 v[230:231], v[42:43], v[42:43], v[230:231]\n"
    "v_pk_add_f32 v[224:225], v[224:225], v[44:45]\n"
    "v_pk_add_f32 v[226:227], v[226:227], v[46:47]\n"
    "v_pk_fma_f32 v[228:229], v[44:45], v[44:45], v[228:229]\n"
    "v_pk_fma_f32 v[230:231], v[46:47], v[46:47], v[230:231]\n"
    "v_pk_add_f32 v[224:225], v[224:225], v[48:49]\n"
    "v_pk_add_f32 v[226:227], v[226:227], v[50:51]\n"
    "v_pk_fma_f32 v[228:229], v[48:49], v[48:49], v[228:229]\n"
    "v_pk_fma_f32 v[230:231], v[50:51], v[50:51], v[230:231]\n"
    "v_pk_add_f32 v[224:225], v[224:225], v[52:53]\n"
    "v_pk_add_f32 v[226:227], v[226:227], v[54:55]\n"
    "v_pk_fma_f32 v[228:229], v[52:53], v[52:53], v[228:229]\n"
    "v_pk_fma_f32 v[230:231], v[54:55], v[54:55], v[230:231]\n"
    "ds_read_b64_tr_b16 a[50:51], v220 offset:3072\n"
    "s_mov_b32 s59, 24576\n"
    "buffer_load_dwordx4 v[144:147], v209, s[80:83], s59 offen offset:0\n"
    "s_waitcnt lgkmcnt(7)\n"
    "v_smfmac_f32_16x16x64_bf16 v[56:59], a[248:251], a[240:247], v240\n"
    "s_waitcnt lgkmcnt(6)\n"
    "ds_read_b128 a[248:251], v252 offset:22176\n"
    "v_smfmac_f32_16x16x64_bf16 v[60:63], a[252:255], a[240:247], v240 abid:1\n"
    "s_nop 0\n"
    "ds_read_b128 a[240:243], v250 offset:54768\n"
    "ds_read_b128 a[244:247], v250 offset:55088\n"
    "ds_read_b128 a[252:255], v252 offset:22512\n"
    "s_waitcnt vmcnt(12)\n"
    "v_or3_b32 v200, v200, v96, v97\n"
    "v_pk_add_f32 v[224:225], v[224:225], v[56:57]\n"
    "v_pk_add_f32 v[226:227], v[226:227], v[58:59]\n"
    "v_pk_fma_f32 v[228:229], v[56:57], v[56:57], v[228:229]\n"
    "v_pk_fma_f32 v[230:231], v[58:59], v[58:59], v[230:231]\n"
    "v_pk_add_f32 v[224:225], v[224:225], v[60:61]\n"
    "v_pk_add_f32 v[226:227], v[226:227], v[62:63]\n"
    "buffer_load_dwordx4 v[148:151], v209, s[80:83], s59 offen offset:256\n"
    "v_pk_fma_f32 v[228:229], v[60:61], v[60:61], v[228:229]\n"
    "v_pk_fma_f32 v[230:231], v[62:63], v[62:63], v[230:231]\n"
    "s_waitcnt lgkmcnt(5)\n"
    "v_smfmac_f32_16x16x64_bf16 v[64:67], a[232:235], a[224:231], v240\n"
    "s_nop 0\n"
    "ds_read_b128 a[232:235], v252 offset:22848\n"
    "v_smfmac_f32_16x16x64_bf16 v[68:71], a[236:239], a[224:231], v240 abid:1\n"
    "ds_read_b128 a[224:227], v250 offset:54848\n"
    "ds_read_b128 a[228:231], v250 offset:55168\n"
    "ds_read_b128 a[236:239], v252 offset:23184\n"
    "v_or3_b32 v200, v200, v98, v99\n"
    "ds_read_b64_tr_b16 a[52:53], v220 offset:8\n"
    "ds_read_b64_tr_b16 a[54:55], v220 offset:3080\n"
    "v_pk_add_f32 v[224:225], v[224:225], v[64:65]\n"
    "buffer_load_dwordx4 v[152:155], v209, s[80:83], s59 offen offset:512\n"
    "v_pk_add_f32 v[226:227], v[226:227], v[66:67]\n"
    "v_pk_fma_f32 v[228:229], v[64:65], v[64:65], v[228:229]\n"
    "v_pk_fma_f32 v[230:231], v[66:67], v[66:67], v[230:231]\n"
    "v_pk_add_f32 v[224:225], v[224:225], v[68:69]\n"
    "v_pk_add_f32 v[226:227], v[226:227], v[70:71]\n"
    "v_pk_fma_f32 v[228:229], v[68:69], v[68:69], v[228:229]\n"
    "v_pk_fma_f32 v[230:231], v[70:71], v[70:71], v[230:231]\n"
    "s_waitcnt lgkmcnt(6)\n"
    "v_smfmac_f32_16x16x64_bf16 v[72:75], a[248:251], a[240:247], v240\n"
    "s_nop 0\n"
    "ds_read_b128 a[248:251], v252 offset:23520\n"
    "v_smfmac_f32_16x16x64_bf16 v[76:79], a[252:255], a[240:247], v240 abid:1\n"
    "ds_read_b128 a[240:243], v250 offset:54928\n"
    "ds_read_b128 a[244:247], v250 offset:55248\n"
    "ds_read_b128 a[252:255], v252 offset:23856\n"
    "s_mov_b32 s59, 27648\n"
    "buffer_load_dwordx4 v[156:159], v209, s[80:83], s59 offen offset:0\n"
    "ds_read_b64_tr_b16 a[56:57], v220 offset:64\n"
    "v_pk_add_f32 v[224:225], v[224:225], v[72:73]\n"
    "v_pk_add_f32 v[226:227], v[226:227], v[74:75]\n"
    "v_pk_fma_f32 v[228:229], v[72:73], v[72:73], v[228:229]\n"
    "v_pk_fma_f32 v[230:231], v[74:75], v[74:75], v[230:231]\n"
    "v_pk_add_f32 v[224:225], v[224:225], v[76:77]\n"
    "v_pk_add_f32 v[226:227], v[226:227], v[78:79]\n"
    "v_pk_fma_f32 v[228:229], v[76:77], v[76:77], v[228:229]\n"
    "v_pk_fma_f32 v[230:231], v[78:79], v[78:79], v[230:231]\n"
    "s_waitcnt lgkmcnt(7)\n"
    "v_smfmac_f32_16x16x64_bf16 v[80:83], a[232:235], a[224:231], v240\n"
    "s_nop 0\n"
    "ds_read_b64_tr_b16 a[58:59], v220 offset:3136\n"
    "v_smfmac_f32_16x16x64_bf16 v[84:87], a[236:239], a[224:231], v240 abid:1\n"
    "ds_read_b64_tr_b16 a[60:61], v220 offset:72\n"
    "ds_read_b64_tr_b16 a[62:63], v220 offset:3144\n"
    "buffer_load_dwordx4 v[160:163], v209, s[80:83], s59 offen offset:256\n"
    "ds_read_b64_tr_b16 v[188:189], v218 offset:128\n"
    "ds_read_b64_tr_b16 v[190:191], v218 offset:3200\n"
    "v_and_b32_e32 v232, 0x7fff7fff, v96\n"
    "v_pk_add_f32 v[224:225], v[224:225], v[80:81]\n"
    "v_pk_add_f32 v[226:227], v[226:227], v[82:83]\n"
    "v_pk_fma_f32 v[228:229], v[80:81], v[80:81], v[228:229]\n"
    "v_pk_fma_f32 v[230:231], v[82:83], v[82:83], v[230:231]\n"
    "v_pk_add_f32 v[224:225], v[224:225], v[84:85]\n"
    "v_pk_add_f32 v[226:227], v[226:227], v[86:87]\n"
    "v_pk_fma_f32 v[228:229], v[84:85], v[84:85], v[228:229]\n"
    "v_pk_fma_f32 v[230:231], v[86:87], v[86:87], v[230:231]\n"
    "s_waitcnt lgkmcnt(7)\n"
    "v_smfmac_f32_16x16x64_bf16 v[88:91], a[248:251], a[240:247], v240\n"
    "s_waitcnt lgkmcnt(6)\n"
    "ds_read_b64_tr_b16 a[64:65], v220 offset:6144\n"
    "v_smfmac_f32_16x16x64_bf16 v[92:95], a[252:255], a[240:247], v240 abid:1\n"
    "ds_read_b64_tr_b16 a[66:67], v220 offset:9216\n"
    "buffer_load_dwordx4 v[164:167], v209, s[80:83], s59 offen offset:512\n"
    "ds_read_b64_tr_b16 a[68:69], v220 offset:6152\n"
    "ds_read_b64_tr_b16 a[70:71], v220 offset:9224\n"
    "ds_read_b64_tr_b16 a[72:73], v220 offset:6208\n"
    "v_pk_add_f32 v[224:225], v[224:225], v[88:89]\n"
    "v_pk_add_f32 v[226:227], v[226:227], v[90:91]\n"
    "v_pk_fma_f32 v[228:229], v[88:89], v[88:89], v[228:229]\n"
    "v_pk_fma_f32 v[230:231], v[90:91], v[90:91], v[230:231]\n"
    "v_pk_add_f32 v[224:225], v[224:225], v[92:93]\n"
    "v_pk_add_f32 v[226:227], v[226:227], v[94:95]\n"
    "v_pk_fma_f32 v[228:229], v[92:93], v[92:93], v[228:229]\n"
    "v_pk_fma_f32 v[230:231], v[94:95], v[94:95], v[230:231]\n"
    "ds_write_b128 v254, v[224:227]\n"
    "v_xor_b32_e32 v209, 32, v219\n"
    "v_lshl_add_u32 v216, s51, 8, v209\n"
    "v_add_u32_e32 v96, v201, v96\n"
    "s_add_u32 vcc_lo, s96, 4096\n"
    "s_addc_u32 vcc_hi, s97, 0\n"
    "global_load_dwordx4 v[244:247], v213, vcc\n"
    "ds_write_b128 v254, v[228:231] offset:1024\n"
    "v_lshrrev_b32_e32 v254, 2, v242\n"
    "v_add_u32_e64 v254, s55, v254\n"
    "v_add_u32_e32 v98, v201, v98\n"
    "s_waitcnt vmcnt(17)\n"
    "v_or3_b32 v200, v200, v100, v101\n"
    "v_or3_b32 v200, v200, v102, v103\n"
    "v_add_u32_e64 v100, v203, v100\n"
    "v_add_u32_e64 v102, v203, v102\n"
    "ds_read_b64_tr_b16 a[74:75], v220 offset:9280\n"
    "ds_read_b64_tr_b16 a[76:77], v220 offset:6216\n"
    "ds_read_b64_tr_b16 a[78:79], v220 offset:9288\n"
    "s_add_u32 vcc_lo, s96, 24576\n"
    "s_nop 0\n"
    "s_addc_u32 vcc_hi, s97, 0\n"
    "global_load_dwordx4 v[236:239], v213, vcc\n"
    "ds_read_b64_tr_b16 v[192:193], v218 offset:6272\n"
    "ds_read_b64_tr_b16 v[194:195], v218 offset:9344\n"
    "ds_read_b64_tr_b16 a[80:81], v220 offset:12288\n"
    "v_xor_b32_e32 v213, 32, v223\n"
    "ds_read_b64_tr_b16 a[82:83], v220 offset:15360\n"
    "ds_read_b64_tr_b16 a[84:85], v220 offset:12296\n"
    "ds_read_b64_tr_b16 a[86:87], v220 offset:15368\n"
    "s_waitcnt vmcnt(17)\n"
    "v_or3_b32 v200, v200, v104, v105\n"
    "ds_read_b64_tr_b16 a[88:89], v220 offset:12352\n"
    "s_waitcnt lgkmcnt(10)\n"
    "s_barrier\n"
    "ds_read_b32 v224, v254 offset:0\n"
    "ds_read_b32 v225, v254 offset:6752\n"
    "ds_read_b32 v226, v254 offset:13504\n"
    "v_or3_b32 v200, v200, v106, v107\n"
    "ds_read_b32 v227, v254 offset:20256\n"
    "ds_read_b32 v228, v254 offset:1024\n"
    "ds_read_b32 v229, v254 offset:7776\n"
    "v_add_u32_e64 v104, v205, v104\n"
    "ds_read_b32 v230, v254 offset:14528\n"
    "ds_read_b32 v231, v254 offset:21280\n"
    "ds_read_b64_tr_b16 a[90:91], v220 offset:15424\n"
    "v_add_u32_e32 v106, v205, v106\n"
    "ds_read_b64_tr_b16 a[92:93], v220 offset:12360\n"
    "ds_read_b64_tr_b16 a[94:95], v220 offset:15432\n"
    "ds_read_b64_tr_b16 v[196:197], v218 offset:12416\n"
    "s_waitcnt vmcnt(16)\n"
    "v_or3_b32 v200, v200, v108, v109\n"
    "ds_read_b64_tr_b16 v[198:199], v218 offset:15488\n"
    "v_or3_b32 v200, v200, v110, v111\n"
    "v_add_u32_e64 v108, v201, v108\n"
    "v_add_u32_e32 v110, v201, v110\n"
    "s_waitcnt vmcnt(15)\n"
    "v_or3_b32 v200, v200, v112, v113\n"
    "v_or3_b32 v200, v200, v114, v115\n"
    "s_waitcnt lgkmcnt(11)\n"
    "v_add_f32_e32 v224, v224, v225\n"
    "v_add_u32_e64 v112, v203, v112\n"
    "v_add_u32_e32 v114, v203, v114\n"
    "s_waitcnt lgkmcnt(9)\n"
    "v_add_f32_e64 v226, v226, v227\n"
    "v_add_f32_e64 v224, v224, v226\n"
    "v_mul_f32_e32 v224, 0x3c2aaaab, v224\n"
    "s_waitcnt lgkmcnt(7)\n"
    "v_add_f32_e32 v228, v228, v229\n"
    "s_waitcnt vmcnt(14)\n"
    "v_or3_b32 v200, v200, v116, v117\n"
    "s_waitcnt lgkmcnt(5)\n"
    "v_add_f32_e64 v230, v230, v231\n"
    "v_add_f32_e64 v228, v228, v230\n"
    "v_mul_f32_e32 v228, 0x3c2aaaab, v228\n"
    "v_fma_f32 v228, -v224, v224, v228\n"
    "v_max_f32_e64 v228, 0, v228\n"
    "v_add_f32_e32 v228, 0x3727c5ac, v228\n"
    "v_rsq_f32_e64 v228, v228\n"
    "v_or3_b32 v200, v200, v118, v119\n"
    "v_mul_f32_e64 v228, s50, v228\n"
    "v_mul_f32_e64 v224, v228, -v224\n"
    "ds_write_b32 v254, v228\n"
    "ds_write_b32 v254, v224 offset:1024\n"
    "v_add_u32_e64 v254, s56, v242\n"
    "s_waitcnt lgkmcnt(2)\n"
    "s_barrier\n"
    "ds_write_b128 v217, v[96:99] offset:24576\n"
    "v_add_u32_e64 v116, v205, v116\n"
    "v_add_u32_e32 v118, v205, v118\n"
    "s_waitcnt vmcnt(13)\n"
    "v_or3_b32 v200, v200, v120, v121\n"
    "v_or3_b32 v200, v200, v122, v123\n"
    "v_add_u32_e64 v120, v201, v120\n"
    "v_add_u32_e32 v122, v201, v122\n"
    "s_waitcnt vmcnt(12)\n"
    "v_or3_b32 v200, v200, v124, v125\n"
    "ds_write_b128 v217, v[100:103] offset:24832\n"
    "s_waitcnt lgkmcnt(2)\n"
    "s_barrier\n"
    "ds_read_b128 v[224:227], v254\n"
    "ds_read_b128 v[228:231], v254 offset:1024\n"
    "ds_write_b128 v217, v[104:107] offset:25088\n"
    "v_or3_b32 v200, v200, v126, v127\n"
    "v_add_u32_e64 v124, v203, v124\n"
    "v_add_u32_e32 v126, v203, v126\n"
    "s_waitcnt vmcnt(11)\n"
    "v_or3_b32 v200, v200, v128, v129\n"
    "v_or3_b32 v200, v200, v130, v131\n"
    "v_add_u32_e64 v128, v205, v128\n"
    "v_add_u32_e32 v130, v205, v130\n"
    "s_waitcnt vmcnt(10)\n"
    "v_or3_b32 v200, v200, v132, v133\n"
    "ds_write_b128 v217, v[108:111] offset:0\n"
    "v_or3_b32 v200, v200, v134, v135\n"
    "v_add_u32_e64 v132, v201, v132\n"
    "v_add_u32_e64 v134, v201, v134\n"
    "s_waitcnt vmcnt(9)\n"
    "v_or3_b32 v200, v200, v136, v137\n"
    "v_or3_b32 v200, v200, v138, v139\n"
    "ds_write_b128 v217, v[112:115] offset:256\n"
    "s_waitcnt lgkmcnt(3)\n"
    "v_pk_fma_f32 v[64:65], v[64:65], v[224:225], v[228:229]\n"
    "v_pk_fma_f32 v[66:67], v[66:67], v[226:227], v[230:231]\n"
    "v_pk_fma_f32 v[68:69], v[68:69], v[224:225], v[228:229]\n"
    "v_pk_fma_f32 v[70:71], v[70:71], v[226:227], v[230:231]\n"
    "v_pk_fma_f32 v[72:73], v[72:73], v[224:225], v[228:229]\n"
    "v_pk_fma_f32 v[74:75], v[74:75], v[226:227], v[230:231]\n"
    "v_pk_fma_f32 v[76:77], v[76:77], v[224:225], v[228:229]\n"
    "v_pk_fma_f32 v[78:79], v[78:79], v[226:227], v[230:231]\n"
    "v_pk_fma_f32 v[80:81], v[80:81], v[224:225], v[228:229]\n"
    "v_pk_fma_f32 v[82:83], v[82:83], v[226:227], v[230:231]\n"
    "v_pk_fma_f32 v[84:85], v[84:85], v[224:225], v[228:229]\n"
    "v_pk_fma_f32 v[86:87], v[86:87], v[226:227], v[230:231]\n"
    "v_pk_fma_f32 v[88:89], v[88:89], v[224:225], v[228:229]\n"
    "v_pk_fma_f32 v[90:91], v[90:91], v[226:227], v[230:231]\n"
    "v_pk_fma_f32 v[92:93], v[92:93], v[224:225], v[228:229]\n"
    "v_pk_fma_f32 v[94:95], v[94:95], v[226:227], v[230:231]\n"
    "v_cvt_pk_bf16_f32 v96, v64, v68\n"
    "v_cvt_pk_bf16_f32 v100, v65, v69\n"
    "v_cvt_pk_bf16_f32 v104, v66, v70\n"
    "v_cvt_pk_bf16_f32 v108, v67, v71\n"
    "v_cvt_pk_bf16_f32 v97, v72, v76\n"
    "v_cvt_pk_bf16_f32 v101, v73, v77\n"
    "v_cvt_pk_bf16_f32 v105, v74, v78\n"
    "v_cvt_pk_bf16_f32 v109, v75, v79\n"
    "v_cvt_pk_bf16_f32 v98, v80, v84\n"
    "v_cvt_pk_bf16_f32 v102, v81, v85\n"
    "v_cvt_pk_bf16_f32 v106, v82, v86\n"
    "v_cvt_pk_bf16_f32 v110, v83, v87\n"
    "v_cvt_pk_bf16_f32 v99, v88, v92\n"
    "ds_write_b128 v253, v[96:99] offset:2048\n"
    "v_cvt_pk_bf16_f32 v103, v89, v93\n"
    "v_cvt_pk_bf16_f32 v107, v90, v94\n"
    "v_cvt_pk_bf16_f32 v111, v91, v95\n"
    "v_pk_fma_f32 v[0:1], v[0:1], v[224:225], v[228:229]\n"
    "v_pk_fma_f32 v[2:3], v[2:3], v[226:227], v[230:231]\n"
    "v_pk_fma_f32 v[4:5], v[4:5], v[224:225], v[228:229]\n"
    "ds_write_b128 v253, v[100:103] offset:5120\n"
    "v_pk_fma_f32 v[6:7], v[6:7], v[226:227], v[230:231]\n"
    "v_pk_fma_f32 v[8:9], v[8:9], v[224:225], v[228:229]\n"
    "v_pk_fma_f32 v[10:11], v[10:11], v[226:227], v[230:231]\n"
    "v_pk_fma_f32 v[12:13], v[12:13], v[224:225], v[228:229]\n"
    "v_pk_fma_f32 v[14:15], v[14:15], v[226:227], v[230:231]\n"
    "v_pk_fma_f32 v[16:17], v[16:17], v[224:225], v[228:229]\n"
    "v_pk_fma_f32 v[18:19], v[18:19], v[226:227], v[230:231]\n"
    "v_pk_fma_f32 v[20:21], v[20:21], v[224:225], v[228:229]\n"
    "v_pk_fma_f32 v[22:23], v[22:23], v[226:227], v[230:231]\n"
    "v_pk_fma_f32 v[24:25], v[24:25], v[224:225], v[228:229]\n"
    "v_pk_fma_f32 v[26:27], v[26:27], v[226:227], v[230:231]\n"
    "v_pk_fma_f32 v[28:29], v[28:29], v[224:225], v[228:229]\n"
    "v_pk_fma_f32 v[30:31], v[30:31], v[226:227], v[230:231]\n"
    "v_cvt_pk_bf16_f32 v80, v0, v4\n"
    "v_cvt_pk_bf16_f32 v84, v1, v5\n"
    "v_cvt_pk_bf16_f32 v88, v2, v6\n"
    "v_cvt_pk_bf16_f32 v92, v3, v7\n"
    "v_cvt_pk_bf16_f32 v81, v8, v12\n"
    "v_cvt_pk_bf16_f32 v85, v9, v13\n"
    "v_cvt_pk_bf16_f32 v89, v10, v14\n"
    "v_cvt_pk_bf16_f32 v93, v11, v15\n"
    "v_cvt_pk_bf16_f32 v82, v16, v20\n"
    "s_waitcnt vmcnt(1)\n"
    "v_lshlrev_b32_e32 v235, 3, v244\n"
    "s_mov_b32 exec_hi, 0\n"
    "buffer_load_dword v233, v235, s[40:43], s53 offen sc1\n"
    "s_mov_b32 exec_hi, -1\n"
    "s_add_u32 s53, s53, 393216\n"
    "v_lshlrev_b32_e64 v235, 3, v244\n"
    "s_mov_b32 exec_hi, 0\n"
    "buffer_load_dword v233, v235, s[40:43], s53 offen sc1\n"
    "s_mov_b32 exec_hi, -1\n"
    "s_add_u32 s53, s53, 393216\n"
    "v_lshlrev_b32_e64 v235, 3, v244\n"
    "s_mov_b32 exec_hi, 0\n"
    "buffer_load_dword v233, v235, s[40:43], s53 offen sc1\n"
    "s_mov_b32 exec_hi, -1\n"
    "s_add_u32 s53, s53, 393216\n"
    "v_lshlrev_b32_e32 v235, 3, v244\n"
    "s_mov_b32 exec_hi, 0\n"
    "buffer_load_dword v233, v235, s[40:43], s53 offen sc1\n"
    "s_mov_b32 exec_hi, -1\n"
    "s_add_u32 s53, s53, 393216\n"
    "s_waitcnt vmcnt(4)\n"
    "v_accvgpr_write_b32 a248, v236\n"
    "v_accvgpr_write_b32 a249, v237\n"
    "v_accvgpr_write_b32 a250, v238\n"
    "v_accvgpr_write_b32 a251, v239\n"
    "v_cvt_pk_bf16_f32 v86, v17, v21\n"
    "v_cvt_pk_bf16_f32 v90, v18, v22\n"
    "v_cvt_pk_bf16_f32 v94, v19, v23\n"
    "v_cvt_pk_bf16_f32 v83, v24, v28\n"
    "ds_write_b128 v253, v[80:83] offset:0\n"
    "v_cvt_pk_bf16_f32 v87, v25, v29\n"
    "v_cvt_pk_bf16_f32 v91, v26, v30\n"
    "v_cvt_pk_bf16_f32 v95, v27, v31\n"
    "v_pk_fma_f32 v[32:33], v[32:33], v[224:225], v[228:229]\n"
    "v_pk_fma_f32 v[34:35], v[34:35], v[226:227], v[230:231]\n"
    "v_pk_fma_f32 v[36:37], v[36:37], v[224:225], v[228:229]\n"
    "ds_write_b128 v253, v[84:87] offset:3072\n"
    "v_pk_fma_f32 v[38:39], v[38:39], v[226:227], v[230:231]\n"
    "v_pk_fma_f32 v[40:41], v[40:41], v[224:225], v[228:229]\n"
    "v_pk_fma_f32 v[42:43], v[42:43], v[226:227], v[230:231]\n"
    "v_pk_fma_f32 v[44:45], v[44:45], v[224:225], v[228:229]\n"
    "v_pk_fma_f32 v[46:47], v[46:47], v[226:227], v[230:231]\n"
    "v_pk_fma_f32 v[48:49], v[48:49], v[224:225], v[228:229]\n"
    "v_pk_fma_f32 v[50:51], v[50:51], v[226:227], v[230:231]\n"
    "v_pk_fma_f32 v[52:53], v[52:53], v[224:225], v[228:229]\n"
    "v_pk_fma_f32 v[54:55], v[54:55], v[226:227], v[230:231]\n"
    "v_pk_fma_f32 v[56:57], v[56:57], v[224:225], v[228:229]\n"
    "v_pk_fma_f32 v[58:59], v[58:59], v[226:227], v[230:231]\n"
    "v_pk_fma_f32 v[60:61], v[60:61], v[224:225], v[228:229]\n"
    "v_pk_fma_f32 v[62:63], v[62:63], v[226:227], v[230:231]\n"
    "v_cvt_pk_bf16_f32 v64, v32, v36\n"
    "v_cvt_pk_bf16_f32 v68, v33, v37\n"
    "v_cvt_pk_bf16_f32 v72, v34, v38\n"
    "v_cvt_pk_bf16_f32 v76, v35, v39\n"
    "v_cvt_pk_bf16_f32 v65, v40, v44\n"
    "v_cvt_pk_bf16_f32 v69, v41, v45\n"
    "v_cvt_pk_bf16_f32 v73, v42, v46\n"
    "v_cvt_pk_bf16_f32 v77, v43, v47\n"
    "v_cvt_pk_bf16_f32 v66, v48, v52\n"
    "v_cvt_pk_bf16_f32 v70, v49, v53\n"
    "v_cvt_pk_bf16_f32 v74, v50, v54\n"
    "v_cvt_pk_bf16_f32 v78, v51, v55\n"
    "v_cvt_pk_bf16_f32 v67, v56, v60\n"
    "ds_write_b128 v253, v[64:67] offset:1024\n"
    "v_cvt_pk_bf16_f32 v71, v57, v61\n"
    "v_cvt_pk_bf16_f32 v75, v58, v62\n"
    "v_cvt_pk_bf16_f32 v79, v59, v63\n"
    "v_mov_b32_e64 v64, v104\n"
    "v_mov_b32_e64 v65, v105\n"
    "v_mov_b32_e64 v66, v106\n"
    "ds_write_b128 v253, v[68:71] offset:4096\n"
    "v_mov_b32_e64 v67, v107\n"
    "v_mov_b32_e64 v68, v108\n"
    "v_mov_b32_e64 v69, v109\n"
    "v_mov_b32_e64 v70, v110\n"
    "v_mov_b32_e64 v71, v111\n"
    "s_waitcnt lgkmcnt(2)\n"
    "s_barrier\n"
    "ds_read_b64_tr_b16 a[96:97], v208 offset:24576\n"
    "ds_read_b64_tr_b16 v[180:181], v216 offset:24576\n"
    "ds_read_b64_tr_b16 a[100:101], v210 offset:24576\n"
    "v_add_u32_e64 v136, v203, v136\n"
    "ds_read_b64_tr_b16 v[184:185], v221 offset:24576\n"
    "ds_read_b64_tr_b16 a[104:105], v212 offset:24576\n"
    "ds_read_b64_tr_b16 a[108:109], v213 offset:24576\n"
    "v_add_u32_e64 v138, v203, v138\n"
    "ds_read_b64_tr_b16 a[112:113], v214 offset:24576\n"
    "ds_read_b64_tr_b16 a[116:117], v215 offset:24576\n"
    "ds_read_b64_tr_b16 a[120:121], v208 offset:24832\n"
    "s_mov_b32 s64, 0\n"
    "s_mov_b32 s59, 0\n"
    "ds_read_b64_tr_b16 a[124:125], v210 offset:24832\n"
    "ds_write_b128 v217, v[116:119] offset:512\n"
    "v_or3_b32 v200, v200, v140, v141\n"
    "v_or3_b32 v200, v200, v142, v143\n"
    "v_add_u32_e64 v140, v205, v140\n"
    "v_add_u32_e64 v142, v205, v142\n"
    "v_or3_b32 v200, v200, v144, v145\n"
    "v_or3_b32 v200, v200, v146, v147\n"
    "v_add_u32_e64 v144, v201, v144\n"
    "v_add_u32_e64 v146, v201, v146\n"
    "v_or3_b32 v200, v200, v148, v149\n"
    "v_or3_b32 v200, v200, v150, v151\n"
    "ds_write_b128 v217, v[120:123] offset:12288\n"
    "v_add_u32_e64 v148, v203, v148\n"
    "v_add_u32_e64 v150, v203, v150\n"
    "v_or3_b32 v200, v200, v152, v153\n"
    "v_or3_b32 v200, v200, v154, v155\n"
    "v_add_u32_e64 v152, v205, v152\n"
    "v_add_u32_e64 v154, v205, v154\n"
    "ds_write_b128 v217, v[124:127] offset:12544\n"
    "s_waitcnt lgkmcnt(2)\n"
    "s_barrier\n"
    "ds_read_b64_tr_b16 a[98:99], v208 offset:0\n"
    "ds_read_b64_tr_b16 v[182:183], v216 offset:0\n"
    "ds_read_b64_tr_b16 a[102:103], v210 offset:0\n"
    "v_or3_b32 v200, v200, v156, v157\n"
    "ds_read_b64_tr_b16 v[186:187], v221 offset:0\n"
    "ds_read_b64_tr_b16 a[106:107], v212 offset:0\n"
    "ds_read_b64_tr_b16 a[110:111], v213 offset:0\n"
    "v_or3_b32 v200, v200, v158, v159\n"
    "ds_read_b64_tr_b16 a[114:115], v214 offset:0\n"
    "ds_read_b64_tr_b16 a[118:119], v215 offset:0\n"
    "ds_read_b64_tr_b16 a[122:123], v208 offset:256\n"
    "v_add_u32_e64 v156, v201, v156\n"
    "ds_read_b64_tr_b16 a[126:127], v210 offset:256\n"
    "ds_write_b128 v217, v[128:131] offset:12800\n"
    "v_add_u32_e64 v158, v201, v158\n"
    "v_or3_b32 v200, v200, v160, v161\n"
    "v_or3_b32 v200, v200, v162, v163\n"
    "v_add_u32_e64 v160, v203, v160\n"
    "v_add_u32_e64 v162, v203, v162\n"
    "v_or3_b32 v200, v200, v164, v165\n"
    "v_or3_b32 v200, v200, v166, v167\n"
    "v_add_u32_e64 v164, v205, v164\n"
    "v_add_u32_e64 v166, v205, v166\n"
    "ds_write_b128 v217, v[132:135] offset:24576\n"
    "v_and_b32_e32 v200, 0x40004000, v200\n"
    "v_and_b32_e64 v246, s67, v246\n"
    "v_and_b32_e64 v247, s67, v247\n"
    "v_add_u32_e64 v246, s98, v246\n"
    "v_add_u32_e64 v247, s98, v247\n"
    "v_add_u32_e64 v245, s99, v245\n"
    "ds_write_b128 v217, v[136:139] offset:24832\n"
    "s_waitcnt lgkmcnt(2)\n"
    "s_barrier\n"
    "ds_read_b64_tr_b16 a[128:129], v208 offset:12288\n"
    "ds_read_b64_tr_b16 a[132:133], v209 offset:12288\n"
    "ds_read_b64_tr_b16 a[136:137], v210 offset:12288\n"
    "v_mov_b32_e64 v235, v222\n"
    "ds_read_b64_tr_b16 a[140:141], v211 offset:12288\n"
    "ds_read_b64_tr_b16 a[144:145], v212 offset:12288\n"
    "ds_read_b64_tr_b16 a[148:149], v213 offset:12288\n"
    "ds_read_b64_tr_b16 a[152:153], v214 offset:12288\n"
    "ds_read_b64_tr_b16 a[156:157], v215 offset:12288\n"
    "ds_read_b64_tr_b16 a[160:161], v208 offset:12544\n"
    "ds_read_b64_tr_b16 a[164:165], v209 offset:12544\n"
    "ds_read_b64_tr_b16 a[168:169], v210 offset:12544\n"
    "ds_read_b64_tr_b16 a[172:173], v211 offset:12544\n"
    "ds_write_b128 v217, v[140:143] offset:25088\n"
    "ds_write_b128 v217, v[144:147] offset:0\n"
    "ds_write_b128 v217, v[148:151] offset:256\n"
    "s_waitcnt lgkmcnt(2)\n"
    "s_barrier\n"
    "ds_read_b64_tr_b16 a[130:131], v208 offset:24576\n"
    "ds_read_b64_tr_b16 a[134:135], v209 offset:24576\n"
    "ds_read_b64_tr_b16 a[138:139], v210 offset:24576\n"
    "ds_read_b64_tr_b16 a[142:143], v211 offset:24576\n"
    "ds_read_b64_tr_b16 a[146:147], v212 offset:24576\n"
    "ds_read_b64_tr_b16 a[150:151], v213 offset:24576\n"
    "ds_read_b64_tr_b16 a[154:155], v214 offset:24576\n"
    "ds_read_b64_tr_b16 a[158:159], v215 offset:24576\n"
    "ds_read_b64_tr_b16 a[162:163], v208 offset:24832\n"
    "ds_read_b64_tr_b16 a[166:167], v209 offset:24832\n"
    "ds_read_b64_tr_b16 a[170:171], v210 offset:24832\n"
    "ds_read_b64_tr_b16 a[174:175], v211 offset:24832\n"
    "ds_write_b128 v217, v[152:155] offset:512\n"
    "ds_write_b128 v217, v[156:159] offset:12288\n"
    "ds_write_b128 v207, v[184:187] offset:36864\n"
    "s_waitcnt lgkmcnt(2)\n"
    "s_barrier\n"
    "ds_write_b128 v234, a[216:219] offset:0\n"
    "ds_write_b128 v234, a[220:223] offset:16\n"
    "ds_read_b64_tr_b16 a[176:177], v208 offset:0\n"
    "ds_read_b64_tr_b16 a[180:181], v209 offset:0\n"
    "ds_read_b64_tr_b16 a[184:185], v210 offset:0\n"
    "ds_read_b64_tr_b16 a[188:189], v211 offset:0\n"
    "ds_read_b64_tr_b16 a[192:193], v212 offset:0\n"
    "ds_read_b64_tr_b16 a[196:197], v213 offset:0\n"
    "ds_read_b64_tr_b16 a[200:201], v214 offset:0\n"
    "ds_read_b64_tr_b16 a[204:205], v215 offset:0\n"
    "ds_read_b64_tr_b16 a[208:209], v208 offset:256\n"
    "ds_read_b64_tr_b16 a[212:213], v209 offset:256\n"
    "ds_read_b64_tr_b16 a[216:217], v210 offset:256\n"
    "ds_read_b64_tr_b16 a[220:221], v211 offset:256\n"
    "ds_write_b128 v207, v[180:183] offset:32768\n"
    "ds_write_b128 v217, v[160:163] offset:12544\n"
    "ds_write_b128 v217, v[164:167] offset:12800\n"
    "v_cmp_le_u32_e32 vcc, 1, v200\n"
    "s_nop 1\n"
    "s_cmp_lg_u64 vcc, 0\n"
    "s_cselect_b32 vcc_lo, 1, 0\n"
    "s_or_b32 s66, s66, vcc_lo\n"
    "v_cmp_le_u32_e32 vcc, 981467136, v232\n"
    "s_nop 1\n"
    "s_cmp_lg_u64 vcc, 0\n"
    "s_cselect_b32 vcc_lo, 2, 0\n"
    "s_or_b32 s66, s66, vcc_lo\n"
    "v_mov_b32_e32 v200, s66\n"
    "v_mov_b32_e32 v232, 162992\n"
    "s_mov_b64 exec, 1\n"
    "ds_or_b32 v232, v200\n"
    "s_mov_b64 exec, -1\n"
    "s_waitcnt lgkmcnt(0)\n"
    "s_barrier\n"
    "ds_read_b64_tr_b16 a[178:179], v208 offset:12288\n"
    "ds_read_b64_tr_b16 a[182:183], v209 offset:12288\n"
    "ds_read_b64_tr_b16 a[186:187], v210 offset:12288\n"
    "ds_read_b64_tr_b16 a[190:191], v211 offset:12288\n"
    "ds_read_b64_tr_b16 a[194:195], v212 offset:12288\n"
    "ds_read_b64_tr_b16 a[198:199], v213 offset:12288\n"
    "ds_read_b64_tr_b16 a[202:203], v214 offset:12288\n"
    "ds_read_b64_tr_b16 a[206:207], v215 offset:12288\n"
    "ds_read_b64_tr_b16 a[210:211], v208 offset:12544\n"
    "ds_read_b64_tr_b16 a[214:215], v209 offset:12544\n"
    "ds_read_b64_tr_b16 a[218:219], v210 offset:12544\n"
    "ds_read_b64_tr_b16 a[222:223], v211 offset:12544\n"
    "ds_write_b128 v207, v[168:171] offset:0\n"
    "ds_write_b128 v207, v[172:175] offset:4096\n"
    "ds_write_b128 v207, v[176:179] offset:8192\n"
    "ds_read_b32 v232, v232\n"
    "s_waitcnt lgkmcnt(1)\n"
    "s_barrier\n"
    "ds_read_b128 v[144:147], v242 offset:0\n"
    "ds_read_b128 v[148:151], v242 offset:1024\n"
    "ds_read_b128 v[152:155], v242 offset:2048\n"
    "ds_read_b128 v[156:159], v242 offset:3072\n"
    "ds_read_b128 v[160:163], v242 offset:4096\n"
    "ds_read_b128 v[164:167], v242 offset:5120\n"
    "ds_read_b128 a[240:243], v244 offset:32768\n"
    "ds_write_b128 v207, v[188:191] offset:12288\n"
    "ds_write_b128 v207, v[192:195] offset:16384\n"
    "s_waitcnt lgkmcnt(8)\n"
    "v_mfma_f32_16x16x32_bf16 v[168:171], a[128:131], v[144:147], 0\n"
    "s_waitcnt lgkmcnt(7)\n"
    "v_mfma_f32_16x16x32_bf16 v[168:171], a[176:179], v[148:151], v[168:171]\n"
    "s_waitcnt lgkmcnt(6)\n"
    "v_mfma_f32_16x16x32_bf16 v[168:171], a[96:99], v[152:155], v[168:171]\n"
    "ds_write_b128 v207, v[196:199] offset:20480\n"
    "v_mfma_f32_16x16x32_bf16 v[172:175], a[132:135], v[144:147], 0\n"
    "s_waitcnt lgkmcnt(5)\n"
    "v_mfma_f32_16x16x32_bf16 v[172:175], a[180:183], v[148:151], v[172:175]\n"
    "s_waitcnt lgkmcnt(3)\n"
    "v_mfma_f32_16x16x32_bf16 v[172:175], a[240:243], v[152:155], v[172:175]\n"
    "v_mfma_f32_16x16x32_bf16 v[176:179], a[128:131], v[156:159], 0\n"
    "v_mfma_f32_16x16x32_bf16 v[176:179], a[176:179], v[160:163], v[176:179]\n"
    "v_mfma_f32_16x16x32_bf16 v[176:179], a[96:99], v[164:167], v[176:179]\n"
    "v_mfma_f32_16x16x32_bf16 v[180:183], a[132:135], v[156:159], 0\n"
    "v_mfma_f32_16x16x32_bf16 v[180:183], a[180:183], v[160:163], v[180:183]\n"
    "s_waitcnt lgkmcnt(0)\n"
    "s_barrier\n"
    "v_mfma_f32_16x16x32_bf16 v[180:183], a[240:243], v[164:167], v[180:183]\n"
    "v_readfirstlane_b32 s66, v232\n"
    "s_nop 1\n"
    "s_cmp_lg_u32 s66, 2\n"
    "s_cbranch_scc1 98f\n"
    ".p2align 6\n"
    "1:\n"
    "v_lshlrev_b32_sdwa v184, 1, v168 dst_sel:DWORD dst_unused:UNUSED_PAD src0_sel:DWORD src1_sel:WORD_1\n"
    "v_lshlrev_b32_sdwa v200, 1, v169 dst_sel:DWORD dst_unused:UNUSED_PAD src0_sel:DWORD src1_sel:WORD_1\n"
    "v_lshlrev_b32_sdwa v185, 1, v170 dst_sel:DWORD dst_unused:UNUSED_PAD src0_sel:DWORD src1_sel:WORD_1\n"
    "v_lshlrev_b32_sdwa v201, 1, v171 dst_sel:DWORD dst_unused:UNUSED_PAD src0_sel:DWORD src1_sel:WORD_1\n"
    "v_mfma_f32_16x16x32_bf16 v[140:143], a[244:247], v[188:191], v[140:143]\n"
    "ds_read_b128 a[240:243], v244 offset:36864\n"
    "ds_read_u16 v184, v184\n"
    "v_mfma_f32_16x16x32_bf16 v[168:171], a[136:139], v[144:147], 0\n"
    "ds_read_u16_d16_hi v200, v200\n"
    "ds_read_u16 v185, v185\n"
    "v_mfma_f32_16x16x32_bf16 v[168:171], a[184:187], v[148:151], v[168:171]\n"
    "buffer_load_dwordx4 v[0:3], v246, s[40:43], s60 offen offset:0\n"
    "buffer_load_dwordx4 v[32:35], v246, s[40:43], s60 offen offset:64\n"
    "ds_read_u16_d16_hi v201, v201\n"
    "v_mfma_f32_16x16x32_bf16 v[168:171], a[100:103], v[152:155], v[168:171]\n"
    "v_lshlrev_b32_sdwa v186, 1, v172 dst_sel:DWORD dst_unused:UNUSED_PAD src0_sel:DWORD src1_sel:WORD_1\n"
    "v_lshlrev_b32_sdwa v202, 1, v173 dst_sel:DWORD dst_unused:UNUSED_PAD src0_sel:DWORD src1_sel:WORD_1\n"
    "s_waitcnt vmcnt(3)\n"
    "v_smfmac_f32_32x32x32_bf16 v[128:143], a[248:251], a[224:231], v241\n"
    "s_nop 0\n"
    "ds_read_u16 v186, v186\n"
    "v_lshlrev_b32_sdwa v187, 1, v174 dst_sel:DWORD dst_unused:UNUSED_PAD src0_sel:DWORD src1_sel:WORD_1\n"
    "v_lshlrev_b32_sdwa v203, 1, v175 dst_sel:DWORD dst_unused:UNUSED_PAD src0_sel:DWORD src1_sel:WORD_1\n"
    "v_lshlrev_b32_sdwa v188, 1, v176 dst_sel:DWORD dst_unused:UNUSED_PAD src0_sel:DWORD src1_sel:WORD_1\n"
    "v_lshlrev_b32_sdwa v204, 1, v177 dst_sel:DWORD dst_unused:UNUSED_PAD src0_sel:DWORD src1_sel:WORD_1\n"
    "v_mfma_f32_16x16x32_bf16 v[172:175], a[140:143], v[144:147], 0\n"
    "ds_read_b128 v[236:239], v244 offset:20480\n"
    "ds_read_u16_d16_hi v202, v202\n"
    "v_mfma_f32_16x16x32_bf16 v[172:175], a[188:191], v[148:151], v[172:175]\n"
    "v_lshlrev_b32_sdwa v189, 1, v178 dst_sel:DWORD dst_unused:UNUSED_PAD src0_sel:DWORD src1_sel:WORD_1\n"
    "v_lshlrev_b32_sdwa v205, 1, v179 dst_sel:DWORD dst_unused:UNUSED_PAD src0_sel:DWORD src1_sel:WORD_1\n"
    "s_waitcnt lgkmcnt(7)\n"
    "v_mfma_f32_16x16x32_bf16 v[172:175], a[240:243], v[152:155], v[172:175]\n"
    "s_nop 0\n"
    "ds_read_u16 v187, v187\n"
    "v_lshlrev_b32_sdwa v190, 1, v180 dst_sel:DWORD dst_unused:UNUSED_PAD src0_sel:DWORD src1_sel:WORD_1\n"
    "v_mfma_f32_16x16x32_bf16 v[176:179], a[136:139], v[156:159], 0\n"
    "v_lshlrev_b32_sdwa v206, 1, v181 dst_sel:DWORD dst_unused:UNUSED_PAD src0_sel:DWORD src1_sel:WORD_1\n"
    "v_lshlrev_b32_sdwa v191, 1, v182 dst_sel:DWORD dst_unused:UNUSED_PAD src0_sel:DWORD src1_sel:WORD_1\n"
    "v_mfma_f32_16x16x32_bf16 v[176:179], a[184:187], v[160:163], v[176:179]\n"
    "ds_read_b128 a[244:247], v244 offset:22528\n"
    "v_lshlrev_b32_sdwa v207, 1, v183 dst_sel:DWORD dst_unused:UNUSED_PAD src0_sel:DWORD src1_sel:WORD_1\n"
    "v_mfma_f32_16x16x32_bf16 v[176:179], a[100:103], v[164:167], v[176:179]\n"
    "ds_read_u16_d16_hi v203, v203\n"
    "v_pk_fma_f16 v234, v234, s49, s49\n"
    "v_mfma_f32_16x16x32_bf16 v[180:183], a[140:143], v[156:159], 0\n"
    "s_waitcnt lgkmcnt(8)\n"
    "v_rcp_f16_e32 v232, v234\n"
    "v_mfma_f32_16x16x32_bf16 v[180:183], a[188:191], v[160:163], v[180:183]\n"
    "ds_read_u16 v188, v188\n"
    "v_rcp_f16_sdwa v232, v234 dst_sel:WORD_1 dst_unused:UNUSED_PRESERVE src0_sel:WORD_1\n"
    "v_mfma_f32_16x16x32_bf16 v[180:183], a[240:243], v[164:167], v[180:183]\n"
    "ds_read_u16_d16_hi v204, v204\n"
    "v_fma_mix_f32 v234, v196, v232, 0 op_sel_hi:[1,1,0]\n"
    "v_mfma_f32_16x16x32_bf16 v[96:99], a[80:83], v[192:195], v[96:99]\n"
    "v_fma_mix_f32 v232, v196, v232, 0 op_sel:[1,1,0] op_sel_hi:[1,1,0]\n"
    "v_or_b32_e32 v184, v184, v200\n"
    "v_mfma_f32_16x16x32_bf16 v[100:103], a[84:87], v[192:195], v[100:103]\n"
    "s_waitcnt lgkmcnt(8)\n"
    "ds_read_u16 v189, v189\n"
    "v_cvt_pk_bf16_f32 v196, v234, v232\n"
    "v_mfma_f32_16x16x32_bf16 v[112:115], a[88:91], v[192:195], v[112:115]\n"
    "s_waitcnt lgkmcnt(7)\n"
    "v_or_b32_e32 v185, v185, v201\n"
    "v_lshlrev_b32_sdwa v201, 1, v171 dst_sel:DWORD dst_unused:UNUSED_PAD src0_sel:DWORD src1_sel:WORD_1\n"
    "v_mfma_f32_16x16x32_bf16 v[116:119], a[92:95], v[192:195], v[116:119]\n"
    "ds_read_u16_d16_hi v205, v205\n"
    "v_cvt_pk_bf16_f32 v96, v96, v97\n"
    "v_mfma_f32_16x16x32_bf16 v[128:131], v[236:239], v[192:195], v[128:131]\n"
    "ds_read_u16 v190, v190\n"
    "buffer_load_dwordx4 v[20:23], v247, s[40:43], s60 offen offset:0\n"
    "buffer_load_dwordx4 v[52:55], v247, s[40:43], s60 offen offset:64\n"
    "v_mfma_f32_16x16x32_bf16 v[104:107], a[80:83], v[196:199], v[104:107]\n"
    "s_waitcnt lgkmcnt(8)\n"
    "v_or_b32_e64 v186, v186, v202\n"
    "v_lshlrev_b32_sdwa v202, 1, v173 dst_sel:DWORD dst_unused:UNUSED_PAD src0_sel:DWORD src1_sel:WORD_1\n"
    "v_mfma_f32_16x16x32_bf16 v[108:111], a[84:87], v[196:199], v[108:111]\n"
    "s_waitcnt lgkmcnt(6)\n"
    "ds_read_u16_d16_hi v206, v206\n"
    "v_cvt_pk_bf16_f32 v97, v98, v99\n"
    "v_mfma_f32_16x16x32_bf16 v[132:135], a[244:247], v[192:195], v[132:135]\n"
    "v_cvt_pk_f16_f32 v192, v168, v169\n"
    "v_lshlrev_b32_sdwa v193, 1, v170 dst_sel:DWORD dst_unused:UNUSED_PAD src0_sel:DWORD src1_sel:WORD_1\n"
    "v_mfma_f32_16x16x32_bf16 v[120:123], a[88:91], v[196:199], v[120:123]\n"
    "ds_read_u16 v191, v191\n"
    "v_lshlrev_b32_sdwa v194, 1, v172 dst_sel:DWORD dst_unused:UNUSED_PAD src0_sel:DWORD src1_sel:WORD_1\n"
    "v_mfma_f32_16x16x32_bf16 v[124:127], a[92:95], v[196:199], v[124:127]\n"
    "s_waitcnt lgkmcnt(7)\n"
    "ds_read_u16_d16_hi v207, v207\n"
    "v_lshlrev_b32_sdwa v195, 1, v174 dst_sel:DWORD dst_unused:UNUSED_PAD src0_sel:DWORD src1_sel:WORD_1\n"
    "v_mfma_f32_16x16x32_bf16 v[136:139], v[236:239], v[196:199], v[136:139]\n"
    "v_or_b32_e32 v187, v187, v203\n"
    "v_lshlrev_b32_sdwa v203, 1, v175 dst_sel:DWORD dst_unused:UNUSED_PAD src0_sel:DWORD src1_sel:WORD_1\n"
    "v_mfma_f32_16x16x32_bf16 v[140:143], a[244:247], v[196:199], v[140:143]\n"
    "ds_read_u16 v193, v193\n"
    "v_cvt_pk_f16_f32 v196, v176, v177\n"
    "v_mfma_f32_16x16x32_bf16 v[168:171], a[144:147], v[144:147], 0\n"
    "v_lshlrev_b32_sdwa v197, 1, v178 dst_sel:DWORD dst_unused:UNUSED_PAD src0_sel:DWORD src1_sel:WORD_1\n"
    "v_lshlrev_b32_sdwa v198, 1, v180 dst_sel:DWORD dst_unused:UNUSED_PAD src0_sel:DWORD src1_sel:WORD_1\n"
    "v_mfma_f32_16x16x32_bf16 v[168:171], a[192:195], v[148:151], v[168:171]\n"
    "s_waitcnt lgkmcnt(7)\n"
    "ds_read_u16_d16_hi v201, v201\n"
    "v_lshlrev_b32_sdwa v199, 1, v182 dst_sel:DWORD dst_unused:UNUSED_PAD src0_sel:DWORD src1_sel:WORD_1\n"
    "v_mfma_f32_16x16x32_bf16 v[168:171], a[104:107], v[152:155], v[168:171]\n"
    "v_or_b32_e32 v188, v188, v204\n"
    "v_cvt_pk_bf16_f32 v98, v100, v101\n"
    "v_mfma_f32_16x16x32_bf16 v[172:175], a[148:151], v[144:147], 0\n"
    "ds_read_u16 v194, v194\n"
    "v_cvt_pk_bf16_f32 v99, v102, v103\n"
    "v_mfma_f32_16x16x32_bf16 v[172:175], a[196:199], v[148:151], v[172:175]\n"
    "s_waitcnt lgkmcnt(7)\n"
    "ds_read_u16_d16_hi v202, v202\n"
    "v_or_b32_e32 v189, v189, v205\n"
    "v_mfma_f32_16x16x32_bf16 v[172:175], a[108:111], v[152:155], v[172:175]\n"
    "v_lshlrev_b32_sdwa v205, 1, v179 dst_sel:DWORD dst_unused:UNUSED_PAD src0_sel:DWORD src1_sel:WORD_1\n"
    "v_cvt_pk_bf16_f32 v112, v112, v113\n"
    "v_mfma_f32_16x16x32_bf16 v[176:179], a[144:147], v[156:159], 0\n"
    "ds_read_b128 v[236:239], v244 offset:0\n"
    "v_cvt_pk_bf16_f32 v113, v114, v115\n"
    "v_mfma_f32_16x16x32_bf16 v[176:179], a[192:195], v[160:163], v[176:179]\n"
    "ds_read_u16 v195, v195\n"
    "v_cvt_pk_bf16_f32 v114, v116, v117\n"
    "v_mfma_f32_16x16x32_bf16 v[176:179], a[104:107], v[164:167], v[176:179]\n"
    "s_waitcnt lgkmcnt(8)\n"
    "v_or_b32_e32 v190, v190, v206\n"
    "v_lshlrev_b32_sdwa v206, 1, v181 dst_sel:DWORD dst_unused:UNUSED_PAD src0_sel:DWORD src1_sel:WORD_1\n"
    "v_mfma_f32_16x16x32_bf16 v[100:103], a[4:7], v[184:187], 0\n"
    "ds_read_u16_d16_hi v203, v203\n"
    "v_cvt_pk_bf16_f32 v115, v118, v119\n"
    "v_cvt_pk_bf16_f32 v128, v128, v129\n"
    "v_cvt_pk_bf16_f32 v129, v130, v131\n"
    "v_cvt_pk_bf16_f32 v130, v132, v133\n"
    "ds_write_b128 v243, v[96:99] offset:24640\n"
    "buffer_load_dwordx4 v[4:7], v246, s[40:43], s61 offen offset:0\n"
    "buffer_load_dwordx4 v[36:39], v246, s[40:43], s61 offen offset:64\n"
    "v_mfma_f32_16x16x32_bf16 v[96:99], a[0:3], v[184:187], 0\n"
    "s_waitcnt lgkmcnt(7)\n"
    "v_or_b32_e32 v191, v191, v207\n"
    "v_lshlrev_b32_sdwa v207, 1, v183 dst_sel:DWORD dst_unused:UNUSED_PAD src0_sel:DWORD src1_sel:WORD_1\n"
    "v_mfma_f32_16x16x32_bf16 v[180:183], a[148:151], v[156:159], 0\n"
    "ds_read_b128 a[252:255], v249 offset:0\n"
    "ds_write_b128 v243, v[112:115] offset:26688\n"
    "v_mfma_f32_16x16x32_bf16 v[180:183], a[196:199], v[160:163], v[180:183]\n"
    "s_waitcnt lgkmcnt(8)\n"
    "v_or_b32_e32 v193, v193, v201\n"
    "v_cvt_pk_bf16_f32 v131, v134, v135\n"
    "v_mfma_f32_16x16x32_bf16 v[180:183], a[108:111], v[164:167], v[180:183]\n"
    "v_cvt_pk_bf16_f32 v104, v104, v105\n"
    "v_cvt_pk_bf16_f32 v105, v106, v107\n"
    "v_mfma_f32_16x16x32_bf16 v[112:115], a[8:11], v[184:187], 0\n"
    "s_waitcnt lgkmcnt(5)\n"
    "ds_read_b128 a[244:247], v244 offset:2048\n"
    "ds_write_b128 v243, v[128:131] offset:28736\n"
    "v_mfma_f32_16x16x32_bf16 v[116:119], a[12:15], v[184:187], 0\n"
    "s_nop 0\n"
    "v_cvt_pk_bf16_f32 v106, v108, v109\n"
    "v_cvt_pk_bf16_f32 v107, v110, v111\n"
    "v_mfma_f32_16x16x32_bf16 v[128:131], v[236:239], v[184:187], 0\n"
    "v_or_b32_e64 v194, v194, v202\n"
    "v_cvt_pk_bf16_f32 v120, v120, v121\n"
    "v_mfma_f32_16x16x32_bf16 v[108:111], a[4:7], v[188:191], 0\n"
    "s_nop 0\n"
    "ds_read_u16 v197, v197\n"
    "ds_write_b128 v243, v[104:107] offset:25664\n"
    "v_mfma_f32_16x16x32_bf16 v[104:107], a[0:3], v[188:191], 0\n"
    "s_waitcnt lgkmcnt(6)\n"
    "v_cvt_pk_bf16_f32 v121, v122, v123\n"
    "v_cvt_pk_bf16_f32 v122, v124, v125\n"
    "v_cvt_pk_bf16_f32 v123, v126, v127\n"
    "v_pk_fma_f16 v232, v192, v192, s48\n"
    "v_mfma_f32_16x16x32_bf16 v[124:127], a[12:15], v[188:191], 0\n"
    "s_waitcnt lgkmcnt(4)\n"
    "buffer_store_dwordx4 a[252:255], v255, s[68:71], s59 offen offset:0 sc1\n"
    "ds_write_b128 v243, v[120:123] offset:27712\n"
    "s_waitcnt lgkmcnt(4)\n"
    "v_mfma_f32_16x16x32_bf16 v[132:135], a[244:247], v[184:187], 0\n"
    "v_cvt_pk_bf16_f32 v136, v136, v137\n"
    "v_cvt_pk_bf16_f32 v137, v138, v139\n"
    "v_mfma_f32_16x16x32_bf16 v[120:123], a[8:11], v[188:191], 0\n"
    "v_cvt_pk_bf16_f32 v138, v140, v141\n"
    "v_or_b32_e64 v195, v195, v203\n"
    "v_cvt_pk_bf16_f32 v139, v142, v143\n"
    "ds_write_b128 v243, v[136:139] offset:29760\n"
    "v_pk_mul_f16 v232, v192, v232\n"
    "v_mfma_f32_16x16x32_bf16 v[136:139], v[236:239], v[188:191], 0\n"
    "v_exp_f16_e64 v234, v232\n"
    "v_mfma_f32_16x16x32_bf16 v[140:143], a[244:247], v[188:191], 0\n"
    "ds_read_u16_d16_hi v205, v205\n"
    "ds_read_u16 v198, v198\n"
    "v_exp_f16_sdwa v234, v232 dst_sel:WORD_1 dst_unused:UNUSED_PRESERVE src0_sel:WORD_1\n"
    "v_cvt_pk_f16_f32 v184, v168, v169\n"
    "v_pk_fma_f16 v234, v234, s49, s49\n"
    "v_lshlrev_b32_sdwa v185, 1, v170 dst_sel:DWORD dst_unused:UNUSED_PAD src0_sel:DWORD src1_sel:WORD_1\n"
    "v_lshlrev_b32_sdwa v201, 1, v171 dst_sel:DWORD dst_unused:UNUSED_PAD src0_sel:DWORD src1_sel:WORD_1\n"
    "v_mfma_f32_16x16x32_bf16 v[168:171], a[152:155], v[144:147], 0\n"
    "ds_read_u16_d16_hi v206, v206\n"
    "ds_read_u16 v199, v199\n"
    "v_mfma_f32_16x16x32_bf16 v[168:171], a[200:203], v[148:151], v[168:171]\n"
    "ds_read_u16_d16_hi v207, v207\n"
    "buffer_load_dwordx4 v[24:27], v247, s[40:43], s61 offen offset:0\n"
    "buffer_load_dwordx4 v[56:59], v247, s[40:43], s61 offen offset:64\n"
    "v_lshlrev_b32_sdwa v186, 1, v172 dst_sel:DWORD dst_unused:UNUSED_PAD src0_sel:DWORD src1_sel:WORD_1\n"
    "v_lshlrev_b32_sdwa v202, 1, v173 dst_sel:DWORD dst_unused:UNUSED_PAD src0_sel:DWORD src1_sel:WORD_1\n"
    "v_lshlrev_b32_sdwa v187, 1, v174 dst_sel:DWORD dst_unused:UNUSED_PAD src0_sel:DWORD src1_sel:WORD_1\n"
    "v_lshlrev_b32_sdwa v203, 1, v175 dst_sel:DWORD dst_unused:UNUSED_PAD src0_sel:DWORD src1_sel:WORD_1\n"
    "v_rcp_f16_e64 v232, v234\n"
    "v_mfma_f32_16x16x32_bf16 v[168:171], a[112:115], v[152:155], v[168:171]\n"
    "ds_read_b128 a[252:255], v249 offset:2048\n"
    "ds_read_u16 v185, v185\n"
    "v_rcp_f16_sdwa v232, v234 dst_sel:WORD_1 dst_unused:UNUSED_PRESERVE src0_sel:WORD_1\n"
    "v_mfma_f32_16x16x32_bf16 v[172:175], a[156:159], v[144:147], 0\n"
    "ds_read_u16_d16_hi v201, v201\n"
    "v_cvt_pk_f16_f32 v188, v176, v177\n"
    "v_mfma_f32_16x16x32_bf16 v[172:175], a[204:207], v[148:151], v[172:175]\n"
    "v_lshlrev_b32_sdwa v189, 1, v178 dst_sel:DWORD dst_unused:UNUSED_PAD src0_sel:DWORD src1_sel:WORD_1\n"
    "v_lshlrev_b32_sdwa v190, 1, v180 dst_sel:DWORD dst_unused:UNUSED_PAD src0_sel:DWORD src1_sel:WORD_1\n"
    "v_lshlrev_b32_sdwa v191, 1, v182 dst_sel:DWORD dst_unused:UNUSED_PAD src0_sel:DWORD src1_sel:WORD_1\n"
    "v_fma_mix_f32 v234, v192, v232, 0 op_sel_hi:[1,1,0]\n"
    "v_mfma_f32_16x16x32_bf16 v[172:175], a[116:119], v[152:155], v[172:175]\n"
    "s_waitcnt lgkmcnt(7)\n"
    "ds_read_u16 v186, v186\n"
    "v_or_b32_e32 v197, v197, v205\n"
    "v_lshlrev_b32_sdwa v205, 1, v179 dst_sel:DWORD dst_unused:UNUSED_PAD src0_sel:DWORD src1_sel:WORD_1\n"
    "v_fma_mix_f32 v232, v192, v232, 0 op_sel:[1,1,0] op_sel_hi:[1,1,0]\n"
    "v_mfma_f32_16x16x32_bf16 v[176:179], a[152:155], v[156:159], 0\n"
    "ds_read_u16_d16_hi v202, v202\n"
    "v_cvt_pk_bf16_f32 v192, v234, v232\n"
    "v_mfma_f32_16x16x32_bf16 v[176:179], a[200:203], v[160:163], v[176:179]\n"
    "ds_read_b128 v[236:239], v244 offset:4096\n"
    "v_pk_fma_f16 v232, v196, v196, s48\n"
    "v_mfma_f32_16x16x32_bf16 v[176:179], a[112:115], v[164:167], v[176:179]\n"
    "s_waitcnt lgkmcnt(8)\n"
    "ds_read_u16 v187, v187\n"
    "v_or_b32_e32 v198, v198, v206\n"
    "v_lshlrev_b32_sdwa v206, 1, v181 dst_sel:DWORD dst_unused:UNUSED_PAD src0_sel:DWORD src1_sel:WORD_1\n"
    "v_pk_mul_f16 v232, v196, v232\n"
    "v_lshlrev_b32_sdwa v200, 1, v169 dst_sel:DWORD dst_unused:UNUSED_PAD src0_sel:DWORD src1_sel:WORD_1\n"
    "v_exp_f16_e64 v234, v232\n"
    "v_mov_b32_e32 v254, 0\n"
    "v_exp_f16_sdwa v234, v232 dst_sel:WORD_1 dst_unused:UNUSED_PRESERVE src0_sel:WORD_1\n"
    "v_mfma_f32_16x16x32_bf16 v[96:99], a[16:19], v[192:195], v[96:99]\n"
    "s_waitcnt lgkmcnt(6)\n"
    "ds_read_u16_d16_hi v203, v203\n"
    "buffer_store_dwordx4 a[252:255], v255, s[68:71], s59 offen offset:64 sc1\n"
    "v_or_b32_e64 v199, v199, v207\n"
    "v_lshlrev_b32_sdwa v207, 1, v183 dst_sel:DWORD dst_unused:UNUSED_PAD src0_sel:DWORD src1_sel:WORD_1\n"
    "v_mfma_f32_16x16x32_bf16 v[180:183], a[156:159], v[156:159], 0\n"
    "ds_read_b128 a[244:247], v244 offset:6144\n"
    "ds_read_b128 a[252:255], v249 offset:4096\n"
    "v_mfma_f32_16x16x32_bf16 v[180:183], a[204:207], v[160:163], v[180:183]\n"
    "ds_read_u16 v189, v189\n"
    "v_pk_fma_f16 v234, v234, s49, s49\n"
    "v_mfma_f32_16x16x32_bf16 v[180:183], a[116:119], v[164:167], v[180:183]\n"
    "ds_read_u16_d16_hi v205, v205\n"
    "v_rcp_f16_e32 v232, v234\n"
    "v_mfma_f32_16x16x32_bf16 v[100:103], a[20:23], v[192:195], v[100:103]\n"
    "s_waitcnt lgkmcnt(9)\n"
    "v_rcp_f16_sdwa v232, v234 dst_sel:WORD_1 dst_unused:UNUSED_PRESERVE src0_sel:WORD_1\n"
    "v_mfma_f32_16x16x32_bf16 v[112:115], a[24:27], v[192:195], v[112:115]\n"
    "ds_read_u16 v190, v190\n"
    "v_or_b32_e64 v185, v185, v201\n"
    "v_mfma_f32_16x16x32_bf16 v[116:119], a[28:31], v[192:195], v[116:119]\n"
    "ds_read_u16_d16_hi v206, v206\n"
    "v_fma_mix_f32 v234, v196, v232, 0 op_sel_hi:[1,1,0]\n"
    "s_waitcnt lgkmcnt(8)\n"
    "v_mfma_f32_16x16x32_bf16 v[128:131], v[236:239], v[192:195], v[128:131]\n"
    "s_nop 0\n"
    "v_fma_mix_f32 v232, v196, v232, 0 op_sel:[1,1,0] op_sel_hi:[1,1,0]\n"
    "v_or_b32_e64 v186, v186, v202\n"
    "v_cvt_pk_bf16_f32 v196, v234, v232\n"
    "v_pk_fma_f16 v232, v184, v184, s48\n"
    "v_lshlrev_b32_sdwa v201, 1, v171 dst_sel:DWORD dst_unused:UNUSED_PAD src0_sel:DWORD src1_sel:WORD_1\n"
    "v_mfma_f32_16x16x32_bf16 v[104:107], a[16:19], v[196:199], v[104:107]\n"
    "buffer_load_dwordx4 v[8:11], v246, s[40:43], s62 offen offset:0\n"
    "buffer_load_dwordx4 v[40:43], v246, s[40:43], s62 offen offset:64\n"
    "ds_read_u16 v191, v191\n"
    "v_mfma_f32_16x16x32_bf16 v[108:111], a[20:23], v[196:199], v[108:111]\n"
    "ds_read_u16_d16_hi v207, v207\n"
    "v_pk_mul_f16 v232, v184, v232\n"
    "s_waitcnt lgkmcnt(7)\n"
    "v_mfma_f32_16x16x32_bf16 v[132:135], a[244:247], v[192:195], v[132:135]\n"
    "s_nop 0\n"
    "ds_read_b128 a[240:243], v244 offset:34816\n"
    "v_or_b32_e32 v187, v187, v203\n"
    "v_mfma_f32_16x16x32_bf16 v[120:123], a[24:27], v[196:199], v[120:123]\n"
    "s_waitcnt lgkmcnt(7)\n"
    "ds_read_u16_d16_hi v200, v200\n"
    "v_exp_f16_e64 v234, v232\n"
    "v_mfma_f32_16x16x32_bf16 v[124:127], a[28:31], v[196:199], v[124:127]\n"
    "v_exp_f16_sdwa v234, v232 dst_sel:WORD_1 dst_unused:UNUSED_PRESERVE src0_sel:WORD_1\n"
    "v_mfma_f32_16x16x32_bf16 v[136:139], v[236:239], v[196:199], v[136:139]\n"
    "ds_read_u16_d16_hi v201, v201\n"
    "v_lshlrev_b32_sdwa v192, 1, v168 dst_sel:DWORD dst_unused:UNUSED_PAD src0_sel:DWORD src1_sel:WORD_1\n"
    "v_mfma_f32_16x16x32_bf16 v[140:143], a[244:247], v[196:199], v[140:143]\n"
    "ds_read_u16 v192, v192\n"
    "v_pk_fma_f16 v234, v234, s49, s49\n"
    "v_lshlrev_b32_sdwa v193, 1, v170 dst_sel:DWORD dst_unused:UNUSED_PAD src0_sel:DWORD src1_sel:WORD_1\n"
    "v_mfma_f32_16x16x32_bf16 v[168:171], a[160:163], v[144:147], 0\n"
    "s_waitcnt lgkmcnt(8)\n"
    "v_or_b32_e32 v189, v189, v205\n"
    "v_lshlrev_b32_sdwa v194, 1, v172 dst_sel:DWORD dst_unused:UNUSED_PAD src0_sel:DWORD src1_sel:WORD_1\n"
    "v_mfma_f32_16x16x32_bf16 v[168:171], a[208:211], v[148:151], v[168:171]\n"
    "ds_read_u16 v193, v193\n"
    "v_lshlrev_b32_sdwa v202, 1, v173 dst_sel:DWORD dst_unused:UNUSED_PAD src0_sel:DWORD src1_sel:WORD_1\n"
    "v_lshlrev_b32_sdwa v195, 1, v174 dst_sel:DWORD dst_unused:UNUSED_PAD src0_sel:DWORD src1_sel:WORD_1\n"
    "v_lshlrev_b32_sdwa v203, 1, v175 dst_sel:DWORD dst_unused:UNUSED_PAD src0_sel:DWORD src1_sel:WORD_1\n"
    "v_rcp_f16_e32 v232, v234\n"
    "v_mfma_f32_16x16x32_bf16 v[168:171], a[120:123], v[152:155], v[168:171]\n"
    "s_waitcnt lgkmcnt(7)\n"
    "ds_read_u16 v194, v194\n"
    "buffer_store_dwordx4 a[252:255], v255, s[68:71], s59 offen offset:128 sc1\n"
    "v_or_b32_e32 v190, v190, v206\n"
    "v_rcp_f16_sdwa v232, v234 dst_sel:WORD_1 dst_unused:UNUSED_PRESERVE src0_sel:WORD_1\n"
    "v_mfma_f32_16x16x32_bf16 v[172:175], a[164:167], v[144:147], 0\n"
    "ds_read_b128 a[252:255], v249 offset:1024\n"
    "ds_read_b128 v[236:239], v244 offset:8192\n"
    "v_mfma_f32_16x16x32_bf16 v[172:175], a[212:215], v[148:151], v[172:175]\n"
    "s_waitcnt lgkmcnt(7)\n"
    "ds_read_u16_d16_hi v202, v202\n"
    "v_lshlrev_b32_sdwa v196, 1, v176 dst_sel:DWORD dst_unused:UNUSED_PAD src0_sel:DWORD src1_sel:WORD_1\n"
    "v_lshlrev_b32_sdwa v204, 1, v177 dst_sel:DWORD dst_unused:UNUSED_PAD src0_sel:DWORD src1_sel:WORD_1\n"
    "v_lshlrev_b32_sdwa v197, 1, v178 dst_sel:DWORD dst_unused:UNUSED_PAD src0_sel:DWORD src1_sel:WORD_1\n"
    "v_lshlrev_b32_sdwa v205, 1, v179 dst_sel:DWORD dst_unused:UNUSED_PAD src0_sel:DWORD src1_sel:WORD_1\n"
    "v_lshlrev_b32_sdwa v198, 1, v180 dst_sel:DWORD dst_unused:UNUSED_PAD src0_sel:DWORD src1_sel:WORD_1\n"
    "v_lshlrev_b32_sdwa v206, 1, v181 dst_sel:DWORD dst_unused:UNUSED_PAD src0_sel:DWORD src1_sel:WORD_1\n"
    "v_lshlrev_b32_sdwa v199, 1, v182 dst_sel:DWORD dst_unused:UNUSED_PAD src0_sel:DWORD src1_sel:WORD_1\n"
    "v_fma_mix_f32 v234, v184, v232, 0 op_sel_hi:[1,1,0]\n"
    "v_mfma_f32_16x16x32_bf16 v[172:175], a[240:243], v[152:155], v[172:175]\n"
    "ds_read_u16 v195, v195\n"
    "ds_read_b128 a[244:247], v244 offset:10240\n"
    "v_or_b32_e64 v191, v191, v207\n"
    "v_lshlrev_b32_sdwa v207, 1, v183 dst_sel:DWORD dst_unused:UNUSED_PAD src0_sel:DWORD src1_sel:WORD_1\n"
    "v_fma_mix_f32 v232, v184, v232, 0 op_sel:[1,1,0] op_sel_hi:[1,1,0]\n"
    "v_mfma_f32_16x16x32_bf16 v[176:179], a[160:163], v[156:159], 0\n"
    "ds_read_u16_d16_hi v203, v203\n"
    "v_cvt_pk_bf16_f32 v184, v234, v232\n"
    "v_mfma_f32_16x16x32_bf16 v[176:179], a[208:211], v[160:163], v[176:179]\n"
    "s_nop 0\n"
    "ds_read_u16 v196, v196\n"
    "v_pk_fma_f16 v232, v188, v188, s48\n"
    "v_mfma_f32_16x16x32_bf16 v[176:179], a[120:123], v[164:167], v[176:179]\n"
    "s_waitcnt lgkmcnt(9)\n"
    "buffer_load_dwordx4 v[12:15], v246, s[40:43], s63 offen offset:0\n"
    "buffer_load_dwordx4 v[44:47], v246, s[40:43], s63 offen offset:64\n"
    "v_mfma_f32_16x16x32_bf16 v[180:183], a[164:167], v[156:159], 0\n"
    "ds_read_u16_d16_hi v204, v204\n"
    "v_pk_mul_f16 v232, v188, v232\n"
    "v_mfma_f32_16x16x32_bf16 v[180:183], a[212:215], v[160:163], v[180:183]\n"
    "s_waitcnt lgkmcnt(9)\n"
    "v_exp_f16_e64 v234, v232\n"
    "v_mfma_f32_16x16x32_bf16 v[180:183], a[240:243], v[164:167], v[180:183]\n"
    "s_waitcnt lgkmcnt(7)\n"
    "ds_read_u16 v197, v197\n"
    "v_exp_f16_sdwa v234, v232 dst_sel:WORD_1 dst_unused:UNUSED_PRESERVE src0_sel:WORD_1\n"
    "v_mfma_f32_16x16x32_bf16 v[96:99], a[32:35], v[184:187], v[96:99]\n"
    "ds_read_u16_d16_hi v205, v205\n"
    "v_pk_fma_f16 v234, v234, s49, s49\n"
    "v_mfma_f32_16x16x32_bf16 v[100:103], a[36:39], v[184:187], v[100:103]\n"
    "v_rcp_f16_e64 v232, v234\n"
    "v_mfma_f32_16x16x32_bf16 v[112:115], a[40:43], v[184:187], v[112:115]\n"
    "ds_read_u16 v198, v198\n"
    "v_rcp_f16_sdwa v232, v234 dst_sel:WORD_1 dst_unused:UNUSED_PRESERVE src0_sel:WORD_1\n"
    "v_mfma_f32_16x16x32_bf16 v[116:119], a[44:47], v[184:187], v[116:119]\n"
    "s_waitcnt lgkmcnt(8)\n"
    "v_fma_mix_f32 v234, v188, v232, 0 op_sel_hi:[1,1,0]\n"
    "v_fma_mix_f32 v232, v188, v232, 0 op_sel:[1,1,0] op_sel_hi:[1,1,0]\n"
    "v_mfma_f32_16x16x32_bf16 v[128:131], v[236:239], v[184:187], v[128:131]\n"
    "s_waitcnt lgkmcnt(6)\n"
    "ds_read_u16_d16_hi v206, v206\n"
    "v_cvt_pk_bf16_f32 v188, v234, v232\n"
    "v_mfma_f32_16x16x32_bf16 v[132:135], a[244:247], v[184:187], v[132:135]\n"
    "ds_read_u16 v199, v199\n"
    "buffer_store_dwordx4 a[252:255], v255, s[68:71], s39 offen offset:0 sc1\n"
    "v_mfma_f32_16x16x32_bf16 v[104:107], a[32:35], v[188:191], v[104:107]\n"
    "s_waitcnt lgkmcnt(7)\n"
    "ds_read_b128 a[252:255], v249 offset:3072\n"
    "v_or_b32_e32 v192, v192, v200\n"
    "v_mfma_f32_16x16x32_bf16 v[108:111], a[36:39], v[188:191], v[108:111]\n"
    "ds_read_u16_d16_hi v207, v207\n"
    "v_or_b32_e64 v193, v193, v201\n"
    "v_mfma_f32_16x16x32_bf16 v[120:123], a[40:43], v[188:191], v[120:123]\n"
    "v_cvt_pk_f16_f32 v184, v168, v169\n"
    "v_lshlrev_b32_sdwa v185, 1, v170 dst_sel:DWORD dst_unused:UNUSED_PAD src0_sel:DWORD src1_sel:WORD_1\n"
    "v_mfma_f32_16x16x32_bf16 v[124:127], a[44:47], v[188:191], v[124:127]\n"
    "ds_read_b128 a[240:243], v244 offset:38912\n"
    "v_lshlrev_b32_sdwa v201, 1, v171 dst_sel:DWORD dst_unused:UNUSED_PAD src0_sel:DWORD src1_sel:WORD_1\n"
    "v_mfma_f32_16x16x32_bf16 v[136:139], v[236:239], v[188:191], v[136:139]\n"
    "s_waitcnt lgkmcnt(8)\n"
    "ds_read_u16 v185, v185\n"
    "v_lshlrev_b32_sdwa v186, 1, v172 dst_sel:DWORD dst_unused:UNUSED_PAD src0_sel:DWORD src1_sel:WORD_1\n"
    "v_mfma_f32_16x16x32_bf16 v[140:143], a[244:247], v[188:191], v[140:143]\n"
    "v_or_b32_e32 v194, v194, v202\n"
    "v_lshlrev_b32_sdwa v202, 1, v173 dst_sel:DWORD dst_unused:UNUSED_PAD src0_sel:DWORD src1_sel:WORD_1\n"
    "v_mfma_f32_16x16x32_bf16 v[168:171], a[168:171], v[144:147], 0\n"
    "s_nop 0\n"
    "ds_read_u16_d16_hi v201, v201\n"
    "v_lshlrev_b32_sdwa v187, 1, v174 dst_sel:DWORD dst_unused:UNUSED_PAD src0_sel:DWORD src1_sel:WORD_1\n"
    "v_mfma_f32_16x16x32_bf16 v[168:171], a[216:219], v[148:151], v[168:171]\n"
    "s_waitcnt lgkmcnt(8)\n"
    "ds_read_u16 v186, v186\n"
    "v_cvt_pk_f16_f32 v188, v176, v177\n"
    "v_mfma_f32_16x16x32_bf16 v[168:171], a[124:127], v[152:155], v[168:171]\n"
    "buffer_load_dwordx4 v[16:19], v246, s[40:43], s52 offen offset:0\n"
    "buffer_load_dwordx4 v[48:51], v246, s[40:43], s52 offen offset:64\n"
    "v_lshlrev_b32_sdwa v189, 1, v178 dst_sel:DWORD dst_unused:UNUSED_PAD src0_sel:DWORD src1_sel:WORD_1\n"
    "v_lshlrev_b32_sdwa v190, 1, v180 dst_sel:DWORD dst_unused:UNUSED_PAD src0_sel:DWORD src1_sel:WORD_1\n"
    "v_lshlrev_b32_sdwa v191, 1, v182 dst_sel:DWORD dst_unused:UNUSED_PAD src0_sel:DWORD src1_sel:WORD_1\n"
    "v_or_b32_e32 v195, v195, v203\n"
    "v_lshlrev_b32_sdwa v203, 1, v175 dst_sel:DWORD dst_unused:UNUSED_PAD src0_sel:DWORD src1_sel:WORD_1\n"
    "v_mfma_f32_16x16x32_bf16 v[172:175], a[172:175], v[144:147], 0\n"
    "s_waitcnt lgkmcnt(7)\n"
    "ds_read_u16_d16_hi v202, v202\n"
    "ds_read_b128 v[236:239], v244 offset:12288\n"
    "v_mfma_f32_16x16x32_bf16 v[172:175], a[220:223], v[148:151], v[172:175]\n"
    "v_or_b32_e64 v196, v196, v204\n"
    "v_or_b32_e64 v197, v197, v205\n"
    "v_lshlrev_b32_sdwa v205, 1, v179 dst_sel:DWORD dst_unused:UNUSED_PAD src0_sel:DWORD src1_sel:WORD_1\n"
    "v_mfma_f32_16x16x32_bf16 v[176:179], a[168:171], v[156:159], 0\n"
    "s_waitcnt lgkmcnt(7)\n"
    "ds_read_u16 v187, v187\n"
    "v_or_b32_e32 v198, v198, v206\n"
    "v_mfma_f32_16x16x32_bf16 v[176:179], a[216:219], v[160:163], v[176:179]\n"
    "ds_read_u16_d16_hi v203, v203\n"
    "v_lshlrev_b32_sdwa v206, 1, v181 dst_sel:DWORD dst_unused:UNUSED_PAD src0_sel:DWORD src1_sel:WORD_1\n"
    "v_mfma_f32_16x16x32_bf16 v[176:179], a[124:127], v[164:167], v[176:179]\n"
    "s_waitcnt lgkmcnt(7)\n"
    "v_or_b32_e32 v199, v199, v207\n"
    "v_lshlrev_b32_sdwa v207, 1, v183 dst_sel:DWORD dst_unused:UNUSED_PAD src0_sel:DWORD src1_sel:WORD_1\n"
    "v_mfma_f32_16x16x32_bf16 v[172:175], a[240:243], v[152:155], v[172:175]\n"
    "ds_read_b128 a[244:247], v244 offset:14336\n"
    "v_pk_fma_f16 v232, v184, v184, s48\n"
    "v_mfma_f32_16x16x32_bf16 v[180:183], a[172:175], v[156:159], 0\n"
    "ds_read_u16 v189, v189\n"
    "v_pk_mul_f16 v232, v184, v232\n"
    "v_mfma_f32_16x16x32_bf16 v[180:183], a[220:223], v[160:163], v[180:183]\n"
    "v_exp_f16_e32 v234, v232\n"
    "v_mfma_f32_16x16x32_bf16 v[180:183], a[240:243], v[164:167], v[180:183]\n"
    "ds_read_u16_d16_hi v205, v205\n"
    "buffer_store_dwordx4 a[252:255], v255, s[68:71], s39 offen offset:64 sc1\n"
    "v_mfma_f32_16x16x32_bf16 v[96:99], a[48:51], v[192:195], v[96:99]\n"
    "s_waitcnt lgkmcnt(8)\n"
    "ds_read_b128 a[252:255], v249 offset:5120\n"
    "ds_read_u16 v190, v190\n"
    "v_mfma_f32_16x16x32_bf16 v[100:103], a[52:55], v[192:195], v[100:103]\n"
    "v_or_b32_e32 v185, v185, v201\n"
    "v_exp_f16_sdwa v234, v232 dst_sel:WORD_1 dst_unused:UNUSED_PRESERVE src0_sel:WORD_1\n"
    "v_mfma_f32_16x16x32_bf16 v[112:115], a[56:59], v[192:195], v[112:115]\n"
    "s_waitcnt lgkmcnt(7)\n"
    "ds_read_b128 v[144:147], v243 offset:0\n"
    "v_or_b32_e64 v186, v186, v202\n"
    "v_mfma_f32_16x16x32_bf16 v[116:119], a[60:63], v[192:195], v[116:119]\n"
    "ds_read_u16_d16_hi v206, v206\n"
    "v_pk_fma_f16 v234, v234, s49, s49\n"
    "v_mfma_f32_16x16x32_bf16 v[128:131], v[236:239], v[192:195], v[128:131]\n"
    "v_lshlrev_b32_sdwa v201, 1, v171 dst_sel:DWORD dst_unused:UNUSED_PAD src0_sel:DWORD src1_sel:WORD_1\n"
    "v_lshlrev_b32_sdwa v202, 1, v173 dst_sel:DWORD dst_unused:UNUSED_PAD src0_sel:DWORD src1_sel:WORD_1\n"
    "v_mfma_f32_16x16x32_bf16 v[104:107], a[48:51], v[196:199], v[104:107]\n"
    "ds_read_b128 v[148:151], v243 offset:1024\n"
    "ds_read_u16 v191, v191\n"
    "v_mfma_f32_16x16x32_bf16 v[108:111], a[52:55], v[196:199], v[108:111]\n"
    "v_rcp_f16_e32 v232, v234\n"
    "s_waitcnt lgkmcnt(8)\n"
    "v_mfma_f32_16x16x32_bf16 v[132:135], a[244:247], v[192:195], v[132:135]\n"
    "ds_read_b128 v[152:155], v243 offset:2048\n"
    "v_or_b32_e64 v187, v187, v203\n"
    "v_mfma_f32_16x16x32_bf16 v[120:123], a[56:59], v[196:199], v[120:123]\n"
    "ds_read_u16_d16_hi v207, v207\n"
    "v_cvt_pk_f16_f32 v192, v168, v169\n"
    "v_mfma_f32_16x16x32_bf16 v[124:127], a[60:63], v[196:199], v[124:127]\n"
    "v_lshlrev_b32_sdwa v193, 1, v170 dst_sel:DWORD dst_unused:UNUSED_PAD src0_sel:DWORD src1_sel:WORD_1\n"
    "v_lshlrev_b32_sdwa v194, 1, v172 dst_sel:DWORD dst_unused:UNUSED_PAD src0_sel:DWORD src1_sel:WORD_1\n"
    "v_mfma_f32_16x16x32_bf16 v[136:139], v[236:239], v[196:199], v[136:139]\n"
    "ds_read_b128 v[156:159], v243 offset:3072\n"
    "v_lshlrev_b32_sdwa v195, 1, v174 dst_sel:DWORD dst_unused:UNUSED_PAD src0_sel:DWORD src1_sel:WORD_1\n"
    "v_mfma_f32_16x16x32_bf16 v[140:143], a[244:247], v[196:199], v[140:143]\n"
    "s_waitcnt lgkmcnt(8)\n"
    "ds_read_b128 v[160:163], v243 offset:4096\n"
    "buffer_store_dwordx4 a[252:255], v255, s[68:71], s39 offen offset:128 sc1\n"
    "v_or_b32_e32 v189, v189, v205\n"
    "v_lshlrev_b32_sdwa v203, 1, v175 dst_sel:DWORD dst_unused:UNUSED_PAD src0_sel:DWORD src1_sel:WORD_1\n"
    "v_rcp_f16_sdwa v232, v234 dst_sel:WORD_1 dst_unused:UNUSED_PRESERVE src0_sel:WORD_1\n"
    "s_waitcnt lgkmcnt(7)\n"
    "v_mfma_f32_16x16x32_bf16 v[168:171], a[128:131], v[144:147], 0\n"
    "s_nop 0\n"
    "ds_read_b128 v[164:167], v243 offset:5120\n"
    "ds_read_b128 a[240:243], v244 offset:32768\n"
    "v_mfma_f32_16x16x32_bf16 v[172:175], a[132:135], v[144:147], 0\n"
    "ds_read_u16 v193, v193\n"
    "ds_read_u16_d16_hi v201, v201\n"
    "v_cvt_pk_f16_f32 v196, v176, v177\n"
    "s_waitcnt lgkmcnt(9)\n"
    "v_mfma_f32_16x16x32_bf16 v[168:171], a[176:179], v[148:151], v[168:171]\n"
    "v_or_b32_e32 v190, v190, v206\n"
    "v_lshlrev_b32_sdwa v197, 1, v178 dst_sel:DWORD dst_unused:UNUSED_PAD src0_sel:DWORD src1_sel:WORD_1\n"
    "v_lshlrev_b32_sdwa v205, 1, v179 dst_sel:DWORD dst_unused:UNUSED_PAD src0_sel:DWORD src1_sel:WORD_1\n"
    "v_lshlrev_b32_sdwa v198, 1, v180 dst_sel:DWORD dst_unused:UNUSED_PAD src0_sel:DWORD src1_sel:WORD_1\n"
    "v_lshlrev_b32_sdwa v206, 1, v181 dst_sel:DWORD dst_unused:UNUSED_PAD src0_sel:DWORD src1_sel:WORD_1\n"
    "v_lshlrev_b32_sdwa v199, 1, v182 dst_sel:DWORD dst_unused:UNUSED_PAD src0_sel:DWORD src1_sel:WORD_1\n"
    "v_fma_mix_f32 v234, v184, v232, 0 op_sel_hi:[1,1,0]\n"
    "v_mfma_f32_16x16x32_bf16 v[172:175], a[180:183], v[148:151], v[172:175]\n"
    "s_waitcnt lgkmcnt(7)\n"
    "ds_read_u16 v194, v194\n"
    "ds_read_u16_d16_hi v202, v202\n"
    "v_mfma_f32_16x16x32_bf16 v[168:171], a[96:99], v[152:155], v[168:171]\n"
    "s_nop 0\n"
    "v_fma_mix_f32 v232, v184, v232, 0 op_sel:[1,1,0] op_sel_hi:[1,1,0]\n"
    "v_cvt_pk_bf16_f32 v184, v234, v232\n"
    "v_pk_fma_f16 v232, v188, v188, s48\n"
    "v_pk_mul_f16 v232, v188, v232\n"
    "v_exp_f16_e32 v234, v232\n"
    "s_waitcnt lgkmcnt(7)\n"
    "v_mfma_f32_16x16x32_bf16 v[176:179], a[128:131], v[156:159], 0\n"
    "ds_read_b128 v[236:239], v244 offset:16384\n"
    "ds_read_u16 v195, v195\n"
    "s_waitcnt lgkmcnt(8)\n"
    "v_mfma_f32_16x16x32_bf16 v[176:179], a[176:179], v[160:163], v[176:179]\n"
    "s_waitcnt lgkmcnt(7)\n"
    "ds_read_u16_d16_hi v203, v203\n"
    "v_or_b32_e32 v191, v191, v207\n"
    "v_mfma_f32_16x16x32_bf16 v[176:179], a[96:99], v[164:167], v[176:179]\n"
    "s_waitcnt lgkmcnt(7)\n"
    "v_lshlrev_b32_sdwa v207, 1, v183 dst_sel:DWORD dst_unused:UNUSED_PAD src0_sel:DWORD src1_sel:WORD_1\n"
    "v_exp_f16_sdwa v234, v232 dst_sel:WORD_1 dst_unused:UNUSED_PRESERVE src0_sel:WORD_1\n"
    "v_mfma_f32_16x16x32_bf16 v[172:175], a[240:243], v[152:155], v[172:175]\n"
    "s_barrier\n"
    "v_mfma_f32_16x16x32_bf16 v[180:183], a[132:135], v[156:159], 0\n"
    "ds_read_b128 a[244:247], v244 offset:18432\n"
    "ds_write_b128 v253, v[64:67] offset:2048\n"
    "v_mfma_f32_16x16x32_bf16 v[180:183], a[180:183], v[160:163], v[180:183]\n"
    "s_waitcnt lgkmcnt(7)\n"
    "v_pk_fma_f16 v234, v234, s49, s49\n"
    "v_or_b32_e64 v193, v193, v201\n"
    "v_mfma_f32_16x16x32_bf16 v[180:183], a[240:243], v[164:167], v[180:183]\n"
    "v_rcp_f16_e64 v232, v234\n"
    "v_mfma_f32_16x16x32_bf16 v[96:99], a[64:67], v[184:187], v[96:99]\n"
    "ds_read_u16 v197, v197\n"
    "ds_write_b128 v253, v[68:71] offset:5120\n"
    "v_mfma_f32_16x16x32_bf16 v[100:103], a[68:71], v[184:187], v[100:103]\n"
    "s_waitcnt lgkmcnt(6)\n"
    "v_rcp_f16_sdwa v232, v234 dst_sel:WORD_1 dst_unused:UNUSED_PRESERVE src0_sel:WORD_1\n"
    "v_mfma_f32_16x16x32_bf16 v[112:115], a[72:75], v[184:187], v[112:115]\n"
    "s_nop 0\n"
    "v_fma_mix_f32 v234, v188, v232, 0 op_sel_hi:[1,1,0]\n"
    "v_fma_mix_f32 v232, v188, v232, 0 op_sel:[1,1,0] op_sel_hi:[1,1,0]\n"
    "v_mfma_f32_16x16x32_bf16 v[116:119], a[76:79], v[184:187], v[116:119]\n"
    "ds_read_u16_d16_hi v205, v205\n"
    "ds_write_b128 v253, v[88:91] offset:0\n"
    "v_mfma_f32_16x16x32_bf16 v[128:131], v[236:239], v[184:187], v[128:131]\n"
    "s_waitcnt lgkmcnt(5)\n"
    "v_or_b32_e32 v194, v194, v202\n"
    "v_cvt_pk_bf16_f32 v188, v234, v232\n"
    "v_mfma_f32_16x16x32_bf16 v[132:135], a[244:247], v[184:187], v[132:135]\n"
    "v_pk_fma_f16 v232, v192, v192, s48\n"
    "v_or_b32_e64 v195, v195, v203\n"
    "v_mfma_f32_16x16x32_bf16 v[104:107], a[64:67], v[188:191], v[104:107]\n"
    "buffer_load_dwordx4 a[232:235], v245, s[40:43], s57 offen offset:64\n"
    "buffer_load_dwordx4 a[224:227], v245, s[40:43], s57 offen offset:0\n"
    "ds_read_u16 v198, v198\n"
    "v_mfma_f32_16x16x32_bf16 v[108:111], a[68:71], v[188:191], v[108:111]\n"
    "ds_read_u16_d16_hi v206, v206\n"
    "ds_write_b128 v253, v[92:95] offset:3072\n"
    "v_mfma_f32_16x16x32_bf16 v[120:123], a[72:75], v[188:191], v[120:123]\n"
    "v_pk_mul_f16 v232, v192, v232\n"
    "v_lshlrev_b32_sdwa v184, 1, v168 dst_sel:DWORD dst_unused:UNUSED_PAD src0_sel:DWORD src1_sel:WORD_1\n"
    "v_mfma_f32_16x16x32_bf16 v[124:127], a[76:79], v[188:191], v[124:127]\n"
    "v_exp_f16_e64 v234, v232\n"
    "v_mfma_f32_16x16x32_bf16 v[136:139], v[236:239], v[188:191], v[136:139]\n"
    "ds_read_u16 v199, v199\n"
    "ds_write_b128 v253, v[72:75] offset:1024\n"
    "v_mfma_f32_16x16x32_bf16 v[140:143], a[244:247], v[188:191], v[140:143]\n"
    "s_waitcnt lgkmcnt(5)\n"
    "v_or_b32_e32 v197, v197, v205\n"
    "v_exp_f16_sdwa v234, v232 dst_sel:WORD_1 dst_unused:UNUSED_PRESERVE src0_sel:WORD_1\n"
    "v_lshlrev_b32_sdwa v200, 1, v169 dst_sel:DWORD dst_unused:UNUSED_PAD src0_sel:DWORD src1_sel:WORD_1\n"
    "v_pk_fma_f16 v234, v234, s49, s49\n"
    "v_lshlrev_b32_sdwa v185, 1, v170 dst_sel:DWORD dst_unused:UNUSED_PAD src0_sel:DWORD src1_sel:WORD_1\n"
    "v_lshlrev_b32_sdwa v201, 1, v171 dst_sel:DWORD dst_unused:UNUSED_PAD src0_sel:DWORD src1_sel:WORD_1\n"
    "v_mfma_f32_16x16x32_bf16 v[168:171], a[136:139], v[144:147], 0\n"
    "ds_read_u16_d16_hi v207, v207\n"
    "ds_write_b128 v253, v[76:79] offset:4096\n"
    "v_mfma_f32_16x16x32_bf16 v[168:171], a[184:187], v[148:151], v[168:171]\n"
    "v_lshlrev_b32_sdwa v186, 1, v172 dst_sel:DWORD dst_unused:UNUSED_PAD src0_sel:DWORD src1_sel:WORD_1\n"
    "v_lshlrev_b32_sdwa v202, 1, v173 dst_sel:DWORD dst_unused:UNUSED_PAD src0_sel:DWORD src1_sel:WORD_1\n"
    "v_lshlrev_b32_sdwa v187, 1, v174 dst_sel:DWORD dst_unused:UNUSED_PAD src0_sel:DWORD src1_sel:WORD_1\n"
    "v_lshlrev_b32_sdwa v203, 1, v175 dst_sel:DWORD dst_unused:UNUSED_PAD src0_sel:DWORD src1_sel:WORD_1\n"
    "v_rcp_f16_e32 v232, v234\n"
    "v_mfma_f32_16x16x32_bf16 v[168:171], a[100:103], v[152:155], v[168:171]\n"
    "s_waitcnt lgkmcnt(4)\n"
    "ds_read_b128 a[240:243], v244 offset:36864\n"
    "ds_read_u16 v184, v184\n"
    "v_or_b32_e64 v198, v198, v206\n"
    "v_rcp_f16_sdwa v232, v234 dst_sel:WORD_1 dst_unused:UNUSED_PRESERVE src0_sel:WORD_1\n"
    "v_mfma_f32_16x16x32_bf16 v[172:175], a[140:143], v[144:147], 0\n"
    "ds_read_u16_d16_hi v200, v200\n"
    "ds_read_u16 v185, v185\n"
    "v_lshlrev_b32_sdwa v188, 1, v176 dst_sel:DWORD dst_unused:UNUSED_PAD src0_sel:DWORD src1_sel:WORD_1\n"
    "v_lshlrev_b32_sdwa v204, 1, v177 dst_sel:DWORD dst_unused:UNUSED_PAD src0_sel:DWORD src1_sel:WORD_1\n"
    "v_lshlrev_b32_sdwa v189, 1, v178 dst_sel:DWORD dst_unused:UNUSED_PAD src0_sel:DWORD src1_sel:WORD_1\n"
    "v_lshlrev_b32_sdwa v205, 1, v179 dst_sel:DWORD dst_unused:UNUSED_PAD src0_sel:DWORD src1_sel:WORD_1\n"
    "v_lshlrev_b32_sdwa v190, 1, v180 dst_sel:DWORD dst_unused:UNUSED_PAD src0_sel:DWORD src1_sel:WORD_1\n"
    "v_lshlrev_b32_sdwa v206, 1, v181 dst_sel:DWORD dst_unused:UNUSED_PAD src0_sel:DWORD src1_sel:WORD_1\n"
    "v_lshlrev_b32_sdwa v191, 1, v182 dst_sel:DWORD dst_unused:UNUSED_PAD src0_sel:DWORD src1_sel:WORD_1\n"
    "v_fma_mix_f32 v234, v192, v232, 0 op_sel_hi:[1,1,0]\n"
    "v_mfma_f32_16x16x32_bf16 v[172:175], a[188:191], v[148:151], v[172:175]\n"
    "ds_read_u16_d16_hi v201, v201\n"
    "ds_read_u16 v186, v186\n"
    "buffer_load_dwordx4 a[236:239], v245, s[40:43], s88 offen offset:64\n"
    "buffer_load_dwordx4 a[228:231], v245, s[40:43], s88 offen offset:0\n"
    "v_fma_mix_f32 v232, v192, v232, 0 op_sel:[1,1,0] op_sel_hi:[1,1,0]\n"
    "v_mfma_f32_16x16x32_bf16 v[176:179], a[136:139], v[156:159], 0\n"
    "s_waitcnt lgkmcnt(6)\n"
    "ds_read_b128 v[236:239], v244 offset:20480\n"
    "ds_read_u16_d16_hi v202, v202\n"
    "v_or_b32_e32 v199, v199, v207\n"
    "v_lshlrev_b32_sdwa v207, 1, v183 dst_sel:DWORD dst_unused:UNUSED_PAD src0_sel:DWORD src1_sel:WORD_1\n"
    "v_cvt_pk_bf16_f32 v192, v234, v232\n"
    "v_mfma_f32_16x16x32_bf16 v[176:179], a[184:187], v[160:163], v[176:179]\n"
    "s_waitcnt lgkmcnt(7)\n"
    "v_pk_fma_f16 v232, v196, v196, s48\n"
    "v_pk_mul_f16 v232, v196, v232\n"
    "v_mfma_f32_16x16x32_bf16 v[172:175], a[240:243], v[152:155], v[172:175]\n"
    "s_nop 0\n"
    "ds_read_u16 v187, v187\n"
    "v_exp_f16_e64 v234, v232\n"
    "v_mfma_f32_16x16x32_bf16 v[176:179], a[100:103], v[164:167], v[176:179]\n"
    "ds_read_b128 a[244:247], v244 offset:22528\n"
    "v_exp_f16_sdwa v234, v232 dst_sel:WORD_1 dst_unused:UNUSED_PRESERVE src0_sel:WORD_1\n"
    "v_mfma_f32_16x16x32_bf16 v[180:183], a[140:143], v[156:159], 0\n"
    "ds_read_u16_d16_hi v203, v203\n"
    "v_pk_fma_f16 v234, v234, s49, s49\n"
    "v_mfma_f32_16x16x32_bf16 v[180:183], a[188:191], v[160:163], v[180:183]\n"
    "s_waitcnt lgkmcnt(8)\n"
    "v_rcp_f16_e32 v232, v234\n"
    "v_mfma_f32_16x16x32_bf16 v[180:183], a[240:243], v[164:167], v[180:183]\n"
    "ds_read_u16 v188, v188\n"
    "v_rcp_f16_sdwa v232, v234 dst_sel:WORD_1 dst_unused:UNUSED_PRESERVE src0_sel:WORD_1\n"
    "v_mfma_f32_16x16x32_bf16 v[96:99], a[80:83], v[192:195], v[96:99]\n"
    "ds_read_u16_d16_hi v204, v204\n"
    "v_fma_mix_f32 v234, v196, v232, 0 op_sel_hi:[1,1,0]\n"
    "v_mfma_f32_16x16x32_bf16 v[100:103], a[84:87], v[192:195], v[100:103]\n"
    "v_fma_mix_f32 v232, v196, v232, 0 op_sel:[1,1,0] op_sel_hi:[1,1,0]\n"
    "v_or_b32_e32 v184, v184, v200\n"
    "v_mfma_f32_16x16x32_bf16 v[112:115], a[88:91], v[192:195], v[112:115]\n"
    "s_waitcnt lgkmcnt(8)\n"
    "ds_read_u16 v189, v189\n"
    "v_cvt_pk_bf16_f32 v196, v234, v232\n"
    "v_mfma_f32_16x16x32_bf16 v[116:119], a[92:95], v[192:195], v[116:119]\n"
    "s_waitcnt lgkmcnt(7)\n"
    "v_or_b32_e32 v185, v185, v201\n"
    "v_lshlrev_b32_sdwa v201, 1, v171 dst_sel:DWORD dst_unused:UNUSED_PAD src0_sel:DWORD src1_sel:WORD_1\n"
    "v_mfma_f32_16x16x32_bf16 v[128:131], v[236:239], v[192:195], v[128:131]\n"
    "ds_read_u16_d16_hi v205, v205\n"
    "v_cvt_pk_bf16_f32 v96, v96, v97\n"
    "v_mfma_f32_16x16x32_bf16 v[104:107], a[80:83], v[196:199], v[104:107]\n"
    "ds_read_u16 v190, v190\n"
    "v_cvt_pk_bf16_f32 v97, v98, v99\n"
    "v_mfma_f32_16x16x32_bf16 v[108:111], a[84:87], v[196:199], v[108:111]\n"
    "s_waitcnt lgkmcnt(8)\n"
    "v_or_b32_e32 v186, v186, v202\n"
    "v_lshlrev_b32_sdwa v202, 1, v173 dst_sel:DWORD dst_unused:UNUSED_PAD src0_sel:DWORD src1_sel:WORD_1\n"
    "v_mfma_f32_16x16x32_bf16 v[120:123], a[88:91], v[196:199], v[120:123]\n"
    "ds_read_u16_d16_hi v206, v206\n"
    "v_cvt_pk_bf16_f32 v98, v100, v101\n"
    "s_waitcnt lgkmcnt(7)\n"
    "v_mfma_f32_16x16x32_bf16 v[132:135], a[244:247], v[192:195], v[132:135]\n"
    "s_nop 0\n"
    "v_cvt_pk_f16_f32 v192, v168, v169\n"
    "v_lshlrev_b32_sdwa v193, 1, v170 dst_sel:DWORD dst_unused:UNUSED_PAD src0_sel:DWORD src1_sel:WORD_1\n"
    "v_mfma_f32_16x16x32_bf16 v[124:127], a[92:95], v[196:199], v[124:127]\n"
    "s_nop 0\n"
    "ds_read_u16 v191, v191\n"
    "v_lshlrev_b32_sdwa v194, 1, v172 dst_sel:DWORD dst_unused:UNUSED_PAD src0_sel:DWORD src1_sel:WORD_1\n"
    "v_mfma_f32_16x16x32_bf16 v[136:139], v[236:239], v[196:199], v[136:139]\n"
    "s_waitcnt lgkmcnt(7)\n"
    "ds_read_u16_d16_hi v207, v207\n"
    "v_lshlrev_b32_sdwa v195, 1, v174 dst_sel:DWORD dst_unused:UNUSED_PAD src0_sel:DWORD src1_sel:WORD_1\n"
    "v_mfma_f32_16x16x32_bf16 v[140:143], a[244:247], v[196:199], v[140:143]\n"
    "v_cvt_pk_f16_f32 v196, v176, v177\n"
    "v_lshlrev_b32_sdwa v197, 1, v178 dst_sel:DWORD dst_unused:UNUSED_PAD src0_sel:DWORD src1_sel:WORD_1\n"
    "v_mfma_f32_16x16x32_bf16 v[168:171], a[144:147], v[144:147], 0\n"
    "ds_read_u16 v193, v193\n"
    "v_lshlrev_b32_sdwa v198, 1, v180 dst_sel:DWORD dst_unused:UNUSED_PAD src0_sel:DWORD src1_sel:WORD_1\n"
    "v_mfma_f32_16x16x32_bf16 v[168:171], a[192:195], v[148:151], v[168:171]\n"
    "v_lshlrev_b32_sdwa v199, 1, v182 dst_sel:DWORD dst_unused:UNUSED_PAD src0_sel:DWORD src1_sel:WORD_1\n"
    "v_or_b32_e32 v187, v187, v203\n"
    "v_mfma_f32_16x16x32_bf16 v[168:171], a[104:107], v[152:155], v[168:171]\n"
    "s_waitcnt lgkmcnt(7)\n"
    "ds_read_u16_d16_hi v201, v201\n"
    "v_lshlrev_b32_sdwa v203, 1, v175 dst_sel:DWORD dst_unused:UNUSED_PAD src0_sel:DWORD src1_sel:WORD_1\n"
    "v_mfma_f32_16x16x32_bf16 v[172:175], a[148:151], v[144:147], 0\n"
    "v_or_b32_e64 v188, v188, v204\n"
    "v_cvt_pk_bf16_f32 v99, v102, v103\n"
    "v_mfma_f32_16x16x32_bf16 v[172:175], a[196:199], v[148:151], v[172:175]\n"
    "ds_read_u16 v194, v194\n"
    "v_cvt_pk_bf16_f32 v112, v112, v113\n"
    "v_mfma_f32_16x16x32_bf16 v[172:175], a[108:111], v[152:155], v[172:175]\n"
    "s_waitcnt lgkmcnt(7)\n"
    "ds_read_u16_d16_hi v202, v202\n"
    "v_or_b32_e32 v189, v189, v205\n"
    "v_lshlrev_b32_sdwa v205, 1, v179 dst_sel:DWORD dst_unused:UNUSED_PAD src0_sel:DWORD src1_sel:WORD_1\n"
    "v_mfma_f32_16x16x32_bf16 v[176:179], a[144:147], v[156:159], 0\n"
    "v_cvt_pk_bf16_f32 v113, v114, v115\n"
    "v_cvt_pk_bf16_f32 v114, v116, v117\n"
    "v_mfma_f32_16x16x32_bf16 v[176:179], a[192:195], v[160:163], v[176:179]\n"
    "ds_read_b128 v[236:239], v244 offset:0\n"
    "v_cvt_pk_bf16_f32 v115, v118, v119\n"
    "v_mfma_f32_16x16x32_bf16 v[176:179], a[104:107], v[164:167], v[176:179]\n"
    "ds_read_u16 v195, v195\n"
    "s_waitcnt lgkmcnt(8)\n"
    "v_or_b32_e32 v190, v190, v206\n"
    "v_mfma_f32_16x16x32_bf16 v[100:103], a[4:7], v[184:187], 0\n"
    "ds_read_u16_d16_hi v203, v203\n"
    "v_lshlrev_b32_sdwa v206, 1, v181 dst_sel:DWORD dst_unused:UNUSED_PAD src0_sel:DWORD src1_sel:WORD_1\n"
    "v_cvt_pk_bf16_f32 v128, v128, v129\n"
    "v_cvt_pk_bf16_f32 v129, v130, v131\n"
    "v_cvt_pk_bf16_f32 v130, v132, v133\n"
    "v_cvt_pk_bf16_f32 v131, v134, v135\n"
    "v_cvt_pk_bf16_f32 v104, v104, v105\n"
    "s_waitcnt vmcnt(23)\n"
    "ds_write2_b32 v248, v0, v1 offset0:0 offset1:20\n"
    "s_waitcnt lgkmcnt(8)\n"
    "v_or_b32_e64 v191, v191, v207\n"
    "v_mfma_f32_16x16x32_bf16 v[116:119], a[12:15], v[184:187], 0\n"
    "buffer_load_dwordx4 v[64:67], v246, s[40:43], s60 offen offset:128\n"
    "ds_write2_b32 v248, v2, v3 offset0:40 offset1:60\n"
    "v_add_u32_e32 v248, 1360, v248\n"
    "v_lshlrev_b32_sdwa v207, 1, v183 dst_sel:DWORD dst_unused:UNUSED_PAD src0_sel:DWORD src1_sel:WORD_1\n"
    "v_mfma_f32_16x16x32_bf16 v[180:183], a[148:151], v[156:159], 0\n"
    "ds_read_b128 a[244:247], v244 offset:2048\n"
    "s_waitcnt vmcnt(22)\n"
    "ds_write2_b32 v235, v20, v21 offset0:0 offset1:20\n"
    "v_mfma_f32_16x16x32_bf16 v[180:183], a[196:199], v[160:163], v[180:183]\n"
    "s_waitcnt lgkmcnt(9)\n"
    "v_or_b32_e64 v193, v193, v201\n"
    "v_cvt_pk_bf16_f32 v105, v106, v107\n"
    "v_mfma_f32_16x16x32_bf16 v[180:183], a[108:111], v[164:167], v[180:183]\n"
    "ds_read_u16 v197, v197\n"
    "ds_write2_b32 v235, v22, v23 offset0:40 offset1:60\n"
    "v_add_u32_e32 v235, 1360, v235\n"
    "v_cvt_pk_bf16_f32 v106, v108, v109\n"
    "v_cvt_pk_bf16_f32 v107, v110, v111\n"
    "v_mfma_f32_16x16x32_bf16 v[108:111], a[4:7], v[188:191], 0\n"
    "s_waitcnt lgkmcnt(8)\n"
    "ds_read_u16_d16_hi v205, v205\n"
    "s_waitcnt vmcnt(20)\n"
    "ds_write2_b32 v248, v4, v5 offset0:0 offset1:20\n"
    "v_or_b32_e64 v194, v194, v202\n"
    "v_cvt_pk_bf16_f32 v120, v120, v121\n"
    "v_cvt_pk_bf16_f32 v121, v122, v123\n"
    "v_cvt_pk_bf16_f32 v122, v124, v125\n"
    "ds_write2_b32 v248, v6, v7 offset0:40 offset1:60\n"
    "v_add_u32_e32 v248, 1360, v248\n"
    "v_cvt_pk_bf16_f32 v123, v126, v127\n"
    "ds_write_b128 v242, v[96:99] offset:24640\n"
    "v_mfma_f32_16x16x32_bf16 v[96:99], a[0:3], v[184:187], 0\n"
    "s_waitcnt lgkmcnt(6)\n"
    "v_cvt_pk_bf16_f32 v136, v136, v137\n"
    "v_pk_fma_f16 v232, v192, v192, s48\n"
    "v_mfma_f32_16x16x32_bf16 v[132:135], a[244:247], v[184:187], 0\n"
    "ds_read_u16 v198, v198\n"
    "s_waitcnt vmcnt(17)\n"
    "ds_write2_b32 v235, v24, v25 offset0:0 offset1:20\n"
    "v_mfma_f32_16x16x32_bf16 v[124:127], a[12:15], v[188:191], 0\n"
    "v_cvt_pk_bf16_f32 v137, v138, v139\n"
    "v_cvt_pk_bf16_f32 v138, v140, v141\n"
    "ds_write_b128 v242, v[112:115] offset:26688\n"
    "v_mfma_f32_16x16x32_bf16 v[112:115], a[8:11], v[184:187], 0\n"
    "s_waitcnt lgkmcnt(4)\n"
    "v_or_b32_e32 v195, v195, v203\n"
    "v_cvt_pk_bf16_f32 v139, v142, v143\n"
    "v_pk_mul_f16 v232, v192, v232\n"
    "v_or_b32_e64 v197, v197, v205\n"
    "ds_write2_b32 v235, v26, v27 offset0:40 offset1:60\n"
    "v_add_u32_e32 v235, -1360, v235\n"
    "v_exp_f16_e64 v234, v232\n"
    "v_mfma_f32_16x16x32_bf16 v[140:143], a[244:247], v[188:191], 0\n"
    "ds_read_u16_d16_hi v206, v206\n"
    "ds_write_b128 v242, v[128:131] offset:28736\n"
    "v_mfma_f32_16x16x32_bf16 v[128:131], v[236:239], v[184:187], 0\n"
    "buffer_load_dwordx4 v[68:71], v246, s[40:43], s61 offen offset:128\n"
    "v_exp_f16_sdwa v234, v232 dst_sel:WORD_1 dst_unused:UNUSED_PRESERVE src0_sel:WORD_1\n"
    "v_cvt_pk_f16_f32 v184, v168, v169\n"
    "v_pk_fma_f16 v234, v234, s49, s49\n"
    "v_lshlrev_b32_sdwa v185, 1, v170 dst_sel:DWORD dst_unused:UNUSED_PAD src0_sel:DWORD src1_sel:WORD_1\n"
    "v_lshlrev_b32_sdwa v201, 1, v171 dst_sel:DWORD dst_unused:UNUSED_PAD src0_sel:DWORD src1_sel:WORD_1\n"
    "v_mfma_f32_16x16x32_bf16 v[168:171], a[152:155], v[144:147], 0\n"
    "ds_read_u16 v199, v199\n"
    "ds_write_b128 v242, v[104:107] offset:25664\n"
    "v_mfma_f32_16x16x32_bf16 v[104:107], a[0:3], v[188:191], 0\n"
    "v_lshlrev_b32_sdwa v186, 1, v172 dst_sel:DWORD dst_unused:UNUSED_PAD src0_sel:DWORD src1_sel:WORD_1\n"
    "v_lshlrev_b32_sdwa v202, 1, v173 dst_sel:DWORD dst_unused:UNUSED_PAD src0_sel:DWORD src1_sel:WORD_1\n"
    "v_mfma_f32_16x16x32_bf16 v[168:171], a[200:203], v[148:151], v[168:171]\n"
    "v_lshlrev_b32_sdwa v187, 1, v174 dst_sel:DWORD dst_unused:UNUSED_PAD src0_sel:DWORD src1_sel:WORD_1\n"
    "v_lshlrev_b32_sdwa v203, 1, v175 dst_sel:DWORD dst_unused:UNUSED_PAD src0_sel:DWORD src1_sel:WORD_1\n"
    "v_rcp_f16_e64 v232, v234\n"
    "v_mfma_f32_16x16x32_bf16 v[168:171], a[112:115], v[152:155], v[168:171]\n"
    "ds_read_u16_d16_hi v207, v207\n"
    "s_waitcnt vmcnt(15)\n"
    "ds_write2_b32 v248, v8, v9 offset0:0 offset1:20\n"
    "s_waitcnt lgkmcnt(4)\n"
    "v_or_b32_e64 v198, v198, v206\n"
    "v_rcp_f16_sdwa v232, v234 dst_sel:WORD_1 dst_unused:UNUSED_PRESERVE src0_sel:WORD_1\n"
    "v_mfma_f32_16x16x32_bf16 v[172:175], a[156:159], v[144:147], 0\n"
    "ds_read_b128 v[216:219], v251 offset:0\n"
    "ds_write_b128 v242, v[120:123] offset:27712\n"
    "v_mfma_f32_16x16x32_bf16 v[120:123], a[8:11], v[188:191], 0\n"
    "v_lshlrev_b32_sdwa v205, 1, v179 dst_sel:DWORD dst_unused:UNUSED_PAD src0_sel:DWORD src1_sel:WORD_1\n"
    "v_lshlrev_b32_sdwa v206, 1, v181 dst_sel:DWORD dst_unused:UNUSED_PAD src0_sel:DWORD src1_sel:WORD_1\n"
    "v_mfma_f32_16x16x32_bf16 v[172:175], a[204:207], v[148:151], v[172:175]\n"
    "v_fma_mix_f32 v234, v192, v232, 0 op_sel_hi:[1,1,0]\n"
    "v_fma_mix_f32 v232, v192, v232, 0 op_sel:[1,1,0] op_sel_hi:[1,1,0]\n"
    "v_mfma_f32_16x16x32_bf16 v[172:175], a[116:119], v[152:155], v[172:175]\n"
    "ds_read_b128 v[220:223], v251 offset:336\n"
    "ds_write2_b32 v248, v10, v11 offset0:40 offset1:60\n"
    "v_add_u32_e32 v248, 1360, v248\n"
    "v_cvt_pk_bf16_f32 v192, v234, v232\n"
    "v_pk_fma_f16 v232, v196, v196, s48\n"
    "v_lshlrev_b32_sdwa v200, 1, v169 dst_sel:DWORD dst_unused:UNUSED_PAD src0_sel:DWORD src1_sel:WORD_1\n"
    "v_pk_mul_f16 v232, v196, v232\n"
    "ds_write_b128 v242, v[136:139] offset:29760\n"
    "v_mfma_f32_16x16x32_bf16 v[136:139], v[236:239], v[188:191], 0\n"
    "s_waitcnt lgkmcnt(4)\n"
    "v_cvt_pk_f16_f32 v188, v176, v177\n"
    "v_lshlrev_b32_sdwa v189, 1, v178 dst_sel:DWORD dst_unused:UNUSED_PAD src0_sel:DWORD src1_sel:WORD_1\n"
    "v_mfma_f32_16x16x32_bf16 v[176:179], a[152:155], v[156:159], 0\n"
    "ds_read_u16 v185, v185\n"
    "s_waitcnt vmcnt(12)\n"
    "ds_write2_b32 v248, v12, v13 offset0:0 offset1:20\n"
    "v_mfma_f32_16x16x32_bf16 v[176:179], a[200:203], v[160:163], v[176:179]\n"
    "v_lshlrev_b32_sdwa v190, 1, v180 dst_sel:DWORD dst_unused:UNUSED_PAD src0_sel:DWORD src1_sel:WORD_1\n"
    "v_lshlrev_b32_sdwa v191, 1, v182 dst_sel:DWORD dst_unused:UNUSED_PAD src0_sel:DWORD src1_sel:WORD_1\n"
    "v_mfma_f32_16x16x32_bf16 v[176:179], a[112:115], v[164:167], v[176:179]\n"
    "ds_read_u16_d16_hi v201, v201\n"
    "ds_write2_b32 v248, v14, v15 offset0:40 offset1:60\n"
    "v_add_u32_e32 v248, 1360, v248\n"
    "buffer_load_dwordx4 v[72:75], v246, s[40:43], s62 offen offset:128\n"
    "v_or_b32_e32 v199, v199, v207\n"
    "s_waitcnt vmcnt(10)\n"
    "ds_write2_b32 v248, v16, v17 offset0:0 offset1:20\n"
    "v_lshlrev_b32_sdwa v207, 1, v183 dst_sel:DWORD dst_unused:UNUSED_PAD src0_sel:DWORD src1_sel:WORD_1\n"
    "v_mfma_f32_16x16x32_bf16 v[180:183], a[156:159], v[156:159], 0\n"
    "v_exp_f16_e64 v234, v232\n"
    "v_mfma_f32_16x16x32_bf16 v[180:183], a[204:207], v[160:163], v[180:183]\n"
    "ds_read_u16 v186, v186\n"
    "ds_write2_b32 v248, v18, v19 offset0:40 offset1:60\n"
    "v_mfma_f32_32x32x1_2b_f32 v[0:31], v254, v254, 0\n"
    "v_add_u32_e32 v248, -5440, v248\n"
    "v_exp_f16_sdwa v234, v232 dst_sel:WORD_1 dst_unused:UNUSED_PRESERVE src0_sel:WORD_1\n"
    "v_lshlrev_b32_sdwa v204, 1, v177 dst_sel:DWORD dst_unused:UNUSED_PAD src0_sel:DWORD src1_sel:WORD_1\n"
    "v_pk_fma_f16 v234, v234, s49, s49\n"
    "v_rcp_f16_e64 v232, v234\n"
    "ds_read_u16_d16_hi v202, v202\n"
    "ds_read_b128 v[236:239], v244 offset:4096\n"
    "v_rcp_f16_sdwa v232, v234 dst_sel:WORD_1 dst_unused:UNUSED_PRESERVE src0_sel:WORD_1\n"
    "ds_read_u16 v187, v187\n"
    "ds_read_u16_d16_hi v203, v203\n"
    "v_mfma_f32_16x16x32_bf16 v[180:183], a[116:119], v[164:167], v[180:183]\n"
    "s_waitcnt lgkmcnt(6)\n"
    "ds_read_b128 a[244:247], v244 offset:6144\n"
    "ds_read_u16 v189, v189\n"
    "v_mfma_f32_16x16x32_bf16 v[96:99], a[16:19], v[192:195], v[96:99]\n"
    "v_or_b32_e32 v185, v185, v201\n"
    "v_fma_mix_f32 v234, v196, v232, 0 op_sel_hi:[1,1,0]\n"
    "v_mfma_f32_16x16x32_bf16 v[100:103], a[20:23], v[192:195], v[100:103]\n"
    "ds_read_u16_d16_hi v205, v205\n"
    "v_fma_mix_f32 v232, v196, v232, 0 op_sel:[1,1,0] op_sel_hi:[1,1,0]\n"
    "v_mfma_f32_16x16x32_bf16 v[112:115], a[24:27], v[192:195], v[112:115]\n"
    "s_waitcnt lgkmcnt(7)\n"
    "s_barrier\n"
    "v_mfma_f32_16x16x32_bf16 v[116:119], a[28:31], v[192:195], v[116:119]\n"
    "ds_read_b128 v[208:211], v250 offset:0\n"
    "ds_read_b128 v[212:215], v250 offset:320\n"
    "v_cvt_pk_bf16_f32 v196, v234, v232\n"
    "v_pk_fma_f16 v232, v184, v184, s48\n"
    "v_lshlrev_b32_sdwa v201, 1, v171 dst_sel:DWORD dst_unused:UNUSED_PAD src0_sel:DWORD src1_sel:WORD_1\n"
    "v_mfma_f32_16x16x32_bf16 v[104:107], a[16:19], v[196:199], v[104:107]\n"
    "s_waitcnt lgkmcnt(7)\n"
    "ds_read_u16 v190, v190\n"
    "v_or_b32_e32 v186, v186, v202\n"
    "v_mfma_f32_16x16x32_bf16 v[128:131], v[236:239], v[192:195], v[128:131]\n"
    "ds_read_u16_d16_hi v206, v206\n"
    "v_pk_mul_f16 v232, v184, v232\n"
    "v_mfma_f32_16x16x32_bf16 v[108:111], a[20:23], v[196:199], v[108:111]\n"
    "v_exp_f16_e64 v234, v232\n"
    "v_mfma_f32_16x16x32_bf16 v[120:123], a[24:27], v[196:199], v[120:123]\n"
    "ds_read_u16 v191, v191\n"
    "v_exp_f16_sdwa v234, v232 dst_sel:WORD_1 dst_unused:UNUSED_PRESERVE src0_sel:WORD_1\n"
    "v_mfma_f32_16x16x32_bf16 v[124:127], a[28:31], v[196:199], v[124:127]\n"
    "s_waitcnt lgkmcnt(7)\n"
    "ds_read_u16_d16_hi v207, v207\n"
    "v_or_b32_e32 v187, v187, v203\n"
    "v_mfma_f32_16x16x32_bf16 v[132:135], a[244:247], v[192:195], v[132:135]\n"
    "v_pk_fma_f16 v234, v234, s49, s49\n"
    "v_lshlrev_b32_sdwa v192, 1, v168 dst_sel:DWORD dst_unused:UNUSED_PAD src0_sel:DWORD src1_sel:WORD_1\n"
    "v_mfma_f32_16x16x32_bf16 v[136:139], v[236:239], v[196:199], v[136:139]\n"
    "ds_read_b128 a[240:243], v244 offset:34816\n"
    "v_lshlrev_b32_sdwa v193, 1, v170 dst_sel:DWORD dst_unused:UNUSED_PAD src0_sel:DWORD src1_sel:WORD_1\n"
    "v_mfma_f32_16x16x32_bf16 v[140:143], a[244:247], v[196:199], v[140:143]\n"
    "buffer_load_dwordx4 v[76:79], v246, s[40:43], s63 offen offset:128\n"
    "ds_read_u16 v192, v192\n"
    "v_mfma_f32_16x16x32_bf16 v[168:171], a[160:163], v[144:147], 0\n"
    "v_lshlrev_b32_sdwa v194, 1, v172 dst_sel:DWORD dst_unused:UNUSED_PAD src0_sel:DWORD src1_sel:WORD_1\n"
    "v_lshlrev_b32_sdwa v202, 1, v173 dst_sel:DWORD dst_unused:UNUSED_PAD src0_sel:DWORD src1_sel:WORD_1\n"
    "v_mfma_f32_16x16x32_bf16 v[168:171], a[208:211], v[148:151], v[168:171]\n"
    "s_waitcnt lgkmcnt(7)\n"
    "ds_read_u16_d16_hi v200, v200\n"
    "v_or_b32_e32 v189, v189, v205\n"
    "s_waitcnt lgkmcnt(7)\n"
    "v_smfmac_f32_16x16x64_bf16 v[0:3], v[216:219], v[208:215], v240\n"
    "s_nop 0\n"
    "ds_read_b128 v[216:219], v252 offset:0\n"
    "v_smfmac_f32_16x16x64_bf16 v[4:7], v[220:223], v[208:215], v240 abid:1\n"
    "ds_read_b128 v[208:211], v250 offset:640\n"
    "v_lshlrev_b32_sdwa v195, 1, v174 dst_sel:DWORD dst_unused:UNUSED_PAD src0_sel:DWORD src1_sel:WORD_1\n"
    "ds_read_b128 v[212:215], v250 offset:960\n"
    "v_lshlrev_b32_sdwa v203, 1, v175 dst_sel:DWORD dst_unused:UNUSED_PAD src0_sel:DWORD src1_sel:WORD_1\n"
    "ds_read_b128 v[220:223], v252 offset:336\n"
    "v_rcp_f16_e32 v232, v234\n"
    "v_mfma_f32_16x16x32_bf16 v[168:171], a[120:123], v[152:155], v[168:171]\n"
    "s_waitcnt lgkmcnt(9)\n"
    "ds_read_u16 v193, v193\n"
    "v_or_b32_e64 v190, v190, v206\n"
    "v_rcp_f16_sdwa v232, v234 dst_sel:WORD_1 dst_unused:UNUSED_PRESERVE src0_sel:WORD_1\n"
    "v_mfma_f32_16x16x32_bf16 v[172:175], a[164:167], v[144:147], 0\n"
    "ds_read_u16_d16_hi v201, v201\n"
    "v_lshlrev_b32_sdwa v196, 1, v176 dst_sel:DWORD dst_unused:UNUSED_PAD src0_sel:DWORD src1_sel:WORD_1\n"
    "v_lshlrev_b32_sdwa v197, 1, v178 dst_sel:DWORD dst_unused:UNUSED_PAD src0_sel:DWORD src1_sel:WORD_1\n"
    "v_lshlrev_b32_sdwa v205, 1, v179 dst_sel:DWORD dst_unused:UNUSED_PAD src0_sel:DWORD src1_sel:WORD_1\n"
    "v_lshlrev_b32_sdwa v198, 1, v180 dst_sel:DWORD dst_unused:UNUSED_PAD src0_sel:DWORD src1_sel:WORD_1\n"
    "v_lshlrev_b32_sdwa v206, 1, v181 dst_sel:DWORD dst_unused:UNUSED_PAD src0_sel:DWORD src1_sel:WORD_1\n"
    "v_lshlrev_b32_sdwa v199, 1, v182 dst_sel:DWORD dst_unused:UNUSED_PAD src0_sel:DWORD src1_sel:WORD_1\n"
    "v_fma_mix_f32 v234, v184, v232, 0 op_sel_hi:[1,1,0]\n"
    "v_mfma_f32_16x16x32_bf16 v[172:175], a[212:215], v[148:151], v[172:175]\n"
    "s_waitcnt lgkmcnt(8)\n"
    "ds_read_u16 v194, v194\n"
    "v_or_b32_e32 v191, v191, v207\n"
    "v_mfma_f32_16x16x32_bf16 v[172:175], a[240:243], v[152:155], v[172:175]\n"
    "ds_read_b128 v[236:239], v244 offset:8192\n"
    "v_lshlrev_b32_sdwa v207, 1, v183 dst_sel:DWORD dst_unused:UNUSED_PAD src0_sel:DWORD src1_sel:WORD_1\n"
    "v_fma_mix_f32 v232, v184, v232, 0 op_sel:[1,1,0] op_sel_hi:[1,1,0]\n"
    "v_mfma_f32_16x16x32_bf16 v[176:179], a[160:163], v[156:159], 0\n"
    "ds_read_u16_d16_hi v202, v202\n"
    "v_cvt_pk_bf16_f32 v184, v234, v232\n"
    "v_mfma_f32_16x16x32_bf16 v[176:179], a[208:211], v[160:163], v[176:179]\n"
    "ds_read_u16 v195, v195\n"
    "v_pk_fma_f16 v232, v188, v188, s48\n"
    "v_mfma_f32_16x16x32_bf16 v[176:179], a[120:123], v[164:167], v[176:179]\n"
    "s_waitcnt lgkmcnt(8)\n"
    "v_pk_mul_f16 v232, v188, v232\n"
    "v_or_b32_e64 v192, v192, v200\n"
    "v_mfma_f32_16x16x32_bf16 v[180:183], a[164:167], v[156:159], 0\n"
    "s_waitcnt lgkmcnt(7)\n"
    "ds_read_b128 a[244:247], v244 offset:10240\n"
    "v_exp_f16_e32 v234, v232\n"
    "v_smfmac_f32_16x16x64_bf16 v[0:3], v[216:219], v[208:215], v240\n"
    "s_waitcnt lgkmcnt(7)\n"
    "ds_read_b128 v[216:219], v251 offset:672\n"
    "ds_read_u16_d16_hi v203, v203\n"
    "v_smfmac_f32_16x16x64_bf16 v[4:7], v[220:223], v[208:215], v240 abid:1\n"
    "s_nop 0\n"
    "v_exp_f16_sdwa v234, v232 dst_sel:WORD_1 dst_unused:UNUSED_PRESERVE src0_sel:WORD_1\n"
    "s_waitcnt vmcnt(5)\n"
    "v_smfmac_f32_32x32x32_bf16 v[112:127], a[248:251], a[232:239], v241\n"
    "ds_read_b128 v[208:211], v250 offset:80\n"
    "ds_read_b128 v[212:215], v250 offset:400\n"
    "v_pk_fma_f16 v234, v234, s49, s49\n"
    "v_rcp_f16_e64 v232, v234\n"
    "s_waitcnt vmcnt(4)\n"
    "v_smfmac_f32_32x32x32_bf16 v[96:111], a[248:251], a[224:231], v241\n"
    "s_waitcnt lgkmcnt(9)\n"
    "ds_read_b128 v[220:223], v251 offset:1008\n"
    "buffer_load_dwordx4 v[80:83], v246, s[40:43], s52 offen offset:128\n"
    "v_rcp_f16_sdwa v232, v234 dst_sel:WORD_1 dst_unused:UNUSED_PRESERVE src0_sel:WORD_1\n"
    "v_or_b32_e64 v193, v193, v201\n"
    "v_fma_mix_f32 v234, v188, v232, 0 op_sel_hi:[1,1,0]\n"
    "v_fma_mix_f32 v232, v188, v232, 0 op_sel:[1,1,0] op_sel_hi:[1,1,0]\n"
    "v_lshlrev_b32_sdwa v201, 1, v171 dst_sel:DWORD dst_unused:UNUSED_PAD src0_sel:DWORD src1_sel:WORD_1\n"
    "v_pk_mov_b32 v[224:225], v[0:1], v[0:1] op_sel:[0,1]\n"
    "v_pk_mov_b32 v[226:227], v[2:3], v[2:3] op_sel:[0,1]\n"
    "v_pk_mul_f32 v[228:229], v[0:1], v[0:1]\n"
    "v_pk_mul_f32 v[230:231], v[2:3], v[2:3]\n"
    "v_pk_add_f32 v[224:225], v[224:225], v[4:5]\n"
    "v_pk_add_f32 v[226:227], v[226:227], v[6:7]\n"
    "v_pk_fma_f32 v[228:229], v[4:5], v[4:5], v[228:229]\n"
    "v_pk_fma_f32 v[230:231], v[6:7], v[6:7], v[230:231]\n"
    "v_mfma_f32_16x16x32_bf16 v[180:183], a[212:215], v[160:163], v[180:183]\n"
    "s_waitcnt lgkmcnt(7)\n"
    "ds_read_b128 a[232:235], v243 offset:24640\n"
    "ds_read_b128 a[236:239], v243 offset:25664\n"
    "v_mfma_f32_16x16x32_bf16 v[180:183], a[240:243], v[164:167], v[180:183]\n"
    "s_waitcnt lgkmcnt(7)\n"
    "ds_read_u16 v196, v196\n"
    "ds_read_u16_d16_hi v204, v204\n"
    "v_mfma_f32_16x16x32_bf16 v[112:115], a[40:43], v[184:187], v[112:115]\n"
    "ds_read_u16 v197, v197\n"
    "v_cvt_pk_bf16_f32 v188, v234, v232\n"
    "v_mfma_f32_16x16x32_bf16 v[116:119], a[44:47], v[184:187], v[116:119]\n"
    "v_or_b32_e64 v194, v194, v202\n"
    "v_lshlrev_b32_sdwa v202, 1, v173 dst_sel:DWORD dst_unused:UNUSED_PAD src0_sel:DWORD src1_sel:WORD_1\n"
    "v_mfma_f32_16x16x32_bf16 v[128:131], v[236:239], v[184:187], v[128:131]\n"
    "s_nop 0\n"
    "ds_read_u16_d16_hi v205, v205\n"
    "v_mfma_f32_16x16x32_bf16 v[132:135], a[244:247], v[184:187], v[132:135]\n"
    "s_waitcnt lgkmcnt(8)\n"
    "ds_read_u16 v198, v198\n"
    "buffer_load_dwordx4 a[224:227], v245, s[40:43], s57 offen offset:128\n"
    "s_waitcnt lgkmcnt(8)\n"
    "v_smfmac_f32_16x16x64_bf16 v[8:11], v[216:219], v[208:215], v240\n"
    "s_waitcnt lgkmcnt(7)\n"
    "v_or_b32_e64 v195, v195, v203\n"
    "v_smfmac_f32_16x16x64_bf16 v[12:15], v[220:223], v[208:215], v240 abid:1\n"
    "ds_read_b128 v[208:211], v250 offset:720\n"
    "ds_read_b128 v[212:215], v250 offset:1040\n"
    "v_mfma_f32_16x16x32_bf16 v[120:123], a[40:43], v[188:191], v[120:123]\n"
    "v_lshlrev_b32_sdwa v203, 1, v175 dst_sel:DWORD dst_unused:UNUSED_PAD src0_sel:DWORD src1_sel:WORD_1\n"
    "ds_read_b128 v[216:219], v252 offset:672\n"
    "s_waitcnt lgkmcnt(8)\n"
    "v_smfmac_f32_32x32x32_bf16 v[96:111], a[248:251], a[232:239], v241\n"
    "s_nop 0\n"
    "ds_read_b128 v[220:223], v252 offset:1008\n"
    "ds_read_u16_d16_hi v206, v206\n"
    "v_mfma_f32_16x16x32_bf16 v[124:127], a[44:47], v[188:191], v[124:127]\n"
    "v_mfma_f32_16x16x32_bf16 v[136:139], v[236:239], v[188:191], v[136:139]\n"
    "ds_read_u16 v199, v199\n"
    "v_mfma_f32_16x16x32_bf16 v[140:143], a[244:247], v[188:191], v[140:143]\n"
    "s_waitcnt lgkmcnt(9)\n"
    "ds_read_u16_d16_hi v207, v207\n"
    "v_or_b32_e32 v196, v196, v204\n"
    "buffer_load_dwordx4 a[228:231], v245, s[40:43], s88 offen offset:128\n"
    "v_mfma_f32_16x16x32_bf16 v[96:99], a[32:35], v[184:187], v[96:99]\n"
    "ds_read_b128 a[232:235], v243 offset:26688\n"
    "v_mfma_f32_16x16x32_bf16 v[100:103], a[36:39], v[184:187], v[100:103]\n"
    "s_nop 0\n"
    "ds_read_b128 a[236:239], v243 offset:27712\n"
    "v_cvt_pk_f16_f32 v184, v168, v169\n"
    "v_mfma_f32_16x16x32_bf16 v[104:107], a[32:35], v[188:191], v[104:107]\n"
    "s_waitcnt lgkmcnt(10)\n"
    "ds_read_b128 a[240:243], v244 offset:38912\n"
    "v_lshlrev_b32_sdwa v185, 1, v170 dst_sel:DWORD dst_unused:UNUSED_PAD src0_sel:DWORD src1_sel:WORD_1\n"
    "v_mfma_f32_16x16x32_bf16 v[108:111], a[36:39], v[188:191], v[108:111]\n"
    "ds_read_u16 v185, v185\n"
    "v_lshlrev_b32_sdwa v186, 1, v172 dst_sel:DWORD dst_unused:UNUSED_PAD src0_sel:DWORD src1_sel:WORD_1\n"
    "v_mfma_f32_16x16x32_bf16 v[168:171], a[168:171], v[144:147], 0\n"
    "v_lshlrev_b32_sdwa v187, 1, v174 dst_sel:DWORD dst_unused:UNUSED_PAD src0_sel:DWORD src1_sel:WORD_1\n"
    "v_cvt_pk_f16_f32 v188, v176, v177\n"
    "v_mfma_f32_16x16x32_bf16 v[168:171], a[216:219], v[148:151], v[168:171]\n"
    "s_waitcnt lgkmcnt(8)\n"
    "ds_read_u16_d16_hi v201, v201\n"
    "v_lshlrev_b32_sdwa v189, 1, v178 dst_sel:DWORD dst_unused:UNUSED_PAD src0_sel:DWORD src1_sel:WORD_1\n"
    "v_smfmac_f32_16x16x64_bf16 v[8:11], v[216:219], v[208:215], v240\n"
    "s_waitcnt lgkmcnt(8)\n"
    "v_lshlrev_b32_sdwa v190, 1, v180 dst_sel:DWORD dst_unused:UNUSED_PAD src0_sel:DWORD src1_sel:WORD_1\n"
    "v_smfmac_f32_16x16x64_bf16 v[12:15], v[220:223], v[208:215], v240 abid:1\n"
    "ds_read_b128 v[208:211], v250 offset:160\n"
    "ds_read_b128 v[212:215], v250 offset:480\n"
    "v_mfma_f32_16x16x32_bf16 v[168:171], a[124:127], v[152:155], v[168:171]\n"
    "buffer_load_dwordx4 v[84:87], v247, s[40:43], s60 offen offset:128\n"
    "ds_read_b128 v[216:219], v251 offset:1344\n"
    "v_lshlrev_b32_sdwa v191, 1, v182 dst_sel:DWORD dst_unused:UNUSED_PAD src0_sel:DWORD src1_sel:WORD_1\n"
    "v_or_b32_e64 v197, v197, v205\n"
    "v_lshlrev_b32_sdwa v205, 1, v179 dst_sel:DWORD dst_unused:UNUSED_PAD src0_sel:DWORD src1_sel:WORD_1\n"
    "v_pk_add_f32 v[224:225], v[224:225], v[8:9]\n"
    "v_pk_add_f32 v[226:227], v[226:227], v[10:11]\n"
    "v_pk_fma_f32 v[228:229], v[8:9], v[8:9], v[228:229]\n"
    "v_pk_fma_f32 v[230:231], v[10:11], v[10:11], v[230:231]\n"
    "v_pk_add_f32 v[224:225], v[224:225], v[12:13]\n"
    "v_pk_add_f32 v[226:227], v[226:227], v[14:15]\n"
    "v_pk_fma_f32 v[228:229], v[12:13], v[12:13], v[228:229]\n"
    "v_pk_fma_f32 v[230:231], v[14:15], v[14:15], v[230:231]\n"
    "v_mfma_f32_16x16x32_bf16 v[172:175], a[172:175], v[144:147], 0\n"
    "s_waitcnt lgkmcnt(9)\n"
    "ds_read_b128 v[220:223], v251 offset:1680\n"
    "ds_read_u16 v186, v186\n"
    "v_mfma_f32_16x16x32_bf16 v[172:175], a[220:223], v[148:151], v[172:175]\n"
    "s_waitcnt lgkmcnt(8)\n"
    "ds_read_u16_d16_hi v202, v202\n"
    "v_or_b32_e32 v198, v198, v206\n"
    "v_smfmac_f32_32x32x32_bf16 v[112:127], a[248:251], a[232:239], v241\n"
    "s_waitcnt lgkmcnt(8)\n"
    "ds_read_b128 v[236:239], v244 offset:12288\n"
    "v_lshlrev_b32_sdwa v206, 1, v181 dst_sel:DWORD dst_unused:UNUSED_PAD src0_sel:DWORD src1_sel:WORD_1\n"
    "v_or_b32_e64 v199, v199, v207\n"
    "v_lshlrev_b32_sdwa v207, 1, v183 dst_sel:DWORD dst_unused:UNUSED_PAD src0_sel:DWORD src1_sel:WORD_1\n"
    "v_pk_fma_f16 v232, v184, v184, s48\n"
    "v_mfma_f32_16x16x32_bf16 v[172:175], a[240:243], v[152:155], v[172:175]\n"
    "ds_read_u16 v187, v187\n"
    "v_pk_mul_f16 v232, v184, v232\n"
    "v_mfma_f32_16x16x32_bf16 v[176:179], a[168:171], v[156:159], 0\n"
    "ds_read_u16_d16_hi v203, v203\n"
    "v_exp_f16_e32 v234, v232\n"
    "v_mfma_f32_16x16x32_bf16 v[176:179], a[216:219], v[160:163], v[176:179]\n"
    "v_exp_f16_sdwa v234, v232 dst_sel:WORD_1 dst_unused:UNUSED_PRESERVE src0_sel:WORD_1\n"
    "v_mfma_f32_16x16x32_bf16 v[176:179], a[124:127], v[164:167], v[176:179]\n"
    "s_waitcnt lgkmcnt(8)\n"
    "ds_read_b128 a[244:247], v244 offset:14336\n"
    "ds_read_u16 v189, v189\n"
    "v_mfma_f32_16x16x32_bf16 v[180:183], a[172:175], v[156:159], 0\n"
    "v_or_b32_e64 v185, v185, v201\n"
    "v_pk_fma_f16 v234, v234, s49, s49\n"
    "s_waitcnt lgkmcnt(8)\n"
    "v_smfmac_f32_16x16x64_bf16 v[16:19], v[216:219], v[208:215], v240\n"
    "s_waitcnt lgkmcnt(7)\n"
    "ds_read_b128 v[216:219], v252 offset:1344\n"
    "v_smfmac_f32_16x16x64_bf16 v[20:23], v[220:223], v[208:215], v240 abid:1\n"
    "ds_read_b128 v[208:211], v250 offset:800\n"
    "v_mfma_f32_16x16x32_bf16 v[180:183], a[220:223], v[160:163], v[180:183]\n"
    "ds_read_b128 v[212:215], v250 offset:1120\n"
    "v_lshlrev_b32_e64 v254, 3, v244\n"
    "v_mfma_f32_16x16x32_bf16 v[180:183], a[240:243], v[164:167], v[180:183]\n"
    "ds_read_b128 v[220:223], v252 offset:1680\n"
    "v_lshlrev_b32_sdwa v201, 1, v171 dst_sel:DWORD dst_unused:UNUSED_PAD src0_sel:DWORD src1_sel:WORD_1\n"
    "v_mfma_f32_16x16x32_bf16 v[96:99], a[48:51], v[192:195], v[96:99]\n"
    "ds_read_b128 a[232:235], v243 offset:28736\n"
    "v_rcp_f16_e32 v232, v234\n"
    "v_mfma_f32_16x16x32_bf16 v[100:103], a[52:55], v[192:195], v[100:103]\n"
    "s_waitcnt lgkmcnt(9)\n"
    "ds_read_u16_d16_hi v205, v205\n"
    "buffer_load_dwordx4 v[88:91], v247, s[40:43], s61 offen offset:128\n"
    "v_mfma_f32_16x16x32_bf16 v[112:115], a[56:59], v[192:195], v[112:115]\n"
    "v_or_b32_e64 v186, v186, v202\n"
    "v_lshlrev_b32_sdwa v202, 1, v173 dst_sel:DWORD dst_unused:UNUSED_PAD src0_sel:DWORD src1_sel:WORD_1\n"
    "v_mfma_f32_16x16x32_bf16 v[116:119], a[60:63], v[192:195], v[116:119]\n"
    "ds_read_b128 a[236:239], v243 offset:29760\n"
    "ds_read_u16 v190, v190\n"
    "v_mfma_f32_16x16x32_bf16 v[128:131], v[236:239], v[192:195], v[128:131]\n"
    "s_nop 0\n"
    "v_rcp_f16_sdwa v232, v234 dst_sel:WORD_1 dst_unused:UNUSED_PRESERVE src0_sel:WORD_1\n"
    "v_mfma_f32_16x16x32_bf16 v[104:107], a[48:51], v[196:199], v[104:107]\n"
    "s_waitcnt lgkmcnt(9)\n"
    "ds_read_b128 v[144:147], v242 offset:0\n"
    "v_or_b32_e64 v187, v187, v203\n"
    "v_mfma_f32_16x16x32_bf16 v[132:135], a[244:247], v[192:195], v[132:135]\n"
    "ds_read_u16_d16_hi v206, v206\n"
    "v_cvt_pk_f16_f32 v192, v168, v169\n"
    "v_mfma_f32_16x16x32_bf16 v[108:111], a[52:55], v[196:199], v[108:111]\n"
    "v_lshlrev_b32_sdwa v193, 1, v170 dst_sel:DWORD dst_unused:UNUSED_PAD src0_sel:DWORD src1_sel:WORD_1\n"
    "v_lshlrev_b32_sdwa v194, 1, v172 dst_sel:DWORD dst_unused:UNUSED_PAD src0_sel:DWORD src1_sel:WORD_1\n"
    "v_mfma_f32_16x16x32_bf16 v[120:123], a[56:59], v[196:199], v[120:123]\n"
    "ds_read_b128 v[148:151], v242 offset:1024\n"
    "v_lshlrev_b32_sdwa v195, 1, v174 dst_sel:DWORD dst_unused:UNUSED_PAD src0_sel:DWORD src1_sel:WORD_1\n"
    "v_mfma_f32_16x16x32_bf16 v[124:127], a[60:63], v[196:199], v[124:127]\n"
    "ds_read_u16 v191, v191\n"
    "v_lshlrev_b32_sdwa v203, 1, v175 dst_sel:DWORD dst_unused:UNUSED_PAD src0_sel:DWORD src1_sel:WORD_1\n"
    "s_waitcnt lgkmcnt(9)\n"
    "v_smfmac_f32_16x16x64_bf16 v[16:19], v[216:219], v[208:215], v240\n"
    "s_waitcnt lgkmcnt(8)\n"
    "ds_read_b128 v[216:219], v251 offset:2016\n"
    "s_mov_b32 exec_hi, 0\n"
    "buffer_load_dword v233, v254, s[72:75], s53 offen sc1\n"
    "s_mov_b32 exec_hi, -1\n"
    "v_smfmac_f32_16x16x64_bf16 v[20:23], v[220:223], v[208:215], v240 abid:1\n"
    "ds_read_b128 v[208:211], v250 offset:240\n"
    "ds_read_b128 v[212:215], v250 offset:560\n"
    "v_fma_mix_f32 v234, v184, v232, 0 op_sel_hi:[1,1,0]\n"
    "v_fma_mix_f32 v232, v184, v232, 0 op_sel:[1,1,0] op_sel_hi:[1,1,0]\n"
    "v_pk_add_f32 v[224:225], v[224:225], v[16:17]\n"
    "v_pk_add_f32 v[226:227], v[226:227], v[18:19]\n"
    "v_pk_fma_f32 v[228:229], v[16:17], v[16:17], v[228:229]\n"
    "v_pk_fma_f32 v[230:231], v[18:19], v[18:19], v[230:231]\n"
    "v_pk_add_f32 v[224:225], v[224:225], v[20:21]\n"
    "v_pk_add_f32 v[226:227], v[226:227], v[22:23]\n"
    "v_pk_fma_f32 v[228:229], v[20:21], v[20:21], v[228:229]\n"
    "v_pk_fma_f32 v[230:231], v[22:23], v[22:23], v[230:231]\n"
    "v_mfma_f32_16x16x32_bf16 v[136:139], v[236:239], v[196:199], v[136:139]\n"
    "s_waitcnt lgkmcnt(8)\n"
    "ds_read_b128 v[220:223], v251 offset:2352\n"
    "ds_read_b128 v[152:155], v242 offset:2048\n"
    "v_mfma_f32_16x16x32_bf16 v[140:143], a[244:247], v[196:199], v[140:143]\n"
    "s_nop 0\n"
    "ds_read_u16_d16_hi v207, v207\n"
    "v_or_b32_e32 v189, v189, v205\n"
    "s_waitcnt lgkmcnt(9)\n"
    "v_mfma_f32_16x16x32_bf16 v[168:171], a[128:131], v[144:147], 0\n"
    "ds_read_b128 v[156:159], v242 offset:3072\n"
    "ds_read_b128 v[160:163], v242 offset:4096\n"
    "v_smfmac_f32_32x32x32_bf16 v[128:143], a[248:251], a[232:239], v241\n"
    "ds_read_b128 v[164:167], v242 offset:5120\n"
    "v_cvt_pk_f16_f32 v196, v176, v177\n"
    "v_lshlrev_b32_sdwa v197, 1, v178 dst_sel:DWORD dst_unused:UNUSED_PAD src0_sel:DWORD src1_sel:WORD_1\n"
    "s_waitcnt lgkmcnt(10)\n"
    "v_or_b32_e32 v190, v190, v206\n"
    "v_mfma_f32_16x16x32_bf16 v[168:171], a[176:179], v[148:151], v[168:171]\n"
    "ds_read_b128 a[240:243], v244 offset:32768\n"
    "ds_read_u16 v193, v193\n"
    "v_mfma_f32_16x16x32_bf16 v[172:175], a[132:135], v[144:147], 0\n"
    "v_lshlrev_b32_sdwa v205, 1, v179 dst_sel:DWORD dst_unused:UNUSED_PAD src0_sel:DWORD src1_sel:WORD_1\n"
    "v_lshlrev_b32_sdwa v198, 1, v180 dst_sel:DWORD dst_unused:UNUSED_PAD src0_sel:DWORD src1_sel:WORD_1\n"
    "v_mfma_f32_16x16x32_bf16 v[172:175], a[180:183], v[148:151], v[172:175]\n"
    "ds_read_u16_d16_hi v201, v201\n"
    "v_lshlrev_b32_sdwa v206, 1, v181 dst_sel:DWORD dst_unused:UNUSED_PAD src0_sel:DWORD src1_sel:WORD_1\n"
    "v_lshlrev_b32_sdwa v199, 1, v182 dst_sel:DWORD dst_unused:UNUSED_PAD src0_sel:DWORD src1_sel:WORD_1\n"
    "v_cvt_pk_bf16_f32 v184, v234, v232\n"
    "v_pk_fma_f16 v232, v188, v188, s48\n"
    "v_mov_b32_e64 v254, 0\n"
    "s_waitcnt lgkmcnt(9)\n"
    "v_smfmac_f32_16x16x64_bf16 v[24:27], v[216:219], v[208:215], v240\n"
    "s_waitcnt lgkmcnt(8)\n"
    "ds_read_b128 v[216:219], v252 offset:2016\n"
    "ds_read_u16 v194, v194\n"
    "v_smfmac_f32_16x16x64_bf16 v[28:31], v[220:223], v[208:215], v240 abid:1\n"
    "s_waitcnt lgkmcnt(9)\n"
    "v_pk_mul_f16 v232, v188, v232\n"
    "v_mfma_f32_16x16x32_bf16 v[168:171], a[96:99], v[152:155], v[168:171]\n"
    "s_nop 0\n"
    "ds_read_b128 v[208:211], v250 offset:880\n"
    "v_exp_f16_e64 v234, v232\n"
    "ds_read_b128 v[212:215], v250 offset:1200\n"
    "v_exp_f16_sdwa v234, v232 dst_sel:WORD_1 dst_unused:UNUSED_PRESERVE src0_sel:WORD_1\n"
    "s_waitcnt lgkmcnt(9)\n"
    "v_mfma_f32_16x16x32_bf16 v[176:179], a[128:131], v[156:159], 0\n"
    "s_waitcnt lgkmcnt(8)\n"
    "ds_read_b128 v[220:223], v252 offset:2352\n"
    "ds_read_u16_d16_hi v202, v202\n"
    "v_mfma_f32_16x16x32_bf16 v[176:179], a[176:179], v[160:163], v[176:179]\n"
    "s_waitcnt lgkmcnt(9)\n"
    "v_or_b32_e64 v191, v191, v207\n"
    "v_lshlrev_b32_sdwa v207, 1, v183 dst_sel:DWORD dst_unused:UNUSED_PAD src0_sel:DWORD src1_sel:WORD_1\n"
    "v_mfma_f32_16x16x32_bf16 v[176:179], a[96:99], v[164:167], v[176:179]\n"
    "s_waitcnt lgkmcnt(8)\n"
    "ds_read_b128 v[236:239], v244 offset:16384\n"
    "v_pk_fma_f16 v234, v234, s49, s49\n"
    "v_mfma_f32_16x16x32_bf16 v[172:175], a[240:243], v[152:155], v[172:175]\n"
    "ds_read_u16 v195, v195\n"
    "v_rcp_f16_e64 v232, v234\n"
    "v_mfma_f32_16x16x32_bf16 v[180:183], a[132:135], v[156:159], 0\n"
    "v_rcp_f16_sdwa v232, v234 dst_sel:WORD_1 dst_unused:UNUSED_PRESERVE src0_sel:WORD_1\n"
    "v_mfma_f32_16x16x32_bf16 v[180:183], a[180:183], v[160:163], v[180:183]\n"
    "ds_read_u16_d16_hi v203, v203\n"
    "v_fma_mix_f32 v234, v188, v232, 0 op_sel_hi:[1,1,0]\n"
    "v_mfma_f32_16x16x32_bf16 v[180:183], a[240:243], v[164:167], v[180:183]\n"
    "v_fma_mix_f32 v232, v188, v232, 0 op_sel:[1,1,0] op_sel_hi:[1,1,0]\n"
    "v_lshlrev_b32_sdwa v200, 1, v169 dst_sel:DWORD dst_unused:UNUSED_PAD src0_sel:DWORD src1_sel:WORD_1\n"
    "v_mfma_f32_16x16x32_bf16 v[96:99], a[64:67], v[184:187], v[96:99]\n"
    "s_waitcnt lgkmcnt(8)\n"
    "ds_read_b128 a[244:247], v244 offset:18432\n"
    "ds_read_u16 v197, v197\n"
    "v_mfma_f32_16x16x32_bf16 v[100:103], a[68:71], v[184:187], v[100:103]\n"
    "v_or_b32_e32 v193, v193, v201\n"
    "v_cvt_pk_bf16_f32 v188, v234, v232\n"
    "v_mfma_f32_16x16x32_bf16 v[112:115], a[72:75], v[184:187], v[112:115]\n"
    "ds_read_u16_d16_hi v205, v205\n"
    "v_pk_fma_f16 v232, v192, v192, s48\n"
    "v_mfma_f32_16x16x32_bf16 v[116:119], a[76:79], v[184:187], v[116:119]\n"
    "s_waitcnt lgkmcnt(8)\n"
    "s_barrier\n"
    "v_smfmac_f32_16x16x64_bf16 v[24:27], v[216:219], v[208:215], v240\n"
    "s_waitcnt lgkmcnt(7)\n"
    "ds_read_u16 v198, v198\n"
    "ds_write2_b32 v248, v32, v33 offset0:0 offset1:20\n"
    "v_smfmac_f32_16x16x64_bf16 v[28:31], v[220:223], v[208:215], v240 abid:1\n"
    "s_waitcnt lgkmcnt(7)\n"
    "v_or_b32_e64 v194, v194, v202\n"
    "v_mfma_f32_16x16x32_bf16 v[128:131], v[236:239], v[184:187], v[128:131]\n"
    "ds_read_u16_d16_hi v206, v206\n"
    "ds_write2_b32 v248, v34, v35 offset0:40 offset1:60\n"
    "v_add_u32_e32 v248, 1360, v248\n"
    "v_pk_add_f32 v[224:225], v[224:225], v[24:25]\n"
    "v_pk_add_f32 v[226:227], v[226:227], v[26:27]\n"
    "v_pk_fma_f32 v[228:229], v[24:25], v[24:25], v[228:229]\n"
    "v_pk_fma_f32 v[230:231], v[26:27], v[26:27], v[230:231]\n"
    "v_pk_add_f32 v[224:225], v[224:225], v[28:29]\n"
    "v_pk_add_f32 v[226:227], v[226:227], v[30:31]\n"
    "v_pk_fma_f32 v[228:229], v[28:29], v[28:29], v[228:229]\n"
    "v_pk_fma_f32 v[230:231], v[30:31], v[30:31], v[230:231]\n"
    "s_waitcnt lgkmcnt(6)\n"
    "v_mfma_f32_16x16x32_bf16 v[132:135], a[244:247], v[184:187], v[132:135]\n"
    "s_nop 0\n"
    "ds_read_u16 v199, v199\n"
    "ds_write2_b32 v235, v52, v53 offset0:0 offset1:20\n"
    "v_mfma_f32_16x16x32_bf16 v[104:107], a[64:67], v[188:191], v[104:107]\n"
    "v_or_b32_e64 v195, v195, v203\n"
    "v_pk_mul_f16 v232, v192, v232\n"
    "v_mfma_f32_16x16x32_bf16 v[108:111], a[68:71], v[188:191], v[108:111]\n"
    "ds_read_u16_d16_hi v207, v207\n"
    "ds_write2_b32 v235, v54, v55 offset0:40 offset1:60\n"
    "v_mfma_f32_16x16x32_bf16 v[120:123], a[72:75], v[188:191], v[120:123]\n"
    "s_waitcnt lgkmcnt(8)\n"
    "v_add_u32_e32 v235, 1360, v235\n"
    "v_exp_f16_e32 v234, v232\n"
    "v_mfma_f32_16x16x32_bf16 v[124:127], a[76:79], v[188:191], v[124:127]\n"
    "ds_read_b128 a[240:243], v244 offset:36864\n"
    "ds_write2_b32 v248, v36, v37 offset0:0 offset1:20\n"
    "v_mfma_f32_16x16x32_bf16 v[136:139], v[236:239], v[188:191], v[136:139]\n"
    "v_or_b32_e64 v197, v197, v205\n"
    "v_exp_f16_sdwa v234, v232 dst_sel:WORD_1 dst_unused:UNUSED_PRESERVE src0_sel:WORD_1\n"
    "v_mfma_f32_16x16x32_bf16 v[140:143], a[244:247], v[188:191], v[140:143]\n"
    "ds_read_u16_d16_hi v200, v200\n"
    "ds_write2_b32 v248, v38, v39 offset0:40 offset1:60\n"
    "v_add_u32_e32 v248, 1360, v248\n"
    "v_pk_fma_f16 v234, v234, s49, s49\n"
    "v_lshlrev_b32_sdwa v184, 1, v168 dst_sel:DWORD dst_unused:UNUSED_PAD src0_sel:DWORD src1_sel:WORD_1\n"
    "v_lshlrev_b32_sdwa v185, 1, v170 dst_sel:DWORD dst_unused:UNUSED_PAD src0_sel:DWORD src1_sel:WORD_1\n"
    "v_lshlrev_b32_sdwa v201, 1, v171 dst_sel:DWORD dst_unused:UNUSED_PAD src0_sel:DWORD src1_sel:WORD_1\n"
    "v_mfma_f32_16x16x32_bf16 v[168:171], a[136:139], v[144:147], 0\n"
    "s_nop 0\n"
    "s_waitcnt lgkmcnt(8)\n"
    "ds_read_u16 v184, v184\n"
    "ds_write2_b32 v235, v56, v57 offset0:0 offset1:20\n"
    "v_mfma_f32_16x16x32_bf16 v[168:171], a[184:187], v[148:151], v[168:171]\n"
    "s_add_u32 s60, s60, 1572864\n"
    "v_lshlrev_b32_sdwa v186, 1, v172 dst_sel:DWORD dst_unused:UNUSED_PAD src0_sel:DWORD src1_sel:WORD_1\n"
    "v_lshlrev_b32_sdwa v202, 1, v173 dst_sel:DWORD dst_unused:UNUSED_PAD src0_sel:DWORD src1_sel:WORD_1\n"
    "v_lshlrev_b32_sdwa v187, 1, v174 dst_sel:DWORD dst_unused:UNUSED_PAD src0_sel:DWORD src1_sel:WORD_1\n"
    "ds_write2_b32 v235, v58, v59 offset0:40 offset1:60\n"
    "v_add_u32_e32 v235, -1360, v235\n"
    "v_lshlrev_b32_sdwa v203, 1, v175 dst_sel:DWORD dst_unused:UNUSED_PAD src0_sel:DWORD src1_sel:WORD_1\n"
    "ds_write2_b32 v248, v40, v41 offset0:0 offset1:20\n"
    "v_rcp_f16_e32 v232, v234\n"
    "v_mfma_f32_16x16x32_bf16 v[168:171], a[100:103], v[152:155], v[168:171]\n"
    "s_waitcnt lgkmcnt(6)\n"
    "ds_read_u16 v185, v185\n"
    "ds_write2_b32 v248, v42, v43 offset0:40 offset1:60\n"
    "v_add_u32_e32 v248, 1360, v248\n"
    "v_or_b32_e64 v198, v198, v206\n"
    "v_rcp_f16_sdwa v232, v234 dst_sel:WORD_1 dst_unused:UNUSED_PRESERVE src0_sel:WORD_1\n"
    "v_mfma_f32_16x16x32_bf16 v[172:175], a[140:143], v[144:147], 0\n"
    "s_add_u32 s61, s61, 1572864\n"
    "ds_read_u16_d16_hi v201, v201\n"
    "ds_write2_b32 v248, v44, v45 offset0:0 offset1:20\n"
    "v_lshlrev_b32_sdwa v188, 1, v176 dst_sel:DWORD dst_unused:UNUSED_PAD src0_sel:DWORD src1_sel:WORD_1\n"
    "v_lshlrev_b32_sdwa v204, 1, v177 dst_sel:DWORD dst_unused:UNUSED_PAD src0_sel:DWORD src1_sel:WORD_1\n"
    "v_lshlrev_b32_sdwa v189, 1, v178 dst_sel:DWORD dst_unused:UNUSED_PAD src0_sel:DWORD src1_sel:WORD_1\n"
    "v_lshlrev_b32_sdwa v205, 1, v179 dst_sel:DWORD dst_unused:UNUSED_PAD src0_sel:DWORD src1_sel:WORD_1\n"
    "v_lshlrev_b32_sdwa v190, 1, v180 dst_sel:DWORD dst_unused:UNUSED_PAD src0_sel:DWORD src1_sel:WORD_1\n"
    "v_lshlrev_b32_sdwa v206, 1, v181 dst_sel:DWORD dst_unused:UNUSED_PAD src0_sel:DWORD src1_sel:WORD_1\n"
    "v_lshlrev_b32_sdwa v191, 1, v182 dst_sel:DWORD dst_unused:UNUSED_PAD src0_sel:DWORD src1_sel:WORD_1\n"
    "v_fma_mix_f32 v234, v192, v232, 0 op_sel_hi:[1,1,0]\n"
    "v_mfma_f32_16x16x32_bf16 v[172:175], a[188:191], v[148:151], v[172:175]\n"
    "s_waitcnt lgkmcnt(4)\n"
    "ds_read_u16 v186, v186\n"
    "ds_write2_b32 v248, v46, v47 offset0:40 offset1:60\n"
    "v_mfma_f32_16x16x32_bf16 v[172:175], a[240:243], v[152:155], v[172:175]\n"
    "s_add_u32 s62, s62, 1572864\n"
    "v_add_u32_e32 v248, 1360, v248\n"
    "v_or_b32_e32 v199, v199, v207\n"
    "v_lshlrev_b32_sdwa v207, 1, v183 dst_sel:DWORD dst_unused:UNUSED_PAD src0_sel:DWORD src1_sel:WORD_1\n"
    "v_fma_mix_f32 v232, v192, v232, 0 op_sel:[1,1,0] op_sel_hi:[1,1,0]\n"
    "v_mfma_f32_16x16x32_bf16 v[176:179], a[136:139], v[156:159], 0\n"
    "ds_read_b128 v[236:239], v244 offset:20480\n"
    "ds_write2_b32 v248, v48, v49 offset0:0 offset1:20\n"
    "v_cvt_pk_bf16_f32 v192, v234, v232\n"
    "v_mfma_f32_16x16x32_bf16 v[176:179], a[184:187], v[160:163], v[176:179]\n"
    "v_pk_fma_f16 v232, v196, v196, s48\n"
    "v_or_b32_e64 v184, v184, v200\n"
    "v_mfma_f32_16x16x32_bf16 v[176:179], a[100:103], v[164:167], v[176:179]\n"
    "s_add_u32 s63, s63, 1572864\n"
    "ds_read_u16_d16_hi v202, v202\n"
    "ds_write2_b32 v248, v50, v51 offset0:40 offset1:60\n"
    "v_mfma_f32_32x32x1_2b_f32 v[32:63], v254, v254, 0\n"
    "s_waitcnt lgkmcnt(6)\n"
    "v_add_u32_e32 v248, -5440, v248\n"
    "v_pk_mul_f16 v232, v196, v232\n"
    "v_or_b32_e32 v185, v185, v201\n"
    "v_exp_f16_e64 v234, v232\n"
    "v_lshlrev_b32_sdwa v201, 1, v171 dst_sel:DWORD dst_unused:UNUSED_PAD src0_sel:DWORD src1_sel:WORD_1\n"
    "v_exp_f16_sdwa v234, v232 dst_sel:WORD_1 dst_unused:UNUSED_PRESERVE src0_sel:WORD_1\n"
    "v_mov_b32_e64 v254, 0\n"
    "v_pk_fma_f16 v234, v234, s49, s49\n"
    "v_rcp_f16_e64 v232, v234\n"
    "ds_read_b128 v[216:219], v251 offset:10752\n"
    "ds_read_b128 v[220:223], v251 offset:11088\n"
    "v_mfma_f32_16x16x32_bf16 v[180:183], a[140:143], v[156:159], 0\n"
    "s_add_u32 s52, s52, 1572864\n"
    "s_add_u32 s53, s53, 393216\n"
    "ds_read_u16 v187, v187\n"
    "ds_read_b128 a[244:247], v244 offset:22528\n"
    "s_waitcnt vmcnt(3)\n"
    "v_smfmac_f32_32x32x32_bf16 v[128:143], a[248:251], a[224:231], v241\n"
    "s_nop 0\n"
    "ds_read_u16_d16_hi v203, v203\n"
    "ds_read_u16 v188, v188\n"
    "v_rcp_f16_sdwa v232, v234 dst_sel:WORD_1 dst_unused:UNUSED_PRESERVE src0_sel:WORD_1\n"
    "ds_read_u16_d16_hi v204, v204\n"
    "v_mfma_f32_16x16x32_bf16 v[180:183], a[188:191], v[160:163], v[180:183]\n"
    "s_waitcnt lgkmcnt(7)\n"
    "v_fma_mix_f32 v234, v196, v232, 0 op_sel_hi:[1,1,0]\n"
    "v_fma_mix_f32 v232, v196, v232, 0 op_sel:[1,1,0] op_sel_hi:[1,1,0]\n"
    "v_mfma_f32_16x16x32_bf16 v[180:183], a[240:243], v[164:167], v[180:183]\n"
    "s_barrier\n"
    "v_mfma_f32_16x16x32_bf16 v[96:99], a[80:83], v[192:195], v[96:99]\n"
    "ds_read_b128 v[208:211], v250 offset:0\n"
    "ds_read_b128 v[212:215], v250 offset:320\n"
    "v_mfma_f32_16x16x32_bf16 v[100:103], a[84:87], v[192:195], v[100:103]\n"
    "ds_read_u16 v189, v189\n"
    "ds_read_u16_d16_hi v205, v205\n"
    "v_mfma_f32_16x16x32_bf16 v[112:115], a[88:91], v[192:195], v[112:115]\n"
    "v_cvt_pk_bf16_f32 v196, v234, v232\n"
    "v_or_b32_e32 v186, v186, v202\n"
    "v_mfma_f32_16x16x32_bf16 v[116:119], a[92:95], v[192:195], v[116:119]\n"
    "ds_read_u16 v190, v190\n"
    "v_lshlrev_b32_sdwa v202, 1, v173 dst_sel:DWORD dst_unused:UNUSED_PAD src0_sel:DWORD src1_sel:WORD_1\n"
    "v_mfma_f32_16x16x32_bf16 v[128:131], v[236:239], v[192:195], v[128:131]\n"
    "s_waitcnt lgkmcnt(8)\n"
    "v_cvt_pk_bf16_f32 v96, v96, v97\n"
    "v_cvt_pk_bf16_f32 v97, v98, v99\n"
    "v_mfma_f32_16x16x32_bf16 v[132:135], a[244:247], v[192:195], v[132:135]\n"
    "ds_read_u16_d16_hi v206, v206\n"
    "v_cvt_pk_f16_f32 v192, v168, v169\n"
    "v_mfma_f32_16x16x32_bf16 v[104:107], a[80:83], v[196:199], v[104:107]\n"
    "v_lshlrev_b32_sdwa v193, 1, v170 dst_sel:DWORD dst_unused:UNUSED_PAD src0_sel:DWORD src1_sel:WORD_1\n"
    "v_lshlrev_b32_sdwa v194, 1, v172 dst_sel:DWORD dst_unused:UNUSED_PAD src0_sel:DWORD src1_sel:WORD_1\n"
    "v_mfma_f32_16x16x32_bf16 v[108:111], a[84:87], v[196:199], v[108:111]\n"
    "s_waitcnt lgkmcnt(8)\n"
    "ds_read_u16 v191, v191\n"
    "v_lshlrev_b32_sdwa v195, 1, v174 dst_sel:DWORD dst_unused:UNUSED_PAD src0_sel:DWORD src1_sel:WORD_1\n"
    "v_mfma_f32_16x16x32_bf16 v[120:123], a[88:91], v[196:199], v[120:123]\n"
    "v_or_b32_e32 v187, v187, v203\n"
    "v_lshlrev_b32_sdwa v203, 1, v175 dst_sel:DWORD dst_unused:UNUSED_PAD src0_sel:DWORD src1_sel:WORD_1\n"
    "v_mfma_f32_16x16x32_bf16 v[124:127], a[92:95], v[196:199], v[124:127]\n"
    "ds_read_u16_d16_hi v207, v207\n"
    "v_cvt_pk_bf16_f32 v98, v100, v101\n"
    "v_mfma_f32_16x16x32_bf16 v[136:139], v[236:239], v[196:199], v[136:139]\n"
    "s_waitcnt lgkmcnt(7)\n"
    "ds_read_u16 v193, v193\n"
    "v_or_b32_e32 v188, v188, v204\n"
    "s_waitcnt lgkmcnt(7)\n"
    "v_smfmac_f32_16x16x64_bf16 v[32:35], v[216:219], v[208:215], v240\n"
    "s_nop 0\n"
    "v_cvt_pk_bf16_f32 v99, v102, v103\n"
    "v_smfmac_f32_16x16x64_bf16 v[36:39], v[220:223], v[208:215], v240 abid:1\n"
    "ds_read_b128 v[208:211], v250 offset:640\n"
    "ds_read_b128 v[212:215], v250 offset:960\n"
    "v_mfma_f32_16x16x32_bf16 v[140:143], a[244:247], v[196:199], v[140:143]\n"
    "v_cvt_pk_f16_f32 v196, v176, v177\n"
    "v_lshlrev_b32_sdwa v197, 1, v178 dst_sel:DWORD dst_unused:UNUSED_PAD src0_sel:DWORD src1_sel:WORD_1\n"
    "v_mfma_f32_16x16x32_bf16 v[168:171], a[144:147], v[144:147], 0\n"
    "s_nop 0\n"
    "ds_read_b128 v[216:219], v252 offset:10752\n"
    "ds_read_b128 v[220:223], v252 offset:11088\n"
    "v_mfma_f32_16x16x32_bf16 v[168:171], a[192:195], v[148:151], v[168:171]\n"
    "s_waitcnt lgkmcnt(9)\n"
    "v_lshlrev_b32_sdwa v198, 1, v180 dst_sel:DWORD dst_unused:UNUSED_PAD src0_sel:DWORD src1_sel:WORD_1\n"
    "v_lshlrev_b32_sdwa v199, 1, v182 dst_sel:DWORD dst_unused:UNUSED_PAD src0_sel:DWORD src1_sel:WORD_1\n"
    "v_mfma_f32_16x16x32_bf16 v[168:171], a[104:107], v[152:155], v[168:171]\n"
    "ds_read_u16_d16_hi v201, v201\n"
    "v_or_b32_e64 v189, v189, v205\n"
    "v_mfma_f32_16x16x32_bf16 v[172:175], a[148:151], v[144:147], 0\n"
    "ds_read_u16 v194, v194\n"
    "v_lshlrev_b32_sdwa v205, 1, v179 dst_sel:DWORD dst_unused:UNUSED_PAD src0_sel:DWORD src1_sel:WORD_1\n"
    "v_mfma_f32_16x16x32_bf16 v[172:175], a[196:199], v[148:151], v[172:175]\n"
    "v_cvt_pk_bf16_f32 v112, v112, v113\n"
    "s_waitcnt lgkmcnt(9)\n"
    "v_or_b32_e32 v190, v190, v206\n"
    "v_mfma_f32_16x16x32_bf16 v[172:175], a[108:111], v[152:155], v[172:175]\n"
    "ds_read_u16_d16_hi v202, v202\n"
    "v_lshlrev_b32_sdwa v206, 1, v181 dst_sel:DWORD dst_unused:UNUSED_PAD src0_sel:DWORD src1_sel:WORD_1\n"
    "v_mfma_f32_16x16x32_bf16 v[176:179], a[144:147], v[156:159], 0\n"
    "v_cvt_pk_bf16_f32 v113, v114, v115\n"
    "v_cvt_pk_bf16_f32 v114, v116, v117\n"
    "v_mfma_f32_16x16x32_bf16 v[176:179], a[192:195], v[160:163], v[176:179]\n"
    "s_nop 0\n"
    "ds_read_b128 v[236:239], v244 offset:0\n"
    "ds_write_b128 v243, v[96:99] offset:24640\n"
    "v_mfma_f32_16x16x32_bf16 v[176:179], a[104:107], v[164:167], v[176:179]\n"
    "s_waitcnt lgkmcnt(10)\n"
    "v_cvt_pk_bf16_f32 v115, v118, v119\n"
    "v_cvt_pk_bf16_f32 v128, v128, v129\n"
    "v_mfma_f32_16x16x32_bf16 v[96:99], a[0:3], v[184:187], 0\n"
    "v_cvt_pk_bf16_f32 v129, v130, v131\n"
    "v_or_b32_e32 v191, v191, v207\n"
    "v_mfma_f32_16x16x32_bf16 v[100:103], a[4:7], v[184:187], 0\n"
    "s_waitcnt lgkmcnt(6)\n"
    "ds_read_b128 a[252:255], v249 offset:0\n"
    "ds_write_b128 v243, v[112:115] offset:26688\n"
    "v_smfmac_f32_16x16x64_bf16 v[32:35], v[216:219], v[208:215], v240\n"
    "s_waitcnt lgkmcnt(7)\n"
    "v_lshlrev_b32_sdwa v207, 1, v183 dst_sel:DWORD dst_unused:UNUSED_PAD src0_sel:DWORD src1_sel:WORD_1\n"
    "v_smfmac_f32_16x16x64_bf16 v[36:39], v[220:223], v[208:215], v240 abid:1\n"
    "v_cvt_pk_bf16_f32 v130, v132, v133\n"
    "v_mfma_f32_16x16x32_bf16 v[180:183], a[148:151], v[156:159], 0\n"
    "s_waitcnt lgkmcnt(6)\n"
    "ds_read_b128 v[208:211], v250 offset:80\n"
    "ds_read_b128 v[212:215], v250 offset:400\n"
    "ds_read_b128 v[216:219], v251 offset:11424\n"
    "v_or_b32_e64 v193, v193, v201\n"
    "ds_read_b128 v[220:223], v251 offset:11760\n"
    "ds_read_u16 v195, v195\n"
    "v_pk_add_f32 v[224:225], v[224:225], v[32:33]\n"
    "v_pk_add_f32 v[226:227], v[226:227], v[34:35]\n"
    "v_pk_fma_f32 v[228:229], v[32:33], v[32:33], v[228:229]\n"
    "v_pk_fma_f32 v[230:231], v[34:35], v[34:35], v[230:231]\n"
    "v_pk_add_f32 v[224:225], v[224:225], v[36:37]\n"
    "v_pk_add_f32 v[226:227], v[226:227], v[38:39]\n"
    "v_pk_fma_f32 v[228:229], v[36:37], v[36:37], v[228:229]\n"
    "v_pk_fma_f32 v[230:231], v[38:39], v[38:39], v[230:231]\n"
    "v_mfma_f32_16x16x32_bf16 v[180:183], a[196:199], v[160:163], v[180:183]\n"
    "s_waitcnt lgkmcnt(7)\n"
    "ds_read_u16_d16_hi v203, v203\n"
    "ds_read_b128 a[244:247], v244 offset:2048\n"
    "v_mfma_f32_16x16x32_bf16 v[180:183], a[108:111], v[164:167], v[180:183]\n"
    "v_cvt_pk_bf16_f32 v131, v134, v135\n"
    "v_cvt_pk_bf16_f32 v104, v104, v105\n"
    "v_mfma_f32_16x16x32_bf16 v[112:115], a[8:11], v[184:187], 0\n"
    "s_waitcnt lgkmcnt(8)\n"
    "ds_read_u16 v197, v197\n"
    "buffer_store_dwordx4 a[252:255], v255, s[44:47], s57 offen offset:0 sc1\n"
    "v_mfma_f32_16x16x32_bf16 v[116:119], a[12:15], v[184:187], 0\n"
    "ds_read_u16_d16_hi v205, v205\n"
    "v_cvt_pk_bf16_f32 v105, v106, v107\n"
    "v_cvt_pk_bf16_f32 v106, v108, v109\n"
    "ds_write_b128 v243, v[128:131] offset:28736\n"
    "v_mfma_f32_16x16x32_bf16 v[128:131], v[236:239], v[184:187], 0\n"
    "s_waitcnt lgkmcnt(7)\n"
    "v_cvt_pk_bf16_f32 v107, v110, v111\n"
    "v_or_b32_e64 v194, v194, v202\n"
    "v_smfmac_f32_16x16x64_bf16 v[40:43], v[216:219], v[208:215], v240\n"
    "s_waitcnt lgkmcnt(6)\n"
    "ds_read_b128 v[216:219], v252 offset:11424\n"
    "ds_write_b128 v243, v[104:107] offset:25664\n"
    "v_smfmac_f32_16x16x64_bf16 v[44:47], v[220:223], v[208:215], v240 abid:1\n"
    "v_cvt_pk_bf16_f32 v120, v120, v121\n"
    "v_mfma_f32_16x16x32_bf16 v[104:107], a[0:3], v[188:191], 0\n"
    "s_nop 0\n"
    "v_cvt_pk_bf16_f32 v121, v122, v123\n"
    "v_cvt_pk_bf16_f32 v122, v124, v125\n"
    "v_mfma_f32_16x16x32_bf16 v[108:111], a[4:7], v[188:191], 0\n"
    "s_waitcnt lgkmcnt(5)\n"
    "ds_read_b128 v[208:211], v250 offset:720\n"
    "ds_read_b128 v[212:215], v250 offset:1040\n"
    "v_mfma_f32_16x16x32_bf16 v[132:135], a[244:247], v[184:187], 0\n"
    "ds_read_b128 v[220:223], v252 offset:11760\n"
    "ds_read_u16 v198, v198\n"
    "v_cvt_pk_bf16_f32 v123, v126, v127\n"
    "v_pk_fma_f16 v232, v192, v192, s48\n"
    "v_mfma_f32_16x16x32_bf16 v[124:127], a[12:15], v[188:191], 0\n"
    "ds_read_u16_d16_hi v206, v206\n"
    "v_cvt_pk_bf16_f32 v136, v136, v137\n"
    "v_cvt_pk_bf16_f32 v137, v138, v139\n"
    "v_cvt_pk_bf16_f32 v138, v140, v141\n"
    "v_or_b32_e64 v195, v195, v203\n"
    "v_cvt_pk_bf16_f32 v139, v142, v143\n"
    "ds_write_b128 v243, v[120:123] offset:27712\n"
    "v_mfma_f32_16x16x32_bf16 v[120:123], a[8:11], v[188:191], 0\n"
    "s_waitcnt lgkmcnt(8)\n"
    "v_pk_mul_f16 v232, v192, v232\n"
    "v_or_b32_e32 v197, v197, v205\n"
    "v_exp_f16_e64 v234, v232\n"
    "v_mfma_f32_16x16x32_bf16 v[140:143], a[244:247], v[188:191], 0\n"
    "ds_read_u16 v199, v199\n"
    "ds_write_b128 v243, v[136:139] offset:29760\n"
    "v_mfma_f32_16x16x32_bf16 v[136:139], v[236:239], v[188:191], 0\n"
    "v_exp_f16_sdwa v234, v232 dst_sel:WORD_1 dst_unused:UNUSED_PRESERVE src0_sel:WORD_1\n"
    "v_cvt_pk_f16_f32 v184, v168, v169\n"
    "s_waitcnt lgkmcnt(6)\n"
    "v_smfmac_f32_16x16x64_bf16 v[40:43], v[216:219], v[208:215], v240\n"
    "s_waitcnt lgkmcnt(5)\n"
    "ds_read_b128 v[216:219], v251 offset:12096\n"
    "ds_read_u16_d16_hi v207, v207\n"
    "v_smfmac_f32_16x16x64_bf16 v[44:47], v[220:223], v[208:215], v240 abid:1\n"
    "ds_read_b128 v[208:211], v250 offset:160\n"
    "v_pk_fma_f16 v234, v234, s49, s49\n"
    "v_lshlrev_b32_sdwa v185, 1, v170 dst_sel:DWORD dst_unused:UNUSED_PAD src0_sel:DWORD src1_sel:WORD_1\n"
    "ds_read_b128 v[212:215], v250 offset:480\n"
    "v_pk_add_f32 v[224:225], v[224:225], v[40:41]\n"
    "v_pk_add_f32 v[226:227], v[226:227], v[42:43]\n"
    "v_pk_fma_f32 v[228:229], v[40:41], v[40:41], v[228:229]\n"
    "v_pk_fma_f32 v[230:231], v[42:43], v[42:43], v[230:231]\n"
    "v_pk_add_f32 v[224:225], v[224:225], v[44:45]\n"
    "v_pk_add_f32 v[226:227], v[226:227], v[46:47]\n"
    "v_pk_fma_f32 v[228:229], v[44:45], v[44:45], v[228:229]\n"
    "v_pk_fma_f32 v[230:231], v[46:47], v[46:47], v[230:231]\n"
    "ds_read_b128 v[220:223], v251 offset:12432\n"
    "ds_read_b128 a[252:255], v249 offset:2048\n"
    "v_lshlrev_b32_sdwa v201, 1, v171 dst_sel:DWORD dst_unused:UNUSED_PAD src0_sel:DWORD src1_sel:WORD_1\n"
    "v_mfma_f32_16x16x32_bf16 v[168:171], a[152:155], v[144:147], 0\n"
    "s_waitcnt lgkmcnt(8)\n"
    "ds_read_u16 v185, v185\n"
    "ds_read_u16_d16_hi v201, v201\n"
    "v_mfma_f32_16x16x32_bf16 v[168:171], a[200:203], v[148:151], v[168:171]\n"
    "s_nop 0\n"
    "v_lshlrev_b32_sdwa v186, 1, v172 dst_sel:DWORD dst_unused:UNUSED_PAD src0_sel:DWORD src1_sel:WORD_1\n"
    "v_lshlrev_b32_sdwa v202, 1, v173 dst_sel:DWORD dst_unused:UNUSED_PAD src0_sel:DWORD src1_sel:WORD_1\n"
    "v_lshlrev_b32_sdwa v187, 1, v174 dst_sel:DWORD dst_unused:UNUSED_PAD src0_sel:DWORD src1_sel:WORD_1\n"
    "v_lshlrev_b32_sdwa v203, 1, v175 dst_sel:DWORD dst_unused:UNUSED_PAD src0_sel:DWORD src1_sel:WORD_1\n"
    "v_rcp_f16_e64 v232, v234\n"
    "v_mfma_f32_16x16x32_bf16 v[168:171], a[112:115], v[152:155], v[168:171]\n"
    "ds_read_u16 v186, v186\n"
    "v_or_b32_e64 v198, v198, v206\n"
    "v_rcp_f16_sdwa v232, v234 dst_sel:WORD_1 dst_unused:UNUSED_PRESERVE src0_sel:WORD_1\n"
    "v_mfma_f32_16x16x32_bf16 v[172:175], a[156:159], v[144:147], 0\n"
    "ds_read_u16_d16_hi v202, v202\n"
    "v_cvt_pk_f16_f32 v188, v176, v177\n"
    "v_lshlrev_b32_sdwa v189, 1, v178 dst_sel:DWORD dst_unused:UNUSED_PAD src0_sel:DWORD src1_sel:WORD_1\n"
    "v_lshlrev_b32_sdwa v205, 1, v179 dst_sel:DWORD dst_unused:UNUSED_PAD src0_sel:DWORD src1_sel:WORD_1\n"
    "v_lshlrev_b32_sdwa v190, 1, v180 dst_sel:DWORD dst_unused:UNUSED_PAD src0_sel:DWORD src1_sel:WORD_1\n"
    "v_lshlrev_b32_sdwa v206, 1, v181 dst_sel:DWORD dst_unused:UNUSED_PAD src0_sel:DWORD src1_sel:WORD_1\n"
    "v_lshlrev_b32_sdwa v191, 1, v182 dst_sel:DWORD dst_unused:UNUSED_PAD src0_sel:DWORD src1_sel:WORD_1\n"
    "v_fma_mix_f32 v234, v192, v232, 0 op_sel_hi:[1,1,0]\n"
    "v_mfma_f32_16x16x32_bf16 v[172:175], a[204:207], v[148:151], v[172:175]\n"
    "s_waitcnt lgkmcnt(7)\n"
    "ds_read_b128 v[236:239], v244 offset:4096\n"
    "ds_read_u16 v187, v187\n"
    "s_waitcnt lgkmcnt(8)\n"
    "v_smfmac_f32_16x16x64_bf16 v[48:51], v[216:219], v[208:215], v240\n"
    "s_waitcnt lgkmcnt(7)\n"
    "ds_read_b128 v[216:219], v252 offset:12096\n"
    "v_smfmac_f32_16x16x64_bf16 v[52:55], v[220:223], v[208:215], v240 abid:1\n"
    "s_waitcnt lgkmcnt(7)\n"
    "ds_read_b128 v[208:211], v250 offset:800\n"
    "buffer_store_dwordx4 a[252:255], v255, s[44:47], s57 offen offset:64 sc1\n"
    "v_mfma_f32_16x16x32_bf16 v[172:175], a[116:119], v[152:155], v[172:175]\n"
    "ds_read_b128 v[212:215], v250 offset:1120\n"
    "ds_read_b128 v[220:223], v252 offset:12432\n"
    "v_or_b32_e64 v199, v199, v207\n"
    "v_lshlrev_b32_sdwa v207, 1, v183 dst_sel:DWORD dst_unused:UNUSED_PAD src0_sel:DWORD src1_sel:WORD_1\n"
    "v_fma_mix_f32 v232, v192, v232, 0 op_sel:[1,1,0] op_sel_hi:[1,1,0]\n"
    "v_mfma_f32_16x16x32_bf16 v[176:179], a[152:155], v[156:159], 0\n"
    "s_nop 0\n"
    "ds_read_u16_d16_hi v203, v203\n"
    "v_cvt_pk_bf16_f32 v192, v234, v232\n"
    "v_mfma_f32_16x16x32_bf16 v[176:179], a[200:203], v[160:163], v[176:179]\n"
    "s_waitcnt lgkmcnt(9)\n"
    "ds_read_b128 a[244:247], v244 offset:6144\n"
    "v_pk_fma_f16 v232, v196, v196, s48\n"
    "v_mfma_f32_16x16x32_bf16 v[176:179], a[112:115], v[164:167], v[176:179]\n"
    "ds_read_b128 a[252:255], v249 offset:4096\n"
    "v_pk_mul_f16 v232, v196, v232\n"
    "v_mfma_f32_16x16x32_bf16 v[180:183], a[156:159], v[156:159], 0\n"
    "ds_read_u16 v189, v189\n"
    "v_exp_f16_e32 v234, v232\n"
    "v_mfma_f32_16x16x32_bf16 v[180:183], a[204:207], v[160:163], v[180:183]\n"
    "v_exp_f16_sdwa v234, v232 dst_sel:WORD_1 dst_unused:UNUSED_PRESERVE src0_sel:WORD_1\n"
    "v_mfma_f32_16x16x32_bf16 v[180:183], a[116:119], v[164:167], v[180:183]\n"
    "s_waitcnt lgkmcnt(9)\n"
    "ds_read_u16_d16_hi v205, v205\n"
    "v_pk_fma_f16 v234, v234, s49, s49\n"
    "v_mfma_f32_16x16x32_bf16 v[96:99], a[16:19], v[192:195], v[96:99]\n"
    "ds_read_u16 v190, v190\n"
    "v_rcp_f16_e64 v232, v234\n"
    "v_mfma_f32_16x16x32_bf16 v[100:103], a[20:23], v[192:195], v[100:103]\n"
    "v_rcp_f16_sdwa v232, v234 dst_sel:WORD_1 dst_unused:UNUSED_PRESERVE src0_sel:WORD_1\n"
    "v_mfma_f32_16x16x32_bf16 v[112:115], a[24:27], v[192:195], v[112:115]\n"
    "ds_read_u16_d16_hi v206, v206\n"
    "v_or_b32_e64 v185, v185, v201\n"
    "s_waitcnt lgkmcnt(8)\n"
    "v_smfmac_f32_16x16x64_bf16 v[48:51], v[216:219], v[208:215], v240\n"
    "s_waitcnt lgkmcnt(7)\n"
    "ds_read_b128 v[216:219], v251 offset:12768\n"
    "v_smfmac_f32_16x16x64_bf16 v[52:55], v[220:223], v[208:215], v240 abid:1\n"
    "ds_read_b128 v[208:211], v250 offset:240\n"
    "v_mfma_f32_16x16x32_bf16 v[116:119], a[28:31], v[192:195], v[116:119]\n"
    "ds_read_b128 v[212:215], v250 offset:560\n"
    "v_fma_mix_f32 v234, v196, v232, 0 op_sel_hi:[1,1,0]\n"
    "v_mfma_f32_16x16x32_bf16 v[128:131], v[236:239], v[192:195], v[128:131]\n"
    "s_waitcnt lgkmcnt(8)\n"
    "ds_read_b128 v[220:223], v251 offset:13104\n"
    "v_fma_mix_f32 v232, v196, v232, 0 op_sel:[1,1,0] op_sel_hi:[1,1,0]\n"
    "v_or_b32_e32 v186, v186, v202\n"
    "v_cvt_pk_bf16_f32 v196, v234, v232\n"
    "v_pk_fma_f16 v232, v184, v184, s48\n"
    "v_pk_add_f32 v[224:225], v[224:225], v[48:49]\n"
    "v_pk_add_f32 v[226:227], v[226:227], v[50:51]\n"
    "v_pk_fma_f32 v[228:229], v[48:49], v[48:49], v[228:229]\n"
    "v_pk_fma_f32 v[230:231], v[50:51], v[50:51], v[230:231]\n"
    "v_pk_add_f32 v[224:225], v[224:225], v[52:53]\n"
    "v_pk_add_f32 v[226:227], v[226:227], v[54:55]\n"
    "v_pk_fma_f32 v[228:229], v[52:53], v[52:53], v[228:229]\n"
    "v_pk_fma_f32 v[230:231], v[54:55], v[54:55], v[230:231]\n"
    "v_mfma_f32_16x16x32_bf16 v[132:135], a[244:247], v[192:195], v[132:135]\n"
    "s_waitcnt lgkmcnt(8)\n"
    "ds_read_u16 v191, v191\n"
    "buffer_store_dwordx4 a[252:255], v255, s[44:47], s57 offen offset:128 sc1\n"
    "v_mfma_f32_16x16x32_bf16 v[104:107], a[16:19], v[196:199], v[104:107]\n"
    "s_waitcnt lgkmcnt(7)\n"
    "ds_read_u16_d16_hi v207, v207\n"
    "ds_read_b128 a[252:255], v249 offset:1024\n"
    "v_mfma_f32_16x16x32_bf16 v[108:111], a[20:23], v[196:199], v[108:111]\n"
    "ds_read_b128 a[240:243], v244 offset:34816\n"
    "v_or_b32_e64 v187, v187, v203\n"
    "v_mfma_f32_16x16x32_bf16 v[120:123], a[24:27], v[196:199], v[120:123]\n"
    "v_pk_mul_f16 v232, v184, v232\n"
    "v_or_b32_e64 v189, v189, v205\n"
    "v_mfma_f32_16x16x32_bf16 v[124:127], a[28:31], v[196:199], v[124:127]\n"
    "v_exp_f16_e32 v234, v232\n"
    "v_mfma_f32_16x16x32_bf16 v[136:139], v[236:239], v[196:199], v[136:139]\n"
    "s_waitcnt lgkmcnt(7)\n"
    "v_exp_f16_sdwa v234, v232 dst_sel:WORD_1 dst_unused:UNUSED_PRESERVE src0_sel:WORD_1\n"
    "v_mfma_f32_16x16x32_bf16 v[140:143], a[244:247], v[196:199], v[140:143]\n"
    "v_pk_fma_f16 v234, v234, s49, s49\n"
    "v_lshlrev_b32_sdwa v192, 1, v168 dst_sel:DWORD dst_unused:UNUSED_PAD src0_sel:DWORD src1_sel:WORD_1\n"
    "s_waitcnt lgkmcnt(5)\n"
    "v_smfmac_f32_16x16x64_bf16 v[56:59], v[216:219], v[208:215], v240\n"
    "s_waitcnt lgkmcnt(4)\n"
    "ds_read_b128 v[216:219], v252 offset:12768\n"
    "ds_read_u16 v192, v192\n"
    "v_smfmac_f32_16x16x64_bf16 v[60:63], v[220:223], v[208:215], v240 abid:1\n"
    "ds_read_b128 v[208:211], v250 offset:880\n"
    "ds_read_b128 v[212:215], v250 offset:1200\n"
    "ds_read_b128 v[220:223], v252 offset:13104\n"
    "v_lshlrev_b32_sdwa v200, 1, v169 dst_sel:DWORD dst_unused:UNUSED_PAD src0_sel:DWORD src1_sel:WORD_1\n"
    "v_lshlrev_b32_sdwa v193, 1, v170 dst_sel:DWORD dst_unused:UNUSED_PAD src0_sel:DWORD src1_sel:WORD_1\n"
    "v_lshlrev_b32_sdwa v201, 1, v171 dst_sel:DWORD dst_unused:UNUSED_PAD src0_sel:DWORD src1_sel:WORD_1\n"
    "v_mfma_f32_16x16x32_bf16 v[168:171], a[160:163], v[144:147], 0\n"
    "s_nop 0\n"
    "ds_read_u16_d16_hi v200, v200\n"
    "ds_read_u16 v193, v193\n"
    "v_mfma_f32_16x16x32_bf16 v[168:171], a[208:211], v[148:151], v[168:171]\n"
    "s_waitcnt lgkmcnt(8)\n"
    "buffer_store_dwordx4 a[252:255], v255, s[44:47], s88 offen offset:0 sc1\n"
    "ds_read_u16_d16_hi v201, v201\n"
    "v_lshlrev_b32_sdwa v194, 1, v172 dst_sel:DWORD dst_unused:UNUSED_PAD src0_sel:DWORD src1_sel:WORD_1\n"
    "v_lshlrev_b32_sdwa v202, 1, v173 dst_sel:DWORD dst_unused:UNUSED_PAD src0_sel:DWORD src1_sel:WORD_1\n"
    "v_lshlrev_b32_sdwa v195, 1, v174 dst_sel:DWORD dst_unused:UNUSED_PAD src0_sel:DWORD src1_sel:WORD_1\n"
    "v_lshlrev_b32_sdwa v203, 1, v175 dst_sel:DWORD dst_unused:UNUSED_PAD src0_sel:DWORD src1_sel:WORD_1\n"
    "v_rcp_f16_e64 v232, v234\n"
    "v_mfma_f32_16x16x32_bf16 v[168:171], a[120:123], v[152:155], v[168:171]\n"
    "ds_read_u16 v194, v194\n"
    "v_or_b32_e64 v190, v190, v206\n"
    "v_rcp_f16_sdwa v232, v234 dst_sel:WORD_1 dst_unused:UNUSED_PRESERVE src0_sel:WORD_1\n"
    "v_mfma_f32_16x16x32_bf16 v[172:175], a[164:167], v[144:147], 0\n"
    "ds_read_b128 v[236:239], v244 offset:8192\n"
    "v_lshlrev_b32_sdwa v196, 1, v176 dst_sel:DWORD dst_unused:UNUSED_PAD src0_sel:DWORD src1_sel:WORD_1\n"
    "v_lshlrev_b32_sdwa v204, 1, v177 dst_sel:DWORD dst_unused:UNUSED_PAD src0_sel:DWORD src1_sel:WORD_1\n"
    "v_lshlrev_b32_sdwa v197, 1, v178 dst_sel:DWORD dst_unused:UNUSED_PAD src0_sel:DWORD src1_sel:WORD_1\n"
    "v_lshlrev_b32_sdwa v205, 1, v179 dst_sel:DWORD dst_unused:UNUSED_PAD src0_sel:DWORD src1_sel:WORD_1\n"
    "v_lshlrev_b32_sdwa v198, 1, v180 dst_sel:DWORD dst_unused:UNUSED_PAD src0_sel:DWORD src1_sel:WORD_1\n"
    "v_lshlrev_b32_sdwa v206, 1, v181 dst_sel:DWORD dst_unused:UNUSED_PAD src0_sel:DWORD src1_sel:WORD_1\n"
    "v_lshlrev_b32_sdwa v199, 1, v182 dst_sel:DWORD dst_unused:UNUSED_PAD src0_sel:DWORD src1_sel:WORD_1\n"
    "v_fma_mix_f32 v234, v184, v232, 0 op_sel_hi:[1,1,0]\n"
    "v_mfma_f32_16x16x32_bf16 v[172:175], a[212:215], v[148:151], v[172:175]\n"
    "s_waitcnt lgkmcnt(6)\n"
    "ds_read_u16_d16_hi v202, v202\n"
    "ds_read_u16 v195, v195\n"
    "v_smfmac_f32_16x16x64_bf16 v[56:59], v[216:219], v[208:215], v240\n"
    "s_waitcnt lgkmcnt(7)\n"
    "ds_read_b128 a[244:247], v244 offset:10240\n"
    "v_smfmac_f32_16x16x64_bf16 v[60:63], v[220:223], v[208:215], v240 abid:1\n"
    "s_barrier\n"
    "v_mfma_f32_16x16x32_bf16 v[172:175], a[240:243], v[152:155], v[172:175]\n"
    "s_waitcnt lgkmcnt(7)\n"
    "ds_read_u16_d16_hi v203, v203\n"
    "ds_write2_b32 v248, v64, v65 offset0:0 offset1:20\n"
    "v_or_b32_e64 v191, v191, v207\n"
    "v_pk_add_f32 v[224:225], v[224:225], v[56:57]\n"
    "v_pk_add_f32 v[226:227], v[226:227], v[58:59]\n"
    "v_pk_fma_f32 v[228:229], v[56:57], v[56:57], v[228:229]\n"
    "v_pk_fma_f32 v[230:231], v[58:59], v[58:59], v[230:231]\n"
    "v_pk_add_f32 v[224:225], v[224:225], v[60:61]\n"
    "v_pk_add_f32 v[226:227], v[226:227], v[62:63]\n"
    "v_pk_fma_f32 v[228:229], v[60:61], v[60:61], v[228:229]\n"
    "v_pk_fma_f32 v[230:231], v[62:63], v[62:63], v[230:231]\n"
    "v_lshlrev_b32_sdwa v207, 1, v183 dst_sel:DWORD dst_unused:UNUSED_PAD src0_sel:DWORD src1_sel:WORD_1\n"
    "v_fma_mix_f32 v232, v184, v232, 0 op_sel:[1,1,0] op_sel_hi:[1,1,0]\n"
    "v_mfma_f32_16x16x32_bf16 v[176:179], a[160:163], v[156:159], 0\n"
    "ds_read_u16 v196, v196\n"
    "ds_write2_b32 v248, v66, v67 offset0:40 offset1:60\n"
    "v_add_u32_e32 v248, 1360, v248\n"
    "v_cvt_pk_bf16_f32 v184, v234, v232\n"
    "v_mfma_f32_16x16x32_bf16 v[176:179], a[208:211], v[160:163], v[176:179]\n"
    "v_pk_fma_f16 v232, v188, v188, s48\n"
    "v_or_b32_e32 v192, v192, v200\n"
    "v_mfma_f32_16x16x32_bf16 v[176:179], a[120:123], v[164:167], v[176:179]\n"
    "s_waitcnt lgkmcnt(7)\n"
    "ds_read_u16_d16_hi v204, v204\n"
    "s_waitcnt vmcnt(6)\n"
    "ds_write2_b32 v235, v84, v85 offset0:0 offset1:20\n"
    "v_mfma_f32_16x16x32_bf16 v[180:183], a[164:167], v[156:159], 0\n"
    "s_nop 0\n"
    "v_pk_mul_f16 v232, v188, v232\n"
    "v_or_b32_e64 v193, v193, v201\n"
    "v_mfma_f32_16x16x32_bf16 v[180:183], a[212:215], v[160:163], v[180:183]\n"
    "ds_read_u16 v197, v197\n"
    "ds_write2_b32 v235, v86, v87 offset0:40 offset1:60\n"
    "v_add_u32_e32 v235, 1360, v235\n"
    "v_exp_f16_e32 v234, v232\n"
    "v_mfma_f32_16x16x32_bf16 v[180:183], a[240:243], v[164:167], v[180:183]\n"
    "s_waitcnt lgkmcnt(6)\n"
    "ds_read_b128 a[252:255], v249 offset:3072\n"
    "ds_write2_b32 v248, v68, v69 offset0:0 offset1:20\n"
    "v_exp_f16_sdwa v234, v232 dst_sel:WORD_1 dst_unused:UNUSED_PRESERVE src0_sel:WORD_1\n"
    "v_lshlrev_b32_sdwa v201, 1, v171 dst_sel:DWORD dst_unused:UNUSED_PAD src0_sel:DWORD src1_sel:WORD_1\n"
    "v_pk_fma_f16 v234, v234, s49, s49\n"
    "v_mfma_f32_16x16x32_bf16 v[96:99], a[32:35], v[184:187], v[96:99]\n"
    "ds_read_u16_d16_hi v205, v205\n"
    "ds_write2_b32 v248, v70, v71 offset0:40 offset1:60\n"
    "v_mfma_f32_16x16x32_bf16 v[100:103], a[36:39], v[184:187], v[100:103]\n"
    "v_add_u32_e32 v248, 1360, v248\n"
    "v_rcp_f16_e32 v232, v234\n"
    "v_mfma_f32_16x16x32_bf16 v[112:115], a[40:43], v[184:187], v[112:115]\n"
    "ds_read_u16 v198, v198\n"
    "ds_read_u16_d16_hi v206, v206\n"
    "v_rcp_f16_sdwa v232, v234 dst_sel:WORD_1 dst_unused:UNUSED_PRESERVE src0_sel:WORD_1\n"
    "v_mfma_f32_16x16x32_bf16 v[116:119], a[44:47], v[184:187], v[116:119]\n"
    "s_waitcnt lgkmcnt(8)\n"
    "ds_read_u16 v199, v199\n"
    "ds_read_u16_d16_hi v207, v207\n"
    "v_fma_mix_f32 v234, v188, v232, 0 op_sel_hi:[1,1,0]\n"
    "v_mfma_f32_16x16x32_bf16 v[128:131], v[236:239], v[184:187], v[128:131]\n"
    "v_fma_mix_f32 v232, v188, v232, 0 op_sel:[1,1,0] op_sel_hi:[1,1,0]\n"
    "v_or_b32_e32 v194, v194, v202\n"
    "v_mfma_f32_16x16x32_bf16 v[132:135], a[244:247], v[184:187], v[132:135]\n"
    "s_waitcnt lgkmcnt(6)\n"
    "ds_read_b128 a[240:243], v244 offset:38912\n"
    "buffer_store_dwordx4 a[252:255], v255, s[44:47], s88 offen offset:64 sc1\n"
    "v_cvt_pk_bf16_f32 v188, v234, v232\n"
    "v_cvt_pk_f16_f32 v184, v168, v169\n"
    "v_lshlrev_b32_sdwa v185, 1, v170 dst_sel:DWORD dst_unused:UNUSED_PAD src0_sel:DWORD src1_sel:WORD_1\n"
    "v_mfma_f32_16x16x32_bf16 v[104:107], a[32:35], v[188:191], v[104:107]\n"
    "ds_read_u16 v185, v185\n"
    "s_waitcnt vmcnt(6)\n"
    "ds_write2_b32 v235, v88, v89 offset0:0 offset1:20\n"
    "v_mfma_f32_16x16x32_bf16 v[108:111], a[36:39], v[188:191], v[108:111]\n"
    "s_waitcnt lgkmcnt(7)\n"
    "v_lshlrev_b32_sdwa v186, 1, v172 dst_sel:DWORD dst_unused:UNUSED_PAD src0_sel:DWORD src1_sel:WORD_1\n"
    "v_lshlrev_b32_sdwa v202, 1, v173 dst_sel:DWORD dst_unused:UNUSED_PAD src0_sel:DWORD src1_sel:WORD_1\n"
    "v_mfma_f32_16x16x32_bf16 v[120:123], a[40:43], v[188:191], v[120:123]\n"
    "ds_read_b128 a[252:255], v249 offset:5120\n"
    "ds_write2_b32 v235, v90, v91 offset0:40 offset1:60\n"
    "v_mfma_f32_16x16x32_bf16 v[124:127], a[44:47], v[188:191], v[124:127]\n"
    "v_add_u32_e32 v235, -1360, v235\n"
    "v_lshlrev_b32_sdwa v187, 1, v174 dst_sel:DWORD dst_unused:UNUSED_PAD src0_sel:DWORD src1_sel:WORD_1\n"
    "v_mfma_f32_16x16x32_bf16 v[136:139], v[236:239], v[188:191], v[136:139]\n"
    "ds_read_u16_d16_hi v201, v201\n"
    "ds_write2_b32 v248, v72, v73 offset0:0 offset1:20\n"
    "v_mfma_f32_16x16x32_bf16 v[140:143], a[244:247], v[188:191], v[140:143]\n"
    "s_waitcnt lgkmcnt(9)\n"
    "v_cvt_pk_f16_f32 v188, v176, v177\n"
    "v_lshlrev_b32_sdwa v189, 1, v178 dst_sel:DWORD dst_unused:UNUSED_PAD src0_sel:DWORD src1_sel:WORD_1\n"
    "v_mfma_f32_16x16x32_bf16 v[168:171], a[168:171], v[144:147], 0\n"
    "ds_read_u16 v186, v186\n"
    "ds_write2_b32 v248, v74, v75 offset0:40 offset1:60\n"
    "v_mfma_f32_16x16x32_bf16 v[168:171], a[216:219], v[148:151], v[168:171]\n"
    "s_waitcnt lgkmcnt(8)\n"
    "v_add_u32_e32 v248, 1360, v248\n"
    "v_lshlrev_b32_sdwa v190, 1, v180 dst_sel:DWORD dst_unused:UNUSED_PAD src0_sel:DWORD src1_sel:WORD_1\n"
    "v_mfma_f32_16x16x32_bf16 v[168:171], a[124:127], v[152:155], v[168:171]\n"
    "ds_read_u16_d16_hi v202, v202\n"
    "ds_read_b128 v[236:239], v244 offset:12288\n"
    "v_lshlrev_b32_sdwa v191, 1, v182 dst_sel:DWORD dst_unused:UNUSED_PAD src0_sel:DWORD src1_sel:WORD_1\n"
    "v_or_b32_e64 v195, v195, v203\n"
    "v_lshlrev_b32_sdwa v203, 1, v175 dst_sel:DWORD dst_unused:UNUSED_PAD src0_sel:DWORD src1_sel:WORD_1\n"
    "v_mfma_f32_16x16x32_bf16 v[172:175], a[172:175], v[144:147], 0\n"
    "ds_read_u16 v187, v187\n"
    "v_or_b32_e32 v196, v196, v204\n"
    "v_mfma_f32_16x16x32_bf16 v[172:175], a[220:223], v[148:151], v[172:175]\n"
    "s_waitcnt lgkmcnt(7)\n"
    "ds_read_u16_d16_hi v203, v203\n"
    "buffer_store_dwordx4 a[252:255], v255, s[44:47], s88 offen offset:128 sc1\n"
    "v_mfma_f32_16x16x32_bf16 v[172:175], a[240:243], v[152:155], v[172:175]\n"
    "ds_read_b128 a[244:247], v244 offset:14336\n"
    "ds_write2_b32 v248, v76, v77 offset0:0 offset1:20\n"
    "v_or_b32_e64 v197, v197, v205\n"
    "v_lshlrev_b32_sdwa v205, 1, v179 dst_sel:DWORD dst_unused:UNUSED_PAD src0_sel:DWORD src1_sel:WORD_1\n"
    "v_mfma_f32_16x16x32_bf16 v[176:179], a[168:171], v[156:159], 0\n"
    "s_waitcnt lgkmcnt(8)\n"
    "v_or_b32_e32 v198, v198, v206\n"
    "v_lshlrev_b32_sdwa v206, 1, v181 dst_sel:DWORD dst_unused:UNUSED_PAD src0_sel:DWORD src1_sel:WORD_1\n"
    "v_mfma_f32_16x16x32_bf16 v[176:179], a[216:219], v[160:163], v[176:179]\n"
    "ds_read_u16 v189, v189\n"
    "ds_write2_b32 v248, v78, v79 offset0:40 offset1:60\n"
    "v_mfma_f32_16x16x32_bf16 v[176:179], a[124:127], v[164:167], v[176:179]\n"
    "v_add_u32_e32 v248, 1360, v248\n"
    "v_or_b32_e64 v199, v199, v207\n"
    "v_mfma_f32_16x16x32_bf16 v[96:99], a[48:51], v[192:195], v[96:99]\n"
    "ds_read_u16_d16_hi v205, v205\n"
    "ds_read_u16 v190, v190\n"
    "v_mfma_f32_16x16x32_bf16 v[100:103], a[52:55], v[192:195], v[100:103]\n"
    "v_lshlrev_b32_sdwa v207, 1, v183 dst_sel:DWORD dst_unused:UNUSED_PAD src0_sel:DWORD src1_sel:WORD_1\n"
    "v_or_b32_e32 v185, v185, v201\n"
    "v_mfma_f32_16x16x32_bf16 v[180:183], a[172:175], v[156:159], 0\n"
    "s_waitcnt lgkmcnt(8)\n"
    "ds_read_b128 v[144:147], v243 offset:0\n"
    "v_or_b32_e64 v186, v186, v202\n"
    "v_mfma_f32_16x16x32_bf16 v[180:183], a[220:223], v[160:163], v[180:183]\n"
    "ds_read_u16_d16_hi v206, v206\n"
    "v_pk_fma_f16 v232, v184, v184, s48\n"
    "v_mfma_f32_16x16x32_bf16 v[180:183], a[240:243], v[164:167], v[180:183]\n"
    "v_pk_mul_f16 v232, v184, v232\n"
    "v_lshlrev_b32_sdwa v201, 1, v171 dst_sel:DWORD dst_unused:UNUSED_PAD src0_sel:DWORD src1_sel:WORD_1\n"
    "v_mfma_f32_16x16x32_bf16 v[112:115], a[56:59], v[192:195], v[112:115]\n"
    "ds_read_b128 v[148:151], v243 offset:1024\n"
    "v_exp_f16_e32 v234, v232\n"
    "v_mfma_f32_16x16x32_bf16 v[116:119], a[60:63], v[192:195], v[116:119]\n"
    "s_waitcnt lgkmcnt(7)\n"
    "ds_read_u16 v191, v191\n"
    "v_or_b32_e64 v187, v187, v203\n"
    "v_mfma_f32_16x16x32_bf16 v[128:131], v[236:239], v[192:195], v[128:131]\n"
    "v_exp_f16_sdwa v234, v232 dst_sel:WORD_1 dst_unused:UNUSED_PRESERVE src0_sel:WORD_1\n"
    "v_mfma_f32_16x16x32_bf16 v[132:135], a[244:247], v[192:195], v[132:135]\n"
    "ds_read_b128 v[152:155], v243 offset:2048\n"
    "ds_write2_b32 v248, v80, v81 offset0:0 offset1:20\n"
    "v_mfma_f32_16x16x32_bf16 v[104:107], a[48:51], v[196:199], v[104:107]\n"
    "v_pk_fma_f16 v234, v234, s49, s49\n"
    "v_cvt_pk_f16_f32 v192, v168, v169\n"
    "v_mfma_f32_16x16x32_bf16 v[108:111], a[52:55], v[196:199], v[108:111]\n"
    "ds_read_u16_d16_hi v207, v207\n"
    "v_lshlrev_b32_sdwa v193, 1, v170 dst_sel:DWORD dst_unused:UNUSED_PAD src0_sel:DWORD src1_sel:WORD_1\n"
    "v_mfma_f32_16x16x32_bf16 v[120:123], a[56:59], v[196:199], v[120:123]\n"
    "ds_read_b128 v[156:159], v243 offset:3072\n"
    "v_lshlrev_b32_sdwa v194, 1, v172 dst_sel:DWORD dst_unused:UNUSED_PAD src0_sel:DWORD src1_sel:WORD_1\n"
    "v_mfma_f32_16x16x32_bf16 v[124:127], a[60:63], v[196:199], v[124:127]\n"
    "s_waitcnt lgkmcnt(9)\n"
    "ds_read_b128 v[160:163], v243 offset:4096\n"
    "v_or_b32_e32 v189, v189, v205\n"
    "v_mfma_f32_16x16x32_bf16 v[136:139], v[236:239], v[196:199], v[136:139]\n"
    "s_nop 0\n"
    "ds_read_b128 v[164:167], v243 offset:5120\n"
    "v_lshlrev_b32_sdwa v202, 1, v173 dst_sel:DWORD dst_unused:UNUSED_PAD src0_sel:DWORD src1_sel:WORD_1\n"
    "v_mfma_f32_16x16x32_bf16 v[140:143], a[244:247], v[196:199], v[140:143]\n"
    "s_waitcnt lgkmcnt(9)\n"
    "ds_read_b128 a[240:243], v244 offset:32768\n"
    "buffer_load_dwordx4 a[232:235], v245, s[40:43], s58 offen offset:64\n"
    "buffer_load_dwordx4 a[224:227], v245, s[40:43], s58 offen offset:0\n"
    "v_mfma_f32_16x16x32_bf16 v[168:171], a[128:131], v[144:147], 0\n"
    "ds_read_u16 v193, v193\n"
    "v_lshlrev_b32_sdwa v195, 1, v174 dst_sel:DWORD dst_unused:UNUSED_PAD src0_sel:DWORD src1_sel:WORD_1\n"
    "v_lshlrev_b32_sdwa v203, 1, v175 dst_sel:DWORD dst_unused:UNUSED_PAD src0_sel:DWORD src1_sel:WORD_1\n"
    "s_waitcnt lgkmcnt(9)\n"
    "v_mfma_f32_16x16x32_bf16 v[168:171], a[176:179], v[148:151], v[168:171]\n"
    "s_nop 0\n"
    "ds_read_u16_d16_hi v201, v201\n"
    "v_rcp_f16_e64 v232, v234\n"
    "v_or_b32_e32 v190, v190, v206\n"
    "v_rcp_f16_sdwa v232, v234 dst_sel:WORD_1 dst_unused:UNUSED_PRESERVE src0_sel:WORD_1\n"
    "v_mfma_f32_16x16x32_bf16 v[172:175], a[132:135], v[144:147], 0\n"
    "s_waitcnt lgkmcnt(8)\n"
    "ds_read_u16 v194, v194\n"
    "v_cvt_pk_f16_f32 v196, v176, v177\n"
    "v_mfma_f32_16x16x32_bf16 v[168:171], a[96:99], v[152:155], v[168:171]\n"
    "ds_read_u16_d16_hi v202, v202\n"
    "v_lshlrev_b32_sdwa v197, 1, v178 dst_sel:DWORD dst_unused:UNUSED_PAD src0_sel:DWORD src1_sel:WORD_1\n"
    "v_mfma_f32_16x16x32_bf16 v[172:175], a[180:183], v[148:151], v[172:175]\n"
    "v_lshlrev_b32_sdwa v205, 1, v179 dst_sel:DWORD dst_unused:UNUSED_PAD src0_sel:DWORD src1_sel:WORD_1\n"
    "v_lshlrev_b32_sdwa v198, 1, v180 dst_sel:DWORD dst_unused:UNUSED_PAD src0_sel:DWORD src1_sel:WORD_1\n"
    "v_lshlrev_b32_sdwa v206, 1, v181 dst_sel:DWORD dst_unused:UNUSED_PAD src0_sel:DWORD src1_sel:WORD_1\n"
    "v_lshlrev_b32_sdwa v199, 1, v182 dst_sel:DWORD dst_unused:UNUSED_PAD src0_sel:DWORD src1_sel:WORD_1\n"
    "v_fma_mix_f32 v234, v184, v232, 0 op_sel_hi:[1,1,0]\n"
    "v_fma_mix_f32 v232, v184, v232, 0 op_sel:[1,1,0] op_sel_hi:[1,1,0]\n"
    "v_lshlrev_b32_sdwa v200, 1, v169 dst_sel:DWORD dst_unused:UNUSED_PAD src0_sel:DWORD src1_sel:WORD_1\n"
    "s_waitcnt lgkmcnt(7)\n"
    "v_mfma_f32_16x16x32_bf16 v[176:179], a[128:131], v[156:159], 0\n"
    "s_nop 0\n"
    "ds_read_b128 v[236:239], v244 offset:16384\n"
    "ds_write2_b32 v248, v82, v83 offset0:40 offset1:60\n"
    "v_mfma_f32_32x32x1_2b_f32 v[64:95], v254, v254, 0\n"
    "v_add_u32_e32 v248, -5440, v248\n"
    "v_or_b32_e64 v191, v191, v207\n"
    "v_lshlrev_b32_sdwa v207, 1, v183 dst_sel:DWORD dst_unused:UNUSED_PAD src0_sel:DWORD src1_sel:WORD_1\n"
    "v_cvt_pk_bf16_f32 v184, v234, v232\n"
    "v_pk_fma_f16 v232, v188, v188, s48\n"
    "v_lshlrev_b32_e64 v254, 3, v244\n"
    "v_pk_mul_f16 v232, v188, v232\n"
    "v_exp_f16_e64 v234, v232\n"
    "ds_read_u16 v195, v195\n"
    "ds_read_b128 v[216:219], v251 offset:21504\n"
    "v_exp_f16_sdwa v234, v232 dst_sel:WORD_1 dst_unused:UNUSED_PRESERVE src0_sel:WORD_1\n"
    "s_waitcnt lgkmcnt(7)\n"
    "v_mfma_f32_16x16x32_bf16 v[172:175], a[240:243], v[152:155], v[172:175]\n"
    "ds_read_b128 v[220:223], v251 offset:21840\n"
    "ds_read_u16_d16_hi v203, v203\n"
    "v_mfma_f32_16x16x32_bf16 v[176:179], a[176:179], v[160:163], v[176:179]\n"
    "s_waitcnt lgkmcnt(8)\n"
    "ds_read_b128 a[244:247], v244 offset:18432\n"
    "ds_read_u16 v197, v197\n"
    "v_mfma_f32_16x16x32_bf16 v[176:179], a[96:99], v[164:167], v[176:179]\n"
    "s_mov_b32 exec_hi, 0\n"
    "buffer_load_dword v233, v254, s[72:75], s53 offen sc1\n"
    "s_mov_b32 exec_hi, -1\n"
    "v_mfma_f32_16x16x32_bf16 v[180:183], a[132:135], v[156:159], 0\n"
    "s_add_u32 s53, s53, 393216\n"
    "ds_read_u16_d16_hi v205, v205\n"
    "v_pk_fma_f16 v234, v234, s49, s49\n"
    "v_mfma_f32_16x16x32_bf16 v[180:183], a[180:183], v[160:163], v[180:183]\n"
    "v_rcp_f16_e32 v232, v234\n"
    "v_mfma_f32_16x16x32_bf16 v[180:183], a[240:243], v[164:167], v[180:183]\n"
    "s_waitcnt lgkmcnt(7)\n"
    "ds_read_u16 v198, v198\n"
    "v_rcp_f16_sdwa v232, v234 dst_sel:WORD_1 dst_unused:UNUSED_PRESERVE src0_sel:WORD_1\n"
    "v_mfma_f32_16x16x32_bf16 v[96:99], a[64:67], v[184:187], v[96:99]\n"
    "s_barrier\n"
    "v_mfma_f32_16x16x32_bf16 v[100:103], a[68:71], v[184:187], v[100:103]\n"
    "s_nop 0\n"
    "ds_read_b128 v[208:211], v250 offset:0\n"
    "ds_read_b128 v[212:215], v250 offset:320\n"
    "v_mfma_f32_16x16x32_bf16 v[112:115], a[72:75], v[184:187], v[112:115]\n"
    "ds_read_u16_d16_hi v206, v206\n"
    "v_or_b32_e64 v193, v193, v201\n"
    "v_mfma_f32_16x16x32_bf16 v[116:119], a[76:79], v[184:187], v[116:119]\n"
    "ds_read_u16 v199, v199\n"
    "v_fma_mix_f32 v234, v188, v232, 0 op_sel_hi:[1,1,0]\n"
    "v_mfma_f32_16x16x32_bf16 v[128:131], v[236:239], v[184:187], v[128:131]\n"
    "v_fma_mix_f32 v232, v188, v232, 0 op_sel:[1,1,0] op_sel_hi:[1,1,0]\n"
    "v_or_b32_e32 v194, v194, v202\n"
    "s_waitcnt lgkmcnt(7)\n"
    "v_mfma_f32_16x16x32_bf16 v[132:135], a[244:247], v[184:187], v[132:135]\n"
    "buffer_load_dwordx4 a[236:239], v245, s[40:43], s89 offen offset:64\n"
    "buffer_load_dwordx4 a[228:231], v245, s[40:43], s89 offen offset:0\n"
    "ds_read_u16_d16_hi v207, v207\n"
    "v_cvt_pk_bf16_f32 v188, v234, v232\n"
    "v_pk_fma_f16 v232, v192, v192, s48\n"
    "v_or_b32_e64 v195, v195, v203\n"
    "v_mfma_f32_16x16x32_bf16 v[104:107], a[64:67], v[188:191], v[104:107]\n"
    "ds_read_b128 a[240:243], v244 offset:36864\n"
    "ds_read_u16_d16_hi v200, v200\n"
    "v_mfma_f32_16x16x32_bf16 v[108:111], a[68:71], v[188:191], v[108:111]\n"
    "v_pk_mul_f16 v232, v192, v232\n"
    "v_lshlrev_b32_sdwa v184, 1, v168 dst_sel:DWORD dst_unused:UNUSED_PAD src0_sel:DWORD src1_sel:WORD_1\n"
    "v_mfma_f32_16x16x32_bf16 v[120:123], a[72:75], v[188:191], v[120:123]\n"
    "s_waitcnt lgkmcnt(8)\n"
    "ds_read_u16 v184, v184\n"
    "v_exp_f16_e32 v234, v232\n"
    "v_mfma_f32_16x16x32_bf16 v[124:127], a[76:79], v[188:191], v[124:127]\n"
    "v_exp_f16_sdwa v234, v232 dst_sel:WORD_1 dst_unused:UNUSED_PRESERVE src0_sel:WORD_1\n"
    "v_mfma_f32_16x16x32_bf16 v[136:139], v[236:239], v[188:191], v[136:139]\n"
    "ds_read_b128 v[236:239], v244 offset:20480\n"
    "v_or_b32_e32 v197, v197, v205\n"
    "s_waitcnt lgkmcnt(7)\n"
    "v_smfmac_f32_16x16x64_bf16 v[64:67], v[216:219], v[208:215], v240\n"
    "ds_read_b128 v[216:219], v252 offset:21504\n"
    "v_smfmac_f32_16x16x64_bf16 v[68:71], v[220:223], v[208:215], v240 abid:1\n"
    "s_nop 0\n"
    "ds_read_b128 v[208:211], v250 offset:640\n"
    "ds_read_b128 v[212:215], v250 offset:960\n"
    "v_mfma_f32_16x16x32_bf16 v[140:143], a[244:247], v[188:191], v[140:143]\n"
    "s_waitcnt lgkmcnt(9)\n"
    "ds_read_b128 v[220:223], v252 offset:21840\n"
    "v_pk_fma_f16 v234, v234, s49, s49\n"
    "v_lshlrev_b32_sdwa v185, 1, v170 dst_sel:DWORD dst_unused:UNUSED_PAD src0_sel:DWORD src1_sel:WORD_1\n"
    "v_lshlrev_b32_sdwa v201, 1, v171 dst_sel:DWORD dst_unused:UNUSED_PAD src0_sel:DWORD src1_sel:WORD_1\n"
    "v_mfma_f32_16x16x32_bf16 v[168:171], a[136:139], v[144:147], 0\n"
    "ds_read_u16 v185, v185\n"
    "v_lshlrev_b32_sdwa v186, 1, v172 dst_sel:DWORD dst_unused:UNUSED_PAD src0_sel:DWORD src1_sel:WORD_1\n"
    "v_mfma_f32_16x16x32_bf16 v[168:171], a[184:187], v[148:151], v[168:171]\n"
    "ds_read_u16_d16_hi v201, v201\n"
    "v_lshlrev_b32_sdwa v202, 1, v173 dst_sel:DWORD dst_unused:UNUSED_PAD src0_sel:DWORD src1_sel:WORD_1\n"
    "v_lshlrev_b32_sdwa v187, 1, v174 dst_sel:DWORD dst_unused:UNUSED_PAD src0_sel:DWORD src1_sel:WORD_1\n"
    "v_lshlrev_b32_sdwa v203, 1, v175 dst_sel:DWORD dst_unused:UNUSED_PAD src0_sel:DWORD src1_sel:WORD_1\n"
    "v_rcp_f16_e32 v232, v234\n"
    "v_mfma_f32_16x16x32_bf16 v[168:171], a[100:103], v[152:155], v[168:171]\n"
    "s_waitcnt lgkmcnt(9)\n"
    "ds_read_u16 v186, v186\n"
    "v_or_b32_e64 v198, v198, v206\n"
    "v_rcp_f16_sdwa v232, v234 dst_sel:WORD_1 dst_unused:UNUSED_PRESERVE src0_sel:WORD_1\n"
    "v_mfma_f32_16x16x32_bf16 v[172:175], a[140:143], v[144:147], 0\n"
    "ds_read_u16_d16_hi v202, v202\n"
    "v_lshlrev_b32_sdwa v188, 1, v176 dst_sel:DWORD dst_unused:UNUSED_PAD src0_sel:DWORD src1_sel:WORD_1\n"
    "v_mfma_f32_16x16x32_bf16 v[172:175], a[188:191], v[148:151], v[172:175]\n"
    "v_lshlrev_b32_sdwa v204, 1, v177 dst_sel:DWORD dst_unused:UNUSED_PAD src0_sel:DWORD src1_sel:WORD_1\n"
    "v_lshlrev_b32_sdwa v189, 1, v178 dst_sel:DWORD dst_unused:UNUSED_PAD src0_sel:DWORD src1_sel:WORD_1\n"
    "v_lshlrev_b32_sdwa v205, 1, v179 dst_sel:DWORD dst_unused:UNUSED_PAD src0_sel:DWORD src1_sel:WORD_1\n"
    "v_lshlrev_b32_sdwa v190, 1, v180 dst_sel:DWORD dst_unused:UNUSED_PAD src0_sel:DWORD src1_sel:WORD_1\n"
    "v_lshlrev_b32_sdwa v206, 1, v181 dst_sel:DWORD dst_unused:UNUSED_PAD src0_sel:DWORD src1_sel:WORD_1\n"
    "v_lshlrev_b32_sdwa v191, 1, v182 dst_sel:DWORD dst_unused:UNUSED_PAD src0_sel:DWORD src1_sel:WORD_1\n"
    "v_fma_mix_f32 v234, v192, v232, 0 op_sel_hi:[1,1,0]\n"
    "v_mfma_f32_16x16x32_bf16 v[172:175], a[240:243], v[152:155], v[172:175]\n"
    "s_waitcnt lgkmcnt(7)\n"
    "ds_read_u16 v187, v187\n"
    "ds_read_b128 a[244:247], v244 offset:22528\n"
    "v_or_b32_e32 v199, v199, v207\n"
    "v_lshlrev_b32_sdwa v207, 1, v183 dst_sel:DWORD dst_unused:UNUSED_PAD src0_sel:DWORD src1_sel:WORD_1\n"
    "v_fma_mix_f32 v232, v192, v232, 0 op_sel:[1,1,0] op_sel_hi:[1,1,0]\n"
    "s_waitcnt lgkmcnt(7)\n"
    "v_smfmac_f32_16x16x64_bf16 v[64:67], v[216:219], v[208:215], v240\n"
    "s_waitcnt lgkmcnt(6)\n"
    "ds_read_b128 v[216:219], v251 offset:22176\n"
    "ds_read_u16_d16_hi v203, v203\n"
    "v_smfmac_f32_16x16x64_bf16 v[68:71], v[220:223], v[208:215], v240 abid:1\n"
    "v_cvt_pk_bf16_f32 v192, v234, v232\n"
    "v_mfma_f32_16x16x32_bf16 v[176:179], a[136:139], v[156:159], 0\n"
    "ds_read_b128 v[208:211], v250 offset:80\n"
    "v_pk_fma_f16 v232, v196, v196, s48\n"
    "v_or_b32_e64 v184, v184, v200\n"
    "v_pk_mul_f16 v232, v196, v232\n"
    "v_lshlrev_b32_e64 v254, 3, v244\n"
    "v_pk_add_f32 v[224:225], v[224:225], v[64:65]\n"
    "v_pk_add_f32 v[226:227], v[226:227], v[66:67]\n"
    "v_pk_fma_f32 v[228:229], v[64:65], v[64:65], v[228:229]\n"
    "v_pk_fma_f32 v[230:231], v[66:67], v[66:67], v[230:231]\n"
    "v_pk_add_f32 v[224:225], v[224:225], v[68:69]\n"
    "v_pk_add_f32 v[226:227], v[226:227], v[70:71]\n"
    "v_pk_fma_f32 v[228:229], v[68:69], v[68:69], v[228:229]\n"
    "v_pk_fma_f32 v[230:231], v[70:71], v[70:71], v[230:231]\n"
    "v_mfma_f32_16x16x32_bf16 v[176:179], a[184:187], v[160:163], v[176:179]\n"
    "s_waitcnt lgkmcnt(7)\n"
    "ds_read_b128 v[212:215], v250 offset:400\n"
    "ds_read_b128 v[220:223], v251 offset:22512\n"
    "v_mfma_f32_16x16x32_bf16 v[176:179], a[100:103], v[164:167], v[176:179]\n"
    "s_nop 0\n"
    "ds_read_u16 v188, v188\n"
    "ds_read_u16_d16_hi v204, v204\n"
    "v_mfma_f32_16x16x32_bf16 v[180:183], a[140:143], v[156:159], 0\n"
    "v_exp_f16_e32 v234, v232\n"
    "v_mfma_f32_16x16x32_bf16 v[180:183], a[188:191], v[160:163], v[180:183]\n"
    "s_waitcnt lgkmcnt(9)\n"
    "ds_read_u16 v189, v189\n"
    "v_exp_f16_sdwa v234, v232 dst_sel:WORD_1 dst_unused:UNUSED_PRESERVE src0_sel:WORD_1\n"
    "v_mfma_f32_16x16x32_bf16 v[180:183], a[240:243], v[164:167], v[180:183]\n"
    "ds_read_u16_d16_hi v205, v205\n"
    "v_pk_fma_f16 v234, v234, s49, s49\n"
    "v_mfma_f32_16x16x32_bf16 v[96:99], a[80:83], v[192:195], v[96:99]\n"
    "v_rcp_f16_e64 v232, v234\n"
    "v_mfma_f32_16x16x32_bf16 v[100:103], a[84:87], v[192:195], v[100:103]\n"
    "ds_read_u16 v190, v190\n"
    "v_rcp_f16_sdwa v232, v234 dst_sel:WORD_1 dst_unused:UNUSED_PRESERVE src0_sel:WORD_1\n"
    "v_mfma_f32_16x16x32_bf16 v[112:115], a[88:91], v[192:195], v[112:115]\n"
    "ds_read_u16_d16_hi v206, v206\n"
    "v_fma_mix_f32 v234, v196, v232, 0 op_sel_hi:[1,1,0]\n"
    "v_mfma_f32_16x16x32_bf16 v[116:119], a[92:95], v[192:195], v[116:119]\n"
    "s_waitcnt lgkmcnt(8)\n"
    "v_fma_mix_f32 v232, v196, v232, 0 op_sel:[1,1,0] op_sel_hi:[1,1,0]\n"
    "v_or_b32_e32 v185, v185, v201\n"
    "s_waitcnt lgkmcnt(7)\n"
    "v_smfmac_f32_16x16x64_bf16 v[72:75], v[216:219], v[208:215], v240\n"
    "s_waitcnt lgkmcnt(6)\n"
    "ds_read_b128 v[216:219], v252 offset:22176\n"
    "s_mov_b32 exec_hi, 0\n"
    "buffer_load_dword v233, v254, s[72:75], s53 offen sc1\n"
    "s_mov_b32 exec_hi, -1\n"
    "v_smfmac_f32_16x16x64_bf16 v[76:79], v[220:223], v[208:215], v240 abid:1\n"
    "s_add_u32 s53, s53, 393216\n"
    "ds_read_b128 v[208:211], v250 offset:720\n"
    "ds_read_b128 v[212:215], v250 offset:1040\n"
    "v_mfma_f32_16x16x32_bf16 v[128:131], v[236:239], v[192:195], v[128:131]\n"
    "ds_read_b128 v[220:223], v252 offset:22512\n"
    "v_cvt_pk_bf16_f32 v196, v234, v232\n"
    "v_mfma_f32_16x16x32_bf16 v[132:135], a[244:247], v[192:195], v[132:135]\n"
    "s_nop 0\n"
    "ds_read_u16 v191, v191\n"
    "v_cvt_pk_f16_f32 v192, v168, v169\n"
    "v_mfma_f32_16x16x32_bf16 v[104:107], a[80:83], v[196:199], v[104:107]\n"
    "s_waitcnt lgkmcnt(9)\n"
    "v_lshlrev_b32_sdwa v193, 1, v170 dst_sel:DWORD dst_unused:UNUSED_PAD src0_sel:DWORD src1_sel:WORD_1\n"
    "v_lshlrev_b32_sdwa v201, 1, v171 dst_sel:DWORD dst_unused:UNUSED_PAD src0_sel:DWORD src1_sel:WORD_1\n"
    "v_mfma_f32_16x16x32_bf16 v[108:111], a[84:87], v[196:199], v[108:111]\n"
    "ds_read_u16_d16_hi v207, v207\n"
    "v_lshlrev_b32_sdwa v194, 1, v172 dst_sel:DWORD dst_unused:UNUSED_PAD src0_sel:DWORD src1_sel:WORD_1\n"
    "v_mfma_f32_16x16x32_bf16 v[120:123], a[88:91], v[196:199], v[120:123]\n"
    "ds_read_u16 v193, v193\n"
    "v_or_b32_e64 v186, v186, v202\n"
    "v_mfma_f32_16x16x32_bf16 v[124:127], a[92:95], v[196:199], v[124:127]\n"
    "v_lshlrev_b32_sdwa v202, 1, v173 dst_sel:DWORD dst_unused:UNUSED_PAD src0_sel:DWORD src1_sel:WORD_1\n"
    "v_lshlrev_b32_sdwa v195, 1, v174 dst_sel:DWORD dst_unused:UNUSED_PAD src0_sel:DWORD src1_sel:WORD_1\n"
    "v_mfma_f32_16x16x32_bf16 v[136:139], v[236:239], v[196:199], v[136:139]\n"
    "s_waitcnt lgkmcnt(9)\n"
    "ds_read_u16_d16_hi v201, v201\n"
    "v_or_b32_e32 v187, v187, v203\n"
    "v_mfma_f32_16x16x32_bf16 v[140:143], a[244:247], v[196:199], v[140:143]\n"
    "v_lshlrev_b32_sdwa v203, 1, v175 dst_sel:DWORD dst_unused:UNUSED_PAD src0_sel:DWORD src1_sel:WORD_1\n"
    "v_cvt_pk_f16_f32 v196, v176, v177\n"
    "v_mfma_f32_16x16x32_bf16 v[168:171], a[144:147], v[144:147], 0\n"
    "s_nop 0\n"
    "ds_read_u16 v194, v194\n"
    "v_lshlrev_b32_sdwa v197, 1, v178 dst_sel:DWORD dst_unused:UNUSED_PAD src0_sel:DWORD src1_sel:WORD_1\n"
    "v_mfma_f32_16x16x32_bf16 v[168:171], a[192:195], v[148:151], v[168:171]\n"
    "s_waitcnt lgkmcnt(8)\n"
    "v_lshlrev_b32_sdwa v198, 1, v180 dst_sel:DWORD dst_unused:UNUSED_PAD src0_sel:DWORD src1_sel:WORD_1\n"
    "v_lshlrev_b32_sdwa v199, 1, v182 dst_sel:DWORD dst_unused:UNUSED_PAD src0_sel:DWORD src1_sel:WORD_1\n"
    "v_mfma_f32_16x16x32_bf16 v[168:171], a[104:107], v[152:155], v[168:171]\n"
    "ds_read_u16_d16_hi v202, v202\n"
    "v_or_b32_e64 v188, v188, v204\n"
    "s_waitcnt lgkmcnt(7)\n"
    "v_smfmac_f32_16x16x64_bf16 v[72:75], v[216:219], v[208:215], v240\n"
    "s_waitcnt lgkmcnt(6)\n"
    "ds_read_b128 v[216:219], v251 offset:22848\n"
    "v_smfmac_f32_16x16x64_bf16 v[76:79], v[220:223], v[208:215], v240 abid:1\n"
    "ds_read_b128 v[208:211], v250 offset:160\n"
    "v_mfma_f32_16x16x32_bf16 v[172:175], a[148:151], v[144:147], 0\n"
    "ds_read_b128 v[212:215], v250 offset:480\n"
    "v_or_b32_e64 v189, v189, v205\n"
    "v_mfma_f32_16x16x32_bf16 v[172:175], a[196:199], v[148:151], v[172:175]\n"
    "ds_read_b128 v[220:223], v251 offset:23184\n"
    "v_lshlrev_b32_sdwa v205, 1, v179 dst_sel:DWORD dst_unused:UNUSED_PAD src0_sel:DWORD src1_sel:WORD_1\n"
    "v_cvt_pk_bf16_f32 v96, v96, v97\n"
    "v_cvt_pk_bf16_f32 v97, v98, v99\n"
    "v_cvt_pk_bf16_f32 v98, v100, v101\n"
    "v_pk_add_f32 v[224:225], v[224:225], v[72:73]\n"
    "v_pk_add_f32 v[226:227], v[226:227], v[74:75]\n"
    "v_pk_fma_f32 v[228:229], v[72:73], v[72:73], v[228:229]\n"
    "v_pk_fma_f32 v[230:231], v[74:75], v[74:75], v[230:231]\n"
    "v_pk_add_f32 v[224:225], v[224:225], v[76:77]\n"
    "v_pk_add_f32 v[226:227], v[226:227], v[78:79]\n"
    "v_pk_fma_f32 v[228:229], v[76:77], v[76:77], v[228:229]\n"
    "v_pk_fma_f32 v[230:231], v[78:79], v[78:79], v[230:231]\n"
    "v_mfma_f32_16x16x32_bf16 v[172:175], a[108:111], v[152:155], v[172:175]\n"
    "s_waitcnt lgkmcnt(7)\n"
    "ds_read_b128 v[236:239], v244 offset:0\n"
    "ds_read_u16 v195, v195\n"
    "v_mfma_f32_16x16x32_bf16 v[176:179], a[144:147], v[156:159], 0\n"
    "s_nop 0\n"
    "ds_read_u16_d16_hi v203, v203\n"
    "v_cvt_pk_bf16_f32 v99, v102, v103\n"
    "v_mfma_f32_16x16x32_bf16 v[176:179], a[192:195], v[160:163], v[176:179]\n"
    "s_waitcnt lgkmcnt(9)\n"
    "ds_read_b128 a[244:247], v244 offset:2048\n"
    "v_or_b32_e32 v190, v190, v206\n"
    "v_mfma_f32_16x16x32_bf16 v[176:179], a[104:107], v[164:167], v[176:179]\n"
    "ds_read_u16 v197, v197\n"
    "v_lshlrev_b32_sdwa v206, 1, v181 dst_sel:DWORD dst_unused:UNUSED_PAD src0_sel:DWORD src1_sel:WORD_1\n"
    "v_mfma_f32_16x16x32_bf16 v[100:103], a[4:7], v[184:187], 0\n"
    "s_nop 0\n"
    "v_cvt_pk_bf16_f32 v112, v112, v113\n"
    "ds_write_b128 v242, v[96:99] offset:24640\n"
    "v_mfma_f32_16x16x32_bf16 v[96:99], a[0:3], v[184:187], 0\n"
    "s_waitcnt lgkmcnt(8)\n"
    "v_cvt_pk_bf16_f32 v113, v114, v115\n"
    "v_cvt_pk_bf16_f32 v114, v116, v117\n"
    "s_waitcnt lgkmcnt(7)\n"
    "v_smfmac_f32_16x16x64_bf16 v[80:83], v[216:219], v[208:215], v240\n"
    "s_waitcnt lgkmcnt(6)\n"
    "ds_read_b128 v[216:219], v252 offset:22848\n"
    "ds_read_u16_d16_hi v205, v205\n"
    "v_smfmac_f32_16x16x64_bf16 v[84:87], v[220:223], v[208:215], v240 abid:1\n"
    "ds_read_b128 v[208:211], v250 offset:800\n"
    "v_cvt_pk_bf16_f32 v115, v118, v119\n"
    "v_cvt_pk_bf16_f32 v128, v128, v129\n"
    "ds_read_b128 v[212:215], v250 offset:1120\n"
    "v_cvt_pk_bf16_f32 v129, v130, v131\n"
    "ds_read_b128 v[220:223], v252 offset:23184\n"
    "v_or_b32_e64 v191, v191, v207\n"
    "v_mfma_f32_16x16x32_bf16 v[116:119], a[12:15], v[184:187], 0\n"
    "ds_read_u16 v198, v198\n"
    "v_lshlrev_b32_sdwa v207, 1, v183 dst_sel:DWORD dst_unused:UNUSED_PAD src0_sel:DWORD src1_sel:WORD_1\n"
    "v_mfma_f32_16x16x32_bf16 v[180:183], a[148:151], v[156:159], 0\n"
    "v_cvt_pk_bf16_f32 v130, v132, v133\n"
    "v_or_b32_e32 v193, v193, v201\n"
    "v_mfma_f32_16x16x32_bf16 v[180:183], a[196:199], v[160:163], v[180:183]\n"
    "s_waitcnt lgkmcnt(8)\n"
    "ds_read_u16_d16_hi v206, v206\n"
    "v_cvt_pk_bf16_f32 v131, v134, v135\n"
    "v_mfma_f32_16x16x32_bf16 v[180:183], a[108:111], v[164:167], v[180:183]\n"
    "ds_read_u16 v199, v199\n"
    "v_cvt_pk_bf16_f32 v104, v104, v105\n"
    "v_mfma_f32_16x16x32_bf16 v[132:135], a[244:247], v[184:187], 0\n"
    "v_cvt_pk_bf16_f32 v105, v106, v107\n"
    "v_cvt_pk_bf16_f32 v106, v108, v109\n"
    "v_cvt_pk_bf16_f32 v107, v110, v111\n"
    "v_mfma_f32_16x16x32_bf16 v[108:111], a[4:7], v[188:191], 0\n"
    "ds_read_u16_d16_hi v207, v207\n"
    "v_or_b32_e64 v194, v194, v202\n"
    "v_cvt_pk_bf16_f32 v120, v120, v121\n"
    "v_cvt_pk_bf16_f32 v121, v122, v123\n"
    "v_cvt_pk_bf16_f32 v122, v124, v125\n"
    "ds_write_b128 v242, v[112:115] offset:26688\n"
    "v_mfma_f32_16x16x32_bf16 v[112:115], a[8:11], v[184:187], 0\n"
    "s_waitcnt lgkmcnt(7)\n"
    "v_cvt_pk_bf16_f32 v123, v126, v127\n"
    "v_add_u32_e32 v254, s54, v242\n"
    "v_cvt_pk_bf16_f32 v136, v136, v137\n"
    "s_waitcnt lgkmcnt(6)\n"
    "v_smfmac_f32_16x16x64_bf16 v[80:83], v[216:219], v[208:215], v240\n"
    "s_waitcnt lgkmcnt(5)\n"
    "ds_read_b128 v[216:219], v251 offset:23520\n"
    "ds_write_b128 v242, v[128:131] offset:28736\n"
    "v_smfmac_f32_16x16x64_bf16 v[84:87], v[220:223], v[208:215], v240 abid:1\n"
    "v_pk_fma_f16 v232, v192, v192, s48\n"
    "v_mfma_f32_16x16x32_bf16 v[128:131], v[236:239], v[184:187], 0\n"
    "v_cvt_pk_bf16_f32 v137, v138, v139\n"
    "v_cvt_pk_bf16_f32 v138, v140, v141\n"
    "v_or_b32_e64 v195, v195, v203\n"
    "ds_read_b128 v[208:211], v250 offset:240\n"
    "ds_read_b128 v[212:215], v250 offset:560\n"
    "v_pk_add_f32 v[224:225], v[224:225], v[80:81]\n"
    "v_pk_add_f32 v[226:227], v[226:227], v[82:83]\n"
    "v_pk_fma_f32 v[228:229], v[80:81], v[80:81], v[228:229]\n"
    "v_pk_fma_f32 v[230:231], v[82:83], v[82:83], v[230:231]\n"
    "v_pk_add_f32 v[224:225], v[224:225], v[84:85]\n"
    "v_pk_add_f32 v[226:227], v[226:227], v[86:87]\n"
    "v_pk_fma_f32 v[228:229], v[84:85], v[84:85], v[228:229]\n"
    "v_pk_fma_f32 v[230:231], v[86:87], v[86:87], v[230:231]\n"
    "v_mfma_f32_16x16x32_bf16 v[124:127], a[12:15], v[188:191], 0\n"
    "s_waitcnt lgkmcnt(6)\n"
    "ds_read_b128 v[220:223], v251 offset:23856\n"
    "ds_write_b128 v242, v[104:107] offset:25664\n"
    "v_mfma_f32_16x16x32_bf16 v[104:107], a[0:3], v[188:191], 0\n"
    "s_waitcnt lgkmcnt(6)\n"
    "v_cvt_pk_bf16_f32 v139, v142, v143\n"
    "v_pk_mul_f16 v232, v192, v232\n"
    "v_or_b32_e64 v197, v197, v205\n"
    "v_exp_f16_e64 v234, v232\n"
    "v_mfma_f32_16x16x32_bf16 v[140:143], a[244:247], v[188:191], 0\n"
    "ds_write_b128 v242, v[120:123] offset:27712\n"
    "v_mfma_f32_16x16x32_bf16 v[120:123], a[8:11], v[188:191], 0\n"
    "v_exp_f16_sdwa v234, v232 dst_sel:WORD_1 dst_unused:UNUSED_PRESERVE src0_sel:WORD_1\n"
    "v_cvt_pk_f16_f32 v184, v168, v169\n"
    "v_pk_fma_f16 v234, v234, s49, s49\n"
    "v_lshlrev_b32_sdwa v185, 1, v170 dst_sel:DWORD dst_unused:UNUSED_PAD src0_sel:DWORD src1_sel:WORD_1\n"
    "s_waitcnt lgkmcnt(3)\n"
    "v_smfmac_f32_16x16x64_bf16 v[88:91], v[216:219], v[208:215], v240\n"
    "ds_read_b128 v[216:219], v252 offset:23520\n"
    "ds_write_b128 v242, v[136:139] offset:29760\n"
    "s_waitcnt lgkmcnt(3)\n"
    "v_smfmac_f32_16x16x64_bf16 v[92:95], v[220:223], v[208:215], v240 abid:1\n"
    "v_lshlrev_b32_sdwa v201, 1, v171 dst_sel:DWORD dst_unused:UNUSED_PAD src0_sel:DWORD src1_sel:WORD_1\n"
    "v_mfma_f32_16x16x32_bf16 v[136:139], v[236:239], v[188:191], 0\n"
    "v_lshlrev_b32_sdwa v186, 1, v172 dst_sel:DWORD dst_unused:UNUSED_PAD src0_sel:DWORD src1_sel:WORD_1\n"
    "v_lshlrev_b32_sdwa v202, 1, v173 dst_sel:DWORD dst_unused:UNUSED_PAD src0_sel:DWORD src1_sel:WORD_1\n"
    "v_mfma_f32_16x16x32_bf16 v[168:171], a[152:155], v[144:147], 0\n"
    "ds_read_b128 v[208:211], v250 offset:880\n"
    "ds_read_b128 v[212:215], v250 offset:1200\n"
    "v_mfma_f32_16x16x32_bf16 v[168:171], a[200:203], v[148:151], v[168:171]\n"
    "ds_read_b128 v[220:223], v252 offset:23856\n"
    "ds_read_u16 v185, v185\n"
    "v_lshlrev_b32_sdwa v187, 1, v174 dst_sel:DWORD dst_unused:UNUSED_PAD src0_sel:DWORD src1_sel:WORD_1\n"
    "v_lshlrev_b32_sdwa v203, 1, v175 dst_sel:DWORD dst_unused:UNUSED_PAD src0_sel:DWORD src1_sel:WORD_1\n"
    "v_rcp_f16_e64 v232, v234\n"
    "v_mfma_f32_16x16x32_bf16 v[168:171], a[112:115], v[152:155], v[168:171]\n"
    "ds_read_u16_d16_hi v201, v201\n"
    "ds_read_u16 v186, v186\n"
    "v_or_b32_e64 v198, v198, v206\n"
    "v_rcp_f16_sdwa v232, v234 dst_sel:WORD_1 dst_unused:UNUSED_PRESERVE src0_sel:WORD_1\n"
    "v_mfma_f32_16x16x32_bf16 v[172:175], a[156:159], v[144:147], 0\n"
    "ds_read_u16_d16_hi v202, v202\n"
    "v_cvt_pk_f16_f32 v188, v176, v177\n"
    "v_mfma_f32_16x16x32_bf16 v[172:175], a[204:207], v[148:151], v[172:175]\n"
    "v_lshlrev_b32_sdwa v189, 1, v178 dst_sel:DWORD dst_unused:UNUSED_PAD src0_sel:DWORD src1_sel:WORD_1\n"
    "v_lshlrev_b32_sdwa v205, 1, v179 dst_sel:DWORD dst_unused:UNUSED_PAD src0_sel:DWORD src1_sel:WORD_1\n"
    "v_lshlrev_b32_sdwa v190, 1, v180 dst_sel:DWORD dst_unused:UNUSED_PAD src0_sel:DWORD src1_sel:WORD_1\n"
    "v_lshlrev_b32_sdwa v206, 1, v181 dst_sel:DWORD dst_unused:UNUSED_PAD src0_sel:DWORD src1_sel:WORD_1\n"
    "v_lshlrev_b32_sdwa v191, 1, v182 dst_sel:DWORD dst_unused:UNUSED_PAD src0_sel:DWORD src1_sel:WORD_1\n"
    "v_fma_mix_f32 v234, v192, v232, 0 op_sel_hi:[1,1,0]\n"
    "v_mfma_f32_16x16x32_bf16 v[172:175], a[116:119], v[152:155], v[172:175]\n"
    "ds_read_b128 v[236:239], v244 offset:4096\n"
    "ds_read_u16 v187, v187\n"
    "v_or_b32_e64 v199, v199, v207\n"
    "v_lshlrev_b32_sdwa v207, 1, v183 dst_sel:DWORD dst_unused:UNUSED_PAD src0_sel:DWORD src1_sel:WORD_1\n"
    "v_fma_mix_f32 v232, v192, v232, 0 op_sel:[1,1,0] op_sel_hi:[1,1,0]\n"
    "s_waitcnt lgkmcnt(7)\n"
    "v_smfmac_f32_16x16x64_bf16 v[88:91], v[216:219], v[208:215], v240\n"
    "s_waitcnt lgkmcnt(6)\n"
    "ds_read_u16_d16_hi v203, v203\n"
    "v_smfmac_f32_16x16x64_bf16 v[92:95], v[220:223], v[208:215], v240 abid:1\n"
    "ds_read_b128 a[244:247], v244 offset:6144\n"
    "v_mfma_f32_16x16x32_bf16 v[176:179], a[152:155], v[156:159], 0\n"
    "ds_read_u16 v189, v189\n"
    "v_cvt_pk_bf16_f32 v192, v234, v232\n"
    "v_mfma_f32_16x16x32_bf16 v[176:179], a[200:203], v[160:163], v[176:179]\n"
    "v_pk_fma_f16 v232, v196, v196, s48\n"
    "v_lshlrev_b32_sdwa v200, 1, v169 dst_sel:DWORD dst_unused:UNUSED_PAD src0_sel:DWORD src1_sel:WORD_1\n"
    "v_pk_mul_f16 v232, v196, v232\n"
    "v_exp_f16_e64 v234, v232\n"
    "v_pk_add_f32 v[224:225], v[224:225], v[88:89]\n"
    "v_pk_add_f32 v[226:227], v[226:227], v[90:91]\n"
    "v_pk_fma_f32 v[228:229], v[88:89], v[88:89], v[228:229]\n"
    "v_pk_fma_f32 v[230:231], v[90:91], v[90:91], v[230:231]\n"
    "v_pk_add_f32 v[224:225], v[224:225], v[92:93]\n"
    "v_pk_add_f32 v[226:227], v[226:227], v[94:95]\n"
    "v_pk_fma_f32 v[228:229], v[92:93], v[92:93], v[228:229]\n"
    "v_pk_fma_f32 v[230:231], v[94:95], v[94:95], v[230:231]\n"
    "v_mfma_f32_16x16x32_bf16 v[176:179], a[112:115], v[164:167], v[176:179]\n"
    "s_waitcnt lgkmcnt(6)\n"
    "ds_read_u16_d16_hi v205, v205\n"
    "ds_write_b128 v254, v[224:227]\n"
    "v_mfma_f32_16x16x32_bf16 v[180:183], a[156:159], v[156:159], 0\n"
    "s_waitcnt lgkmcnt(6)\n"
    "v_exp_f16_sdwa v234, v232 dst_sel:WORD_1 dst_unused:UNUSED_PRESERVE src0_sel:WORD_1\n"
    "v_mfma_f32_16x16x32_bf16 v[180:183], a[204:207], v[160:163], v[180:183]\n"
    "v_pk_fma_f16 v234, v234, s49, s49\n"
    "v_or_b32_e64 v185, v185, v201\n"
    "v_mfma_f32_16x16x32_bf16 v[180:183], a[116:119], v[164:167], v[180:183]\n"
    "ds_read_u16 v190, v190\n"
    "ds_write_b128 v254, v[228:231] offset:1024\n"
    "v_mfma_f32_16x16x32_bf16 v[96:99], a[16:19], v[192:195], v[96:99]\n"
    "v_lshrrev_b32_e64 v254, 2, v242\n"
    "v_rcp_f16_e64 v232, v234\n"
    "v_mfma_f32_16x16x32_bf16 v[100:103], a[20:23], v[192:195], v[100:103]\n"
    "s_waitcnt lgkmcnt(5)\n"
    "v_add_u32_e32 v254, s55, v254\n"
    "v_rcp_f16_sdwa v232, v234 dst_sel:WORD_1 dst_unused:UNUSED_PRESERVE src0_sel:WORD_1\n"
    "v_mfma_f32_16x16x32_bf16 v[112:115], a[24:27], v[192:195], v[112:115]\n"
    "ds_read_u16_d16_hi v206, v206\n"
    "ds_read_u16 v191, v191\n"
    "v_mfma_f32_16x16x32_bf16 v[116:119], a[28:31], v[192:195], v[116:119]\n"
    "ds_read_u16_d16_hi v207, v207\n"
    "v_fma_mix_f32 v234, v196, v232, 0 op_sel_hi:[1,1,0]\n"
    "v_mfma_f32_16x16x32_bf16 v[128:131], v[236:239], v[192:195], v[128:131]\n"
    "ds_read_b128 a[240:243], v244 offset:34816\n"
    "v_fma_mix_f32 v232, v196, v232, 0 op_sel:[1,1,0] op_sel_hi:[1,1,0]\n"
    "v_mfma_f32_16x16x32_bf16 v[132:135], a[244:247], v[192:195], v[132:135]\n"
    "s_waitcnt lgkmcnt(6)\n"
    "v_or_b32_e32 v186, v186, v202\n"
    "v_cvt_pk_bf16_f32 v196, v234, v232\n"
    "v_pk_fma_f16 v232, v184, v184, s48\n"
    "v_or_b32_e64 v187, v187, v203\n"
    "v_mfma_f32_16x16x32_bf16 v[104:107], a[16:19], v[196:199], v[104:107]\n"
    "ds_read_u16_d16_hi v200, v200\n"
    "v_pk_mul_f16 v232, v184, v232\n"
    "v_mfma_f32_16x16x32_bf16 v[108:111], a[20:23], v[196:199], v[108:111]\n"
    "v_exp_f16_e64 v234, v232\n"
    "s_waitcnt lgkmcnt(5)\n"
    "s_barrier\n"
    "v_mfma_f32_16x16x32_bf16 v[120:123], a[24:27], v[196:199], v[120:123]\n"
    "ds_read_b32 v224, v254 offset:0\n"
    "ds_read_b32 v225, v254 offset:6752\n"
    "v_mfma_f32_16x16x32_bf16 v[124:127], a[28:31], v[196:199], v[124:127]\n"
    "s_nop 0\n"
    "ds_read_b32 v226, v254 offset:13504\n"
    "ds_read_b32 v227, v254 offset:20256\n"
    "v_mfma_f32_16x16x32_bf16 v[136:139], v[236:239], v[196:199], v[136:139]\n"
    "s_waitcnt lgkmcnt(8)\n"
    "ds_read_b32 v228, v254 offset:1024\n"
    "ds_read_b32 v229, v254 offset:7776\n"
    "v_mfma_f32_16x16x32_bf16 v[140:143], a[244:247], v[196:199], v[140:143]\n"
    "ds_read_b32 v230, v254 offset:14528\n"
    "ds_read_b32 v231, v254 offset:21280\n"
    "s_waitcnt vmcnt(2)\n"
    "v_smfmac_f32_32x32x32_bf16 v[112:127], a[248:251], a[232:239], v241\n"
    "v_exp_f16_sdwa v234, v232 dst_sel:WORD_1 dst_unused:UNUSED_PRESERVE src0_sel:WORD_1\n"
    "v_or_b32_e32 v189, v189, v205\n"
    "v_pk_fma_f16 v234, v234, s49, s49\n"
    "v_lshlrev_b32_sdwa v192, 1, v168 dst_sel:DWORD dst_unused:UNUSED_PAD src0_sel:DWORD src1_sel:WORD_1\n"
    "s_waitcnt vmcnt(1)\n"
    "v_smfmac_f32_32x32x32_bf16 v[96:111], a[248:251], a[224:231], v241\n"
    "s_waitcnt lgkmcnt(9)\n"
    "ds_read_u16 v192, v192\n"
    "ds_read_b128 v[236:239], v244 offset:8192\n"
    "v_lshlrev_b32_sdwa v193, 1, v170 dst_sel:DWORD dst_unused:UNUSED_PAD src0_sel:DWORD src1_sel:WORD_1\n"
    "v_lshlrev_b32_sdwa v201, 1, v171 dst_sel:DWORD dst_unused:UNUSED_PAD src0_sel:DWORD src1_sel:WORD_1\n"
    "v_lshlrev_b32_sdwa v194, 1, v172 dst_sel:DWORD dst_unused:UNUSED_PAD src0_sel:DWORD src1_sel:WORD_1\n"
    "v_mfma_f32_16x16x32_bf16 v[168:171], a[160:163], v[144:147], 0\n"
    "s_waitcnt lgkmcnt(7)\n"
    "ds_read_u16 v193, v193\n"
    "ds_read_u16_d16_hi v201, v201\n"
    "v_mfma_f32_16x16x32_bf16 v[168:171], a[208:211], v[148:151], v[168:171]\n"
    "s_waitcnt lgkmcnt(7)\n"
    "ds_read_u16 v194, v194\n"
    "buffer_load_dwordx4 a[224:227], v245, s[40:43], s58 offen offset:128\n"
    "v_add_f32_e64 v224, v224, v225\n"
    "v_add_f32_e32 v226, v226, v227\n"
    "s_waitcnt lgkmcnt(6)\n"
    "v_add_f32_e32 v228, v228, v229\n"
    "s_waitcnt lgkmcnt(5)\n"
    "v_add_f32_e64 v230, v230, v231\n"
    "v_add_f32_e64 v224, v224, v226\n"
    "v_add_f32_e64 v228, v228, v230\n"
    "v_mul_f32_e32 v224, 0x3c2aaaab, v224\n"
    "v_mul_f32_e32 v228, 0x3c2aaaab, v228\n"
    "v_lshlrev_b32_sdwa v202, 1, v173 dst_sel:DWORD dst_unused:UNUSED_PAD src0_sel:DWORD src1_sel:WORD_1\n"
    "v_fma_f32 v228, -v224, v224, v228\n"
    "v_lshlrev_b32_sdwa v195, 1, v174 dst_sel:DWORD dst_unused:UNUSED_PAD src0_sel:DWORD src1_sel:WORD_1\n"
    "v_max_f32_e64 v228, 0, v228\n"
    "v_lshlrev_b32_sdwa v203, 1, v175 dst_sel:DWORD dst_unused:UNUSED_PAD src0_sel:DWORD src1_sel:WORD_1\n"
    "v_add_f32_e32 v228, 0x3727c5ac, v228\n"
    "v_rcp_f16_e64 v232, v234\n"
    "v_mfma_f32_16x16x32_bf16 v[168:171], a[120:123], v[152:155], v[168:171]\n"
    "ds_read_b128 a[232:235], v243 offset:24640\n"
    "ds_read_u16_d16_hi v202, v202\n"
    "v_rsq_f32_e64 v228, v228\n"
    "v_or_b32_e64 v190, v190, v206\n"
    "v_mul_f32_e64 v228, s50, v228\n"
    "ds_write_b32 v254, v228\n"
    "ds_read_b128 a[236:239], v243 offset:25664\n"
    "v_mul_f32_e64 v224, v228, -v224\n"
    "ds_write_b32 v254, v224 offset:1024\n"
    "ds_read_u16 v195, v195\n"
    "v_add_u32_e64 v254, s56, v242\n"
    "v_rcp_f16_sdwa v232, v234 dst_sel:WORD_1 dst_unused:UNUSED_PRESERVE src0_sel:WORD_1\n"
    "v_mfma_f32_16x16x32_bf16 v[172:175], a[164:167], v[144:147], 0\n"
    "ds_read_b128 a[244:247], v244 offset:10240\n"
    "buffer_load_dwordx4 a[228:231], v245, s[40:43], s89 offen offset:128\n"
    "v_mfma_f32_16x16x32_bf16 v[172:175], a[212:215], v[148:151], v[172:175]\n"
    "ds_read_u16_d16_hi v203, v203\n"
    "v_lshlrev_b32_sdwa v196, 1, v176 dst_sel:DWORD dst_unused:UNUSED_PAD src0_sel:DWORD src1_sel:WORD_1\n"
    "v_lshlrev_b32_sdwa v204, 1, v177 dst_sel:DWORD dst_unused:UNUSED_PAD src0_sel:DWORD src1_sel:WORD_1\n"
    "v_lshlrev_b32_sdwa v197, 1, v178 dst_sel:DWORD dst_unused:UNUSED_PAD src0_sel:DWORD src1_sel:WORD_1\n"
    "v_lshlrev_b32_sdwa v205, 1, v179 dst_sel:DWORD dst_unused:UNUSED_PAD src0_sel:DWORD src1_sel:WORD_1\n"
    "v_lshlrev_b32_sdwa v198, 1, v180 dst_sel:DWORD dst_unused:UNUSED_PAD src0_sel:DWORD src1_sel:WORD_1\n"
    "v_lshlrev_b32_sdwa v206, 1, v181 dst_sel:DWORD dst_unused:UNUSED_PAD src0_sel:DWORD src1_sel:WORD_1\n"
    "v_lshlrev_b32_sdwa v199, 1, v182 dst_sel:DWORD dst_unused:UNUSED_PAD src0_sel:DWORD src1_sel:WORD_1\n"
    "v_fma_mix_f32 v234, v184, v232, 0 op_sel_hi:[1,1,0]\n"
    "v_mfma_f32_16x16x32_bf16 v[172:175], a[240:243], v[152:155], v[172:175]\n"
    "s_waitcnt lgkmcnt(7)\n"
    "ds_read_u16 v196, v196\n"
    "ds_read_u16_d16_hi v204, v204\n"
    "v_or_b32_e32 v191, v191, v207\n"
    "v_lshlrev_b32_sdwa v207, 1, v183 dst_sel:DWORD dst_unused:UNUSED_PAD src0_sel:DWORD src1_sel:WORD_1\n"
    "v_fma_mix_f32 v232, v184, v232, 0 op_sel:[1,1,0] op_sel_hi:[1,1,0]\n"
    "v_mfma_f32_16x16x32_bf16 v[176:179], a[160:163], v[156:159], 0\n"
    "s_waitcnt lgkmcnt(5)\n"
    "ds_read_u16 v197, v197\n"
    "v_cvt_pk_bf16_f32 v184, v234, v232\n"
    "s_barrier\n"
    "v_mfma_f32_16x16x32_bf16 v[176:179], a[208:211], v[160:163], v[176:179]\n"
    "ds_read_b128 v[224:227], v254\n"
    "ds_read_b128 v[228:231], v254 offset:1024\n"
    "v_smfmac_f32_32x32x32_bf16 v[96:111], a[248:251], a[232:239], v241\n"
    "ds_read_u16_d16_hi v205, v205\n"
    "v_lshlrev_b32_e64 v254, 3, v244\n"
    "v_pk_fma_f16 v232, v188, v188, s48\n"
    "v_or_b32_e64 v192, v192, v200\n"
    "v_pk_mul_f16 v232, v188, v232\n"
    "v_mfma_f32_16x16x32_bf16 v[176:179], a[120:123], v[164:167], v[176:179]\n"
    "s_mov_b32 exec_hi, 0\n"
    "buffer_load_dword v233, v254, s[72:75], s53 offen sc1\n"
    "s_mov_b32 exec_hi, -1\n"
    "v_mfma_f32_16x16x32_bf16 v[180:183], a[164:167], v[156:159], 0\n"
    "s_add_u32 s53, s53, 393216\n"
    "ds_read_u16 v198, v198\n"
    "ds_read_u16_d16_hi v206, v206\n"
    "v_mfma_f32_16x16x32_bf16 v[180:183], a[212:215], v[160:163], v[180:183]\n"
    "v_exp_f16_e64 v234, v232\n"
    "v_mfma_f32_16x16x32_bf16 v[180:183], a[240:243], v[164:167], v[180:183]\n"
    "ds_read_u16 v199, v199\n"
    "v_exp_f16_sdwa v234, v232 dst_sel:WORD_1 dst_unused:UNUSED_PRESERVE src0_sel:WORD_1\n"
    "v_mfma_f32_16x16x32_bf16 v[96:99], a[32:35], v[184:187], v[96:99]\n"
    "ds_read_u16_d16_hi v207, v207\n"
    "ds_read_b128 a[232:235], v243 offset:26688\n"
    "v_pk_fma_f16 v234, v234, s49, s49\n"
    "v_mfma_f32_16x16x32_bf16 v[100:103], a[36:39], v[184:187], v[100:103]\n"
    "s_waitcnt lgkmcnt(9)\n"
    "ds_read_b128 a[236:239], v243 offset:27712\n"
    "v_rcp_f16_e32 v232, v234\n"
    "v_mfma_f32_16x16x32_bf16 v[112:115], a[40:43], v[184:187], v[112:115]\n"
    "s_waitcnt lgkmcnt(7)\n"
    "v_rcp_f16_sdwa v232, v234 dst_sel:WORD_1 dst_unused:UNUSED_PRESERVE src0_sel:WORD_1\n"
    "v_or_b32_e32 v193, v193, v201\n"
    "v_fma_mix_f32 v234, v188, v232, 0 op_sel_hi:[1,1,0]\n"
    "v_fma_mix_f32 v232, v188, v232, 0 op_sel:[1,1,0] op_sel_hi:[1,1,0]\n"
    "v_pk_fma_f32 v[64:65], v[64:65], v[224:225], v[228:229]\n"
    "v_pk_fma_f32 v[66:67], v[66:67], v[226:227], v[230:231]\n"
    "v_pk_fma_f32 v[68:69], v[68:69], v[224:225], v[228:229]\n"
    "v_pk_fma_f32 v[70:71], v[70:71], v[226:227], v[230:231]\n"
    "v_pk_fma_f32 v[72:73], v[72:73], v[224:225], v[228:229]\n"
    "v_pk_fma_f32 v[74:75], v[74:75], v[226:227], v[230:231]\n"
    "v_pk_fma_f32 v[76:77], v[76:77], v[224:225], v[228:229]\n"
    "v_pk_fma_f32 v[78:79], v[78:79], v[226:227], v[230:231]\n"
    "v_pk_fma_f32 v[80:81], v[80:81], v[224:225], v[228:229]\n"
    "v_pk_fma_f32 v[82:83], v[82:83], v[226:227], v[230:231]\n"
    "v_pk_fma_f32 v[84:85], v[84:85], v[224:225], v[228:229]\n"
    "v_pk_fma_f32 v[86:87], v[86:87], v[226:227], v[230:231]\n"
    "v_pk_fma_f32 v[88:89], v[88:89], v[224:225], v[228:229]\n"
    "v_pk_fma_f32 v[90:91], v[90:91], v[226:227], v[230:231]\n"
    "v_pk_fma_f32 v[92:93], v[92:93], v[224:225], v[228:229]\n"
    "v_pk_fma_f32 v[94:95], v[94:95], v[226:227], v[230:231]\n"
    "v_pk_fma_f32 v[0:1], v[0:1], v[224:225], v[228:229]\n"
    "v_pk_fma_f32 v[2:3], v[2:3], v[226:227], v[230:231]\n"
    "v_pk_fma_f32 v[4:5], v[4:5], v[224:225], v[228:229]\n"
    "v_pk_fma_f32 v[6:7], v[6:7], v[226:227], v[230:231]\n"
    "v_pk_fma_f32 v[8:9], v[8:9], v[224:225], v[228:229]\n"
    "v_pk_fma_f32 v[10:11], v[10:11], v[226:227], v[230:231]\n"
    "v_pk_fma_f32 v[12:13], v[12:13], v[224:225], v[228:229]\n"
    "v_pk_fma_f32 v[14:15], v[14:15], v[226:227], v[230:231]\n"
    "v_pk_fma_f32 v[16:17], v[16:17], v[224:225], v[228:229]\n"
    "v_pk_fma_f32 v[18:19], v[18:19], v[226:227], v[230:231]\n"
    "v_pk_fma_f32 v[20:21], v[20:21], v[224:225], v[228:229]\n"
    "v_pk_fma_f32 v[22:23], v[22:23], v[226:227], v[230:231]\n"
    "v_pk_fma_f32 v[24:25], v[24:25], v[224:225], v[228:229]\n"
    "v_pk_fma_f32 v[26:27], v[26:27], v[226:227], v[230:231]\n"
    "v_pk_fma_f32 v[28:29], v[28:29], v[224:225], v[228:229]\n"
    "v_pk_fma_f32 v[30:31], v[30:31], v[226:227], v[230:231]\n"
    "v_pk_fma_f32 v[32:33], v[32:33], v[224:225], v[228:229]\n"
    "v_pk_fma_f32 v[34:35], v[34:35], v[226:227], v[230:231]\n"
    "v_pk_fma_f32 v[36:37], v[36:37], v[224:225], v[228:229]\n"
    "v_pk_fma_f32 v[38:39], v[38:39], v[226:227], v[230:231]\n"
    "v_pk_fma_f32 v[40:41], v[40:41], v[224:225], v[228:229]\n"
    "v_pk_fma_f32 v[42:43], v[42:43], v[226:227], v[230:231]\n"
    "v_pk_fma_f32 v[44:45], v[44:45], v[224:225], v[228:229]\n"
    "v_pk_fma_f32 v[46:47], v[46:47], v[226:227], v[230:231]\n"
    "v_pk_fma_f32 v[48:49], v[48:49], v[224:225], v[228:229]\n"
    "v_pk_fma_f32 v[50:51], v[50:51], v[226:227], v[230:231]\n"
    "v_pk_fma_f32 v[52:53], v[52:53], v[224:225], v[228:229]\n"
    "v_pk_fma_f32 v[54:55], v[54:55], v[226:227], v[230:231]\n"
    "v_pk_fma_f32 v[56:57], v[56:57], v[224:225], v[228:229]\n"
    "v_pk_fma_f32 v[58:59], v[58:59], v[226:227], v[230:231]\n"
    "v_pk_fma_f32 v[60:61], v[60:61], v[224:225], v[228:229]\n"
    "v_pk_fma_f32 v[62:63], v[62:63], v[226:227], v[230:231]\n"
    "v_mfma_f32_16x16x32_bf16 v[116:119], a[44:47], v[184:187], v[116:119]\n"
    "ds_read_b128 a[240:243], v244 offset:38912\n"
    "v_cvt_pk_bf16_f32 v208, v64, v68\n"
    "v_mfma_f32_16x16x32_bf16 v[128:131], v[236:239], v[184:187], v[128:131]\n"
    "v_cvt_pk_bf16_f32 v212, v65, v69\n"
    "v_cvt_pk_bf16_f32 v216, v66, v70\n"
    "v_mfma_f32_16x16x32_bf16 v[132:135], a[244:247], v[184:187], v[132:135]\n"
    "v_cvt_pk_bf16_f32 v220, v67, v71\n"
    "v_cvt_pk_bf16_f32 v209, v72, v76\n"
    "v_cvt_pk_bf16_f32 v213, v73, v77\n"
    "v_cvt_pk_bf16_f32 v217, v74, v78\n"
    "v_cvt_pk_bf16_f32 v221, v75, v79\n"
    "v_cvt_pk_bf16_f32 v210, v80, v84\n"
    "v_cvt_pk_bf16_f32 v214, v81, v85\n"
    "v_cvt_pk_bf16_f32 v218, v82, v86\n"
    "v_cvt_pk_bf16_f32 v222, v83, v87\n"
    "v_cvt_pk_bf16_f32 v211, v88, v92\n"
    "v_cvt_pk_bf16_f32 v215, v89, v93\n"
    "v_cvt_pk_bf16_f32 v219, v90, v94\n"
    "v_cvt_pk_bf16_f32 v223, v91, v95\n"
    "v_cvt_pk_bf16_f32 v80, v0, v4\n"
    "v_cvt_pk_bf16_f32 v84, v1, v5\n"
    "v_cvt_pk_bf16_f32 v188, v234, v232\n"
    "ds_write_b128 v253, v[208:211] offset:2048\n"
    "v_cvt_pk_bf16_f32 v88, v2, v6\n"
    "v_mfma_f32_16x16x32_bf16 v[104:107], a[32:35], v[188:191], v[104:107]\n"
    "s_waitcnt lgkmcnt(1)\n"
    "v_cvt_pk_bf16_f32 v92, v3, v7\n"
    "v_cvt_pk_bf16_f32 v81, v8, v12\n"
    "v_mfma_f32_16x16x32_bf16 v[108:111], a[36:39], v[188:191], v[108:111]\n"
    "s_nop 0\n"
    "ds_write_b128 v253, v[212:215] offset:5120\n"
    "v_mfma_f32_16x16x32_bf16 v[120:123], a[40:43], v[188:191], v[120:123]\n"
    "v_cvt_pk_bf16_f32 v85, v9, v13\n"
    "v_cvt_pk_bf16_f32 v89, v10, v14\n"
    "v_mfma_f32_16x16x32_bf16 v[124:127], a[44:47], v[188:191], v[124:127]\n"
    "v_cvt_pk_bf16_f32 v93, v11, v15\n"
    "v_cvt_pk_bf16_f32 v82, v16, v20\n"
    "v_mfma_f32_16x16x32_bf16 v[136:139], v[236:239], v[188:191], v[136:139]\n"
    "ds_read_b128 v[236:239], v244 offset:12288\n"
    "v_cvt_pk_bf16_f32 v86, v17, v21\n"
    "v_mfma_f32_16x16x32_bf16 v[140:143], a[244:247], v[188:191], v[140:143]\n"
    "v_cvt_pk_bf16_f32 v90, v18, v22\n"
    "v_cvt_pk_bf16_f32 v94, v19, v23\n"
    "v_smfmac_f32_32x32x32_bf16 v[112:127], a[248:251], a[232:239], v241\n"
    "v_cvt_pk_bf16_f32 v83, v24, v28\n"
    "v_cvt_pk_f16_f32 v184, v168, v169\n"
    "v_cvt_pk_bf16_f32 v87, v25, v29\n"
    "v_lshlrev_b32_sdwa v185, 1, v170 dst_sel:DWORD dst_unused:UNUSED_PAD src0_sel:DWORD src1_sel:WORD_1\n"
    "v_lshlrev_b32_sdwa v201, 1, v171 dst_sel:DWORD dst_unused:UNUSED_PAD src0_sel:DWORD src1_sel:WORD_1\n"
    "v_mfma_f32_16x16x32_bf16 v[168:171], a[168:171], v[144:147], 0\n"
    "ds_read_u16 v185, v185\n"
    "ds_write_b128 v253, v[80:83] offset:0\n"
    "v_lshlrev_b32_sdwa v186, 1, v172 dst_sel:DWORD dst_unused:UNUSED_PAD src0_sel:DWORD src1_sel:WORD_1\n"
    "v_cvt_pk_bf16_f32 v91, v26, v30\n"
    "v_or_b32_e64 v194, v194, v202\n"
    "v_mfma_f32_16x16x32_bf16 v[168:171], a[216:219], v[148:151], v[168:171]\n"
    "v_lshlrev_b32_sdwa v202, 1, v173 dst_sel:DWORD dst_unused:UNUSED_PAD src0_sel:DWORD src1_sel:WORD_1\n"
    "v_lshlrev_b32_sdwa v187, 1, v174 dst_sel:DWORD dst_unused:UNUSED_PAD src0_sel:DWORD src1_sel:WORD_1\n"
    "v_mfma_f32_16x16x32_bf16 v[168:171], a[124:127], v[152:155], v[168:171]\n"
    "ds_read_u16_d16_hi v201, v201\n"
    "ds_write_b128 v253, v[84:87] offset:3072\n"
    "v_cvt_pk_bf16_f32 v95, v27, v31\n"
    "v_cvt_pk_f16_f32 v188, v176, v177\n"
    "v_lshlrev_b32_sdwa v189, 1, v178 dst_sel:DWORD dst_unused:UNUSED_PAD src0_sel:DWORD src1_sel:WORD_1\n"
    "v_lshlrev_b32_sdwa v190, 1, v180 dst_sel:DWORD dst_unused:UNUSED_PAD src0_sel:DWORD src1_sel:WORD_1\n"
    "v_lshlrev_b32_sdwa v191, 1, v182 dst_sel:DWORD dst_unused:UNUSED_PAD src0_sel:DWORD src1_sel:WORD_1\n"
    "v_or_b32_e64 v195, v195, v203\n"
    "v_lshlrev_b32_sdwa v203, 1, v175 dst_sel:DWORD dst_unused:UNUSED_PAD src0_sel:DWORD src1_sel:WORD_1\n"
    "v_mfma_f32_16x16x32_bf16 v[172:175], a[172:175], v[144:147], 0\n"
    "ds_read_u16 v186, v186\n"
    "ds_read_u16_d16_hi v202, v202\n"
    "v_mfma_f32_16x16x32_bf16 v[172:175], a[220:223], v[148:151], v[172:175]\n"
    "ds_read_u16 v187, v187\n"
    "ds_read_u16_d16_hi v203, v203\n"
    "v_mfma_f32_16x16x32_bf16 v[172:175], a[240:243], v[152:155], v[172:175]\n"
    "v_cvt_pk_bf16_f32 v64, v32, v36\n"
    "v_cvt_pk_bf16_f32 v68, v33, v37\n"
    "v_cvt_pk_bf16_f32 v72, v34, v38\n"
    "v_cvt_pk_bf16_f32 v76, v35, v39\n"
    "v_cvt_pk_bf16_f32 v65, v40, v44\n"
    "v_or_b32_e64 v196, v196, v204\n"
    "v_cvt_pk_bf16_f32 v69, v41, v45\n"
    "v_cvt_pk_bf16_f32 v73, v42, v46\n"
    "v_cvt_pk_bf16_f32 v77, v43, v47\n"
    "v_cvt_pk_bf16_f32 v66, v48, v52\n"
    "v_cvt_pk_bf16_f32 v70, v49, v53\n"
    "v_or_b32_e64 v197, v197, v205\n"
    "v_lshlrev_b32_sdwa v205, 1, v179 dst_sel:DWORD dst_unused:UNUSED_PAD src0_sel:DWORD src1_sel:WORD_1\n"
    "v_mfma_f32_16x16x32_bf16 v[176:179], a[168:171], v[156:159], 0\n"
    "s_nop 0\n"
    "ds_read_b128 a[244:247], v244 offset:14336\n"
    "ds_read_u16 v189, v189\n"
    "v_mfma_f32_16x16x32_bf16 v[176:179], a[216:219], v[160:163], v[176:179]\n"
    "s_waitcnt lgkmcnt(6)\n"
    "ds_read_b128 a[232:235], v243 offset:28736\n"
    "ds_read_u16_d16_hi v205, v205\n"
    "v_mfma_f32_16x16x32_bf16 v[176:179], a[124:127], v[164:167], v[176:179]\n"
    "v_cvt_pk_bf16_f32 v74, v50, v54\n"
    "v_cvt_pk_bf16_f32 v78, v51, v55\n"
    "v_cvt_pk_bf16_f32 v67, v56, v60\n"
    "v_cvt_pk_bf16_f32 v71, v57, v61\n"
    "v_cvt_pk_bf16_f32 v75, v58, v62\n"
    "v_or_b32_e32 v198, v198, v206\n"
    "v_mfma_f32_16x16x32_bf16 v[96:99], a[48:51], v[192:195], v[96:99]\n"
    "ds_read_b128 a[236:239], v243 offset:29760\n"
    "ds_write_b128 v253, v[64:67] offset:1024\n"
    "v_mfma_f32_16x16x32_bf16 v[100:103], a[52:55], v[192:195], v[100:103]\n"
    "s_waitcnt lgkmcnt(8)\n"
    "v_lshlrev_b32_sdwa v206, 1, v181 dst_sel:DWORD dst_unused:UNUSED_PAD src0_sel:DWORD src1_sel:WORD_1\n"
    "v_cvt_pk_bf16_f32 v79, v59, v63\n"
    "v_mfma_f32_16x16x32_bf16 v[112:115], a[56:59], v[192:195], v[112:115]\n"
    "v_or_b32_e64 v199, v199, v207\n"
    "v_lshlrev_b32_sdwa v207, 1, v183 dst_sel:DWORD dst_unused:UNUSED_PAD src0_sel:DWORD src1_sel:WORD_1\n"
    "v_mfma_f32_16x16x32_bf16 v[180:183], a[172:175], v[156:159], 0\n"
    "ds_read_u16 v190, v190\n"
    "ds_write_b128 v253, v[68:71] offset:4096\n"
    "v_mfma_f32_16x16x32_bf16 v[180:183], a[220:223], v[160:163], v[180:183]\n"
    "s_waitcnt lgkmcnt(7)\n"
    "v_or_b32_e32 v185, v185, v201\n"
    "v_or_b32_e64 v186, v186, v202\n"
    "v_mfma_f32_16x16x32_bf16 v[180:183], a[240:243], v[164:167], v[180:183]\n"
    "v_pk_fma_f16 v232, v184, v184, s48\n"
    "v_or_b32_e64 v187, v187, v203\n"
    "v_mfma_f32_16x16x32_bf16 v[116:119], a[60:63], v[192:195], v[116:119]\n"
    "ds_read_u16_d16_hi v206, v206\n"
    "ds_read_u16 v191, v191\n"
    "v_mfma_f32_16x16x32_bf16 v[128:131], v[236:239], v[192:195], v[128:131]\n"
    "ds_read_u16_d16_hi v207, v207\n"
    "v_pk_mul_f16 v232, v184, v232\n"
    "v_mfma_f32_16x16x32_bf16 v[132:135], a[244:247], v[192:195], v[132:135]\n"
    "v_exp_f16_e32 v234, v232\n"
    "v_mfma_f32_16x16x32_bf16 v[104:107], a[48:51], v[196:199], v[104:107]\n"
    "s_waitcnt lgkmcnt(6)\n"
    "ds_read_b128 a[240:243], v244 offset:32768\n"
    "v_exp_f16_sdwa v234, v232 dst_sel:WORD_1 dst_unused:UNUSED_PRESERVE src0_sel:WORD_1\n"
    "v_mfma_f32_16x16x32_bf16 v[108:111], a[52:55], v[196:199], v[108:111]\n"
    "v_or_b32_e64 v189, v189, v205\n"
    "v_pk_fma_f16 v234, v234, s49, s49\n"
    "v_mfma_f32_16x16x32_bf16 v[120:123], a[56:59], v[196:199], v[120:123]\n"
    "v_cvt_pk_f16_f32 v192, v168, v169\n"
    "v_lshlrev_b32_sdwa v193, 1, v170 dst_sel:DWORD dst_unused:UNUSED_PAD src0_sel:DWORD src1_sel:WORD_1\n"
    "v_mfma_f32_16x16x32_bf16 v[124:127], a[60:63], v[196:199], v[124:127]\n"
    "ds_read_u16 v193, v193\n"
    "v_lshlrev_b32_sdwa v201, 1, v171 dst_sel:DWORD dst_unused:UNUSED_PAD src0_sel:DWORD src1_sel:WORD_1\n"
    "v_mfma_f32_16x16x32_bf16 v[136:139], v[236:239], v[196:199], v[136:139]\n"
    "s_nop 0\n"
    "ds_read_u16_d16_hi v201, v201\n"
    "ds_read_b128 v[236:239], v244 offset:16384\n"
    "v_mfma_f32_16x16x32_bf16 v[140:143], a[244:247], v[196:199], v[140:143]\n"
    "s_waitcnt lgkmcnt(7)\n"
    "v_lshlrev_b32_sdwa v194, 1, v172 dst_sel:DWORD dst_unused:UNUSED_PAD src0_sel:DWORD src1_sel:WORD_1\n"
    "v_lshlrev_b32_sdwa v202, 1, v173 dst_sel:DWORD dst_unused:UNUSED_PAD src0_sel:DWORD src1_sel:WORD_1\n"
    "v_lshlrev_b32_sdwa v195, 1, v174 dst_sel:DWORD dst_unused:UNUSED_PAD src0_sel:DWORD src1_sel:WORD_1\n"
    "v_lshlrev_b32_sdwa v203, 1, v175 dst_sel:DWORD dst_unused:UNUSED_PAD src0_sel:DWORD src1_sel:WORD_1\n"
    "v_rcp_f16_e64 v232, v234\n"
    "s_barrier\n"
    "v_smfmac_f32_32x32x32_bf16 v[128:143], a[248:251], a[232:239], v241\n"
    "s_waitcnt lgkmcnt(6)\n"
    "ds_read_b128 v[144:147], v242 offset:0\n"
    "ds_read_b128 v[148:151], v242 offset:1024\n"
    "v_or_b32_e64 v190, v190, v206\n"
    "v_mov_b32_e64 v64, v216\n"
    "v_rcp_f16_sdwa v232, v234 dst_sel:WORD_1 dst_unused:UNUSED_PRESERVE src0_sel:WORD_1\n"
    "v_cvt_pk_f16_f32 v196, v176, v177\n"
    "v_mov_b32_e64 v65, v217\n"
    "v_lshlrev_b32_sdwa v197, 1, v178 dst_sel:DWORD dst_unused:UNUSED_PAD src0_sel:DWORD src1_sel:WORD_1\n"
    "v_mov_b32_e64 v66, v218\n"
    "v_lshlrev_b32_sdwa v205, 1, v179 dst_sel:DWORD dst_unused:UNUSED_PAD src0_sel:DWORD src1_sel:WORD_1\n"
    "v_mov_b32_e64 v67, v219\n"
    "v_lshlrev_b32_sdwa v198, 1, v180 dst_sel:DWORD dst_unused:UNUSED_PAD src0_sel:DWORD src1_sel:WORD_1\n"
    "v_mov_b32_e64 v68, v220\n"
    "v_lshlrev_b32_sdwa v206, 1, v181 dst_sel:DWORD dst_unused:UNUSED_PAD src0_sel:DWORD src1_sel:WORD_1\n"
    "v_mov_b32_e64 v69, v221\n"
    "v_lshlrev_b32_sdwa v199, 1, v182 dst_sel:DWORD dst_unused:UNUSED_PAD src0_sel:DWORD src1_sel:WORD_1\n"
    "v_mov_b32_e64 v70, v222\n"
    "v_mov_b32_e64 v71, v223\n"
    "v_fma_mix_f32 v234, v184, v232, 0 op_sel_hi:[1,1,0]\n"
    "s_waitcnt lgkmcnt(5)\n"
    "v_or_b32_e32 v191, v191, v207\n"
    "v_lshlrev_b32_sdwa v207, 1, v183 dst_sel:DWORD dst_unused:UNUSED_PAD src0_sel:DWORD src1_sel:WORD_1\n"
    "v_fma_mix_f32 v232, v184, v232, 0 op_sel:[1,1,0] op_sel_hi:[1,1,0]\n"
    "v_cvt_pk_bf16_f32 v184, v234, v232\n"
    "ds_read_b128 v[152:155], v242 offset:2048\n"
    "ds_read_b128 v[156:159], v242 offset:3072\n"
    "v_pk_fma_f16 v232, v188, v188, s48\n"
    "s_waitcnt lgkmcnt(4)\n"
    "v_or_b32_e32 v193, v193, v201\n"
    "s_waitcnt lgkmcnt(3)\n"
    "v_mfma_f32_16x16x32_bf16 v[168:171], a[128:131], v[144:147], 0\n"
    "s_nop 0\n"
    "ds_read_b128 v[160:163], v242 offset:4096\n"
    "ds_read_b128 v[164:167], v242 offset:5120\n"
    "s_waitcnt lgkmcnt(4)\n"
    "v_mfma_f32_16x16x32_bf16 v[168:171], a[176:179], v[148:151], v[168:171]\n"
    "s_nop 0\n"
    "ds_read_u16 v194, v194\n"
    "ds_read_u16_d16_hi v202, v202\n"
    "v_mfma_f32_16x16x32_bf16 v[172:175], a[132:135], v[144:147], 0\n"
    "ds_read_u16 v195, v195\n"
    "v_pk_mul_f16 v232, v188, v232\n"
    "v_mfma_f32_16x16x32_bf16 v[172:175], a[180:183], v[148:151], v[172:175]\n"
    "ds_read_u16_d16_hi v203, v203\n"
    "v_exp_f16_e64 v234, v232\n"
    "ds_read_b128 a[244:247], v244 offset:18432\n"
    "v_exp_f16_sdwa v234, v232 dst_sel:WORD_1 dst_unused:UNUSED_PRESERVE src0_sel:WORD_1\n"
    "v_mfma_f32_16x16x32_bf16 v[96:99], a[64:67], v[184:187], v[96:99]\n"
    "s_waitcnt lgkmcnt(8)\n"
    "v_pk_fma_f16 v234, v234, s49, s49\n"
    "v_rcp_f16_e64 v232, v234\n"
    "v_mfma_f32_16x16x32_bf16 v[168:171], a[96:99], v[152:155], v[168:171]\n"
    "s_waitcnt lgkmcnt(7)\n"
    "ds_read_u16 v197, v197\n"
    "v_rcp_f16_sdwa v232, v234 dst_sel:WORD_1 dst_unused:UNUSED_PRESERVE src0_sel:WORD_1\n"
    "v_mfma_f32_16x16x32_bf16 v[172:175], a[240:243], v[152:155], v[172:175]\n"
    "s_nop 0\n"
    "ds_read_u16_d16_hi v205, v205\n"
    "v_fma_mix_f32 v234, v188, v232, 0 op_sel_hi:[1,1,0]\n"
    "v_mfma_f32_16x16x32_bf16 v[176:179], a[128:131], v[156:159], 0\n"
    "s_waitcnt lgkmcnt(7)\n"
    "v_fma_mix_f32 v232, v188, v232, 0 op_sel:[1,1,0] op_sel_hi:[1,1,0]\n"
    "v_cvt_pk_bf16_f32 v188, v234, v232\n"
    "v_mfma_f32_16x16x32_bf16 v[176:179], a[176:179], v[160:163], v[176:179]\n"
    "ds_read_u16 v198, v198\n"
    "v_pk_fma_f16 v232, v192, v192, s48\n"
    "v_mfma_f32_16x16x32_bf16 v[176:179], a[96:99], v[164:167], v[176:179]\n"
    "ds_read_u16_d16_hi v206, v206\n"
    "v_pk_mul_f16 v232, v192, v232\n"
    "v_mfma_f32_16x16x32_bf16 v[180:183], a[132:135], v[156:159], 0\n"
    "v_exp_f16_e64 v234, v232\n"
    "v_mfma_f32_16x16x32_bf16 v[180:183], a[180:183], v[160:163], v[180:183]\n"
    "ds_read_u16 v199, v199\n"
    "v_exp_f16_sdwa v234, v232 dst_sel:WORD_1 dst_unused:UNUSED_PRESERVE src0_sel:WORD_1\n"
    "v_mfma_f32_16x16x32_bf16 v[180:183], a[240:243], v[164:167], v[180:183]\n"
    "s_waitcnt lgkmcnt(8)\n"
    "v_or_b32_e32 v194, v194, v202\n"
    "v_pk_fma_f16 v234, v234, s49, s49\n"
    "v_mfma_f32_16x16x32_bf16 v[100:103], a[68:71], v[184:187], v[100:103]\n"
    "ds_read_u16_d16_hi v207, v207\n"
    "v_rcp_f16_e64 v232, v234\n"
    "v_mfma_f32_16x16x32_bf16 v[112:115], a[72:75], v[184:187], v[112:115]\n"
    "v_rcp_f16_sdwa v232, v234 dst_sel:WORD_1 dst_unused:UNUSED_PRESERVE src0_sel:WORD_1\n"
    "v_mfma_f32_16x16x32_bf16 v[116:119], a[76:79], v[184:187], v[116:119]\n"
    "s_waitcnt lgkmcnt(6)\n"
    "v_or_b32_e32 v195, v195, v203\n"
    "v_fma_mix_f32 v234, v192, v232, 0 op_sel_hi:[1,1,0]\n"
    "v_mfma_f32_16x16x32_bf16 v[128:131], v[236:239], v[184:187], v[128:131]\n"
    "v_fma_mix_f32 v232, v192, v232, 0 op_sel:[1,1,0] op_sel_hi:[1,1,0]\n"
    "v_cvt_pk_bf16_f32 v192, v234, v232\n"
    "v_mfma_f32_16x16x32_bf16 v[132:135], a[244:247], v[184:187], v[132:135]\n"
    "v_pk_fma_f16 v232, v196, v196, s48\n"
    "v_pk_mul_f16 v232, v196, v232\n"
    "v_mfma_f32_16x16x32_bf16 v[104:107], a[64:67], v[188:191], v[104:107]\n"
    "v_exp_f16_e32 v234, v232\n"
    "v_mfma_f32_16x16x32_bf16 v[108:111], a[68:71], v[188:191], v[108:111]\n"
    "s_waitcnt lgkmcnt(4)\n"
    "v_or_b32_e32 v197, v197, v205\n"
    "v_exp_f16_sdwa v234, v232 dst_sel:WORD_1 dst_unused:UNUSED_PRESERVE src0_sel:WORD_1\n"
    "v_mfma_f32_16x16x32_bf16 v[120:123], a[72:75], v[188:191], v[120:123]\n"
    "v_mfma_f32_16x16x32_bf16 v[124:127], a[76:79], v[188:191], v[124:127]\n"
    "v_mfma_f32_16x16x32_bf16 v[136:139], v[236:239], v[188:191], v[136:139]\n"
    "s_waitcnt lgkmcnt(2)\n"
    "v_or_b32_e32 v198, v198, v206\n"
    "s_waitcnt lgkmcnt(0)\n"
    "v_or_b32_e32 v199, v199, v207\n"
    "s_mov_b32 s59, s58\n"
    "s_add_u32 s57, s57, 1572864\n"
    "s_add_u32 s58, s58, 1572864\n"
    "s_mov_b32 s70, s46\n"
    "s_add_u32 s88, s57, 98304\n"
    "s_add_u32 s89, s58, 98304\n"
    "s_add_u32 s39, s59, 98304\n"
    "s_add_u32 s64, s64, 1\n"
    "s_cmp_lt_u32 s64, 2\n"
    "s_cselect_b64 s[72:73], s[72:73], s[76:77]\n"
    "s_cselect_b32 s74, s74, s78\n"
    "s_cselect_b32 s53, s53, s79\n"
    "s_cmp_lg_u32 s64, 3\n"
    "s_cbranch_scc1 1b\n"
    ".Lreplk_last:\n"
    "v_lshlrev_b32_sdwa v184, 1, v168 dst_sel:DWORD dst_unused:UNUSED_PAD src0_sel:DWORD src1_sel:WORD_1\n"
    "v_lshlrev_b32_sdwa v200, 1, v169 dst_sel:DWORD dst_unused:UNUSED_PAD src0_sel:DWORD src1_sel:WORD_1\n"
    "v_lshlrev_b32_sdwa v185, 1, v170 dst_sel:DWORD dst_unused:UNUSED_PAD src0_sel:DWORD src1_sel:WORD_1\n"
    "v_lshlrev_b32_sdwa v201, 1, v171 dst_sel:DWORD dst_unused:UNUSED_PAD src0_sel:DWORD src1_sel:WORD_1\n"
    "v_mfma_f32_16x16x32_bf16 v[140:143], a[244:247], v[188:191], v[140:143]\n"
    "ds_read_b128 v[56:59], v244 offset:36864\n"
    "ds_read_u16 v184, v184\n"
    "v_mfma_f32_16x16x32_bf16 v[168:171], a[136:139], v[144:147], 0\n"
    "ds_read_u16_d16_hi v200, v200\n"
    "ds_read_u16 v185, v185\n"
    "v_mfma_f32_16x16x32_bf16 v[168:171], a[184:187], v[148:151], v[168:171]\n"
    "v_lshlrev_b32_sdwa v186, 1, v172 dst_sel:DWORD dst_unused:UNUSED_PAD src0_sel:DWORD src1_sel:WORD_1\n"
    "v_lshlrev_b32_sdwa v202, 1, v173 dst_sel:DWORD dst_unused:UNUSED_PAD src0_sel:DWORD src1_sel:WORD_1\n"
    "v_mfma_f32_16x16x32_bf16 v[168:171], a[100:103], v[152:155], v[168:171]\n"
    "ds_read_u16_d16_hi v201, v201\n"
    "v_lshlrev_b32_sdwa v187, 1, v174 dst_sel:DWORD dst_unused:UNUSED_PAD src0_sel:DWORD src1_sel:WORD_1\n"
    "s_waitcnt vmcnt(1)\n"
    "v_smfmac_f32_32x32x32_bf16 v[128:143], a[248:251], a[224:231], v241\n"
    "s_nop 0\n"
    "ds_read_u16 v186, v186\n"
    "v_lshlrev_b32_sdwa v203, 1, v175 dst_sel:DWORD dst_unused:UNUSED_PAD src0_sel:DWORD src1_sel:WORD_1\n"
    "v_lshlrev_b32_sdwa v188, 1, v176 dst_sel:DWORD dst_unused:UNUSED_PAD src0_sel:DWORD src1_sel:WORD_1\n"
    "v_lshlrev_b32_sdwa v204, 1, v177 dst_sel:DWORD dst_unused:UNUSED_PAD src0_sel:DWORD src1_sel:WORD_1\n"
    "v_lshlrev_b32_sdwa v189, 1, v178 dst_sel:DWORD dst_unused:UNUSED_PAD src0_sel:DWORD src1_sel:WORD_1\n"
    "v_mfma_f32_16x16x32_bf16 v[172:175], a[140:143], v[144:147], 0\n"
    "ds_read_b128 v[236:239], v244 offset:20480\n"
    "v_lshlrev_b32_sdwa v205, 1, v179 dst_sel:DWORD dst_unused:UNUSED_PAD src0_sel:DWORD src1_sel:WORD_1\n"
    "v_mfma_f32_16x16x32_bf16 v[172:175], a[188:191], v[148:151], v[172:175]\n"
    "s_nop 0\n"
    "ds_read_u16_d16_hi v202, v202\n"
    "v_lshlrev_b32_sdwa v190, 1, v180 dst_sel:DWORD dst_unused:UNUSED_PAD src0_sel:DWORD src1_sel:WORD_1\n"
    "v_mfma_f32_16x16x32_bf16 v[176:179], a[136:139], v[156:159], 0\n"
    "s_waitcnt lgkmcnt(7)\n"
    "v_lshlrev_b32_sdwa v206, 1, v181 dst_sel:DWORD dst_unused:UNUSED_PAD src0_sel:DWORD src1_sel:WORD_1\n"
    "v_lshlrev_b32_sdwa v191, 1, v182 dst_sel:DWORD dst_unused:UNUSED_PAD src0_sel:DWORD src1_sel:WORD_1\n"
    "v_mfma_f32_16x16x32_bf16 v[172:175], v[56:59], v[152:155], v[172:175]\n"
    "ds_read_u16 v187, v187\n"
    "buffer_load_dwordx4 a[232:235], v245, s[40:43], s57 offen offset:64\n"
    "buffer_load_dwordx4 a[224:227], v245, s[40:43], s57 offen offset:0\n"
    "buffer_load_dwordx4 v[208:211], v245, s[40:43], s57 offen offset:128\n"
    "v_mfma_f32_16x16x32_bf16 v[176:179], a[184:187], v[160:163], v[176:179]\n"
    "ds_read_b128 a[244:247], v244 offset:22528\n"
    "v_lshlrev_b32_sdwa v207, 1, v183 dst_sel:DWORD dst_unused:UNUSED_PAD src0_sel:DWORD src1_sel:WORD_1\n"
    "v_mfma_f32_16x16x32_bf16 v[176:179], a[100:103], v[164:167], v[176:179]\n"
    "ds_read_u16_d16_hi v203, v203\n"
    "v_pk_fma_f16 v234, v234, s49, s49\n"
    "v_mfma_f32_16x16x32_bf16 v[180:183], a[140:143], v[156:159], 0\n"
    "s_waitcnt lgkmcnt(8)\n"
    "v_rcp_f16_e32 v232, v234\n"
    "v_mfma_f32_16x16x32_bf16 v[180:183], a[188:191], v[160:163], v[180:183]\n"
    "ds_read_u16 v188, v188\n"
    "v_rcp_f16_sdwa v232, v234 dst_sel:WORD_1 dst_unused:UNUSED_PRESERVE src0_sel:WORD_1\n"
    "v_mfma_f32_16x16x32_bf16 v[180:183], v[56:59], v[164:167], v[180:183]\n"
    "ds_read_u16_d16_hi v204, v204\n"
    "v_fma_mix_f32 v234, v196, v232, 0 op_sel_hi:[1,1,0]\n"
    "v_mfma_f32_16x16x32_bf16 v[96:99], a[80:83], v[192:195], v[96:99]\n"
    "v_fma_mix_f32 v232, v196, v232, 0 op_sel:[1,1,0] op_sel_hi:[1,1,0]\n"
    "v_or_b32_e32 v184, v184, v200\n"
    "v_mfma_f32_16x16x32_bf16 v[100:103], a[84:87], v[192:195], v[100:103]\n"
    "s_waitcnt lgkmcnt(8)\n"
    "ds_read_u16 v189, v189\n"
    "v_cvt_pk_bf16_f32 v196, v234, v232\n"
    "v_mfma_f32_16x16x32_bf16 v[112:115], a[88:91], v[192:195], v[112:115]\n"
    "s_waitcnt lgkmcnt(7)\n"
    "v_or_b32_e32 v185, v185, v201\n"
    "v_lshlrev_b32_sdwa v201, 1, v171 dst_sel:DWORD dst_unused:UNUSED_PAD src0_sel:DWORD src1_sel:WORD_1\n"
    "v_mfma_f32_16x16x32_bf16 v[116:119], a[92:95], v[192:195], v[116:119]\n"
    "ds_read_u16_d16_hi v205, v205\n"
    "v_cvt_pk_bf16_f32 v96, v96, v97\n"
    "v_mfma_f32_16x16x32_bf16 v[128:131], v[236:239], v[192:195], v[128:131]\n"
    "ds_read_u16 v190, v190\n"
    "v_cvt_pk_bf16_f32 v97, v98, v99\n"
    "v_mfma_f32_16x16x32_bf16 v[104:107], a[80:83], v[196:199], v[104:107]\n"
    "s_waitcnt lgkmcnt(8)\n"
    "v_or_b32_e32 v186, v186, v202\n"
    "v_lshlrev_b32_sdwa v202, 1, v173 dst_sel:DWORD dst_unused:UNUSED_PAD src0_sel:DWORD src1_sel:WORD_1\n"
    "v_mfma_f32_16x16x32_bf16 v[108:111], a[84:87], v[196:199], v[108:111]\n"
    "ds_read_u16_d16_hi v206, v206\n"
    "v_cvt_pk_bf16_f32 v98, v100, v101\n"
    "s_waitcnt lgkmcnt(7)\n"
    "v_mfma_f32_16x16x32_bf16 v[132:135], a[244:247], v[192:195], v[132:135]\n"
    "s_nop 0\n"
    "v_cvt_pk_f16_f32 v192, v168, v169\n"
    "v_lshlrev_b32_sdwa v193, 1, v170 dst_sel:DWORD dst_unused:UNUSED_PAD src0_sel:DWORD src1_sel:WORD_1\n"
    "v_mfma_f32_16x16x32_bf16 v[120:123], a[88:91], v[196:199], v[120:123]\n"
    "s_nop 0\n"
    "ds_read_u16 v191, v191\n"
    "v_lshlrev_b32_sdwa v194, 1, v172 dst_sel:DWORD dst_unused:UNUSED_PAD src0_sel:DWORD src1_sel:WORD_1\n"
    "v_mfma_f32_16x16x32_bf16 v[124:127], a[92:95], v[196:199], v[124:127]\n"
    "s_waitcnt lgkmcnt(7)\n"
    "ds_read_u16_d16_hi v207, v207\n"
    "buffer_load_dwordx4 a[236:239], v245, s[40:43], s88 offen offset:64\n"
    "buffer_load_dwordx4 a[228:231], v245, s[40:43], s88 offen offset:0\n"
    "buffer_load_dwordx4 v[212:215], v245, s[40:43], s88 offen offset:128\n"
    "v_mfma_f32_16x16x32_bf16 v[136:139], v[236:239], v[196:199], v[136:139]\n"
    "v_lshlrev_b32_sdwa v195, 1, v174 dst_sel:DWORD dst_unused:UNUSED_PAD src0_sel:DWORD src1_sel:WORD_1\n"
    "v_or_b32_e32 v187, v187, v203\n"
    "v_mfma_f32_16x16x32_bf16 v[140:143], a[244:247], v[196:199], v[140:143]\n"
    "ds_read_u16 v193, v193\n"
    "v_lshlrev_b32_sdwa v203, 1, v175 dst_sel:DWORD dst_unused:UNUSED_PAD src0_sel:DWORD src1_sel:WORD_1\n"
    "v_mfma_f32_16x16x32_bf16 v[168:171], a[144:147], v[144:147], 0\n"
    "s_waitcnt lgkmcnt(7)\n"
    "ds_read_u16_d16_hi v201, v201\n"
    "v_cvt_pk_f16_f32 v196, v176, v177\n"
    "v_mfma_f32_16x16x32_bf16 v[168:171], a[192:195], v[148:151], v[168:171]\n"
    "v_lshlrev_b32_sdwa v197, 1, v178 dst_sel:DWORD dst_unused:UNUSED_PAD src0_sel:DWORD src1_sel:WORD_1\n"
    "v_lshlrev_b32_sdwa v198, 1, v180 dst_sel:DWORD dst_unused:UNUSED_PAD src0_sel:DWORD src1_sel:WORD_1\n"
    "v_mfma_f32_16x16x32_bf16 v[168:171], a[104:107], v[152:155], v[168:171]\n"
    "ds_read_u16 v194, v194\n"
    "v_lshlrev_b32_sdwa v199, 1, v182 dst_sel:DWORD dst_unused:UNUSED_PAD src0_sel:DWORD src1_sel:WORD_1\n"
    "v_mfma_f32_16x16x32_bf16 v[172:175], a[148:151], v[144:147], 0\n"
    "v_or_b32_e64 v188, v188, v204\n"
    "v_cvt_pk_bf16_f32 v99, v102, v103\n"
    "v_mfma_f32_16x16x32_bf16 v[172:175], a[196:199], v[148:151], v[172:175]\n"
    "s_waitcnt lgkmcnt(7)\n"
    "ds_read_u16_d16_hi v202, v202\n"
    "v_or_b32_e32 v189, v189, v205\n"
    "v_mfma_f32_16x16x32_bf16 v[172:175], a[108:111], v[152:155], v[172:175]\n"
    "v_lshlrev_b32_sdwa v205, 1, v179 dst_sel:DWORD dst_unused:UNUSED_PAD src0_sel:DWORD src1_sel:WORD_1\n"
    "v_cvt_pk_bf16_f32 v112, v112, v113\n"
    "v_mfma_f32_16x16x32_bf16 v[176:179], a[144:147], v[156:159], 0\n"
    "ds_read_b128 v[0:3], v244 offset:0\n"
    "ds_write_b128 v243, v[96:99] offset:24640\n"
    "v_mfma_f32_16x16x32_bf16 v[176:179], a[192:195], v[160:163], v[176:179]\n"
    "s_waitcnt lgkmcnt(8)\n"
    "v_or_b32_e32 v190, v190, v206\n"
    "v_lshlrev_b32_sdwa v206, 1, v181 dst_sel:DWORD dst_unused:UNUSED_PAD src0_sel:DWORD src1_sel:WORD_1\n"
    "v_mfma_f32_16x16x32_bf16 v[176:179], a[104:107], v[164:167], v[176:179]\n"
    "v_cvt_pk_bf16_f32 v113, v114, v115\n"
    "v_cvt_pk_bf16_f32 v114, v116, v117\n"
    "v_mfma_f32_16x16x32_bf16 v[96:99], a[0:3], v[184:187], 0\n"
    "s_nop 0\n"
    "ds_read_b128 a[252:255], v249 offset:0\n"
    "ds_read_u16 v195, v195\n"
    "v_mfma_f32_16x16x32_bf16 v[100:103], a[4:7], v[184:187], 0\n"
    "s_waitcnt lgkmcnt(8)\n"
    "ds_read_u16_d16_hi v203, v203\n"
    "v_cvt_pk_bf16_f32 v115, v118, v119\n"
    "v_cvt_pk_bf16_f32 v128, v128, v129\n"
    "v_cvt_pk_bf16_f32 v129, v130, v131\n"
    "v_or_b32_e64 v191, v191, v207\n"
    "v_mfma_f32_16x16x32_bf16 v[116:119], a[12:15], v[184:187], 0\n"
    "ds_read_b128 v[4:7], v244 offset:2048\n"
    "v_lshlrev_b32_sdwa v207, 1, v183 dst_sel:DWORD dst_unused:UNUSED_PAD src0_sel:DWORD src1_sel:WORD_1\n"
    "v_mfma_f32_16x16x32_bf16 v[180:183], a[148:151], v[156:159], 0\n"
    "ds_read_u16 v197, v197\n"
    "v_cvt_pk_bf16_f32 v130, v132, v133\n"
    "v_mfma_f32_16x16x32_bf16 v[180:183], a[196:199], v[160:163], v[180:183]\n"
    "s_waitcnt lgkmcnt(9)\n"
    "v_or_b32_e32 v193, v193, v201\n"
    "v_cvt_pk_bf16_f32 v131, v134, v135\n"
    "v_mfma_f32_16x16x32_bf16 v[180:183], a[108:111], v[164:167], v[180:183]\n"
    "ds_read_u16_d16_hi v205, v205\n"
    "v_cvt_pk_bf16_f32 v104, v104, v105\n"
    "v_cvt_pk_bf16_f32 v105, v106, v107\n"
    "v_cvt_pk_bf16_f32 v106, v108, v109\n"
    "v_cvt_pk_bf16_f32 v107, v110, v111\n"
    "v_mfma_f32_16x16x32_bf16 v[108:111], a[4:7], v[188:191], 0\n"
    "s_waitcnt lgkmcnt(7)\n"
    "ds_read_u16 v198, v198\n"
    "v_or_b32_e32 v194, v194, v202\n"
    "v_cvt_pk_bf16_f32 v120, v120, v121\n"
    "v_cvt_pk_bf16_f32 v121, v122, v123\n"
    "v_cvt_pk_bf16_f32 v122, v124, v125\n"
    "v_cvt_pk_bf16_f32 v123, v126, v127\n"
    "ds_write_b128 v243, v[112:115] offset:26688\n"
    "s_waitcnt lgkmcnt(7)\n"
    "buffer_store_dwordx4 a[252:255], v255, s[68:71], s59 offen offset:0 sc1\n"
    "v_mfma_f32_16x16x32_bf16 v[112:115], a[8:11], v[184:187], 0\n"
    "ds_read_u16_d16_hi v206, v206\n"
    "ds_write_b128 v243, v[128:131] offset:28736\n"
    "v_mfma_f32_16x16x32_bf16 v[128:131], v[0:3], v[184:187], 0\n"
    "s_waitcnt lgkmcnt(6)\n"
    "v_pk_fma_f16 v232, v192, v192, s48\n"
    "v_cvt_pk_bf16_f32 v136, v136, v137\n"
    "v_mfma_f32_16x16x32_bf16 v[132:135], v[4:7], v[184:187], 0\n"
    "v_cvt_pk_bf16_f32 v137, v138, v139\n"
    "v_cvt_pk_bf16_f32 v138, v140, v141\n"
    "v_mfma_f32_16x16x32_bf16 v[124:127], a[12:15], v[188:191], 0\n"
    "ds_read_u16 v199, v199\n"
    "ds_write_b128 v243, v[104:107] offset:25664\n"
    "v_mfma_f32_16x16x32_bf16 v[104:107], a[0:3], v[188:191], 0\n"
    "v_or_b32_e64 v195, v195, v203\n"
    "v_cvt_pk_bf16_f32 v139, v142, v143\n"
    "v_pk_mul_f16 v232, v192, v232\n"
    "s_waitcnt lgkmcnt(6)\n"
    "v_or_b32_e32 v197, v197, v205\n"
    "v_exp_f16_e64 v234, v232\n"
    "v_mfma_f32_16x16x32_bf16 v[140:143], v[4:7], v[188:191], 0\n"
    "ds_read_u16_d16_hi v207, v207\n"
    "ds_write_b128 v243, v[120:123] offset:27712\n"
    "v_mfma_f32_16x16x32_bf16 v[120:123], a[8:11], v[188:191], 0\n"
    "v_exp_f16_sdwa v234, v232 dst_sel:WORD_1 dst_unused:UNUSED_PRESERVE src0_sel:WORD_1\n"
    "v_cvt_pk_f16_f32 v184, v168, v169\n"
    "v_pk_fma_f16 v234, v234, s49, s49\n"
    "v_lshlrev_b32_sdwa v185, 1, v170 dst_sel:DWORD dst_unused:UNUSED_PAD src0_sel:DWORD src1_sel:WORD_1\n"
    "v_lshlrev_b32_sdwa v201, 1, v171 dst_sel:DWORD dst_unused:UNUSED_PAD src0_sel:DWORD src1_sel:WORD_1\n"
    "v_mfma_f32_16x16x32_bf16 v[168:171], a[152:155], v[144:147], 0\n"
    "s_waitcnt lgkmcnt(4)\n"
    "ds_read_b128 a[252:255], v249 offset:2048\n"
    "ds_write_b128 v243, v[136:139] offset:29760\n"
    "v_mfma_f32_16x16x32_bf16 v[136:139], v[0:3], v[188:191], 0\n"
    "s_nop 0\n"
    "v_lshlrev_b32_sdwa v186, 1, v172 dst_sel:DWORD dst_unused:UNUSED_PAD src0_sel:DWORD src1_sel:WORD_1\n"
    "v_lshlrev_b32_sdwa v202, 1, v173 dst_sel:DWORD dst_unused:UNUSED_PAD src0_sel:DWORD src1_sel:WORD_1\n"
    "v_mfma_f32_16x16x32_bf16 v[168:171], a[200:203], v[148:151], v[168:171]\n"
    "v_lshlrev_b32_sdwa v187, 1, v174 dst_sel:DWORD dst_unused:UNUSED_PAD src0_sel:DWORD src1_sel:WORD_1\n"
    "v_lshlrev_b32_sdwa v203, 1, v175 dst_sel:DWORD dst_unused:UNUSED_PAD src0_sel:DWORD src1_sel:WORD_1\n"
    "v_rcp_f16_e64 v232, v234\n"
    "v_mfma_f32_16x16x32_bf16 v[168:171], a[112:115], v[152:155], v[168:171]\n"
    "ds_read_u16 v185, v185\n"
    "ds_read_u16_d16_hi v201, v201\n"
    "v_or_b32_e64 v198, v198, v206\n"
    "v_rcp_f16_sdwa v232, v234 dst_sel:WORD_1 dst_unused:UNUSED_PRESERVE src0_sel:WORD_1\n"
    "v_mfma_f32_16x16x32_bf16 v[172:175], a[156:159], v[144:147], 0\n"
    "ds_read_u16 v186, v186\n"
    "ds_read_u16_d16_hi v202, v202\n"
    "v_mfma_f32_16x16x32_bf16 v[172:175], a[204:207], v[148:151], v[172:175]\n"
    "v_cvt_pk_f16_f32 v188, v176, v177\n"
    "v_lshlrev_b32_sdwa v189, 1, v178 dst_sel:DWORD dst_unused:UNUSED_PAD src0_sel:DWORD src1_sel:WORD_1\n"
    "v_lshlrev_b32_sdwa v205, 1, v179 dst_sel:DWORD dst_unused:UNUSED_PAD src0_sel:DWORD src1_sel:WORD_1\n"
    "v_lshlrev_b32_sdwa v190, 1, v180 dst_sel:DWORD dst_unused:UNUSED_PAD src0_sel:DWORD src1_sel:WORD_1\n"
    "v_lshlrev_b32_sdwa v206, 1, v181 dst_sel:DWORD dst_unused:UNUSED_PAD src0_sel:DWORD src1_sel:WORD_1\n"
    "v_lshlrev_b32_sdwa v191, 1, v182 dst_sel:DWORD dst_unused:UNUSED_PAD src0_sel:DWORD src1_sel:WORD_1\n"
    "v_fma_mix_f32 v234, v192, v232, 0 op_sel_hi:[1,1,0]\n"
    "v_mfma_f32_16x16x32_bf16 v[172:175], a[116:119], v[152:155], v[172:175]\n"
    "s_waitcnt lgkmcnt(5)\n"
    "ds_read_b128 v[8:11], v244 offset:4096\n"
    "buffer_store_dwordx4 a[252:255], v255, s[68:71], s59 offen offset:64 sc1\n"
    "v_or_b32_e32 v199, v199, v207\n"
    "v_lshlrev_b32_sdwa v207, 1, v183 dst_sel:DWORD dst_unused:UNUSED_PAD src0_sel:DWORD src1_sel:WORD_1\n"
    "v_fma_mix_f32 v232, v192, v232, 0 op_sel:[1,1,0] op_sel_hi:[1,1,0]\n"
    "v_mfma_f32_16x16x32_bf16 v[176:179], a[152:155], v[156:159], 0\n"
    "ds_read_u16 v187, v187\n"
    "ds_read_u16_d16_hi v203, v203\n"
    "v_cvt_pk_bf16_f32 v192, v234, v232\n"
    "v_mfma_f32_16x16x32_bf16 v[176:179], a[200:203], v[160:163], v[176:179]\n"
    "ds_read_b128 v[12:15], v244 offset:6144\n"
    "ds_read_b128 a[252:255], v249 offset:4096\n"
    "v_mfma_f32_16x16x32_bf16 v[176:179], a[112:115], v[164:167], v[176:179]\n"
    "ds_read_u16 v189, v189\n"
    "v_pk_fma_f16 v232, v196, v196, s48\n"
    "v_mfma_f32_16x16x32_bf16 v[180:183], a[156:159], v[156:159], 0\n"
    "v_pk_mul_f16 v232, v196, v232\n"
    "v_lshlrev_b32_sdwa v200, 1, v169 dst_sel:DWORD dst_unused:UNUSED_PAD src0_sel:DWORD src1_sel:WORD_1\n"
    "v_mfma_f32_16x16x32_bf16 v[180:183], a[204:207], v[160:163], v[180:183]\n"
    "s_waitcnt lgkmcnt(8)\n"
    "ds_read_u16_d16_hi v205, v205\n"
    "v_exp_f16_e32 v234, v232\n"
    "v_mfma_f32_16x16x32_bf16 v[180:183], a[116:119], v[164:167], v[180:183]\n"
    "ds_read_u16 v190, v190\n"
    "v_exp_f16_sdwa v234, v232 dst_sel:WORD_1 dst_unused:UNUSED_PRESERVE src0_sel:WORD_1\n"
    "v_mfma_f32_16x16x32_bf16 v[96:99], a[16:19], v[192:195], v[96:99]\n"
    "v_pk_fma_f16 v234, v234, s49, s49\n"
    "v_or_b32_e32 v185, v185, v201\n"
    "v_mfma_f32_16x16x32_bf16 v[100:103], a[20:23], v[192:195], v[100:103]\n"
    "s_waitcnt lgkmcnt(7)\n"
    "ds_read_u16_d16_hi v206, v206\n"
    "v_rcp_f16_e64 v232, v234\n"
    "v_mfma_f32_16x16x32_bf16 v[112:115], a[24:27], v[192:195], v[112:115]\n"
    "ds_read_u16 v191, v191\n"
    "v_rcp_f16_sdwa v232, v234 dst_sel:WORD_1 dst_unused:UNUSED_PRESERVE src0_sel:WORD_1\n"
    "v_mfma_f32_16x16x32_bf16 v[116:119], a[28:31], v[192:195], v[116:119]\n"
    "v_fma_mix_f32 v234, v196, v232, 0 op_sel_hi:[1,1,0]\n"
    "v_fma_mix_f32 v232, v196, v232, 0 op_sel:[1,1,0] op_sel_hi:[1,1,0]\n"
    "v_mfma_f32_16x16x32_bf16 v[128:131], v[8:11], v[192:195], v[128:131]\n"
    "ds_read_u16_d16_hi v207, v207\n"
    "v_or_b32_e64 v186, v186, v202\n"
    "s_waitcnt lgkmcnt(7)\n"
    "v_mfma_f32_16x16x32_bf16 v[132:135], v[12:15], v[192:195], v[132:135]\n"
    "ds_read_b128 v[52:55], v244 offset:34816\n"
    "s_waitcnt lgkmcnt(7)\n"
    "buffer_store_dwordx4 a[252:255], v255, s[68:71], s59 offen offset:128 sc1\n"
    "v_cvt_pk_bf16_f32 v196, v234, v232\n"
    "v_pk_fma_f16 v232, v184, v184, s48\n"
    "v_or_b32_e64 v187, v187, v203\n"
    "v_mfma_f32_16x16x32_bf16 v[104:107], a[16:19], v[196:199], v[104:107]\n"
    "ds_read_b128 a[252:255], v249 offset:1024\n"
    "ds_read_u16_d16_hi v200, v200\n"
    "v_mfma_f32_16x16x32_bf16 v[108:111], a[20:23], v[196:199], v[108:111]\n"
    "v_pk_mul_f16 v232, v184, v232\n"
    "v_lshlrev_b32_sdwa v192, 1, v168 dst_sel:DWORD dst_unused:UNUSED_PAD src0_sel:DWORD src1_sel:WORD_1\n"
    "v_mfma_f32_16x16x32_bf16 v[120:123], a[24:27], v[196:199], v[120:123]\n"
    "s_waitcnt lgkmcnt(7)\n"
    "ds_read_u16 v192, v192\n"
    "v_exp_f16_e32 v234, v232\n"
    "v_mfma_f32_16x16x32_bf16 v[124:127], a[28:31], v[196:199], v[124:127]\n"
    "v_or_b32_e32 v189, v189, v205\n"
    "v_exp_f16_sdwa v234, v232 dst_sel:WORD_1 dst_unused:UNUSED_PRESERVE src0_sel:WORD_1\n"
    "v_mfma_f32_16x16x32_bf16 v[136:139], v[8:11], v[196:199], v[136:139]\n"
    "ds_read_b128 v[16:19], v244 offset:8192\n"
    "v_pk_fma_f16 v234, v234, s49, s49\n"
    "v_mfma_f32_16x16x32_bf16 v[140:143], v[12:15], v[196:199], v[140:143]\n"
    "s_waitcnt lgkmcnt(7)\n"
    "v_lshlrev_b32_sdwa v193, 1, v170 dst_sel:DWORD dst_unused:UNUSED_PAD src0_sel:DWORD src1_sel:WORD_1\n"
    "v_lshlrev_b32_sdwa v201, 1, v171 dst_sel:DWORD dst_unused:UNUSED_PAD src0_sel:DWORD src1_sel:WORD_1\n"
    "v_mfma_f32_16x16x32_bf16 v[168:171], a[160:163], v[144:147], 0\n"
    "ds_read_u16 v193, v193\n"
    "ds_read_u16_d16_hi v201, v201\n"
    "v_mfma_f32_16x16x32_bf16 v[168:171], a[208:211], v[148:151], v[168:171]\n"
    "v_lshlrev_b32_sdwa v194, 1, v172 dst_sel:DWORD dst_unused:UNUSED_PAD src0_sel:DWORD src1_sel:WORD_1\n"
    "v_lshlrev_b32_sdwa v202, 1, v173 dst_sel:DWORD dst_unused:UNUSED_PAD src0_sel:DWORD src1_sel:WORD_1\n"
    "v_lshlrev_b32_sdwa v195, 1, v174 dst_sel:DWORD dst_unused:UNUSED_PAD src0_sel:DWORD src1_sel:WORD_1\n"
    "v_lshlrev_b32_sdwa v203, 1, v175 dst_sel:DWORD dst_unused:UNUSED_PAD src0_sel:DWORD src1_sel:WORD_1\n"
    "v_rcp_f16_e32 v232, v234\n"
    "v_mfma_f32_16x16x32_bf16 v[168:171], a[120:123], v[152:155], v[168:171]\n"
    "s_waitcnt lgkmcnt(6)\n"
    "ds_read_u16 v194, v194\n"
    "s_waitcnt lgkmcnt(6)\n"
    "buffer_store_dwordx4 a[252:255], v255, s[68:71], s39 offen offset:0 sc1\n"
    "v_or_b32_e32 v190, v190, v206\n"
    "v_rcp_f16_sdwa v232, v234 dst_sel:WORD_1 dst_unused:UNUSED_PRESERVE src0_sel:WORD_1\n"
    "v_mfma_f32_16x16x32_bf16 v[172:175], a[164:167], v[144:147], 0\n"
    "ds_read_u16_d16_hi v202, v202\n"
    "ds_read_u16 v195, v195\n"
    "v_mfma_f32_16x16x32_bf16 v[172:175], a[212:215], v[148:151], v[172:175]\n"
    "ds_read_b128 v[20:23], v244 offset:10240\n"
    "ds_read_u16_d16_hi v203, v203\n"
    "v_lshlrev_b32_sdwa v196, 1, v176 dst_sel:DWORD dst_unused:UNUSED_PAD src0_sel:DWORD src1_sel:WORD_1\n"
    "v_lshlrev_b32_sdwa v204, 1, v177 dst_sel:DWORD dst_unused:UNUSED_PAD src0_sel:DWORD src1_sel:WORD_1\n"
    "v_lshlrev_b32_sdwa v197, 1, v178 dst_sel:DWORD dst_unused:UNUSED_PAD src0_sel:DWORD src1_sel:WORD_1\n"
    "v_lshlrev_b32_sdwa v205, 1, v179 dst_sel:DWORD dst_unused:UNUSED_PAD src0_sel:DWORD src1_sel:WORD_1\n"
    "v_lshlrev_b32_sdwa v198, 1, v180 dst_sel:DWORD dst_unused:UNUSED_PAD src0_sel:DWORD src1_sel:WORD_1\n"
    "v_lshlrev_b32_sdwa v206, 1, v181 dst_sel:DWORD dst_unused:UNUSED_PAD src0_sel:DWORD src1_sel:WORD_1\n"
    "v_lshlrev_b32_sdwa v199, 1, v182 dst_sel:DWORD dst_unused:UNUSED_PAD src0_sel:DWORD src1_sel:WORD_1\n"
    "v_fma_mix_f32 v234, v184, v232, 0 op_sel_hi:[1,1,0]\n"
    "v_mfma_f32_16x16x32_bf16 v[172:175], v[52:55], v[152:155], v[172:175]\n"
    "s_waitcnt lgkmcnt(7)\n"
    "ds_read_u16 v196, v196\n"
    "ds_read_u16_d16_hi v204, v204\n"
    "v_or_b32_e32 v191, v191, v207\n"
    "v_lshlrev_b32_sdwa v207, 1, v183 dst_sel:DWORD dst_unused:UNUSED_PAD src0_sel:DWORD src1_sel:WORD_1\n"
    "v_fma_mix_f32 v232, v184, v232, 0 op_sel:[1,1,0] op_sel_hi:[1,1,0]\n"
    "v_mfma_f32_16x16x32_bf16 v[176:179], a[160:163], v[156:159], 0\n"
    "ds_read_u16 v197, v197\n"
    "v_cvt_pk_bf16_f32 v184, v234, v232\n"
    "v_mfma_f32_16x16x32_bf16 v[176:179], a[208:211], v[160:163], v[176:179]\n"
    "v_pk_fma_f16 v232, v188, v188, s48\n"
    "v_or_b32_e32 v192, v192, v200\n"
    "v_mfma_f32_16x16x32_bf16 v[176:179], a[120:123], v[164:167], v[176:179]\n"
    "s_waitcnt lgkmcnt(8)\n"
    "ds_read_b128 a[252:255], v249 offset:3072\n"
    "v_pk_mul_f16 v232, v188, v232\n"
    "v_mfma_f32_16x16x32_bf16 v[180:183], a[164:167], v[156:159], 0\n"
    "ds_read_u16_d16_hi v205, v205\n"
    "v_exp_f16_e32 v234, v232\n"
    "v_mfma_f32_16x16x32_bf16 v[180:183], a[212:215], v[160:163], v[180:183]\n"
    "s_waitcnt lgkmcnt(8)\n"
    "v_exp_f16_sdwa v234, v232 dst_sel:WORD_1 dst_unused:UNUSED_PRESERVE src0_sel:WORD_1\n"
    "v_mfma_f32_16x16x32_bf16 v[180:183], v[52:55], v[164:167], v[180:183]\n"
    "ds_read_u16 v198, v198\n"
    "v_pk_fma_f16 v234, v234, s49, s49\n"
    "v_mfma_f32_16x16x32_bf16 v[96:99], a[32:35], v[184:187], v[96:99]\n"
    "ds_read_u16_d16_hi v206, v206\n"
    "v_rcp_f16_e64 v232, v234\n"
    "v_mfma_f32_16x16x32_bf16 v[100:103], a[36:39], v[184:187], v[100:103]\n"
    "v_rcp_f16_sdwa v232, v234 dst_sel:WORD_1 dst_unused:UNUSED_PRESERVE src0_sel:WORD_1\n"
    "v_mfma_f32_16x16x32_bf16 v[112:115], a[40:43], v[184:187], v[112:115]\n"
    "ds_read_u16 v199, v199\n"
    "v_fma_mix_f32 v234, v188, v232, 0 op_sel_hi:[1,1,0]\n"
    "v_mfma_f32_16x16x32_bf16 v[116:119], a[44:47], v[184:187], v[116:119]\n"
    "s_waitcnt lgkmcnt(8)\n"
    "v_fma_mix_f32 v232, v188, v232, 0 op_sel:[1,1,0] op_sel_hi:[1,1,0]\n"
    "v_or_b32_e32 v193, v193, v201\n"
    "v_mfma_f32_16x16x32_bf16 v[128:131], v[16:19], v[184:187], v[128:131]\n"
    "ds_read_u16_d16_hi v207, v207\n"
    "v_cvt_pk_bf16_f32 v188, v234, v232\n"
    "v_mfma_f32_16x16x32_bf16 v[132:135], v[20:23], v[184:187], v[132:135]\n"
    "s_nop 0\n"
    "ds_read_b128 v[60:63], v244 offset:38912\n"
    "v_cvt_pk_f16_f32 v184, v168, v169\n"
    "v_mfma_f32_16x16x32_bf16 v[104:107], a[32:35], v[188:191], v[104:107]\n"
    "s_waitcnt lgkmcnt(8)\n"
    "ds_read_b128 v[24:27], v244 offset:12288\n"
    "v_lshlrev_b32_sdwa v185, 1, v170 dst_sel:DWORD dst_unused:UNUSED_PAD src0_sel:DWORD src1_sel:WORD_1\n"
    "v_mfma_f32_16x16x32_bf16 v[108:111], a[36:39], v[188:191], v[108:111]\n"
    "ds_read_u16 v185, v185\n"
    "s_waitcnt lgkmcnt(8)\n"
    "buffer_store_dwordx4 a[252:255], v255, s[68:71], s39 offen offset:64 sc1\n"
    "v_mfma_f32_16x16x32_bf16 v[120:123], a[40:43], v[188:191], v[120:123]\n"
    "v_lshlrev_b32_sdwa v201, 1, v171 dst_sel:DWORD dst_unused:UNUSED_PAD src0_sel:DWORD src1_sel:WORD_1\n"
    "v_lshlrev_b32_sdwa v186, 1, v172 dst_sel:DWORD dst_unused:UNUSED_PAD src0_sel:DWORD src1_sel:WORD_1\n"
    "v_mfma_f32_16x16x32_bf16 v[124:127], a[44:47], v[188:191], v[124:127]\n"
    "s_waitcnt lgkmcnt(7)\n"
    "ds_read_b128 a[252:255], v249 offset:5120\n"
    "ds_read_u16_d16_hi v201, v201\n"
    "v_mfma_f32_16x16x32_bf16 v[136:139], v[16:19], v[188:191], v[136:139]\n"
    "v_or_b32_e64 v194, v194, v202\n"
    "v_lshlrev_b32_sdwa v202, 1, v173 dst_sel:DWORD dst_unused:UNUSED_PAD src0_sel:DWORD src1_sel:WORD_1\n"
    "v_mfma_f32_16x16x32_bf16 v[140:143], v[20:23], v[188:191], v[140:143]\n"
    "ds_read_u16 v186, v186\n"
    "v_lshlrev_b32_sdwa v187, 1, v174 dst_sel:DWORD dst_unused:UNUSED_PAD src0_sel:DWORD src1_sel:WORD_1\n"
    "v_mfma_f32_16x16x32_bf16 v[168:171], a[168:171], v[144:147], 0\n"
    "s_nop 0\n"
    "ds_read_u16_d16_hi v202, v202\n"
    "v_cvt_pk_f16_f32 v188, v176, v177\n"
    "v_mfma_f32_16x16x32_bf16 v[168:171], a[216:219], v[148:151], v[168:171]\n"
    "s_waitcnt lgkmcnt(9)\n"
    "v_lshlrev_b32_sdwa v189, 1, v178 dst_sel:DWORD dst_unused:UNUSED_PAD src0_sel:DWORD src1_sel:WORD_1\n"
    "v_lshlrev_b32_sdwa v190, 1, v180 dst_sel:DWORD dst_unused:UNUSED_PAD src0_sel:DWORD src1_sel:WORD_1\n"
    "v_mfma_f32_16x16x32_bf16 v[168:171], a[124:127], v[152:155], v[168:171]\n"
    "ds_read_u16 v187, v187\n"
    "v_lshlrev_b32_sdwa v191, 1, v182 dst_sel:DWORD dst_unused:UNUSED_PAD src0_sel:DWORD src1_sel:WORD_1\n"
    "v_or_b32_e64 v195, v195, v203\n"
    "v_lshlrev_b32_sdwa v203, 1, v175 dst_sel:DWORD dst_unused:UNUSED_PAD src0_sel:DWORD src1_sel:WORD_1\n"
    "v_mfma_f32_16x16x32_bf16 v[172:175], a[172:175], v[144:147], 0\n"
    "ds_read_u16_d16_hi v203, v203\n"
    "v_or_b32_e64 v196, v196, v204\n"
    "v_mfma_f32_16x16x32_bf16 v[172:175], a[220:223], v[148:151], v[172:175]\n"
    "s_waitcnt lgkmcnt(8)\n"
    "v_or_b32_e32 v197, v197, v205\n"
    "v_lshlrev_b32_sdwa v205, 1, v179 dst_sel:DWORD dst_unused:UNUSED_PAD src0_sel:DWORD src1_sel:WORD_1\n"
    "v_mfma_f32_16x16x32_bf16 v[172:175], v[60:63], v[152:155], v[172:175]\n"
    "ds_read_b128 v[28:31], v244 offset:14336\n"
    "v_or_b32_e64 v198, v198, v206\n"
    "v_mfma_f32_16x16x32_bf16 v[176:179], a[168:171], v[156:159], 0\n"
    "ds_read_u16 v189, v189\n"
    "v_lshlrev_b32_sdwa v206, 1, v181 dst_sel:DWORD dst_unused:UNUSED_PAD src0_sel:DWORD src1_sel:WORD_1\n"
    "v_mfma_f32_16x16x32_bf16 v[176:179], a[216:219], v[160:163], v[176:179]\n"
    "s_waitcnt lgkmcnt(7)\n"
    "buffer_store_dwordx4 a[252:255], v255, s[68:71], s39 offen offset:128 sc1\n"
    "ds_read_u16_d16_hi v205, v205\n"
    "v_mfma_f32_16x16x32_bf16 v[176:179], a[124:127], v[164:167], v[176:179]\n"
    "v_or_b32_e32 v199, v199, v207\n"
    "v_lshlrev_b32_sdwa v207, 1, v183 dst_sel:DWORD dst_unused:UNUSED_PAD src0_sel:DWORD src1_sel:WORD_1\n"
    "v_mfma_f32_16x16x32_bf16 v[180:183], a[172:175], v[156:159], 0\n"
    "s_waitcnt lgkmcnt(7)\n"
    "ds_read_u16 v190, v190\n"
    "v_or_b32_e32 v185, v185, v201\n"
    "v_mfma_f32_16x16x32_bf16 v[180:183], a[220:223], v[160:163], v[180:183]\n"
    "ds_read_b128 v[144:147], v243 offset:0\n"
    "v_pk_fma_f16 v232, v184, v184, s48\n"
    "v_mfma_f32_16x16x32_bf16 v[180:183], v[60:63], v[164:167], v[180:183]\n"
    "ds_read_u16_d16_hi v206, v206\n"
    "v_pk_mul_f16 v232, v184, v232\n"
    "v_mfma_f32_16x16x32_bf16 v[96:99], a[48:51], v[192:195], v[96:99]\n"
    "v_exp_f16_e32 v234, v232\n"
    "v_mfma_f32_16x16x32_bf16 v[100:103], a[52:55], v[192:195], v[100:103]\n"
    "s_waitcnt lgkmcnt(8)\n"
    "ds_read_b128 v[148:151], v243 offset:1024\n"
    "v_or_b32_e64 v186, v186, v202\n"
    "v_mfma_f32_16x16x32_bf16 v[112:115], a[56:59], v[192:195], v[112:115]\n"
    "ds_read_u16 v191, v191\n"
    "v_exp_f16_sdwa v234, v232 dst_sel:WORD_1 dst_unused:UNUSED_PRESERVE src0_sel:WORD_1\n"
    "v_mfma_f32_16x16x32_bf16 v[116:119], a[60:63], v[192:195], v[116:119]\n"
    "v_pk_fma_f16 v234, v234, s49, s49\n"
    "v_lshlrev_b32_sdwa v201, 1, v171 dst_sel:DWORD dst_unused:UNUSED_PAD src0_sel:DWORD src1_sel:WORD_1\n"
    "v_mfma_f32_16x16x32_bf16 v[128:131], v[24:27], v[192:195], v[128:131]\n"
    "s_waitcnt lgkmcnt(7)\n"
    "ds_read_b128 v[152:155], v243 offset:2048\n"
    "ds_read_u16_d16_hi v207, v207\n"
    "v_mfma_f32_16x16x32_bf16 v[132:135], v[28:31], v[192:195], v[132:135]\n"
    "v_or_b32_e32 v187, v187, v203\n"
    "v_cvt_pk_f16_f32 v192, v168, v169\n"
    "v_mfma_f32_16x16x32_bf16 v[104:107], a[48:51], v[196:199], v[104:107]\n"
    "ds_read_b128 v[156:159], v243 offset:3072\n"
    "v_lshlrev_b32_sdwa v193, 1, v170 dst_sel:DWORD dst_unused:UNUSED_PAD src0_sel:DWORD src1_sel:WORD_1\n"
    "v_mfma_f32_16x16x32_bf16 v[108:111], a[52:55], v[196:199], v[108:111]\n"
    "ds_read_b128 v[160:163], v243 offset:4096\n"
    "v_lshlrev_b32_sdwa v194, 1, v172 dst_sel:DWORD dst_unused:UNUSED_PAD src0_sel:DWORD src1_sel:WORD_1\n"
    "v_mfma_f32_16x16x32_bf16 v[120:123], a[56:59], v[196:199], v[120:123]\n"
    "ds_read_b128 v[164:167], v243 offset:5120\n"
    "s_waitcnt lgkmcnt(10)\n"
    "v_or_b32_e32 v189, v189, v205\n"
    "v_mfma_f32_16x16x32_bf16 v[124:127], a[60:63], v[196:199], v[124:127]\n"
    "s_nop 0\n"
    "ds_read_b128 v[48:51], v244 offset:32768\n"
    "v_lshlrev_b32_sdwa v202, 1, v173 dst_sel:DWORD dst_unused:UNUSED_PAD src0_sel:DWORD src1_sel:WORD_1\n"
    "v_mfma_f32_16x16x32_bf16 v[136:139], v[24:27], v[196:199], v[136:139]\n"
    "s_waitcnt lgkmcnt(9)\n"
    "ds_read_u16 v193, v193\n"
    "v_lshlrev_b32_sdwa v195, 1, v174 dst_sel:DWORD dst_unused:UNUSED_PAD src0_sel:DWORD src1_sel:WORD_1\n"
    "v_mfma_f32_16x16x32_bf16 v[140:143], v[28:31], v[196:199], v[140:143]\n"
    "v_lshlrev_b32_sdwa v203, 1, v175 dst_sel:DWORD dst_unused:UNUSED_PAD src0_sel:DWORD src1_sel:WORD_1\n"
    "v_rcp_f16_e32 v232, v234\n"
    "v_mfma_f32_16x16x32_bf16 v[168:171], a[128:131], v[144:147], 0\n"
    "s_waitcnt lgkmcnt(8)\n"
    "ds_read_u16_d16_hi v201, v201\n"
    "v_or_b32_e64 v190, v190, v206\n"
    "v_mfma_f32_16x16x32_bf16 v[168:171], a[176:179], v[148:151], v[168:171]\n"
    "ds_read_u16 v194, v194\n"
    "v_rcp_f16_sdwa v232, v234 dst_sel:WORD_1 dst_unused:UNUSED_PRESERVE src0_sel:WORD_1\n"
    "v_mfma_f32_16x16x32_bf16 v[172:175], a[132:135], v[144:147], 0\n"
    "v_cvt_pk_f16_f32 v196, v176, v177\n"
    "v_lshlrev_b32_sdwa v197, 1, v178 dst_sel:DWORD dst_unused:UNUSED_PAD src0_sel:DWORD src1_sel:WORD_1\n"
    "s_waitcnt lgkmcnt(8)\n"
    "v_mfma_f32_16x16x32_bf16 v[168:171], a[96:99], v[152:155], v[168:171]\n"
    "s_nop 0\n"
    "ds_read_u16_d16_hi v202, v202\n"
    "v_lshlrev_b32_sdwa v205, 1, v179 dst_sel:DWORD dst_unused:UNUSED_PAD src0_sel:DWORD src1_sel:WORD_1\n"
    "v_lshlrev_b32_sdwa v198, 1, v180 dst_sel:DWORD dst_unused:UNUSED_PAD src0_sel:DWORD src1_sel:WORD_1\n"
    "v_lshlrev_b32_sdwa v206, 1, v181 dst_sel:DWORD dst_unused:UNUSED_PAD src0_sel:DWORD src1_sel:WORD_1\n"
    "v_lshlrev_b32_sdwa v199, 1, v182 dst_sel:DWORD dst_unused:UNUSED_PAD src0_sel:DWORD src1_sel:WORD_1\n"
    "v_fma_mix_f32 v234, v184, v232, 0 op_sel_hi:[1,1,0]\n"
    "v_mfma_f32_16x16x32_bf16 v[172:175], a[180:183], v[148:151], v[172:175]\n"
    "ds_read_b128 v[32:35], v244 offset:16384\n"
    "ds_read_u16 v195, v195\n"
    "s_waitcnt lgkmcnt(9)\n"
    "v_or_b32_e32 v191, v191, v207\n"
    "v_lshlrev_b32_sdwa v207, 1, v183 dst_sel:DWORD dst_unused:UNUSED_PAD src0_sel:DWORD src1_sel:WORD_1\n"
    "v_fma_mix_f32 v232, v184, v232, 0 op_sel:[1,1,0] op_sel_hi:[1,1,0]\n"
    "v_mfma_f32_16x16x32_bf16 v[176:179], a[128:131], v[156:159], 0\n"
    "s_waitcnt lgkmcnt(7)\n"
    "ds_read_u16_d16_hi v203, v203\n"
    "v_cvt_pk_bf16_f32 v184, v234, v232\n"
    "v_mfma_f32_16x16x32_bf16 v[176:179], a[176:179], v[160:163], v[176:179]\n"
    "s_waitcnt lgkmcnt(7)\n"
    "v_pk_fma_f16 v232, v188, v188, s48\n"
    "v_lshlrev_b32_sdwa v200, 1, v169 dst_sel:DWORD dst_unused:UNUSED_PAD src0_sel:DWORD src1_sel:WORD_1\n"
    "v_mfma_f32_16x16x32_bf16 v[172:175], v[48:51], v[152:155], v[172:175]\n"
    "s_barrier\n"
    "v_mfma_f32_16x16x32_bf16 v[176:179], a[96:99], v[164:167], v[176:179]\n"
    "ds_read_b128 v[36:39], v244 offset:18432\n"
    "ds_write_b128 v253, v[64:67] offset:2048\n"
    "v_mfma_f32_16x16x32_bf16 v[180:183], a[132:135], v[156:159], 0\n"
    "s_waitcnt lgkmcnt(7)\n"
    "v_pk_mul_f16 v232, v188, v232\n"
    "v_or_b32_e64 v193, v193, v201\n"
    "v_mfma_f32_16x16x32_bf16 v[180:183], a[180:183], v[160:163], v[180:183]\n"
    "v_exp_f16_e64 v234, v232\n"
    "v_mfma_f32_16x16x32_bf16 v[180:183], v[48:51], v[164:167], v[180:183]\n"
    "ds_read_u16 v197, v197\n"
    "ds_write_b128 v253, v[68:71] offset:5120\n"
    "v_exp_f16_sdwa v234, v232 dst_sel:WORD_1 dst_unused:UNUSED_PRESERVE src0_sel:WORD_1\n"
    "v_mfma_f32_16x16x32_bf16 v[96:99], a[64:67], v[184:187], v[96:99]\n"
    "s_waitcnt lgkmcnt(6)\n"
    "v_pk_fma_f16 v234, v234, s49, s49\n"
    "v_or_b32_e32 v194, v194, v202\n"
    "v_mfma_f32_16x16x32_bf16 v[100:103], a[68:71], v[184:187], v[100:103]\n"
    "v_rcp_f16_e32 v232, v234\n"
    "v_mfma_f32_16x16x32_bf16 v[112:115], a[72:75], v[184:187], v[112:115]\n"
    "ds_read_u16_d16_hi v205, v205\n"
    "ds_write_b128 v253, v[88:91] offset:0\n"
    "v_rcp_f16_sdwa v232, v234 dst_sel:WORD_1 dst_unused:UNUSED_PRESERVE src0_sel:WORD_1\n"
    "v_mfma_f32_16x16x32_bf16 v[116:119], a[76:79], v[184:187], v[116:119]\n"
    "s_waitcnt lgkmcnt(4)\n"
    "v_fma_mix_f32 v234, v188, v232, 0 op_sel_hi:[1,1,0]\n"
    "v_fma_mix_f32 v232, v188, v232, 0 op_sel:[1,1,0] op_sel_hi:[1,1,0]\n"
    "v_mfma_f32_16x16x32_bf16 v[128:131], v[32:35], v[184:187], v[128:131]\n"
    "v_cvt_pk_bf16_f32 v188, v234, v232\n"
    "v_pk_fma_f16 v232, v192, v192, s48\n"
    "v_mfma_f32_16x16x32_bf16 v[132:135], v[36:39], v[184:187], v[132:135]\n"
    "ds_read_u16 v198, v198\n"
    "ds_write_b128 v253, v[92:95] offset:3072\n"
    "v_mfma_f32_16x16x32_bf16 v[104:107], a[64:67], v[188:191], v[104:107]\n"
    "v_or_b32_e64 v195, v195, v203\n"
    "v_pk_mul_f16 v232, v192, v232\n"
    "v_mfma_f32_16x16x32_bf16 v[108:111], a[68:71], v[188:191], v[108:111]\n"
    "v_exp_f16_e64 v234, v232\n"
    "v_mfma_f32_16x16x32_bf16 v[120:123], a[72:75], v[188:191], v[120:123]\n"
    "ds_read_u16_d16_hi v206, v206\n"
    "ds_read_u16 v199, v199\n"
    "v_mfma_f32_16x16x32_bf16 v[124:127], a[76:79], v[188:191], v[124:127]\n"
    "ds_read_u16_d16_hi v207, v207\n"
    "ds_write_b128 v253, v[72:75] offset:1024\n"
    "v_mfma_f32_16x16x32_bf16 v[136:139], v[32:35], v[188:191], v[136:139]\n"
    "s_waitcnt lgkmcnt(6)\n"
    "v_or_b32_e32 v197, v197, v205\n"
    "v_exp_f16_sdwa v234, v232 dst_sel:WORD_1 dst_unused:UNUSED_PRESERVE src0_sel:WORD_1\n"
    "v_mfma_f32_16x16x32_bf16 v[140:143], v[36:39], v[188:191], v[140:143]\n"
    "v_pk_fma_f16 v234, v234, s49, s49\n"
    "v_lshlrev_b32_sdwa v184, 1, v168 dst_sel:DWORD dst_unused:UNUSED_PAD src0_sel:DWORD src1_sel:WORD_1\n"
    "v_lshlrev_b32_sdwa v185, 1, v170 dst_sel:DWORD dst_unused:UNUSED_PAD src0_sel:DWORD src1_sel:WORD_1\n"
    "v_lshlrev_b32_sdwa v201, 1, v171 dst_sel:DWORD dst_unused:UNUSED_PAD src0_sel:DWORD src1_sel:WORD_1\n"
    "v_mfma_f32_16x16x32_bf16 v[168:171], a[136:139], v[144:147], 0\n"
    "ds_read_u16 v184, v184\n"
    "ds_write_b128 v253, v[76:79] offset:4096\n"
    "v_mfma_f32_16x16x32_bf16 v[168:171], a[184:187], v[148:151], v[168:171]\n"
    "v_lshlrev_b32_sdwa v186, 1, v172 dst_sel:DWORD dst_unused:UNUSED_PAD src0_sel:DWORD src1_sel:WORD_1\n"
    "v_lshlrev_b32_sdwa v202, 1, v173 dst_sel:DWORD dst_unused:UNUSED_PAD src0_sel:DWORD src1_sel:WORD_1\n"
    "v_lshlrev_b32_sdwa v187, 1, v174 dst_sel:DWORD dst_unused:UNUSED_PAD src0_sel:DWORD src1_sel:WORD_1\n"
    "v_lshlrev_b32_sdwa v203, 1, v175 dst_sel:DWORD dst_unused:UNUSED_PAD src0_sel:DWORD src1_sel:WORD_1\n"
    "v_rcp_f16_e64 v232, v234\n"
    "v_mfma_f32_16x16x32_bf16 v[168:171], a[100:103], v[152:155], v[168:171]\n"
    "ds_read_u16_d16_hi v200, v200\n"
    "ds_read_u16 v185, v185\n"
    "s_waitcnt lgkmcnt(7)\n"
    "v_or_b32_e32 v198, v198, v206\n"
    "v_rcp_f16_sdwa v232, v234 dst_sel:WORD_1 dst_unused:UNUSED_PRESERVE src0_sel:WORD_1\n"
    "v_mfma_f32_16x16x32_bf16 v[172:175], a[140:143], v[144:147], 0\n"
    "ds_read_u16_d16_hi v201, v201\n"
    "v_lshlrev_b32_sdwa v188, 1, v176 dst_sel:DWORD dst_unused:UNUSED_PAD src0_sel:DWORD src1_sel:WORD_1\n"
    "v_lshlrev_b32_sdwa v204, 1, v177 dst_sel:DWORD dst_unused:UNUSED_PAD src0_sel:DWORD src1_sel:WORD_1\n"
    "v_lshlrev_b32_sdwa v189, 1, v178 dst_sel:DWORD dst_unused:UNUSED_PAD src0_sel:DWORD src1_sel:WORD_1\n"
    "v_lshlrev_b32_sdwa v205, 1, v179 dst_sel:DWORD dst_unused:UNUSED_PAD src0_sel:DWORD src1_sel:WORD_1\n"
    "v_lshlrev_b32_sdwa v190, 1, v180 dst_sel:DWORD dst_unused:UNUSED_PAD src0_sel:DWORD src1_sel:WORD_1\n"
    "v_lshlrev_b32_sdwa v206, 1, v181 dst_sel:DWORD dst_unused:UNUSED_PAD src0_sel:DWORD src1_sel:WORD_1\n"
    "v_lshlrev_b32_sdwa v191, 1, v182 dst_sel:DWORD dst_unused:UNUSED_PAD src0_sel:DWORD src1_sel:WORD_1\n"
    "v_fma_mix_f32 v234, v192, v232, 0 op_sel_hi:[1,1,0]\n"
    "v_mfma_f32_16x16x32_bf16 v[172:175], a[188:191], v[148:151], v[172:175]\n"
    "s_waitcnt lgkmcnt(5)\n"
    "ds_read_u16 v186, v186\n"
    "ds_read_b128 v[40:43], v244 offset:20480\n"
    "v_mfma_f32_16x16x32_bf16 v[172:175], v[56:59], v[152:155], v[172:175]\n"
    "s_nop 0\n"
    "ds_read_u16_d16_hi v202, v202\n"
    "v_or_b32_e64 v199, v199, v207\n"
    "v_lshlrev_b32_sdwa v207, 1, v183 dst_sel:DWORD dst_unused:UNUSED_PAD src0_sel:DWORD src1_sel:WORD_1\n"
    "v_fma_mix_f32 v232, v192, v232, 0 op_sel:[1,1,0] op_sel_hi:[1,1,0]\n"
    "v_mfma_f32_16x16x32_bf16 v[176:179], a[136:139], v[156:159], 0\n"
    "ds_read_u16 v187, v187\n"
    "v_cvt_pk_bf16_f32 v192, v234, v232\n"
    "v_mfma_f32_16x16x32_bf16 v[176:179], a[184:187], v[160:163], v[176:179]\n"
    "v_pk_fma_f16 v232, v196, v196, s48\n"
    "v_pk_mul_f16 v232, v196, v232\n"
    "v_mfma_f32_16x16x32_bf16 v[176:179], a[100:103], v[164:167], v[176:179]\n"
    "ds_read_b128 v[44:47], v244 offset:22528\n"
    "v_exp_f16_e32 v234, v232\n"
    "v_mfma_f32_16x16x32_bf16 v[180:183], a[140:143], v[156:159], 0\n"
    "ds_read_u16_d16_hi v203, v203\n"
    "v_exp_f16_sdwa v234, v232 dst_sel:WORD_1 dst_unused:UNUSED_PRESERVE src0_sel:WORD_1\n"
    "v_mfma_f32_16x16x32_bf16 v[180:183], a[188:191], v[160:163], v[180:183]\n"
    "s_waitcnt lgkmcnt(8)\n"
    "ds_read_u16 v188, v188\n"
    "v_pk_fma_f16 v234, v234, s49, s49\n"
    "v_mfma_f32_16x16x32_bf16 v[180:183], v[56:59], v[164:167], v[180:183]\n"
    "v_rcp_f16_e32 v232, v234\n"
    "v_mfma_f32_16x16x32_bf16 v[96:99], a[80:83], v[192:195], v[96:99]\n"
    "ds_read_u16_d16_hi v204, v204\n"
    "v_rcp_f16_sdwa v232, v234 dst_sel:WORD_1 dst_unused:UNUSED_PRESERVE src0_sel:WORD_1\n"
    "v_mfma_f32_16x16x32_bf16 v[100:103], a[84:87], v[192:195], v[100:103]\n"
    "s_waitcnt lgkmcnt(8)\n"
    "ds_read_u16 v189, v189\n"
    "v_fma_mix_f32 v234, v196, v232, 0 op_sel_hi:[1,1,0]\n"
    "v_mfma_f32_16x16x32_bf16 v[112:115], a[88:91], v[192:195], v[112:115]\n"
    "v_fma_mix_f32 v232, v196, v232, 0 op_sel:[1,1,0] op_sel_hi:[1,1,0]\n"
    "v_or_b32_e32 v184, v184, v200\n"
    "v_mfma_f32_16x16x32_bf16 v[116:119], a[92:95], v[192:195], v[116:119]\n"
    "s_waitcnt lgkmcnt(7)\n"
    "ds_read_u16_d16_hi v205, v205\n"
    "v_cvt_pk_bf16_f32 v196, v234, v232\n"
    "v_mfma_f32_16x16x32_bf16 v[128:131], v[40:43], v[192:195], v[128:131]\n"
    "v_or_b32_e64 v185, v185, v201\n"
    "v_lshlrev_b32_sdwa v201, 1, v171 dst_sel:DWORD dst_unused:UNUSED_PAD src0_sel:DWORD src1_sel:WORD_1\n"
    "v_mfma_f32_16x16x32_bf16 v[104:107], a[80:83], v[196:199], v[104:107]\n"
    "ds_read_u16 v190, v190\n"
    "s_waitcnt lgkmcnt(8)\n"
    "v_or_b32_e32 v186, v186, v202\n"
    "v_mfma_f32_16x16x32_bf16 v[108:111], a[84:87], v[196:199], v[108:111]\n"
    "ds_read_u16_d16_hi v206, v206\n"
    "v_lshlrev_b32_sdwa v202, 1, v173 dst_sel:DWORD dst_unused:UNUSED_PAD src0_sel:DWORD src1_sel:WORD_1\n"
    "s_waitcnt lgkmcnt(7)\n"
    "v_mfma_f32_16x16x32_bf16 v[132:135], v[44:47], v[192:195], v[132:135]\n"
    "s_nop 0\n"
    "v_cvt_pk_f16_f32 v192, v168, v169\n"
    "v_lshlrev_b32_sdwa v193, 1, v170 dst_sel:DWORD dst_unused:UNUSED_PAD src0_sel:DWORD src1_sel:WORD_1\n"
    "v_mfma_f32_16x16x32_bf16 v[120:123], a[88:91], v[196:199], v[120:123]\n"
    "ds_read_u16 v191, v191\n"
    "v_lshlrev_b32_sdwa v194, 1, v172 dst_sel:DWORD dst_unused:UNUSED_PAD src0_sel:DWORD src1_sel:WORD_1\n"
    "v_mfma_f32_16x16x32_bf16 v[124:127], a[92:95], v[196:199], v[124:127]\n"
    "v_lshlrev_b32_sdwa v195, 1, v174 dst_sel:DWORD dst_unused:UNUSED_PAD src0_sel:DWORD src1_sel:WORD_1\n"
    "s_waitcnt lgkmcnt(7)\n"
    "v_or_b32_e32 v187, v187, v203\n"
    "v_mfma_f32_16x16x32_bf16 v[136:139], v[40:43], v[196:199], v[136:139]\n"
    "ds_read_u16_d16_hi v207, v207\n"
    "v_lshlrev_b32_sdwa v203, 1, v175 dst_sel:DWORD dst_unused:UNUSED_PAD src0_sel:DWORD src1_sel:WORD_1\n"
    "v_mfma_f32_16x16x32_bf16 v[140:143], v[44:47], v[196:199], v[140:143]\n"
    "ds_read_u16 v193, v193\n"
    "v_cvt_pk_f16_f32 v196, v176, v177\n"
    "v_mfma_f32_16x16x32_bf16 v[168:171], a[144:147], v[144:147], 0\n"
    "v_lshlrev_b32_sdwa v197, 1, v178 dst_sel:DWORD dst_unused:UNUSED_PAD src0_sel:DWORD src1_sel:WORD_1\n"
    "v_lshlrev_b32_sdwa v198, 1, v180 dst_sel:DWORD dst_unused:UNUSED_PAD src0_sel:DWORD src1_sel:WORD_1\n"
    "v_mfma_f32_16x16x32_bf16 v[168:171], a[192:195], v[148:151], v[168:171]\n"
    "s_waitcnt lgkmcnt(7)\n"
    "ds_read_u16_d16_hi v201, v201\n"
    "v_lshlrev_b32_sdwa v199, 1, v182 dst_sel:DWORD dst_unused:UNUSED_PAD src0_sel:DWORD src1_sel:WORD_1\n"
    "v_mfma_f32_16x16x32_bf16 v[168:171], a[104:107], v[152:155], v[168:171]\n"
    "v_or_b32_e32 v188, v188, v204\n"
    "v_cvt_pk_bf16_f32 v96, v96, v97\n"
    "v_mfma_f32_16x16x32_bf16 v[172:175], a[148:151], v[144:147], 0\n"
    "ds_read_u16 v194, v194\n"
    "v_cvt_pk_bf16_f32 v97, v98, v99\n"
    "v_mfma_f32_16x16x32_bf16 v[172:175], a[196:199], v[148:151], v[172:175]\n"
    "s_waitcnt lgkmcnt(7)\n"
    "ds_read_u16_d16_hi v202, v202\n"
    "v_or_b32_e32 v189, v189, v205\n"
    "v_mfma_f32_16x16x32_bf16 v[172:175], a[108:111], v[152:155], v[172:175]\n"
    "v_lshlrev_b32_sdwa v205, 1, v179 dst_sel:DWORD dst_unused:UNUSED_PAD src0_sel:DWORD src1_sel:WORD_1\n"
    "v_cvt_pk_bf16_f32 v98, v100, v101\n"
    "v_mfma_f32_16x16x32_bf16 v[176:179], a[144:147], v[156:159], 0\n"
    "ds_read_u16 v195, v195\n"
    "v_cvt_pk_bf16_f32 v99, v102, v103\n"
    "v_mfma_f32_16x16x32_bf16 v[176:179], a[192:195], v[160:163], v[176:179]\n"
    "v_cvt_pk_bf16_f32 v112, v112, v113\n"
    "v_cvt_pk_bf16_f32 v113, v114, v115\n"
    "v_mfma_f32_16x16x32_bf16 v[176:179], a[104:107], v[164:167], v[176:179]\n"
    "s_waitcnt lgkmcnt(7)\n"
    "ds_read_u16_d16_hi v203, v203\n"
    "v_or_b32_e32 v190, v190, v206\n"
    "v_mfma_f32_16x16x32_bf16 v[100:103], a[4:7], v[184:187], 0\n"
    "s_nop 0\n"
    "v_lshlrev_b32_sdwa v206, 1, v181 dst_sel:DWORD dst_unused:UNUSED_PAD src0_sel:DWORD src1_sel:WORD_1\n"
    "ds_write_b128 v242, v[96:99] offset:24640\n"
    "v_mfma_f32_16x16x32_bf16 v[96:99], a[0:3], v[184:187], 0\n"
    "s_waitcnt lgkmcnt(7)\n"
    "v_cvt_pk_bf16_f32 v114, v116, v117\n"
    "v_cvt_pk_bf16_f32 v115, v118, v119\n"
    "v_cvt_pk_bf16_f32 v128, v128, v129\n"
    "v_cvt_pk_bf16_f32 v129, v130, v131\n"
    "ds_write_b128 v242, v[112:115] offset:26688\n"
    "v_mfma_f32_16x16x32_bf16 v[112:115], a[8:11], v[184:187], 0\n"
    "s_waitcnt lgkmcnt(6)\n"
    "v_or_b32_e32 v191, v191, v207\n"
    "v_lshlrev_b32_sdwa v207, 1, v183 dst_sel:DWORD dst_unused:UNUSED_PAD src0_sel:DWORD src1_sel:WORD_1\n"
    "v_mfma_f32_16x16x32_bf16 v[180:183], a[148:151], v[156:159], 0\n"
    "ds_read_u16 v197, v197\n"
    "ds_read_u16_d16_hi v205, v205\n"
    "v_mfma_f32_16x16x32_bf16 v[180:183], a[196:199], v[160:163], v[180:183]\n"
    "ds_read_u16 v198, v198\n"
    "v_cvt_pk_bf16_f32 v130, v132, v133\n"
    "v_mfma_f32_16x16x32_bf16 v[180:183], a[108:111], v[164:167], v[180:183]\n"
    "v_or_b32_e32 v193, v193, v201\n"
    "v_cvt_pk_bf16_f32 v131, v134, v135\n"
    "v_mfma_f32_16x16x32_bf16 v[116:119], a[12:15], v[184:187], 0\n"
    "s_waitcnt lgkmcnt(7)\n"
    "ds_read_u16_d16_hi v206, v206\n"
    "v_cvt_pk_bf16_f32 v104, v104, v105\n"
    "v_mfma_f32_16x16x32_bf16 v[132:135], v[4:7], v[184:187], 0\n"
    "v_cvt_pk_bf16_f32 v105, v106, v107\n"
    "v_cvt_pk_bf16_f32 v106, v108, v109\n"
    "ds_write_b128 v242, v[128:131] offset:28736\n"
    "v_mfma_f32_16x16x32_bf16 v[128:131], v[0:3], v[184:187], 0\n"
    "s_waitcnt lgkmcnt(6)\n"
    "v_cvt_pk_bf16_f32 v107, v110, v111\n"
    "v_or_b32_e32 v194, v194, v202\n"
    "v_mfma_f32_16x16x32_bf16 v[108:111], a[4:7], v[188:191], 0\n"
    "ds_read_u16 v199, v199\n"
    "v_cvt_pk_bf16_f32 v120, v120, v121\n"
    "ds_write_b128 v242, v[104:107] offset:25664\n"
    "v_mfma_f32_16x16x32_bf16 v[104:107], a[0:3], v[188:191], 0\n"
    "v_cvt_pk_bf16_f32 v121, v122, v123\n"
    "v_cvt_pk_bf16_f32 v122, v124, v125\n"
    "v_cvt_pk_bf16_f32 v123, v126, v127\n"
    "v_cvt_pk_bf16_f32 v136, v136, v137\n"
    "ds_write_b128 v242, v[120:123] offset:27712\n"
    "v_mfma_f32_16x16x32_bf16 v[120:123], a[8:11], v[188:191], 0\n"
    "s_waitcnt lgkmcnt(6)\n"
    "v_pk_fma_f16 v232, v192, v192, s48\n"
    "v_cvt_pk_bf16_f32 v137, v138, v139\n"
    "v_mfma_f32_16x16x32_bf16 v[124:127], a[12:15], v[188:191], 0\n"
    "s_nop 0\n"
    "ds_read_u16_d16_hi v207, v207\n"
    "v_cvt_pk_bf16_f32 v138, v140, v141\n"
    "v_or_b32_e64 v195, v195, v203\n"
    "v_cvt_pk_bf16_f32 v139, v142, v143\n"
    "ds_write_b128 v242, v[136:139] offset:29760\n"
    "v_pk_mul_f16 v232, v192, v232\n"
    "v_mfma_f32_16x16x32_bf16 v[136:139], v[0:3], v[188:191], 0\n"
    "s_waitcnt lgkmcnt(5)\n"
    "v_exp_f16_e32 v234, v232\n"
    "v_mfma_f32_16x16x32_bf16 v[140:143], v[4:7], v[188:191], 0\n"
    "v_or_b32_e64 v197, v197, v205\n"
    "v_exp_f16_sdwa v234, v232 dst_sel:WORD_1 dst_unused:UNUSED_PRESERVE src0_sel:WORD_1\n"
    "v_cvt_pk_f16_f32 v184, v168, v169\n"
    "v_pk_fma_f16 v234, v234, s49, s49\n"
    "v_lshlrev_b32_sdwa v185, 1, v170 dst_sel:DWORD dst_unused:UNUSED_PAD src0_sel:DWORD src1_sel:WORD_1\n"
    "v_lshlrev_b32_sdwa v201, 1, v171 dst_sel:DWORD dst_unused:UNUSED_PAD src0_sel:DWORD src1_sel:WORD_1\n"
    "v_mfma_f32_16x16x32_bf16 v[168:171], a[152:155], v[144:147], 0\n"
    "ds_read_u16 v185, v185\n"
    "ds_read_u16_d16_hi v201, v201\n"
    "v_mfma_f32_16x16x32_bf16 v[168:171], a[200:203], v[148:151], v[168:171]\n"
    "v_lshlrev_b32_sdwa v186, 1, v172 dst_sel:DWORD dst_unused:UNUSED_PAD src0_sel:DWORD src1_sel:WORD_1\n"
    "v_lshlrev_b32_sdwa v202, 1, v173 dst_sel:DWORD dst_unused:UNUSED_PAD src0_sel:DWORD src1_sel:WORD_1\n"
    "v_lshlrev_b32_sdwa v187, 1, v174 dst_sel:DWORD dst_unused:UNUSED_PAD src0_sel:DWORD src1_sel:WORD_1\n"
    "v_lshlrev_b32_sdwa v203, 1, v175 dst_sel:DWORD dst_unused:UNUSED_PAD src0_sel:DWORD src1_sel:WORD_1\n"
    "v_rcp_f16_e64 v232, v234\n"
    "v_mfma_f32_16x16x32_bf16 v[168:171], a[112:115], v[152:155], v[168:171]\n"
    "ds_read_u16 v186, v186\n"
    "ds_read_u16_d16_hi v202, v202\n"
    "v_or_b32_e64 v198, v198, v206\n"
    "v_rcp_f16_sdwa v232, v234 dst_sel:WORD_1 dst_unused:UNUSED_PRESERVE src0_sel:WORD_1\n"
    "v_mfma_f32_16x16x32_bf16 v[172:175], a[156:159], v[144:147], 0\n"
    "s_waitcnt lgkmcnt(4)\n"
    "ds_read_u16 v187, v187\n"
    "ds_read_u16_d16_hi v203, v203\n"
    "v_mfma_f32_16x16x32_bf16 v[172:175], a[204:207], v[148:151], v[172:175]\n"
    "s_barrier\n"
    "v_cvt_pk_f16_f32 v188, v176, v177\n"
    "v_lshlrev_b32_sdwa v189, 1, v178 dst_sel:DWORD dst_unused:UNUSED_PAD src0_sel:DWORD src1_sel:WORD_1\n"
    "v_lshlrev_b32_sdwa v205, 1, v179 dst_sel:DWORD dst_unused:UNUSED_PAD src0_sel:DWORD src1_sel:WORD_1\n"
    "v_lshlrev_b32_sdwa v190, 1, v180 dst_sel:DWORD dst_unused:UNUSED_PAD src0_sel:DWORD src1_sel:WORD_1\n"
    "v_lshlrev_b32_sdwa v206, 1, v181 dst_sel:DWORD dst_unused:UNUSED_PAD src0_sel:DWORD src1_sel:WORD_1\n"
    "v_lshlrev_b32_sdwa v191, 1, v182 dst_sel:DWORD dst_unused:UNUSED_PAD src0_sel:DWORD src1_sel:WORD_1\n"
    "v_fma_mix_f32 v234, v192, v232, 0 op_sel_hi:[1,1,0]\n"
    "v_mfma_f32_16x16x32_bf16 v[172:175], a[116:119], v[152:155], v[172:175]\n"
    "ds_read_u16 v189, v189\n"
    "v_or_b32_e64 v199, v199, v207\n"
    "v_lshlrev_b32_sdwa v207, 1, v183 dst_sel:DWORD dst_unused:UNUSED_PAD src0_sel:DWORD src1_sel:WORD_1\n"
    "v_fma_mix_f32 v232, v192, v232, 0 op_sel:[1,1,0] op_sel_hi:[1,1,0]\n"
    "v_mfma_f32_16x16x32_bf16 v[176:179], a[152:155], v[156:159], 0\n"
    "s_nop 0\n"
    "ds_read_u16_d16_hi v205, v205\n"
    "v_cvt_pk_bf16_f32 v192, v234, v232\n"
    "v_mfma_f32_16x16x32_bf16 v[176:179], a[200:203], v[160:163], v[176:179]\n"
    "s_waitcnt lgkmcnt(6)\n"
    "ds_read_u16 v190, v190\n"
    "v_pk_fma_f16 v232, v196, v196, s48\n"
    "v_mfma_f32_16x16x32_bf16 v[176:179], a[112:115], v[164:167], v[176:179]\n"
    "ds_read_u16_d16_hi v206, v206\n"
    "v_pk_mul_f16 v232, v196, v232\n"
    "v_mfma_f32_16x16x32_bf16 v[180:183], a[156:159], v[156:159], 0\n"
    "ds_read_u16 v191, v191\n"
    "v_exp_f16_e32 v234, v232\n"
    "v_mfma_f32_16x16x32_bf16 v[180:183], a[204:207], v[160:163], v[180:183]\n"
    "s_waitcnt lgkmcnt(7)\n"
    "ds_read_u16_d16_hi v207, v207\n"
    "v_exp_f16_sdwa v234, v232 dst_sel:WORD_1 dst_unused:UNUSED_PRESERVE src0_sel:WORD_1\n"
    "v_mfma_f32_16x16x32_bf16 v[180:183], a[116:119], v[164:167], v[180:183]\n"
    "v_pk_fma_f16 v234, v234, s49, s49\n"
    "v_or_b32_e64 v185, v185, v201\n"
    "v_mfma_f32_16x16x32_bf16 v[96:99], a[16:19], v[192:195], v[96:99]\n"
    "v_rcp_f16_e32 v232, v234\n"
    "v_mfma_f32_16x16x32_bf16 v[100:103], a[20:23], v[192:195], v[100:103]\n"
    "v_rcp_f16_sdwa v232, v234 dst_sel:WORD_1 dst_unused:UNUSED_PRESERVE src0_sel:WORD_1\n"
    "v_mfma_f32_16x16x32_bf16 v[112:115], a[24:27], v[192:195], v[112:115]\n"
    "s_waitcnt lgkmcnt(6)\n"
    "v_fma_mix_f32 v234, v196, v232, 0 op_sel_hi:[1,1,0]\n"
    "v_fma_mix_f32 v232, v196, v232, 0 op_sel:[1,1,0] op_sel_hi:[1,1,0]\n"
    "v_mfma_f32_16x16x32_bf16 v[116:119], a[28:31], v[192:195], v[116:119]\n"
    "v_or_b32_e64 v186, v186, v202\n"
    "v_cvt_pk_bf16_f32 v196, v234, v232\n"
    "v_mfma_f32_16x16x32_bf16 v[128:131], v[8:11], v[192:195], v[128:131]\n"
    "v_pk_fma_f16 v232, v184, v184, s48\n"
    "v_or_b32_e32 v187, v187, v203\n"
    "v_mfma_f32_16x16x32_bf16 v[132:135], v[12:15], v[192:195], v[132:135]\n"
    "s_waitcnt lgkmcnt(4)\n"
    "v_pk_mul_f16 v232, v184, v232\n"
    "v_or_b32_e64 v189, v189, v205\n"
    "v_mfma_f32_16x16x32_bf16 v[104:107], a[16:19], v[196:199], v[104:107]\n"
    "v_exp_f16_e32 v234, v232\n"
    "v_mfma_f32_16x16x32_bf16 v[108:111], a[20:23], v[196:199], v[108:111]\n"
    "v_exp_f16_sdwa v234, v232 dst_sel:WORD_1 dst_unused:UNUSED_PRESERVE src0_sel:WORD_1\n"
    "v_mfma_f32_16x16x32_bf16 v[120:123], a[24:27], v[196:199], v[120:123]\n"
    "v_pk_fma_f16 v234, v234, s49, s49\n"
    "v_lshlrev_b32_sdwa v192, 1, v168 dst_sel:DWORD dst_unused:UNUSED_PAD src0_sel:DWORD src1_sel:WORD_1\n"
    "v_mfma_f32_16x16x32_bf16 v[124:127], a[28:31], v[196:199], v[124:127]\n"
    "s_waitcnt lgkmcnt(2)\n"
    "ds_read_u16 v192, v192\n"
    "v_lshlrev_b32_sdwa v200, 1, v169 dst_sel:DWORD dst_unused:UNUSED_PAD src0_sel:DWORD src1_sel:WORD_1\n"
    "v_mfma_f32_16x16x32_bf16 v[136:139], v[8:11], v[196:199], v[136:139]\n"
    "ds_read_u16_d16_hi v200, v200\n"
    "v_lshlrev_b32_sdwa v193, 1, v170 dst_sel:DWORD dst_unused:UNUSED_PAD src0_sel:DWORD src1_sel:WORD_1\n"
    "v_mfma_f32_16x16x32_bf16 v[140:143], v[12:15], v[196:199], v[140:143]\n"
    "ds_read_u16 v193, v193\n"
    "v_lshlrev_b32_sdwa v201, 1, v171 dst_sel:DWORD dst_unused:UNUSED_PAD src0_sel:DWORD src1_sel:WORD_1\n"
    "s_waitcnt vmcnt(8)\n"
    "v_smfmac_f32_32x32x32_bf16 v[112:127], a[248:251], a[232:239], v241\n"
    "s_waitcnt lgkmcnt(3)\n"
    "ds_read_u16_d16_hi v201, v201\n"
    "ds_read_b128 a[232:235], v243 offset:24640\n"
    "v_lshlrev_b32_sdwa v194, 1, v172 dst_sel:DWORD dst_unused:UNUSED_PAD src0_sel:DWORD src1_sel:WORD_1\n"
    "v_lshlrev_b32_sdwa v202, 1, v173 dst_sel:DWORD dst_unused:UNUSED_PAD src0_sel:DWORD src1_sel:WORD_1\n"
    "v_lshlrev_b32_sdwa v195, 1, v174 dst_sel:DWORD dst_unused:UNUSED_PAD src0_sel:DWORD src1_sel:WORD_1\n"
    "s_waitcnt vmcnt(7)\n"
    "v_smfmac_f32_32x32x32_bf16 v[96:111], a[248:251], a[224:231], v241\n"
    "s_nop 0\n"
    "ds_read_u16 v194, v194\n"
    "ds_read_u16_d16_hi v202, v202\n"
    "v_lshlrev_b32_sdwa v203, 1, v175 dst_sel:DWORD dst_unused:UNUSED_PAD src0_sel:DWORD src1_sel:WORD_1\n"
    "v_rcp_f16_e32 v232, v234\n"
    "s_waitcnt vmcnt(6)\n"
    "v_smfmac_f32_32x32x32_bf16 v[128:143], a[248:251], v[208:215], v241\n"
    "ds_read_b128 a[236:239], v243 offset:25664\n"
    "v_or_b32_e64 v190, v190, v206\n"
    "v_rcp_f16_sdwa v232, v234 dst_sel:WORD_1 dst_unused:UNUSED_PRESERVE src0_sel:WORD_1\n"
    "v_lshlrev_b32_sdwa v196, 1, v176 dst_sel:DWORD dst_unused:UNUSED_PAD src0_sel:DWORD src1_sel:WORD_1\n"
    "v_mfma_f32_16x16x32_bf16 v[168:171], a[160:163], v[144:147], 0\n"
    "ds_read_u16 v195, v195\n"
    "v_lshlrev_b32_sdwa v204, 1, v177 dst_sel:DWORD dst_unused:UNUSED_PAD src0_sel:DWORD src1_sel:WORD_1\n"
    "v_mfma_f32_16x16x32_bf16 v[168:171], a[208:211], v[148:151], v[168:171]\n"
    "ds_read_u16_d16_hi v203, v203\n"
    "v_lshlrev_b32_sdwa v197, 1, v178 dst_sel:DWORD dst_unused:UNUSED_PAD src0_sel:DWORD src1_sel:WORD_1\n"
    "v_mfma_f32_16x16x32_bf16 v[168:171], a[120:123], v[152:155], v[168:171]\n"
    "v_lshlrev_b32_sdwa v205, 1, v179 dst_sel:DWORD dst_unused:UNUSED_PAD src0_sel:DWORD src1_sel:WORD_1\n"
    "v_lshlrev_b32_sdwa v198, 1, v180 dst_sel:DWORD dst_unused:UNUSED_PAD src0_sel:DWORD src1_sel:WORD_1\n"
    "v_mfma_f32_16x16x32_bf16 v[172:175], a[164:167], v[144:147], 0\n"
    "v_lshlrev_b32_sdwa v206, 1, v181 dst_sel:DWORD dst_unused:UNUSED_PAD src0_sel:DWORD src1_sel:WORD_1\n"
    "v_lshlrev_b32_sdwa v199, 1, v182 dst_sel:DWORD dst_unused:UNUSED_PAD src0_sel:DWORD src1_sel:WORD_1\n"
    "v_fma_mix_f32 v234, v184, v232, 0 op_sel_hi:[1,1,0]\n"
    "v_mfma_f32_16x16x32_bf16 v[172:175], a[212:215], v[148:151], v[172:175]\n"
    "s_waitcnt lgkmcnt(7)\n"
    "ds_read_u16 v196, v196\n"
    "ds_read_u16_d16_hi v204, v204\n"
    "v_mfma_f32_16x16x32_bf16 v[172:175], v[52:55], v[152:155], v[172:175]\n"
    "v_or_b32_e32 v191, v191, v207\n"
    "v_lshlrev_b32_sdwa v207, 1, v183 dst_sel:DWORD dst_unused:UNUSED_PAD src0_sel:DWORD src1_sel:WORD_1\n"
    "v_fma_mix_f32 v232, v184, v232, 0 op_sel:[1,1,0] op_sel_hi:[1,1,0]\n"
    "v_mfma_f32_16x16x32_bf16 v[176:179], a[160:163], v[156:159], 0\n"
    "ds_read_u16 v197, v197\n"
    "v_cvt_pk_bf16_f32 v184, v234, v232\n"
    "v_mfma_f32_16x16x32_bf16 v[176:179], a[208:211], v[160:163], v[176:179]\n"
    "ds_read_u16_d16_hi v205, v205\n"
    "v_pk_fma_f16 v232, v188, v188, s48\n"
    "v_mfma_f32_16x16x32_bf16 v[176:179], a[120:123], v[164:167], v[176:179]\n"
    "s_waitcnt lgkmcnt(8)\n"
    "v_pk_mul_f16 v232, v188, v232\n"
    "v_or_b32_e32 v192, v192, v200\n"
    "v_mfma_f32_16x16x32_bf16 v[180:183], a[164:167], v[156:159], 0\n"
    "ds_read_u16 v198, v198\n"
    "v_exp_f16_e32 v234, v232\n"
    "s_waitcnt lgkmcnt(7)\n"
    "v_smfmac_f32_32x32x32_bf16 v[96:111], a[248:251], a[232:239], v241\n"
    "ds_read_u16_d16_hi v206, v206\n"
    "v_exp_f16_sdwa v234, v232 dst_sel:WORD_1 dst_unused:UNUSED_PRESERVE src0_sel:WORD_1\n"
    "v_or_b32_e64 v193, v193, v201\n"
    "v_pk_fma_f16 v234, v234, s49, s49\n"
    "v_mfma_f32_16x16x32_bf16 v[180:183], a[212:215], v[160:163], v[180:183]\n"
    "ds_read_u16 v199, v199\n"
    "v_rcp_f16_e32 v232, v234\n"
    "v_mfma_f32_16x16x32_bf16 v[180:183], v[52:55], v[164:167], v[180:183]\n"
    "v_rcp_f16_sdwa v232, v234 dst_sel:WORD_1 dst_unused:UNUSED_PRESERVE src0_sel:WORD_1\n"
    "v_mfma_f32_16x16x32_bf16 v[112:115], a[40:43], v[184:187], v[112:115]\n"
    "s_waitcnt lgkmcnt(7)\n"
    "ds_read_u16_d16_hi v207, v207\n"
    "v_fma_mix_f32 v234, v188, v232, 0 op_sel_hi:[1,1,0]\n"
    "v_mfma_f32_16x16x32_bf16 v[96:99], a[32:35], v[184:187], v[96:99]\n"
    "ds_read_b128 a[232:235], v243 offset:26688\n"
    "v_fma_mix_f32 v232, v188, v232, 0 op_sel:[1,1,0] op_sel_hi:[1,1,0]\n"
    "v_mfma_f32_16x16x32_bf16 v[100:103], a[36:39], v[184:187], v[100:103]\n"
    "ds_read_b128 a[236:239], v243 offset:27712\n"
    "v_cvt_pk_bf16_f32 v188, v234, v232\n"
    "v_mfma_f32_16x16x32_bf16 v[116:119], a[44:47], v[184:187], v[116:119]\n"
    "s_waitcnt lgkmcnt(8)\n"
    "v_lshlrev_b32_sdwa v201, 1, v171 dst_sel:DWORD dst_unused:UNUSED_PAD src0_sel:DWORD src1_sel:WORD_1\n"
    "v_or_b32_e32 v194, v194, v202\n"
    "v_mfma_f32_16x16x32_bf16 v[128:131], v[16:19], v[184:187], v[128:131]\n"
    "ds_read_u16_d16_hi v201, v201\n"
    "v_lshlrev_b32_sdwa v202, 1, v173 dst_sel:DWORD dst_unused:UNUSED_PAD src0_sel:DWORD src1_sel:WORD_1\n"
    "v_mfma_f32_16x16x32_bf16 v[132:135], v[20:23], v[184:187], v[132:135]\n"
    "ds_read_u16_d16_hi v202, v202\n"
    "v_cvt_pk_f16_f32 v184, v168, v169\n"
    "v_mfma_f32_16x16x32_bf16 v[104:107], a[32:35], v[188:191], v[104:107]\n"
    "v_lshlrev_b32_sdwa v185, 1, v170 dst_sel:DWORD dst_unused:UNUSED_PAD src0_sel:DWORD src1_sel:WORD_1\n"
    "v_lshlrev_b32_sdwa v186, 1, v172 dst_sel:DWORD dst_unused:UNUSED_PAD src0_sel:DWORD src1_sel:WORD_1\n"
    "v_mfma_f32_16x16x32_bf16 v[108:111], a[36:39], v[188:191], v[108:111]\n"
    "s_waitcnt lgkmcnt(8)\n"
    "ds_read_u16 v185, v185\n"
    "v_lshlrev_b32_sdwa v187, 1, v174 dst_sel:DWORD dst_unused:UNUSED_PAD src0_sel:DWORD src1_sel:WORD_1\n"
    "v_mfma_f32_16x16x32_bf16 v[120:123], a[40:43], v[188:191], v[120:123]\n"
    "v_or_b32_e32 v195, v195, v203\n"
    "v_lshlrev_b32_sdwa v203, 1, v175 dst_sel:DWORD dst_unused:UNUSED_PAD src0_sel:DWORD src1_sel:WORD_1\n"
    "v_mfma_f32_16x16x32_bf16 v[124:127], a[44:47], v[188:191], v[124:127]\n"
    "ds_read_u16 v186, v186\n"
    "v_or_b32_e32 v196, v196, v204\n"
    "v_mfma_f32_16x16x32_bf16 v[136:139], v[16:19], v[188:191], v[136:139]\n"
    "s_waitcnt lgkmcnt(8)\n"
    "ds_read_u16 v187, v187\n"
    "v_or_b32_e64 v197, v197, v205\n"
    "v_mfma_f32_16x16x32_bf16 v[140:143], v[20:23], v[188:191], v[140:143]\n"
    "v_cvt_pk_f16_f32 v188, v176, v177\n"
    "v_lshlrev_b32_sdwa v189, 1, v178 dst_sel:DWORD dst_unused:UNUSED_PAD src0_sel:DWORD src1_sel:WORD_1\n"
    "v_mfma_f32_16x16x32_bf16 v[168:171], a[168:171], v[144:147], 0\n"
    "ds_read_u16_d16_hi v203, v203\n"
    "v_lshlrev_b32_sdwa v205, 1, v179 dst_sel:DWORD dst_unused:UNUSED_PAD src0_sel:DWORD src1_sel:WORD_1\n"
    "v_mfma_f32_16x16x32_bf16 v[168:171], a[216:219], v[148:151], v[168:171]\n"
    "v_lshlrev_b32_sdwa v190, 1, v180 dst_sel:DWORD dst_unused:UNUSED_PAD src0_sel:DWORD src1_sel:WORD_1\n"
    "v_lshlrev_b32_sdwa v191, 1, v182 dst_sel:DWORD dst_unused:UNUSED_PAD src0_sel:DWORD src1_sel:WORD_1\n"
    "v_mfma_f32_16x16x32_bf16 v[168:171], a[124:127], v[152:155], v[168:171]\n"
    "s_waitcnt lgkmcnt(6)\n"
    "ds_read_u16 v189, v189\n"
    "v_or_b32_e32 v198, v198, v206\n"
    "v_smfmac_f32_32x32x32_bf16 v[112:127], a[248:251], a[232:239], v241\n"
    "ds_read_b128 a[232:235], v243 offset:28736\n"
    "v_lshlrev_b32_sdwa v206, 1, v181 dst_sel:DWORD dst_unused:UNUSED_PAD src0_sel:DWORD src1_sel:WORD_1\n"
    "v_or_b32_e64 v199, v199, v207\n"
    "v_lshlrev_b32_sdwa v207, 1, v183 dst_sel:DWORD dst_unused:UNUSED_PAD src0_sel:DWORD src1_sel:WORD_1\n"
    "v_pk_fma_f16 v232, v184, v184, s48\n"
    "v_mfma_f32_16x16x32_bf16 v[172:175], a[172:175], v[144:147], 0\n"
    "ds_read_u16_d16_hi v205, v205\n"
    "v_pk_mul_f16 v232, v184, v232\n"
    "v_mfma_f32_16x16x32_bf16 v[172:175], a[220:223], v[148:151], v[172:175]\n"
    "ds_read_b128 a[236:239], v243 offset:29760\n"
    "v_exp_f16_e64 v234, v232\n"
    "v_mfma_f32_16x16x32_bf16 v[172:175], v[60:63], v[152:155], v[172:175]\n"
    "ds_read_u16 v190, v190\n"
    "v_exp_f16_sdwa v234, v232 dst_sel:WORD_1 dst_unused:UNUSED_PRESERVE src0_sel:WORD_1\n"
    "v_mfma_f32_16x16x32_bf16 v[176:179], a[168:171], v[156:159], 0\n"
    "s_waitcnt lgkmcnt(8)\n"
    "v_or_b32_e32 v185, v185, v201\n"
    "v_pk_fma_f16 v234, v234, s49, s49\n"
    "v_mfma_f32_16x16x32_bf16 v[176:179], a[216:219], v[160:163], v[176:179]\n"
    "ds_read_b128 v[144:147], v242 offset:0\n"
    "ds_read_u16_d16_hi v206, v206\n"
    "v_mfma_f32_16x16x32_bf16 v[176:179], a[124:127], v[164:167], v[176:179]\n"
    "s_waitcnt lgkmcnt(9)\n"
    "v_or_b32_e32 v186, v186, v202\n"
    "v_lshlrev_b32_sdwa v201, 1, v171 dst_sel:DWORD dst_unused:UNUSED_PAD src0_sel:DWORD src1_sel:WORD_1\n"
    "v_mfma_f32_16x16x32_bf16 v[180:183], a[172:175], v[156:159], 0\n"
    "ds_read_b128 v[148:151], v242 offset:1024\n"
    "v_lshlrev_b32_sdwa v202, 1, v173 dst_sel:DWORD dst_unused:UNUSED_PAD src0_sel:DWORD src1_sel:WORD_1\n"
    "v_mfma_f32_16x16x32_bf16 v[180:183], a[220:223], v[160:163], v[180:183]\n"
    "ds_read_u16 v191, v191\n"
    "v_rcp_f16_e64 v232, v234\n"
    "v_mfma_f32_16x16x32_bf16 v[180:183], v[60:63], v[164:167], v[180:183]\n"
    "s_waitcnt lgkmcnt(9)\n"
    "v_or_b32_e32 v187, v187, v203\n"
    "v_lshlrev_b32_sdwa v203, 1, v175 dst_sel:DWORD dst_unused:UNUSED_PAD src0_sel:DWORD src1_sel:WORD_1\n"
    "v_mfma_f32_16x16x32_bf16 v[96:99], a[48:51], v[192:195], v[96:99]\n"
    "ds_read_b128 v[152:155], v242 offset:2048\n"
    "v_rcp_f16_sdwa v232, v234 dst_sel:WORD_1 dst_unused:UNUSED_PRESERVE src0_sel:WORD_1\n"
    "v_mfma_f32_16x16x32_bf16 v[100:103], a[52:55], v[192:195], v[100:103]\n"
    "ds_read_u16_d16_hi v207, v207\n"
    "v_fma_mix_f32 v234, v184, v232, 0 op_sel_hi:[1,1,0]\n"
    "v_mfma_f32_16x16x32_bf16 v[112:115], a[56:59], v[192:195], v[112:115]\n"
    "ds_read_b128 v[156:159], v242 offset:3072\n"
    "v_fma_mix_f32 v232, v184, v232, 0 op_sel:[1,1,0] op_sel_hi:[1,1,0]\n"
    "v_mfma_f32_16x16x32_bf16 v[116:119], a[60:63], v[192:195], v[116:119]\n"
    "ds_read_b128 v[160:163], v242 offset:4096\n"
    "s_waitcnt lgkmcnt(9)\n"
    "v_or_b32_e32 v189, v189, v205\n"
    "v_mfma_f32_16x16x32_bf16 v[128:131], v[24:27], v[192:195], v[128:131]\n"
    "ds_read_b128 v[164:167], v242 offset:5120\n"
    "v_lshlrev_b32_sdwa v205, 1, v179 dst_sel:DWORD dst_unused:UNUSED_PAD src0_sel:DWORD src1_sel:WORD_1\n"
    "v_mfma_f32_16x16x32_bf16 v[132:135], v[28:31], v[192:195], v[132:135]\n"
    "ds_read_u16_d16_hi v201, v201\n"
    "v_cvt_pk_f16_f32 v192, v168, v169\n"
    "v_mfma_f32_16x16x32_bf16 v[104:107], a[48:51], v[196:199], v[104:107]\n"
    "s_nop 0\n"
    "v_lshlrev_b32_sdwa v193, 1, v170 dst_sel:DWORD dst_unused:UNUSED_PAD src0_sel:DWORD src1_sel:WORD_1\n"
    "v_lshlrev_b32_sdwa v194, 1, v172 dst_sel:DWORD dst_unused:UNUSED_PAD src0_sel:DWORD src1_sel:WORD_1\n"
    "v_mfma_f32_16x16x32_bf16 v[108:111], a[52:55], v[196:199], v[108:111]\n"
    "s_waitcnt lgkmcnt(9)\n"
    "ds_read_u16 v193, v193\n"
    "v_lshlrev_b32_sdwa v195, 1, v174 dst_sel:DWORD dst_unused:UNUSED_PAD src0_sel:DWORD src1_sel:WORD_1\n"
    "v_mfma_f32_16x16x32_bf16 v[120:123], a[56:59], v[196:199], v[120:123]\n"
    "v_cvt_pk_bf16_f32 v184, v234, v232\n"
    "v_pk_fma_f16 v232, v188, v188, s48\n"
    "v_mfma_f32_16x16x32_bf16 v[124:127], a[60:63], v[196:199], v[124:127]\n"
    "s_waitcnt lgkmcnt(8)\n"
    "ds_read_u16 v194, v194\n"
    "v_or_b32_e32 v190, v190, v206\n"
    "v_mfma_f32_16x16x32_bf16 v[136:139], v[24:27], v[196:199], v[136:139]\n"
    "ds_read_u16_d16_hi v202, v202\n"
    "v_lshlrev_b32_sdwa v206, 1, v181 dst_sel:DWORD dst_unused:UNUSED_PAD src0_sel:DWORD src1_sel:WORD_1\n"
    "v_mfma_f32_16x16x32_bf16 v[140:143], v[28:31], v[196:199], v[140:143]\n"
    "v_cvt_pk_f16_f32 v196, v176, v177\n"
    "v_lshlrev_b32_sdwa v197, 1, v178 dst_sel:DWORD dst_unused:UNUSED_PAD src0_sel:DWORD src1_sel:WORD_1\n"
    "v_mfma_f32_16x16x32_bf16 v[168:171], a[128:131], v[144:147], 0\n"
    "ds_read_u16 v195, v195\n"
    "v_lshlrev_b32_sdwa v198, 1, v180 dst_sel:DWORD dst_unused:UNUSED_PAD src0_sel:DWORD src1_sel:WORD_1\n"
    "v_mfma_f32_16x16x32_bf16 v[168:171], a[176:179], v[148:151], v[168:171]\n"
    "s_waitcnt lgkmcnt(9)\n"
    "v_lshlrev_b32_sdwa v199, 1, v182 dst_sel:DWORD dst_unused:UNUSED_PAD src0_sel:DWORD src1_sel:WORD_1\n"
    "v_pk_mul_f16 v232, v188, v232\n"
    "v_smfmac_f32_32x32x32_bf16 v[128:143], a[248:251], a[232:239], v241\n"
    "s_waitcnt lgkmcnt(7)\n"
    "ds_read_u16_d16_hi v203, v203\n"
    "v_or_b32_e64 v191, v191, v207\n"
    "v_lshlrev_b32_sdwa v207, 1, v183 dst_sel:DWORD dst_unused:UNUSED_PAD src0_sel:DWORD src1_sel:WORD_1\n"
    "v_exp_f16_e64 v234, v232\n"
    "v_mfma_f32_16x16x32_bf16 v[168:171], a[96:99], v[152:155], v[168:171]\n"
    "ds_read_u16 v197, v197\n"
    "v_exp_f16_sdwa v234, v232 dst_sel:WORD_1 dst_unused:UNUSED_PRESERVE src0_sel:WORD_1\n"
    "v_mfma_f32_16x16x32_bf16 v[172:175], a[132:135], v[144:147], 0\n"
    "ds_read_u16_d16_hi v205, v205\n"
    "v_pk_fma_f16 v234, v234, s49, s49\n"
    "v_mfma_f32_16x16x32_bf16 v[172:175], a[180:183], v[148:151], v[172:175]\n"
    "v_rcp_f16_e64 v232, v234\n"
    "v_mfma_f32_16x16x32_bf16 v[172:175], v[48:51], v[152:155], v[172:175]\n"
    "ds_read_u16 v198, v198\n"
    "v_rcp_f16_sdwa v232, v234 dst_sel:WORD_1 dst_unused:UNUSED_PRESERVE src0_sel:WORD_1\n"
    "v_mfma_f32_16x16x32_bf16 v[176:179], a[128:131], v[156:159], 0\n"
    "s_waitcnt lgkmcnt(7)\n"
    "ds_read_u16_d16_hi v206, v206\n"
    "v_or_b32_e32 v193, v193, v201\n"
    "v_mfma_f32_16x16x32_bf16 v[176:179], a[176:179], v[160:163], v[176:179]\n"
    "v_fma_mix_f32 v234, v188, v232, 0 op_sel_hi:[1,1,0]\n"
    "v_fma_mix_f32 v232, v188, v232, 0 op_sel:[1,1,0] op_sel_hi:[1,1,0]\n"
    "v_mfma_f32_16x16x32_bf16 v[176:179], a[96:99], v[164:167], v[176:179]\n"
    "ds_read_u16 v199, v199\n"
    "v_cvt_pk_bf16_f32 v188, v234, v232\n"
    "v_mfma_f32_16x16x32_bf16 v[180:183], a[132:135], v[156:159], 0\n"
    "v_pk_fma_f16 v232, v192, v192, s48\n"
    "v_lshlrev_b32_sdwa v200, 1, v169 dst_sel:DWORD dst_unused:UNUSED_PAD src0_sel:DWORD src1_sel:WORD_1\n"
    "v_mfma_f32_16x16x32_bf16 v[180:183], a[180:183], v[160:163], v[180:183]\n"
    "s_waitcnt lgkmcnt(7)\n"
    "ds_read_u16_d16_hi v207, v207\n"
    "v_or_b32_e32 v194, v194, v202\n"
    "v_mfma_f32_16x16x32_bf16 v[180:183], v[48:51], v[164:167], v[180:183]\n"
    "v_pk_mul_f16 v232, v192, v232\n"
    "v_lshlrev_b32_sdwa v201, 1, v171 dst_sel:DWORD dst_unused:UNUSED_PAD src0_sel:DWORD src1_sel:WORD_1\n"
    "v_mfma_f32_16x16x32_bf16 v[96:99], a[64:67], v[184:187], v[96:99]\n"
    "ds_read_u16_d16_hi v200, v200\n"
    "v_exp_f16_e32 v234, v232\n"
    "v_mfma_f32_16x16x32_bf16 v[100:103], a[68:71], v[184:187], v[100:103]\n"
    "s_waitcnt lgkmcnt(7)\n"
    "ds_read_u16_d16_hi v201, v201\n"
    "v_or_b32_e64 v195, v195, v203\n"
    "v_mfma_f32_16x16x32_bf16 v[112:115], a[72:75], v[184:187], v[112:115]\n"
    "v_exp_f16_sdwa v234, v232 dst_sel:WORD_1 dst_unused:UNUSED_PRESERVE src0_sel:WORD_1\n"
    "v_mfma_f32_16x16x32_bf16 v[116:119], a[76:79], v[184:187], v[116:119]\n"
    "v_pk_fma_f16 v234, v234, s49, s49\n"
    "v_lshlrev_b32_sdwa v202, 1, v173 dst_sel:DWORD dst_unused:UNUSED_PAD src0_sel:DWORD src1_sel:WORD_1\n"
    "v_mfma_f32_16x16x32_bf16 v[128:131], v[32:35], v[184:187], v[128:131]\n"
    "ds_read_u16_d16_hi v202, v202\n"
    "s_waitcnt lgkmcnt(7)\n"
    "v_or_b32_e32 v197, v197, v205\n"
    "v_mfma_f32_16x16x32_bf16 v[132:135], v[36:39], v[184:187], v[132:135]\n"
    "v_lshlrev_b32_sdwa v184, 1, v168 dst_sel:DWORD dst_unused:UNUSED_PAD src0_sel:DWORD src1_sel:WORD_1\n"
    "v_lshlrev_b32_sdwa v185, 1, v170 dst_sel:DWORD dst_unused:UNUSED_PAD src0_sel:DWORD src1_sel:WORD_1\n"
    "v_mfma_f32_16x16x32_bf16 v[104:107], a[64:67], v[188:191], v[104:107]\n"
    "ds_read_u16 v184, v184\n"
    "ds_read_u16 v185, v185\n"
    "v_mfma_f32_16x16x32_bf16 v[108:111], a[68:71], v[188:191], v[108:111]\n"
    "v_lshlrev_b32_sdwa v186, 1, v172 dst_sel:DWORD dst_unused:UNUSED_PAD src0_sel:DWORD src1_sel:WORD_1\n"
    "v_lshlrev_b32_sdwa v187, 1, v174 dst_sel:DWORD dst_unused:UNUSED_PAD src0_sel:DWORD src1_sel:WORD_1\n"
    "v_mfma_f32_16x16x32_bf16 v[120:123], a[72:75], v[188:191], v[120:123]\n"
    "s_waitcnt lgkmcnt(7)\n"
    "ds_read_u16 v186, v186\n"
    "v_lshlrev_b32_sdwa v203, 1, v175 dst_sel:DWORD dst_unused:UNUSED_PAD src0_sel:DWORD src1_sel:WORD_1\n"
    "v_mfma_f32_16x16x32_bf16 v[124:127], a[76:79], v[188:191], v[124:127]\n"
    "v_rcp_f16_e32 v232, v234\n"
    "v_mfma_f32_16x16x32_bf16 v[136:139], v[32:35], v[188:191], v[136:139]\n"
    "ds_read_u16 v187, v187\n"
    "v_or_b32_e32 v198, v198, v206\n"
    "v_mfma_f32_16x16x32_bf16 v[140:143], v[36:39], v[188:191], v[140:143]\n"
    "s_waitcnt lgkmcnt(7)\n"
    "v_rcp_f16_sdwa v232, v234 dst_sel:WORD_1 dst_unused:UNUSED_PRESERVE src0_sel:WORD_1\n"
    "v_mfma_f32_16x16x32_bf16 v[168:171], a[136:139], v[144:147], 0\n"
    "ds_read_u16_d16_hi v203, v203\n"
    "v_lshlrev_b32_sdwa v188, 1, v176 dst_sel:DWORD dst_unused:UNUSED_PAD src0_sel:DWORD src1_sel:WORD_1\n"
    "v_mfma_f32_16x16x32_bf16 v[168:171], a[184:187], v[148:151], v[168:171]\n"
    "v_lshlrev_b32_sdwa v204, 1, v177 dst_sel:DWORD dst_unused:UNUSED_PAD src0_sel:DWORD src1_sel:WORD_1\n"
    "v_lshlrev_b32_sdwa v189, 1, v178 dst_sel:DWORD dst_unused:UNUSED_PAD src0_sel:DWORD src1_sel:WORD_1\n"
    "v_mfma_f32_16x16x32_bf16 v[168:171], a[100:103], v[152:155], v[168:171]\n"
    "v_lshlrev_b32_sdwa v205, 1, v179 dst_sel:DWORD dst_unused:UNUSED_PAD src0_sel:DWORD src1_sel:WORD_1\n"
    "v_lshlrev_b32_sdwa v190, 1, v180 dst_sel:DWORD dst_unused:UNUSED_PAD src0_sel:DWORD src1_sel:WORD_1\n"
    "v_mfma_f32_16x16x32_bf16 v[172:175], a[140:143], v[144:147], 0\n"
    "v_lshlrev_b32_sdwa v206, 1, v181 dst_sel:DWORD dst_unused:UNUSED_PAD src0_sel:DWORD src1_sel:WORD_1\n"
    "v_lshlrev_b32_sdwa v191, 1, v182 dst_sel:DWORD dst_unused:UNUSED_PAD src0_sel:DWORD src1_sel:WORD_1\n"
    "v_fma_mix_f32 v234, v192, v232, 0 op_sel_hi:[1,1,0]\n"
    "v_mfma_f32_16x16x32_bf16 v[172:175], a[188:191], v[148:151], v[172:175]\n"
    "ds_read_u16 v188, v188\n"
    "ds_read_u16_d16_hi v204, v204\n"
    "v_mfma_f32_16x16x32_bf16 v[172:175], v[56:59], v[152:155], v[172:175]\n"
    "v_or_b32_e64 v199, v199, v207\n"
    "v_lshlrev_b32_sdwa v207, 1, v183 dst_sel:DWORD dst_unused:UNUSED_PAD src0_sel:DWORD src1_sel:WORD_1\n"
    "v_fma_mix_f32 v232, v192, v232, 0 op_sel:[1,1,0] op_sel_hi:[1,1,0]\n"
    "v_mfma_f32_16x16x32_bf16 v[176:179], a[136:139], v[156:159], 0\n"
    "s_waitcnt lgkmcnt(6)\n"
    "ds_read_u16 v189, v189\n"
    "v_cvt_pk_bf16_f32 v192, v234, v232\n"
    "v_mfma_f32_16x16x32_bf16 v[176:179], a[184:187], v[160:163], v[176:179]\n"
    "ds_read_u16_d16_hi v205, v205\n"
    "v_pk_fma_f16 v232, v196, v196, s48\n"
    "v_mfma_f32_16x16x32_bf16 v[176:179], a[100:103], v[164:167], v[176:179]\n"
    "s_waitcnt lgkmcnt(7)\n"
    "ds_read_u16 v190, v190\n"
    "v_pk_mul_f16 v232, v196, v232\n"
    "v_mfma_f32_16x16x32_bf16 v[180:183], a[140:143], v[156:159], 0\n"
    "ds_read_u16_d16_hi v206, v206\n"
    "v_exp_f16_e32 v234, v232\n"
    "v_mfma_f32_16x16x32_bf16 v[180:183], a[188:191], v[160:163], v[180:183]\n"
    "s_waitcnt lgkmcnt(8)\n"
    "buffer_load_dwordx4 a[232:235], v245, s[40:43], s58 offen offset:64\n"
    "buffer_load_dwordx4 a[224:227], v245, s[40:43], s58 offen offset:0\n"
    "buffer_load_dwordx4 v[208:211], v245, s[40:43], s58 offen offset:128\n"
    "ds_read_u16 v191, v191\n"
    "v_mfma_f32_16x16x32_bf16 v[180:183], v[56:59], v[164:167], v[180:183]\n"
    "v_exp_f16_sdwa v234, v232 dst_sel:WORD_1 dst_unused:UNUSED_PRESERVE src0_sel:WORD_1\n"
    "v_or_b32_e64 v184, v184, v200\n"
    "v_pk_fma_f16 v234, v234, s49, s49\n"
    "v_mfma_f32_16x16x32_bf16 v[96:99], a[80:83], v[192:195], v[96:99]\n"
    "s_waitcnt lgkmcnt(7)\n"
    "ds_read_u16_d16_hi v207, v207\n"
    "v_rcp_f16_e32 v232, v234\n"
    "v_mfma_f32_16x16x32_bf16 v[100:103], a[84:87], v[192:195], v[100:103]\n"
    "v_rcp_f16_sdwa v232, v234 dst_sel:WORD_1 dst_unused:UNUSED_PRESERVE src0_sel:WORD_1\n"
    "v_mfma_f32_16x16x32_bf16 v[112:115], a[88:91], v[192:195], v[112:115]\n"
    "v_fma_mix_f32 v234, v196, v232, 0 op_sel_hi:[1,1,0]\n"
    "v_fma_mix_f32 v232, v196, v232, 0 op_sel:[1,1,0] op_sel_hi:[1,1,0]\n"
    "v_mfma_f32_16x16x32_bf16 v[116:119], a[92:95], v[192:195], v[116:119]\n"
    "s_waitcnt lgkmcnt(6)\n"
    "v_cvt_pk_bf16_f32 v196, v234, v232\n"
    "v_or_b32_e32 v185, v185, v201\n"
    "v_mfma_f32_16x16x32_bf16 v[128:131], v[40:43], v[192:195], v[128:131]\n"
    "v_lshlrev_b32_sdwa v201, 1, v171 dst_sel:DWORD dst_unused:UNUSED_PAD src0_sel:DWORD src1_sel:WORD_1\n"
    "v_or_b32_e32 v186, v186, v202\n"
    "v_mfma_f32_16x16x32_bf16 v[132:135], v[44:47], v[192:195], v[132:135]\n"
    "ds_read_u16_d16_hi v201, v201\n"
    "v_cvt_pk_f16_f32 v192, v168, v169\n"
    "v_mfma_f32_16x16x32_bf16 v[104:107], a[80:83], v[196:199], v[104:107]\n"
    "s_waitcnt lgkmcnt(5)\n"
    "v_lshlrev_b32_sdwa v193, 1, v170 dst_sel:DWORD dst_unused:UNUSED_PAD src0_sel:DWORD src1_sel:WORD_1\n"
    "v_lshlrev_b32_sdwa v194, 1, v172 dst_sel:DWORD dst_unused:UNUSED_PAD src0_sel:DWORD src1_sel:WORD_1\n"
    "v_mfma_f32_16x16x32_bf16 v[108:111], a[84:87], v[196:199], v[108:111]\n"
    "ds_read_u16 v193, v193\n"
    "buffer_load_dwordx4 a[236:239], v245, s[40:43], s89 offen offset:64\n"
    "buffer_load_dwordx4 a[228:231], v245, s[40:43], s89 offen offset:0\n"
    "buffer_load_dwordx4 v[212:215], v245, s[40:43], s89 offen offset:128\n"
    "v_mfma_f32_16x16x32_bf16 v[120:123], a[88:91], v[196:199], v[120:123]\n"
    "s_nop 0\n"
    "ds_read_u16 v194, v194\n"
    "v_lshlrev_b32_sdwa v202, 1, v173 dst_sel:DWORD dst_unused:UNUSED_PAD src0_sel:DWORD src1_sel:WORD_1\n"
    "v_mfma_f32_16x16x32_bf16 v[124:127], a[92:95], v[196:199], v[124:127]\n"
    "s_waitcnt lgkmcnt(5)\n"
    "ds_read_u16_d16_hi v202, v202\n"
    "v_lshlrev_b32_sdwa v195, 1, v174 dst_sel:DWORD dst_unused:UNUSED_PAD src0_sel:DWORD src1_sel:WORD_1\n"
    "v_mfma_f32_16x16x32_bf16 v[136:139], v[40:43], v[196:199], v[136:139]\n"
    "ds_read_u16 v195, v195\n"
    "v_or_b32_e64 v187, v187, v203\n"
    "v_mfma_f32_16x16x32_bf16 v[140:143], v[44:47], v[196:199], v[140:143]\n"
    "v_lshlrev_b32_sdwa v203, 1, v175 dst_sel:DWORD dst_unused:UNUSED_PAD src0_sel:DWORD src1_sel:WORD_1\n"
    "v_cvt_pk_f16_f32 v196, v176, v177\n"
    "v_mfma_f32_16x16x32_bf16 v[168:171], a[144:147], v[144:147], 0\n"
    "s_nop 0\n"
    "ds_read_u16_d16_hi v203, v203\n"
    "v_lshlrev_b32_sdwa v197, 1, v178 dst_sel:DWORD dst_unused:UNUSED_PAD src0_sel:DWORD src1_sel:WORD_1\n"
    "v_mfma_f32_16x16x32_bf16 v[168:171], a[192:195], v[148:151], v[168:171]\n"
    "s_waitcnt lgkmcnt(6)\n"
    "ds_read_u16 v197, v197\n"
    "v_lshlrev_b32_sdwa v198, 1, v180 dst_sel:DWORD dst_unused:UNUSED_PAD src0_sel:DWORD src1_sel:WORD_1\n"
    "v_mfma_f32_16x16x32_bf16 v[168:171], a[104:107], v[152:155], v[168:171]\n"
    "v_lshlrev_b32_sdwa v199, 1, v182 dst_sel:DWORD dst_unused:UNUSED_PAD src0_sel:DWORD src1_sel:WORD_1\n"
    "v_or_b32_e64 v188, v188, v204\n"
    "v_mfma_f32_16x16x32_bf16 v[172:175], a[148:151], v[144:147], 0\n"
    "ds_read_u16 v198, v198\n"
    "v_or_b32_e32 v189, v189, v205\n"
    "v_mfma_f32_16x16x32_bf16 v[172:175], a[196:199], v[148:151], v[172:175]\n"
    "ds_read_u16 v199, v199\n"
    "v_lshlrev_b32_sdwa v205, 1, v179 dst_sel:DWORD dst_unused:UNUSED_PAD src0_sel:DWORD src1_sel:WORD_1\n"
    "v_mfma_f32_16x16x32_bf16 v[172:175], a[108:111], v[152:155], v[172:175]\n"
    "s_waitcnt lgkmcnt(7)\n"
    "ds_read_u16_d16_hi v205, v205\n"
    "v_cvt_pk_bf16_f32 v96, v96, v97\n"
    "v_mfma_f32_16x16x32_bf16 v[176:179], a[144:147], v[156:159], 0\n"
    "v_cvt_pk_bf16_f32 v97, v98, v99\n"
    "v_cvt_pk_bf16_f32 v98, v100, v101\n"
    "v_mfma_f32_16x16x32_bf16 v[176:179], a[192:195], v[160:163], v[176:179]\n"
    "v_cvt_pk_bf16_f32 v99, v102, v103\n"
    "v_or_b32_e32 v190, v190, v206\n"
    "v_mfma_f32_16x16x32_bf16 v[176:179], a[104:107], v[164:167], v[176:179]\n"
    "s_waitcnt lgkmcnt(6)\n"
    "ds_write_b128 v243, v[96:99] offset:24640\n"
    "v_mfma_f32_16x16x32_bf16 v[96:99], a[0:3], v[184:187], 0\n"
    "v_lshlrev_b32_sdwa v206, 1, v181 dst_sel:DWORD dst_unused:UNUSED_PAD src0_sel:DWORD src1_sel:WORD_1\n"
    "v_cvt_pk_bf16_f32 v112, v112, v113\n"
    "v_mfma_f32_16x16x32_bf16 v[100:103], a[4:7], v[184:187], 0\n"
    "v_cvt_pk_bf16_f32 v113, v114, v115\n"
    "v_cvt_pk_bf16_f32 v114, v116, v117\n"
    "v_cvt_pk_bf16_f32 v115, v118, v119\n"
    "ds_write_b128 v243, v[112:115] offset:26688\n"
    "v_mfma_f32_16x16x32_bf16 v[112:115], a[8:11], v[184:187], 0\n"
    "s_waitcnt lgkmcnt(5)\n"
    "v_cvt_pk_bf16_f32 v128, v128, v129\n"
    "v_cvt_pk_bf16_f32 v129, v130, v131\n"
    "v_or_b32_e32 v191, v191, v207\n"
    "v_mfma_f32_16x16x32_bf16 v[116:119], a[12:15], v[184:187], 0\n"
    "ds_read_b128 a[252:255], v249 offset:0\n"
    "ds_read_u16_d16_hi v206, v206\n"
    "v_lshlrev_b32_sdwa v207, 1, v183 dst_sel:DWORD dst_unused:UNUSED_PAD src0_sel:DWORD src1_sel:WORD_1\n"
    "v_mfma_f32_16x16x32_bf16 v[180:183], a[148:151], v[156:159], 0\n"
    "ds_read_u16_d16_hi v207, v207\n"
    "v_cvt_pk_bf16_f32 v130, v132, v133\n"
    "v_mfma_f32_16x16x32_bf16 v[180:183], a[196:199], v[160:163], v[180:183]\n"
    "v_or_b32_e64 v193, v193, v201\n"
    "v_cvt_pk_bf16_f32 v131, v134, v135\n"
    "v_mfma_f32_16x16x32_bf16 v[180:183], a[108:111], v[164:167], v[180:183]\n"
    "s_nop 0\n"
    "ds_write_b128 v243, v[128:131] offset:28736\n"
    "v_mfma_f32_16x16x32_bf16 v[128:131], v[0:3], v[184:187], 0\n"
    "s_waitcnt lgkmcnt(5)\n"
    "v_cvt_pk_bf16_f32 v104, v104, v105\n"
    "v_cvt_pk_bf16_f32 v105, v106, v107\n"
    "v_mfma_f32_16x16x32_bf16 v[132:135], v[4:7], v[184:187], 0\n"
    "v_cvt_pk_bf16_f32 v106, v108, v109\n"
    "v_cvt_pk_bf16_f32 v107, v110, v111\n"
    "v_mfma_f32_16x16x32_bf16 v[108:111], a[4:7], v[188:191], 0\n"
    "ds_write_b128 v243, v[104:107] offset:25664\n"
    "s_waitcnt lgkmcnt(4)\n"
    "buffer_store_dwordx4 a[252:255], v255, s[44:47], s57 offen offset:0 sc1\n"
    "v_mfma_f32_16x16x32_bf16 v[104:107], a[0:3], v[188:191], 0\n"
    "v_or_b32_e32 v194, v194, v202\n"
    "v_cvt_pk_bf16_f32 v120, v120, v121\n"
    "v_cvt_pk_bf16_f32 v121, v122, v123\n"
    "v_cvt_pk_bf16_f32 v122, v124, v125\n"
    "v_cvt_pk_bf16_f32 v123, v126, v127\n"
    "ds_write_b128 v243, v[120:123] offset:27712\n"
    "v_mfma_f32_16x16x32_bf16 v[120:123], a[8:11], v[188:191], 0\n"
    "s_waitcnt lgkmcnt(2)\n"
    "v_pk_fma_f16 v232, v192, v192, s48\n"
    "v_cvt_pk_bf16_f32 v136, v136, v137\n"
    "v_mfma_f32_16x16x32_bf16 v[124:127], a[12:15], v[188:191], 0\n"
    "s_nop 0\n"
    "ds_read_b128 a[252:255], v249 offset:2048\n"
    "v_cvt_pk_bf16_f32 v137, v138, v139\n"
    "v_cvt_pk_bf16_f32 v138, v140, v141\n"
    "v_or_b32_e64 v195, v195, v203\n"
    "v_cvt_pk_bf16_f32 v139, v142, v143\n"
    "ds_write_b128 v243, v[136:139] offset:29760\n"
    "v_pk_mul_f16 v232, v192, v232\n"
    "v_mfma_f32_16x16x32_bf16 v[136:139], v[0:3], v[188:191], 0\n"
    "v_exp_f16_e64 v234, v232\n"
    "v_mfma_f32_16x16x32_bf16 v[140:143], v[4:7], v[188:191], 0\n"
    "v_or_b32_e64 v197, v197, v205\n"
    "v_exp_f16_sdwa v234, v232 dst_sel:WORD_1 dst_unused:UNUSED_PRESERVE src0_sel:WORD_1\n"
    "v_cvt_pk_f16_f32 v184, v168, v169\n"
    "v_pk_fma_f16 v234, v234, s49, s49\n"
    "v_lshlrev_b32_sdwa v185, 1, v170 dst_sel:DWORD dst_unused:UNUSED_PAD src0_sel:DWORD src1_sel:WORD_1\n"
    "v_lshlrev_b32_sdwa v201, 1, v171 dst_sel:DWORD dst_unused:UNUSED_PAD src0_sel:DWORD src1_sel:WORD_1\n"
    "v_mfma_f32_16x16x32_bf16 v[168:171], a[152:155], v[144:147], 0\n"
    "s_nop 0\n"
    "ds_read_u16 v185, v185\n"
    "ds_read_u16_d16_hi v201, v201\n"
    "v_mfma_f32_16x16x32_bf16 v[168:171], a[200:203], v[148:151], v[168:171]\n"
    "s_waitcnt lgkmcnt(3)\n"
    "buffer_store_dwordx4 a[252:255], v255, s[44:47], s57 offen offset:64 sc1\n"
    "v_lshlrev_b32_sdwa v186, 1, v172 dst_sel:DWORD dst_unused:UNUSED_PAD src0_sel:DWORD src1_sel:WORD_1\n"
    "v_lshlrev_b32_sdwa v202, 1, v173 dst_sel:DWORD dst_unused:UNUSED_PAD src0_sel:DWORD src1_sel:WORD_1\n"
    "v_lshlrev_b32_sdwa v187, 1, v174 dst_sel:DWORD dst_unused:UNUSED_PAD src0_sel:DWORD src1_sel:WORD_1\n"
    "v_lshlrev_b32_sdwa v203, 1, v175 dst_sel:DWORD dst_unused:UNUSED_PAD src0_sel:DWORD src1_sel:WORD_1\n"
    "v_rcp_f16_e64 v232, v234\n"
    "v_mfma_f32_16x16x32_bf16 v[168:171], a[112:115], v[152:155], v[168:171]\n"
    "ds_read_u16 v186, v186\n"
    "ds_read_u16_d16_hi v202, v202\n"
    "v_or_b32_e64 v198, v198, v206\n"
    "v_rcp_f16_sdwa v232, v234 dst_sel:WORD_1 dst_unused:UNUSED_PRESERVE src0_sel:WORD_1\n"
    "v_mfma_f32_16x16x32_bf16 v[172:175], a[156:159], v[144:147], 0\n"
    "ds_read_u16 v187, v187\n"
    "ds_read_u16_d16_hi v203, v203\n"
    "v_cvt_pk_f16_f32 v188, v176, v177\n"
    "v_lshlrev_b32_sdwa v189, 1, v178 dst_sel:DWORD dst_unused:UNUSED_PAD src0_sel:DWORD src1_sel:WORD_1\n"
    "v_lshlrev_b32_sdwa v205, 1, v179 dst_sel:DWORD dst_unused:UNUSED_PAD src0_sel:DWORD src1_sel:WORD_1\n"
    "v_lshlrev_b32_sdwa v190, 1, v180 dst_sel:DWORD dst_unused:UNUSED_PAD src0_sel:DWORD src1_sel:WORD_1\n"
    "v_lshlrev_b32_sdwa v206, 1, v181 dst_sel:DWORD dst_unused:UNUSED_PAD src0_sel:DWORD src1_sel:WORD_1\n"
    "v_lshlrev_b32_sdwa v191, 1, v182 dst_sel:DWORD dst_unused:UNUSED_PAD src0_sel:DWORD src1_sel:WORD_1\n"
    "v_fma_mix_f32 v234, v192, v232, 0 op_sel_hi:[1,1,0]\n"
    "v_mfma_f32_16x16x32_bf16 v[172:175], a[204:207], v[148:151], v[172:175]\n"
    "ds_read_b128 a[252:255], v249 offset:4096\n"
    "ds_read_u16 v189, v189\n"
    "v_mfma_f32_16x16x32_bf16 v[172:175], a[116:119], v[152:155], v[172:175]\n"
    "v_or_b32_e64 v199, v199, v207\n"
    "v_lshlrev_b32_sdwa v207, 1, v183 dst_sel:DWORD dst_unused:UNUSED_PAD src0_sel:DWORD src1_sel:WORD_1\n"
    "v_fma_mix_f32 v232, v192, v232, 0 op_sel:[1,1,0] op_sel_hi:[1,1,0]\n"
    "v_mfma_f32_16x16x32_bf16 v[176:179], a[152:155], v[156:159], 0\n"
    "s_nop 0\n"
    "ds_read_u16_d16_hi v205, v205\n"
    "v_cvt_pk_bf16_f32 v192, v234, v232\n"
    "v_mfma_f32_16x16x32_bf16 v[176:179], a[200:203], v[160:163], v[176:179]\n"
    "s_waitcnt lgkmcnt(7)\n"
    "ds_read_u16 v190, v190\n"
    "v_pk_fma_f16 v232, v196, v196, s48\n"
    "v_mfma_f32_16x16x32_bf16 v[176:179], a[112:115], v[164:167], v[176:179]\n"
    "v_pk_mul_f16 v232, v196, v232\n"
    "v_or_b32_e64 v185, v185, v201\n"
    "v_mfma_f32_16x16x32_bf16 v[180:183], a[156:159], v[156:159], 0\n"
    "ds_read_u16_d16_hi v206, v206\n"
    "v_exp_f16_e64 v234, v232\n"
    "v_mfma_f32_16x16x32_bf16 v[180:183], a[204:207], v[160:163], v[180:183]\n"
    "ds_read_u16 v191, v191\n"
    "v_exp_f16_sdwa v234, v232 dst_sel:WORD_1 dst_unused:UNUSED_PRESERVE src0_sel:WORD_1\n"
    "v_mfma_f32_16x16x32_bf16 v[180:183], a[116:119], v[164:167], v[180:183]\n"
    "s_waitcnt lgkmcnt(8)\n"
    "v_pk_fma_f16 v234, v234, s49, s49\n"
    "v_or_b32_e32 v186, v186, v202\n"
    "v_mfma_f32_16x16x32_bf16 v[96:99], a[16:19], v[192:195], v[96:99]\n"
    "ds_read_u16_d16_hi v207, v207\n"
    "v_rcp_f16_e32 v232, v234\n"
    "v_mfma_f32_16x16x32_bf16 v[100:103], a[20:23], v[192:195], v[100:103]\n"
    "v_rcp_f16_sdwa v232, v234 dst_sel:WORD_1 dst_unused:UNUSED_PRESERVE src0_sel:WORD_1\n"
    "v_mfma_f32_16x16x32_bf16 v[112:115], a[24:27], v[192:195], v[112:115]\n"
    "s_waitcnt lgkmcnt(6)\n"
    "buffer_store_dwordx4 a[252:255], v255, s[44:47], s57 offen offset:128 sc1\n"
    "v_mfma_f32_16x16x32_bf16 v[116:119], a[28:31], v[192:195], v[116:119]\n"
    "v_fma_mix_f32 v234, v196, v232, 0 op_sel_hi:[1,1,0]\n"
    "v_fma_mix_f32 v232, v196, v232, 0 op_sel:[1,1,0] op_sel_hi:[1,1,0]\n"
    "v_mfma_f32_16x16x32_bf16 v[128:131], v[8:11], v[192:195], v[128:131]\n"
    "ds_read_b128 a[252:255], v249 offset:1024\n"
    "v_cvt_pk_bf16_f32 v196, v234, v232\n"
    "v_mfma_f32_16x16x32_bf16 v[132:135], v[12:15], v[192:195], v[132:135]\n"
    "v_pk_fma_f16 v232, v184, v184, s48\n"
    "v_or_b32_e32 v187, v187, v203\n"
    "v_mfma_f32_16x16x32_bf16 v[104:107], a[16:19], v[196:199], v[104:107]\n"
    "s_waitcnt lgkmcnt(5)\n"
    "v_pk_mul_f16 v232, v184, v232\n"
    "v_or_b32_e64 v189, v189, v205\n"
    "v_mfma_f32_16x16x32_bf16 v[108:111], a[20:23], v[196:199], v[108:111]\n"
    "v_exp_f16_e32 v234, v232\n"
    "v_mfma_f32_16x16x32_bf16 v[120:123], a[24:27], v[196:199], v[120:123]\n"
    "v_exp_f16_sdwa v234, v232 dst_sel:WORD_1 dst_unused:UNUSED_PRESERVE src0_sel:WORD_1\n"
    "v_mfma_f32_16x16x32_bf16 v[124:127], a[28:31], v[196:199], v[124:127]\n"
    "s_waitcnt lgkmcnt(3)\n"
    "v_pk_fma_f16 v234, v234, s49, s49\n"
    "v_lshlrev_b32_sdwa v192, 1, v168 dst_sel:DWORD dst_unused:UNUSED_PAD src0_sel:DWORD src1_sel:WORD_1\n"
    "v_mfma_f32_16x16x32_bf16 v[136:139], v[8:11], v[196:199], v[136:139]\n"
    "ds_read_u16 v192, v192\n"
    "v_lshlrev_b32_sdwa v200, 1, v169 dst_sel:DWORD dst_unused:UNUSED_PAD src0_sel:DWORD src1_sel:WORD_1\n"
    "v_mfma_f32_16x16x32_bf16 v[140:143], v[12:15], v[196:199], v[140:143]\n"
    "ds_read_u16_d16_hi v200, v200\n"
    "v_lshlrev_b32_sdwa v193, 1, v170 dst_sel:DWORD dst_unused:UNUSED_PAD src0_sel:DWORD src1_sel:WORD_1\n"
    "v_lshlrev_b32_sdwa v201, 1, v171 dst_sel:DWORD dst_unused:UNUSED_PAD src0_sel:DWORD src1_sel:WORD_1\n"
    "v_mfma_f32_16x16x32_bf16 v[168:171], a[160:163], v[144:147], 0\n"
    "s_waitcnt lgkmcnt(2)\n"
    "ds_read_u16 v193, v193\n"
    "ds_read_u16_d16_hi v201, v201\n"
    "v_mfma_f32_16x16x32_bf16 v[168:171], a[208:211], v[148:151], v[168:171]\n"
    "s_nop 0\n"
    "buffer_store_dwordx4 a[252:255], v255, s[44:47], s88 offen offset:0 sc1\n"
    "v_lshlrev_b32_sdwa v194, 1, v172 dst_sel:DWORD dst_unused:UNUSED_PAD src0_sel:DWORD src1_sel:WORD_1\n"
    "v_lshlrev_b32_sdwa v202, 1, v173 dst_sel:DWORD dst_unused:UNUSED_PAD src0_sel:DWORD src1_sel:WORD_1\n"
    "v_lshlrev_b32_sdwa v195, 1, v174 dst_sel:DWORD dst_unused:UNUSED_PAD src0_sel:DWORD src1_sel:WORD_1\n"
    "v_lshlrev_b32_sdwa v203, 1, v175 dst_sel:DWORD dst_unused:UNUSED_PAD src0_sel:DWORD src1_sel:WORD_1\n"
    "v_rcp_f16_e64 v232, v234\n"
    "v_mfma_f32_16x16x32_bf16 v[168:171], a[120:123], v[152:155], v[168:171]\n"
    "ds_read_u16 v194, v194\n"
    "ds_read_u16_d16_hi v202, v202\n"
    "v_or_b32_e64 v190, v190, v206\n"
    "v_rcp_f16_sdwa v232, v234 dst_sel:WORD_1 dst_unused:UNUSED_PRESERVE src0_sel:WORD_1\n"
    "v_mfma_f32_16x16x32_bf16 v[172:175], a[164:167], v[144:147], 0\n"
    "ds_read_u16 v195, v195\n"
    "ds_read_u16_d16_hi v203, v203\n"
    "v_lshlrev_b32_sdwa v196, 1, v176 dst_sel:DWORD dst_unused:UNUSED_PAD src0_sel:DWORD src1_sel:WORD_1\n"
    "v_lshlrev_b32_sdwa v204, 1, v177 dst_sel:DWORD dst_unused:UNUSED_PAD src0_sel:DWORD src1_sel:WORD_1\n"
    "v_lshlrev_b32_sdwa v197, 1, v178 dst_sel:DWORD dst_unused:UNUSED_PAD src0_sel:DWORD src1_sel:WORD_1\n"
    "v_lshlrev_b32_sdwa v205, 1, v179 dst_sel:DWORD dst_unused:UNUSED_PAD src0_sel:DWORD src1_sel:WORD_1\n"
    "v_lshlrev_b32_sdwa v198, 1, v180 dst_sel:DWORD dst_unused:UNUSED_PAD src0_sel:DWORD src1_sel:WORD_1\n"
    "v_lshlrev_b32_sdwa v206, 1, v181 dst_sel:DWORD dst_unused:UNUSED_PAD src0_sel:DWORD src1_sel:WORD_1\n"
    "v_lshlrev_b32_sdwa v199, 1, v182 dst_sel:DWORD dst_unused:UNUSED_PAD src0_sel:DWORD src1_sel:WORD_1\n"
    "v_fma_mix_f32 v234, v184, v232, 0 op_sel_hi:[1,1,0]\n"
    "v_mfma_f32_16x16x32_bf16 v[172:175], a[212:215], v[148:151], v[172:175]\n"
    "ds_read_u16 v196, v196\n"
    "v_or_b32_e32 v191, v191, v207\n"
    "v_mfma_f32_16x16x32_bf16 v[172:175], v[52:55], v[152:155], v[172:175]\n"
    "s_waitcnt lgkmcnt(7)\n"
    "ds_read_u16_d16_hi v204, v204\n"
    "v_lshlrev_b32_sdwa v207, 1, v183 dst_sel:DWORD dst_unused:UNUSED_PAD src0_sel:DWORD src1_sel:WORD_1\n"
    "v_fma_mix_f32 v232, v184, v232, 0 op_sel:[1,1,0] op_sel_hi:[1,1,0]\n"
    "v_mfma_f32_16x16x32_bf16 v[176:179], a[160:163], v[156:159], 0\n"
    "ds_read_u16 v197, v197\n"
    "v_cvt_pk_bf16_f32 v184, v234, v232\n"
    "v_mfma_f32_16x16x32_bf16 v[176:179], a[208:211], v[160:163], v[176:179]\n"
    "v_pk_fma_f16 v232, v188, v188, s48\n"
    "v_or_b32_e32 v192, v192, v200\n"
    "v_mfma_f32_16x16x32_bf16 v[176:179], a[120:123], v[164:167], v[176:179]\n"
    "s_waitcnt lgkmcnt(7)\n"
    "ds_read_b128 a[252:255], v249 offset:3072\n"
    "v_pk_mul_f16 v232, v188, v232\n"
    "v_mfma_f32_16x16x32_bf16 v[180:183], a[164:167], v[156:159], 0\n"
    "ds_read_u16_d16_hi v205, v205\n"
    "v_exp_f16_e32 v234, v232\n"
    "v_mfma_f32_16x16x32_bf16 v[180:183], a[212:215], v[160:163], v[180:183]\n"
    "v_exp_f16_sdwa v234, v232 dst_sel:WORD_1 dst_unused:UNUSED_PRESERVE src0_sel:WORD_1\n"
    "v_mfma_f32_16x16x32_bf16 v[180:183], v[52:55], v[164:167], v[180:183]\n"
    "s_waitcnt lgkmcnt(7)\n"
    "ds_read_u16 v198, v198\n"
    "v_pk_fma_f16 v234, v234, s49, s49\n"
    "v_mfma_f32_16x16x32_bf16 v[96:99], a[32:35], v[184:187], v[96:99]\n"
    "ds_read_u16_d16_hi v206, v206\n"
    "v_rcp_f16_e64 v232, v234\n"
    "v_mfma_f32_16x16x32_bf16 v[100:103], a[36:39], v[184:187], v[100:103]\n"
    "v_rcp_f16_sdwa v232, v234 dst_sel:WORD_1 dst_unused:UNUSED_PRESERVE src0_sel:WORD_1\n"
    "v_mfma_f32_16x16x32_bf16 v[112:115], a[40:43], v[184:187], v[112:115]\n"
    "ds_read_u16 v199, v199\n"
    "v_fma_mix_f32 v234, v188, v232, 0 op_sel_hi:[1,1,0]\n"
    "v_mfma_f32_16x16x32_bf16 v[116:119], a[44:47], v[184:187], v[116:119]\n"
    "s_waitcnt lgkmcnt(8)\n"
    "v_fma_mix_f32 v232, v188, v232, 0 op_sel:[1,1,0] op_sel_hi:[1,1,0]\n"
    "v_or_b32_e32 v193, v193, v201\n"
    "v_mfma_f32_16x16x32_bf16 v[128:131], v[16:19], v[184:187], v[128:131]\n"
    "ds_read_u16_d16_hi v207, v207\n"
    "v_cvt_pk_bf16_f32 v188, v234, v232\n"
    "v_mfma_f32_16x16x32_bf16 v[132:135], v[20:23], v[184:187], v[132:135]\n"
    "v_cvt_pk_f16_f32 v184, v168, v169\n"
    "v_lshlrev_b32_sdwa v185, 1, v170 dst_sel:DWORD dst_unused:UNUSED_PAD src0_sel:DWORD src1_sel:WORD_1\n"
    "v_mfma_f32_16x16x32_bf16 v[104:107], a[32:35], v[188:191], v[104:107]\n"
    "s_waitcnt lgkmcnt(7)\n"
    "ds_read_u16 v185, v185\n"
    "v_lshlrev_b32_sdwa v201, 1, v171 dst_sel:DWORD dst_unused:UNUSED_PAD src0_sel:DWORD src1_sel:WORD_1\n"
    "v_mfma_f32_16x16x32_bf16 v[108:111], a[36:39], v[188:191], v[108:111]\n"
    "ds_read_u16_d16_hi v201, v201\n"
    "s_waitcnt lgkmcnt(7)\n"
    "buffer_store_dwordx4 a[252:255], v255, s[44:47], s88 offen offset:64 sc1\n"
    "v_mfma_f32_16x16x32_bf16 v[120:123], a[40:43], v[188:191], v[120:123]\n"
    "v_lshlrev_b32_sdwa v186, 1, v172 dst_sel:DWORD dst_unused:UNUSED_PAD src0_sel:DWORD src1_sel:WORD_1\n"
    "v_or_b32_e32 v194, v194, v202\n"
    "v_mfma_f32_16x16x32_bf16 v[124:127], a[44:47], v[188:191], v[124:127]\n"
    "s_waitcnt lgkmcnt(6)\n"
    "ds_read_b128 a[252:255], v249 offset:5120\n"
    "ds_read_u16 v186, v186\n"
    "v_mfma_f32_16x16x32_bf16 v[136:139], v[16:19], v[188:191], v[136:139]\n"
    "v_lshlrev_b32_sdwa v202, 1, v173 dst_sel:DWORD dst_unused:UNUSED_PAD src0_sel:DWORD src1_sel:WORD_1\n"
    "v_lshlrev_b32_sdwa v187, 1, v174 dst_sel:DWORD dst_unused:UNUSED_PAD src0_sel:DWORD src1_sel:WORD_1\n"
    "v_mfma_f32_16x16x32_bf16 v[140:143], v[20:23], v[188:191], v[140:143]\n"
    "ds_read_u16_d16_hi v202, v202\n"
    "v_cvt_pk_f16_f32 v188, v176, v177\n"
    "v_mfma_f32_16x16x32_bf16 v[168:171], a[168:171], v[144:147], 0\n"
    "ds_read_u16 v187, v187\n"
    "v_lshlrev_b32_sdwa v189, 1, v178 dst_sel:DWORD dst_unused:UNUSED_PAD src0_sel:DWORD src1_sel:WORD_1\n"
    "v_mfma_f32_16x16x32_bf16 v[168:171], a[216:219], v[148:151], v[168:171]\n"
    "s_waitcnt lgkmcnt(8)\n"
    "v_lshlrev_b32_sdwa v190, 1, v180 dst_sel:DWORD dst_unused:UNUSED_PAD src0_sel:DWORD src1_sel:WORD_1\n"
    "v_lshlrev_b32_sdwa v191, 1, v182 dst_sel:DWORD dst_unused:UNUSED_PAD src0_sel:DWORD src1_sel:WORD_1\n"
    "v_mfma_f32_16x16x32_bf16 v[168:171], a[124:127], v[152:155], v[168:171]\n"
    "v_or_b32_e32 v195, v195, v203\n"
    "v_lshlrev_b32_sdwa v203, 1, v175 dst_sel:DWORD dst_unused:UNUSED_PAD src0_sel:DWORD src1_sel:WORD_1\n"
    "v_mfma_f32_16x16x32_bf16 v[172:175], a[172:175], v[144:147], 0\n"
    "ds_read_u16_d16_hi v203, v203\n"
    "ds_read_u16 v189, v189\n"
    "v_mfma_f32_16x16x32_bf16 v[172:175], a[220:223], v[148:151], v[172:175]\n"
    "s_waitcnt lgkmcnt(8)\n"
    "v_or_b32_e32 v196, v196, v204\n"
    "v_or_b32_e64 v197, v197, v205\n"
    "v_mfma_f32_16x16x32_bf16 v[172:175], v[60:63], v[152:155], v[172:175]\n"
    "ds_read_u16 v190, v190\n"
    "v_lshlrev_b32_sdwa v205, 1, v179 dst_sel:DWORD dst_unused:UNUSED_PAD src0_sel:DWORD src1_sel:WORD_1\n"
    "v_mfma_f32_16x16x32_bf16 v[176:179], a[168:171], v[156:159], 0\n"
    "v_or_b32_e64 v198, v198, v206\n"
    "v_lshlrev_b32_sdwa v206, 1, v181 dst_sel:DWORD dst_unused:UNUSED_PAD src0_sel:DWORD src1_sel:WORD_1\n"
    "v_mfma_f32_16x16x32_bf16 v[176:179], a[216:219], v[160:163], v[176:179]\n"
    "ds_read_u16_d16_hi v205, v205\n"
    "s_waitcnt lgkmcnt(7)\n"
    "buffer_store_dwordx4 a[252:255], v255, s[44:47], s88 offen offset:128 sc1\n"
    "v_mfma_f32_16x16x32_bf16 v[176:179], a[124:127], v[164:167], v[176:179]\n"
    "s_nop 0\n"
    "ds_read_b128 v[144:147], v243 offset:0\n"
    "ds_read_u16_d16_hi v206, v206\n"
    "v_mfma_f32_16x16x32_bf16 v[96:99], a[48:51], v[192:195], v[96:99]\n"
    "v_or_b32_e64 v199, v199, v207\n"
    "v_lshlrev_b32_sdwa v207, 1, v183 dst_sel:DWORD dst_unused:UNUSED_PAD src0_sel:DWORD src1_sel:WORD_1\n"
    "v_mfma_f32_16x16x32_bf16 v[180:183], a[172:175], v[156:159], 0\n"
    "ds_read_b128 v[148:151], v243 offset:1024\n"
    "v_or_b32_e32 v185, v185, v201\n"
    "v_mfma_f32_16x16x32_bf16 v[180:183], a[220:223], v[160:163], v[180:183]\n"
    "s_waitcnt lgkmcnt(8)\n"
    "ds_read_u16 v191, v191\n"
    "v_or_b32_e64 v186, v186, v202\n"
    "v_mfma_f32_16x16x32_bf16 v[180:183], v[60:63], v[164:167], v[180:183]\n"
    "v_pk_fma_f16 v232, v184, v184, s48\n"
    "v_lshlrev_b32_sdwa v201, 1, v171 dst_sel:DWORD dst_unused:UNUSED_PAD src0_sel:DWORD src1_sel:WORD_1\n"
    "v_mfma_f32_16x16x32_bf16 v[100:103], a[52:55], v[192:195], v[100:103]\n"
    "ds_read_b128 v[152:155], v243 offset:2048\n"
    "v_pk_mul_f16 v232, v184, v232\n"
    "v_mfma_f32_16x16x32_bf16 v[112:115], a[56:59], v[192:195], v[112:115]\n"
    "ds_read_u16_d16_hi v207, v207\n"
    "s_waitcnt lgkmcnt(9)\n"
    "v_or_b32_e32 v187, v187, v203\n"
    "v_mfma_f32_16x16x32_bf16 v[116:119], a[60:63], v[192:195], v[116:119]\n"
    "v_exp_f16_e64 v234, v232\n"
    "v_mfma_f32_16x16x32_bf16 v[128:131], v[24:27], v[192:195], v[128:131]\n"
    "ds_read_b128 v[156:159], v243 offset:3072\n"
    "ds_read_b128 v[160:163], v243 offset:4096\n"
    "v_mfma_f32_16x16x32_bf16 v[132:135], v[28:31], v[192:195], v[132:135]\n"
    "v_exp_f16_sdwa v234, v232 dst_sel:WORD_1 dst_unused:UNUSED_PRESERVE src0_sel:WORD_1\n"
    "v_mfma_f32_16x16x32_bf16 v[104:107], a[48:51], v[196:199], v[104:107]\n"
    "ds_read_b128 v[164:167], v243 offset:5120\n"
    "ds_read_u16_d16_hi v201, v201\n"
    "v_mfma_f32_16x16x32_bf16 v[108:111], a[52:55], v[196:199], v[108:111]\n"
    "v_pk_fma_f16 v234, v234, s49, s49\n"
    "s_waitcnt lgkmcnt(9)\n"
    "v_or_b32_e32 v189, v189, v205\n"
    "v_mfma_f32_16x16x32_bf16 v[120:123], a[56:59], v[196:199], v[120:123]\n"
    "v_cvt_pk_f16_f32 v192, v168, v169\n"
    "v_lshlrev_b32_sdwa v193, 1, v170 dst_sel:DWORD dst_unused:UNUSED_PAD src0_sel:DWORD src1_sel:WORD_1\n"
    "v_mfma_f32_16x16x32_bf16 v[124:127], a[60:63], v[196:199], v[124:127]\n"
    "s_nop 0\n"
    "ds_read_u16 v193, v193\n"
    "v_lshlrev_b32_sdwa v194, 1, v172 dst_sel:DWORD dst_unused:UNUSED_PAD src0_sel:DWORD src1_sel:WORD_1\n"
    "v_mfma_f32_16x16x32_bf16 v[136:139], v[24:27], v[196:199], v[136:139]\n"
    "s_waitcnt lgkmcnt(8)\n"
    "ds_read_u16 v194, v194\n"
    "v_lshlrev_b32_sdwa v202, 1, v173 dst_sel:DWORD dst_unused:UNUSED_PAD src0_sel:DWORD src1_sel:WORD_1\n"
    "v_mfma_f32_16x16x32_bf16 v[140:143], v[28:31], v[196:199], v[140:143]\n"
    "v_lshlrev_b32_sdwa v195, 1, v174 dst_sel:DWORD dst_unused:UNUSED_PAD src0_sel:DWORD src1_sel:WORD_1\n"
    "v_lshlrev_b32_sdwa v203, 1, v175 dst_sel:DWORD dst_unused:UNUSED_PAD src0_sel:DWORD src1_sel:WORD_1\n"
    "v_mfma_f32_16x16x32_bf16 v[168:171], a[128:131], v[144:147], 0\n"
    "ds_read_u16_d16_hi v202, v202\n"
    "v_rcp_f16_e32 v232, v234\n"
    "v_mfma_f32_16x16x32_bf16 v[168:171], a[176:179], v[148:151], v[168:171]\n"
    "s_waitcnt lgkmcnt(8)\n"
    "ds_read_u16 v195, v195\n"
    "v_or_b32_e32 v190, v190, v206\n"
    "v_mfma_f32_16x16x32_bf16 v[168:171], a[96:99], v[152:155], v[168:171]\n"
    "v_rcp_f16_sdwa v232, v234 dst_sel:WORD_1 dst_unused:UNUSED_PRESERVE src0_sel:WORD_1\n"
    "v_mfma_f32_16x16x32_bf16 v[172:175], a[132:135], v[144:147], 0\n"
    "s_waitcnt lgkmcnt(7)\n"
    "ds_read_u16_d16_hi v203, v203\n"
    "v_cvt_pk_f16_f32 v196, v176, v177\n"
    "v_mfma_f32_16x16x32_bf16 v[172:175], a[180:183], v[148:151], v[172:175]\n"
    "v_lshlrev_b32_sdwa v197, 1, v178 dst_sel:DWORD dst_unused:UNUSED_PAD src0_sel:DWORD src1_sel:WORD_1\n"
    "v_lshlrev_b32_sdwa v205, 1, v179 dst_sel:DWORD dst_unused:UNUSED_PAD src0_sel:DWORD src1_sel:WORD_1\n"
    "v_lshlrev_b32_sdwa v198, 1, v180 dst_sel:DWORD dst_unused:UNUSED_PAD src0_sel:DWORD src1_sel:WORD_1\n"
    "v_lshlrev_b32_sdwa v206, 1, v181 dst_sel:DWORD dst_unused:UNUSED_PAD src0_sel:DWORD src1_sel:WORD_1\n"
    "v_lshlrev_b32_sdwa v199, 1, v182 dst_sel:DWORD dst_unused:UNUSED_PAD src0_sel:DWORD src1_sel:WORD_1\n"
    "v_fma_mix_f32 v234, v184, v232, 0 op_sel_hi:[1,1,0]\n"
    "v_mfma_f32_16x16x32_bf16 v[172:175], v[48:51], v[152:155], v[172:175]\n"
    "s_waitcnt lgkmcnt(6)\n"
    "ds_read_u16 v197, v197\n"
    "v_or_b32_e32 v191, v191, v207\n"
    "v_lshlrev_b32_sdwa v207, 1, v183 dst_sel:DWORD dst_unused:UNUSED_PAD src0_sel:DWORD src1_sel:WORD_1\n"
    "v_fma_mix_f32 v232, v184, v232, 0 op_sel:[1,1,0] op_sel_hi:[1,1,0]\n"
    "v_mfma_f32_16x16x32_bf16 v[176:179], a[128:131], v[156:159], 0\n"
    "s_barrier\n"
    "v_cvt_pk_bf16_f32 v184, v234, v232\n"
    "v_mfma_f32_16x16x32_bf16 v[176:179], a[176:179], v[160:163], v[176:179]\n"
    "s_waitcnt lgkmcnt(5)\n"
    "ds_read_u16_d16_hi v205, v205\n"
    "ds_read_u16 v198, v198\n"
    "v_mfma_f32_16x16x32_bf16 v[176:179], a[96:99], v[164:167], v[176:179]\n"
    "v_pk_fma_f16 v232, v188, v188, s48\n"
    "v_or_b32_e64 v193, v193, v201\n"
    "v_mfma_f32_16x16x32_bf16 v[180:183], a[132:135], v[156:159], 0\n"
    "ds_read_u16_d16_hi v206, v206\n"
    "v_pk_mul_f16 v232, v188, v232\n"
    "v_mfma_f32_16x16x32_bf16 v[180:183], a[180:183], v[160:163], v[180:183]\n"
    "ds_read_u16 v199, v199\n"
    "v_exp_f16_e32 v234, v232\n"
    "v_mfma_f32_16x16x32_bf16 v[180:183], v[48:51], v[164:167], v[180:183]\n"
    "s_waitcnt lgkmcnt(7)\n"
    "ds_read_u16_d16_hi v207, v207\n"
    "v_exp_f16_sdwa v234, v232 dst_sel:WORD_1 dst_unused:UNUSED_PRESERVE src0_sel:WORD_1\n"
    "v_mfma_f32_16x16x32_bf16 v[96:99], a[64:67], v[184:187], v[96:99]\n"
    "v_pk_fma_f16 v234, v234, s49, s49\n"
    "v_or_b32_e64 v194, v194, v202\n"
    "v_mfma_f32_16x16x32_bf16 v[100:103], a[68:71], v[184:187], v[100:103]\n"
    "v_rcp_f16_e32 v232, v234\n"
    "v_mfma_f32_16x16x32_bf16 v[112:115], a[72:75], v[184:187], v[112:115]\n"
    "s_waitcnt lgkmcnt(6)\n"
    "v_rcp_f16_sdwa v232, v234 dst_sel:WORD_1 dst_unused:UNUSED_PRESERVE src0_sel:WORD_1\n"
    "v_mfma_f32_16x16x32_bf16 v[116:119], a[76:79], v[184:187], v[116:119]\n"
    "v_fma_mix_f32 v234, v188, v232, 0 op_sel_hi:[1,1,0]\n"
    "v_fma_mix_f32 v232, v188, v232, 0 op_sel:[1,1,0] op_sel_hi:[1,1,0]\n"
    "v_mfma_f32_16x16x32_bf16 v[128:131], v[32:35], v[184:187], v[128:131]\n"
    "v_cvt_pk_bf16_f32 v188, v234, v232\n"
    "v_pk_fma_f16 v232, v192, v192, s48\n"
    "v_mfma_f32_16x16x32_bf16 v[132:135], v[36:39], v[184:187], v[132:135]\n"
    "s_waitcnt lgkmcnt(4)\n"
    "v_or_b32_e32 v195, v195, v203\n"
    "v_pk_mul_f16 v232, v192, v232\n"
    "v_mfma_f32_16x16x32_bf16 v[104:107], a[64:67], v[188:191], v[104:107]\n"
    "v_exp_f16_e64 v234, v232\n"
    "v_mfma_f32_16x16x32_bf16 v[108:111], a[68:71], v[188:191], v[108:111]\n"
    "v_exp_f16_sdwa v234, v232 dst_sel:WORD_1 dst_unused:UNUSED_PRESERVE src0_sel:WORD_1\n"
    "v_mfma_f32_16x16x32_bf16 v[120:123], a[72:75], v[188:191], v[120:123]\n"
    "v_or_b32_e32 v197, v197, v205\n"
    "v_pk_fma_f16 v234, v234, s49, s49\n"
    "v_mfma_f32_16x16x32_bf16 v[124:127], a[76:79], v[188:191], v[124:127]\n"
    "s_waitcnt lgkmcnt(2)\n"
    "v_lshlrev_b32_sdwa v184, 1, v168 dst_sel:DWORD dst_unused:UNUSED_PAD src0_sel:DWORD src1_sel:WORD_1\n"
    "v_lshlrev_b32_sdwa v200, 1, v169 dst_sel:DWORD dst_unused:UNUSED_PAD src0_sel:DWORD src1_sel:WORD_1\n"
    "v_mfma_f32_16x16x32_bf16 v[136:139], v[32:35], v[188:191], v[136:139]\n"
    "ds_read_u16 v184, v184\n"
    "ds_read_u16_d16_hi v200, v200\n"
    "v_mfma_f32_16x16x32_bf16 v[140:143], v[36:39], v[188:191], v[140:143]\n"
    "s_nop 0\n"
    "v_lshlrev_b32_sdwa v185, 1, v170 dst_sel:DWORD dst_unused:UNUSED_PAD src0_sel:DWORD src1_sel:WORD_1\n"
    "v_lshlrev_b32_sdwa v201, 1, v171 dst_sel:DWORD dst_unused:UNUSED_PAD src0_sel:DWORD src1_sel:WORD_1\n"
    "v_mfma_f32_16x16x32_bf16 v[168:171], a[136:139], v[144:147], 0\n"
    "s_waitcnt lgkmcnt(2)\n"
    "ds_read_u16 v185, v185\n"
    "ds_read_u16_d16_hi v201, v201\n"
    "v_mfma_f32_16x16x32_bf16 v[168:171], a[184:187], v[148:151], v[168:171]\n"
    "v_lshlrev_b32_sdwa v186, 1, v172 dst_sel:DWORD dst_unused:UNUSED_PAD src0_sel:DWORD src1_sel:WORD_1\n"
    "v_lshlrev_b32_sdwa v202, 1, v173 dst_sel:DWORD dst_unused:UNUSED_PAD src0_sel:DWORD src1_sel:WORD_1\n"
    "v_lshlrev_b32_sdwa v187, 1, v174 dst_sel:DWORD dst_unused:UNUSED_PAD src0_sel:DWORD src1_sel:WORD_1\n"
    "v_lshlrev_b32_sdwa v203, 1, v175 dst_sel:DWORD dst_unused:UNUSED_PAD src0_sel:DWORD src1_sel:WORD_1\n"
    "v_rcp_f16_e64 v232, v234\n"
    "v_mfma_f32_16x16x32_bf16 v[168:171], a[100:103], v[152:155], v[168:171]\n"
    "ds_read_u16 v186, v186\n"
    "ds_read_u16_d16_hi v202, v202\n"
    "v_or_b32_e64 v198, v198, v206\n"
    "v_rcp_f16_sdwa v232, v234 dst_sel:WORD_1 dst_unused:UNUSED_PRESERVE src0_sel:WORD_1\n"
    "v_mfma_f32_16x16x32_bf16 v[172:175], a[140:143], v[144:147], 0\n"
    "ds_read_u16 v187, v187\n"
    "v_lshlrev_b32_sdwa v188, 1, v176 dst_sel:DWORD dst_unused:UNUSED_PAD src0_sel:DWORD src1_sel:WORD_1\n"
    "v_mfma_f32_16x16x32_bf16 v[172:175], a[188:191], v[148:151], v[172:175]\n"
    "v_lshlrev_b32_sdwa v204, 1, v177 dst_sel:DWORD dst_unused:UNUSED_PAD src0_sel:DWORD src1_sel:WORD_1\n"
    "v_lshlrev_b32_sdwa v189, 1, v178 dst_sel:DWORD dst_unused:UNUSED_PAD src0_sel:DWORD src1_sel:WORD_1\n"
    "v_lshlrev_b32_sdwa v205, 1, v179 dst_sel:DWORD dst_unused:UNUSED_PAD src0_sel:DWORD src1_sel:WORD_1\n"
    "v_lshlrev_b32_sdwa v190, 1, v180 dst_sel:DWORD dst_unused:UNUSED_PAD src0_sel:DWORD src1_sel:WORD_1\n"
    "v_lshlrev_b32_sdwa v206, 1, v181 dst_sel:DWORD dst_unused:UNUSED_PAD src0_sel:DWORD src1_sel:WORD_1\n"
    "v_lshlrev_b32_sdwa v191, 1, v182 dst_sel:DWORD dst_unused:UNUSED_PAD src0_sel:DWORD src1_sel:WORD_1\n"
    "v_fma_mix_f32 v234, v192, v232, 0 op_sel_hi:[1,1,0]\n"
    "v_mfma_f32_16x16x32_bf16 v[172:175], v[56:59], v[152:155], v[172:175]\n"
    "ds_read_u16_d16_hi v203, v203\n"
    "ds_read_u16 v188, v188\n"
    "v_or_b32_e32 v199, v199, v207\n"
    "v_lshlrev_b32_sdwa v207, 1, v183 dst_sel:DWORD dst_unused:UNUSED_PAD src0_sel:DWORD src1_sel:WORD_1\n"
    "v_fma_mix_f32 v232, v192, v232, 0 op_sel:[1,1,0] op_sel_hi:[1,1,0]\n"
    "v_mfma_f32_16x16x32_bf16 v[176:179], a[136:139], v[156:159], 0\n"
    "s_waitcnt lgkmcnt(7)\n"
    "ds_read_u16_d16_hi v204, v204\n"
    "v_cvt_pk_bf16_f32 v192, v234, v232\n"
    "v_mfma_f32_16x16x32_bf16 v[176:179], a[184:187], v[160:163], v[176:179]\n"
    "v_pk_fma_f16 v232, v196, v196, s48\n"
    "v_or_b32_e64 v184, v184, v200\n"
    "v_mfma_f32_16x16x32_bf16 v[176:179], a[100:103], v[164:167], v[176:179]\n"
    "ds_read_u16 v189, v189\n"
    "v_pk_mul_f16 v232, v196, v232\n"
    "v_mfma_f32_16x16x32_bf16 v[180:183], a[140:143], v[156:159], 0\n"
    "v_exp_f16_e32 v234, v232\n"
    "v_mfma_f32_16x16x32_bf16 v[180:183], a[188:191], v[160:163], v[180:183]\n"
    "s_waitcnt lgkmcnt(7)\n"
    "ds_read_u16_d16_hi v205, v205\n"
    "v_exp_f16_sdwa v234, v232 dst_sel:WORD_1 dst_unused:UNUSED_PRESERVE src0_sel:WORD_1\n"
    "v_mfma_f32_16x16x32_bf16 v[180:183], v[56:59], v[164:167], v[180:183]\n"
    "ds_read_u16 v190, v190\n"
    "v_pk_fma_f16 v234, v234, s49, s49\n"
    "v_mfma_f32_16x16x32_bf16 v[96:99], a[80:83], v[192:195], v[96:99]\n"
    "v_rcp_f16_e32 v232, v234\n"
    "v_mfma_f32_16x16x32_bf16 v[100:103], a[84:87], v[192:195], v[100:103]\n"
    "s_waitcnt lgkmcnt(7)\n"
    "ds_read_u16_d16_hi v206, v206\n"
    "v_rcp_f16_sdwa v232, v234 dst_sel:WORD_1 dst_unused:UNUSED_PRESERVE src0_sel:WORD_1\n"
    "v_mfma_f32_16x16x32_bf16 v[112:115], a[88:91], v[192:195], v[112:115]\n"
    "v_fma_mix_f32 v234, v196, v232, 0 op_sel_hi:[1,1,0]\n"
    "v_fma_mix_f32 v232, v196, v232, 0 op_sel:[1,1,0] op_sel_hi:[1,1,0]\n"
    "v_mfma_f32_16x16x32_bf16 v[116:119], a[92:95], v[192:195], v[116:119]\n"
    "ds_read_u16 v191, v191\n"
    "v_cvt_pk_bf16_f32 v196, v234, v232\n"
    "v_mfma_f32_16x16x32_bf16 v[128:131], v[40:43], v[192:195], v[128:131]\n"
    "s_waitcnt lgkmcnt(7)\n"
    "ds_read_u16_d16_hi v207, v207\n"
    "v_or_b32_e32 v185, v185, v201\n"
    "v_mfma_f32_16x16x32_bf16 v[132:135], v[44:47], v[192:195], v[132:135]\n"
    "v_cvt_pk_f16_f32 v192, v168, v169\n"
    "v_lshlrev_b32_sdwa v193, 1, v170 dst_sel:DWORD dst_unused:UNUSED_PAD src0_sel:DWORD src1_sel:WORD_1\n"
    "v_mfma_f32_16x16x32_bf16 v[104:107], a[80:83], v[196:199], v[104:107]\n"
    "ds_read_u16 v193, v193\n"
    "v_lshlrev_b32_sdwa v201, 1, v171 dst_sel:DWORD dst_unused:UNUSED_PAD src0_sel:DWORD src1_sel:WORD_1\n"
    "v_mfma_f32_16x16x32_bf16 v[108:111], a[84:87], v[196:199], v[108:111]\n"
    "v_lshlrev_b32_sdwa v194, 1, v172 dst_sel:DWORD dst_unused:UNUSED_PAD src0_sel:DWORD src1_sel:WORD_1\n"
    "v_or_b32_e32 v186, v186, v202\n"
    "v_mfma_f32_16x16x32_bf16 v[120:123], a[88:91], v[196:199], v[120:123]\n"
    "s_waitcnt lgkmcnt(7)\n"
    "ds_read_u16_d16_hi v201, v201\n"
    "v_lshlrev_b32_sdwa v202, 1, v173 dst_sel:DWORD dst_unused:UNUSED_PAD src0_sel:DWORD src1_sel:WORD_1\n"
    "v_mfma_f32_16x16x32_bf16 v[124:127], a[92:95], v[196:199], v[124:127]\n"
    "v_lshlrev_b32_sdwa v195, 1, v174 dst_sel:DWORD dst_unused:UNUSED_PAD src0_sel:DWORD src1_sel:WORD_1\n"
    "v_or_b32_e32 v187, v187, v203\n"
    "v_mfma_f32_16x16x32_bf16 v[136:139], v[40:43], v[196:199], v[136:139]\n"
    "ds_read_u16 v194, v194\n"
    "v_lshlrev_b32_sdwa v203, 1, v175 dst_sel:DWORD dst_unused:UNUSED_PAD src0_sel:DWORD src1_sel:WORD_1\n"
    "v_mfma_f32_16x16x32_bf16 v[140:143], v[44:47], v[196:199], v[140:143]\n"
    "s_waitcnt lgkmcnt(7)\n"
    "ds_read_u16_d16_hi v202, v202\n"
    "v_cvt_pk_f16_f32 v196, v176, v177\n"
    "v_mfma_f32_16x16x32_bf16 v[168:171], a[144:147], v[144:147], 0\n"
    "v_lshlrev_b32_sdwa v197, 1, v178 dst_sel:DWORD dst_unused:UNUSED_PAD src0_sel:DWORD src1_sel:WORD_1\n"
    "v_lshlrev_b32_sdwa v198, 1, v180 dst_sel:DWORD dst_unused:UNUSED_PAD src0_sel:DWORD src1_sel:WORD_1\n"
    "v_mfma_f32_16x16x32_bf16 v[168:171], a[192:195], v[148:151], v[168:171]\n"
    "ds_read_u16 v195, v195\n"
    "v_lshlrev_b32_sdwa v199, 1, v182 dst_sel:DWORD dst_unused:UNUSED_PAD src0_sel:DWORD src1_sel:WORD_1\n"
    "v_mfma_f32_16x16x32_bf16 v[168:171], a[104:107], v[152:155], v[168:171]\n"
    "v_or_b32_e64 v188, v188, v204\n"
    "v_or_b32_e32 v189, v189, v205\n"
    "v_mfma_f32_16x16x32_bf16 v[172:175], a[148:151], v[144:147], 0\n"
    "s_waitcnt lgkmcnt(7)\n"
    "ds_read_u16_d16_hi v203, v203\n"
    "v_lshlrev_b32_sdwa v205, 1, v179 dst_sel:DWORD dst_unused:UNUSED_PAD src0_sel:DWORD src1_sel:WORD_1\n"
    "v_mfma_f32_16x16x32_bf16 v[172:175], a[196:199], v[148:151], v[172:175]\n"
    "v_cvt_pk_bf16_f32 v96, v96, v97\n"
    "v_cvt_pk_bf16_f32 v97, v98, v99\n"
    "v_mfma_f32_16x16x32_bf16 v[172:175], a[108:111], v[152:155], v[172:175]\n"
    "ds_read_u16 v197, v197\n"
    "v_cvt_pk_bf16_f32 v98, v100, v101\n"
    "v_mfma_f32_16x16x32_bf16 v[176:179], a[144:147], v[156:159], 0\n"
    "s_waitcnt lgkmcnt(7)\n"
    "ds_read_u16_d16_hi v205, v205\n"
    "v_cvt_pk_bf16_f32 v99, v102, v103\n"
    "v_mfma_f32_16x16x32_bf16 v[176:179], a[192:195], v[160:163], v[176:179]\n"
    "v_or_b32_e32 v190, v190, v206\n"
    "v_lshlrev_b32_sdwa v206, 1, v181 dst_sel:DWORD dst_unused:UNUSED_PAD src0_sel:DWORD src1_sel:WORD_1\n"
    "v_mfma_f32_16x16x32_bf16 v[176:179], a[104:107], v[164:167], v[176:179]\n"
    "ds_read_u16 v198, v198\n"
    "v_cvt_pk_bf16_f32 v112, v112, v113\n"
    "v_mfma_f32_16x16x32_bf16 v[100:103], a[4:7], v[184:187], 0\n"
    "s_nop 0\n"
    "v_cvt_pk_bf16_f32 v113, v114, v115\n"
    "v_cvt_pk_bf16_f32 v114, v116, v117\n"
    "ds_write_b128 v242, v[96:99] offset:24640\n"
    "v_mfma_f32_16x16x32_bf16 v[96:99], a[0:3], v[184:187], 0\n"
    "s_waitcnt lgkmcnt(7)\n"
    "v_cvt_pk_bf16_f32 v115, v118, v119\n"
    "v_cvt_pk_bf16_f32 v128, v128, v129\n"
    "v_cvt_pk_bf16_f32 v129, v130, v131\n"
    "v_or_b32_e32 v191, v191, v207\n"
    "v_mfma_f32_16x16x32_bf16 v[116:119], a[12:15], v[184:187], 0\n"
    "s_waitcnt lgkmcnt(6)\n"
    "ds_read_u16_d16_hi v206, v206\n"
    "ds_write_b128 v242, v[112:115] offset:26688\n"
    "v_mfma_f32_16x16x32_bf16 v[112:115], a[8:11], v[184:187], 0\n"
    "v_lshlrev_b32_sdwa v207, 1, v183 dst_sel:DWORD dst_unused:UNUSED_PAD src0_sel:DWORD src1_sel:WORD_1\n"
    "v_cvt_pk_bf16_f32 v130, v132, v133\n"
    "v_mfma_f32_16x16x32_bf16 v[180:183], a[148:151], v[156:159], 0\n"
    "v_or_b32_e64 v193, v193, v201\n"
    "v_cvt_pk_bf16_f32 v131, v134, v135\n"
    "v_mfma_f32_16x16x32_bf16 v[180:183], a[196:199], v[160:163], v[180:183]\n"
    "s_waitcnt lgkmcnt(6)\n"
    "ds_read_u16 v199, v199\n"
    "ds_write_b128 v242, v[128:131] offset:28736\n"
    "v_mfma_f32_16x16x32_bf16 v[180:183], a[108:111], v[164:167], v[180:183]\n"
    "v_cvt_pk_bf16_f32 v104, v104, v105\n"
    "v_cvt_pk_bf16_f32 v105, v106, v107\n"
    "v_mfma_f32_16x16x32_bf16 v[128:131], v[0:3], v[184:187], 0\n"
    "s_waitcnt lgkmcnt(6)\n"
    "v_cvt_pk_bf16_f32 v106, v108, v109\n"
    "v_cvt_pk_bf16_f32 v107, v110, v111\n"
    "v_mfma_f32_16x16x32_bf16 v[132:135], v[4:7], v[184:187], 0\n"
    "ds_read_u16_d16_hi v207, v207\n"
    "ds_write_b128 v242, v[104:107] offset:25664\n"
    "v_mfma_f32_16x16x32_bf16 v[104:107], a[0:3], v[188:191], 0\n"
    "v_or_b32_e64 v194, v194, v202\n"
    "v_cvt_pk_bf16_f32 v120, v120, v121\n"
    "v_mfma_f32_16x16x32_bf16 v[108:111], a[4:7], v[188:191], 0\n"
    "v_cvt_pk_bf16_f32 v121, v122, v123\n"
    "v_cvt_pk_bf16_f32 v122, v124, v125\n"
    "v_cvt_pk_bf16_f32 v123, v126, v127\n"
    "ds_write_b128 v242, v[120:123] offset:27712\n"
    "v_mfma_f32_16x16x32_bf16 v[120:123], a[8:11], v[188:191], 0\n"
    "s_waitcnt lgkmcnt(5)\n"
    "v_cvt_pk_bf16_f32 v136, v136, v137\n"
    "v_pk_fma_f16 v232, v192, v192, s48\n"
    "v_mfma_f32_16x16x32_bf16 v[124:127], a[12:15], v[188:191], 0\n"
    "s_nop 0\n"
    "v_cvt_pk_bf16_f32 v137, v138, v139\n"
    "v_cvt_pk_bf16_f32 v138, v140, v141\n"
    "v_or_b32_e64 v195, v195, v203\n"
    "v_cvt_pk_bf16_f32 v139, v142, v143\n"
    "ds_write_b128 v242, v[136:139] offset:29760\n"
    "v_pk_mul_f16 v232, v192, v232\n"
    "v_mfma_f32_16x16x32_bf16 v[136:139], v[0:3], v[188:191], 0\n"
    "v_exp_f16_e64 v234, v232\n"
    "v_mfma_f32_16x16x32_bf16 v[140:143], v[4:7], v[188:191], 0\n"
    "v_exp_f16_sdwa v234, v232 dst_sel:WORD_1 dst_unused:UNUSED_PRESERVE src0_sel:WORD_1\n"
    "v_or_b32_e64 v197, v197, v205\n"
    "v_pk_fma_f16 v234, v234, s49, s49\n"
    "v_cvt_pk_f16_f32 v184, v168, v169\n"
    "v_lshlrev_b32_sdwa v185, 1, v170 dst_sel:DWORD dst_unused:UNUSED_PAD src0_sel:DWORD src1_sel:WORD_1\n"
    "v_lshlrev_b32_sdwa v201, 1, v171 dst_sel:DWORD dst_unused:UNUSED_PAD src0_sel:DWORD src1_sel:WORD_1\n"
    "v_mfma_f32_16x16x32_bf16 v[168:171], a[152:155], v[144:147], 0\n"
    "s_waitcnt lgkmcnt(1)\n"
    "ds_read_u16 v185, v185\n"
    "ds_read_u16_d16_hi v201, v201\n"
    "v_mfma_f32_16x16x32_bf16 v[168:171], a[200:203], v[148:151], v[168:171]\n"
    "s_nop 0\n"
    "v_lshlrev_b32_sdwa v186, 1, v172 dst_sel:DWORD dst_unused:UNUSED_PAD src0_sel:DWORD src1_sel:WORD_1\n"
    "v_lshlrev_b32_sdwa v202, 1, v173 dst_sel:DWORD dst_unused:UNUSED_PAD src0_sel:DWORD src1_sel:WORD_1\n"
    "v_lshlrev_b32_sdwa v187, 1, v174 dst_sel:DWORD dst_unused:UNUSED_PAD src0_sel:DWORD src1_sel:WORD_1\n"
    "v_lshlrev_b32_sdwa v203, 1, v175 dst_sel:DWORD dst_unused:UNUSED_PAD src0_sel:DWORD src1_sel:WORD_1\n"
    "v_rcp_f16_e32 v232, v234\n"
    "v_mfma_f32_16x16x32_bf16 v[168:171], a[112:115], v[152:155], v[168:171]\n"
    "s_waitcnt lgkmcnt(2)\n"
    "ds_read_u16 v186, v186\n"
    "ds_read_u16_d16_hi v202, v202\n"
    "v_or_b32_e32 v198, v198, v206\n"
    "v_rcp_f16_sdwa v232, v234 dst_sel:WORD_1 dst_unused:UNUSED_PRESERVE src0_sel:WORD_1\n"
    "v_mfma_f32_16x16x32_bf16 v[172:175], a[156:159], v[144:147], 0\n"
    "s_barrier\n"
    "v_mfma_f32_16x16x32_bf16 v[172:175], a[204:207], v[148:151], v[172:175]\n"
    "ds_read_u16 v187, v187\n"
    "ds_read_u16_d16_hi v203, v203\n"
    "v_cvt_pk_f16_f32 v188, v176, v177\n"
    "v_lshlrev_b32_sdwa v189, 1, v178 dst_sel:DWORD dst_unused:UNUSED_PAD src0_sel:DWORD src1_sel:WORD_1\n"
    "v_lshlrev_b32_sdwa v205, 1, v179 dst_sel:DWORD dst_unused:UNUSED_PAD src0_sel:DWORD src1_sel:WORD_1\n"
    "v_lshlrev_b32_sdwa v190, 1, v180 dst_sel:DWORD dst_unused:UNUSED_PAD src0_sel:DWORD src1_sel:WORD_1\n"
    "v_lshlrev_b32_sdwa v206, 1, v181 dst_sel:DWORD dst_unused:UNUSED_PAD src0_sel:DWORD src1_sel:WORD_1\n"
    "v_lshlrev_b32_sdwa v191, 1, v182 dst_sel:DWORD dst_unused:UNUSED_PAD src0_sel:DWORD src1_sel:WORD_1\n"
    "v_fma_mix_f32 v234, v192, v232, 0 op_sel_hi:[1,1,0]\n"
    "v_mfma_f32_16x16x32_bf16 v[172:175], a[116:119], v[152:155], v[172:175]\n"
    "ds_read_u16 v189, v189\n"
    "v_or_b32_e64 v199, v199, v207\n"
    "v_lshlrev_b32_sdwa v207, 1, v183 dst_sel:DWORD dst_unused:UNUSED_PAD src0_sel:DWORD src1_sel:WORD_1\n"
    "v_fma_mix_f32 v232, v192, v232, 0 op_sel:[1,1,0] op_sel_hi:[1,1,0]\n"
    "v_mfma_f32_16x16x32_bf16 v[176:179], a[152:155], v[156:159], 0\n"
    "s_nop 0\n"
    "ds_read_u16_d16_hi v205, v205\n"
    "v_cvt_pk_bf16_f32 v192, v234, v232\n"
    "v_mfma_f32_16x16x32_bf16 v[176:179], a[200:203], v[160:163], v[176:179]\n"
    "s_waitcnt lgkmcnt(6)\n"
    "ds_read_u16 v190, v190\n"
    "v_pk_fma_f16 v232, v196, v196, s48\n"
    "v_mfma_f32_16x16x32_bf16 v[176:179], a[112:115], v[164:167], v[176:179]\n"
    "ds_read_u16_d16_hi v206, v206\n"
    "v_pk_mul_f16 v232, v196, v232\n"
    "v_mfma_f32_16x16x32_bf16 v[180:183], a[156:159], v[156:159], 0\n"
    "ds_read_u16 v191, v191\n"
    "v_exp_f16_e32 v234, v232\n"
    "v_mfma_f32_16x16x32_bf16 v[180:183], a[204:207], v[160:163], v[180:183]\n"
    "s_waitcnt lgkmcnt(7)\n"
    "ds_read_u16_d16_hi v207, v207\n"
    "v_exp_f16_sdwa v234, v232 dst_sel:WORD_1 dst_unused:UNUSED_PRESERVE src0_sel:WORD_1\n"
    "v_mfma_f32_16x16x32_bf16 v[180:183], a[116:119], v[164:167], v[180:183]\n"
    "v_pk_fma_f16 v234, v234, s49, s49\n"
    "v_or_b32_e64 v185, v185, v201\n"
    "v_mfma_f32_16x16x32_bf16 v[96:99], a[16:19], v[192:195], v[96:99]\n"
    "v_rcp_f16_e32 v232, v234\n"
    "v_mfma_f32_16x16x32_bf16 v[100:103], a[20:23], v[192:195], v[100:103]\n"
    "v_rcp_f16_sdwa v232, v234 dst_sel:WORD_1 dst_unused:UNUSED_PRESERVE src0_sel:WORD_1\n"
    "v_mfma_f32_16x16x32_bf16 v[112:115], a[24:27], v[192:195], v[112:115]\n"
    "s_waitcnt lgkmcnt(6)\n"
    "v_fma_mix_f32 v234, v196, v232, 0 op_sel_hi:[1,1,0]\n"
    "v_fma_mix_f32 v232, v196, v232, 0 op_sel:[1,1,0] op_sel_hi:[1,1,0]\n"
    "v_mfma_f32_16x16x32_bf16 v[116:119], a[28:31], v[192:195], v[116:119]\n"
    "v_or_b32_e64 v186, v186, v202\n"
    "v_cvt_pk_bf16_f32 v196, v234, v232\n"
    "v_mfma_f32_16x16x32_bf16 v[128:131], v[8:11], v[192:195], v[128:131]\n"
    "v_pk_fma_f16 v232, v184, v184, s48\n"
    "v_or_b32_e32 v187, v187, v203\n"
    "v_mfma_f32_16x16x32_bf16 v[132:135], v[12:15], v[192:195], v[132:135]\n"
    "s_waitcnt lgkmcnt(4)\n"
    "v_pk_mul_f16 v232, v184, v232\n"
    "v_or_b32_e64 v189, v189, v205\n"
    "v_mfma_f32_16x16x32_bf16 v[104:107], a[16:19], v[196:199], v[104:107]\n"
    "v_exp_f16_e32 v234, v232\n"
    "v_mfma_f32_16x16x32_bf16 v[108:111], a[20:23], v[196:199], v[108:111]\n"
    "v_exp_f16_sdwa v234, v232 dst_sel:WORD_1 dst_unused:UNUSED_PRESERVE src0_sel:WORD_1\n"
    "v_mfma_f32_16x16x32_bf16 v[120:123], a[24:27], v[196:199], v[120:123]\n"
    "v_pk_fma_f16 v234, v234, s49, s49\n"
    "v_lshlrev_b32_sdwa v192, 1, v168 dst_sel:DWORD dst_unused:UNUSED_PAD src0_sel:DWORD src1_sel:WORD_1\n"
    "v_mfma_f32_16x16x32_bf16 v[124:127], a[28:31], v[196:199], v[124:127]\n"
    "s_waitcnt lgkmcnt(2)\n"
    "ds_read_u16 v192, v192\n"
    "v_lshlrev_b32_sdwa v200, 1, v169 dst_sel:DWORD dst_unused:UNUSED_PAD src0_sel:DWORD src1_sel:WORD_1\n"
    "v_mfma_f32_16x16x32_bf16 v[136:139], v[8:11], v[196:199], v[136:139]\n"
    "ds_read_u16_d16_hi v200, v200\n"
    "v_lshlrev_b32_sdwa v193, 1, v170 dst_sel:DWORD dst_unused:UNUSED_PAD src0_sel:DWORD src1_sel:WORD_1\n"
    "v_mfma_f32_16x16x32_bf16 v[140:143], v[12:15], v[196:199], v[140:143]\n"
    "ds_read_u16 v193, v193\n"
    "v_lshlrev_b32_sdwa v201, 1, v171 dst_sel:DWORD dst_unused:UNUSED_PAD src0_sel:DWORD src1_sel:WORD_1\n"
    "s_waitcnt vmcnt(8)\n"
    "v_smfmac_f32_32x32x32_bf16 v[112:127], a[248:251], a[232:239], v241\n"
    "s_waitcnt lgkmcnt(3)\n"
    "ds_read_u16_d16_hi v201, v201\n"
    "ds_read_b128 a[232:235], v243 offset:24640\n"
    "v_lshlrev_b32_sdwa v194, 1, v172 dst_sel:DWORD dst_unused:UNUSED_PAD src0_sel:DWORD src1_sel:WORD_1\n"
    "v_lshlrev_b32_sdwa v202, 1, v173 dst_sel:DWORD dst_unused:UNUSED_PAD src0_sel:DWORD src1_sel:WORD_1\n"
    "v_lshlrev_b32_sdwa v195, 1, v174 dst_sel:DWORD dst_unused:UNUSED_PAD src0_sel:DWORD src1_sel:WORD_1\n"
    "s_waitcnt vmcnt(7)\n"
    "v_smfmac_f32_32x32x32_bf16 v[96:111], a[248:251], a[224:231], v241\n"
    "s_nop 0\n"
    "ds_read_u16 v194, v194\n"
    "ds_read_u16_d16_hi v202, v202\n"
    "v_lshlrev_b32_sdwa v203, 1, v175 dst_sel:DWORD dst_unused:UNUSED_PAD src0_sel:DWORD src1_sel:WORD_1\n"
    "v_rcp_f16_e32 v232, v234\n"
    "s_waitcnt vmcnt(6)\n"
    "v_smfmac_f32_32x32x32_bf16 v[128:143], a[248:251], v[208:215], v241\n"
    "ds_read_b128 a[236:239], v243 offset:25664\n"
    "v_or_b32_e64 v190, v190, v206\n"
    "v_rcp_f16_sdwa v232, v234 dst_sel:WORD_1 dst_unused:UNUSED_PRESERVE src0_sel:WORD_1\n"
    "v_lshlrev_b32_sdwa v196, 1, v176 dst_sel:DWORD dst_unused:UNUSED_PAD src0_sel:DWORD src1_sel:WORD_1\n"
    "v_mfma_f32_16x16x32_bf16 v[168:171], a[160:163], v[144:147], 0\n"
    "ds_read_u16 v195, v195\n"
    "v_lshlrev_b32_sdwa v204, 1, v177 dst_sel:DWORD dst_unused:UNUSED_PAD src0_sel:DWORD src1_sel:WORD_1\n"
    "v_mfma_f32_16x16x32_bf16 v[168:171], a[208:211], v[148:151], v[168:171]\n"
    "ds_read_u16_d16_hi v203, v203\n"
    "v_lshlrev_b32_sdwa v197, 1, v178 dst_sel:DWORD dst_unused:UNUSED_PAD src0_sel:DWORD src1_sel:WORD_1\n"
    "v_mfma_f32_16x16x32_bf16 v[168:171], a[120:123], v[152:155], v[168:171]\n"
    "v_lshlrev_b32_sdwa v205, 1, v179 dst_sel:DWORD dst_unused:UNUSED_PAD src0_sel:DWORD src1_sel:WORD_1\n"
    "v_lshlrev_b32_sdwa v198, 1, v180 dst_sel:DWORD dst_unused:UNUSED_PAD src0_sel:DWORD src1_sel:WORD_1\n"
    "v_mfma_f32_16x16x32_bf16 v[172:175], a[164:167], v[144:147], 0\n"
    "ds_read_u16 v196, v196\n"
    "v_lshlrev_b32_sdwa v206, 1, v181 dst_sel:DWORD dst_unused:UNUSED_PAD src0_sel:DWORD src1_sel:WORD_1\n"
    "v_mfma_f32_16x16x32_bf16 v[172:175], a[212:215], v[148:151], v[172:175]\n"
    "v_lshlrev_b32_sdwa v199, 1, v182 dst_sel:DWORD dst_unused:UNUSED_PAD src0_sel:DWORD src1_sel:WORD_1\n"
    "v_fma_mix_f32 v234, v184, v232, 0 op_sel_hi:[1,1,0]\n"
    "v_mfma_f32_16x16x32_bf16 v[172:175], v[52:55], v[152:155], v[172:175]\n"
    "s_waitcnt lgkmcnt(8)\n"
    "ds_read_u16_d16_hi v204, v204\n"
    "v_or_b32_e32 v191, v191, v207\n"
    "v_lshlrev_b32_sdwa v207, 1, v183 dst_sel:DWORD dst_unused:UNUSED_PAD src0_sel:DWORD src1_sel:WORD_1\n"
    "v_fma_mix_f32 v232, v184, v232, 0 op_sel:[1,1,0] op_sel_hi:[1,1,0]\n"
    "v_mfma_f32_16x16x32_bf16 v[176:179], a[160:163], v[156:159], 0\n"
    "ds_read_u16 v197, v197\n"
    "v_cvt_pk_bf16_f32 v184, v234, v232\n"
    "v_mfma_f32_16x16x32_bf16 v[176:179], a[208:211], v[160:163], v[176:179]\n"
    "ds_read_u16_d16_hi v205, v205\n"
    "v_pk_fma_f16 v232, v188, v188, s48\n"
    "v_mfma_f32_16x16x32_bf16 v[176:179], a[120:123], v[164:167], v[176:179]\n"
    "s_waitcnt lgkmcnt(8)\n"
    "v_pk_mul_f16 v232, v188, v232\n"
    "v_or_b32_e32 v192, v192, v200\n"
    "v_mfma_f32_16x16x32_bf16 v[180:183], a[164:167], v[156:159], 0\n"
    "ds_read_u16 v198, v198\n"
    "v_exp_f16_e32 v234, v232\n"
    "s_waitcnt lgkmcnt(7)\n"
    "v_smfmac_f32_32x32x32_bf16 v[96:111], a[248:251], a[232:239], v241\n"
    "ds_read_u16_d16_hi v206, v206\n"
    "v_exp_f16_sdwa v234, v232 dst_sel:WORD_1 dst_unused:UNUSED_PRESERVE src0_sel:WORD_1\n"
    "v_or_b32_e64 v193, v193, v201\n"
    "v_pk_fma_f16 v234, v234, s49, s49\n"
    "v_mfma_f32_16x16x32_bf16 v[180:183], a[212:215], v[160:163], v[180:183]\n"
    "ds_read_u16 v199, v199\n"
    "v_rcp_f16_e32 v232, v234\n"
    "v_mfma_f32_16x16x32_bf16 v[180:183], v[52:55], v[164:167], v[180:183]\n"
    "v_rcp_f16_sdwa v232, v234 dst_sel:WORD_1 dst_unused:UNUSED_PRESERVE src0_sel:WORD_1\n"
    "v_mfma_f32_16x16x32_bf16 v[112:115], a[40:43], v[184:187], v[112:115]\n"
    "s_waitcnt lgkmcnt(7)\n"
    "ds_read_u16_d16_hi v207, v207\n"
    "v_fma_mix_f32 v234, v188, v232, 0 op_sel_hi:[1,1,0]\n"
    "v_mfma_f32_16x16x32_bf16 v[96:99], a[32:35], v[184:187], v[96:99]\n"
    "ds_read_b128 a[232:235], v243 offset:26688\n"
    "v_fma_mix_f32 v232, v188, v232, 0 op_sel:[1,1,0] op_sel_hi:[1,1,0]\n"
    "v_mfma_f32_16x16x32_bf16 v[100:103], a[36:39], v[184:187], v[100:103]\n"
    "ds_read_b128 a[236:239], v243 offset:27712\n"
    "v_cvt_pk_bf16_f32 v188, v234, v232\n"
    "v_mfma_f32_16x16x32_bf16 v[116:119], a[44:47], v[184:187], v[116:119]\n"
    "s_waitcnt lgkmcnt(8)\n"
    "v_lshlrev_b32_sdwa v201, 1, v171 dst_sel:DWORD dst_unused:UNUSED_PAD src0_sel:DWORD src1_sel:WORD_1\n"
    "v_or_b32_e32 v194, v194, v202\n"
    "v_mfma_f32_16x16x32_bf16 v[128:131], v[16:19], v[184:187], v[128:131]\n"
    "ds_read_u16_d16_hi v201, v201\n"
    "v_lshlrev_b32_sdwa v202, 1, v173 dst_sel:DWORD dst_unused:UNUSED_PAD src0_sel:DWORD src1_sel:WORD_1\n"
    "v_mfma_f32_16x16x32_bf16 v[132:135], v[20:23], v[184:187], v[132:135]\n"
    "ds_read_u16_d16_hi v202, v202\n"
    "v_cvt_pk_f16_f32 v184, v168, v169\n"
    "v_mfma_f32_16x16x32_bf16 v[104:107], a[32:35], v[188:191], v[104:107]\n"
    "v_lshlrev_b32_sdwa v185, 1, v170 dst_sel:DWORD dst_unused:UNUSED_PAD src0_sel:DWORD src1_sel:WORD_1\n"
    "v_lshlrev_b32_sdwa v186, 1, v172 dst_sel:DWORD dst_unused:UNUSED_PAD src0_sel:DWORD src1_sel:WORD_1\n"
    "v_mfma_f32_16x16x32_bf16 v[108:111], a[36:39], v[188:191], v[108:111]\n"
    "s_waitcnt lgkmcnt(8)\n"
    "ds_read_u16 v185, v185\n"
    "v_lshlrev_b32_sdwa v187, 1, v174 dst_sel:DWORD dst_unused:UNUSED_PAD src0_sel:DWORD src1_sel:WORD_1\n"
    "v_mfma_f32_16x16x32_bf16 v[120:123], a[40:43], v[188:191], v[120:123]\n"
    "v_or_b32_e32 v195, v195, v203\n"
    "v_lshlrev_b32_sdwa v203, 1, v175 dst_sel:DWORD dst_unused:UNUSED_PAD src0_sel:DWORD src1_sel:WORD_1\n"
    "v_mfma_f32_16x16x32_bf16 v[124:127], a[44:47], v[188:191], v[124:127]\n"
    "ds_read_u16 v186, v186\n"
    "v_or_b32_e32 v196, v196, v204\n"
    "v_mfma_f32_16x16x32_bf16 v[136:139], v[16:19], v[188:191], v[136:139]\n"
    "s_waitcnt lgkmcnt(8)\n"
    "ds_read_u16 v187, v187\n"
    "v_or_b32_e64 v197, v197, v205\n"
    "v_mfma_f32_16x16x32_bf16 v[140:143], v[20:23], v[188:191], v[140:143]\n"
    "v_cvt_pk_f16_f32 v188, v176, v177\n"
    "v_lshlrev_b32_sdwa v189, 1, v178 dst_sel:DWORD dst_unused:UNUSED_PAD src0_sel:DWORD src1_sel:WORD_1\n"
    "v_mfma_f32_16x16x32_bf16 v[168:171], a[168:171], v[144:147], 0\n"
    "ds_read_u16_d16_hi v203, v203\n"
    "v_lshlrev_b32_sdwa v205, 1, v179 dst_sel:DWORD dst_unused:UNUSED_PAD src0_sel:DWORD src1_sel:WORD_1\n"
    "v_mfma_f32_16x16x32_bf16 v[168:171], a[216:219], v[148:151], v[168:171]\n"
    "v_lshlrev_b32_sdwa v190, 1, v180 dst_sel:DWORD dst_unused:UNUSED_PAD src0_sel:DWORD src1_sel:WORD_1\n"
    "v_lshlrev_b32_sdwa v191, 1, v182 dst_sel:DWORD dst_unused:UNUSED_PAD src0_sel:DWORD src1_sel:WORD_1\n"
    "v_mfma_f32_16x16x32_bf16 v[168:171], a[124:127], v[152:155], v[168:171]\n"
    "s_waitcnt lgkmcnt(6)\n"
    "ds_read_u16 v189, v189\n"
    "v_or_b32_e32 v198, v198, v206\n"
    "v_smfmac_f32_32x32x32_bf16 v[112:127], a[248:251], a[232:239], v241\n"
    "ds_read_b128 a[232:235], v243 offset:28736\n"
    "v_lshlrev_b32_sdwa v206, 1, v181 dst_sel:DWORD dst_unused:UNUSED_PAD src0_sel:DWORD src1_sel:WORD_1\n"
    "v_or_b32_e64 v199, v199, v207\n"
    "v_lshlrev_b32_sdwa v207, 1, v183 dst_sel:DWORD dst_unused:UNUSED_PAD src0_sel:DWORD src1_sel:WORD_1\n"
    "v_pk_fma_f16 v232, v184, v184, s48\n"
    "v_mfma_f32_16x16x32_bf16 v[172:175], a[172:175], v[144:147], 0\n"
    "ds_read_u16_d16_hi v205, v205\n"
    "v_pk_mul_f16 v232, v184, v232\n"
    "v_mfma_f32_16x16x32_bf16 v[172:175], a[220:223], v[148:151], v[172:175]\n"
    "ds_read_b128 a[236:239], v243 offset:29760\n"
    "v_exp_f16_e64 v234, v232\n"
    "v_mfma_f32_16x16x32_bf16 v[172:175], v[60:63], v[152:155], v[172:175]\n"
    "ds_read_u16 v190, v190\n"
    "v_exp_f16_sdwa v234, v232 dst_sel:WORD_1 dst_unused:UNUSED_PRESERVE src0_sel:WORD_1\n"
    "v_mfma_f32_16x16x32_bf16 v[176:179], a[168:171], v[156:159], 0\n"
    "s_waitcnt lgkmcnt(8)\n"
    "v_or_b32_e32 v185, v185, v201\n"
    "v_pk_fma_f16 v234, v234, s49, s49\n"
    "v_mfma_f32_16x16x32_bf16 v[176:179], a[216:219], v[160:163], v[176:179]\n"
    "ds_read_u16_d16_hi v206, v206\n"
    "v_lshlrev_b32_sdwa v201, 1, v171 dst_sel:DWORD dst_unused:UNUSED_PAD src0_sel:DWORD src1_sel:WORD_1\n"
    "v_mfma_f32_16x16x32_bf16 v[176:179], a[124:127], v[164:167], v[176:179]\n"
    "s_waitcnt lgkmcnt(8)\n"
    "ds_read_u16 v191, v191\n"
    "v_or_b32_e32 v186, v186, v202\n"
    "v_mfma_f32_16x16x32_bf16 v[180:183], a[172:175], v[156:159], 0\n"
    "v_lshlrev_b32_sdwa v202, 1, v173 dst_sel:DWORD dst_unused:UNUSED_PAD src0_sel:DWORD src1_sel:WORD_1\n"
    "v_rcp_f16_e64 v232, v234\n"
    "v_mfma_f32_16x16x32_bf16 v[180:183], a[220:223], v[160:163], v[180:183]\n"
    "ds_read_u16_d16_hi v207, v207\n"
    "v_rcp_f16_sdwa v232, v234 dst_sel:WORD_1 dst_unused:UNUSED_PRESERVE src0_sel:WORD_1\n"
    "v_mfma_f32_16x16x32_bf16 v[180:183], v[60:63], v[164:167], v[180:183]\n"
    "s_waitcnt lgkmcnt(8)\n"
    "ds_read_u16_d16_hi v201, v201\n"
    "v_or_b32_e32 v187, v187, v203\n"
    "v_mfma_f32_16x16x32_bf16 v[96:99], a[48:51], v[192:195], v[96:99]\n"
    "v_lshlrev_b32_sdwa v203, 1, v175 dst_sel:DWORD dst_unused:UNUSED_PAD src0_sel:DWORD src1_sel:WORD_1\n"
    "v_fma_mix_f32 v234, v184, v232, 0 op_sel_hi:[1,1,0]\n"
    "v_mfma_f32_16x16x32_bf16 v[100:103], a[52:55], v[192:195], v[100:103]\n"
    "ds_read_u16_d16_hi v202, v202\n"
    "v_fma_mix_f32 v232, v184, v232, 0 op_sel:[1,1,0] op_sel_hi:[1,1,0]\n"
    "v_mfma_f32_16x16x32_bf16 v[112:115], a[56:59], v[192:195], v[112:115]\n"
    "v_cvt_pk_bf16_f32 v184, v234, v232\n"
    "v_pk_fma_f16 v232, v188, v188, s48\n"
    "v_mfma_f32_16x16x32_bf16 v[116:119], a[60:63], v[192:195], v[116:119]\n"
    "ds_read_u16_d16_hi v203, v203\n"
    "s_waitcnt lgkmcnt(7)\n"
    "v_or_b32_e32 v189, v189, v205\n"
    "v_mfma_f32_16x16x32_bf16 v[128:131], v[24:27], v[192:195], v[128:131]\n"
    "v_lshlrev_b32_sdwa v205, 1, v179 dst_sel:DWORD dst_unused:UNUSED_PAD src0_sel:DWORD src1_sel:WORD_1\n"
    "v_pk_mul_f16 v232, v188, v232\n"
    "v_mfma_f32_16x16x32_bf16 v[132:135], v[28:31], v[192:195], v[132:135]\n"
    "ds_read_u16_d16_hi v205, v205\n"
    "v_cvt_pk_f16_f32 v192, v168, v169\n"
    "v_mfma_f32_16x16x32_bf16 v[104:107], a[48:51], v[196:199], v[104:107]\n"
    "v_lshlrev_b32_sdwa v193, 1, v170 dst_sel:DWORD dst_unused:UNUSED_PAD src0_sel:DWORD src1_sel:WORD_1\n"
    "v_lshlrev_b32_sdwa v194, 1, v172 dst_sel:DWORD dst_unused:UNUSED_PAD src0_sel:DWORD src1_sel:WORD_1\n"
    "v_mfma_f32_16x16x32_bf16 v[108:111], a[52:55], v[196:199], v[108:111]\n"
    "ds_read_u16 v193, v193\n"
    "v_lshlrev_b32_sdwa v195, 1, v174 dst_sel:DWORD dst_unused:UNUSED_PAD src0_sel:DWORD src1_sel:WORD_1\n"
    "v_mfma_f32_16x16x32_bf16 v[120:123], a[56:59], v[196:199], v[120:123]\n"
    "s_waitcnt lgkmcnt(7)\n"
    "ds_read_u16 v194, v194\n"
    "v_or_b32_e32 v190, v190, v206\n"
    "v_mfma_f32_16x16x32_bf16 v[124:127], a[60:63], v[196:199], v[124:127]\n"
    "v_lshlrev_b32_sdwa v206, 1, v181 dst_sel:DWORD dst_unused:UNUSED_PAD src0_sel:DWORD src1_sel:WORD_1\n"
    "v_exp_f16_e32 v234, v232\n"
    "v_mfma_f32_16x16x32_bf16 v[136:139], v[24:27], v[196:199], v[136:139]\n"
    "ds_read_u16 v195, v195\n"
    "v_exp_f16_sdwa v234, v232 dst_sel:WORD_1 dst_unused:UNUSED_PRESERVE src0_sel:WORD_1\n"
    "v_mfma_f32_16x16x32_bf16 v[140:143], v[28:31], v[196:199], v[140:143]\n"
    "s_waitcnt lgkmcnt(7)\n"
    "ds_read_u16_d16_hi v206, v206\n"
    "v_cvt_pk_f16_f32 v196, v176, v177\n"
    "v_mfma_f32_16x16x32_bf16 v[96:99], a[64:67], v[184:187], v[96:99]\n"
    "v_lshlrev_b32_sdwa v197, 1, v178 dst_sel:DWORD dst_unused:UNUSED_PAD src0_sel:DWORD src1_sel:WORD_1\n"
    "v_lshlrev_b32_sdwa v198, 1, v180 dst_sel:DWORD dst_unused:UNUSED_PAD src0_sel:DWORD src1_sel:WORD_1\n"
    "v_smfmac_f32_32x32x32_bf16 v[128:143], a[248:251], a[232:239], v241\n"
    "ds_read_u16 v197, v197\n"
    "v_lshlrev_b32_sdwa v199, 1, v182 dst_sel:DWORD dst_unused:UNUSED_PAD src0_sel:DWORD src1_sel:WORD_1\n"
    "v_or_b32_e64 v191, v191, v207\n"
    "v_lshlrev_b32_sdwa v207, 1, v183 dst_sel:DWORD dst_unused:UNUSED_PAD src0_sel:DWORD src1_sel:WORD_1\n"
    "v_pk_fma_f16 v234, v234, s49, s49\n"
    "v_mfma_f32_16x16x32_bf16 v[100:103], a[68:71], v[184:187], v[100:103]\n"
    "ds_read_u16 v198, v198\n"
    "v_rcp_f16_e64 v232, v234\n"
    "v_mfma_f32_16x16x32_bf16 v[112:115], a[72:75], v[184:187], v[112:115]\n"
    "ds_read_u16 v199, v199\n"
    "v_rcp_f16_sdwa v232, v234 dst_sel:WORD_1 dst_unused:UNUSED_PRESERVE src0_sel:WORD_1\n"
    "v_mfma_f32_16x16x32_bf16 v[116:119], a[76:79], v[184:187], v[116:119]\n"
    "ds_read_u16_d16_hi v207, v207\n"
    "v_fma_mix_f32 v234, v188, v232, 0 op_sel_hi:[1,1,0]\n"
    "v_mfma_f32_16x16x32_bf16 v[128:131], v[32:35], v[184:187], v[128:131]\n"
    "v_fma_mix_f32 v232, v188, v232, 0 op_sel:[1,1,0] op_sel_hi:[1,1,0]\n"
    "v_cvt_pk_bf16_f32 v188, v234, v232\n"
    "v_mfma_f32_16x16x32_bf16 v[132:135], v[36:39], v[184:187], v[132:135]\n"
    "s_waitcnt lgkmcnt(7)\n"
    "v_or_b32_e32 v193, v193, v201\n"
    "v_pk_fma_f16 v232, v192, v192, s48\n"
    "v_mfma_f32_16x16x32_bf16 v[104:107], a[64:67], v[188:191], v[104:107]\n"
    "v_pk_mul_f16 v232, v192, v232\n"
    "v_exp_f16_e64 v234, v232\n"
    "v_mfma_f32_16x16x32_bf16 v[108:111], a[68:71], v[188:191], v[108:111]\n"
    "s_waitcnt lgkmcnt(6)\n"
    "v_or_b32_e32 v194, v194, v202\n"
    "v_exp_f16_sdwa v234, v232 dst_sel:WORD_1 dst_unused:UNUSED_PRESERVE src0_sel:WORD_1\n"
    "v_mfma_f32_16x16x32_bf16 v[120:123], a[72:75], v[188:191], v[120:123]\n"
    "s_waitcnt lgkmcnt(5)\n"
    "v_or_b32_e32 v195, v195, v203\n"
    "v_pk_fma_f16 v234, v234, s49, s49\n"
    "v_mfma_f32_16x16x32_bf16 v[124:127], a[76:79], v[188:191], v[124:127]\n"
    "v_rcp_f16_e64 v232, v234\n"
    "v_mfma_f32_16x16x32_bf16 v[136:139], v[32:35], v[188:191], v[136:139]\n"
    "v_rcp_f16_sdwa v232, v234 dst_sel:WORD_1 dst_unused:UNUSED_PRESERVE src0_sel:WORD_1\n"
    "v_mfma_f32_16x16x32_bf16 v[140:143], v[36:39], v[188:191], v[140:143]\n"
    "v_fma_mix_f32 v234, v192, v232, 0 op_sel_hi:[1,1,0]\n"
    "s_waitcnt lgkmcnt(3)\n"
    "v_or_b32_e32 v197, v197, v205\n"
    "v_fma_mix_f32 v232, v192, v232, 0 op_sel:[1,1,0] op_sel_hi:[1,1,0]\n"
    "v_cvt_pk_bf16_f32 v192, v234, v232\n"
    "v_pk_fma_f16 v232, v196, v196, s48\n"
    "v_pk_mul_f16 v232, v196, v232\n"
    "v_exp_f16_e32 v234, v232\n"
    "s_waitcnt lgkmcnt(2)\n"
    "v_or_b32_e64 v198, v198, v206\n"
    "v_exp_f16_sdwa v234, v232 dst_sel:WORD_1 dst_unused:UNUSED_PRESERVE src0_sel:WORD_1\n"
    "v_mfma_f32_16x16x32_bf16 v[96:99], a[80:83], v[192:195], v[96:99]\n"
    "v_pk_fma_f16 v234, v234, s49, s49\n"
    "v_rcp_f16_e64 v232, v234\n"
    "v_mfma_f32_16x16x32_bf16 v[100:103], a[84:87], v[192:195], v[100:103]\n"
    "v_rcp_f16_sdwa v232, v234 dst_sel:WORD_1 dst_unused:UNUSED_PRESERVE src0_sel:WORD_1\n"
    "v_mfma_f32_16x16x32_bf16 v[112:115], a[88:91], v[192:195], v[112:115]\n"
    "s_waitcnt lgkmcnt(0)\n"
    "v_or_b32_e32 v199, v199, v207\n"
    "v_fma_mix_f32 v234, v196, v232, 0 op_sel_hi:[1,1,0]\n"
    "v_mfma_f32_16x16x32_bf16 v[116:119], a[92:95], v[192:195], v[116:119]\n"
    "v_fma_mix_f32 v232, v196, v232, 0 op_sel:[1,1,0] op_sel_hi:[1,1,0]\n"
    "v_cvt_pk_bf16_f32 v96, v96, v97\n"
    "v_mfma_f32_16x16x32_bf16 v[128:131], v[40:43], v[192:195], v[128:131]\n"
    "v_cvt_pk_bf16_f32 v196, v234, v232\n"
    "v_cvt_pk_bf16_f32 v97, v98, v99\n"
    "v_mfma_f32_16x16x32_bf16 v[132:135], v[44:47], v[192:195], v[132:135]\n"
    "v_cvt_pk_bf16_f32 v98, v100, v101\n"
    "v_cvt_pk_bf16_f32 v99, v102, v103\n"
    "v_mfma_f32_16x16x32_bf16 v[104:107], a[80:83], v[196:199], v[104:107]\n"
    "ds_write_b128 v243, v[96:99] offset:24640\n"
    "v_mfma_f32_16x16x32_bf16 v[108:111], a[84:87], v[196:199], v[108:111]\n"
    "v_cvt_pk_bf16_f32 v112, v112, v113\n"
    "v_cvt_pk_bf16_f32 v113, v114, v115\n"
    "v_mfma_f32_16x16x32_bf16 v[120:123], a[88:91], v[196:199], v[120:123]\n"
    "v_cvt_pk_bf16_f32 v114, v116, v117\n"
    "v_cvt_pk_bf16_f32 v115, v118, v119\n"
    "v_mfma_f32_16x16x32_bf16 v[124:127], a[92:95], v[196:199], v[124:127]\n"
    "ds_read_b128 a[224:227], v249 offset:0\n"
    "ds_write_b128 v243, v[112:115] offset:26688\n"
    "v_mfma_f32_16x16x32_bf16 v[136:139], v[40:43], v[196:199], v[136:139]\n"
    "v_cvt_pk_bf16_f32 v128, v128, v129\n"
    "v_cvt_pk_bf16_f32 v129, v130, v131\n"
    "v_mfma_f32_16x16x32_bf16 v[140:143], v[44:47], v[196:199], v[140:143]\n"
    "v_cvt_pk_bf16_f32 v130, v132, v133\n"
    "v_cvt_pk_bf16_f32 v131, v134, v135\n"
    "v_cvt_pk_bf16_f32 v104, v104, v105\n"
    "ds_read_b128 a[228:231], v249 offset:2048\n"
    "ds_write_b128 v243, v[128:131] offset:28736\n"
    "v_cvt_pk_bf16_f32 v105, v106, v107\n"
    "v_cvt_pk_bf16_f32 v106, v108, v109\n"
    "v_cvt_pk_bf16_f32 v107, v110, v111\n"
    "v_cvt_pk_bf16_f32 v120, v120, v121\n"
    "v_cvt_pk_bf16_f32 v121, v122, v123\n"
    "v_cvt_pk_bf16_f32 v122, v124, v125\n"
    "v_cvt_pk_bf16_f32 v123, v126, v127\n"
    "v_cvt_pk_bf16_f32 v136, v136, v137\n"
    "ds_read_b128 a[232:235], v249 offset:4096\n"
    "s_nop 0\n"
    "s_waitcnt lgkmcnt(4)\n"
    "buffer_store_dwordx4 a[224:227], v255, s[44:47], s58 offen offset:0 sc1\n"
    "ds_write_b128 v243, v[104:107] offset:25664\n"
    "v_cvt_pk_bf16_f32 v137, v138, v139\n"
    "v_cvt_pk_bf16_f32 v138, v140, v141\n"
    "v_cvt_pk_bf16_f32 v139, v142, v143\n"
    "ds_read_b128 a[236:239], v249 offset:1024\n"
    "ds_write_b128 v243, v[120:123] offset:27712\n"
    "ds_read_b128 a[252:255], v249 offset:3072\n"
    "ds_write_b128 v243, v[136:139] offset:29760\n"
    "s_waitcnt lgkmcnt(4)\n"
    "buffer_store_dwordx4 a[228:231], v255, s[44:47], s58 offen offset:64 sc1\n"
    "ds_read_b128 a[240:243], v249 offset:5120\n"
    "buffer_store_dwordx4 a[232:235], v255, s[44:47], s58 offen offset:128 sc1\n"
    "s_waitcnt lgkmcnt(3)\n"
    "buffer_store_dwordx4 a[236:239], v255, s[44:47], s89 offen offset:0 sc1\n"
    "s_waitcnt lgkmcnt(2)\n"
    "buffer_store_dwordx4 a[252:255], v255, s[44:47], s89 offen offset:64 sc1\n"
    "s_waitcnt lgkmcnt(0)\n"
    "buffer_store_dwordx4 a[240:243], v255, s[44:47], s89 offen offset:128 sc1\n"
    "97:\n"
    "98:\n"
    "s_waitcnt vmcnt(0) lgkmcnt(0)\n"
    ".Lreplk_end:\n"
    : "+{s66}"(flg), "+{v208}"(tid), "+{s65}"(blk), "+{s[98:99]}"(ka)
    : "{s[96:97]}"(CTAB)
    : "memory", "scc", "vcc", "m0", "v0", "v1", "v2", "v3", "v4", "v5", "v6", "v7", "v8", "v9", "v10", "v11", "v12", "v13", "v14", "v15", "v16", "v17", "v18", "v19", "v20", "v21", "v22", "v23", "v24", "v25", "v26", "v27", "v28", "v29", "v30", "v31", "v32", "v33", "v34", "v35", "v36", "v37", "v38", "v39", "v40", "v41", "v42", "v43", "v44", "v45", "v46", "v47", "v48", "v49", "v50", "v51", "v52", "v53", "v54", "v55", "v56", "v57", "v58", "v59", "v60", "v61", "v62", "v63", "v64", "v65", "v66", "v67", "v68", "v69", "v70", "v71", "v72", "v73", "v74", "v75", "v76", "v77", "v78", "v79", "v80", "v81", "v82", "v83", "v84", "v85", "v86", "v87", "v88", "v89", "v90", "v91", "v92", "v93", "v94", "v95", "v96", "v97", "v98", "v99", "v100", "v101", "v102", "v103", "v104", "v105", "v106", "v107", "v108", "v109", "v110", "v111", "v112", "v113", "v114", "v115", "v116", "v117", "v118", "v119", "v120", "v121", "v122", "v123", "v124", "v125", "v126", "v127", "v128", "v129", "v130", "v131", "v132", "v133", "v134", "v135", "v136", "v137", "v138", "v139", "v140", "v141", "v142", "v143", "v144", "v145", "v146", "v147", "v148", "v149", "v150", "v151", "v152", "v153", "v154", "v155", "v156", "v157", "v158", "v159", "v160", "v161", "v162", "v163", "v164", "v165", "v166", "v167", "v168", "v169", "v170", "v171", "v172", "v173", "v174", "v175", "v176", "v177", "v178", "v179", "v180", "v181", "v182", "v183", "v184", "v185", "v186", "v187", "v188", "v189", "v190", "v191", "v192", "v193", "v194", "v195", "v196", "v197", "v198", "v199", "v200", "v201", "v202", "v203", "v204", "v205", "v206", "v207", "v209", "v210", "v211", "v212", "v213", "v214", "v215", "v216", "v217", "v218", "v219", "v220", "v221", "v222", "v223", "v224", "v225", "v226", "v227", "v228", "v229", "v230", "v231", "v232", "v233", "v234", "v235", "v236", "v237", "v238", "v239", "v240", "v241", "v242", "v243", "v244", "v245", "v246", "v247", "v248", "v249", "v250", "v251", "v252", "v253", "v254", "v255", "a0", "a1", "a2", "a3", "a4", "a5", "a6", "a7", "a8", "a9", "a10", "a11", "a12", "a13", "a14", "a15", "a16", "a17", "a18", "a19", "a20", "a21", "a22", "a23", "a24", "a25", "a26", "a27", "a28", "a29", "a30", "a31", "a32", "a33", "a34", "a35", "a36", "a37", "a38", "a39", "a40", "a41", "a42", "a43", "a44", "a45", "a46", "a47", "a48", "a49", "a50", "a51", "a52", "a53", "a54", "a55", "a56", "a57", "a58", "a59", "a60", "a61", "a62", "a63", "a64", "a65", "a66", "a67", "a68", "a69", "a70", "a71", "a72", "a73", "a74", "a75", "a76", "a77", "a78", "a79", "a80", "a81", "a82", "a83", "a84", "a85", "a86", "a87", "a88", "a89", "a90", "a91", "a92", "a93", "a94", "a95", "a96", "a97", "a98", "a99", "a100", "a101", "a102", "a103", "a104", "a105", "a106", "a107", "a108", "a109", "a110", "a111", "a112", "a113", "a114", "a115", "a116", "a117", "a118", "a119", "a120", "a121", "a122", "a123", "a124", "a125", "a126", "a127", "a128", "a129", "a130", "a131", "a132", "a133", "a134", "a135", "a136", "a137", "a138", "a139", "a140", "a141", "a142", "a143", "a144", "a145", "a146", "a147", "a148", "a149", "a150", "a151", "a152", "a153", "a154", "a155", "a156", "a157", "a158", "a159", "a160", "a161", "a162", "a163", "a164", "a165", "a166", "a167", "a168", "a169", "a170", "a171", "a172", "a173", "a174", "a175", "a176", "a177", "a178", "a179", "a180", "a181", "a182", "a183", "a184", "a185", "a186", "a187", "a188", "a189", "a190", "a191", "a192", "a193", "a194", "a195", "a196", "a197", "a198", "a199", "a200", "a201", "a202", "a203", "a204", "a205", "a206", "a207", "a208", "a209", "a210", "a211", "a212", "a213", "a214", "a215", "a216", "a217", "a218", "a219", "a220", "a221", "a222", "a223", "a224", "a225", "a226", "a227", "a228", "a229", "a230", "a231", "a232", "a233", "a234", "a235", "a236", "a237", "a238", "a239", "a240", "a241", "a242", "a243", "a244", "a245", "a246", "a247", "a248", "a249", "a250", "a251", "a252", "a253", "a254", "a255", "s36", "s37", "s38", "s39", "s40", "s41", "s42", "s43", "s44", "s45", "s46", "s47", "s48", "s49", "s50", "s51", "s52", "s53", "s54", "s55", "s56", "s57", "s58", "s59", "s60", "s61", "s62", "s63", "s64", "s67", "s68", "s69", "s70", "s71", "s72", "s73", "s74", "s75", "s76", "s77", "s78", "s79", "s80", "s81", "s82", "s83", "s84", "s85", "s86", "s87", "s88", "s89", "s90", "s91", "s92", "s93", "s94", "s95");
  if (flg != 2u) {
    const int l2 = __builtin_amdgcn_mbcnt_hi(~0u, __builtin_amdgcn_mbcnt_lo(~0u, 0u));
    slow_path(x, dwl, dws, w1, w2, out, (float*)smem, w * 64 + l2, blockIdx.x >> 3, (blockIdx.x & 7) * 4);
  }
}

extern "C" void kernel_launch(void* const* d_in, const int* in_sizes, int n_in, void* d_out, int out_size, void* d_ws,
                              size_t ws_size, hipStream_t stream) {
  replk_kernel<<<256, 256, 0, stream>>>((const u16*)d_in[0], (const u16*)d_in[1], (const u16*)d_in[2], (const u16*)d_in[3],
                                        (const u16*)d_in[4], (u16*)d_out
                                        );
}
```

```cpp
#include <hip/hip_runtime.h>
#include <hip/hip_bf16.h>

typedef unsigned u32;
typedef unsigned short u16;
typedef unsigned long long u64;
typedef u32 u32x4 __attribute__((ext_vector_type(4)));
typedef u32 u32x8 __attribute__((ext_vector_type(8)));
typedef u32 u32x16 __attribute__((ext_vector_type(16)));
typedef _Float16 f16x2 __attribute__((ext_vector_type(2)));
typedef __bf16 bf16x2 __attribute__((ext_vector_type(2)));
typedef float f32x2 __attribute__((ext_vector_type(2)));
#define DI __device__ __forceinline__
#define LDSP(T, off) ((__attribute__((address_space(3))) T*)(smem3 + (off)))

constexpr int IMG = 512;
constexpr int CH = 96;
constexpr int HID = 384;
constexpr int PXB = 192;
constexpr int ROWB = 98304;
constexpr int TOEP_CS = 336;
constexpr int XT_RS = 320;
constexpr int XT_PP = 1360;
constexpr int XT_CG = 6752;
constexpr int GS = 6160;
constexpr int L_LUT = 0;
constexpr int L_TOEP = 8208;
constexpr int L_W = 40960;
constexpr int L_LUTN = 65536;
constexpr int L_XT = 81920;
constexpr int L_H = 108928;
constexpr int L_EX = 133568;
constexpr int XT_Q = 27024;
constexpr int L_MISC = 162992;
constexpr int L_TOTAL = 163056;
constexpr int LUTK = 106;
constexpr u32 XBYTES = IMG * IMG * PXB;
constexpr float GAMMA = -0.4684938781597355f;
constexpr float GC1 = -2.302208198144325f, GC3 = -0.1029432395800235f;
constexpr u32 MU_PK = 0x44ea44eau, CC_PK = 0xb77fb77fu;
__device__ const u32 CTAB[3 * 1024] = {
671109120,671098880,134490112,673187840,671372952,671229956,145500164,673318996,671636784,671361032,156510216,673450152,671900616,671492108,167520268,673581308,672164452,684206128,178530352,686295376,672428284,684337204,189540404,686426532,672692116,684468280,200550456,686557688,672955948,684599356,211560508,686688844,673219784,697313376,222570592,699402912,673483616,697444452,233580644,699534068,673747448,697575528,244590696,699665224,674011280,697706604,255600748,699796380,674254849,710420624,266610832,712510448,674518681,710551700,277620884,712641604,674782513,710682776,288630936,712772760,675046345,710813852,299640988,712903916,
684223077,723527872,310651072,721423680,684486909,723658948,321661124,721554836,683702165,723790024,332671176,721685992,683965997,723921100,343681228,721817148,685278409,736635120,354691312,734531216,685542241,736766196,365701364,734662372,684757497,736897272,376711416,734793528,685021329,737028348,387721468,734924684,686313474,749742368,398731552,747638752,686577306,749873444,409741604,747769908,685792562,750004520,420751656,747901064,686056394,750135596,431761708,748032220,687368806,762849616,442771792,760746288,687632638,762980692,453781844,760877444,686847894,763111768,464791896,761008600,687111726,763242844,475801948,761139756,
697337034,771762560,486812032,773853824,697600866,771893636,497822084,773984980,697864698,772024712,508832136,774116136,698128530,772155788,519842188,774247292,696274947,784869808,530852272,786961360,696538779,785000884,541862324,787092516,696802611,785131960,552872376,787223672,697066443,785263036,563882428,787354828,699427431,797977056,574892512,800068896,699691263,798108132,585902564,800200052,699955095,798239208,596912616,800331208,700218927,798370284,607922668,800462364,698385611,811084304,618932752,813176432,698649443,811215380,629942804,813307588,698913275,811346456,640952856,813438744,699177107,811477532,651962908,813569900,
710430724,824191552,135539264,822089664,710694556,824322628,146549316,822220820,709909812,824453704,157559368,822351976,710173644,824584780,168569420,822483132,709388904,837298800,179579504,835197200,709652736,837429876,190589556,835328356,708867992,837560952,201599608,835459512,709131824,837692028,212609660,835590668,712541388,850406048,223619744,848304736,712805220,850537124,234629796,848435892,712020476,850668200,245639848,848567048,712284308,850799276,256649900,848698204,711479301,863513296,267659984,861412272,711743133,863644372,278670036,861543428,710958389,863775448,289680088,861674584,711222221,863906524,300690140,861805740,
723544681,677394944,311704064,679484672,723808513,677526020,322714116,679615828,724072345,677657096,333724168,679746984,724336177,677788172,344734220,679878140,724600013,690502192,355744304,692592208,724863845,690633268,366754356,692723364,725127677,690764344,377764408,692854520,725391509,690895420,388774460,692985676,721440774,703609440,399784544,705699744,721704606,703740516,410794596,705830900,721968438,703871592,421804648,705962056,722232270,704002668,432814700,706093212,722496106,716716688,443824784,718807280,722759938,716847764,454834836,718938436,723023770,716978840,465844888,719069592,723287602,717109916,476854940,719200748,
736658638,725629632,487865024,731914816,736922470,725760708,498875076,732045972,736137726,725891784,509885128,732177128,736401558,726022860,520895180,732308284,737693703,738736880,531905264,745022352,737957535,738867956,542915316,745153508,737172791,738999032,553925368,745284664,737436623,739130108,564935420,745415820,734554731,751844128,575945504,758129888,734818563,751975204,586955556,758261044,734033819,752106280,597965608,758392200,734297651,752237356,608975660,758523356,735610063,764951376,619985744,771237424,735873895,765082452,630995796,771368580,735089151,765213528,642005848,771499736,735352983,765344604,653015900,771630892,
749752328,778058624,136592256,780150656,750016160,778189700,147602308,780281812,750279992,778320776,158612360,780443436,750543824,778451852,169622412,780574508,748710508,791165872,180632496,793288492,748974340,791296948,191642548,793419564,749238172,791428024,202652600,793550636,749502004,791559100,213662652,793681708,747668688,804273120,224672736,806395692,747932520,804404196,235682788,806526764,748196352,804535272,246692840,806657836,748460184,804666348,257702892,806788908,746606601,817380368,268712976,819502892,746870433,817511444,279723028,819633964,747134265,817642520,290733080,819765036,747398097,817773596,301743132,819896108,
762866285,826293312,312753216,832610092,763130117,826424388,323763268,832741164,762345373,826555464,334773320,832872236,762609205,826686540,345783372,833003308,761824465,839400560,356793456,845717292,762088297,839531636,367803508,845848364,761303553,839662712,378813560,845979436,761567385,839793788,389823612,846110508,760762378,852507808,400833696,858824492,761026210,852638884,411843748,858955564,760241466,852769960,422853800,859086636,760505298,852901036,433863852,859217708,759720558,865615056,444873936,871931692,759984390,865746132,455883988,872062764,759199646,865877208,466894040,872193836,759463478,866008284,477904092,872324908,
771785938,671098882,488908800,673226540,772049770,671229958,499918852,673357612,772313602,671361034,510928904,673488684,772577434,671492110,521938956,673619756,772821003,684206130,532949040,686333740,773084835,684337206,543959092,686464812,773348667,684468282,554969144,686595884,773612499,684599358,565979196,686726956,773876335,697313378,576989280,699440940,774140167,697444454,587999332,699572012,774403999,697575530,599009384,699703084,774667831,697706606,610019436,699834156,774931667,710420626,621029520,712548140,775195499,710551702,632039572,712679212,775459331,710682778,643049624,712810284,775723163,710813854,654059676,712941356,
784879628,723527874,137636032,721461036,785143460,723658950,148646084,721592108,784358716,723790026,159656136,721723180,784622548,723921102,170666188,721854252,785934960,736635122,181676272,734568236,786198792,736766198,192686324,734699308,785414048,736897274,203696376,734830380,785677880,737028350,214706428,734961452,786990292,749742370,225716512,747675436,787254124,749873446,236726564,747806508,786469380,750004522,247736616,747937580,786733212,750135598,258746668,748068652,788025357,762849618,269756752,760782636,788289189,762980694,280766804,760913708,787504445,763111770,291776856,761044780,787768277,763242846,302786908,761175852,
797993585,771762562,313796992,773889836,798257417,771893638,324807044,774020908,798521249,772024714,335817096,774151980,798785081,772155790,346827148,774283052,796951765,784869810,357837232,786997036,797215597,785000886,368847284,787128108,797479429,785131962,379857336,787259180,797743261,785263038,390867388,787390252,800083982,797977058,401877472,800104236,800347814,798108134,412887524,800235308,800611646,798239210,423897576,800366380,800875478,798370286,434907628,800497452,799042162,811084306,445917712,813211436,799305994,811215382,456927764,813342508,799569826,811346458,467937816,813473580,799833658,811477534,478947868,813604652,
811107542,824191554,489957952,822124332,811371374,824322630,500968004,822255404,810586630,824453706,511978056,822386476,810850462,824584782,522988108,822517548,810045455,837298802,533998192,835231532,810309287,837429878,545008244,835362604,809524543,837560954,556018296,835493676,809788375,837692030,567028348,835624748,813197939,850406050,578038432,848338732,813461771,850537126,589048484,848469804,812677027,850668202,600058536,848600876,812940859,850799278,611068588,848731948,812156119,863513298,622078672,861445932,812419951,863644374,633088724,861577004,811635207,863775450,644098776,861708076,811899039,863906526,655108828,861839148,
824201232,677394946,138689024,679517996,824465064,677526022,149699076,679649068,824728896,677657098,160709128,679780140,824992728,677788174,171719180,679911212,825256564,690502194,182729264,692625196,825520396,690633270,193739316,692756268,825784228,690764346,204749368,692887340,826048060,690895422,215759420,693018412,822117592,703609442,226769504,705732396,822381424,703740518,237779556,705863468,822645256,703871594,248789608,705994540,822909088,704002670,259799660,706125612,823152657,716716690,270809744,718839596,823416489,716847766,281819796,718970668,823680321,716978842,292829848,719101740,823944153,717109918,303839900,719232812,
837315189,725629634,314849984,731946796,837579021,725760710,325860036,732077868,836794277,725891786,336870088,732208940,837058109,726022862,347880140,732340012,838370521,738736882,358890224,745053996,838634353,738867958,369900276,745185068,837849609,738999034,380910328,745316140,838113441,739130110,391920380,745447212,835211282,751844130,402930464,758161196,835475114,751975206,413940516,758292268,834690370,752106282,424950568,758423340,834954202,752237358,435960620,758554412,836266614,764951378,446970704,771268396,836530446,765082454,457980756,771399468,835745702,765213530,468990808,771530540,836009534,765344606,480000860,771661612,
850429146,778058626,491010944,780181292,850692978,778189702,502020996,780312364,850956810,778320778,513031048,780443436,851220642,778451854,524041100,780574508,849367059,791165874,535051184,793288492,849630891,791296950,546061236,793419564,849894723,791428026,557071288,793550636,850158555,791559102,568081340,793681708,848325239,804273122,579091424,806395692,848589071,804404198,590101476,806526764,848852903,804535274,601111528,806657836,849116735,804666350,612121580,806788908,847283419,817380370,623131664,819502892,847547251,817511446,634141716,819633964,847811083,817642522,645151768,819765036,848074915,817773598,656161820,819896108,
863541411,826293314,656161856,832610092,863803555,826424390,656161860,832741164,863017123,826555466,656161864,832872236,863279267,826686542,656161868,833003308,862492835,839400562,656161904,845717292,862754979,839531638,656161908,845848364,861968547,839662714,656161912,845979436,862230691,839793790,656161916,846110508,861444259,852507810,656161952,858824492,861706403,852638886,656161956,858955564,860919971,852769962,656161960,859086636,861182115,852901038,656161964,859217708,860395683,865615058,656162000,871931692,860657827,865746134,656162004,872062764,859871395,865877210,656162008,872193836,860133539,866008286,656162012,872324908,
2152998912u,1885629024,1342187520,1342212212,2152998916u,1885891172,1452812292,1364494564,2151688200u,1886153320,1563437064,1386776836,2218797068u,1886415468,1674061836,1409059156,2164598800u,1886677616,1342253072,1342474360,2164598804u,1886939764,1452877844,1364756712,2164271128u,1887201912,1563502616,1387038984,2231379996u,1887464060,1674127388,1409321304,1083254816,1887726208,1342318624,1342998652,1083254820,1887988356,1452943396,1365281004,1082140712,1888250504,1563568168,1387563276,1149249580,1888512652,1674192940,1409845596,1108617264,1888774800,1342384176,1342736512,1108617268,1889036948,1453008948,1365018864,1107306552,1889299096,1563633720,1387301136,1174415420,1889561244,1674258492,1409583456,
269756480,1892969168,1342449728,1342474372,269756484,1893231316,1453074500,1364756692,268445768,1893493464,1563699272,1387038964,335554636,1893755612,1674324044,1409321316,268773456,1891920576,1342515280,1342736520,268773460,1892182724,1453140052,1365018840,268445784,1892444872,1563764824,1387301112,335554652,1892707020,1674389596,1409583464,1343301728,1890871984,1342580832,1343260812,1343301732,1891134132,1453205604,1365543132,1342187624,1891396280,1563830376,1387825404,1409296492,1891658428,1674455148,1410107756,1343498352,1889823392,1342646384,1342998672,1343498356,1890085540,1453271156,1365280992,1342187640,1890347688,1563895928,1387563264,1409296508,1890609836,1674520700,1409845616,
538192000,1895066352,1342711936,1364494484,538192004,1895328500,1453336708,1386776772,536881288,1895590648,1563961480,1409059108,603990156,1895852796,1674586252,1431341364,537208976,1894017760,1342777488,1364756632,537208980,1894279908,1453402260,1387038920,536881304,1894542056,1564027032,1409321256,603990172,1894804204,1674651804,1431603512,1611737248,1897163536,1342843040,1365280924,1611737252,1897425684,1453467812,1387563212,1610623144,1897687832,1564092584,1409845548,1677732012,1897949980,1674717356,1432127804,1611933872,1896114944,1342908592,1365018784,1611933876,1896377092,1453533364,1387301072,1610623160,1896639240,1564158136,1409583408,1677732028,1896901388,1674782908,1431865664,
810821824,1900309312,1342974144,1364756644,810821828,1900571460,1453598916,1387038900,809511112,1900833608,1564223688,1409321236,876619980,1901095756,1674848460,1431603524,822421712,1901357904,1343039696,1365018792,822421716,1901620052,1453664468,1387301048,822094040,1901882200,1564289240,1409583384,889202908,1902144348,1674914012,1431865672,1888561376,1898212128,1343105248,1365543084,1888561380,1898474276,1453730020,1387825340,1887447272,1898736424,1564354792,1410107676,1954556140,1898998572,1674979564,1432389964,1913923824,1899260720,1343170800,1365280944,1913923828,1899522868,1453795572,1387563200,1912613112,1899785016,1564420344,1409845536,1979721980,1900047164,1675045116,1432127824,
2152999168u,1784705124,1347430656,1452835440,2152999172u,1784967272,1458055428,1475117792,2151688456u,1785229420,1568680200,1497400064,2218797324u,1785491568,1679304972,1519682384,2164599056u,1785753716,1347496208,1453097588,2164599060u,1786015864,1458120980,1475379940,2164271384u,1786278012,1568745752,1497662212,2231380252u,1786540160,1679370524,1519944532,1083255072,1786802308,1347561760,1453621880,1083255076,1787064456,1458186532,1475904232,1082140968,1787326604,1568811304,1498186504,1149249836,1787588752,1679436076,1520468824,1108617520,1787850900,1347627312,1453359740,1108617524,1788113048,1458252084,1475642092,1107306808,1788375196,1568876856,1497924364,1174415676,1788637344,1679501628,1520206684,
269756736,1792045268,1347692864,1453097600,269756740,1792307416,1458317636,1475379920,268446024,1792569564,1568942408,1497662192,335554892,1792831712,1679567180,1519944544,268773712,1790996676,1347758416,1453359748,268773716,1791258824,1458383188,1475642068,268446040,1791520972,1569007960,1497924340,335554908,1791783120,1679632732,1520206692,1343301984,1789948084,1347823968,1453884040,1343301988,1790210232,1458448740,1476166360,1342187880,1790472380,1569073512,1498448632,1409296748,1790734528,1679698284,1520730984,1343498608,1788899492,1347889520,1453621900,1343498612,1789161640,1458514292,1475904220,1342187896,1789423788,1569139064,1498186492,1409296764,1789685936,1679763836,1520468844,
538192256,1794142452,1347955072,1475117712,538192260,1794404600,1458579844,1497400000,536881544,1794666748,1569204616,1519682336,603990412,1794928896,1679829388,1541964592,537209232,1793093860,1348020624,1475379860,537209236,1793356008,1458645396,1497662148,536881560,1793618156,1569270168,1519944484,603990428,1793880304,1679894940,1542226740,1611737504,1796239636,1348086176,1475904152,1611737508,1796501784,1458710948,1498186440,1610623400,1796763932,1569335720,1520468776,1677732268,1797026080,1679960492,1542751032,1611934128,1795191044,1348151728,1475642012,1611934132,1795453192,1458776500,1497924300,1610623416,1795715340,1569401272,1520206636,1677732284,1795977488,1680026044,1542488892,
810822080,1799385412,1348217280,1475379872,810822084,1799647560,1458842052,1497662128,809511368,1799909708,1569466824,1519944464,876620236,1800171856,1680091596,1542226752,822421968,1800434004,1348282832,1475642020,822421972,1800696152,1458907604,1497924276,822094296,1800958300,1569532376,1520206612,889203164,1801220448,1680157148,1542488900,1888561632,1797288228,1348348384,1476166312,1888561636,1797550376,1458973156,1498448568,1887447528,1797812524,1569597928,1520730904,1954556396,1798074672,1680222700,1543013192,1913924080,1798336820,1348413936,1475904172,1913924084,1798598968,1459038708,1498186428,1912613368,1798861116,1569663480,1520468764,1979722236,1799123264,1680288252,1542751052,
2152999424u,2087482984,1352673280,1563464828,2152999428u,2087745132,1463298052,1585747180,2151688712u,2088007280,1573922824,1608029452,2218797580u,2088269428,1684547596,1630311772,2164599312u,2088531576,1352738832,1563726976,2164599316u,2088793724,1463363604,1586009328,2164271640u,2089055872,1573988376,1608291600,2231380508u,2089318020,1684613148,1630573920,1083255328,2089580168,1352804384,1564251268,1083255332,2089842316,1463429156,1586533620,1082141224,2090104464,1574053928,1608815892,1149250092,2090366612,1684678700,1631098212,1108617776,2090628760,1352869936,1563989128,1108617780,2090890908,1463494708,1586271480,1107307064,2091153056,1574119480,1608553752,1174415932,2091415204,1684744252,1630836072,
269756992,2094823128,1352935488,1563726988,269756996,2095085276,1463560260,1586009308,268446280,2095347424,1574185032,1608291580,335555148,2095609572,1684809804,1630573932,268773968,2093774536,1353001040,1563989136,268773972,2094036684,1463625812,1586271456,268446296,2094298832,1574250584,1608553728,335555164,2094560980,1684875356,1630836080,1343302240,2092725944,1353066592,1564513428,1343302244,2092988092,1463691364,1586795748,1342188136,2093250240,1574316136,1609078020,1409297004,2093512388,1684940908,1631360372,1343498864,2091677352,1353132144,1564251288,1343498868,2091939500,1463756916,1586533608,1342188152,2092201648,1574381688,1608815880,1409297020,2092463796,1685006460,1631098232,
538192512,2096920312,1353197696,1585747100,538192516,2097182460,1463822468,1608029388,536881800,2097444608,1574447240,1630311724,603990668,2097706756,1685072012,1652593980,537209488,2095871720,1353263248,1586009248,537209492,2096133868,1463888020,1608291536,536881816,2096396016,1574512792,1630573872,603990684,2096658164,1685137564,1652856128,1611737760,2099017496,1353328800,1586533540,1611737764,2099279644,1463953572,1608815828,1610623656,2099541792,1574578344,1631098164,1677732524,2099803940,1685203116,1653380420,1611934384,2097968904,1353394352,1586271400,1611934388,2098231052,1464019124,1608553688,1610623672,2098493200,1574643896,1630836024,1677732540,2098755348,1685268668,1653118280,
810822336,2102163272,1353459904,1586009260,810822340,2102425420,1464084676,1608291516,809511624,2102687568,1574709448,1630573852,876620492,2102949716,1685334220,1652856140,822422224,2103211864,1353525456,1586271408,822422228,2103474012,1464150228,1608553664,822094552,2103736160,1574775000,1630836000,889203420,2103998308,1685399772,1653118288,1888561888,2100066088,1353591008,1586795700,1888561892,2100328236,1464215780,1609077956,1887447784,2100590384,1574840552,1631360292,1954556652,2100852532,1685465324,1653642580,1913924336,2101114680,1353656560,1586533560,1913924340,2101376828,1464281332,1608815816,1912613624,2101638976,1574906104,1631098152,1979722492,2101901124,1685530876,1653380440,
2152999680u,1986559084,1357916416,1674088056,2152999684u,1986821232,1468541188,1696370408,2151688968u,1987083380,1579165960,1718652680,2218797836u,1987345528,1689790732,1740935000,2164599568u,1987607676,1357981968,1674350204,2164599572u,1987869824,1468606740,1696632556,2164271896u,1988131972,1579231512,1718914828,2231380764u,1988394120,1689856284,1741197148,1083255584,1988656268,1358047520,1674874496,1083255588,1988918416,1468672292,1697156848,1082141480,1989180564,1579297064,1719439120,1149250348,1989442712,1689921836,1741721440,1108618032,1989704860,1358113072,1674612356,1108618036,1989967008,1468737844,1696894708,1107307320,1990229156,1579362616,1719176980,1174416188,1990491304,1689987388,1741459300,
269757248,1993899228,1358178624,1674350216,269757252,1994161376,1468803396,1696632536,268446536,1994423524,1579428168,1718914808,335555404,1994685672,1690052940,1741197160,268774224,1992850636,1358244176,1674612364,268774228,1993112784,1468868948,1696894684,268446552,1993374932,1579493720,1719176956,335555420,1993637080,1690118492,1741459308,1343302496,1991802044,1358309728,1675136656,1343302500,1992064192,1468934500,1697418976,1342188392,1992326340,1579559272,1719701248,1409297260,1992588488,1690184044,1741983600,1343499120,1990753452,1358375280,1674874516,1343499124,1991015600,1469000052,1697156836,1342188408,1991277748,1579624824,1719439108,1409297276,1991539896,1690249596,1741721460,
538192768,1995996412,1358440832,1696370328,538192772,1996258560,1469065604,1718652616,536882056,1996520708,1579690376,1740934952,603990924,1996782856,1690315148,1763217208,537209744,1994947820,1358506384,1696632476,537209748,1995209968,1469131156,1718914764,536882072,1995472116,1579755928,1741197100,603990940,1995734264,1690380700,1763479356,1611738016,1998093596,1358571936,1697156768,1611738020,1998355744,1469196708,1719439056,1610623912,1998617892,1579821480,1741721392,1677732780,1998880040,1690446252,1764003648,1611934640,1997045004,1358637488,1696894628,1611934644,1997307152,1469262260,1719176916,1610623928,1997569300,1579887032,1741459252,1677732796,1997831448,1690511804,1763741508,
810822592,2001239372,1358703040,1696632488,810822596,2001501520,1469327812,1718914744,809511880,2001763668,1579952584,1741197080,876620748,2002025816,1690577356,1763479368,822422480,2002287964,1358768592,1696894636,822422484,2002550112,1469393364,1719176892,822094808,2002812260,1580018136,1741459228,889203676,2003074408,1690642908,1763741516,1888562144,1999142188,1358834144,1697418928,1888562148,1999404336,1469458916,1719701184,1887448040,1999666484,1580083688,1741983520,1954556908,1999928632,1690708460,1764265808,1913924592,2000190780,1358899696,1697156788,1913924596,2000452928,1469524468,1719439044,1912613880,2000715076,1580149240,1741721380,1979722748,2000977224,1690774012,1764003668,
136579076,1343253088,52428,12582912,135530496,1453879396,843229,12648449,134481920,1564505704,1634030,12713986,145229824,1675132012,2411724,12779523,136841224,1343318640,3198156,13369356,135792808,1453944948,3988957,13434893,134744232,1564571256,4779758,13500430,139724968,1675197564,5557452,13565967,137103372,1343384208,6343884,14155800,136054952,1454010516,7134685,14221337,135006376,1564636824,7925486,14286874,139724968,1675263132,8703180,14352411,137365520,1343449728,9489612,14942244,136316928,1454076036,10280413,15007781,135268352,1564702344,11071214,15073318,145229824,1675328652,11848908,15138855,
139724868,1365535332,117964,12582960,139724852,1476161640,908765,12648497,138676260,1586787948,1699566,12714034,137627668,1697414256,2477260,12779571,145229896,1365600884,3263692,13369404,145229880,1476227192,4054493,13434941,138938408,1586853500,4845294,13500478,137889816,1697479808,5622988,13566015,145229900,1365666452,6409420,14155848,145229884,1476292760,7200221,14221385,139200556,1586919068,7991022,14286922,138151964,1697545376,8768716,14352459,139724880,1365731972,9555148,14942292,139724864,1476358280,10345949,15007829,139462704,1586984588,11136750,15073366,138414112,1697610896,11914444,15138903,
137627668,1387817576,183500,12583008,136579076,1498443884,974301,12648545,135530496,1609070192,1765102,12714082,134481920,1719696500,2542796,12779619,137889816,1387883128,3329228,13369452,136841224,1498509436,4120029,13434989,135792808,1609135744,4910830,13500526,134744232,1719762052,5688524,13566063,138151964,1387948696,6474956,14155896,137103372,1498575004,7265757,14221433,136054952,1609201312,8056558,14286970,135006376,1719827620,8834252,14352507,138414112,1388014216,9620684,14942340,137365520,1498640524,10411485,15007877,136316928,1609266832,11202286,15073414,135268352,1719893140,11979980,15138951,
139724968,1410099820,249036,12583056,139724868,1520726128,1039837,12648593,139724852,1631352436,1830638,12714130,138676260,1741978744,2608332,12779667,145229824,1410165372,3394764,13369500,145229896,1520791680,4185565,13435037,145229880,1631417988,4976366,13500574,138938408,1742044296,5754060,13566111,145229824,1410230940,6540492,14155944,145229900,1520857248,7331293,14221481,145229884,1631483556,8122094,14287018,139200556,1742109864,8899788,14352555,139724968,1410296460,9686220,14942388,139724880,1520922768,10477021,15007925,139724864,1631549076,11267822,15073462,139462704,1742175384,12045516,15138999,
180619940,1348496080,52428,12582912,179571360,1459122388,843229,12648449,178522784,1569748696,1634030,12713986,189270688,1680375004,2411724,12779523,180882088,1348561600,3198156,13369356,179833672,1459187908,3988957,13434893,178785096,1569814216,4779758,13500430,183765832,1680440524,5557452,13565967,181144236,1348627104,6343884,14155800,180095816,1459253412,7134685,14221337,179047240,1569879720,7925486,14286874,183765832,1680506028,8703180,14352411,181406384,1348692656,9489612,14942244,180357792,1459318964,10280413,15007781,179309216,1569945272,11071214,15073318,189270688,1680571580,11848908,15138855,
183765732,1370778324,117964,12582960,183765716,1481404632,908765,12648497,182717124,1592030940,1699566,12714034,181668532,1702657248,2477260,12779571,189270760,1370843844,3263692,13369404,189270744,1481470152,4054493,13434941,182979272,1592096460,4845294,13500478,181930680,1702722768,5622988,13566015,189270764,1370909348,6409420,14155848,189270748,1481535656,7200221,14221385,183241420,1592161964,7991022,14286922,182192828,1702788272,8768716,14352459,183765744,1370974900,9555148,14942292,183765728,1481601208,10345949,15007829,183503568,1592227516,11136750,15073366,182454976,1702853824,11914444,15138903,
181668532,1393060568,183500,12583008,180619940,1503686876,974301,12648545,179571360,1614313184,1765102,12714082,178522784,1724939492,2542796,12779619,181930680,1393126088,3329228,13369452,180882088,1503752396,4120029,13434989,179833672,1614378704,4910830,13500526,178785096,1725005012,5688524,13566063,182192828,1393191592,6474956,14155896,181144236,1503817900,7265757,14221433,180095816,1614444208,8056558,14286970,179047240,1725070516,8834252,14352507,182454976,1393257144,9620684,14942340,181406384,1503883452,10411485,15007877,180357792,1614509760,11202286,15073414,179309216,1725136068,11979980,15138951,
183765832,1415342812,249036,12583056,183765732,1525969120,1039837,12648593,183765716,1636595428,1830638,12714130,182717124,1747221736,2608332,12779667,189270688,1415408332,3394764,13369500,189270760,1526034640,4185565,13435037,189270744,1636660948,4976366,13500574,182979272,1747287256,5754060,13566111,189270688,1415473836,6540492,14155944,189270764,1526100144,7331293,14221481,189270748,1636726452,8122094,14287018,183241420,1747352760,8899788,14352555,183765832,1415539388,9686220,14942388,183765744,1526165696,10477021,15007925,183765728,1636792004,11267822,15073462,183503568,1747418312,12045516,15138999,
224660804,1353738992,52428,12582912,223612224,1464365300,843229,12648449,222563648,1574991608,1634030,12713986,233311552,1685617916,2411724,12779523,224922952,1353804512,3198156,13369356,223874536,1464430820,3988957,13434893,222825960,1575057128,4779758,13500430,227806696,1685683436,5557452,13565967,225185100,1353870080,6343884,14155800,224136680,1464496388,7134685,14221337,223088104,1575122696,7925486,14286874,227806696,1685749004,8703180,14352411,225447248,1353935632,9489612,14942244,224398656,1464561940,10280413,15007781,223350080,1575188248,11071214,15073318,233311552,1685814556,11848908,15138855,
227806596,1376021236,117964,12582960,227806580,1486647544,908765,12648497,226757988,1597273852,1699566,12714034,225709396,1707900160,2477260,12779571,233311624,1376086756,3263692,13369404,233311608,1486713064,4054493,13434941,227020136,1597339372,4845294,13500478,225971544,1707965680,5622988,13566015,233311628,1376152324,6409420,14155848,233311612,1486778632,7200221,14221385,227282284,1597404940,7991022,14286922,226233692,1708031248,8768716,14352459,227806608,1376217876,9555148,14942292,227806592,1486844184,10345949,15007829,227544432,1597470492,11136750,15073366,226495840,1708096800,11914444,15138903,
225709396,1398303480,183500,12583008,224660804,1508929788,974301,12648545,223612224,1619556096,1765102,12714082,222563648,1730182404,2542796,12779619,225971544,1398369000,3329228,13369452,224922952,1508995308,4120029,13434989,223874536,1619621616,4910830,13500526,222825960,1730247924,5688524,13566063,226233692,1398434568,6474956,14155896,225185100,1509060876,7265757,14221433,224136680,1619687184,8056558,14286970,223088104,1730313492,8834252,14352507,226495840,1398500120,9620684,14942340,225447248,1509126428,10411485,15007877,224398656,1619752736,11202286,15073414,223350080,1730379044,11979980,15138951,
227806696,1420585724,249036,12583056,227806596,1531212032,1039837,12648593,227806580,1641838340,1830638,12714130,226757988,1752464648,2608332,12779667,233311552,1420651244,3394764,13369500,233311624,1531277552,4185565,13435037,233311608,1641903860,4976366,13500574,227020136,1752530168,5754060,13566111,233311552,1420716812,6540492,14155944,233311628,1531343120,7331293,14221481,233311612,1641969428,8122094,14287018,227282284,1752595736,8899788,14352555,227806696,1420782364,9686220,14942388,227806608,1531408672,10477021,15007925,227806592,1642034980,11267822,15073462,227544432,1752661288,12045516,15138999,
268701668,1358981952,52428,12582912,267653088,1469608260,843229,12648449,266604512,1580234568,1634030,12713986,277352416,1690860876,2411724,12779523,268963816,1359047504,3198156,13369356,267915400,1469673812,3988957,13434893,266866824,1580300120,4779758,13500430,271847560,1690926428,5557452,13565967,269225964,1359113008,6343884,14155800,268177544,1469739316,7134685,14221337,267128968,1580365624,7925486,14286874,271847560,1690991932,8703180,14352411,269488112,1359178528,9489612,14942244,268439520,1469804836,10280413,15007781,267390944,1580431144,11071214,15073318,277352416,1691057452,11848908,15138855,
271847460,1381264196,117964,12582960,271847444,1491890504,908765,12648497,270798852,1602516812,1699566,12714034,269750260,1713143120,2477260,12779571,277352488,1381329748,3263692,13369404,277352472,1491956056,4054493,13434941,271061000,1602582364,4845294,13500478,270012408,1713208672,5622988,13566015,277352492,1381395252,6409420,14155848,277352476,1492021560,7200221,14221385,271323148,1602647868,7991022,14286922,270274556,1713274176,8768716,14352459,271847472,1381460772,9555148,14942292,271847456,1492087080,10345949,15007829,271585296,1602713388,11136750,15073366,270536704,1713339696,11914444,15138903,
269750260,1403546440,183500,12583008,268701668,1514172748,974301,12648545,267653088,1624799056,1765102,12714082,266604512,1735425364,2542796,12779619,270012408,1403611992,3329228,13369452,268963816,1514238300,4120029,13434989,267915400,1624864608,4910830,13500526,266866824,1735490916,5688524,13566063,270274556,1403677496,6474956,14155896,269225964,1514303804,7265757,14221433,268177544,1624930112,8056558,14286970,267128968,1735556420,8834252,14352507,270536704,1403743016,9620684,14942340,269488112,1514369324,10411485,15007877,268439520,1624995632,11202286,15073414,267390944,1735621940,11979980,15138951,
271847560,1425828684,249036,12583056,271847460,1536454992,1039837,12648593,271847444,1647081300,1830638,12714130,270798852,1757707608,2608332,12779667,277352416,1425894236,3394764,13369500,277352488,1536520544,4185565,13435037,277352472,1647146852,4976366,13500574,271061000,1757773160,5754060,13566111,277352416,1425959740,6540492,14155944,277352492,1536586048,7331293,14221481,277352476,1647212356,8122094,14287018,271323148,1757838664,8899788,14352555,271847560,1426025260,9686220,14942388,271847472,1536651568,10477021,15007925,271847456,1647277876,11267822,15073462,271585296,1757904184,12045516,15138999};

DI float bf_lo(u32 v) { return __uint_as_float(v << 16); }
DI u32 pk_bf16(float a, float b) { f32x2 t = {a, b}; return __builtin_bit_cast(u32, __builtin_convertvector(t, bf16x2)); }

DI void slow_path(const u16* __restrict__ x, const u16* __restrict__ dwl, const u16* __restrict__ dws, const u16* __restrict__ w1,
                  const u16* __restrict__ w2, u16* __restrict__ out, float* sm, int t, int tx, int ty0) {
  float* o = sm;
  float* g = sm + 8 * CH;
  float* st = g + 8 * HID;
#pragma unroll 1
  for (int b = 0; b < 128; ++b) {
    const int y = ty0 * 16 + (b >> 1), x0 = tx * 16 + (b & 1) * 8;
    __syncthreads();
#pragma unroll 1
    for (int i = t; i < 8 * CH; i += 256) {
      const int p = i / CH, c = i - p * CH;
      float acc = 0.f;
#pragma unroll 1
      for (int a = 0; a < 5; ++a)
#pragma unroll 1
        for (int e = 0; e < 5; ++e) {
          const int yy = y + a - 2, xc = x0 + p + e - 2;
          if (yy < 0 || yy >= IMG || xc < 0 || xc >= IMG) continue;
          const float xv = bf_lo(x[(size_t)(yy * IMG + xc) * CH + c]);
          acc = __builtin_fmaf(bf_lo(dws[(a * 5 + e) * CH + c]), xv, acc);
          if (a >= 1 && a <= 3 && e >= 1 && e <= 3) acc = __builtin_fmaf(bf_lo(dwl[((a - 1) * 3 + (e - 1)) * CH + c]), xv, acc);
        }
      o[i] = acc;
    }
    __syncthreads();
    if (t < 8) {
      float m = 0.f, v = 0.f;
#pragma unroll 1
      for (int c = 0; c < CH; ++c) m += o[t * CH + c];
      m *= (1.0f / CH);
#pragma unroll 1
      for (int c = 0; c < CH; ++c) { const float d = o[t * CH + c] - m; v = __builtin_fmaf(d, d, v); }
      st[2 * t] = m; st[2 * t + 1] = __builtin_amdgcn_rsqf(v * (1.0f / CH) + 1e-5f);
    }
    __syncthreads();
#pragma unroll 1
    for (int i = t; i < 8 * HID; i += 256) {
      const int p = i / HID, e = i - p * HID;
      const float m = st[2 * p], r = st[2 * p + 1];
      float u = 0.f;
#pragma unroll 1
      for (int c = 0; c < CH; ++c) u = __builtin_fmaf((o[p * CH + c] - m) * r, bf_lo(w1[c * HID + e]), u);
      const float z = u * __builtin_fmaf(u * u, GC3, GC1);
      g[i] = u * __builtin_amdgcn_rcpf(1.0f + __builtin_amdgcn_exp2f(z));
    }
    __syncthreads();
#pragma unroll 1
    for (int i = t; i < 8 * CH; i += 256) {
      const int p = i / CH, c = i - p * CH;
      float acc = 0.f;
#pragma unroll 1
      for (int e = 0; e < HID; ++e) acc = __builtin_fmaf(g[p * HID + e], bf_lo(w2[e * CH + c]), acc);
      const size_t idx = (size_t)(y * IMG + x0 + p) * CH + c;
      out[idx] = (u16)pk_bf16(bf_lo(x[idx]) + acc, 0.f);
    }
  }
}

__global__ __launch_bounds__(256, 1) void replk_kernel(const u16* __restrict__ x, const u16* __restrict__ dwl,
    const u16* __restrict__ dws, const u16* __restrict__ w1, const u16* __restrict__ w2, u16* __restrict__ out
    ) {
  __shared__ __attribute__((aligned(16))) char smem[L_TOTAL];
  __attribute__((address_space(3))) char* smem3 = (__attribute__((address_space(3))) char*)smem;
  const int w = __builtin_amdgcn_readfirstlane(threadIdx.x >> 6);
  u32 tid = threadIdx.x, blk = blockIdx.x;
  u32 flg = (u32)(size_t)smem3 ? 1u : 0u;
  u64 ka = (u64)__builtin_amdgcn_kernarg_segment_ptr();
  asm volatile(
    ".p2align 3\n"
    "s_load_dwordx2 s[40:41], s[98:99], 0x0\n"
    "s_load_dwordx2 s[88:89], s[98:99], 0x10\n"
    "s_load_dwordx2 s[92:93], s[98:99], 0x8\n"
    "s_load_dwordx2 s[84:85], s[98:99], 0x20\n"
    "s_load_dwordx2 s[80:81], s[98:99], 0x18\n"
    "s_load_dwordx2 s[44:45], s[98:99], 0x28\n"
    "v_lshlrev_b32_e32 v213, 4, v208\n"
    "v_lshrrev_b32_e32 v0, 6, v208\n"
    "s_add_u32 vcc_lo, s96, 0\n"
    "s_addc_u32 vcc_hi, s97, 0\n"
    "global_load_dwordx4 v[168:171], v213, vcc\n"
    "s_add_u32 vcc_lo, s96, 4096\n"
    "s_nop 0\n"
    "s_addc_u32 vcc_hi, s97, 0\n"
    "global_load_dwordx4 v[172:175], v213, vcc\n"
    "s_add_u32 vcc_lo, s96, 8192\n"
    "s_addc_u32 vcc_hi, s97, 0\n"
    "global_load_dwordx4 v[176:179], v213, vcc\n"
    "v_readfirstlane_b32 s60, v0\n"
    "v_min_u32_e32 v1, 239, v208\n"
    "v_mul_u32_u24_e32 v2, 1366, v1\n"
    "s_lshr_b32 s61, s65, 3\n"
    "s_and_b32 s62, s65, 7\n"
    "v_lshrrev_b32_e32 v2, 16, v2\n"
    "v_lshlrev_b32_e32 v3, 2, v1\n"
    "s_lshl_b32 s62, s62, 6\n"
    "s_movk_i32 s63, 768\n"
    "v_mad_u32_u24 v211, v2, s63, v3\n"
    "v_mul_u32_u24_e32 v4, 384, v2\n"
    "v_add_u32_e32 v2, -1, v2\n"
    "s_movk_i32 s90, 4800\n"
    "s_mov_b32 s91, 0x20000\n"
    "v_add_u32_e32 v3, -576, v3\n"
    "v_cmp_gt_u32_e32 vcc, 3, v2\n"
    "v_add_u32_e32 v3, v3, v4\n"
    "v_bfrev_b32_e32 v4, 1\n"
    "s_movk_i32 s94, 1728\n"
    "s_mov_b32 s95, 0x20000\n"
    "v_cndmask_b32_e32 v212, v4, v3, vcc\n"
    "s_lshl_b32 s99, s62, 5\n"
    "s_add_u32 s99, s99, s61\n"
    "s_mulk_i32 s99, 3072\n"
    "s_lshr_b32 s51, s60, 1\n"
    "s_mul_i32 s63, s51, 18432\n"
    "s_lshl_b32 s52, s60, 13\n"
    "s_add_u32 s63, s63, s52\n"
    "v_add_u32_e32 v210, s63, v213\n"
    "s_mul_i32 s38, s60, 9216\n"
    "s_add_u32 s38, s38, 40960\n"
    "s_mov_b32 s86, 73728\n"
    "s_mov_b32 s87, 0x20000\n"
    "v_and_b32_e32 v5, 63, v208\n"
    "s_cmp_lt_u32 s60, 2\n"
    "s_cselect_b32 s63, 17, 15\n"
    "v_min_u32_e32 v5, s63, v5\n"
    "s_and_b32 s52, s61, 15\n"
    "s_lshl_b32 s52, s52, 1\n"
    "s_add_u32 s52, s52, s60\n"
    "s_sub_u32 s52, s52, 2\n"
    "s_lshl_b32 s52, s52, 4\n"
    "s_mul_i32 s53, s61, 18\n"
    "s_getpc_b64 s[76:77]\n"
    ".Lreplk_pc0:\n"
    "s_cmp_lt_u32 s60, 2\n"
    "s_cselect_b32 s52, s53, s52\n"
    "s_mov_b32 s53, .Lreplk_end-.Lreplk_pc0-4\n"
    "s_cselect_b32 s53, 0x20000, s53\n"
    "v_add_lshl_u32 v5, s52, v5, 7\n"
    "v_min_u32_e32 v214, s53, v5\n"
    "s_cmp_eq_u32 s61, 0\n"
    "s_cselect_b32 s52, 24, 0\n"
    "s_cmp_eq_u32 s61, 31\n"
    "s_cselect_b32 s53, 40, s52\n"
    "s_sub_u32 s53, 256, s53\n"
    "v_subrev_u32_e32 v6, s52, v208\n"
    "s_sub_u32 s63, s99, 384\n"
    "v_cmp_gt_u32_e32 vcc, s53, v6\n"
    "v_lshl_add_u32 v7, v1, 4, s63\n"
    "v_bfe_u32 v8, v208, 4, 2\n"
    "v_lshl_or_b32 v8, v0, 3, v8\n"
    "v_and_b32_e32 v9, 0xf0, v213\n"
    "v_cndmask_b32_e32 v208, v4, v7, vcc\n"
    "v_mul_u32_u24_e32 v8, 768, v8\n"
    "s_mov_b32 s42, 50331648\n"
    "s_mov_b32 s43, 0x20000\n"
    "v_or_b32_e32 v209, v8, v9\n"
    "s_mov_b32 s82, 73728\n"
    "s_mov_b32 s83, 0x20000\n"
    "s_cmp_eq_u32 s62, 0\n"
    "s_cselect_b32 s78, 0, s42\n"
    "s_mov_b32 s79, 0x20000\n"
    "s_waitcnt lgkmcnt(0)\n"
    "s_cmp_eq_u32 s60, 0\n"
    "s_cselect_b64 s[70:71], s[80:81], s[76:77]\n"
    "s_cmp_eq_u32 s60, 1\n"
    "s_cselect_b64 s[70:71], s[84:85], s[70:71]\n"
    "s_sub_u32 s76, s40, 196608\n"
    "s_subb_u32 s77, s41, 0\n"
    "buffer_load_dword v224, v211, s[88:91], 0 offen offset:0\n"
    "buffer_load_dword v225, v211, s[88:91], 0 offen offset:192\n"
    "buffer_load_dword v226, v211, s[88:91], 0 offen offset:384\n"
    "buffer_load_dword v227, v211, s[88:91], 0 offen offset:576\n"
    "buffer_load_dword v228, v211, s[88:91], 0 offen offset:768\n"
    "buffer_load_dword v229, v212, s[92:95], 0 offen offset:0\n"
    "buffer_load_dword v230, v212, s[92:95], 0 offen offset:192\n"
    "buffer_load_dword v231, v212, s[92:95], 0 offen offset:384\n"
    "global_load_dword v233, v214, s[70:71]\n"
    "v_mov_b32_e64 v88, 0\n"
    "v_mov_b32_e32 v240, 0xdddd8888\n"
    "v_mov_b32_e64 v89, 0\n"
    "v_mov_b32_e64 v200, 0\n"
    "v_mov_b32_e64 v90, 0\n"
    "v_mov_b32_e64 v91, 0\n"
    "v_mov_b32_e64 v59, 0\n"
    "v_mov_b32_e64 v60, 0\n"
    "v_mov_b32_e64 v67, 0\n"
    "v_mov_b32_e64 v68, 0\n"
    "v_mov_b32_e64 v75, 0\n"
    "v_mov_b32_e64 v76, 0\n"
    "v_mov_b32_e64 v83, 0\n"
    "v_mov_b32_e64 v84, 0\n"
    "v_mov_b32_e32 v36, 0x75089ba4\n"
    "v_mov_b32_e32 v38, 0xbdd2d3e8\n"
    "v_mov_b32_e32 v32, 0xc0135761\n"
    "v_lshlrev_b32_e64 v34, 16, v213\n"
    "v_lshlrev_b32_e64 v234, 1, v213\n"
    "v_or_b32_e32 v0, 0x8000, v34\n"
    "v_or_b32_e32 v1, 0x18000, v34\n"
    "v_or_b32_e32 v2, 0x28000, v34\n"
    "v_or_b32_e32 v3, 0x38000, v34\n"
    "v_or_b32_e32 v4, 0x48000, v34\n"
    "v_or_b32_e32 v5, 0x58000, v34\n"
    "v_or_b32_e32 v6, 0x68000, v34\n"
    "v_or_b32_e32 v7, 0x78000, v34\n"
    "v_cmp_ne_u32_e64 s[72:73], 0, v34\n"
    "s_nop 0\n"
    "s_nop 1\n"
    "v_cndmask_b32_e64 v0, 0, v0, s[72:73]\n"
    "v_pk_mul_f32 v[0:1], v[0:1], v[36:37] op_sel_hi:[1,0]\n"
    "v_pk_mul_f32 v[2:3], v[2:3], v[36:37] op_sel_hi:[1,0]\n"
    "v_pk_mul_f32 v[4:5], v[4:5], v[36:37] op_sel_hi:[1,0]\n"
    "v_pk_mul_f32 v[6:7], v[6:7], v[36:37] op_sel_hi:[1,0]\n"
    "v_pk_mul_f32 v[8:9], v[0:1], v[0:1]\n"
    "v_pk_mul_f32 v[10:11], v[2:3], v[2:3]\n"
    "v_pk_mul_f32 v[12:13], v[4:5], v[4:5]\n"
    "v_pk_mul_f32 v[14:15], v[6:7], v[6:7]\n"
    "v_pk_fma_f32 v[8:9], v[8:9], v[38:39], v[32:33] op_sel_hi:[1,0,0]\n"
    "v_pk_fma_f32 v[10:11], v[10:11], v[38:39], v[32:33] op_sel_hi:[1,0,0]\n"
    "v_pk_fma_f32 v[12:13], v[12:13], v[38:39], v[32:33] op_sel_hi:[1,0,0]\n"
    "v_pk_fma_f32 v[14:15], v[14:15], v[38:39], v[32:33] op_sel_hi:[1,0,0]\n"
    "v_pk_mul_f32 v[8:9], v[8:9], v[0:1]\n"
    "v_pk_mul_f32 v[10:11], v[10:11], v[2:3]\n"
    "v_pk_mul_f32 v[12:13], v[12:13], v[4:5]\n"
    "v_pk_mul_f32 v[14:15], v[14:15], v[6:7]\n"
    "v_exp_f32_e64 v8, v8\n"
    "v_exp_f32_e64 v9, v9\n"
    "v_exp_f32_e64 v10, v10\n"
    "v_exp_f32_e64 v11, v11\n"
    "v_exp_f32_e64 v12, v12\n"
    "v_exp_f32_e64 v13, v13\n"
    "v_exp_f32_e64 v14, v14\n"
    "v_exp_f32_e64 v15, v15\n"
    "v_pk_add_f32 v[16:17], v[8:9], 1.0 op_sel_hi:[1,0]\n"
    "v_pk_add_f32 v[18:19], v[10:11], 1.0 op_sel_hi:[1,0]\n"
    "v_pk_add_f32 v[20:21], v[12:13], 1.0 op_sel_hi:[1,0]\n"
    "v_pk_add_f32 v[22:23], v[14:15], 1.0 op_sel_hi:[1,0]\n"
    "v_rcp_f32_e64 v16, v16\n"
    "v_rcp_f32_e64 v17, v17\n"
    "v_rcp_f32_e64 v18, v18\n"
    "v_rcp_f32_e64 v19, v19\n"
    "v_rcp_f32_e64 v20, v20\n"
    "v_rcp_f32_e64 v21, v21\n"
    "v_rcp_f32_e64 v22, v22\n"
    "v_rcp_f32_e64 v23, v23\n"
    "v_pk_mul_f32 v[16:17], v[16:17], v[0:1]\n"
    "v_pk_mul_f32 v[18:19], v[18:19], v[2:3]\n"
    "v_pk_mul_f32 v[20:21], v[20:21], v[4:5]\n"
    "v_pk_mul_f32 v[22:23], v[22:23], v[6:7]\n"
    "v_pk_mul_f32 v[8:9], v[8:9], v[16:17]\n"
    "v_pk_mul_f32 v[10:11], v[10:11], v[18:19]\n"
    "v_pk_mul_f32 v[12:13], v[12:13], v[20:21]\n"
    "v_pk_mul_f32 v[14:15], v[14:15], v[22:23]\n"
    "v_cvt_pk_bf16_f32 v188, v16, v17\n"
    "v_cvt_pk_bf16_f32 v189, v18, v19\n"
    "v_cvt_pk_bf16_f32 v190, v20, v21\n"
    "v_cvt_pk_bf16_f32 v191, v22, v23\n"
    "v_cvt_pk_bf16_f32 v24, -v8, -v9\n"
    "v_cvt_pk_bf16_f32 v25, -v10, -v11\n"
    "v_cvt_pk_bf16_f32 v26, -v12, -v13\n"
    "v_cvt_pk_bf16_f32 v27, -v14, -v15\n"
    "ds_write_b128 v234, v[24:27] offset:0\n"
    "v_or_b32_e32 v0, 0x88000, v34\n"
    "v_or_b32_e32 v1, 0x98000, v34\n"
    "v_or_b32_e32 v2, 0xa8000, v34\n"
    "v_or_b32_e32 v3, 0xb8000, v34\n"
    "v_or_b32_e32 v4, 0xc8000, v34\n"
    "v_or_b32_e32 v5, 0xd8000, v34\n"
    "v_or_b32_e32 v6, 0xe8000, v34\n"
    "v_or_b32_e32 v7, 0xf8000, v34\n"
    "v_pk_mul_f32 v[0:1], v[0:1], v[36:37] op_sel_hi:[1,0]\n"
    "v_pk_mul_f32 v[2:3], v[2:3], v[36:37] op_sel_hi:[1,0]\n"
    "v_pk_mul_f32 v[4:5], v[4:5], v[36:37] op_sel_hi:[1,0]\n"
    "v_pk_mul_f32 v[6:7], v[6:7], v[36:37] op_sel_hi:[1,0]\n"
    "v_pk_mul_f32 v[8:9], v[0:1], v[0:1]\n"
    "v_pk_mul_f32 v[10:11], v[2:3], v[2:3]\n"
    "v_pk_mul_f32 v[12:13], v[4:5], v[4:5]\n"
    "v_pk_mul_f32 v[14:15], v[6:7], v[6:7]\n"
    "v_pk_fma_f32 v[8:9], v[8:9], v[38:39], v[32:33] op_sel_hi:[1,0,0]\n"
    "v_pk_fma_f32 v[10:11], v[10:11], v[38:39], v[32:33] op_sel_hi:[1,0,0]\n"
    "v_pk_fma_f32 v[12:13], v[12:13], v[38:39], v[32:33] op_sel_hi:[1,0,0]\n"
    "v_pk_fma_f32 v[14:15], v[14:15], v[38:39], v[32:33] op_sel_hi:[1,0,0]\n"
    "v_pk_mul_f32 v[8:9], v[8:9], v[0:1]\n"
    "v_pk_mul_f32 v[10:11], v[10:11], v[2:3]\n"
    "v_pk_mul_f32 v[12:13], v[12:13], v[4:5]\n"
    "v_pk_mul_f32 v[14:15], v[14:15], v[6:7]\n"
    "v_exp_f32_e64 v8, v8\n"
    "v_exp_f32_e64 v9, v9\n"
    "v_exp_f32_e64 v10, v10\n"
    "v_exp_f32_e64 v11, v11\n"
    "v_exp_f32_e64 v12, v12\n"
    "v_exp_f32_e64 v13, v13\n"
    "v_exp_f32_e64 v14, v14\n"
    "v_exp_f32_e64 v15, v15\n"
    "v_pk_add_f32 v[16:17], v[8:9], 1.0 op_sel_hi:[1,0]\n"
    "v_pk_add_f32 v[18:19], v[10:11], 1.0 op_sel_hi:[1,0]\n"
    "v_pk_add_f32 v[20:21], v[12:13], 1.0 op_sel_hi:[1,0]\n"
    "v_pk_add_f32 v[22:23], v[14:15], 1.0 op_sel_hi:[1,0]\n"
    "v_rcp_f32_e64 v16, v16\n"
    "v_rcp_f32_e64 v17, v17\n"
    "v_rcp_f32_e64 v18, v18\n"
    "v_rcp_f32_e64 v19, v19\n"
    "v_rcp_f32_e64 v20, v20\n"
    "v_rcp_f32_e64 v21, v21\n"
    "v_rcp_f32_e64 v22, v22\n"
    "v_rcp_f32_e64 v23, v23\n"
    "v_pk_mul_f32 v[16:17], v[16:17], v[0:1]\n"
    "v_pk_mul_f32 v[18:19], v[18:19], v[2:3]\n"
    "v_pk_mul_f32 v[20:21], v[20:21], v[4:5]\n"
    "v_pk_mul_f32 v[22:23], v[22:23], v[6:7]\n"
    "v_pk_mul_f32 v[8:9], v[8:9], v[16:17]\n"
    "v_pk_mul_f32 v[10:11], v[10:11], v[18:19]\n"
    "v_pk_mul_f32 v[12:13], v[12:13], v[20:21]\n"
    "v_pk_mul_f32 v[14:15], v[14:15], v[22:23]\n"
    "v_cvt_pk_bf16_f32 v192, v16, v17\n"
    "v_cvt_pk_bf16_f32 v193, v18, v19\n"
    "v_cvt_pk_bf16_f32 v194, v20, v21\n"
    "v_cvt_pk_bf16_f32 v195, v22, v23\n"
    "v_cvt_pk_bf16_f32 v24, -v8, -v9\n"
    "v_cvt_pk_bf16_f32 v25, -v10, -v11\n"
    "v_cvt_pk_bf16_f32 v26, -v12, -v13\n"
    "v_cvt_pk_bf16_f32 v27, -v14, -v15\n"
    "ds_write_b128 v234, v[24:27] offset:16\n"
    "v_add_u32_e32 v234, 65536, v234\n"
    "s_add_u32 m0, s38, 0\n"
    "s_mov_b32 s64, 0\n"
    "buffer_load_dwordx4 v210, s[84:87], s64 offen offset:0 lds\n"
    "s_nop 0\n"
    "s_waitcnt vmcnt(12)\n"
    "v_lshlrev_b32_sdwa v216, 2, v168 dst_sel:DWORD dst_unused:UNUSED_PAD src0_sel:DWORD src1_sel:WORD_0\n"
    "v_lshlrev_b32_sdwa v217, 2, v168 dst_sel:DWORD dst_unused:UNUSED_PAD src0_sel:DWORD src1_sel:WORD_1\n"
    "v_lshlrev_b32_sdwa v218, 2, v169 dst_sel:DWORD dst_unused:UNUSED_PAD src0_sel:DWORD src1_sel:WORD_0\n"
    "v_lshlrev_b32_sdwa v219, 2, v169 dst_sel:DWORD dst_unused:UNUSED_PAD src0_sel:DWORD src1_sel:WORD_1\n"
    "v_lshlrev_b32_sdwa v220, 2, v170 dst_sel:DWORD dst_unused:UNUSED_PAD src0_sel:DWORD src1_sel:WORD_0\n"
    "v_lshlrev_b32_sdwa v221, 2, v170 dst_sel:DWORD dst_unused:UNUSED_PAD src0_sel:DWORD src1_sel:WORD_1\n"
    "v_lshlrev_b32_sdwa v222, 2, v171 dst_sel:DWORD dst_unused:UNUSED_PAD src0_sel:DWORD src1_sel:WORD_0\n"
    "v_lshlrev_b32_sdwa v223, 2, v171 dst_sel:DWORD dst_unused:UNUSED_PAD src0_sel:DWORD src1_sel:WORD_1\n"
    "ds_write_b128 v222, v[88:91]\n"
    "buffer_load_dwordx4 v210, s[84:87], s64 offen offset:1024 lds\n"
    "s_nop 0\n"
    "s_waitcnt vmcnt(12)\n"
    "v_lshlrev_b32_sdwa v207, 2, v172 dst_sel:DWORD dst_unused:UNUSED_PAD src0_sel:DWORD src1_sel:WORD_0\n"
    "v_lshlrev_b32_sdwa v242, 2, v173 dst_sel:DWORD dst_unused:UNUSED_PAD src0_sel:DWORD src1_sel:WORD_0\n"
    "v_lshlrev_b32_sdwa v243, 2, v173 dst_sel:DWORD dst_unused:UNUSED_PAD src0_sel:DWORD src1_sel:WORD_1\n"
    "v_lshlrev_b32_sdwa v244, 2, v174 dst_sel:DWORD dst_unused:UNUSED_PAD src0_sel:DWORD src1_sel:WORD_0\n"
    "v_lshlrev_b32_sdwa v248, 2, v174 dst_sel:DWORD dst_unused:UNUSED_PAD src0_sel:DWORD src1_sel:WORD_1\n"
    "v_lshlrev_b32_sdwa v249, 2, v175 dst_sel:DWORD dst_unused:UNUSED_PAD src0_sel:DWORD src1_sel:WORD_0\n"
    "v_lshlrev_b32_sdwa v250, 2, v175 dst_sel:DWORD dst_unused:UNUSED_PAD src0_sel:DWORD src1_sel:WORD_1\n"
    "v_bfe_i32 v201, v172, 16, 1\n"
    "v_and_b32_e32 v201, 0x35003500, v201\n"
    "v_bfe_i32 v203, v172, 18, 1\n"
    "v_and_b32_e32 v203, 0x35003500, v203\n"
    "v_bfe_i32 v205, v172, 20, 1\n"
    "v_and_b32_e32 v205, 0x35003500, v205\n"
    "v_bfe_i32 v92, v172, 26, 1\n"
    "v_xor_b32_e32 v92, 0xffff, v92\n"
    "v_and_b32_e32 v92, 0x3f803f80, v92\n"
    "v_bfe_i32 v236, v172, 22, 1\n"
    "v_and_b32_e64 v236, v92, v236\n"
    "v_bfe_i32 v237, v172, 23, 1\n"
    "v_and_b32_e64 v237, v92, v237\n"
    "buffer_load_dwordx4 v210, s[84:87], s64 offen offset:2048 lds\n"
    "s_add_u32 m0, s38, 3072\n"
    "s_mov_b32 s64, 3072\n"
    "buffer_load_dwordx4 v210, s[84:87], s64 offen offset:0 lds\n"
    "buffer_load_dwordx4 v210, s[84:87], s64 offen offset:1024 lds\n"
    "s_nop 0\n"
    "s_waitcnt vmcnt(14)\n"
    "v_lshlrev_b32_sdwa v251, 2, v176 dst_sel:DWORD dst_unused:UNUSED_PAD src0_sel:DWORD src1_sel:WORD_0\n"
    "buffer_load_dwordx4 v210, s[84:87], s64 offen offset:2048 lds\n"
    "v_lshlrev_b32_sdwa v252, 2, v176 dst_sel:DWORD dst_unused:UNUSED_PAD src0_sel:DWORD src1_sel:WORD_1\n"
    "s_add_u32 m0, s38, 6144\n"
    "s_mov_b32 s64, 6144\n"
    "buffer_load_dwordx4 v210, s[84:87], s64 offen offset:0 lds\n"
    "buffer_load_dwordx4 v210, s[84:87], s64 offen offset:1024 lds\n"
    "v_lshlrev_b32_sdwa v253, 2, v177 dst_sel:DWORD dst_unused:UNUSED_PAD src0_sel:DWORD src1_sel:WORD_0\n"
    "v_lshlrev_b32_sdwa v254, 2, v177 dst_sel:DWORD dst_unused:UNUSED_PAD src0_sel:DWORD src1_sel:WORD_1\n"
    "buffer_load_dwordx4 v210, s[84:87], s64 offen offset:2048 lds\n"
    "s_mov_b32 s59, 49152\n"
    "buffer_load_dwordx4 v[96:99], v209, s[80:83], s59 offen offset:0\n"
    "v_lshlrev_b32_sdwa v241, 0, v178 dst_sel:DWORD dst_unused:UNUSED_PAD src0_sel:DWORD src1_sel:WORD_0\n"
    "buffer_load_dwordx4 v[100:103], v209, s[80:83], s59 offen offset:256\n"
    "buffer_load_dwordx4 v[104:107], v209, s[80:83], s59 offen offset:512\n"
    "v_lshlrev_b32_sdwa v245, 4, v178 dst_sel:DWORD dst_unused:UNUSED_PAD src0_sel:DWORD src1_sel:WORD_1\n"
    "v_lshlrev_b32_sdwa v255, 4, v179 dst_sel:DWORD dst_unused:UNUSED_PAD src0_sel:DWORD src1_sel:WORD_0\n"
    "v_lshlrev_b32_sdwa v247, 4, v179 dst_sel:DWORD dst_unused:UNUSED_PAD src0_sel:DWORD src1_sel:WORD_1\n"
    "s_waitcnt vmcnt(20)\n"
    "v_lshlrev_b32_e32 v40, 16, v224\n"
    "s_waitcnt vmcnt(19)\n"
    "v_lshlrev_b32_e32 v41, 16, v225\n"
    "s_waitcnt vmcnt(18)\n"
    "v_lshlrev_b32_e32 v42, 16, v226\n"
    "s_waitcnt vmcnt(17)\n"
    "v_lshlrev_b32_e32 v43, 16, v227\n"
    "s_waitcnt vmcnt(16)\n"
    "v_lshlrev_b32_e32 v44, 16, v228\n"
    "s_waitcnt vmcnt(15)\n"
    "v_lshlrev_b32_e32 v45, 16, v229\n"
    "s_waitcnt vmcnt(14)\n"
    "v_lshlrev_b32_e32 v46, 16, v230\n"
    "s_waitcnt vmcnt(13)\n"
    "v_lshlrev_b32_e32 v47, 16, v231\n"
    "v_add_f32_e64 v41, v41, v45\n"
    "v_add_f32_e64 v42, v42, v46\n"
    "s_mov_b32 s59, 52224\n"
    "buffer_load_dwordx4 v[108:111], v209, s[80:83], s59 offen offset:0\n"
    "v_add_f32_e64 v43, v43, v47\n"
    "v_cvt_pk_bf16_f32 v56, v40, v41\n"
    "v_cvt_pk_bf16_f32 v57, v42, v43\n"
    "v_cvt_pk_bf16_f32 v58, v44, 0\n"
    "v_cvt_pk_bf16_f32 v64, 0, v40\n"
    "v_cvt_pk_bf16_f32 v65, v41, v42\n"
    "v_cvt_pk_bf16_f32 v66, v43, v44\n"
    "v_cvt_pk_bf16_f32 v61, v40, v41\n"
    "v_cvt_pk_bf16_f32 v62, v42, v43\n"
    "v_cvt_pk_bf16_f32 v63, v44, 0\n"
    "v_cvt_pk_bf16_f32 v69, 0, v40\n"
    "v_cvt_pk_bf16_f32 v70, v41, v42\n"
    "v_cvt_pk_bf16_f32 v71, v43, v44\n"
    "ds_write_b128 v221, v[56:59] offset:0\n"
    "buffer_load_dwordx4 v[112:115], v209, s[80:83], s59 offen offset:256\n"
    "v_and_b32_e32 v48, 0xffff0000, v224\n"
    "v_and_b32_e32 v49, 0xffff0000, v225\n"
    "v_and_b32_e32 v50, 0xffff0000, v226\n"
    "v_and_b32_e32 v51, 0xffff0000, v227\n"
    "v_and_b32_e32 v52, 0xffff0000, v228\n"
    "ds_write_b128 v221, v[64:67] offset:16\n"
    "v_and_b32_e32 v53, 0xffff0000, v229\n"
    "v_and_b32_e32 v54, 0xffff0000, v230\n"
    "v_and_b32_e32 v55, 0xffff0000, v231\n"
    "v_add_f32_e64 v49, v49, v53\n"
    "v_add_f32_e64 v50, v50, v54\n"
    "v_add_f32_e64 v51, v51, v55\n"
    "ds_write_b128 v221, v[60:63] offset:32\n"
    "buffer_load_dwordx4 v[116:119], v209, s[80:83], s59 offen offset:512\n"
    "v_cvt_pk_bf16_f32 v72, v48, v49\n"
    "v_cvt_pk_bf16_f32 v73, v50, v51\n"
    "v_cvt_pk_bf16_f32 v74, v52, 0\n"
    "v_cvt_pk_bf16_f32 v80, 0, v48\n"
    "v_cvt_pk_bf16_f32 v81, v49, v50\n"
    "ds_write_b128 v221, v[68:71] offset:48\n"
    "v_cvt_pk_bf16_f32 v82, v51, v52\n"
    "v_cvt_pk_bf16_f32 v77, v48, v49\n"
    "s_nop 0\n"
    "s_mov_b32 s59, 0\n"
    "buffer_load_dwordx4 v[120:123], v209, s[80:83], s59 offen offset:0\n"
    "v_cvt_pk_bf16_f32 v78, v50, v51\n"
    "v_cvt_pk_bf16_f32 v79, v52, 0\n"
    "v_cvt_pk_bf16_f32 v85, 0, v48\n"
    "v_cvt_pk_bf16_f32 v86, v49, v50\n"
    "v_cvt_pk_bf16_f32 v87, v51, v52\n"
    "ds_write_b128 v221, v[72:75] offset:336\n"
    "v_bfe_i32 v238, v172, 24, 1\n"
    "v_and_b32_e64 v238, v92, v238\n"
    "v_bfe_i32 v239, v172, 25, 1\n"
    "v_and_b32_e64 v239, v92, v239\n"
    "v_bfe_u32 v93, v172, 28, 2\n"
    "v_mul_u32_u24_e32 v93, 393216, v93\n"
    "ds_write_b128 v221, v[80:83] offset:352\n"
    "buffer_load_dwordx4 v[124:127], v209, s[80:83], s59 offen offset:256\n"
    "v_xor_b32_e32 v224, 0, v219\n"
    "v_xor_b32_e32 v225, 32, v219\n"
    "v_xor_b32_e32 v226, 64, v219\n"
    "v_xor_b32_e32 v227, 96, v219\n"
    "v_xor_b32_e32 v228, 0, v223\n"
    "ds_write_b128 v221, v[76:79] offset:368\n"
    "v_xor_b32_e32 v229, 32, v223\n"
    "v_xor_b32_e32 v230, 64, v223\n"
    "v_xor_b32_e32 v231, 96, v223\n"
    "buffer_load_dwordx4 v[128:131], v209, s[80:83], s59 offen offset:512\n"
    "ds_write_b128 v221, v[84:87] offset:384\n"
    "v_lshl_add_u32 v196, s51, 8, v225\n"
    "v_lshl_add_u32 v197, s51, 8, v227\n"
    "v_mov_b32_e64 v222, v254\n"
    "v_mov_b32_e64 v254, 0\n"
    "s_nop 1\n"
    "v_mfma_f32_32x32x1_2b_f32 v[0:31], v254, v254, 0\n"
    "s_mov_b32 s65, 0\n"
    "buffer_load_dwordx4 a[224:227], v208, s[76:79], s65 offen\n"
    "v_mfma_f32_32x32x1_2b_f32 v[32:63], v254, v254, 0\n"
    "s_mov_b32 s65, 98304\n"
    "buffer_load_dwordx4 a[228:231], v208, s[76:79], s65 offen\n"
    "s_mov_b32 s48, 0x44ea44ea\n"
    "s_mov_b32 s49, 0xb77fb77f\n"
    "s_mov_b32 s50, 0x89efde6e\n"
    "s_mov_b32 s46, s42\n"
    "s_mov_b32 s47, s43\n"
    "s_mul_i32 s54, s60, 592\n"
    "s_add_u32 s54, s54, -27008\n"
    "s_mul_i32 s55, s60, -1284\n"
    "s_add_u32 s55, s55, 54688\n"
    "s_mul_i32 s56, s60, -6160\n"
    "s_add_u32 s56, s56, -27008\n"
    "s_add_u32 s98, s99, 1375872\n"
    "s_and_b32 s36, s60, 1\n"
    "s_lshl_b32 s36, s36, 13\n"
    "s_add_u32 s36, s36, 327680\n"
    "s_mul_i32 s53, s60, 98304\n"
    "s_add_u32 s98, s53, s98\n"
    "s_xor_b32 s53, s60, 1\n"
    "s_mul_i32 s53, s53, 393216\n"
    "s_add_u32 s99, s99, s53\n"
    "s_add_u32 s53, s98, 393216\n"
    "s_sub_u32 s53, s53, s36\n"
    "s_and_b32 s37, s61, 1\n"
    "s_lshl_b32 s37, s37, 2\n"
    "s_add_u32 s37, s37, s60\n"
    "s_lshl_b32 s37, s37, 12\n"
    "s_cmp_eq_u32 s61, 0\n"
    "s_cselect_b32 s67, 2, 0\n"
    "s_cmp_eq_u32 s61, 31\n"
    "s_cselect_b32 s67, 1, s67\n"
    "s_lshl_b32 s67, s67, 30\n"
    "s_mov_b64 s[68:69], s[44:45]\n"
    "s_mov_b32 s70, 0\n"
    "s_mov_b32 s71, s47\n"
    "s_mov_b64 s[72:73], s[40:41]\n"
    "s_mov_b64 s[74:75], s[42:43]\n"
    "s_mov_b32 s57, 0\n"
    "s_mov_b32 s58, 196608\n"
    "s_mov_b32 s60, 0\n"
    "s_mov_b32 s61, 393216\n"
    "s_mov_b32 s62, 786432\n"
    "s_mov_b32 s63, 1179648\n"
    "s_mov_b32 s52, 1572864\n"
    "s_mov_b32 s88, 98304\n"
    "s_mov_b32 s89, 294912\n"
    "s_mov_b32 s39, 0\n"
    "s_getpc_b64 s[76:77]\n"
    ".Lreplk_pc1:\n"
    "s_add_u32 s76, s76, .Lreplk_last-.Lreplk_pc1\n"
    "s_addc_u32 s77, s77, 0\n"
    "s_sub_u32 s76, s76, s36\n"
    "s_subb_u32 s77, s77, 0\n"
    "s_add_u32 s78, s36, .Lreplk_end-.Lreplk_last\n"
    "s_mov_b32 s79, s37\n"
    "s_mov_b32 s65, 196608\n"
    "buffer_load_dwordx4 a[232:235], v208, s[40:43], s65 offen\n"
    "s_mov_b32 s65, 294912\n"
    "buffer_load_dwordx4 a[236:239], v208, s[40:43], s65 offen\n"
    "v_add3_u32 v247, v247, v93, s98\n"
    "v_add_u32_e64 v246, s98, v255\n"
    "v_add_u32_e64 v245, s99, v245\n"
    "v_add_u32_e64 v255, s99, v255\n"
    "v_and_b32_e64 v93, s67, v172\n"
    "v_and_b32_e32 v94, 0x80000000, v93\n"
    "s_mov_b32 s65, 589824\n"
    "buffer_load_dwordx4 a[240:243], v208, s[40:43], s65 offen\n"
    "v_and_b32_e32 v93, 0x40000000, v93\n"
    "v_or_b32_e32 v246, v246, v94\n"
    "v_or_b32_e32 v247, v247, v93\n"
    "s_waitcnt vmcnt(14)\n"
    "s_barrier\n"
    "ds_read_b64_tr_b16 a[0:1], v220 offset:0\n"
    "ds_read_b64_tr_b16 a[2:3], v220 offset:3072\n"
    "ds_read_b64_tr_b16 a[4:5], v220 offset:8\n"
    "s_waitcnt vmcnt(13)\n"
    "v_or3_b32 v200, v200, v96, v97\n"
    "ds_read_b64_tr_b16 a[6:7], v220 offset:3080\n"
    "s_mov_b32 s59, 3072\n"
    "buffer_load_dwordx4 v[132:135], v209, s[80:83], s59 offen offset:0\n"
    "ds_read_b64_tr_b16 a[8:9], v220 offset:64\n"
    "ds_read_b64_tr_b16 a[10:11], v220 offset:3136\n"
    "ds_read_b64_tr_b16 a[12:13], v220 offset:72\n"
    "v_or3_b32 v200, v200, v98, v99\n"
    "ds_read_b64_tr_b16 a[14:15], v220 offset:3144\n"
    "ds_read_b64_tr_b16 v[168:169], v218 offset:128\n"
    "ds_read_b64_tr_b16 v[170:171], v218 offset:3200\n"
    "v_and_b32_e32 v232, 0x7fff7fff, v96\n"
    "ds_read_b64_tr_b16 a[16:17], v220 offset:6144\n"
    "ds_read_b64_tr_b16 a[18:19], v220 offset:9216\n"
    "ds_read_b64_tr_b16 a[20:21], v220 offset:6152\n"
    "v_add_u32_e64 v96, v201, v96\n"
    "ds_read_b64_tr_b16 a[22:23], v220 offset:9224\n"
    "ds_read_b64_tr_b16 a[24:25], v220 offset:6208\n"
    "buffer_load_dwordx4 v[136:139], v209, s[80:83], s59 offen offset:256\n"
    "ds_read_b64_tr_b16 a[26:27], v220 offset:9280\n"
    "ds_read_b64_tr_b16 a[28:29], v220 offset:6216\n"
    "v_add_u32_e32 v98, v201, v98\n"
    "ds_read_b64_tr_b16 a[30:31], v220 offset:9288\n"
    "ds_read_b64_tr_b16 v[172:173], v218 offset:6272\n"
    "ds_read_b64_tr_b16 v[174:175], v218 offset:9344\n"
    "s_waitcnt vmcnt(14)\n"
    "v_or3_b32 v200, v200, v100, v101\n"
    "ds_read_b64_tr_b16 a[32:33], v220 offset:12288\n"
    "ds_read_b64_tr_b16 a[34:35], v220 offset:15360\n"
    "ds_read_b64_tr_b16 a[36:37], v220 offset:12296\n"
    "ds_read_b64_tr_b16 a[38:39], v220 offset:15368\n"
    "v_or3_b32 v200, v200, v102, v103\n"
    "ds_read_b64_tr_b16 a[40:41], v220 offset:12352\n"
    "buffer_load_dwordx4 v[140:143], v209, s[80:83], s59 offen offset:512\n"
    "ds_read_b64_tr_b16 a[42:43], v220 offset:15424\n"
    "ds_read_b64_tr_b16 a[44:45], v220 offset:12360\n"
    "ds_read_b64_tr_b16 a[46:47], v220 offset:15432\n"
    "v_add_u32_e64 v100, v203, v100\n"
    "ds_read_b64_tr_b16 v[176:177], v218 offset:12416\n"
    "ds_read_b64_tr_b16 v[178:179], v218 offset:15488\n"
    "v_add_u32_e32 v102, v203, v102\n"
    "s_waitcnt vmcnt(14)\n"
    "v_or3_b32 v200, v200, v104, v105\n"
    "v_or3_b32 v200, v200, v106, v107\n"
    "v_add_u32_e64 v104, v205, v104\n"
    "v_add_u32_e32 v106, v205, v106\n"
    "s_waitcnt vmcnt(13)\n"
    "v_or3_b32 v200, v200, v108, v109\n"
    "v_or3_b32 v200, v200, v110, v111\n"
    "v_add_u32_e64 v108, v201, v108\n"
    "v_add_u32_e32 v110, v201, v110\n"
    "s_waitcnt vmcnt(12)\n"
    "v_or3_b32 v200, v200, v112, v113\n"
    "s_mov_b32 s59, 24576\n"
    "buffer_load_dwordx4 v[144:147], v209, s[80:83], s59 offen offset:0\n"
    "v_or3_b32 v200, v200, v114, v115\n"
    "v_add_u32_e64 v112, v203, v112\n"
    "v_add_u32_e32 v114, v203, v114\n"
    "s_waitcnt vmcnt(12)\n"
    "v_or3_b32 v200, v200, v116, v117\n"
    "v_or3_b32 v200, v200, v118, v119\n"
    "v_add_u32_e64 v116, v205, v116\n"
    "v_add_u32_e64 v118, v205, v118\n"
    "v_mfma_f32_32x32x1_2b_f32 v[64:95], v254, v254, 0\n"
    "v_add_u32_e32 v254, s54, v242\n"
    "s_waitcnt lgkmcnt(0)\n"
    "s_barrier\n"
    "buffer_load_dwordx4 v[148:151], v209, s[80:83], s59 offen offset:256\n"
    "ds_write_b128 v217, v[96:99] offset:24576\n"
    "s_waitcnt vmcnt(12)\n"
    "v_or3_b32 v200, v200, v120, v121\n"
    "v_or3_b32 v200, v200, v122, v123\n"
    "v_add_u32_e64 v120, v201, v120\n"
    "v_add_u32_e32 v122, v201, v122\n"
    "ds_write_b128 v217, v[100:103] offset:24832\n"
    "buffer_load_dwordx4 v[152:155], v209, s[80:83], s59 offen offset:512\n"
    "s_waitcnt vmcnt(12)\n"
    "v_or3_b32 v200, v200, v124, v125\n"
    "v_or3_b32 v200, v200, v126, v127\n"
    "v_add_u32_e64 v124, v203, v124\n"
    "v_add_u32_e64 v126, v203, v126\n"
    "ds_write_b128 v217, v[104:107] offset:25088\n"
    "s_mov_b32 s59, 27648\n"
    "buffer_load_dwordx4 v[156:159], v209, s[80:83], s59 offen offset:0\n"
    "ds_write_b128 v217, v[108:111] offset:0\n"
    "s_waitcnt vmcnt(12)\n"
    "v_or3_b32 v200, v200, v128, v129\n"
    "v_or3_b32 v200, v200, v130, v131\n"
    "v_add_u32_e32 v128, v205, v128\n"
    "v_add_u32_e64 v130, v205, v130\n"
    "ds_write_b128 v217, v[112:115] offset:256\n"
    "s_waitcnt lgkmcnt(2)\n"
    "s_barrier\n"
    "ds_read_b64_tr_b16 a[48:49], v224 offset:24576\n"
    "ds_read_b64_tr_b16 v[180:181], v196 offset:24576\n"
    "ds_read_b64_tr_b16 a[52:53], v226 offset:24576\n"
    "v_mov_b32_e64 v112, v188\n"
    "ds_read_b64_tr_b16 v[184:185], v197 offset:24576\n"
    "ds_read_b64_tr_b16 a[56:57], v228 offset:24576\n"
    "ds_read_b64_tr_b16 a[60:61], v229 offset:24576\n"
    "v_mov_b32_e64 v113, v189\n"
    "ds_read_b64_tr_b16 a[64:65], v230 offset:24576\n"
    "ds_read_b64_tr_b16 a[68:69], v231 offset:24576\n"
    "ds_read_b64_tr_b16 a[72:73], v224 offset:24832\n"
    "v_mov_b32_e64 v114, v190\n"
    "ds_read_b64_tr_b16 a[76:77], v226 offset:24832\n"
    "ds_write_b128 v217, v[116:119] offset:512\n"
    "buffer_load_dwordx4 v[160:163], v209, s[80:83], s59 offen offset:256\n"
    "v_mov_b32_e64 v115, v191\n"
    "v_mov_b32_e64 v116, v192\n"
    "v_mov_b32_e64 v117, v193\n"
    "v_mov_b32_e64 v118, v194\n"
    "v_mov_b32_e64 v119, v195\n"
    "ds_write_b128 v217, v[120:123] offset:12288\n"
    "ds_write_b128 v217, v[124:127] offset:12544\n"
    "s_waitcnt lgkmcnt(2)\n"
    "s_barrier\n"
    "ds_read_b64_tr_b16 a[50:51], v224 offset:0\n"
    "ds_read_b64_tr_b16 v[182:183], v196 offset:0\n"
    "ds_read_b64_tr_b16 a[54:55], v226 offset:0\n"
    "ds_read_b64_tr_b16 v[186:187], v197 offset:0\n"
    "ds_read_b64_tr_b16 a[58:59], v228 offset:0\n"
    "ds_read_b64_tr_b16 a[62:63], v229 offset:0\n"
    "ds_read_b64_tr_b16 a[66:67], v230 offset:0\n"
    "ds_read_b64_tr_b16 a[70:71], v231 offset:0\n"
    "buffer_load_dwordx4 v[164:167], v209, s[80:83], s59 offen offset:512\n"
    "ds_read_b64_tr_b16 a[74:75], v224 offset:256\n"
    "ds_read_b64_tr_b16 a[78:79], v226 offset:256\n"
    "ds_write_b128 v217, v[128:131] offset:12800\n"
    "s_waitcnt vmcnt(13)\n"
    "ds_write2_b32 v216, a224, a225 offset0:0 offset1:20\n"
    "s_waitcnt lgkmcnt(6)\n"
    "ds_write_b128 v207, v[184:187] offset:36864\n"
    "s_waitcnt lgkmcnt(2)\n"
    "s_barrier\n"
    "ds_read_b64_tr_b16 a[80:81], v224 offset:12288\n"
    "ds_read_b64_tr_b16 a[84:85], v225 offset:12288\n"
    "ds_read_b64_tr_b16 a[88:89], v226 offset:12288\n"
    "s_mov_b32 s65, 688128\n"
    "buffer_load_dwordx4 a[244:247], v208, s[40:43], s65 offen\n"
    "ds_read_b64_tr_b16 a[92:93], v227 offset:12288\n"
    "ds_read_b64_tr_b16 a[96:97], v228 offset:12288\n"
    "ds_read_b64_tr_b16 a[100:101], v229 offset:12288\n"
    "ds_read_b64_tr_b16 a[104:105], v230 offset:12288\n"
    "ds_read_b64_tr_b16 a[108:109], v231 offset:12288\n"
    "ds_read_b64_tr_b16 a[112:113], v224 offset:12544\n"
    "ds_read_b64_tr_b16 a[116:117], v225 offset:12544\n"
    "ds_read_b64_tr_b16 a[120:121], v226 offset:12544\n"
    "ds_read_b64_tr_b16 a[124:125], v227 offset:12544\n"
    "ds_write2_b32 v216, a226, a227 offset0:40 offset1:60\n"
    "s_waitcnt vmcnt(13)\n"
    "ds_write2_b32 v216, a228, a229 offset0:80 offset1:100\n"
    "ds_write2_b32 v216, a230, a231 offset0:120 offset1:140\n"
    "v_add_u32_e32 v216, 1360, v216\n"
    "s_waitcnt vmcnt(12)\n"
    "ds_write2_b32 v216, a232, a233 offset0:0 offset1:20\n"
    "s_mov_b32 s65, 983040\n"
    "buffer_load_dwordx4 a[248:251], v208, s[40:43], s65 offen\n"
    "ds_write2_b32 v216, a234, a235 offset0:40 offset1:60\n"
    "s_waitcnt vmcnt(12)\n"
    "ds_write2_b32 v216, a236, a237 offset0:80 offset1:100\n"
    "ds_write2_b32 v216, a238, a239 offset0:120 offset1:140\n"
    "v_add_u32_e32 v216, 1360, v216\n"
    "s_mov_b32 s65, 1081344\n"
    "buffer_load_dwordx4 a[252:255], v208, s[40:43], s65 offen\n"
    "s_waitcnt vmcnt(12)\n"
    "ds_write2_b32 v216, a240, a241 offset0:0 offset1:20\n"
    "ds_write2_b32 v216, a242, a243 offset0:40 offset1:60\n"
    "s_mov_b32 s65, 1376256\n"
    "buffer_load_dwordx4 a[176:179], v208, s[40:43], s65 offen\n"
    "s_waitcnt vmcnt(12)\n"
    "v_or3_b32 v200, v200, v132, v133\n"
    "v_or3_b32 v200, v200, v134, v135\n"
    "v_add_u32_e32 v132, v201, v132\n"
    "v_add_u32_e64 v134, v201, v134\n"
    "ds_write_b128 v217, v[132:135] offset:24576\n"
    "s_mov_b32 s65, 1474560\n"
    "buffer_load_dwordx4 a[180:183], v208, s[40:43], s65 offen\n"
    "s_waitcnt vmcnt(12)\n"
    "v_or3_b32 v200, v200, v136, v137\n"
    "v_or3_b32 v200, v200, v138, v139\n"
    "v_add_u32_e32 v136, v203, v136\n"
    "v_add_u32_e64 v138, v203, v138\n"
    "ds_write_b128 v217, v[136:139] offset:24832\n"
    "s_mov_b32 s65, 0\n"
    "buffer_load_dwordx4 a[184:187], v208, s[40:43], s65 offen\n"
    "s_waitcnt vmcnt(12)\n"
    "v_or3_b32 v200, v200, v140, v141\n"
    "v_or3_b32 v200, v200, v142, v143\n"
    "v_add_u32_e64 v140, v205, v140\n"
    "v_add_u32_e64 v142, v205, v142\n"
    "ds_write_b128 v217, v[140:143] offset:25088\n"
    "s_mov_b32 s65, 98304\n"
    "buffer_load_dwordx4 a[188:191], v208, s[40:43], s65 offen\n"
    "s_waitcnt vmcnt(12)\n"
    "v_or3_b32 v200, v200, v144, v145\n"
    "v_or3_b32 v200, v200, v146, v147\n"
    "v_add_u32_e32 v144, v201, v144\n"
    "v_add_u32_e64 v146, v201, v146\n"
    "ds_write_b128 v217, v[144:147] offset:0\n"
    "s_waitcnt lgkmcnt(1)\n"
    "s_barrier\n"
    "s_mov_b32 s65, 393216\n"
    "buffer_load_dwordx4 a[192:195], v208, s[40:43], s65 offen\n"
    "ds_read_b64_tr_b16 a[82:83], v224 offset:24576\n"
    "ds_read_b64_tr_b16 a[86:87], v225 offset:24576\n"
    "ds_read_b64_tr_b16 a[90:91], v226 offset:24576\n"
    "s_nop 0\n"
    "s_waitcnt vmcnt(12)\n"
    "v_or3_b32 v200, v200, v148, v149\n"
    "ds_read_b64_tr_b16 a[94:95], v227 offset:24576\n"
    "ds_read_b64_tr_b16 a[98:99], v228 offset:24576\n"
    "ds_read_b64_tr_b16 a[102:103], v229 offset:24576\n"
    "ds_read_b64_tr_b16 a[106:107], v230 offset:24576\n"
    "v_or3_b32 v200, v200, v150, v151\n"
    "ds_read_b64_tr_b16 a[110:111], v231 offset:24576\n"
    "s_mov_b32 s65, 491520\n"
    "buffer_load_dwordx4 a[196:199], v208, s[40:43], s65 offen\n"
    "ds_read_b64_tr_b16 a[114:115], v224 offset:24832\n"
    "ds_read_b64_tr_b16 a[118:119], v225 offset:24832\n"
    "ds_read_b64_tr_b16 a[122:123], v226 offset:24832\n"
    "v_add_u32_e64 v148, v203, v148\n"
    "ds_read_b64_tr_b16 a[126:127], v227 offset:24832\n"
    "v_add_u32_e32 v150, v203, v150\n"
    "ds_write_b128 v217, v[148:151] offset:256\n"
    "s_waitcnt vmcnt(12)\n"
    "v_or3_b32 v200, v200, v152, v153\n"
    "v_or3_b32 v200, v200, v154, v155\n"
    "v_add_u32_e64 v152, v205, v152\n"
    "s_mov_b32 s65, 786432\n"
    "buffer_load_dwordx4 a[200:203], v208, s[40:43], s65 offen\n"
    "v_add_u32_e32 v154, v205, v154\n"
    "ds_write_b128 v217, v[152:155] offset:512\n"
    "s_waitcnt vmcnt(12)\n"
    "v_or3_b32 v200, v200, v156, v157\n"
    "v_or3_b32 v200, v200, v158, v159\n"
    "v_add_u32_e64 v156, v201, v156\n"
    "v_add_u32_e64 v158, v201, v158\n"
    "ds_write_b128 v217, v[156:159] offset:12288\n"
    "s_mov_b32 s65, 884736\n"
    "buffer_load_dwordx4 a[204:207], v208, s[40:43], s65 offen\n"
    "s_waitcnt vmcnt(12)\n"
    "v_or3_b32 v200, v200, v160, v161\n"
    "v_or3_b32 v200, v200, v162, v163\n"
    "v_add_u32_e32 v160, v203, v160\n"
    "v_add_u32_e64 v162, v203, v162\n"
    "ds_write_b128 v217, v[160:163] offset:12544\n"
    "s_waitcnt lgkmcnt(2)\n"
    "s_barrier\n"
    "s_mov_b32 s65, 1179648\n"
    "buffer_load_dwordx4 a[208:211], v208, s[40:43], s65 offen\n"
    "ds_read_b64_tr_b16 a[128:129], v224 offset:0\n"
    "ds_read_b64_tr_b16 a[132:133], v225 offset:0\n"
    "ds_read_b64_tr_b16 a[136:137], v226 offset:0\n"
    "s_nop 0\n"
    "s_waitcnt vmcnt(12)\n"
    "v_or3_b32 v200, v200, v164, v165\n"
    "ds_read_b64_tr_b16 a[140:141], v227 offset:0\n"
    "ds_read_b64_tr_b16 a[144:145], v228 offset:0\n"
    "ds_read_b64_tr_b16 a[148:149], v229 offset:0\n"
    "ds_read_b64_tr_b16 a[152:153], v230 offset:0\n"
    "v_or3_b32 v200, v200, v166, v167\n"
    "ds_read_b64_tr_b16 a[156:157], v231 offset:0\n"
    "ds_read_b64_tr_b16 a[160:161], v224 offset:256\n"
    "ds_read_b64_tr_b16 a[164:165], v225 offset:256\n"
    "v_add_u32_e64 v164, v205, v164\n"
    "ds_read_b64_tr_b16 a[168:169], v226 offset:256\n"
    "ds_read_b64_tr_b16 a[172:173], v227 offset:256\n"
    "v_add_u32_e32 v166, v205, v166\n"
    "ds_write_b128 v217, v[164:167] offset:12800\n"
    "v_and_b32_e32 v200, 0x40004000, v200\n"
    "s_mov_b32 s65, 1277952\n"
    "buffer_load_dwordx4 a[212:215], v208, s[40:43], s65 offen\n"
    "s_waitcnt vmcnt(12)\n"
    "ds_write2_b32 v216, a244, a245 offset0:80 offset1:100\n"
    "ds_write2_b32 v216, a246, a247 offset0:120 offset1:140\n"
    "s_waitcnt lgkmcnt(2)\n"
    "s_barrier\n"
    "ds_read_b64_tr_b16 a[130:131], v224 offset:12288\n"
    "ds_read_b64_tr_b16 a[134:135], v225 offset:12288\n"
    "ds_read_b64_tr_b16 a[138:139], v226 offset:12288\n"
    "v_add_u32_e32 v216, 1360, v216\n"
    "ds_read_b64_tr_b16 a[142:143], v227 offset:12288\n"
    "ds_read_b64_tr_b16 a[146:147], v228 offset:12288\n"
    "ds_read_b64_tr_b16 a[150:151], v229 offset:12288\n"
    "ds_read_b64_tr_b16 a[154:155], v230 offset:12288\n"
    "ds_read_b64_tr_b16 a[158:159], v231 offset:12288\n"
    "ds_read_b64_tr_b16 a[162:163], v224 offset:12544\n"
    "ds_read_b64_tr_b16 a[166:167], v225 offset:12544\n"
    "ds_read_b64_tr_b16 a[170:171], v226 offset:12544\n"
    "ds_read_b64_tr_b16 a[174:175], v227 offset:12544\n"
    "s_mov_b32 s65, 1572864\n"
    "buffer_load_dwordx4 a[216:219], v208, s[40:43], s65 offen\n"
    "s_waitcnt vmcnt(12)\n"
    "ds_write2_b32 v216, a248, a249 offset0:0 offset1:20\n"
    "ds_write2_b32 v216, a250, a251 offset0:40 offset1:60\n"
    "s_mov_b32 s65, 1671168\n"
    "buffer_load_dwordx4 a[220:223], v208, s[40:43], s65 offen\n"
    "v_cmp_le_u32_e32 vcc, 1, v200\n"
    "s_nop 1\n"
    "s_cmp_lg_u64 vcc, 0\n"
    "s_cselect_b32 vcc_lo, 1, 0\n"
    "s_or_b32 s66, s66, vcc_lo\n"
    "v_cmp_le_u32_e32 vcc, 981467136, v232\n"
    "s_nop 1\n"
    "s_cmp_lg_u64 vcc, 0\n"
    "s_cselect_b32 vcc_lo, 2, 0\n"
    "s_or_b32 s66, s66, vcc_lo\n"
    "v_mov_b32_e32 v200, s66\n"
    "v_mov_b32_e32 v232, 162992\n"
    "s_mov_b64 exec, 1\n"
    "ds_or_b32 v232, v200\n"
    "s_mov_b64 exec, -1\n"
    "s_waitcnt vmcnt(12)\n"
    "ds_write2_b32 v216, a252, a253 offset0:80 offset1:100\n"
    "ds_write2_b32 v216, a254, a255 offset0:120 offset1:140\n"
    "v_add_u32_e32 v216, 1360, v216\n"
    "s_waitcnt lgkmcnt(2)\n"
    "s_barrier\n"
    "ds_read_b32 v232, v232\n"
    "s_nop 0\n"
    "s_add_u32 m0, s38, 0\n"
    "s_mov_b32 s64, 18432\n"
    "buffer_load_dwordx4 v210, s[84:87], s64 offen offset:0 lds\n"
    "s_waitcnt vmcnt(12)\n"
    "ds_write2_b32 v216, a176, a177 offset0:0 offset1:20\n"
    "ds_write2_b32 v216, a178, a179 offset0:40 offset1:60\n"
    "buffer_load_dwordx4 v210, s[84:87], s64 offen offset:1024 lds\n"
    "s_waitcnt vmcnt(12)\n"
    "ds_write2_b32 v216, a180, a181 offset0:80 offset1:100\n"
    "ds_write2_b32 v216, a182, a183 offset0:120 offset1:140\n"
    "v_add_u32_e32 v216, -4800, v216\n"
    "s_waitcnt lgkmcnt(0)\n"
    "s_barrier\n"
    "ds_read_b128 a[224:227], v250 offset:0\n"
    "ds_read_b128 a[228:231], v250 offset:320\n"
    "ds_read_b128 a[232:235], v251 offset:0\n"
    "ds_read_b128 a[236:239], v251 offset:336\n"
    "ds_read_b128 a[240:243], v250 offset:80\n"
    "buffer_load_dwordx4 v210, s[84:87], s64 offen offset:2048 lds\n"
    "ds_read_b128 a[244:247], v250 offset:400\n"
    "ds_read_b128 a[248:251], v251 offset:672\n"
    "ds_read_b128 a[252:255], v251 offset:1008\n"
    "s_waitcnt vmcnt(12)\n"
    "ds_write2_b32 v216, a184, a185 offset0:0 offset1:20\n"
    "ds_write2_b32 v216, a186, a187 offset0:40 offset1:60\n"
    "ds_read_b128 v[136:139], v252 offset:1344\n"
    "ds_read_b128 v[140:143], v252 offset:1680\n"
    "s_waitcnt lgkmcnt(9)\n"
    "v_smfmac_f32_16x16x64_bf16 v[0:3], a[232:235], a[224:231], v240\n"
    "ds_read_b128 a[232:235], v251 offset:1344\n"
    "s_waitcnt lgkmcnt(9)\n"
    "v_smfmac_f32_16x16x64_bf16 v[4:7], a[236:239], a[224:231], v240 abid:1\n"
    "s_nop 0\n"
    "ds_read_b128 a[224:227], v250 offset:160\n"
    "ds_read_b128 a[228:231], v250 offset:480\n"
    "ds_read_b128 a[236:239], v251 offset:1680\n"
    "s_waitcnt lgkmcnt(9)\n"
    "v_smfmac_f32_16x16x64_bf16 v[8:11], a[248:251], a[240:247], v240\n"
    "s_add_u32 m0, s38, 3072\n"
    "s_mov_b32 s64, 21504\n"
    "buffer_load_dwordx4 v210, s[84:87], s64 offen offset:0 lds\n"
    "s_waitcnt lgkmcnt(8)\n"
    "v_smfmac_f32_16x16x64_bf16 v[12:15], a[252:255], a[240:247], v240 abid:1\n"
    "ds_read_b128 a[240:243], v250 offset:240\n"
    "ds_read_b128 a[244:247], v250 offset:560\n"
    "ds_read_b128 a[248:251], v251 offset:2016\n"
    "ds_read_b128 a[252:255], v251 offset:2352\n"
    "s_waitcnt vmcnt(12)\n"
    "ds_write2_b32 v216, a188, a189 offset0:80 offset1:100\n"
    "ds_write2_b32 v216, a190, a191 offset0:120 offset1:140\n"
    "v_add_u32_e32 v216, 1360, v216\n"
    "s_waitcnt lgkmcnt(7)\n"
    "v_smfmac_f32_16x16x64_bf16 v[16:19], a[232:235], a[224:231], v240\n"
    "ds_read_b128 a[232:235], v251 offset:10752\n"
    "s_waitcnt lgkmcnt(7)\n"
    "v_smfmac_f32_16x16x64_bf16 v[20:23], a[236:239], a[224:231], v240 abid:1\n"
    "s_nop 0\n"
    "ds_read_b128 a[224:227], v250 offset:27024\n"
    "ds_read_b128 a[228:231], v250 offset:27344\n"
    "ds_read_b128 a[236:239], v251 offset:11088\n"
    "s_waitcnt lgkmcnt(7)\n"
    "v_smfmac_f32_16x16x64_bf16 v[24:27], a[248:251], a[240:247], v240\n"
    "s_nop 0\n"
    "ds_read_b128 a[248:251], v251 offset:11424\n"
    "buffer_load_dwordx4 v210, s[84:87], s64 offen offset:1024 lds\n"
    "s_waitcnt lgkmcnt(7)\n"
    "v_smfmac_f32_16x16x64_bf16 v[28:31], a[252:255], a[240:247], v240 abid:1\n"
    "s_nop 0\n"
    "ds_read_b128 a[240:243], v250 offset:27104\n"
    "ds_read_b128 a[244:247], v250 offset:27424\n"
    "ds_read_b128 a[252:255], v251 offset:11760\n"
    "s_waitcnt vmcnt(12)\n"
    "ds_write2_b32 v216, a192, a193 offset0:0 offset1:20\n"
    "ds_write2_b32 v216, a194, a195 offset0:40 offset1:60\n"
    "s_waitcnt lgkmcnt(7)\n"
    "v_smfmac_f32_16x16x64_bf16 v[32:35], a[232:235], a[224:231], v240\n"
    "ds_read_b128 a[232:235], v251 offset:12096\n"
    "s_waitcnt lgkmcnt(7)\n"
    "v_smfmac_f32_16x16x64_bf16 v[36:39], a[236:239], a[224:231], v240 abid:1\n"
    "s_nop 0\n"
    "ds_read_b128 a[224:227], v250 offset:27184\n"
    "ds_read_b128 a[228:231], v250 offset:27504\n"
    "ds_read_b128 a[236:239], v251 offset:12432\n"
    "s_waitcnt lgkmcnt(7)\n"
    "v_smfmac_f32_16x16x64_bf16 v[40:43], a[248:251], a[240:247], v240\n"
    "ds_read_b128 a[248:251], v251 offset:12768\n"
    "s_waitcnt lgkmcnt(7)\n"
    "v_smfmac_f32_16x16x64_bf16 v[44:47], a[252:255], a[240:247], v240 abid:1\n"
    "ds_read_b128 a[240:243], v250 offset:27264\n"
    "ds_read_b128 a[244:247], v250 offset:27584\n"
    "ds_read_b128 a[252:255], v251 offset:13104\n"
    "buffer_load_dwordx4 v210, s[84:87], s64 offen offset:2048 lds\n"
    "s_waitcnt vmcnt(12)\n"
    "ds_write2_b32 v216, a196, a197 offset0:80 offset1:100\n"
    "ds_write2_b32 v216, a198, a199 offset0:120 offset1:140\n"
    "v_add_u32_e32 v216, 1360, v216\n"
    "s_waitcnt lgkmcnt(7)\n"
    "v_smfmac_f32_16x16x64_bf16 v[48:51], a[232:235], a[224:231], v240\n"
    "ds_read_b128 a[232:235], v251 offset:21504\n"
    "s_waitcnt lgkmcnt(7)\n"
    "v_smfmac_f32_16x16x64_bf16 v[52:55], a[236:239], a[224:231], v240 abid:1\n"
    "s_nop 0\n"
    "ds_read_b128 a[224:227], v250 offset:54048\n"
    "ds_read_b128 a[228:231], v250 offset:54368\n"
    "ds_read_b128 a[236:239], v251 offset:21840\n"
    "s_waitcnt lgkmcnt(7)\n"
    "v_smfmac_f32_16x16x64_bf16 v[56:59], a[248:251], a[240:247], v240\n"
    "ds_read_b128 a[248:251], v251 offset:22176\n"
    "s_waitcnt lgkmcnt(7)\n"
    "v_smfmac_f32_16x16x64_bf16 v[60:63], a[252:255], a[240:247], v240 abid:1\n"
    "ds_read_b128 a[240:243], v250 offset:54128\n"
    "ds_read_b128 a[244:247], v250 offset:54448\n"
    "ds_read_b128 a[252:255], v251 offset:22512\n"
    "s_add_u32 m0, s38, 6144\n"
    "s_mov_b32 s64, 24576\n"
    "buffer_load_dwordx4 v210, s[84:87], s64 offen offset:0 lds\n"
    "s_waitcnt vmcnt(12)\n"
    "ds_write2_b32 v216, a200, a201 offset0:0 offset1:20\n"
    "s_waitcnt lgkmcnt(6)\n"
    "v_smfmac_f32_16x16x64_bf16 v[64:67], a[232:235], a[224:231], v240\n"
    "ds_read_b128 a[232:235], v251 offset:22848\n"
    "s_waitcnt lgkmcnt(6)\n"
    "v_smfmac_f32_16x16x64_bf16 v[68:71], a[236:239], a[224:231], v240 abid:1\n"
    "s_nop 0\n"
    "ds_read_b128 a[224:227], v250 offset:54208\n"
    "ds_read_b128 a[228:231], v250 offset:54528\n"
    "ds_read_b128 a[236:239], v251 offset:23184\n"
    "ds_write2_b32 v216, a202, a203 offset0:40 offset1:60\n"
    "s_waitcnt lgkmcnt(7)\n"
    "v_smfmac_f32_16x16x64_bf16 v[72:75], a[248:251], a[240:247], v240\n"
    "ds_read_b128 a[248:251], v251 offset:23520\n"
    "s_waitcnt lgkmcnt(7)\n"
    "v_smfmac_f32_16x16x64_bf16 v[76:79], a[252:255], a[240:247], v240 abid:1\n"
    "ds_read_b128 a[240:243], v250 offset:54288\n"
    "ds_read_b128 a[244:247], v250 offset:54608\n"
    "ds_read_b128 a[252:255], v251 offset:23856\n"
    "buffer_load_dwordx4 v210, s[84:87], s64 offen offset:1024 lds\n"
    "s_waitcnt lgkmcnt(6)\n"
    "v_smfmac_f32_16x16x64_bf16 v[80:83], a[232:235], a[224:231], v240\n"
    "s_waitcnt lgkmcnt(5)\n"
    "s_waitcnt vmcnt(12)\n"
    "ds_write2_b32 v216, a204, a205 offset0:80 offset1:100\n"
    "v_smfmac_f32_16x16x64_bf16 v[84:87], a[236:239], a[224:231], v240 abid:1\n"
    "s_nop 0\n"
    "ds_write2_b32 v216, a206, a207 offset0:120 offset1:140\n"
    "v_add_u32_e32 v216, 1360, v216\n"
    "ds_read_b128 a[232:235], v252 offset:0\n"
    "s_waitcnt lgkmcnt(4)\n"
    "v_smfmac_f32_16x16x64_bf16 v[88:91], a[248:251], a[240:247], v240\n"
    "s_waitcnt lgkmcnt(3)\n"
    "ds_read_b128 a[236:239], v252 offset:336\n"
    "v_smfmac_f32_16x16x64_bf16 v[92:95], a[252:255], a[240:247], v240 abid:1\n"
    "ds_read_b128 a[248:251], v252 offset:672\n"
    "ds_read_b128 a[252:255], v252 offset:1008\n"
    "buffer_load_dwordx4 v210, s[84:87], s64 offen offset:2048 lds\n"
    "s_waitcnt vmcnt(12)\n"
    "ds_write2_b32 v216, a208, a209 offset0:0 offset1:20\n"
    "s_mov_b32 s64, 0\n"
    "s_mov_b32 s59, 0\n"
    "ds_write2_b32 v216, a210, a211 offset0:40 offset1:60\n"
    "v_lshlrev_b32_e64 v235, 3, v244\n"
    "s_mov_b32 exec_hi, 0\n"
    "buffer_load_dword v233, v235, s[40:43], s53 offen sc1\n"
    "s_mov_b32 exec_hi, -1\n"
    "s_add_u32 s53, s53, 393216\n"
    "s_waitcnt vmcnt(12)\n"
    "ds_write2_b32 v216, a212, a213 offset0:80 offset1:100\n"
    "ds_write2_b32 v216, a214, a215 offset0:120 offset1:140\n"
    "v_add_u32_e32 v216, 1360, v216\n"
    "v_lshlrev_b32_e32 v235, 3, v244\n"
    "s_mov_b32 exec_hi, 0\n"
    "buffer_load_dword v233, v235, s[40:43], s53 offen sc1\n"
    "s_mov_b32 exec_hi, -1\n"
    "s_add_u32 s53, s53, 393216\n"
    "s_waitcnt vmcnt(12)\n"
    "ds_write2_b32 v216, a216, a217 offset0:0 offset1:20\n"
    "ds_write2_b32 v216, a218, a219 offset0:40 offset1:60\n"
    "v_lshlrev_b32_e32 v235, 3, v244\n"
    "s_mov_b32 exec_hi, 0\n"
    "buffer_load_dword v233, v235, s[40:43], s53 offen sc1\n"
    "s_mov_b32 exec_hi, -1\n"
    "s_add_u32 s53, s53, 393216\n"
    "s_waitcnt vmcnt(12)\n"
    "ds_write2_b32 v216, a220, a221 offset0:80 offset1:100\n"
    "ds_write2_b32 v216, a222, a223 offset0:120 offset1:140\n"
    "v_lshlrev_b32_e64 v235, 3, v244\n"
    "s_mov_b32 exec_hi, 0\n"
    "buffer_load_dword v233, v235, s[40:43], s53 offen sc1\n"
    "s_mov_b32 exec_hi, -1\n"
    "s_add_u32 s53, s53, 393216\n"
    "v_mov_b32_e64 v235, v222\n"
    "s_waitcnt lgkmcnt(0)\n"
    "s_barrier\n"
    "ds_read_b128 a[224:227], v250 offset:640\n"
    "ds_read_b128 a[228:231], v250 offset:960\n"
    "ds_read_b128 a[240:243], v250 offset:720\n"
    "ds_read_b128 a[244:247], v250 offset:1040\n"
    "ds_read_b128 v[128:131], v250 offset:800\n"
    "ds_read_b128 v[132:135], v250 offset:1120\n"
    "s_waitcnt lgkmcnt(4)\n"
    "v_smfmac_f32_16x16x64_bf16 v[4:7], a[236:239], a[224:231], v240 abid:1\n"
    "s_nop 0\n"
    "ds_read_b128 a[236:239], v252 offset:2352\n"
    "v_smfmac_f32_16x16x64_bf16 v[0:3], a[232:235], a[224:231], v240\n"
    "ds_read_b128 a[224:227], v250 offset:880\n"
    "s_waitcnt lgkmcnt(4)\n"
    "v_smfmac_f32_16x16x64_bf16 v[8:11], a[248:251], a[240:247], v240\n"
    "ds_read_b128 a[228:231], v250 offset:1200\n"
    "v_smfmac_f32_16x16x64_bf16 v[12:15], a[252:255], a[240:247], v240 abid:1\n"
    "s_waitcnt lgkmcnt(3)\n"
    "ds_read_b128 a[232:235], v252 offset:2016\n"
    "v_smfmac_f32_16x16x64_bf16 v[16:19], v[136:139], v[128:135], v240\n"
    "ds_read_b128 a[240:243], v250 offset:27664\n"
    "v_smfmac_f32_16x16x64_bf16 v[20:23], v[140:143], v[128:135], v240 abid:1\n"
    "ds_read_b128 a[244:247], v250 offset:27984\n"
    "ds_read_b128 a[248:251], v252 offset:10752\n"
    "ds_read_b128 a[252:255], v252 offset:11088\n"
    "v_pk_mov_b32 v[224:225], v[0:1], v[0:1] op_sel:[0,1]\n"
    "v_pk_mov_b32 v[226:227], v[2:3], v[2:3] op_sel:[0,1]\n"
    "v_pk_mul_f32 v[228:229], v[0:1], v[0:1]\n"
    "v_pk_mul_f32 v[230:231], v[2:3], v[2:3]\n"
    "v_pk_add_f32 v[224:225], v[224:225], v[4:5]\n"
    "v_pk_add_f32 v[226:227], v[226:227], v[6:7]\n"
    "v_pk_fma_f32 v[228:229], v[4:5], v[4:5], v[228:229]\n"
    "v_pk_fma_f32 v[230:231], v[6:7], v[6:7], v[230:231]\n"
    "v_pk_add_f32 v[224:225], v[224:225], v[8:9]\n"
    "v_pk_add_f32 v[226:227], v[226:227], v[10:11]\n"
    "v_pk_fma_f32 v[228:229], v[8:9], v[8:9], v[228:229]\n"
    "v_pk_fma_f32 v[230:231], v[10:11], v[10:11], v[230:231]\n"
    "v_pk_add_f32 v[224:225], v[224:225], v[12:13]\n"
    "v_pk_add_f32 v[226:227], v[226:227], v[14:15]\n"
    "v_pk_fma_f32 v[228:229], v[12:13], v[12:13], v[228:229]\n"
    "v_pk_fma_f32 v[230:231], v[14:15], v[14:15], v[230:231]\n"
    "v_pk_add_f32 v[224:225], v[224:225], v[16:17]\n"
    "v_pk_add_f32 v[226:227], v[226:227], v[18:19]\n"
    "v_pk_fma_f32 v[228:229], v[16:17], v[16:17], v[228:229]\n"
    "v_pk_fma_f32 v[230:231], v[18:19], v[18:19], v[230:231]\n"
    "v_pk_add_f32 v[224:225], v[224:225], v[20:21]\n"
    "v_pk_add_f32 v[226:227], v[226:227], v[22:23]\n"
    "v_pk_fma_f32 v[228:229], v[20:21], v[20:21], v[228:229]\n"
    "v_pk_fma_f32 v[230:231], v[22:23], v[22:23], v[230:231]\n"
    "s_waitcnt lgkmcnt(3)\n"
    "v_smfmac_f32_16x16x64_bf16 v[24:27], a[232:235], a[224:231], v240\n"
    "s_nop 0\n"
    "ds_read_b128 v[128:131], v250 offset:27744\n"
    "v_smfmac_f32_16x16x64_bf16 v[28:31], a[236:239], a[224:231], v240 abid:1\n"
    "s_waitcnt lgkmcnt(2)\n"
    "ds_read_b128 v[132:135], v250 offset:28064\n"
    "v_smfmac_f32_16x16x64_bf16 v[32:35], a[248:251], a[240:247], v240\n"
    "s_waitcnt lgkmcnt(2)\n"
    "ds_read_b128 v[136:139], v252 offset:11424\n"
    "v_smfmac_f32_16x16x64_bf16 v[36:39], a[252:255], a[240:247], v240 abid:1\n"
    "ds_read_b128 v[140:143], v252 offset:11760\n"
    "ds_read_b128 a[224:227], v250 offset:27824\n"
    "ds_read_b128 a[228:231], v250 offset:28144\n"
    "v_pk_add_f32 v[224:225], v[224:225], v[24:25]\n"
    "v_pk_add_f32 v[226:227], v[226:227], v[26:27]\n"
    "v_pk_fma_f32 v[228:229], v[24:25], v[24:25], v[228:229]\n"
    "v_pk_fma_f32 v[230:231], v[26:27], v[26:27], v[230:231]\n"
    "v_pk_add_f32 v[224:225], v[224:225], v[28:29]\n"
    "v_pk_add_f32 v[226:227], v[226:227], v[30:31]\n"
    "v_pk_fma_f32 v[228:229], v[28:29], v[28:29], v[228:229]\n"
    "v_pk_fma_f32 v[230:231], v[30:31], v[30:31], v[230:231]\n"
    "v_pk_add_f32 v[224:225], v[224:225], v[32:33]\n"
    "v_pk_add_f32 v[226:227], v[226:227], v[34:35]\n"
    "v_pk_fma_f32 v[228:229], v[32:33], v[32:33], v[228:229]\n"
    "v_pk_fma_f32 v[230:231], v[34:35], v[34:35], v[230:231]\n"
    "v_pk_add_f32 v[224:225], v[224:225], v[36:37]\n"
    "v_pk_add_f32 v[226:227], v[226:227], v[38:39]\n"
    "v_pk_fma_f32 v[228:229], v[36:37], v[36:37], v[228:229]\n"
    "v_pk_fma_f32 v[230:231], v[38:39], v[38:39], v[230:231]\n"
    "ds_read_b128 a[232:235], v252 offset:12096\n"
    "ds_read_b128 a[236:239], v252 offset:12432\n"
    "ds_read_b128 a[240:243], v250 offset:27904\n"
    "ds_read_b128 a[244:247], v250 offset:28224\n"
    "ds_read_b128 a[248:251], v252 offset:12768\n"
    "s_waitcnt lgkmcnt(8)\n"
    "v_smfmac_f32_16x16x64_bf16 v[40:43], v[136:139], v[128:135], v240\n"
    "s_waitcnt lgkmcnt(7)\n"
    "ds_read_b128 a[252:255], v252 offset:13104\n"
    "v_smfmac_f32_16x16x64_bf16 v[44:47], v[140:143], v[128:135], v240 abid:1\n"
    "ds_read_b128 v[128:131], v250 offset:54688\n"
    "ds_read_b128 v[132:135], v250 offset:55008\n"
    "ds_read_b128 v[136:139], v252 offset:21504\n"
    "s_waitcnt vmcnt(4)\n"
    "s_barrier\n"
    "v_pk_add_f32 v[224:225], v[224:225], v[40:41]\n"
    "v_pk_add_f32 v[226:227], v[226:227], v[42:43]\n"
    "v_pk_fma_f32 v[228:229], v[40:41], v[40:41], v[228:229]\n"
    "v_pk_fma_f32 v[230:231], v[42:43], v[42:43], v[230:231]\n"
    "v_pk_add_f32 v[224:225], v[224:225], v[44:45]\n"
    "v_pk_add_f32 v[226:227], v[226:227], v[46:47]\n"
    "v_pk_fma_f32 v[228:229], v[44:45], v[44:45], v[228:229]\n"
    "v_pk_fma_f32 v[230:231], v[46:47], v[46:47], v[230:231]\n"
    "s_waitcnt lgkmcnt(8)\n"
    "v_smfmac_f32_16x16x64_bf16 v[48:51], a[232:235], a[224:231], v240\n"
    "s_waitcnt lgkmcnt(7)\n"
    "ds_read_b128 v[140:143], v252 offset:21840\n"
    "v_smfmac_f32_16x16x64_bf16 v[52:55], a[236:239], a[224:231], v240 abid:1\n"
    "ds_read_b128 a[224:227], v250 offset:54768\n"
    "ds_read_b128 a[228:231], v250 offset:55088\n"
    "ds_read_b128 a[232:235], v252 offset:22176\n"
    "ds_read_b128 a[236:239], v252 offset:22512\n"
    "s_waitcnt lgkmcnt(9)\n"
    "v_smfmac_f32_16x16x64_bf16 v[56:59], a[248:251], a[240:247], v240\n"
    "s_waitcnt lgkmcnt(8)\n"
    "ds_read_b128 a[248:251], v252 offset:22848\n"
    "v_smfmac_f32_16x16x64_bf16 v[60:63], a[252:255], a[240:247], v240 abid:1\n"
    "ds_read_b128 a[240:243], v250 offset:54848\n"
    "ds_read_b128 a[244:247], v250 offset:55168\n"
    "ds_read_b128 a[252:255], v252 offset:23184\n"
    "v_pk_add_f32 v[224:225], v[224:225], v[48:49]\n"
    "v_pk_add_f32 v[226:227], v[226:227], v[50:51]\n"
    "v_pk_fma_f32 v[228:229], v[48:49], v[48:49], v[228:229]\n"
    "v_pk_fma_f32 v[230:231], v[50:51], v[50:51], v[230:231]\n"
    "v_pk_add_f32 v[224:225], v[224:225], v[52:53]\n"
    "v_pk_add_f32 v[226:227], v[226:227], v[54:55]\n"
    "v_pk_fma_f32 v[228:229], v[52:53], v[52:53], v[228:229]\n"
    "v_pk_fma_f32 v[230:231], v[54:55], v[54:55], v[230:231]\n"
    "v_pk_add_f32 v[224:225], v[224:225], v[56:57]\n"
    "v_pk_add_f32 v[226:227], v[226:227], v[58:59]\n"
    "v_pk_fma_f32 v[228:229], v[56:57], v[56:57], v[228:229]\n"
    "v_pk_fma_f32 v[230:231], v[58:59], v[58:59], v[230:231]\n"
    "v_pk_add_f32 v[224:225], v[224:225], v[60:61]\n"
    "v_pk_add_f32 v[226:227], v[226:227], v[62:63]\n"
    "v_pk_fma_f32 v[228:229], v[60:61], v[60:61], v[228:229]\n"
    "v_pk_fma_f32 v[230:231], v[62:63], v[62:63], v[230:231]\n"
    "s_waitcnt lgkmcnt(7)\n"
    "v_smfmac_f32_16x16x64_bf16 v[64:67], v[136:139], v[128:135], v240\n"
    "s_nop 0\n"
    "ds_read_b128 v[136:139], v252 offset:23520\n"
    "v_smfmac_f32_16x16x64_bf16 v[68:71], v[140:143], v[128:135], v240 abid:1\n"
    "s_waitcnt lgkmcnt(6)\n"
    "ds_read_b128 v[128:131], v250 offset:54928\n"
    "v_smfmac_f32_16x16x64_bf16 v[72:75], a[232:235], a[224:231], v240\n"
    "s_waitcnt lgkmcnt(6)\n"
    "ds_read_b128 v[132:135], v250 offset:55248\n"
    "v_smfmac_f32_16x16x64_bf16 v[76:79], a[236:239], a[224:231], v240 abid:1\n"
    "ds_read_b128 v[140:143], v252 offset:23856\n"
    "ds_read_b64_tr_b16 a[176:177], v220 offset:0\n"
    "ds_read_b64_tr_b16 a[178:179], v220 offset:3072\n"
    "v_pk_add_f32 v[224:225], v[224:225], v[64:65]\n"
    "v_pk_add_f32 v[226:227], v[226:227], v[66:67]\n"
    "v_pk_fma_f32 v[228:229], v[64:65], v[64:65], v[228:229]\n"
    "v_pk_fma_f32 v[230:231], v[66:67], v[66:67], v[230:231]\n"
    "v_pk_add_f32 v[224:225], v[224:225], v[68:69]\n"
    "v_pk_add_f32 v[226:227], v[226:227], v[70:71]\n"
    "v_pk_fma_f32 v[228:229], v[68:69], v[68:69], v[228:229]\n"
    "v_pk_fma_f32 v[230:231], v[70:71], v[70:71], v[230:231]\n"
    "v_pk_add_f32 v[224:225], v[224:225], v[72:73]\n"
    "v_pk_add_f32 v[226:227], v[226:227], v[74:75]\n"
    "v_pk_fma_f32 v[228:229], v[72:73], v[72:73], v[228:229]\n"
    "v_pk_fma_f32 v[230:231], v[74:75], v[74:75], v[230:231]\n"
    "v_pk_add_f32 v[224:225], v[224:225], v[76:77]\n"
    "v_pk_add_f32 v[226:227], v[226:227], v[78:79]\n"
    "v_pk_fma_f32 v[228:229], v[76:77], v[76:77], v[228:229]\n"
    "v_pk_fma_f32 v[230:231], v[78:79], v[78:79], v[230:231]\n"
    "s_waitcnt lgkmcnt(5)\n"
    "v_smfmac_f32_16x16x64_bf16 v[80:83], a[248:251], a[240:247], v240\n"
    "s_nop 0\n"
    "v_accvgpr_write_b32 a248, v236\n"
    "v_smfmac_f32_16x16x64_bf16 v[84:87], a[252:255], a[240:247], v240 abid:1\n"
    "s_waitcnt lgkmcnt(3)\n"
    "v_accvgpr_write_b32 a249, v237\n"
    "v_smfmac_f32_16x16x64_bf16 v[88:91], v[136:139], v[128:135], v240\n"
    "s_waitcnt lgkmcnt(2)\n"
    "v_accvgpr_write_b32 a250, v238\n"
    "v_smfmac_f32_16x16x64_bf16 v[92:95], v[140:143], v[128:135], v240 abid:1\n"
    "v_accvgpr_write_b32 a251, v239\n"
    "ds_read_b64_tr_b16 a[180:181], v220 offset:8\n"
    "v_pk_add_f32 v[224:225], v[224:225], v[80:81]\n"
    "v_pk_add_f32 v[226:227], v[226:227], v[82:83]\n"
    "v_pk_fma_f32 v[228:229], v[80:81], v[80:81], v[228:229]\n"
    "v_pk_fma_f32 v[230:231], v[82:83], v[82:83], v[230:231]\n"
    "v_pk_add_f32 v[224:225], v[224:225], v[84:85]\n"
    "v_pk_add_f32 v[226:227], v[226:227], v[86:87]\n"
    "v_pk_fma_f32 v[228:229], v[84:85], v[84:85], v[228:229]\n"
    "v_pk_fma_f32 v[230:231], v[86:87], v[86:87], v[230:231]\n"
    "v_pk_add_f32 v[224:225], v[224:225], v[88:89]\n"
    "v_pk_add_f32 v[226:227], v[226:227], v[90:91]\n"
    "v_pk_fma_f32 v[228:229], v[88:89], v[88:89], v[228:229]\n"
    "v_pk_fma_f32 v[230:231], v[90:91], v[90:91], v[230:231]\n"
    "v_pk_add_f32 v[224:225], v[224:225], v[92:93]\n"
    "v_pk_add_f32 v[226:227], v[226:227], v[94:95]\n"
    "v_pk_fma_f32 v[228:229], v[92:93], v[92:93], v[228:229]\n"
    "v_pk_fma_f32 v[230:231], v[94:95], v[94:95], v[230:231]\n"
    "ds_write_b128 v254, v[224:227]\n"
    "ds_write_b128 v254, v[228:231] offset:1024\n"
    "v_lshrrev_b32_e64 v254, 2, v242\n"
    "v_add_u32_e64 v254, s55, v254\n"
    "ds_read_b64_tr_b16 a[182:183], v220 offset:3080\n"
    "ds_read_b64_tr_b16 a[184:185], v220 offset:64\n"
    "ds_read_b64_tr_b16 a[186:187], v220 offset:3136\n"
    "ds_read_b64_tr_b16 a[188:189], v220 offset:72\n"
    "ds_read_b64_tr_b16 a[190:191], v220 offset:3144\n"
    "ds_read_b64_tr_b16 v[188:189], v218 offset:128\n"
    "ds_read_b64_tr_b16 v[190:191], v218 offset:3200\n"
    "ds_read_b64_tr_b16 a[192:193], v220 offset:6144\n"
    "ds_read_b64_tr_b16 a[194:195], v220 offset:9216\n"
    "ds_read_b64_tr_b16 a[196:197], v220 offset:6152\n"
    "ds_read_b64_tr_b16 a[198:199], v220 offset:9224\n"
    "s_waitcnt lgkmcnt(11)\n"
    "s_barrier\n"
    "ds_read_b32 v224, v254 offset:0\n"
    "ds_read_b32 v225, v254 offset:6752\n"
    "ds_read_b32 v226, v254 offset:13504\n"
    "ds_read_b32 v227, v254 offset:20256\n"
    "ds_read_b32 v228, v254 offset:1024\n"
    "ds_read_b32 v229, v254 offset:7776\n"
    "ds_read_b32 v230, v254 offset:14528\n"
    "ds_read_b32 v231, v254 offset:21280\n"
    "ds_read_b64_tr_b16 a[200:201], v220 offset:6208\n"
    "ds_read_b64_tr_b16 a[202:203], v220 offset:9280\n"
    "ds_read_b64_tr_b16 a[204:205], v220 offset:6216\n"
    "ds_read_b64_tr_b16 a[206:207], v220 offset:9288\n"
    "ds_read_b64_tr_b16 v[192:193], v218 offset:6272\n"
    "ds_read_b64_tr_b16 v[194:195], v218 offset:9344\n"
    "ds_read_b64_tr_b16 a[208:209], v220 offset:12288\n"
    "ds_read_b64_tr_b16 a[210:211], v220 offset:15360\n"
    "ds_read_b64_tr_b16 a[212:213], v220 offset:12296\n"
    "s_waitcnt lgkmcnt(15)\n"
    "v_add_f32_e32 v224, v224, v225\n"
    "ds_read_b64_tr_b16 a[214:215], v220 offset:15368\n"
    "s_waitcnt lgkmcnt(14)\n"
    "v_add_f32_e32 v226, v226, v227\n"
    "v_add_f32_e64 v224, v224, v226\n"
    "v_mul_f32_e32 v224, 0x3c2aaaab, v224\n"
    "s_waitcnt lgkmcnt(12)\n"
    "v_add_f32_e32 v228, v228, v229\n"
    "ds_read_b64_tr_b16 a[216:217], v220 offset:12352\n"
    "s_waitcnt lgkmcnt(11)\n"
    "v_add_f32_e32 v230, v230, v231\n"
    "v_add_f32_e64 v228, v228, v230\n"
    "v_mul_f32_e32 v228, 0x3c2aaaab, v228\n"
    "v_fma_f32 v228, -v224, v224, v228\n"
    "v_max_f32_e64 v228, 0, v228\n"
    "v_add_f32_e32 v228, 0x3727c5ac, v228\n"
    "v_rsq_f32_e64 v228, v228\n"
    "ds_read_b64_tr_b16 a[218:219], v220 offset:15424\n"
    "v_mul_f32_e64 v228, s50, v228\n"
    "v_mul_f32_e64 v224, v228, -v224\n"
    "ds_write_b32 v254, v228\n"
    "ds_write_b32 v254, v224 offset:1024\n"
    "v_add_u32_e64 v254, s56, v242\n"
    "ds_read_b64_tr_b16 a[220:221], v220 offset:12360\n"
    "ds_read_b64_tr_b16 a[222:223], v220 offset:15432\n"
    "ds_read_b64_tr_b16 v[196:197], v218 offset:12416\n"
    "ds_read_b64_tr_b16 v[198:199], v218 offset:15488\n"
    "s_waitcnt lgkmcnt(4)\n"
    "s_barrier\n"
    "ds_read_b128 v[224:227], v254\n"
    "ds_read_b128 v[228:231], v254 offset:1024\n"
    "s_waitcnt lgkmcnt(2)\n"
    "s_barrier\n"
    "ds_write_b128 v234, v[112:115] offset:0\n"
    "ds_write_b128 v234, v[116:119] offset:16\n"
    "s_nop 0\n"
    "s_waitcnt lgkmcnt(2)\n"
    "v_pk_fma_f32 v[64:65], v[64:65], v[224:225], v[228:229]\n"
    "v_pk_fma_f32 v[66:67], v[66:67], v[226:227], v[230:231]\n"
    "v_pk_fma_f32 v[68:69], v[68:69], v[224:225], v[228:229]\n"
    "v_pk_fma_f32 v[70:71], v[70:71], v[226:227], v[230:231]\n"
    "v_pk_fma_f32 v[72:73], v[72:73], v[224:225], v[228:229]\n"
    "v_pk_fma_f32 v[74:75], v[74:75], v[226:227], v[230:231]\n"
    "v_pk_fma_f32 v[76:77], v[76:77], v[224:225], v[228:229]\n"
    "v_pk_fma_f32 v[78:79], v[78:79], v[226:227], v[230:231]\n"
    "v_pk_fma_f32 v[80:81], v[80:81], v[224:225], v[228:229]\n"
    "v_pk_fma_f32 v[82:83], v[82:83], v[226:227], v[230:231]\n"
    "v_pk_fma_f32 v[84:85], v[84:85], v[224:225], v[228:229]\n"
    "v_pk_fma_f32 v[86:87], v[86:87], v[226:227], v[230:231]\n"
    "v_pk_fma_f32 v[88:89], v[88:89], v[224:225], v[228:229]\n"
    "v_pk_fma_f32 v[90:91], v[90:91], v[226:227], v[230:231]\n"
    "v_pk_fma_f32 v[92:93], v[92:93], v[224:225], v[228:229]\n"
    "v_pk_fma_f32 v[94:95], v[94:95], v[226:227], v[230:231]\n"
    "v_cvt_pk_bf16_f32 v96, v64, v68\n"
    "v_cvt_pk_bf16_f32 v100, v65, v69\n"
    "v_cvt_pk_bf16_f32 v104, v66, v70\n"
    "v_cvt_pk_bf16_f32 v108, v67, v71\n"
    "v_cvt_pk_bf16_f32 v97, v72, v76\n"
    "v_cvt_pk_bf16_f32 v101, v73, v77\n"
    "v_cvt_pk_bf16_f32 v105, v74, v78\n"
    "v_cvt_pk_bf16_f32 v109, v75, v79\n"
    "v_cvt_pk_bf16_f32 v98, v80, v84\n"
    "v_cvt_pk_bf16_f32 v102, v81, v85\n"
    "v_cvt_pk_bf16_f32 v106, v82, v86\n"
    "v_cvt_pk_bf16_f32 v110, v83, v87\n"
    "v_cvt_pk_bf16_f32 v99, v88, v92\n"
    "ds_write_b128 v253, v[96:99] offset:2048\n"
    "v_cvt_pk_bf16_f32 v103, v89, v93\n"
    "v_cvt_pk_bf16_f32 v107, v90, v94\n"
    "v_cvt_pk_bf16_f32 v111, v91, v95\n"
    "v_pk_fma_f32 v[0:1], v[0:1], v[224:225], v[228:229]\n"
    "v_pk_fma_f32 v[2:3], v[2:3], v[226:227], v[230:231]\n"
    "v_pk_fma_f32 v[4:5], v[4:5], v[224:225], v[228:229]\n"
    "ds_write_b128 v253, v[100:103] offset:5120\n"
    "v_pk_fma_f32 v[6:7], v[6:7], v[226:227], v[230:231]\n"
    "v_pk_fma_f32 v[8:9], v[8:9], v[224:225], v[228:229]\n"
    "v_pk_fma_f32 v[10:11], v[10:11], v[226:227], v[230:231]\n"
    "v_pk_fma_f32 v[12:13], v[12:13], v[224:225], v[228:229]\n"
    "v_pk_fma_f32 v[14:15], v[14:15], v[226:227], v[230:231]\n"
    "v_pk_fma_f32 v[16:17], v[16:17], v[224:225], v[228:229]\n"
    "v_pk_fma_f32 v[18:19], v[18:19], v[226:227], v[230:231]\n"
    "v_pk_fma_f32 v[20:21], v[20:21], v[224:225], v[228:229]\n"
    "v_pk_fma_f32 v[22:23], v[22:23], v[226:227], v[230:231]\n"
    "v_pk_fma_f32 v[24:25], v[24:25], v[224:225], v[228:229]\n"
    "v_pk_fma_f32 v[26:27], v[26:27], v[226:227], v[230:231]\n"
    "v_pk_fma_f32 v[28:29], v[28:29], v[224:225], v[228:229]\n"
    "v_pk_fma_f32 v[30:31], v[30:31], v[226:227], v[230:231]\n"
    "v_cvt_pk_bf16_f32 v80, v0, v4\n"
    "v_cvt_pk_bf16_f32 v84, v1, v5\n"
    "v_cvt_pk_bf16_f32 v88, v2, v6\n"
    "v_cvt_pk_bf16_f32 v92, v3, v7\n"
    "v_cvt_pk_bf16_f32 v81, v8, v12\n"
    "v_cvt_pk_bf16_f32 v85, v9, v13\n"
    "v_cvt_pk_bf16_f32 v89, v10, v14\n"
    "v_cvt_pk_bf16_f32 v93, v11, v15\n"
    "v_cvt_pk_bf16_f32 v82, v16, v20\n"
    "v_cvt_pk_bf16_f32 v86, v17, v21\n"
    "v_cvt_pk_bf16_f32 v90, v18, v22\n"
    "v_cvt_pk_bf16_f32 v94, v19, v23\n"
    "v_cvt_pk_bf16_f32 v83, v24, v28\n"
    "ds_write_b128 v253, v[80:83] offset:0\n"
    "v_cvt_pk_bf16_f32 v87, v25, v29\n"
    "v_cvt_pk_bf16_f32 v91, v26, v30\n"
    "v_cvt_pk_bf16_f32 v95, v27, v31\n"
    "v_pk_fma_f32 v[32:33], v[32:33], v[224:225], v[228:229]\n"
    "v_pk_fma_f32 v[34:35], v[34:35], v[226:227], v[230:231]\n"
    "v_pk_fma_f32 v[36:37], v[36:37], v[224:225], v[228:229]\n"
    "ds_write_b128 v253, v[84:87] offset:3072\n"
    "v_pk_fma_f32 v[38:39], v[38:39], v[226:227], v[230:231]\n"
    "v_pk_fma_f32 v[40:41], v[40:41], v[224:225], v[228:229]\n"
    "v_pk_fma_f32 v[42:43], v[42:43], v[226:227], v[230:231]\n"
    "v_pk_fma_f32 v[44:45], v[44:45], v[224:225], v[228:229]\n"
    "v_pk_fma_f32 v[46:47], v[46:47], v[226:227], v[230:231]\n"
    "v_pk_fma_f32 v[48:49], v[48:49], v[224:225], v[228:229]\n"
    "v_pk_fma_f32 v[50:51], v[50:51], v[226:227], v[230:231]\n"
    "v_pk_fma_f32 v[52:53], v[52:53], v[224:225], v[228:229]\n"
    "v_pk_fma_f32 v[54:55], v[54:55], v[226:227], v[230:231]\n"
    "v_pk_fma_f32 v[56:57], v[56:57], v[224:225], v[228:229]\n"
    "v_pk_fma_f32 v[58:59], v[58:59], v[226:227], v[230:231]\n"
    "v_pk_fma_f32 v[60:61], v[60:61], v[224:225], v[228:229]\n"
    "v_pk_fma_f32 v[62:63], v[62:63], v[226:227], v[230:231]\n"
    "v_cvt_pk_bf16_f32 v64, v32, v36\n"
    "v_cvt_pk_bf16_f32 v68, v33, v37\n"
    "v_cvt_pk_bf16_f32 v72, v34, v38\n"
    "v_cvt_pk_bf16_f32 v76, v35, v39\n"
    "v_cvt_pk_bf16_f32 v65, v40, v44\n"
    "v_cvt_pk_bf16_f32 v69, v41, v45\n"
    "v_cvt_pk_bf16_f32 v73, v42, v46\n"
    "v_cvt_pk_bf16_f32 v77, v43, v47\n"
    "v_cvt_pk_bf16_f32 v66, v48, v52\n"
    "v_cvt_pk_bf16_f32 v70, v49, v53\n"
    "v_cvt_pk_bf16_f32 v74, v50, v54\n"
    "v_cvt_pk_bf16_f32 v78, v51, v55\n"
    "v_cvt_pk_bf16_f32 v67, v56, v60\n"
    "ds_write_b128 v253, v[64:67] offset:1024\n"
    "v_cvt_pk_bf16_f32 v71, v57, v61\n"
    "v_cvt_pk_bf16_f32 v75, v58, v62\n"
    "v_cvt_pk_bf16_f32 v79, v59, v63\n"
    "v_mov_b32_e64 v64, v104\n"
    "v_mov_b32_e64 v65, v105\n"
    "v_mov_b32_e64 v66, v106\n"
    "ds_write_b128 v253, v[68:71] offset:4096\n"
    "v_mov_b32_e64 v67, v107\n"
    "v_mov_b32_e64 v68, v108\n"
    "v_mov_b32_e64 v69, v109\n"
    "v_mov_b32_e64 v70, v110\n"
    "v_mov_b32_e64 v71, v111\n"
    "ds_write_b128 v207, v[180:183] offset:32768\n"
    "ds_write_b128 v207, v[168:171] offset:0\n"
    "ds_write_b128 v207, v[172:175] offset:4096\n"
    "ds_write_b128 v207, v[176:179] offset:8192\n"
    "ds_write_b128 v207, v[188:191] offset:12288\n"
    "ds_write_b128 v207, v[192:195] offset:16384\n"
    "s_waitcnt lgkmcnt(2)\n"
    "s_barrier\n"
    "ds_write_b128 v207, v[196:199] offset:20480\n"
    "ds_read_b128 v[144:147], v242 offset:0\n"
    "ds_read_b128 v[148:151], v242 offset:1024\n"
    "ds_read_b128 v[152:155], v242 offset:2048\n"
    "ds_read_b128 v[156:159], v242 offset:3072\n"
    "ds_read_b128 v[160:163], v242 offset:4096\n"
    "ds_read_b128 v[164:167], v242 offset:5120\n"
    "ds_read_b128 a[240:243], v244 offset:32768\n"
    "s_waitcnt lgkmcnt(7)\n"
    "s_barrier\n"
    "s_waitcnt lgkmcnt(6)\n"
    "v_mfma_f32_16x16x32_bf16 v[168:171], a[80:83], v[144:147], 0\n"
    "v_mfma_f32_16x16x32_bf16 v[172:175], a[84:87], v[144:147], 0\n"
    "s_waitcnt lgkmcnt(5)\n"
    "v_mfma_f32_16x16x32_bf16 v[168:171], a[128:131], v[148:151], v[168:171]\n"
    "s_waitcnt lgkmcnt(4)\n"
    "v_mfma_f32_16x16x32_bf16 v[168:171], a[48:51], v[152:155], v[168:171]\n"
    "s_waitcnt lgkmcnt(3)\n"
    "v_mfma_f32_16x16x32_bf16 v[172:175], a[132:135], v[148:151], v[172:175]\n"
    "v_mfma_f32_16x16x32_bf16 v[176:179], a[80:83], v[156:159], 0\n"
    "s_waitcnt lgkmcnt(1)\n"
    "v_mfma_f32_16x16x32_bf16 v[176:179], a[128:131], v[160:163], v[176:179]\n"
    "s_waitcnt lgkmcnt(0)\n"
    "v_mfma_f32_16x16x32_bf16 v[172:175], a[240:243], v[152:155], v[172:175]\n"
    "v_mfma_f32_16x16x32_bf16 v[176:179], a[48:51], v[164:167], v[176:179]\n"
    "v_mfma_f32_16x16x32_bf16 v[180:183], a[84:87], v[156:159], 0\n"
    "v_mfma_f32_16x16x32_bf16 v[180:183], a[132:135], v[160:163], v[180:183]\n"
    "v_mfma_f32_16x16x32_bf16 v[180:183], a[240:243], v[164:167], v[180:183]\n"
    "v_readfirstlane_b32 s66, v232\n"
    "s_nop 1\n"
    "s_cmp_lg_u32 s66, 2\n"
    "s_cbranch_scc1 98f\n"
    ".p2align 6\n"
    "1:\n"
    "v_lshlrev_b32_sdwa v184, 1, v168 dst_sel:DWORD dst_unused:UNUSED_PAD src0_sel:DWORD src1_sel:WORD_1\n"
    "v_lshlrev_b32_sdwa v200, 1, v169 dst_sel:DWORD dst_unused:UNUSED_PAD src0_sel:DWORD src1_sel:WORD_1\n"
    "v_lshlrev_b32_sdwa v185, 1, v170 dst_sel:DWORD dst_unused:UNUSED_PAD src0_sel:DWORD src1_sel:WORD_1\n"
    "v_lshlrev_b32_sdwa v201, 1, v171 dst_sel:DWORD dst_unused:UNUSED_PAD src0_sel:DWORD src1_sel:WORD_1\n"
    "v_mfma_f32_16x16x32_bf16 v[140:143], a[244:247], v[188:191], v[140:143]\n"
    "ds_read_b128 a[240:243], v244 offset:36864\n"
    "ds_read_u16 v184, v184\n"
    "v_mfma_f32_16x16x32_bf16 v[168:171], a[88:91], v[144:147], 0\n"
    "ds_read_u16_d16_hi v200, v200\n"
    "ds_read_u16 v185, v185\n"
    "v_mfma_f32_16x16x32_bf16 v[168:171], a[136:139], v[148:151], v[168:171]\n"
    "buffer_load_dwordx4 v[0:3], v246, s[40:43], s60 offen offset:0\n"
    "buffer_load_dwordx4 v[32:35], v246, s[40:43], s60 offen offset:64\n"
    "ds_read_u16_d16_hi v201, v201\n"
    "v_mfma_f32_16x16x32_bf16 v[168:171], a[52:55], v[152:155], v[168:171]\n"
    "v_lshlrev_b32_sdwa v186, 1, v172 dst_sel:DWORD dst_unused:UNUSED_PAD src0_sel:DWORD src1_sel:WORD_1\n"
    "v_lshlrev_b32_sdwa v202, 1, v173 dst_sel:DWORD dst_unused:UNUSED_PAD src0_sel:DWORD src1_sel:WORD_1\n"
    "s_waitcnt vmcnt(3)\n"
    "v_smfmac_f32_32x32x32_bf16 v[128:143], a[248:251], a[224:231], v241\n"
    "s_nop 0\n"
    "ds_read_u16 v186, v186\n"
    "v_lshlrev_b32_sdwa v187, 1, v174 dst_sel:DWORD dst_unused:UNUSED_PAD src0_sel:DWORD src1_sel:WORD_1\n"
    "v_lshlrev_b32_sdwa v203, 1, v175 dst_sel:DWORD dst_unused:UNUSED_PAD src0_sel:DWORD src1_sel:WORD_1\n"
    "v_lshlrev_b32_sdwa v188, 1, v176 dst_sel:DWORD dst_unused:UNUSED_PAD src0_sel:DWORD src1_sel:WORD_1\n"
    "v_lshlrev_b32_sdwa v204, 1, v177 dst_sel:DWORD dst_unused:UNUSED_PAD src0_sel:DWORD src1_sel:WORD_1\n"
    "v_mfma_f32_16x16x32_bf16 v[172:175], a[92:95], v[144:147], 0\n"
    "ds_read_b128 v[236:239], v244 offset:20480\n"
    "ds_read_u16_d16_hi v202, v202\n"
    "v_mfma_f32_16x16x32_bf16 v[172:175], a[140:143], v[148:151], v[172:175]\n"
    "v_lshlrev_b32_sdwa v189, 1, v178 dst_sel:DWORD dst_unused:UNUSED_PAD src0_sel:DWORD src1_sel:WORD_1\n"
    "v_lshlrev_b32_sdwa v205, 1, v179 dst_sel:DWORD dst_unused:UNUSED_PAD src0_sel:DWORD src1_sel:WORD_1\n"
    "s_waitcnt lgkmcnt(7)\n"
    "v_mfma_f32_16x16x32_bf16 v[172:175], a[240:243], v[152:155], v[172:175]\n"
    "s_nop 0\n"
    "ds_read_u16 v187, v187\n"
    "v_lshlrev_b32_sdwa v190, 1, v180 dst_sel:DWORD dst_unused:UNUSED_PAD src0_sel:DWORD src1_sel:WORD_1\n"
    "v_mfma_f32_16x16x32_bf16 v[176:179], a[88:91], v[156:159], 0\n"
    "v_lshlrev_b32_sdwa v206, 1, v181 dst_sel:DWORD dst_unused:UNUSED_PAD src0_sel:DWORD src1_sel:WORD_1\n"
    "v_lshlrev_b32_sdwa v191, 1, v182 dst_sel:DWORD dst_unused:UNUSED_PAD src0_sel:DWORD src1_sel:WORD_1\n"
    "v_mfma_f32_16x16x32_bf16 v[176:179], a[136:139], v[160:163], v[176:179]\n"
    "ds_read_b128 a[244:247], v244 offset:22528\n"
    "v_lshlrev_b32_sdwa v207, 1, v183 dst_sel:DWORD dst_unused:UNUSED_PAD src0_sel:DWORD src1_sel:WORD_1\n"
    "v_mfma_f32_16x16x32_bf16 v[176:179], a[52:55], v[164:167], v[176:179]\n"
    "ds_read_u16_d16_hi v203, v203\n"
    "v_pk_fma_f16 v234, v234, s49, s49\n"
    "v_mfma_f32_16x16x32_bf16 v[180:183], a[92:95], v[156:159], 0\n"
    "s_waitcnt lgkmcnt(8)\n"
    "v_rcp_f16_e32 v232, v234\n"
    "v_mfma_f32_16x16x32_bf16 v[180:183], a[140:143], v[160:163], v[180:183]\n"
    "ds_read_u16 v188, v188\n"
    "v_rcp_f16_sdwa v232, v234 dst_sel:WORD_1 dst_unused:UNUSED_PRESERVE src0_sel:WORD_1\n"
    "v_mfma_f32_16x16x32_bf16 v[180:183], a[240:243], v[164:167], v[180:183]\n"
    "ds_read_u16_d16_hi v204, v204\n"
    "v_fma_mix_f32 v234, v196, v232, 0 op_sel_hi:[1,1,0]\n"
    "v_mfma_f32_16x16x32_bf16 v[96:99], a[208:211], v[192:195], v[96:99]\n"
    "v_fma_mix_f32 v232, v196, v232, 0 op_sel:[1,1,0] op_sel_hi:[1,1,0]\n"
    "v_or_b32_e32 v184, v184, v200\n"
    "v_mfma_f32_16x16x32_bf16 v[100:103], a[212:215], v[192:195], v[100:103]\n"
    "s_waitcnt lgkmcnt(8)\n"
    "ds_read_u16 v189, v189\n"
    "v_cvt_pk_bf16_f32 v196, v234, v232\n"
    "v_mfma_f32_16x16x32_bf16 v[112:115], a[216:219], v[192:195], v[112:115]\n"
    "s_waitcnt lgkmcnt(7)\n"
    "v_or_b32_e32 v185, v185, v201\n"
    "v_lshlrev_b32_sdwa v201, 1, v171 dst_sel:DWORD dst_unused:UNUSED_PAD src0_sel:DWORD src1_sel:WORD_1\n"
    "v_mfma_f32_16x16x32_bf16 v[116:119], a[220:223], v[192:195], v[116:119]\n"
    "ds_read_u16_d16_hi v205, v205\n"
    "v_cvt_pk_bf16_f32 v96, v96, v97\n"
    "v_mfma_f32_16x16x32_bf16 v[128:131], v[236:239], v[192:195], v[128:131]\n"
    "ds_read_u16 v190, v190\n"
    "buffer_load_dwordx4 v[20:23], v247, s[40:43], s60 offen offset:0\n"
    "buffer_load_dwordx4 v[52:55], v247, s[40:43], s60 offen offset:64\n"
    "v_mfma_f32_16x16x32_bf16 v[104:107], a[208:211], v[196:199], v[104:107]\n"
    "s_waitcnt lgkmcnt(8)\n"
    "v_or_b32_e64 v186, v186, v202\n"
    "v_lshlrev_b32_sdwa v202, 1, v173 dst_sel:DWORD dst_unused:UNUSED_PAD src0_sel:DWORD src1_sel:WORD_1\n"
    "v_mfma_f32_16x16x32_bf16 v[108:111], a[212:215], v[196:199], v[108:111]\n"
    "s_waitcnt lgkmcnt(6)\n"
    "ds_read_u16_d16_hi v206, v206\n"
    "v_cvt_pk_bf16_f32 v97, v98, v99\n"
    "v_mfma_f32_16x16x32_bf16 v[132:135], a[244:247], v[192:195], v[132:135]\n"
    "v_cvt_pk_f16_f32 v192, v168, v169\n"
    "v_lshlrev_b32_sdwa v193, 1, v170 dst_sel:DWORD dst_unused:UNUSED_PAD src0_sel:DWORD src1_sel:WORD_1\n"
    "v_mfma_f32_16x16x32_bf16 v[120:123], a[216:219], v[196:199], v[120:123]\n"
    "ds_read_u16 v191, v191\n"
    "v_lshlrev_b32_sdwa v194, 1, v172 dst_sel:DWORD dst_unused:UNUSED_PAD src0_sel:DWORD src1_sel:WORD_1\n"
    "v_mfma_f32_16x16x32_bf16 v[124:127], a[220:223], v[196:199], v[124:127]\n"
    "s_waitcnt lgkmcnt(7)\n"
    "ds_read_u16_d16_hi v207, v207\n"
    "v_lshlrev_b32_sdwa v195, 1, v174 dst_sel:DWORD dst_unused:UNUSED_PAD src0_sel:DWORD src1_sel:WORD_1\n"
    "v_mfma_f32_16x16x32_bf16 v[136:139], v[236:239], v[196:199], v[136:139]\n"
    "v_or_b32_e32 v187, v187, v203\n"
    "v_lshlrev_b32_sdwa v203, 1, v175 dst_sel:DWORD dst_unused:UNUSED_PAD src0_sel:DWORD src1_sel:WORD_1\n"
    "v_mfma_f32_16x16x32_bf16 v[140:143], a[244:247], v[196:199], v[140:143]\n"
    "ds_read_u16 v193, v193\n"
    "v_cvt_pk_f16_f32 v196, v176, v177\n"
    "v_mfma_f32_16x16x32_bf16 v[168:171], a[96:99], v[144:147], 0\n"
    "v_lshlrev_b32_sdwa v197, 1, v178 dst_sel:DWORD dst_unused:UNUSED_PAD src0_sel:DWORD src1_sel:WORD_1\n"
    "v_lshlrev_b32_sdwa v198, 1, v180 dst_sel:DWORD dst_unused:UNUSED_PAD src0_sel:DWORD src1_sel:WORD_1\n"
    "v_mfma_f32_16x16x32_bf16 v[168:171], a[144:147], v[148:151], v[168:171]\n"
    "s_waitcnt lgkmcnt(7)\n"
    "ds_read_u16_d16_hi v201, v201\n"
    "v_lshlrev_b32_sdwa v199, 1, v182 dst_sel:DWORD dst_unused:UNUSED_PAD src0_sel:DWORD src1_sel:WORD_1\n"
    "v_mfma_f32_16x16x32_bf16 v[168:171], a[56:59], v[152:155], v[168:171]\n"
    "v_or_b32_e32 v188, v188, v204\n"
    "v_cvt_pk_bf16_f32 v98, v100, v101\n"
    "v_mfma_f32_16x16x32_bf16 v[172:175], a[100:103], v[144:147], 0\n"
    "ds_read_u16 v194, v194\n"
    "v_cvt_pk_bf16_f32 v99, v102, v103\n"
    "v_mfma_f32_16x16x32_bf16 v[172:175], a[148:151], v[148:151], v[172:175]\n"
    "s_waitcnt lgkmcnt(7)\n"
    "ds_read_u16_d16_hi v202, v202\n"
    "v_or_b32_e32 v189, v189, v205\n"
    "v_mfma_f32_16x16x32_bf16 v[172:175], a[60:63], v[152:155], v[172:175]\n"
    "v_lshlrev_b32_sdwa v205, 1, v179 dst_sel:DWORD dst_unused:UNUSED_PAD src0_sel:DWORD src1_sel:WORD_1\n"
    "v_cvt_pk_bf16_f32 v112, v112, v113\n"
    "v_mfma_f32_16x16x32_bf16 v[176:179], a[96:99], v[156:159], 0\n"
    "ds_read_b128 v[236:239], v244 offset:0\n"
    "v_cvt_pk_bf16_f32 v113, v114, v115\n"
    "v_mfma_f32_16x16x32_bf16 v[176:179], a[144:147], v[160:163], v[176:179]\n"
    "ds_read_u16 v195, v195\n"
    "v_cvt_pk_bf16_f32 v114, v116, v117\n"
    "v_mfma_f32_16x16x32_bf16 v[176:179], a[56:59], v[164:167], v[176:179]\n"
    "s_waitcnt lgkmcnt(8)\n"
    "v_or_b32_e32 v190, v190, v206\n"
    "v_lshlrev_b32_sdwa v206, 1, v181 dst_sel:DWORD dst_unused:UNUSED_PAD src0_sel:DWORD src1_sel:WORD_1\n"
    "v_mfma_f32_16x16x32_bf16 v[100:103], a[4:7], v[184:187], 0\n"
    "ds_read_u16_d16_hi v203, v203\n"
    "v_cvt_pk_bf16_f32 v115, v118, v119\n"
    "v_cvt_pk_bf16_f32 v128, v128, v129\n"
    "v_cvt_pk_bf16_f32 v129, v130, v131\n"
    "v_cvt_pk_bf16_f32 v130, v132, v133\n"
    "ds_write_b128 v243, v[96:99] offset:24640\n"
    "buffer_load_dwordx4 v[4:7], v246, s[40:43], s61 offen offset:0\n"
    "buffer_load_dwordx4 v[36:39], v246, s[40:43], s61 offen offset:64\n"
    "v_mfma_f32_16x16x32_bf16 v[96:99], a[0:3], v[184:187], 0\n"
    "s_waitcnt lgkmcnt(7)\n"
    "v_or_b32_e32 v191, v191, v207\n"
    "v_lshlrev_b32_sdwa v207, 1, v183 dst_sel:DWORD dst_unused:UNUSED_PAD src0_sel:DWORD src1_sel:WORD_1\n"
    "v_mfma_f32_16x16x32_bf16 v[180:183], a[100:103], v[156:159], 0\n"
    "ds_read_b128 a[252:255], v249 offset:0\n"
    "ds_write_b128 v243, v[112:115] offset:26688\n"
    "v_mfma_f32_16x16x32_bf16 v[180:183], a[148:151], v[160:163], v[180:183]\n"
    "s_waitcnt lgkmcnt(8)\n"
    "v_or_b32_e32 v193, v193, v201\n"
    "v_cvt_pk_bf16_f32 v131, v134, v135\n"
    "v_mfma_f32_16x16x32_bf16 v[180:183], a[60:63], v[164:167], v[180:183]\n"
    "v_cvt_pk_bf16_f32 v104, v104, v105\n"
    "v_cvt_pk_bf16_f32 v105, v106, v107\n"
    "v_mfma_f32_16x16x32_bf16 v[112:115], a[8:11], v[184:187], 0\n"
    "s_waitcnt lgkmcnt(5)\n"
    "ds_read_b128 a[244:247], v244 offset:2048\n"
    "ds_write_b128 v243, v[128:131] offset:28736\n"
    "v_mfma_f32_16x16x32_bf16 v[116:119], a[12:15], v[184:187], 0\n"
    "s_nop 0\n"
    "v_cvt_pk_bf16_f32 v106, v108, v109\n"
    "v_cvt_pk_bf16_f32 v107, v110, v111\n"
    "v_mfma_f32_16x16x32_bf16 v[128:131], v[236:239], v[184:187], 0\n"
    "v_or_b32_e64 v194, v194, v202\n"
    "v_cvt_pk_bf16_f32 v120, v120, v121\n"
    "v_mfma_f32_16x16x32_bf16 v[108:111], a[4:7], v[188:191], 0\n"
    "s_nop 0\n"
    "ds_read_u16 v197, v197\n"
    "ds_write_b128 v243, v[104:107] offset:25664\n"
    "v_mfma_f32_16x16x32_bf16 v[104:107], a[0:3], v[188:191], 0\n"
    "s_waitcnt lgkmcnt(6)\n"
    "v_cvt_pk_bf16_f32 v121, v122, v123\n"
    "v_cvt_pk_bf16_f32 v122, v124, v125\n"
    "v_cvt_pk_bf16_f32 v123, v126, v127\n"
    "v_pk_fma_f16 v232, v192, v192, s48\n"
    "v_mfma_f32_16x16x32_bf16 v[124:127], a[12:15], v[188:191], 0\n"
    "s_waitcnt lgkmcnt(4)\n"
    "buffer_store_dwordx4 a[252:255], v255, s[68:71], s59 offen offset:0 sc1\n"
    "ds_write_b128 v243, v[120:123] offset:27712\n"
    "s_waitcnt lgkmcnt(4)\n"
    "v_mfma_f32_16x16x32_bf16 v[132:135], a[244:247], v[184:187], 0\n"
    "v_cvt_pk_bf16_f32 v136, v136, v137\n"
    "v_cvt_pk_bf16_f32 v137, v138, v139\n"
    "v_mfma_f32_16x16x32_bf16 v[120:123], a[8:11], v[188:191], 0\n"
    "v_cvt_pk_bf16_f32 v138, v140, v141\n"
    "v_or_b32_e64 v195, v195, v203\n"
    "v_cvt_pk_bf16_f32 v139, v142, v143\n"
    "ds_write_b128 v243, v[136:139] offset:29760\n"
    "v_pk_mul_f16 v232, v192, v232\n"
    "v_mfma_f32_16x16x32_bf16 v[136:139], v[236:239], v[188:191], 0\n"
    "v_exp_f16_e64 v234, v232\n"
    "v_mfma_f32_16x16x32_bf16 v[140:143], a[244:247], v[188:191], 0\n"
    "ds_read_u16_d16_hi v205, v205\n"
    "ds_read_u16 v198, v198\n"
    "v_exp_f16_sdwa v234, v232 dst_sel:WORD_1 dst_unused:UNUSED_PRESERVE src0_sel:WORD_1\n"
    "v_cvt_pk_f16_f32 v184, v168, v169\n"
    "v_pk_fma_f16 v234, v234, s49, s49\n"
    "v_lshlrev_b32_sdwa v185, 1, v170 dst_sel:DWORD dst_unused:UNUSED_PAD src0_sel:DWORD src1_sel:WORD_1\n"
    "v_lshlrev_b32_sdwa v201, 1, v171 dst_sel:DWORD dst_unused:UNUSED_PAD src0_sel:DWORD src1_sel:WORD_1\n"
    "v_mfma_f32_16x16x32_bf16 v[168:171], a[104:107], v[144:147], 0\n"
    "ds_read_u16_d16_hi v206, v206\n"
    "ds_read_u16 v199, v199\n"
    "v_mfma_f32_16x16x32_bf16 v[168:171], a[152:155], v[148:151], v[168:171]\n"
    "ds_read_u16_d16_hi v207, v207\n"
    "buffer_load_dwordx4 v[24:27], v247, s[40:43], s61 offen offset:0\n"
    "buffer_load_dwordx4 v[56:59], v247, s[40:43], s61 offen offset:64\n"
    "v_lshlrev_b32_sdwa v186, 1, v172 dst_sel:DWORD dst_unused:UNUSED_PAD src0_sel:DWORD src1_sel:WORD_1\n"
    "v_lshlrev_b32_sdwa v202, 1, v173 dst_sel:DWORD dst_unused:UNUSED_PAD src0_sel:DWORD src1_sel:WORD_1\n"
    "v_lshlrev_b32_sdwa v187, 1, v174 dst_sel:DWORD dst_unused:UNUSED_PAD src0_sel:DWORD src1_sel:WORD_1\n"
    "v_lshlrev_b32_sdwa v203, 1, v175 dst_sel:DWORD dst_unused:UNUSED_PAD src0_sel:DWORD src1_sel:WORD_1\n"
    "v_rcp_f16_e64 v232, v234\n"
    "v_mfma_f32_16x16x32_bf16 v[168:171], a[64:67], v[152:155], v[168:171]\n"
    "ds_read_b128 a[252:255], v249 offset:2048\n"
    "ds_read_u16 v185, v185\n"
    "v_rcp_f16_sdwa v232, v234 dst_sel:WORD_1 dst_unused:UNUSED_PRESERVE src0_sel:WORD_1\n"
    "v_mfma_f32_16x16x32_bf16 v[172:175], a[108:111], v[144:147], 0\n"
    "ds_read_u16_d16_hi v201, v201\n"
    "v_cvt_pk_f16_f32 v188, v176, v177\n"
    "v_mfma_f32_16x16x32_bf16 v[172:175], a[156:159], v[148:151], v[172:175]\n"
    "v_lshlrev_b32_sdwa v189, 1, v178 dst_sel:DWORD dst_unused:UNUSED_PAD src0_sel:DWORD src1_sel:WORD_1\n"
    "v_lshlrev_b32_sdwa v190, 1, v180 dst_sel:DWORD dst_unused:UNUSED_PAD src0_sel:DWORD src1_sel:WORD_1\n"
    "v_lshlrev_b32_sdwa v191, 1, v182 dst_sel:DWORD dst_unused:UNUSED_PAD src0_sel:DWORD src1_sel:WORD_1\n"
    "v_fma_mix_f32 v234, v192, v232, 0 op_sel_hi:[1,1,0]\n"
    "v_mfma_f32_16x16x32_bf16 v[172:175], a[68:71], v[152:155], v[172:175]\n"
    "s_waitcnt lgkmcnt(7)\n"
    "ds_read_u16 v186, v186\n"
    "v_or_b32_e32 v197, v197, v205\n"
    "v_lshlrev_b32_sdwa v205, 1, v179 dst_sel:DWORD dst_unused:UNUSED_PAD src0_sel:DWORD src1_sel:WORD_1\n"
    "v_fma_mix_f32 v232, v192, v232, 0 op_sel:[1,1,0] op_sel_hi:[1,1,0]\n"
    "v_mfma_f32_16x16x32_bf16 v[176:179], a[104:107], v[156:159], 0\n"
    "ds_read_u16_d16_hi v202, v202\n"
    "v_cvt_pk_bf16_f32 v192, v234, v232\n"
    "v_mfma_f32_16x16x32_bf16 v[176:179], a[152:155], v[160:163], v[176:179]\n"
    "ds_read_b128 v[236:239], v244 offset:4096\n"
    "v_pk_fma_f16 v232, v196, v196, s48\n"
    "v_mfma_f32_16x16x32_bf16 v[176:179], a[64:67], v[164:167], v[176:179]\n"
    "s_waitcnt lgkmcnt(8)\n"
    "ds_read_u16 v187, v187\n"
    "v_or_b32_e32 v198, v198, v206\n"
    "v_lshlrev_b32_sdwa v206, 1, v181 dst_sel:DWORD dst_unused:UNUSED_PAD src0_sel:DWORD src1_sel:WORD_1\n"
    "v_pk_mul_f16 v232, v196, v232\n"
    "v_lshlrev_b32_sdwa v200, 1, v169 dst_sel:DWORD dst_unused:UNUSED_PAD src0_sel:DWORD src1_sel:WORD_1\n"
    "v_exp_f16_e64 v234, v232\n"
    "v_mov_b32_e32 v254, 0\n"
    "v_exp_f16_sdwa v234, v232 dst_sel:WORD_1 dst_unused:UNUSED_PRESERVE src0_sel:WORD_1\n"
    "v_mfma_f32_16x16x32_bf16 v[96:99], a[16:19], v[192:195], v[96:99]\n"
    "s_waitcnt lgkmcnt(6)\n"
    "ds_read_u16_d16_hi v203, v203\n"
    "buffer_store_dwordx4 a[252:255], v255, s[68:71], s59 offen offset:64 sc1\n"
    "v_or_b32_e64 v199, v199, v207\n"
    "v_lshlrev_b32_sdwa v207, 1, v183 dst_sel:DWORD dst_unused:UNUSED_PAD src0_sel:DWORD src1_sel:WORD_1\n"
    "v_mfma_f32_16x16x32_bf16 v[180:183], a[108:111], v[156:159], 0\n"
    "ds_read_b128 a[244:247], v244 offset:6144\n"
    "ds_read_b128 a[252:255], v249 offset:4096\n"
    "v_mfma_f32_16x16x32_bf16 v[180:183], a[156:159], v[160:163], v[180:183]\n"
    "ds_read_u16 v189, v189\n"
    "v_pk_fma_f16 v234, v234, s49, s49\n"
    "v_mfma_f32_16x16x32_bf16 v[180:183], a[68:71], v[164:167], v[180:183]\n"
    "ds_read_u16_d16_hi v205, v205\n"
    "v_rcp_f16_e32 v232, v234\n"
    "v_mfma_f32_16x16x32_bf16 v[100:103], a[20:23], v[192:195], v[100:103]\n"
    "s_waitcnt lgkmcnt(9)\n"
    "v_rcp_f16_sdwa v232, v234 dst_sel:WORD_1 dst_unused:UNUSED_PRESERVE src0_sel:WORD_1\n"
    "v_mfma_f32_16x16x32_bf16 v[112:115], a[24:27], v[192:195], v[112:115]\n"
    "ds_read_u16 v190, v190\n"
    "v_or_b32_e64 v185, v185, v201\n"
    "v_mfma_f32_16x16x32_bf16 v[116:119], a[28:31], v[192:195], v[116:119]\n"
    "ds_read_u16_d16_hi v206, v206\n"
    "v_fma_mix_f32 v234, v196, v232, 0 op_sel_hi:[1,1,0]\n"
    "s_waitcnt lgkmcnt(8)\n"
    "v_mfma_f32_16x16x32_bf16 v[128:131], v[236:239], v[192:195], v[128:131]\n"
    "s_nop 0\n"
    "v_fma_mix_f32 v232, v196, v232, 0 op_sel:[1,1,0] op_sel_hi:[1,1,0]\n"
    "v_or_b32_e64 v186, v186, v202\n"
    "v_cvt_pk_bf16_f32 v196, v234, v232\n"
    "v_pk_fma_f16 v232, v184, v184, s48\n"
    "v_lshlrev_b32_sdwa v201, 1, v171 dst_sel:DWORD dst_unused:UNUSED_PAD src0_sel:DWORD src1_sel:WORD_1\n"
    "v_mfma_f32_16x16x32_bf16 v[104:107], a[16:19], v[196:199], v[104:107]\n"
    "buffer_load_dwordx4 v[8:11], v246, s[40:43], s62 offen offset:0\n"
    "buffer_load_dwordx4 v[40:43], v246, s[40:43], s62 offen offset:64\n"
    "ds_read_u16 v191, v191\n"
    "v_mfma_f32_16x16x32_bf16 v[108:111], a[20:23], v[196:199], v[108:111]\n"
    "ds_read_u16_d16_hi v207, v207\n"
    "v_pk_mul_f16 v232, v184, v232\n"
    "s_waitcnt lgkmcnt(7)\n"
    "v_mfma_f32_16x16x32_bf16 v[132:135], a[244:247], v[192:195], v[132:135]\n"
    "s_nop 0\n"
    "ds_read_b128 a[240:243], v244 offset:34816\n"
    "v_or_b32_e32 v187, v187, v203\n"
    "v_mfma_f32_16x16x32_bf16 v[120:123], a[24:27], v[196:199], v[120:123]\n"
    "s_waitcnt lgkmcnt(7)\n"
    "ds_read_u16_d16_hi v200, v200\n"
    "v_exp_f16_e64 v234, v232\n"
    "v_mfma_f32_16x16x32_bf16 v[124:127], a[28:31], v[196:199], v[124:127]\n"
    "v_exp_f16_sdwa v234, v232 dst_sel:WORD_1 dst_unused:UNUSED_PRESERVE src0_sel:WORD_1\n"
    "v_mfma_f32_16x16x32_bf16 v[136:139], v[236:239], v[196:199], v[136:139]\n"
    "ds_read_u16_d16_hi v201, v201\n"
    "v_lshlrev_b32_sdwa v192, 1, v168 dst_sel:DWORD dst_unused:UNUSED_PAD src0_sel:DWORD src1_sel:WORD_1\n"
    "v_mfma_f32_16x16x32_bf16 v[140:143], a[244:247], v[196:199], v[140:143]\n"
    "ds_read_u16 v192, v192\n"
    "v_pk_fma_f16 v234, v234, s49, s49\n"
    "v_lshlrev_b32_sdwa v193, 1, v170 dst_sel:DWORD dst_unused:UNUSED_PAD src0_sel:DWORD src1_sel:WORD_1\n"
    "v_mfma_f32_16x16x32_bf16 v[168:171], a[112:115], v[144:147], 0\n"
    "s_waitcnt lgkmcnt(8)\n"
    "v_or_b32_e32 v189, v189, v205\n"
    "v_lshlrev_b32_sdwa v194, 1, v172 dst_sel:DWORD dst_unused:UNUSED_PAD src0_sel:DWORD src1_sel:WORD_1\n"
    "v_mfma_f32_16x16x32_bf16 v[168:171], a[160:163], v[148:151], v[168:171]\n"
    "ds_read_u16 v193, v193\n"
    "v_lshlrev_b32_sdwa v202, 1, v173 dst_sel:DWORD dst_unused:UNUSED_PAD src0_sel:DWORD src1_sel:WORD_1\n"
    "v_lshlrev_b32_sdwa v195, 1, v174 dst_sel:DWORD dst_unused:UNUSED_PAD src0_sel:DWORD src1_sel:WORD_1\n"
    "v_lshlrev_b32_sdwa v203, 1, v175 dst_sel:DWORD dst_unused:UNUSED_PAD src0_sel:DWORD src1_sel:WORD_1\n"
    "v_rcp_f16_e32 v232, v234\n"
    "v_mfma_f32_16x16x32_bf16 v[168:171], a[72:75], v[152:155], v[168:171]\n"
    "s_waitcnt lgkmcnt(7)\n"
    "ds_read_u16 v194, v194\n"
    "buffer_store_dwordx4 a[252:255], v255, s[68:71], s59 offen offset:128 sc1\n"
    "v_or_b32_e32 v190, v190, v206\n"
    "v_rcp_f16_sdwa v232, v234 dst_sel:WORD_1 dst_unused:UNUSED_PRESERVE src0_sel:WORD_1\n"
    "v_mfma_f32_16x16x32_bf16 v[172:175], a[116:119], v[144:147], 0\n"
    "ds_read_b128 a[252:255], v249 offset:1024\n"
    "ds_read_b128 v[236:239], v244 offset:8192\n"
    "v_mfma_f32_16x16x32_bf16 v[172:175], a[164:167], v[148:151], v[172:175]\n"
    "s_waitcnt lgkmcnt(7)\n"
    "ds_read_u16_d16_hi v202, v202\n"
    "v_lshlrev_b32_sdwa v196, 1, v176 dst_sel:DWORD dst_unused:UNUSED_PAD src0_sel:DWORD src1_sel:WORD_1\n"
    "v_lshlrev_b32_sdwa v204, 1, v177 dst_sel:DWORD dst_unused:UNUSED_PAD src0_sel:DWORD src1_sel:WORD_1\n"
    "v_lshlrev_b32_sdwa v197, 1, v178 dst_sel:DWORD dst_unused:UNUSED_PAD src0_sel:DWORD src1_sel:WORD_1\n"
    "v_lshlrev_b32_sdwa v205, 1, v179 dst_sel:DWORD dst_unused:UNUSED_PAD src0_sel:DWORD src1_sel:WORD_1\n"
    "v_lshlrev_b32_sdwa v198, 1, v180 dst_sel:DWORD dst_unused:UNUSED_PAD src0_sel:DWORD src1_sel:WORD_1\n"
    "v_lshlrev_b32_sdwa v206, 1, v181 dst_sel:DWORD dst_unused:UNUSED_PAD src0_sel:DWORD src1_sel:WORD_1\n"
    "v_lshlrev_b32_sdwa v199, 1, v182 dst_sel:DWORD dst_unused:UNUSED_PAD src0_sel:DWORD src1_sel:WORD_1\n"
    "v_fma_mix_f32 v234, v184, v232, 0 op_sel_hi:[1,1,0]\n"
    "v_mfma_f32_16x16x32_bf16 v[172:175], a[240:243], v[152:155], v[172:175]\n"
    "ds_read_u16 v195, v195\n"
    "ds_read_b128 a[244:247], v244 offset:10240\n"
    "v_or_b32_e64 v191, v191, v207\n"
    "v_lshlrev_b32_sdwa v207, 1, v183 dst_sel:DWORD dst_unused:UNUSED_PAD src0_sel:DWORD src1_sel:WORD_1\n"
    "v_fma_mix_f32 v232, v184, v232, 0 op_sel:[1,1,0] op_sel_hi:[1,1,0]\n"
    "v_mfma_f32_16x16x32_bf16 v[176:179], a[112:115], v[156:159], 0\n"
    "ds_read_u16_d16_hi v203, v203\n"
    "v_cvt_pk_bf16_f32 v184, v234, v232\n"
    "v_mfma_f32_16x16x32_bf16 v[176:179], a[160:163], v[160:163], v[176:179]\n"
    "s_nop 0\n"
    "ds_read_u16 v196, v196\n"
    "v_pk_fma_f16 v232, v188, v188, s48\n"
    "v_mfma_f32_16x16x32_bf16 v[176:179], a[72:75], v[164:167], v[176:179]\n"
    "s_waitcnt lgkmcnt(9)\n"
    "buffer_load_dwordx4 v[12:15], v246, s[40:43], s63 offen offset:0\n"
    "buffer_load_dwordx4 v[44:47], v246, s[40:43], s63 offen offset:64\n"
    "v_mfma_f32_16x16x32_bf16 v[180:183], a[116:119], v[156:159], 0\n"
    "ds_read_u16_d16_hi v204, v204\n"
    "v_pk_mul_f16 v232, v188, v232\n"
    "v_mfma_f32_16x16x32_bf16 v[180:183], a[164:167], v[160:163], v[180:183]\n"
    "s_waitcnt lgkmcnt(9)\n"
    "v_exp_f16_e64 v234, v232\n"
    "v_mfma_f32_16x16x32_bf16 v[180:183], a[240:243], v[164:167], v[180:183]\n"
    "s_waitcnt lgkmcnt(7)\n"
    "ds_read_u16 v197, v197\n"
    "v_exp_f16_sdwa v234, v232 dst_sel:WORD_1 dst_unused:UNUSED_PRESERVE src0_sel:WORD_1\n"
    "v_mfma_f32_16x16x32_bf16 v[96:99], a[32:35], v[184:187], v[96:99]\n"
    "ds_read_u16_d16_hi v205, v205\n"
    "v_pk_fma_f16 v234, v234, s49, s49\n"
    "v_mfma_f32_16x16x32_bf16 v[100:103], a[36:39], v[184:187], v[100:103]\n"
    "v_rcp_f16_e64 v232, v234\n"
    "v_mfma_f32_16x16x32_bf16 v[112:115], a[40:43], v[184:187], v[112:115]\n"
    "ds_read_u16 v198, v198\n"
    "v_rcp_f16_sdwa v232, v234 dst_sel:WORD_1 dst_unused:UNUSED_PRESERVE src0_sel:WORD_1\n"
    "v_mfma_f32_16x16x32_bf16 v[116:119], a[44:47], v[184:187], v[116:119]\n"
    "s_waitcnt lgkmcnt(8)\n"
    "v_fma_mix_f32 v234, v188, v232, 0 op_sel_hi:[1,1,0]\n"
    "v_fma_mix_f32 v232, v188, v232, 0 op_sel:[1,1,0] op_sel_hi:[1,1,0]\n"
    "v_mfma_f32_16x16x32_bf16 v[128:131], v[236:239], v[184:187], v[128:131]\n"
    "s_waitcnt lgkmcnt(6)\n"
    "ds_read_u16_d16_hi v206, v206\n"
    "v_cvt_pk_bf16_f32 v188, v234, v232\n"
    "v_mfma_f32_16x16x32_bf16 v[132:135], a[244:247], v[184:187], v[132:135]\n"
    "ds_read_u16 v199, v199\n"
    "buffer_store_dwordx4 a[252:255], v255, s[68:71], s39 offen offset:0 sc1\n"
    "v_mfma_f32_16x16x32_bf16 v[104:107], a[32:35], v[188:191], v[104:107]\n"
    "s_waitcnt lgkmcnt(7)\n"
    "ds_read_b128 a[252:255], v249 offset:3072\n"
    "v_or_b32_e32 v192, v192, v200\n"
    "v_mfma_f32_16x16x32_bf16 v[108:111], a[36:39], v[188:191], v[108:111]\n"
    "ds_read_u16_d16_hi v207, v207\n"
    "v_or_b32_e64 v193, v193, v201\n"
    "v_mfma_f32_16x16x32_bf16 v[120:123], a[40:43], v[188:191], v[120:123]\n"
    "v_cvt_pk_f16_f32 v184, v168, v169\n"
    "v_lshlrev_b32_sdwa v185, 1, v170 dst_sel:DWORD dst_unused:UNUSED_PAD src0_sel:DWORD src1_sel:WORD_1\n"
    "v_mfma_f32_16x16x32_bf16 v[124:127], a[44:47], v[188:191], v[124:127]\n"
    "ds_read_b128 a[240:243], v244 offset:38912\n"
    "v_lshlrev_b32_sdwa v201, 1, v171 dst_sel:DWORD dst_unused:UNUSED_PAD src0_sel:DWORD src1_sel:WORD_1\n"
    "v_mfma_f32_16x16x32_bf16 v[136:139], v[236:239], v[188:191], v[136:139]\n"
    "s_waitcnt lgkmcnt(8)\n"
    "ds_read_u16 v185, v185\n"
    "v_lshlrev_b32_sdwa v186, 1, v172 dst_sel:DWORD dst_unused:UNUSED_PAD src0_sel:DWORD src1_sel:WORD_1\n"
    "v_mfma_f32_16x16x32_bf16 v[140:143], a[244:247], v[188:191], v[140:143]\n"
    "v_or_b32_e32 v194, v194, v202\n"
    "v_lshlrev_b32_sdwa v202, 1, v173 dst_sel:DWORD dst_unused:UNUSED_PAD src0_sel:DWORD src1_sel:WORD_1\n"
    "v_mfma_f32_16x16x32_bf16 v[168:171], a[120:123], v[144:147], 0\n"
    "s_nop 0\n"
    "ds_read_u16_d16_hi v201, v201\n"
    "v_lshlrev_b32_sdwa v187, 1, v174 dst_sel:DWORD dst_unused:UNUSED_PAD src0_sel:DWORD src1_sel:WORD_1\n"
    "v_mfma_f32_16x16x32_bf16 v[168:171], a[168:171], v[148:151], v[168:171]\n"
    "s_waitcnt lgkmcnt(8)\n"
    "ds_read_u16 v186, v186\n"
    "v_cvt_pk_f16_f32 v188, v176, v177\n"
    "v_mfma_f32_16x16x32_bf16 v[168:171], a[76:79], v[152:155], v[168:171]\n"
    "buffer_load_dwordx4 v[16:19], v246, s[40:43], s52 offen offset:0\n"
    "buffer_load_dwordx4 v[48:51], v246, s[40:43], s52 offen offset:64\n"
    "v_lshlrev_b32_sdwa v189, 1, v178 dst_sel:DWORD dst_unused:UNUSED_PAD src0_sel:DWORD src1_sel:WORD_1\n"
    "v_lshlrev_b32_sdwa v190, 1, v180 dst_sel:DWORD dst_unused:UNUSED_PAD src0_sel:DWORD src1_sel:WORD_1\n"
    "v_lshlrev_b32_sdwa v191, 1, v182 dst_sel:DWORD dst_unused:UNUSED_PAD src0_sel:DWORD src1_sel:WORD_1\n"
    "v_or_b32_e32 v195, v195, v203\n"
    "v_lshlrev_b32_sdwa v203, 1, v175 dst_sel:DWORD dst_unused:UNUSED_PAD src0_sel:DWORD src1_sel:WORD_1\n"
    "v_mfma_f32_16x16x32_bf16 v[172:175], a[124:127], v[144:147], 0\n"
    "s_waitcnt lgkmcnt(7)\n"
    "ds_read_u16_d16_hi v202, v202\n"
    "ds_read_b128 v[236:239], v244 offset:12288\n"
    "v_mfma_f32_16x16x32_bf16 v[172:175], a[172:175], v[148:151], v[172:175]\n"
    "v_or_b32_e64 v196, v196, v204\n"
    "v_or_b32_e64 v197, v197, v205\n"
    "v_lshlrev_b32_sdwa v205, 1, v179 dst_sel:DWORD dst_unused:UNUSED_PAD src0_sel:DWORD src1_sel:WORD_1\n"
    "v_mfma_f32_16x16x32_bf16 v[176:179], a[120:123], v[156:159], 0\n"
    "s_waitcnt lgkmcnt(7)\n"
    "ds_read_u16 v187, v187\n"
    "v_or_b32_e32 v198, v198, v206\n"
    "v_mfma_f32_16x16x32_bf16 v[176:179], a[168:171], v[160:163], v[176:179]\n"
    "ds_read_u16_d16_hi v203, v203\n"
    "v_lshlrev_b32_sdwa v206, 1, v181 dst_sel:DWORD dst_unused:UNUSED_PAD src0_sel:DWORD src1_sel:WORD_1\n"
    "v_mfma_f32_16x16x32_bf16 v[176:179], a[76:79], v[164:167], v[176:179]\n"
    "s_waitcnt lgkmcnt(7)\n"
    "v_or_b32_e32 v199, v199, v207\n"
    "v_lshlrev_b32_sdwa v207, 1, v183 dst_sel:DWORD dst_unused:UNUSED_PAD src0_sel:DWORD src1_sel:WORD_1\n"
    "v_mfma_f32_16x16x32_bf16 v[172:175], a[240:243], v[152:155], v[172:175]\n"
    "ds_read_b128 a[244:247], v244 offset:14336\n"
    "v_pk_fma_f16 v232, v184, v184, s48\n"
    "v_mfma_f32_16x16x32_bf16 v[180:183], a[124:127], v[156:159], 0\n"
    "ds_read_u16 v189, v189\n"
    "v_pk_mul_f16 v232, v184, v232\n"
    "v_mfma_f32_16x16x32_bf16 v[180:183], a[172:175], v[160:163], v[180:183]\n"
    "v_exp_f16_e32 v234, v232\n"
    "v_mfma_f32_16x16x32_bf16 v[180:183], a[240:243], v[164:167], v[180:183]\n"
    "ds_read_u16_d16_hi v205, v205\n"
    "buffer_store_dwordx4 a[252:255], v255, s[68:71], s39 offen offset:64 sc1\n"
    "v_mfma_f32_16x16x32_bf16 v[96:99], a[176:179], v[192:195], v[96:99]\n"
    "s_waitcnt lgkmcnt(8)\n"
    "ds_read_b128 a[252:255], v249 offset:5120\n"
    "ds_read_u16 v190, v190\n"
    "v_mfma_f32_16x16x32_bf16 v[100:103], a[180:183], v[192:195], v[100:103]\n"
    "v_or_b32_e32 v185, v185, v201\n"
    "v_exp_f16_sdwa v234, v232 dst_sel:WORD_1 dst_unused:UNUSED_PRESERVE src0_sel:WORD_1\n"
    "v_mfma_f32_16x16x32_bf16 v[112:115], a[184:187], v[192:195], v[112:115]\n"
    "s_waitcnt lgkmcnt(7)\n"
    "ds_read_b128 v[144:147], v243 offset:0\n"
    "v_or_b32_e64 v186, v186, v202\n"
    "v_mfma_f32_16x16x32_bf16 v[116:119], a[188:191], v[192:195], v[116:119]\n"
    "ds_read_u16_d16_hi v206, v206\n"
    "v_pk_fma_f16 v234, v234, s49, s49\n"
    "v_mfma_f32_16x16x32_bf16 v[128:131], v[236:239], v[192:195], v[128:131]\n"
    "v_lshlrev_b32_sdwa v201, 1, v171 dst_sel:DWORD dst_unused:UNUSED_PAD src0_sel:DWORD src1_sel:WORD_1\n"
    "v_lshlrev_b32_sdwa v202, 1, v173 dst_sel:DWORD dst_unused:UNUSED_PAD src0_sel:DWORD src1_sel:WORD_1\n"
    "v_mfma_f32_16x16x32_bf16 v[104:107], a[176:179], v[196:199], v[104:107]\n"
    "ds_read_b128 v[148:151], v243 offset:1024\n"
    "ds_read_u16 v191, v191\n"
    "v_mfma_f32_16x16x32_bf16 v[108:111], a[180:183], v[196:199], v[108:111]\n"
    "v_rcp_f16_e32 v232, v234\n"
    "s_waitcnt lgkmcnt(8)\n"
    "v_mfma_f32_16x16x32_bf16 v[132:135], a[244:247], v[192:195], v[132:135]\n"
    "ds_read_b128 v[152:155], v243 offset:2048\n"
    "v_or_b32_e64 v187, v187, v203\n"
    "v_mfma_f32_16x16x32_bf16 v[120:123], a[184:187], v[196:199], v[120:123]\n"
    "ds_read_u16_d16_hi v207, v207\n"
    "v_cvt_pk_f16_f32 v192, v168, v169\n"
    "v_mfma_f32_16x16x32_bf16 v[124:127], a[188:191], v[196:199], v[124:127]\n"
    "v_lshlrev_b32_sdwa v193, 1, v170 dst_sel:DWORD dst_unused:UNUSED_PAD src0_sel:DWORD src1_sel:WORD_1\n"
    "v_lshlrev_b32_sdwa v194, 1, v172 dst_sel:DWORD dst_unused:UNUSED_PAD src0_sel:DWORD src1_sel:WORD_1\n"
    "v_mfma_f32_16x16x32_bf16 v[136:139], v[236:239], v[196:199], v[136:139]\n"
    "ds_read_b128 v[156:159], v243 offset:3072\n"
    "v_lshlrev_b32_sdwa v195, 1, v174 dst_sel:DWORD dst_unused:UNUSED_PAD src0_sel:DWORD src1_sel:WORD_1\n"
    "v_mfma_f32_16x16x32_bf16 v[140:143], a[244:247], v[196:199], v[140:143]\n"
    "s_waitcnt lgkmcnt(8)\n"
    "ds_read_b128 v[160:163], v243 offset:4096\n"
    "buffer_store_dwordx4 a[252:255], v255, s[68:71], s39 offen offset:128 sc1\n"
    "v_or_b32_e32 v189, v189, v205\n"
    "v_lshlrev_b32_sdwa v203, 1, v175 dst_sel:DWORD dst_unused:UNUSED_PAD src0_sel:DWORD src1_sel:WORD_1\n"
    "v_rcp_f16_sdwa v232, v234 dst_sel:WORD_1 dst_unused:UNUSED_PRESERVE src0_sel:WORD_1\n"
    "s_waitcnt lgkmcnt(7)\n"
    "v_mfma_f32_16x16x32_bf16 v[168:171], a[80:83], v[144:147], 0\n"
    "s_nop 0\n"
    "ds_read_b128 v[164:167], v243 offset:5120\n"
    "ds_read_b128 a[240:243], v244 offset:32768\n"
    "v_mfma_f32_16x16x32_bf16 v[172:175], a[84:87], v[144:147], 0\n"
    "ds_read_u16 v193, v193\n"
    "ds_read_u16_d16_hi v201, v201\n"
    "v_cvt_pk_f16_f32 v196, v176, v177\n"
    "s_waitcnt lgkmcnt(9)\n"
    "v_mfma_f32_16x16x32_bf16 v[168:171], a[128:131], v[148:151], v[168:171]\n"
    "v_or_b32_e32 v190, v190, v206\n"
    "v_lshlrev_b32_sdwa v197, 1, v178 dst_sel:DWORD dst_unused:UNUSED_PAD src0_sel:DWORD src1_sel:WORD_1\n"
    "v_lshlrev_b32_sdwa v205, 1, v179 dst_sel:DWORD dst_unused:UNUSED_PAD src0_sel:DWORD src1_sel:WORD_1\n"
    "v_lshlrev_b32_sdwa v198, 1, v180 dst_sel:DWORD dst_unused:UNUSED_PAD src0_sel:DWORD src1_sel:WORD_1\n"
    "v_lshlrev_b32_sdwa v206, 1, v181 dst_sel:DWORD dst_unused:UNUSED_PAD src0_sel:DWORD src1_sel:WORD_1\n"
    "v_lshlrev_b32_sdwa v199, 1, v182 dst_sel:DWORD dst_unused:UNUSED_PAD src0_sel:DWORD src1_sel:WORD_1\n"
    "v_fma_mix_f32 v234, v184, v232, 0 op_sel_hi:[1,1,0]\n"
    "v_mfma_f32_16x16x32_bf16 v[172:175], a[132:135], v[148:151], v[172:175]\n"
    "s_waitcnt lgkmcnt(7)\n"
    "ds_read_u16 v194, v194\n"
    "ds_read_u16_d16_hi v202, v202\n"
    "v_mfma_f32_16x16x32_bf16 v[168:171], a[48:51], v[152:155], v[168:171]\n"
    "s_nop 0\n"
    "v_fma_mix_f32 v232, v184, v232, 0 op_sel:[1,1,0] op_sel_hi:[1,1,0]\n"
    "v_cvt_pk_bf16_f32 v184, v234, v232\n"
    "v_pk_fma_f16 v232, v188, v188, s48\n"
    "v_pk_mul_f16 v232, v188, v232\n"
    "v_exp_f16_e32 v234, v232\n"
    "s_waitcnt lgkmcnt(7)\n"
    "v_mfma_f32_16x16x32_bf16 v[176:179], a[80:83], v[156:159], 0\n"
    "ds_read_b128 v[236:239], v244 offset:16384\n"
    "ds_read_u16 v195, v195\n"
    "s_waitcnt lgkmcnt(8)\n"
    "v_mfma_f32_16x16x32_bf16 v[176:179], a[128:131], v[160:163], v[176:179]\n"
    "s_waitcnt lgkmcnt(7)\n"
    "ds_read_u16_d16_hi v203, v203\n"
    "v_or_b32_e32 v191, v191, v207\n"
    "v_mfma_f32_16x16x32_bf16 v[176:179], a[48:51], v[164:167], v[176:179]\n"
    "s_waitcnt lgkmcnt(7)\n"
    "v_lshlrev_b32_sdwa v207, 1, v183 dst_sel:DWORD dst_unused:UNUSED_PAD src0_sel:DWORD src1_sel:WORD_1\n"
    "v_exp_f16_sdwa v234, v232 dst_sel:WORD_1 dst_unused:UNUSED_PRESERVE src0_sel:WORD_1\n"
    "v_mfma_f32_16x16x32_bf16 v[172:175], a[240:243], v[152:155], v[172:175]\n"
    "s_barrier\n"
    "v_mfma_f32_16x16x32_bf16 v[180:183], a[84:87], v[156:159], 0\n"
    "ds_read_b128 a[244:247], v244 offset:18432\n"
    "ds_write_b128 v253, v[64:67] offset:2048\n"
    "v_mfma_f32_16x16x32_bf16 v[180:183], a[132:135], v[160:163], v[180:183]\n"
    "s_waitcnt lgkmcnt(7)\n"
    "v_pk_fma_f16 v234, v234, s49, s49\n"
    "v_or_b32_e64 v193, v193, v201\n"
    "v_mfma_f32_16x16x32_bf16 v[180:183], a[240:243], v[164:167], v[180:183]\n"
    "v_rcp_f16_e64 v232, v234\n"
    "v_mfma_f32_16x16x32_bf16 v[96:99], a[192:195], v[184:187], v[96:99]\n"
    "ds_read_u16 v197, v197\n"
    "ds_write_b128 v253, v[68:71] offset:5120\n"
    "v_mfma_f32_16x16x32_bf16 v[100:103], a[196:199], v[184:187], v[100:103]\n"
    "s_waitcnt lgkmcnt(6)\n"
    "v_rcp_f16_sdwa v232, v234 dst_sel:WORD_1 dst_unused:UNUSED_PRESERVE src0_sel:WORD_1\n"
    "v_mfma_f32_16x16x32_bf16 v[112:115], a[200:203], v[184:187], v[112:115]\n"
    "s_nop 0\n"
    "v_fma_mix_f32 v234, v188, v232, 0 op_sel_hi:[1,1,0]\n"
    "v_fma_mix_f32 v232, v188, v232, 0 op_sel:[1,1,0] op_sel_hi:[1,1,0]\n"
    "v_mfma_f32_16x16x32_bf16 v[116:119], a[204:207], v[184:187], v[116:119]\n"
    "ds_read_u16_d16_hi v205, v205\n"
    "ds_write_b128 v253, v[88:91] offset:0\n"
    "v_mfma_f32_16x16x32_bf16 v[128:131], v[236:239], v[184:187], v[128:131]\n"
    "s_waitcnt lgkmcnt(5)\n"
    "v_or_b32_e32 v194, v194, v202\n"
    "v_cvt_pk_bf16_f32 v188, v234, v232\n"
    "v_mfma_f32_16x16x32_bf16 v[132:135], a[244:247], v[184:187], v[132:135]\n"
    "v_pk_fma_f16 v232, v192, v192, s48\n"
    "v_or_b32_e64 v195, v195, v203\n"
    "v_mfma_f32_16x16x32_bf16 v[104:107], a[192:195], v[188:191], v[104:107]\n"
    "buffer_load_dwordx4 a[232:235], v245, s[40:43], s57 offen offset:64\n"
    "buffer_load_dwordx4 a[224:227], v245, s[40:43], s57 offen offset:0\n"
    "ds_read_u16 v198, v198\n"
    "v_mfma_f32_16x16x32_bf16 v[108:111], a[196:199], v[188:191], v[108:111]\n"
    "ds_read_u16_d16_hi v206, v206\n"
    "ds_write_b128 v253, v[92:95] offset:3072\n"
    "v_mfma_f32_16x16x32_bf16 v[120:123], a[200:203], v[188:191], v[120:123]\n"
    "v_pk_mul_f16 v232, v192, v232\n"
    "v_lshlrev_b32_sdwa v184, 1, v168 dst_sel:DWORD dst_unused:UNUSED_PAD src0_sel:DWORD src1_sel:WORD_1\n"
    "v_mfma_f32_16x16x32_bf16 v[124:127], a[204:207], v[188:191], v[124:127]\n"
    "v_exp_f16_e64 v234, v232\n"
    "v_mfma_f32_16x16x32_bf16 v[136:139], v[236:239], v[188:191], v[136:139]\n"
    "ds_read_u16 v199, v199\n"
    "ds_write_b128 v253, v[72:75] offset:1024\n"
    "v_mfma_f32_16x16x32_bf16 v[140:143], a[244:247], v[188:191], v[140:143]\n"
    "s_waitcnt lgkmcnt(5)\n"
    "v_or_b32_e32 v197, v197, v205\n"
    "v_exp_f16_sdwa v234, v232 dst_sel:WORD_1 dst_unused:UNUSED_PRESERVE src0_sel:WORD_1\n"
    "v_lshlrev_b32_sdwa v200, 1, v169 dst_sel:DWORD dst_unused:UNUSED_PAD src0_sel:DWORD src1_sel:WORD_1\n"
    "v_pk_fma_f16 v234, v234, s49, s49\n"
    "v_lshlrev_b32_sdwa v185, 1, v170 dst_sel:DWORD dst_unused:UNUSED_PAD src0_sel:DWORD src1_sel:WORD_1\n"
    "v_lshlrev_b32_sdwa v201, 1, v171 dst_sel:DWORD dst_unused:UNUSED_PAD src0_sel:DWORD src1_sel:WORD_1\n"
    "v_mfma_f32_16x16x32_bf16 v[168:171], a[88:91], v[144:147], 0\n"
    "ds_read_u16_d16_hi v207, v207\n"
    "ds_write_b128 v253, v[76:79] offset:4096\n"
    "v_mfma_f32_16x16x32_bf16 v[168:171], a[136:139], v[148:151], v[168:171]\n"
    "v_lshlrev_b32_sdwa v186, 1, v172 dst_sel:DWORD dst_unused:UNUSED_PAD src0_sel:DWORD src1_sel:WORD_1\n"
    "v_lshlrev_b32_sdwa v202, 1, v173 dst_sel:DWORD dst_unused:UNUSED_PAD src0_sel:DWORD src1_sel:WORD_1\n"
    "v_lshlrev_b32_sdwa v187, 1, v174 dst_sel:DWORD dst_unused:UNUSED_PAD src0_sel:DWORD src1_sel:WORD_1\n"
    "v_lshlrev_b32_sdwa v203, 1, v175 dst_sel:DWORD dst_unused:UNUSED_PAD src0_sel:DWORD src1_sel:WORD_1\n"
    "v_rcp_f16_e32 v232, v234\n"
    "v_mfma_f32_16x16x32_bf16 v[168:171], a[52:55], v[152:155], v[168:171]\n"
    "s_waitcnt lgkmcnt(4)\n"
    "ds_read_b128 a[240:243], v244 offset:36864\n"
    "ds_read_u16 v184, v184\n"
    "v_or_b32_e64 v198, v198, v206\n"
    "v_rcp_f16_sdwa v232, v234 dst_sel:WORD_1 dst_unused:UNUSED_PRESERVE src0_sel:WORD_1\n"
    "v_mfma_f32_16x16x32_bf16 v[172:175], a[92:95], v[144:147], 0\n"
    "ds_read_u16_d16_hi v200, v200\n"
    "ds_read_u16 v185, v185\n"
    "v_lshlrev_b32_sdwa v188, 1, v176 dst_sel:DWORD dst_unused:UNUSED_PAD src0_sel:DWORD src1_sel:WORD_1\n"
    "v_lshlrev_b32_sdwa v204, 1, v177 dst_sel:DWORD dst_unused:UNUSED_PAD src0_sel:DWORD src1_sel:WORD_1\n"
    "v_lshlrev_b32_sdwa v189, 1, v178 dst_sel:DWORD dst_unused:UNUSED_PAD src0_sel:DWORD src1_sel:WORD_1\n"
    "v_lshlrev_b32_sdwa v205, 1, v179 dst_sel:DWORD dst_unused:UNUSED_PAD src0_sel:DWORD src1_sel:WORD_1\n"
    "v_lshlrev_b32_sdwa v190, 1, v180 dst_sel:DWORD dst_unused:UNUSED_PAD src0_sel:DWORD src1_sel:WORD_1\n"
    "v_lshlrev_b32_sdwa v206, 1, v181 dst_sel:DWORD dst_unused:UNUSED_PAD src0_sel:DWORD src1_sel:WORD_1\n"
    "v_lshlrev_b32_sdwa v191, 1, v182 dst_sel:DWORD dst_unused:UNUSED_PAD src0_sel:DWORD src1_sel:WORD_1\n"
    "v_fma_mix_f32 v234, v192, v232, 0 op_sel_hi:[1,1,0]\n"
    "v_mfma_f32_16x16x32_bf16 v[172:175], a[140:143], v[148:151], v[172:175]\n"
    "ds_read_u16_d16_hi v201, v201\n"
    "ds_read_u16 v186, v186\n"
    "buffer_load_dwordx4 a[236:239], v245, s[40:43], s88 offen offset:64\n"
    "buffer_load_dwordx4 a[228:231], v245, s[40:43], s88 offen offset:0\n"
    "v_fma_mix_f32 v232, v192, v232, 0 op_sel:[1,1,0] op_sel_hi:[1,1,0]\n"
    "v_mfma_f32_16x16x32_bf16 v[176:179], a[88:91], v[156:159], 0\n"
    "s_waitcnt lgkmcnt(6)\n"
    "ds_read_b128 v[236:239], v244 offset:20480\n"
    "ds_read_u16_d16_hi v202, v202\n"
    "v_or_b32_e32 v199, v199, v207\n"
    "v_lshlrev_b32_sdwa v207, 1, v183 dst_sel:DWORD dst_unused:UNUSED_PAD src0_sel:DWORD src1_sel:WORD_1\n"
    "v_cvt_pk_bf16_f32 v192, v234, v232\n"
    "v_mfma_f32_16x16x32_bf16 v[176:179], a[136:139], v[160:163], v[176:179]\n"
    "s_waitcnt lgkmcnt(7)\n"
    "v_pk_fma_f16 v232, v196, v196, s48\n"
    "v_pk_mul_f16 v232, v196, v232\n"
    "v_mfma_f32_16x16x32_bf16 v[172:175], a[240:243], v[152:155], v[172:175]\n"
    "s_nop 0\n"
    "ds_read_u16 v187, v187\n"
    "v_exp_f16_e64 v234, v232\n"
    "v_mfma_f32_16x16x32_bf16 v[176:179], a[52:55], v[164:167], v[176:179]\n"
    "ds_read_b128 a[244:247], v244 offset:22528\n"
    "v_exp_f16_sdwa v234, v232 dst_sel:WORD_1 dst_unused:UNUSED_PRESERVE src0_sel:WORD_1\n"
    "v_mfma_f32_16x16x32_bf16 v[180:183], a[92:95], v[156:159], 0\n"
    "ds_read_u16_d16_hi v203, v203\n"
    "v_pk_fma_f16 v234, v234, s49, s49\n"
    "v_mfma_f32_16x16x32_bf16 v[180:183], a[140:143], v[160:163], v[180:183]\n"
    "s_waitcnt lgkmcnt(8)\n"
    "v_rcp_f16_e32 v232, v234\n"
    "v_mfma_f32_16x16x32_bf16 v[180:183], a[240:243], v[164:167], v[180:183]\n"
    "ds_read_u16 v188, v188\n"
    "v_rcp_f16_sdwa v232, v234 dst_sel:WORD_1 dst_unused:UNUSED_PRESERVE src0_sel:WORD_1\n"
    "v_mfma_f32_16x16x32_bf16 v[96:99], a[208:211], v[192:195], v[96:99]\n"
    "ds_read_u16_d16_hi v204, v204\n"
    "v_fma_mix_f32 v234, v196, v232, 0 op_sel_hi:[1,1,0]\n"
    "v_mfma_f32_16x16x32_bf16 v[100:103], a[212:215], v[192:195], v[100:103]\n"
    "v_fma_mix_f32 v232, v196, v232, 0 op_sel:[1,1,0] op_sel_hi:[1,1,0]\n"
    "v_or_b32_e32 v184, v184, v200\n"
    "v_mfma_f32_16x16x32_bf16 v[112:115], a[216:219], v[192:195], v[112:115]\n"
    "s_waitcnt lgkmcnt(8)\n"
    "ds_read_u16 v189, v189\n"
    "v_cvt_pk_bf16_f32 v196, v234, v232\n"
    "v_mfma_f32_16x16x32_bf16 v[116:119], a[220:223], v[192:195], v[116:119]\n"
    "s_waitcnt lgkmcnt(7)\n"
    "v_or_b32_e32 v185, v185, v201\n"
    "v_lshlrev_b32_sdwa v201, 1, v171 dst_sel:DWORD dst_unused:UNUSED_PAD src0_sel:DWORD src1_sel:WORD_1\n"
    "v_mfma_f32_16x16x32_bf16 v[128:131], v[236:239], v[192:195], v[128:131]\n"
    "ds_read_u16_d16_hi v205, v205\n"
    "v_cvt_pk_bf16_f32 v96, v96, v97\n"
    "v_mfma_f32_16x16x32_bf16 v[104:107], a[208:211], v[196:199], v[104:107]\n"
    "ds_read_u16 v190, v190\n"
    "v_cvt_pk_bf16_f32 v97, v98, v99\n"
    "v_mfma_f32_16x16x32_bf16 v[108:111], a[212:215], v[196:199], v[108:111]\n"
    "s_waitcnt lgkmcnt(8)\n"
    "v_or_b32_e32 v186, v186, v202\n"
    "v_lshlrev_b32_sdwa v202, 1, v173 dst_sel:DWORD dst_unused:UNUSED_PAD src0_sel:DWORD src1_sel:WORD_1\n"
    "v_mfma_f32_16x16x32_bf16 v[120:123], a[216:219], v[196:199], v[120:123]\n"
    "ds_read_u16_d16_hi v206, v206\n"
    "v_cvt_pk_bf16_f32 v98, v100, v101\n"
    "s_waitcnt lgkmcnt(7)\n"
    "v_mfma_f32_16x16x32_bf16 v[132:135], a[244:247], v[192:195], v[132:135]\n"
    "s_nop 0\n"
    "v_cvt_pk_f16_f32 v192, v168, v169\n"
    "v_lshlrev_b32_sdwa v193, 1, v170 dst_sel:DWORD dst_unused:UNUSED_PAD src0_sel:DWORD src1_sel:WORD_1\n"
    "v_mfma_f32_16x16x32_bf16 v[124:127], a[220:223], v[196:199], v[124:127]\n"
    "s_nop 0\n"
    "ds_read_u16 v191, v191\n"
    "v_lshlrev_b32_sdwa v194, 1, v172 dst_sel:DWORD dst_unused:UNUSED_PAD src0_sel:DWORD src1_sel:WORD_1\n"
    "v_mfma_f32_16x16x32_bf16 v[136:139], v[236:239], v[196:199], v[136:139]\n"
    "s_waitcnt lgkmcnt(7)\n"
    "ds_read_u16_d16_hi v207, v207\n"
    "v_lshlrev_b32_sdwa v195, 1, v174 dst_sel:DWORD dst_unused:UNUSED_PAD src0_sel:DWORD src1_sel:WORD_1\n"
    "v_mfma_f32_16x16x32_bf16 v[140:143], a[244:247], v[196:199], v[140:143]\n"
    "v_cvt_pk_f16_f32 v196, v176, v177\n"
    "v_lshlrev_b32_sdwa v197, 1, v178 dst_sel:DWORD dst_unused:UNUSED_PAD src0_sel:DWORD src1_sel:WORD_1\n"
    "v_mfma_f32_16x16x32_bf16 v[168:171], a[96:99], v[144:147], 0\n"
    "ds_read_u16 v193, v193\n"
    "v_lshlrev_b32_sdwa v198, 1, v180 dst_sel:DWORD dst_unused:UNUSED_PAD src0_sel:DWORD src1_sel:WORD_1\n"
    "v_mfma_f32_16x16x32_bf16 v[168:171], a[144:147], v[148:151], v[168:171]\n"
    "v_lshlrev_b32_sdwa v199, 1, v182 dst_sel:DWORD dst_unused:UNUSED_PAD src0_sel:DWORD src1_sel:WORD_1\n"
    "v_or_b32_e32 v187, v187, v203\n"
    "v_mfma_f32_16x16x32_bf16 v[168:171], a[56:59], v[152:155], v[168:171]\n"
    "s_waitcnt lgkmcnt(7)\n"
    "ds_read_u16_d16_hi v201, v201\n"
    "v_lshlrev_b32_sdwa v203, 1, v175 dst_sel:DWORD dst_unused:UNUSED_PAD src0_sel:DWORD src1_sel:WORD_1\n"
    "v_mfma_f32_16x16x32_bf16 v[172:175], a[100:103], v[144:147], 0\n"
    "v_or_b32_e64 v188, v188, v204\n"
    "v_cvt_pk_bf16_f32 v99, v102, v103\n"
    "v_mfma_f32_16x16x32_bf16 v[172:175], a[148:151], v[148:151], v[172:175]\n"
    "ds_read_u16 v194, v194\n"
    "v_cvt_pk_bf16_f32 v112, v112, v113\n"
    "v_mfma_f32_16x16x32_bf16 v[172:175], a[60:63], v[152:155], v[172:175]\n"
    "s_waitcnt lgkmcnt(7)\n"
    "ds_read_u16_d16_hi v202, v202\n"
    "v_or_b32_e32 v189, v189, v205\n"
    "v_lshlrev_b32_sdwa v205, 1, v179 dst_sel:DWORD dst_unused:UNUSED_PAD src0_sel:DWORD src1_sel:WORD_1\n"
    "v_mfma_f32_16x16x32_bf16 v[176:179], a[96:99], v[156:159], 0\n"
    "v_cvt_pk_bf16_f32 v113, v114, v115\n"
    "v_cvt_pk_bf16_f32 v114, v116, v117\n"
    "v_mfma_f32_16x16x32_bf16 v[176:179], a[144:147], v[160:163], v[176:179]\n"
    "ds_read_b128 v[236:239], v244 offset:0\n"
    "v_cvt_pk_bf16_f32 v115, v118, v119\n"
    "v_mfma_f32_16x16x32_bf16 v[176:179], a[56:59], v[164:167], v[176:179]\n"
    "ds_read_u16 v195, v195\n"
    "s_waitcnt lgkmcnt(8)\n"
    "v_or_b32_e32 v190, v190, v206\n"
    "v_mfma_f32_16x16x32_bf16 v[100:103], a[4:7], v[184:187], 0\n"
    "ds_read_u16_d16_hi v203, v203\n"
    "v_lshlrev_b32_sdwa v206, 1, v181 dst_sel:DWORD dst_unused:UNUSED_PAD src0_sel:DWORD src1_sel:WORD_1\n"
    "v_cvt_pk_bf16_f32 v128, v128, v129\n"
    "v_cvt_pk_bf16_f32 v129, v130, v131\n"
    "v_cvt_pk_bf16_f32 v130, v132, v133\n"
    "v_cvt_pk_bf16_f32 v131, v134, v135\n"
    "v_cvt_pk_bf16_f32 v104, v104, v105\n"
    "s_waitcnt vmcnt(23)\n"
    "ds_write2_b32 v248, v0, v1 offset0:0 offset1:20\n"
    "s_waitcnt lgkmcnt(8)\n"
    "v_or_b32_e64 v191, v191, v207\n"
    "v_mfma_f32_16x16x32_bf16 v[116:119], a[12:15], v[184:187], 0\n"
    "buffer_load_dwordx4 v[64:67], v246, s[40:43], s60 offen offset:128\n"
    "ds_write2_b32 v248, v2, v3 offset0:40 offset1:60\n"
    "v_add_u32_e32 v248, 1360, v248\n"
    "v_lshlrev_b32_sdwa v207, 1, v183 dst_sel:DWORD dst_unused:UNUSED_PAD src0_sel:DWORD src1_sel:WORD_1\n"
    "v_mfma_f32_16x16x32_bf16 v[180:183], a[100:103], v[156:159], 0\n"
    "ds_read_b128 a[244:247], v244 offset:2048\n"
    "s_waitcnt vmcnt(22)\n"
    "ds_write2_b32 v235, v20, v21 offset0:0 offset1:20\n"
    "v_mfma_f32_16x16x32_bf16 v[180:183], a[148:151], v[160:163], v[180:183]\n"
    "s_waitcnt lgkmcnt(9)\n"
    "v_or_b32_e64 v193, v193, v201\n"
    "v_cvt_pk_bf16_f32 v105, v106, v107\n"
    "v_mfma_f32_16x16x32_bf16 v[180:183], a[60:63], v[164:167], v[180:183]\n"
    "ds_read_u16 v197, v197\n"
    "ds_write2_b32 v235, v22, v23 offset0:40 offset1:60\n"
    "v_add_u32_e32 v235, 1360, v235\n"
    "v_cvt_pk_bf16_f32 v106, v108, v109\n"
    "v_cvt_pk_bf16_f32 v107, v110, v111\n"
    "v_mfma_f32_16x16x32_bf16 v[108:111], a[4:7], v[188:191], 0\n"
    "s_waitcnt lgkmcnt(8)\n"
    "ds_read_u16_d16_hi v205, v205\n"
    "s_waitcnt vmcnt(20)\n"
    "ds_write2_b32 v248, v4, v5 offset0:0 offset1:20\n"
    "v_or_b32_e64 v194, v194, v202\n"
    "v_cvt_pk_bf16_f32 v120, v120, v121\n"
    "v_cvt_pk_bf16_f32 v121, v122, v123\n"
    "v_cvt_pk_bf16_f32 v122, v124, v125\n"
    "ds_write2_b32 v248, v6, v7 offset0:40 offset1:60\n"
    "v_add_u32_e32 v248, 1360, v248\n"
    "v_cvt_pk_bf16_f32 v123, v126, v127\n"
    "ds_write_b128 v242, v[96:99] offset:24640\n"
    "v_mfma_f32_16x16x32_bf16 v[96:99], a[0:3], v[184:187], 0\n"
    "s_waitcnt lgkmcnt(6)\n"
    "v_cvt_pk_bf16_f32 v136, v136, v137\n"
    "v_pk_fma_f16 v232, v192, v192, s48\n"
    "v_mfma_f32_16x16x32_bf16 v[132:135], a[244:247], v[184:187], 0\n"
    "ds_read_u16 v198, v198\n"
    "s_waitcnt vmcnt(17)\n"
    "ds_write2_b32 v235, v24, v25 offset0:0 offset1:20\n"
    "v_mfma_f32_16x16x32_bf16 v[124:127], a[12:15], v[188:191], 0\n"
    "v_cvt_pk_bf16_f32 v137, v138, v139\n"
    "v_cvt_pk_bf16_f32 v138, v140, v141\n"
    "ds_write_b128 v242, v[112:115] offset:26688\n"
    "v_mfma_f32_16x16x32_bf16 v[112:115], a[8:11], v[184:187], 0\n"
    "s_waitcnt lgkmcnt(4)\n"
    "v_or_b32_e32 v195, v195, v203\n"
    "v_cvt_pk_bf16_f32 v139, v142, v143\n"
    "v_pk_mul_f16 v232, v192, v232\n"
    "v_or_b32_e64 v197, v197, v205\n"
    "ds_write2_b32 v235, v26, v27 offset0:40 offset1:60\n"
    "v_add_u32_e32 v235, -1360, v235\n"
    "v_exp_f16_e64 v234, v232\n"
    "v_mfma_f32_16x16x32_bf16 v[140:143], a[244:247], v[188:191], 0\n"
    "ds_read_u16_d16_hi v206, v206\n"
    "ds_write_b128 v242, v[128:131] offset:28736\n"
    "v_mfma_f32_16x16x32_bf16 v[128:131], v[236:239], v[184:187], 0\n"
    "buffer_load_dwordx4 v[68:71], v246, s[40:43], s61 offen offset:128\n"
    "v_exp_f16_sdwa v234, v232 dst_sel:WORD_1 dst_unused:UNUSED_PRESERVE src0_sel:WORD_1\n"
    "v_cvt_pk_f16_f32 v184, v168, v169\n"
    "v_pk_fma_f16 v234, v234, s49, s49\n"
    "v_lshlrev_b32_sdwa v185, 1, v170 dst_sel:DWORD dst_unused:UNUSED_PAD src0_sel:DWORD src1_sel:WORD_1\n"
    "v_lshlrev_b32_sdwa v201, 1, v171 dst_sel:DWORD dst_unused:UNUSED_PAD src0_sel:DWORD src1_sel:WORD_1\n"
    "v_mfma_f32_16x16x32_bf16 v[168:171], a[104:107], v[144:147], 0\n"
    "ds_read_u16 v199, v199\n"
    "ds_write_b128 v242, v[104:107] offset:25664\n"
    "v_mfma_f32_16x16x32_bf16 v[104:107], a[0:3], v[188:191], 0\n"
    "v_lshlrev_b32_sdwa v186, 1, v172 dst_sel:DWORD dst_unused:UNUSED_PAD src0_sel:DWORD src1_sel:WORD_1\n"
    "v_lshlrev_b32_sdwa v202, 1, v173 dst_sel:DWORD dst_unused:UNUSED_PAD src0_sel:DWORD src1_sel:WORD_1\n"
    "v_mfma_f32_16x16x32_bf16 v[168:171], a[152:155], v[148:151], v[168:171]\n"
    "v_lshlrev_b32_sdwa v187, 1, v174 dst_sel:DWORD dst_unused:UNUSED_PAD src0_sel:DWORD src1_sel:WORD_1\n"
    "v_lshlrev_b32_sdwa v203, 1, v175 dst_sel:DWORD dst_unused:UNUSED_PAD src0_sel:DWORD src1_sel:WORD_1\n"
    "v_rcp_f16_e64 v232, v234\n"
    "v_mfma_f32_16x16x32_bf16 v[168:171], a[64:67], v[152:155], v[168:171]\n"
    "ds_read_u16_d16_hi v207, v207\n"
    "s_waitcnt vmcnt(15)\n"
    "ds_write2_b32 v248, v8, v9 offset0:0 offset1:20\n"
    "s_waitcnt lgkmcnt(4)\n"
    "v_or_b32_e64 v198, v198, v206\n"
    "v_rcp_f16_sdwa v232, v234 dst_sel:WORD_1 dst_unused:UNUSED_PRESERVE src0_sel:WORD_1\n"
    "v_mfma_f32_16x16x32_bf16 v[172:175], a[108:111], v[144:147], 0\n"
    "ds_read_b128 v[216:219], v251 offset:0\n"
    "ds_write_b128 v242, v[120:123] offset:27712\n"
    "v_mfma_f32_16x16x32_bf16 v[120:123], a[8:11], v[188:191], 0\n"
    "v_lshlrev_b32_sdwa v205, 1, v179 dst_sel:DWORD dst_unused:UNUSED_PAD src0_sel:DWORD src1_sel:WORD_1\n"
    "v_lshlrev_b32_sdwa v206, 1, v181 dst_sel:DWORD dst_unused:UNUSED_PAD src0_sel:DWORD src1_sel:WORD_1\n"
    "v_mfma_f32_16x16x32_bf16 v[172:175], a[156:159], v[148:151], v[172:175]\n"
    "v_fma_mix_f32 v234, v192, v232, 0 op_sel_hi:[1,1,0]\n"
    "v_fma_mix_f32 v232, v192, v232, 0 op_sel:[1,1,0] op_sel_hi:[1,1,0]\n"
    "v_mfma_f32_16x16x32_bf16 v[172:175], a[68:71], v[152:155], v[172:175]\n"
    "ds_read_b128 v[220:223], v251 offset:336\n"
    "ds_write2_b32 v248, v10, v11 offset0:40 offset1:60\n"
    "v_add_u32_e32 v248, 1360, v248\n"
    "v_cvt_pk_bf16_f32 v192, v234, v232\n"
    "v_pk_fma_f16 v232, v196, v196, s48\n"
    "v_lshlrev_b32_sdwa v200, 1, v169 dst_sel:DWORD dst_unused:UNUSED_PAD src0_sel:DWORD src1_sel:WORD_1\n"
    "v_pk_mul_f16 v232, v196, v232\n"
    "ds_write_b128 v242, v[136:139] offset:29760\n"
    "v_mfma_f32_16x16x32_bf16 v[136:139], v[236:239], v[188:191], 0\n"
    "s_waitcnt lgkmcnt(4)\n"
    "v_cvt_pk_f16_f32 v188, v176, v177\n"
    "v_lshlrev_b32_sdwa v189, 1, v178 dst_sel:DWORD dst_unused:UNUSED_PAD src0_sel:DWORD src1_sel:WORD_1\n"
    "v_mfma_f32_16x16x32_bf16 v[176:179], a[104:107], v[156:159], 0\n"
    "ds_read_u16 v185, v185\n"
    "s_waitcnt vmcnt(12)\n"
    "ds_write2_b32 v248, v12, v13 offset0:0 offset1:20\n"
    "v_mfma_f32_16x16x32_bf16 v[176:179], a[152:155], v[160:163], v[176:179]\n"
    "v_lshlrev_b32_sdwa v190, 1, v180 dst_sel:DWORD dst_unused:UNUSED_PAD src0_sel:DWORD src1_sel:WORD_1\n"
    "v_lshlrev_b32_sdwa v191, 1, v182 dst_sel:DWORD dst_unused:UNUSED_PAD src0_sel:DWORD src1_sel:WORD_1\n"
    "v_mfma_f32_16x16x32_bf16 v[176:179], a[64:67], v[164:167], v[176:179]\n"
    "ds_read_u16_d16_hi v201, v201\n"
    "ds_write2_b32 v248, v14, v15 offset0:40 offset1:60\n"
    "v_add_u32_e32 v248, 1360, v248\n"
    "buffer_load_dwordx4 v[72:75], v246, s[40:43], s62 offen offset:128\n"
    "v_or_b32_e32 v199, v199, v207\n"
    "s_waitcnt vmcnt(10)\n"
    "ds_write2_b32 v248, v16, v17 offset0:0 offset1:20\n"
    "v_lshlrev_b32_sdwa v207, 1, v183 dst_sel:DWORD dst_unused:UNUSED_PAD src0_sel:DWORD src1_sel:WORD_1\n"
    "v_mfma_f32_16x16x32_bf16 v[180:183], a[108:111], v[156:159], 0\n"
    "v_exp_f16_e64 v234, v232\n"
    "v_mfma_f32_16x16x32_bf16 v[180:183], a[156:159], v[160:163], v[180:183]\n"
    "ds_read_u16 v186, v186\n"
    "ds_write2_b32 v248, v18, v19 offset0:40 offset1:60\n"
    "v_mfma_f32_32x32x1_2b_f32 v[0:31], v254, v254, 0\n"
    "v_add_u32_e32 v248, -5440, v248\n"
    "v_exp_f16_sdwa v234, v232 dst_sel:WORD_1 dst_unused:UNUSED_PRESERVE src0_sel:WORD_1\n"
    "v_lshlrev_b32_sdwa v204, 1, v177 dst_sel:DWORD dst_unused:UNUSED_PAD src0_sel:DWORD src1_sel:WORD_1\n"
    "v_pk_fma_f16 v234, v234, s49, s49\n"
    "v_rcp_f16_e64 v232, v234\n"
    "ds_read_u16_d16_hi v202, v202\n"
    "ds_read_b128 v[236:239], v244 offset:4096\n"
    "v_rcp_f16_sdwa v232, v234 dst_sel:WORD_1 dst_unused:UNUSED_PRESERVE src0_sel:WORD_1\n"
    "ds_read_u16 v187, v187\n"
    "ds_read_u16_d16_hi v203, v203\n"
    "v_mfma_f32_16x16x32_bf16 v[180:183], a[68:71], v[164:167], v[180:183]\n"
    "s_waitcnt lgkmcnt(6)\n"
    "ds_read_b128 a[244:247], v244 offset:6144\n"
    "ds_read_u16 v189, v189\n"
    "v_mfma_f32_16x16x32_bf16 v[96:99], a[16:19], v[192:195], v[96:99]\n"
    "v_or_b32_e32 v185, v185, v201\n"
    "v_fma_mix_f32 v234, v196, v232, 0 op_sel_hi:[1,1,0]\n"
    "v_mfma_f32_16x16x32_bf16 v[100:103], a[20:23], v[192:195], v[100:103]\n"
    "ds_read_u16_d16_hi v205, v205\n"
    "v_fma_mix_f32 v232, v196, v232, 0 op_sel:[1,1,0] op_sel_hi:[1,1,0]\n"
    "v_mfma_f32_16x16x32_bf16 v[112:115], a[24:27], v[192:195], v[112:115]\n"
    "s_waitcnt lgkmcnt(7)\n"
    "s_barrier\n"
    "v_mfma_f32_16x16x32_bf16 v[116:119], a[28:31], v[192:195], v[116:119]\n"
    "ds_read_b128 v[208:211], v250 offset:0\n"
    "ds_read_b128 v[212:215], v250 offset:320\n"
    "v_cvt_pk_bf16_f32 v196, v234, v232\n"
    "v_pk_fma_f16 v232, v184, v184, s48\n"
    "v_lshlrev_b32_sdwa v201, 1, v171 dst_sel:DWORD dst_unused:UNUSED_PAD src0_sel:DWORD src1_sel:WORD_1\n"
    "v_mfma_f32_16x16x32_bf16 v[104:107], a[16:19], v[196:199], v[104:107]\n"
    "s_waitcnt lgkmcnt(7)\n"
    "ds_read_u16 v190, v190\n"
    "v_or_b32_e32 v186, v186, v202\n"
    "v_mfma_f32_16x16x32_bf16 v[128:131], v[236:239], v[192:195], v[128:131]\n"
    "ds_read_u16_d16_hi v206, v206\n"
    "v_pk_mul_f16 v232, v184, v232\n"
    "v_mfma_f32_16x16x32_bf16 v[108:111], a[20:23], v[196:199], v[108:111]\n"
    "v_exp_f16_e64 v234, v232\n"
    "v_mfma_f32_16x16x32_bf16 v[120:123], a[24:27], v[196:199], v[120:123]\n"
    "ds_read_u16 v191, v191\n"
    "v_exp_f16_sdwa v234, v232 dst_sel:WORD_1 dst_unused:UNUSED_PRESERVE src0_sel:WORD_1\n"
    "v_mfma_f32_16x16x32_bf16 v[124:127], a[28:31], v[196:199], v[124:127]\n"
    "s_waitcnt lgkmcnt(7)\n"
    "ds_read_u16_d16_hi v207, v207\n"
    "v_or_b32_e32 v187, v187, v203\n"
    "v_mfma_f32_16x16x32_bf16 v[132:135], a[244:247], v[192:195], v[132:135]\n"
    "v_pk_fma_f16 v234, v234, s49, s49\n"
    "v_lshlrev_b32_sdwa v192, 1, v168 dst_sel:DWORD dst_unused:UNUSED_PAD src0_sel:DWORD src1_sel:WORD_1\n"
    "v_mfma_f32_16x16x32_bf16 v[136:139], v[236:239], v[196:199], v[136:139]\n"
    "ds_read_b128 a[240:243], v244 offset:34816\n"
    "v_lshlrev_b32_sdwa v193, 1, v170 dst_sel:DWORD dst_unused:UNUSED_PAD src0_sel:DWORD src1_sel:WORD_1\n"
    "v_mfma_f32_16x16x32_bf16 v[140:143], a[244:247], v[196:199], v[140:143]\n"
    "buffer_load_dwordx4 v[76:79], v246, s[40:43], s63 offen offset:128\n"
    "ds_read_u16 v192, v192\n"
    "v_mfma_f32_16x16x32_bf16 v[168:171], a[112:115], v[144:147], 0\n"
    "v_lshlrev_b32_sdwa v194, 1, v172 dst_sel:DWORD dst_unused:UNUSED_PAD src0_sel:DWORD src1_sel:WORD_1\n"
    "v_lshlrev_b32_sdwa v202, 1, v173 dst_sel:DWORD dst_unused:UNUSED_PAD src0_sel:DWORD src1_sel:WORD_1\n"
    "v_mfma_f32_16x16x32_bf16 v[168:171], a[160:163], v[148:151], v[168:171]\n"
    "s_waitcnt lgkmcnt(7)\n"
    "ds_read_u16_d16_hi v200, v200\n"
    "v_or_b32_e32 v189, v189, v205\n"
    "s_waitcnt lgkmcnt(7)\n"
    "v_smfmac_f32_16x16x64_bf16 v[0:3], v[216:219], v[208:215], v240\n"
    "s_nop 0\n"
    "ds_read_b128 v[216:219], v252 offset:0\n"
    "v_smfmac_f32_16x16x64_bf16 v[4:7], v[220:223], v[208:215], v240 abid:1\n"
    "ds_read_b128 v[208:211], v250 offset:640\n"
    "v_lshlrev_b32_sdwa v195, 1, v174 dst_sel:DWORD dst_unused:UNUSED_PAD src0_sel:DWORD src1_sel:WORD_1\n"
    "ds_read_b128 v[212:215], v250 offset:960\n"
    "v_lshlrev_b32_sdwa v203, 1, v175 dst_sel:DWORD dst_unused:UNUSED_PAD src0_sel:DWORD src1_sel:WORD_1\n"
    "ds_read_b128 v[220:223], v252 offset:336\n"
    "v_rcp_f16_e32 v232, v234\n"
    "v_mfma_f32_16x16x32_bf16 v[168:171], a[72:75], v[152:155], v[168:171]\n"
    "s_waitcnt lgkmcnt(9)\n"
    "ds_read_u16 v193, v193\n"
    "v_or_b32_e64 v190, v190, v206\n"
    "v_rcp_f16_sdwa v232, v234 dst_sel:WORD_1 dst_unused:UNUSED_PRESERVE src0_sel:WORD_1\n"
    "v_mfma_f32_16x16x32_bf16 v[172:175], a[116:119], v[144:147], 0\n"
    "ds_read_u16_d16_hi v201, v201\n"
    "v_lshlrev_b32_sdwa v196, 1, v176 dst_sel:DWORD dst_unused:UNUSED_PAD src0_sel:DWORD src1_sel:WORD_1\n"
    "v_lshlrev_b32_sdwa v197, 1, v178 dst_sel:DWORD dst_unused:UNUSED_PAD src0_sel:DWORD src1_sel:WORD_1\n"
    "v_lshlrev_b32_sdwa v205, 1, v179 dst_sel:DWORD dst_unused:UNUSED_PAD src0_sel:DWORD src1_sel:WORD_1\n"
    "v_lshlrev_b32_sdwa v198, 1, v180 dst_sel:DWORD dst_unused:UNUSED_PAD src0_sel:DWORD src1_sel:WORD_1\n"
    "v_lshlrev_b32_sdwa v206, 1, v181 dst_sel:DWORD dst_unused:UNUSED_PAD src0_sel:DWORD src1_sel:WORD_1\n"
    "v_lshlrev_b32_sdwa v199, 1, v182 dst_sel:DWORD dst_unused:UNUSED_PAD src0_sel:DWORD src1_sel:WORD_1\n"
    "v_fma_mix_f32 v234, v184, v232, 0 op_sel_hi:[1,1,0]\n"
    "v_mfma_f32_16x16x32_bf16 v[172:175], a[164:167], v[148:151], v[172:175]\n"
    "s_waitcnt lgkmcnt(8)\n"
    "ds_read_u16 v194, v194\n"
    "v_or_b32_e32 v191, v191, v207\n"
    "v_mfma_f32_16x16x32_bf16 v[172:175], a[240:243], v[152:155], v[172:175]\n"
    "ds_read_b128 v[236:239], v244 offset:8192\n"
    "v_lshlrev_b32_sdwa v207, 1, v183 dst_sel:DWORD dst_unused:UNUSED_PAD src0_sel:DWORD src1_sel:WORD_1\n"
    "v_fma_mix_f32 v232, v184, v232, 0 op_sel:[1,1,0] op_sel_hi:[1,1,0]\n"
    "v_mfma_f32_16x16x32_bf16 v[176:179], a[112:115], v[156:159], 0\n"
    "ds_read_u16_d16_hi v202, v202\n"
    "v_cvt_pk_bf16_f32 v184, v234, v232\n"
    "v_mfma_f32_16x16x32_bf16 v[176:179], a[160:163], v[160:163], v[176:179]\n"
    "ds_read_u16 v195, v195\n"
    "v_pk_fma_f16 v232, v188, v188, s48\n"
    "v_mfma_f32_16x16x32_bf16 v[176:179], a[72:75], v[164:167], v[176:179]\n"
    "s_waitcnt lgkmcnt(8)\n"
    "v_pk_mul_f16 v232, v188, v232\n"
    "v_or_b32_e64 v192, v192, v200\n"
    "v_mfma_f32_16x16x32_bf16 v[180:183], a[116:119], v[156:159], 0\n"
    "s_waitcnt lgkmcnt(7)\n"
    "ds_read_b128 a[244:247], v244 offset:10240\n"
    "v_exp_f16_e32 v234, v232\n"
    "v_smfmac_f32_16x16x64_bf16 v[0:3], v[216:219], v[208:215], v240\n"
    "s_waitcnt lgkmcnt(7)\n"
    "ds_read_b128 v[216:219], v251 offset:672\n"
    "ds_read_u16_d16_hi v203, v203\n"
    "v_smfmac_f32_16x16x64_bf16 v[4:7], v[220:223], v[208:215], v240 abid:1\n"
    "s_nop 0\n"
    "v_exp_f16_sdwa v234, v232 dst_sel:WORD_1 dst_unused:UNUSED_PRESERVE src0_sel:WORD_1\n"
    "s_waitcnt vmcnt(5)\n"
    "v_smfmac_f32_32x32x32_bf16 v[112:127], a[248:251], a[232:239], v241\n"
    "ds_read_b128 v[208:211], v250 offset:80\n"
    "ds_read_b128 v[212:215], v250 offset:400\n"
    "v_pk_fma_f16 v234, v234, s49, s49\n"
    "v_rcp_f16_e64 v232, v234\n"
    "s_waitcnt vmcnt(4)\n"
    "v_smfmac_f32_32x32x32_bf16 v[96:111], a[248:251], a[224:231], v241\n"
    "s_waitcnt lgkmcnt(9)\n"
    "ds_read_b128 v[220:223], v251 offset:1008\n"
    "buffer_load_dwordx4 v[80:83], v246, s[40:43], s52 offen offset:128\n"
    "v_rcp_f16_sdwa v232, v234 dst_sel:WORD_1 dst_unused:UNUSED_PRESERVE src0_sel:WORD_1\n"
    "v_or_b32_e64 v193, v193, v201\n"
    "v_fma_mix_f32 v234, v188, v232, 0 op_sel_hi:[1,1,0]\n"
    "v_fma_mix_f32 v232, v188, v232, 0 op_sel:[1,1,0] op_sel_hi:[1,1,0]\n"
    "v_lshlrev_b32_sdwa v201, 1, v171 dst_sel:DWORD dst_unused:UNUSED_PAD src0_sel:DWORD src1_sel:WORD_1\n"
    "v_pk_mov_b32 v[224:225], v[0:1], v[0:1] op_sel:[0,1]\n"
    "v_pk_mov_b32 v[226:227], v[2:3], v[2:3] op_sel:[0,1]\n"
    "v_pk_mul_f32 v[228:229], v[0:1], v[0:1]\n"
    "v_pk_mul_f32 v[230:231], v[2:3], v[2:3]\n"
    "v_pk_add_f32 v[224:225], v[224:225], v[4:5]\n"
    "v_pk_add_f32 v[226:227], v[226:227], v[6:7]\n"
    "v_pk_fma_f32 v[228:229], v[4:5], v[4:5], v[228:229]\n"
    "v_pk_fma_f32 v[230:231], v[6:7], v[6:7], v[230:231]\n"
    "v_mfma_f32_16x16x32_bf16 v[180:183], a[164:167], v[160:163], v[180:183]\n"
    "s_waitcnt lgkmcnt(7)\n"
    "ds_read_b128 a[232:235], v243 offset:24640\n"
    "ds_read_b128 a[236:239], v243 offset:25664\n"
    "v_mfma_f32_16x16x32_bf16 v[180:183], a[240:243], v[164:167], v[180:183]\n"
    "s_waitcnt lgkmcnt(7)\n"
    "ds_read_u16 v196, v196\n"
    "ds_read_u16_d16_hi v204, v204\n"
    "v_mfma_f32_16x16x32_bf16 v[112:115], a[40:43], v[184:187], v[112:115]\n"
    "ds_read_u16 v197, v197\n"
    "v_cvt_pk_bf16_f32 v188, v234, v232\n"
    "v_mfma_f32_16x16x32_bf16 v[116:119], a[44:47], v[184:187], v[116:119]\n"
    "v_or_b32_e64 v194, v194, v202\n"
    "v_lshlrev_b32_sdwa v202, 1, v173 dst_sel:DWORD dst_unused:UNUSED_PAD src0_sel:DWORD src1_sel:WORD_1\n"
    "v_mfma_f32_16x16x32_bf16 v[128:131], v[236:239], v[184:187], v[128:131]\n"
    "s_nop 0\n"
    "ds_read_u16_d16_hi v205, v205\n"
    "v_mfma_f32_16x16x32_bf16 v[132:135], a[244:247], v[184:187], v[132:135]\n"
    "s_waitcnt lgkmcnt(8)\n"
    "ds_read_u16 v198, v198\n"
    "buffer_load_dwordx4 a[224:227], v245, s[40:43], s57 offen offset:128\n"
    "s_waitcnt lgkmcnt(8)\n"
    "v_smfmac_f32_16x16x64_bf16 v[8:11], v[216:219], v[208:215], v240\n"
    "s_waitcnt lgkmcnt(7)\n"
    "v_or_b32_e64 v195, v195, v203\n"
    "v_smfmac_f32_16x16x64_bf16 v[12:15], v[220:223], v[208:215], v240 abid:1\n"
    "ds_read_b128 v[208:211], v250 offset:720\n"
    "ds_read_b128 v[212:215], v250 offset:1040\n"
    "v_mfma_f32_16x16x32_bf16 v[120:123], a[40:43], v[188:191], v[120:123]\n"
    "v_lshlrev_b32_sdwa v203, 1, v175 dst_sel:DWORD dst_unused:UNUSED_PAD src0_sel:DWORD src1_sel:WORD_1\n"
    "ds_read_b128 v[216:219], v252 offset:672\n"
    "s_waitcnt lgkmcnt(8)\n"
    "v_smfmac_f32_32x32x32_bf16 v[96:111], a[248:251], a[232:239], v241\n"
    "s_nop 0\n"
    "ds_read_b128 v[220:223], v252 offset:1008\n"
    "ds_read_u16_d16_hi v206, v206\n"
    "v_mfma_f32_16x16x32_bf16 v[124:127], a[44:47], v[188:191], v[124:127]\n"
    "v_mfma_f32_16x16x32_bf16 v[136:139], v[236:239], v[188:191], v[136:139]\n"
    "ds_read_u16 v199, v199\n"
    "v_mfma_f32_16x16x32_bf16 v[140:143], a[244:247], v[188:191], v[140:143]\n"
    "s_waitcnt lgkmcnt(9)\n"
    "ds_read_u16_d16_hi v207, v207\n"
    "v_or_b32_e32 v196, v196, v204\n"
    "buffer_load_dwordx4 a[228:231], v245, s[40:43], s88 offen offset:128\n"
    "v_mfma_f32_16x16x32_bf16 v[96:99], a[32:35], v[184:187], v[96:99]\n"
    "ds_read_b128 a[232:235], v243 offset:26688\n"
    "v_mfma_f32_16x16x32_bf16 v[100:103], a[36:39], v[184:187], v[100:103]\n"
    "s_nop 0\n"
    "ds_read_b128 a[236:239], v243 offset:27712\n"
    "v_cvt_pk_f16_f32 v184, v168, v169\n"
    "v_mfma_f32_16x16x32_bf16 v[104:107], a[32:35], v[188:191], v[104:107]\n"
    "s_waitcnt lgkmcnt(10)\n"
    "ds_read_b128 a[240:243], v244 offset:38912\n"
    "v_lshlrev_b32_sdwa v185, 1, v170 dst_sel:DWORD dst_unused:UNUSED_PAD src0_sel:DWORD src1_sel:WORD_1\n"
    "v_mfma_f32_16x16x32_bf16 v[108:111], a[36:39], v[188:191], v[108:111]\n"
    "ds_read_u16 v185, v185\n"
    "v_lshlrev_b32_sdwa v186, 1, v172 dst_sel:DWORD dst_unused:UNUSED_PAD src0_sel:DWORD src1_sel:WORD_1\n"
    "v_mfma_f32_16x16x32_bf16 v[168:171], a[120:123], v[144:147], 0\n"
    "v_lshlrev_b32_sdwa v187, 1, v174 dst_sel:DWORD dst_unused:UNUSED_PAD src0_sel:DWORD src1_sel:WORD_1\n"
    "v_cvt_pk_f16_f32 v188, v176, v177\n"
    "v_mfma_f32_16x16x32_bf16 v[168:171], a[168:171], v[148:151], v[168:171]\n"
    "s_waitcnt lgkmcnt(8)\n"
    "ds_read_u16_d16_hi v201, v201\n"
    "v_lshlrev_b32_sdwa v189, 1, v178 dst_sel:DWORD dst_unused:UNUSED_PAD src0_sel:DWORD src1_sel:WORD_1\n"
    "v_smfmac_f32_16x16x64_bf16 v[8:11], v[216:219], v[208:215], v240\n"
    "s_waitcnt lgkmcnt(8)\n"
    "v_lshlrev_b32_sdwa v190, 1, v180 dst_sel:DWORD dst_unused:UNUSED_PAD src0_sel:DWORD src1_sel:WORD_1\n"
    "v_smfmac_f32_16x16x64_bf16 v[12:15], v[220:223], v[208:215], v240 abid:1\n"
    "ds_read_b128 v[208:211], v250 offset:160\n"
    "ds_read_b128 v[212:215], v250 offset:480\n"
    "v_mfma_f32_16x16x32_bf16 v[168:171], a[76:79], v[152:155], v[168:171]\n"
    "buffer_load_dwordx4 v[84:87], v247, s[40:43], s60 offen offset:128\n"
    "ds_read_b128 v[216:219], v251 offset:1344\n"
    "v_lshlrev_b32_sdwa v191, 1, v182 dst_sel:DWORD dst_unused:UNUSED_PAD src0_sel:DWORD src1_sel:WORD_1\n"
    "v_or_b32_e64 v197, v197, v205\n"
    "v_lshlrev_b32_sdwa v205, 1, v179 dst_sel:DWORD dst_unused:UNUSED_PAD src0_sel:DWORD src1_sel:WORD_1\n"
    "v_pk_add_f32 v[224:225], v[224:225], v[8:9]\n"
    "v_pk_add_f32 v[226:227], v[226:227], v[10:11]\n"
    "v_pk_fma_f32 v[228:229], v[8:9], v[8:9], v[228:229]\n"
    "v_pk_fma_f32 v[230:231], v[10:11], v[10:11], v[230:231]\n"
    "v_pk_add_f32 v[224:225], v[224:225], v[12:13]\n"
    "v_pk_add_f32 v[226:227], v[226:227], v[14:15]\n"
    "v_pk_fma_f32 v[228:229], v[12:13], v[12:13], v[228:229]\n"
    "v_pk_fma_f32 v[230:231], v[14:15], v[14:15], v[230:231]\n"
    "v_mfma_f32_16x16x32_bf16 v[172:175], a[124:127], v[144:147], 0\n"
    "s_waitcnt lgkmcnt(9)\n"
    "ds_read_b128 v[220:223], v251 offset:1680\n"
    "ds_read_u16 v186, v186\n"
    "v_mfma_f32_16x16x32_bf16 v[172:175], a[172:175], v[148:151], v[172:175]\n"
    "s_waitcnt lgkmcnt(8)\n"
    "ds_read_u16_d16_hi v202, v202\n"
    "v_or_b32_e32 v198, v198, v206\n"
    "v_smfmac_f32_32x32x32_bf16 v[112:127], a[248:251], a[232:239], v241\n"
    "s_waitcnt lgkmcnt(8)\n"
    "ds_read_b128 v[236:239], v244 offset:12288\n"
    "v_lshlrev_b32_sdwa v206, 1, v181 dst_sel:DWORD dst_unused:UNUSED_PAD src0_sel:DWORD src1_sel:WORD_1\n"
    "v_or_b32_e64 v199, v199, v207\n"
    "v_lshlrev_b32_sdwa v207, 1, v183 dst_sel:DWORD dst_unused:UNUSED_PAD src0_sel:DWORD src1_sel:WORD_1\n"
    "v_pk_fma_f16 v232, v184, v184, s48\n"
    "v_mfma_f32_16x16x32_bf16 v[172:175], a[240:243], v[152:155], v[172:175]\n"
    "ds_read_u16 v187, v187\n"
    "v_pk_mul_f16 v232, v184, v232\n"
    "v_mfma_f32_16x16x32_bf16 v[176:179], a[120:123], v[156:159], 0\n"
    "ds_read_u16_d16_hi v203, v203\n"
    "v_exp_f16_e32 v234, v232\n"
    "v_mfma_f32_16x16x32_bf16 v[176:179], a[168:171], v[160:163], v[176:179]\n"
    "v_exp_f16_sdwa v234, v232 dst_sel:WORD_1 dst_unused:UNUSED_PRESERVE src0_sel:WORD_1\n"
    "v_mfma_f32_16x16x32_bf16 v[176:179], a[76:79], v[164:167], v[176:179]\n"
    "s_waitcnt lgkmcnt(8)\n"
    "ds_read_b128 a[244:247], v244 offset:14336\n"
    "ds_read_u16 v189, v189\n"
    "v_mfma_f32_16x16x32_bf16 v[180:183], a[124:127], v[156:159], 0\n"
    "v_or_b32_e64 v185, v185, v201\n"
    "v_pk_fma_f16 v234, v234, s49, s49\n"
    "s_waitcnt lgkmcnt(8)\n"
    "v_smfmac_f32_16x16x64_bf16 v[16:19], v[216:219], v[208:215], v240\n"
    "s_waitcnt lgkmcnt(7)\n"
    "ds_read_b128 v[216:219], v252 offset:1344\n"
    "v_smfmac_f32_16x16x64_bf16 v[20:23], v[220:223], v[208:215], v240 abid:1\n"
    "ds_read_b128 v[208:211], v250 offset:800\n"
    "v_mfma_f32_16x16x32_bf16 v[180:183], a[172:175], v[160:163], v[180:183]\n"
    "ds_read_b128 v[212:215], v250 offset:1120\n"
    "v_lshlrev_b32_e64 v254, 3, v244\n"
    "v_mfma_f32_16x16x32_bf16 v[180:183], a[240:243], v[164:167], v[180:183]\n"
    "ds_read_b128 v[220:223], v252 offset:1680\n"
    "v_lshlrev_b32_sdwa v201, 1, v171 dst_sel:DWORD dst_unused:UNUSED_PAD src0_sel:DWORD src1_sel:WORD_1\n"
    "v_mfma_f32_16x16x32_bf16 v[96:99], a[176:179], v[192:195], v[96:99]\n"
    "ds_read_b128 a[232:235], v243 offset:28736\n"
    "v_rcp_f16_e32 v232, v234\n"
    "v_mfma_f32_16x16x32_bf16 v[100:103], a[180:183], v[192:195], v[100:103]\n"
    "s_waitcnt lgkmcnt(9)\n"
    "ds_read_u16_d16_hi v205, v205\n"
    "buffer_load_dwordx4 v[88:91], v247, s[40:43], s61 offen offset:128\n"
    "v_mfma_f32_16x16x32_bf16 v[112:115], a[184:187], v[192:195], v[112:115]\n"
    "v_or_b32_e64 v186, v186, v202\n"
    "v_lshlrev_b32_sdwa v202, 1, v173 dst_sel:DWORD dst_unused:UNUSED_PAD src0_sel:DWORD src1_sel:WORD_1\n"
    "v_mfma_f32_16x16x32_bf16 v[116:119], a[188:191], v[192:195], v[116:119]\n"
    "ds_read_b128 a[236:239], v243 offset:29760\n"
    "ds_read_u16 v190, v190\n"
    "v_mfma_f32_16x16x32_bf16 v[128:131], v[236:239], v[192:195], v[128:131]\n"
    "s_nop 0\n"
    "v_rcp_f16_sdwa v232, v234 dst_sel:WORD_1 dst_unused:UNUSED_PRESERVE src0_sel:WORD_1\n"
    "v_mfma_f32_16x16x32_bf16 v[104:107], a[176:179], v[196:199], v[104:107]\n"
    "s_waitcnt lgkmcnt(9)\n"
    "ds_read_b128 v[144:147], v242 offset:0\n"
    "v_or_b32_e64 v187, v187, v203\n"
    "v_mfma_f32_16x16x32_bf16 v[132:135], a[244:247], v[192:195], v[132:135]\n"
    "ds_read_u16_d16_hi v206, v206\n"
    "v_cvt_pk_f16_f32 v192, v168, v169\n"
    "v_mfma_f32_16x16x32_bf16 v[108:111], a[180:183], v[196:199], v[108:111]\n"
    "v_lshlrev_b32_sdwa v193, 1, v170 dst_sel:DWORD dst_unused:UNUSED_PAD src0_sel:DWORD src1_sel:WORD_1\n"
    "v_lshlrev_b32_sdwa v194, 1, v172 dst_sel:DWORD dst_unused:UNUSED_PAD src0_sel:DWORD src1_sel:WORD_1\n"
    "v_mfma_f32_16x16x32_bf16 v[120:123], a[184:187], v[196:199], v[120:123]\n"
    "ds_read_b128 v[148:151], v242 offset:1024\n"
    "v_lshlrev_b32_sdwa v195, 1, v174 dst_sel:DWORD dst_unused:UNUSED_PAD src0_sel:DWORD src1_sel:WORD_1\n"
    "v_mfma_f32_16x16x32_bf16 v[124:127], a[188:191], v[196:199], v[124:127]\n"
    "ds_read_u16 v191, v191\n"
    "v_lshlrev_b32_sdwa v203, 1, v175 dst_sel:DWORD dst_unused:UNUSED_PAD src0_sel:DWORD src1_sel:WORD_1\n"
    "s_waitcnt lgkmcnt(9)\n"
    "v_smfmac_f32_16x16x64_bf16 v[16:19], v[216:219], v[208:215], v240\n"
    "s_waitcnt lgkmcnt(8)\n"
    "ds_read_b128 v[216:219], v251 offset:2016\n"
    "s_mov_b32 exec_hi, 0\n"
    "buffer_load_dword v233, v254, s[72:75], s53 offen sc1\n"
    "s_mov_b32 exec_hi, -1\n"
    "v_smfmac_f32_16x16x64_bf16 v[20:23], v[220:223], v[208:215], v240 abid:1\n"
    "ds_read_b128 v[208:211], v250 offset:240\n"
    "ds_read_b128 v[212:215], v250 offset:560\n"
    "v_fma_mix_f32 v234, v184, v232, 0 op_sel_hi:[1,1,0]\n"
    "v_fma_mix_f32 v232, v184, v232, 0 op_sel:[1,1,0] op_sel_hi:[1,1,0]\n"
    "v_pk_add_f32 v[224:225], v[224:225], v[16:17]\n"
    "v_pk_add_f32 v[226:227], v[226:227], v[18:19]\n"
    "v_pk_fma_f32 v[228:229], v[16:17], v[16:17], v[228:229]\n"
    "v_pk_fma_f32 v[230:231], v[18:19], v[18:19], v[230:231]\n"
    "v_pk_add_f32 v[224:225], v[224:225], v[20:21]\n"
    "v_pk_add_f32 v[226:227], v[226:227], v[22:23]\n"
    "v_pk_fma_f32 v[228:229], v[20:21], v[20:21], v[228:229]\n"
    "v_pk_fma_f32 v[230:231], v[22:23], v[22:23], v[230:231]\n"
    "v_mfma_f32_16x16x32_bf16 v[136:139], v[236:239], v[196:199], v[136:139]\n"
    "s_waitcnt lgkmcnt(8)\n"
    "ds_read_b128 v[220:223], v251 offset:2352\n"
    "ds_read_b128 v[152:155], v242 offset:2048\n"
    "v_mfma_f32_16x16x32_bf16 v[140:143], a[244:247], v[196:199], v[140:143]\n"
    "s_nop 0\n"
    "ds_read_u16_d16_hi v207, v207\n"
    "v_or_b32_e32 v189, v189, v205\n"
    "s_waitcnt lgkmcnt(9)\n"
    "v_mfma_f32_16x16x32_bf16 v[168:171], a[80:83], v[144:147], 0\n"
    "ds_read_b128 v[156:159], v242 offset:3072\n"
    "ds_read_b128 v[160:163], v242 offset:4096\n"
    "v_smfmac_f32_32x32x32_bf16 v[128:143], a[248:251], a[232:239], v241\n"
    "ds_read_b128 v[164:167], v242 offset:5120\n"
    "v_cvt_pk_f16_f32 v196, v176, v177\n"
    "v_lshlrev_b32_sdwa v197, 1, v178 dst_sel:DWORD dst_unused:UNUSED_PAD src0_sel:DWORD src1_sel:WORD_1\n"
    "s_waitcnt lgkmcnt(10)\n"
    "v_or_b32_e32 v190, v190, v206\n"
    "v_mfma_f32_16x16x32_bf16 v[168:171], a[128:131], v[148:151], v[168:171]\n"
    "ds_read_b128 a[240:243], v244 offset:32768\n"
    "ds_read_u16 v193, v193\n"
    "v_mfma_f32_16x16x32_bf16 v[172:175], a[84:87], v[144:147], 0\n"
    "v_lshlrev_b32_sdwa v205, 1, v179 dst_sel:DWORD dst_unused:UNUSED_PAD src0_sel:DWORD src1_sel:WORD_1\n"
    "v_lshlrev_b32_sdwa v198, 1, v180 dst_sel:DWORD dst_unused:UNUSED_PAD src0_sel:DWORD src1_sel:WORD_1\n"
    "v_mfma_f32_16x16x32_bf16 v[172:175], a[132:135], v[148:151], v[172:175]\n"
    "ds_read_u16_d16_hi v201, v201\n"
    "v_lshlrev_b32_sdwa v206, 1, v181 dst_sel:DWORD dst_unused:UNUSED_PAD src0_sel:DWORD src1_sel:WORD_1\n"
    "v_lshlrev_b32_sdwa v199, 1, v182 dst_sel:DWORD dst_unused:UNUSED_PAD src0_sel:DWORD src1_sel:WORD_1\n"
    "v_cvt_pk_bf16_f32 v184, v234, v232\n"
    "v_pk_fma_f16 v232, v188, v188, s48\n"
    "v_mov_b32_e64 v254, 0\n"
    "s_waitcnt lgkmcnt(9)\n"
    "v_smfmac_f32_16x16x64_bf16 v[24:27], v[216:219], v[208:215], v240\n"
    "s_waitcnt lgkmcnt(8)\n"
    "ds_read_b128 v[216:219], v252 offset:2016\n"
    "ds_read_u16 v194, v194\n"
    "v_smfmac_f32_16x16x64_bf16 v[28:31], v[220:223], v[208:215], v240 abid:1\n"
    "s_waitcnt lgkmcnt(9)\n"
    "v_pk_mul_f16 v232, v188, v232\n"
    "v_mfma_f32_16x16x32_bf16 v[168:171], a[48:51], v[152:155], v[168:171]\n"
    "s_nop 0\n"
    "ds_read_b128 v[208:211], v250 offset:880\n"
    "v_exp_f16_e64 v234, v232\n"
    "ds_read_b128 v[212:215], v250 offset:1200\n"
    "v_exp_f16_sdwa v234, v232 dst_sel:WORD_1 dst_unused:UNUSED_PRESERVE src0_sel:WORD_1\n"
    "s_waitcnt lgkmcnt(9)\n"
    "v_mfma_f32_16x16x32_bf16 v[176:179], a[80:83], v[156:159], 0\n"
    "s_waitcnt lgkmcnt(8)\n"
    "ds_read_b128 v[220:223], v252 offset:2352\n"
    "ds_read_u16_d16_hi v202, v202\n"
    "v_mfma_f32_16x16x32_bf16 v[176:179], a[128:131], v[160:163], v[176:179]\n"
    "s_waitcnt lgkmcnt(9)\n"
    "v_or_b32_e64 v191, v191, v207\n"
    "v_lshlrev_b32_sdwa v207, 1, v183 dst_sel:DWORD dst_unused:UNUSED_PAD src0_sel:DWORD src1_sel:WORD_1\n"
    "v_mfma_f32_16x16x32_bf16 v[176:179], a[48:51], v[164:167], v[176:179]\n"
    "s_waitcnt lgkmcnt(8)\n"
    "ds_read_b128 v[236:239], v244 offset:16384\n"
    "v_pk_fma_f16 v234, v234, s49, s49\n"
    "v_mfma_f32_16x16x32_bf16 v[172:175], a[240:243], v[152:155], v[172:175]\n"
    "ds_read_u16 v195, v195\n"
    "v_rcp_f16_e64 v232, v234\n"
    "v_mfma_f32_16x16x32_bf16 v[180:183], a[84:87], v[156:159], 0\n"
    "v_rcp_f16_sdwa v232, v234 dst_sel:WORD_1 dst_unused:UNUSED_PRESERVE src0_sel:WORD_1\n"
    "v_mfma_f32_16x16x32_bf16 v[180:183], a[132:135], v[160:163], v[180:183]\n"
    "ds_read_u16_d16_hi v203, v203\n"
    "v_fma_mix_f32 v234, v188, v232, 0 op_sel_hi:[1,1,0]\n"
    "v_mfma_f32_16x16x32_bf16 v[180:183], a[240:243], v[164:167], v[180:183]\n"
    "v_fma_mix_f32 v232, v188, v232, 0 op_sel:[1,1,0] op_sel_hi:[1,1,0]\n"
    "v_lshlrev_b32_sdwa v200, 1, v169 dst_sel:DWORD dst_unused:UNUSED_PAD src0_sel:DWORD src1_sel:WORD_1\n"
    "v_mfma_f32_16x16x32_bf16 v[96:99], a[192:195], v[184:187], v[96:99]\n"
    "s_waitcnt lgkmcnt(8)\n"
    "ds_read_b128 a[244:247], v244 offset:18432\n"
    "ds_read_u16 v197, v197\n"
    "v_mfma_f32_16x16x32_bf16 v[100:103], a[196:199], v[184:187], v[100:103]\n"
    "v_or_b32_e32 v193, v193, v201\n"
    "v_cvt_pk_bf16_f32 v188, v234, v232\n"
    "v_mfma_f32_16x16x32_bf16 v[112:115], a[200:203], v[184:187], v[112:115]\n"
    "ds_read_u16_d16_hi v205, v205\n"
    "v_pk_fma_f16 v232, v192, v192, s48\n"
    "v_mfma_f32_16x16x32_bf16 v[116:119], a[204:207], v[184:187], v[116:119]\n"
    "s_waitcnt lgkmcnt(8)\n"
    "s_barrier\n"
    "v_smfmac_f32_16x16x64_bf16 v[24:27], v[216:219], v[208:215], v240\n"
    "s_waitcnt lgkmcnt(7)\n"
    "ds_read_u16 v198, v198\n"
    "ds_write2_b32 v248, v32, v33 offset0:0 offset1:20\n"
    "v_smfmac_f32_16x16x64_bf16 v[28:31], v[220:223], v[208:215], v240 abid:1\n"
    "s_waitcnt lgkmcnt(7)\n"
    "v_or_b32_e64 v194, v194, v202\n"
    "v_mfma_f32_16x16x32_bf16 v[128:131], v[236:239], v[184:187], v[128:131]\n"
    "ds_read_u16_d16_hi v206, v206\n"
    "ds_write2_b32 v248, v34, v35 offset0:40 offset1:60\n"
    "v_add_u32_e32 v248, 1360, v248\n"
    "v_pk_add_f32 v[224:225], v[224:225], v[24:25]\n"
    "v_pk_add_f32 v[226:227], v[226:227], v[26:27]\n"
    "v_pk_fma_f32 v[228:229], v[24:25], v[24:25], v[228:229]\n"
    "v_pk_fma_f32 v[230:231], v[26:27], v[26:27], v[230:231]\n"
    "v_pk_add_f32 v[224:225], v[224:225], v[28:29]\n"
    "v_pk_add_f32 v[226:227], v[226:227], v[30:31]\n"
    "v_pk_fma_f32 v[228:229], v[28:29], v[28:29], v[228:229]\n"
    "v_pk_fma_f32 v[230:231], v[30:31], v[30:31], v[230:231]\n"
    "s_waitcnt lgkmcnt(6)\n"
    "v_mfma_f32_16x16x32_bf16 v[132:135], a[244:247], v[184:187], v[132:135]\n"
    "s_nop 0\n"
    "ds_read_u16 v199, v199\n"
    "ds_write2_b32 v235, v52, v53 offset0:0 offset1:20\n"
    "v_mfma_f32_16x16x32_bf16 v[104:107], a[192:195], v[188:191], v[104:107]\n"
    "v_or_b32_e64 v195, v195, v203\n"
    "v_pk_mul_f16 v232, v192, v232\n"
    "v_mfma_f32_16x16x32_bf16 v[108:111], a[196:199], v[188:191], v[108:111]\n"
    "ds_read_u16_d16_hi v207, v207\n"
    "ds_write2_b32 v235, v54, v55 offset0:40 offset1:60\n"
    "v_mfma_f32_16x16x32_bf16 v[120:123], a[200:203], v[188:191], v[120:123]\n"
    "s_waitcnt lgkmcnt(8)\n"
    "v_add_u32_e32 v235, 1360, v235\n"
    "v_exp_f16_e32 v234, v232\n"
    "v_mfma_f32_16x16x32_bf16 v[124:127], a[204:207], v[188:191], v[124:127]\n"
    "ds_read_b128 a[240:243], v244 offset:36864\n"
    "ds_write2_b32 v248, v36, v37 offset0:0 offset1:20\n"
    "v_mfma_f32_16x16x32_bf16 v[136:139], v[236:239], v[188:191], v[136:139]\n"
    "v_or_b32_e64 v197, v197, v205\n"
    "v_exp_f16_sdwa v234, v232 dst_sel:WORD_1 dst_unused:UNUSED_PRESERVE src0_sel:WORD_1\n"
    "v_mfma_f32_16x16x32_bf16 v[140:143], a[244:247], v[188:191], v[140:143]\n"
    "ds_read_u16_d16_hi v200, v200\n"
    "ds_write2_b32 v248, v38, v39 offset0:40 offset1:60\n"
    "v_add_u32_e32 v248, 1360, v248\n"
    "v_pk_fma_f16 v234, v234, s49, s49\n"
    "v_lshlrev_b32_sdwa v184, 1, v168 dst_sel:DWORD dst_unused:UNUSED_PAD src0_sel:DWORD src1_sel:WORD_1\n"
    "v_lshlrev_b32_sdwa v185, 1, v170 dst_sel:DWORD dst_unused:UNUSED_PAD src0_sel:DWORD src1_sel:WORD_1\n"
    "v_lshlrev_b32_sdwa v201, 1, v171 dst_sel:DWORD dst_unused:UNUSED_PAD src0_sel:DWORD src1_sel:WORD_1\n"
    "v_mfma_f32_16x16x32_bf16 v[168:171], a[88:91], v[144:147], 0\n"
    "s_nop 0\n"
    "s_waitcnt lgkmcnt(8)\n"
    "ds_read_u16 v184, v184\n"
    "ds_write2_b32 v235, v56, v57 offset0:0 offset1:20\n"
    "v_mfma_f32_16x16x32_bf16 v[168:171], a[136:139], v[148:151], v[168:171]\n"
    "s_add_u32 s60, s60, 1572864\n"
    "v_lshlrev_b32_sdwa v186, 1, v172 dst_sel:DWORD dst_unused:UNUSED_PAD src0_sel:DWORD src1_sel:WORD_1\n"
    "v_lshlrev_b32_sdwa v202, 1, v173 dst_sel:DWORD dst_unused:UNUSED_PAD src0_sel:DWORD src1_sel:WORD_1\n"
    "v_lshlrev_b32_sdwa v187, 1, v174 dst_sel:DWORD dst_unused:UNUSED_PAD src0_sel:DWORD src1_sel:WORD_1\n"
    "ds_write2_b32 v235, v58, v59 offset0:40 offset1:60\n"
    "v_add_u32_e32 v235, -1360, v235\n"
    "v_lshlrev_b32_sdwa v203, 1, v175 dst_sel:DWORD dst_unused:UNUSED_PAD src0_sel:DWORD src1_sel:WORD_1\n"
    "ds_write2_b32 v248, v40, v41 offset0:0 offset1:20\n"
    "v_rcp_f16_e32 v232, v234\n"
    "v_mfma_f32_16x16x32_bf16 v[168:171], a[52:55], v[152:155], v[168:171]\n"
    "s_waitcnt lgkmcnt(6)\n"
    "ds_read_u16 v185, v185\n"
    "ds_write2_b32 v248, v42, v43 offset0:40 offset1:60\n"
    "v_add_u32_e32 v248, 1360, v248\n"
    "v_or_b32_e64 v198, v198, v206\n"
    "v_rcp_f16_sdwa v232, v234 dst_sel:WORD_1 dst_unused:UNUSED_PRESERVE src0_sel:WORD_1\n"
    "v_mfma_f32_16x16x32_bf16 v[172:175], a[92:95], v[144:147], 0\n"
    "s_add_u32 s61, s61, 1572864\n"
    "ds_read_u16_d16_hi v201, v201\n"
    "ds_write2_b32 v248, v44, v45 offset0:0 offset1:20\n"
    "v_lshlrev_b32_sdwa v188, 1, v176 dst_sel:DWORD dst_unused:UNUSED_PAD src0_sel:DWORD src1_sel:WORD_1\n"
    "v_lshlrev_b32_sdwa v204, 1, v177 dst_sel:DWORD dst_unused:UNUSED_PAD src0_sel:DWORD src1_sel:WORD_1\n"
    "v_lshlrev_b32_sdwa v189, 1, v178 dst_sel:DWORD dst_unused:UNUSED_PAD src0_sel:DWORD src1_sel:WORD_1\n"
    "v_lshlrev_b32_sdwa v205, 1, v179 dst_sel:DWORD dst_unused:UNUSED_PAD src0_sel:DWORD src1_sel:WORD_1\n"
    "v_lshlrev_b32_sdwa v190, 1, v180 dst_sel:DWORD dst_unused:UNUSED_PAD src0_sel:DWORD src1_sel:WORD_1\n"
    "v_lshlrev_b32_sdwa v206, 1, v181 dst_sel:DWORD dst_unused:UNUSED_PAD src0_sel:DWORD src1_sel:WORD_1\n"
    "v_lshlrev_b32_sdwa v191, 1, v182 dst_sel:DWORD dst_unused:UNUSED_PAD src0_sel:DWORD src1_sel:WORD_1\n"
    "v_fma_mix_f32 v234, v192, v232, 0 op_sel_hi:[1,1,0]\n"
    "v_mfma_f32_16x16x32_bf16 v[172:175], a[140:143], v[148:151], v[172:175]\n"
    "s_waitcnt lgkmcnt(4)\n"
    "ds_read_u16 v186, v186\n"
    "ds_write2_b32 v248, v46, v47 offset0:40 offset1:60\n"
    "v_mfma_f32_16x16x32_bf16 v[172:175], a[240:243], v[152:155], v[172:175]\n"
    "s_add_u32 s62, s62, 1572864\n"
    "v_add_u32_e32 v248, 1360, v248\n"
    "v_or_b32_e32 v199, v199, v207\n"
    "v_lshlrev_b32_sdwa v207, 1, v183 dst_sel:DWORD dst_unused:UNUSED_PAD src0_sel:DWORD src1_sel:WORD_1\n"
    "v_fma_mix_f32 v232, v192, v232, 0 op_sel:[1,1,0] op_sel_hi:[1,1,0]\n"
    "v_mfma_f32_16x16x32_bf16 v[176:179], a[88:91], v[156:159], 0\n"
    "ds_read_b128 v[236:239], v244 offset:20480\n"
    "ds_write2_b32 v248, v48, v49 offset0:0 offset1:20\n"
    "v_cvt_pk_bf16_f32 v192, v234, v232\n"
    "v_mfma_f32_16x16x32_bf16 v[176:179], a[136:139], v[160:163], v[176:179]\n"
    "v_pk_fma_f16 v232, v196, v196, s48\n"
    "v_or_b32_e64 v184, v184, v200\n"
    "v_mfma_f32_16x16x32_bf16 v[176:179], a[52:55], v[164:167], v[176:179]\n"
    "s_add_u32 s63, s63, 1572864\n"
    "ds_read_u16_d16_hi v202, v202\n"
    "ds_write2_b32 v248, v50, v51 offset0:40 offset1:60\n"
    "v_mfma_f32_32x32x1_2b_f32 v[32:63], v254, v254, 0\n"
    "s_waitcnt lgkmcnt(6)\n"
    "v_add_u32_e32 v248, -5440, v248\n"
    "v_pk_mul_f16 v232, v196, v232\n"
    "v_or_b32_e32 v185, v185, v201\n"
    "v_exp_f16_e64 v234, v232\n"
    "v_lshlrev_b32_sdwa v201, 1, v171 dst_sel:DWORD dst_unused:UNUSED_PAD src0_sel:DWORD src1_sel:WORD_1\n"
    "v_exp_f16_sdwa v234, v232 dst_sel:WORD_1 dst_unused:UNUSED_PRESERVE src0_sel:WORD_1\n"
    "v_mov_b32_e64 v254, 0\n"
    "v_pk_fma_f16 v234, v234, s49, s49\n"
    "v_rcp_f16_e64 v232, v234\n"
    "ds_read_b128 v[216:219], v251 offset:10752\n"
    "ds_read_b128 v[220:223], v251 offset:11088\n"
    "v_mfma_f32_16x16x32_bf16 v[180:183], a[92:95], v[156:159], 0\n"
    "s_add_u32 s52, s52, 1572864\n"
    "s_add_u32 s53, s53, 393216\n"
    "ds_read_u16 v187, v187\n"
    "ds_read_b128 a[244:247], v244 offset:22528\n"
    "s_waitcnt vmcnt(3)\n"
    "v_smfmac_f32_32x32x32_bf16 v[128:143], a[248:251], a[224:231], v241\n"
    "s_nop 0\n"
    "ds_read_u16_d16_hi v203, v203\n"
    "ds_read_u16 v188, v188\n"
    "v_rcp_f16_sdwa v232, v234 dst_sel:WORD_1 dst_unused:UNUSED_PRESERVE src0_sel:WORD_1\n"
    "ds_read_u16_d16_hi v204, v204\n"
    "v_mfma_f32_16x16x32_bf16 v[180:183], a[140:143], v[160:163], v[180:183]\n"
    "s_waitcnt lgkmcnt(7)\n"
    "v_fma_mix_f32 v234, v196, v232, 0 op_sel_hi:[1,1,0]\n"
    "v_fma_mix_f32 v232, v196, v232, 0 op_sel:[1,1,0] op_sel_hi:[1,1,0]\n"
    "v_mfma_f32_16x16x32_bf16 v[180:183], a[240:243], v[164:167], v[180:183]\n"
    "s_barrier\n"
    "v_mfma_f32_16x16x32_bf16 v[96:99], a[208:211], v[192:195], v[96:99]\n"
    "ds_read_b128 v[208:211], v250 offset:0\n"
    "ds_read_b128 v[212:215], v250 offset:320\n"
    "v_mfma_f32_16x16x32_bf16 v[100:103], a[212:215], v[192:195], v[100:103]\n"
    "ds_read_u16 v189, v189\n"
    "ds_read_u16_d16_hi v205, v205\n"
    "v_mfma_f32_16x16x32_bf16 v[112:115], a[216:219], v[192:195], v[112:115]\n"
    "v_cvt_pk_bf16_f32 v196, v234, v232\n"
    "v_or_b32_e32 v186, v186, v202\n"
    "v_mfma_f32_16x16x32_bf16 v[116:119], a[220:223], v[192:195], v[116:119]\n"
    "ds_read_u16 v190, v190\n"
    "v_lshlrev_b32_sdwa v202, 1, v173 dst_sel:DWORD dst_unused:UNUSED_PAD src0_sel:DWORD src1_sel:WORD_1\n"
    "v_mfma_f32_16x16x32_bf16 v[128:131], v[236:239], v[192:195], v[128:131]\n"
    "s_waitcnt lgkmcnt(8)\n"
    "v_cvt_pk_bf16_f32 v96, v96, v97\n"
    "v_cvt_pk_bf16_f32 v97, v98, v99\n"
    "v_mfma_f32_16x16x32_bf16 v[132:135], a[244:247], v[192:195], v[132:135]\n"
    "ds_read_u16_d16_hi v206, v206\n"
    "v_cvt_pk_f16_f32 v192, v168, v169\n"
    "v_mfma_f32_16x16x32_bf16 v[104:107], a[208:211], v[196:199], v[104:107]\n"
    "v_lshlrev_b32_sdwa v193, 1, v170 dst_sel:DWORD dst_unused:UNUSED_PAD src0_sel:DWORD src1_sel:WORD_1\n"
    "v_lshlrev_b32_sdwa v194, 1, v172 dst_sel:DWORD dst_unused:UNUSED_PAD src0_sel:DWORD src1_sel:WORD_1\n"
    "v_mfma_f32_16x16x32_bf16 v[108:111], a[212:215], v[196:199], v[108:111]\n"
    "s_waitcnt lgkmcnt(8)\n"
    "ds_read_u16 v191, v191\n"
    "v_lshlrev_b32_sdwa v195, 1, v174 dst_sel:DWORD dst_unused:UNUSED_PAD src0_sel:DWORD src1_sel:WORD_1\n"
    "v_mfma_f32_16x16x32_bf16 v[120:123], a[216:219], v[196:199], v[120:123]\n"
    "v_or_b32_e32 v187, v187, v203\n"
    "v_lshlrev_b32_sdwa v203, 1, v175 dst_sel:DWORD dst_unused:UNUSED_PAD src0_sel:DWORD src1_sel:WORD_1\n"
    "v_mfma_f32_16x16x32_bf16 v[124:127], a[220:223], v[196:199], v[124:127]\n"
    "ds_read_u16_d16_hi v207, v207\n"
    "v_cvt_pk_bf16_f32 v98, v100, v101\n"
    "v_mfma_f32_16x16x32_bf16 v[136:139], v[236:239], v[196:199], v[136:139]\n"
    "s_waitcnt lgkmcnt(7)\n"
    "ds_read_u16 v193, v193\n"
    "v_or_b32_e32 v188, v188, v204\n"
    "s_waitcnt lgkmcnt(7)\n"
    "v_smfmac_f32_16x16x64_bf16 v[32:35], v[216:219], v[208:215], v240\n"
    "s_nop 0\n"
    "v_cvt_pk_bf16_f32 v99, v102, v103\n"
    "v_smfmac_f32_16x16x64_bf16 v[36:39], v[220:223], v[208:215], v240 abid:1\n"
    "ds_read_b128 v[208:211], v250 offset:640\n"
    "ds_read_b128 v[212:215], v250 offset:960\n"
    "v_mfma_f32_16x16x32_bf16 v[140:143], a[244:247], v[196:199], v[140:143]\n"
    "v_cvt_pk_f16_f32 v196, v176, v177\n"
    "v_lshlrev_b32_sdwa v197, 1, v178 dst_sel:DWORD dst_unused:UNUSED_PAD src0_sel:DWORD src1_sel:WORD_1\n"
    "v_mfma_f32_16x16x32_bf16 v[168:171], a[96:99], v[144:147], 0\n"
    "s_nop 0\n"
    "ds_read_b128 v[216:219], v252 offset:10752\n"
    "ds_read_b128 v[220:223], v252 offset:11088\n"
    "v_mfma_f32_16x16x32_bf16 v[168:171], a[144:147], v[148:151], v[168:171]\n"
    "s_waitcnt lgkmcnt(9)\n"
    "v_lshlrev_b32_sdwa v198, 1, v180 dst_sel:DWORD dst_unused:UNUSED_PAD src0_sel:DWORD src1_sel:WORD_1\n"
    "v_lshlrev_b32_sdwa v199, 1, v182 dst_sel:DWORD dst_unused:UNUSED_PAD src0_sel:DWORD src1_sel:WORD_1\n"
    "v_mfma_f32_16x16x32_bf16 v[168:171], a[56:59], v[152:155], v[168:171]\n"
    "ds_read_u16_d16_hi v201, v201\n"
    "v_or_b32_e64 v189, v189, v205\n"
    "v_mfma_f32_16x16x32_bf16 v[172:175], a[100:103], v[144:147], 0\n"
    "ds_read_u16 v194, v194\n"
    "v_lshlrev_b32_sdwa v205, 1, v179 dst_sel:DWORD dst_unused:UNUSED_PAD src0_sel:DWORD src1_sel:WORD_1\n"
    "v_mfma_f32_16x16x32_bf16 v[172:175], a[148:151], v[148:151], v[172:175]\n"
    "v_cvt_pk_bf16_f32 v112, v112, v113\n"
    "s_waitcnt lgkmcnt(9)\n"
    "v_or_b32_e32 v190, v190, v206\n"
    "v_mfma_f32_16x16x32_bf16 v[172:175], a[60:63], v[152:155], v[172:175]\n"
    "ds_read_u16_d16_hi v202, v202\n"
    "v_lshlrev_b32_sdwa v206, 1, v181 dst_sel:DWORD dst_unused:UNUSED_PAD src0_sel:DWORD src1_sel:WORD_1\n"
    "v_mfma_f32_16x16x32_bf16 v[176:179], a[96:99], v[156:159], 0\n"
    "v_cvt_pk_bf16_f32 v113, v114, v115\n"
    "v_cvt_pk_bf16_f32 v114, v116, v117\n"
    "v_mfma_f32_16x16x32_bf16 v[176:179], a[144:147], v[160:163], v[176:179]\n"
    "s_nop 0\n"
    "ds_read_b128 v[236:239], v244 offset:0\n"
    "ds_write_b128 v243, v[96:99] offset:24640\n"
    "v_mfma_f32_16x16x32_bf16 v[176:179], a[56:59], v[164:167], v[176:179]\n"
    "s_waitcnt lgkmcnt(10)\n"
    "v_cvt_pk_bf16_f32 v115, v118, v119\n"
    "v_cvt_pk_bf16_f32 v128, v128, v129\n"
    "v_mfma_f32_16x16x32_bf16 v[96:99], a[0:3], v[184:187], 0\n"
    "v_cvt_pk_bf16_f32 v129, v130, v131\n"
    "v_or_b32_e32 v191, v191, v207\n"
    "v_mfma_f32_16x16x32_bf16 v[100:103], a[4:7], v[184:187], 0\n"
    "s_waitcnt lgkmcnt(6)\n"
    "ds_read_b128 a[252:255], v249 offset:0\n"
    "ds_write_b128 v243, v[112:115] offset:26688\n"
    "v_smfmac_f32_16x16x64_bf16 v[32:35], v[216:219], v[208:215], v240\n"
    "s_waitcnt lgkmcnt(7)\n"
    "v_lshlrev_b32_sdwa v207, 1, v183 dst_sel:DWORD dst_unused:UNUSED_PAD src0_sel:DWORD src1_sel:WORD_1\n"
    "v_smfmac_f32_16x16x64_bf16 v[36:39], v[220:223], v[208:215], v240 abid:1\n"
    "v_cvt_pk_bf16_f32 v130, v132, v133\n"
    "v_mfma_f32_16x16x32_bf16 v[180:183], a[100:103], v[156:159], 0\n"
    "s_waitcnt lgkmcnt(6)\n"
    "ds_read_b128 v[208:211], v250 offset:80\n"
    "ds_read_b128 v[212:215], v250 offset:400\n"
    "ds_read_b128 v[216:219], v251 offset:11424\n"
    "v_or_b32_e64 v193, v193, v201\n"
    "ds_read_b128 v[220:223], v251 offset:11760\n"
    "ds_read_u16 v195, v195\n"
    "v_pk_add_f32 v[224:225], v[224:225], v[32:33]\n"
    "v_pk_add_f32 v[226:227], v[226:227], v[34:35]\n"
    "v_pk_fma_f32 v[228:229], v[32:33], v[32:33], v[228:229]\n"
    "v_pk_fma_f32 v[230:231], v[34:35], v[34:35], v[230:231]\n"
    "v_pk_add_f32 v[224:225], v[224:225], v[36:37]\n"
    "v_pk_add_f32 v[226:227], v[226:227], v[38:39]\n"
    "v_pk_fma_f32 v[228:229], v[36:37], v[36:37], v[228:229]\n"
    "v_pk_fma_f32 v[230:231], v[38:39], v[38:39], v[230:231]\n"
    "v_mfma_f32_16x16x32_bf16 v[180:183], a[148:151], v[160:163], v[180:183]\n"
    "s_waitcnt lgkmcnt(7)\n"
    "ds_read_u16_d16_hi v203, v203\n"
    "ds_read_b128 a[244:247], v244 offset:2048\n"
    "v_mfma_f32_16x16x32_bf16 v[180:183], a[60:63], v[164:167], v[180:183]\n"
    "v_cvt_pk_bf16_f32 v131, v134, v135\n"
    "v_cvt_pk_bf16_f32 v104, v104, v105\n"
    "v_mfma_f32_16x16x32_bf16 v[112:115], a[8:11], v[184:187], 0\n"
    "s_waitcnt lgkmcnt(8)\n"
    "ds_read_u16 v197, v197\n"
    "buffer_store_dwordx4 a[252:255], v255, s[44:47], s57 offen offset:0 sc1\n"
    "v_mfma_f32_16x16x32_bf16 v[116:119], a[12:15], v[184:187], 0\n"
    "ds_read_u16_d16_hi v205, v205\n"
    "v_cvt_pk_bf16_f32 v105, v106, v107\n"
    "v_cvt_pk_bf16_f32 v106, v108, v109\n"
    "ds_write_b128 v243, v[128:131] offset:28736\n"
    "v_mfma_f32_16x16x32_bf16 v[128:131], v[236:239], v[184:187], 0\n"
    "s_waitcnt lgkmcnt(7)\n"
    "v_cvt_pk_bf16_f32 v107, v110, v111\n"
    "v_or_b32_e64 v194, v194, v202\n"
    "v_smfmac_f32_16x16x64_bf16 v[40:43], v[216:219], v[208:215], v240\n"
    "s_waitcnt lgkmcnt(6)\n"
    "ds_read_b128 v[216:219], v252 offset:11424\n"
    "ds_write_b128 v243, v[104:107] offset:25664\n"
    "v_smfmac_f32_16x16x64_bf16 v[44:47], v[220:223], v[208:215], v240 abid:1\n"
    "v_cvt_pk_bf16_f32 v120, v120, v121\n"
    "v_mfma_f32_16x16x32_bf16 v[104:107], a[0:3], v[188:191], 0\n"
    "s_nop 0\n"
    "v_cvt_pk_bf16_f32 v121, v122, v123\n"
    "v_cvt_pk_bf16_f32 v122, v124, v125\n"
    "v_mfma_f32_16x16x32_bf16 v[108:111], a[4:7], v[188:191], 0\n"
    "s_waitcnt lgkmcnt(5)\n"
    "ds_read_b128 v[208:211], v250 offset:720\n"
    "ds_read_b128 v[212:215], v250 offset:1040\n"
    "v_mfma_f32_16x16x32_bf16 v[132:135], a[244:247], v[184:187], 0\n"
    "ds_read_b128 v[220:223], v252 offset:11760\n"
    "ds_read_u16 v198, v198\n"
    "v_cvt_pk_bf16_f32 v123, v126, v127\n"
    "v_pk_fma_f16 v232, v192, v192, s48\n"
    "v_mfma_f32_16x16x32_bf16 v[124:127], a[12:15], v[188:191], 0\n"
    "ds_read_u16_d16_hi v206, v206\n"
    "v_cvt_pk_bf16_f32 v136, v136, v137\n"
    "v_cvt_pk_bf16_f32 v137, v138, v139\n"
    "v_cvt_pk_bf16_f32 v138, v140, v141\n"
    "v_or_b32_e64 v195, v195, v203\n"
    "v_cvt_pk_bf16_f32 v139, v142, v143\n"
    "ds_write_b128 v243, v[120:123] offset:27712\n"
    "v_mfma_f32_16x16x32_bf16 v[120:123], a[8:11], v[188:191], 0\n"
    "s_waitcnt lgkmcnt(8)\n"
    "v_pk_mul_f16 v232, v192, v232\n"
    "v_or_b32_e32 v197, v197, v205\n"
    "v_exp_f16_e64 v234, v232\n"
    "v_mfma_f32_16x16x32_bf16 v[140:143], a[244:247], v[188:191], 0\n"
    "ds_read_u16 v199, v199\n"
    "ds_write_b128 v243, v[136:139] offset:29760\n"
    "v_mfma_f32_16x16x32_bf16 v[136:139], v[236:239], v[188:191], 0\n"
    "v_exp_f16_sdwa v234, v232 dst_sel:WORD_1 dst_unused:UNUSED_PRESERVE src0_sel:WORD_1\n"
    "v_cvt_pk_f16_f32 v184, v168, v169\n"
    "s_waitcnt lgkmcnt(6)\n"
    "v_smfmac_f32_16x16x64_bf16 v[40:43], v[216:219], v[208:215], v240\n"
    "s_waitcnt lgkmcnt(5)\n"
    "ds_read_b128 v[216:219], v251 offset:12096\n"
    "ds_read_u16_d16_hi v207, v207\n"
    "v_smfmac_f32_16x16x64_bf16 v[44:47], v[220:223], v[208:215], v240 abid:1\n"
    "ds_read_b128 v[208:211], v250 offset:160\n"
    "v_pk_fma_f16 v234, v234, s49, s49\n"
    "v_lshlrev_b32_sdwa v185, 1, v170 dst_sel:DWORD dst_unused:UNUSED_PAD src0_sel:DWORD src1_sel:WORD_1\n"
    "ds_read_b128 v[212:215], v250 offset:480\n"
    "v_pk_add_f32 v[224:225], v[224:225], v[40:41]\n"
    "v_pk_add_f32 v[226:227], v[226:227], v[42:43]\n"
    "v_pk_fma_f32 v[228:229], v[40:41], v[40:41], v[228:229]\n"
    "v_pk_fma_f32 v[230:231], v[42:43], v[42:43], v[230:231]\n"
    "v_pk_add_f32 v[224:225], v[224:225], v[44:45]\n"
    "v_pk_add_f32 v[226:227], v[226:227], v[46:47]\n"
    "v_pk_fma_f32 v[228:229], v[44:45], v[44:45], v[228:229]\n"
    "v_pk_fma_f32 v[230:231], v[46:47], v[46:47], v[230:231]\n"
    "ds_read_b128 v[220:223], v251 offset:12432\n"
    "ds_read_b128 a[252:255], v249 offset:2048\n"
    "v_lshlrev_b32_sdwa v201, 1, v171 dst_sel:DWORD dst_unused:UNUSED_PAD src0_sel:DWORD src1_sel:WORD_1\n"
    "v_mfma_f32_16x16x32_bf16 v[168:171], a[104:107], v[144:147], 0\n"
    "s_waitcnt lgkmcnt(8)\n"
    "ds_read_u16 v185, v185\n"
    "ds_read_u16_d16_hi v201, v201\n"
    "v_mfma_f32_16x16x32_bf16 v[168:171], a[152:155], v[148:151], v[168:171]\n"
    "s_nop 0\n"
    "v_lshlrev_b32_sdwa v186, 1, v172 dst_sel:DWORD dst_unused:UNUSED_PAD src0_sel:DWORD src1_sel:WORD_1\n"
    "v_lshlrev_b32_sdwa v202, 1, v173 dst_sel:DWORD dst_unused:UNUSED_PAD src0_sel:DWORD src1_sel:WORD_1\n"
    "v_lshlrev_b32_sdwa v187, 1, v174 dst_sel:DWORD dst_unused:UNUSED_PAD src0_sel:DWORD src1_sel:WORD_1\n"
    "v_lshlrev_b32_sdwa v203, 1, v175 dst_sel:DWORD dst_unused:UNUSED_PAD src0_sel:DWORD src1_sel:WORD_1\n"
    "v_rcp_f16_e64 v232, v234\n"
    "v_mfma_f32_16x16x32_bf16 v[168:171], a[64:67], v[152:155], v[168:171]\n"
    "ds_read_u16 v186, v186\n"
    "v_or_b32_e64 v198, v198, v206\n"
    "v_rcp_f16_sdwa v232, v234 dst_sel:WORD_1 dst_unused:UNUSED_PRESERVE src0_sel:WORD_1\n"
    "v_mfma_f32_16x16x32_bf16 v[172:175], a[108:111], v[144:147], 0\n"
    "ds_read_u16_d16_hi v202, v202\n"
    "v_cvt_pk_f16_f32 v188, v176, v177\n"
    "v_lshlrev_b32_sdwa v189, 1, v178 dst_sel:DWORD dst_unused:UNUSED_PAD src0_sel:DWORD src1_sel:WORD_1\n"
    "v_lshlrev_b32_sdwa v205, 1, v179 dst_sel:DWORD dst_unused:UNUSED_PAD src0_sel:DWORD src1_sel:WORD_1\n"
    "v_lshlrev_b32_sdwa v190, 1, v180 dst_sel:DWORD dst_unused:UNUSED_PAD src0_sel:DWORD src1_sel:WORD_1\n"
    "v_lshlrev_b32_sdwa v206, 1, v181 dst_sel:DWORD dst_unused:UNUSED_PAD src0_sel:DWORD src1_sel:WORD_1\n"
    "v_lshlrev_b32_sdwa v191, 1, v182 dst_sel:DWORD dst_unused:UNUSED_PAD src0_sel:DWORD src1_sel:WORD_1\n"
    "v_fma_mix_f32 v234, v192, v232, 0 op_sel_hi:[1,1,0]\n"
    "v_mfma_f32_16x16x32_bf16 v[172:175], a[156:159], v[148:151], v[172:175]\n"
    "s_waitcnt lgkmcnt(7)\n"
    "ds_read_b128 v[236:239], v244 offset:4096\n"
    "ds_read_u16 v187, v187\n"
    "s_waitcnt lgkmcnt(8)\n"
    "v_smfmac_f32_16x16x64_bf16 v[48:51], v[216:219], v[208:215], v240\n"
    "s_waitcnt lgkmcnt(7)\n"
    "ds_read_b128 v[216:219], v252 offset:12096\n"
    "v_smfmac_f32_16x16x64_bf16 v[52:55], v[220:223], v[208:215], v240 abid:1\n"
    "s_waitcnt lgkmcnt(7)\n"
    "ds_read_b128 v[208:211], v250 offset:800\n"
    "buffer_store_dwordx4 a[252:255], v255, s[44:47], s57 offen offset:64 sc1\n"
    "v_mfma_f32_16x16x32_bf16 v[172:175], a[68:71], v[152:155], v[172:175]\n"
    "ds_read_b128 v[212:215], v250 offset:1120\n"
    "ds_read_b128 v[220:223], v252 offset:12432\n"
    "v_or_b32_e64 v199, v199, v207\n"
    "v_lshlrev_b32_sdwa v207, 1, v183 dst_sel:DWORD dst_unused:UNUSED_PAD src0_sel:DWORD src1_sel:WORD_1\n"
    "v_fma_mix_f32 v232, v192, v232, 0 op_sel:[1,1,0] op_sel_hi:[1,1,0]\n"
    "v_mfma_f32_16x16x32_bf16 v[176:179], a[104:107], v[156:159], 0\n"
    "s_nop 0\n"
    "ds_read_u16_d16_hi v203, v203\n"
    "v_cvt_pk_bf16_f32 v192, v234, v232\n"
    "v_mfma_f32_16x16x32_bf16 v[176:179], a[152:155], v[160:163], v[176:179]\n"
    "s_waitcnt lgkmcnt(9)\n"
    "ds_read_b128 a[244:247], v244 offset:6144\n"
    "v_pk_fma_f16 v232, v196, v196, s48\n"
    "v_mfma_f32_16x16x32_bf16 v[176:179], a[64:67], v[164:167], v[176:179]\n"
    "ds_read_b128 a[252:255], v249 offset:4096\n"
    "v_pk_mul_f16 v232, v196, v232\n"
    "v_mfma_f32_16x16x32_bf16 v[180:183], a[108:111], v[156:159], 0\n"
    "ds_read_u16 v189, v189\n"
    "v_exp_f16_e32 v234, v232\n"
    "v_mfma_f32_16x16x32_bf16 v[180:183], a[156:159], v[160:163], v[180:183]\n"
    "v_exp_f16_sdwa v234, v232 dst_sel:WORD_1 dst_unused:UNUSED_PRESERVE src0_sel:WORD_1\n"
    "v_mfma_f32_16x16x32_bf16 v[180:183], a[68:71], v[164:167], v[180:183]\n"
    "s_waitcnt lgkmcnt(9)\n"
    "ds_read_u16_d16_hi v205, v205\n"
    "v_pk_fma_f16 v234, v234, s49, s49\n"
    "v_mfma_f32_16x16x32_bf16 v[96:99], a[16:19], v[192:195], v[96:99]\n"
    "ds_read_u16 v190, v190\n"
    "v_rcp_f16_e64 v232, v234\n"
    "v_mfma_f32_16x16x32_bf16 v[100:103], a[20:23], v[192:195], v[100:103]\n"
    "v_rcp_f16_sdwa v232, v234 dst_sel:WORD_1 dst_unused:UNUSED_PRESERVE src0_sel:WORD_1\n"
    "v_mfma_f32_16x16x32_bf16 v[112:115], a[24:27], v[192:195], v[112:115]\n"
    "ds_read_u16_d16_hi v206, v206\n"
    "v_or_b32_e64 v185, v185, v201\n"
    "s_waitcnt lgkmcnt(8)\n"
    "v_smfmac_f32_16x16x64_bf16 v[48:51], v[216:219], v[208:215], v240\n"
    "s_waitcnt lgkmcnt(7)\n"
    "ds_read_b128 v[216:219], v251 offset:12768\n"
    "v_smfmac_f32_16x16x64_bf16 v[52:55], v[220:223], v[208:215], v240 abid:1\n"
    "ds_read_b128 v[208:211], v250 offset:240\n"
    "v_mfma_f32_16x16x32_bf16 v[116:119], a[28:31], v[192:195], v[116:119]\n"
    "ds_read_b128 v[212:215], v250 offset:560\n"
    "v_fma_mix_f32 v234, v196, v232, 0 op_sel_hi:[1,1,0]\n"
    "v_mfma_f32_16x16x32_bf16 v[128:131], v[236:239], v[192:195], v[128:131]\n"
    "s_waitcnt lgkmcnt(8)\n"
    "ds_read_b128 v[220:223], v251 offset:13104\n"
    "v_fma_mix_f32 v232, v196, v232, 0 op_sel:[1,1,0] op_sel_hi:[1,1,0]\n"
    "v_or_b32_e32 v186, v186, v202\n"
    "v_cvt_pk_bf16_f32 v196, v234, v232\n"
    "v_pk_fma_f16 v232, v184, v184, s48\n"
    "v_pk_add_f32 v[224:225], v[224:225], v[48:49]\n"
    "v_pk_add_f32 v[226:227], v[226:227], v[50:51]\n"
    "v_pk_fma_f32 v[228:229], v[48:49], v[48:49], v[228:229]\n"
    "v_pk_fma_f32 v[230:231], v[50:51], v[50:51], v[230:231]\n"
    "v_pk_add_f32 v[224:225], v[224:225], v[52:53]\n"
    "v_pk_add_f32 v[226:227], v[226:227], v[54:55]\n"
    "v_pk_fma_f32 v[228:229], v[52:53], v[52:53], v[228:229]\n"
    "v_pk_fma_f32 v[230:231], v[54:55], v[54:55], v[230:231]\n"
    "v_mfma_f32_16x16x32_bf16 v[132:135], a[244:247], v[192:195], v[132:135]\n"
    "s_waitcnt lgkmcnt(8)\n"
    "ds_read_u16 v191, v191\n"
    "buffer_store_dwordx4 a[252:255], v255, s[44:47], s57 offen offset:128 sc1\n"
    "v_mfma_f32_16x16x32_bf16 v[104:107], a[16:19], v[196:199], v[104:107]\n"
    "s_waitcnt lgkmcnt(7)\n"
    "ds_read_u16_d16_hi v207, v207\n"
    "ds_read_b128 a[252:255], v249 offset:1024\n"
    "v_mfma_f32_16x16x32_bf16 v[108:111], a[20:23], v[196:199], v[108:111]\n"
    "ds_read_b128 a[240:243], v244 offset:34816\n"
    "v_or_b32_e64 v187, v187, v203\n"
    "v_mfma_f32_16x16x32_bf16 v[120:123], a[24:27], v[196:199], v[120:123]\n"
    "v_pk_mul_f16 v232, v184, v232\n"
    "v_or_b32_e64 v189, v189, v205\n"
    "v_mfma_f32_16x16x32_bf16 v[124:127], a[28:31], v[196:199], v[124:127]\n"
    "v_exp_f16_e32 v234, v232\n"
    "v_mfma_f32_16x16x32_bf16 v[136:139], v[236:239], v[196:199], v[136:139]\n"
    "s_waitcnt lgkmcnt(7)\n"
    "v_exp_f16_sdwa v234, v232 dst_sel:WORD_1 dst_unused:UNUSED_PRESERVE src0_sel:WORD_1\n"
    "v_mfma_f32_16x16x32_bf16 v[140:143], a[244:247], v[196:199], v[140:143]\n"
    "v_pk_fma_f16 v234, v234, s49, s49\n"
    "v_lshlrev_b32_sdwa v192, 1, v168 dst_sel:DWORD dst_unused:UNUSED_PAD src0_sel:DWORD src1_sel:WORD_1\n"
    "s_waitcnt lgkmcnt(5)\n"
    "v_smfmac_f32_16x16x64_bf16 v[56:59], v[216:219], v[208:215], v240\n"
    "s_waitcnt lgkmcnt(4)\n"
    "ds_read_b128 v[216:219], v252 offset:12768\n"
    "ds_read_u16 v192, v192\n"
    "v_smfmac_f32_16x16x64_bf16 v[60:63], v[220:223], v[208:215], v240 abid:1\n"
    "ds_read_b128 v[208:211], v250 offset:880\n"
    "ds_read_b128 v[212:215], v250 offset:1200\n"
    "ds_read_b128 v[220:223], v252 offset:13104\n"
    "v_lshlrev_b32_sdwa v200, 1, v169 dst_sel:DWORD dst_unused:UNUSED_PAD src0_sel:DWORD src1_sel:WORD_1\n"
    "v_lshlrev_b32_sdwa v193, 1, v170 dst_sel:DWORD dst_unused:UNUSED_PAD src0_sel:DWORD src1_sel:WORD_1\n"
    "v_lshlrev_b32_sdwa v201, 1, v171 dst_sel:DWORD dst_unused:UNUSED_PAD src0_sel:DWORD src1_sel:WORD_1\n"
    "v_mfma_f32_16x16x32_bf16 v[168:171], a[112:115], v[144:147], 0\n"
    "s_nop 0\n"
    "ds_read_u16_d16_hi v200, v200\n"
    "ds_read_u16 v193, v193\n"
    "v_mfma_f32_16x16x32_bf16 v[168:171], a[160:163], v[148:151], v[168:171]\n"
    "s_waitcnt lgkmcnt(8)\n"
    "buffer_store_dwordx4 a[252:255], v255, s[44:47], s88 offen offset:0 sc1\n"
    "ds_read_u16_d16_hi v201, v201\n"
    "v_lshlrev_b32_sdwa v194, 1, v172 dst_sel:DWORD dst_unused:UNUSED_PAD src0_sel:DWORD src1_sel:WORD_1\n"
    "v_lshlrev_b32_sdwa v202, 1, v173 dst_sel:DWORD dst_unused:UNUSED_PAD src0_sel:DWORD src1_sel:WORD_1\n"
    "v_lshlrev_b32_sdwa v195, 1, v174 dst_sel:DWORD dst_unused:UNUSED_PAD src0_sel:DWORD src1_sel:WORD_1\n"
    "v_lshlrev_b32_sdwa v203, 1, v175 dst_sel:DWORD dst_unused:UNUSED_PAD src0_sel:DWORD src1_sel:WORD_1\n"
    "v_rcp_f16_e64 v232, v234\n"
    "v_mfma_f32_16x16x32_bf16 v[168:171], a[72:75], v[152:155], v[168:171]\n"
    "ds_read_u16 v194, v194\n"
    "v_or_b32_e64 v190, v190, v206\n"
    "v_rcp_f16_sdwa v232, v234 dst_sel:WORD_1 dst_unused:UNUSED_PRESERVE src0_sel:WORD_1\n"
    "v_mfma_f32_16x16x32_bf16 v[172:175], a[116:119], v[144:147], 0\n"
    "ds_read_b128 v[236:239], v244 offset:8192\n"
    "v_lshlrev_b32_sdwa v196, 1, v176 dst_sel:DWORD dst_unused:UNUSED_PAD src0_sel:DWORD src1_sel:WORD_1\n"
    "v_lshlrev_b32_sdwa v204, 1, v177 dst_sel:DWORD dst_unused:UNUSED_PAD src0_sel:DWORD src1_sel:WORD_1\n"
    "v_lshlrev_b32_sdwa v197, 1, v178 dst_sel:DWORD dst_unused:UNUSED_PAD src0_sel:DWORD src1_sel:WORD_1\n"
    "v_lshlrev_b32_sdwa v205, 1, v179 dst_sel:DWORD dst_unused:UNUSED_PAD src0_sel:DWORD src1_sel:WORD_1\n"
    "v_lshlrev_b32_sdwa v198, 1, v180 dst_sel:DWORD dst_unused:UNUSED_PAD src0_sel:DWORD src1_sel:WORD_1\n"
    "v_lshlrev_b32_sdwa v206, 1, v181 dst_sel:DWORD dst_unused:UNUSED_PAD src0_sel:DWORD src1_sel:WORD_1\n"
    "v_lshlrev_b32_sdwa v199, 1, v182 dst_sel:DWORD dst_unused:UNUSED_PAD src0_sel:DWORD src1_sel:WORD_1\n"
    "v_fma_mix_f32 v234, v184, v232, 0 op_sel_hi:[1,1,0]\n"
    "v_mfma_f32_16x16x32_bf16 v[172:175], a[164:167], v[148:151], v[172:175]\n"
    "s_waitcnt lgkmcnt(6)\n"
    "ds_read_u16_d16_hi v202, v202\n"
    "ds_read_u16 v195, v195\n"
    "v_smfmac_f32_16x16x64_bf16 v[56:59], v[216:219], v[208:215], v240\n"
    "s_waitcnt lgkmcnt(7)\n"
    "ds_read_b128 a[244:247], v244 offset:10240\n"
    "v_smfmac_f32_16x16x64_bf16 v[60:63], v[220:223], v[208:215], v240 abid:1\n"
    "s_barrier\n"
    "v_mfma_f32_16x16x32_bf16 v[172:175], a[240:243], v[152:155], v[172:175]\n"
    "s_waitcnt lgkmcnt(7)\n"
    "ds_read_u16_d16_hi v203, v203\n"
    "ds_write2_b32 v248, v64, v65 offset0:0 offset1:20\n"
    "v_or_b32_e64 v191, v191, v207\n"
    "v_pk_add_f32 v[224:225], v[224:225], v[56:57]\n"
    "v_pk_add_f32 v[226:227], v[226:227], v[58:59]\n"
    "v_pk_fma_f32 v[228:229], v[56:57], v[56:57], v[228:229]\n"
    "v_pk_fma_f32 v[230:231], v[58:59], v[58:59], v[230:231]\n"
    "v_pk_add_f32 v[224:225], v[224:225], v[60:61]\n"
    "v_pk_add_f32 v[226:227], v[226:227], v[62:63]\n"
    "v_pk_fma_f32 v[228:229], v[60:61], v[60:61], v[228:229]\n"
    "v_pk_fma_f32 v[230:231], v[62:63], v[62:63], v[230:231]\n"
    "v_lshlrev_b32_sdwa v207, 1, v183 dst_sel:DWORD dst_unused:UNUSED_PAD src0_sel:DWORD src1_sel:WORD_1\n"
    "v_fma_mix_f32 v232, v184, v232, 0 op_sel:[1,1,0] op_sel_hi:[1,1,0]\n"
    "v_mfma_f32_16x16x32_bf16 v[176:179], a[112:115], v[156:159], 0\n"
    "ds_read_u16 v196, v196\n"
    "ds_write2_b32 v248, v66, v67 offset0:40 offset1:60\n"
    "v_add_u32_e32 v248, 1360, v248\n"
    "v_cvt_pk_bf16_f32 v184, v234, v232\n"
    "v_mfma_f32_16x16x32_bf16 v[176:179], a[160:163], v[160:163], v[176:179]\n"
    "v_pk_fma_f16 v232, v188, v188, s48\n"
    "v_or_b32_e32 v192, v192, v200\n"
    "v_mfma_f32_16x16x32_bf16 v[176:179], a[72:75], v[164:167], v[176:179]\n"
    "s_waitcnt lgkmcnt(7)\n"
    "ds_read_u16_d16_hi v204, v204\n"
    "s_waitcnt vmcnt(6)\n"
    "ds_write2_b32 v235, v84, v85 offset0:0 offset1:20\n"
    "v_mfma_f32_16x16x32_bf16 v[180:183], a[116:119], v[156:159], 0\n"
    "s_nop 0\n"
    "v_pk_mul_f16 v232, v188, v232\n"
    "v_or_b32_e64 v193, v193, v201\n"
    "v_mfma_f32_16x16x32_bf16 v[180:183], a[164:167], v[160:163], v[180:183]\n"
    "ds_read_u16 v197, v197\n"
    "ds_write2_b32 v235, v86, v87 offset0:40 offset1:60\n"
    "v_add_u32_e32 v235, 1360, v235\n"
    "v_exp_f16_e32 v234, v232\n"
    "v_mfma_f32_16x16x32_bf16 v[180:183], a[240:243], v[164:167], v[180:183]\n"
    "s_waitcnt lgkmcnt(6)\n"
    "ds_read_b128 a[252:255], v249 offset:3072\n"
    "ds_write2_b32 v248, v68, v69 offset0:0 offset1:20\n"
    "v_exp_f16_sdwa v234, v232 dst_sel:WORD_1 dst_unused:UNUSED_PRESERVE src0_sel:WORD_1\n"
    "v_lshlrev_b32_sdwa v201, 1, v171 dst_sel:DWORD dst_unused:UNUSED_PAD src0_sel:DWORD src1_sel:WORD_1\n"
    "v_pk_fma_f16 v234, v234, s49, s49\n"
    "v_mfma_f32_16x16x32_bf16 v[96:99], a[32:35], v[184:187], v[96:99]\n"
    "ds_read_u16_d16_hi v205, v205\n"
    "ds_write2_b32 v248, v70, v71 offset0:40 offset1:60\n"
    "v_mfma_f32_16x16x32_bf16 v[100:103], a[36:39], v[184:187], v[100:103]\n"
    "v_add_u32_e32 v248, 1360, v248\n"
    "v_rcp_f16_e32 v232, v234\n"
    "v_mfma_f32_16x16x32_bf16 v[112:115], a[40:43], v[184:187], v[112:115]\n"
    "ds_read_u16 v198, v198\n"
    "ds_read_u16_d16_hi v206, v206\n"
    "v_rcp_f16_sdwa v232, v234 dst_sel:WORD_1 dst_unused:UNUSED_PRESERVE src0_sel:WORD_1\n"
    "v_mfma_f32_16x16x32_bf16 v[116:119], a[44:47], v[184:187], v[116:119]\n"
    "s_waitcnt lgkmcnt(8)\n"
    "ds_read_u16 v199, v199\n"
    "ds_read_u16_d16_hi v207, v207\n"
    "v_fma_mix_f32 v234, v188, v232, 0 op_sel_hi:[1,1,0]\n"
    "v_mfma_f32_16x16x32_bf16 v[128:131], v[236:239], v[184:187], v[128:131]\n"
    "v_fma_mix_f32 v232, v188, v232, 0 op_sel:[1,1,0] op_sel_hi:[1,1,0]\n"
    "v_or_b32_e32 v194, v194, v202\n"
    "v_mfma_f32_16x16x32_bf16 v[132:135], a[244:247], v[184:187], v[132:135]\n"
    "s_waitcnt lgkmcnt(6)\n"
    "ds_read_b128 a[240:243], v244 offset:38912\n"
    "buffer_store_dwordx4 a[252:255], v255, s[44:47], s88 offen offset:64 sc1\n"
    "v_cvt_pk_bf16_f32 v188, v234, v232\n"
    "v_cvt_pk_f16_f32 v184, v168, v169\n"
    "v_lshlrev_b32_sdwa v185, 1, v170 dst_sel:DWORD dst_unused:UNUSED_PAD src0_sel:DWORD src1_sel:WORD_1\n"
    "v_mfma_f32_16x16x32_bf16 v[104:107], a[32:35], v[188:191], v[104:107]\n"
    "ds_read_u16 v185, v185\n"
    "s_waitcnt vmcnt(6)\n"
    "ds_write2_b32 v235, v88, v89 offset0:0 offset1:20\n"
    "v_mfma_f32_16x16x32_bf16 v[108:111], a[36:39], v[188:191], v[108:111]\n"
    "s_waitcnt lgkmcnt(7)\n"
    "v_lshlrev_b32_sdwa v186, 1, v172 dst_sel:DWORD dst_unused:UNUSED_PAD src0_sel:DWORD src1_sel:WORD_1\n"
    "v_lshlrev_b32_sdwa v202, 1, v173 dst_sel:DWORD dst_unused:UNUSED_PAD src0_sel:DWORD src1_sel:WORD_1\n"
    "v_mfma_f32_16x16x32_bf16 v[120:123], a[40:43], v[188:191], v[120:123]\n"
    "ds_read_b128 a[252:255], v249 offset:5120\n"
    "ds_write2_b32 v235, v90, v91 offset0:40 offset1:60\n"
    "v_mfma_f32_16x16x32_bf16 v[124:127], a[44:47], v[188:191], v[124:127]\n"
    "v_add_u32_e32 v235, -1360, v235\n"
    "v_lshlrev_b32_sdwa v187, 1, v174 dst_sel:DWORD dst_unused:UNUSED_PAD src0_sel:DWORD src1_sel:WORD_1\n"
    "v_mfma_f32_16x16x32_bf16 v[136:139], v[236:239], v[188:191], v[136:139]\n"
    "ds_read_u16_d16_hi v201, v201\n"
    "ds_write2_b32 v248, v72, v73 offset0:0 offset1:20\n"
    "v_mfma_f32_16x16x32_bf16 v[140:143], a[244:247], v[188:191], v[140:143]\n"
    "s_waitcnt lgkmcnt(9)\n"
    "v_cvt_pk_f16_f32 v188, v176, v177\n"
    "v_lshlrev_b32_sdwa v189, 1, v178 dst_sel:DWORD dst_unused:UNUSED_PAD src0_sel:DWORD src1_sel:WORD_1\n"
    "v_mfma_f32_16x16x32_bf16 v[168:171], a[120:123], v[144:147], 0\n"
    "ds_read_u16 v186, v186\n"
    "ds_write2_b32 v248, v74, v75 offset0:40 offset1:60\n"
    "v_mfma_f32_16x16x32_bf16 v[168:171], a[168:171], v[148:151], v[168:171]\n"
    "s_waitcnt lgkmcnt(8)\n"
    "v_add_u32_e32 v248, 1360, v248\n"
    "v_lshlrev_b32_sdwa v190, 1, v180 dst_sel:DWORD dst_unused:UNUSED_PAD src0_sel:DWORD src1_sel:WORD_1\n"
    "v_mfma_f32_16x16x32_bf16 v[168:171], a[76:79], v[152:155], v[168:171]\n"
    "ds_read_u16_d16_hi v202, v202\n"
    "ds_read_b128 v[236:239], v244 offset:12288\n"
    "v_lshlrev_b32_sdwa v191, 1, v182 dst_sel:DWORD dst_unused:UNUSED_PAD src0_sel:DWORD src1_sel:WORD_1\n"
    "v_or_b32_e64 v195, v195, v203\n"
    "v_lshlrev_b32_sdwa v203, 1, v175 dst_sel:DWORD dst_unused:UNUSED_PAD src0_sel:DWORD src1_sel:WORD_1\n"
    "v_mfma_f32_16x16x32_bf16 v[172:175], a[124:127], v[144:147], 0\n"
    "ds_read_u16 v187, v187\n"
    "v_or_b32_e32 v196, v196, v204\n"
    "v_mfma_f32_16x16x32_bf16 v[172:175], a[172:175], v[148:151], v[172:175]\n"
    "s_waitcnt lgkmcnt(7)\n"
    "ds_read_u16_d16_hi v203, v203\n"
    "buffer_store_dwordx4 a[252:255], v255, s[44:47], s88 offen offset:128 sc1\n"
    "v_mfma_f32_16x16x32_bf16 v[172:175], a[240:243], v[152:155], v[172:175]\n"
    "ds_read_b128 a[244:247], v244 offset:14336\n"
    "ds_write2_b32 v248, v76, v77 offset0:0 offset1:20\n"
    "v_or_b32_e64 v197, v197, v205\n"
    "v_lshlrev_b32_sdwa v205, 1, v179 dst_sel:DWORD dst_unused:UNUSED_PAD src0_sel:DWORD src1_sel:WORD_1\n"
    "v_mfma_f32_16x16x32_bf16 v[176:179], a[120:123], v[156:159], 0\n"
    "s_waitcnt lgkmcnt(8)\n"
    "v_or_b32_e32 v198, v198, v206\n"
    "v_lshlrev_b32_sdwa v206, 1, v181 dst_sel:DWORD dst_unused:UNUSED_PAD src0_sel:DWORD src1_sel:WORD_1\n"
    "v_mfma_f32_16x16x32_bf16 v[176:179], a[168:171], v[160:163], v[176:179]\n"
    "ds_read_u16 v189, v189\n"
    "ds_write2_b32 v248, v78, v79 offset0:40 offset1:60\n"
    "v_mfma_f32_16x16x32_bf16 v[176:179], a[76:79], v[164:167], v[176:179]\n"
    "v_add_u32_e32 v248, 1360, v248\n"
    "v_or_b32_e64 v199, v199, v207\n"
    "v_mfma_f32_16x16x32_bf16 v[96:99], a[176:179], v[192:195], v[96:99]\n"
    "ds_read_u16_d16_hi v205, v205\n"
    "ds_read_u16 v190, v190\n"
    "v_mfma_f32_16x16x32_bf16 v[100:103], a[180:183], v[192:195], v[100:103]\n"
    "v_lshlrev_b32_sdwa v207, 1, v183 dst_sel:DWORD dst_unused:UNUSED_PAD src0_sel:DWORD src1_sel:WORD_1\n"
    "v_or_b32_e32 v185, v185, v201\n"
    "v_mfma_f32_16x16x32_bf16 v[180:183], a[124:127], v[156:159], 0\n"
    "s_waitcnt lgkmcnt(8)\n"
    "ds_read_b128 v[144:147], v243 offset:0\n"
    "v_or_b32_e64 v186, v186, v202\n"
    "v_mfma_f32_16x16x32_bf16 v[180:183], a[172:175], v[160:163], v[180:183]\n"
    "ds_read_u16_d16_hi v206, v206\n"
    "v_pk_fma_f16 v232, v184, v184, s48\n"
    "v_mfma_f32_16x16x32_bf16 v[180:183], a[240:243], v[164:167], v[180:183]\n"
    "v_pk_mul_f16 v232, v184, v232\n"
    "v_lshlrev_b32_sdwa v201, 1, v171 dst_sel:DWORD dst_unused:UNUSED_PAD src0_sel:DWORD src1_sel:WORD_1\n"
    "v_mfma_f32_16x16x32_bf16 v[112:115], a[184:187], v[192:195], v[112:115]\n"
    "ds_read_b128 v[148:151], v243 offset:1024\n"
    "v_exp_f16_e32 v234, v232\n"
    "v_mfma_f32_16x16x32_bf16 v[116:119], a[188:191], v[192:195], v[116:119]\n"
    "s_waitcnt lgkmcnt(7)\n"
    "ds_read_u16 v191, v191\n"
    "v_or_b32_e64 v187, v187, v203\n"
    "v_mfma_f32_16x16x32_bf16 v[128:131], v[236:239], v[192:195], v[128:131]\n"
    "v_exp_f16_sdwa v234, v232 dst_sel:WORD_1 dst_unused:UNUSED_PRESERVE src0_sel:WORD_1\n"
    "v_mfma_f32_16x16x32_bf16 v[132:135], a[244:247], v[192:195], v[132:135]\n"
    "ds_read_b128 v[152:155], v243 offset:2048\n"
    "ds_write2_b32 v248, v80, v81 offset0:0 offset1:20\n"
    "v_mfma_f32_16x16x32_bf16 v[104:107], a[176:179], v[196:199], v[104:107]\n"
    "v_pk_fma_f16 v234, v234, s49, s49\n"
    "v_cvt_pk_f16_f32 v192, v168, v169\n"
    "v_mfma_f32_16x16x32_bf16 v[108:111], a[180:183], v[196:199], v[108:111]\n"
    "ds_read_u16_d16_hi v207, v207\n"
    "v_lshlrev_b32_sdwa v193, 1, v170 dst_sel:DWORD dst_unused:UNUSED_PAD src0_sel:DWORD src1_sel:WORD_1\n"
    "v_mfma_f32_16x16x32_bf16 v[120:123], a[184:187], v[196:199], v[120:123]\n"
    "ds_read_b128 v[156:159], v243 offset:3072\n"
    "v_lshlrev_b32_sdwa v194, 1, v172 dst_sel:DWORD dst_unused:UNUSED_PAD src0_sel:DWORD src1_sel:WORD_1\n"
    "v_mfma_f32_16x16x32_bf16 v[124:127], a[188:191], v[196:199], v[124:127]\n"
    "s_waitcnt lgkmcnt(9)\n"
    "ds_read_b128 v[160:163], v243 offset:4096\n"
    "v_or_b32_e32 v189, v189, v205\n"
    "v_mfma_f32_16x16x32_bf16 v[136:139], v[236:239], v[196:199], v[136:139]\n"
    "s_nop 0\n"
    "ds_read_b128 v[164:167], v243 offset:5120\n"
    "v_lshlrev_b32_sdwa v202, 1, v173 dst_sel:DWORD dst_unused:UNUSED_PAD src0_sel:DWORD src1_sel:WORD_1\n"
    "v_mfma_f32_16x16x32_bf16 v[140:143], a[244:247], v[196:199], v[140:143]\n"
    "s_waitcnt lgkmcnt(9)\n"
    "ds_read_b128 a[240:243], v244 offset:32768\n"
    "buffer_load_dwordx4 a[232:235], v245, s[40:43], s58 offen offset:64\n"
    "buffer_load_dwordx4 a[224:227], v245, s[40:43], s58 offen offset:0\n"
    "v_mfma_f32_16x16x32_bf16 v[168:171], a[80:83], v[144:147], 0\n"
    "ds_read_u16 v193, v193\n"
    "v_lshlrev_b32_sdwa v195, 1, v174 dst_sel:DWORD dst_unused:UNUSED_PAD src0_sel:DWORD src1_sel:WORD_1\n"
    "v_lshlrev_b32_sdwa v203, 1, v175 dst_sel:DWORD dst_unused:UNUSED_PAD src0_sel:DWORD src1_sel:WORD_1\n"
    "s_waitcnt lgkmcnt(9)\n"
    "v_mfma_f32_16x16x32_bf16 v[168:171], a[128:131], v[148:151], v[168:171]\n"
    "s_nop 0\n"
    "ds_read_u16_d16_hi v201, v201\n"
    "v_rcp_f16_e64 v232, v234\n"
    "v_or_b32_e32 v190, v190, v206\n"
    "v_rcp_f16_sdwa v232, v234 dst_sel:WORD_1 dst_unused:UNUSED_PRESERVE src0_sel:WORD_1\n"
    "v_mfma_f32_16x16x32_bf16 v[172:175], a[84:87], v[144:147], 0\n"
    "s_waitcnt lgkmcnt(8)\n"
    "ds_read_u16 v194, v194\n"
    "v_cvt_pk_f16_f32 v196, v176, v177\n"
    "v_mfma_f32_16x16x32_bf16 v[168:171], a[48:51], v[152:155], v[168:171]\n"
    "ds_read_u16_d16_hi v202, v202\n"
    "v_lshlrev_b32_sdwa v197, 1, v178 dst_sel:DWORD dst_unused:UNUSED_PAD src0_sel:DWORD src1_sel:WORD_1\n"
    "v_mfma_f32_16x16x32_bf16 v[172:175], a[132:135], v[148:151], v[172:175]\n"
    "v_lshlrev_b32_sdwa v205, 1, v179 dst_sel:DWORD dst_unused:UNUSED_PAD src0_sel:DWORD src1_sel:WORD_1\n"
    "v_lshlrev_b32_sdwa v198, 1, v180 dst_sel:DWORD dst_unused:UNUSED_PAD src0_sel:DWORD src1_sel:WORD_1\n"
    "v_lshlrev_b32_sdwa v206, 1, v181 dst_sel:DWORD dst_unused:UNUSED_PAD src0_sel:DWORD src1_sel:WORD_1\n"
    "v_lshlrev_b32_sdwa v199, 1, v182 dst_sel:DWORD dst_unused:UNUSED_PAD src0_sel:DWORD src1_sel:WORD_1\n"
    "v_fma_mix_f32 v234, v184, v232, 0 op_sel_hi:[1,1,0]\n"
    "v_fma_mix_f32 v232, v184, v232, 0 op_sel:[1,1,0] op_sel_hi:[1,1,0]\n"
    "v_lshlrev_b32_sdwa v200, 1, v169 dst_sel:DWORD dst_unused:UNUSED_PAD src0_sel:DWORD src1_sel:WORD_1\n"
    "s_waitcnt lgkmcnt(7)\n"
    "v_mfma_f32_16x16x32_bf16 v[176:179], a[80:83], v[156:159], 0\n"
    "s_nop 0\n"
    "ds_read_b128 v[236:239], v244 offset:16384\n"
    "ds_write2_b32 v248, v82, v83 offset0:40 offset1:60\n"
    "v_mfma_f32_32x32x1_2b_f32 v[64:95], v254, v254, 0\n"
    "v_add_u32_e32 v248, -5440, v248\n"
    "v_or_b32_e64 v191, v191, v207\n"
    "v_lshlrev_b32_sdwa v207, 1, v183 dst_sel:DWORD dst_unused:UNUSED_PAD src0_sel:DWORD src1_sel:WORD_1\n"
    "v_cvt_pk_bf16_f32 v184, v234, v232\n"
    "v_pk_fma_f16 v232, v188, v188, s48\n"
    "v_lshlrev_b32_e64 v254, 3, v244\n"
    "v_pk_mul_f16 v232, v188, v232\n"
    "v_exp_f16_e64 v234, v232\n"
    "ds_read_u16 v195, v195\n"
    "ds_read_b128 v[216:219], v251 offset:21504\n"
    "v_exp_f16_sdwa v234, v232 dst_sel:WORD_1 dst_unused:UNUSED_PRESERVE src0_sel:WORD_1\n"
    "s_waitcnt lgkmcnt(7)\n"
    "v_mfma_f32_16x16x32_bf16 v[172:175], a[240:243], v[152:155], v[172:175]\n"
    "ds_read_b128 v[220:223], v251 offset:21840\n"
    "ds_read_u16_d16_hi v203, v203\n"
    "v_mfma_f32_16x16x32_bf16 v[176:179], a[128:131], v[160:163], v[176:179]\n"
    "s_waitcnt lgkmcnt(8)\n"
    "ds_read_b128 a[244:247], v244 offset:18432\n"
    "ds_read_u16 v197, v197\n"
    "v_mfma_f32_16x16x32_bf16 v[176:179], a[48:51], v[164:167], v[176:179]\n"
    "s_mov_b32 exec_hi, 0\n"
    "buffer_load_dword v233, v254, s[72:75], s53 offen sc1\n"
    "s_mov_b32 exec_hi, -1\n"
    "v_mfma_f32_16x16x32_bf16 v[180:183], a[84:87], v[156:159], 0\n"
    "s_add_u32 s53, s53, 393216\n"
    "ds_read_u16_d16_hi v205, v205\n"
    "v_pk_fma_f16 v234, v234, s49, s49\n"
    "v_mfma_f32_16x16x32_bf16 v[180:183], a[132:135], v[160:163], v[180:183]\n"
    "v_rcp_f16_e32 v232, v234\n"
    "v_mfma_f32_16x16x32_bf16 v[180:183], a[240:243], v[164:167], v[180:183]\n"
    "s_waitcnt lgkmcnt(7)\n"
    "ds_read_u16 v198, v198\n"
    "v_rcp_f16_sdwa v232, v234 dst_sel:WORD_1 dst_unused:UNUSED_PRESERVE src0_sel:WORD_1\n"
    "v_mfma_f32_16x16x32_bf16 v[96:99], a[192:195], v[184:187], v[96:99]\n"
    "s_barrier\n"
    "v_mfma_f32_16x16x32_bf16 v[100:103], a[196:199], v[184:187], v[100:103]\n"
    "s_nop 0\n"
    "ds_read_b128 v[208:211], v250 offset:0\n"
    "ds_read_b128 v[212:215], v250 offset:320\n"
    "v_mfma_f32_16x16x32_bf16 v[112:115], a[200:203], v[184:187], v[112:115]\n"
    "ds_read_u16_d16_hi v206, v206\n"
    "v_or_b32_e64 v193, v193, v201\n"
    "v_mfma_f32_16x16x32_bf16 v[116:119], a[204:207], v[184:187], v[116:119]\n"
    "ds_read_u16 v199, v199\n"
    "v_fma_mix_f32 v234, v188, v232, 0 op_sel_hi:[1,1,0]\n"
    "v_mfma_f32_16x16x32_bf16 v[128:131], v[236:239], v[184:187], v[128:131]\n"
    "v_fma_mix_f32 v232, v188, v232, 0 op_sel:[1,1,0] op_sel_hi:[1,1,0]\n"
    "v_or_b32_e32 v194, v194, v202\n"
    "s_waitcnt lgkmcnt(7)\n"
    "v_mfma_f32_16x16x32_bf16 v[132:135], a[244:247], v[184:187], v[132:135]\n"
    "buffer_load_dwordx4 a[236:239], v245, s[40:43], s89 offen offset:64\n"
    "buffer_load_dwordx4 a[228:231], v245, s[40:43], s89 offen offset:0\n"
    "ds_read_u16_d16_hi v207, v207\n"
    "v_cvt_pk_bf16_f32 v188, v234, v232\n"
    "v_pk_fma_f16 v232, v192, v192, s48\n"
    "v_or_b32_e64 v195, v195, v203\n"
    "v_mfma_f32_16x16x32_bf16 v[104:107], a[192:195], v[188:191], v[104:107]\n"
    "ds_read_b128 a[240:243], v244 offset:36864\n"
    "ds_read_u16_d16_hi v200, v200\n"
    "v_mfma_f32_16x16x32_bf16 v[108:111], a[196:199], v[188:191], v[108:111]\n"
    "v_pk_mul_f16 v232, v192, v232\n"
    "v_lshlrev_b32_sdwa v184, 1, v168 dst_sel:DWORD dst_unused:UNUSED_PAD src0_sel:DWORD src1_sel:WORD_1\n"
    "v_mfma_f32_16x16x32_bf16 v[120:123], a[200:203], v[188:191], v[120:123]\n"
    "s_waitcnt lgkmcnt(8)\n"
    "ds_read_u16 v184, v184\n"
    "v_exp_f16_e32 v234, v232\n"
    "v_mfma_f32_16x16x32_bf16 v[124:127], a[204:207], v[188:191], v[124:127]\n"
    "v_exp_f16_sdwa v234, v232 dst_sel:WORD_1 dst_unused:UNUSED_PRESERVE src0_sel:WORD_1\n"
    "v_mfma_f32_16x16x32_bf16 v[136:139], v[236:239], v[188:191], v[136:139]\n"
    "ds_read_b128 v[236:239], v244 offset:20480\n"
    "v_or_b32_e32 v197, v197, v205\n"
    "s_waitcnt lgkmcnt(7)\n"
    "v_smfmac_f32_16x16x64_bf16 v[64:67], v[216:219], v[208:215], v240\n"
    "ds_read_b128 v[216:219], v252 offset:21504\n"
    "v_smfmac_f32_16x16x64_bf16 v[68:71], v[220:223], v[208:215], v240 abid:1\n"
    "s_nop 0\n"
    "ds_read_b128 v[208:211], v250 offset:640\n"
    "ds_read_b128 v[212:215], v250 offset:960\n"
    "v_mfma_f32_16x16x32_bf16 v[140:143], a[244:247], v[188:191], v[140:143]\n"
    "s_waitcnt lgkmcnt(9)\n"
    "ds_read_b128 v[220:223], v252 offset:21840\n"
    "v_pk_fma_f16 v234, v234, s49, s49\n"
    "v_lshlrev_b32_sdwa v185, 1, v170 dst_sel:DWORD dst_unused:UNUSED_PAD src0_sel:DWORD src1_sel:WORD_1\n"
    "v_lshlrev_b32_sdwa v201, 1, v171 dst_sel:DWORD dst_unused:UNUSED_PAD src0_sel:DWORD src1_sel:WORD_1\n"
    "v_mfma_f32_16x16x32_bf16 v[168:171], a[88:91], v[144:147], 0\n"
    "ds_read_u16 v185, v185\n"
    "v_lshlrev_b32_sdwa v186, 1, v172 dst_sel:DWORD dst_unused:UNUSED_PAD src0_sel:DWORD src1_sel:WORD_1\n"
    "v_mfma_f32_16x16x32_bf16 v[168:171], a[136:139], v[148:151], v[168:171]\n"
    "ds_read_u16_d16_hi v201, v201\n"
    "v_lshlrev_b32_sdwa v202, 1, v173 dst_sel:DWORD dst_unused:UNUSED_PAD src0_sel:DWORD src1_sel:WORD_1\n"
    "v_lshlrev_b32_sdwa v187, 1, v174 dst_sel:DWORD dst_unused:UNUSED_PAD src0_sel:DWORD src1_sel:WORD_1\n"
    "v_lshlrev_b32_sdwa v203, 1, v175 dst_sel:DWORD dst_unused:UNUSED_PAD src0_sel:DWORD src1_sel:WORD_1\n"
    "v_rcp_f16_e32 v232, v234\n"
    "v_mfma_f32_16x16x32_bf16 v[168:171], a[52:55], v[152:155], v[168:171]\n"
    "s_waitcnt lgkmcnt(9)\n"
    "ds_read_u16 v186, v186\n"
    "v_or_b32_e64 v198, v198, v206\n"
    "v_rcp_f16_sdwa v232, v234 dst_sel:WORD_1 dst_unused:UNUSED_PRESERVE src0_sel:WORD_1\n"
    "v_mfma_f32_16x16x32_bf16 v[172:175], a[92:95], v[144:147], 0\n"
    "ds_read_u16_d16_hi v202, v202\n"
    "v_lshlrev_b32_sdwa v188, 1, v176 dst_sel:DWORD dst_unused:UNUSED_PAD src0_sel:DWORD src1_sel:WORD_1\n"
    "v_mfma_f32_16x16x32_bf16 v[172:175], a[140:143], v[148:151], v[172:175]\n"
    "v_lshlrev_b32_sdwa v204, 1, v177 dst_sel:DWORD dst_unused:UNUSED_PAD src0_sel:DWORD src1_sel:WORD_1\n"
    "v_lshlrev_b32_sdwa v189, 1, v178 dst_sel:DWORD dst_unused:UNUSED_PAD src0_sel:DWORD src1_sel:WORD_1\n"
    "v_lshlrev_b32_sdwa v205, 1, v179 dst_sel:DWORD dst_unused:UNUSED_PAD src0_sel:DWORD src1_sel:WORD_1\n"
    "v_lshlrev_b32_sdwa v190, 1, v180 dst_sel:DWORD dst_unused:UNUSED_PAD src0_sel:DWORD src1_sel:WORD_1\n"
    "v_lshlrev_b32_sdwa v206, 1, v181 dst_sel:DWORD dst_unused:UNUSED_PAD src0_sel:DWORD src1_sel:WORD_1\n"
    "v_lshlrev_b32_sdwa v191, 1, v182 dst_sel:DWORD dst_unused:UNUSED_PAD src0_sel:DWORD src1_sel:WORD_1\n"
    "v_fma_mix_f32 v234, v192, v232, 0 op_sel_hi:[1,1,0]\n"
    "v_mfma_f32_16x16x32_bf16 v[172:175], a[240:243], v[152:155], v[172:175]\n"
    "s_waitcnt lgkmcnt(7)\n"
    "ds_read_u16 v187, v187\n"
    "ds_read_b128 a[244:247], v244 offset:22528\n"
    "v_or_b32_e32 v199, v199, v207\n"
    "v_lshlrev_b32_sdwa v207, 1, v183 dst_sel:DWORD dst_unused:UNUSED_PAD src0_sel:DWORD src1_sel:WORD_1\n"
    "v_fma_mix_f32 v232, v192, v232, 0 op_sel:[1,1,0] op_sel_hi:[1,1,0]\n"
    "s_waitcnt lgkmcnt(7)\n"
    "v_smfmac_f32_16x16x64_bf16 v[64:67], v[216:219], v[208:215], v240\n"
    "s_waitcnt lgkmcnt(6)\n"
    "ds_read_b128 v[216:219], v251 offset:22176\n"
    "ds_read_u16_d16_hi v203, v203\n"
    "v_smfmac_f32_16x16x64_bf16 v[68:71], v[220:223], v[208:215], v240 abid:1\n"
    "v_cvt_pk_bf16_f32 v192, v234, v232\n"
    "v_mfma_f32_16x16x32_bf16 v[176:179], a[88:91], v[156:159], 0\n"
    "ds_read_b128 v[208:211], v250 offset:80\n"
    "v_pk_fma_f16 v232, v196, v196, s48\n"
    "v_or_b32_e64 v184, v184, v200\n"
    "v_pk_mul_f16 v232, v196, v232\n"
    "v_lshlrev_b32_e64 v254, 3, v244\n"
    "v_pk_add_f32 v[224:225], v[224:225], v[64:65]\n"
    "v_pk_add_f32 v[226:227], v[226:227], v[66:67]\n"
    "v_pk_fma_f32 v[228:229], v[64:65], v[64:65], v[228:229]\n"
    "v_pk_fma_f32 v[230:231], v[66:67], v[66:67], v[230:231]\n"
    "v_pk_add_f32 v[224:225], v[224:225], v[68:69]\n"
    "v_pk_add_f32 v[226:227], v[226:227], v[70:71]\n"
    "v_pk_fma_f32 v[228:229], v[68:69], v[68:69], v[228:229]\n"
    "v_pk_fma_f32 v[230:231], v[70:71], v[70:71], v[230:231]\n"
    "v_mfma_f32_16x16x32_bf16 v[176:179], a[136:139], v[160:163], v[176:179]\n"
    "s_waitcnt lgkmcnt(7)\n"
    "ds_read_b128 v[212:215], v250 offset:400\n"
    "ds_read_b128 v[220:223], v251 offset:22512\n"
    "v_mfma_f32_16x16x32_bf16 v[176:179], a[52:55], v[164:167], v[176:179]\n"
    "s_nop 0\n"
    "ds_read_u16 v188, v188\n"
    "ds_read_u16_d16_hi v204, v204\n"
    "v_mfma_f32_16x16x32_bf16 v[180:183], a[92:95], v[156:159], 0\n"
    "v_exp_f16_e32 v234, v232\n"
    "v_mfma_f32_16x16x32_bf16 v[180:183], a[140:143], v[160:163], v[180:183]\n"
    "s_waitcnt lgkmcnt(9)\n"
    "ds_read_u16 v189, v189\n"
    "v_exp_f16_sdwa v234, v232 dst_sel:WORD_1 dst_unused:UNUSED_PRESERVE src0_sel:WORD_1\n"
    "v_mfma_f32_16x16x32_bf16 v[180:183], a[240:243], v[164:167], v[180:183]\n"
    "ds_read_u16_d16_hi v205, v205\n"
    "v_pk_fma_f16 v234, v234, s49, s49\n"
    "v_mfma_f32_16x16x32_bf16 v[96:99], a[208:211], v[192:195], v[96:99]\n"
    "v_rcp_f16_e64 v232, v234\n"
    "v_mfma_f32_16x16x32_bf16 v[100:103], a[212:215], v[192:195], v[100:103]\n"
    "ds_read_u16 v190, v190\n"
    "v_rcp_f16_sdwa v232, v234 dst_sel:WORD_1 dst_unused:UNUSED_PRESERVE src0_sel:WORD_1\n"
    "v_mfma_f32_16x16x32_bf16 v[112:115], a[216:219], v[192:195], v[112:115]\n"
    "ds_read_u16_d16_hi v206, v206\n"
    "v_fma_mix_f32 v234, v196, v232, 0 op_sel_hi:[1,1,0]\n"
    "v_mfma_f32_16x16x32_bf16 v[116:119], a[220:223], v[192:195], v[116:119]\n"
    "s_waitcnt lgkmcnt(8)\n"
    "v_fma_mix_f32 v232, v196, v232, 0 op_sel:[1,1,0] op_sel_hi:[1,1,0]\n"
    "v_or_b32_e32 v185, v185, v201\n"
    "s_waitcnt lgkmcnt(7)\n"
    "v_smfmac_f32_16x16x64_bf16 v[72:75], v[216:219], v[208:215], v240\n"
    "s_waitcnt lgkmcnt(6)\n"
    "ds_read_b128 v[216:219], v252 offset:22176\n"
    "s_mov_b32 exec_hi, 0\n"
    "buffer_load_dword v233, v254, s[72:75], s53 offen sc1\n"
    "s_mov_b32 exec_hi, -1\n"
    "v_smfmac_f32_16x16x64_bf16 v[76:79], v[220:223], v[208:215], v240 abid:1\n"
    "s_add_u32 s53, s53, 393216\n"
    "ds_read_b128 v[208:211], v250 offset:720\n"
    "ds_read_b128 v[212:215], v250 offset:1040\n"
    "v_mfma_f32_16x16x32_bf16 v[128:131], v[236:239], v[192:195], v[128:131]\n"
    "ds_read_b128 v[220:223], v252 offset:22512\n"
    "v_cvt_pk_bf16_f32 v196, v234, v232\n"
    "v_mfma_f32_16x16x32_bf16 v[132:135], a[244:247], v[192:195], v[132:135]\n"
    "s_nop 0\n"
    "ds_read_u16 v191, v191\n"
    "v_cvt_pk_f16_f32 v192, v168, v169\n"
    "v_mfma_f32_16x16x32_bf16 v[104:107], a[208:211], v[196:199], v[104:107]\n"
    "s_waitcnt lgkmcnt(9)\n"
    "v_lshlrev_b32_sdwa v193, 1, v170 dst_sel:DWORD dst_unused:UNUSED_PAD src0_sel:DWORD src1_sel:WORD_1\n"
    "v_lshlrev_b32_sdwa v201, 1, v171 dst_sel:DWORD dst_unused:UNUSED_PAD src0_sel:DWORD src1_sel:WORD_1\n"
    "v_mfma_f32_16x16x32_bf16 v[108:111], a[212:215], v[196:199], v[108:111]\n"
    "ds_read_u16_d16_hi v207, v207\n"
    "v_lshlrev_b32_sdwa v194, 1, v172 dst_sel:DWORD dst_unused:UNUSED_PAD src0_sel:DWORD src1_sel:WORD_1\n"
    "v_mfma_f32_16x16x32_bf16 v[120:123], a[216:219], v[196:199], v[120:123]\n"
    "ds_read_u16 v193, v193\n"
    "v_or_b32_e64 v186, v186, v202\n"
    "v_mfma_f32_16x16x32_bf16 v[124:127], a[220:223], v[196:199], v[124:127]\n"
    "v_lshlrev_b32_sdwa v202, 1, v173 dst_sel:DWORD dst_unused:UNUSED_PAD src0_sel:DWORD src1_sel:WORD_1\n"
    "v_lshlrev_b32_sdwa v195, 1, v174 dst_sel:DWORD dst_unused:UNUSED_PAD src0_sel:DWORD src1_sel:WORD_1\n"
    "v_mfma_f32_16x16x32_bf16 v[136:139], v[236:239], v[196:199], v[136:139]\n"
    "s_waitcnt lgkmcnt(9)\n"
    "ds_read_u16_d16_hi v201, v201\n"
    "v_or_b32_e32 v187, v187, v203\n"
    "v_mfma_f32_16x16x32_bf16 v[140:143], a[244:247], v[196:199], v[140:143]\n"
    "v_lshlrev_b32_sdwa v203, 1, v175 dst_sel:DWORD dst_unused:UNUSED_PAD src0_sel:DWORD src1_sel:WORD_1\n"
    "v_cvt_pk_f16_f32 v196, v176, v177\n"
    "v_mfma_f32_16x16x32_bf16 v[168:171], a[96:99], v[144:147], 0\n"
    "s_nop 0\n"
    "ds_read_u16 v194, v194\n"
    "v_lshlrev_b32_sdwa v197, 1, v178 dst_sel:DWORD dst_unused:UNUSED_PAD src0_sel:DWORD src1_sel:WORD_1\n"
    "v_mfma_f32_16x16x32_bf16 v[168:171], a[144:147], v[148:151], v[168:171]\n"
    "s_waitcnt lgkmcnt(8)\n"
    "v_lshlrev_b32_sdwa v198, 1, v180 dst_sel:DWORD dst_unused:UNUSED_PAD src0_sel:DWORD src1_sel:WORD_1\n"
    "v_lshlrev_b32_sdwa v199, 1, v182 dst_sel:DWORD dst_unused:UNUSED_PAD src0_sel:DWORD src1_sel:WORD_1\n"
    "v_mfma_f32_16x16x32_bf16 v[168:171], a[56:59], v[152:155], v[168:171]\n"
    "ds_read_u16_d16_hi v202, v202\n"
    "v_or_b32_e64 v188, v188, v204\n"
    "s_waitcnt lgkmcnt(7)\n"
    "v_smfmac_f32_16x16x64_bf16 v[72:75], v[216:219], v[208:215], v240\n"
    "s_waitcnt lgkmcnt(6)\n"
    "ds_read_b128 v[216:219], v251 offset:22848\n"
    "v_smfmac_f32_16x16x64_bf16 v[76:79], v[220:223], v[208:215], v240 abid:1\n"
    "ds_read_b128 v[208:211], v250 offset:160\n"
    "v_mfma_f32_16x16x32_bf16 v[172:175], a[100:103], v[144:147], 0\n"
    "ds_read_b128 v[212:215], v250 offset:480\n"
    "v_or_b32_e64 v189, v189, v205\n"
    "v_mfma_f32_16x16x32_bf16 v[172:175], a[148:151], v[148:151], v[172:175]\n"
    "ds_read_b128 v[220:223], v251 offset:23184\n"
    "v_lshlrev_b32_sdwa v205, 1, v179 dst_sel:DWORD dst_unused:UNUSED_PAD src0_sel:DWORD src1_sel:WORD_1\n"
    "v_cvt_pk_bf16_f32 v96, v96, v97\n"
    "v_cvt_pk_bf16_f32 v97, v98, v99\n"
    "v_cvt_pk_bf16_f32 v98, v100, v101\n"
    "v_pk_add_f32 v[224:225], v[224:225], v[72:73]\n"
    "v_pk_add_f32 v[226:227], v[226:227], v[74:75]\n"
    "v_pk_fma_f32 v[228:229], v[72:73], v[72:73], v[228:229]\n"
    "v_pk_fma_f32 v[230:231], v[74:75], v[74:75], v[230:231]\n"
    "v_pk_add_f32 v[224:225], v[224:225], v[76:77]\n"
    "v_pk_add_f32 v[226:227], v[226:227], v[78:79]\n"
    "v_pk_fma_f32 v[228:229], v[76:77], v[76:77], v[228:229]\n"
    "v_pk_fma_f32 v[230:231], v[78:79], v[78:79], v[230:231]\n"
    "v_mfma_f32_16x16x32_bf16 v[172:175], a[60:63], v[152:155], v[172:175]\n"
    "s_waitcnt lgkmcnt(7)\n"
    "ds_read_b128 v[236:239], v244 offset:0\n"
    "ds_read_u16 v195, v195\n"
    "v_mfma_f32_16x16x32_bf16 v[176:179], a[96:99], v[156:159], 0\n"
    "s_nop 0\n"
    "ds_read_u16_d16_hi v203, v203\n"
    "v_cvt_pk_bf16_f32 v99, v102, v103\n"
    "v_mfma_f32_16x16x32_bf16 v[176:179], a[144:147], v[160:163], v[176:179]\n"
    "s_waitcnt lgkmcnt(9)\n"
    "ds_read_b128 a[244:247], v244 offset:2048\n"
    "v_or_b32_e32 v190, v190, v206\n"
    "v_mfma_f32_16x16x32_bf16 v[176:179], a[56:59], v[164:167], v[176:179]\n"
    "ds_read_u16 v197, v197\n"
    "v_lshlrev_b32_sdwa v206, 1, v181 dst_sel:DWORD dst_unused:UNUSED_PAD src0_sel:DWORD src1_sel:WORD_1\n"
    "v_mfma_f32_16x16x32_bf16 v[100:103], a[4:7], v[184:187], 0\n"
    "s_nop 0\n"
    "v_cvt_pk_bf16_f32 v112, v112, v113\n"
    "ds_write_b128 v242, v[96:99] offset:24640\n"
    "v_mfma_f32_16x16x32_bf16 v[96:99], a[0:3], v[184:187], 0\n"
    "s_waitcnt lgkmcnt(8)\n"
    "v_cvt_pk_bf16_f32 v113, v114, v115\n"
    "v_cvt_pk_bf16_f32 v114, v116, v117\n"
    "s_waitcnt lgkmcnt(7)\n"
    "v_smfmac_f32_16x16x64_bf16 v[80:83], v[216:219], v[208:215], v240\n"
    "s_waitcnt lgkmcnt(6)\n"
    "ds_read_b128 v[216:219], v252 offset:22848\n"
    "ds_read_u16_d16_hi v205, v205\n"
    "v_smfmac_f32_16x16x64_bf16 v[84:87], v[220:223], v[208:215], v240 abid:1\n"
    "ds_read_b128 v[208:211], v250 offset:800\n"
    "v_cvt_pk_bf16_f32 v115, v118, v119\n"
    "v_cvt_pk_bf16_f32 v128, v128, v129\n"
    "ds_read_b128 v[212:215], v250 offset:1120\n"
    "v_cvt_pk_bf16_f32 v129, v130, v131\n"
    "ds_read_b128 v[220:223], v252 offset:23184\n"
    "v_or_b32_e64 v191, v191, v207\n"
    "v_mfma_f32_16x16x32_bf16 v[116:119], a[12:15], v[184:187], 0\n"
    "ds_read_u16 v198, v198\n"
    "v_lshlrev_b32_sdwa v207, 1, v183 dst_sel:DWORD dst_unused:UNUSED_PAD src0_sel:DWORD src1_sel:WORD_1\n"
    "v_mfma_f32_16x16x32_bf16 v[180:183], a[100:103], v[156:159], 0\n"
    "v_cvt_pk_bf16_f32 v130, v132, v133\n"
    "v_or_b32_e32 v193, v193, v201\n"
    "v_mfma_f32_16x16x32_bf16 v[180:183], a[148:151], v[160:163], v[180:183]\n"
    "s_waitcnt lgkmcnt(8)\n"
    "ds_read_u16_d16_hi v206, v206\n"
    "v_cvt_pk_bf16_f32 v131, v134, v135\n"
    "v_mfma_f32_16x16x32_bf16 v[180:183], a[60:63], v[164:167], v[180:183]\n"
    "ds_read_u16 v199, v199\n"
    "v_cvt_pk_bf16_f32 v104, v104, v105\n"
    "v_mfma_f32_16x16x32_bf16 v[132:135], a[244:247], v[184:187], 0\n"
    "v_cvt_pk_bf16_f32 v105, v106, v107\n"
    "v_cvt_pk_bf16_f32 v106, v108, v109\n"
    "v_cvt_pk_bf16_f32 v107, v110, v111\n"
    "v_mfma_f32_16x16x32_bf16 v[108:111], a[4:7], v[188:191], 0\n"
    "ds_read_u16_d16_hi v207, v207\n"
    "v_or_b32_e64 v194, v194, v202\n"
    "v_cvt_pk_bf16_f32 v120, v120, v121\n"
    "v_cvt_pk_bf16_f32 v121, v122, v123\n"
    "v_cvt_pk_bf16_f32 v122, v124, v125\n"
    "ds_write_b128 v242, v[112:115] offset:26688\n"
    "v_mfma_f32_16x16x32_bf16 v[112:115], a[8:11], v[184:187], 0\n"
    "s_waitcnt lgkmcnt(7)\n"
    "v_cvt_pk_bf16_f32 v123, v126, v127\n"
    "v_add_u32_e32 v254, s54, v242\n"
    "v_cvt_pk_bf16_f32 v136, v136, v137\n"
    "s_waitcnt lgkmcnt(6)\n"
    "v_smfmac_f32_16x16x64_bf16 v[80:83], v[216:219], v[208:215], v240\n"
    "s_waitcnt lgkmcnt(5)\n"
    "ds_read_b128 v[216:219], v251 offset:23520\n"
    "ds_write_b128 v242, v[128:131] offset:28736\n"
    "v_smfmac_f32_16x16x64_bf16 v[84:87], v[220:223], v[208:215], v240 abid:1\n"
    "v_pk_fma_f16 v232, v192, v192, s48\n"
    "v_mfma_f32_16x16x32_bf16 v[128:131], v[236:239], v[184:187], 0\n"
    "v_cvt_pk_bf16_f32 v137, v138, v139\n"
    "v_cvt_pk_bf16_f32 v138, v140, v141\n"
    "v_or_b32_e64 v195, v195, v203\n"
    "ds_read_b128 v[208:211], v250 offset:240\n"
    "ds_read_b128 v[212:215], v250 offset:560\n"
    "v_pk_add_f32 v[224:225], v[224:225], v[80:81]\n"
    "v_pk_add_f32 v[226:227], v[226:227], v[82:83]\n"
    "v_pk_fma_f32 v[228:229], v[80:81], v[80:81], v[228:229]\n"
    "v_pk_fma_f32 v[230:231], v[82:83], v[82:83], v[230:231]\n"
    "v_pk_add_f32 v[224:225], v[224:225], v[84:85]\n"
    "v_pk_add_f32 v[226:227], v[226:227], v[86:87]\n"
    "v_pk_fma_f32 v[228:229], v[84:85], v[84:85], v[228:229]\n"
    "v_pk_fma_f32 v[230:231], v[86:87], v[86:87], v[230:231]\n"
    "v_mfma_f32_16x16x32_bf16 v[124:127], a[12:15], v[188:191], 0\n"
    "s_waitcnt lgkmcnt(6)\n"
    "ds_read_b128 v[220:223], v251 offset:23856\n"
    "ds_write_b128 v242, v[104:107] offset:25664\n"
    "v_mfma_f32_16x16x32_bf16 v[104:107], a[0:3], v[188:191], 0\n"
    "s_waitcnt lgkmcnt(6)\n"
    "v_cvt_pk_bf16_f32 v139, v142, v143\n"
    "v_pk_mul_f16 v232, v192, v232\n"
    "v_or_b32_e64 v197, v197, v205\n"
    "v_exp_f16_e64 v234, v232\n"
    "v_mfma_f32_16x16x32_bf16 v[140:143], a[244:247], v[188:191], 0\n"
    "ds_write_b128 v242, v[120:123] offset:27712\n"
    "v_mfma_f32_16x16x32_bf16 v[120:123], a[8:11], v[188:191], 0\n"
    "v_exp_f16_sdwa v234, v232 dst_sel:WORD_1 dst_unused:UNUSED_PRESERVE src0_sel:WORD_1\n"
    "v_cvt_pk_f16_f32 v184, v168, v169\n"
    "v_pk_fma_f16 v234, v234, s49, s49\n"
    "v_lshlrev_b32_sdwa v185, 1, v170 dst_sel:DWORD dst_unused:UNUSED_PAD src0_sel:DWORD src1_sel:WORD_1\n"
    "s_waitcnt lgkmcnt(3)\n"
    "v_smfmac_f32_16x16x64_bf16 v[88:91], v[216:219], v[208:215], v240\n"
    "ds_read_b128 v[216:219], v252 offset:23520\n"
    "ds_write_b128 v242, v[136:139] offset:29760\n"
    "s_waitcnt lgkmcnt(3)\n"
    "v_smfmac_f32_16x16x64_bf16 v[92:95], v[220:223], v[208:215], v240 abid:1\n"
    "v_lshlrev_b32_sdwa v201, 1, v171 dst_sel:DWORD dst_unused:UNUSED_PAD src0_sel:DWORD src1_sel:WORD_1\n"
    "v_mfma_f32_16x16x32_bf16 v[136:139], v[236:239], v[188:191], 0\n"
    "v_lshlrev_b32_sdwa v186, 1, v172 dst_sel:DWORD dst_unused:UNUSED_PAD src0_sel:DWORD src1_sel:WORD_1\n"
    "v_lshlrev_b32_sdwa v202, 1, v173 dst_sel:DWORD dst_unused:UNUSED_PAD src0_sel:DWORD src1_sel:WORD_1\n"
    "v_mfma_f32_16x16x32_bf16 v[168:171], a[104:107], v[144:147], 0\n"
    "ds_read_b128 v[208:211], v250 offset:880\n"
    "ds_read_b128 v[212:215], v250 offset:1200\n"
    "v_mfma_f32_16x16x32_bf16 v[168:171], a[152:155], v[148:151], v[168:171]\n"
    "ds_read_b128 v[220:223], v252 offset:23856\n"
    "ds_read_u16 v185, v185\n"
    "v_lshlrev_b32_sdwa v187, 1, v174 dst_sel:DWORD dst_unused:UNUSED_PAD src0_sel:DWORD src1_sel:WORD_1\n"
    "v_lshlrev_b32_sdwa v203, 1, v175 dst_sel:DWORD dst_unused:UNUSED_PAD src0_sel:DWORD src1_sel:WORD_1\n"
    "v_rcp_f16_e64 v232, v234\n"
    "v_mfma_f32_16x16x32_bf16 v[168:171], a[64:67], v[152:155], v[168:171]\n"
    "ds_read_u16_d16_hi v201, v201\n"
    "ds_read_u16 v186, v186\n"
    "v_or_b32_e64 v198, v198, v206\n"
    "v_rcp_f16_sdwa v232, v234 dst_sel:WORD_1 dst_unused:UNUSED_PRESERVE src0_sel:WORD_1\n"
    "v_mfma_f32_16x16x32_bf16 v[172:175], a[108:111], v[144:147], 0\n"
    "ds_read_u16_d16_hi v202, v202\n"
    "v_cvt_pk_f16_f32 v188, v176, v177\n"
    "v_mfma_f32_16x16x32_bf16 v[172:175], a[156:159], v[148:151], v[172:175]\n"
    "v_lshlrev_b32_sdwa v189, 1, v178 dst_sel:DWORD dst_unused:UNUSED_PAD src0_sel:DWORD src1_sel:WORD_1\n"
    "v_lshlrev_b32_sdwa v205, 1, v179 dst_sel:DWORD dst_unused:UNUSED_PAD src0_sel:DWORD src1_sel:WORD_1\n"
    "v_lshlrev_b32_sdwa v190, 1, v180 dst_sel:DWORD dst_unused:UNUSED_PAD src0_sel:DWORD src1_sel:WORD_1\n"
    "v_lshlrev_b32_sdwa v206, 1, v181 dst_sel:DWORD dst_unused:UNUSED_PAD src0_sel:DWORD src1_sel:WORD_1\n"
    "v_lshlrev_b32_sdwa v191, 1, v182 dst_sel:DWORD dst_unused:UNUSED_PAD src0_sel:DWORD src1_sel:WORD_1\n"
    "v_fma_mix_f32 v234, v192, v232, 0 op_sel_hi:[1,1,0]\n"
    "v_mfma_f32_16x16x32_bf16 v[172:175], a[68:71], v[152:155], v[172:175]\n"
    "ds_read_b128 v[236:239], v244 offset:4096\n"
    "ds_read_u16 v187, v187\n"
    "v_or_b32_e64 v199, v199, v207\n"
    "v_lshlrev_b32_sdwa v207, 1, v183 dst_sel:DWORD dst_unused:UNUSED_PAD src0_sel:DWORD src1_sel:WORD_1\n"
    "v_fma_mix_f32 v232, v192, v232, 0 op_sel:[1,1,0] op_sel_hi:[1,1,0]\n"
    "s_waitcnt lgkmcnt(7)\n"
    "v_smfmac_f32_16x16x64_bf16 v[88:91], v[216:219], v[208:215], v240\n"
    "s_waitcnt lgkmcnt(6)\n"
    "ds_read_u16_d16_hi v203, v203\n"
    "v_smfmac_f32_16x16x64_bf16 v[92:95], v[220:223], v[208:215], v240 abid:1\n"
    "ds_read_b128 a[244:247], v244 offset:6144\n"
    "v_mfma_f32_16x16x32_bf16 v[176:179], a[104:107], v[156:159], 0\n"
    "ds_read_u16 v189, v189\n"
    "v_cvt_pk_bf16_f32 v192, v234, v232\n"
    "v_mfma_f32_16x16x32_bf16 v[176:179], a[152:155], v[160:163], v[176:179]\n"
    "v_pk_fma_f16 v232, v196, v196, s48\n"
    "v_lshlrev_b32_sdwa v200, 1, v169 dst_sel:DWORD dst_unused:UNUSED_PAD src0_sel:DWORD src1_sel:WORD_1\n"
    "v_pk_mul_f16 v232, v196, v232\n"
    "v_exp_f16_e64 v234, v232\n"
    "v_pk_add_f32 v[224:225], v[224:225], v[88:89]\n"
    "v_pk_add_f32 v[226:227], v[226:227], v[90:91]\n"
    "v_pk_fma_f32 v[228:229], v[88:89], v[88:89], v[228:229]\n"
    "v_pk_fma_f32 v[230:231], v[90:91], v[90:91], v[230:231]\n"
    "v_pk_add_f32 v[224:225], v[224:225], v[92:93]\n"
    "v_pk_add_f32 v[226:227], v[226:227], v[94:95]\n"
    "v_pk_fma_f32 v[228:229], v[92:93], v[92:93], v[228:229]\n"
    "v_pk_fma_f32 v[230:231], v[94:95], v[94:95], v[230:231]\n"
    "v_mfma_f32_16x16x32_bf16 v[176:179], a[64:67], v[164:167], v[176:179]\n"
    "s_waitcnt lgkmcnt(6)\n"
    "ds_read_u16_d16_hi v205, v205\n"
    "ds_write_b128 v254, v[224:227]\n"
    "v_mfma_f32_16x16x32_bf16 v[180:183], a[108:111], v[156:159], 0\n"
    "s_waitcnt lgkmcnt(6)\n"
    "v_exp_f16_sdwa v234, v232 dst_sel:WORD_1 dst_unused:UNUSED_PRESERVE src0_sel:WORD_1\n"
    "v_mfma_f32_16x16x32_bf16 v[180:183], a[156:159], v[160:163], v[180:183]\n"
    "v_pk_fma_f16 v234, v234, s49, s49\n"
    "v_or_b32_e64 v185, v185, v201\n"
    "v_mfma_f32_16x16x32_bf16 v[180:183], a[68:71], v[164:167], v[180:183]\n"
    "ds_read_u16 v190, v190\n"
    "ds_write_b128 v254, v[228:231] offset:1024\n"
    "v_mfma_f32_16x16x32_bf16 v[96:99], a[16:19], v[192:195], v[96:99]\n"
    "v_lshrrev_b32_e64 v254, 2, v242\n"
    "v_rcp_f16_e64 v232, v234\n"
    "v_mfma_f32_16x16x32_bf16 v[100:103], a[20:23], v[192:195], v[100:103]\n"
    "s_waitcnt lgkmcnt(5)\n"
    "v_add_u32_e32 v254, s55, v254\n"
    "v_rcp_f16_sdwa v232, v234 dst_sel:WORD_1 dst_unused:UNUSED_PRESERVE src0_sel:WORD_1\n"
    "v_mfma_f32_16x16x32_bf16 v[112:115], a[24:27], v[192:195], v[112:115]\n"
    "ds_read_u16_d16_hi v206, v206\n"
    "ds_read_u16 v191, v191\n"
    "v_mfma_f32_16x16x32_bf16 v[116:119], a[28:31], v[192:195], v[116:119]\n"
    "ds_read_u16_d16_hi v207, v207\n"
    "v_fma_mix_f32 v234, v196, v232, 0 op_sel_hi:[1,1,0]\n"
    "v_mfma_f32_16x16x32_bf16 v[128:131], v[236:239], v[192:195], v[128:131]\n"
    "ds_read_b128 a[240:243], v244 offset:34816\n"
    "v_fma_mix_f32 v232, v196, v232, 0 op_sel:[1,1,0] op_sel_hi:[1,1,0]\n"
    "v_mfma_f32_16x16x32_bf16 v[132:135], a[244:247], v[192:195], v[132:135]\n"
    "s_waitcnt lgkmcnt(6)\n"
    "v_or_b32_e32 v186, v186, v202\n"
    "v_cvt_pk_bf16_f32 v196, v234, v232\n"
    "v_pk_fma_f16 v232, v184, v184, s48\n"
    "v_or_b32_e64 v187, v187, v203\n"
    "v_mfma_f32_16x16x32_bf16 v[104:107], a[16:19], v[196:199], v[104:107]\n"
    "ds_read_u16_d16_hi v200, v200\n"
    "v_pk_mul_f16 v232, v184, v232\n"
    "v_mfma_f32_16x16x32_bf16 v[108:111], a[20:23], v[196:199], v[108:111]\n"
    "v_exp_f16_e64 v234, v232\n"
    "s_waitcnt lgkmcnt(5)\n"
    "s_barrier\n"
    "v_mfma_f32_16x16x32_bf16 v[120:123], a[24:27], v[196:199], v[120:123]\n"
    "ds_read_b32 v224, v254 offset:0\n"
    "ds_read_b32 v225, v254 offset:6752\n"
    "v_mfma_f32_16x16x32_bf16 v[124:127], a[28:31], v[196:199], v[124:127]\n"
    "s_nop 0\n"
    "ds_read_b32 v226, v254 offset:13504\n"
    "ds_read_b32 v227, v254 offset:20256\n"
    "v_mfma_f32_16x16x32_bf16 v[136:139], v[236:239], v[196:199], v[136:139]\n"
    "s_waitcnt lgkmcnt(8)\n"
    "ds_read_b32 v228, v254 offset:1024\n"
    "ds_read_b32 v229, v254 offset:7776\n"
    "v_mfma_f32_16x16x32_bf16 v[140:143], a[244:247], v[196:199], v[140:143]\n"
    "ds_read_b32 v230, v254 offset:14528\n"
    "ds_read_b32 v231, v254 offset:21280\n"
    "s_waitcnt vmcnt(2)\n"
    "v_smfmac_f32_32x32x32_bf16 v[112:127], a[248:251], a[232:239], v241\n"
    "v_exp_f16_sdwa v234, v232 dst_sel:WORD_1 dst_unused:UNUSED_PRESERVE src0_sel:WORD_1\n"
    "v_or_b32_e32 v189, v189, v205\n"
    "v_pk_fma_f16 v234, v234, s49, s49\n"
    "v_lshlrev_b32_sdwa v192, 1, v168 dst_sel:DWORD dst_unused:UNUSED_PAD src0_sel:DWORD src1_sel:WORD_1\n"
    "s_waitcnt vmcnt(1)\n"
    "v_smfmac_f32_32x32x32_bf16 v[96:111], a[248:251], a[224:231], v241\n"
    "s_waitcnt lgkmcnt(9)\n"
    "ds_read_u16 v192, v192\n"
    "ds_read_b128 v[236:239], v244 offset:8192\n"
    "v_lshlrev_b32_sdwa v193, 1, v170 dst_sel:DWORD dst_unused:UNUSED_PAD src0_sel:DWORD src1_sel:WORD_1\n"
    "v_lshlrev_b32_sdwa v201, 1, v171 dst_sel:DWORD dst_unused:UNUSED_PAD src0_sel:DWORD src1_sel:WORD_1\n"
    "v_lshlrev_b32_sdwa v194, 1, v172 dst_sel:DWORD dst_unused:UNUSED_PAD src0_sel:DWORD src1_sel:WORD_1\n"
    "v_mfma_f32_16x16x32_bf16 v[168:171], a[112:115], v[144:147], 0\n"
    "s_waitcnt lgkmcnt(7)\n"
    "ds_read_u16 v193, v193\n"
    "ds_read_u16_d16_hi v201, v201\n"
    "v_mfma_f32_16x16x32_bf16 v[168:171], a[160:163], v[148:151], v[168:171]\n"
    "s_waitcnt lgkmcnt(7)\n"
    "ds_read_u16 v194, v194\n"
    "buffer_load_dwordx4 a[224:227], v245, s[40:43], s58 offen offset:128\n"
    "v_add_f32_e64 v224, v224, v225\n"
    "v_add_f32_e32 v226, v226, v227\n"
    "s_waitcnt lgkmcnt(6)\n"
    "v_add_f32_e32 v228, v228, v229\n"
    "s_waitcnt lgkmcnt(5)\n"
    "v_add_f32_e64 v230, v230, v231\n"
    "v_add_f32_e64 v224, v224, v226\n"
    "v_add_f32_e64 v228, v228, v230\n"
    "v_mul_f32_e32 v224, 0x3c2aaaab, v224\n"
    "v_mul_f32_e32 v228, 0x3c2aaaab, v228\n"
    "v_lshlrev_b32_sdwa v202, 1, v173 dst_sel:DWORD dst_unused:UNUSED_PAD src0_sel:DWORD src1_sel:WORD_1\n"
    "v_fma_f32 v228, -v224, v224, v228\n"
    "v_lshlrev_b32_sdwa v195, 1, v174 dst_sel:DWORD dst_unused:UNUSED_PAD src0_sel:DWORD src1_sel:WORD_1\n"
    "v_max_f32_e64 v228, 0, v228\n"
    "v_lshlrev_b32_sdwa v203, 1, v175 dst_sel:DWORD dst_unused:UNUSED_PAD src0_sel:DWORD src1_sel:WORD_1\n"
    "v_add_f32_e32 v228, 0x3727c5ac, v228\n"
    "v_rcp_f16_e64 v232, v234\n"
    "v_mfma_f32_16x16x32_bf16 v[168:171], a[72:75], v[152:155], v[168:171]\n"
    "ds_read_b128 a[232:235], v243 offset:24640\n"
    "ds_read_u16_d16_hi v202, v202\n"
    "v_rsq_f32_e64 v228, v228\n"
    "v_or_b32_e64 v190, v190, v206\n"
    "v_mul_f32_e64 v228, s50, v228\n"
    "ds_write_b32 v254, v228\n"
    "ds_read_b128 a[236:239], v243 offset:25664\n"
    "v_mul_f32_e64 v224, v228, -v224\n"
    "ds_write_b32 v254, v224 offset:1024\n"
    "ds_read_u16 v195, v195\n"
    "v_add_u32_e64 v254, s56, v242\n"
    "v_rcp_f16_sdwa v232, v234 dst_sel:WORD_1 dst_unused:UNUSED_PRESERVE src0_sel:WORD_1\n"
    "v_mfma_f32_16x16x32_bf16 v[172:175], a[116:119], v[144:147], 0\n"
    "ds_read_b128 a[244:247], v244 offset:10240\n"
    "buffer_load_dwordx4 a[228:231], v245, s[40:43], s89 offen offset:128\n"
    "v_mfma_f32_16x16x32_bf16 v[172:175], a[164:167], v[148:151], v[172:175]\n"
    "ds_read_u16_d16_hi v203, v203\n"
    "v_lshlrev_b32_sdwa v196, 1, v176 dst_sel:DWORD dst_unused:UNUSED_PAD src0_sel:DWORD src1_sel:WORD_1\n"
    "v_lshlrev_b32_sdwa v204, 1, v177 dst_sel:DWORD dst_unused:UNUSED_PAD src0_sel:DWORD src1_sel:WORD_1\n"
    "v_lshlrev_b32_sdwa v197, 1, v178 dst_sel:DWORD dst_unused:UNUSED_PAD src0_sel:DWORD src1_sel:WORD_1\n"
    "v_lshlrev_b32_sdwa v205, 1, v179 dst_sel:DWORD dst_unused:UNUSED_PAD src0_sel:DWORD src1_sel:WORD_1\n"
    "v_lshlrev_b32_sdwa v198, 1, v180 dst_sel:DWORD dst_unused:UNUSED_PAD src0_sel:DWORD src1_sel:WORD_1\n"
    "v_lshlrev_b32_sdwa v206, 1, v181 dst_sel:DWORD dst_unused:UNUSED_PAD src0_sel:DWORD src1_sel:WORD_1\n"
    "v_lshlrev_b32_sdwa v199, 1, v182 dst_sel:DWORD dst_unused:UNUSED_PAD src0_sel:DWORD src1_sel:WORD_1\n"
    "v_fma_mix_f32 v234, v184, v232, 0 op_sel_hi:[1,1,0]\n"
    "v_mfma_f32_16x16x32_bf16 v[172:175], a[240:243], v[152:155], v[172:175]\n"
    "s_waitcnt lgkmcnt(7)\n"
    "ds_read_u16 v196, v196\n"
    "ds_read_u16_d16_hi v204, v204\n"
    "v_or_b32_e32 v191, v191, v207\n"
    "v_lshlrev_b32_sdwa v207, 1, v183 dst_sel:DWORD dst_unused:UNUSED_PAD src0_sel:DWORD src1_sel:WORD_1\n"
    "v_fma_mix_f32 v232, v184, v232, 0 op_sel:[1,1,0] op_sel_hi:[1,1,0]\n"
    "v_mfma_f32_16x16x32_bf16 v[176:179], a[112:115], v[156:159], 0\n"
    "s_waitcnt lgkmcnt(5)\n"
    "ds_read_u16 v197, v197\n"
    "v_cvt_pk_bf16_f32 v184, v234, v232\n"
    "s_barrier\n"
    "v_mfma_f32_16x16x32_bf16 v[176:179], a[160:163], v[160:163], v[176:179]\n"
    "ds_read_b128 v[224:227], v254\n"
    "ds_read_b128 v[228:231], v254 offset:1024\n"
    "v_smfmac_f32_32x32x32_bf16 v[96:111], a[248:251], a[232:239], v241\n"
    "ds_read_u16_d16_hi v205, v205\n"
    "v_lshlrev_b32_e64 v254, 3, v244\n"
    "v_pk_fma_f16 v232, v188, v188, s48\n"
    "v_or_b32_e64 v192, v192, v200\n"
    "v_pk_mul_f16 v232, v188, v232\n"
    "v_mfma_f32_16x16x32_bf16 v[176:179], a[72:75], v[164:167], v[176:179]\n"
    "s_mov_b32 exec_hi, 0\n"
    "buffer_load_dword v233, v254, s[72:75], s53 offen sc1\n"
    "s_mov_b32 exec_hi, -1\n"
    "v_mfma_f32_16x16x32_bf16 v[180:183], a[116:119], v[156:159], 0\n"
    "s_add_u32 s53, s53, 393216\n"
    "ds_read_u16 v198, v198\n"
    "ds_read_u16_d16_hi v206, v206\n"
    "v_mfma_f32_16x16x32_bf16 v[180:183], a[164:167], v[160:163], v[180:183]\n"
    "v_exp_f16_e64 v234, v232\n"
    "v_mfma_f32_16x16x32_bf16 v[180:183], a[240:243], v[164:167], v[180:183]\n"
    "ds_read_u16 v199, v199\n"
    "v_exp_f16_sdwa v234, v232 dst_sel:WORD_1 dst_unused:UNUSED_PRESERVE src0_sel:WORD_1\n"
    "v_mfma_f32_16x16x32_bf16 v[96:99], a[32:35], v[184:187], v[96:99]\n"
    "ds_read_u16_d16_hi v207, v207\n"
    "ds_read_b128 a[232:235], v243 offset:26688\n"
    "v_pk_fma_f16 v234, v234, s49, s49\n"
    "v_mfma_f32_16x16x32_bf16 v[100:103], a[36:39], v[184:187], v[100:103]\n"
    "s_waitcnt lgkmcnt(9)\n"
    "ds_read_b128 a[236:239], v243 offset:27712\n"
    "v_rcp_f16_e32 v232, v234\n"
    "v_mfma_f32_16x16x32_bf16 v[112:115], a[40:43], v[184:187], v[112:115]\n"
    "s_waitcnt lgkmcnt(7)\n"
    "v_rcp_f16_sdwa v232, v234 dst_sel:WORD_1 dst_unused:UNUSED_PRESERVE src0_sel:WORD_1\n"
    "v_or_b32_e32 v193, v193, v201\n"
    "v_fma_mix_f32 v234, v188, v232, 0 op_sel_hi:[1,1,0]\n"
    "v_fma_mix_f32 v232, v188, v232, 0 op_sel:[1,1,0] op_sel_hi:[1,1,0]\n"
    "v_pk_fma_f32 v[64:65], v[64:65], v[224:225], v[228:229]\n"
    "v_pk_fma_f32 v[66:67], v[66:67], v[226:227], v[230:231]\n"
    "v_pk_fma_f32 v[68:69], v[68:69], v[224:225], v[228:229]\n"
    "v_pk_fma_f32 v[70:71], v[70:71], v[226:227], v[230:231]\n"
    "v_pk_fma_f32 v[72:73], v[72:73], v[224:225], v[228:229]\n"
    "v_pk_fma_f32 v[74:75], v[74:75], v[226:227], v[230:231]\n"
    "v_pk_fma_f32 v[76:77], v[76:77], v[224:225], v[228:229]\n"
    "v_pk_fma_f32 v[78:79], v[78:79], v[226:227], v[230:231]\n"
    "v_pk_fma_f32 v[80:81], v[80:81], v[224:225], v[228:229]\n"
    "v_pk_fma_f32 v[82:83], v[82:83], v[226:227], v[230:231]\n"
    "v_pk_fma_f32 v[84:85], v[84:85], v[224:225], v[228:229]\n"
    "v_pk_fma_f32 v[86:87], v[86:87], v[226:227], v[230:231]\n"
    "v_pk_fma_f32 v[88:89], v[88:89], v[224:225], v[228:229]\n"
    "v_pk_fma_f32 v[90:91], v[90:91], v[226:227], v[230:231]\n"
    "v_pk_fma_f32 v[92:93], v[92:93], v[224:225], v[228:229]\n"
    "v_pk_fma_f32 v[94:95], v[94:95], v[226:227], v[230:231]\n"
    "v_pk_fma_f32 v[0:1], v[0:1], v[224:225], v[228:229]\n"
    "v_pk_fma_f32 v[2:3], v[2:3], v[226:227], v[230:231]\n"
    "v_pk_fma_f32 v[4:5], v[4:5], v[224:225], v[228:229]\n"
    "v_pk_fma_f32 v[6:7], v[6:7], v[226:227], v[230:231]\n"
    "v_pk_fma_f32 v[8:9], v[8:9], v[224:225], v[228:229]\n"
    "v_pk_fma_f32 v[10:11], v[10:11], v[226:227], v[230:231]\n"
    "v_pk_fma_f32 v[12:13], v[12:13], v[224:225], v[228:229]\n"
    "v_pk_fma_f32 v[14:15], v[14:15], v[226:227], v[230:231]\n"
    "v_pk_fma_f32 v[16:17], v[16:17], v[224:225], v[228:229]\n"
    "v_pk_fma_f32 v[18:19], v[18:19], v[226:227], v[230:231]\n"
    "v_pk_fma_f32 v[20:21], v[20:21], v[224:225], v[228:229]\n"
    "v_pk_fma_f32 v[22:23], v[22:23], v[226:227], v[230:231]\n"
    "v_pk_fma_f32 v[24:25], v[24:25], v[224:225], v[228:229]\n"
    "v_pk_fma_f32 v[26:27], v[26:27], v[226:227], v[230:231]\n"
    "v_pk_fma_f32 v[28:29], v[28:29], v[224:225], v[228:229]\n"
    "v_pk_fma_f32 v[30:31], v[30:31], v[226:227], v[230:231]\n"
    "v_pk_fma_f32 v[32:33], v[32:33], v[224:225], v[228:229]\n"
    "v_pk_fma_f32 v[34:35], v[34:35], v[226:227], v[230:231]\n"
    "v_pk_fma_f32 v[36:37], v[36:37], v[224:225], v[228:229]\n"
    "v_pk_fma_f32 v[38:39], v[38:39], v[226:227], v[230:231]\n"
    "v_pk_fma_f32 v[40:41], v[40:41], v[224:225], v[228:229]\n"
    "v_pk_fma_f32 v[42:43], v[42:43], v[226:227], v[230:231]\n"
    "v_pk_fma_f32 v[44:45], v[44:45], v[224:225], v[228:229]\n"
    "v_pk_fma_f32 v[46:47], v[46:47], v[226:227], v[230:231]\n"
    "v_pk_fma_f32 v[48:49], v[48:49], v[224:225], v[228:229]\n"
    "v_pk_fma_f32 v[50:51], v[50:51], v[226:227], v[230:231]\n"
    "v_pk_fma_f32 v[52:53], v[52:53], v[224:225], v[228:229]\n"
    "v_pk_fma_f32 v[54:55], v[54:55], v[226:227], v[230:231]\n"
    "v_pk_fma_f32 v[56:57], v[56:57], v[224:225], v[228:229]\n"
    "v_pk_fma_f32 v[58:59], v[58:59], v[226:227], v[230:231]\n"
    "v_pk_fma_f32 v[60:61], v[60:61], v[224:225], v[228:229]\n"
    "v_pk_fma_f32 v[62:63], v[62:63], v[226:227], v[230:231]\n"
    "v_mfma_f32_16x16x32_bf16 v[116:119], a[44:47], v[184:187], v[116:119]\n"
    "ds_read_b128 a[240:243], v244 offset:38912\n"
    "v_cvt_pk_bf16_f32 v208, v64, v68\n"
    "v_mfma_f32_16x16x32_bf16 v[128:131], v[236:239], v[184:187], v[128:131]\n"
    "v_cvt_pk_bf16_f32 v212, v65, v69\n"
    "v_cvt_pk_bf16_f32 v216, v66, v70\n"
    "v_mfma_f32_16x16x32_bf16 v[132:135], a[244:247], v[184:187], v[132:135]\n"
    "v_cvt_pk_bf16_f32 v220, v67, v71\n"
    "v_cvt_pk_bf16_f32 v209, v72, v76\n"
    "v_cvt_pk_bf16_f32 v213, v73, v77\n"
    "v_cvt_pk_bf16_f32 v217, v74, v78\n"
    "v_cvt_pk_bf16_f32 v221, v75, v79\n"
    "v_cvt_pk_bf16_f32 v210, v80, v84\n"
    "v_cvt_pk_bf16_f32 v214, v81, v85\n"
    "v_cvt_pk_bf16_f32 v218, v82, v86\n"
    "v_cvt_pk_bf16_f32 v222, v83, v87\n"
    "v_cvt_pk_bf16_f32 v211, v88, v92\n"
    "v_cvt_pk_bf16_f32 v215, v89, v93\n"
    "v_cvt_pk_bf16_f32 v219, v90, v94\n"
    "v_cvt_pk_bf16_f32 v223, v91, v95\n"
    "v_cvt_pk_bf16_f32 v80, v0, v4\n"
    "v_cvt_pk_bf16_f32 v84, v1, v5\n"
    "v_cvt_pk_bf16_f32 v188, v234, v232\n"
    "ds_write_b128 v253, v[208:211] offset:2048\n"
    "v_cvt_pk_bf16_f32 v88, v2, v6\n"
    "v_mfma_f32_16x16x32_bf16 v[104:107], a[32:35], v[188:191], v[104:107]\n"
    "s_waitcnt lgkmcnt(1)\n"
    "v_cvt_pk_bf16_f32 v92, v3, v7\n"
    "v_cvt_pk_bf16_f32 v81, v8, v12\n"
    "v_mfma_f32_16x16x32_bf16 v[108:111], a[36:39], v[188:191], v[108:111]\n"
    "s_nop 0\n"
    "ds_write_b128 v253, v[212:215] offset:5120\n"
    "v_mfma_f32_16x16x32_bf16 v[120:123], a[40:43], v[188:191], v[120:123]\n"
    "v_cvt_pk_bf16_f32 v85, v9, v13\n"
    "v_cvt_pk_bf16_f32 v89, v10, v14\n"
    "v_mfma_f32_16x16x32_bf16 v[124:127], a[44:47], v[188:191], v[124:127]\n"
    "v_cvt_pk_bf16_f32 v93, v11, v15\n"
    "v_cvt_pk_bf16_f32 v82, v16, v20\n"
    "v_mfma_f32_16x16x32_bf16 v[136:139], v[236:239], v[188:191], v[136:139]\n"
    "ds_read_b128 v[236:239], v244 offset:12288\n"
    "v_cvt_pk_bf16_f32 v86, v17, v21\n"
    "v_mfma_f32_16x16x32_bf16 v[140:143], a[244:247], v[188:191], v[140:143]\n"
    "v_cvt_pk_bf16_f32 v90, v18, v22\n"
    "v_cvt_pk_bf16_f32 v94, v19, v23\n"
    "v_smfmac_f32_32x32x32_bf16 v[112:127], a[248:251], a[232:239], v241\n"
    "v_cvt_pk_bf16_f32 v83, v24, v28\n"
    "v_cvt_pk_f16_f32 v184, v168, v169\n"
    "v_cvt_pk_bf16_f32 v87, v25, v29\n"
    "v_lshlrev_b32_sdwa v185, 1, v170 dst_sel:DWORD dst_unused:UNUSED_PAD src0_sel:DWORD src1_sel:WORD_1\n"
    "v_lshlrev_b32_sdwa v201, 1, v171 dst_sel:DWORD dst_unused:UNUSED_PAD src0_sel:DWORD src1_sel:WORD_1\n"
    "v_mfma_f32_16x16x32_bf16 v[168:171], a[120:123], v[144:147], 0\n"
    "ds_read_u16 v185, v185\n"
    "ds_write_b128 v253, v[80:83] offset:0\n"
    "v_lshlrev_b32_sdwa v186, 1, v172 dst_sel:DWORD dst_unused:UNUSED_PAD src0_sel:DWORD src1_sel:WORD_1\n"
    "v_cvt_pk_bf16_f32 v91, v26, v30\n"
    "v_or_b32_e64 v194, v194, v202\n"
    "v_mfma_f32_16x16x32_bf16 v[168:171], a[168:171], v[148:151], v[168:171]\n"
    "v_lshlrev_b32_sdwa v202, 1, v173 dst_sel:DWORD dst_unused:UNUSED_PAD src0_sel:DWORD src1_sel:WORD_1\n"
    "v_lshlrev_b32_sdwa v187, 1, v174 dst_sel:DWORD dst_unused:UNUSED_PAD src0_sel:DWORD src1_sel:WORD_1\n"
    "v_mfma_f32_16x16x32_bf16 v[168:171], a[76:79], v[152:155], v[168:171]\n"
    "ds_read_u16_d16_hi v201, v201\n"
    "ds_write_b128 v253, v[84:87] offset:3072\n"
    "v_cvt_pk_bf16_f32 v95, v27, v31\n"
    "v_cvt_pk_f16_f32 v188, v176, v177\n"
    "v_lshlrev_b32_sdwa v189, 1, v178 dst_sel:DWORD dst_unused:UNUSED_PAD src0_sel:DWORD src1_sel:WORD_1\n"
    "v_lshlrev_b32_sdwa v190, 1, v180 dst_sel:DWORD dst_unused:UNUSED_PAD src0_sel:DWORD src1_sel:WORD_1\n"
    "v_lshlrev_b32_sdwa v191, 1, v182 dst_sel:DWORD dst_unused:UNUSED_PAD src0_sel:DWORD src1_sel:WORD_1\n"
    "v_or_b32_e64 v195, v195, v203\n"
    "v_lshlrev_b32_sdwa v203, 1, v175 dst_sel:DWORD dst_unused:UNUSED_PAD src0_sel:DWORD src1_sel:WORD_1\n"
    "v_mfma_f32_16x16x32_bf16 v[172:175], a[124:127], v[144:147], 0\n"
    "ds_read_u16 v186, v186\n"
    "ds_read_u16_d16_hi v202, v202\n"
    "v_mfma_f32_16x16x32_bf16 v[172:175], a[172:175], v[148:151], v[172:175]\n"
    "ds_read_u16 v187, v187\n"
    "ds_read_u16_d16_hi v203, v203\n"
    "v_mfma_f32_16x16x32_bf16 v[172:175], a[240:243], v[152:155], v[172:175]\n"
    "v_cvt_pk_bf16_f32 v64, v32, v36\n"
    "v_cvt_pk_bf16_f32 v68, v33, v37\n"
    "v_cvt_pk_bf16_f32 v72, v34, v38\n"
    "v_cvt_pk_bf16_f32 v76, v35, v39\n"
    "v_cvt_pk_bf16_f32 v65, v40, v44\n"
    "v_or_b32_e64 v196, v196, v204\n"
    "v_cvt_pk_bf16_f32 v69, v41, v45\n"
    "v_cvt_pk_bf16_f32 v73, v42, v46\n"
    "v_cvt_pk_bf16_f32 v77, v43, v47\n"
    "v_cvt_pk_bf16_f32 v66, v48, v52\n"
    "v_cvt_pk_bf16_f32 v70, v49, v53\n"
    "v_or_b32_e64 v197, v197, v205\n"
    "v_lshlrev_b32_sdwa v205, 1, v179 dst_sel:DWORD dst_unused:UNUSED_PAD src0_sel:DWORD src1_sel:WORD_1\n"
    "v_mfma_f32_16x16x32_bf16 v[176:179], a[120:123], v[156:159], 0\n"
    "s_nop 0\n"
    "ds_read_b128 a[244:247], v244 offset:14336\n"
    "ds_read_u16 v189, v189\n"
    "v_mfma_f32_16x16x32_bf16 v[176:179], a[168:171], v[160:163], v[176:179]\n"
    "s_waitcnt lgkmcnt(6)\n"
    "ds_read_b128 a[232:235], v243 offset:28736\n"
    "ds_read_u16_d16_hi v205, v205\n"
    "v_mfma_f32_16x16x32_bf16 v[176:179], a[76:79], v[164:167], v[176:179]\n"
    "v_cvt_pk_bf16_f32 v74, v50, v54\n"
    "v_cvt_pk_bf16_f32 v78, v51, v55\n"
    "v_cvt_pk_bf16_f32 v67, v56, v60\n"
    "v_cvt_pk_bf16_f32 v71, v57, v61\n"
    "v_cvt_pk_bf16_f32 v75, v58, v62\n"
    "v_or_b32_e32 v198, v198, v206\n"
    "v_mfma_f32_16x16x32_bf16 v[96:99], a[176:179], v[192:195], v[96:99]\n"
    "ds_read_b128 a[236:239], v243 offset:29760\n"
    "ds_write_b128 v253, v[64:67] offset:1024\n"
    "v_mfma_f32_16x16x32_bf16 v[100:103], a[180:183], v[192:195], v[100:103]\n"
    "s_waitcnt lgkmcnt(8)\n"
    "v_lshlrev_b32_sdwa v206, 1, v181 dst_sel:DWORD dst_unused:UNUSED_PAD src0_sel:DWORD src1_sel:WORD_1\n"
    "v_cvt_pk_bf16_f32 v79, v59, v63\n"
    "v_mfma_f32_16x16x32_bf16 v[112:115], a[184:187], v[192:195], v[112:115]\n"
    "v_or_b32_e64 v199, v199, v207\n"
    "v_lshlrev_b32_sdwa v207, 1, v183 dst_sel:DWORD dst_unused:UNUSED_PAD src0_sel:DWORD src1_sel:WORD_1\n"
    "v_mfma_f32_16x16x32_bf16 v[180:183], a[124:127], v[156:159], 0\n"
    "ds_read_u16 v190, v190\n"
    "ds_write_b128 v253, v[68:71] offset:4096\n"
    "v_mfma_f32_16x16x32_bf16 v[180:183], a[172:175], v[160:163], v[180:183]\n"
    "s_waitcnt lgkmcnt(7)\n"
    "v_or_b32_e32 v185, v185, v201\n"
    "v_or_b32_e64 v186, v186, v202\n"
    "v_mfma_f32_16x16x32_bf16 v[180:183], a[240:243], v[164:167], v[180:183]\n"
    "v_pk_fma_f16 v232, v184, v184, s48\n"
    "v_or_b32_e64 v187, v187, v203\n"
    "v_mfma_f32_16x16x32_bf16 v[116:119], a[188:191], v[192:195], v[116:119]\n"
    "ds_read_u16_d16_hi v206, v206\n"
    "ds_read_u16 v191, v191\n"
    "v_mfma_f32_16x16x32_bf16 v[128:131], v[236:239], v[192:195], v[128:131]\n"
    "ds_read_u16_d16_hi v207, v207\n"
    "v_pk_mul_f16 v232, v184, v232\n"
    "v_mfma_f32_16x16x32_bf16 v[132:135], a[244:247], v[192:195], v[132:135]\n"
    "v_exp_f16_e32 v234, v232\n"
    "v_mfma_f32_16x16x32_bf16 v[104:107], a[176:179], v[196:199], v[104:107]\n"
    "s_waitcnt lgkmcnt(6)\n"
    "ds_read_b128 a[240:243], v244 offset:32768\n"
    "v_exp_f16_sdwa v234, v232 dst_sel:WORD_1 dst_unused:UNUSED_PRESERVE src0_sel:WORD_1\n"
    "v_mfma_f32_16x16x32_bf16 v[108:111], a[180:183], v[196:199], v[108:111]\n"
    "v_or_b32_e64 v189, v189, v205\n"
    "v_pk_fma_f16 v234, v234, s49, s49\n"
    "v_mfma_f32_16x16x32_bf16 v[120:123], a[184:187], v[196:199], v[120:123]\n"
    "v_cvt_pk_f16_f32 v192, v168, v169\n"
    "v_lshlrev_b32_sdwa v193, 1, v170 dst_sel:DWORD dst_unused:UNUSED_PAD src0_sel:DWORD src1_sel:WORD_1\n"
    "v_mfma_f32_16x16x32_bf16 v[124:127], a[188:191], v[196:199], v[124:127]\n"
    "ds_read_u16 v193, v193\n"
    "v_lshlrev_b32_sdwa v201, 1, v171 dst_sel:DWORD dst_unused:UNUSED_PAD src0_sel:DWORD src1_sel:WORD_1\n"
    "v_mfma_f32_16x16x32_bf16 v[136:139], v[236:239], v[196:199], v[136:139]\n"
    "s_nop 0\n"
    "ds_read_u16_d16_hi v201, v201\n"
    "ds_read_b128 v[236:239], v244 offset:16384\n"
    "v_mfma_f32_16x16x32_bf16 v[140:143], a[244:247], v[196:199], v[140:143]\n"
    "s_waitcnt lgkmcnt(7)\n"
    "v_lshlrev_b32_sdwa v194, 1, v172 dst_sel:DWORD dst_unused:UNUSED_PAD src0_sel:DWORD src1_sel:WORD_1\n"
    "v_lshlrev_b32_sdwa v202, 1, v173 dst_sel:DWORD dst_unused:UNUSED_PAD src0_sel:DWORD src1_sel:WORD_1\n"
    "v_lshlrev_b32_sdwa v195, 1, v174 dst_sel:DWORD dst_unused:UNUSED_PAD src0_sel:DWORD src1_sel:WORD_1\n"
    "v_lshlrev_b32_sdwa v203, 1, v175 dst_sel:DWORD dst_unused:UNUSED_PAD src0_sel:DWORD src1_sel:WORD_1\n"
    "v_rcp_f16_e64 v232, v234\n"
    "s_barrier\n"
    "v_smfmac_f32_32x32x32_bf16 v[128:143], a[248:251], a[232:239], v241\n"
    "s_waitcnt lgkmcnt(6)\n"
    "ds_read_b128 v[144:147], v242 offset:0\n"
    "ds_read_b128 v[148:151], v242 offset:1024\n"
    "v_or_b32_e64 v190, v190, v206\n"
    "v_mov_b32_e64 v64, v216\n"
    "v_rcp_f16_sdwa v232, v234 dst_sel:WORD_1 dst_unused:UNUSED_PRESERVE src0_sel:WORD_1\n"
    "v_cvt_pk_f16_f32 v196, v176, v177\n"
    "v_mov_b32_e64 v65, v217\n"
    "v_lshlrev_b32_sdwa v197, 1, v178 dst_sel:DWORD dst_unused:UNUSED_PAD src0_sel:DWORD src1_sel:WORD_1\n"
    "v_mov_b32_e64 v66, v218\n"
    "v_lshlrev_b32_sdwa v205, 1, v179 dst_sel:DWORD dst_unused:UNUSED_PAD src0_sel:DWORD src1_sel:WORD_1\n"
    "v_mov_b32_e64 v67, v219\n"
    "v_lshlrev_b32_sdwa v198, 1, v180 dst_sel:DWORD dst_unused:UNUSED_PAD src0_sel:DWORD src1_sel:WORD_1\n"
    "v_mov_b32_e64 v68, v220\n"
    "v_lshlrev_b32_sdwa v206, 1, v181 dst_sel:DWORD dst_unused:UNUSED_PAD src0_sel:DWORD src1_sel:WORD_1\n"
    "v_mov_b32_e64 v69, v221\n"
    "v_lshlrev_b32_sdwa v199, 1, v182 dst_sel:DWORD dst_unused:UNUSED_PAD src0_sel:DWORD src1_sel:WORD_1\n"
    "v_mov_b32_e64 v70, v222\n"
    "v_mov_b32_e64 v71, v223\n"
    "v_fma_mix_f32 v234, v184, v232, 0 op_sel_hi:[1,1,0]\n"
    "s_waitcnt lgkmcnt(5)\n"
    "v_or_b32_e32 v191, v191, v207\n"
    "v_lshlrev_b32_sdwa v207, 1, v183 dst_sel:DWORD dst_unused:UNUSED_PAD src0_sel:DWORD src1_sel:WORD_1\n"
    "v_fma_mix_f32 v232, v184, v232, 0 op_sel:[1,1,0] op_sel_hi:[1,1,0]\n"
    "v_cvt_pk_bf16_f32 v184, v234, v232\n"
    "ds_read_b128 v[152:155], v242 offset:2048\n"
    "ds_read_b128 v[156:159], v242 offset:3072\n"
    "v_pk_fma_f16 v232, v188, v188, s48\n"
    "s_waitcnt lgkmcnt(4)\n"
    "v_or_b32_e32 v193, v193, v201\n"
    "s_waitcnt lgkmcnt(3)\n"
    "v_mfma_f32_16x16x32_bf16 v[168:171], a[80:83], v[144:147], 0\n"
    "s_nop 0\n"
    "ds_read_b128 v[160:163], v242 offset:4096\n"
    "ds_read_b128 v[164:167], v242 offset:5120\n"
    "s_waitcnt lgkmcnt(4)\n"
    "v_mfma_f32_16x16x32_bf16 v[168:171], a[128:131], v[148:151], v[168:171]\n"
    "s_nop 0\n"
    "ds_read_u16 v194, v194\n"
    "ds_read_u16_d16_hi v202, v202\n"
    "v_mfma_f32_16x16x32_bf16 v[172:175], a[84:87], v[144:147], 0\n"
    "ds_read_u16 v195, v195\n"
    "v_pk_mul_f16 v232, v188, v232\n"
    "v_mfma_f32_16x16x32_bf16 v[172:175], a[132:135], v[148:151], v[172:175]\n"
    "ds_read_u16_d16_hi v203, v203\n"
    "v_exp_f16_e64 v234, v232\n"
    "ds_read_b128 a[244:247], v244 offset:18432\n"
    "v_exp_f16_sdwa v234, v232 dst_sel:WORD_1 dst_unused:UNUSED_PRESERVE src0_sel:WORD_1\n"
    "v_mfma_f32_16x16x32_bf16 v[96:99], a[192:195], v[184:187], v[96:99]\n"
    "s_waitcnt lgkmcnt(8)\n"
    "v_pk_fma_f16 v234, v234, s49, s49\n"
    "v_rcp_f16_e64 v232, v234\n"
    "v_mfma_f32_16x16x32_bf16 v[168:171], a[48:51], v[152:155], v[168:171]\n"
    "s_waitcnt lgkmcnt(7)\n"
    "ds_read_u16 v197, v197\n"
    "v_rcp_f16_sdwa v232, v234 dst_sel:WORD_1 dst_unused:UNUSED_PRESERVE src0_sel:WORD_1\n"
    "v_mfma_f32_16x16x32_bf16 v[172:175], a[240:243], v[152:155], v[172:175]\n"
    "s_nop 0\n"
    "ds_read_u16_d16_hi v205, v205\n"
    "v_fma_mix_f32 v234, v188, v232, 0 op_sel_hi:[1,1,0]\n"
    "v_mfma_f32_16x16x32_bf16 v[176:179], a[80:83], v[156:159], 0\n"
    "s_waitcnt lgkmcnt(7)\n"
    "v_fma_mix_f32 v232, v188, v232, 0 op_sel:[1,1,0] op_sel_hi:[1,1,0]\n"
    "v_cvt_pk_bf16_f32 v188, v234, v232\n"
    "v_mfma_f32_16x16x32_bf16 v[176:179], a[128:131], v[160:163], v[176:179]\n"
    "ds_read_u16 v198, v198\n"
    "v_pk_fma_f16 v232, v192, v192, s48\n"
    "v_mfma_f32_16x16x32_bf16 v[176:179], a[48:51], v[164:167], v[176:179]\n"
    "ds_read_u16_d16_hi v206, v206\n"
    "v_pk_mul_f16 v232, v192, v232\n"
    "v_mfma_f32_16x16x32_bf16 v[180:183], a[84:87], v[156:159], 0\n"
    "v_exp_f16_e64 v234, v232\n"
    "v_mfma_f32_16x16x32_bf16 v[180:183], a[132:135], v[160:163], v[180:183]\n"
    "ds_read_u16 v199, v199\n"
    "v_exp_f16_sdwa v234, v232 dst_sel:WORD_1 dst_unused:UNUSED_PRESERVE src0_sel:WORD_1\n"
    "v_mfma_f32_16x16x32_bf16 v[180:183], a[240:243], v[164:167], v[180:183]\n"
    "s_waitcnt lgkmcnt(8)\n"
    "v_or_b32_e32 v194, v194, v202\n"
    "v_pk_fma_f16 v234, v234, s49, s49\n"
    "v_mfma_f32_16x16x32_bf16 v[100:103], a[196:199], v[184:187], v[100:103]\n"
    "ds_read_u16_d16_hi v207, v207\n"
    "v_rcp_f16_e64 v232, v234\n"
    "v_mfma_f32_16x16x32_bf16 v[112:115], a[200:203], v[184:187], v[112:115]\n"
    "v_rcp_f16_sdwa v232, v234 dst_sel:WORD_1 dst_unused:UNUSED_PRESERVE src0_sel:WORD_1\n"
    "v_mfma_f32_16x16x32_bf16 v[116:119], a[204:207], v[184:187], v[116:119]\n"
    "s_waitcnt lgkmcnt(6)\n"
    "v_or_b32_e32 v195, v195, v203\n"
    "v_fma_mix_f32 v234, v192, v232, 0 op_sel_hi:[1,1,0]\n"
    "v_mfma_f32_16x16x32_bf16 v[128:131], v[236:239], v[184:187], v[128:131]\n"
    "v_fma_mix_f32 v232, v192, v232, 0 op_sel:[1,1,0] op_sel_hi:[1,1,0]\n"
    "v_cvt_pk_bf16_f32 v192, v234, v232\n"
    "v_mfma_f32_16x16x32_bf16 v[132:135], a[244:247], v[184:187], v[132:135]\n"
    "v_pk_fma_f16 v232, v196, v196, s48\n"
    "v_pk_mul_f16 v232, v196, v232\n"
    "v_mfma_f32_16x16x32_bf16 v[104:107], a[192:195], v[188:191], v[104:107]\n"
    "v_exp_f16_e32 v234, v232\n"
    "v_mfma_f32_16x16x32_bf16 v[108:111], a[196:199], v[188:191], v[108:111]\n"
    "s_waitcnt lgkmcnt(4)\n"
    "v_or_b32_e32 v197, v197, v205\n"
    "v_exp_f16_sdwa v234, v232 dst_sel:WORD_1 dst_unused:UNUSED_PRESERVE src0_sel:WORD_1\n"
    "v_mfma_f32_16x16x32_bf16 v[120:123], a[200:203], v[188:191], v[120:123]\n"
    "v_mfma_f32_16x16x32_bf16 v[124:127], a[204:207], v[188:191], v[124:127]\n"
    "v_mfma_f32_16x16x32_bf16 v[136:139], v[236:239], v[188:191], v[136:139]\n"
    "s_waitcnt lgkmcnt(2)\n"
    "v_or_b32_e32 v198, v198, v206\n"
    "s_waitcnt lgkmcnt(0)\n"
    "v_or_b32_e32 v199, v199, v207\n"
    "s_mov_b32 s59, s58\n"
    "s_add_u32 s57, s57, 1572864\n"
    "s_add_u32 s58, s58, 1572864\n"
    "s_mov_b32 s70, s46\n"
    "s_add_u32 s88, s57, 98304\n"
    "s_add_u32 s89, s58, 98304\n"
    "s_add_u32 s39, s59, 98304\n"
    "s_add_u32 s64, s64, 1\n"
    "s_cmp_lt_u32 s64, 2\n"
    "s_cselect_b64 s[72:73], s[72:73], s[76:77]\n"
    "s_cselect_b32 s74, s74, s78\n"
    "s_cselect_b32 s53, s53, s79\n"
    "s_cmp_lg_u32 s64, 3\n"
    "s_cbranch_scc1 1b\n"
    ".Lreplk_last:\n"
    "v_lshlrev_b32_sdwa v184, 1, v168 dst_sel:DWORD dst_unused:UNUSED_PAD src0_sel:DWORD src1_sel:WORD_1\n"
    "v_lshlrev_b32_sdwa v200, 1, v169 dst_sel:DWORD dst_unused:UNUSED_PAD src0_sel:DWORD src1_sel:WORD_1\n"
    "v_lshlrev_b32_sdwa v185, 1, v170 dst_sel:DWORD dst_unused:UNUSED_PAD src0_sel:DWORD src1_sel:WORD_1\n"
    "v_lshlrev_b32_sdwa v201, 1, v171 dst_sel:DWORD dst_unused:UNUSED_PAD src0_sel:DWORD src1_sel:WORD_1\n"
    "v_mfma_f32_16x16x32_bf16 v[140:143], a[244:247], v[188:191], v[140:143]\n"
    "ds_read_b128 v[56:59], v244 offset:36864\n"
    "ds_read_u16 v184, v184\n"
    "v_mfma_f32_16x16x32_bf16 v[168:171], a[88:91], v[144:147], 0\n"
    "ds_read_u16_d16_hi v200, v200\n"
    "ds_read_u16 v185, v185\n"
    "v_mfma_f32_16x16x32_bf16 v[168:171], a[136:139], v[148:151], v[168:171]\n"
    "v_lshlrev_b32_sdwa v186, 1, v172 dst_sel:DWORD dst_unused:UNUSED_PAD src0_sel:DWORD src1_sel:WORD_1\n"
    "v_lshlrev_b32_sdwa v202, 1, v173 dst_sel:DWORD dst_unused:UNUSED_PAD src0_sel:DWORD src1_sel:WORD_1\n"
    "v_mfma_f32_16x16x32_bf16 v[168:171], a[52:55], v[152:155], v[168:171]\n"
    "ds_read_u16_d16_hi v201, v201\n"
    "v_lshlrev_b32_sdwa v187, 1, v174 dst_sel:DWORD dst_unused:UNUSED_PAD src0_sel:DWORD src1_sel:WORD_1\n"
    "s_waitcnt vmcnt(1)\n"
    "v_smfmac_f32_32x32x32_bf16 v[128:143], a[248:251], a[224:231], v241\n"
    "s_nop 0\n"
    "ds_read_u16 v186, v186\n"
    "v_lshlrev_b32_sdwa v203, 1, v175 dst_sel:DWORD dst_unused:UNUSED_PAD src0_sel:DWORD src1_sel:WORD_1\n"
    "v_lshlrev_b32_sdwa v188, 1, v176 dst_sel:DWORD dst_unused:UNUSED_PAD src0_sel:DWORD src1_sel:WORD_1\n"
    "v_lshlrev_b32_sdwa v204, 1, v177 dst_sel:DWORD dst_unused:UNUSED_PAD src0_sel:DWORD src1_sel:WORD_1\n"
    "v_lshlrev_b32_sdwa v189, 1, v178 dst_sel:DWORD dst_unused:UNUSED_PAD src0_sel:DWORD src1_sel:WORD_1\n"
    "v_mfma_f32_16x16x32_bf16 v[172:175], a[92:95], v[144:147], 0\n"
    "ds_read_b128 v[236:239], v244 offset:20480\n"
    "v_lshlrev_b32_sdwa v205, 1, v179 dst_sel:DWORD dst_unused:UNUSED_PAD src0_sel:DWORD src1_sel:WORD_1\n"
    "v_mfma_f32_16x16x32_bf16 v[172:175], a[140:143], v[148:151], v[172:175]\n"
    "s_nop 0\n"
    "ds_read_u16_d16_hi v202, v202\n"
    "v_lshlrev_b32_sdwa v190, 1, v180 dst_sel:DWORD dst_unused:UNUSED_PAD src0_sel:DWORD src1_sel:WORD_1\n"
    "v_mfma_f32_16x16x32_bf16 v[176:179], a[88:91], v[156:159], 0\n"
    "s_waitcnt lgkmcnt(7)\n"
    "v_lshlrev_b32_sdwa v206, 1, v181 dst_sel:DWORD dst_unused:UNUSED_PAD src0_sel:DWORD src1_sel:WORD_1\n"
    "v_lshlrev_b32_sdwa v191, 1, v182 dst_sel:DWORD dst_unused:UNUSED_PAD src0_sel:DWORD src1_sel:WORD_1\n"
    "v_mfma_f32_16x16x32_bf16 v[172:175], v[56:59], v[152:155], v[172:175]\n"
    "ds_read_u16 v187, v187\n"
    "buffer_load_dwordx4 a[232:235], v245, s[40:43], s57 offen offset:64\n"
    "buffer_load_dwordx4 a[224:227], v245, s[40:43], s57 offen offset:0\n"
    "buffer_load_dwordx4 v[208:211], v245, s[40:43], s57 offen offset:128\n"
    "v_mfma_f32_16x16x32_bf16 v[176:179], a[136:139], v[160:163], v[176:179]\n"
    "ds_read_b128 a[244:247], v244 offset:22528\n"
    "v_lshlrev_b32_sdwa v207, 1, v183 dst_sel:DWORD dst_unused:UNUSED_PAD src0_sel:DWORD src1_sel:WORD_1\n"
    "v_mfma_f32_16x16x32_bf16 v[176:179], a[52:55], v[164:167], v[176:179]\n"
    "ds_read_u16_d16_hi v203, v203\n"
    "v_pk_fma_f16 v234, v234, s49, s49\n"
    "v_mfma_f32_16x16x32_bf16 v[180:183], a[92:95], v[156:159], 0\n"
    "s_waitcnt lgkmcnt(8)\n"
    "v_rcp_f16_e32 v232, v234\n"
    "v_mfma_f32_16x16x32_bf16 v[180:183], a[140:143], v[160:163], v[180:183]\n"
    "ds_read_u16 v188, v188\n"
    "v_rcp_f16_sdwa v232, v234 dst_sel:WORD_1 dst_unused:UNUSED_PRESERVE src0_sel:WORD_1\n"
    "v_mfma_f32_16x16x32_bf16 v[180:183], v[56:59], v[164:167], v[180:183]\n"
    "ds_read_u16_d16_hi v204, v204\n"
    "v_fma_mix_f32 v234, v196, v232, 0 op_sel_hi:[1,1,0]\n"
    "v_mfma_f32_16x16x32_bf16 v[96:99], a[208:211], v[192:195], v[96:99]\n"
    "v_fma_mix_f32 v232, v196, v232, 0 op_sel:[1,1,0] op_sel_hi:[1,1,0]\n"
    "v_or_b32_e32 v184, v184, v200\n"
    "v_mfma_f32_16x16x32_bf16 v[100:103], a[212:215], v[192:195], v[100:103]\n"
    "s_waitcnt lgkmcnt(8)\n"
    "ds_read_u16 v189, v189\n"
    "v_cvt_pk_bf16_f32 v196, v234, v232\n"
    "v_mfma_f32_16x16x32_bf16 v[112:115], a[216:219], v[192:195], v[112:115]\n"
    "s_waitcnt lgkmcnt(7)\n"
    "v_or_b32_e32 v185, v185, v201\n"
    "v_lshlrev_b32_sdwa v201, 1, v171 dst_sel:DWORD dst_unused:UNUSED_PAD src0_sel:DWORD src1_sel:WORD_1\n"
    "v_mfma_f32_16x16x32_bf16 v[116:119], a[220:223], v[192:195], v[116:119]\n"
    "ds_read_u16_d16_hi v205, v205\n"
    "v_cvt_pk_bf16_f32 v96, v96, v97\n"
    "v_mfma_f32_16x16x32_bf16 v[128:131], v[236:239], v[192:195], v[128:131]\n"
    "ds_read_u16 v190, v190\n"
    "v_cvt_pk_bf16_f32 v97, v98, v99\n"
    "v_mfma_f32_16x16x32_bf16 v[104:107], a[208:211], v[196:199], v[104:107]\n"
    "s_waitcnt lgkmcnt(8)\n"
    "v_or_b32_e32 v186, v186, v202\n"
    "v_lshlrev_b32_sdwa v202, 1, v173 dst_sel:DWORD dst_unused:UNUSED_PAD src0_sel:DWORD src1_sel:WORD_1\n"
    "v_mfma_f32_16x16x32_bf16 v[108:111], a[212:215], v[196:199], v[108:111]\n"
    "ds_read_u16_d16_hi v206, v206\n"
    "v_cvt_pk_bf16_f32 v98, v100, v101\n"
    "s_waitcnt lgkmcnt(7)\n"
    "v_mfma_f32_16x16x32_bf16 v[132:135], a[244:247], v[192:195], v[132:135]\n"
    "s_nop 0\n"
    "v_cvt_pk_f16_f32 v192, v168, v169\n"
    "v_lshlrev_b32_sdwa v193, 1, v170 dst_sel:DWORD dst_unused:UNUSED_PAD src0_sel:DWORD src1_sel:WORD_1\n"
    "v_mfma_f32_16x16x32_bf16 v[120:123], a[216:219], v[196:199], v[120:123]\n"
    "s_nop 0\n"
    "ds_read_u16 v191, v191\n"
    "v_lshlrev_b32_sdwa v194, 1, v172 dst_sel:DWORD dst_unused:UNUSED_PAD src0_sel:DWORD src1_sel:WORD_1\n"
    "v_mfma_f32_16x16x32_bf16 v[124:127], a[220:223], v[196:199], v[124:127]\n"
    "s_waitcnt lgkmcnt(7)\n"
    "ds_read_u16_d16_hi v207, v207\n"
    "buffer_load_dwordx4 a[236:239], v245, s[40:43], s88 offen offset:64\n"
    "buffer_load_dwordx4 a[228:231], v245, s[40:43], s88 offen offset:0\n"
    "buffer_load_dwordx4 v[212:215], v245, s[40:43], s88 offen offset:128\n"
    "v_mfma_f32_16x16x32_bf16 v[136:139], v[236:239], v[196:199], v[136:139]\n"
    "v_lshlrev_b32_sdwa v195, 1, v174 dst_sel:DWORD dst_unused:UNUSED_PAD src0_sel:DWORD src1_sel:WORD_1\n"
    "v_or_b32_e32 v187, v187, v203\n"
    "v_mfma_f32_16x16x32_bf16 v[140:143], a[244:247], v[196:199], v[140:143]\n"
    "ds_read_u16 v193, v193\n"
    "v_lshlrev_b32_sdwa v203, 1, v175 dst_sel:DWORD dst_unused:UNUSED_PAD src0_sel:DWORD src1_sel:WORD_1\n"
    "v_mfma_f32_16x16x32_bf16 v[168:171], a[96:99], v[144:147], 0\n"
    "s_waitcnt lgkmcnt(7)\n"
    "ds_read_u16_d16_hi v201, v201\n"
    "v_cvt_pk_f16_f32 v196, v176, v177\n"
    "v_mfma_f32_16x16x32_bf16 v[168:171], a[144:147], v[148:151], v[168:171]\n"
    "v_lshlrev_b32_sdwa v197, 1, v178 dst_sel:DWORD dst_unused:UNUSED_PAD src0_sel:DWORD src1_sel:WORD_1\n"
    "v_lshlrev_b32_sdwa v198, 1, v180 dst_sel:DWORD dst_unused:UNUSED_PAD src0_sel:DWORD src1_sel:WORD_1\n"
    "v_mfma_f32_16x16x32_bf16 v[168:171], a[56:59], v[152:155], v[168:171]\n"
    "ds_read_u16 v194, v194\n"
    "v_lshlrev_b32_sdwa v199, 1, v182 dst_sel:DWORD dst_unused:UNUSED_PAD src0_sel:DWORD src1_sel:WORD_1\n"
    "v_mfma_f32_16x16x32_bf16 v[172:175], a[100:103], v[144:147], 0\n"
    "v_or_b32_e64 v188, v188, v204\n"
    "v_cvt_pk_bf16_f32 v99, v102, v103\n"
    "v_mfma_f32_16x16x32_bf16 v[172:175], a[148:151], v[148:151], v[172:175]\n"
    "s_waitcnt lgkmcnt(7)\n"
    "ds_read_u16_d16_hi v202, v202\n"
    "v_or_b32_e32 v189, v189, v205\n"
    "v_mfma_f32_16x16x32_bf16 v[172:175], a[60:63], v[152:155], v[172:175]\n"
    "v_lshlrev_b32_sdwa v205, 1, v179 dst_sel:DWORD dst_unused:UNUSED_PAD src0_sel:DWORD src1_sel:WORD_1\n"
    "v_cvt_pk_bf16_f32 v112, v112, v113\n"
    "v_mfma_f32_16x16x32_bf16 v[176:179], a[96:99], v[156:159], 0\n"
    "ds_read_b128 v[0:3], v244 offset:0\n"
    "ds_write_b128 v243, v[96:99] offset:24640\n"
    "v_mfma_f32_16x16x32_bf16 v[176:179], a[144:147], v[160:163], v[176:179]\n"
    "s_waitcnt lgkmcnt(8)\n"
    "v_or_b32_e32 v190, v190, v206\n"
    "v_lshlrev_b32_sdwa v206, 1, v181 dst_sel:DWORD dst_unused:UNUSED_PAD src0_sel:DWORD src1_sel:WORD_1\n"
    "v_mfma_f32_16x16x32_bf16 v[176:179], a[56:59], v[164:167], v[176:179]\n"
    "v_cvt_pk_bf16_f32 v113, v114, v115\n"
    "v_cvt_pk_bf16_f32 v114, v116, v117\n"
    "v_mfma_f32_16x16x32_bf16 v[96:99], a[0:3], v[184:187], 0\n"
    "s_nop 0\n"
    "ds_read_b128 a[252:255], v249 offset:0\n"
    "ds_read_u16 v195, v195\n"
    "v_mfma_f32_16x16x32_bf16 v[100:103], a[4:7], v[184:187], 0\n"
    "s_waitcnt lgkmcnt(8)\n"
    "ds_read_u16_d16_hi v203, v203\n"
    "v_cvt_pk_bf16_f32 v115, v118, v119\n"
    "v_cvt_pk_bf16_f32 v128, v128, v129\n"
    "v_cvt_pk_bf16_f32 v129, v130, v131\n"
    "v_or_b32_e64 v191, v191, v207\n"
    "v_mfma_f32_16x16x32_bf16 v[116:119], a[12:15], v[184:187], 0\n"
    "ds_read_b128 v[4:7], v244 offset:2048\n"
    "v_lshlrev_b32_sdwa v207, 1, v183 dst_sel:DWORD dst_unused:UNUSED_PAD src0_sel:DWORD src1_sel:WORD_1\n"
    "v_mfma_f32_16x16x32_bf16 v[180:183], a[100:103], v[156:159], 0\n"
    "ds_read_u16 v197, v197\n"
    "v_cvt_pk_bf16_f32 v130, v132, v133\n"
    "v_mfma_f32_16x16x32_bf16 v[180:183], a[148:151], v[160:163], v[180:183]\n"
    "s_waitcnt lgkmcnt(9)\n"
    "v_or_b32_e32 v193, v193, v201\n"
    "v_cvt_pk_bf16_f32 v131, v134, v135\n"
    "v_mfma_f32_16x16x32_bf16 v[180:183], a[60:63], v[164:167], v[180:183]\n"
    "ds_read_u16_d16_hi v205, v205\n"
    "v_cvt_pk_bf16_f32 v104, v104, v105\n"
    "v_cvt_pk_bf16_f32 v105, v106, v107\n"
    "v_cvt_pk_bf16_f32 v106, v108, v109\n"
    "v_cvt_pk_bf16_f32 v107, v110, v111\n"
    "v_mfma_f32_16x16x32_bf16 v[108:111], a[4:7], v[188:191], 0\n"
    "s_waitcnt lgkmcnt(7)\n"
    "ds_read_u16 v198, v198\n"
    "v_or_b32_e32 v194, v194, v202\n"
    "v_cvt_pk_bf16_f32 v120, v120, v121\n"
    "v_cvt_pk_bf16_f32 v121, v122, v123\n"
    "v_cvt_pk_bf16_f32 v122, v124, v125\n"
    "v_cvt_pk_bf16_f32 v123, v126, v127\n"
    "ds_write_b128 v243, v[112:115] offset:26688\n"
    "s_waitcnt lgkmcnt(7)\n"
    "buffer_store_dwordx4 a[252:255], v255, s[68:71], s59 offen offset:0 sc1\n"
    "v_mfma_f32_16x16x32_bf16 v[112:115], a[8:11], v[184:187], 0\n"
    "ds_read_u16_d16_hi v206, v206\n"
    "ds_write_b128 v243, v[128:131] offset:28736\n"
    "v_mfma_f32_16x16x32_bf16 v[128:131], v[0:3], v[184:187], 0\n"
    "s_waitcnt lgkmcnt(6)\n"
    "v_pk_fma_f16 v232, v192, v192, s48\n"
    "v_cvt_pk_bf16_f32 v136, v136, v137\n"
    "v_mfma_f32_16x16x32_bf16 v[132:135], v[4:7], v[184:187], 0\n"
    "v_cvt_pk_bf16_f32 v137, v138, v139\n"
    "v_cvt_pk_bf16_f32 v138, v140, v141\n"
    "v_mfma_f32_16x16x32_bf16 v[124:127], a[12:15], v[188:191], 0\n"
    "ds_read_u16 v199, v199\n"
    "ds_write_b128 v243, v[104:107] offset:25664\n"
    "v_mfma_f32_16x16x32_bf16 v[104:107], a[0:3], v[188:191], 0\n"
    "v_or_b32_e64 v195, v195, v203\n"
    "v_cvt_pk_bf16_f32 v139, v142, v143\n"
    "v_pk_mul_f16 v232, v192, v232\n"
    "s_waitcnt lgkmcnt(6)\n"
    "v_or_b32_e32 v197, v197, v205\n"
    "v_exp_f16_e64 v234, v232\n"
    "v_mfma_f32_16x16x32_bf16 v[140:143], v[4:7], v[188:191], 0\n"
    "ds_read_u16_d16_hi v207, v207\n"
    "ds_write_b128 v243, v[120:123] offset:27712\n"
    "v_mfma_f32_16x16x32_bf16 v[120:123], a[8:11], v[188:191], 0\n"
    "v_exp_f16_sdwa v234, v232 dst_sel:WORD_1 dst_unused:UNUSED_PRESERVE src0_sel:WORD_1\n"
    "v_cvt_pk_f16_f32 v184, v168, v169\n"
    "v_pk_fma_f16 v234, v234, s49, s49\n"
    "v_lshlrev_b32_sdwa v185, 1, v170 dst_sel:DWORD dst_unused:UNUSED_PAD src0_sel:DWORD src1_sel:WORD_1\n"
    "v_lshlrev_b32_sdwa v201, 1, v171 dst_sel:DWORD dst_unused:UNUSED_PAD src0_sel:DWORD src1_sel:WORD_1\n"
    "v_mfma_f32_16x16x32_bf16 v[168:171], a[104:107], v[144:147], 0\n"
    "s_waitcnt lgkmcnt(4)\n"
    "ds_read_b128 a[252:255], v249 offset:2048\n"
    "ds_write_b128 v243, v[136:139] offset:29760\n"
    "v_mfma_f32_16x16x32_bf16 v[136:139], v[0:3], v[188:191], 0\n"
    "s_nop 0\n"
    "v_lshlrev_b32_sdwa v186, 1, v172 dst_sel:DWORD dst_unused:UNUSED_PAD src0_sel:DWORD src1_sel:WORD_1\n"
    "v_lshlrev_b32_sdwa v202, 1, v173 dst_sel:DWORD dst_unused:UNUSED_PAD src0_sel:DWORD src1_sel:WORD_1\n"
    "v_mfma_f32_16x16x32_bf16 v[168:171], a[152:155], v[148:151], v[168:171]\n"
    "v_lshlrev_b32_sdwa v187, 1, v174 dst_sel:DWORD dst_unused:UNUSED_PAD src0_sel:DWORD src1_sel:WORD_1\n"
    "v_lshlrev_b32_sdwa v203, 1, v175 dst_sel:DWORD dst_unused:UNUSED_PAD src0_sel:DWORD src1_sel:WORD_1\n"
    "v_rcp_f16_e64 v232, v234\n"
    "v_mfma_f32_16x16x32_bf16 v[168:171], a[64:67], v[152:155], v[168:171]\n"
    "ds_read_u16 v185, v185\n"
    "ds_read_u16_d16_hi v201, v201\n"
    "v_or_b32_e64 v198, v198, v206\n"
    "v_rcp_f16_sdwa v232, v234 dst_sel:WORD_1 dst_unused:UNUSED_PRESERVE src0_sel:WORD_1\n"
    "v_mfma_f32_16x16x32_bf16 v[172:175], a[108:111], v[144:147], 0\n"
    "ds_read_u16 v186, v186\n"
    "ds_read_u16_d16_hi v202, v202\n"
    "v_mfma_f32_16x16x32_bf16 v[172:175], a[156:159], v[148:151], v[172:175]\n"
    "v_cvt_pk_f16_f32 v188, v176, v177\n"
    "v_lshlrev_b32_sdwa v189, 1, v178 dst_sel:DWORD dst_unused:UNUSED_PAD src0_sel:DWORD src1_sel:WORD_1\n"
    "v_lshlrev_b32_sdwa v205, 1, v179 dst_sel:DWORD dst_unused:UNUSED_PAD src0_sel:DWORD src1_sel:WORD_1\n"
    "v_lshlrev_b32_sdwa v190, 1, v180 dst_sel:DWORD dst_unused:UNUSED_PAD src0_sel:DWORD src1_sel:WORD_1\n"
    "v_lshlrev_b32_sdwa v206, 1, v181 dst_sel:DWORD dst_unused:UNUSED_PAD src0_sel:DWORD src1_sel:WORD_1\n"
    "v_lshlrev_b32_sdwa v191, 1, v182 dst_sel:DWORD dst_unused:UNUSED_PAD src0_sel:DWORD src1_sel:WORD_1\n"
    "v_fma_mix_f32 v234, v192, v232, 0 op_sel_hi:[1,1,0]\n"
    "v_mfma_f32_16x16x32_bf16 v[172:175], a[68:71], v[152:155], v[172:175]\n"
    "s_waitcnt lgkmcnt(5)\n"
    "ds_read_b128 v[8:11], v244 offset:4096\n"
    "buffer_store_dwordx4 a[252:255], v255, s[68:71], s59 offen offset:64 sc1\n"
    "v_or_b32_e32 v199, v199, v207\n"
    "v_lshlrev_b32_sdwa v207, 1, v183 dst_sel:DWORD dst_unused:UNUSED_PAD src0_sel:DWORD src1_sel:WORD_1\n"
    "v_fma_mix_f32 v232, v192, v232, 0 op_sel:[1,1,0] op_sel_hi:[1,1,0]\n"
    "v_mfma_f32_16x16x32_bf16 v[176:179], a[104:107], v[156:159], 0\n"
    "ds_read_u16 v187, v187\n"
    "ds_read_u16_d16_hi v203, v203\n"
    "v_cvt_pk_bf16_f32 v192, v234, v232\n"
    "v_mfma_f32_16x16x32_bf16 v[176:179], a[152:155], v[160:163], v[176:179]\n"
    "ds_read_b128 v[12:15], v244 offset:6144\n"
    "ds_read_b128 a[252:255], v249 offset:4096\n"
    "v_mfma_f32_16x16x32_bf16 v[176:179], a[64:67], v[164:167], v[176:179]\n"
    "ds_read_u16 v189, v189\n"
    "v_pk_fma_f16 v232, v196, v196, s48\n"
    "v_mfma_f32_16x16x32_bf16 v[180:183], a[108:111], v[156:159], 0\n"
    "v_pk_mul_f16 v232, v196, v232\n"
    "v_lshlrev_b32_sdwa v200, 1, v169 dst_sel:DWORD dst_unused:UNUSED_PAD src0_sel:DWORD src1_sel:WORD_1\n"
    "v_mfma_f32_16x16x32_bf16 v[180:183], a[156:159], v[160:163], v[180:183]\n"
    "s_waitcnt lgkmcnt(8)\n"
    "ds_read_u16_d16_hi v205, v205\n"
    "v_exp_f16_e32 v234, v232\n"
    "v_mfma_f32_16x16x32_bf16 v[180:183], a[68:71], v[164:167], v[180:183]\n"
    "ds_read_u16 v190, v190\n"
    "v_exp_f16_sdwa v234, v232 dst_sel:WORD_1 dst_unused:UNUSED_PRESERVE src0_sel:WORD_1\n"
    "v_mfma_f32_16x16x32_bf16 v[96:99], a[16:19], v[192:195], v[96:99]\n"
    "v_pk_fma_f16 v234, v234, s49, s49\n"
    "v_or_b32_e32 v185, v185, v201\n"
    "v_mfma_f32_16x16x32_bf16 v[100:103], a[20:23], v[192:195], v[100:103]\n"
    "s_waitcnt lgkmcnt(7)\n"
    "ds_read_u16_d16_hi v206, v206\n"
    "v_rcp_f16_e64 v232, v234\n"
    "v_mfma_f32_16x16x32_bf16 v[112:115], a[24:27], v[192:195], v[112:115]\n"
    "ds_read_u16 v191, v191\n"
    "v_rcp_f16_sdwa v232, v234 dst_sel:WORD_1 dst_unused:UNUSED_PRESERVE src0_sel:WORD_1\n"
    "v_mfma_f32_16x16x32_bf16 v[116:119], a[28:31], v[192:195], v[116:119]\n"
    "v_fma_mix_f32 v234, v196, v232, 0 op_sel_hi:[1,1,0]\n"
    "v_fma_mix_f32 v232, v196, v232, 0 op_sel:[1,1,0] op_sel_hi:[1,1,0]\n"
    "v_mfma_f32_16x16x32_bf16 v[128:131], v[8:11], v[192:195], v[128:131]\n"
    "ds_read_u16_d16_hi v207, v207\n"
    "v_or_b32_e64 v186, v186, v202\n"
    "s_waitcnt lgkmcnt(7)\n"
    "v_mfma_f32_16x16x32_bf16 v[132:135], v[12:15], v[192:195], v[132:135]\n"
    "ds_read_b128 v[52:55], v244 offset:34816\n"
    "s_waitcnt lgkmcnt(7)\n"
    "buffer_store_dwordx4 a[252:255], v255, s[68:71], s59 offen offset:128 sc1\n"
    "v_cvt_pk_bf16_f32 v196, v234, v232\n"
    "v_pk_fma_f16 v232, v184, v184, s48\n"
    "v_or_b32_e64 v187, v187, v203\n"
    "v_mfma_f32_16x16x32_bf16 v[104:107], a[16:19], v[196:199], v[104:107]\n"
    "ds_read_b128 a[252:255], v249 offset:1024\n"
    "ds_read_u16_d16_hi v200, v200\n"
    "v_mfma_f32_16x16x32_bf16 v[108:111], a[20:23], v[196:199], v[108:111]\n"
    "v_pk_mul_f16 v232, v184, v232\n"
    "v_lshlrev_b32_sdwa v192, 1, v168 dst_sel:DWORD dst_unused:UNUSED_PAD src0_sel:DWORD src1_sel:WORD_1\n"
    "v_mfma_f32_16x16x32_bf16 v[120:123], a[24:27], v[196:199], v[120:123]\n"
    "s_waitcnt lgkmcnt(7)\n"
    "ds_read_u16 v192, v192\n"
    "v_exp_f16_e32 v234, v232\n"
    "v_mfma_f32_16x16x32_bf16 v[124:127], a[28:31], v[196:199], v[124:127]\n"
    "v_or_b32_e32 v189, v189, v205\n"
    "v_exp_f16_sdwa v234, v232 dst_sel:WORD_1 dst_unused:UNUSED_PRESERVE src0_sel:WORD_1\n"
    "v_mfma_f32_16x16x32_bf16 v[136:139], v[8:11], v[196:199], v[136:139]\n"
    "ds_read_b128 v[16:19], v244 offset:8192\n"
    "v_pk_fma_f16 v234, v234, s49, s49\n"
    "v_mfma_f32_16x16x32_bf16 v[140:143], v[12:15], v[196:199], v[140:143]\n"
    "s_waitcnt lgkmcnt(7)\n"
    "v_lshlrev_b32_sdwa v193, 1, v170 dst_sel:DWORD dst_unused:UNUSED_PAD src0_sel:DWORD src1_sel:WORD_1\n"
    "v_lshlrev_b32_sdwa v201, 1, v171 dst_sel:DWORD dst_unused:UNUSED_PAD src0_sel:DWORD src1_sel:WORD_1\n"
    "v_mfma_f32_16x16x32_bf16 v[168:171], a[112:115], v[144:147], 0\n"
    "ds_read_u16 v193, v193\n"
    "ds_read_u16_d16_hi v201, v201\n"
    "v_mfma_f32_16x16x32_bf16 v[168:171], a[160:163], v[148:151], v[168:171]\n"
    "v_lshlrev_b32_sdwa v194, 1, v172 dst_sel:DWORD dst_unused:UNUSED_PAD src0_sel:DWORD src1_sel:WORD_1\n"
    "v_lshlrev_b32_sdwa v202, 1, v173 dst_sel:DWORD dst_unused:UNUSED_PAD src0_sel:DWORD src1_sel:WORD_1\n"
    "v_lshlrev_b32_sdwa v195, 1, v174 dst_sel:DWORD dst_unused:UNUSED_PAD src0_sel:DWORD src1_sel:WORD_1\n"
    "v_lshlrev_b32_sdwa v203, 1, v175 dst_sel:DWORD dst_unused:UNUSED_PAD src0_sel:DWORD src1_sel:WORD_1\n"
    "v_rcp_f16_e32 v232, v234\n"
    "v_mfma_f32_16x16x32_bf16 v[168:171], a[72:75], v[152:155], v[168:171]\n"
    "s_waitcnt lgkmcnt(6)\n"
    "ds_read_u16 v194, v194\n"
    "s_waitcnt lgkmcnt(6)\n"
    "buffer_store_dwordx4 a[252:255], v255, s[68:71], s39 offen offset:0 sc1\n"
    "v_or_b32_e32 v190, v190, v206\n"
    "v_rcp_f16_sdwa v232, v234 dst_sel:WORD_1 dst_unused:UNUSED_PRESERVE src0_sel:WORD_1\n"
    "v_mfma_f32_16x16x32_bf16 v[172:175], a[116:119], v[144:147], 0\n"
    "ds_read_u16_d16_hi v202, v202\n"
    "ds_read_u16 v195, v195\n"
    "v_mfma_f32_16x16x32_bf16 v[172:175], a[164:167], v[148:151], v[172:175]\n"
    "ds_read_b128 v[20:23], v244 offset:10240\n"
    "ds_read_u16_d16_hi v203, v203\n"
    "v_lshlrev_b32_sdwa v196, 1, v176 dst_sel:DWORD dst_unused:UNUSED_PAD src0_sel:DWORD src1_sel:WORD_1\n"
    "v_lshlrev_b32_sdwa v204, 1, v177 dst_sel:DWORD dst_unused:UNUSED_PAD src0_sel:DWORD src1_sel:WORD_1\n"
    "v_lshlrev_b32_sdwa v197, 1, v178 dst_sel:DWORD dst_unused:UNUSED_PAD src0_sel:DWORD src1_sel:WORD_1\n"
    "v_lshlrev_b32_sdwa v205, 1, v179 dst_sel:DWORD dst_unused:UNUSED_PAD src0_sel:DWORD src1_sel:WORD_1\n"
    "v_lshlrev_b32_sdwa v198, 1, v180 dst_sel:DWORD dst_unused:UNUSED_PAD src0_sel:DWORD src1_sel:WORD_1\n"
    "v_lshlrev_b32_sdwa v206, 1, v181 dst_sel:DWORD dst_unused:UNUSED_PAD src0_sel:DWORD src1_sel:WORD_1\n"
    "v_lshlrev_b32_sdwa v199, 1, v182 dst_sel:DWORD dst_unused:UNUSED_PAD src0_sel:DWORD src1_sel:WORD_1\n"
    "v_fma_mix_f32 v234, v184, v232, 0 op_sel_hi:[1,1,0]\n"
    "v_mfma_f32_16x16x32_bf16 v[172:175], v[52:55], v[152:155], v[172:175]\n"
    "s_waitcnt lgkmcnt(7)\n"
    "ds_read_u16 v196, v196\n"
    "ds_read_u16_d16_hi v204, v204\n"
    "v_or_b32_e32 v191, v191, v207\n"
    "v_lshlrev_b32_sdwa v207, 1, v183 dst_sel:DWORD dst_unused:UNUSED_PAD src0_sel:DWORD src1_sel:WORD_1\n"
    "v_fma_mix_f32 v232, v184, v232, 0 op_sel:[1,1,0] op_sel_hi:[1,1,0]\n"
    "v_mfma_f32_16x16x32_bf16 v[176:179], a[112:115], v[156:159], 0\n"
    "ds_read_u16 v197, v197\n"
    "v_cvt_pk_bf16_f32 v184, v234, v232\n"
    "v_mfma_f32_16x16x32_bf16 v[176:179], a[160:163], v[160:163], v[176:179]\n"
    "v_pk_fma_f16 v232, v188, v188, s48\n"
    "v_or_b32_e32 v192, v192, v200\n"
    "v_mfma_f32_16x16x32_bf16 v[176:179], a[72:75], v[164:167], v[176:179]\n"
    "s_waitcnt lgkmcnt(8)\n"
    "ds_read_b128 a[252:255], v249 offset:3072\n"
    "v_pk_mul_f16 v232, v188, v232\n"
    "v_mfma_f32_16x16x32_bf16 v[180:183], a[116:119], v[156:159], 0\n"
    "ds_read_u16_d16_hi v205, v205\n"
    "v_exp_f16_e32 v234, v232\n"
    "v_mfma_f32_16x16x32_bf16 v[180:183], a[164:167], v[160:163], v[180:183]\n"
    "s_waitcnt lgkmcnt(8)\n"
    "v_exp_f16_sdwa v234, v232 dst_sel:WORD_1 dst_unused:UNUSED_PRESERVE src0_sel:WORD_1\n"
    "v_mfma_f32_16x16x32_bf16 v[180:183], v[52:55], v[164:167], v[180:183]\n"
    "ds_read_u16 v198, v198\n"
    "v_pk_fma_f16 v234, v234, s49, s49\n"
    "v_mfma_f32_16x16x32_bf16 v[96:99], a[32:35], v[184:187], v[96:99]\n"
    "ds_read_u16_d16_hi v206, v206\n"
    "v_rcp_f16_e64 v232, v234\n"
    "v_mfma_f32_16x16x32_bf16 v[100:103], a[36:39], v[184:187], v[100:103]\n"
    "v_rcp_f16_sdwa v232, v234 dst_sel:WORD_1 dst_unused:UNUSED_PRESERVE src0_sel:WORD_1\n"
    "v_mfma_f32_16x16x32_bf16 v[112:115], a[40:43], v[184:187], v[112:115]\n"
    "ds_read_u16 v199, v199\n"
    "v_fma_mix_f32 v234, v188, v232, 0 op_sel_hi:[1,1,0]\n"
    "v_mfma_f32_16x16x32_bf16 v[116:119], a[44:47], v[184:187], v[116:119]\n"
    "s_waitcnt lgkmcnt(8)\n"
    "v_fma_mix_f32 v232, v188, v232, 0 op_sel:[1,1,0] op_sel_hi:[1,1,0]\n"
    "v_or_b32_e32 v193, v193, v201\n"
    "v_mfma_f32_16x16x32_bf16 v[128:131], v[16:19], v[184:187], v[128:131]\n"
    "ds_read_u16_d16_hi v207, v207\n"
    "v_cvt_pk_bf16_f32 v188, v234, v232\n"
    "v_mfma_f32_16x16x32_bf16 v[132:135], v[20:23], v[184:187], v[132:135]\n"
    "s_nop 0\n"
    "ds_read_b128 v[60:63], v244 offset:38912\n"
    "v_cvt_pk_f16_f32 v184, v168, v169\n"
    "v_mfma_f32_16x16x32_bf16 v[104:107], a[32:35], v[188:191], v[104:107]\n"
    "s_waitcnt lgkmcnt(8)\n"
    "ds_read_b128 v[24:27], v244 offset:12288\n"
    "v_lshlrev_b32_sdwa v185, 1, v170 dst_sel:DWORD dst_unused:UNUSED_PAD src0_sel:DWORD src1_sel:WORD_1\n"
    "v_mfma_f32_16x16x32_bf16 v[108:111], a[36:39], v[188:191], v[108:111]\n"
    "ds_read_u16 v185, v185\n"
    "s_waitcnt lgkmcnt(8)\n"
    "buffer_store_dwordx4 a[252:255], v255, s[68:71], s39 offen offset:64 sc1\n"
    "v_mfma_f32_16x16x32_bf16 v[120:123], a[40:43], v[188:191], v[120:123]\n"
    "v_lshlrev_b32_sdwa v201, 1, v171 dst_sel:DWORD dst_unused:UNUSED_PAD src0_sel:DWORD src1_sel:WORD_1\n"
    "v_lshlrev_b32_sdwa v186, 1, v172 dst_sel:DWORD dst_unused:UNUSED_PAD src0_sel:DWORD src1_sel:WORD_1\n"
    "v_mfma_f32_16x16x32_bf16 v[124:127], a[44:47], v[188:191], v[124:127]\n"
    "s_waitcnt lgkmcnt(7)\n"
    "ds_read_b128 a[252:255], v249 offset:5120\n"
    "ds_read_u16_d16_hi v201, v201\n"
    "v_mfma_f32_16x16x32_bf16 v[136:139], v[16:19], v[188:191], v[136:139]\n"
    "v_or_b32_e64 v194, v194, v202\n"
    "v_lshlrev_b32_sdwa v202, 1, v173 dst_sel:DWORD dst_unused:UNUSED_PAD src0_sel:DWORD src1_sel:WORD_1\n"
    "v_mfma_f32_16x16x32_bf16 v[140:143], v[20:23], v[188:191], v[140:143]\n"
    "ds_read_u16 v186, v186\n"
    "v_lshlrev_b32_sdwa v187, 1, v174 dst_sel:DWORD dst_unused:UNUSED_PAD src0_sel:DWORD src1_sel:WORD_1\n"
    "v_mfma_f32_16x16x32_bf16 v[168:171], a[120:123], v[144:147], 0\n"
    "s_nop 0\n"
    "ds_read_u16_d16_hi v202, v202\n"
    "v_cvt_pk_f16_f32 v188, v176, v177\n"
    "v_mfma_f32_16x16x32_bf16 v[168:171], a[168:171], v[148:151], v[168:171]\n"
    "s_waitcnt lgkmcnt(9)\n"
    "v_lshlrev_b32_sdwa v189, 1, v178 dst_sel:DWORD dst_unused:UNUSED_PAD src0_sel:DWORD src1_sel:WORD_1\n"
    "v_lshlrev_b32_sdwa v190, 1, v180 dst_sel:DWORD dst_unused:UNUSED_PAD src0_sel:DWORD src1_sel:WORD_1\n"
    "v_mfma_f32_16x16x32_bf16 v[168:171], a[76:79], v[152:155], v[168:171]\n"
    "ds_read_u16 v187, v187\n"
    "v_lshlrev_b32_sdwa v191, 1, v182 dst_sel:DWORD dst_unused:UNUSED_PAD src0_sel:DWORD src1_sel:WORD_1\n"
    "v_or_b32_e64 v195, v195, v203\n"
    "v_lshlrev_b32_sdwa v203, 1, v175 dst_sel:DWORD dst_unused:UNUSED_PAD src0_sel:DWORD src1_sel:WORD_1\n"
    "v_mfma_f32_16x16x32_bf16 v[172:175], a[124:127], v[144:147], 0\n"
    "ds_read_u16_d16_hi v203, v203\n"
    "v_or_b32_e64 v196, v196, v204\n"
    "v_mfma_f32_16x16x32_bf16 v[172:175], a[172:175], v[148:151], v[172:175]\n"
    "s_waitcnt lgkmcnt(8)\n"
    "v_or_b32_e32 v197, v197, v205\n"
    "v_lshlrev_b32_sdwa v205, 1, v179 dst_sel:DWORD dst_unused:UNUSED_PAD src0_sel:DWORD src1_sel:WORD_1\n"
    "v_mfma_f32_16x16x32_bf16 v[172:175], v[60:63], v[152:155], v[172:175]\n"
    "ds_read_b128 v[28:31], v244 offset:14336\n"
    "v_or_b32_e64 v198, v198, v206\n"
    "v_mfma_f32_16x16x32_bf16 v[176:179], a[120:123], v[156:159], 0\n"
    "ds_read_u16 v189, v189\n"
    "v_lshlrev_b32_sdwa v206, 1, v181 dst_sel:DWORD dst_unused:UNUSED_PAD src0_sel:DWORD src1_sel:WORD_1\n"
    "v_mfma_f32_16x16x32_bf16 v[176:179], a[168:171], v[160:163], v[176:179]\n"
    "s_waitcnt lgkmcnt(7)\n"
    "buffer_store_dwordx4 a[252:255], v255, s[68:71], s39 offen offset:128 sc1\n"
    "ds_read_u16_d16_hi v205, v205\n"
    "v_mfma_f32_16x16x32_bf16 v[176:179], a[76:79], v[164:167], v[176:179]\n"
    "v_or_b32_e32 v199, v199, v207\n"
    "v_lshlrev_b32_sdwa v207, 1, v183 dst_sel:DWORD dst_unused:UNUSED_PAD src0_sel:DWORD src1_sel:WORD_1\n"
    "v_mfma_f32_16x16x32_bf16 v[180:183], a[124:127], v[156:159], 0\n"
    "s_waitcnt lgkmcnt(7)\n"
    "ds_read_u16 v190, v190\n"
    "v_or_b32_e32 v185, v185, v201\n"
    "v_mfma_f32_16x16x32_bf16 v[180:183], a[172:175], v[160:163], v[180:183]\n"
    "ds_read_b128 v[144:147], v243 offset:0\n"
    "v_pk_fma_f16 v232, v184, v184, s48\n"
    "v_mfma_f32_16x16x32_bf16 v[180:183], v[60:63], v[164:167], v[180:183]\n"
    "ds_read_u16_d16_hi v206, v206\n"
    "v_pk_mul_f16 v232, v184, v232\n"
    "v_mfma_f32_16x16x32_bf16 v[96:99], a[176:179], v[192:195], v[96:99]\n"
    "v_exp_f16_e32 v234, v232\n"
    "v_mfma_f32_16x16x32_bf16 v[100:103], a[180:183], v[192:195], v[100:103]\n"
    "s_waitcnt lgkmcnt(8)\n"
    "ds_read_b128 v[148:151], v243 offset:1024\n"
    "v_or_b32_e64 v186, v186, v202\n"
    "v_mfma_f32_16x16x32_bf16 v[112:115], a[184:187], v[192:195], v[112:115]\n"
    "ds_read_u16 v191, v191\n"
    "v_exp_f16_sdwa v234, v232 dst_sel:WORD_1 dst_unused:UNUSED_PRESERVE src0_sel:WORD_1\n"
    "v_mfma_f32_16x16x32_bf16 v[116:119], a[188:191], v[192:195], v[116:119]\n"
    "v_pk_fma_f16 v234, v234, s49, s49\n"
    "v_lshlrev_b32_sdwa v201, 1, v171 dst_sel:DWORD dst_unused:UNUSED_PAD src0_sel:DWORD src1_sel:WORD_1\n"
    "v_mfma_f32_16x16x32_bf16 v[128:131], v[24:27], v[192:195], v[128:131]\n"
    "s_waitcnt lgkmcnt(7)\n"
    "ds_read_b128 v[152:155], v243 offset:2048\n"
    "ds_read_u16_d16_hi v207, v207\n"
    "v_mfma_f32_16x16x32_bf16 v[132:135], v[28:31], v[192:195], v[132:135]\n"
    "v_or_b32_e32 v187, v187, v203\n"
    "v_cvt_pk_f16_f32 v192, v168, v169\n"
    "v_mfma_f32_16x16x32_bf16 v[104:107], a[176:179], v[196:199], v[104:107]\n"
    "ds_read_b128 v[156:159], v243 offset:3072\n"
    "v_lshlrev_b32_sdwa v193, 1, v170 dst_sel:DWORD dst_unused:UNUSED_PAD src0_sel:DWORD src1_sel:WORD_1\n"
    "v_mfma_f32_16x16x32_bf16 v[108:111], a[180:183], v[196:199], v[108:111]\n"
    "ds_read_b128 v[160:163], v243 offset:4096\n"
    "v_lshlrev_b32_sdwa v194, 1, v172 dst_sel:DWORD dst_unused:UNUSED_PAD src0_sel:DWORD src1_sel:WORD_1\n"
    "v_mfma_f32_16x16x32_bf16 v[120:123], a[184:187], v[196:199], v[120:123]\n"
    "ds_read_b128 v[164:167], v243 offset:5120\n"
    "s_waitcnt lgkmcnt(10)\n"
    "v_or_b32_e32 v189, v189, v205\n"
    "v_mfma_f32_16x16x32_bf16 v[124:127], a[188:191], v[196:199], v[124:127]\n"
    "s_nop 0\n"
    "ds_read_b128 v[48:51], v244 offset:32768\n"
    "v_lshlrev_b32_sdwa v202, 1, v173 dst_sel:DWORD dst_unused:UNUSED_PAD src0_sel:DWORD src1_sel:WORD_1\n"
    "v_mfma_f32_16x16x32_bf16 v[136:139], v[24:27], v[196:199], v[136:139]\n"
    "s_waitcnt lgkmcnt(9)\n"
    "ds_read_u16 v193, v193\n"
    "v_lshlrev_b32_sdwa v195, 1, v174 dst_sel:DWORD dst_unused:UNUSED_PAD src0_sel:DWORD src1_sel:WORD_1\n"
    "v_mfma_f32_16x16x32_bf16 v[140:143], v[28:31], v[196:199], v[140:143]\n"
    "v_lshlrev_b32_sdwa v203, 1, v175 dst_sel:DWORD dst_unused:UNUSED_PAD src0_sel:DWORD src1_sel:WORD_1\n"
    "v_rcp_f16_e32 v232, v234\n"
    "v_mfma_f32_16x16x32_bf16 v[168:171], a[80:83], v[144:147], 0\n"
    "s_waitcnt lgkmcnt(8)\n"
    "ds_read_u16_d16_hi v201, v201\n"
    "v_or_b32_e64 v190, v190, v206\n"
    "v_mfma_f32_16x16x32_bf16 v[168:171], a[128:131], v[148:151], v[168:171]\n"
    "ds_read_u16 v194, v194\n"
    "v_rcp_f16_sdwa v232, v234 dst_sel:WORD_1 dst_unused:UNUSED_PRESERVE src0_sel:WORD_1\n"
    "v_mfma_f32_16x16x32_bf16 v[172:175], a[84:87], v[144:147], 0\n"
    "v_cvt_pk_f16_f32 v196, v176, v177\n"
    "v_lshlrev_b32_sdwa v197, 1, v178 dst_sel:DWORD dst_unused:UNUSED_PAD src0_sel:DWORD src1_sel:WORD_1\n"
    "s_waitcnt lgkmcnt(8)\n"
    "v_mfma_f32_16x16x32_bf16 v[168:171], a[48:51], v[152:155], v[168:171]\n"
    "s_nop 0\n"
    "ds_read_u16_d16_hi v202, v202\n"
    "v_lshlrev_b32_sdwa v205, 1, v179 dst_sel:DWORD dst_unused:UNUSED_PAD src0_sel:DWORD src1_sel:WORD_1\n"
    "v_lshlrev_b32_sdwa v198, 1, v180 dst_sel:DWORD dst_unused:UNUSED_PAD src0_sel:DWORD src1_sel:WORD_1\n"
    "v_lshlrev_b32_sdwa v206, 1, v181 dst_sel:DWORD dst_unused:UNUSED_PAD src0_sel:DWORD src1_sel:WORD_1\n"
    "v_lshlrev_b32_sdwa v199, 1, v182 dst_sel:DWORD dst_unused:UNUSED_PAD src0_sel:DWORD src1_sel:WORD_1\n"
    "v_fma_mix_f32 v234, v184, v232, 0 op_sel_hi:[1,1,0]\n"
    "v_mfma_f32_16x16x32_bf16 v[172:175], a[132:135], v[148:151], v[172:175]\n"
    "ds_read_b128 v[32:35], v244 offset:16384\n"
    "ds_read_u16 v195, v195\n"
    "s_waitcnt lgkmcnt(9)\n"
    "v_or_b32_e32 v191, v191, v207\n"
    "v_lshlrev_b32_sdwa v207, 1, v183 dst_sel:DWORD dst_unused:UNUSED_PAD src0_sel:DWORD src1_sel:WORD_1\n"
    "v_fma_mix_f32 v232, v184, v232, 0 op_sel:[1,1,0] op_sel_hi:[1,1,0]\n"
    "v_mfma_f32_16x16x32_bf16 v[176:179], a[80:83], v[156:159], 0\n"
    "s_waitcnt lgkmcnt(7)\n"
    "ds_read_u16_d16_hi v203, v203\n"
    "v_cvt_pk_bf16_f32 v184, v234, v232\n"
    "v_mfma_f32_16x16x32_bf16 v[176:179], a[128:131], v[160:163], v[176:179]\n"
    "s_waitcnt lgkmcnt(7)\n"
    "v_pk_fma_f16 v232, v188, v188, s48\n"
    "v_lshlrev_b32_sdwa v200, 1, v169 dst_sel:DWORD dst_unused:UNUSED_PAD src0_sel:DWORD src1_sel:WORD_1\n"
    "v_mfma_f32_16x16x32_bf16 v[172:175], v[48:51], v[152:155], v[172:175]\n"
    "s_barrier\n"
    "v_mfma_f32_16x16x32_bf16 v[176:179], a[48:51], v[164:167], v[176:179]\n"
    "ds_read_b128 v[36:39], v244 offset:18432\n"
    "ds_write_b128 v253, v[64:67] offset:2048\n"
    "v_mfma_f32_16x16x32_bf16 v[180:183], a[84:87], v[156:159], 0\n"
    "s_waitcnt lgkmcnt(7)\n"
    "v_pk_mul_f16 v232, v188, v232\n"
    "v_or_b32_e64 v193, v193, v201\n"
    "v_mfma_f32_16x16x32_bf16 v[180:183], a[132:135], v[160:163], v[180:183]\n"
    "v_exp_f16_e64 v234, v232\n"
    "v_mfma_f32_16x16x32_bf16 v[180:183], v[48:51], v[164:167], v[180:183]\n"
    "ds_read_u16 v197, v197\n"
    "ds_write_b128 v253, v[68:71] offset:5120\n"
    "v_exp_f16_sdwa v234, v232 dst_sel:WORD_1 dst_unused:UNUSED_PRESERVE src0_sel:WORD_1\n"
    "v_mfma_f32_16x16x32_bf16 v[96:99], a[192:195], v[184:187], v[96:99]\n"
    "s_waitcnt lgkmcnt(6)\n"
    "v_pk_fma_f16 v234, v234, s49, s49\n"
    "v_or_b32_e32 v194, v194, v202\n"
    "v_mfma_f32_16x16x32_bf16 v[100:103], a[196:199], v[184:187], v[100:103]\n"
    "v_rcp_f16_e32 v232, v234\n"
    "v_mfma_f32_16x16x32_bf16 v[112:115], a[200:203], v[184:187], v[112:115]\n"
    "ds_read_u16_d16_hi v205, v205\n"
    "ds_write_b128 v253, v[88:91] offset:0\n"
    "v_rcp_f16_sdwa v232, v234 dst_sel:WORD_1 dst_unused:UNUSED_PRESERVE src0_sel:WORD_1\n"
    "v_mfma_f32_16x16x32_bf16 v[116:119], a[204:207], v[184:187], v[116:119]\n"
    "s_waitcnt lgkmcnt(4)\n"
    "v_fma_mix_f32 v234, v188, v232, 0 op_sel_hi:[1,1,0]\n"
    "v_fma_mix_f32 v232, v188, v232, 0 op_sel:[1,1,0] op_sel_hi:[1,1,0]\n"
    "v_mfma_f32_16x16x32_bf16 v[128:131], v[32:35], v[184:187], v[128:131]\n"
    "v_cvt_pk_bf16_f32 v188, v234, v232\n"
    "v_pk_fma_f16 v232, v192, v192, s48\n"
    "v_mfma_f32_16x16x32_bf16 v[132:135], v[36:39], v[184:187], v[132:135]\n"
    "ds_read_u16 v198, v198\n"
    "ds_write_b128 v253, v[92:95] offset:3072\n"
    "v_mfma_f32_16x16x32_bf16 v[104:107], a[192:195], v[188:191], v[104:107]\n"
    "v_or_b32_e64 v195, v195, v203\n"
    "v_pk_mul_f16 v232, v192, v232\n"
    "v_mfma_f32_16x16x32_bf16 v[108:111], a[196:199], v[188:191], v[108:111]\n"
    "v_exp_f16_e64 v234, v232\n"
    "v_mfma_f32_16x16x32_bf16 v[120:123], a[200:203], v[188:191], v[120:123]\n"
    "ds_read_u16_d16_hi v206, v206\n"
    "ds_read_u16 v199, v199\n"
    "v_mfma_f32_16x16x32_bf16 v[124:127], a[204:207], v[188:191], v[124:127]\n"
    "ds_read_u16_d16_hi v207, v207\n"
    "ds_write_b128 v253, v[72:75] offset:1024\n"
    "v_mfma_f32_16x16x32_bf16 v[136:139], v[32:35], v[188:191], v[136:139]\n"
    "s_waitcnt lgkmcnt(6)\n"
    "v_or_b32_e32 v197, v197, v205\n"
    "v_exp_f16_sdwa v234, v232 dst_sel:WORD_1 dst_unused:UNUSED_PRESERVE src0_sel:WORD_1\n"
    "v_mfma_f32_16x16x32_bf16 v[140:143], v[36:39], v[188:191], v[140:143]\n"
    "v_pk_fma_f16 v234, v234, s49, s49\n"
    "v_lshlrev_b32_sdwa v184, 1, v168 dst_sel:DWORD dst_unused:UNUSED_PAD src0_sel:DWORD src1_sel:WORD_1\n"
    "v_lshlrev_b32_sdwa v185, 1, v170 dst_sel:DWORD dst_unused:UNUSED_PAD src0_sel:DWORD src1_sel:WORD_1\n"
    "v_lshlrev_b32_sdwa v201, 1, v171 dst_sel:DWORD dst_unused:UNUSED_PAD src0_sel:DWORD src1_sel:WORD_1\n"
    "v_mfma_f32_16x16x32_bf16 v[168:171], a[88:91], v[144:147], 0\n"
    "ds_read_u16 v184, v184\n"
    "ds_write_b128 v253, v[76:79] offset:4096\n"
    "v_mfma_f32_16x16x32_bf16 v[168:171], a[136:139], v[148:151], v[168:171]\n"
    "v_lshlrev_b32_sdwa v186, 1, v172 dst_sel:DWORD dst_unused:UNUSED_PAD src0_sel:DWORD src1_sel:WORD_1\n"
    "v_lshlrev_b32_sdwa v202, 1, v173 dst_sel:DWORD dst_unused:UNUSED_PAD src0_sel:DWORD src1_sel:WORD_1\n"
    "v_lshlrev_b32_sdwa v187, 1, v174 dst_sel:DWORD dst_unused:UNUSED_PAD src0_sel:DWORD src1_sel:WORD_1\n"
    "v_lshlrev_b32_sdwa v203, 1, v175 dst_sel:DWORD dst_unused:UNUSED_PAD src0_sel:DWORD src1_sel:WORD_1\n"
    "v_rcp_f16_e64 v232, v234\n"
    "v_mfma_f32_16x16x32_bf16 v[168:171], a[52:55], v[152:155], v[168:171]\n"
    "ds_read_u16_d16_hi v200, v200\n"
    "ds_read_u16 v185, v185\n"
    "s_waitcnt lgkmcnt(7)\n"
    "v_or_b32_e32 v198, v198, v206\n"
    "v_rcp_f16_sdwa v232, v234 dst_sel:WORD_1 dst_unused:UNUSED_PRESERVE src0_sel:WORD_1\n"
    "v_mfma_f32_16x16x32_bf16 v[172:175], a[92:95], v[144:147], 0\n"
    "ds_read_u16_d16_hi v201, v201\n"
    "v_lshlrev_b32_sdwa v188, 1, v176 dst_sel:DWORD dst_unused:UNUSED_PAD src0_sel:DWORD src1_sel:WORD_1\n"
    "v_lshlrev_b32_sdwa v204, 1, v177 dst_sel:DWORD dst_unused:UNUSED_PAD src0_sel:DWORD src1_sel:WORD_1\n"
    "v_lshlrev_b32_sdwa v189, 1, v178 dst_sel:DWORD dst_unused:UNUSED_PAD src0_sel:DWORD src1_sel:WORD_1\n"
    "v_lshlrev_b32_sdwa v205, 1, v179 dst_sel:DWORD dst_unused:UNUSED_PAD src0_sel:DWORD src1_sel:WORD_1\n"
    "v_lshlrev_b32_sdwa v190, 1, v180 dst_sel:DWORD dst_unused:UNUSED_PAD src0_sel:DWORD src1_sel:WORD_1\n"
    "v_lshlrev_b32_sdwa v206, 1, v181 dst_sel:DWORD dst_unused:UNUSED_PAD src0_sel:DWORD src1_sel:WORD_1\n"
    "v_lshlrev_b32_sdwa v191, 1, v182 dst_sel:DWORD dst_unused:UNUSED_PAD src0_sel:DWORD src1_sel:WORD_1\n"
    "v_fma_mix_f32 v234, v192, v232, 0 op_sel_hi:[1,1,0]\n"
    "v_mfma_f32_16x16x32_bf16 v[172:175], a[140:143], v[148:151], v[172:175]\n"
    "s_waitcnt lgkmcnt(5)\n"
    "ds_read_u16 v186, v186\n"
    "ds_read_b128 v[40:43], v244 offset:20480\n"
    "v_mfma_f32_16x16x32_bf16 v[172:175], v[56:59], v[152:155], v[172:175]\n"
    "s_nop 0\n"
    "ds_read_u16_d16_hi v202, v202\n"
    "v_or_b32_e64 v199, v199, v207\n"
    "v_lshlrev_b32_sdwa v207, 1, v183 dst_sel:DWORD dst_unused:UNUSED_PAD src0_sel:DWORD src1_sel:WORD_1\n"
    "v_fma_mix_f32 v232, v192, v232, 0 op_sel:[1,1,0] op_sel_hi:[1,1,0]\n"
    "v_mfma_f32_16x16x32_bf16 v[176:179], a[88:91], v[156:159], 0\n"
    "ds_read_u16 v187, v187\n"
    "v_cvt_pk_bf16_f32 v192, v234, v232\n"
    "v_mfma_f32_16x16x32_bf16 v[176:179], a[136:139], v[160:163], v[176:179]\n"
    "v_pk_fma_f16 v232, v196, v196, s48\n"
    "v_pk_mul_f16 v232, v196, v232\n"
    "v_mfma_f32_16x16x32_bf16 v[176:179], a[52:55], v[164:167], v[176:179]\n"
    "ds_read_b128 v[44:47], v244 offset:22528\n"
    "v_exp_f16_e32 v234, v232\n"
    "v_mfma_f32_16x16x32_bf16 v[180:183], a[92:95], v[156:159], 0\n"
    "ds_read_u16_d16_hi v203, v203\n"
    "v_exp_f16_sdwa v234, v232 dst_sel:WORD_1 dst_unused:UNUSED_PRESERVE src0_sel:WORD_1\n"
    "v_mfma_f32_16x16x32_bf16 v[180:183], a[140:143], v[160:163], v[180:183]\n"
    "s_waitcnt lgkmcnt(8)\n"
    "ds_read_u16 v188, v188\n"
    "v_pk_fma_f16 v234, v234, s49, s49\n"
    "v_mfma_f32_16x16x32_bf16 v[180:183], v[56:59], v[164:167], v[180:183]\n"
    "v_rcp_f16_e32 v232, v234\n"
    "v_mfma_f32_16x16x32_bf16 v[96:99], a[208:211], v[192:195], v[96:99]\n"
    "ds_read_u16_d16_hi v204, v204\n"
    "v_rcp_f16_sdwa v232, v234 dst_sel:WORD_1 dst_unused:UNUSED_PRESERVE src0_sel:WORD_1\n"
    "v_mfma_f32_16x16x32_bf16 v[100:103], a[212:215], v[192:195], v[100:103]\n"
    "s_waitcnt lgkmcnt(8)\n"
    "ds_read_u16 v189, v189\n"
    "v_fma_mix_f32 v234, v196, v232, 0 op_sel_hi:[1,1,0]\n"
    "v_mfma_f32_16x16x32_bf16 v[112:115], a[216:219], v[192:195], v[112:115]\n"
    "v_fma_mix_f32 v232, v196, v232, 0 op_sel:[1,1,0] op_sel_hi:[1,1,0]\n"
    "v_or_b32_e32 v184, v184, v200\n"
    "v_mfma_f32_16x16x32_bf16 v[116:119], a[220:223], v[192:195], v[116:119]\n"
    "s_waitcnt lgkmcnt(7)\n"
    "ds_read_u16_d16_hi v205, v205\n"
    "v_cvt_pk_bf16_f32 v196, v234, v232\n"
    "v_mfma_f32_16x16x32_bf16 v[128:131], v[40:43], v[192:195], v[128:131]\n"
    "v_or_b32_e64 v185, v185, v201\n"
    "v_lshlrev_b32_sdwa v201, 1, v171 dst_sel:DWORD dst_unused:UNUSED_PAD src0_sel:DWORD src1_sel:WORD_1\n"
    "v_mfma_f32_16x16x32_bf16 v[104:107], a[208:211], v[196:199], v[104:107]\n"
    "ds_read_u16 v190, v190\n"
    "s_waitcnt lgkmcnt(8)\n"
    "v_or_b32_e32 v186, v186, v202\n"
    "v_mfma_f32_16x16x32_bf16 v[108:111], a[212:215], v[196:199], v[108:111]\n"
    "ds_read_u16_d16_hi v206, v206\n"
    "v_lshlrev_b32_sdwa v202, 1, v173 dst_sel:DWORD dst_unused:UNUSED_PAD src0_sel:DWORD src1_sel:WORD_1\n"
    "s_waitcnt lgkmcnt(7)\n"
    "v_mfma_f32_16x16x32_bf16 v[132:135], v[44:47], v[192:195], v[132:135]\n"
    "s_nop 0\n"
    "v_cvt_pk_f16_f32 v192, v168, v169\n"
    "v_lshlrev_b32_sdwa v193, 1, v170 dst_sel:DWORD dst_unused:UNUSED_PAD src0_sel:DWORD src1_sel:WORD_1\n"
    "v_mfma_f32_16x16x32_bf16 v[120:123], a[216:219], v[196:199], v[120:123]\n"
    "ds_read_u16 v191, v191\n"
    "v_lshlrev_b32_sdwa v194, 1, v172 dst_sel:DWORD dst_unused:UNUSED_PAD src0_sel:DWORD src1_sel:WORD_1\n"
    "v_mfma_f32_16x16x32_bf16 v[124:127], a[220:223], v[196:199], v[124:127]\n"
    "v_lshlrev_b32_sdwa v195, 1, v174 dst_sel:DWORD dst_unused:UNUSED_PAD src0_sel:DWORD src1_sel:WORD_1\n"
    "s_waitcnt lgkmcnt(7)\n"
    "v_or_b32_e32 v187, v187, v203\n"
    "v_mfma_f32_16x16x32_bf16 v[136:139], v[40:43], v[196:199], v[136:139]\n"
    "ds_read_u16_d16_hi v207, v207\n"
    "v_lshlrev_b32_sdwa v203, 1, v175 dst_sel:DWORD dst_unused:UNUSED_PAD src0_sel:DWORD src1_sel:WORD_1\n"
    "v_mfma_f32_16x16x32_bf16 v[140:143], v[44:47], v[196:199], v[140:143]\n"
    "ds_read_u16 v193, v193\n"
    "v_cvt_pk_f16_f32 v196, v176, v177\n"
    "v_mfma_f32_16x16x32_bf16 v[168:171], a[96:99], v[144:147], 0\n"
    "v_lshlrev_b32_sdwa v197, 1, v178 dst_sel:DWORD dst_unused:UNUSED_PAD src0_sel:DWORD src1_sel:WORD_1\n"
    "v_lshlrev_b32_sdwa v198, 1, v180 dst_sel:DWORD dst_unused:UNUSED_PAD src0_sel:DWORD src1_sel:WORD_1\n"
    "v_mfma_f32_16x16x32_bf16 v[168:171], a[144:147], v[148:151], v[168:171]\n"
    "s_waitcnt lgkmcnt(7)\n"
    "ds_read_u16_d16_hi v201, v201\n"
    "v_lshlrev_b32_sdwa v199, 1, v182 dst_sel:DWORD dst_unused:UNUSED_PAD src0_sel:DWORD src1_sel:WORD_1\n"
    "v_mfma_f32_16x16x32_bf16 v[168:171], a[56:59], v[152:155], v[168:171]\n"
    "v_or_b32_e32 v188, v188, v204\n"
    "v_cvt_pk_bf16_f32 v96, v96, v97\n"
    "v_mfma_f32_16x16x32_bf16 v[172:175], a[100:103], v[144:147], 0\n"
    "ds_read_u16 v194, v194\n"
    "v_cvt_pk_bf16_f32 v97, v98, v99\n"
    "v_mfma_f32_16x16x32_bf16 v[172:175], a[148:151], v[148:151], v[172:175]\n"
    "s_waitcnt lgkmcnt(7)\n"
    "ds_read_u16_d16_hi v202, v202\n"
    "v_or_b32_e32 v189, v189, v205\n"
    "v_mfma_f32_16x16x32_bf16 v[172:175], a[60:63], v[152:155], v[172:175]\n"
    "v_lshlrev_b32_sdwa v205, 1, v179 dst_sel:DWORD dst_unused:UNUSED_PAD src0_sel:DWORD src1_sel:WORD_1\n"
    "v_cvt_pk_bf16_f32 v98, v100, v101\n"
    "v_mfma_f32_16x16x32_bf16 v[176:179], a[96:99], v[156:159], 0\n"
    "ds_read_u16 v195, v195\n"
    "v_cvt_pk_bf16_f32 v99, v102, v103\n"
    "v_mfma_f32_16x16x32_bf16 v[176:179], a[144:147], v[160:163], v[176:179]\n"
    "v_cvt_pk_bf16_f32 v112, v112, v113\n"
    "v_cvt_pk_bf16_f32 v113, v114, v115\n"
    "v_mfma_f32_16x16x32_bf16 v[176:179], a[56:59], v[164:167], v[176:179]\n"
    "s_waitcnt lgkmcnt(7)\n"
    "ds_read_u16_d16_hi v203, v203\n"
    "v_or_b32_e32 v190, v190, v206\n"
    "v_mfma_f32_16x16x32_bf16 v[100:103], a[4:7], v[184:187], 0\n"
    "s_nop 0\n"
    "v_lshlrev_b32_sdwa v206, 1, v181 dst_sel:DWORD dst_unused:UNUSED_PAD src0_sel:DWORD src1_sel:WORD_1\n"
    "ds_write_b128 v242, v[96:99] offset:24640\n"
    "v_mfma_f32_16x16x32_bf16 v[96:99], a[0:3], v[184:187], 0\n"
    "s_waitcnt lgkmcnt(7)\n"
    "v_cvt_pk_bf16_f32 v114, v116, v117\n"
    "v_cvt_pk_bf16_f32 v115, v118, v119\n"
    "v_cvt_pk_bf16_f32 v128, v128, v129\n"
    "v_cvt_pk_bf16_f32 v129, v130, v131\n"
    "ds_write_b128 v242, v[112:115] offset:26688\n"
    "v_mfma_f32_16x16x32_bf16 v[112:115], a[8:11], v[184:187], 0\n"
    "s_waitcnt lgkmcnt(6)\n"
    "v_or_b32_e32 v191, v191, v207\n"
    "v_lshlrev_b32_sdwa v207, 1, v183 dst_sel:DWORD dst_unused:UNUSED_PAD src0_sel:DWORD src1_sel:WORD_1\n"
    "v_mfma_f32_16x16x32_bf16 v[180:183], a[100:103], v[156:159], 0\n"
    "ds_read_u16 v197, v197\n"
    "ds_read_u16_d16_hi v205, v205\n"
    "v_mfma_f32_16x16x32_bf16 v[180:183], a[148:151], v[160:163], v[180:183]\n"
    "ds_read_u16 v198, v198\n"
    "v_cvt_pk_bf16_f32 v130, v132, v133\n"
    "v_mfma_f32_16x16x32_bf16 v[180:183], a[60:63], v[164:167], v[180:183]\n"
    "v_or_b32_e32 v193, v193, v201\n"
    "v_cvt_pk_bf16_f32 v131, v134, v135\n"
    "v_mfma_f32_16x16x32_bf16 v[116:119], a[12:15], v[184:187], 0\n"
    "s_waitcnt lgkmcnt(7)\n"
    "ds_read_u16_d16_hi v206, v206\n"
    "v_cvt_pk_bf16_f32 v104, v104, v105\n"
    "v_mfma_f32_16x16x32_bf16 v[132:135], v[4:7], v[184:187], 0\n"
    "v_cvt_pk_bf16_f32 v105, v106, v107\n"
    "v_cvt_pk_bf16_f32 v106, v108, v109\n"
    "ds_write_b128 v242, v[128:131] offset:28736\n"
    "v_mfma_f32_16x16x32_bf16 v[128:131], v[0:3], v[184:187], 0\n"
    "s_waitcnt lgkmcnt(6)\n"
    "v_cvt_pk_bf16_f32 v107, v110, v111\n"
    "v_or_b32_e32 v194, v194, v202\n"
    "v_mfma_f32_16x16x32_bf16 v[108:111], a[4:7], v[188:191], 0\n"
    "ds_read_u16 v199, v199\n"
    "v_cvt_pk_bf16_f32 v120, v120, v121\n"
    "ds_write_b128 v242, v[104:107] offset:25664\n"
    "v_mfma_f32_16x16x32_bf16 v[104:107], a[0:3], v[188:191], 0\n"
    "v_cvt_pk_bf16_f32 v121, v122, v123\n"
    "v_cvt_pk_bf16_f32 v122, v124, v125\n"
    "v_cvt_pk_bf16_f32 v123, v126, v127\n"
    "v_cvt_pk_bf16_f32 v136, v136, v137\n"
    "ds_write_b128 v242, v[120:123] offset:27712\n"
    "v_mfma_f32_16x16x32_bf16 v[120:123], a[8:11], v[188:191], 0\n"
    "s_waitcnt lgkmcnt(6)\n"
    "v_pk_fma_f16 v232, v192, v192, s48\n"
    "v_cvt_pk_bf16_f32 v137, v138, v139\n"
    "v_mfma_f32_16x16x32_bf16 v[124:127], a[12:15], v[188:191], 0\n"
    "s_nop 0\n"
    "ds_read_u16_d16_hi v207, v207\n"
    "v_cvt_pk_bf16_f32 v138, v140, v141\n"
    "v_or_b32_e64 v195, v195, v203\n"
    "v_cvt_pk_bf16_f32 v139, v142, v143\n"
    "ds_write_b128 v242, v[136:139] offset:29760\n"
    "v_pk_mul_f16 v232, v192, v232\n"
    "v_mfma_f32_16x16x32_bf16 v[136:139], v[0:3], v[188:191], 0\n"
    "s_waitcnt lgkmcnt(5)\n"
    "v_exp_f16_e32 v234, v232\n"
    "v_mfma_f32_16x16x32_bf16 v[140:143], v[4:7], v[188:191], 0\n"
    "v_or_b32_e64 v197, v197, v205\n"
    "v_exp_f16_sdwa v234, v232 dst_sel:WORD_1 dst_unused:UNUSED_PRESERVE src0_sel:WORD_1\n"
    "v_cvt_pk_f16_f32 v184, v168, v169\n"
    "v_pk_fma_f16 v234, v234, s49, s49\n"
    "v_lshlrev_b32_sdwa v185, 1, v170 dst_sel:DWORD dst_unused:UNUSED_PAD src0_sel:DWORD src1_sel:WORD_1\n"
    "v_lshlrev_b32_sdwa v201, 1, v171 dst_sel:DWORD dst_unused:UNUSED_PAD src0_sel:DWORD src1_sel:WORD_1\n"
    "v_mfma_f32_16x16x32_bf16 v[168:171], a[104:107], v[144:147], 0\n"
    "ds_read_u16 v185, v185\n"
    "ds_read_u16_d16_hi v201, v201\n"
    "v_mfma_f32_16x16x32_bf16 v[168:171], a[152:155], v[148:151], v[168:171]\n"
    "v_lshlrev_b32_sdwa v186, 1, v172 dst_sel:DWORD dst_unused:UNUSED_PAD src0_sel:DWORD src1_sel:WORD_1\n"
    "v_lshlrev_b32_sdwa v202, 1, v173 dst_sel:DWORD dst_unused:UNUSED_PAD src0_sel:DWORD src1_sel:WORD_1\n"
    "v_lshlrev_b32_sdwa v187, 1, v174 dst_sel:DWORD dst_unused:UNUSED_PAD src0_sel:DWORD src1_sel:WORD_1\n"
    "v_lshlrev_b32_sdwa v203, 1, v175 dst_sel:DWORD dst_unused:UNUSED_PAD src0_sel:DWORD src1_sel:WORD_1\n"
    "v_rcp_f16_e64 v232, v234\n"
    "v_mfma_f32_16x16x32_bf16 v[168:171], a[64:67], v[152:155], v[168:171]\n"
    "ds_read_u16 v186, v186\n"
    "ds_read_u16_d16_hi v202, v202\n"
    "v_or_b32_e64 v198, v198, v206\n"
    "v_rcp_f16_sdwa v232, v234 dst_sel:WORD_1 dst_unused:UNUSED_PRESERVE src0_sel:WORD_1\n"
    "v_mfma_f32_16x16x32_bf16 v[172:175], a[108:111], v[144:147], 0\n"
    "s_waitcnt lgkmcnt(4)\n"
    "ds_read_u16 v187, v187\n"
    "ds_read_u16_d16_hi v203, v203\n"
    "v_mfma_f32_16x16x32_bf16 v[172:175], a[156:159], v[148:151], v[172:175]\n"
    "s_barrier\n"
    "v_cvt_pk_f16_f32 v188, v176, v177\n"
    "v_lshlrev_b32_sdwa v189, 1, v178 dst_sel:DWORD dst_unused:UNUSED_PAD src0_sel:DWORD src1_sel:WORD_1\n"
    "v_lshlrev_b32_sdwa v205, 1, v179 dst_sel:DWORD dst_unused:UNUSED_PAD src0_sel:DWORD src1_sel:WORD_1\n"
    "v_lshlrev_b32_sdwa v190, 1, v180 dst_sel:DWORD dst_unused:UNUSED_PAD src0_sel:DWORD src1_sel:WORD_1\n"
    "v_lshlrev_b32_sdwa v206, 1, v181 dst_sel:DWORD dst_unused:UNUSED_PAD src0_sel:DWORD src1_sel:WORD_1\n"
    "v_lshlrev_b32_sdwa v191, 1, v182 dst_sel:DWORD dst_unused:UNUSED_PAD src0_sel:DWORD src1_sel:WORD_1\n"
    "v_fma_mix_f32 v234, v192, v232, 0 op_sel_hi:[1,1,0]\n"
    "v_mfma_f32_16x16x32_bf16 v[172:175], a[68:71], v[152:155], v[172:175]\n"
    "ds_read_u16 v189, v189\n"
    "v_or_b32_e64 v199, v199, v207\n"
    "v_lshlrev_b32_sdwa v207, 1, v183 dst_sel:DWORD dst_unused:UNUSED_PAD src0_sel:DWORD src1_sel:WORD_1\n"
    "v_fma_mix_f32 v232, v192, v232, 0 op_sel:[1,1,0] op_sel_hi:[1,1,0]\n"
    "v_mfma_f32_16x16x32_bf16 v[176:179], a[104:107], v[156:159], 0\n"
    "s_nop 0\n"
    "ds_read_u16_d16_hi v205, v205\n"
    "v_cvt_pk_bf16_f32 v192, v234, v232\n"
    "v_mfma_f32_16x16x32_bf16 v[176:179], a[152:155], v[160:163], v[176:179]\n"
    "s_waitcnt lgkmcnt(6)\n"
    "ds_read_u16 v190, v190\n"
    "v_pk_fma_f16 v232, v196, v196, s48\n"
    "v_mfma_f32_16x16x32_bf16 v[176:179], a[64:67], v[164:167], v[176:179]\n"
    "ds_read_u16_d16_hi v206, v206\n"
    "v_pk_mul_f16 v232, v196, v232\n"
    "v_mfma_f32_16x16x32_bf16 v[180:183], a[108:111], v[156:159], 0\n"
    "ds_read_u16 v191, v191\n"
    "v_exp_f16_e32 v234, v232\n"
    "v_mfma_f32_16x16x32_bf16 v[180:183], a[156:159], v[160:163], v[180:183]\n"
    "s_waitcnt lgkmcnt(7)\n"
    "ds_read_u16_d16_hi v207, v207\n"
    "v_exp_f16_sdwa v234, v232 dst_sel:WORD_1 dst_unused:UNUSED_PRESERVE src0_sel:WORD_1\n"
    "v_mfma_f32_16x16x32_bf16 v[180:183], a[68:71], v[164:167], v[180:183]\n"
    "v_pk_fma_f16 v234, v234, s49, s49\n"
    "v_or_b32_e64 v185, v185, v201\n"
    "v_mfma_f32_16x16x32_bf16 v[96:99], a[16:19], v[192:195], v[96:99]\n"
    "v_rcp_f16_e32 v232, v234\n"
    "v_mfma_f32_16x16x32_bf16 v[100:103], a[20:23], v[192:195], v[100:103]\n"
    "v_rcp_f16_sdwa v232, v234 dst_sel:WORD_1 dst_unused:UNUSED_PRESERVE src0_sel:WORD_1\n"
    "v_mfma_f32_16x16x32_bf16 v[112:115], a[24:27], v[192:195], v[112:115]\n"
    "s_waitcnt lgkmcnt(6)\n"
    "v_fma_mix_f32 v234, v196, v232, 0 op_sel_hi:[1,1,0]\n"
    "v_fma_mix_f32 v232, v196, v232, 0 op_sel:[1,1,0] op_sel_hi:[1,1,0]\n"
    "v_mfma_f32_16x16x32_bf16 v[116:119], a[28:31], v[192:195], v[116:119]\n"
    "v_or_b32_e64 v186, v186, v202\n"
    "v_cvt_pk_bf16_f32 v196, v234, v232\n"
    "v_mfma_f32_16x16x32_bf16 v[128:131], v[8:11], v[192:195], v[128:131]\n"
    "v_pk_fma_f16 v232, v184, v184, s48\n"
    "v_or_b32_e32 v187, v187, v203\n"
    "v_mfma_f32_16x16x32_bf16 v[132:135], v[12:15], v[192:195], v[132:135]\n"
    "s_waitcnt lgkmcnt(4)\n"
    "v_pk_mul_f16 v232, v184, v232\n"
    "v_or_b32_e64 v189, v189, v205\n"
    "v_mfma_f32_16x16x32_bf16 v[104:107], a[16:19], v[196:199], v[104:107]\n"
    "v_exp_f16_e32 v234, v232\n"
    "v_mfma_f32_16x16x32_bf16 v[108:111], a[20:23], v[196:199], v[108:111]\n"
    "v_exp_f16_sdwa v234, v232 dst_sel:WORD_1 dst_unused:UNUSED_PRESERVE src0_sel:WORD_1\n"
    "v_mfma_f32_16x16x32_bf16 v[120:123], a[24:27], v[196:199], v[120:123]\n"
    "v_pk_fma_f16 v234, v234, s49, s49\n"
    "v_lshlrev_b32_sdwa v192, 1, v168 dst_sel:DWORD dst_unused:UNUSED_PAD src0_sel:DWORD src1_sel:WORD_1\n"
    "v_mfma_f32_16x16x32_bf16 v[124:127], a[28:31], v[196:199], v[124:127]\n"
    "s_waitcnt lgkmcnt(2)\n"
    "ds_read_u16 v192, v192\n"
    "v_lshlrev_b32_sdwa v200, 1, v169 dst_sel:DWORD dst_unused:UNUSED_PAD src0_sel:DWORD src1_sel:WORD_1\n"
    "v_mfma_f32_16x16x32_bf16 v[136:139], v[8:11], v[196:199], v[136:139]\n"
    "ds_read_u16_d16_hi v200, v200\n"
    "v_lshlrev_b32_sdwa v193, 1, v170 dst_sel:DWORD dst_unused:UNUSED_PAD src0_sel:DWORD src1_sel:WORD_1\n"
    "v_mfma_f32_16x16x32_bf16 v[140:143], v[12:15], v[196:199], v[140:143]\n"
    "ds_read_u16 v193, v193\n"
    "v_lshlrev_b32_sdwa v201, 1, v171 dst_sel:DWORD dst_unused:UNUSED_PAD src0_sel:DWORD src1_sel:WORD_1\n"
    "s_waitcnt vmcnt(8)\n"
    "v_smfmac_f32_32x32x32_bf16 v[112:127], a[248:251], a[232:239], v241\n"
    "s_waitcnt lgkmcnt(3)\n"
    "ds_read_u16_d16_hi v201, v201\n"
    "ds_read_b128 a[232:235], v243 offset:24640\n"
    "v_lshlrev_b32_sdwa v194, 1, v172 dst_sel:DWORD dst_unused:UNUSED_PAD src0_sel:DWORD src1_sel:WORD_1\n"
    "v_lshlrev_b32_sdwa v202, 1, v173 dst_sel:DWORD dst_unused:UNUSED_PAD src0_sel:DWORD src1_sel:WORD_1\n"
    "v_lshlrev_b32_sdwa v195, 1, v174 dst_sel:DWORD dst_unused:UNUSED_PAD src0_sel:DWORD src1_sel:WORD_1\n"
    "s_waitcnt vmcnt(7)\n"
    "v_smfmac_f32_32x32x32_bf16 v[96:111], a[248:251], a[224:231], v241\n"
    "s_nop 0\n"
    "ds_read_u16 v194, v194\n"
    "ds_read_u16_d16_hi v202, v202\n"
    "v_lshlrev_b32_sdwa v203, 1, v175 dst_sel:DWORD dst_unused:UNUSED_PAD src0_sel:DWORD src1_sel:WORD_1\n"
    "v_rcp_f16_e32 v232, v234\n"
    "s_waitcnt vmcnt(6)\n"
    "v_smfmac_f32_32x32x32_bf16 v[128:143], a[248:251], v[208:215], v241\n"
    "ds_read_b128 a[236:239], v243 offset:25664\n"
    "v_or_b32_e64 v190, v190, v206\n"
    "v_rcp_f16_sdwa v232, v234 dst_sel:WORD_1 dst_unused:UNUSED_PRESERVE src0_sel:WORD_1\n"
    "v_lshlrev_b32_sdwa v196, 1, v176 dst_sel:DWORD dst_unused:UNUSED_PAD src0_sel:DWORD src1_sel:WORD_1\n"
    "v_mfma_f32_16x16x32_bf16 v[168:171], a[112:115], v[144:147], 0\n"
    "ds_read_u16 v195, v195\n"
    "v_lshlrev_b32_sdwa v204, 1, v177 dst_sel:DWORD dst_unused:UNUSED_PAD src0_sel:DWORD src1_sel:WORD_1\n"
    "v_mfma_f32_16x16x32_bf16 v[168:171], a[160:163], v[148:151], v[168:171]\n"
    "ds_read_u16_d16_hi v203, v203\n"
    "v_lshlrev_b32_sdwa v197, 1, v178 dst_sel:DWORD dst_unused:UNUSED_PAD src0_sel:DWORD src1_sel:WORD_1\n"
    "v_mfma_f32_16x16x32_bf16 v[168:171], a[72:75], v[152:155], v[168:171]\n"
    "v_lshlrev_b32_sdwa v205, 1, v179 dst_sel:DWORD dst_unused:UNUSED_PAD src0_sel:DWORD src1_sel:WORD_1\n"
    "v_lshlrev_b32_sdwa v198, 1, v180 dst_sel:DWORD dst_unused:UNUSED_PAD src0_sel:DWORD src1_sel:WORD_1\n"
    "v_mfma_f32_16x16x32_bf16 v[172:175], a[116:119], v[144:147], 0\n"
    "v_lshlrev_b32_sdwa v206, 1, v181 dst_sel:DWORD dst_unused:UNUSED_PAD src0_sel:DWORD src1_sel:WORD_1\n"
    "v_lshlrev_b32_sdwa v199, 1, v182 dst_sel:DWORD dst_unused:UNUSED_PAD src0_sel:DWORD src1_sel:WORD_1\n"
    "v_fma_mix_f32 v234, v184, v232, 0 op_sel_hi:[1,1,0]\n"
    "v_mfma_f32_16x16x32_bf16 v[172:175], a[164:167], v[148:151], v[172:175]\n"
    "s_waitcnt lgkmcnt(7)\n"
    "ds_read_u16 v196, v196\n"
    "ds_read_u16_d16_hi v204, v204\n"
    "v_mfma_f32_16x16x32_bf16 v[172:175], v[52:55], v[152:155], v[172:175]\n"
    "v_or_b32_e32 v191, v191, v207\n"
    "v_lshlrev_b32_sdwa v207, 1, v183 dst_sel:DWORD dst_unused:UNUSED_PAD src0_sel:DWORD src1_sel:WORD_1\n"
    "v_fma_mix_f32 v232, v184, v232, 0 op_sel:[1,1,0] op_sel_hi:[1,1,0]\n"
    "v_mfma_f32_16x16x32_bf16 v[176:179], a[112:115], v[156:159], 0\n"
    "ds_read_u16 v197, v197\n"
    "v_cvt_pk_bf16_f32 v184, v234, v232\n"
    "v_mfma_f32_16x16x32_bf16 v[176:179], a[160:163], v[160:163], v[176:179]\n"
    "ds_read_u16_d16_hi v205, v205\n"
    "v_pk_fma_f16 v232, v188, v188, s48\n"
    "v_mfma_f32_16x16x32_bf16 v[176:179], a[72:75], v[164:167], v[176:179]\n"
    "s_waitcnt lgkmcnt(8)\n"
    "v_pk_mul_f16 v232, v188, v232\n"
    "v_or_b32_e32 v192, v192, v200\n"
    "v_mfma_f32_16x16x32_bf16 v[180:183], a[116:119], v[156:159], 0\n"
    "ds_read_u16 v198, v198\n"
    "v_exp_f16_e32 v234, v232\n"
    "s_waitcnt lgkmcnt(7)\n"
    "v_smfmac_f32_32x32x32_bf16 v[96:111], a[248:251], a[232:239], v241\n"
    "ds_read_u16_d16_hi v206, v206\n"
    "v_exp_f16_sdwa v234, v232 dst_sel:WORD_1 dst_unused:UNUSED_PRESERVE src0_sel:WORD_1\n"
    "v_or_b32_e64 v193, v193, v201\n"
    "v_pk_fma_f16 v234, v234, s49, s49\n"
    "v_mfma_f32_16x16x32_bf16 v[180:183], a[164:167], v[160:163], v[180:183]\n"
    "ds_read_u16 v199, v199\n"
    "v_rcp_f16_e32 v232, v234\n"
    "v_mfma_f32_16x16x32_bf16 v[180:183], v[52:55], v[164:167], v[180:183]\n"
    "v_rcp_f16_sdwa v232, v234 dst_sel:WORD_1 dst_unused:UNUSED_PRESERVE src0_sel:WORD_1\n"
    "v_mfma_f32_16x16x32_bf16 v[112:115], a[40:43], v[184:187], v[112:115]\n"
    "s_waitcnt lgkmcnt(7)\n"
    "ds_read_u16_d16_hi v207, v207\n"
    "v_fma_mix_f32 v234, v188, v232, 0 op_sel_hi:[1,1,0]\n"
    "v_mfma_f32_16x16x32_bf16 v[96:99], a[32:35], v[184:187], v[96:99]\n"
    "ds_read_b128 a[232:235], v243 offset:26688\n"
    "v_fma_mix_f32 v232, v188, v232, 0 op_sel:[1,1,0] op_sel_hi:[1,1,0]\n"
    "v_mfma_f32_16x16x32_bf16 v[100:103], a[36:39], v[184:187], v[100:103]\n"
    "ds_read_b128 a[236:239], v243 offset:27712\n"
    "v_cvt_pk_bf16_f32 v188, v234, v232\n"
    "v_mfma_f32_16x16x32_bf16 v[116:119], a[44:47], v[184:187], v[116:119]\n"
    "s_waitcnt lgkmcnt(8)\n"
    "v_lshlrev_b32_sdwa v201, 1, v171 dst_sel:DWORD dst_unused:UNUSED_PAD src0_sel:DWORD src1_sel:WORD_1\n"
    "v_or_b32_e32 v194, v194, v202\n"
    "v_mfma_f32_16x16x32_bf16 v[128:131], v[16:19], v[184:187], v[128:131]\n"
    "ds_read_u16_d16_hi v201, v201\n"
    "v_lshlrev_b32_sdwa v202, 1, v173 dst_sel:DWORD dst_unused:UNUSED_PAD src0_sel:DWORD src1_sel:WORD_1\n"
    "v_mfma_f32_16x16x32_bf16 v[132:135], v[20:23], v[184:187], v[132:135]\n"
    "ds_read_u16_d16_hi v202, v202\n"
    "v_cvt_pk_f16_f32 v184, v168, v169\n"
    "v_mfma_f32_16x16x32_bf16 v[104:107], a[32:35], v[188:191], v[104:107]\n"
    "v_lshlrev_b32_sdwa v185, 1, v170 dst_sel:DWORD dst_unused:UNUSED_PAD src0_sel:DWORD src1_sel:WORD_1\n"
    "v_lshlrev_b32_sdwa v186, 1, v172 dst_sel:DWORD dst_unused:UNUSED_PAD src0_sel:DWORD src1_sel:WORD_1\n"
    "v_mfma_f32_16x16x32_bf16 v[108:111], a[36:39], v[188:191], v[108:111]\n"
    "s_waitcnt lgkmcnt(8)\n"
    "ds_read_u16 v185, v185\n"
    "v_lshlrev_b32_sdwa v187, 1, v174 dst_sel:DWORD dst_unused:UNUSED_PAD src0_sel:DWORD src1_sel:WORD_1\n"
    "v_mfma_f32_16x16x32_bf16 v[120:123], a[40:43], v[188:191], v[120:123]\n"
    "v_or_b32_e32 v195, v195, v203\n"
    "v_lshlrev_b32_sdwa v203, 1, v175 dst_sel:DWORD dst_unused:UNUSED_PAD src0_sel:DWORD src1_sel:WORD_1\n"
    "v_mfma_f32_16x16x32_bf16 v[124:127], a[44:47], v[188:191], v[124:127]\n"
    "ds_read_u16 v186, v186\n"
    "v_or_b32_e32 v196, v196, v204\n"
    "v_mfma_f32_16x16x32_bf16 v[136:139], v[16:19], v[188:191], v[136:139]\n"
    "s_waitcnt lgkmcnt(8)\n"
    "ds_read_u16 v187, v187\n"
    "v_or_b32_e64 v197, v197, v205\n"
    "v_mfma_f32_16x16x32_bf16 v[140:143], v[20:23], v[188:191], v[140:143]\n"
    "v_cvt_pk_f16_f32 v188, v176, v177\n"
    "v_lshlrev_b32_sdwa v189, 1, v178 dst_sel:DWORD dst_unused:UNUSED_PAD src0_sel:DWORD src1_sel:WORD_1\n"
    "v_mfma_f32_16x16x32_bf16 v[168:171], a[120:123], v[144:147], 0\n"
    "ds_read_u16_d16_hi v203, v203\n"
    "v_lshlrev_b32_sdwa v205, 1, v179 dst_sel:DWORD dst_unused:UNUSED_PAD src0_sel:DWORD src1_sel:WORD_1\n"
    "v_mfma_f32_16x16x32_bf16 v[168:171], a[168:171], v[148:151], v[168:171]\n"
    "v_lshlrev_b32_sdwa v190, 1, v180 dst_sel:DWORD dst_unused:UNUSED_PAD src0_sel:DWORD src1_sel:WORD_1\n"
    "v_lshlrev_b32_sdwa v191, 1, v182 dst_sel:DWORD dst_unused:UNUSED_PAD src0_sel:DWORD src1_sel:WORD_1\n"
    "v_mfma_f32_16x16x32_bf16 v[168:171], a[76:79], v[152:155], v[168:171]\n"
    "s_waitcnt lgkmcnt(6)\n"
    "ds_read_u16 v189, v189\n"
    "v_or_b32_e32 v198, v198, v206\n"
    "v_smfmac_f32_32x32x32_bf16 v[112:127], a[248:251], a[232:239], v241\n"
    "ds_read_b128 a[232:235], v243 offset:28736\n"
    "v_lshlrev_b32_sdwa v206, 1, v181 dst_sel:DWORD dst_unused:UNUSED_PAD src0_sel:DWORD src1_sel:WORD_1\n"
    "v_or_b32_e64 v199, v199, v207\n"
    "v_lshlrev_b32_sdwa v207, 1, v183 dst_sel:DWORD dst_unused:UNUSED_PAD src0_sel:DWORD src1_sel:WORD_1\n"
    "v_pk_fma_f16 v232, v184, v184, s48\n"
    "v_mfma_f32_16x16x32_bf16 v[172:175], a[124:127], v[144:147], 0\n"
    "ds_read_u16_d16_hi v205, v205\n"
    "v_pk_mul_f16 v232, v184, v232\n"
    "v_mfma_f32_16x16x32_bf16 v[172:175], a[172:175], v[148:151], v[172:175]\n"
    "ds_read_b128 a[236:239], v243 offset:29760\n"
    "v_exp_f16_e64 v234, v232\n"
    "v_mfma_f32_16x16x32_bf16 v[172:175], v[60:63], v[152:155], v[172:175]\n"
    "ds_read_u16 v190, v190\n"
    "v_exp_f16_sdwa v234, v232 dst_sel:WORD_1 dst_unused:UNUSED_PRESERVE src0_sel:WORD_1\n"
    "v_mfma_f32_16x16x32_bf16 v[176:179], a[120:123], v[156:159], 0\n"
    "s_waitcnt lgkmcnt(8)\n"
    "v_or_b32_e32 v185, v185, v201\n"
    "v_pk_fma_f16 v234, v234, s49, s49\n"
    "v_mfma_f32_16x16x32_bf16 v[176:179], a[168:171], v[160:163], v[176:179]\n"
    "ds_read_b128 v[144:147], v242 offset:0\n"
    "ds_read_u16_d16_hi v206, v206\n"
    "v_mfma_f32_16x16x32_bf16 v[176:179], a[76:79], v[164:167], v[176:179]\n"
    "s_waitcnt lgkmcnt(9)\n"
    "v_or_b32_e32 v186, v186, v202\n"
    "v_lshlrev_b32_sdwa v201, 1, v171 dst_sel:DWORD dst_unused:UNUSED_PAD src0_sel:DWORD src1_sel:WORD_1\n"
    "v_mfma_f32_16x16x32_bf16 v[180:183], a[124:127], v[156:159], 0\n"
    "ds_read_b128 v[148:151], v242 offset:1024\n"
    "v_lshlrev_b32_sdwa v202, 1, v173 dst_sel:DWORD dst_unused:UNUSED_PAD src0_sel:DWORD src1_sel:WORD_1\n"
    "v_mfma_f32_16x16x32_bf16 v[180:183], a[172:175], v[160:163], v[180:183]\n"
    "ds_read_u16 v191, v191\n"
    "v_rcp_f16_e64 v232, v234\n"
    "v_mfma_f32_16x16x32_bf16 v[180:183], v[60:63], v[164:167], v[180:183]\n"
    "s_waitcnt lgkmcnt(9)\n"
    "v_or_b32_e32 v187, v187, v203\n"
    "v_lshlrev_b32_sdwa v203, 1, v175 dst_sel:DWORD dst_unused:UNUSED_PAD src0_sel:DWORD src1_sel:WORD_1\n"
    "v_mfma_f32_16x16x32_bf16 v[96:99], a[176:179], v[192:195], v[96:99]\n"
    "ds_read_b128 v[152:155], v242 offset:2048\n"
    "v_rcp_f16_sdwa v232, v234 dst_sel:WORD_1 dst_unused:UNUSED_PRESERVE src0_sel:WORD_1\n"
    "v_mfma_f32_16x16x32_bf16 v[100:103], a[180:183], v[192:195], v[100:103]\n"
    "ds_read_u16_d16_hi v207, v207\n"
    "v_fma_mix_f32 v234, v184, v232, 0 op_sel_hi:[1,1,0]\n"
    "v_mfma_f32_16x16x32_bf16 v[112:115], a[184:187], v[192:195], v[112:115]\n"
    "ds_read_b128 v[156:159], v242 offset:3072\n"
    "v_fma_mix_f32 v232, v184, v232, 0 op_sel:[1,1,0] op_sel_hi:[1,1,0]\n"
    "v_mfma_f32_16x16x32_bf16 v[116:119], a[188:191], v[192:195], v[116:119]\n"
    "ds_read_b128 v[160:163], v242 offset:4096\n"
    "s_waitcnt lgkmcnt(9)\n"
    "v_or_b32_e32 v189, v189, v205\n"
    "v_mfma_f32_16x16x32_bf16 v[128:131], v[24:27], v[192:195], v[128:131]\n"
    "ds_read_b128 v[164:167], v242 offset:5120\n"
    "v_lshlrev_b32_sdwa v205, 1, v179 dst_sel:DWORD dst_unused:UNUSED_PAD src0_sel:DWORD src1_sel:WORD_1\n"
    "v_mfma_f32_16x16x32_bf16 v[132:135], v[28:31], v[192:195], v[132:135]\n"
    "ds_read_u16_d16_hi v201, v201\n"
    "v_cvt_pk_f16_f32 v192, v168, v169\n"
    "v_mfma_f32_16x16x32_bf16 v[104:107], a[176:179], v[196:199], v[104:107]\n"
    "s_nop 0\n"
    "v_lshlrev_b32_sdwa v193, 1, v170 dst_sel:DWORD dst_unused:UNUSED_PAD src0_sel:DWORD src1_sel:WORD_1\n"
    "v_lshlrev_b32_sdwa v194, 1, v172 dst_sel:DWORD dst_unused:UNUSED_PAD src0_sel:DWORD src1_sel:WORD_1\n"
    "v_mfma_f32_16x16x32_bf16 v[108:111], a[180:183], v[196:199], v[108:111]\n"
    "s_waitcnt lgkmcnt(9)\n"
    "ds_read_u16 v193, v193\n"
    "v_lshlrev_b32_sdwa v195, 1, v174 dst_sel:DWORD dst_unused:UNUSED_PAD src0_sel:DWORD src1_sel:WORD_1\n"
    "v_mfma_f32_16x16x32_bf16 v[120:123], a[184:187], v[196:199], v[120:123]\n"
    "v_cvt_pk_bf16_f32 v184, v234, v232\n"
    "v_pk_fma_f16 v232, v188, v188, s48\n"
    "v_mfma_f32_16x16x32_bf16 v[124:127], a[188:191], v[196:199], v[124:127]\n"
    "s_waitcnt lgkmcnt(8)\n"
    "ds_read_u16 v194, v194\n"
    "v_or_b32_e32 v190, v190, v206\n"
    "v_mfma_f32_16x16x32_bf16 v[136:139], v[24:27], v[196:199], v[136:139]\n"
    "ds_read_u16_d16_hi v202, v202\n"
    "v_lshlrev_b32_sdwa v206, 1, v181 dst_sel:DWORD dst_unused:UNUSED_PAD src0_sel:DWORD src1_sel:WORD_1\n"
    "v_mfma_f32_16x16x32_bf16 v[140:143], v[28:31], v[196:199], v[140:143]\n"
    "v_cvt_pk_f16_f32 v196, v176, v177\n"
    "v_lshlrev_b32_sdwa v197, 1, v178 dst_sel:DWORD dst_unused:UNUSED_PAD src0_sel:DWORD src1_sel:WORD_1\n"
    "v_mfma_f32_16x16x32_bf16 v[168:171], a[80:83], v[144:147], 0\n"
    "ds_read_u16 v195, v195\n"
    "v_lshlrev_b32_sdwa v198, 1, v180 dst_sel:DWORD dst_unused:UNUSED_PAD src0_sel:DWORD src1_sel:WORD_1\n"
    "v_mfma_f32_16x16x32_bf16 v[168:171], a[128:131], v[148:151], v[168:171]\n"
    "s_waitcnt lgkmcnt(9)\n"
    "v_lshlrev_b32_sdwa v199, 1, v182 dst_sel:DWORD dst_unused:UNUSED_PAD src0_sel:DWORD src1_sel:WORD_1\n"
    "v_pk_mul_f16 v232, v188, v232\n"
    "v_smfmac_f32_32x32x32_bf16 v[128:143], a[248:251], a[232:239], v241\n"
    "s_waitcnt lgkmcnt(7)\n"
    "ds_read_u16_d16_hi v203, v203\n"
    "v_or_b32_e64 v191, v191, v207\n"
    "v_lshlrev_b32_sdwa v207, 1, v183 dst_sel:DWORD dst_unused:UNUSED_PAD src0_sel:DWORD src1_sel:WORD_1\n"
    "v_exp_f16_e64 v234, v232\n"
    "v_mfma_f32_16x16x32_bf16 v[168:171], a[48:51], v[152:155], v[168:171]\n"
    "ds_read_u16 v197, v197\n"
    "v_exp_f16_sdwa v234, v232 dst_sel:WORD_1 dst_unused:UNUSED_PRESERVE src0_sel:WORD_1\n"
    "v_mfma_f32_16x16x32_bf16 v[172:175], a[84:87], v[144:147], 0\n"
    "ds_read_u16_d16_hi v205, v205\n"
    "v_pk_fma_f16 v234, v234, s49, s49\n"
    "v_mfma_f32_16x16x32_bf16 v[172:175], a[132:135], v[148:151], v[172:175]\n"
    "v_rcp_f16_e64 v232, v234\n"
    "v_mfma_f32_16x16x32_bf16 v[172:175], v[48:51], v[152:155], v[172:175]\n"
    "ds_read_u16 v198, v198\n"
    "v_rcp_f16_sdwa v232, v234 dst_sel:WORD_1 dst_unused:UNUSED_PRESERVE src0_sel:WORD_1\n"
    "v_mfma_f32_16x16x32_bf16 v[176:179], a[80:83], v[156:159], 0\n"
    "s_waitcnt lgkmcnt(7)\n"
    "ds_read_u16_d16_hi v206, v206\n"
    "v_or_b32_e32 v193, v193, v201\n"
    "v_mfma_f32_16x16x32_bf16 v[176:179], a[128:131], v[160:163], v[176:179]\n"
    "v_fma_mix_f32 v234, v188, v232, 0 op_sel_hi:[1,1,0]\n"
    "v_fma_mix_f32 v232, v188, v232, 0 op_sel:[1,1,0] op_sel_hi:[1,1,0]\n"
    "v_mfma_f32_16x16x32_bf16 v[176:179], a[48:51], v[164:167], v[176:179]\n"
    "ds_read_u16 v199, v199\n"
    "v_cvt_pk_bf16_f32 v188, v234, v232\n"
    "v_mfma_f32_16x16x32_bf16 v[180:183], a[84:87], v[156:159], 0\n"
    "v_pk_fma_f16 v232, v192, v192, s48\n"
    "v_lshlrev_b32_sdwa v200, 1, v169 dst_sel:DWORD dst_unused:UNUSED_PAD src0_sel:DWORD src1_sel:WORD_1\n"
    "v_mfma_f32_16x16x32_bf16 v[180:183], a[132:135], v[160:163], v[180:183]\n"
    "s_waitcnt lgkmcnt(7)\n"
    "ds_read_u16_d16_hi v207, v207\n"
    "v_or_b32_e32 v194, v194, v202\n"
    "v_mfma_f32_16x16x32_bf16 v[180:183], v[48:51], v[164:167], v[180:183]\n"
    "v_pk_mul_f16 v232, v192, v232\n"
    "v_lshlrev_b32_sdwa v201, 1, v171 dst_sel:DWORD dst_unused:UNUSED_PAD src0_sel:DWORD src1_sel:WORD_1\n"
    "v_mfma_f32_16x16x32_bf16 v[96:99], a[192:195], v[184:187], v[96:99]\n"
    "ds_read_u16_d16_hi v200, v200\n"
    "v_exp_f16_e32 v234, v232\n"
    "v_mfma_f32_16x16x32_bf16 v[100:103], a[196:199], v[184:187], v[100:103]\n"
    "s_waitcnt lgkmcnt(7)\n"
    "ds_read_u16_d16_hi v201, v201\n"
    "v_or_b32_e64 v195, v195, v203\n"
    "v_mfma_f32_16x16x32_bf16 v[112:115], a[200:203], v[184:187], v[112:115]\n"
    "v_exp_f16_sdwa v234, v232 dst_sel:WORD_1 dst_unused:UNUSED_PRESERVE src0_sel:WORD_1\n"
    "v_mfma_f32_16x16x32_bf16 v[116:119], a[204:207], v[184:187], v[116:119]\n"
    "v_pk_fma_f16 v234, v234, s49, s49\n"
    "v_lshlrev_b32_sdwa v202, 1, v173 dst_sel:DWORD dst_unused:UNUSED_PAD src0_sel:DWORD src1_sel:WORD_1\n"
    "v_mfma_f32_16x16x32_bf16 v[128:131], v[32:35], v[184:187], v[128:131]\n"
    "ds_read_u16_d16_hi v202, v202\n"
    "s_waitcnt lgkmcnt(7)\n"
    "v_or_b32_e32 v197, v197, v205\n"
    "v_mfma_f32_16x16x32_bf16 v[132:135], v[36:39], v[184:187], v[132:135]\n"
    "v_lshlrev_b32_sdwa v184, 1, v168 dst_sel:DWORD dst_unused:UNUSED_PAD src0_sel:DWORD src1_sel:WORD_1\n"
    "v_lshlrev_b32_sdwa v185, 1, v170 dst_sel:DWORD dst_unused:UNUSED_PAD src0_sel:DWORD src1_sel:WORD_1\n"
    "v_mfma_f32_16x16x32_bf16 v[104:107], a[192:195], v[188:191], v[104:107]\n"
    "ds_read_u16 v184, v184\n"
    "ds_read_u16 v185, v185\n"
    "v_mfma_f32_16x16x32_bf16 v[108:111], a[196:199], v[188:191], v[108:111]\n"
    "v_lshlrev_b32_sdwa v186, 1, v172 dst_sel:DWORD dst_unused:UNUSED_PAD src0_sel:DWORD src1_sel:WORD_1\n"
    "v_lshlrev_b32_sdwa v187, 1, v174 dst_sel:DWORD dst_unused:UNUSED_PAD src0_sel:DWORD src1_sel:WORD_1\n"
    "v_mfma_f32_16x16x32_bf16 v[120:123], a[200:203], v[188:191], v[120:123]\n"
    "s_waitcnt lgkmcnt(7)\n"
    "ds_read_u16 v186, v186\n"
    "v_lshlrev_b32_sdwa v203, 1, v175 dst_sel:DWORD dst_unused:UNUSED_PAD src0_sel:DWORD src1_sel:WORD_1\n"
    "v_mfma_f32_16x16x32_bf16 v[124:127], a[204:207], v[188:191], v[124:127]\n"
    "v_rcp_f16_e32 v232, v234\n"
    "v_mfma_f32_16x16x32_bf16 v[136:139], v[32:35], v[188:191], v[136:139]\n"
    "ds_read_u16 v187, v187\n"
    "v_or_b32_e32 v198, v198, v206\n"
    "v_mfma_f32_16x16x32_bf16 v[140:143], v[36:39], v[188:191], v[140:143]\n"
    "s_waitcnt lgkmcnt(7)\n"
    "v_rcp_f16_sdwa v232, v234 dst_sel:WORD_1 dst_unused:UNUSED_PRESERVE src0_sel:WORD_1\n"
    "v_mfma_f32_16x16x32_bf16 v[168:171], a[88:91], v[144:147], 0\n"
    "ds_read_u16_d16_hi v203, v203\n"
    "v_lshlrev_b32_sdwa v188, 1, v176 dst_sel:DWORD dst_unused:UNUSED_PAD src0_sel:DWORD src1_sel:WORD_1\n"
    "v_mfma_f32_16x16x32_bf16 v[168:171], a[136:139], v[148:151], v[168:171]\n"
    "v_lshlrev_b32_sdwa v204, 1, v177 dst_sel:DWORD dst_unused:UNUSED_PAD src0_sel:DWORD src1_sel:WORD_1\n"
    "v_lshlrev_b32_sdwa v189, 1, v178 dst_sel:DWORD dst_unused:UNUSED_PAD src0_sel:DWORD src1_sel:WORD_1\n"
    "v_mfma_f32_16x16x32_bf16 v[168:171], a[52:55], v[152:155], v[168:171]\n"
    "v_lshlrev_b32_sdwa v205, 1, v179 dst_sel:DWORD dst_unused:UNUSED_PAD src0_sel:DWORD src1_sel:WORD_1\n"
    "v_lshlrev_b32_sdwa v190, 1, v180 dst_sel:DWORD dst_unused:UNUSED_PAD src0_sel:DWORD src1_sel:WORD_1\n"
    "v_mfma_f32_16x16x32_bf16 v[172:175], a[92:95], v[144:147], 0\n"
    "v_lshlrev_b32_sdwa v206, 1, v181 dst_sel:DWORD dst_unused:UNUSED_PAD src0_sel:DWORD src1_sel:WORD_1\n"
    "v_lshlrev_b32_sdwa v191, 1, v182 dst_sel:DWORD dst_unused:UNUSED_PAD src0_sel:DWORD src1_sel:WORD_1\n"
    "v_fma_mix_f32 v234, v192, v232, 0 op_sel_hi:[1,1,0]\n"
    "v_mfma_f32_16x16x32_bf16 v[172:175], a[140:143], v[148:151], v[172:175]\n"
    "ds_read_u16 v188, v188\n"
    "ds_read_u16_d16_hi v204, v204\n"
    "v_mfma_f32_16x16x32_bf16 v[172:175], v[56:59], v[152:155], v[172:175]\n"
    "v_or_b32_e64 v199, v199, v207\n"
    "v_lshlrev_b32_sdwa v207, 1, v183 dst_sel:DWORD dst_unused:UNUSED_PAD src0_sel:DWORD src1_sel:WORD_1\n"
    "v_fma_mix_f32 v232, v192, v232, 0 op_sel:[1,1,0] op_sel_hi:[1,1,0]\n"
    "v_mfma_f32_16x16x32_bf16 v[176:179], a[88:91], v[156:159], 0\n"
    "s_waitcnt lgkmcnt(6)\n"
    "ds_read_u16 v189, v189\n"
    "v_cvt_pk_bf16_f32 v192, v234, v232\n"
    "v_mfma_f32_16x16x32_bf16 v[176:179], a[136:139], v[160:163], v[176:179]\n"
    "ds_read_u16_d16_hi v205, v205\n"
    "v_pk_fma_f16 v232, v196, v196, s48\n"
    "v_mfma_f32_16x16x32_bf16 v[176:179], a[52:55], v[164:167], v[176:179]\n"
    "s_waitcnt lgkmcnt(7)\n"
    "ds_read_u16 v190, v190\n"
    "v_pk_mul_f16 v232, v196, v232\n"
    "v_mfma_f32_16x16x32_bf16 v[180:183], a[92:95], v[156:159], 0\n"
    "ds_read_u16_d16_hi v206, v206\n"
    "v_exp_f16_e32 v234, v232\n"
    "v_mfma_f32_16x16x32_bf16 v[180:183], a[140:143], v[160:163], v[180:183]\n"
    "s_waitcnt lgkmcnt(8)\n"
    "buffer_load_dwordx4 a[232:235], v245, s[40:43], s58 offen offset:64\n"
    "buffer_load_dwordx4 a[224:227], v245, s[40:43], s58 offen offset:0\n"
    "buffer_load_dwordx4 v[208:211], v245, s[40:43], s58 offen offset:128\n"
    "ds_read_u16 v191, v191\n"
    "v_mfma_f32_16x16x32_bf16 v[180:183], v[56:59], v[164:167], v[180:183]\n"
    "v_exp_f16_sdwa v234, v232 dst_sel:WORD_1 dst_unused:UNUSED_PRESERVE src0_sel:WORD_1\n"
    "v_or_b32_e64 v184, v184, v200\n"
    "v_pk_fma_f16 v234, v234, s49, s49\n"
    "v_mfma_f32_16x16x32_bf16 v[96:99], a[208:211], v[192:195], v[96:99]\n"
    "s_waitcnt lgkmcnt(7)\n"
    "ds_read_u16_d16_hi v207, v207\n"
    "v_rcp_f16_e32 v232, v234\n"
    "v_mfma_f32_16x16x32_bf16 v[100:103], a[212:215], v[192:195], v[100:103]\n"
    "v_rcp_f16_sdwa v232, v234 dst_sel:WORD_1 dst_unused:UNUSED_PRESERVE src0_sel:WORD_1\n"
    "v_mfma_f32_16x16x32_bf16 v[112:115], a[216:219], v[192:195], v[112:115]\n"
    "v_fma_mix_f32 v234, v196, v232, 0 op_sel_hi:[1,1,0]\n"
    "v_fma_mix_f32 v232, v196, v232, 0 op_sel:[1,1,0] op_sel_hi:[1,1,0]\n"
    "v_mfma_f32_16x16x32_bf16 v[116:119], a[220:223], v[192:195], v[116:119]\n"
    "s_waitcnt lgkmcnt(6)\n"
    "v_cvt_pk_bf16_f32 v196, v234, v232\n"
    "v_or_b32_e32 v185, v185, v201\n"
    "v_mfma_f32_16x16x32_bf16 v[128:131], v[40:43], v[192:195], v[128:131]\n"
    "v_lshlrev_b32_sdwa v201, 1, v171 dst_sel:DWORD dst_unused:UNUSED_PAD src0_sel:DWORD src1_sel:WORD_1\n"
    "v_or_b32_e32 v186, v186, v202\n"
    "v_mfma_f32_16x16x32_bf16 v[132:135], v[44:47], v[192:195], v[132:135]\n"
    "ds_read_u16_d16_hi v201, v201\n"
    "v_cvt_pk_f16_f32 v192, v168, v169\n"
    "v_mfma_f32_16x16x32_bf16 v[104:107], a[208:211], v[196:199], v[104:107]\n"
    "s_waitcnt lgkmcnt(5)\n"
    "v_lshlrev_b32_sdwa v193, 1, v170 dst_sel:DWORD dst_unused:UNUSED_PAD src0_sel:DWORD src1_sel:WORD_1\n"
    "v_lshlrev_b32_sdwa v194, 1, v172 dst_sel:DWORD dst_unused:UNUSED_PAD src0_sel:DWORD src1_sel:WORD_1\n"
    "v_mfma_f32_16x16x32_bf16 v[108:111], a[212:215], v[196:199], v[108:111]\n"
    "ds_read_u16 v193, v193\n"
    "buffer_load_dwordx4 a[236:239], v245, s[40:43], s89 offen offset:64\n"
    "buffer_load_dwordx4 a[228:231], v245, s[40:43], s89 offen offset:0\n"
    "buffer_load_dwordx4 v[212:215], v245, s[40:43], s89 offen offset:128\n"
    "v_mfma_f32_16x16x32_bf16 v[120:123], a[216:219], v[196:199], v[120:123]\n"
    "s_nop 0\n"
    "ds_read_u16 v194, v194\n"
    "v_lshlrev_b32_sdwa v202, 1, v173 dst_sel:DWORD dst_unused:UNUSED_PAD src0_sel:DWORD src1_sel:WORD_1\n"
    "v_mfma_f32_16x16x32_bf16 v[124:127], a[220:223], v[196:199], v[124:127]\n"
    "s_waitcnt lgkmcnt(5)\n"
    "ds_read_u16_d16_hi v202, v202\n"
    "v_lshlrev_b32_sdwa v195, 1, v174 dst_sel:DWORD dst_unused:UNUSED_PAD src0_sel:DWORD src1_sel:WORD_1\n"
    "v_mfma_f32_16x16x32_bf16 v[136:139], v[40:43], v[196:199], v[136:139]\n"
    "ds_read_u16 v195, v195\n"
    "v_or_b32_e64 v187, v187, v203\n"
    "v_mfma_f32_16x16x32_bf16 v[140:143], v[44:47], v[196:199], v[140:143]\n"
    "v_lshlrev_b32_sdwa v203, 1, v175 dst_sel:DWORD dst_unused:UNUSED_PAD src0_sel:DWORD src1_sel:WORD_1\n"
    "v_cvt_pk_f16_f32 v196, v176, v177\n"
    "v_mfma_f32_16x16x32_bf16 v[168:171], a[96:99], v[144:147], 0\n"
    "s_nop 0\n"
    "ds_read_u16_d16_hi v203, v203\n"
    "v_lshlrev_b32_sdwa v197, 1, v178 dst_sel:DWORD dst_unused:UNUSED_PAD src0_sel:DWORD src1_sel:WORD_1\n"
    "v_mfma_f32_16x16x32_bf16 v[168:171], a[144:147], v[148:151], v[168:171]\n"
    "s_waitcnt lgkmcnt(6)\n"
    "ds_read_u16 v197, v197\n"
    "v_lshlrev_b32_sdwa v198, 1, v180 dst_sel:DWORD dst_unused:UNUSED_PAD src0_sel:DWORD src1_sel:WORD_1\n"
    "v_mfma_f32_16x16x32_bf16 v[168:171], a[56:59], v[152:155], v[168:171]\n"
    "v_lshlrev_b32_sdwa v199, 1, v182 dst_sel:DWORD dst_unused:UNUSED_PAD src0_sel:DWORD src1_sel:WORD_1\n"
    "v_or_b32_e64 v188, v188, v204\n"
    "v_mfma_f32_16x16x32_bf16 v[172:175], a[100:103], v[144:147], 0\n"
    "ds_read_u16 v198, v198\n"
    "v_or_b32_e32 v189, v189, v205\n"
    "v_mfma_f32_16x16x32_bf16 v[172:175], a[148:151], v[148:151], v[172:175]\n"
    "ds_read_u16 v199, v199\n"
    "v_lshlrev_b32_sdwa v205, 1, v179 dst_sel:DWORD dst_unused:UNUSED_PAD src0_sel:DWORD src1_sel:WORD_1\n"
    "v_mfma_f32_16x16x32_bf16 v[172:175], a[60:63], v[152:155], v[172:175]\n"
    "s_waitcnt lgkmcnt(7)\n"
    "ds_read_u16_d16_hi v205, v205\n"
    "v_cvt_pk_bf16_f32 v96, v96, v97\n"
    "v_mfma_f32_16x16x32_bf16 v[176:179], a[96:99], v[156:159], 0\n"
    "v_cvt_pk_bf16_f32 v97, v98, v99\n"
    "v_cvt_pk_bf16_f32 v98, v100, v101\n"
    "v_mfma_f32_16x16x32_bf16 v[176:179], a[144:147], v[160:163], v[176:179]\n"
    "v_cvt_pk_bf16_f32 v99, v102, v103\n"
    "v_or_b32_e32 v190, v190, v206\n"
    "v_mfma_f32_16x16x32_bf16 v[176:179], a[56:59], v[164:167], v[176:179]\n"
    "s_waitcnt lgkmcnt(6)\n"
    "ds_write_b128 v243, v[96:99] offset:24640\n"
    "v_mfma_f32_16x16x32_bf16 v[96:99], a[0:3], v[184:187], 0\n"
    "v_lshlrev_b32_sdwa v206, 1, v181 dst_sel:DWORD dst_unused:UNUSED_PAD src0_sel:DWORD src1_sel:WORD_1\n"
    "v_cvt_pk_bf16_f32 v112, v112, v113\n"
    "v_mfma_f32_16x16x32_bf16 v[100:103], a[4:7], v[184:187], 0\n"
    "v_cvt_pk_bf16_f32 v113, v114, v115\n"
    "v_cvt_pk_bf16_f32 v114, v116, v117\n"
    "v_cvt_pk_bf16_f32 v115, v118, v119\n"
    "ds_write_b128 v243, v[112:115] offset:26688\n"
    "v_mfma_f32_16x16x32_bf16 v[112:115], a[8:11], v[184:187], 0\n"
    "s_waitcnt lgkmcnt(5)\n"
    "v_cvt_pk_bf16_f32 v128, v128, v129\n"
    "v_cvt_pk_bf16_f32 v129, v130, v131\n"
    "v_or_b32_e32 v191, v191, v207\n"
    "v_mfma_f32_16x16x32_bf16 v[116:119], a[12:15], v[184:187], 0\n"
    "ds_read_b128 a[252:255], v249 offset:0\n"
    "ds_read_u16_d16_hi v206, v206\n"
    "v_lshlrev_b32_sdwa v207, 1, v183 dst_sel:DWORD dst_unused:UNUSED_PAD src0_sel:DWORD src1_sel:WORD_1\n"
    "v_mfma_f32_16x16x32_bf16 v[180:183], a[100:103], v[156:159], 0\n"
    "ds_read_u16_d16_hi v207, v207\n"
    "v_cvt_pk_bf16_f32 v130, v132, v133\n"
    "v_mfma_f32_16x16x32_bf16 v[180:183], a[148:151], v[160:163], v[180:183]\n"
    "v_or_b32_e64 v193, v193, v201\n"
    "v_cvt_pk_bf16_f32 v131, v134, v135\n"
    "v_mfma_f32_16x16x32_bf16 v[180:183], a[60:63], v[164:167], v[180:183]\n"
    "s_nop 0\n"
    "ds_write_b128 v243, v[128:131] offset:28736\n"
    "v_mfma_f32_16x16x32_bf16 v[128:131], v[0:3], v[184:187], 0\n"
    "s_waitcnt lgkmcnt(5)\n"
    "v_cvt_pk_bf16_f32 v104, v104, v105\n"
    "v_cvt_pk_bf16_f32 v105, v106, v107\n"
    "v_mfma_f32_16x16x32_bf16 v[132:135], v[4:7], v[184:187], 0\n"
    "v_cvt_pk_bf16_f32 v106, v108, v109\n"
    "v_cvt_pk_bf16_f32 v107, v110, v111\n"
    "v_mfma_f32_16x16x32_bf16 v[108:111], a[4:7], v[188:191], 0\n"
    "ds_write_b128 v243, v[104:107] offset:25664\n"
    "s_waitcnt lgkmcnt(4)\n"
    "buffer_store_dwordx4 a[252:255], v255, s[44:47], s57 offen offset:0 sc1\n"
    "v_mfma_f32_16x16x32_bf16 v[104:107], a[0:3], v[188:191], 0\n"
    "v_or_b32_e32 v194, v194, v202\n"
    "v_cvt_pk_bf16_f32 v120, v120, v121\n"
    "v_cvt_pk_bf16_f32 v121, v122, v123\n"
    "v_cvt_pk_bf16_f32 v122, v124, v125\n"
    "v_cvt_pk_bf16_f32 v123, v126, v127\n"
    "ds_write_b128 v243, v[120:123] offset:27712\n"
    "v_mfma_f32_16x16x32_bf16 v[120:123], a[8:11], v[188:191], 0\n"
    "s_waitcnt lgkmcnt(2)\n"
    "v_pk_fma_f16 v232, v192, v192, s48\n"
    "v_cvt_pk_bf16_f32 v136, v136, v137\n"
    "v_mfma_f32_16x16x32_bf16 v[124:127], a[12:15], v[188:191], 0\n"
    "s_nop 0\n"
    "ds_read_b128 a[252:255], v249 offset:2048\n"
    "v_cvt_pk_bf16_f32 v137, v138, v139\n"
    "v_cvt_pk_bf16_f32 v138, v140, v141\n"
    "v_or_b32_e64 v195, v195, v203\n"
    "v_cvt_pk_bf16_f32 v139, v142, v143\n"
    "ds_write_b128 v243, v[136:139] offset:29760\n"
    "v_pk_mul_f16 v232, v192, v232\n"
    "v_mfma_f32_16x16x32_bf16 v[136:139], v[0:3], v[188:191], 0\n"
    "v_exp_f16_e64 v234, v232\n"
    "v_mfma_f32_16x16x32_bf16 v[140:143], v[4:7], v[188:191], 0\n"
    "v_or_b32_e64 v197, v197, v205\n"
    "v_exp_f16_sdwa v234, v232 dst_sel:WORD_1 dst_unused:UNUSED_PRESERVE src0_sel:WORD_1\n"
    "v_cvt_pk_f16_f32 v184, v168, v169\n"
    "v_pk_fma_f16 v234, v234, s49, s49\n"
    "v_lshlrev_b32_sdwa v185, 1, v170 dst_sel:DWORD dst_unused:UNUSED_PAD src0_sel:DWORD src1_sel:WORD_1\n"
    "v_lshlrev_b32_sdwa v201, 1, v171 dst_sel:DWORD dst_unused:UNUSED_PAD src0_sel:DWORD src1_sel:WORD_1\n"
    "v_mfma_f32_16x16x32_bf16 v[168:171], a[104:107], v[144:147], 0\n"
    "s_nop 0\n"
    "ds_read_u16 v185, v185\n"
    "ds_read_u16_d16_hi v201, v201\n"
    "v_mfma_f32_16x16x32_bf16 v[168:171], a[152:155], v[148:151], v[168:171]\n"
    "s_waitcnt lgkmcnt(3)\n"
    "buffer_store_dwordx4 a[252:255], v255, s[44:47], s57 offen offset:64 sc1\n"
    "v_lshlrev_b32_sdwa v186, 1, v172 dst_sel:DWORD dst_unused:UNUSED_PAD src0_sel:DWORD src1_sel:WORD_1\n"
    "v_lshlrev_b32_sdwa v202, 1, v173 dst_sel:DWORD dst_unused:UNUSED_PAD src0_sel:DWORD src1_sel:WORD_1\n"
    "v_lshlrev_b32_sdwa v187, 1, v174 dst_sel:DWORD dst_unused:UNUSED_PAD src0_sel:DWORD src1_sel:WORD_1\n"
    "v_lshlrev_b32_sdwa v203, 1, v175 dst_sel:DWORD dst_unused:UNUSED_PAD src0_sel:DWORD src1_sel:WORD_1\n"
    "v_rcp_f16_e64 v232, v234\n"
    "v_mfma_f32_16x16x32_bf16 v[168:171], a[64:67], v[152:155], v[168:171]\n"
    "ds_read_u16 v186, v186\n"
    "ds_read_u16_d16_hi v202, v202\n"
    "v_or_b32_e64 v198, v198, v206\n"
    "v_rcp_f16_sdwa v232, v234 dst_sel:WORD_1 dst_unused:UNUSED_PRESERVE src0_sel:WORD_1\n"
    "v_mfma_f32_16x16x32_bf16 v[172:175], a[108:111], v[144:147], 0\n"
    "ds_read_u16 v187, v187\n"
    "ds_read_u16_d16_hi v203, v203\n"
    "v_cvt_pk_f16_f32 v188, v176, v177\n"
    "v_lshlrev_b32_sdwa v189, 1, v178 dst_sel:DWORD dst_unused:UNUSED_PAD src0_sel:DWORD src1_sel:WORD_1\n"
    "v_lshlrev_b32_sdwa v205, 1, v179 dst_sel:DWORD dst_unused:UNUSED_PAD src0_sel:DWORD src1_sel:WORD_1\n"
    "v_lshlrev_b32_sdwa v190, 1, v180 dst_sel:DWORD dst_unused:UNUSED_PAD src0_sel:DWORD src1_sel:WORD_1\n"
    "v_lshlrev_b32_sdwa v206, 1, v181 dst_sel:DWORD dst_unused:UNUSED_PAD src0_sel:DWORD src1_sel:WORD_1\n"
    "v_lshlrev_b32_sdwa v191, 1, v182 dst_sel:DWORD dst_unused:UNUSED_PAD src0_sel:DWORD src1_sel:WORD_1\n"
    "v_fma_mix_f32 v234, v192, v232, 0 op_sel_hi:[1,1,0]\n"
    "v_mfma_f32_16x16x32_bf16 v[172:175], a[156:159], v[148:151], v[172:175]\n"
    "ds_read_b128 a[252:255], v249 offset:4096\n"
    "ds_read_u16 v189, v189\n"
    "v_mfma_f32_16x16x32_bf16 v[172:175], a[68:71], v[152:155], v[172:175]\n"
    "v_or_b32_e64 v199, v199, v207\n"
    "v_lshlrev_b32_sdwa v207, 1, v183 dst_sel:DWORD dst_unused:UNUSED_PAD src0_sel:DWORD src1_sel:WORD_1\n"
    "v_fma_mix_f32 v232, v192, v232, 0 op_sel:[1,1,0] op_sel_hi:[1,1,0]\n"
    "v_mfma_f32_16x16x32_bf16 v[176:179], a[104:107], v[156:159], 0\n"
    "s_nop 0\n"
    "ds_read_u16_d16_hi v205, v205\n"
    "v_cvt_pk_bf16_f32 v192, v234, v232\n"
    "v_mfma_f32_16x16x32_bf16 v[176:179], a[152:155], v[160:163], v[176:179]\n"
    "s_waitcnt lgkmcnt(7)\n"
    "ds_read_u16 v190, v190\n"
    "v_pk_fma_f16 v232, v196, v196, s48\n"
    "v_mfma_f32_16x16x32_bf16 v[176:179], a[64:67], v[164:167], v[176:179]\n"
    "v_pk_mul_f16 v232, v196, v232\n"
    "v_or_b32_e64 v185, v185, v201\n"
    "v_mfma_f32_16x16x32_bf16 v[180:183], a[108:111], v[156:159], 0\n"
    "ds_read_u16_d16_hi v206, v206\n"
    "v_exp_f16_e64 v234, v232\n"
    "v_mfma_f32_16x16x32_bf16 v[180:183], a[156:159], v[160:163], v[180:183]\n"
    "ds_read_u16 v191, v191\n"
    "v_exp_f16_sdwa v234, v232 dst_sel:WORD_1 dst_unused:UNUSED_PRESERVE src0_sel:WORD_1\n"
    "v_mfma_f32_16x16x32_bf16 v[180:183], a[68:71], v[164:167], v[180:183]\n"
    "s_waitcnt lgkmcnt(8)\n"
    "v_pk_fma_f16 v234, v234, s49, s49\n"
    "v_or_b32_e32 v186, v186, v202\n"
    "v_mfma_f32_16x16x32_bf16 v[96:99], a[16:19], v[192:195], v[96:99]\n"
    "ds_read_u16_d16_hi v207, v207\n"
    "v_rcp_f16_e32 v232, v234\n"
    "v_mfma_f32_16x16x32_bf16 v[100:103], a[20:23], v[192:195], v[100:103]\n"
    "v_rcp_f16_sdwa v232, v234 dst_sel:WORD_1 dst_unused:UNUSED_PRESERVE src0_sel:WORD_1\n"
    "v_mfma_f32_16x16x32_bf16 v[112:115], a[24:27], v[192:195], v[112:115]\n"
    "s_waitcnt lgkmcnt(6)\n"
    "buffer_store_dwordx4 a[252:255], v255, s[44:47], s57 offen offset:128 sc1\n"
    "v_mfma_f32_16x16x32_bf16 v[116:119], a[28:31], v[192:195], v[116:119]\n"
    "v_fma_mix_f32 v234, v196, v232, 0 op_sel_hi:[1,1,0]\n"
    "v_fma_mix_f32 v232, v196, v232, 0 op_sel:[1,1,0] op_sel_hi:[1,1,0]\n"
    "v_mfma_f32_16x16x32_bf16 v[128:131], v[8:11], v[192:195], v[128:131]\n"
    "ds_read_b128 a[252:255], v249 offset:1024\n"
    "v_cvt_pk_bf16_f32 v196, v234, v232\n"
    "v_mfma_f32_16x16x32_bf16 v[132:135], v[12:15], v[192:195], v[132:135]\n"
    "v_pk_fma_f16 v232, v184, v184, s48\n"
    "v_or_b32_e32 v187, v187, v203\n"
    "v_mfma_f32_16x16x32_bf16 v[104:107], a[16:19], v[196:199], v[104:107]\n"
    "s_waitcnt lgkmcnt(5)\n"
    "v_pk_mul_f16 v232, v184, v232\n"
    "v_or_b32_e64 v189, v189, v205\n"
    "v_mfma_f32_16x16x32_bf16 v[108:111], a[20:23], v[196:199], v[108:111]\n"
    "v_exp_f16_e32 v234, v232\n"
    "v_mfma_f32_16x16x32_bf16 v[120:123], a[24:27], v[196:199], v[120:123]\n"
    "v_exp_f16_sdwa v234, v232 dst_sel:WORD_1 dst_unused:UNUSED_PRESERVE src0_sel:WORD_1\n"
    "v_mfma_f32_16x16x32_bf16 v[124:127], a[28:31], v[196:199], v[124:127]\n"
    "s_waitcnt lgkmcnt(3)\n"
    "v_pk_fma_f16 v234, v234, s49, s49\n"
    "v_lshlrev_b32_sdwa v192, 1, v168 dst_sel:DWORD dst_unused:UNUSED_PAD src0_sel:DWORD src1_sel:WORD_1\n"
    "v_mfma_f32_16x16x32_bf16 v[136:139], v[8:11], v[196:199], v[136:139]\n"
    "ds_read_u16 v192, v192\n"
    "v_lshlrev_b32_sdwa v200, 1, v169 dst_sel:DWORD dst_unused:UNUSED_PAD src0_sel:DWORD src1_sel:WORD_1\n"
    "v_mfma_f32_16x16x32_bf16 v[140:143], v[12:15], v[196:199], v[140:143]\n"
    "ds_read_u16_d16_hi v200, v200\n"
    "v_lshlrev_b32_sdwa v193, 1, v170 dst_sel:DWORD dst_unused:UNUSED_PAD src0_sel:DWORD src1_sel:WORD_1\n"
    "v_lshlrev_b32_sdwa v201, 1, v171 dst_sel:DWORD dst_unused:UNUSED_PAD src0_sel:DWORD src1_sel:WORD_1\n"
    "v_mfma_f32_16x16x32_bf16 v[168:171], a[112:115], v[144:147], 0\n"
    "s_waitcnt lgkmcnt(2)\n"
    "ds_read_u16 v193, v193\n"
    "ds_read_u16_d16_hi v201, v201\n"
    "v_mfma_f32_16x16x32_bf16 v[168:171], a[160:163], v[148:151], v[168:171]\n"
    "s_nop 0\n"
    "buffer_store_dwordx4 a[252:255], v255, s[44:47], s88 offen offset:0 sc1\n"
    "v_lshlrev_b32_sdwa v194, 1, v172 dst_sel:DWORD dst_unused:UNUSED_PAD src0_sel:DWORD src1_sel:WORD_1\n"
    "v_lshlrev_b32_sdwa v202, 1, v173 dst_sel:DWORD dst_unused:UNUSED_PAD src0_sel:DWORD src1_sel:WORD_1\n"
    "v_lshlrev_b32_sdwa v195, 1, v174 dst_sel:DWORD dst_unused:UNUSED_PAD src0_sel:DWORD src1_sel:WORD_1\n"
    "v_lshlrev_b32_sdwa v203, 1, v175 dst_sel:DWORD dst_unused:UNUSED_PAD src0_sel:DWORD src1_sel:WORD_1\n"
    "v_rcp_f16_e64 v232, v234\n"
    "v_mfma_f32_16x16x32_bf16 v[168:171], a[72:75], v[152:155], v[168:171]\n"
    "ds_read_u16 v194, v194\n"
    "ds_read_u16_d16_hi v202, v202\n"
    "v_or_b32_e64 v190, v190, v206\n"
    "v_rcp_f16_sdwa v232, v234 dst_sel:WORD_1 dst_unused:UNUSED_PRESERVE src0_sel:WORD_1\n"
    "v_mfma_f32_16x16x32_bf16 v[172:175], a[116:119], v[144:147], 0\n"
    "ds_read_u16 v195, v195\n"
    "ds_read_u16_d16_hi v203, v203\n"
    "v_lshlrev_b32_sdwa v196, 1, v176 dst_sel:DWORD dst_unused:UNUSED_PAD src0_sel:DWORD src1_sel:WORD_1\n"
    "v_lshlrev_b32_sdwa v204, 1, v177 dst_sel:DWORD dst_unused:UNUSED_PAD src0_sel:DWORD src1_sel:WORD_1\n"
    "v_lshlrev_b32_sdwa v197, 1, v178 dst_sel:DWORD dst_unused:UNUSED_PAD src0_sel:DWORD src1_sel:WORD_1\n"
    "v_lshlrev_b32_sdwa v205, 1, v179 dst_sel:DWORD dst_unused:UNUSED_PAD src0_sel:DWORD src1_sel:WORD_1\n"
    "v_lshlrev_b32_sdwa v198, 1, v180 dst_sel:DWORD dst_unused:UNUSED_PAD src0_sel:DWORD src1_sel:WORD_1\n"
    "v_lshlrev_b32_sdwa v206, 1, v181 dst_sel:DWORD dst_unused:UNUSED_PAD src0_sel:DWORD src1_sel:WORD_1\n"
    "v_lshlrev_b32_sdwa v199, 1, v182 dst_sel:DWORD dst_unused:UNUSED_PAD src0_sel:DWORD src1_sel:WORD_1\n"
    "v_fma_mix_f32 v234, v184, v232, 0 op_sel_hi:[1,1,0]\n"
    "v_mfma_f32_16x16x32_bf16 v[172:175], a[164:167], v[148:151], v[172:175]\n"
    "ds_read_u16 v196, v196\n"
    "v_or_b32_e32 v191, v191, v207\n"
    "v_mfma_f32_16x16x32_bf16 v[172:175], v[52:55], v[152:155], v[172:175]\n"
    "s_waitcnt lgkmcnt(7)\n"
    "ds_read_u16_d16_hi v204, v204\n"
    "v_lshlrev_b32_sdwa v207, 1, v183 dst_sel:DWORD dst_unused:UNUSED_PAD src0_sel:DWORD src1_sel:WORD_1\n"
    "v_fma_mix_f32 v232, v184, v232, 0 op_sel:[1,1,0] op_sel_hi:[1,1,0]\n"
    "v_mfma_f32_16x16x32_bf16 v[176:179], a[112:115], v[156:159], 0\n"
    "ds_read_u16 v197, v197\n"
    "v_cvt_pk_bf16_f32 v184, v234, v232\n"
    "v_mfma_f32_16x16x32_bf16 v[176:179], a[160:163], v[160:163], v[176:179]\n"
    "v_pk_fma_f16 v232, v188, v188, s48\n"
    "v_or_b32_e32 v192, v192, v200\n"
    "v_mfma_f32_16x16x32_bf16 v[176:179], a[72:75], v[164:167], v[176:179]\n"
    "s_waitcnt lgkmcnt(7)\n"
    "ds_read_b128 a[252:255], v249 offset:3072\n"
    "v_pk_mul_f16 v232, v188, v232\n"
    "v_mfma_f32_16x16x32_bf16 v[180:183], a[116:119], v[156:159], 0\n"
    "ds_read_u16_d16_hi v205, v205\n"
    "v_exp_f16_e32 v234, v232\n"
    "v_mfma_f32_16x16x32_bf16 v[180:183], a[164:167], v[160:163], v[180:183]\n"
    "v_exp_f16_sdwa v234, v232 dst_sel:WORD_1 dst_unused:UNUSED_PRESERVE src0_sel:WORD_1\n"
    "v_mfma_f32_16x16x32_bf16 v[180:183], v[52:55], v[164:167], v[180:183]\n"
    "s_waitcnt lgkmcnt(7)\n"
    "ds_read_u16 v198, v198\n"
    "v_pk_fma_f16 v234, v234, s49, s49\n"
    "v_mfma_f32_16x16x32_bf16 v[96:99], a[32:35], v[184:187], v[96:99]\n"
    "ds_read_u16_d16_hi v206, v206\n"
    "v_rcp_f16_e64 v232, v234\n"
    "v_mfma_f32_16x16x32_bf16 v[100:103], a[36:39], v[184:187], v[100:103]\n"
    "v_rcp_f16_sdwa v232, v234 dst_sel:WORD_1 dst_unused:UNUSED_PRESERVE src0_sel:WORD_1\n"
    "v_mfma_f32_16x16x32_bf16 v[112:115], a[40:43], v[184:187], v[112:115]\n"
    "ds_read_u16 v199, v199\n"
    "v_fma_mix_f32 v234, v188, v232, 0 op_sel_hi:[1,1,0]\n"
    "v_mfma_f32_16x16x32_bf16 v[116:119], a[44:47], v[184:187], v[116:119]\n"
    "s_waitcnt lgkmcnt(8)\n"
    "v_fma_mix_f32 v232, v188, v232, 0 op_sel:[1,1,0] op_sel_hi:[1,1,0]\n"
    "v_or_b32_e32 v193, v193, v201\n"
    "v_mfma_f32_16x16x32_bf16 v[128:131], v[16:19], v[184:187], v[128:131]\n"
    "ds_read_u16_d16_hi v207, v207\n"
    "v_cvt_pk_bf16_f32 v188, v234, v232\n"
    "v_mfma_f32_16x16x32_bf16 v[132:135], v[20:23], v[184:187], v[132:135]\n"
    "v_cvt_pk_f16_f32 v184, v168, v169\n"
    "v_lshlrev_b32_sdwa v185, 1, v170 dst_sel:DWORD dst_unused:UNUSED_PAD src0_sel:DWORD src1_sel:WORD_1\n"
    "v_mfma_f32_16x16x32_bf16 v[104:107], a[32:35], v[188:191], v[104:107]\n"
    "s_waitcnt lgkmcnt(7)\n"
    "ds_read_u16 v185, v185\n"
    "v_lshlrev_b32_sdwa v201, 1, v171 dst_sel:DWORD dst_unused:UNUSED_PAD src0_sel:DWORD src1_sel:WORD_1\n"
    "v_mfma_f32_16x16x32_bf16 v[108:111], a[36:39], v[188:191], v[108:111]\n"
    "ds_read_u16_d16_hi v201, v201\n"
    "s_waitcnt lgkmcnt(7)\n"
    "buffer_store_dwordx4 a[252:255], v255, s[44:47], s88 offen offset:64 sc1\n"
    "v_mfma_f32_16x16x32_bf16 v[120:123], a[40:43], v[188:191], v[120:123]\n"
    "v_lshlrev_b32_sdwa v186, 1, v172 dst_sel:DWORD dst_unused:UNUSED_PAD src0_sel:DWORD src1_sel:WORD_1\n"
    "v_or_b32_e32 v194, v194, v202\n"
    "v_mfma_f32_16x16x32_bf16 v[124:127], a[44:47], v[188:191], v[124:127]\n"
    "s_waitcnt lgkmcnt(6)\n"
    "ds_read_b128 a[252:255], v249 offset:5120\n"
    "ds_read_u16 v186, v186\n"
    "v_mfma_f32_16x16x32_bf16 v[136:139], v[16:19], v[188:191], v[136:139]\n"
    "v_lshlrev_b32_sdwa v202, 1, v173 dst_sel:DWORD dst_unused:UNUSED_PAD src0_sel:DWORD src1_sel:WORD_1\n"
    "v_lshlrev_b32_sdwa v187, 1, v174 dst_sel:DWORD dst_unused:UNUSED_PAD src0_sel:DWORD src1_sel:WORD_1\n"
    "v_mfma_f32_16x16x32_bf16 v[140:143], v[20:23], v[188:191], v[140:143]\n"
    "ds_read_u16_d16_hi v202, v202\n"
    "v_cvt_pk_f16_f32 v188, v176, v177\n"
    "v_mfma_f32_16x16x32_bf16 v[168:171], a[120:123], v[144:147], 0\n"
    "ds_read_u16 v187, v187\n"
    "v_lshlrev_b32_sdwa v189, 1, v178 dst_sel:DWORD dst_unused:UNUSED_PAD src0_sel:DWORD src1_sel:WORD_1\n"
    "v_mfma_f32_16x16x32_bf16 v[168:171], a[168:171], v[148:151], v[168:171]\n"
    "s_waitcnt lgkmcnt(8)\n"
    "v_lshlrev_b32_sdwa v190, 1, v180 dst_sel:DWORD dst_unused:UNUSED_PAD src0_sel:DWORD src1_sel:WORD_1\n"
    "v_lshlrev_b32_sdwa v191, 1, v182 dst_sel:DWORD dst_unused:UNUSED_PAD src0_sel:DWORD src1_sel:WORD_1\n"
    "v_mfma_f32_16x16x32_bf16 v[168:171], a[76:79], v[152:155], v[168:171]\n"
    "v_or_b32_e32 v195, v195, v203\n"
    "v_lshlrev_b32_sdwa v203, 1, v175 dst_sel:DWORD dst_unused:UNUSED_PAD src0_sel:DWORD src1_sel:WORD_1\n"
    "v_mfma_f32_16x16x32_bf16 v[172:175], a[124:127], v[144:147], 0\n"
    "ds_read_u16_d16_hi v203, v203\n"
    "ds_read_u16 v189, v189\n"
    "v_mfma_f32_16x16x32_bf16 v[172:175], a[172:175], v[148:151], v[172:175]\n"
    "s_waitcnt lgkmcnt(8)\n"
    "v_or_b32_e32 v196, v196, v204\n"
    "v_or_b32_e64 v197, v197, v205\n"
    "v_mfma_f32_16x16x32_bf16 v[172:175], v[60:63], v[152:155], v[172:175]\n"
    "ds_read_u16 v190, v190\n"
    "v_lshlrev_b32_sdwa v205, 1, v179 dst_sel:DWORD dst_unused:UNUSED_PAD src0_sel:DWORD src1_sel:WORD_1\n"
    "v_mfma_f32_16x16x32_bf16 v[176:179], a[120:123], v[156:159], 0\n"
    "v_or_b32_e64 v198, v198, v206\n"
    "v_lshlrev_b32_sdwa v206, 1, v181 dst_sel:DWORD dst_unused:UNUSED_PAD src0_sel:DWORD src1_sel:WORD_1\n"
    "v_mfma_f32_16x16x32_bf16 v[176:179], a[168:171], v[160:163], v[176:179]\n"
    "ds_read_u16_d16_hi v205, v205\n"
    "s_waitcnt lgkmcnt(7)\n"
    "buffer_store_dwordx4 a[252:255], v255, s[44:47], s88 offen offset:128 sc1\n"
    "v_mfma_f32_16x16x32_bf16 v[176:179], a[76:79], v[164:167], v[176:179]\n"
    "s_nop 0\n"
    "ds_read_b128 v[144:147], v243 offset:0\n"
    "ds_read_u16_d16_hi v206, v206\n"
    "v_mfma_f32_16x16x32_bf16 v[96:99], a[176:179], v[192:195], v[96:99]\n"
    "v_or_b32_e64 v199, v199, v207\n"
    "v_lshlrev_b32_sdwa v207, 1, v183 dst_sel:DWORD dst_unused:UNUSED_PAD src0_sel:DWORD src1_sel:WORD_1\n"
    "v_mfma_f32_16x16x32_bf16 v[180:183], a[124:127], v[156:159], 0\n"
    "ds_read_b128 v[148:151], v243 offset:1024\n"
    "v_or_b32_e32 v185, v185, v201\n"
    "v_mfma_f32_16x16x32_bf16 v[180:183], a[172:175], v[160:163], v[180:183]\n"
    "s_waitcnt lgkmcnt(8)\n"
    "ds_read_u16 v191, v191\n"
    "v_or_b32_e64 v186, v186, v202\n"
    "v_mfma_f32_16x16x32_bf16 v[180:183], v[60:63], v[164:167], v[180:183]\n"
    "v_pk_fma_f16 v232, v184, v184, s48\n"
    "v_lshlrev_b32_sdwa v201, 1, v171 dst_sel:DWORD dst_unused:UNUSED_PAD src0_sel:DWORD src1_sel:WORD_1\n"
    "v_mfma_f32_16x16x32_bf16 v[100:103], a[180:183], v[192:195], v[100:103]\n"
    "ds_read_b128 v[152:155], v243 offset:2048\n"
    "v_pk_mul_f16 v232, v184, v232\n"
    "v_mfma_f32_16x16x32_bf16 v[112:115], a[184:187], v[192:195], v[112:115]\n"
    "ds_read_u16_d16_hi v207, v207\n"
    "s_waitcnt lgkmcnt(9)\n"
    "v_or_b32_e32 v187, v187, v203\n"
    "v_mfma_f32_16x16x32_bf16 v[116:119], a[188:191], v[192:195], v[116:119]\n"
    "v_exp_f16_e64 v234, v232\n"
    "v_mfma_f32_16x16x32_bf16 v[128:131], v[24:27], v[192:195], v[128:131]\n"
    "ds_read_b128 v[156:159], v243 offset:3072\n"
    "ds_read_b128 v[160:163], v243 offset:4096\n"
    "v_mfma_f32_16x16x32_bf16 v[132:135], v[28:31], v[192:195], v[132:135]\n"
    "v_exp_f16_sdwa v234, v232 dst_sel:WORD_1 dst_unused:UNUSED_PRESERVE src0_sel:WORD_1\n"
    "v_mfma_f32_16x16x32_bf16 v[104:107], a[176:179], v[196:199], v[104:107]\n"
    "ds_read_b128 v[164:167], v243 offset:5120\n"
    "ds_read_u16_d16_hi v201, v201\n"
    "v_mfma_f32_16x16x32_bf16 v[108:111], a[180:183], v[196:199], v[108:111]\n"
    "v_pk_fma_f16 v234, v234, s49, s49\n"
    "s_waitcnt lgkmcnt(9)\n"
    "v_or_b32_e32 v189, v189, v205\n"
    "v_mfma_f32_16x16x32_bf16 v[120:123], a[184:187], v[196:199], v[120:123]\n"
    "v_cvt_pk_f16_f32 v192, v168, v169\n"
    "v_lshlrev_b32_sdwa v193, 1, v170 dst_sel:DWORD dst_unused:UNUSED_PAD src0_sel:DWORD src1_sel:WORD_1\n"
    "v_mfma_f32_16x16x32_bf16 v[124:127], a[188:191], v[196:199], v[124:127]\n"
    "s_nop 0\n"
    "ds_read_u16 v193, v193\n"
    "v_lshlrev_b32_sdwa v194, 1, v172 dst_sel:DWORD dst_unused:UNUSED_PAD src0_sel:DWORD src1_sel:WORD_1\n"
    "v_mfma_f32_16x16x32_bf16 v[136:139], v[24:27], v[196:199], v[136:139]\n"
    "s_waitcnt lgkmcnt(8)\n"
    "ds_read_u16 v194, v194\n"
    "v_lshlrev_b32_sdwa v202, 1, v173 dst_sel:DWORD dst_unused:UNUSED_PAD src0_sel:DWORD src1_sel:WORD_1\n"
    "v_mfma_f32_16x16x32_bf16 v[140:143], v[28:31], v[196:199], v[140:143]\n"
    "v_lshlrev_b32_sdwa v195, 1, v174 dst_sel:DWORD dst_unused:UNUSED_PAD src0_sel:DWORD src1_sel:WORD_1\n"
    "v_lshlrev_b32_sdwa v203, 1, v175 dst_sel:DWORD dst_unused:UNUSED_PAD src0_sel:DWORD src1_sel:WORD_1\n"
    "v_mfma_f32_16x16x32_bf16 v[168:171], a[80:83], v[144:147], 0\n"
    "ds_read_u16_d16_hi v202, v202\n"
    "v_rcp_f16_e32 v232, v234\n"
    "v_mfma_f32_16x16x32_bf16 v[168:171], a[128:131], v[148:151], v[168:171]\n"
    "s_waitcnt lgkmcnt(8)\n"
    "ds_read_u16 v195, v195\n"
    "v_or_b32_e32 v190, v190, v206\n"
    "v_mfma_f32_16x16x32_bf16 v[168:171], a[48:51], v[152:155], v[168:171]\n"
    "v_rcp_f16_sdwa v232, v234 dst_sel:WORD_1 dst_unused:UNUSED_PRESERVE src0_sel:WORD_1\n"
    "v_mfma_f32_16x16x32_bf16 v[172:175], a[84:87], v[144:147], 0\n"
    "s_waitcnt lgkmcnt(7)\n"
    "ds_read_u16_d16_hi v203, v203\n"
    "v_cvt_pk_f16_f32 v196, v176, v177\n"
    "v_mfma_f32_16x16x32_bf16 v[172:175], a[132:135], v[148:151], v[172:175]\n"
    "v_lshlrev_b32_sdwa v197, 1, v178 dst_sel:DWORD dst_unused:UNUSED_PAD src0_sel:DWORD src1_sel:WORD_1\n"
    "v_lshlrev_b32_sdwa v205, 1, v179 dst_sel:DWORD dst_unused:UNUSED_PAD src0_sel:DWORD src1_sel:WORD_1\n"
    "v_lshlrev_b32_sdwa v198, 1, v180 dst_sel:DWORD dst_unused:UNUSED_PAD src0_sel:DWORD src1_sel:WORD_1\n"
    "v_lshlrev_b32_sdwa v206, 1, v181 dst_sel:DWORD dst_unused:UNUSED_PAD src0_sel:DWORD src1_sel:WORD_1\n"
    "v_lshlrev_b32_sdwa v199, 1, v182 dst_sel:DWORD dst_unused:UNUSED_PAD src0_sel:DWORD src1_sel:WORD_1\n"
    "v_fma_mix_f32 v234, v184, v232, 0 op_sel_hi:[1,1,0]\n"
    "v_mfma_f32_16x16x32_bf16 v[172:175], v[48:51], v[152:155], v[172:175]\n"
    "s_waitcnt lgkmcnt(6)\n"
    "ds_read_u16 v197, v197\n"
    "v_or_b32_e32 v191, v191, v207\n"
    "v_lshlrev_b32_sdwa v207, 1, v183 dst_sel:DWORD dst_unused:UNUSED_PAD src0_sel:DWORD src1_sel:WORD_1\n"
    "v_fma_mix_f32 v232, v184, v232, 0 op_sel:[1,1,0] op_sel_hi:[1,1,0]\n"
    "v_mfma_f32_16x16x32_bf16 v[176:179], a[80:83], v[156:159], 0\n"
    "s_barrier\n"
    "v_cvt_pk_bf16_f32 v184, v234, v232\n"
    "v_mfma_f32_16x16x32_bf16 v[176:179], a[128:131], v[160:163], v[176:179]\n"
    "s_waitcnt lgkmcnt(5)\n"
    "ds_read_u16_d16_hi v205, v205\n"
    "ds_read_u16 v198, v198\n"
    "v_mfma_f32_16x16x32_bf16 v[176:179], a[48:51], v[164:167], v[176:179]\n"
    "v_pk_fma_f16 v232, v188, v188, s48\n"
    "v_or_b32_e64 v193, v193, v201\n"
    "v_mfma_f32_16x16x32_bf16 v[180:183], a[84:87], v[156:159], 0\n"
    "ds_read_u16_d16_hi v206, v206\n"
    "v_pk_mul_f16 v232, v188, v232\n"
    "v_mfma_f32_16x16x32_bf16 v[180:183], a[132:135], v[160:163], v[180:183]\n"
    "ds_read_u16 v199, v199\n"
    "v_exp_f16_e32 v234, v232\n"
    "v_mfma_f32_16x16x32_bf16 v[180:183], v[48:51], v[164:167], v[180:183]\n"
    "s_waitcnt lgkmcnt(7)\n"
    "ds_read_u16_d16_hi v207, v207\n"
    "v_exp_f16_sdwa v234, v232 dst_sel:WORD_1 dst_unused:UNUSED_PRESERVE src0_sel:WORD_1\n"
    "v_mfma_f32_16x16x32_bf16 v[96:99], a[192:195], v[184:187], v[96:99]\n"
    "v_pk_fma_f16 v234, v234, s49, s49\n"
    "v_or_b32_e64 v194, v194, v202\n"
    "v_mfma_f32_16x16x32_bf16 v[100:103], a[196:199], v[184:187], v[100:103]\n"
    "v_rcp_f16_e32 v232, v234\n"
    "v_mfma_f32_16x16x32_bf16 v[112:115], a[200:203], v[184:187], v[112:115]\n"
    "s_waitcnt lgkmcnt(6)\n"
    "v_rcp_f16_sdwa v232, v234 dst_sel:WORD_1 dst_unused:UNUSED_PRESERVE src0_sel:WORD_1\n"
    "v_mfma_f32_16x16x32_bf16 v[116:119], a[204:207], v[184:187], v[116:119]\n"
    "v_fma_mix_f32 v234, v188, v232, 0 op_sel_hi:[1,1,0]\n"
    "v_fma_mix_f32 v232, v188, v232, 0 op_sel:[1,1,0] op_sel_hi:[1,1,0]\n"
    "v_mfma_f32_16x16x32_bf16 v[128:131], v[32:35], v[184:187], v[128:131]\n"
    "v_cvt_pk_bf16_f32 v188, v234, v232\n"
    "v_pk_fma_f16 v232, v192, v192, s48\n"
    "v_mfma_f32_16x16x32_bf16 v[132:135], v[36:39], v[184:187], v[132:135]\n"
    "s_waitcnt lgkmcnt(4)\n"
    "v_or_b32_e32 v195, v195, v203\n"
    "v_pk_mul_f16 v232, v192, v232\n"
    "v_mfma_f32_16x16x32_bf16 v[104:107], a[192:195], v[188:191], v[104:107]\n"
    "v_exp_f16_e64 v234, v232\n"
    "v_mfma_f32_16x16x32_bf16 v[108:111], a[196:199], v[188:191], v[108:111]\n"
    "v_exp_f16_sdwa v234, v232 dst_sel:WORD_1 dst_unused:UNUSED_PRESERVE src0_sel:WORD_1\n"
    "v_mfma_f32_16x16x32_bf16 v[120:123], a[200:203], v[188:191], v[120:123]\n"
    "v_or_b32_e32 v197, v197, v205\n"
    "v_pk_fma_f16 v234, v234, s49, s49\n"
    "v_mfma_f32_16x16x32_bf16 v[124:127], a[204:207], v[188:191], v[124:127]\n"
    "s_waitcnt lgkmcnt(2)\n"
    "v_lshlrev_b32_sdwa v184, 1, v168 dst_sel:DWORD dst_unused:UNUSED_PAD src0_sel:DWORD src1_sel:WORD_1\n"
    "v_lshlrev_b32_sdwa v200, 1, v169 dst_sel:DWORD dst_unused:UNUSED_PAD src0_sel:DWORD src1_sel:WORD_1\n"
    "v_mfma_f32_16x16x32_bf16 v[136:139], v[32:35], v[188:191], v[136:139]\n"
    "ds_read_u16 v184, v184\n"
    "ds_read_u16_d16_hi v200, v200\n"
    "v_mfma_f32_16x16x32_bf16 v[140:143], v[36:39], v[188:191], v[140:143]\n"
    "s_nop 0\n"
    "v_lshlrev_b32_sdwa v185, 1, v170 dst_sel:DWORD dst_unused:UNUSED_PAD src0_sel:DWORD src1_sel:WORD_1\n"
    "v_lshlrev_b32_sdwa v201, 1, v171 dst_sel:DWORD dst_unused:UNUSED_PAD src0_sel:DWORD src1_sel:WORD_1\n"
    "v_mfma_f32_16x16x32_bf16 v[168:171], a[88:91], v[144:147], 0\n"
    "s_waitcnt lgkmcnt(2)\n"
    "ds_read_u16 v185, v185\n"
    "ds_read_u16_d16_hi v201, v201\n"
    "v_mfma_f32_16x16x32_bf16 v[168:171], a[136:139], v[148:151], v[168:171]\n"
    "v_lshlrev_b32_sdwa v186, 1, v172 dst_sel:DWORD dst_unused:UNUSED_PAD src0_sel:DWORD src1_sel:WORD_1\n"
    "v_lshlrev_b32_sdwa v202, 1, v173 dst_sel:DWORD dst_unused:UNUSED_PAD src0_sel:DWORD src1_sel:WORD_1\n"
    "v_lshlrev_b32_sdwa v187, 1, v174 dst_sel:DWORD dst_unused:UNUSED_PAD src0_sel:DWORD src1_sel:WORD_1\n"
    "v_lshlrev_b32_sdwa v203, 1, v175 dst_sel:DWORD dst_unused:UNUSED_PAD src0_sel:DWORD src1_sel:WORD_1\n"
    "v_rcp_f16_e64 v232, v234\n"
    "v_mfma_f32_16x16x32_bf16 v[168:171], a[52:55], v[152:155], v[168:171]\n"
    "ds_read_u16 v186, v186\n"
    "ds_read_u16_d16_hi v202, v202\n"
    "v_or_b32_e64 v198, v198, v206\n"
    "v_rcp_f16_sdwa v232, v234 dst_sel:WORD_1 dst_unused:UNUSED_PRESERVE src0_sel:WORD_1\n"
    "v_mfma_f32_16x16x32_bf16 v[172:175], a[92:95], v[144:147], 0\n"
    "ds_read_u16 v187, v187\n"
    "v_lshlrev_b32_sdwa v188, 1, v176 dst_sel:DWORD dst_unused:UNUSED_PAD src0_sel:DWORD src1_sel:WORD_1\n"
    "v_mfma_f32_16x16x32_bf16 v[172:175], a[140:143], v[148:151], v[172:175]\n"
    "v_lshlrev_b32_sdwa v204, 1, v177 dst_sel:DWORD dst_unused:UNUSED_PAD src0_sel:DWORD src1_sel:WORD_1\n"
    "v_lshlrev_b32_sdwa v189, 1, v178 dst_sel:DWORD dst_unused:UNUSED_PAD src0_sel:DWORD src1_sel:WORD_1\n"
    "v_lshlrev_b32_sdwa v205, 1, v179 dst_sel:DWORD dst_unused:UNUSED_PAD src0_sel:DWORD src1_sel:WORD_1\n"
    "v_lshlrev_b32_sdwa v190, 1, v180 dst_sel:DWORD dst_unused:UNUSED_PAD src0_sel:DWORD src1_sel:WORD_1\n"
    "v_lshlrev_b32_sdwa v206, 1, v181 dst_sel:DWORD dst_unused:UNUSED_PAD src0_sel:DWORD src1_sel:WORD_1\n"
    "v_lshlrev_b32_sdwa v191, 1, v182 dst_sel:DWORD dst_unused:UNUSED_PAD src0_sel:DWORD src1_sel:WORD_1\n"
    "v_fma_mix_f32 v234, v192, v232, 0 op_sel_hi:[1,1,0]\n"
    "v_mfma_f32_16x16x32_bf16 v[172:175], v[56:59], v[152:155], v[172:175]\n"
    "ds_read_u16_d16_hi v203, v203\n"
    "ds_read_u16 v188, v188\n"
    "v_or_b32_e32 v199, v199, v207\n"
    "v_lshlrev_b32_sdwa v207, 1, v183 dst_sel:DWORD dst_unused:UNUSED_PAD src0_sel:DWORD src1_sel:WORD_1\n"
    "v_fma_mix_f32 v232, v192, v232, 0 op_sel:[1,1,0] op_sel_hi:[1,1,0]\n"
    "v_mfma_f32_16x16x32_bf16 v[176:179], a[88:91], v[156:159], 0\n"
    "s_waitcnt lgkmcnt(7)\n"
    "ds_read_u16_d16_hi v204, v204\n"
    "v_cvt_pk_bf16_f32 v192, v234, v232\n"
    "v_mfma_f32_16x16x32_bf16 v[176:179], a[136:139], v[160:163], v[176:179]\n"
    "v_pk_fma_f16 v232, v196, v196, s48\n"
    "v_or_b32_e64 v184, v184, v200\n"
    "v_mfma_f32_16x16x32_bf16 v[176:179], a[52:55], v[164:167], v[176:179]\n"
    "ds_read_u16 v189, v189\n"
    "v_pk_mul_f16 v232, v196, v232\n"
    "v_mfma_f32_16x16x32_bf16 v[180:183], a[92:95], v[156:159], 0\n"
    "v_exp_f16_e32 v234, v232\n"
    "v_mfma_f32_16x16x32_bf16 v[180:183], a[140:143], v[160:163], v[180:183]\n"
    "s_waitcnt lgkmcnt(7)\n"
    "ds_read_u16_d16_hi v205, v205\n"
    "v_exp_f16_sdwa v234, v232 dst_sel:WORD_1 dst_unused:UNUSED_PRESERVE src0_sel:WORD_1\n"
    "v_mfma_f32_16x16x32_bf16 v[180:183], v[56:59], v[164:167], v[180:183]\n"
    "ds_read_u16 v190, v190\n"
    "v_pk_fma_f16 v234, v234, s49, s49\n"
    "v_mfma_f32_16x16x32_bf16 v[96:99], a[208:211], v[192:195], v[96:99]\n"
    "v_rcp_f16_e32 v232, v234\n"
    "v_mfma_f32_16x16x32_bf16 v[100:103], a[212:215], v[192:195], v[100:103]\n"
    "s_waitcnt lgkmcnt(7)\n"
    "ds_read_u16_d16_hi v206, v206\n"
    "v_rcp_f16_sdwa v232, v234 dst_sel:WORD_1 dst_unused:UNUSED_PRESERVE src0_sel:WORD_1\n"
    "v_mfma_f32_16x16x32_bf16 v[112:115], a[216:219], v[192:195], v[112:115]\n"
    "v_fma_mix_f32 v234, v196, v232, 0 op_sel_hi:[1,1,0]\n"
    "v_fma_mix_f32 v232, v196, v232, 0 op_sel:[1,1,0] op_sel_hi:[1,1,0]\n"
    "v_mfma_f32_16x16x32_bf16 v[116:119], a[220:223], v[192:195], v[116:119]\n"
    "ds_read_u16 v191, v191\n"
    "v_cvt_pk_bf16_f32 v196, v234, v232\n"
    "v_mfma_f32_16x16x32_bf16 v[128:131], v[40:43], v[192:195], v[128:131]\n"
    "s_waitcnt lgkmcnt(7)\n"
    "ds_read_u16_d16_hi v207, v207\n"
    "v_or_b32_e32 v185, v185, v201\n"
    "v_mfma_f32_16x16x32_bf16 v[132:135], v[44:47], v[192:195], v[132:135]\n"
    "v_cvt_pk_f16_f32 v192, v168, v169\n"
    "v_lshlrev_b32_sdwa v193, 1, v170 dst_sel:DWORD dst_unused:UNUSED_PAD src0_sel:DWORD src1_sel:WORD_1\n"
    "v_mfma_f32_16x16x32_bf16 v[104:107], a[208:211], v[196:199], v[104:107]\n"
    "ds_read_u16 v193, v193\n"
    "v_lshlrev_b32_sdwa v201, 1, v171 dst_sel:DWORD dst_unused:UNUSED_PAD src0_sel:DWORD src1_sel:WORD_1\n"
    "v_mfma_f32_16x16x32_bf16 v[108:111], a[212:215], v[196:199], v[108:111]\n"
    "v_lshlrev_b32_sdwa v194, 1, v172 dst_sel:DWORD dst_unused:UNUSED_PAD src0_sel:DWORD src1_sel:WORD_1\n"
    "v_or_b32_e32 v186, v186, v202\n"
    "v_mfma_f32_16x16x32_bf16 v[120:123], a[216:219], v[196:199], v[120:123]\n"
    "s_waitcnt lgkmcnt(7)\n"
    "ds_read_u16_d16_hi v201, v201\n"
    "v_lshlrev_b32_sdwa v202, 1, v173 dst_sel:DWORD dst_unused:UNUSED_PAD src0_sel:DWORD src1_sel:WORD_1\n"
    "v_mfma_f32_16x16x32_bf16 v[124:127], a[220:223], v[196:199], v[124:127]\n"
    "v_lshlrev_b32_sdwa v195, 1, v174 dst_sel:DWORD dst_unused:UNUSED_PAD src0_sel:DWORD src1_sel:WORD_1\n"
    "v_or_b32_e32 v187, v187, v203\n"
    "v_mfma_f32_16x16x32_bf16 v[136:139], v[40:43], v[196:199], v[136:139]\n"
    "ds_read_u16 v194, v194\n"
    "v_lshlrev_b32_sdwa v203, 1, v175 dst_sel:DWORD dst_unused:UNUSED_PAD src0_sel:DWORD src1_sel:WORD_1\n"
    "v_mfma_f32_16x16x32_bf16 v[140:143], v[44:47], v[196:199], v[140:143]\n"
    "s_waitcnt lgkmcnt(7)\n"
    "ds_read_u16_d16_hi v202, v202\n"
    "v_cvt_pk_f16_f32 v196, v176, v177\n"
    "v_mfma_f32_16x16x32_bf16 v[168:171], a[96:99], v[144:147], 0\n"
    "v_lshlrev_b32_sdwa v197, 1, v178 dst_sel:DWORD dst_unused:UNUSED_PAD src0_sel:DWORD src1_sel:WORD_1\n"
    "v_lshlrev_b32_sdwa v198, 1, v180 dst_sel:DWORD dst_unused:UNUSED_PAD src0_sel:DWORD src1_sel:WORD_1\n"
    "v_mfma_f32_16x16x32_bf16 v[168:171], a[144:147], v[148:151], v[168:171]\n"
    "ds_read_u16 v195, v195\n"
    "v_lshlrev_b32_sdwa v199, 1, v182 dst_sel:DWORD dst_unused:UNUSED_PAD src0_sel:DWORD src1_sel:WORD_1\n"
    "v_mfma_f32_16x16x32_bf16 v[168:171], a[56:59], v[152:155], v[168:171]\n"
    "v_or_b32_e64 v188, v188, v204\n"
    "v_or_b32_e32 v189, v189, v205\n"
    "v_mfma_f32_16x16x32_bf16 v[172:175], a[100:103], v[144:147], 0\n"
    "s_waitcnt lgkmcnt(7)\n"
    "ds_read_u16_d16_hi v203, v203\n"
    "v_lshlrev_b32_sdwa v205, 1, v179 dst_sel:DWORD dst_unused:UNUSED_PAD src0_sel:DWORD src1_sel:WORD_1\n"
    "v_mfma_f32_16x16x32_bf16 v[172:175], a[148:151], v[148:151], v[172:175]\n"
    "v_cvt_pk_bf16_f32 v96, v96, v97\n"
    "v_cvt_pk_bf16_f32 v97, v98, v99\n"
    "v_mfma_f32_16x16x32_bf16 v[172:175], a[60:63], v[152:155], v[172:175]\n"
    "ds_read_u16 v197, v197\n"
    "v_cvt_pk_bf16_f32 v98, v100, v101\n"
    "v_mfma_f32_16x16x32_bf16 v[176:179], a[96:99], v[156:159], 0\n"
    "s_waitcnt lgkmcnt(7)\n"
    "ds_read_u16_d16_hi v205, v205\n"
    "v_cvt_pk_bf16_f32 v99, v102, v103\n"
    "v_mfma_f32_16x16x32_bf16 v[176:179], a[144:147], v[160:163], v[176:179]\n"
    "v_or_b32_e32 v190, v190, v206\n"
    "v_lshlrev_b32_sdwa v206, 1, v181 dst_sel:DWORD dst_unused:UNUSED_PAD src0_sel:DWORD src1_sel:WORD_1\n"
    "v_mfma_f32_16x16x32_bf16 v[176:179], a[56:59], v[164:167], v[176:179]\n"
    "ds_read_u16 v198, v198\n"
    "v_cvt_pk_bf16_f32 v112, v112, v113\n"
    "v_mfma_f32_16x16x32_bf16 v[100:103], a[4:7], v[184:187], 0\n"
    "s_nop 0\n"
    "v_cvt_pk_bf16_f32 v113, v114, v115\n"
    "v_cvt_pk_bf16_f32 v114, v116, v117\n"
    "ds_write_b128 v242, v[96:99] offset:24640\n"
    "v_mfma_f32_16x16x32_bf16 v[96:99], a[0:3], v[184:187], 0\n"
    "s_waitcnt lgkmcnt(7)\n"
    "v_cvt_pk_bf16_f32 v115, v118, v119\n"
    "v_cvt_pk_bf16_f32 v128, v128, v129\n"
    "v_cvt_pk_bf16_f32 v129, v130, v131\n"
    "v_or_b32_e32 v191, v191, v207\n"
    "v_mfma_f32_16x16x32_bf16 v[116:119], a[12:15], v[184:187], 0\n"
    "s_waitcnt lgkmcnt(6)\n"
    "ds_read_u16_d16_hi v206, v206\n"
    "ds_write_b128 v242, v[112:115] offset:26688\n"
    "v_mfma_f32_16x16x32_bf16 v[112:115], a[8:11], v[184:187], 0\n"
    "v_lshlrev_b32_sdwa v207, 1, v183 dst_sel:DWORD dst_unused:UNUSED_PAD src0_sel:DWORD src1_sel:WORD_1\n"
    "v_cvt_pk_bf16_f32 v130, v132, v133\n"
    "v_mfma_f32_16x16x32_bf16 v[180:183], a[100:103], v[156:159], 0\n"
    "v_or_b32_e64 v193, v193, v201\n"
    "v_cvt_pk_bf16_f32 v131, v134, v135\n"
    "v_mfma_f32_16x16x32_bf16 v[180:183], a[148:151], v[160:163], v[180:183]\n"
    "s_waitcnt lgkmcnt(6)\n"
    "ds_read_u16 v199, v199\n"
    "ds_write_b128 v242, v[128:131] offset:28736\n"
    "v_mfma_f32_16x16x32_bf16 v[180:183], a[60:63], v[164:167], v[180:183]\n"
    "v_cvt_pk_bf16_f32 v104, v104, v105\n"
    "v_cvt_pk_bf16_f32 v105, v106, v107\n"
    "v_mfma_f32_16x16x32_bf16 v[128:131], v[0:3], v[184:187], 0\n"
    "s_waitcnt lgkmcnt(6)\n"
    "v_cvt_pk_bf16_f32 v106, v108, v109\n"
    "v_cvt_pk_bf16_f32 v107, v110, v111\n"
    "v_mfma_f32_16x16x32_bf16 v[132:135], v[4:7], v[184:187], 0\n"
    "ds_read_u16_d16_hi v207, v207\n"
    "ds_write_b128 v242, v[104:107] offset:25664\n"
    "v_mfma_f32_16x16x32_bf16 v[104:107], a[0:3], v[188:191], 0\n"
    "v_or_b32_e64 v194, v194, v202\n"
    "v_cvt_pk_bf16_f32 v120, v120, v121\n"
    "v_mfma_f32_16x16x32_bf16 v[108:111], a[4:7], v[188:191], 0\n"
    "v_cvt_pk_bf16_f32 v121, v122, v123\n"
    "v_cvt_pk_bf16_f32 v122, v124, v125\n"
    "v_cvt_pk_bf16_f32 v123, v126, v127\n"
    "ds_write_b128 v242, v[120:123] offset:27712\n"
    "v_mfma_f32_16x16x32_bf16 v[120:123], a[8:11], v[188:191], 0\n"
    "s_waitcnt lgkmcnt(5)\n"
    "v_cvt_pk_bf16_f32 v136, v136, v137\n"
    "v_pk_fma_f16 v232, v192, v192, s48\n"
    "v_mfma_f32_16x16x32_bf16 v[124:127], a[12:15], v[188:191], 0\n"
    "s_nop 0\n"
    "v_cvt_pk_bf16_f32 v137, v138, v139\n"
    "v_cvt_pk_bf16_f32 v138, v140, v141\n"
    "v_or_b32_e64 v195, v195, v203\n"
    "v_cvt_pk_bf16_f32 v139, v142, v143\n"
    "ds_write_b128 v242, v[136:139] offset:29760\n"
    "v_pk_mul_f16 v232, v192, v232\n"
    "v_mfma_f32_16x16x32_bf16 v[136:139], v[0:3], v[188:191], 0\n"
    "v_exp_f16_e64 v234, v232\n"
    "v_mfma_f32_16x16x32_bf16 v[140:143], v[4:7], v[188:191], 0\n"
    "v_exp_f16_sdwa v234, v232 dst_sel:WORD_1 dst_unused:UNUSED_PRESERVE src0_sel:WORD_1\n"
    "v_or_b32_e64 v197, v197, v205\n"
    "v_pk_fma_f16 v234, v234, s49, s49\n"
    "v_cvt_pk_f16_f32 v184, v168, v169\n"
    "v_lshlrev_b32_sdwa v185, 1, v170 dst_sel:DWORD dst_unused:UNUSED_PAD src0_sel:DWORD src1_sel:WORD_1\n"
    "v_lshlrev_b32_sdwa v201, 1, v171 dst_sel:DWORD dst_unused:UNUSED_PAD src0_sel:DWORD src1_sel:WORD_1\n"
    "v_mfma_f32_16x16x32_bf16 v[168:171], a[104:107], v[144:147], 0\n"
    "s_waitcnt lgkmcnt(1)\n"
    "ds_read_u16 v185, v185\n"
    "ds_read_u16_d16_hi v201, v201\n"
    "v_mfma_f32_16x16x32_bf16 v[168:171], a[152:155], v[148:151], v[168:171]\n"
    "s_nop 0\n"
    "v_lshlrev_b32_sdwa v186, 1, v172 dst_sel:DWORD dst_unused:UNUSED_PAD src0_sel:DWORD src1_sel:WORD_1\n"
    "v_lshlrev_b32_sdwa v202, 1, v173 dst_sel:DWORD dst_unused:UNUSED_PAD src0_sel:DWORD src1_sel:WORD_1\n"
    "v_lshlrev_b32_sdwa v187, 1, v174 dst_sel:DWORD dst_unused:UNUSED_PAD src0_sel:DWORD src1_sel:WORD_1\n"
    "v_lshlrev_b32_sdwa v203, 1, v175 dst_sel:DWORD dst_unused:UNUSED_PAD src0_sel:DWORD src1_sel:WORD_1\n"
    "v_rcp_f16_e32 v232, v234\n"
    "v_mfma_f32_16x16x32_bf16 v[168:171], a[64:67], v[152:155], v[168:171]\n"
    "s_waitcnt lgkmcnt(2)\n"
    "ds_read_u16 v186, v186\n"
    "ds_read_u16_d16_hi v202, v202\n"
    "v_or_b32_e32 v198, v198, v206\n"
    "v_rcp_f16_sdwa v232, v234 dst_sel:WORD_1 dst_unused:UNUSED_PRESERVE src0_sel:WORD_1\n"
    "v_mfma_f32_16x16x32_bf16 v[172:175], a[108:111], v[144:147], 0\n"
    "s_barrier\n"
    "v_mfma_f32_16x16x32_bf16 v[172:175], a[156:159], v[148:151], v[172:175]\n"
    "ds_read_u16 v187, v187\n"
    "ds_read_u16_d16_hi v203, v203\n"
    "v_cvt_pk_f16_f32 v188, v176, v177\n"
    "v_lshlrev_b32_sdwa v189, 1, v178 dst_sel:DWORD dst_unused:UNUSED_PAD src0_sel:DWORD src1_sel:WORD_1\n"
    "v_lshlrev_b32_sdwa v205, 1, v179 dst_sel:DWORD dst_unused:UNUSED_PAD src0_sel:DWORD src1_sel:WORD_1\n"
    "v_lshlrev_b32_sdwa v190, 1, v180 dst_sel:DWORD dst_unused:UNUSED_PAD src0_sel:DWORD src1_sel:WORD_1\n"
    "v_lshlrev_b32_sdwa v206, 1, v181 dst_sel:DWORD dst_unused:UNUSED_PAD src0_sel:DWORD src1_sel:WORD_1\n"
    "v_lshlrev_b32_sdwa v191, 1, v182 dst_sel:DWORD dst_unused:UNUSED_PAD src0_sel:DWORD src1_sel:WORD_1\n"
    "v_fma_mix_f32 v234, v192, v232, 0 op_sel_hi:[1,1,0]\n"
    "v_mfma_f32_16x16x32_bf16 v[172:175], a[68:71], v[152:155], v[172:175]\n"
    "ds_read_u16 v189, v189\n"
    "v_or_b32_e64 v199, v199, v207\n"
    "v_lshlrev_b32_sdwa v207, 1, v183 dst_sel:DWORD dst_unused:UNUSED_PAD src0_sel:DWORD src1_sel:WORD_1\n"
    "v_fma_mix_f32 v232, v192, v232, 0 op_sel:[1,1,0] op_sel_hi:[1,1,0]\n"
    "v_mfma_f32_16x16x32_bf16 v[176:179], a[104:107], v[156:159], 0\n"
    "s_nop 0\n"
    "ds_read_u16_d16_hi v205, v205\n"
    "v_cvt_pk_bf16_f32 v192, v234, v232\n"
    "v_mfma_f32_16x16x32_bf16 v[176:179], a[152:155], v[160:163], v[176:179]\n"
    "s_waitcnt lgkmcnt(6)\n"
    "ds_read_u16 v190, v190\n"
    "v_pk_fma_f16 v232, v196, v196, s48\n"
    "v_mfma_f32_16x16x32_bf16 v[176:179], a[64:67], v[164:167], v[176:179]\n"
    "ds_read_u16_d16_hi v206, v206\n"
    "v_pk_mul_f16 v232, v196, v232\n"
    "v_mfma_f32_16x16x32_bf16 v[180:183], a[108:111], v[156:159], 0\n"
    "ds_read_u16 v191, v191\n"
    "v_exp_f16_e32 v234, v232\n"
    "v_mfma_f32_16x16x32_bf16 v[180:183], a[156:159], v[160:163], v[180:183]\n"
    "s_waitcnt lgkmcnt(7)\n"
    "ds_read_u16_d16_hi v207, v207\n"
    "v_exp_f16_sdwa v234, v232 dst_sel:WORD_1 dst_unused:UNUSED_PRESERVE src0_sel:WORD_1\n"
    "v_mfma_f32_16x16x32_bf16 v[180:183], a[68:71], v[164:167], v[180:183]\n"
    "v_pk_fma_f16 v234, v234, s49, s49\n"
    "v_or_b32_e64 v185, v185, v201\n"
    "v_mfma_f32_16x16x32_bf16 v[96:99], a[16:19], v[192:195], v[96:99]\n"
    "v_rcp_f16_e32 v232, v234\n"
    "v_mfma_f32_16x16x32_bf16 v[100:103], a[20:23], v[192:195], v[100:103]\n"
    "v_rcp_f16_sdwa v232, v234 dst_sel:WORD_1 dst_unused:UNUSED_PRESERVE src0_sel:WORD_1\n"
    "v_mfma_f32_16x16x32_bf16 v[112:115], a[24:27], v[192:195], v[112:115]\n"
    "s_waitcnt lgkmcnt(6)\n"
    "v_fma_mix_f32 v234, v196, v232, 0 op_sel_hi:[1,1,0]\n"
    "v_fma_mix_f32 v232, v196, v232, 0 op_sel:[1,1,0] op_sel_hi:[1,1,0]\n"
    "v_mfma_f32_16x16x32_bf16 v[116:119], a[28:31], v[192:195], v[116:119]\n"
    "v_or_b32_e64 v186, v186, v202\n"
    "v_cvt_pk_bf16_f32 v196, v234, v232\n"
    "v_mfma_f32_16x16x32_bf16 v[128:131], v[8:11], v[192:195], v[128:131]\n"
    "v_pk_fma_f16 v232, v184, v184, s48\n"
    "v_or_b32_e32 v187, v187, v203\n"
    "v_mfma_f32_16x16x32_bf16 v[132:135], v[12:15], v[192:195], v[132:135]\n"
    "s_waitcnt lgkmcnt(4)\n"
    "v_pk_mul_f16 v232, v184, v232\n"
    "v_or_b32_e64 v189, v189, v205\n"
    "v_mfma_f32_16x16x32_bf16 v[104:107], a[16:19], v[196:199], v[104:107]\n"
    "v_exp_f16_e32 v234, v232\n"
    "v_mfma_f32_16x16x32_bf16 v[108:111], a[20:23], v[196:199], v[108:111]\n"
    "v_exp_f16_sdwa v234, v232 dst_sel:WORD_1 dst_unused:UNUSED_PRESERVE src0_sel:WORD_1\n"
    "v_mfma_f32_16x16x32_bf16 v[120:123], a[24:27], v[196:199], v[120:123]\n"
    "v_pk_fma_f16 v234, v234, s49, s49\n"
    "v_lshlrev_b32_sdwa v192, 1, v168 dst_sel:DWORD dst_unused:UNUSED_PAD src0_sel:DWORD src1_sel:WORD_1\n"
    "v_mfma_f32_16x16x32_bf16 v[124:127], a[28:31], v[196:199], v[124:127]\n"
    "s_waitcnt lgkmcnt(2)\n"
    "ds_read_u16 v192, v192\n"
    "v_lshlrev_b32_sdwa v200, 1, v169 dst_sel:DWORD dst_unused:UNUSED_PAD src0_sel:DWORD src1_sel:WORD_1\n"
    "v_mfma_f32_16x16x32_bf16 v[136:139], v[8:11], v[196:199], v[136:139]\n"
    "ds_read_u16_d16_hi v200, v200\n"
    "v_lshlrev_b32_sdwa v193, 1, v170 dst_sel:DWORD dst_unused:UNUSED_PAD src0_sel:DWORD src1_sel:WORD_1\n"
    "v_mfma_f32_16x16x32_bf16 v[140:143], v[12:15], v[196:199], v[140:143]\n"
    "ds_read_u16 v193, v193\n"
    "v_lshlrev_b32_sdwa v201, 1, v171 dst_sel:DWORD dst_unused:UNUSED_PAD src0_sel:DWORD src1_sel:WORD_1\n"
    "s_waitcnt vmcnt(8)\n"
    "v_smfmac_f32_32x32x32_bf16 v[112:127], a[248:251], a[232:239], v241\n"
    "s_waitcnt lgkmcnt(3)\n"
    "ds_read_u16_d16_hi v201, v201\n"
    "ds_read_b128 a[232:235], v243 offset:24640\n"
    "v_lshlrev_b32_sdwa v194, 1, v172 dst_sel:DWORD dst_unused:UNUSED_PAD src0_sel:DWORD src1_sel:WORD_1\n"
    "v_lshlrev_b32_sdwa v202, 1, v173 dst_sel:DWORD dst_unused:UNUSED_PAD src0_sel:DWORD src1_sel:WORD_1\n"
    "v_lshlrev_b32_sdwa v195, 1, v174 dst_sel:DWORD dst_unused:UNUSED_PAD src0_sel:DWORD src1_sel:WORD_1\n"
    "s_waitcnt vmcnt(7)\n"
    "v_smfmac_f32_32x32x32_bf16 v[96:111], a[248:251], a[224:231], v241\n"
    "s_nop 0\n"
    "ds_read_u16 v194, v194\n"
    "ds_read_u16_d16_hi v202, v202\n"
    "v_lshlrev_b32_sdwa v203, 1, v175 dst_sel:DWORD dst_unused:UNUSED_PAD src0_sel:DWORD src1_sel:WORD_1\n"
    "v_rcp_f16_e32 v232, v234\n"
    "s_waitcnt vmcnt(6)\n"
    "v_smfmac_f32_32x32x32_bf16 v[128:143], a[248:251], v[208:215], v241\n"
    "ds_read_b128 a[236:239], v243 offset:25664\n"
    "v_or_b32_e64 v190, v190, v206\n"
    "v_rcp_f16_sdwa v232, v234 dst_sel:WORD_1 dst_unused:UNUSED_PRESERVE src0_sel:WORD_1\n"
    "v_lshlrev_b32_sdwa v196, 1, v176 dst_sel:DWORD dst_unused:UNUSED_PAD src0_sel:DWORD src1_sel:WORD_1\n"
    "v_mfma_f32_16x16x32_bf16 v[168:171], a[112:115], v[144:147], 0\n"
    "ds_read_u16 v195, v195\n"
    "v_lshlrev_b32_sdwa v204, 1, v177 dst_sel:DWORD dst_unused:UNUSED_PAD src0_sel:DWORD src1_sel:WORD_1\n"
    "v_mfma_f32_16x16x32_bf16 v[168:171], a[160:163], v[148:151], v[168:171]\n"
    "ds_read_u16_d16_hi v203, v203\n"
    "v_lshlrev_b32_sdwa v197, 1, v178 dst_sel:DWORD dst_unused:UNUSED_PAD src0_sel:DWORD src1_sel:WORD_1\n"
    "v_mfma_f32_16x16x32_bf16 v[168:171], a[72:75], v[152:155], v[168:171]\n"
    "v_lshlrev_b32_sdwa v205, 1, v179 dst_sel:DWORD dst_unused:UNUSED_PAD src0_sel:DWORD src1_sel:WORD_1\n"
    "v_lshlrev_b32_sdwa v198, 1, v180 dst_sel:DWORD dst_unused:UNUSED_PAD src0_sel:DWORD src1_sel:WORD_1\n"
    "v_mfma_f32_16x16x32_bf16 v[172:175], a[116:119], v[144:147], 0\n"
    "ds_read_u16 v196, v196\n"
    "v_lshlrev_b32_sdwa v206, 1, v181 dst_sel:DWORD dst_unused:UNUSED_PAD src0_sel:DWORD src1_sel:WORD_1\n"
    "v_mfma_f32_16x16x32_bf16 v[172:175], a[164:167], v[148:151], v[172:175]\n"
    "v_lshlrev_b32_sdwa v199, 1, v182 dst_sel:DWORD dst_unused:UNUSED_PAD src0_sel:DWORD src1_sel:WORD_1\n"
    "v_fma_mix_f32 v234, v184, v232, 0 op_sel_hi:[1,1,0]\n"
    "v_mfma_f32_16x16x32_bf16 v[172:175], v[52:55], v[152:155], v[172:175]\n"
    "s_waitcnt lgkmcnt(8)\n"
    "ds_read_u16_d16_hi v204, v204\n"
    "v_or_b32_e32 v191, v191, v207\n"
    "v_lshlrev_b32_sdwa v207, 1, v183 dst_sel:DWORD dst_unused:UNUSED_PAD src0_sel:DWORD src1_sel:WORD_1\n"
    "v_fma_mix_f32 v232, v184, v232, 0 op_sel:[1,1,0] op_sel_hi:[1,1,0]\n"
    "v_mfma_f32_16x16x32_bf16 v[176:179], a[112:115], v[156:159], 0\n"
    "ds_read_u16 v197, v197\n"
    "v_cvt_pk_bf16_f32 v184, v234, v232\n"
    "v_mfma_f32_16x16x32_bf16 v[176:179], a[160:163], v[160:163], v[176:179]\n"
    "ds_read_u16_d16_hi v205, v205\n"
    "v_pk_fma_f16 v232, v188, v188, s48\n"
    "v_mfma_f32_16x16x32_bf16 v[176:179], a[72:75], v[164:167], v[176:179]\n"
    "s_waitcnt lgkmcnt(8)\n"
    "v_pk_mul_f16 v232, v188, v232\n"
    "v_or_b32_e32 v192, v192, v200\n"
    "v_mfma_f32_16x16x32_bf16 v[180:183], a[116:119], v[156:159], 0\n"
    "ds_read_u16 v198, v198\n"
    "v_exp_f16_e32 v234, v232\n"
    "s_waitcnt lgkmcnt(7)\n"
    "v_smfmac_f32_32x32x32_bf16 v[96:111], a[248:251], a[232:239], v241\n"
    "ds_read_u16_d16_hi v206, v206\n"
    "v_exp_f16_sdwa v234, v232 dst_sel:WORD_1 dst_unused:UNUSED_PRESERVE src0_sel:WORD_1\n"
    "v_or_b32_e64 v193, v193, v201\n"
    "v_pk_fma_f16 v234, v234, s49, s49\n"
    "v_mfma_f32_16x16x32_bf16 v[180:183], a[164:167], v[160:163], v[180:183]\n"
    "ds_read_u16 v199, v199\n"
    "v_rcp_f16_e32 v232, v234\n"
    "v_mfma_f32_16x16x32_bf16 v[180:183], v[52:55], v[164:167], v[180:183]\n"
    "v_rcp_f16_sdwa v232, v234 dst_sel:WORD_1 dst_unused:UNUSED_PRESERVE src0_sel:WORD_1\n"
    "v_mfma_f32_16x16x32_bf16 v[112:115], a[40:43], v[184:187], v[112:115]\n"
    "s_waitcnt lgkmcnt(7)\n"
    "ds_read_u16_d16_hi v207, v207\n"
    "v_fma_mix_f32 v234, v188, v232, 0 op_sel_hi:[1,1,0]\n"
    "v_mfma_f32_16x16x32_bf16 v[96:99], a[32:35], v[184:187], v[96:99]\n"
    "ds_read_b128 a[232:235], v243 offset:26688\n"
    "v_fma_mix_f32 v232, v188, v232, 0 op_sel:[1,1,0] op_sel_hi:[1,1,0]\n"
    "v_mfma_f32_16x16x32_bf16 v[100:103], a[36:39], v[184:187], v[100:103]\n"
    "ds_read_b128 a[236:239], v243 offset:27712\n"
    "v_cvt_pk_bf16_f32 v188, v234, v232\n"
    "v_mfma_f32_16x16x32_bf16 v[116:119], a[44:47], v[184:187], v[116:119]\n"
    "s_waitcnt lgkmcnt(8)\n"
    "v_lshlrev_b32_sdwa v201, 1, v171 dst_sel:DWORD dst_unused:UNUSED_PAD src0_sel:DWORD src1_sel:WORD_1\n"
    "v_or_b32_e32 v194, v194, v202\n"
    "v_mfma_f32_16x16x32_bf16 v[128:131], v[16:19], v[184:187], v[128:131]\n"
    "ds_read_u16_d16_hi v201, v201\n"
    "v_lshlrev_b32_sdwa v202, 1, v173 dst_sel:DWORD dst_unused:UNUSED_PAD src0_sel:DWORD src1_sel:WORD_1\n"
    "v_mfma_f32_16x16x32_bf16 v[132:135], v[20:23], v[184:187], v[132:135]\n"
    "ds_read_u16_d16_hi v202, v202\n"
    "v_cvt_pk_f16_f32 v184, v168, v169\n"
    "v_mfma_f32_16x16x32_bf16 v[104:107], a[32:35], v[188:191], v[104:107]\n"
    "v_lshlrev_b32_sdwa v185, 1, v170 dst_sel:DWORD dst_unused:UNUSED_PAD src0_sel:DWORD src1_sel:WORD_1\n"
    "v_lshlrev_b32_sdwa v186, 1, v172 dst_sel:DWORD dst_unused:UNUSED_PAD src0_sel:DWORD src1_sel:WORD_1\n"
    "v_mfma_f32_16x16x32_bf16 v[108:111], a[36:39], v[188:191], v[108:111]\n"
    "s_waitcnt lgkmcnt(8)\n"
    "ds_read_u16 v185, v185\n"
    "v_lshlrev_b32_sdwa v187, 1, v174 dst_sel:DWORD dst_unused:UNUSED_PAD src0_sel:DWORD src1_sel:WORD_1\n"
    "v_mfma_f32_16x16x32_bf16 v[120:123], a[40:43], v[188:191], v[120:123]\n"
    "v_or_b32_e32 v195, v195, v203\n"
    "v_lshlrev_b32_sdwa v203, 1, v175 dst_sel:DWORD dst_unused:UNUSED_PAD src0_sel:DWORD src1_sel:WORD_1\n"
    "v_mfma_f32_16x16x32_bf16 v[124:127], a[44:47], v[188:191], v[124:127]\n"
    "ds_read_u16 v186, v186\n"
    "v_or_b32_e32 v196, v196, v204\n"
    "v_mfma_f32_16x16x32_bf16 v[136:139], v[16:19], v[188:191], v[136:139]\n"
    "s_waitcnt lgkmcnt(8)\n"
    "ds_read_u16 v187, v187\n"
    "v_or_b32_e64 v197, v197, v205\n"
    "v_mfma_f32_16x16x32_bf16 v[140:143], v[20:23], v[188:191], v[140:143]\n"
    "v_cvt_pk_f16_f32 v188, v176, v177\n"
    "v_lshlrev_b32_sdwa v189, 1, v178 dst_sel:DWORD dst_unused:UNUSED_PAD src0_sel:DWORD src1_sel:WORD_1\n"
    "v_mfma_f32_16x16x32_bf16 v[168:171], a[120:123], v[144:147], 0\n"
    "ds_read_u16_d16_hi v203, v203\n"
    "v_lshlrev_b32_sdwa v205, 1, v179 dst_sel:DWORD dst_unused:UNUSED_PAD src0_sel:DWORD src1_sel:WORD_1\n"
    "v_mfma_f32_16x16x32_bf16 v[168:171], a[168:171], v[148:151], v[168:171]\n"
    "v_lshlrev_b32_sdwa v190, 1, v180 dst_sel:DWORD dst_unused:UNUSED_PAD src0_sel:DWORD src1_sel:WORD_1\n"
    "v_lshlrev_b32_sdwa v191, 1, v182 dst_sel:DWORD dst_unused:UNUSED_PAD src0_sel:DWORD src1_sel:WORD_1\n"
    "v_mfma_f32_16x16x32_bf16 v[168:171], a[76:79], v[152:155], v[168:171]\n"
    "s_waitcnt lgkmcnt(6)\n"
    "ds_read_u16 v189, v189\n"
    "v_or_b32_e32 v198, v198, v206\n"
    "v_smfmac_f32_32x32x32_bf16 v[112:127], a[248:251], a[232:239], v241\n"
    "ds_read_b128 a[232:235], v243 offset:28736\n"
    "v_lshlrev_b32_sdwa v206, 1, v181 dst_sel:DWORD dst_unused:UNUSED_PAD src0_sel:DWORD src1_sel:WORD_1\n"
    "v_or_b32_e64 v199, v199, v207\n"
    "v_lshlrev_b32_sdwa v207, 1, v183 dst_sel:DWORD dst_unused:UNUSED_PAD src0_sel:DWORD src1_sel:WORD_1\n"
    "v_pk_fma_f16 v232, v184, v184, s48\n"
    "v_mfma_f32_16x16x32_bf16 v[172:175], a[124:127], v[144:147], 0\n"
    "ds_read_u16_d16_hi v205, v205\n"
    "v_pk_mul_f16 v232, v184, v232\n"
    "v_mfma_f32_16x16x32_bf16 v[172:175], a[172:175], v[148:151], v[172:175]\n"
    "ds_read_b128 a[236:239], v243 offset:29760\n"
    "v_exp_f16_e64 v234, v232\n"
    "v_mfma_f32_16x16x32_bf16 v[172:175], v[60:63], v[152:155], v[172:175]\n"
    "ds_read_u16 v190, v190\n"
    "v_exp_f16_sdwa v234, v232 dst_sel:WORD_1 dst_unused:UNUSED_PRESERVE src0_sel:WORD_1\n"
    "v_mfma_f32_16x16x32_bf16 v[176:179], a[120:123], v[156:159], 0\n"
    "s_waitcnt lgkmcnt(8)\n"
    "v_or_b32_e32 v185, v185, v201\n"
    "v_pk_fma_f16 v234, v234, s49, s49\n"
    "v_mfma_f32_16x16x32_bf16 v[176:179], a[168:171], v[160:163], v[176:179]\n"
    "ds_read_u16_d16_hi v206, v206\n"
    "v_lshlrev_b32_sdwa v201, 1, v171 dst_sel:DWORD dst_unused:UNUSED_PAD src0_sel:DWORD src1_sel:WORD_1\n"
    "v_mfma_f32_16x16x32_bf16 v[176:179], a[76:79], v[164:167], v[176:179]\n"
    "s_waitcnt lgkmcnt(8)\n"
    "ds_read_u16 v191, v191\n"
    "v_or_b32_e32 v186, v186, v202\n"
    "v_mfma_f32_16x16x32_bf16 v[180:183], a[124:127], v[156:159], 0\n"
    "v_lshlrev_b32_sdwa v202, 1, v173 dst_sel:DWORD dst_unused:UNUSED_PAD src0_sel:DWORD src1_sel:WORD_1\n"
    "v_rcp_f16_e64 v232, v234\n"
    "v_mfma_f32_16x16x32_bf16 v[180:183], a[172:175], v[160:163], v[180:183]\n"
    "ds_read_u16_d16_hi v207, v207\n"
    "v_rcp_f16_sdwa v232, v234 dst_sel:WORD_1 dst_unused:UNUSED_PRESERVE src0_sel:WORD_1\n"
    "v_mfma_f32_16x16x32_bf16 v[180:183], v[60:63], v[164:167], v[180:183]\n"
    "s_waitcnt lgkmcnt(8)\n"
    "ds_read_u16_d16_hi v201, v201\n"
    "v_or_b32_e32 v187, v187, v203\n"
    "v_mfma_f32_16x16x32_bf16 v[96:99], a[176:179], v[192:195], v[96:99]\n"
    "v_lshlrev_b32_sdwa v203, 1, v175 dst_sel:DWORD dst_unused:UNUSED_PAD src0_sel:DWORD src1_sel:WORD_1\n"
    "v_fma_mix_f32 v234, v184, v232, 0 op_sel_hi:[1,1,0]\n"
    "v_mfma_f32_16x16x32_bf16 v[100:103], a[180:183], v[192:195], v[100:103]\n"
    "ds_read_u16_d16_hi v202, v202\n"
    "v_fma_mix_f32 v232, v184, v232, 0 op_sel:[1,1,0] op_sel_hi:[1,1,0]\n"
    "v_mfma_f32_16x16x32_bf16 v[112:115], a[184:187], v[192:195], v[112:115]\n"
    "v_cvt_pk_bf16_f32 v184, v234, v232\n"
    "v_pk_fma_f16 v232, v188, v188, s48\n"
    "v_mfma_f32_16x16x32_bf16 v[116:119], a[188:191], v[192:195], v[116:119]\n"
    "ds_read_u16_d16_hi v203, v203\n"
    "s_waitcnt lgkmcnt(7)\n"
    "v_or_b32_e32 v189, v189, v205\n"
    "v_mfma_f32_16x16x32_bf16 v[128:131], v[24:27], v[192:195], v[128:131]\n"
    "v_lshlrev_b32_sdwa v205, 1, v179 dst_sel:DWORD dst_unused:UNUSED_PAD src0_sel:DWORD src1_sel:WORD_1\n"
    "v_pk_mul_f16 v232, v188, v232\n"
    "v_mfma_f32_16x16x32_bf16 v[132:135], v[28:31], v[192:195], v[132:135]\n"
    "ds_read_u16_d16_hi v205, v205\n"
    "v_cvt_pk_f16_f32 v192, v168, v169\n"
    "v_mfma_f32_16x16x32_bf16 v[104:107], a[176:179], v[196:199], v[104:107]\n"
    "v_lshlrev_b32_sdwa v193, 1, v170 dst_sel:DWORD dst_unused:UNUSED_PAD src0_sel:DWORD src1_sel:WORD_1\n"
    "v_lshlrev_b32_sdwa v194, 1, v172 dst_sel:DWORD dst_unused:UNUSED_PAD src0_sel:DWORD src1_sel:WORD_1\n"
    "v_mfma_f32_16x16x32_bf16 v[108:111], a[180:183], v[196:199], v[108:111]\n"
    "ds_read_u16 v193, v193\n"
    "v_lshlrev_b32_sdwa v195, 1, v174 dst_sel:DWORD dst_unused:UNUSED_PAD src0_sel:DWORD src1_sel:WORD_1\n"
    "v_mfma_f32_16x16x32_bf16 v[120:123], a[184:187], v[196:199], v[120:123]\n"
    "s_waitcnt lgkmcnt(7)\n"
    "ds_read_u16 v194, v194\n"
    "v_or_b32_e32 v190, v190, v206\n"
    "v_mfma_f32_16x16x32_bf16 v[124:127], a[188:191], v[196:199], v[124:127]\n"
    "v_lshlrev_b32_sdwa v206, 1, v181 dst_sel:DWORD dst_unused:UNUSED_PAD src0_sel:DWORD src1_sel:WORD_1\n"
    "v_exp_f16_e32 v234, v232\n"
    "v_mfma_f32_16x16x32_bf16 v[136:139], v[24:27], v[196:199], v[136:139]\n"
    "ds_read_u16 v195, v195\n"
    "v_exp_f16_sdwa v234, v232 dst_sel:WORD_1 dst_unused:UNUSED_PRESERVE src0_sel:WORD_1\n"
    "v_mfma_f32_16x16x32_bf16 v[140:143], v[28:31], v[196:199], v[140:143]\n"
    "s_waitcnt lgkmcnt(7)\n"
    "ds_read_u16_d16_hi v206, v206\n"
    "v_cvt_pk_f16_f32 v196, v176, v177\n"
    "v_mfma_f32_16x16x32_bf16 v[96:99], a[192:195], v[184:187], v[96:99]\n"
    "v_lshlrev_b32_sdwa v197, 1, v178 dst_sel:DWORD dst_unused:UNUSED_PAD src0_sel:DWORD src1_sel:WORD_1\n"
    "v_lshlrev_b32_sdwa v198, 1, v180 dst_sel:DWORD dst_unused:UNUSED_PAD src0_sel:DWORD src1_sel:WORD_1\n"
    "v_smfmac_f32_32x32x32_bf16 v[128:143], a[248:251], a[232:239], v241\n"
    "ds_read_u16 v197, v197\n"
    "v_lshlrev_b32_sdwa v199, 1, v182 dst_sel:DWORD dst_unused:UNUSED_PAD src0_sel:DWORD src1_sel:WORD_1\n"
    "v_or_b32_e64 v191, v191, v207\n"
    "v_lshlrev_b32_sdwa v207, 1, v183 dst_sel:DWORD dst_unused:UNUSED_PAD src0_sel:DWORD src1_sel:WORD_1\n"
    "v_pk_fma_f16 v234, v234, s49, s49\n"
    "v_mfma_f32_16x16x32_bf16 v[100:103], a[196:199], v[184:187], v[100:103]\n"
    "ds_read_u16 v198, v198\n"
    "v_rcp_f16_e64 v232, v234\n"
    "v_mfma_f32_16x16x32_bf16 v[112:115], a[200:203], v[184:187], v[112:115]\n"
    "ds_read_u16 v199, v199\n"
    "v_rcp_f16_sdwa v232, v234 dst_sel:WORD_1 dst_unused:UNUSED_PRESERVE src0_sel:WORD_1\n"
    "v_mfma_f32_16x16x32_bf16 v[116:119], a[204:207], v[184:187], v[116:119]\n"
    "ds_read_u16_d16_hi v207, v207\n"
    "v_fma_mix_f32 v234, v188, v232, 0 op_sel_hi:[1,1,0]\n"
    "v_mfma_f32_16x16x32_bf16 v[128:131], v[32:35], v[184:187], v[128:131]\n"
    "v_fma_mix_f32 v232, v188, v232, 0 op_sel:[1,1,0] op_sel_hi:[1,1,0]\n"
    "v_cvt_pk_bf16_f32 v188, v234, v232\n"
    "v_mfma_f32_16x16x32_bf16 v[132:135], v[36:39], v[184:187], v[132:135]\n"
    "s_waitcnt lgkmcnt(7)\n"
    "v_or_b32_e32 v193, v193, v201\n"
    "v_pk_fma_f16 v232, v192, v192, s48\n"
    "v_mfma_f32_16x16x32_bf16 v[104:107], a[192:195], v[188:191], v[104:107]\n"
    "v_pk_mul_f16 v232, v192, v232\n"
    "v_exp_f16_e64 v234, v232\n"
    "v_mfma_f32_16x16x32_bf16 v[108:111], a[196:199], v[188:191], v[108:111]\n"
    "s_waitcnt lgkmcnt(6)\n"
    "v_or_b32_e32 v194, v194, v202\n"
    "v_exp_f16_sdwa v234, v232 dst_sel:WORD_1 dst_unused:UNUSED_PRESERVE src0_sel:WORD_1\n"
    "v_mfma_f32_16x16x32_bf16 v[120:123], a[200:203], v[188:191], v[120:123]\n"
    "s_waitcnt lgkmcnt(5)\n"
    "v_or_b32_e32 v195, v195, v203\n"
    "v_pk_fma_f16 v234, v234, s49, s49\n"
    "v_mfma_f32_16x16x32_bf16 v[124:127], a[204:207], v[188:191], v[124:127]\n"
    "v_rcp_f16_e64 v232, v234\n"
    "v_mfma_f32_16x16x32_bf16 v[136:139], v[32:35], v[188:191], v[136:139]\n"
    "v_rcp_f16_sdwa v232, v234 dst_sel:WORD_1 dst_unused:UNUSED_PRESERVE src0_sel:WORD_1\n"
    "v_mfma_f32_16x16x32_bf16 v[140:143], v[36:39], v[188:191], v[140:143]\n"
    "v_fma_mix_f32 v234, v192, v232, 0 op_sel_hi:[1,1,0]\n"
    "s_waitcnt lgkmcnt(3)\n"
    "v_or_b32_e32 v197, v197, v205\n"
    "v_fma_mix_f32 v232, v192, v232, 0 op_sel:[1,1,0] op_sel_hi:[1,1,0]\n"
    "v_cvt_pk_bf16_f32 v192, v234, v232\n"
    "v_pk_fma_f16 v232, v196, v196, s48\n"
    "v_pk_mul_f16 v232, v196, v232\n"
    "v_exp_f16_e32 v234, v232\n"
    "s_waitcnt lgkmcnt(2)\n"
    "v_or_b32_e64 v198, v198, v206\n"
    "v_exp_f16_sdwa v234, v232 dst_sel:WORD_1 dst_unused:UNUSED_PRESERVE src0_sel:WORD_1\n"
    "v_mfma_f32_16x16x32_bf16 v[96:99], a[208:211], v[192:195], v[96:99]\n"
    "v_pk_fma_f16 v234, v234, s49, s49\n"
    "v_rcp_f16_e64 v232, v234\n"
    "v_mfma_f32_16x16x32_bf16 v[100:103], a[212:215], v[192:195], v[100:103]\n"
    "v_rcp_f16_sdwa v232, v234 dst_sel:WORD_1 dst_unused:UNUSED_PRESERVE src0_sel:WORD_1\n"
    "v_mfma_f32_16x16x32_bf16 v[112:115], a[216:219], v[192:195], v[112:115]\n"
    "s_waitcnt lgkmcnt(0)\n"
    "v_or_b32_e32 v199, v199, v207\n"
    "v_fma_mix_f32 v234, v196, v232, 0 op_sel_hi:[1,1,0]\n"
    "v_mfma_f32_16x16x32_bf16 v[116:119], a[220:223], v[192:195], v[116:119]\n"
    "v_fma_mix_f32 v232, v196, v232, 0 op_sel:[1,1,0] op_sel_hi:[1,1,0]\n"
    "v_cvt_pk_bf16_f32 v96, v96, v97\n"
    "v_mfma_f32_16x16x32_bf16 v[128:131], v[40:43], v[192:195], v[128:131]\n"
    "v_cvt_pk_bf16_f32 v196, v234, v232\n"
    "v_cvt_pk_bf16_f32 v97, v98, v99\n"
    "v_mfma_f32_16x16x32_bf16 v[132:135], v[44:47], v[192:195], v[132:135]\n"
    "v_cvt_pk_bf16_f32 v98, v100, v101\n"
    "v_cvt_pk_bf16_f32 v99, v102, v103\n"
    "v_mfma_f32_16x16x32_bf16 v[104:107], a[208:211], v[196:199], v[104:107]\n"
    "ds_write_b128 v243, v[96:99] offset:24640\n"
    "v_mfma_f32_16x16x32_bf16 v[108:111], a[212:215], v[196:199], v[108:111]\n"
    "v_cvt_pk_bf16_f32 v112, v112, v113\n"
    "v_cvt_pk_bf16_f32 v113, v114, v115\n"
    "v_mfma_f32_16x16x32_bf16 v[120:123], a[216:219], v[196:199], v[120:123]\n"
    "v_cvt_pk_bf16_f32 v114, v116, v117\n"
    "v_cvt_pk_bf16_f32 v115, v118, v119\n"
    "v_mfma_f32_16x16x32_bf16 v[124:127], a[220:223], v[196:199], v[124:127]\n"
    "ds_read_b128 a[224:227], v249 offset:0\n"
    "ds_write_b128 v243, v[112:115] offset:26688\n"
    "v_mfma_f32_16x16x32_bf16 v[136:139], v[40:43], v[196:199], v[136:139]\n"
    "v_cvt_pk_bf16_f32 v128, v128, v129\n"
    "v_cvt_pk_bf16_f32 v129, v130, v131\n"
    "v_mfma_f32_16x16x32_bf16 v[140:143], v[44:47], v[196:199], v[140:143]\n"
    "v_cvt_pk_bf16_f32 v130, v132, v133\n"
    "v_cvt_pk_bf16_f32 v131, v134, v135\n"
    "v_cvt_pk_bf16_f32 v104, v104, v105\n"
    "ds_read_b128 a[228:231], v249 offset:2048\n"
    "ds_write_b128 v243, v[128:131] offset:28736\n"
    "v_cvt_pk_bf16_f32 v105, v106, v107\n"
    "v_cvt_pk_bf16_f32 v106, v108, v109\n"
    "v_cvt_pk_bf16_f32 v107, v110, v111\n"
    "v_cvt_pk_bf16_f32 v120, v120, v121\n"
    "v_cvt_pk_bf16_f32 v121, v122, v123\n"
    "v_cvt_pk_bf16_f32 v122, v124, v125\n"
    "v_cvt_pk_bf16_f32 v123, v126, v127\n"
    "v_cvt_pk_bf16_f32 v136, v136, v137\n"
    "ds_read_b128 a[232:235], v249 offset:4096\n"
    "s_nop 0\n"
    "s_waitcnt lgkmcnt(4)\n"
    "buffer_store_dwordx4 a[224:227], v255, s[44:47], s58 offen offset:0 sc1\n"
    "ds_write_b128 v243, v[104:107] offset:25664\n"
    "v_cvt_pk_bf16_f32 v137, v138, v139\n"
    "v_cvt_pk_bf16_f32 v138, v140, v141\n"
    "v_cvt_pk_bf16_f32 v139, v142, v143\n"
    "ds_read_b128 a[236:239], v249 offset:1024\n"
    "ds_write_b128 v243, v[120:123] offset:27712\n"
    "ds_read_b128 a[252:255], v249 offset:3072\n"
    "ds_write_b128 v243, v[136:139] offset:29760\n"
    "s_waitcnt lgkmcnt(4)\n"
    "buffer_store_dwordx4 a[228:231], v255, s[44:47], s58 offen offset:64 sc1\n"
    "ds_read_b128 a[240:243], v249 offset:5120\n"
    "buffer_store_dwordx4 a[232:235], v255, s[44:47], s58 offen offset:128 sc1\n"
    "s_waitcnt lgkmcnt(3)\n"
    "buffer_store_dwordx4 a[236:239], v255, s[44:47], s89 offen offset:0 sc1\n"
    "s_waitcnt lgkmcnt(2)\n"
    "buffer_store_dwordx4 a[252:255], v255, s[44:47], s89 offen offset:64 sc1\n"
    "s_waitcnt lgkmcnt(0)\n"
    "buffer_store_dwordx4 a[240:243], v255, s[44:47], s89 offen offset:128 sc1\n"
    "97:\n"
    "98:\n"
    "s_waitcnt vmcnt(0) lgkmcnt(0)\n"
    ".Lreplk_end:\n"
    : "+{s66}"(flg), "+{v208}"(tid), "+{s65}"(blk), "+{s[98:99]}"(ka)
    : "{s[96:97]}"(CTAB)
    : "memory", "scc", "vcc", "m0", "v0", "v1", "v2", "v3", "v4", "v5", "v6", "v7", "v8", "v9", "v10", "v11", "v12", "v13", "v14", "v15", "v16", "v17", "v18", "v19", "v20", "v21", "v22", "v23", "v24", "v25", "v26", "v27", "v28", "v29", "v30", "v31", "v32", "v33", "v34", "v35", "v36", "v37", "v38", "v39", "v40", "v41", "v42", "v43", "v44", "v45", "v46", "v47", "v48", "v49", "v50", "v51", "v52", "v53", "v54", "v55", "v56", "v57", "v58", "v59", "v60", "v61", "v62", "v63", "v64", "v65", "v66", "v67", "v68", "v69", "v70", "v71", "v72", "v73", "v74", "v75", "v76", "v77", "v78", "v79", "v80", "v81", "v82", "v83", "v84", "v85", "v86", "v87", "v88", "v89", "v90", "v91", "v92", "v93", "v94", "v95", "v96", "v97", "v98", "v99", "v100", "v101", "v102", "v103", "v104", "v105", "v106", "v107", "v108", "v109", "v110", "v111", "v112", "v113", "v114", "v115", "v116", "v117", "v118", "v119", "v120", "v121", "v122", "v123", "v124", "v125", "v126", "v127", "v128", "v129", "v130", "v131", "v132", "v133", "v134", "v135", "v136", "v137", "v138", "v139", "v140", "v141", "v142", "v143", "v144", "v145", "v146", "v147", "v148", "v149", "v150", "v151", "v152", "v153", "v154", "v155", "v156", "v157", "v158", "v159", "v160", "v161", "v162", "v163", "v164", "v165", "v166", "v167", "v168", "v169", "v170", "v171", "v172", "v173", "v174", "v175", "v176", "v177", "v178", "v179", "v180", "v181", "v182", "v183", "v184", "v185", "v186", "v187", "v188", "v189", "v190", "v191", "v192", "v193", "v194", "v195", "v196", "v197", "v198", "v199", "v200", "v201", "v202", "v203", "v204", "v205", "v206", "v207", "v209", "v210", "v211", "v212", "v213", "v214", "v215", "v216", "v217", "v218", "v219", "v220", "v221", "v222", "v223", "v224", "v225", "v226", "v227", "v228", "v229", "v230", "v231", "v232", "v233", "v234", "v235", "v236", "v237", "v238", "v239", "v240", "v241", "v242", "v243", "v244", "v245", "v246", "v247", "v248", "v249", "v250", "v251", "v252", "v253", "v254", "v255", "a0", "a1", "a2", "a3", "a4", "a5", "a6", "a7", "a8", "a9", "a10", "a11", "a12", "a13", "a14", "a15", "a16", "a17", "a18", "a19", "a20", "a21", "a22", "a23", "a24", "a25", "a26", "a27", "a28", "a29", "a30", "a31", "a32", "a33", "a34", "a35", "a36", "a37", "a38", "a39", "a40", "a41", "a42", "a43", "a44", "a45", "a46", "a47", "a48", "a49", "a50", "a51", "a52", "a53", "a54", "a55", "a56", "a57", "a58", "a59", "a60", "a61", "a62", "a63", "a64", "a65", "a66", "a67", "a68", "a69", "a70", "a71", "a72", "a73", "a74", "a75", "a76", "a77", "a78", "a79", "a80", "a81", "a82", "a83", "a84", "a85", "a86", "a87", "a88", "a89", "a90", "a91", "a92", "a93", "a94", "a95", "a96", "a97", "a98", "a99", "a100", "a101", "a102", "a103", "a104", "a105", "a106", "a107", "a108", "a109", "a110", "a111", "a112", "a113", "a114", "a115", "a116", "a117", "a118", "a119", "a120", "a121", "a122", "a123", "a124", "a125", "a126", "a127", "a128", "a129", "a130", "a131", "a132", "a133", "a134", "a135", "a136", "a137", "a138", "a139", "a140", "a141", "a142", "a143", "a144", "a145", "a146", "a147", "a148", "a149", "a150", "a151", "a152", "a153", "a154", "a155", "a156", "a157", "a158", "a159", "a160", "a161", "a162", "a163", "a164", "a165", "a166", "a167", "a168", "a169", "a170", "a171", "a172", "a173", "a174", "a175", "a176", "a177", "a178", "a179", "a180", "a181", "a182", "a183", "a184", "a185", "a186", "a187", "a188", "a189", "a190", "a191", "a192", "a193", "a194", "a195", "a196", "a197", "a198", "a199", "a200", "a201", "a202", "a203", "a204", "a205", "a206", "a207", "a208", "a209", "a210", "a211", "a212", "a213", "a214", "a215", "a216", "a217", "a218", "a219", "a220", "a221", "a222", "a223", "a224", "a225", "a226", "a227", "a228", "a229", "a230", "a231", "a232", "a233", "a234", "a235", "a236", "a237", "a238", "a239", "a240", "a241", "a242", "a243", "a244", "a245", "a246", "a247", "a248", "a249", "a250", "a251", "a252", "a253", "a254", "a255", "s36", "s37", "s38", "s39", "s40", "s41", "s42", "s43", "s44", "s45", "s46", "s47", "s48", "s49", "s50", "s51", "s52", "s53", "s54", "s55", "s56", "s57", "s58", "s59", "s60", "s61", "s62", "s63", "s64", "s67", "s68", "s69", "s70", "s71", "s72", "s73", "s74", "s75", "s76", "s77", "s78", "s79", "s80", "s81", "s82", "s83", "s84", "s85", "s86", "s87", "s88", "s89", "s90", "s91", "s92", "s93", "s94", "s95");
  if (flg != 2u) {
    const int l2 = __builtin_amdgcn_mbcnt_hi(~0u, __builtin_amdgcn_mbcnt_lo(~0u, 0u));
    slow_path(x, dwl, dws, w1, w2, out, (float*)smem, w * 64 + l2, blockIdx.x >> 3, (blockIdx.x & 7) * 4);
  }
}

extern "C" void kernel_launch(void* const* d_in, const int* in_sizes, int n_in, void* d_out, int out_size, void* d_ws,
                              size_t ws_size, hipStream_t stream) {
  replk_kernel<<<256, 256, 0, stream>>>((const u16*)d_in[0], (const u16*)d_in[1], (const u16*)d_in[2], (const u16*)d_in[3],
                                        (const u16*)d_in[4], (u16*)d_out
                                        );
}
```

```cpp
#include <hip/hip_runtime.h>
#include <hip/hip_bf16.h>

typedef unsigned u32;
typedef unsigned short u16;
typedef unsigned long long u64;
typedef u32 u32x4 __attribute__((ext_vector_type(4)));
typedef u32 u32x8 __attribute__((ext_vector_type(8)));
typedef u32 u32x16 __attribute__((ext_vector_type(16)));
typedef _Float16 f16x2 __attribute__((ext_vector_type(2)));
typedef __bf16 bf16x2 __attribute__((ext_vector_type(2)));
typedef float f32x2 __attribute__((ext_vector_type(2)));
#define DI __device__ __forceinline__
#define LDSP(T, off) ((__attribute__((address_space(3))) T*)(smem3 + (off)))

constexpr int IMG = 512;
constexpr int CH = 96;
constexpr int HID = 384;
constexpr int PXB = 192;
constexpr int ROWB = 98304;
constexpr int TOEP_CS = 336;
constexpr int XT_RS = 320;
constexpr int XT_PP = 1360;
constexpr int XT_CG = 6752;
constexpr int GS = 6160;
constexpr int L_LUT = 0;
constexpr int L_TOEP = 8208;
constexpr int L_W = 40960;
constexpr int L_LUTN = 65536;
constexpr int L_XT = 81920;
constexpr int L_H = 108928;
constexpr int L_EX = 133568;
constexpr int XT_Q = 27024;
constexpr int L_MISC = 162992;
constexpr int L_TOTAL = 163056;
constexpr int LUTK = 106;
constexpr u32 XBYTES = IMG * IMG * PXB;
constexpr float GAMMA = -0.4684938781597355f;
constexpr float GC1 = -2.302208198144325f, GC3 = -0.1029432395800235f;
constexpr u32 MU_PK = 0x44ea44eau, CC_PK = 0xb77fb77fu;
__device__ const u32 CTAB[3 * 1024] = {
671109120,671098880,134490112,673187840,671372952,671229956,145500164,673318996,671636784,671361032,156510216,673450152,671900616,671492108,167520268,673581308,672164452,684206128,178530352,686295376,672428284,684337204,189540404,686426532,672692116,684468280,200550456,686557688,672955948,684599356,211560508,686688844,673219784,697313376,222570592,699402912,673483616,697444452,233580644,699534068,673747448,697575528,244590696,699665224,674011280,697706604,255600748,699796380,674254849,710420624,266610832,712510448,674518681,710551700,277620884,712641604,674782513,710682776,288630936,712772760,675046345,710813852,299640988,712903916,
684223077,723527872,310651072,721423680,684486909,723658948,321661124,721554836,683702165,723790024,332671176,721685992,683965997,723921100,343681228,721817148,685278409,736635120,354691312,734531216,685542241,736766196,365701364,734662372,684757497,736897272,376711416,734793528,685021329,737028348,387721468,734924684,686313474,749742368,398731552,747638752,686577306,749873444,409741604,747769908,685792562,750004520,420751656,747901064,686056394,750135596,431761708,748032220,687368806,762849616,442771792,760746288,687632638,762980692,453781844,760877444,686847894,763111768,464791896,761008600,687111726,763242844,475801948,761139756,
697337034,771762560,486812032,773853824,697600866,771893636,497822084,773984980,697864698,772024712,508832136,774116136,698128530,772155788,519842188,774247292,696274947,784869808,530852272,786961360,696538779,785000884,541862324,787092516,696802611,785131960,552872376,787223672,697066443,785263036,563882428,787354828,699427431,797977056,574892512,800068896,699691263,798108132,585902564,800200052,699955095,798239208,596912616,800331208,700218927,798370284,607922668,800462364,698385611,811084304,618932752,813176432,698649443,811215380,629942804,813307588,698913275,811346456,640952856,813438744,699177107,811477532,651962908,813569900,
710430724,824191552,135539264,822089664,710694556,824322628,146549316,822220820,709909812,824453704,157559368,822351976,710173644,824584780,168569420,822483132,709388904,837298800,179579504,835197200,709652736,837429876,190589556,835328356,708867992,837560952,201599608,835459512,709131824,837692028,212609660,835590668,712541388,850406048,223619744,848304736,712805220,850537124,234629796,848435892,712020476,850668200,245639848,848567048,712284308,850799276,256649900,848698204,711479301,863513296,267659984,861412272,711743133,863644372,278670036,861543428,710958389,863775448,289680088,861674584,711222221,863906524,300690140,861805740,
723544681,677394944,311704064,679484672,723808513,677526020,322714116,679615828,724072345,677657096,333724168,679746984,724336177,677788172,344734220,679878140,724600013,690502192,355744304,692592208,724863845,690633268,366754356,692723364,725127677,690764344,377764408,692854520,725391509,690895420,388774460,692985676,721440774,703609440,399784544,705699744,721704606,703740516,410794596,705830900,721968438,703871592,421804648,705962056,722232270,704002668,432814700,706093212,722496106,716716688,443824784,718807280,722759938,716847764,454834836,718938436,723023770,716978840,465844888,719069592,723287602,717109916,476854940,719200748,
736658638,725629632,487865024,731914816,736922470,725760708,498875076,732045972,736137726,725891784,509885128,732177128,736401558,726022860,520895180,732308284,737693703,738736880,531905264,745022352,737957535,738867956,542915316,745153508,737172791,738999032,553925368,745284664,737436623,739130108,564935420,745415820,734554731,751844128,575945504,758129888,734818563,751975204,586955556,758261044,734033819,752106280,597965608,758392200,734297651,752237356,608975660,758523356,735610063,764951376,619985744,771237424,735873895,765082452,630995796,771368580,735089151,765213528,642005848,771499736,735352983,765344604,653015900,771630892,
749752328,778058624,136592256,780150656,750016160,778189700,147602308,780281812,750279992,778320776,158612360,780443436,750543824,778451852,169622412,780574508,748710508,791165872,180632496,793288492,748974340,791296948,191642548,793419564,749238172,791428024,202652600,793550636,749502004,791559100,213662652,793681708,747668688,804273120,224672736,806395692,747932520,804404196,235682788,806526764,748196352,804535272,246692840,806657836,748460184,804666348,257702892,806788908,746606601,817380368,268712976,819502892,746870433,817511444,279723028,819633964,747134265,817642520,290733080,819765036,747398097,817773596,301743132,819896108,
762866285,826293312,312753216,832610092,763130117,826424388,323763268,832741164,762345373,826555464,334773320,832872236,762609205,826686540,345783372,833003308,761824465,839400560,356793456,845717292,762088297,839531636,367803508,845848364,761303553,839662712,378813560,845979436,761567385,839793788,389823612,846110508,760762378,852507808,400833696,858824492,761026210,852638884,411843748,858955564,760241466,852769960,422853800,859086636,760505298,852901036,433863852,859217708,759720558,865615056,444873936,871931692,759984390,865746132,455883988,872062764,759199646,865877208,466894040,872193836,759463478,866008284,477904092,872324908,
771785938,671098882,488908800,673226540,772049770,671229958,499918852,673357612,772313602,671361034,510928904,673488684,772577434,671492110,521938956,673619756,772821003,684206130,532949040,686333740,773084835,684337206,543959092,686464812,773348667,684468282,554969144,686595884,773612499,684599358,565979196,686726956,773876335,697313378,576989280,699440940,774140167,697444454,587999332,699572012,774403999,697575530,599009384,699703084,774667831,697706606,610019436,699834156,774931667,710420626,621029520,712548140,775195499,710551702,632039572,712679212,775459331,710682778,643049624,712810284,775723163,710813854,654059676,712941356,
784879628,723527874,137636032,721461036,785143460,723658950,148646084,721592108,784358716,723790026,159656136,721723180,784622548,723921102,170666188,721854252,785934960,736635122,181676272,734568236,786198792,736766198,192686324,734699308,785414048,736897274,203696376,734830380,785677880,737028350,214706428,734961452,786990292,749742370,225716512,747675436,787254124,749873446,236726564,747806508,786469380,750004522,247736616,747937580,786733212,750135598,258746668,748068652,788025357,762849618,269756752,760782636,788289189,762980694,280766804,760913708,787504445,763111770,291776856,761044780,787768277,763242846,302786908,761175852,
797993585,771762562,313796992,773889836,798257417,771893638,324807044,774020908,798521249,772024714,335817096,774151980,798785081,772155790,346827148,774283052,796951765,784869810,357837232,786997036,797215597,785000886,368847284,787128108,797479429,785131962,379857336,787259180,797743261,785263038,390867388,787390252,800083982,797977058,401877472,800104236,800347814,798108134,412887524,800235308,800611646,798239210,423897576,800366380,800875478,798370286,434907628,800497452,799042162,811084306,445917712,813211436,799305994,811215382,456927764,813342508,799569826,811346458,467937816,813473580,799833658,811477534,478947868,813604652,
811107542,824191554,489957952,822124332,811371374,824322630,500968004,822255404,810586630,824453706,511978056,822386476,810850462,824584782,522988108,822517548,810045455,837298802,533998192,835231532,810309287,837429878,545008244,835362604,809524543,837560954,556018296,835493676,809788375,837692030,567028348,835624748,813197939,850406050,578038432,848338732,813461771,850537126,589048484,848469804,812677027,850668202,600058536,848600876,812940859,850799278,611068588,848731948,812156119,863513298,622078672,861445932,812419951,863644374,633088724,861577004,811635207,863775450,644098776,861708076,811899039,863906526,655108828,861839148,
824201232,677394946,138689024,679517996,824465064,677526022,149699076,679649068,824728896,677657098,160709128,679780140,824992728,677788174,171719180,679911212,825256564,690502194,182729264,692625196,825520396,690633270,193739316,692756268,825784228,690764346,204749368,692887340,826048060,690895422,215759420,693018412,822117592,703609442,226769504,705732396,822381424,703740518,237779556,705863468,822645256,703871594,248789608,705994540,822909088,704002670,259799660,706125612,823152657,716716690,270809744,718839596,823416489,716847766,281819796,718970668,823680321,716978842,292829848,719101740,823944153,717109918,303839900,719232812,
837315189,725629634,314849984,731946796,837579021,725760710,325860036,732077868,836794277,725891786,336870088,732208940,837058109,726022862,347880140,732340012,838370521,738736882,358890224,745053996,838634353,738867958,369900276,745185068,837849609,738999034,380910328,745316140,838113441,739130110,391920380,745447212,835211282,751844130,402930464,758161196,835475114,751975206,413940516,758292268,834690370,752106282,424950568,758423340,834954202,752237358,435960620,758554412,836266614,764951378,446970704,771268396,836530446,765082454,457980756,771399468,835745702,765213530,468990808,771530540,836009534,765344606,480000860,771661612,
850429146,778058626,491010944,780181292,850692978,778189702,502020996,780312364,850956810,778320778,513031048,780443436,851220642,778451854,524041100,780574508,849367059,791165874,535051184,793288492,849630891,791296950,546061236,793419564,849894723,791428026,557071288,793550636,850158555,791559102,568081340,793681708,848325239,804273122,579091424,806395692,848589071,804404198,590101476,806526764,848852903,804535274,601111528,806657836,849116735,804666350,612121580,806788908,847283419,817380370,623131664,819502892,847547251,817511446,634141716,819633964,847811083,817642522,645151768,819765036,848074915,817773598,656161820,819896108,
863541411,826293314,656161856,832610092,863803555,826424390,656161860,832741164,863017123,826555466,656161864,832872236,863279267,826686542,656161868,833003308,862492835,839400562,656161904,845717292,862754979,839531638,656161908,845848364,861968547,839662714,656161912,845979436,862230691,839793790,656161916,846110508,861444259,852507810,656161952,858824492,861706403,852638886,656161956,858955564,860919971,852769962,656161960,859086636,861182115,852901038,656161964,859217708,860395683,865615058,656162000,871931692,860657827,865746134,656162004,872062764,859871395,865877210,656162008,872193836,860133539,866008286,656162012,872324908,
2152998912u,1885629024,1342187520,1342212212,2152998916u,1885891172,1452812292,1364494564,2151688200u,1886153320,1563437064,1386776836,2218797068u,1886415468,1674061836,1409059156,2164598800u,1886677616,1342253072,1342474360,2164598804u,1886939764,1452877844,1364756712,2164271128u,1887201912,1563502616,1387038984,2231379996u,1887464060,1674127388,1409321304,1083254816,1887726208,1342318624,1342998652,1083254820,1887988356,1452943396,1365281004,1082140712,1888250504,1563568168,1387563276,1149249580,1888512652,1674192940,1409845596,1108617264,1888774800,1342384176,1342736512,1108617268,1889036948,1453008948,1365018864,1107306552,1889299096,1563633720,1387301136,1174415420,1889561244,1674258492,1409583456,
269756480,1892969168,1342449728,1342474372,269756484,1893231316,1453074500,1364756692,268445768,1893493464,1563699272,1387038964,335554636,1893755612,1674324044,1409321316,268773456,1891920576,1342515280,1342736520,268773460,1892182724,1453140052,1365018840,268445784,1892444872,1563764824,1387301112,335554652,1892707020,1674389596,1409583464,1343301728,1890871984,1342580832,1343260812,1343301732,1891134132,1453205604,1365543132,1342187624,1891396280,1563830376,1387825404,1409296492,1891658428,1674455148,1410107756,1343498352,1889823392,1342646384,1342998672,1343498356,1890085540,1453271156,1365280992,1342187640,1890347688,1563895928,1387563264,1409296508,1890609836,1674520700,1409845616,
538192000,1895066352,1342711936,1364494484,538192004,1895328500,1453336708,1386776772,536881288,1895590648,1563961480,1409059108,603990156,1895852796,1674586252,1431341364,537208976,1894017760,1342777488,1364756632,537208980,1894279908,1453402260,1387038920,536881304,1894542056,1564027032,1409321256,603990172,1894804204,1674651804,1431603512,1611737248,1897163536,1342843040,1365280924,1611737252,1897425684,1453467812,1387563212,1610623144,1897687832,1564092584,1409845548,1677732012,1897949980,1674717356,1432127804,1611933872,1896114944,1342908592,1365018784,1611933876,1896377092,1453533364,1387301072,1610623160,1896639240,1564158136,1409583408,1677732028,1896901388,1674782908,1431865664,
810821824,1900309312,1342974144,1364756644,810821828,1900571460,1453598916,1387038900,809511112,1900833608,1564223688,1409321236,876619980,1901095756,1674848460,1431603524,822421712,1901357904,1343039696,1365018792,822421716,1901620052,1453664468,1387301048,822094040,1901882200,1564289240,1409583384,889202908,1902144348,1674914012,1431865672,1888561376,1898212128,1343105248,1365543084,1888561380,1898474276,1453730020,1387825340,1887447272,1898736424,1564354792,1410107676,1954556140,1898998572,1674979564,1432389964,1913923824,1899260720,1343170800,1365280944,1913923828,1899522868,1453795572,1387563200,1912613112,1899785016,1564420344,1409845536,1979721980,1900047164,1675045116,1432127824,
2152999168u,1784705124,1347430656,1452835440,2152999172u,1784967272,1458055428,1475117792,2151688456u,1785229420,1568680200,1497400064,2218797324u,1785491568,1679304972,1519682384,2164599056u,1785753716,1347496208,1453097588,2164599060u,1786015864,1458120980,1475379940,2164271384u,1786278012,1568745752,1497662212,2231380252u,1786540160,1679370524,1519944532,1083255072,1786802308,1347561760,1453621880,1083255076,1787064456,1458186532,1475904232,1082140968,1787326604,1568811304,1498186504,1149249836,1787588752,1679436076,1520468824,1108617520,1787850900,1347627312,1453359740,1108617524,1788113048,1458252084,1475642092,1107306808,1788375196,1568876856,1497924364,1174415676,1788637344,1679501628,1520206684,
269756736,1792045268,1347692864,1453097600,269756740,1792307416,1458317636,1475379920,268446024,1792569564,1568942408,1497662192,335554892,1792831712,1679567180,1519944544,268773712,1790996676,1347758416,1453359748,268773716,1791258824,1458383188,1475642068,268446040,1791520972,1569007960,1497924340,335554908,1791783120,1679632732,1520206692,1343301984,1789948084,1347823968,1453884040,1343301988,1790210232,1458448740,1476166360,1342187880,1790472380,1569073512,1498448632,1409296748,1790734528,1679698284,1520730984,1343498608,1788899492,1347889520,1453621900,1343498612,1789161640,1458514292,1475904220,1342187896,1789423788,1569139064,1498186492,1409296764,1789685936,1679763836,1520468844,
538192256,1794142452,1347955072,1475117712,538192260,1794404600,1458579844,1497400000,536881544,1794666748,1569204616,1519682336,603990412,1794928896,1679829388,1541964592,537209232,1793093860,1348020624,1475379860,537209236,1793356008,1458645396,1497662148,536881560,1793618156,1569270168,1519944484,603990428,1793880304,1679894940,1542226740,1611737504,1796239636,1348086176,1475904152,1611737508,1796501784,1458710948,1498186440,1610623400,1796763932,1569335720,1520468776,1677732268,1797026080,1679960492,1542751032,1611934128,1795191044,1348151728,1475642012,1611934132,1795453192,1458776500,1497924300,1610623416,1795715340,1569401272,1520206636,1677732284,1795977488,1680026044,1542488892,
810822080,1799385412,1348217280,1475379872,810822084,1799647560,1458842052,1497662128,809511368,1799909708,1569466824,1519944464,876620236,1800171856,1680091596,1542226752,822421968,1800434004,1348282832,1475642020,822421972,1800696152,1458907604,1497924276,822094296,1800958300,1569532376,1520206612,889203164,1801220448,1680157148,1542488900,1888561632,1797288228,1348348384,1476166312,1888561636,1797550376,1458973156,1498448568,1887447528,1797812524,1569597928,1520730904,1954556396,1798074672,1680222700,1543013192,1913924080,1798336820,1348413936,1475904172,1913924084,1798598968,1459038708,1498186428,1912613368,1798861116,1569663480,1520468764,1979722236,1799123264,1680288252,1542751052,
2152999424u,2087482984,1352673280,1563464828,2152999428u,2087745132,1463298052,1585747180,2151688712u,2088007280,1573922824,1608029452,2218797580u,2088269428,1684547596,1630311772,2164599312u,2088531576,1352738832,1563726976,2164599316u,2088793724,1463363604,1586009328,2164271640u,2089055872,1573988376,1608291600,2231380508u,2089318020,1684613148,1630573920,1083255328,2089580168,1352804384,1564251268,1083255332,2089842316,1463429156,1586533620,1082141224,2090104464,1574053928,1608815892,1149250092,2090366612,1684678700,1631098212,1108617776,2090628760,1352869936,1563989128,1108617780,2090890908,1463494708,1586271480,1107307064,2091153056,1574119480,1608553752,1174415932,2091415204,1684744252,1630836072,
269756992,2094823128,1352935488,1563726988,269756996,2095085276,1463560260,1586009308,268446280,2095347424,1574185032,1608291580,335555148,2095609572,1684809804,1630573932,268773968,2093774536,1353001040,1563989136,268773972,2094036684,1463625812,1586271456,268446296,2094298832,1574250584,1608553728,335555164,2094560980,1684875356,1630836080,1343302240,2092725944,1353066592,1564513428,1343302244,2092988092,1463691364,1586795748,1342188136,2093250240,1574316136,1609078020,1409297004,2093512388,1684940908,1631360372,1343498864,2091677352,1353132144,1564251288,1343498868,2091939500,1463756916,1586533608,1342188152,2092201648,1574381688,1608815880,1409297020,2092463796,1685006460,1631098232,
538192512,2096920312,1353197696,1585747100,538192516,2097182460,1463822468,1608029388,536881800,2097444608,1574447240,1630311724,603990668,2097706756,1685072012,1652593980,537209488,2095871720,1353263248,1586009248,537209492,2096133868,1463888020,1608291536,536881816,2096396016,1574512792,1630573872,603990684,2096658164,1685137564,1652856128,1611737760,2099017496,1353328800,1586533540,1611737764,2099279644,1463953572,1608815828,1610623656,2099541792,1574578344,1631098164,1677732524,2099803940,1685203116,1653380420,1611934384,2097968904,1353394352,1586271400,1611934388,2098231052,1464019124,1608553688,1610623672,2098493200,1574643896,1630836024,1677732540,2098755348,1685268668,1653118280,
810822336,2102163272,1353459904,1586009260,810822340,2102425420,1464084676,1608291516,809511624,2102687568,1574709448,1630573852,876620492,2102949716,1685334220,1652856140,822422224,2103211864,1353525456,1586271408,822422228,2103474012,1464150228,1608553664,822094552,2103736160,1574775000,1630836000,889203420,2103998308,1685399772,1653118288,1888561888,2100066088,1353591008,1586795700,1888561892,2100328236,1464215780,1609077956,1887447784,2100590384,1574840552,1631360292,1954556652,2100852532,1685465324,1653642580,1913924336,2101114680,1353656560,1586533560,1913924340,2101376828,1464281332,1608815816,1912613624,2101638976,1574906104,1631098152,1979722492,2101901124,1685530876,1653380440,
2152999680u,1986559084,1357916416,1674088056,2152999684u,1986821232,1468541188,1696370408,2151688968u,1987083380,1579165960,1718652680,2218797836u,1987345528,1689790732,1740935000,2164599568u,1987607676,1357981968,1674350204,2164599572u,1987869824,1468606740,1696632556,2164271896u,1988131972,1579231512,1718914828,2231380764u,1988394120,1689856284,1741197148,1083255584,1988656268,1358047520,1674874496,1083255588,1988918416,1468672292,1697156848,1082141480,1989180564,1579297064,1719439120,1149250348,1989442712,1689921836,1741721440,1108618032,1989704860,1358113072,1674612356,1108618036,1989967008,1468737844,1696894708,1107307320,1990229156,1579362616,1719176980,1174416188,1990491304,1689987388,1741459300,
269757248,1993899228,1358178624,1674350216,269757252,1994161376,1468803396,1696632536,268446536,1994423524,1579428168,1718914808,335555404,1994685672,1690052940,1741197160,268774224,1992850636,1358244176,1674612364,268774228,1993112784,1468868948,1696894684,268446552,1993374932,1579493720,1719176956,335555420,1993637080,1690118492,1741459308,1343302496,1991802044,1358309728,1675136656,1343302500,1992064192,1468934500,1697418976,1342188392,1992326340,1579559272,1719701248,1409297260,1992588488,1690184044,1741983600,1343499120,1990753452,1358375280,1674874516,1343499124,1991015600,1469000052,1697156836,1342188408,1991277748,1579624824,1719439108,1409297276,1991539896,1690249596,1741721460,
538192768,1995996412,1358440832,1696370328,538192772,1996258560,1469065604,1718652616,536882056,1996520708,1579690376,1740934952,603990924,1996782856,1690315148,1763217208,537209744,1994947820,1358506384,1696632476,537209748,1995209968,1469131156,1718914764,536882072,1995472116,1579755928,1741197100,603990940,1995734264,1690380700,1763479356,1611738016,1998093596,1358571936,1697156768,1611738020,1998355744,1469196708,1719439056,1610623912,1998617892,1579821480,1741721392,1677732780,1998880040,1690446252,1764003648,1611934640,1997045004,1358637488,1696894628,1611934644,1997307152,1469262260,1719176916,1610623928,1997569300,1579887032,1741459252,1677732796,1997831448,1690511804,1763741508,
810822592,2001239372,1358703040,1696632488,810822596,2001501520,1469327812,1718914744,809511880,2001763668,1579952584,1741197080,876620748,2002025816,1690577356,1763479368,822422480,2002287964,1358768592,1696894636,822422484,2002550112,1469393364,1719176892,822094808,2002812260,1580018136,1741459228,889203676,2003074408,1690642908,1763741516,1888562144,1999142188,1358834144,1697418928,1888562148,1999404336,1469458916,1719701184,1887448040,1999666484,1580083688,1741983520,1954556908,1999928632,1690708460,1764265808,1913924592,2000190780,1358899696,1697156788,1913924596,2000452928,1469524468,1719439044,1912613880,2000715076,1580149240,1741721380,1979722748,2000977224,1690774012,1764003668,
136579076,1343253088,52428,12582912,135530496,1453879396,843229,12648449,134481920,1564505704,1634030,12713986,145229824,1675132012,2411724,12779523,136841224,1343318640,3198156,13369356,135792808,1453944948,3988957,13434893,134744232,1564571256,4779758,13500430,139724968,1675197564,5557452,13565967,137103372,1343384208,6343884,14155800,136054952,1454010516,7134685,14221337,135006376,1564636824,7925486,14286874,139724968,1675263132,8703180,14352411,137365520,1343449728,9489612,14942244,136316928,1454076036,10280413,15007781,135268352,1564702344,11071214,15073318,145229824,1675328652,11848908,15138855,
139724868,1365535332,117964,12582960,139724852,1476161640,908765,12648497,138676260,1586787948,1699566,12714034,137627668,1697414256,2477260,12779571,145229896,1365600884,3263692,13369404,145229880,1476227192,4054493,13434941,138938408,1586853500,4845294,13500478,137889816,1697479808,5622988,13566015,145229900,1365666452,6409420,14155848,145229884,1476292760,7200221,14221385,139200556,1586919068,7991022,14286922,138151964,1697545376,8768716,14352459,139724880,1365731972,9555148,14942292,139724864,1476358280,10345949,15007829,139462704,1586984588,11136750,15073366,138414112,1697610896,11914444,15138903,
137627668,1387817576,183500,12583008,136579076,1498443884,974301,12648545,135530496,1609070192,1765102,12714082,134481920,1719696500,2542796,12779619,137889816,1387883128,3329228,13369452,136841224,1498509436,4120029,13434989,135792808,1609135744,4910830,13500526,134744232,1719762052,5688524,13566063,138151964,1387948696,6474956,14155896,137103372,1498575004,7265757,14221433,136054952,1609201312,8056558,14286970,135006376,1719827620,8834252,14352507,138414112,1388014216,9620684,14942340,137365520,1498640524,10411485,15007877,136316928,1609266832,11202286,15073414,135268352,1719893140,11979980,15138951,
139724968,1410099820,249036,12583056,139724868,1520726128,1039837,12648593,139724852,1631352436,1830638,12714130,138676260,1741978744,2608332,12779667,145229824,1410165372,3394764,13369500,145229896,1520791680,4185565,13435037,145229880,1631417988,4976366,13500574,138938408,1742044296,5754060,13566111,145229824,1410230940,6540492,14155944,145229900,1520857248,7331293,14221481,145229884,1631483556,8122094,14287018,139200556,1742109864,8899788,14352555,139724968,1410296460,9686220,14942388,139724880,1520922768,10477021,15007925,139724864,1631549076,11267822,15073462,139462704,1742175384,12045516,15138999,
180619940,1348496080,52428,12582912,179571360,1459122388,843229,12648449,178522784,1569748696,1634030,12713986,189270688,1680375004,2411724,12779523,180882088,1348561600,3198156,13369356,179833672,1459187908,3988957,13434893,178785096,1569814216,4779758,13500430,183765832,1680440524,5557452,13565967,181144236,1348627104,6343884,14155800,180095816,1459253412,7134685,14221337,179047240,1569879720,7925486,14286874,183765832,1680506028,8703180,14352411,181406384,1348692656,9489612,14942244,180357792,1459318964,10280413,15007781,179309216,1569945272,11071214,15073318,189270688,1680571580,11848908,15138855,
183765732,1370778324,117964,12582960,183765716,1481404632,908765,12648497,182717124,1592030940,1699566,12714034,181668532,1702657248,2477260,12779571,189270760,1370843844,3263692,13369404,189270744,1481470152,4054493,13434941,182979272,1592096460,4845294,13500478,181930680,1702722768,5622988,13566015,189270764,1370909348,6409420,14155848,189270748,1481535656,7200221,14221385,183241420,1592161964,7991022,14286922,182192828,1702788272,8768716,14352459,183765744,1370974900,9555148,14942292,183765728,1481601208,10345949,15007829,183503568,1592227516,11136750,15073366,182454976,1702853824,11914444,15138903,
181668532,1393060568,183500,12583008,180619940,1503686876,974301,12648545,179571360,1614313184,1765102,12714082,178522784,1724939492,2542796,12779619,181930680,1393126088,3329228,13369452,180882088,1503752396,4120029,13434989,179833672,1614378704,4910830,13500526,178785096,1725005012,5688524,13566063,182192828,1393191592,6474956,14155896,181144236,1503817900,7265757,14221433,180095816,1614444208,8056558,14286970,179047240,1725070516,8834252,14352507,182454976,1393257144,9620684,14942340,181406384,1503883452,10411485,15007877,180357792,1614509760,11202286,15073414,179309216,1725136068,11979980,15138951,
183765832,1415342812,249036,12583056,183765732,1525969120,1039837,12648593,183765716,1636595428,1830638,12714130,182717124,1747221736,2608332,12779667,189270688,1415408332,3394764,13369500,189270760,1526034640,4185565,13435037,189270744,1636660948,4976366,13500574,182979272,1747287256,5754060,13566111,189270688,1415473836,6540492,14155944,189270764,1526100144,7331293,14221481,189270748,1636726452,8122094,14287018,183241420,1747352760,8899788,14352555,183765832,1415539388,9686220,14942388,183765744,1526165696,10477021,15007925,183765728,1636792004,11267822,15073462,183503568,1747418312,12045516,15138999,
224660804,1353738992,52428,12582912,223612224,1464365300,843229,12648449,222563648,1574991608,1634030,12713986,233311552,1685617916,2411724,12779523,224922952,1353804512,3198156,13369356,223874536,1464430820,3988957,13434893,222825960,1575057128,4779758,13500430,227806696,1685683436,5557452,13565967,225185100,1353870080,6343884,14155800,224136680,1464496388,7134685,14221337,223088104,1575122696,7925486,14286874,227806696,1685749004,8703180,14352411,225447248,1353935632,9489612,14942244,224398656,1464561940,10280413,15007781,223350080,1575188248,11071214,15073318,233311552,1685814556,11848908,15138855,
227806596,1376021236,117964,12582960,227806580,1486647544,908765,12648497,226757988,1597273852,1699566,12714034,225709396,1707900160,2477260,12779571,233311624,1376086756,3263692,13369404,233311608,1486713064,4054493,13434941,227020136,1597339372,4845294,13500478,225971544,1707965680,5622988,13566015,233311628,1376152324,6409420,14155848,233311612,1486778632,7200221,14221385,227282284,1597404940,7991022,14286922,226233692,1708031248,8768716,14352459,227806608,1376217876,9555148,14942292,227806592,1486844184,10345949,15007829,227544432,1597470492,11136750,15073366,226495840,1708096800,11914444,15138903,
225709396,1398303480,183500,12583008,224660804,1508929788,974301,12648545,223612224,1619556096,1765102,12714082,222563648,1730182404,2542796,12779619,225971544,1398369000,3329228,13369452,224922952,1508995308,4120029,13434989,223874536,1619621616,4910830,13500526,222825960,1730247924,5688524,13566063,226233692,1398434568,6474956,14155896,225185100,1509060876,7265757,14221433,224136680,1619687184,8056558,14286970,223088104,1730313492,8834252,14352507,226495840,1398500120,9620684,14942340,225447248,1509126428,10411485,15007877,224398656,1619752736,11202286,15073414,223350080,1730379044,11979980,15138951,
227806696,1420585724,249036,12583056,227806596,1531212032,1039837,12648593,227806580,1641838340,1830638,12714130,226757988,1752464648,2608332,12779667,233311552,1420651244,3394764,13369500,233311624,1531277552,4185565,13435037,233311608,1641903860,4976366,13500574,227020136,1752530168,5754060,13566111,233311552,1420716812,6540492,14155944,233311628,1531343120,7331293,14221481,233311612,1641969428,8122094,14287018,227282284,1752595736,8899788,14352555,227806696,1420782364,9686220,14942388,227806608,1531408672,10477021,15007925,227806592,1642034980,11267822,15073462,227544432,1752661288,12045516,15138999,
268701668,1358981952,52428,12582912,267653088,1469608260,843229,12648449,266604512,1580234568,1634030,12713986,277352416,1690860876,2411724,12779523,268963816,1359047504,3198156,13369356,267915400,1469673812,3988957,13434893,266866824,1580300120,4779758,13500430,271847560,1690926428,5557452,13565967,269225964,1359113008,6343884,14155800,268177544,1469739316,7134685,14221337,267128968,1580365624,7925486,14286874,271847560,1690991932,8703180,14352411,269488112,1359178528,9489612,14942244,268439520,1469804836,10280413,15007781,267390944,1580431144,11071214,15073318,277352416,1691057452,11848908,15138855,
271847460,1381264196,117964,12582960,271847444,1491890504,908765,12648497,270798852,1602516812,1699566,12714034,269750260,1713143120,2477260,12779571,277352488,1381329748,3263692,13369404,277352472,1491956056,4054493,13434941,271061000,1602582364,4845294,13500478,270012408,1713208672,5622988,13566015,277352492,1381395252,6409420,14155848,277352476,1492021560,7200221,14221385,271323148,1602647868,7991022,14286922,270274556,1713274176,8768716,14352459,271847472,1381460772,9555148,14942292,271847456,1492087080,10345949,15007829,271585296,1602713388,11136750,15073366,270536704,1713339696,11914444,15138903,
269750260,1403546440,183500,12583008,268701668,1514172748,974301,12648545,267653088,1624799056,1765102,12714082,266604512,1735425364,2542796,12779619,270012408,1403611992,3329228,13369452,268963816,1514238300,4120029,13434989,267915400,1624864608,4910830,13500526,266866824,1735490916,5688524,13566063,270274556,1403677496,6474956,14155896,269225964,1514303804,7265757,14221433,268177544,1624930112,8056558,14286970,267128968,1735556420,8834252,14352507,270536704,1403743016,9620684,14942340,269488112,1514369324,10411485,15007877,268439520,1624995632,11202286,15073414,267390944,1735621940,11979980,15138951,
271847560,1425828684,249036,12583056,271847460,1536454992,1039837,12648593,271847444,1647081300,1830638,12714130,270798852,1757707608,2608332,12779667,277352416,1425894236,3394764,13369500,277352488,1536520544,4185565,13435037,277352472,1647146852,4976366,13500574,271061000,1757773160,5754060,13566111,277352416,1425959740,6540492,14155944,277352492,1536586048,7331293,14221481,277352476,1647212356,8122094,14287018,271323148,1757838664,8899788,14352555,271847560,1426025260,9686220,14942388,271847472,1536651568,10477021,15007925,271847456,1647277876,11267822,15073462,271585296,1757904184,12045516,15138999};

DI float bf_lo(u32 v) { return __uint_as_float(v << 16); }
DI u32 pk_bf16(float a, float b) { f32x2 t = {a, b}; return __builtin_bit_cast(u32, __builtin_convertvector(t, bf16x2)); }

DI void slow_path(const u16* __restrict__ x, const u16* __restrict__ dwl, const u16* __restrict__ dws, const u16* __restrict__ w1,
                  const u16* __restrict__ w2, u16* __restrict__ out, float* sm, int t, int tx, int ty0) {
  float* o = sm;
  float* g = sm + 8 * CH;
  float* st = g + 8 * HID;
#pragma unroll 1
  for (int b = 0; b < 128; ++b) {
    const int y = ty0 * 16 + (b >> 1), x0 = tx * 16 + (b & 1) * 8;
    __syncthreads();
#pragma unroll 1
    for (int i = t; i < 8 * CH; i += 256) {
      const int p = i / CH, c = i - p * CH;
      float acc = 0.f;
#pragma unroll 1
      for (int a = 0; a < 5; ++a)
#pragma unroll 1
        for (int e = 0; e < 5; ++e) {
          const int yy = y + a - 2, xc = x0 + p + e - 2;
          if (yy < 0 || yy >= IMG || xc < 0 || xc >= IMG) continue;
          const float xv = bf_lo(x[(size_t)(yy * IMG + xc) * CH + c]);
          acc = __builtin_fmaf(bf_lo(dws[(a * 5 + e) * CH + c]), xv, acc);
          if (a >= 1 && a <= 3 && e >= 1 && e <= 3) acc = __builtin_fmaf(bf_lo(dwl[((a - 1) * 3 + (e - 1)) * CH + c]), xv, acc);
        }
      o[i] = acc;
    }
    __syncthreads();
    if (t < 8) {
      float m = 0.f, v = 0.f;
#pragma unroll 1
      for (int c = 0; c < CH; ++c) m += o[t * CH + c];
      m *= (1.0f / CH);
#pragma unroll 1
      for (int c = 0; c < CH; ++c) { const float d = o[t * CH + c] - m; v = __builtin_fmaf(d, d, v); }
      st[2 * t] = m; st[2 * t + 1] = __builtin_amdgcn_rsqf(v * (1.0f / CH) + 1e-5f);
    }
    __syncthreads();
#pragma unroll 1
    for (int i = t; i < 8 * HID; i += 256) {
      const int p = i / HID, e = i - p * HID;
      const float m = st[2 * p], r = st[2 * p + 1];
      float u = 0.f;
#pragma unroll 1
      for (int c = 0; c < CH; ++c) u = __builtin_fmaf((o[p * CH + c] - m) * r, bf_lo(w1[c * HID + e]), u);
      const float z = u * __builtin_fmaf(u * u, GC3, GC1);
      g[i] = u * __builtin_amdgcn_rcpf(1.0f + __builtin_amdgcn_exp2f(z));
    }
    __syncthreads();
#pragma unroll 1
    for (int i = t; i < 8 * CH; i += 256) {
      const int p = i / CH, c = i - p * CH;
      float acc = 0.f;
#pragma unroll 1
      for (int e = 0; e < HID; ++e) acc = __builtin_fmaf(g[p * HID + e], bf_lo(w2[e * CH + c]), acc);
      const size_t idx = (size_t)(y * IMG + x0 + p) * CH + c;
      out[idx] = (u16)pk_bf16(bf_lo(x[idx]) + acc, 0.f);
    }
  }
}

__global__ __launch_bounds__(256, 1) void replk_kernel(const u16* __restrict__ x, const u16* __restrict__ dwl,
    const u16* __restrict__ dws, const u16* __restrict__ w1, const u16* __restrict__ w2, u16* __restrict__ out
    ) {
  __shared__ __attribute__((aligned(16))) char smem[L_TOTAL];
  __attribute__((address_space(3))) char* smem3 = (__attribute__((address_space(3))) char*)smem;
  const int w = __builtin_amdgcn_readfirstlane(threadIdx.x >> 6);
  u32 tid = threadIdx.x, blk = blockIdx.x;
  u32 flg = (u32)(size_t)smem3 ? 1u : 0u;
  u64 ka = (u64)__builtin_amdgcn_kernarg_segment_ptr();
  asm volatile(
    ".p2align 3\n"
    "s_load_dwordx2 s[40:41], s[98:99], 0x0\n"
    "s_load_dwordx2 s[88:89], s[98:99], 0x10\n"
    "s_load_dwordx2 s[92:93], s[98:99], 0x8\n"
    "s_load_dwordx2 s[84:85], s[98:99], 0x20\n"
    "s_load_dwordx2 s[80:81], s[98:99], 0x18\n"
    "s_load_dwordx2 s[44:45], s[98:99], 0x28\n"
    "v_lshlrev_b32_e32 v213, 4, v208\n"
    "v_lshrrev_b32_e32 v0, 6, v208\n"
    "s_add_u32 vcc_lo, s96, 0\n"
    "s_addc_u32 vcc_hi, s97, 0\n"
    "global_load_dwordx4 v[168:171], v213, vcc\n"
    "s_add_u32 vcc_lo, s96, 4096\n"
    "s_addc_u32 vcc_hi, s97, 0\n"
    "global_load_dwordx4 v[172:175], v213, vcc\n"
    "s_add_u32 vcc_lo, s96, 8192\n"
    "s_addc_u32 vcc_hi, s97, 0\n"
    "global_load_dwordx4 v[176:179], v213, vcc\n"
    "s_getpc_b64 s[76:77]\n"
    ".Lreplk_pc0:\n"
    "s_bfe_u32 s63, s65, 0x30003\n"
    "v_lshrrev_b32_e32 v5, 2, v208\n"
    "s_lshl_b32 s63, s63, 6\n"
    "v_add_lshl_u32 v5, s63, v5, 7\n"
    "v_min_u32_e32 v5, .Lreplk_end-.Lreplk_pc0-4, v5\n"
    "global_load_dword v233, v5, s[76:77]\n"
    "v_readfirstlane_b32 s60, v0\n"
    "v_min_u32_e32 v1, 239, v208\n"
    "v_mul_u32_u24_e32 v2, 1366, v1\n"
    "s_lshr_b32 s61, s65, 3\n"
    "s_and_b32 s62, s65, 7\n"
    "v_lshrrev_b32_e32 v2, 16, v2\n"
    "v_lshlrev_b32_e32 v3, 2, v1\n"
    "s_lshl_b32 s62, s62, 6\n"
    "s_movk_i32 s63, 768\n"
    "v_mad_u32_u24 v211, v2, s63, v3\n"
    "v_mul_u32_u24_e32 v4, 384, v2\n"
    "v_add_u32_e32 v2, -1, v2\n"
    "s_movk_i32 s90, 4800\n"
    "s_mov_b32 s91, 0x20000\n"
    "v_add_u32_e32 v3, -576, v3\n"
    "v_cmp_gt_u32_e32 vcc, 3, v2\n"
    "v_add_u32_e32 v3, v3, v4\n"
    "v_bfrev_b32_e32 v4, 1\n"
    "s_movk_i32 s94, 1728\n"
    "s_mov_b32 s95, 0x20000\n"
    "v_cndmask_b32_e32 v212, v4, v3, vcc\n"
    "s_lshl_b32 s99, s62, 5\n"
    "s_add_u32 s99, s99, s61\n"
    "s_mulk_i32 s99, 3072\n"
    "s_lshr_b32 s51, s60, 1\n"
    "s_mul_i32 s63, s51, 18432\n"
    "s_lshl_b32 s52, s60, 13\n"
    "s_add_u32 s63, s63, s52\n"
    "v_add_u32_e32 v210, s63, v213\n"
    "s_mul_i32 s38, s60, 9216\n"
    "s_add_u32 s38, s38, 40960\n"
    "s_mov_b32 s86, 73728\n"
    "s_mov_b32 s87, 0x20000\n"
    "v_and_b32_e32 v5, 63, v208\n"
    "v_min_u32_e32 v5, 8, v5\n"
    "s_and_b32 s52, s60, 1\n"
    "s_lshl_b32 s53, s61, 1\n"
    "s_add_u32 s52, s52, s53\n"
    "s_mul_i32 s52, s52, 9\n"
    "v_add_lshl_u32 v214, s52, v5, 7\n"
    "s_cmp_eq_u32 s61, 0\n"
    "s_cselect_b32 s52, 24, 0\n"
    "s_cmp_eq_u32 s61, 31\n"
    "s_cselect_b32 s53, 40, s52\n"
    "s_sub_u32 s53, 256, s53\n"
    "v_subrev_u32_e32 v6, s52, v208\n"
    "s_sub_u32 s63, s99, 384\n"
    "v_cmp_gt_u32_e32 vcc, s53, v6\n"
    "v_lshl_add_u32 v7, v1, 4, s63\n"
    "v_bfe_u32 v8, v208, 4, 2\n"
    "v_lshl_or_b32 v8, v0, 3, v8\n"
    "v_and_b32_e32 v9, 0xf0, v213\n"
    "v_cndmask_b32_e32 v208, v4, v7, vcc\n"
    "v_mul_u32_u24_e32 v8, 768, v8\n"
    "s_mov_b32 s42, 50331648\n"
    "s_mov_b32 s43, 0x20000\n"
    "v_or_b32_e32 v209, v8, v9\n"
    "s_mov_b32 s82, 73728\n"
    "s_mov_b32 s83, 0x20000\n"
    "s_cmp_eq_u32 s62, 0\n"
    "s_cselect_b32 s78, 0, s42\n"
    "s_mov_b32 s79, 0x20000\n"
    "s_waitcnt lgkmcnt(0)\n"
    "s_cmp_lt_u32 s60, 2\n"
    "s_nop 0\n"
    "s_cselect_b64 s[70:71], s[80:81], s[84:85]\n"
    "s_sub_u32 s76, s40, 196608\n"
    "s_subb_u32 s77, s41, 0\n"
    "s_mov_b32 s65, 0\n"
    "buffer_load_dwordx4 a[224:227], v208, s[76:79], s65 offen\n"
    "s_mov_b32 s65, 98304\n"
    "buffer_load_dwordx4 a[228:231], v208, s[76:79], s65 offen\n"
    "s_mov_b32 s65, 196608\n"
    "buffer_load_dwordx4 a[232:235], v208, s[40:43], s65 offen\n"
    "s_mov_b32 s65, 294912\n"
    "buffer_load_dwordx4 a[236:239], v208, s[40:43], s65 offen\n"
    "buffer_load_dword v224, v211, s[88:91], 0 offen offset:0\n"
    "buffer_load_dword v225, v211, s[88:91], 0 offen offset:192\n"
    "buffer_load_dword v226, v211, s[88:91], 0 offen offset:384\n"
    "buffer_load_dword v227, v211, s[88:91], 0 offen offset:576\n"
    "buffer_load_dword v228, v211, s[88:91], 0 offen offset:768\n"
    "v_mov_b32_e64 v88, 0\n"
    "v_mov_b32_e32 v240, 0xdddd8888\n"
    "v_mov_b32_e64 v89, 0\n"
    "v_mov_b32_e64 v200, 0\n"
    "v_mov_b32_e64 v90, 0\n"
    "v_mov_b32_e64 v91, 0\n"
    "v_mov_b32_e64 v59, 0\n"
    "v_mov_b32_e64 v60, 0\n"
    "v_mov_b32_e64 v67, 0\n"
    "v_mov_b32_e64 v68, 0\n"
    "v_mov_b32_e64 v75, 0\n"
    "v_mov_b32_e64 v76, 0\n"
    "v_mov_b32_e64 v83, 0\n"
    "v_mov_b32_e64 v84, 0\n"
    "v_mov_b32_e32 v36, 0x75089ba4\n"
    "v_mov_b32_e32 v38, 0xbdd2d3e8\n"
    "v_mov_b32_e32 v32, 0xc0135761\n"
    "v_lshlrev_b32_e64 v34, 16, v213\n"
    "v_lshlrev_b32_e64 v234, 1, v213\n"
    "v_or_b32_e32 v0, 0x8000, v34\n"
    "v_or_b32_e32 v1, 0x18000, v34\n"
    "v_or_b32_e32 v2, 0x28000, v34\n"
    "v_or_b32_e32 v3, 0x38000, v34\n"
    "v_or_b32_e32 v4, 0x48000, v34\n"
    "v_or_b32_e32 v5, 0x58000, v34\n"
    "v_or_b32_e32 v6, 0x68000, v34\n"
    "v_or_b32_e32 v7, 0x78000, v34\n"
    "v_cmp_ne_u32_e64 s[72:73], 0, v34\n"
    "s_nop 0\n"
    "s_nop 1\n"
    "v_cndmask_b32_e64 v0, 0, v0, s[72:73]\n"
    "v_pk_mul_f32 v[0:1], v[0:1], v[36:37] op_sel_hi:[1,0]\n"
    "v_pk_mul_f32 v[2:3], v[2:3], v[36:37] op_sel_hi:[1,0]\n"
    "v_pk_mul_f32 v[4:5], v[4:5], v[36:37] op_sel_hi:[1,0]\n"
    "v_pk_mul_f32 v[6:7], v[6:7], v[36:37] op_sel_hi:[1,0]\n"
    "v_pk_mul_f32 v[8:9], v[0:1], v[0:1]\n"
    "v_pk_mul_f32 v[10:11], v[2:3], v[2:3]\n"
    "v_pk_mul_f32 v[12:13], v[4:5], v[4:5]\n"
    "v_pk_mul_f32 v[14:15], v[6:7], v[6:7]\n"
    "v_pk_fma_f32 v[8:9], v[8:9], v[38:39], v[32:33] op_sel_hi:[1,0,0]\n"
    "v_pk_fma_f32 v[10:11], v[10:11], v[38:39], v[32:33] op_sel_hi:[1,0,0]\n"
    "v_pk_fma_f32 v[12:13], v[12:13], v[38:39], v[32:33] op_sel_hi:[1,0,0]\n"
    "v_pk_fma_f32 v[14:15], v[14:15], v[38:39], v[32:33] op_sel_hi:[1,0,0]\n"
    "v_pk_mul_f32 v[8:9], v[8:9], v[0:1]\n"
    "v_pk_mul_f32 v[10:11], v[10:11], v[2:3]\n"
    "v_pk_mul_f32 v[12:13], v[12:13], v[4:5]\n"
    "v_pk_mul_f32 v[14:15], v[14:15], v[6:7]\n"
    "v_exp_f32_e64 v8, v8\n"
    "v_exp_f32_e64 v9, v9\n"
    "v_exp_f32_e64 v10, v10\n"
    "v_exp_f32_e64 v11, v11\n"
    "v_exp_f32_e64 v12, v12\n"
    "v_exp_f32_e64 v13, v13\n"
    "v_exp_f32_e64 v14, v14\n"
    "v_exp_f32_e64 v15, v15\n"
    "v_pk_add_f32 v[16:17], v[8:9], 1.0 op_sel_hi:[1,0]\n"
    "v_pk_add_f32 v[18:19], v[10:11], 1.0 op_sel_hi:[1,0]\n"
    "v_pk_add_f32 v[20:21], v[12:13], 1.0 op_sel_hi:[1,0]\n"
    "v_pk_add_f32 v[22:23], v[14:15], 1.0 op_sel_hi:[1,0]\n"
    "v_rcp_f32_e64 v16, v16\n"
    "v_rcp_f32_e64 v17, v17\n"
    "v_rcp_f32_e64 v18, v18\n"
    "v_rcp_f32_e64 v19, v19\n"
    "v_rcp_f32_e64 v20, v20\n"
    "v_rcp_f32_e64 v21, v21\n"
    "v_rcp_f32_e64 v22, v22\n"
    "v_rcp_f32_e64 v23, v23\n"
    "v_pk_mul_f32 v[16:17], v[16:17], v[0:1]\n"
    "v_pk_mul_f32 v[18:19], v[18:19], v[2:3]\n"
    "v_pk_mul_f32 v[20:21], v[20:21], v[4:5]\n"
    "v_pk_mul_f32 v[22:23], v[22:23], v[6:7]\n"
    "v_pk_mul_f32 v[8:9], v[8:9], v[16:17]\n"
    "v_pk_mul_f32 v[10:11], v[10:11], v[18:19]\n"
    "v_pk_mul_f32 v[12:13], v[12:13], v[20:21]\n"
    "v_pk_mul_f32 v[14:15], v[14:15], v[22:23]\n"
    "v_cvt_pk_bf16_f32 v188, v16, v17\n"
    "v_cvt_pk_bf16_f32 v189, v18, v19\n"
    "v_cvt_pk_bf16_f32 v190, v20, v21\n"
    "v_cvt_pk_bf16_f32 v191, v22, v23\n"
    "v_cvt_pk_bf16_f32 v24, -v8, -v9\n"
    "v_cvt_pk_bf16_f32 v25, -v10, -v11\n"
    "v_cvt_pk_bf16_f32 v26, -v12, -v13\n"
    "v_cvt_pk_bf16_f32 v27, -v14, -v15\n"
    "ds_write_b128 v234, v[24:27] offset:0\n"
    "v_or_b32_e32 v0, 0x88000, v34\n"
    "v_or_b32_e32 v1, 0x98000, v34\n"
    "v_or_b32_e32 v2, 0xa8000, v34\n"
    "v_or_b32_e32 v3, 0xb8000, v34\n"
    "v_or_b32_e32 v4, 0xc8000, v34\n"
    "v_or_b32_e32 v5, 0xd8000, v34\n"
    "v_or_b32_e32 v6, 0xe8000, v34\n"
    "v_or_b32_e32 v7, 0xf8000, v34\n"
    "v_pk_mul_f32 v[0:1], v[0:1], v[36:37] op_sel_hi:[1,0]\n"
    "v_pk_mul_f32 v[2:3], v[2:3], v[36:37] op_sel_hi:[1,0]\n"
    "v_pk_mul_f32 v[4:5], v[4:5], v[36:37] op_sel_hi:[1,0]\n"
    "v_pk_mul_f32 v[6:7], v[6:7], v[36:37] op_sel_hi:[1,0]\n"
    "v_pk_mul_f32 v[8:9], v[0:1], v[0:1]\n"
    "v_pk_mul_f32 v[10:11], v[2:3], v[2:3]\n"
    "v_pk_mul_f32 v[12:13], v[4:5], v[4:5]\n"
    "v_pk_mul_f32 v[14:15], v[6:7], v[6:7]\n"
    "v_pk_fma_f32 v[8:9], v[8:9], v[38:39], v[32:33] op_sel_hi:[1,0,0]\n"
    "v_pk_fma_f32 v[10:11], v[10:11], v[38:39], v[32:33] op_sel_hi:[1,0,0]\n"
    "v_pk_fma_f32 v[12:13], v[12:13], v[38:39], v[32:33] op_sel_hi:[1,0,0]\n"
    "v_pk_fma_f32 v[14:15], v[14:15], v[38:39], v[32:33] op_sel_hi:[1,0,0]\n"
    "v_pk_mul_f32 v[8:9], v[8:9], v[0:1]\n"
    "v_pk_mul_f32 v[10:11], v[10:11], v[2:3]\n"
    "v_pk_mul_f32 v[12:13], v[12:13], v[4:5]\n"
    "v_pk_mul_f32 v[14:15], v[14:15], v[6:7]\n"
    "v_exp_f32_e64 v8, v8\n"
    "v_exp_f32_e64 v9, v9\n"
    "v_exp_f32_e64 v10, v10\n"
    "v_exp_f32_e64 v11, v11\n"
    "v_exp_f32_e64 v12, v12\n"
    "v_exp_f32_e64 v13, v13\n"
    "v_exp_f32_e64 v14, v14\n"
    "v_exp_f32_e64 v15, v15\n"
    "v_pk_add_f32 v[16:17], v[8:9], 1.0 op_sel_hi:[1,0]\n"
    "v_pk_add_f32 v[18:19], v[10:11], 1.0 op_sel_hi:[1,0]\n"
    "v_pk_add_f32 v[20:21], v[12:13], 1.0 op_sel_hi:[1,0]\n"
    "v_pk_add_f32 v[22:23], v[14:15], 1.0 op_sel_hi:[1,0]\n"
    "v_rcp_f32_e64 v16, v16\n"
    "v_rcp_f32_e64 v17, v17\n"
    "v_rcp_f32_e64 v18, v18\n"
    "v_rcp_f32_e64 v19, v19\n"
    "v_rcp_f32_e64 v20, v20\n"
    "v_rcp_f32_e64 v21, v21\n"
    "v_rcp_f32_e64 v22, v22\n"
    "v_rcp_f32_e64 v23, v23\n"
    "v_pk_mul_f32 v[16:17], v[16:17], v[0:1]\n"
    "v_pk_mul_f32 v[18:19], v[18:19], v[2:3]\n"
    "v_pk_mul_f32 v[20:21], v[20:21], v[4:5]\n"
    "v_pk_mul_f32 v[22:23], v[22:23], v[6:7]\n"
    "v_pk_mul_f32 v[8:9], v[8:9], v[16:17]\n"
    "v_pk_mul_f32 v[10:11], v[10:11], v[18:19]\n"
    "v_pk_mul_f32 v[12:13], v[12:13], v[20:21]\n"
    "v_pk_mul_f32 v[14:15], v[14:15], v[22:23]\n"
    "v_cvt_pk_bf16_f32 v192, v16, v17\n"
    "v_cvt_pk_bf16_f32 v193, v18, v19\n"
    "v_cvt_pk_bf16_f32 v194, v20, v21\n"
    "v_cvt_pk_bf16_f32 v195, v22, v23\n"
    "v_cvt_pk_bf16_f32 v24, -v8, -v9\n"
    "v_cvt_pk_bf16_f32 v25, -v10, -v11\n"
    "v_cvt_pk_bf16_f32 v26, -v12, -v13\n"
    "v_cvt_pk_bf16_f32 v27, -v14, -v15\n"
    "ds_write_b128 v234, v[24:27] offset:16\n"
    "v_add_u32_e32 v234, 65536, v234\n"
    "buffer_load_dword v229, v212, s[92:95], 0 offen offset:0\n"
    "s_nop 0\n"
    "s_waitcnt vmcnt(13)\n"
    "v_lshlrev_b32_sdwa v216, 2, v168 dst_sel:DWORD dst_unused:UNUSED_PAD src0_sel:DWORD src1_sel:WORD_0\n"
    "v_lshlrev_b32_sdwa v217, 2, v168 dst_sel:DWORD dst_unused:UNUSED_PAD src0_sel:DWORD src1_sel:WORD_1\n"
    "v_lshlrev_b32_sdwa v218, 2, v169 dst_sel:DWORD dst_unused:UNUSED_PAD src0_sel:DWORD src1_sel:WORD_0\n"
    "v_lshlrev_b32_sdwa v219, 2, v169 dst_sel:DWORD dst_unused:UNUSED_PAD src0_sel:DWORD src1_sel:WORD_1\n"
    "v_lshlrev_b32_sdwa v220, 2, v170 dst_sel:DWORD dst_unused:UNUSED_PAD src0_sel:DWORD src1_sel:WORD_0\n"
    "v_lshlrev_b32_sdwa v221, 2, v170 dst_sel:DWORD dst_unused:UNUSED_PAD src0_sel:DWORD src1_sel:WORD_1\n"
    "v_lshlrev_b32_sdwa v222, 2, v171 dst_sel:DWORD dst_unused:UNUSED_PAD src0_sel:DWORD src1_sel:WORD_0\n"
    "v_lshlrev_b32_sdwa v223, 2, v171 dst_sel:DWORD dst_unused:UNUSED_PAD src0_sel:DWORD src1_sel:WORD_1\n"
    "ds_write_b128 v222, v[88:91]\n"
    "buffer_load_dword v230, v212, s[92:95], 0 offen offset:192\n"
    "s_nop 0\n"
    "s_waitcnt vmcnt(13)\n"
    "v_lshlrev_b32_sdwa v207, 2, v172 dst_sel:DWORD dst_unused:UNUSED_PAD src0_sel:DWORD src1_sel:WORD_0\n"
    "v_lshlrev_b32_sdwa v242, 2, v173 dst_sel:DWORD dst_unused:UNUSED_PAD src0_sel:DWORD src1_sel:WORD_0\n"
    "v_lshlrev_b32_sdwa v243, 2, v173 dst_sel:DWORD dst_unused:UNUSED_PAD src0_sel:DWORD src1_sel:WORD_1\n"
    "v_lshlrev_b32_sdwa v244, 2, v174 dst_sel:DWORD dst_unused:UNUSED_PAD src0_sel:DWORD src1_sel:WORD_0\n"
    "v_lshlrev_b32_sdwa v248, 2, v174 dst_sel:DWORD dst_unused:UNUSED_PAD src0_sel:DWORD src1_sel:WORD_1\n"
    "v_lshlrev_b32_sdwa v249, 2, v175 dst_sel:DWORD dst_unused:UNUSED_PAD src0_sel:DWORD src1_sel:WORD_0\n"
    "v_lshlrev_b32_sdwa v250, 2, v175 dst_sel:DWORD dst_unused:UNUSED_PAD src0_sel:DWORD src1_sel:WORD_1\n"
    "v_bfe_i32 v201, v172, 16, 1\n"
    "v_and_b32_e32 v201, 0x35003500, v201\n"
    "v_bfe_i32 v203, v172, 18, 1\n"
    "v_and_b32_e32 v203, 0x35003500, v203\n"
    "v_bfe_i32 v205, v172, 20, 1\n"
    "v_and_b32_e32 v205, 0x35003500, v205\n"
    "v_bfe_i32 v92, v172, 26, 1\n"
    "v_xor_b32_e32 v92, 0xffff, v92\n"
    "v_and_b32_e32 v92, 0x3f803f80, v92\n"
    "v_bfe_i32 v236, v172, 22, 1\n"
    "v_and_b32_e64 v236, v92, v236\n"
    "v_bfe_i32 v237, v172, 23, 1\n"
    "v_and_b32_e32 v237, v92, v237\n"
    "buffer_load_dword v231, v212, s[92:95], 0 offen offset:384\n"
    "s_waitcnt vmcnt(13)\n"
    "v_lshlrev_b32_sdwa v251, 2, v176 dst_sel:DWORD dst_unused:UNUSED_PAD src0_sel:DWORD src1_sel:WORD_0\n"
    "v_lshlrev_b32_sdwa v252, 2, v176 dst_sel:DWORD dst_unused:UNUSED_PAD src0_sel:DWORD src1_sel:WORD_1\n"
    "v_lshlrev_b32_sdwa v253, 2, v177 dst_sel:DWORD dst_unused:UNUSED_PAD src0_sel:DWORD src1_sel:WORD_0\n"
    "v_lshlrev_b32_sdwa v254, 2, v177 dst_sel:DWORD dst_unused:UNUSED_PAD src0_sel:DWORD src1_sel:WORD_1\n"
    "v_lshlrev_b32_sdwa v241, 0, v178 dst_sel:DWORD dst_unused:UNUSED_PAD src0_sel:DWORD src1_sel:WORD_0\n"
    "v_lshlrev_b32_sdwa v245, 4, v178 dst_sel:DWORD dst_unused:UNUSED_PAD src0_sel:DWORD src1_sel:WORD_1\n"
    "v_lshlrev_b32_sdwa v255, 4, v179 dst_sel:DWORD dst_unused:UNUSED_PAD src0_sel:DWORD src1_sel:WORD_0\n"
    "v_lshlrev_b32_sdwa v247, 4, v179 dst_sel:DWORD dst_unused:UNUSED_PAD src0_sel:DWORD src1_sel:WORD_1\n"
    "v_bfe_i32 v238, v172, 24, 1\n"
    "v_and_b32_e64 v238, v92, v238\n"
    "v_bfe_i32 v239, v172, 25, 1\n"
    "v_and_b32_e64 v239, v92, v239\n"
    "v_bfe_u32 v93, v172, 28, 2\n"
    "v_mul_u32_u24_e32 v93, 393216, v93\n"
    "v_mov_b32_e64 v222, v254\n"
    "v_mov_b32_e32 v254, 0\n"
    "s_nop 1\n"
    "v_mfma_f32_32x32x1_2b_f32 v[0:31], v254, v254, 0\n"
    "global_load_dword v233, v214, s[70:71]\n"
    "s_mov_b32 s48, 0x44ea44ea\n"
    "s_mov_b32 s49, 0xb77fb77f\n"
    "s_mov_b32 s50, 0x89efde6e\n"
    "s_mov_b32 s46, s42\n"
    "s_mov_b32 s47, s43\n"
    "s_mul_i32 s54, s60, 592\n"
    "s_add_u32 s54, s54, -27008\n"
    "s_mul_i32 s55, s60, -1284\n"
    "s_add_u32 s55, s55, 54688\n"
    "s_mul_i32 s56, s60, -6160\n"
    "s_add_u32 s56, s56, -27008\n"
    "s_add_u32 s98, s99, 1375872\n"
    "s_and_b32 s36, s60, 1\n"
    "s_lshl_b32 s36, s36, 13\n"
    "s_add_u32 s36, s36, 327680\n"
    "s_mul_i32 s53, s60, 98304\n"
    "s_add_u32 s98, s53, s98\n"
    "s_xor_b32 s53, s60, 1\n"
    "s_mul_i32 s53, s53, 393216\n"
    "s_add_u32 s99, s99, s53\n"
    "s_add_u32 s53, s98, 393216\n"
    "s_sub_u32 s53, s53, s36\n"
    "s_and_b32 s37, s61, 1\n"
    "s_lshl_b32 s37, s37, 2\n"
    "s_add_u32 s37, s37, s60\n"
    "s_lshl_b32 s37, s37, 12\n"
    "s_cmp_eq_u32 s61, 0\n"
    "s_cselect_b32 s67, 2, 0\n"
    "s_cmp_eq_u32 s61, 31\n"
    "s_cselect_b32 s67, 1, s67\n"
    "s_lshl_b32 s67, s67, 30\n"
    "s_mov_b64 s[68:69], s[44:45]\n"
    "s_mov_b32 s70, 0\n"
    "s_mov_b32 s71, s47\n"
    "s_mov_b64 s[72:73], s[40:41]\n"
    "s_mov_b64 s[74:75], s[42:43]\n"
    "s_mov_b32 s57, 0\n"
    "s_mov_b32 s58, 196608\n"
    "s_mov_b32 s60, 0\n"
    "s_mov_b32 s61, 393216\n"
    "s_mov_b32 s62, 786432\n"
    "s_mov_b32 s63, 1179648\n"
    "s_mov_b32 s52, 1572864\n"
    "s_mov_b32 s88, 98304\n"
    "s_mov_b32 s89, 294912\n"
    "s_mov_b32 s39, 0\n"
    "s_getpc_b64 s[76:77]\n"
    ".Lreplk_pc1:\n"
    "s_add_u32 s76, s76, .Lreplk_last-.Lreplk_pc1\n"
    "s_addc_u32 s77, s77, 0\n"
    "s_sub_u32 s76, s76, s36\n"
    "s_subb_u32 s77, s77, 0\n"
    "s_add_u32 s78, s36, .Lreplk_end-.Lreplk_last\n"
    "s_mov_b32 s79, s37\n"
    "s_add_u32 m0, s38, 0\n"
    "s_mov_b32 s64, 0\n"
    "buffer_load_dwordx4 v210, s[84:87], s64 offen offset:0 lds\n"
    "s_waitcnt vmcnt(13)\n"
    "ds_write2_b32 v216, a224, a225 offset0:0 offset1:20\n"
    "v_add3_u32 v247, v247, v93, s98\n"
    "v_add_u32_e32 v246, s98, v255\n"
    "v_add_u32_e64 v245, s99, v245\n"
    "v_add_u32_e64 v255, s99, v255\n"
    "ds_write2_b32 v216, a226, a227 offset0:40 offset1:60\n"
    "v_and_b32_e64 v93, s67, v172\n"
    "v_and_b32_e32 v94, 0x80000000, v93\n"
    "s_waitcnt vmcnt(12)\n"
    "ds_write2_b32 v216, a228, a229 offset0:80 offset1:100\n"
    "v_and_b32_e32 v93, 0x40000000, v93\n"
    "v_or_b32_e64 v246, v246, v94\n"
    "v_or_b32_e64 v247, v247, v93\n"
    "ds_write2_b32 v216, a230, a231 offset0:120 offset1:140\n"
    "v_add_u32_e32 v216, 1360, v216\n"
    "buffer_load_dwordx4 v210, s[84:87], s64 offen offset:1024 lds\n"
    "s_waitcnt vmcnt(12)\n"
    "ds_write2_b32 v216, a232, a233 offset0:0 offset1:20\n"
    "ds_write2_b32 v216, a234, a235 offset0:40 offset1:60\n"
    "buffer_load_dwordx4 v210, s[84:87], s64 offen offset:2048 lds\n"
    "s_waitcnt vmcnt(12)\n"
    "ds_write2_b32 v216, a236, a237 offset0:80 offset1:100\n"
    "s_add_u32 m0, s38, 3072\n"
    "s_mov_b32 s64, 3072\n"
    "buffer_load_dwordx4 v210, s[84:87], s64 offen offset:0 lds\n"
    "buffer_load_dwordx4 v210, s[84:87], s64 offen offset:1024 lds\n"
    "ds_write2_b32 v216, a238, a239 offset0:120 offset1:140\n"
    "buffer_load_dwordx4 v210, s[84:87], s64 offen offset:2048 lds\n"
    "s_add_u32 m0, s38, 6144\n"
    "s_mov_b32 s64, 6144\n"
    "buffer_load_dwordx4 v210, s[84:87], s64 offen offset:0 lds\n"
    "buffer_load_dwordx4 v210, s[84:87], s64 offen offset:1024 lds\n"
    "v_add_u32_e32 v216, 1360, v216\n"
    "s_waitcnt vmcnt(16)\n"
    "v_lshlrev_b32_e32 v40, 16, v224\n"
    "buffer_load_dwordx4 v210, s[84:87], s64 offen offset:2048 lds\n"
    "s_mov_b32 s65, 589824\n"
    "buffer_load_dwordx4 a[240:243], v208, s[40:43], s65 offen\n"
    "s_waitcnt vmcnt(17)\n"
    "v_lshlrev_b32_e32 v41, 16, v225\n"
    "s_mov_b32 s65, 688128\n"
    "buffer_load_dwordx4 a[244:247], v208, s[40:43], s65 offen\n"
    "s_mov_b32 s59, 49152\n"
    "buffer_load_dwordx4 v[96:99], v209, s[80:83], s59 offen offset:0\n"
    "s_waitcnt vmcnt(18)\n"
    "v_lshlrev_b32_e32 v42, 16, v226\n"
    "s_waitcnt vmcnt(17)\n"
    "v_lshlrev_b32_e32 v43, 16, v227\n"
    "s_waitcnt vmcnt(16)\n"
    "v_lshlrev_b32_e32 v44, 16, v228\n"
    "s_waitcnt vmcnt(15)\n"
    "v_lshlrev_b32_e32 v45, 16, v229\n"
    "s_waitcnt vmcnt(14)\n"
    "v_lshlrev_b32_e32 v46, 16, v230\n"
    "s_waitcnt vmcnt(13)\n"
    "v_lshlrev_b32_e32 v47, 16, v231\n"
    "v_add_f32_e64 v41, v41, v45\n"
    "v_add_f32_e64 v42, v42, v46\n"
    "v_add_f32_e64 v43, v43, v47\n"
    "v_cvt_pk_bf16_f32 v56, v40, v41\n"
    "v_cvt_pk_bf16_f32 v57, v42, v43\n"
    "v_cvt_pk_bf16_f32 v58, v44, 0\n"
    "v_cvt_pk_bf16_f32 v64, 0, v40\n"
    "buffer_load_dwordx4 v[100:103], v209, s[80:83], s59 offen offset:256\n"
    "ds_write_b128 v221, v[56:59] offset:0\n"
    "v_cvt_pk_bf16_f32 v65, v41, v42\n"
    "v_cvt_pk_bf16_f32 v66, v43, v44\n"
    "v_cvt_pk_bf16_f32 v61, v40, v41\n"
    "v_cvt_pk_bf16_f32 v62, v42, v43\n"
    "v_cvt_pk_bf16_f32 v63, v44, 0\n"
    "v_cvt_pk_bf16_f32 v69, 0, v40\n"
    "v_cvt_pk_bf16_f32 v70, v41, v42\n"
    "v_cvt_pk_bf16_f32 v71, v43, v44\n"
    "ds_write_b128 v221, v[64:67] offset:16\n"
    "buffer_load_dwordx4 v[104:107], v209, s[80:83], s59 offen offset:512\n"
    "v_and_b32_e32 v48, 0xffff0000, v224\n"
    "v_and_b32_e32 v49, 0xffff0000, v225\n"
    "v_and_b32_e32 v50, 0xffff0000, v226\n"
    "v_and_b32_e32 v51, 0xffff0000, v227\n"
    "v_and_b32_e32 v52, 0xffff0000, v228\n"
    "ds_write_b128 v221, v[60:63] offset:32\n"
    "v_and_b32_e32 v53, 0xffff0000, v229\n"
    "v_and_b32_e32 v54, 0xffff0000, v230\n"
    "v_and_b32_e32 v55, 0xffff0000, v231\n"
    "v_add_f32_e64 v49, v49, v53\n"
    "v_add_f32_e64 v50, v50, v54\n"
    "v_add_f32_e64 v51, v51, v55\n"
    "ds_write_b128 v221, v[68:71] offset:48\n"
    "s_mov_b32 s65, 983040\n"
    "buffer_load_dwordx4 a[248:251], v208, s[40:43], s65 offen\n"
    "v_cvt_pk_bf16_f32 v72, v48, v49\n"
    "v_cvt_pk_bf16_f32 v73, v50, v51\n"
    "v_cvt_pk_bf16_f32 v74, v52, 0\n"
    "v_cvt_pk_bf16_f32 v80, 0, v48\n"
    "v_cvt_pk_bf16_f32 v81, v49, v50\n"
    "v_cvt_pk_bf16_f32 v82, v51, v52\n"
    "v_cvt_pk_bf16_f32 v77, v48, v49\n"
    "v_cvt_pk_bf16_f32 v78, v50, v51\n"
    "v_cvt_pk_bf16_f32 v79, v52, 0\n"
    "v_cvt_pk_bf16_f32 v85, 0, v48\n"
    "v_cvt_pk_bf16_f32 v86, v49, v50\n"
    "v_cvt_pk_bf16_f32 v87, v51, v52\n"
    "ds_write_b128 v221, v[72:75] offset:336\n"
    "s_mov_b32 s65, 1081344\n"
    "buffer_load_dwordx4 a[252:255], v208, s[40:43], s65 offen\n"
    "v_xor_b32_e32 v224, 0, v219\n"
    "v_xor_b32_e32 v225, 32, v219\n"
    "v_xor_b32_e32 v226, 64, v219\n"
    "v_xor_b32_e32 v227, 96, v219\n"
    "ds_write_b128 v221, v[80:83] offset:352\n"
    "v_xor_b32_e32 v228, 0, v223\n"
    "v_xor_b32_e32 v229, 32, v223\n"
    "s_mov_b32 s59, 52224\n"
    "buffer_load_dwordx4 v[108:111], v209, s[80:83], s59 offen offset:0\n"
    "v_xor_b32_e32 v230, 64, v223\n"
    "v_xor_b32_e32 v231, 96, v223\n"
    "ds_write_b128 v221, v[76:79] offset:368\n"
    "v_lshl_add_u32 v196, s51, 8, v225\n"
    "v_lshl_add_u32 v197, s51, 8, v227\n"
    "v_mfma_f32_32x32x1_2b_f32 v[32:63], v254, v254, 0\n"
    "ds_write_b128 v221, v[84:87] offset:384\n"
    "buffer_load_dwordx4 v[112:115], v209, s[80:83], s59 offen offset:256\n"
    "v_mfma_f32_32x32x1_2b_f32 v[64:95], v254, v254, 0\n"
    "v_add_u32_e64 v254, s54, v242\n"
    "buffer_load_dwordx4 v[116:119], v209, s[80:83], s59 offen offset:512\n"
    "s_mov_b32 s65, 1376256\n"
    "buffer_load_dwordx4 a[176:179], v208, s[40:43], s65 offen\n"
    "s_mov_b32 s65, 1474560\n"
    "buffer_load_dwordx4 a[180:183], v208, s[40:43], s65 offen\n"
    "s_waitcnt vmcnt(12)\n"
    "s_barrier\n"
    "ds_read_b64_tr_b16 a[0:1], v220 offset:0\n"
    "ds_read_b64_tr_b16 a[2:3], v220 offset:3072\n"
    "ds_read_b64_tr_b16 a[4:5], v220 offset:8\n"
    "ds_read_b64_tr_b16 a[6:7], v220 offset:3080\n"
    "ds_read_b64_tr_b16 a[8:9], v220 offset:64\n"
    "ds_read_b64_tr_b16 a[10:11], v220 offset:3136\n"
    "ds_read_b64_tr_b16 a[12:13], v220 offset:72\n"
    "ds_read_b64_tr_b16 a[14:15], v220 offset:3144\n"
    "ds_read_b64_tr_b16 v[168:169], v218 offset:128\n"
    "ds_read_b64_tr_b16 v[170:171], v218 offset:3200\n"
    "ds_read_b64_tr_b16 a[16:17], v220 offset:6144\n"
    "ds_read_b64_tr_b16 a[18:19], v220 offset:9216\n"
    "ds_read_b64_tr_b16 a[20:21], v220 offset:6152\n"
    "ds_read_b64_tr_b16 a[22:23], v220 offset:9224\n"
    "ds_read_b64_tr_b16 a[24:25], v220 offset:6208\n"
    "ds_read_b64_tr_b16 a[26:27], v220 offset:9280\n"
    "ds_read_b64_tr_b16 a[28:29], v220 offset:6216\n"
    "ds_read_b64_tr_b16 a[30:31], v220 offset:9288\n"
    "ds_read_b64_tr_b16 v[172:173], v218 offset:6272\n"
    "ds_read_b64_tr_b16 v[174:175], v218 offset:9344\n"
    "ds_read_b64_tr_b16 a[32:33], v220 offset:12288\n"
    "ds_read_b64_tr_b16 a[34:35], v220 offset:15360\n"
    "s_nop 0\n"
    "s_mov_b32 s59, 0\n"
    "buffer_load_dwordx4 v[120:123], v209, s[80:83], s59 offen offset:0\n"
    "ds_read_b64_tr_b16 a[36:37], v220 offset:12296\n"
    "ds_read_b64_tr_b16 a[38:39], v220 offset:15368\n"
    "ds_read_b64_tr_b16 a[40:41], v220 offset:12352\n"
    "ds_read_b64_tr_b16 a[42:43], v220 offset:15424\n"
    "ds_read_b64_tr_b16 a[44:45], v220 offset:12360\n"
    "ds_read_b64_tr_b16 a[46:47], v220 offset:15432\n"
    "ds_read_b64_tr_b16 v[176:177], v218 offset:12416\n"
    "ds_read_b64_tr_b16 v[178:179], v218 offset:15488\n"
    "s_waitcnt vmcnt(12)\n"
    "ds_write2_b32 v216, a240, a241 offset0:0 offset1:20\n"
    "ds_write2_b32 v216, a242, a243 offset0:40 offset1:60\n"
    "s_waitcnt lgkmcnt(2)\n"
    "s_barrier\n"
    "buffer_load_dwordx4 v[124:127], v209, s[80:83], s59 offen offset:256\n"
    "s_waitcnt vmcnt(12)\n"
    "ds_write2_b32 v216, a244, a245 offset0:80 offset1:100\n"
    "ds_write2_b32 v216, a246, a247 offset0:120 offset1:140\n"
    "v_add_u32_e32 v216, 1360, v216\n"
    "buffer_load_dwordx4 v[128:131], v209, s[80:83], s59 offen offset:512\n"
    "s_waitcnt vmcnt(12)\n"
    "v_or3_b32 v200, v200, v96, v97\n"
    "v_or3_b32 v200, v200, v98, v99\n"
    "v_and_b32_e32 v232, 0x7fff7fff, v96\n"
    "v_add_u32_e32 v96, v201, v96\n"
    "v_add_u32_e64 v98, v201, v98\n"
    "ds_write_b128 v217, v[96:99] offset:24576\n"
    "s_mov_b32 s65, 0\n"
    "buffer_load_dwordx4 a[184:187], v208, s[40:43], s65 offen\n"
    "s_waitcnt vmcnt(12)\n"
    "v_or3_b32 v200, v200, v100, v101\n"
    "v_or3_b32 v200, v200, v102, v103\n"
    "v_add_u32_e64 v100, v203, v100\n"
    "v_add_u32_e64 v102, v203, v102\n"
    "ds_write_b128 v217, v[100:103] offset:24832\n"
    "s_mov_b32 s65, 98304\n"
    "buffer_load_dwordx4 a[188:191], v208, s[40:43], s65 offen\n"
    "s_waitcnt vmcnt(12)\n"
    "v_or3_b32 v200, v200, v104, v105\n"
    "v_or3_b32 v200, v200, v106, v107\n"
    "v_add_u32_e32 v104, v205, v104\n"
    "v_add_u32_e64 v106, v205, v106\n"
    "ds_write_b128 v217, v[104:107] offset:25088\n"
    "s_waitcnt lgkmcnt(0)\n"
    "s_barrier\n"
    "ds_read_b64_tr_b16 a[48:49], v224 offset:24576\n"
    "ds_read_b64_tr_b16 v[180:181], v196 offset:24576\n"
    "ds_read_b64_tr_b16 a[52:53], v226 offset:24576\n"
    "ds_read_b64_tr_b16 v[184:185], v197 offset:24576\n"
    "s_mov_b32 s59, 3072\n"
    "buffer_load_dwordx4 v[132:135], v209, s[80:83], s59 offen offset:0\n"
    "ds_read_b64_tr_b16 a[56:57], v228 offset:24576\n"
    "ds_read_b64_tr_b16 a[60:61], v229 offset:24576\n"
    "ds_read_b64_tr_b16 a[64:65], v230 offset:24576\n"
    "ds_read_b64_tr_b16 a[68:69], v231 offset:24576\n"
    "ds_read_b64_tr_b16 a[72:73], v224 offset:24832\n"
    "ds_read_b64_tr_b16 a[76:77], v226 offset:24832\n"
    "s_waitcnt vmcnt(12)\n"
    "ds_write2_b32 v216, a248, a249 offset0:0 offset1:20\n"
    "ds_write2_b32 v216, a250, a251 offset0:40 offset1:60\n"
    "buffer_load_dwordx4 v[136:139], v209, s[80:83], s59 offen offset:256\n"
    "s_waitcnt vmcnt(12)\n"
    "ds_write2_b32 v216, a252, a253 offset0:80 offset1:100\n"
    "ds_write2_b32 v216, a254, a255 offset0:120 offset1:140\n"
    "v_add_u32_e32 v216, 1360, v216\n"
    "buffer_load_dwordx4 v[140:143], v209, s[80:83], s59 offen offset:512\n"
    "s_waitcnt vmcnt(12)\n"
    "v_or3_b32 v200, v200, v108, v109\n"
    "v_or3_b32 v200, v200, v110, v111\n"
    "v_add_u32_e32 v108, v201, v108\n"
    "v_add_u32_e64 v110, v201, v110\n"
    "ds_write_b128 v217, v[108:111] offset:0\n"
    "s_mov_b32 s65, 393216\n"
    "buffer_load_dwordx4 a[192:195], v208, s[40:43], s65 offen\n"
    "s_waitcnt vmcnt(12)\n"
    "v_or3_b32 v200, v200, v112, v113\n"
    "v_or3_b32 v200, v200, v114, v115\n"
    "v_add_u32_e32 v112, v203, v112\n"
    "v_add_u32_e64 v114, v203, v114\n"
    "ds_write_b128 v217, v[112:115] offset:256\n"
    "v_mov_b32_e64 v112, v188\n"
    "v_mov_b32_e64 v113, v189\n"
    "v_mov_b32_e64 v114, v190\n"
    "v_mov_b32_e32 v115, v191\n"
    "s_mov_b32 s65, 491520\n"
    "buffer_load_dwordx4 a[196:199], v208, s[40:43], s65 offen\n"
    "s_waitcnt vmcnt(12)\n"
    "v_or3_b32 v200, v200, v116, v117\n"
    "v_or3_b32 v200, v200, v118, v119\n"
    "v_add_u32_e64 v116, v205, v116\n"
    "v_add_u32_e64 v118, v205, v118\n"
    "ds_write_b128 v217, v[116:119] offset:512\n"
    "v_mov_b32_e64 v116, v192\n"
    "v_mov_b32_e64 v117, v193\n"
    "v_mov_b32_e64 v118, v194\n"
    "v_mov_b32_e64 v119, v195\n"
    "s_waitcnt lgkmcnt(0)\n"
    "s_barrier\n"
    "ds_read_b64_tr_b16 a[50:51], v224 offset:0\n"
    "ds_read_b64_tr_b16 v[182:183], v196 offset:0\n"
    "ds_read_b64_tr_b16 a[54:55], v226 offset:0\n"
    "ds_read_b64_tr_b16 v[186:187], v197 offset:0\n"
    "s_mov_b32 s59, 24576\n"
    "buffer_load_dwordx4 v[144:147], v209, s[80:83], s59 offen offset:0\n"
    "ds_read_b64_tr_b16 a[58:59], v228 offset:0\n"
    "ds_read_b64_tr_b16 a[62:63], v229 offset:0\n"
    "ds_read_b64_tr_b16 a[66:67], v230 offset:0\n"
    "ds_read_b64_tr_b16 a[70:71], v231 offset:0\n"
    "ds_read_b64_tr_b16 a[74:75], v224 offset:256\n"
    "ds_read_b64_tr_b16 a[78:79], v226 offset:256\n"
    "s_waitcnt vmcnt(12)\n"
    "ds_write2_b32 v216, a176, a177 offset0:0 offset1:20\n"
    "ds_write2_b32 v216, a178, a179 offset0:40 offset1:60\n"
    "s_waitcnt lgkmcnt(8)\n"
    "ds_write_b128 v207, v[184:187] offset:36864\n"
    "buffer_load_dwordx4 v[148:151], v209, s[80:83], s59 offen offset:256\n"
    "s_waitcnt vmcnt(12)\n"
    "ds_write2_b32 v216, a180, a181 offset0:80 offset1:100\n"
    "ds_write2_b32 v216, a182, a183 offset0:120 offset1:140\n"
    "v_add_u32_e32 v216, -4800, v216\n"
    "buffer_load_dwordx4 v[152:155], v209, s[80:83], s59 offen offset:512\n"
    "s_waitcnt vmcnt(12)\n"
    "v_or3_b32 v200, v200, v120, v121\n"
    "v_or3_b32 v200, v200, v122, v123\n"
    "v_add_u32_e64 v120, v201, v120\n"
    "v_add_u32_e64 v122, v201, v122\n"
    "ds_write_b128 v217, v[120:123] offset:12288\n"
    "s_waitcnt lgkmcnt(1)\n"
    "s_barrier\n"
    "s_mov_b32 s65, 786432\n"
    "buffer_load_dwordx4 a[200:203], v208, s[40:43], s65 offen\n"
    "s_waitcnt vmcnt(12)\n"
    "v_or3_b32 v200, v200, v124, v125\n"
    "v_or3_b32 v200, v200, v126, v127\n"
    "v_add_u32_e32 v124, v203, v124\n"
    "v_add_u32_e64 v126, v203, v126\n"
    "ds_write_b128 v217, v[124:127] offset:12544\n"
    "s_mov_b32 s65, 884736\n"
    "buffer_load_dwordx4 a[204:207], v208, s[40:43], s65 offen\n"
    "s_waitcnt vmcnt(12)\n"
    "v_or3_b32 v200, v200, v128, v129\n"
    "v_or3_b32 v200, v200, v130, v131\n"
    "v_add_u32_e32 v128, v205, v128\n"
    "v_add_u32_e64 v130, v205, v130\n"
    "ds_write_b128 v217, v[128:131] offset:12800\n"
    "ds_read_b128 a[224:227], v250 offset:0\n"
    "ds_read_b128 a[228:231], v250 offset:320\n"
    "ds_read_b128 a[232:235], v251 offset:0\n"
    "ds_read_b128 a[236:239], v251 offset:336\n"
    "ds_read_b128 a[240:243], v250 offset:80\n"
    "ds_read_b128 a[244:247], v250 offset:400\n"
    "ds_read_b128 a[248:251], v251 offset:672\n"
    "ds_read_b128 a[252:255], v251 offset:1008\n"
    "s_waitcnt lgkmcnt(8)\n"
    "s_barrier\n"
    "ds_read_b64_tr_b16 a[80:81], v224 offset:12288\n"
    "ds_read_b64_tr_b16 a[84:85], v225 offset:12288\n"
    "ds_read_b64_tr_b16 a[88:89], v226 offset:12288\n"
    "ds_read_b64_tr_b16 a[92:93], v227 offset:12288\n"
    "s_mov_b32 s59, 27648\n"
    "buffer_load_dwordx4 v[156:159], v209, s[80:83], s59 offen offset:0\n"
    "ds_read_b64_tr_b16 a[96:97], v228 offset:12288\n"
    "ds_read_b64_tr_b16 a[100:101], v229 offset:12288\n"
    "ds_read_b64_tr_b16 a[104:105], v230 offset:12288\n"
    "ds_read_b64_tr_b16 a[108:109], v231 offset:12288\n"
    "s_waitcnt lgkmcnt(13)\n"
    "v_smfmac_f32_16x16x64_bf16 v[0:3], a[232:235], a[224:231], v240\n"
    "s_waitcnt lgkmcnt(12)\n"
    "ds_read_b64_tr_b16 a[112:113], v224 offset:12544\n"
    "ds_read_b64_tr_b16 a[116:117], v225 offset:12544\n"
    "ds_read_b64_tr_b16 a[120:121], v226 offset:12544\n"
    "v_smfmac_f32_16x16x64_bf16 v[4:7], a[236:239], a[224:231], v240 abid:1\n"
    "ds_read_b64_tr_b16 a[124:125], v227 offset:12544\n"
    "s_waitcnt vmcnt(12)\n"
    "ds_write2_b32 v216, a184, a185 offset0:0 offset1:20\n"
    "s_waitcnt lgkmcnt(14)\n"
    "v_smfmac_f32_16x16x64_bf16 v[8:11], a[248:251], a[240:247], v240\n"
    "s_nop 0\n"
    "ds_write2_b32 v216, a186, a187 offset0:40 offset1:60\n"
    "s_waitcnt lgkmcnt(13)\n"
    "v_smfmac_f32_16x16x64_bf16 v[12:15], a[252:255], a[240:247], v240 abid:1\n"
    "ds_read_b128 a[224:227], v250 offset:160\n"
    "ds_read_b128 a[228:231], v250 offset:480\n"
    "ds_read_b128 a[232:235], v251 offset:1344\n"
    "ds_read_b128 a[236:239], v251 offset:1680\n"
    "ds_read_b128 a[240:243], v250 offset:240\n"
    "ds_read_b128 a[244:247], v250 offset:560\n"
    "ds_read_b128 a[248:251], v251 offset:2016\n"
    "ds_read_b128 a[252:255], v251 offset:2352\n"
    "buffer_load_dwordx4 v[160:163], v209, s[80:83], s59 offen offset:256\n"
    "s_waitcnt vmcnt(12)\n"
    "ds_write2_b32 v216, a188, a189 offset0:80 offset1:100\n"
    "ds_write2_b32 v216, a190, a191 offset0:120 offset1:140\n"
    "v_add_u32_e32 v216, 1360, v216\n"
    "buffer_load_dwordx4 v[164:167], v209, s[80:83], s59 offen offset:512\n"
    "s_waitcnt vmcnt(12)\n"
    "v_or3_b32 v200, v200, v132, v133\n"
    "v_or3_b32 v200, v200, v134, v135\n"
    "v_add_u32_e64 v132, v201, v132\n"
    "v_add_u32_e64 v134, v201, v134\n"
    "ds_write_b128 v217, v[132:135] offset:24576\n"
    "s_waitcnt lgkmcnt(5)\n"
    "v_smfmac_f32_16x16x64_bf16 v[16:19], a[232:235], a[224:231], v240\n"
    "s_waitcnt lgkmcnt(4)\n"
    "v_smfmac_f32_16x16x64_bf16 v[20:23], a[236:239], a[224:231], v240 abid:1\n"
    "s_waitcnt lgkmcnt(3)\n"
    "s_mov_b32 s65, 1179648\n"
    "buffer_load_dwordx4 a[208:211], v208, s[40:43], s65 offen\n"
    "s_waitcnt vmcnt(12)\n"
    "v_or3_b32 v200, v200, v136, v137\n"
    "v_or3_b32 v200, v200, v138, v139\n"
    "v_add_u32_e64 v136, v203, v136\n"
    "v_add_u32_e64 v138, v203, v138\n"
    "ds_write_b128 v217, v[136:139] offset:24832\n"
    "v_smfmac_f32_16x16x64_bf16 v[24:27], a[248:251], a[240:247], v240\n"
    "v_smfmac_f32_16x16x64_bf16 v[28:31], a[252:255], a[240:247], v240 abid:1\n"
    "s_mov_b32 s65, 1277952\n"
    "buffer_load_dwordx4 a[212:215], v208, s[40:43], s65 offen\n"
    "s_waitcnt vmcnt(12)\n"
    "v_or3_b32 v200, v200, v140, v141\n"
    "v_or3_b32 v200, v200, v142, v143\n"
    "v_add_u32_e32 v140, v205, v140\n"
    "v_add_u32_e64 v142, v205, v142\n"
    "ds_write_b128 v217, v[140:143] offset:25088\n"
    "ds_read_b128 a[224:227], v250 offset:27024\n"
    "ds_read_b128 a[228:231], v250 offset:27344\n"
    "ds_read_b128 a[232:235], v251 offset:10752\n"
    "ds_read_b128 a[236:239], v251 offset:11088\n"
    "ds_read_b128 a[240:243], v250 offset:27104\n"
    "ds_read_b128 a[244:247], v250 offset:27424\n"
    "ds_read_b128 a[248:251], v251 offset:11424\n"
    "ds_read_b128 a[252:255], v251 offset:11760\n"
    "ds_read_b128 v[136:139], v252 offset:1344\n"
    "ds_read_b128 v[140:143], v252 offset:1680\n"
    "s_waitcnt lgkmcnt(10)\n"
    "s_barrier\n"
    "ds_read_b64_tr_b16 a[82:83], v224 offset:24576\n"
    "ds_read_b64_tr_b16 a[86:87], v225 offset:24576\n"
    "ds_read_b64_tr_b16 a[90:91], v226 offset:24576\n"
    "s_mov_b32 s65, 1572864\n"
    "buffer_load_dwordx4 a[216:219], v208, s[40:43], s65 offen\n"
    "ds_read_b64_tr_b16 a[94:95], v227 offset:24576\n"
    "ds_read_b64_tr_b16 a[98:99], v228 offset:24576\n"
    "ds_read_b64_tr_b16 a[102:103], v229 offset:24576\n"
    "ds_read_b64_tr_b16 a[106:107], v230 offset:24576\n"
    "ds_read_b64_tr_b16 a[110:111], v231 offset:24576\n"
    "s_waitcnt lgkmcnt(15)\n"
    "v_smfmac_f32_16x16x64_bf16 v[32:35], a[232:235], a[224:231], v240\n"
    "s_waitcnt lgkmcnt(14)\n"
    "ds_read_b64_tr_b16 a[114:115], v224 offset:24832\n"
    "ds_read_b64_tr_b16 a[118:119], v225 offset:24832\n"
    "ds_read_b64_tr_b16 a[122:123], v226 offset:24832\n"
    "v_smfmac_f32_16x16x64_bf16 v[36:39], a[236:239], a[224:231], v240 abid:1\n"
    "ds_read_b64_tr_b16 a[126:127], v227 offset:24832\n"
    "s_waitcnt vmcnt(12)\n"
    "ds_write2_b32 v216, a192, a193 offset0:0 offset1:20\n"
    "s_waitcnt lgkmcnt(15)\n"
    "v_smfmac_f32_16x16x64_bf16 v[40:43], a[248:251], a[240:247], v240\n"
    "ds_write2_b32 v216, a194, a195 offset0:40 offset1:60\n"
    "v_smfmac_f32_16x16x64_bf16 v[44:47], a[252:255], a[240:247], v240 abid:1\n"
    "ds_read_b128 a[224:227], v250 offset:27184\n"
    "ds_read_b128 a[228:231], v250 offset:27504\n"
    "ds_read_b128 a[232:235], v251 offset:12096\n"
    "ds_read_b128 a[236:239], v251 offset:12432\n"
    "ds_read_b128 a[240:243], v250 offset:27264\n"
    "ds_read_b128 a[244:247], v250 offset:27584\n"
    "ds_read_b128 a[248:251], v251 offset:12768\n"
    "ds_read_b128 a[252:255], v251 offset:13104\n"
    "s_mov_b32 s65, 1671168\n"
    "buffer_load_dwordx4 a[220:223], v208, s[40:43], s65 offen\n"
    "s_waitcnt vmcnt(12)\n"
    "ds_write2_b32 v216, a196, a197 offset0:80 offset1:100\n"
    "ds_write2_b32 v216, a198, a199 offset0:120 offset1:140\n"
    "v_add_u32_e32 v216, 1360, v216\n"
    "s_waitcnt vmcnt(11)\n"
    "v_or3_b32 v200, v200, v144, v145\n"
    "v_or3_b32 v200, v200, v146, v147\n"
    "v_add_u32_e64 v144, v201, v144\n"
    "v_add_u32_e64 v146, v201, v146\n"
    "ds_write_b128 v217, v[144:147] offset:0\n"
    "s_waitcnt lgkmcnt(8)\n"
    "v_smfmac_f32_16x16x64_bf16 v[48:51], a[232:235], a[224:231], v240\n"
    "s_waitcnt lgkmcnt(7)\n"
    "v_smfmac_f32_16x16x64_bf16 v[52:55], a[236:239], a[224:231], v240 abid:1\n"
    "ds_read_b128 a[224:227], v250 offset:54048\n"
    "ds_read_b128 a[228:231], v250 offset:54368\n"
    "ds_read_b128 a[232:235], v251 offset:21504\n"
    "s_waitcnt vmcnt(10)\n"
    "v_or3_b32 v200, v200, v148, v149\n"
    "v_or3_b32 v200, v200, v150, v151\n"
    "v_add_u32_e32 v148, v203, v148\n"
    "v_add_u32_e64 v150, v203, v150\n"
    "ds_write_b128 v217, v[148:151] offset:256\n"
    "s_waitcnt lgkmcnt(6)\n"
    "v_smfmac_f32_16x16x64_bf16 v[56:59], a[248:251], a[240:247], v240\n"
    "v_smfmac_f32_16x16x64_bf16 v[60:63], a[252:255], a[240:247], v240 abid:1\n"
    "ds_read_b128 a[236:239], v251 offset:21840\n"
    "s_waitcnt vmcnt(9)\n"
    "v_or3_b32 v200, v200, v152, v153\n"
    "v_or3_b32 v200, v200, v154, v155\n"
    "v_add_u32_e64 v152, v205, v152\n"
    "v_add_u32_e32 v154, v205, v154\n"
    "ds_write_b128 v217, v[152:155] offset:512\n"
    "s_waitcnt lgkmcnt(3)\n"
    "v_smfmac_f32_16x16x64_bf16 v[64:67], a[232:235], a[224:231], v240\n"
    "ds_read_b128 a[240:243], v250 offset:54128\n"
    "ds_read_b128 a[244:247], v250 offset:54448\n"
    "ds_read_b128 a[248:251], v251 offset:22176\n"
    "ds_read_b128 a[252:255], v251 offset:22512\n"
    "ds_read_b128 a[232:235], v251 offset:22848\n"
    "s_waitcnt lgkmcnt(6)\n"
    "v_smfmac_f32_16x16x64_bf16 v[68:71], a[236:239], a[224:231], v240 abid:1\n"
    "s_nop 0\n"
    "ds_read_b128 a[224:227], v250 offset:54208\n"
    "ds_read_b128 a[228:231], v250 offset:54528\n"
    "ds_read_b128 a[236:239], v251 offset:23184\n"
    "s_waitcnt lgkmcnt(8)\n"
    "s_barrier\n"
    "ds_read_b64_tr_b16 a[128:129], v224 offset:0\n"
    "ds_read_b64_tr_b16 a[132:133], v225 offset:0\n"
    "ds_read_b64_tr_b16 a[136:137], v226 offset:0\n"
    "ds_read_b64_tr_b16 a[140:141], v227 offset:0\n"
    "s_waitcnt vmcnt(8)\n"
    "ds_write2_b32 v216, a200, a201 offset0:0 offset1:20\n"
    "s_waitcnt lgkmcnt(10)\n"
    "v_smfmac_f32_16x16x64_bf16 v[72:75], a[248:251], a[240:247], v240\n"
    "ds_read_b64_tr_b16 a[144:145], v228 offset:0\n"
    "ds_read_b64_tr_b16 a[148:149], v229 offset:0\n"
    "ds_read_b64_tr_b16 a[152:153], v230 offset:0\n"
    "s_waitcnt lgkmcnt(12)\n"
    "v_smfmac_f32_16x16x64_bf16 v[76:79], a[252:255], a[240:247], v240 abid:1\n"
    "s_nop 0\n"
    "ds_read_b64_tr_b16 a[156:157], v231 offset:0\n"
    "ds_read_b64_tr_b16 a[160:161], v224 offset:256\n"
    "ds_read_b64_tr_b16 a[164:165], v225 offset:256\n"
    "ds_read_b64_tr_b16 a[168:169], v226 offset:256\n"
    "ds_read_b64_tr_b16 a[172:173], v227 offset:256\n"
    "s_waitcnt lgkmcnt(14)\n"
    "v_smfmac_f32_16x16x64_bf16 v[80:83], a[232:235], a[224:231], v240\n"
    "s_waitcnt lgkmcnt(13)\n"
    "ds_write2_b32 v216, a202, a203 offset0:40 offset1:60\n"
    "v_smfmac_f32_16x16x64_bf16 v[84:87], a[236:239], a[224:231], v240 abid:1\n"
    "ds_read_b128 a[240:243], v250 offset:54288\n"
    "ds_read_b128 a[244:247], v250 offset:54608\n"
    "ds_read_b128 a[248:251], v251 offset:23520\n"
    "ds_read_b128 a[252:255], v251 offset:23856\n"
    "ds_read_b128 a[232:235], v252 offset:0\n"
    "ds_read_b128 a[236:239], v252 offset:336\n"
    "s_waitcnt vmcnt(7)\n"
    "ds_write2_b32 v216, a204, a205 offset0:80 offset1:100\n"
    "ds_write2_b32 v216, a206, a207 offset0:120 offset1:140\n"
    "v_add_u32_e32 v216, 1360, v216\n"
    "s_waitcnt vmcnt(6)\n"
    "v_or3_b32 v200, v200, v156, v157\n"
    "v_or3_b32 v200, v200, v158, v159\n"
    "v_add_u32_e64 v156, v201, v156\n"
    "v_add_u32_e32 v158, v201, v158\n"
    "ds_write_b128 v217, v[156:159] offset:12288\n"
    "s_waitcnt lgkmcnt(4)\n"
    "v_smfmac_f32_16x16x64_bf16 v[88:91], a[248:251], a[240:247], v240\n"
    "v_smfmac_f32_16x16x64_bf16 v[92:95], a[252:255], a[240:247], v240 abid:1\n"
    "ds_read_b128 a[248:251], v252 offset:672\n"
    "ds_read_b128 a[252:255], v252 offset:1008\n"
    "s_waitcnt vmcnt(5)\n"
    "v_or3_b32 v200, v200, v160, v161\n"
    "v_or3_b32 v200, v200, v162, v163\n"
    "v_add_u32_e32 v160, v203, v160\n"
    "v_add_u32_e32 v162, v203, v162\n"
    "ds_write_b128 v217, v[160:163] offset:12544\n"
    "s_waitcnt vmcnt(4)\n"
    "v_or3_b32 v200, v200, v164, v165\n"
    "v_or3_b32 v200, v200, v166, v167\n"
    "v_add_u32_e64 v164, v205, v164\n"
    "v_add_u32_e64 v166, v205, v166\n"
    "ds_write_b128 v217, v[164:167] offset:12800\n"
    "v_and_b32_e32 v200, 0x40004000, v200\n"
    "v_cmp_le_u32_e32 vcc, 1, v200\n"
    "s_nop 1\n"
    "s_cmp_lg_u64 vcc, 0\n"
    "s_cselect_b32 vcc_lo, 1, 0\n"
    "s_or_b32 s66, s66, vcc_lo\n"
    "v_cmp_le_u32_e32 vcc, 981467136, v232\n"
    "s_nop 1\n"
    "s_cmp_lg_u64 vcc, 0\n"
    "s_cselect_b32 vcc_lo, 2, 0\n"
    "s_or_b32 s66, s66, vcc_lo\n"
    "v_mov_b32_e32 v200, s66\n"
    "v_mov_b32_e32 v232, 162992\n"
    "s_mov_b64 exec, 1\n"
    "ds_or_b32 v232, v200\n"
    "s_mov_b64 exec, -1\n"
    "s_waitcnt lgkmcnt(1)\n"
    "s_barrier\n"
    "ds_read_b64_tr_b16 a[130:131], v224 offset:12288\n"
    "ds_read_b64_tr_b16 a[134:135], v225 offset:12288\n"
    "ds_read_b64_tr_b16 a[138:139], v226 offset:12288\n"
    "ds_read_b64_tr_b16 a[142:143], v227 offset:12288\n"
    "ds_read_b64_tr_b16 a[146:147], v228 offset:12288\n"
    "s_nop 0\n"
    "s_waitcnt vmcnt(3)\n"
    "ds_write2_b32 v216, a208, a209 offset0:0 offset1:20\n"
    "ds_read_b64_tr_b16 a[150:151], v229 offset:12288\n"
    "ds_read_b64_tr_b16 a[154:155], v230 offset:12288\n"
    "ds_read_b64_tr_b16 a[158:159], v231 offset:12288\n"
    "ds_read_b64_tr_b16 a[162:163], v224 offset:12544\n"
    "ds_read_b64_tr_b16 a[166:167], v225 offset:12544\n"
    "ds_read_b64_tr_b16 a[170:171], v226 offset:12544\n"
    "ds_read_b64_tr_b16 a[174:175], v227 offset:12544\n"
    "ds_write2_b32 v216, a210, a211 offset0:40 offset1:60\n"
    "s_waitcnt vmcnt(2)\n"
    "ds_write2_b32 v216, a212, a213 offset0:80 offset1:100\n"
    "s_waitcnt lgkmcnt(1)\n"
    "s_barrier\n"
    "s_add_u32 m0, s38, 0\n"
    "s_mov_b32 s64, 18432\n"
    "buffer_load_dwordx4 v210, s[84:87], s64 offen offset:0 lds\n"
    "buffer_load_dwordx4 v210, s[84:87], s64 offen offset:1024 lds\n"
    "buffer_load_dwordx4 v210, s[84:87], s64 offen offset:2048 lds\n"
    "s_add_u32 m0, s38, 3072\n"
    "s_mov_b32 s64, 21504\n"
    "buffer_load_dwordx4 v210, s[84:87], s64 offen offset:0 lds\n"
    "buffer_load_dwordx4 v210, s[84:87], s64 offen offset:1024 lds\n"
    "buffer_load_dwordx4 v210, s[84:87], s64 offen offset:2048 lds\n"
    "s_add_u32 m0, s38, 6144\n"
    "s_mov_b32 s64, 24576\n"
    "buffer_load_dwordx4 v210, s[84:87], s64 offen offset:0 lds\n"
    "buffer_load_dwordx4 v210, s[84:87], s64 offen offset:1024 lds\n"
    "buffer_load_dwordx4 v210, s[84:87], s64 offen offset:2048 lds\n"
    "v_lshlrev_b32_e64 v235, 3, v244\n"
    "s_mov_b32 exec_hi, 0\n"
    "buffer_load_dword v233, v235, s[40:43], s53 offen sc1\n"
    "s_mov_b32 exec_hi, -1\n"
    "s_add_u32 s53, s53, 393216\n"
    "ds_write2_b32 v216, a214, a215 offset0:120 offset1:140\n"
    "v_add_u32_e32 v216, 1360, v216\n"
    "s_mov_b32 s64, 0\n"
    "s_mov_b32 s59, 0\n"
    "ds_read_b32 v232, v232\n"
    "v_lshlrev_b32_e32 v235, 3, v244\n"
    "s_mov_b32 exec_hi, 0\n"
    "buffer_load_dword v233, v235, s[40:43], s53 offen sc1\n"
    "s_mov_b32 exec_hi, -1\n"
    "s_add_u32 s53, s53, 393216\n"
    "s_waitcnt vmcnt(12)\n"
    "ds_write2_b32 v216, a216, a217 offset0:0 offset1:20\n"
    "ds_write2_b32 v216, a218, a219 offset0:40 offset1:60\n"
    "v_lshlrev_b32_e32 v235, 3, v244\n"
    "s_mov_b32 exec_hi, 0\n"
    "buffer_load_dword v233, v235, s[40:43], s53 offen sc1\n"
    "s_mov_b32 exec_hi, -1\n"
    "s_add_u32 s53, s53, 393216\n"
    "s_waitcnt vmcnt(12)\n"
    "ds_write2_b32 v216, a220, a221 offset0:80 offset1:100\n"
    "ds_write2_b32 v216, a222, a223 offset0:120 offset1:140\n"
    "v_lshlrev_b32_e64 v235, 3, v244\n"
    "s_mov_b32 exec_hi, 0\n"
    "buffer_load_dword v233, v235, s[40:43], s53 offen sc1\n"
    "s_mov_b32 exec_hi, -1\n"
    "s_add_u32 s53, s53, 393216\n"
    "v_mov_b32_e64 v235, v222\n"
    "s_waitcnt lgkmcnt(0)\n"
    "s_barrier\n"
    "ds_read_b128 a[224:227], v250 offset:640\n"
    "ds_read_b128 a[228:231], v250 offset:960\n"
    "ds_read_b128 a[240:243], v250 offset:720\n"
    "ds_read_b128 a[244:247], v250 offset:1040\n"
    "ds_read_b128 v[128:131], v250 offset:800\n"
    "ds_read_b128 v[132:135], v250 offset:1120\n"
    "s_waitcnt lgkmcnt(4)\n"
    "v_smfmac_f32_16x16x64_bf16 v[4:7], a[236:239], a[224:231], v240 abid:1\n"
    "s_nop 0\n"
    "ds_read_b128 a[236:239], v252 offset:2352\n"
    "v_smfmac_f32_16x16x64_bf16 v[0:3], a[232:235], a[224:231], v240\n"
    "ds_read_b128 a[224:227], v250 offset:880\n"
    "s_waitcnt lgkmcnt(4)\n"
    "v_smfmac_f32_16x16x64_bf16 v[8:11], a[248:251], a[240:247], v240\n"
    "ds_read_b128 a[228:231], v250 offset:1200\n"
    "v_smfmac_f32_16x16x64_bf16 v[12:15], a[252:255], a[240:247], v240 abid:1\n"
    "s_waitcnt lgkmcnt(3)\n"
    "ds_read_b128 a[232:235], v252 offset:2016\n"
    "v_smfmac_f32_16x16x64_bf16 v[16:19], v[136:139], v[128:135], v240\n"
    "ds_read_b128 a[240:243], v250 offset:27664\n"
    "v_smfmac_f32_16x16x64_bf16 v[20:23], v[140:143], v[128:135], v240 abid:1\n"
    "ds_read_b128 a[244:247], v250 offset:27984\n"
    "ds_read_b128 a[248:251], v252 offset:10752\n"
    "ds_read_b128 a[252:255], v252 offset:11088\n"
    "v_pk_mov_b32 v[224:225], v[0:1], v[0:1] op_sel:[0,1]\n"
    "v_pk_mov_b32 v[226:227], v[2:3], v[2:3] op_sel:[0,1]\n"
    "v_pk_mul_f32 v[228:229], v[0:1], v[0:1]\n"
    "v_pk_mul_f32 v[230:231], v[2:3], v[2:3]\n"
    "v_pk_add_f32 v[224:225], v[224:225], v[4:5]\n"
    "v_pk_add_f32 v[226:227], v[226:227], v[6:7]\n"
    "v_pk_fma_f32 v[228:229], v[4:5], v[4:5], v[228:229]\n"
    "v_pk_fma_f32 v[230:231], v[6:7], v[6:7], v[230:231]\n"
    "v_pk_add_f32 v[224:225], v[224:225], v[8:9]\n"
    "v_pk_add_f32 v[226:227], v[226:227], v[10:11]\n"
    "v_pk_fma_f32 v[228:229], v[8:9], v[8:9], v[228:229]\n"
    "v_pk_fma_f32 v[230:231], v[10:11], v[10:11], v[230:231]\n"
    "v_pk_add_f32 v[224:225], v[224:225], v[12:13]\n"
    "v_pk_add_f32 v[226:227], v[226:227], v[14:15]\n"
    "v_pk_fma_f32 v[228:229], v[12:13], v[12:13], v[228:229]\n"
    "v_pk_fma_f32 v[230:231], v[14:15], v[14:15], v[230:231]\n"
    "v_pk_add_f32 v[224:225], v[224:225], v[16:17]\n"
    "v_pk_add_f32 v[226:227], v[226:227], v[18:19]\n"
    "v_pk_fma_f32 v[228:229], v[16:17], v[16:17], v[228:229]\n"
    "v_pk_fma_f32 v[230:231], v[18:19], v[18:19], v[230:231]\n"
    "v_pk_add_f32 v[224:225], v[224:225], v[20:21]\n"
    "v_pk_add_f32 v[226:227], v[226:227], v[22:23]\n"
    "v_pk_fma_f32 v[228:229], v[20:21], v[20:21], v[228:229]\n"
    "v_pk_fma_f32 v[230:231], v[22:23], v[22:23], v[230:231]\n"
    "s_waitcnt lgkmcnt(3)\n"
    "v_smfmac_f32_16x16x64_bf16 v[24:27], a[232:235], a[224:231], v240\n"
    "s_nop 0\n"
    "ds_read_b128 v[128:131], v250 offset:27744\n"
    "v_smfmac_f32_16x16x64_bf16 v[28:31], a[236:239], a[224:231], v240 abid:1\n"
    "s_waitcnt lgkmcnt(2)\n"
    "ds_read_b128 v[132:135], v250 offset:28064\n"
    "v_smfmac_f32_16x16x64_bf16 v[32:35], a[248:251], a[240:247], v240\n"
    "s_waitcnt lgkmcnt(2)\n"
    "ds_read_b128 v[136:139], v252 offset:11424\n"
    "v_smfmac_f32_16x16x64_bf16 v[36:39], a[252:255], a[240:247], v240 abid:1\n"
    "ds_read_b128 v[140:143], v252 offset:11760\n"
    "ds_read_b128 a[224:227], v250 offset:27824\n"
    "ds_read_b128 a[228:231], v250 offset:28144\n"
    "v_pk_add_f32 v[224:225], v[224:225], v[24:25]\n"
    "v_pk_add_f32 v[226:227], v[226:227], v[26:27]\n"
    "v_pk_fma_f32 v[228:229], v[24:25], v[24:25], v[228:229]\n"
    "v_pk_fma_f32 v[230:231], v[26:27], v[26:27], v[230:231]\n"
    "v_pk_add_f32 v[224:225], v[224:225], v[28:29]\n"
    "v_pk_add_f32 v[226:227], v[226:227], v[30:31]\n"
    "v_pk_fma_f32 v[228:229], v[28:29], v[28:29], v[228:229]\n"
    "v_pk_fma_f32 v[230:231], v[30:31], v[30:31], v[230:231]\n"
    "v_pk_add_f32 v[224:225], v[224:225], v[32:33]\n"
    "v_pk_add_f32 v[226:227], v[226:227], v[34:35]\n"
    "v_pk_fma_f32 v[228:229], v[32:33], v[32:33], v[228:229]\n"
    "v_pk_fma_f32 v[230:231], v[34:35], v[34:35], v[230:231]\n"
    "v_pk_add_f32 v[224:225], v[224:225], v[36:37]\n"
    "v_pk_add_f32 v[226:227], v[226:227], v[38:39]\n"
    "v_pk_fma_f32 v[228:229], v[36:37], v[36:37], v[228:229]\n"
    "v_pk_fma_f32 v[230:231], v[38:39], v[38:39], v[230:231]\n"
    "ds_read_b128 a[232:235], v252 offset:12096\n"
    "ds_read_b128 a[236:239], v252 offset:12432\n"
    "ds_read_b128 a[240:243], v250 offset:27904\n"
    "ds_read_b128 a[244:247], v250 offset:28224\n"
    "ds_read_b128 a[248:251], v252 offset:12768\n"
    "s_waitcnt lgkmcnt(8)\n"
    "v_smfmac_f32_16x16x64_bf16 v[40:43], v[136:139], v[128:135], v240\n"
    "s_waitcnt lgkmcnt(7)\n"
    "ds_read_b128 a[252:255], v252 offset:13104\n"
    "v_smfmac_f32_16x16x64_bf16 v[44:47], v[140:143], v[128:135], v240 abid:1\n"
    "ds_read_b128 v[128:131], v250 offset:54688\n"
    "ds_read_b128 v[132:135], v250 offset:55008\n"
    "ds_read_b128 v[136:139], v252 offset:21504\n"
    "ds_read_b128 v[140:143], v252 offset:21840\n"
    "s_nop 0\n"
    "s_nop 0\n"
    "v_pk_add_f32 v[224:225], v[224:225], v[40:41]\n"
    "v_pk_add_f32 v[226:227], v[226:227], v[42:43]\n"
    "v_pk_fma_f32 v[228:229], v[40:41], v[40:41], v[228:229]\n"
    "v_pk_fma_f32 v[230:231], v[42:43], v[42:43], v[230:231]\n"
    "v_pk_add_f32 v[224:225], v[224:225], v[44:45]\n"
    "v_pk_add_f32 v[226:227], v[226:227], v[46:47]\n"
    "v_pk_fma_f32 v[228:229], v[44:45], v[44:45], v[228:229]\n"
    "v_pk_fma_f32 v[230:231], v[46:47], v[46:47], v[230:231]\n"
    "s_waitcnt lgkmcnt(9)\n"
    "v_smfmac_f32_16x16x64_bf16 v[48:51], a[232:235], a[224:231], v240\n"
    "s_nop 0\n"
    "ds_read_b128 a[232:235], v252 offset:22176\n"
    "s_waitcnt lgkmcnt(9)\n"
    "v_smfmac_f32_16x16x64_bf16 v[52:55], a[236:239], a[224:231], v240 abid:1\n"
    "ds_read_b128 a[224:227], v250 offset:54768\n"
    "ds_read_b128 a[228:231], v250 offset:55088\n"
    "ds_read_b128 a[236:239], v252 offset:22512\n"
    "s_nop 1\n"
    "v_pk_add_f32 v[224:225], v[224:225], v[48:49]\n"
    "v_pk_add_f32 v[226:227], v[226:227], v[50:51]\n"
    "v_pk_fma_f32 v[228:229], v[48:49], v[48:49], v[228:229]\n"
    "v_pk_fma_f32 v[230:231], v[50:51], v[50:51], v[230:231]\n"
    "v_pk_add_f32 v[224:225], v[224:225], v[52:53]\n"
    "v_pk_add_f32 v[226:227], v[226:227], v[54:55]\n"
    "v_pk_fma_f32 v[228:229], v[52:53], v[52:53], v[228:229]\n"
    "v_pk_fma_f32 v[230:231], v[54:55], v[54:55], v[230:231]\n"
    "s_waitcnt lgkmcnt(8)\n"
    "v_smfmac_f32_16x16x64_bf16 v[56:59], a[248:251], a[240:247], v240\n"
    "s_nop 0\n"
    "ds_read_b128 a[248:251], v252 offset:22848\n"
    "v_smfmac_f32_16x16x64_bf16 v[60:63], a[252:255], a[240:247], v240 abid:1\n"
    "ds_read_b128 a[240:243], v250 offset:54848\n"
    "ds_read_b128 a[244:247], v250 offset:55168\n"
    "ds_read_b128 a[252:255], v252 offset:23184\n"
    "s_waitcnt lgkmcnt(9)\n"
    "v_smfmac_f32_16x16x64_bf16 v[64:67], v[136:139], v[128:135], v240\n"
    "s_waitcnt lgkmcnt(8)\n"
    "ds_read_b128 v[136:139], v252 offset:23520\n"
    "v_smfmac_f32_16x16x64_bf16 v[68:71], v[140:143], v[128:135], v240 abid:1\n"
    "ds_read_b128 v[128:131], v250 offset:54928\n"
    "ds_read_b128 v[132:135], v250 offset:55248\n"
    "ds_read_b128 v[140:143], v252 offset:23856\n"
    "s_waitcnt vmcnt(4)\n"
    "s_barrier\n"
    "v_pk_add_f32 v[224:225], v[224:225], v[56:57]\n"
    "v_pk_add_f32 v[226:227], v[226:227], v[58:59]\n"
    "v_pk_fma_f32 v[228:229], v[56:57], v[56:57], v[228:229]\n"
    "v_pk_fma_f32 v[230:231], v[58:59], v[58:59], v[230:231]\n"
    "v_pk_add_f32 v[224:225], v[224:225], v[60:61]\n"
    "v_pk_add_f32 v[226:227], v[226:227], v[62:63]\n"
    "v_pk_fma_f32 v[228:229], v[60:61], v[60:61], v[228:229]\n"
    "v_pk_fma_f32 v[230:231], v[62:63], v[62:63], v[230:231]\n"
    "v_pk_add_f32 v[224:225], v[224:225], v[64:65]\n"
    "v_pk_add_f32 v[226:227], v[226:227], v[66:67]\n"
    "v_pk_fma_f32 v[228:229], v[64:65], v[64:65], v[228:229]\n"
    "v_pk_fma_f32 v[230:231], v[66:67], v[66:67], v[230:231]\n"
    "v_pk_add_f32 v[224:225], v[224:225], v[68:69]\n"
    "v_pk_add_f32 v[226:227], v[226:227], v[70:71]\n"
    "v_pk_fma_f32 v[228:229], v[68:69], v[68:69], v[228:229]\n"
    "v_pk_fma_f32 v[230:231], v[70:71], v[70:71], v[230:231]\n"
    "s_waitcnt lgkmcnt(5)\n"
    "v_smfmac_f32_16x16x64_bf16 v[72:75], a[232:235], a[224:231], v240\n"
    "s_waitcnt lgkmcnt(4)\n"
    "ds_read_b64_tr_b16 a[176:177], v220 offset:0\n"
    "v_smfmac_f32_16x16x64_bf16 v[76:79], a[236:239], a[224:231], v240 abid:1\n"
    "ds_read_b64_tr_b16 a[178:179], v220 offset:3072\n"
    "v_smfmac_f32_16x16x64_bf16 v[80:83], a[248:251], a[240:247], v240\n"
    "v_accvgpr_write_b32 a248, v236\n"
    "v_smfmac_f32_16x16x64_bf16 v[84:87], a[252:255], a[240:247], v240 abid:1\n"
    "s_waitcnt lgkmcnt(3)\n"
    "v_accvgpr_write_b32 a249, v237\n"
    "v_smfmac_f32_16x16x64_bf16 v[88:91], v[136:139], v[128:135], v240\n"
    "s_waitcnt lgkmcnt(2)\n"
    "v_accvgpr_write_b32 a250, v238\n"
    "v_smfmac_f32_16x16x64_bf16 v[92:95], v[140:143], v[128:135], v240 abid:1\n"
    "v_accvgpr_write_b32 a251, v239\n"
    "ds_read_b64_tr_b16 a[180:181], v220 offset:8\n"
    "v_pk_add_f32 v[224:225], v[224:225], v[72:73]\n"
    "v_pk_add_f32 v[226:227], v[226:227], v[74:75]\n"
    "v_pk_fma_f32 v[228:229], v[72:73], v[72:73], v[228:229]\n"
    "v_pk_fma_f32 v[230:231], v[74:75], v[74:75], v[230:231]\n"
    "v_pk_add_f32 v[224:225], v[224:225], v[76:77]\n"
    "v_pk_add_f32 v[226:227], v[226:227], v[78:79]\n"
    "v_pk_fma_f32 v[228:229], v[76:77], v[76:77], v[228:229]\n"
    "v_pk_fma_f32 v[230:231], v[78:79], v[78:79], v[230:231]\n"
    "v_pk_add_f32 v[224:225], v[224:225], v[80:81]\n"
    "v_pk_add_f32 v[226:227], v[226:227], v[82:83]\n"
    "v_pk_fma_f32 v[228:229], v[80:81], v[80:81], v[228:229]\n"
    "v_pk_fma_f32 v[230:231], v[82:83], v[82:83], v[230:231]\n"
    "v_pk_add_f32 v[224:225], v[224:225], v[84:85]\n"
    "v_pk_add_f32 v[226:227], v[226:227], v[86:87]\n"
    "v_pk_fma_f32 v[228:229], v[84:85], v[84:85], v[228:229]\n"
    "v_pk_fma_f32 v[230:231], v[86:87], v[86:87], v[230:231]\n"
    "v_pk_add_f32 v[224:225], v[224:225], v[88:89]\n"
    "v_pk_add_f32 v[226:227], v[226:227], v[90:91]\n"
    "v_pk_fma_f32 v[228:229], v[88:89], v[88:89], v[228:229]\n"
    "v_pk_fma_f32 v[230:231], v[90:91], v[90:91], v[230:231]\n"
    "v_pk_add_f32 v[224:225], v[224:225], v[92:93]\n"
    "v_pk_add_f32 v[226:227], v[226:227], v[94:95]\n"
    "v_pk_fma_f32 v[228:229], v[92:93], v[92:93], v[228:229]\n"
    "v_pk_fma_f32 v[230:231], v[94:95], v[94:95], v[230:231]\n"
    "ds_write_b128 v254, v[224:227]\n"
    "ds_write_b128 v254, v[228:231] offset:1024\n"
    "v_lshrrev_b32_e64 v254, 2, v242\n"
    "v_add_u32_e64 v254, s55, v254\n"
    "ds_read_b64_tr_b16 a[182:183], v220 offset:3080\n"
    "ds_read_b64_tr_b16 a[184:185], v220 offset:64\n"
    "ds_read_b64_tr_b16 a[186:187], v220 offset:3136\n"
    "ds_read_b64_tr_b16 a[188:189], v220 offset:72\n"
    "ds_read_b64_tr_b16 a[190:191], v220 offset:3144\n"
    "ds_read_b64_tr_b16 v[188:189], v218 offset:128\n"
    "ds_read_b64_tr_b16 v[190:191], v218 offset:3200\n"
    "ds_read_b64_tr_b16 a[192:193], v220 offset:6144\n"
    "ds_read_b64_tr_b16 a[194:195], v220 offset:9216\n"
    "ds_read_b64_tr_b16 a[196:197], v220 offset:6152\n"
    "ds_read_b64_tr_b16 a[198:199], v220 offset:9224\n"
    "s_waitcnt lgkmcnt(11)\n"
    "s_barrier\n"
    "ds_read_b32 v224, v254 offset:0\n"
    "ds_read_b32 v225, v254 offset:6752\n"
    "ds_read_b32 v226, v254 offset:13504\n"
    "ds_read_b32 v227, v254 offset:20256\n"
    "ds_read_b32 v228, v254 offset:1024\n"
    "ds_read_b32 v229, v254 offset:7776\n"
    "ds_read_b32 v230, v254 offset:14528\n"
    "ds_read_b32 v231, v254 offset:21280\n"
    "ds_read_b64_tr_b16 a[200:201], v220 offset:6208\n"
    "ds_read_b64_tr_b16 a[202:203], v220 offset:9280\n"
    "ds_read_b64_tr_b16 a[204:205], v220 offset:6216\n"
    "ds_read_b64_tr_b16 a[206:207], v220 offset:9288\n"
    "ds_read_b64_tr_b16 v[192:193], v218 offset:6272\n"
    "ds_read_b64_tr_b16 v[194:195], v218 offset:9344\n"
    "ds_read_b64_tr_b16 a[208:209], v220 offset:12288\n"
    "ds_read_b64_tr_b16 a[210:211], v220 offset:15360\n"
    "ds_read_b64_tr_b16 a[212:213], v220 offset:12296\n"
    "s_waitcnt lgkmcnt(15)\n"
    "v_add_f32_e32 v224, v224, v225\n"
    "ds_read_b64_tr_b16 a[214:215], v220 offset:15368\n"
    "s_waitcnt lgkmcnt(14)\n"
    "v_add_f32_e32 v226, v226, v227\n"
    "v_add_f32_e64 v224, v224, v226\n"
    "v_mul_f32_e32 v224, 0x3c2aaaab, v224\n"
    "s_waitcnt lgkmcnt(12)\n"
    "v_add_f32_e32 v228, v228, v229\n"
    "ds_read_b64_tr_b16 a[216:217], v220 offset:12352\n"
    "s_waitcnt lgkmcnt(11)\n"
    "v_add_f32_e32 v230, v230, v231\n"
    "v_add_f32_e64 v228, v228, v230\n"
    "v_mul_f32_e32 v228, 0x3c2aaaab, v228\n"
    "v_fma_f32 v228, -v224, v224, v228\n"
    "v_max_f32_e64 v228, 0, v228\n"
    "v_add_f32_e32 v228, 0x3727c5ac, v228\n"
    "v_rsq_f32_e64 v228, v228\n"
    "ds_read_b64_tr_b16 a[218:219], v220 offset:15424\n"
    "v_mul_f32_e64 v228, s50, v228\n"
    "v_mul_f32_e64 v224, v228, -v224\n"
    "ds_write_b32 v254, v228\n"
    "ds_write_b32 v254, v224 offset:1024\n"
    "v_add_u32_e64 v254, s56, v242\n"
    "ds_read_b64_tr_b16 a[220:221], v220 offset:12360\n"
    "ds_read_b64_tr_b16 a[222:223], v220 offset:15432\n"
    "ds_read_b64_tr_b16 v[196:197], v218 offset:12416\n"
    "ds_read_b64_tr_b16 v[198:199], v218 offset:15488\n"
    "s_waitcnt lgkmcnt(4)\n"
    "s_barrier\n"
    "ds_read_b128 v[224:227], v254\n"
    "ds_read_b128 v[228:231], v254 offset:1024\n"
    "s_waitcnt lgkmcnt(2)\n"
    "s_barrier\n"
    "ds_write_b128 v234, v[112:115] offset:0\n"
    "ds_write_b128 v234, v[116:119] offset:16\n"
    "s_nop 0\n"
    "s_waitcnt lgkmcnt(2)\n"
    "v_pk_fma_f32 v[64:65], v[64:65], v[224:225], v[228:229]\n"
    "v_pk_fma_f32 v[66:67], v[66:67], v[226:227], v[230:231]\n"
    "v_pk_fma_f32 v[68:69], v[68:69], v[224:225], v[228:229]\n"
    "v_pk_fma_f32 v[70:71], v[70:71], v[226:227], v[230:231]\n"
    "v_pk_fma_f32 v[72:73], v[72:73], v[224:225], v[228:229]\n"
    "v_pk_fma_f32 v[74:75], v[74:75], v[226:227], v[230:231]\n"
    "v_pk_fma_f32 v[76:77], v[76:77], v[224:225], v[228:229]\n"
    "v_pk_fma_f32 v[78:79], v[78:79], v[226:227], v[230:231]\n"
    "v_pk_fma_f32 v[80:81], v[80:81], v[224:225], v[228:229]\n"
    "v_pk_fma_f32 v[82:83], v[82:83], v[226:227], v[230:231]\n"
    "v_pk_fma_f32 v[84:85], v[84:85], v[224:225], v[228:229]\n"
    "v_pk_fma_f32 v[86:87], v[86:87], v[226:227], v[230:231]\n"
    "v_pk_fma_f32 v[88:89], v[88:89], v[224:225], v[228:229]\n"
    "v_pk_fma_f32 v[90:91], v[90:91], v[226:227], v[230:231]\n"
    "v_pk_fma_f32 v[92:93], v[92:93], v[224:225], v[228:229]\n"
    "v_pk_fma_f32 v[94:95], v[94:95], v[226:227], v[230:231]\n"
    "v_cvt_pk_bf16_f32 v96, v64, v68\n"
    "v_cvt_pk_bf16_f32 v100, v65, v69\n"
    "v_cvt_pk_bf16_f32 v104, v66, v70\n"
    "v_cvt_pk_bf16_f32 v108, v67, v71\n"
    "v_cvt_pk_bf16_f32 v97, v72, v76\n"
    "v_cvt_pk_bf16_f32 v101, v73, v77\n"
    "v_cvt_pk_bf16_f32 v105, v74, v78\n"
    "v_cvt_pk_bf16_f32 v109, v75, v79\n"
    "v_cvt_pk_bf16_f32 v98, v80, v84\n"
    "v_cvt_pk_bf16_f32 v102, v81, v85\n"
    "v_cvt_pk_bf16_f32 v106, v82, v86\n"
    "v_cvt_pk_bf16_f32 v110, v83, v87\n"
    "v_cvt_pk_bf16_f32 v99, v88, v92\n"
    "ds_write_b128 v253, v[96:99] offset:2048\n"
    "v_cvt_pk_bf16_f32 v103, v89, v93\n"
    "v_cvt_pk_bf16_f32 v107, v90, v94\n"
    "v_cvt_pk_bf16_f32 v111, v91, v95\n"
    "v_pk_fma_f32 v[0:1], v[0:1], v[224:225], v[228:229]\n"
    "v_pk_fma_f32 v[2:3], v[2:3], v[226:227], v[230:231]\n"
    "v_pk_fma_f32 v[4:5], v[4:5], v[224:225], v[228:229]\n"
    "ds_write_b128 v253, v[100:103] offset:5120\n"
    "v_pk_fma_f32 v[6:7], v[6:7], v[226:227], v[230:231]\n"
    "v_pk_fma_f32 v[8:9], v[8:9], v[224:225], v[228:229]\n"
    "v_pk_fma_f32 v[10:11], v[10:11], v[226:227], v[230:231]\n"
    "v_pk_fma_f32 v[12:13], v[12:13], v[224:225], v[228:229]\n"
    "v_pk_fma_f32 v[14:15], v[14:15], v[226:227], v[230:231]\n"
    "v_pk_fma_f32 v[16:17], v[16:17], v[224:225], v[228:229]\n"
    "v_pk_fma_f32 v[18:19], v[18:19], v[226:227], v[230:231]\n"
    "v_pk_fma_f32 v[20:21], v[20:21], v[224:225], v[228:229]\n"
    "v_pk_fma_f32 v[22:23], v[22:23], v[226:227], v[230:231]\n"
    "v_pk_fma_f32 v[24:25], v[24:25], v[224:225], v[228:229]\n"
    "v_pk_fma_f32 v[26:27], v[26:27], v[226:227], v[230:231]\n"
    "v_pk_fma_f32 v[28:29], v[28:29], v[224:225], v[228:229]\n"
    "v_pk_fma_f32 v[30:31], v[30:31], v[226:227], v[230:231]\n"
    "v_cvt_pk_bf16_f32 v80, v0, v4\n"
    "v_cvt_pk_bf16_f32 v84, v1, v5\n"
    "v_cvt_pk_bf16_f32 v88, v2, v6\n"
    "v_cvt_pk_bf16_f32 v92, v3, v7\n"
    "v_cvt_pk_bf16_f32 v81, v8, v12\n"
    "v_cvt_pk_bf16_f32 v85, v9, v13\n"
    "v_cvt_pk_bf16_f32 v89, v10, v14\n"
    "v_cvt_pk_bf16_f32 v93, v11, v15\n"
    "v_cvt_pk_bf16_f32 v82, v16, v20\n"
    "v_cvt_pk_bf16_f32 v86, v17, v21\n"
    "v_cvt_pk_bf16_f32 v90, v18, v22\n"
    "v_cvt_pk_bf16_f32 v94, v19, v23\n"
    "v_cvt_pk_bf16_f32 v83, v24, v28\n"
    "ds_write_b128 v253, v[80:83] offset:0\n"
    "v_cvt_pk_bf16_f32 v87, v25, v29\n"
    "v_cvt_pk_bf16_f32 v91, v26, v30\n"
    "v_cvt_pk_bf16_f32 v95, v27, v31\n"
    "v_pk_fma_f32 v[32:33], v[32:33], v[224:225], v[228:229]\n"
    "v_pk_fma_f32 v[34:35], v[34:35], v[226:227], v[230:231]\n"
    "v_pk_fma_f32 v[36:37], v[36:37], v[224:225], v[228:229]\n"
    "ds_write_b128 v253, v[84:87] offset:3072\n"
    "v_pk_fma_f32 v[38:39], v[38:39], v[226:227], v[230:231]\n"
    "v_pk_fma_f32 v[40:41], v[40:41], v[224:225], v[228:229]\n"
    "v_pk_fma_f32 v[42:43], v[42:43], v[226:227], v[230:231]\n"
    "v_pk_fma_f32 v[44:45], v[44:45], v[224:225], v[228:229]\n"
    "v_pk_fma_f32 v[46:47], v[46:47], v[226:227], v[230:231]\n"
    "v_pk_fma_f32 v[48:49], v[48:49], v[224:225], v[228:229]\n"
    "v_pk_fma_f32 v[50:51], v[50:51], v[226:227], v[230:231]\n"
    "v_pk_fma_f32 v[52:53], v[52:53], v[224:225], v[228:229]\n"
    "v_pk_fma_f32 v[54:55], v[54:55], v[226:227], v[230:231]\n"
    "v_pk_fma_f32 v[56:57], v[56:57], v[224:225], v[228:229]\n"
    "v_pk_fma_f32 v[58:59], v[58:59], v[226:227], v[230:231]\n"
    "v_pk_fma_f32 v[60:61], v[60:61], v[224:225], v[228:229]\n"
    "v_pk_fma_f32 v[62:63], v[62:63], v[226:227], v[230:231]\n"
    "v_cvt_pk_bf16_f32 v64, v32, v36\n"
    "v_cvt_pk_bf16_f32 v68, v33, v37\n"
    "v_cvt_pk_bf16_f32 v72, v34, v38\n"
    "v_cvt_pk_bf16_f32 v76, v35, v39\n"
    "v_cvt_pk_bf16_f32 v65, v40, v44\n"
    "v_cvt_pk_bf16_f32 v69, v41, v45\n"
    "v_cvt_pk_bf16_f32 v73, v42, v46\n"
    "v_cvt_pk_bf16_f32 v77, v43, v47\n"
    "v_cvt_pk_bf16_f32 v66, v48, v52\n"
    "v_cvt_pk_bf16_f32 v70, v49, v53\n"
    "v_cvt_pk_bf16_f32 v74, v50, v54\n"
    "v_cvt_pk_bf16_f32 v78, v51, v55\n"
    "v_cvt_pk_bf16_f32 v67, v56, v60\n"
    "ds_write_b128 v253, v[64:67] offset:1024\n"
    "v_cvt_pk_bf16_f32 v71, v57, v61\n"
    "v_cvt_pk_bf16_f32 v75, v58, v62\n"
    "v_cvt_pk_bf16_f32 v79, v59, v63\n"
    "v_mov_b32_e64 v64, v104\n"
    "v_mov_b32_e64 v65, v105\n"
    "v_mov_b32_e64 v66, v106\n"
    "ds_write_b128 v253, v[68:71] offset:4096\n"
    "v_mov_b32_e64 v67, v107\n"
    "v_mov_b32_e64 v68, v108\n"
    "v_mov_b32_e64 v69, v109\n"
    "v_mov_b32_e64 v70, v110\n"
    "v_mov_b32_e64 v71, v111\n"
    "ds_write_b128 v207, v[180:183] offset:32768\n"
    "ds_write_b128 v207, v[168:171] offset:0\n"
    "ds_write_b128 v207, v[172:175] offset:4096\n"
    "ds_write_b128 v207, v[176:179] offset:8192\n"
    "ds_write_b128 v207, v[188:191] offset:12288\n"
    "ds_write_b128 v207, v[192:195] offset:16384\n"
    "s_waitcnt lgkmcnt(2)\n"
    "s_barrier\n"
    "ds_write_b128 v207, v[196:199] offset:20480\n"
    "ds_read_b128 v[144:147], v242 offset:0\n"
    "ds_read_b128 v[148:151], v242 offset:1024\n"
    "ds_read_b128 v[152:155], v242 offset:2048\n"
    "ds_read_b128 v[156:159], v242 offset:3072\n"
    "ds_read_b128 v[160:163], v242 offset:4096\n"
    "ds_read_b128 v[164:167], v242 offset:5120\n"
    "ds_read_b128 a[240:243], v244 offset:32768\n"
    "s_waitcnt lgkmcnt(7)\n"
    "s_barrier\n"
    "s_waitcnt lgkmcnt(6)\n"
    "v_mfma_f32_16x16x32_bf16 v[168:171], a[80:83], v[144:147], 0\n"
    "v_mfma_f32_16x16x32_bf16 v[172:175], a[84:87], v[144:147], 0\n"
    "s_waitcnt lgkmcnt(5)\n"
    "v_mfma_f32_16x16x32_bf16 v[168:171], a[128:131], v[148:151], v[168:171]\n"
    "s_waitcnt lgkmcnt(4)\n"
    "v_mfma_f32_16x16x32_bf16 v[168:171], a[48:51], v[152:155], v[168:171]\n"
    "s_waitcnt lgkmcnt(3)\n"
    "v_mfma_f32_16x16x32_bf16 v[172:175], a[132:135], v[148:151], v[172:175]\n"
    "v_mfma_f32_16x16x32_bf16 v[176:179], a[80:83], v[156:159], 0\n"
    "s_waitcnt lgkmcnt(1)\n"
    "v_mfma_f32_16x16x32_bf16 v[176:179], a[128:131], v[160:163], v[176:179]\n"
    "s_waitcnt lgkmcnt(0)\n"
    "v_mfma_f32_16x16x32_bf16 v[172:175], a[240:243], v[152:155], v[172:175]\n"
    "v_mfma_f32_16x16x32_bf16 v[176:179], a[48:51], v[164:167], v[176:179]\n"
    "v_mfma_f32_16x16x32_bf16 v[180:183], a[84:87], v[156:159], 0\n"
    "v_mfma_f32_16x16x32_bf16 v[180:183], a[132:135], v[160:163], v[180:183]\n"
    "v_mfma_f32_16x16x32_bf16 v[180:183], a[240:243], v[164:167], v[180:183]\n"
    "v_readfirstlane_b32 s66, v232\n"
    "s_nop 1\n"
    "s_cmp_lg_u32 s66, 2\n"
    "s_cbranch_scc1 98f\n"
    ".p2align 6\n"
    "1:\n"
    "v_lshlrev_b32_sdwa v184, 1, v168 dst_sel:DWORD dst_unused:UNUSED_PAD src0_sel:DWORD src1_sel:WORD_1\n"
    "v_lshlrev_b32_sdwa v200, 1, v169 dst_sel:DWORD dst_unused:UNUSED_PAD src0_sel:DWORD src1_sel:WORD_1\n"
    "v_lshlrev_b32_sdwa v185, 1, v170 dst_sel:DWORD dst_unused:UNUSED_PAD src0_sel:DWORD src1_sel:WORD_1\n"
    "v_lshlrev_b32_sdwa v201, 1, v171 dst_sel:DWORD dst_unused:UNUSED_PAD src0_sel:DWORD src1_sel:WORD_1\n"
    "v_mfma_f32_16x16x32_bf16 v[140:143], a[244:247], v[188:191], v[140:143]\n"
    "ds_read_b128 a[240:243], v244 offset:36864\n"
    "ds_read_u16 v184, v184\n"
    "v_mfma_f32_16x16x32_bf16 v[168:171], a[88:91], v[144:147], 0\n"
    "ds_read_u16_d16_hi v200, v200\n"
    "ds_read_u16 v185, v185\n"
    "v_mfma_f32_16x16x32_bf16 v[168:171], a[136:139], v[148:151], v[168:171]\n"
    "buffer_load_dwordx4 v[0:3], v246, s[40:43], s60 offen offset:0\n"
    "buffer_load_dwordx4 v[32:35], v246, s[40:43], s60 offen offset:64\n"
    "ds_read_u16_d16_hi v201, v201\n"
    "v_mfma_f32_16x16x32_bf16 v[168:171], a[52:55], v[152:155], v[168:171]\n"
    "v_lshlrev_b32_sdwa v186, 1, v172 dst_sel:DWORD dst_unused:UNUSED_PAD src0_sel:DWORD src1_sel:WORD_1\n"
    "v_lshlrev_b32_sdwa v202, 1, v173 dst_sel:DWORD dst_unused:UNUSED_PAD src0_sel:DWORD src1_sel:WORD_1\n"
    "s_waitcnt vmcnt(3)\n"
    "v_smfmac_f32_32x32x32_bf16 v[128:143], a[248:251], a[224:231], v241\n"
    "s_nop 0\n"
    "ds_read_u16 v186, v186\n"
    "v_lshlrev_b32_sdwa v187, 1, v174 dst_sel:DWORD dst_unused:UNUSED_PAD src0_sel:DWORD src1_sel:WORD_1\n"
    "v_lshlrev_b32_sdwa v203, 1, v175 dst_sel:DWORD dst_unused:UNUSED_PAD src0_sel:DWORD src1_sel:WORD_1\n"
    "v_lshlrev_b32_sdwa v188, 1, v176 dst_sel:DWORD dst_unused:UNUSED_PAD src0_sel:DWORD src1_sel:WORD_1\n"
    "v_lshlrev_b32_sdwa v204, 1, v177 dst_sel:DWORD dst_unused:UNUSED_PAD src0_sel:DWORD src1_sel:WORD_1\n"
    "v_mfma_f32_16x16x32_bf16 v[172:175], a[92:95], v[144:147], 0\n"
    "ds_read_b128 v[236:239], v244 offset:20480\n"
    "ds_read_u16_d16_hi v202, v202\n"
    "v_mfma_f32_16x16x32_bf16 v[172:175], a[140:143], v[148:151], v[172:175]\n"
    "v_lshlrev_b32_sdwa v189, 1, v178 dst_sel:DWORD dst_unused:UNUSED_PAD src0_sel:DWORD src1_sel:WORD_1\n"
    "v_lshlrev_b32_sdwa v205, 1, v179 dst_sel:DWORD dst_unused:UNUSED_PAD src0_sel:DWORD src1_sel:WORD_1\n"
    "s_waitcnt lgkmcnt(7)\n"
    "v_mfma_f32_16x16x32_bf16 v[172:175], a[240:243], v[152:155], v[172:175]\n"
    "s_nop 0\n"
    "ds_read_u16 v187, v187\n"
    "v_lshlrev_b32_sdwa v190, 1, v180 dst_sel:DWORD dst_unused:UNUSED_PAD src0_sel:DWORD src1_sel:WORD_1\n"
    "v_mfma_f32_16x16x32_bf16 v[176:179], a[88:91], v[156:159], 0\n"
    "v_lshlrev_b32_sdwa v206, 1, v181 dst_sel:DWORD dst_unused:UNUSED_PAD src0_sel:DWORD src1_sel:WORD_1\n"
    "v_lshlrev_b32_sdwa v191, 1, v182 dst_sel:DWORD dst_unused:UNUSED_PAD src0_sel:DWORD src1_sel:WORD_1\n"
    "v_mfma_f32_16x16x32_bf16 v[176:179], a[136:139], v[160:163], v[176:179]\n"
    "ds_read_b128 a[244:247], v244 offset:22528\n"
    "v_lshlrev_b32_sdwa v207, 1, v183 dst_sel:DWORD dst_unused:UNUSED_PAD src0_sel:DWORD src1_sel:WORD_1\n"
    "v_mfma_f32_16x16x32_bf16 v[176:179], a[52:55], v[164:167], v[176:179]\n"
    "ds_read_u16_d16_hi v203, v203\n"
    "v_pk_fma_f16 v234, v234, s49, s49\n"
    "v_mfma_f32_16x16x32_bf16 v[180:183], a[92:95], v[156:159], 0\n"
    "s_waitcnt lgkmcnt(8)\n"
    "v_rcp_f16_e32 v232, v234\n"
    "v_mfma_f32_16x16x32_bf16 v[180:183], a[140:143], v[160:163], v[180:183]\n"
    "ds_read_u16 v188, v188\n"
    "v_rcp_f16_sdwa v232, v234 dst_sel:WORD_1 dst_unused:UNUSED_PRESERVE src0_sel:WORD_1\n"
    "v_mfma_f32_16x16x32_bf16 v[180:183], a[240:243], v[164:167], v[180:183]\n"
    "ds_read_u16_d16_hi v204, v204\n"
    "v_fma_mix_f32 v234, v196, v232, 0 op_sel_hi:[1,1,0]\n"
    "v_mfma_f32_16x16x32_bf16 v[96:99], a[208:211], v[192:195], v[96:99]\n"
    "v_fma_mix_f32 v232, v196, v232, 0 op_sel:[1,1,0] op_sel_hi:[1,1,0]\n"
    "v_or_b32_e32 v184, v184, v200\n"
    "v_mfma_f32_16x16x32_bf16 v[100:103], a[212:215], v[192:195], v[100:103]\n"
    "s_waitcnt lgkmcnt(8)\n"
    "ds_read_u16 v189, v189\n"
    "v_cvt_pk_bf16_f32 v196, v234, v232\n"
    "v_mfma_f32_16x16x32_bf16 v[112:115], a[216:219], v[192:195], v[112:115]\n"
    "s_waitcnt lgkmcnt(7)\n"
    "v_or_b32_e32 v185, v185, v201\n"
    "v_lshlrev_b32_sdwa v201, 1, v171 dst_sel:DWORD dst_unused:UNUSED_PAD src0_sel:DWORD src1_sel:WORD_1\n"
    "v_mfma_f32_16x16x32_bf16 v[116:119], a[220:223], v[192:195], v[116:119]\n"
    "ds_read_u16_d16_hi v205, v205\n"
    "v_cvt_pk_bf16_f32 v96, v96, v97\n"
    "v_mfma_f32_16x16x32_bf16 v[128:131], v[236:239], v[192:195], v[128:131]\n"
    "ds_read_u16 v190, v190\n"
    "buffer_load_dwordx4 v[20:23], v247, s[40:43], s60 offen offset:0\n"
    "buffer_load_dwordx4 v[52:55], v247, s[40:43], s60 offen offset:64\n"
    "v_mfma_f32_16x16x32_bf16 v[104:107], a[208:211], v[196:199], v[104:107]\n"
    "s_waitcnt lgkmcnt(8)\n"
    "v_or_b32_e64 v186, v186, v202\n"
    "v_lshlrev_b32_sdwa v202, 1, v173 dst_sel:DWORD dst_unused:UNUSED_PAD src0_sel:DWORD src1_sel:WORD_1\n"
    "v_mfma_f32_16x16x32_bf16 v[108:111], a[212:215], v[196:199], v[108:111]\n"
    "s_waitcnt lgkmcnt(6)\n"
    "ds_read_u16_d16_hi v206, v206\n"
    "v_cvt_pk_bf16_f32 v97, v98, v99\n"
    "v_mfma_f32_16x16x32_bf16 v[132:135], a[244:247], v[192:195], v[132:135]\n"
    "v_cvt_pk_f16_f32 v192, v168, v169\n"
    "v_lshlrev_b32_sdwa v193, 1, v170 dst_sel:DWORD dst_unused:UNUSED_PAD src0_sel:DWORD src1_sel:WORD_1\n"
    "v_mfma_f32_16x16x32_bf16 v[120:123], a[216:219], v[196:199], v[120:123]\n"
    "ds_read_u16 v191, v191\n"
    "v_lshlrev_b32_sdwa v194, 1, v172 dst_sel:DWORD dst_unused:UNUSED_PAD src0_sel:DWORD src1_sel:WORD_1\n"
    "v_mfma_f32_16x16x32_bf16 v[124:127], a[220:223], v[196:199], v[124:127]\n"
    "s_waitcnt lgkmcnt(7)\n"
    "ds_read_u16_d16_hi v207, v207\n"
    "v_lshlrev_b32_sdwa v195, 1, v174 dst_sel:DWORD dst_unused:UNUSED_PAD src0_sel:DWORD src1_sel:WORD_1\n"
    "v_mfma_f32_16x16x32_bf16 v[136:139], v[236:239], v[196:199], v[136:139]\n"
    "v_or_b32_e32 v187, v187, v203\n"
    "v_lshlrev_b32_sdwa v203, 1, v175 dst_sel:DWORD dst_unused:UNUSED_PAD src0_sel:DWORD src1_sel:WORD_1\n"
    "v_mfma_f32_16x16x32_bf16 v[140:143], a[244:247], v[196:199], v[140:143]\n"
    "ds_read_u16 v193, v193\n"
    "v_cvt_pk_f16_f32 v196, v176, v177\n"
    "v_mfma_f32_16x16x32_bf16 v[168:171], a[96:99], v[144:147], 0\n"
    "v_lshlrev_b32_sdwa v197, 1, v178 dst_sel:DWORD dst_unused:UNUSED_PAD src0_sel:DWORD src1_sel:WORD_1\n"
    "v_lshlrev_b32_sdwa v198, 1, v180 dst_sel:DWORD dst_unused:UNUSED_PAD src0_sel:DWORD src1_sel:WORD_1\n"
    "v_mfma_f32_16x16x32_bf16 v[168:171], a[144:147], v[148:151], v[168:171]\n"
    "s_waitcnt lgkmcnt(7)\n"
    "ds_read_u16_d16_hi v201, v201\n"
    "v_lshlrev_b32_sdwa v199, 1, v182 dst_sel:DWORD dst_unused:UNUSED_PAD src0_sel:DWORD src1_sel:WORD_1\n"
    "v_mfma_f32_16x16x32_bf16 v[168:171], a[56:59], v[152:155], v[168:171]\n"
    "v_or_b32_e32 v188, v188, v204\n"
    "v_cvt_pk_bf16_f32 v98, v100, v101\n"
    "v_mfma_f32_16x16x32_bf16 v[172:175], a[100:103], v[144:147], 0\n"
    "ds_read_u16 v194, v194\n"
    "v_cvt_pk_bf16_f32 v99, v102, v103\n"
    "v_mfma_f32_16x16x32_bf16 v[172:175], a[148:151], v[148:151], v[172:175]\n"
    "s_waitcnt lgkmcnt(7)\n"
    "ds_read_u16_d16_hi v202, v202\n"
    "v_or_b32_e32 v189, v189, v205\n"
    "v_mfma_f32_16x16x32_bf16 v[172:175], a[60:63], v[152:155], v[172:175]\n"
    "v_lshlrev_b32_sdwa v205, 1, v179 dst_sel:DWORD dst_unused:UNUSED_PAD src0_sel:DWORD src1_sel:WORD_1\n"
    "v_cvt_pk_bf16_f32 v112, v112, v113\n"
    "v_mfma_f32_16x16x32_bf16 v[176:179], a[96:99], v[156:159], 0\n"
    "ds_read_b128 v[236:239], v244 offset:0\n"
    "v_cvt_pk_bf16_f32 v113, v114, v115\n"
    "v_mfma_f32_16x16x32_bf16 v[176:179], a[144:147], v[160:163], v[176:179]\n"
    "ds_read_u16 v195, v195\n"
    "v_cvt_pk_bf16_f32 v114, v116, v117\n"
    "v_mfma_f32_16x16x32_bf16 v[176:179], a[56:59], v[164:167], v[176:179]\n"
    "s_waitcnt lgkmcnt(8)\n"
    "v_or_b32_e32 v190, v190, v206\n"
    "v_lshlrev_b32_sdwa v206, 1, v181 dst_sel:DWORD dst_unused:UNUSED_PAD src0_sel:DWORD src1_sel:WORD_1\n"
    "v_mfma_f32_16x16x32_bf16 v[100:103], a[4:7], v[184:187], 0\n"
    "ds_read_u16_d16_hi v203, v203\n"
    "v_cvt_pk_bf16_f32 v115, v118, v119\n"
    "v_cvt_pk_bf16_f32 v128, v128, v129\n"
    "v_cvt_pk_bf16_f32 v129, v130, v131\n"
    "v_cvt_pk_bf16_f32 v130, v132, v133\n"
    "ds_write_b128 v243, v[96:99] offset:24640\n"
    "buffer_load_dwordx4 v[4:7], v246, s[40:43], s61 offen offset:0\n"
    "buffer_load_dwordx4 v[36:39], v246, s[40:43], s61 offen offset:64\n"
    "v_mfma_f32_16x16x32_bf16 v[96:99], a[0:3], v[184:187], 0\n"
    "s_waitcnt lgkmcnt(7)\n"
    "v_or_b32_e32 v191, v191, v207\n"
    "v_lshlrev_b32_sdwa v207, 1, v183 dst_sel:DWORD dst_unused:UNUSED_PAD src0_sel:DWORD src1_sel:WORD_1\n"
    "v_mfma_f32_16x16x32_bf16 v[180:183], a[100:103], v[156:159], 0\n"
    "ds_read_b128 a[252:255], v249 offset:0\n"
    "ds_write_b128 v243, v[112:115] offset:26688\n"
    "v_mfma_f32_16x16x32_bf16 v[180:183], a[148:151], v[160:163], v[180:183]\n"
    "s_waitcnt lgkmcnt(8)\n"
    "v_or_b32_e32 v193, v193, v201\n"
    "v_cvt_pk_bf16_f32 v131, v134, v135\n"
    "v_mfma_f32_16x16x32_bf16 v[180:183], a[60:63], v[164:167], v[180:183]\n"
    "v_cvt_pk_bf16_f32 v104, v104, v105\n"
    "v_cvt_pk_bf16_f32 v105, v106, v107\n"
    "v_mfma_f32_16x16x32_bf16 v[112:115], a[8:11], v[184:187], 0\n"
    "s_waitcnt lgkmcnt(5)\n"
    "ds_read_b128 a[244:247], v244 offset:2048\n"
    "ds_write_b128 v243, v[128:131] offset:28736\n"
    "v_mfma_f32_16x16x32_bf16 v[116:119], a[12:15], v[184:187], 0\n"
    "s_nop 0\n"
    "v_cvt_pk_bf16_f32 v106, v108, v109\n"
    "v_cvt_pk_bf16_f32 v107, v110, v111\n"
    "v_mfma_f32_16x16x32_bf16 v[128:131], v[236:239], v[184:187], 0\n"
    "v_or_b32_e64 v194, v194, v202\n"
    "v_cvt_pk_bf16_f32 v120, v120, v121\n"
    "v_mfma_f32_16x16x32_bf16 v[108:111], a[4:7], v[188:191], 0\n"
    "s_nop 0\n"
    "ds_read_u16 v197, v197\n"
    "ds_write_b128 v243, v[104:107] offset:25664\n"
    "v_mfma_f32_16x16x32_bf16 v[104:107], a[0:3], v[188:191], 0\n"
    "s_waitcnt lgkmcnt(6)\n"
    "v_cvt_pk_bf16_f32 v121, v122, v123\n"
    "v_cvt_pk_bf16_f32 v122, v124, v125\n"
    "v_cvt_pk_bf16_f32 v123, v126, v127\n"
    "v_pk_fma_f16 v232, v192, v192, s48\n"
    "v_mfma_f32_16x16x32_bf16 v[124:127], a[12:15], v[188:191], 0\n"
    "s_waitcnt lgkmcnt(4)\n"
    "buffer_store_dwordx4 a[252:255], v255, s[68:71], s59 offen offset:0 sc1\n"
    "ds_write_b128 v243, v[120:123] offset:27712\n"
    "s_waitcnt lgkmcnt(4)\n"
    "v_mfma_f32_16x16x32_bf16 v[132:135], a[244:247], v[184:187], 0\n"
    "v_cvt_pk_bf16_f32 v136, v136, v137\n"
    "v_cvt_pk_bf16_f32 v137, v138, v139\n"
    "v_mfma_f32_16x16x32_bf16 v[120:123], a[8:11], v[188:191], 0\n"
    "v_cvt_pk_bf16_f32 v138, v140, v141\n"
    "v_or_b32_e64 v195, v195, v203\n"
    "v_cvt_pk_bf16_f32 v139, v142, v143\n"
    "ds_write_b128 v243, v[136:139] offset:29760\n"
    "v_pk_mul_f16 v232, v192, v232\n"
    "v_mfma_f32_16x16x32_bf16 v[136:139], v[236:239], v[188:191], 0\n"
    "v_exp_f16_e64 v234, v232\n"
    "v_mfma_f32_16x16x32_bf16 v[140:143], a[244:247], v[188:191], 0\n"
    "ds_read_u16_d16_hi v205, v205\n"
    "ds_read_u16 v198, v198\n"
    "v_exp_f16_sdwa v234, v232 dst_sel:WORD_1 dst_unused:UNUSED_PRESERVE src0_sel:WORD_1\n"
    "v_cvt_pk_f16_f32 v184, v168, v169\n"
    "v_pk_fma_f16 v234, v234, s49, s49\n"
    "v_lshlrev_b32_sdwa v185, 1, v170 dst_sel:DWORD dst_unused:UNUSED_PAD src0_sel:DWORD src1_sel:WORD_1\n"
    "v_lshlrev_b32_sdwa v201, 1, v171 dst_sel:DWORD dst_unused:UNUSED_PAD src0_sel:DWORD src1_sel:WORD_1\n"
    "v_mfma_f32_16x16x32_bf16 v[168:171], a[104:107], v[144:147], 0\n"
    "ds_read_u16_d16_hi v206, v206\n"
    "ds_read_u16 v199, v199\n"
    "v_mfma_f32_16x16x32_bf16 v[168:171], a[152:155], v[148:151], v[168:171]\n"
    "ds_read_u16_d16_hi v207, v207\n"
    "buffer_load_dwordx4 v[24:27], v247, s[40:43], s61 offen offset:0\n"
    "buffer_load_dwordx4 v[56:59], v247, s[40:43], s61 offen offset:64\n"
    "v_lshlrev_b32_sdwa v186, 1, v172 dst_sel:DWORD dst_unused:UNUSED_PAD src0_sel:DWORD src1_sel:WORD_1\n"
    "v_lshlrev_b32_sdwa v202, 1, v173 dst_sel:DWORD dst_unused:UNUSED_PAD src0_sel:DWORD src1_sel:WORD_1\n"
    "v_lshlrev_b32_sdwa v187, 1, v174 dst_sel:DWORD dst_unused:UNUSED_PAD src0_sel:DWORD src1_sel:WORD_1\n"
    "v_lshlrev_b32_sdwa v203, 1, v175 dst_sel:DWORD dst_unused:UNUSED_PAD src0_sel:DWORD src1_sel:WORD_1\n"
    "v_rcp_f16_e64 v232, v234\n"
    "v_mfma_f32_16x16x32_bf16 v[168:171], a[64:67], v[152:155], v[168:171]\n"
    "ds_read_b128 a[252:255], v249 offset:2048\n"
    "ds_read_u16 v185, v185\n"
    "v_rcp_f16_sdwa v232, v234 dst_sel:WORD_1 dst_unused:UNUSED_PRESERVE src0_sel:WORD_1\n"
    "v_mfma_f32_16x16x32_bf16 v[172:175], a[108:111], v[144:147], 0\n"
    "ds_read_u16_d16_hi v201, v201\n"
    "v_cvt_pk_f16_f32 v188, v176, v177\n"
    "v_mfma_f32_16x16x32_bf16 v[172:175], a[156:159], v[148:151], v[172:175]\n"
    "v_lshlrev_b32_sdwa v189, 1, v178 dst_sel:DWORD dst_unused:UNUSED_PAD src0_sel:DWORD src1_sel:WORD_1\n"
    "v_lshlrev_b32_sdwa v190, 1, v180 dst_sel:DWORD dst_unused:UNUSED_PAD src0_sel:DWORD src1_sel:WORD_1\n"
    "v_lshlrev_b32_sdwa v191, 1, v182 dst_sel:DWORD dst_unused:UNUSED_PAD src0_sel:DWORD src1_sel:WORD_1\n"
    "v_fma_mix_f32 v234, v192, v232, 0 op_sel_hi:[1,1,0]\n"
    "v_mfma_f32_16x16x32_bf16 v[172:175], a[68:71], v[152:155], v[172:175]\n"
    "s_waitcnt lgkmcnt(7)\n"
    "ds_read_u16 v186, v186\n"
    "v_or_b32_e32 v197, v197, v205\n"
    "v_lshlrev_b32_sdwa v205, 1, v179 dst_sel:DWORD dst_unused:UNUSED_PAD src0_sel:DWORD src1_sel:WORD_1\n"
    "v_fma_mix_f32 v232, v192, v232, 0 op_sel:[1,1,0] op_sel_hi:[1,1,0]\n"
    "v_mfma_f32_16x16x32_bf16 v[176:179], a[104:107], v[156:159], 0\n"
    "ds_read_u16_d16_hi v202, v202\n"
    "v_cvt_pk_bf16_f32 v192, v234, v232\n"
    "v_mfma_f32_16x16x32_bf16 v[176:179], a[152:155], v[160:163], v[176:179]\n"
    "ds_read_b128 v[236:239], v244 offset:4096\n"
    "v_pk_fma_f16 v232, v196, v196, s48\n"
    "v_mfma_f32_16x16x32_bf16 v[176:179], a[64:67], v[164:167], v[176:179]\n"
    "s_waitcnt lgkmcnt(8)\n"
    "ds_read_u16 v187, v187\n"
    "v_or_b32_e32 v198, v198, v206\n"
    "v_lshlrev_b32_sdwa v206, 1, v181 dst_sel:DWORD dst_unused:UNUSED_PAD src0_sel:DWORD src1_sel:WORD_1\n"
    "v_pk_mul_f16 v232, v196, v232\n"
    "v_lshlrev_b32_sdwa v200, 1, v169 dst_sel:DWORD dst_unused:UNUSED_PAD src0_sel:DWORD src1_sel:WORD_1\n"
    "v_exp_f16_e64 v234, v232\n"
    "v_mov_b32_e32 v254, 0\n"
    "v_exp_f16_sdwa v234, v232 dst_sel:WORD_1 dst_unused:UNUSED_PRESERVE src0_sel:WORD_1\n"
    "v_mfma_f32_16x16x32_bf16 v[96:99], a[16:19], v[192:195], v[96:99]\n"
    "s_waitcnt lgkmcnt(6)\n"
    "ds_read_u16_d16_hi v203, v203\n"
    "buffer_store_dwordx4 a[252:255], v255, s[68:71], s59 offen offset:64 sc1\n"
    "v_or_b32_e64 v199, v199, v207\n"
    "v_lshlrev_b32_sdwa v207, 1, v183 dst_sel:DWORD dst_unused:UNUSED_PAD src0_sel:DWORD src1_sel:WORD_1\n"
    "v_mfma_f32_16x16x32_bf16 v[180:183], a[108:111], v[156:159], 0\n"
    "ds_read_b128 a[244:247], v244 offset:6144\n"
    "ds_read_b128 a[252:255], v249 offset:4096\n"
    "v_mfma_f32_16x16x32_bf16 v[180:183], a[156:159], v[160:163], v[180:183]\n"
    "ds_read_u16 v189, v189\n"
    "v_pk_fma_f16 v234, v234, s49, s49\n"
    "v_mfma_f32_16x16x32_bf16 v[180:183], a[68:71], v[164:167], v[180:183]\n"
    "ds_read_u16_d16_hi v205, v205\n"
    "v_rcp_f16_e32 v232, v234\n"
    "v_mfma_f32_16x16x32_bf16 v[100:103], a[20:23], v[192:195], v[100:103]\n"
    "s_waitcnt lgkmcnt(9)\n"
    "v_rcp_f16_sdwa v232, v234 dst_sel:WORD_1 dst_unused:UNUSED_PRESERVE src0_sel:WORD_1\n"
    "v_mfma_f32_16x16x32_bf16 v[112:115], a[24:27], v[192:195], v[112:115]\n"
    "ds_read_u16 v190, v190\n"
    "v_or_b32_e64 v185, v185, v201\n"
    "v_mfma_f32_16x16x32_bf16 v[116:119], a[28:31], v[192:195], v[116:119]\n"
    "ds_read_u16_d16_hi v206, v206\n"
    "v_fma_mix_f32 v234, v196, v232, 0 op_sel_hi:[1,1,0]\n"
    "s_waitcnt lgkmcnt(8)\n"
    "v_mfma_f32_16x16x32_bf16 v[128:131], v[236:239], v[192:195], v[128:131]\n"
    "s_nop 0\n"
    "v_fma_mix_f32 v232, v196, v232, 0 op_sel:[1,1,0] op_sel_hi:[1,1,0]\n"
    "v_or_b32_e64 v186, v186, v202\n"
    "v_cvt_pk_bf16_f32 v196, v234, v232\n"
    "v_pk_fma_f16 v232, v184, v184, s48\n"
    "v_lshlrev_b32_sdwa v201, 1, v171 dst_sel:DWORD dst_unused:UNUSED_PAD src0_sel:DWORD src1_sel:WORD_1\n"
    "v_mfma_f32_16x16x32_bf16 v[104:107], a[16:19], v[196:199], v[104:107]\n"
    "buffer_load_dwordx4 v[8:11], v246, s[40:43], s62 offen offset:0\n"
    "buffer_load_dwordx4 v[40:43], v246, s[40:43], s62 offen offset:64\n"
    "ds_read_u16 v191, v191\n"
    "v_mfma_f32_16x16x32_bf16 v[108:111], a[20:23], v[196:199], v[108:111]\n"
    "ds_read_u16_d16_hi v207, v207\n"
    "v_pk_mul_f16 v232, v184, v232\n"
    "s_waitcnt lgkmcnt(7)\n"
    "v_mfma_f32_16x16x32_bf16 v[132:135], a[244:247], v[192:195], v[132:135]\n"
    "s_nop 0\n"
    "ds_read_b128 a[240:243], v244 offset:34816\n"
    "v_or_b32_e32 v187, v187, v203\n"
    "v_mfma_f32_16x16x32_bf16 v[120:123], a[24:27], v[196:199], v[120:123]\n"
    "s_waitcnt lgkmcnt(7)\n"
    "ds_read_u16_d16_hi v200, v200\n"
    "v_exp_f16_e64 v234, v232\n"
    "v_mfma_f32_16x16x32_bf16 v[124:127], a[28:31], v[196:199], v[124:127]\n"
    "v_exp_f16_sdwa v234, v232 dst_sel:WORD_1 dst_unused:UNUSED_PRESERVE src0_sel:WORD_1\n"
    "v_mfma_f32_16x16x32_bf16 v[136:139], v[236:239], v[196:199], v[136:139]\n"
    "ds_read_u16_d16_hi v201, v201\n"
    "v_lshlrev_b32_sdwa v192, 1, v168 dst_sel:DWORD dst_unused:UNUSED_PAD src0_sel:DWORD src1_sel:WORD_1\n"
    "v_mfma_f32_16x16x32_bf16 v[140:143], a[244:247], v[196:199], v[140:143]\n"
    "ds_read_u16 v192, v192\n"
    "v_pk_fma_f16 v234, v234, s49, s49\n"
    "v_lshlrev_b32_sdwa v193, 1, v170 dst_sel:DWORD dst_unused:UNUSED_PAD src0_sel:DWORD src1_sel:WORD_1\n"
    "v_mfma_f32_16x16x32_bf16 v[168:171], a[112:115], v[144:147], 0\n"
    "s_waitcnt lgkmcnt(8)\n"
    "v_or_b32_e32 v189, v189, v205\n"
    "v_lshlrev_b32_sdwa v194, 1, v172 dst_sel:DWORD dst_unused:UNUSED_PAD src0_sel:DWORD src1_sel:WORD_1\n"
    "v_mfma_f32_16x16x32_bf16 v[168:171], a[160:163], v[148:151], v[168:171]\n"
    "ds_read_u16 v193, v193\n"
    "v_lshlrev_b32_sdwa v202, 1, v173 dst_sel:DWORD dst_unused:UNUSED_PAD src0_sel:DWORD src1_sel:WORD_1\n"
    "v_lshlrev_b32_sdwa v195, 1, v174 dst_sel:DWORD dst_unused:UNUSED_PAD src0_sel:DWORD src1_sel:WORD_1\n"
    "v_lshlrev_b32_sdwa v203, 1, v175 dst_sel:DWORD dst_unused:UNUSED_PAD src0_sel:DWORD src1_sel:WORD_1\n"
    "v_rcp_f16_e32 v232, v234\n"
    "v_mfma_f32_16x16x32_bf16 v[168:171], a[72:75], v[152:155], v[168:171]\n"
    "s_waitcnt lgkmcnt(7)\n"
    "ds_read_u16 v194, v194\n"
    "buffer_store_dwordx4 a[252:255], v255, s[68:71], s59 offen offset:128 sc1\n"
    "v_or_b32_e32 v190, v190, v206\n"
    "v_rcp_f16_sdwa v232, v234 dst_sel:WORD_1 dst_unused:UNUSED_PRESERVE src0_sel:WORD_1\n"
    "v_mfma_f32_16x16x32_bf16 v[172:175], a[116:119], v[144:147], 0\n"
    "ds_read_b128 a[252:255], v249 offset:1024\n"
    "ds_read_b128 v[236:239], v244 offset:8192\n"
    "v_mfma_f32_16x16x32_bf16 v[172:175], a[164:167], v[148:151], v[172:175]\n"
    "s_waitcnt lgkmcnt(7)\n"
    "ds_read_u16_d16_hi v202, v202\n"
    "v_lshlrev_b32_sdwa v196, 1, v176 dst_sel:DWORD dst_unused:UNUSED_PAD src0_sel:DWORD src1_sel:WORD_1\n"
    "v_lshlrev_b32_sdwa v204, 1, v177 dst_sel:DWORD dst_unused:UNUSED_PAD src0_sel:DWORD src1_sel:WORD_1\n"
    "v_lshlrev_b32_sdwa v197, 1, v178 dst_sel:DWORD dst_unused:UNUSED_PAD src0_sel:DWORD src1_sel:WORD_1\n"
    "v_lshlrev_b32_sdwa v205, 1, v179 dst_sel:DWORD dst_unused:UNUSED_PAD src0_sel:DWORD src1_sel:WORD_1\n"
    "v_lshlrev_b32_sdwa v198, 1, v180 dst_sel:DWORD dst_unused:UNUSED_PAD src0_sel:DWORD src1_sel:WORD_1\n"
    "v_lshlrev_b32_sdwa v206, 1, v181 dst_sel:DWORD dst_unused:UNUSED_PAD src0_sel:DWORD src1_sel:WORD_1\n"
    "v_lshlrev_b32_sdwa v199, 1, v182 dst_sel:DWORD dst_unused:UNUSED_PAD src0_sel:DWORD src1_sel:WORD_1\n"
    "v_fma_mix_f32 v234, v184, v232, 0 op_sel_hi:[1,1,0]\n"
    "v_mfma_f32_16x16x32_bf16 v[172:175], a[240:243], v[152:155], v[172:175]\n"
    "ds_read_u16 v195, v195\n"
    "ds_read_b128 a[244:247], v244 offset:10240\n"
    "v_or_b32_e64 v191, v191, v207\n"
    "v_lshlrev_b32_sdwa v207, 1, v183 dst_sel:DWORD dst_unused:UNUSED_PAD src0_sel:DWORD src1_sel:WORD_1\n"
    "v_fma_mix_f32 v232, v184, v232, 0 op_sel:[1,1,0] op_sel_hi:[1,1,0]\n"
    "v_mfma_f32_16x16x32_bf16 v[176:179], a[112:115], v[156:159], 0\n"
    "ds_read_u16_d16_hi v203, v203\n"
    "v_cvt_pk_bf16_f32 v184, v234, v232\n"
    "v_mfma_f32_16x16x32_bf16 v[176:179], a[160:163], v[160:163], v[176:179]\n"
    "s_nop 0\n"
    "ds_read_u16 v196, v196\n"
    "v_pk_fma_f16 v232, v188, v188, s48\n"
    "v_mfma_f32_16x16x32_bf16 v[176:179], a[72:75], v[164:167], v[176:179]\n"
    "s_waitcnt lgkmcnt(9)\n"
    "buffer_load_dwordx4 v[12:15], v246, s[40:43], s63 offen offset:0\n"
    "buffer_load_dwordx4 v[44:47], v246, s[40:43], s63 offen offset:64\n"
    "v_mfma_f32_16x16x32_bf16 v[180:183], a[116:119], v[156:159], 0\n"
    "ds_read_u16_d16_hi v204, v204\n"
    "v_pk_mul_f16 v232, v188, v232\n"
    "v_mfma_f32_16x16x32_bf16 v[180:183], a[164:167], v[160:163], v[180:183]\n"
    "s_waitcnt lgkmcnt(9)\n"
    "v_exp_f16_e64 v234, v232\n"
    "v_mfma_f32_16x16x32_bf16 v[180:183], a[240:243], v[164:167], v[180:183]\n"
    "s_waitcnt lgkmcnt(7)\n"
    "ds_read_u16 v197, v197\n"
    "v_exp_f16_sdwa v234, v232 dst_sel:WORD_1 dst_unused:UNUSED_PRESERVE src0_sel:WORD_1\n"
    "v_mfma_f32_16x16x32_bf16 v[96:99], a[32:35], v[184:187], v[96:99]\n"
    "ds_read_u16_d16_hi v205, v205\n"
    "v_pk_fma_f16 v234, v234, s49, s49\n"
    "v_mfma_f32_16x16x32_bf16 v[100:103], a[36:39], v[184:187], v[100:103]\n"
    "v_rcp_f16_e64 v232, v234\n"
    "v_mfma_f32_16x16x32_bf16 v[112:115], a[40:43], v[184:187], v[112:115]\n"
    "ds_read_u16 v198, v198\n"
    "v_rcp_f16_sdwa v232, v234 dst_sel:WORD_1 dst_unused:UNUSED_PRESERVE src0_sel:WORD_1\n"
    "v_mfma_f32_16x16x32_bf16 v[116:119], a[44:47], v[184:187], v[116:119]\n"
    "s_waitcnt lgkmcnt(8)\n"
    "v_fma_mix_f32 v234, v188, v232, 0 op_sel_hi:[1,1,0]\n"
    "v_fma_mix_f32 v232, v188, v232, 0 op_sel:[1,1,0] op_sel_hi:[1,1,0]\n"
    "v_mfma_f32_16x16x32_bf16 v[128:131], v[236:239], v[184:187], v[128:131]\n"
    "s_waitcnt lgkmcnt(6)\n"
    "ds_read_u16_d16_hi v206, v206\n"
    "v_cvt_pk_bf16_f32 v188, v234, v232\n"
    "v_mfma_f32_16x16x32_bf16 v[132:135], a[244:247], v[184:187], v[132:135]\n"
    "ds_read_u16 v199, v199\n"
    "buffer_store_dwordx4 a[252:255], v255, s[68:71], s39 offen offset:0 sc1\n"
    "v_mfma_f32_16x16x32_bf16 v[104:107], a[32:35], v[188:191], v[104:107]\n"
    "s_waitcnt lgkmcnt(7)\n"
    "ds_read_b128 a[252:255], v249 offset:3072\n"
    "v_or_b32_e32 v192, v192, v200\n"
    "v_mfma_f32_16x16x32_bf16 v[108:111], a[36:39], v[188:191], v[108:111]\n"
    "ds_read_u16_d16_hi v207, v207\n"
    "v_or_b32_e64 v193, v193, v201\n"
    "v_mfma_f32_16x16x32_bf16 v[120:123], a[40:43], v[188:191], v[120:123]\n"
    "v_cvt_pk_f16_f32 v184, v168, v169\n"
    "v_lshlrev_b32_sdwa v185, 1, v170 dst_sel:DWORD dst_unused:UNUSED_PAD src0_sel:DWORD src1_sel:WORD_1\n"
    "v_mfma_f32_16x16x32_bf16 v[124:127], a[44:47], v[188:191], v[124:127]\n"
    "ds_read_b128 a[240:243], v244 offset:38912\n"
    "v_lshlrev_b32_sdwa v201, 1, v171 dst_sel:DWORD dst_unused:UNUSED_PAD src0_sel:DWORD src1_sel:WORD_1\n"
    "v_mfma_f32_16x16x32_bf16 v[136:139], v[236:239], v[188:191], v[136:139]\n"
    "s_waitcnt lgkmcnt(8)\n"
    "ds_read_u16 v185, v185\n"
    "v_lshlrev_b32_sdwa v186, 1, v172 dst_sel:DWORD dst_unused:UNUSED_PAD src0_sel:DWORD src1_sel:WORD_1\n"
    "v_mfma_f32_16x16x32_bf16 v[140:143], a[244:247], v[188:191], v[140:143]\n"
    "v_or_b32_e32 v194, v194, v202\n"
    "v_lshlrev_b32_sdwa v202, 1, v173 dst_sel:DWORD dst_unused:UNUSED_PAD src0_sel:DWORD src1_sel:WORD_1\n"
    "v_mfma_f32_16x16x32_bf16 v[168:171], a[120:123], v[144:147], 0\n"
    "s_nop 0\n"
    "ds_read_u16_d16_hi v201, v201\n"
    "v_lshlrev_b32_sdwa v187, 1, v174 dst_sel:DWORD dst_unused:UNUSED_PAD src0_sel:DWORD src1_sel:WORD_1\n"
    "v_mfma_f32_16x16x32_bf16 v[168:171], a[168:171], v[148:151], v[168:171]\n"
    "s_waitcnt lgkmcnt(8)\n"
    "ds_read_u16 v186, v186\n"
    "v_cvt_pk_f16_f32 v188, v176, v177\n"
    "v_mfma_f32_16x16x32_bf16 v[168:171], a[76:79], v[152:155], v[168:171]\n"
    "buffer_load_dwordx4 v[16:19], v246, s[40:43], s52 offen offset:0\n"
    "buffer_load_dwordx4 v[48:51], v246, s[40:43], s52 offen offset:64\n"
    "v_lshlrev_b32_sdwa v189, 1, v178 dst_sel:DWORD dst_unused:UNUSED_PAD src0_sel:DWORD src1_sel:WORD_1\n"
    "v_lshlrev_b32_sdwa v190, 1, v180 dst_sel:DWORD dst_unused:UNUSED_PAD src0_sel:DWORD src1_sel:WORD_1\n"
    "v_lshlrev_b32_sdwa v191, 1, v182 dst_sel:DWORD dst_unused:UNUSED_PAD src0_sel:DWORD src1_sel:WORD_1\n"
    "v_or_b32_e32 v195, v195, v203\n"
    "v_lshlrev_b32_sdwa v203, 1, v175 dst_sel:DWORD dst_unused:UNUSED_PAD src0_sel:DWORD src1_sel:WORD_1\n"
    "v_mfma_f32_16x16x32_bf16 v[172:175], a[124:127], v[144:147], 0\n"
    "s_waitcnt lgkmcnt(7)\n"
    "ds_read_u16_d16_hi v202, v202\n"
    "ds_read_b128 v[236:239], v244 offset:12288\n"
    "v_mfma_f32_16x16x32_bf16 v[172:175], a[172:175], v[148:151], v[172:175]\n"
    "v_or_b32_e64 v196, v196, v204\n"
    "v_or_b32_e64 v197, v197, v205\n"
    "v_lshlrev_b32_sdwa v205, 1, v179 dst_sel:DWORD dst_unused:UNUSED_PAD src0_sel:DWORD src1_sel:WORD_1\n"
    "v_mfma_f32_16x16x32_bf16 v[176:179], a[120:123], v[156:159], 0\n"
    "s_waitcnt lgkmcnt(7)\n"
    "ds_read_u16 v187, v187\n"
    "v_or_b32_e32 v198, v198, v206\n"
    "v_mfma_f32_16x16x32_bf16 v[176:179], a[168:171], v[160:163], v[176:179]\n"
    "ds_read_u16_d16_hi v203, v203\n"
    "v_lshlrev_b32_sdwa v206, 1, v181 dst_sel:DWORD dst_unused:UNUSED_PAD src0_sel:DWORD src1_sel:WORD_1\n"
    "v_mfma_f32_16x16x32_bf16 v[176:179], a[76:79], v[164:167], v[176:179]\n"
    "s_waitcnt lgkmcnt(7)\n"
    "v_or_b32_e32 v199, v199, v207\n"
    "v_lshlrev_b32_sdwa v207, 1, v183 dst_sel:DWORD dst_unused:UNUSED_PAD src0_sel:DWORD src1_sel:WORD_1\n"
    "v_mfma_f32_16x16x32_bf16 v[172:175], a[240:243], v[152:155], v[172:175]\n"
    "ds_read_b128 a[244:247], v244 offset:14336\n"
    "v_pk_fma_f16 v232, v184, v184, s48\n"
    "v_mfma_f32_16x16x32_bf16 v[180:183], a[124:127], v[156:159], 0\n"
    "ds_read_u16 v189, v189\n"
    "v_pk_mul_f16 v232, v184, v232\n"
    "v_mfma_f32_16x16x32_bf16 v[180:183], a[172:175], v[160:163], v[180:183]\n"
    "v_exp_f16_e32 v234, v232\n"
    "v_mfma_f32_16x16x32_bf16 v[180:183], a[240:243], v[164:167], v[180:183]\n"
    "ds_read_u16_d16_hi v205, v205\n"
    "buffer_store_dwordx4 a[252:255], v255, s[68:71], s39 offen offset:64 sc1\n"
    "v_mfma_f32_16x16x32_bf16 v[96:99], a[176:179], v[192:195], v[96:99]\n"
    "s_waitcnt lgkmcnt(8)\n"
    "ds_read_b128 a[252:255], v249 offset:5120\n"
    "ds_read_u16 v190, v190\n"
    "v_mfma_f32_16x16x32_bf16 v[100:103], a[180:183], v[192:195], v[100:103]\n"
    "v_or_b32_e32 v185, v185, v201\n"
    "v_exp_f16_sdwa v234, v232 dst_sel:WORD_1 dst_unused:UNUSED_PRESERVE src0_sel:WORD_1\n"
    "v_mfma_f32_16x16x32_bf16 v[112:115], a[184:187], v[192:195], v[112:115]\n"
    "s_waitcnt lgkmcnt(7)\n"
    "ds_read_b128 v[144:147], v243 offset:0\n"
    "v_or_b32_e64 v186, v186, v202\n"
    "v_mfma_f32_16x16x32_bf16 v[116:119], a[188:191], v[192:195], v[116:119]\n"
    "ds_read_u16_d16_hi v206, v206\n"
    "v_pk_fma_f16 v234, v234, s49, s49\n"
    "v_mfma_f32_16x16x32_bf16 v[128:131], v[236:239], v[192:195], v[128:131]\n"
    "v_lshlrev_b32_sdwa v201, 1, v171 dst_sel:DWORD dst_unused:UNUSED_PAD src0_sel:DWORD src1_sel:WORD_1\n"
    "v_lshlrev_b32_sdwa v202, 1, v173 dst_sel:DWORD dst_unused:UNUSED_PAD src0_sel:DWORD src1_sel:WORD_1\n"
    "v_mfma_f32_16x16x32_bf16 v[104:107], a[176:179], v[196:199], v[104:107]\n"
    "ds_read_b128 v[148:151], v243 offset:1024\n"
    "ds_read_u16 v191, v191\n"
    "v_mfma_f32_16x16x32_bf16 v[108:111], a[180:183], v[196:199], v[108:111]\n"
    "v_rcp_f16_e32 v232, v234\n"
    "s_waitcnt lgkmcnt(8)\n"
    "v_mfma_f32_16x16x32_bf16 v[132:135], a[244:247], v[192:195], v[132:135]\n"
    "ds_read_b128 v[152:155], v243 offset:2048\n"
    "v_or_b32_e64 v187, v187, v203\n"
    "v_mfma_f32_16x16x32_bf16 v[120:123], a[184:187], v[196:199], v[120:123]\n"
    "ds_read_u16_d16_hi v207, v207\n"
    "v_cvt_pk_f16_f32 v192, v168, v169\n"
    "v_mfma_f32_16x16x32_bf16 v[124:127], a[188:191], v[196:199], v[124:127]\n"
    "v_lshlrev_b32_sdwa v193, 1, v170 dst_sel:DWORD dst_unused:UNUSED_PAD src0_sel:DWORD src1_sel:WORD_1\n"
    "v_lshlrev_b32_sdwa v194, 1, v172 dst_sel:DWORD dst_unused:UNUSED_PAD src0_sel:DWORD src1_sel:WORD_1\n"
    "v_mfma_f32_16x16x32_bf16 v[136:139], v[236:239], v[196:199], v[136:139]\n"
    "ds_read_b128 v[156:159], v243 offset:3072\n"
    "v_lshlrev_b32_sdwa v195, 1, v174 dst_sel:DWORD dst_unused:UNUSED_PAD src0_sel:DWORD src1_sel:WORD_1\n"
    "v_mfma_f32_16x16x32_bf16 v[140:143], a[244:247], v[196:199], v[140:143]\n"
    "s_waitcnt lgkmcnt(8)\n"
    "ds_read_b128 v[160:163], v243 offset:4096\n"
    "buffer_store_dwordx4 a[252:255], v255, s[68:71], s39 offen offset:128 sc1\n"
    "v_or_b32_e32 v189, v189, v205\n"
    "v_lshlrev_b32_sdwa v203, 1, v175 dst_sel:DWORD dst_unused:UNUSED_PAD src0_sel:DWORD src1_sel:WORD_1\n"
    "v_rcp_f16_sdwa v232, v234 dst_sel:WORD_1 dst_unused:UNUSED_PRESERVE src0_sel:WORD_1\n"
    "s_waitcnt lgkmcnt(7)\n"
    "v_mfma_f32_16x16x32_bf16 v[168:171], a[80:83], v[144:147], 0\n"
    "s_nop 0\n"
    "ds_read_b128 v[164:167], v243 offset:5120\n"
    "ds_read_b128 a[240:243], v244 offset:32768\n"
    "v_mfma_f32_16x16x32_bf16 v[172:175], a[84:87], v[144:147], 0\n"
    "ds_read_u16 v193, v193\n"
    "ds_read_u16_d16_hi v201, v201\n"
    "v_cvt_pk_f16_f32 v196, v176, v177\n"
    "s_waitcnt lgkmcnt(9)\n"
    "v_mfma_f32_16x16x32_bf16 v[168:171], a[128:131], v[148:151], v[168:171]\n"
    "v_or_b32_e32 v190, v190, v206\n"
    "v_lshlrev_b32_sdwa v197, 1, v178 dst_sel:DWORD dst_unused:UNUSED_PAD src0_sel:DWORD src1_sel:WORD_1\n"
    "v_lshlrev_b32_sdwa v205, 1, v179 dst_sel:DWORD dst_unused:UNUSED_PAD src0_sel:DWORD src1_sel:WORD_1\n"
    "v_lshlrev_b32_sdwa v198, 1, v180 dst_sel:DWORD dst_unused:UNUSED_PAD src0_sel:DWORD src1_sel:WORD_1\n"
    "v_lshlrev_b32_sdwa v206, 1, v181 dst_sel:DWORD dst_unused:UNUSED_PAD src0_sel:DWORD src1_sel:WORD_1\n"
    "v_lshlrev_b32_sdwa v199, 1, v182 dst_sel:DWORD dst_unused:UNUSED_PAD src0_sel:DWORD src1_sel:WORD_1\n"
    "v_fma_mix_f32 v234, v184, v232, 0 op_sel_hi:[1,1,0]\n"
    "v_mfma_f32_16x16x32_bf16 v[172:175], a[132:135], v[148:151], v[172:175]\n"
    "s_waitcnt lgkmcnt(7)\n"
    "ds_read_u16 v194, v194\n"
    "ds_read_u16_d16_hi v202, v202\n"
    "v_mfma_f32_16x16x32_bf16 v[168:171], a[48:51], v[152:155], v[168:171]\n"
    "s_nop 0\n"
    "v_fma_mix_f32 v232, v184, v232, 0 op_sel:[1,1,0] op_sel_hi:[1,1,0]\n"
    "v_cvt_pk_bf16_f32 v184, v234, v232\n"
    "v_pk_fma_f16 v232, v188, v188, s48\n"
    "v_pk_mul_f16 v232, v188, v232\n"
    "v_exp_f16_e32 v234, v232\n"
    "s_waitcnt lgkmcnt(7)\n"
    "v_mfma_f32_16x16x32_bf16 v[176:179], a[80:83], v[156:159], 0\n"
    "ds_read_b128 v[236:239], v244 offset:16384\n"
    "ds_read_u16 v195, v195\n"
    "s_waitcnt lgkmcnt(8)\n"
    "v_mfma_f32_16x16x32_bf16 v[176:179], a[128:131], v[160:163], v[176:179]\n"
    "s_waitcnt lgkmcnt(7)\n"
    "ds_read_u16_d16_hi v203, v203\n"
    "v_or_b32_e32 v191, v191, v207\n"
    "v_mfma_f32_16x16x32_bf16 v[176:179], a[48:51], v[164:167], v[176:179]\n"
    "s_waitcnt lgkmcnt(7)\n"
    "v_lshlrev_b32_sdwa v207, 1, v183 dst_sel:DWORD dst_unused:UNUSED_PAD src0_sel:DWORD src1_sel:WORD_1\n"
    "v_exp_f16_sdwa v234, v232 dst_sel:WORD_1 dst_unused:UNUSED_PRESERVE src0_sel:WORD_1\n"
    "v_mfma_f32_16x16x32_bf16 v[172:175], a[240:243], v[152:155], v[172:175]\n"
    "s_barrier\n"
    "v_mfma_f32_16x16x32_bf16 v[180:183], a[84:87], v[156:159], 0\n"
    "ds_read_b128 a[244:247], v244 offset:18432\n"
    "ds_write_b128 v253, v[64:67] offset:2048\n"
    "v_mfma_f32_16x16x32_bf16 v[180:183], a[132:135], v[160:163], v[180:183]\n"
    "s_waitcnt lgkmcnt(7)\n"
    "v_pk_fma_f16 v234, v234, s49, s49\n"
    "v_or_b32_e64 v193, v193, v201\n"
    "v_mfma_f32_16x16x32_bf16 v[180:183], a[240:243], v[164:167], v[180:183]\n"
    "v_rcp_f16_e64 v232, v234\n"
    "v_mfma_f32_16x16x32_bf16 v[96:99], a[192:195], v[184:187], v[96:99]\n"
    "ds_read_u16 v197, v197\n"
    "ds_write_b128 v253, v[68:71] offset:5120\n"
    "v_mfma_f32_16x16x32_bf16 v[100:103], a[196:199], v[184:187], v[100:103]\n"
    "s_waitcnt lgkmcnt(6)\n"
    "v_rcp_f16_sdwa v232, v234 dst_sel:WORD_1 dst_unused:UNUSED_PRESERVE src0_sel:WORD_1\n"
    "v_mfma_f32_16x16x32_bf16 v[112:115], a[200:203], v[184:187], v[112:115]\n"
    "s_nop 0\n"
    "v_fma_mix_f32 v234, v188, v232, 0 op_sel_hi:[1,1,0]\n"
    "v_fma_mix_f32 v232, v188, v232, 0 op_sel:[1,1,0] op_sel_hi:[1,1,0]\n"
    "v_mfma_f32_16x16x32_bf16 v[116:119], a[204:207], v[184:187], v[116:119]\n"
    "ds_read_u16_d16_hi v205, v205\n"
    "ds_write_b128 v253, v[88:91] offset:0\n"
    "v_mfma_f32_16x16x32_bf16 v[128:131], v[236:239], v[184:187], v[128:131]\n"
    "s_waitcnt lgkmcnt(5)\n"
    "v_or_b32_e32 v194, v194, v202\n"
    "v_cvt_pk_bf16_f32 v188, v234, v232\n"
    "v_mfma_f32_16x16x32_bf16 v[132:135], a[244:247], v[184:187], v[132:135]\n"
    "v_pk_fma_f16 v232, v192, v192, s48\n"
    "v_or_b32_e64 v195, v195, v203\n"
    "v_mfma_f32_16x16x32_bf16 v[104:107], a[192:195], v[188:191], v[104:107]\n"
    "buffer_load_dwordx4 a[232:235], v245, s[40:43], s57 offen offset:64\n"
    "buffer_load_dwordx4 a[224:227], v245, s[40:43], s57 offen offset:0\n"
    "ds_read_u16 v198, v198\n"
    "v_mfma_f32_16x16x32_bf16 v[108:111], a[196:199], v[188:191], v[108:111]\n"
    "ds_read_u16_d16_hi v206, v206\n"
    "ds_write_b128 v253, v[92:95] offset:3072\n"
    "v_mfma_f32_16x16x32_bf16 v[120:123], a[200:203], v[188:191], v[120:123]\n"
    "v_pk_mul_f16 v232, v192, v232\n"
    "v_lshlrev_b32_sdwa v184, 1, v168 dst_sel:DWORD dst_unused:UNUSED_PAD src0_sel:DWORD src1_sel:WORD_1\n"
    "v_mfma_f32_16x16x32_bf16 v[124:127], a[204:207], v[188:191], v[124:127]\n"
    "v_exp_f16_e64 v234, v232\n"
    "v_mfma_f32_16x16x32_bf16 v[136:139], v[236:239], v[188:191], v[136:139]\n"
    "ds_read_u16 v199, v199\n"
    "ds_write_b128 v253, v[72:75] offset:1024\n"
    "v_mfma_f32_16x16x32_bf16 v[140:143], a[244:247], v[188:191], v[140:143]\n"
    "s_waitcnt lgkmcnt(5)\n"
    "v_or_b32_e32 v197, v197, v205\n"
    "v_exp_f16_sdwa v234, v232 dst_sel:WORD_1 dst_unused:UNUSED_PRESERVE src0_sel:WORD_1\n"
    "v_lshlrev_b32_sdwa v200, 1, v169 dst_sel:DWORD dst_unused:UNUSED_PAD src0_sel:DWORD src1_sel:WORD_1\n"
    "v_pk_fma_f16 v234, v234, s49, s49\n"
    "v_lshlrev_b32_sdwa v185, 1, v170 dst_sel:DWORD dst_unused:UNUSED_PAD src0_sel:DWORD src1_sel:WORD_1\n"
    "v_lshlrev_b32_sdwa v201, 1, v171 dst_sel:DWORD dst_unused:UNUSED_PAD src0_sel:DWORD src1_sel:WORD_1\n"
    "v_mfma_f32_16x16x32_bf16 v[168:171], a[88:91], v[144:147], 0\n"
    "ds_read_u16_d16_hi v207, v207\n"
    "ds_write_b128 v253, v[76:79] offset:4096\n"
    "v_mfma_f32_16x16x32_bf16 v[168:171], a[136:139], v[148:151], v[168:171]\n"
    "v_lshlrev_b32_sdwa v186, 1, v172 dst_sel:DWORD dst_unused:UNUSED_PAD src0_sel:DWORD src1_sel:WORD_1\n"
    "v_lshlrev_b32_sdwa v202, 1, v173 dst_sel:DWORD dst_unused:UNUSED_PAD src0_sel:DWORD src1_sel:WORD_1\n"
    "v_lshlrev_b32_sdwa v187, 1, v174 dst_sel:DWORD dst_unused:UNUSED_PAD src0_sel:DWORD src1_sel:WORD_1\n"
    "v_lshlrev_b32_sdwa v203, 1, v175 dst_sel:DWORD dst_unused:UNUSED_PAD src0_sel:DWORD src1_sel:WORD_1\n"
    "v_rcp_f16_e32 v232, v234\n"
    "v_mfma_f32_16x16x32_bf16 v[168:171], a[52:55], v[152:155], v[168:171]\n"
    "s_waitcnt lgkmcnt(4)\n"
    "ds_read_b128 a[240:243], v244 offset:36864\n"
    "ds_read_u16 v184, v184\n"
    "v_or_b32_e64 v198, v198, v206\n"
    "v_rcp_f16_sdwa v232, v234 dst_sel:WORD_1 dst_unused:UNUSED_PRESERVE src0_sel:WORD_1\n"
    "v_mfma_f32_16x16x32_bf16 v[172:175], a[92:95], v[144:147], 0\n"
    "ds_read_u16_d16_hi v200, v200\n"
    "ds_read_u16 v185, v185\n"
    "v_lshlrev_b32_sdwa v188, 1, v176 dst_sel:DWORD dst_unused:UNUSED_PAD src0_sel:DWORD src1_sel:WORD_1\n"
    "v_lshlrev_b32_sdwa v204, 1, v177 dst_sel:DWORD dst_unused:UNUSED_PAD src0_sel:DWORD src1_sel:WORD_1\n"
    "v_lshlrev_b32_sdwa v189, 1, v178 dst_sel:DWORD dst_unused:UNUSED_PAD src0_sel:DWORD src1_sel:WORD_1\n"
    "v_lshlrev_b32_sdwa v205, 1, v179 dst_sel:DWORD dst_unused:UNUSED_PAD src0_sel:DWORD src1_sel:WORD_1\n"
    "v_lshlrev_b32_sdwa v190, 1, v180 dst_sel:DWORD dst_unused:UNUSED_PAD src0_sel:DWORD src1_sel:WORD_1\n"
    "v_lshlrev_b32_sdwa v206, 1, v181 dst_sel:DWORD dst_unused:UNUSED_PAD src0_sel:DWORD src1_sel:WORD_1\n"
    "v_lshlrev_b32_sdwa v191, 1, v182 dst_sel:DWORD dst_unused:UNUSED_PAD src0_sel:DWORD src1_sel:WORD_1\n"
    "v_fma_mix_f32 v234, v192, v232, 0 op_sel_hi:[1,1,0]\n"
    "v_mfma_f32_16x16x32_bf16 v[172:175], a[140:143], v[148:151], v[172:175]\n"
    "ds_read_u16_d16_hi v201, v201\n"
    "ds_read_u16 v186, v186\n"
    "buffer_load_dwordx4 a[236:239], v245, s[40:43], s88 offen offset:64\n"
    "buffer_load_dwordx4 a[228:231], v245, s[40:43], s88 offen offset:0\n"
    "v_fma_mix_f32 v232, v192, v232, 0 op_sel:[1,1,0] op_sel_hi:[1,1,0]\n"
    "v_mfma_f32_16x16x32_bf16 v[176:179], a[88:91], v[156:159], 0\n"
    "s_waitcnt lgkmcnt(6)\n"
    "ds_read_b128 v[236:239], v244 offset:20480\n"
    "ds_read_u16_d16_hi v202, v202\n"
    "v_or_b32_e32 v199, v199, v207\n"
    "v_lshlrev_b32_sdwa v207, 1, v183 dst_sel:DWORD dst_unused:UNUSED_PAD src0_sel:DWORD src1_sel:WORD_1\n"
    "v_cvt_pk_bf16_f32 v192, v234, v232\n"
    "v_mfma_f32_16x16x32_bf16 v[176:179], a[136:139], v[160:163], v[176:179]\n"
    "s_waitcnt lgkmcnt(7)\n"
    "v_pk_fma_f16 v232, v196, v196, s48\n"
    "v_pk_mul_f16 v232, v196, v232\n"
    "v_mfma_f32_16x16x32_bf16 v[172:175], a[240:243], v[152:155], v[172:175]\n"
    "s_nop 0\n"
    "ds_read_u16 v187, v187\n"
    "v_exp_f16_e64 v234, v232\n"
    "v_mfma_f32_16x16x32_bf16 v[176:179], a[52:55], v[164:167], v[176:179]\n"
    "ds_read_b128 a[244:247], v244 offset:22528\n"
    "v_exp_f16_sdwa v234, v232 dst_sel:WORD_1 dst_unused:UNUSED_PRESERVE src0_sel:WORD_1\n"
    "v_mfma_f32_16x16x32_bf16 v[180:183], a[92:95], v[156:159], 0\n"
    "ds_read_u16_d16_hi v203, v203\n"
    "v_pk_fma_f16 v234, v234, s49, s49\n"
    "v_mfma_f32_16x16x32_bf16 v[180:183], a[140:143], v[160:163], v[180:183]\n"
    "s_waitcnt lgkmcnt(8)\n"
    "v_rcp_f16_e32 v232, v234\n"
    "v_mfma_f32_16x16x32_bf16 v[180:183], a[240:243], v[164:167], v[180:183]\n"
    "ds_read_u16 v188, v188\n"
    "v_rcp_f16_sdwa v232, v234 dst_sel:WORD_1 dst_unused:UNUSED_PRESERVE src0_sel:WORD_1\n"
    "v_mfma_f32_16x16x32_bf16 v[96:99], a[208:211], v[192:195], v[96:99]\n"
    "ds_read_u16_d16_hi v204, v204\n"
    "v_fma_mix_f32 v234, v196, v232, 0 op_sel_hi:[1,1,0]\n"
    "v_mfma_f32_16x16x32_bf16 v[100:103], a[212:215], v[192:195], v[100:103]\n"
    "v_fma_mix_f32 v232, v196, v232, 0 op_sel:[1,1,0] op_sel_hi:[1,1,0]\n"
    "v_or_b32_e32 v184, v184, v200\n"
    "v_mfma_f32_16x16x32_bf16 v[112:115], a[216:219], v[192:195], v[112:115]\n"
    "s_waitcnt lgkmcnt(8)\n"
    "ds_read_u16 v189, v189\n"
    "v_cvt_pk_bf16_f32 v196, v234, v232\n"
    "v_mfma_f32_16x16x32_bf16 v[116:119], a[220:223], v[192:195], v[116:119]\n"
    "s_waitcnt lgkmcnt(7)\n"
    "v_or_b32_e32 v185, v185, v201\n"
    "v_lshlrev_b32_sdwa v201, 1, v171 dst_sel:DWORD dst_unused:UNUSED_PAD src0_sel:DWORD src1_sel:WORD_1\n"
    "v_mfma_f32_16x16x32_bf16 v[128:131], v[236:239], v[192:195], v[128:131]\n"
    "ds_read_u16_d16_hi v205, v205\n"
    "v_cvt_pk_bf16_f32 v96, v96, v97\n"
    "v_mfma_f32_16x16x32_bf16 v[104:107], a[208:211], v[196:199], v[104:107]\n"
    "ds_read_u16 v190, v190\n"
    "v_cvt_pk_bf16_f32 v97, v98, v99\n"
    "v_mfma_f32_16x16x32_bf16 v[108:111], a[212:215], v[196:199], v[108:111]\n"
    "s_waitcnt lgkmcnt(8)\n"
    "v_or_b32_e32 v186, v186, v202\n"
    "v_lshlrev_b32_sdwa v202, 1, v173 dst_sel:DWORD dst_unused:UNUSED_PAD src0_sel:DWORD src1_sel:WORD_1\n"
    "v_mfma_f32_16x16x32_bf16 v[120:123], a[216:219], v[196:199], v[120:123]\n"
    "ds_read_u16_d16_hi v206, v206\n"
    "v_cvt_pk_bf16_f32 v98, v100, v101\n"
    "s_waitcnt lgkmcnt(7)\n"
    "v_mfma_f32_16x16x32_bf16 v[132:135], a[244:247], v[192:195], v[132:135]\n"
    "s_nop 0\n"
    "v_cvt_pk_f16_f32 v192, v168, v169\n"
    "v_lshlrev_b32_sdwa v193, 1, v170 dst_sel:DWORD dst_unused:UNUSED_PAD src0_sel:DWORD src1_sel:WORD_1\n"
    "v_mfma_f32_16x16x32_bf16 v[124:127], a[220:223], v[196:199], v[124:127]\n"
    "s_nop 0\n"
    "ds_read_u16 v191, v191\n"
    "v_lshlrev_b32_sdwa v194, 1, v172 dst_sel:DWORD dst_unused:UNUSED_PAD src0_sel:DWORD src1_sel:WORD_1\n"
    "v_mfma_f32_16x16x32_bf16 v[136:139], v[236:239], v[196:199], v[136:139]\n"
    "s_waitcnt lgkmcnt(7)\n"
    "ds_read_u16_d16_hi v207, v207\n"
    "v_lshlrev_b32_sdwa v195, 1, v174 dst_sel:DWORD dst_unused:UNUSED_PAD src0_sel:DWORD src1_sel:WORD_1\n"
    "v_mfma_f32_16x16x32_bf16 v[140:143], a[244:247], v[196:199], v[140:143]\n"
    "v_cvt_pk_f16_f32 v196, v176, v177\n"
    "v_lshlrev_b32_sdwa v197, 1, v178 dst_sel:DWORD dst_unused:UNUSED_PAD src0_sel:DWORD src1_sel:WORD_1\n"
    "v_mfma_f32_16x16x32_bf16 v[168:171], a[96:99], v[144:147], 0\n"
    "ds_read_u16 v193, v193\n"
    "v_lshlrev_b32_sdwa v198, 1, v180 dst_sel:DWORD dst_unused:UNUSED_PAD src0_sel:DWORD src1_sel:WORD_1\n"
    "v_mfma_f32_16x16x32_bf16 v[168:171], a[144:147], v[148:151], v[168:171]\n"
    "v_lshlrev_b32_sdwa v199, 1, v182 dst_sel:DWORD dst_unused:UNUSED_PAD src0_sel:DWORD src1_sel:WORD_1\n"
    "v_or_b32_e32 v187, v187, v203\n"
    "v_mfma_f32_16x16x32_bf16 v[168:171], a[56:59], v[152:155], v[168:171]\n"
    "s_waitcnt lgkmcnt(7)\n"
    "ds_read_u16_d16_hi v201, v201\n"
    "v_lshlrev_b32_sdwa v203, 1, v175 dst_sel:DWORD dst_unused:UNUSED_PAD src0_sel:DWORD src1_sel:WORD_1\n"
    "v_mfma_f32_16x16x32_bf16 v[172:175], a[100:103], v[144:147], 0\n"
    "v_or_b32_e64 v188, v188, v204\n"
    "v_cvt_pk_bf16_f32 v99, v102, v103\n"
    "v_mfma_f32_16x16x32_bf16 v[172:175], a[148:151], v[148:151], v[172:175]\n"
    "ds_read_u16 v194, v194\n"
    "v_cvt_pk_bf16_f32 v112, v112, v113\n"
    "v_mfma_f32_16x16x32_bf16 v[172:175], a[60:63], v[152:155], v[172:175]\n"
    "s_waitcnt lgkmcnt(7)\n"
    "ds_read_u16_d16_hi v202, v202\n"
    "v_or_b32_e32 v189, v189, v205\n"
    "v_lshlrev_b32_sdwa v205, 1, v179 dst_sel:DWORD dst_unused:UNUSED_PAD src0_sel:DWORD src1_sel:WORD_1\n"
    "v_mfma_f32_16x16x32_bf16 v[176:179], a[96:99], v[156:159], 0\n"
    "v_cvt_pk_bf16_f32 v113, v114, v115\n"
    "v_cvt_pk_bf16_f32 v114, v116, v117\n"
    "v_mfma_f32_16x16x32_bf16 v[176:179], a[144:147], v[160:163], v[176:179]\n"
    "ds_read_b128 v[236:239], v244 offset:0\n"
    "v_cvt_pk_bf16_f32 v115, v118, v119\n"
    "v_mfma_f32_16x16x32_bf16 v[176:179], a[56:59], v[164:167], v[176:179]\n"
    "ds_read_u16 v195, v195\n"
    "s_waitcnt lgkmcnt(8)\n"
    "v_or_b32_e32 v190, v190, v206\n"
    "v_mfma_f32_16x16x32_bf16 v[100:103], a[4:7], v[184:187], 0\n"
    "ds_read_u16_d16_hi v203, v203\n"
    "v_lshlrev_b32_sdwa v206, 1, v181 dst_sel:DWORD dst_unused:UNUSED_PAD src0_sel:DWORD src1_sel:WORD_1\n"
    "v_cvt_pk_bf16_f32 v128, v128, v129\n"
    "v_cvt_pk_bf16_f32 v129, v130, v131\n"
    "v_cvt_pk_bf16_f32 v130, v132, v133\n"
    "v_cvt_pk_bf16_f32 v131, v134, v135\n"
    "v_cvt_pk_bf16_f32 v104, v104, v105\n"
    "s_waitcnt vmcnt(23)\n"
    "ds_write2_b32 v248, v0, v1 offset0:0 offset1:20\n"
    "s_waitcnt lgkmcnt(8)\n"
    "v_or_b32_e64 v191, v191, v207\n"
    "v_mfma_f32_16x16x32_bf16 v[116:119], a[12:15], v[184:187], 0\n"
    "buffer_load_dwordx4 v[64:67], v246, s[40:43], s60 offen offset:128\n"
    "ds_write2_b32 v248, v2, v3 offset0:40 offset1:60\n"
    "v_add_u32_e32 v248, 1360, v248\n"
    "v_lshlrev_b32_sdwa v207, 1, v183 dst_sel:DWORD dst_unused:UNUSED_PAD src0_sel:DWORD src1_sel:WORD_1\n"
    "v_mfma_f32_16x16x32_bf16 v[180:183], a[100:103], v[156:159], 0\n"
    "ds_read_b128 a[244:247], v244 offset:2048\n"
    "s_waitcnt vmcnt(22)\n"
    "ds_write2_b32 v235, v20, v21 offset0:0 offset1:20\n"
    "v_mfma_f32_16x16x32_bf16 v[180:183], a[148:151], v[160:163], v[180:183]\n"
    "s_waitcnt lgkmcnt(9)\n"
    "v_or_b32_e64 v193, v193, v201\n"
    "v_cvt_pk_bf16_f32 v105, v106, v107\n"
    "v_mfma_f32_16x16x32_bf16 v[180:183], a[60:63], v[164:167], v[180:183]\n"
    "ds_read_u16 v197, v197\n"
    "ds_write2_b32 v235, v22, v23 offset0:40 offset1:60\n"
    "v_add_u32_e32 v235, 1360, v235\n"
    "v_cvt_pk_bf16_f32 v106, v108, v109\n"
    "v_cvt_pk_bf16_f32 v107, v110, v111\n"
    "v_mfma_f32_16x16x32_bf16 v[108:111], a[4:7], v[188:191], 0\n"
    "s_waitcnt lgkmcnt(8)\n"
    "ds_read_u16_d16_hi v205, v205\n"
    "s_waitcnt vmcnt(20)\n"
    "ds_write2_b32 v248, v4, v5 offset0:0 offset1:20\n"
    "v_or_b32_e64 v194, v194, v202\n"
    "v_cvt_pk_bf16_f32 v120, v120, v121\n"
    "v_cvt_pk_bf16_f32 v121, v122, v123\n"
    "v_cvt_pk_bf16_f32 v122, v124, v125\n"
    "ds_write2_b32 v248, v6, v7 offset0:40 offset1:60\n"
    "v_add_u32_e32 v248, 1360, v248\n"
    "v_cvt_pk_bf16_f32 v123, v126, v127\n"
    "ds_write_b128 v242, v[96:99] offset:24640\n"
    "v_mfma_f32_16x16x32_bf16 v[96:99], a[0:3], v[184:187], 0\n"
    "s_waitcnt lgkmcnt(6)\n"
    "v_cvt_pk_bf16_f32 v136, v136, v137\n"
    "v_pk_fma_f16 v232, v192, v192, s48\n"
    "v_mfma_f32_16x16x32_bf16 v[132:135], a[244:247], v[184:187], 0\n"
    "ds_read_u16 v198, v198\n"
    "s_waitcnt vmcnt(17)\n"
    "ds_write2_b32 v235, v24, v25 offset0:0 offset1:20\n"
    "v_mfma_f32_16x16x32_bf16 v[124:127], a[12:15], v[188:191], 0\n"
    "v_cvt_pk_bf16_f32 v137, v138, v139\n"
    "v_cvt_pk_bf16_f32 v138, v140, v141\n"
    "ds_write_b128 v242, v[112:115] offset:26688\n"
    "v_mfma_f32_16x16x32_bf16 v[112:115], a[8:11], v[184:187], 0\n"
    "s_waitcnt lgkmcnt(4)\n"
    "v_or_b32_e32 v195, v195, v203\n"
    "v_cvt_pk_bf16_f32 v139, v142, v143\n"
    "v_pk_mul_f16 v232, v192, v232\n"
    "v_or_b32_e64 v197, v197, v205\n"
    "ds_write2_b32 v235, v26, v27 offset0:40 offset1:60\n"
    "v_add_u32_e32 v235, -1360, v235\n"
    "v_exp_f16_e64 v234, v232\n"
    "v_mfma_f32_16x16x32_bf16 v[140:143], a[244:247], v[188:191], 0\n"
    "ds_read_u16_d16_hi v206, v206\n"
    "ds_write_b128 v242, v[128:131] offset:28736\n"
    "v_mfma_f32_16x16x32_bf16 v[128:131], v[236:239], v[184:187], 0\n"
    "buffer_load_dwordx4 v[68:71], v246, s[40:43], s61 offen offset:128\n"
    "v_exp_f16_sdwa v234, v232 dst_sel:WORD_1 dst_unused:UNUSED_PRESERVE src0_sel:WORD_1\n"
    "v_cvt_pk_f16_f32 v184, v168, v169\n"
    "v_pk_fma_f16 v234, v234, s49, s49\n"
    "v_lshlrev_b32_sdwa v185, 1, v170 dst_sel:DWORD dst_unused:UNUSED_PAD src0_sel:DWORD src1_sel:WORD_1\n"
    "v_lshlrev_b32_sdwa v201, 1, v171 dst_sel:DWORD dst_unused:UNUSED_PAD src0_sel:DWORD src1_sel:WORD_1\n"
    "v_mfma_f32_16x16x32_bf16 v[168:171], a[104:107], v[144:147], 0\n"
    "ds_read_u16 v199, v199\n"
    "ds_write_b128 v242, v[104:107] offset:25664\n"
    "v_mfma_f32_16x16x32_bf16 v[104:107], a[0:3], v[188:191], 0\n"
    "v_lshlrev_b32_sdwa v186, 1, v172 dst_sel:DWORD dst_unused:UNUSED_PAD src0_sel:DWORD src1_sel:WORD_1\n"
    "v_lshlrev_b32_sdwa v202, 1, v173 dst_sel:DWORD dst_unused:UNUSED_PAD src0_sel:DWORD src1_sel:WORD_1\n"
    "v_mfma_f32_16x16x32_bf16 v[168:171], a[152:155], v[148:151], v[168:171]\n"
    "v_lshlrev_b32_sdwa v187, 1, v174 dst_sel:DWORD dst_unused:UNUSED_PAD src0_sel:DWORD src1_sel:WORD_1\n"
    "v_lshlrev_b32_sdwa v203, 1, v175 dst_sel:DWORD dst_unused:UNUSED_PAD src0_sel:DWORD src1_sel:WORD_1\n"
    "v_rcp_f16_e64 v232, v234\n"
    "v_mfma_f32_16x16x32_bf16 v[168:171], a[64:67], v[152:155], v[168:171]\n"
    "ds_read_u16_d16_hi v207, v207\n"
    "s_waitcnt vmcnt(15)\n"
    "ds_write2_b32 v248, v8, v9 offset0:0 offset1:20\n"
    "s_waitcnt lgkmcnt(4)\n"
    "v_or_b32_e64 v198, v198, v206\n"
    "v_rcp_f16_sdwa v232, v234 dst_sel:WORD_1 dst_unused:UNUSED_PRESERVE src0_sel:WORD_1\n"
    "v_mfma_f32_16x16x32_bf16 v[172:175], a[108:111], v[144:147], 0\n"
    "ds_read_b128 v[216:219], v251 offset:0\n"
    "ds_write_b128 v242, v[120:123] offset:27712\n"
    "v_mfma_f32_16x16x32_bf16 v[120:123], a[8:11], v[188:191], 0\n"
    "v_lshlrev_b32_sdwa v205, 1, v179 dst_sel:DWORD dst_unused:UNUSED_PAD src0_sel:DWORD src1_sel:WORD_1\n"
    "v_lshlrev_b32_sdwa v206, 1, v181 dst_sel:DWORD dst_unused:UNUSED_PAD src0_sel:DWORD src1_sel:WORD_1\n"
    "v_mfma_f32_16x16x32_bf16 v[172:175], a[156:159], v[148:151], v[172:175]\n"
    "v_fma_mix_f32 v234, v192, v232, 0 op_sel_hi:[1,1,0]\n"
    "v_fma_mix_f32 v232, v192, v232, 0 op_sel:[1,1,0] op_sel_hi:[1,1,0]\n"
    "v_mfma_f32_16x16x32_bf16 v[172:175], a[68:71], v[152:155], v[172:175]\n"
    "ds_read_b128 v[220:223], v251 offset:336\n"
    "ds_write2_b32 v248, v10, v11 offset0:40 offset1:60\n"
    "v_add_u32_e32 v248, 1360, v248\n"
    "v_cvt_pk_bf16_f32 v192, v234, v232\n"
    "v_pk_fma_f16 v232, v196, v196, s48\n"
    "v_lshlrev_b32_sdwa v200, 1, v169 dst_sel:DWORD dst_unused:UNUSED_PAD src0_sel:DWORD src1_sel:WORD_1\n"
    "v_pk_mul_f16 v232, v196, v232\n"
    "ds_write_b128 v242, v[136:139] offset:29760\n"
    "v_mfma_f32_16x16x32_bf16 v[136:139], v[236:239], v[188:191], 0\n"
    "s_waitcnt lgkmcnt(4)\n"
    "v_cvt_pk_f16_f32 v188, v176, v177\n"
    "v_lshlrev_b32_sdwa v189, 1, v178 dst_sel:DWORD dst_unused:UNUSED_PAD src0_sel:DWORD src1_sel:WORD_1\n"
    "v_mfma_f32_16x16x32_bf16 v[176:179], a[104:107], v[156:159], 0\n"
    "ds_read_u16 v185, v185\n"
    "s_waitcnt vmcnt(12)\n"
    "ds_write2_b32 v248, v12, v13 offset0:0 offset1:20\n"
    "v_mfma_f32_16x16x32_bf16 v[176:179], a[152:155], v[160:163], v[176:179]\n"
    "v_lshlrev_b32_sdwa v190, 1, v180 dst_sel:DWORD dst_unused:UNUSED_PAD src0_sel:DWORD src1_sel:WORD_1\n"
    "v_lshlrev_b32_sdwa v191, 1, v182 dst_sel:DWORD dst_unused:UNUSED_PAD src0_sel:DWORD src1_sel:WORD_1\n"
    "v_mfma_f32_16x16x32_bf16 v[176:179], a[64:67], v[164:167], v[176:179]\n"
    "ds_read_u16_d16_hi v201, v201\n"
    "ds_write2_b32 v248, v14, v15 offset0:40 offset1:60\n"
    "v_add_u32_e32 v248, 1360, v248\n"
    "buffer_load_dwordx4 v[72:75], v246, s[40:43], s62 offen offset:128\n"
    "v_or_b32_e32 v199, v199, v207\n"
    "s_waitcnt vmcnt(10)\n"
    "ds_write2_b32 v248, v16, v17 offset0:0 offset1:20\n"
    "v_lshlrev_b32_sdwa v207, 1, v183 dst_sel:DWORD dst_unused:UNUSED_PAD src0_sel:DWORD src1_sel:WORD_1\n"
    "v_mfma_f32_16x16x32_bf16 v[180:183], a[108:111], v[156:159], 0\n"
    "v_exp_f16_e64 v234, v232\n"
    "v_mfma_f32_16x16x32_bf16 v[180:183], a[156:159], v[160:163], v[180:183]\n"
    "ds_read_u16 v186, v186\n"
    "ds_write2_b32 v248, v18, v19 offset0:40 offset1:60\n"
    "v_mfma_f32_32x32x1_2b_f32 v[0:31], v254, v254, 0\n"
    "v_add_u32_e32 v248, -5440, v248\n"
    "v_exp_f16_sdwa v234, v232 dst_sel:WORD_1 dst_unused:UNUSED_PRESERVE src0_sel:WORD_1\n"
    "v_lshlrev_b32_sdwa v204, 1, v177 dst_sel:DWORD dst_unused:UNUSED_PAD src0_sel:DWORD src1_sel:WORD_1\n"
    "v_pk_fma_f16 v234, v234, s49, s49\n"
    "v_rcp_f16_e64 v232, v234\n"
    "ds_read_u16_d16_hi v202, v202\n"
    "ds_read_b128 v[236:239], v244 offset:4096\n"
    "v_rcp_f16_sdwa v232, v234 dst_sel:WORD_1 dst_unused:UNUSED_PRESERVE src0_sel:WORD_1\n"
    "ds_read_u16 v187, v187\n"
    "ds_read_u16_d16_hi v203, v203\n"
    "v_mfma_f32_16x16x32_bf16 v[180:183], a[68:71], v[164:167], v[180:183]\n"
    "s_waitcnt lgkmcnt(6)\n"
    "ds_read_b128 a[244:247], v244 offset:6144\n"
    "ds_read_u16 v189, v189\n"
    "v_mfma_f32_16x16x32_bf16 v[96:99], a[16:19], v[192:195], v[96:99]\n"
    "v_or_b32_e32 v185, v185, v201\n"
    "v_fma_mix_f32 v234, v196, v232, 0 op_sel_hi:[1,1,0]\n"
    "v_mfma_f32_16x16x32_bf16 v[100:103], a[20:23], v[192:195], v[100:103]\n"
    "ds_read_u16_d16_hi v205, v205\n"
    "v_fma_mix_f32 v232, v196, v232, 0 op_sel:[1,1,0] op_sel_hi:[1,1,0]\n"
    "v_mfma_f32_16x16x32_bf16 v[112:115], a[24:27], v[192:195], v[112:115]\n"
    "s_waitcnt lgkmcnt(7)\n"
    "s_barrier\n"
    "v_mfma_f32_16x16x32_bf16 v[116:119], a[28:31], v[192:195], v[116:119]\n"
    "ds_read_b128 v[208:211], v250 offset:0\n"
    "ds_read_b128 v[212:215], v250 offset:320\n"
    "v_cvt_pk_bf16_f32 v196, v234, v232\n"
    "v_pk_fma_f16 v232, v184, v184, s48\n"
    "v_lshlrev_b32_sdwa v201, 1, v171 dst_sel:DWORD dst_unused:UNUSED_PAD src0_sel:DWORD src1_sel:WORD_1\n"
    "v_mfma_f32_16x16x32_bf16 v[104:107], a[16:19], v[196:199], v[104:107]\n"
    "s_waitcnt lgkmcnt(7)\n"
    "ds_read_u16 v190, v190\n"
    "v_or_b32_e32 v186, v186, v202\n"
    "v_mfma_f32_16x16x32_bf16 v[128:131], v[236:239], v[192:195], v[128:131]\n"
    "ds_read_u16_d16_hi v206, v206\n"
    "v_pk_mul_f16 v232, v184, v232\n"
    "v_mfma_f32_16x16x32_bf16 v[108:111], a[20:23], v[196:199], v[108:111]\n"
    "v_exp_f16_e64 v234, v232\n"
    "v_mfma_f32_16x16x32_bf16 v[120:123], a[24:27], v[196:199], v[120:123]\n"
    "ds_read_u16 v191, v191\n"
    "v_exp_f16_sdwa v234, v232 dst_sel:WORD_1 dst_unused:UNUSED_PRESERVE src0_sel:WORD_1\n"
    "v_mfma_f32_16x16x32_bf16 v[124:127], a[28:31], v[196:199], v[124:127]\n"
    "s_waitcnt lgkmcnt(7)\n"
    "ds_read_u16_d16_hi v207, v207\n"
    "v_or_b32_e32 v187, v187, v203\n"
    "v_mfma_f32_16x16x32_bf16 v[132:135], a[244:247], v[192:195], v[132:135]\n"
    "v_pk_fma_f16 v234, v234, s49, s49\n"
    "v_lshlrev_b32_sdwa v192, 1, v168 dst_sel:DWORD dst_unused:UNUSED_PAD src0_sel:DWORD src1_sel:WORD_1\n"
    "v_mfma_f32_16x16x32_bf16 v[136:139], v[236:239], v[196:199], v[136:139]\n"
    "ds_read_b128 a[240:243], v244 offset:34816\n"
    "v_lshlrev_b32_sdwa v193, 1, v170 dst_sel:DWORD dst_unused:UNUSED_PAD src0_sel:DWORD src1_sel:WORD_1\n"
    "v_mfma_f32_16x16x32_bf16 v[140:143], a[244:247], v[196:199], v[140:143]\n"
    "buffer_load_dwordx4 v[76:79], v246, s[40:43], s63 offen offset:128\n"
    "ds_read_u16 v192, v192\n"
    "v_mfma_f32_16x16x32_bf16 v[168:171], a[112:115], v[144:147], 0\n"
    "v_lshlrev_b32_sdwa v194, 1, v172 dst_sel:DWORD dst_unused:UNUSED_PAD src0_sel:DWORD src1_sel:WORD_1\n"
    "v_lshlrev_b32_sdwa v202, 1, v173 dst_sel:DWORD dst_unused:UNUSED_PAD src0_sel:DWORD src1_sel:WORD_1\n"
    "v_mfma_f32_16x16x32_bf16 v[168:171], a[160:163], v[148:151], v[168:171]\n"
    "s_waitcnt lgkmcnt(7)\n"
    "ds_read_u16_d16_hi v200, v200\n"
    "v_or_b32_e32 v189, v189, v205\n"
    "s_waitcnt lgkmcnt(7)\n"
    "v_smfmac_f32_16x16x64_bf16 v[0:3], v[216:219], v[208:215], v240\n"
    "s_nop 0\n"
    "ds_read_b128 v[216:219], v252 offset:0\n"
    "v_smfmac_f32_16x16x64_bf16 v[4:7], v[220:223], v[208:215], v240 abid:1\n"
    "ds_read_b128 v[208:211], v250 offset:640\n"
    "v_lshlrev_b32_sdwa v195, 1, v174 dst_sel:DWORD dst_unused:UNUSED_PAD src0_sel:DWORD src1_sel:WORD_1\n"
    "ds_read_b128 v[212:215], v250 offset:960\n"
    "v_lshlrev_b32_sdwa v203, 1, v175 dst_sel:DWORD dst_unused:UNUSED_PAD src0_sel:DWORD src1_sel:WORD_1\n"
    "ds_read_b128 v[220:223], v252 offset:336\n"
    "v_rcp_f16_e32 v232, v234\n"
    "v_mfma_f32_16x16x32_bf16 v[168:171], a[72:75], v[152:155], v[168:171]\n"
    "s_waitcnt lgkmcnt(9)\n"
    "ds_read_u16 v193, v193\n"
    "v_or_b32_e64 v190, v190, v206\n"
    "v_rcp_f16_sdwa v232, v234 dst_sel:WORD_1 dst_unused:UNUSED_PRESERVE src0_sel:WORD_1\n"
    "v_mfma_f32_16x16x32_bf16 v[172:175], a[116:119], v[144:147], 0\n"
    "ds_read_u16_d16_hi v201, v201\n"
    "v_lshlrev_b32_sdwa v196, 1, v176 dst_sel:DWORD dst_unused:UNUSED_PAD src0_sel:DWORD src1_sel:WORD_1\n"
    "v_lshlrev_b32_sdwa v197, 1, v178 dst_sel:DWORD dst_unused:UNUSED_PAD src0_sel:DWORD src1_sel:WORD_1\n"
    "v_lshlrev_b32_sdwa v205, 1, v179 dst_sel:DWORD dst_unused:UNUSED_PAD src0_sel:DWORD src1_sel:WORD_1\n"
    "v_lshlrev_b32_sdwa v198, 1, v180 dst_sel:DWORD dst_unused:UNUSED_PAD src0_sel:DWORD src1_sel:WORD_1\n"
    "v_lshlrev_b32_sdwa v206, 1, v181 dst_sel:DWORD dst_unused:UNUSED_PAD src0_sel:DWORD src1_sel:WORD_1\n"
    "v_lshlrev_b32_sdwa v199, 1, v182 dst_sel:DWORD dst_unused:UNUSED_PAD src0_sel:DWORD src1_sel:WORD_1\n"
    "v_fma_mix_f32 v234, v184, v232, 0 op_sel_hi:[1,1,0]\n"
    "v_mfma_f32_16x16x32_bf16 v[172:175], a[164:167], v[148:151], v[172:175]\n"
    "s_waitcnt lgkmcnt(8)\n"
    "ds_read_u16 v194, v194\n"
    "v_or_b32_e32 v191, v191, v207\n"
    "v_mfma_f32_16x16x32_bf16 v[172:175], a[240:243], v[152:155], v[172:175]\n"
    "ds_read_b128 v[236:239], v244 offset:8192\n"
    "v_lshlrev_b32_sdwa v207, 1, v183 dst_sel:DWORD dst_unused:UNUSED_PAD src0_sel:DWORD src1_sel:WORD_1\n"
    "v_fma_mix_f32 v232, v184, v232, 0 op_sel:[1,1,0] op_sel_hi:[1,1,0]\n"
    "v_mfma_f32_16x16x32_bf16 v[176:179], a[112:115], v[156:159], 0\n"
    "ds_read_u16_d16_hi v202, v202\n"
    "v_cvt_pk_bf16_f32 v184, v234, v232\n"
    "v_mfma_f32_16x16x32_bf16 v[176:179], a[160:163], v[160:163], v[176:179]\n"
    "ds_read_u16 v195, v195\n"
    "v_pk_fma_f16 v232, v188, v188, s48\n"
    "v_mfma_f32_16x16x32_bf16 v[176:179], a[72:75], v[164:167], v[176:179]\n"
    "s_waitcnt lgkmcnt(8)\n"
    "v_pk_mul_f16 v232, v188, v232\n"
    "v_or_b32_e64 v192, v192, v200\n"
    "v_mfma_f32_16x16x32_bf16 v[180:183], a[116:119], v[156:159], 0\n"
    "s_waitcnt lgkmcnt(7)\n"
    "ds_read_b128 a[244:247], v244 offset:10240\n"
    "v_exp_f16_e32 v234, v232\n"
    "v_smfmac_f32_16x16x64_bf16 v[0:3], v[216:219], v[208:215], v240\n"
    "s_waitcnt lgkmcnt(7)\n"
    "ds_read_b128 v[216:219], v251 offset:672\n"
    "ds_read_u16_d16_hi v203, v203\n"
    "v_smfmac_f32_16x16x64_bf16 v[4:7], v[220:223], v[208:215], v240 abid:1\n"
    "s_nop 0\n"
    "v_exp_f16_sdwa v234, v232 dst_sel:WORD_1 dst_unused:UNUSED_PRESERVE src0_sel:WORD_1\n"
    "s_waitcnt vmcnt(5)\n"
    "v_smfmac_f32_32x32x32_bf16 v[112:127], a[248:251], a[232:239], v241\n"
    "ds_read_b128 v[208:211], v250 offset:80\n"
    "ds_read_b128 v[212:215], v250 offset:400\n"
    "v_pk_fma_f16 v234, v234, s49, s49\n"
    "v_rcp_f16_e64 v232, v234\n"
    "s_waitcnt vmcnt(4)\n"
    "v_smfmac_f32_32x32x32_bf16 v[96:111], a[248:251], a[224:231], v241\n"
    "s_waitcnt lgkmcnt(9)\n"
    "ds_read_b128 v[220:223], v251 offset:1008\n"
    "buffer_load_dwordx4 v[80:83], v246, s[40:43], s52 offen offset:128\n"
    "v_rcp_f16_sdwa v232, v234 dst_sel:WORD_1 dst_unused:UNUSED_PRESERVE src0_sel:WORD_1\n"
    "v_or_b32_e64 v193, v193, v201\n"
    "v_fma_mix_f32 v234, v188, v232, 0 op_sel_hi:[1,1,0]\n"
    "v_fma_mix_f32 v232, v188, v232, 0 op_sel:[1,1,0] op_sel_hi:[1,1,0]\n"
    "v_lshlrev_b32_sdwa v201, 1, v171 dst_sel:DWORD dst_unused:UNUSED_PAD src0_sel:DWORD src1_sel:WORD_1\n"
    "v_pk_mov_b32 v[224:225], v[0:1], v[0:1] op_sel:[0,1]\n"
    "v_pk_mov_b32 v[226:227], v[2:3], v[2:3] op_sel:[0,1]\n"
    "v_pk_mul_f32 v[228:229], v[0:1], v[0:1]\n"
    "v_pk_mul_f32 v[230:231], v[2:3], v[2:3]\n"
    "v_pk_add_f32 v[224:225], v[224:225], v[4:5]\n"
    "v_pk_add_f32 v[226:227], v[226:227], v[6:7]\n"
    "v_pk_fma_f32 v[228:229], v[4:5], v[4:5], v[228:229]\n"
    "v_pk_fma_f32 v[230:231], v[6:7], v[6:7], v[230:231]\n"
    "v_mfma_f32_16x16x32_bf16 v[180:183], a[164:167], v[160:163], v[180:183]\n"
    "s_waitcnt lgkmcnt(7)\n"
    "ds_read_b128 a[232:235], v243 offset:24640\n"
    "ds_read_b128 a[236:239], v243 offset:25664\n"
    "v_mfma_f32_16x16x32_bf16 v[180:183], a[240:243], v[164:167], v[180:183]\n"
    "s_waitcnt lgkmcnt(7)\n"
    "ds_read_u16 v196, v196\n"
    "ds_read_u16_d16_hi v204, v204\n"
    "v_mfma_f32_16x16x32_bf16 v[112:115], a[40:43], v[184:187], v[112:115]\n"
    "ds_read_u16 v197, v197\n"
    "v_cvt_pk_bf16_f32 v188, v234, v232\n"
    "v_mfma_f32_16x16x32_bf16 v[116:119], a[44:47], v[184:187], v[116:119]\n"
    "v_or_b32_e64 v194, v194, v202\n"
    "v_lshlrev_b32_sdwa v202, 1, v173 dst_sel:DWORD dst_unused:UNUSED_PAD src0_sel:DWORD src1_sel:WORD_1\n"
    "v_mfma_f32_16x16x32_bf16 v[128:131], v[236:239], v[184:187], v[128:131]\n"
    "s_nop 0\n"
    "ds_read_u16_d16_hi v205, v205\n"
    "v_mfma_f32_16x16x32_bf16 v[132:135], a[244:247], v[184:187], v[132:135]\n"
    "s_waitcnt lgkmcnt(8)\n"
    "ds_read_u16 v198, v198\n"
    "buffer_load_dwordx4 a[224:227], v245, s[40:43], s57 offen offset:128\n"
    "s_waitcnt lgkmcnt(8)\n"
    "v_smfmac_f32_16x16x64_bf16 v[8:11], v[216:219], v[208:215], v240\n"
    "s_waitcnt lgkmcnt(7)\n"
    "v_or_b32_e64 v195, v195, v203\n"
    "v_smfmac_f32_16x16x64_bf16 v[12:15], v[220:223], v[208:215], v240 abid:1\n"
    "ds_read_b128 v[208:211], v250 offset:720\n"
    "ds_read_b128 v[212:215], v250 offset:1040\n"
    "v_mfma_f32_16x16x32_bf16 v[120:123], a[40:43], v[188:191], v[120:123]\n"
    "v_lshlrev_b32_sdwa v203, 1, v175 dst_sel:DWORD dst_unused:UNUSED_PAD src0_sel:DWORD src1_sel:WORD_1\n"
    "ds_read_b128 v[216:219], v252 offset:672\n"
    "s_waitcnt lgkmcnt(8)\n"
    "v_smfmac_f32_32x32x32_bf16 v[96:111], a[248:251], a[232:239], v241\n"
    "s_nop 0\n"
    "ds_read_b128 v[220:223], v252 offset:1008\n"
    "ds_read_u16_d16_hi v206, v206\n"
    "v_mfma_f32_16x16x32_bf16 v[124:127], a[44:47], v[188:191], v[124:127]\n"
    "v_mfma_f32_16x16x32_bf16 v[136:139], v[236:239], v[188:191], v[136:139]\n"
    "ds_read_u16 v199, v199\n"
    "v_mfma_f32_16x16x32_bf16 v[140:143], a[244:247], v[188:191], v[140:143]\n"
    "s_waitcnt lgkmcnt(9)\n"
    "ds_read_u16_d16_hi v207, v207\n"
    "v_or_b32_e32 v196, v196, v204\n"
    "buffer_load_dwordx4 a[228:231], v245, s[40:43], s88 offen offset:128\n"
    "v_mfma_f32_16x16x32_bf16 v[96:99], a[32:35], v[184:187], v[96:99]\n"
    "ds_read_b128 a[232:235], v243 offset:26688\n"
    "v_mfma_f32_16x16x32_bf16 v[100:103], a[36:39], v[184:187], v[100:103]\n"
    "s_nop 0\n"
    "ds_read_b128 a[236:239], v243 offset:27712\n"
    "v_cvt_pk_f16_f32 v184, v168, v169\n"
    "v_mfma_f32_16x16x32_bf16 v[104:107], a[32:35], v[188:191], v[104:107]\n"
    "s_waitcnt lgkmcnt(10)\n"
    "ds_read_b128 a[240:243], v244 offset:38912\n"
    "v_lshlrev_b32_sdwa v185, 1, v170 dst_sel:DWORD dst_unused:UNUSED_PAD src0_sel:DWORD src1_sel:WORD_1\n"
    "v_mfma_f32_16x16x32_bf16 v[108:111], a[36:39], v[188:191], v[108:111]\n"
    "ds_read_u16 v185, v185\n"
    "v_lshlrev_b32_sdwa v186, 1, v172 dst_sel:DWORD dst_unused:UNUSED_PAD src0_sel:DWORD src1_sel:WORD_1\n"
    "v_mfma_f32_16x16x32_bf16 v[168:171], a[120:123], v[144:147], 0\n"
    "v_lshlrev_b32_sdwa v187, 1, v174 dst_sel:DWORD dst_unused:UNUSED_PAD src0_sel:DWORD src1_sel:WORD_1\n"
    "v_cvt_pk_f16_f32 v188, v176, v177\n"
    "v_mfma_f32_16x16x32_bf16 v[168:171], a[168:171], v[148:151], v[168:171]\n"
    "s_waitcnt lgkmcnt(8)\n"
    "ds_read_u16_d16_hi v201, v201\n"
    "v_lshlrev_b32_sdwa v189, 1, v178 dst_sel:DWORD dst_unused:UNUSED_PAD src0_sel:DWORD src1_sel:WORD_1\n"
    "v_smfmac_f32_16x16x64_bf16 v[8:11], v[216:219], v[208:215], v240\n"
    "s_waitcnt lgkmcnt(8)\n"
    "v_lshlrev_b32_sdwa v190, 1, v180 dst_sel:DWORD dst_unused:UNUSED_PAD src0_sel:DWORD src1_sel:WORD_1\n"
    "v_smfmac_f32_16x16x64_bf16 v[12:15], v[220:223], v[208:215], v240 abid:1\n"
    "ds_read_b128 v[208:211], v250 offset:160\n"
    "ds_read_b128 v[212:215], v250 offset:480\n"
    "v_mfma_f32_16x16x32_bf16 v[168:171], a[76:79], v[152:155], v[168:171]\n"
    "buffer_load_dwordx4 v[84:87], v247, s[40:43], s60 offen offset:128\n"
    "ds_read_b128 v[216:219], v251 offset:1344\n"
    "v_lshlrev_b32_sdwa v191, 1, v182 dst_sel:DWORD dst_unused:UNUSED_PAD src0_sel:DWORD src1_sel:WORD_1\n"
    "v_or_b32_e64 v197, v197, v205\n"
    "v_lshlrev_b32_sdwa v205, 1, v179 dst_sel:DWORD dst_unused:UNUSED_PAD src0_sel:DWORD src1_sel:WORD_1\n"
    "v_pk_add_f32 v[224:225], v[224:225], v[8:9]\n"
    "v_pk_add_f32 v[226:227], v[226:227], v[10:11]\n"
    "v_pk_fma_f32 v[228:229], v[8:9], v[8:9], v[228:229]\n"
    "v_pk_fma_f32 v[230:231], v[10:11], v[10:11], v[230:231]\n"
    "v_pk_add_f32 v[224:225], v[224:225], v[12:13]\n"
    "v_pk_add_f32 v[226:227], v[226:227], v[14:15]\n"
    "v_pk_fma_f32 v[228:229], v[12:13], v[12:13], v[228:229]\n"
    "v_pk_fma_f32 v[230:231], v[14:15], v[14:15], v[230:231]\n"
    "v_mfma_f32_16x16x32_bf16 v[172:175], a[124:127], v[144:147], 0\n"
    "s_waitcnt lgkmcnt(9)\n"
    "ds_read_b128 v[220:223], v251 offset:1680\n"
    "ds_read_u16 v186, v186\n"
    "v_mfma_f32_16x16x32_bf16 v[172:175], a[172:175], v[148:151], v[172:175]\n"
    "s_waitcnt lgkmcnt(8)\n"
    "ds_read_u16_d16_hi v202, v202\n"
    "v_or_b32_e32 v198, v198, v206\n"
    "v_smfmac_f32_32x32x32_bf16 v[112:127], a[248:251], a[232:239], v241\n"
    "s_waitcnt lgkmcnt(8)\n"
    "ds_read_b128 v[236:239], v244 offset:12288\n"
    "v_lshlrev_b32_sdwa v206, 1, v181 dst_sel:DWORD dst_unused:UNUSED_PAD src0_sel:DWORD src1_sel:WORD_1\n"
    "v_or_b32_e64 v199, v199, v207\n"
    "v_lshlrev_b32_sdwa v207, 1, v183 dst_sel:DWORD dst_unused:UNUSED_PAD src0_sel:DWORD src1_sel:WORD_1\n"
    "v_pk_fma_f16 v232, v184, v184, s48\n"
    "v_mfma_f32_16x16x32_bf16 v[172:175], a[240:243], v[152:155], v[172:175]\n"
    "ds_read_u16 v187, v187\n"
    "v_pk_mul_f16 v232, v184, v232\n"
    "v_mfma_f32_16x16x32_bf16 v[176:179], a[120:123], v[156:159], 0\n"
    "ds_read_u16_d16_hi v203, v203\n"
    "v_exp_f16_e32 v234, v232\n"
    "v_mfma_f32_16x16x32_bf16 v[176:179], a[168:171], v[160:163], v[176:179]\n"
    "v_exp_f16_sdwa v234, v232 dst_sel:WORD_1 dst_unused:UNUSED_PRESERVE src0_sel:WORD_1\n"
    "v_mfma_f32_16x16x32_bf16 v[176:179], a[76:79], v[164:167], v[176:179]\n"
    "s_waitcnt lgkmcnt(8)\n"
    "ds_read_b128 a[244:247], v244 offset:14336\n"
    "ds_read_u16 v189, v189\n"
    "v_mfma_f32_16x16x32_bf16 v[180:183], a[124:127], v[156:159], 0\n"
    "v_or_b32_e64 v185, v185, v201\n"
    "v_pk_fma_f16 v234, v234, s49, s49\n"
    "s_waitcnt lgkmcnt(8)\n"
    "v_smfmac_f32_16x16x64_bf16 v[16:19], v[216:219], v[208:215], v240\n"
    "s_waitcnt lgkmcnt(7)\n"
    "ds_read_b128 v[216:219], v252 offset:1344\n"
    "v_smfmac_f32_16x16x64_bf16 v[20:23], v[220:223], v[208:215], v240 abid:1\n"
    "ds_read_b128 v[208:211], v250 offset:800\n"
    "v_mfma_f32_16x16x32_bf16 v[180:183], a[172:175], v[160:163], v[180:183]\n"
    "ds_read_b128 v[212:215], v250 offset:1120\n"
    "v_lshlrev_b32_e64 v254, 3, v244\n"
    "v_mfma_f32_16x16x32_bf16 v[180:183], a[240:243], v[164:167], v[180:183]\n"
    "ds_read_b128 v[220:223], v252 offset:1680\n"
    "v_lshlrev_b32_sdwa v201, 1, v171 dst_sel:DWORD dst_unused:UNUSED_PAD src0_sel:DWORD src1_sel:WORD_1\n"
    "v_mfma_f32_16x16x32_bf16 v[96:99], a[176:179], v[192:195], v[96:99]\n"
    "ds_read_b128 a[232:235], v243 offset:28736\n"
    "v_rcp_f16_e32 v232, v234\n"
    "v_mfma_f32_16x16x32_bf16 v[100:103], a[180:183], v[192:195], v[100:103]\n"
    "s_waitcnt lgkmcnt(9)\n"
    "ds_read_u16_d16_hi v205, v205\n"
    "buffer_load_dwordx4 v[88:91], v247, s[40:43], s61 offen offset:128\n"
    "v_mfma_f32_16x16x32_bf16 v[112:115], a[184:187], v[192:195], v[112:115]\n"
    "v_or_b32_e64 v186, v186, v202\n"
    "v_lshlrev_b32_sdwa v202, 1, v173 dst_sel:DWORD dst_unused:UNUSED_PAD src0_sel:DWORD src1_sel:WORD_1\n"
    "v_mfma_f32_16x16x32_bf16 v[116:119], a[188:191], v[192:195], v[116:119]\n"
    "ds_read_b128 a[236:239], v243 offset:29760\n"
    "ds_read_u16 v190, v190\n"
    "v_mfma_f32_16x16x32_bf16 v[128:131], v[236:239], v[192:195], v[128:131]\n"
    "s_nop 0\n"
    "v_rcp_f16_sdwa v232, v234 dst_sel:WORD_1 dst_unused:UNUSED_PRESERVE src0_sel:WORD_1\n"
    "v_mfma_f32_16x16x32_bf16 v[104:107], a[176:179], v[196:199], v[104:107]\n"
    "s_waitcnt lgkmcnt(9)\n"
    "ds_read_b128 v[144:147], v242 offset:0\n"
    "v_or_b32_e64 v187, v187, v203\n"
    "v_mfma_f32_16x16x32_bf16 v[132:135], a[244:247], v[192:195], v[132:135]\n"
    "ds_read_u16_d16_hi v206, v206\n"
    "v_cvt_pk_f16_f32 v192, v168, v169\n"
    "v_mfma_f32_16x16x32_bf16 v[108:111], a[180:183], v[196:199], v[108:111]\n"
    "v_lshlrev_b32_sdwa v193, 1, v170 dst_sel:DWORD dst_unused:UNUSED_PAD src0_sel:DWORD src1_sel:WORD_1\n"
    "v_lshlrev_b32_sdwa v194, 1, v172 dst_sel:DWORD dst_unused:UNUSED_PAD src0_sel:DWORD src1_sel:WORD_1\n"
    "v_mfma_f32_16x16x32_bf16 v[120:123], a[184:187], v[196:199], v[120:123]\n"
    "ds_read_b128 v[148:151], v242 offset:1024\n"
    "v_lshlrev_b32_sdwa v195, 1, v174 dst_sel:DWORD dst_unused:UNUSED_PAD src0_sel:DWORD src1_sel:WORD_1\n"
    "v_mfma_f32_16x16x32_bf16 v[124:127], a[188:191], v[196:199], v[124:127]\n"
    "ds_read_u16 v191, v191\n"
    "v_lshlrev_b32_sdwa v203, 1, v175 dst_sel:DWORD dst_unused:UNUSED_PAD src0_sel:DWORD src1_sel:WORD_1\n"
    "s_waitcnt lgkmcnt(9)\n"
    "v_smfmac_f32_16x16x64_bf16 v[16:19], v[216:219], v[208:215], v240\n"
    "s_waitcnt lgkmcnt(8)\n"
    "ds_read_b128 v[216:219], v251 offset:2016\n"
    "s_mov_b32 exec_hi, 0\n"
    "buffer_load_dword v233, v254, s[72:75], s53 offen sc1\n"
    "s_mov_b32 exec_hi, -1\n"
    "v_smfmac_f32_16x16x64_bf16 v[20:23], v[220:223], v[208:215], v240 abid:1\n"
    "ds_read_b128 v[208:211], v250 offset:240\n"
    "ds_read_b128 v[212:215], v250 offset:560\n"
    "v_fma_mix_f32 v234, v184, v232, 0 op_sel_hi:[1,1,0]\n"
    "v_fma_mix_f32 v232, v184, v232, 0 op_sel:[1,1,0] op_sel_hi:[1,1,0]\n"
    "v_pk_add_f32 v[224:225], v[224:225], v[16:17]\n"
    "v_pk_add_f32 v[226:227], v[226:227], v[18:19]\n"
    "v_pk_fma_f32 v[228:229], v[16:17], v[16:17], v[228:229]\n"
    "v_pk_fma_f32 v[230:231], v[18:19], v[18:19], v[230:231]\n"
    "v_pk_add_f32 v[224:225], v[224:225], v[20:21]\n"
    "v_pk_add_f32 v[226:227], v[226:227], v[22:23]\n"
    "v_pk_fma_f32 v[228:229], v[20:21], v[20:21], v[228:229]\n"
    "v_pk_fma_f32 v[230:231], v[22:23], v[22:23], v[230:231]\n"
    "v_mfma_f32_16x16x32_bf16 v[136:139], v[236:239], v[196:199], v[136:139]\n"
    "s_waitcnt lgkmcnt(8)\n"
    "ds_read_b128 v[220:223], v251 offset:2352\n"
    "ds_read_b128 v[152:155], v242 offset:2048\n"
    "v_mfma_f32_16x16x32_bf16 v[140:143], a[244:247], v[196:199], v[140:143]\n"
    "s_nop 0\n"
    "ds_read_u16_d16_hi v207, v207\n"
    "v_or_b32_e32 v189, v189, v205\n"
    "s_waitcnt lgkmcnt(9)\n"
    "v_mfma_f32_16x16x32_bf16 v[168:171], a[80:83], v[144:147], 0\n"
    "ds_read_b128 v[156:159], v242 offset:3072\n"
    "ds_read_b128 v[160:163], v242 offset:4096\n"
    "v_smfmac_f32_32x32x32_bf16 v[128:143], a[248:251], a[232:239], v241\n"
    "ds_read_b128 v[164:167], v242 offset:5120\n"
    "v_cvt_pk_f16_f32 v196, v176, v177\n"
    "v_lshlrev_b32_sdwa v197, 1, v178 dst_sel:DWORD dst_unused:UNUSED_PAD src0_sel:DWORD src1_sel:WORD_1\n"
    "s_waitcnt lgkmcnt(10)\n"
    "v_or_b32_e32 v190, v190, v206\n"
    "v_mfma_f32_16x16x32_bf16 v[168:171], a[128:131], v[148:151], v[168:171]\n"
    "ds_read_b128 a[240:243], v244 offset:32768\n"
    "ds_read_u16 v193, v193\n"
    "v_mfma_f32_16x16x32_bf16 v[172:175], a[84:87], v[144:147], 0\n"
    "v_lshlrev_b32_sdwa v205, 1, v179 dst_sel:DWORD dst_unused:UNUSED_PAD src0_sel:DWORD src1_sel:WORD_1\n"
    "v_lshlrev_b32_sdwa v198, 1, v180 dst_sel:DWORD dst_unused:UNUSED_PAD src0_sel:DWORD src1_sel:WORD_1\n"
    "v_mfma_f32_16x16x32_bf16 v[172:175], a[132:135], v[148:151], v[172:175]\n"
    "ds_read_u16_d16_hi v201, v201\n"
    "v_lshlrev_b32_sdwa v206, 1, v181 dst_sel:DWORD dst_unused:UNUSED_PAD src0_sel:DWORD src1_sel:WORD_1\n"
    "v_lshlrev_b32_sdwa v199, 1, v182 dst_sel:DWORD dst_unused:UNUSED_PAD src0_sel:DWORD src1_sel:WORD_1\n"
    "v_cvt_pk_bf16_f32 v184, v234, v232\n"
    "v_pk_fma_f16 v232, v188, v188, s48\n"
    "v_mov_b32_e64 v254, 0\n"
    "s_waitcnt lgkmcnt(9)\n"
    "v_smfmac_f32_16x16x64_bf16 v[24:27], v[216:219], v[208:215], v240\n"
    "s_waitcnt lgkmcnt(8)\n"
    "ds_read_b128 v[216:219], v252 offset:2016\n"
    "ds_read_u16 v194, v194\n"
    "v_smfmac_f32_16x16x64_bf16 v[28:31], v[220:223], v[208:215], v240 abid:1\n"
    "s_waitcnt lgkmcnt(9)\n"
    "v_pk_mul_f16 v232, v188, v232\n"
    "v_mfma_f32_16x16x32_bf16 v[168:171], a[48:51], v[152:155], v[168:171]\n"
    "s_nop 0\n"
    "ds_read_b128 v[208:211], v250 offset:880\n"
    "v_exp_f16_e64 v234, v232\n"
    "ds_read_b128 v[212:215], v250 offset:1200\n"
    "v_exp_f16_sdwa v234, v232 dst_sel:WORD_1 dst_unused:UNUSED_PRESERVE src0_sel:WORD_1\n"
    "s_waitcnt lgkmcnt(9)\n"
    "v_mfma_f32_16x16x32_bf16 v[176:179], a[80:83], v[156:159], 0\n"
    "s_waitcnt lgkmcnt(8)\n"
    "ds_read_b128 v[220:223], v252 offset:2352\n"
    "ds_read_u16_d16_hi v202, v202\n"
    "v_mfma_f32_16x16x32_bf16 v[176:179], a[128:131], v[160:163], v[176:179]\n"
    "s_waitcnt lgkmcnt(9)\n"
    "v_or_b32_e64 v191, v191, v207\n"
    "v_lshlrev_b32_sdwa v207, 1, v183 dst_sel:DWORD dst_unused:UNUSED_PAD src0_sel:DWORD src1_sel:WORD_1\n"
    "v_mfma_f32_16x16x32_bf16 v[176:179], a[48:51], v[164:167], v[176:179]\n"
    "s_waitcnt lgkmcnt(8)\n"
    "ds_read_b128 v[236:239], v244 offset:16384\n"
    "v_pk_fma_f16 v234, v234, s49, s49\n"
    "v_mfma_f32_16x16x32_bf16 v[172:175], a[240:243], v[152:155], v[172:175]\n"
    "ds_read_u16 v195, v195\n"
    "v_rcp_f16_e64 v232, v234\n"
    "v_mfma_f32_16x16x32_bf16 v[180:183], a[84:87], v[156:159], 0\n"
    "v_rcp_f16_sdwa v232, v234 dst_sel:WORD_1 dst_unused:UNUSED_PRESERVE src0_sel:WORD_1\n"
    "v_mfma_f32_16x16x32_bf16 v[180:183], a[132:135], v[160:163], v[180:183]\n"
    "ds_read_u16_d16_hi v203, v203\n"
    "v_fma_mix_f32 v234, v188, v232, 0 op_sel_hi:[1,1,0]\n"
    "v_mfma_f32_16x16x32_bf16 v[180:183], a[240:243], v[164:167], v[180:183]\n"
    "v_fma_mix_f32 v232, v188, v232, 0 op_sel:[1,1,0] op_sel_hi:[1,1,0]\n"
    "v_lshlrev_b32_sdwa v200, 1, v169 dst_sel:DWORD dst_unused:UNUSED_PAD src0_sel:DWORD src1_sel:WORD_1\n"
    "v_mfma_f32_16x16x32_bf16 v[96:99], a[192:195], v[184:187], v[96:99]\n"
    "s_waitcnt lgkmcnt(8)\n"
    "ds_read_b128 a[244:247], v244 offset:18432\n"
    "ds_read_u16 v197, v197\n"
    "v_mfma_f32_16x16x32_bf16 v[100:103], a[196:199], v[184:187], v[100:103]\n"
    "v_or_b32_e32 v193, v193, v201\n"
    "v_cvt_pk_bf16_f32 v188, v234, v232\n"
    "v_mfma_f32_16x16x32_bf16 v[112:115], a[200:203], v[184:187], v[112:115]\n"
    "ds_read_u16_d16_hi v205, v205\n"
    "v_pk_fma_f16 v232, v192, v192, s48\n"
    "v_mfma_f32_16x16x32_bf16 v[116:119], a[204:207], v[184:187], v[116:119]\n"
    "s_waitcnt lgkmcnt(8)\n"
    "s_barrier\n"
    "v_smfmac_f32_16x16x64_bf16 v[24:27], v[216:219], v[208:215], v240\n"
    "s_waitcnt lgkmcnt(7)\n"
    "ds_read_u16 v198, v198\n"
    "ds_write2_b32 v248, v32, v33 offset0:0 offset1:20\n"
    "v_smfmac_f32_16x16x64_bf16 v[28:31], v[220:223], v[208:215], v240 abid:1\n"
    "s_waitcnt lgkmcnt(7)\n"
    "v_or_b32_e64 v194, v194, v202\n"
    "v_mfma_f32_16x16x32_bf16 v[128:131], v[236:239], v[184:187], v[128:131]\n"
    "ds_read_u16_d16_hi v206, v206\n"
    "ds_write2_b32 v248, v34, v35 offset0:40 offset1:60\n"
    "v_add_u32_e32 v248, 1360, v248\n"
    "v_pk_add_f32 v[224:225], v[224:225], v[24:25]\n"
    "v_pk_add_f32 v[226:227], v[226:227], v[26:27]\n"
    "v_pk_fma_f32 v[228:229], v[24:25], v[24:25], v[228:229]\n"
    "v_pk_fma_f32 v[230:231], v[26:27], v[26:27], v[230:231]\n"
    "v_pk_add_f32 v[224:225], v[224:225], v[28:29]\n"
    "v_pk_add_f32 v[226:227], v[226:227], v[30:31]\n"
    "v_pk_fma_f32 v[228:229], v[28:29], v[28:29], v[228:229]\n"
    "v_pk_fma_f32 v[230:231], v[30:31], v[30:31], v[230:231]\n"
    "s_waitcnt lgkmcnt(6)\n"
    "v_mfma_f32_16x16x32_bf16 v[132:135], a[244:247], v[184:187], v[132:135]\n"
    "s_nop 0\n"
    "ds_read_u16 v199, v199\n"
    "ds_write2_b32 v235, v52, v53 offset0:0 offset1:20\n"
    "v_mfma_f32_16x16x32_bf16 v[104:107], a[192:195], v[188:191], v[104:107]\n"
    "v_or_b32_e64 v195, v195, v203\n"
    "v_pk_mul_f16 v232, v192, v232\n"
    "v_mfma_f32_16x16x32_bf16 v[108:111], a[196:199], v[188:191], v[108:111]\n"
    "ds_read_u16_d16_hi v207, v207\n"
    "ds_write2_b32 v235, v54, v55 offset0:40 offset1:60\n"
    "v_mfma_f32_16x16x32_bf16 v[120:123], a[200:203], v[188:191], v[120:123]\n"
    "s_waitcnt lgkmcnt(8)\n"
    "v_add_u32_e32 v235, 1360, v235\n"
    "v_exp_f16_e32 v234, v232\n"
    "v_mfma_f32_16x16x32_bf16 v[124:127], a[204:207], v[188:191], v[124:127]\n"
    "ds_read_b128 a[240:243], v244 offset:36864\n"
    "ds_write2_b32 v248, v36, v37 offset0:0 offset1:20\n"
    "v_mfma_f32_16x16x32_bf16 v[136:139], v[236:239], v[188:191], v[136:139]\n"
    "v_or_b32_e64 v197, v197, v205\n"
    "v_exp_f16_sdwa v234, v232 dst_sel:WORD_1 dst_unused:UNUSED_PRESERVE src0_sel:WORD_1\n"
    "v_mfma_f32_16x16x32_bf16 v[140:143], a[244:247], v[188:191], v[140:143]\n"
    "ds_read_u16_d16_hi v200, v200\n"
    "ds_write2_b32 v248, v38, v39 offset0:40 offset1:60\n"
    "v_add_u32_e32 v248, 1360, v248\n"
    "v_pk_fma_f16 v234, v234, s49, s49\n"
    "v_lshlrev_b32_sdwa v184, 1, v168 dst_sel:DWORD dst_unused:UNUSED_PAD src0_sel:DWORD src1_sel:WORD_1\n"
    "v_lshlrev_b32_sdwa v185, 1, v170 dst_sel:DWORD dst_unused:UNUSED_PAD src0_sel:DWORD src1_sel:WORD_1\n"
    "v_lshlrev_b32_sdwa v201, 1, v171 dst_sel:DWORD dst_unused:UNUSED_PAD src0_sel:DWORD src1_sel:WORD_1\n"
    "v_mfma_f32_16x16x32_bf16 v[168:171], a[88:91], v[144:147], 0\n"
    "s_nop 0\n"
    "s_waitcnt lgkmcnt(8)\n"
    "ds_read_u16 v184, v184\n"
    "ds_write2_b32 v235, v56, v57 offset0:0 offset1:20\n"
    "v_mfma_f32_16x16x32_bf16 v[168:171], a[136:139], v[148:151], v[168:171]\n"
    "s_add_u32 s60, s60, 1572864\n"
    "v_lshlrev_b32_sdwa v186, 1, v172 dst_sel:DWORD dst_unused:UNUSED_PAD src0_sel:DWORD src1_sel:WORD_1\n"
    "v_lshlrev_b32_sdwa v202, 1, v173 dst_sel:DWORD dst_unused:UNUSED_PAD src0_sel:DWORD src1_sel:WORD_1\n"
    "v_lshlrev_b32_sdwa v187, 1, v174 dst_sel:DWORD dst_unused:UNUSED_PAD src0_sel:DWORD src1_sel:WORD_1\n"
    "ds_write2_b32 v235, v58, v59 offset0:40 offset1:60\n"
    "v_add_u32_e32 v235, -1360, v235\n"
    "v_lshlrev_b32_sdwa v203, 1, v175 dst_sel:DWORD dst_unused:UNUSED_PAD src0_sel:DWORD src1_sel:WORD_1\n"
    "ds_write2_b32 v248, v40, v41 offset0:0 offset1:20\n"
    "v_rcp_f16_e32 v232, v234\n"
    "v_mfma_f32_16x16x32_bf16 v[168:171], a[52:55], v[152:155], v[168:171]\n"
    "s_waitcnt lgkmcnt(6)\n"
    "ds_read_u16 v185, v185\n"
    "ds_write2_b32 v248, v42, v43 offset0:40 offset1:60\n"
    "v_add_u32_e32 v248, 1360, v248\n"
    "v_or_b32_e64 v198, v198, v206\n"
    "v_rcp_f16_sdwa v232, v234 dst_sel:WORD_1 dst_unused:UNUSED_PRESERVE src0_sel:WORD_1\n"
    "v_mfma_f32_16x16x32_bf16 v[172:175], a[92:95], v[144:147], 0\n"
    "s_add_u32 s61, s61, 1572864\n"
    "ds_read_u16_d16_hi v201, v201\n"
    "ds_write2_b32 v248, v44, v45 offset0:0 offset1:20\n"
    "v_lshlrev_b32_sdwa v188, 1, v176 dst_sel:DWORD dst_unused:UNUSED_PAD src0_sel:DWORD src1_sel:WORD_1\n"
    "v_lshlrev_b32_sdwa v204, 1, v177 dst_sel:DWORD dst_unused:UNUSED_PAD src0_sel:DWORD src1_sel:WORD_1\n"
    "v_lshlrev_b32_sdwa v189, 1, v178 dst_sel:DWORD dst_unused:UNUSED_PAD src0_sel:DWORD src1_sel:WORD_1\n"
    "v_lshlrev_b32_sdwa v205, 1, v179 dst_sel:DWORD dst_unused:UNUSED_PAD src0_sel:DWORD src1_sel:WORD_1\n"
    "v_lshlrev_b32_sdwa v190, 1, v180 dst_sel:DWORD dst_unused:UNUSED_PAD src0_sel:DWORD src1_sel:WORD_1\n"
    "v_lshlrev_b32_sdwa v206, 1, v181 dst_sel:DWORD dst_unused:UNUSED_PAD src0_sel:DWORD src1_sel:WORD_1\n"
    "v_lshlrev_b32_sdwa v191, 1, v182 dst_sel:DWORD dst_unused:UNUSED_PAD src0_sel:DWORD src1_sel:WORD_1\n"
    "v_fma_mix_f32 v234, v192, v232, 0 op_sel_hi:[1,1,0]\n"
    "v_mfma_f32_16x16x32_bf16 v[172:175], a[140:143], v[148:151], v[172:175]\n"
    "s_waitcnt lgkmcnt(4)\n"
    "ds_read_u16 v186, v186\n"
    "ds_write2_b32 v248, v46, v47 offset0:40 offset1:60\n"
    "v_mfma_f32_16x16x32_bf16 v[172:175], a[240:243], v[152:155], v[172:175]\n"
    "s_add_u32 s62, s62, 1572864\n"
    "v_add_u32_e32 v248, 1360, v248\n"
    "v_or_b32_e32 v199, v199, v207\n"
    "v_lshlrev_b32_sdwa v207, 1, v183 dst_sel:DWORD dst_unused:UNUSED_PAD src0_sel:DWORD src1_sel:WORD_1\n"
    "v_fma_mix_f32 v232, v192, v232, 0 op_sel:[1,1,0] op_sel_hi:[1,1,0]\n"
    "v_mfma_f32_16x16x32_bf16 v[176:179], a[88:91], v[156:159], 0\n"
    "ds_read_b128 v[236:239], v244 offset:20480\n"
    "ds_write2_b32 v248, v48, v49 offset0:0 offset1:20\n"
    "v_cvt_pk_bf16_f32 v192, v234, v232\n"
    "v_mfma_f32_16x16x32_bf16 v[176:179], a[136:139], v[160:163], v[176:179]\n"
    "v_pk_fma_f16 v232, v196, v196, s48\n"
    "v_or_b32_e64 v184, v184, v200\n"
    "v_mfma_f32_16x16x32_bf16 v[176:179], a[52:55], v[164:167], v[176:179]\n"
    "s_add_u32 s63, s63, 1572864\n"
    "ds_read_u16_d16_hi v202, v202\n"
    "ds_write2_b32 v248, v50, v51 offset0:40 offset1:60\n"
    "v_mfma_f32_32x32x1_2b_f32 v[32:63], v254, v254, 0\n"
    "s_waitcnt lgkmcnt(6)\n"
    "v_add_u32_e32 v248, -5440, v248\n"
    "v_pk_mul_f16 v232, v196, v232\n"
    "v_or_b32_e32 v185, v185, v201\n"
    "v_exp_f16_e64 v234, v232\n"
    "v_lshlrev_b32_sdwa v201, 1, v171 dst_sel:DWORD dst_unused:UNUSED_PAD src0_sel:DWORD src1_sel:WORD_1\n"
    "v_exp_f16_sdwa v234, v232 dst_sel:WORD_1 dst_unused:UNUSED_PRESERVE src0_sel:WORD_1\n"
    "v_mov_b32_e64 v254, 0\n"
    "v_pk_fma_f16 v234, v234, s49, s49\n"
    "v_rcp_f16_e64 v232, v234\n"
    "ds_read_b128 v[216:219], v251 offset:10752\n"
    "ds_read_b128 v[220:223], v251 offset:11088\n"
    "v_mfma_f32_16x16x32_bf16 v[180:183], a[92:95], v[156:159], 0\n"
    "s_add_u32 s52, s52, 1572864\n"
    "s_add_u32 s53, s53, 393216\n"
    "ds_read_u16 v187, v187\n"
    "ds_read_b128 a[244:247], v244 offset:22528\n"
    "s_waitcnt vmcnt(3)\n"
    "v_smfmac_f32_32x32x32_bf16 v[128:143], a[248:251], a[224:231], v241\n"
    "s_nop 0\n"
    "ds_read_u16_d16_hi v203, v203\n"
    "ds_read_u16 v188, v188\n"
    "v_rcp_f16_sdwa v232, v234 dst_sel:WORD_1 dst_unused:UNUSED_PRESERVE src0_sel:WORD_1\n"
    "ds_read_u16_d16_hi v204, v204\n"
    "v_mfma_f32_16x16x32_bf16 v[180:183], a[140:143], v[160:163], v[180:183]\n"
    "s_waitcnt lgkmcnt(7)\n"
    "v_fma_mix_f32 v234, v196, v232, 0 op_sel_hi:[1,1,0]\n"
    "v_fma_mix_f32 v232, v196, v232, 0 op_sel:[1,1,0] op_sel_hi:[1,1,0]\n"
    "v_mfma_f32_16x16x32_bf16 v[180:183], a[240:243], v[164:167], v[180:183]\n"
    "s_barrier\n"
    "v_mfma_f32_16x16x32_bf16 v[96:99], a[208:211], v[192:195], v[96:99]\n"
    "ds_read_b128 v[208:211], v250 offset:0\n"
    "ds_read_b128 v[212:215], v250 offset:320\n"
    "v_mfma_f32_16x16x32_bf16 v[100:103], a[212:215], v[192:195], v[100:103]\n"
    "ds_read_u16 v189, v189\n"
    "ds_read_u16_d16_hi v205, v205\n"
    "v_mfma_f32_16x16x32_bf16 v[112:115], a[216:219], v[192:195], v[112:115]\n"
    "v_cvt_pk_bf16_f32 v196, v234, v232\n"
    "v_or_b32_e32 v186, v186, v202\n"
    "v_mfma_f32_16x16x32_bf16 v[116:119], a[220:223], v[192:195], v[116:119]\n"
    "ds_read_u16 v190, v190\n"
    "v_lshlrev_b32_sdwa v202, 1, v173 dst_sel:DWORD dst_unused:UNUSED_PAD src0_sel:DWORD src1_sel:WORD_1\n"
    "v_mfma_f32_16x16x32_bf16 v[128:131], v[236:239], v[192:195], v[128:131]\n"
    "s_waitcnt lgkmcnt(8)\n"
    "v_cvt_pk_bf16_f32 v96, v96, v97\n"
    "v_cvt_pk_bf16_f32 v97, v98, v99\n"
    "v_mfma_f32_16x16x32_bf16 v[132:135], a[244:247], v[192:195], v[132:135]\n"
    "ds_read_u16_d16_hi v206, v206\n"
    "v_cvt_pk_f16_f32 v192, v168, v169\n"
    "v_mfma_f32_16x16x32_bf16 v[104:107], a[208:211], v[196:199], v[104:107]\n"
    "v_lshlrev_b32_sdwa v193, 1, v170 dst_sel:DWORD dst_unused:UNUSED_PAD src0_sel:DWORD src1_sel:WORD_1\n"
    "v_lshlrev_b32_sdwa v194, 1, v172 dst_sel:DWORD dst_unused:UNUSED_PAD src0_sel:DWORD src1_sel:WORD_1\n"
    "v_mfma_f32_16x16x32_bf16 v[108:111], a[212:215], v[196:199], v[108:111]\n"
    "s_waitcnt lgkmcnt(8)\n"
    "ds_read_u16 v191, v191\n"
    "v_lshlrev_b32_sdwa v195, 1, v174 dst_sel:DWORD dst_unused:UNUSED_PAD src0_sel:DWORD src1_sel:WORD_1\n"
    "v_mfma_f32_16x16x32_bf16 v[120:123], a[216:219], v[196:199], v[120:123]\n"
    "v_or_b32_e32 v187, v187, v203\n"
    "v_lshlrev_b32_sdwa v203, 1, v175 dst_sel:DWORD dst_unused:UNUSED_PAD src0_sel:DWORD src1_sel:WORD_1\n"
    "v_mfma_f32_16x16x32_bf16 v[124:127], a[220:223], v[196:199], v[124:127]\n"
    "ds_read_u16_d16_hi v207, v207\n"
    "v_cvt_pk_bf16_f32 v98, v100, v101\n"
    "v_mfma_f32_16x16x32_bf16 v[136:139], v[236:239], v[196:199], v[136:139]\n"
    "s_waitcnt lgkmcnt(7)\n"
    "ds_read_u16 v193, v193\n"
    "v_or_b32_e32 v188, v188, v204\n"
    "s_waitcnt lgkmcnt(7)\n"
    "v_smfmac_f32_16x16x64_bf16 v[32:35], v[216:219], v[208:215], v240\n"
    "s_nop 0\n"
    "v_cvt_pk_bf16_f32 v99, v102, v103\n"
    "v_smfmac_f32_16x16x64_bf16 v[36:39], v[220:223], v[208:215], v240 abid:1\n"
    "ds_read_b128 v[208:211], v250 offset:640\n"
    "ds_read_b128 v[212:215], v250 offset:960\n"
    "v_mfma_f32_16x16x32_bf16 v[140:143], a[244:247], v[196:199], v[140:143]\n"
    "v_cvt_pk_f16_f32 v196, v176, v177\n"
    "v_lshlrev_b32_sdwa v197, 1, v178 dst_sel:DWORD dst_unused:UNUSED_PAD src0_sel:DWORD src1_sel:WORD_1\n"
    "v_mfma_f32_16x16x32_bf16 v[168:171], a[96:99], v[144:147], 0\n"
    "s_nop 0\n"
    "ds_read_b128 v[216:219], v252 offset:10752\n"
    "ds_read_b128 v[220:223], v252 offset:11088\n"
    "v_mfma_f32_16x16x32_bf16 v[168:171], a[144:147], v[148:151], v[168:171]\n"
    "s_waitcnt lgkmcnt(9)\n"
    "v_lshlrev_b32_sdwa v198, 1, v180 dst_sel:DWORD dst_unused:UNUSED_PAD src0_sel:DWORD src1_sel:WORD_1\n"
    "v_lshlrev_b32_sdwa v199, 1, v182 dst_sel:DWORD dst_unused:UNUSED_PAD src0_sel:DWORD src1_sel:WORD_1\n"
    "v_mfma_f32_16x16x32_bf16 v[168:171], a[56:59], v[152:155], v[168:171]\n"
    "ds_read_u16_d16_hi v201, v201\n"
    "v_or_b32_e64 v189, v189, v205\n"
    "v_mfma_f32_16x16x32_bf16 v[172:175], a[100:103], v[144:147], 0\n"
    "ds_read_u16 v194, v194\n"
    "v_lshlrev_b32_sdwa v205, 1, v179 dst_sel:DWORD dst_unused:UNUSED_PAD src0_sel:DWORD src1_sel:WORD_1\n"
    "v_mfma_f32_16x16x32_bf16 v[172:175], a[148:151], v[148:151], v[172:175]\n"
    "v_cvt_pk_bf16_f32 v112, v112, v113\n"
    "s_waitcnt lgkmcnt(9)\n"
    "v_or_b32_e32 v190, v190, v206\n"
    "v_mfma_f32_16x16x32_bf16 v[172:175], a[60:63], v[152:155], v[172:175]\n"
    "ds_read_u16_d16_hi v202, v202\n"
    "v_lshlrev_b32_sdwa v206, 1, v181 dst_sel:DWORD dst_unused:UNUSED_PAD src0_sel:DWORD src1_sel:WORD_1\n"
    "v_mfma_f32_16x16x32_bf16 v[176:179], a[96:99], v[156:159], 0\n"
    "v_cvt_pk_bf16_f32 v113, v114, v115\n"
    "v_cvt_pk_bf16_f32 v114, v116, v117\n"
    "v_mfma_f32_16x16x32_bf16 v[176:179], a[144:147], v[160:163], v[176:179]\n"
    "s_nop 0\n"
    "ds_read_b128 v[236:239], v244 offset:0\n"
    "ds_write_b128 v243, v[96:99] offset:24640\n"
    "v_mfma_f32_16x16x32_bf16 v[176:179], a[56:59], v[164:167], v[176:179]\n"
    "s_waitcnt lgkmcnt(10)\n"
    "v_cvt_pk_bf16_f32 v115, v118, v119\n"
    "v_cvt_pk_bf16_f32 v128, v128, v129\n"
    "v_mfma_f32_16x16x32_bf16 v[96:99], a[0:3], v[184:187], 0\n"
    "v_cvt_pk_bf16_f32 v129, v130, v131\n"
    "v_or_b32_e32 v191, v191, v207\n"
    "v_mfma_f32_16x16x32_bf16 v[100:103], a[4:7], v[184:187], 0\n"
    "s_waitcnt lgkmcnt(6)\n"
    "ds_read_b128 a[252:255], v249 offset:0\n"
    "ds_write_b128 v243, v[112:115] offset:26688\n"
    "v_smfmac_f32_16x16x64_bf16 v[32:35], v[216:219], v[208:215], v240\n"
    "s_waitcnt lgkmcnt(7)\n"
    "v_lshlrev_b32_sdwa v207, 1, v183 dst_sel:DWORD dst_unused:UNUSED_PAD src0_sel:DWORD src1_sel:WORD_1\n"
    "v_smfmac_f32_16x16x64_bf16 v[36:39], v[220:223], v[208:215], v240 abid:1\n"
    "v_cvt_pk_bf16_f32 v130, v132, v133\n"
    "v_mfma_f32_16x16x32_bf16 v[180:183], a[100:103], v[156:159], 0\n"
    "s_waitcnt lgkmcnt(6)\n"
    "ds_read_b128 v[208:211], v250 offset:80\n"
    "ds_read_b128 v[212:215], v250 offset:400\n"
    "ds_read_b128 v[216:219], v251 offset:11424\n"
    "v_or_b32_e64 v193, v193, v201\n"
    "ds_read_b128 v[220:223], v251 offset:11760\n"
    "ds_read_u16 v195, v195\n"
    "v_pk_add_f32 v[224:225], v[224:225], v[32:33]\n"
    "v_pk_add_f32 v[226:227], v[226:227], v[34:35]\n"
    "v_pk_fma_f32 v[228:229], v[32:33], v[32:33], v[228:229]\n"
    "v_pk_fma_f32 v[230:231], v[34:35], v[34:35], v[230:231]\n"
    "v_pk_add_f32 v[224:225], v[224:225], v[36:37]\n"
    "v_pk_add_f32 v[226:227], v[226:227], v[38:39]\n"
    "v_pk_fma_f32 v[228:229], v[36:37], v[36:37], v[228:229]\n"
    "v_pk_fma_f32 v[230:231], v[38:39], v[38:39], v[230:231]\n"
    "v_mfma_f32_16x16x32_bf16 v[180:183], a[148:151], v[160:163], v[180:183]\n"
    "s_waitcnt lgkmcnt(7)\n"
    "ds_read_u16_d16_hi v203, v203\n"
    "ds_read_b128 a[244:247], v244 offset:2048\n"
    "v_mfma_f32_16x16x32_bf16 v[180:183], a[60:63], v[164:167], v[180:183]\n"
    "v_cvt_pk_bf16_f32 v131, v134, v135\n"
    "v_cvt_pk_bf16_f32 v104, v104, v105\n"
    "v_mfma_f32_16x16x32_bf16 v[112:115], a[8:11], v[184:187], 0\n"
    "s_waitcnt lgkmcnt(8)\n"
    "ds_read_u16 v197, v197\n"
    "buffer_store_dwordx4 a[252:255], v255, s[44:47], s57 offen offset:0 sc1\n"
    "v_mfma_f32_16x16x32_bf16 v[116:119], a[12:15], v[184:187], 0\n"
    "ds_read_u16_d16_hi v205, v205\n"
    "v_cvt_pk_bf16_f32 v105, v106, v107\n"
    "v_cvt_pk_bf16_f32 v106, v108, v109\n"
    "ds_write_b128 v243, v[128:131] offset:28736\n"
    "v_mfma_f32_16x16x32_bf16 v[128:131], v[236:239], v[184:187], 0\n"
    "s_waitcnt lgkmcnt(7)\n"
    "v_cvt_pk_bf16_f32 v107, v110, v111\n"
    "v_or_b32_e64 v194, v194, v202\n"
    "v_smfmac_f32_16x16x64_bf16 v[40:43], v[216:219], v[208:215], v240\n"
    "s_waitcnt lgkmcnt(6)\n"
    "ds_read_b128 v[216:219], v252 offset:11424\n"
    "ds_write_b128 v243, v[104:107] offset:25664\n"
    "v_smfmac_f32_16x16x64_bf16 v[44:47], v[220:223], v[208:215], v240 abid:1\n"
    "v_cvt_pk_bf16_f32 v120, v120, v121\n"
    "v_mfma_f32_16x16x32_bf16 v[104:107], a[0:3], v[188:191], 0\n"
    "s_nop 0\n"
    "v_cvt_pk_bf16_f32 v121, v122, v123\n"
    "v_cvt_pk_bf16_f32 v122, v124, v125\n"
    "v_mfma_f32_16x16x32_bf16 v[108:111], a[4:7], v[188:191], 0\n"
    "s_waitcnt lgkmcnt(5)\n"
    "ds_read_b128 v[208:211], v250 offset:720\n"
    "ds_read_b128 v[212:215], v250 offset:1040\n"
    "v_mfma_f32_16x16x32_bf16 v[132:135], a[244:247], v[184:187], 0\n"
    "ds_read_b128 v[220:223], v252 offset:11760\n"
    "ds_read_u16 v198, v198\n"
    "v_cvt_pk_bf16_f32 v123, v126, v127\n"
    "v_pk_fma_f16 v232, v192, v192, s48\n"
    "v_mfma_f32_16x16x32_bf16 v[124:127], a[12:15], v[188:191], 0\n"
    "ds_read_u16_d16_hi v206, v206\n"
    "v_cvt_pk_bf16_f32 v136, v136, v137\n"
    "v_cvt_pk_bf16_f32 v137, v138, v139\n"
    "v_cvt_pk_bf16_f32 v138, v140, v141\n"
    "v_or_b32_e64 v195, v195, v203\n"
    "v_cvt_pk_bf16_f32 v139, v142, v143\n"
    "ds_write_b128 v243, v[120:123] offset:27712\n"
    "v_mfma_f32_16x16x32_bf16 v[120:123], a[8:11], v[188:191], 0\n"
    "s_waitcnt lgkmcnt(8)\n"
    "v_pk_mul_f16 v232, v192, v232\n"
    "v_or_b32_e32 v197, v197, v205\n"
    "v_exp_f16_e64 v234, v232\n"
    "v_mfma_f32_16x16x32_bf16 v[140:143], a[244:247], v[188:191], 0\n"
    "ds_read_u16 v199, v199\n"
    "ds_write_b128 v243, v[136:139] offset:29760\n"
    "v_mfma_f32_16x16x32_bf16 v[136:139], v[236:239], v[188:191], 0\n"
    "v_exp_f16_sdwa v234, v232 dst_sel:WORD_1 dst_unused:UNUSED_PRESERVE src0_sel:WORD_1\n"
    "v_cvt_pk_f16_f32 v184, v168, v169\n"
    "s_waitcnt lgkmcnt(6)\n"
    "v_smfmac_f32_16x16x64_bf16 v[40:43], v[216:219], v[208:215], v240\n"
    "s_waitcnt lgkmcnt(5)\n"
    "ds_read_b128 v[216:219], v251 offset:12096\n"
    "ds_read_u16_d16_hi v207, v207\n"
    "v_smfmac_f32_16x16x64_bf16 v[44:47], v[220:223], v[208:215], v240 abid:1\n"
    "ds_read_b128 v[208:211], v250 offset:160\n"
    "v_pk_fma_f16 v234, v234, s49, s49\n"
    "v_lshlrev_b32_sdwa v185, 1, v170 dst_sel:DWORD dst_unused:UNUSED_PAD src0_sel:DWORD src1_sel:WORD_1\n"
    "ds_read_b128 v[212:215], v250 offset:480\n"
    "v_pk_add_f32 v[224:225], v[224:225], v[40:41]\n"
    "v_pk_add_f32 v[226:227], v[226:227], v[42:43]\n"
    "v_pk_fma_f32 v[228:229], v[40:41], v[40:41], v[228:229]\n"
    "v_pk_fma_f32 v[230:231], v[42:43], v[42:43], v[230:231]\n"
    "v_pk_add_f32 v[224:225], v[224:225], v[44:45]\n"
    "v_pk_add_f32 v[226:227], v[226:227], v[46:47]\n"
    "v_pk_fma_f32 v[228:229], v[44:45], v[44:45], v[228:229]\n"
    "v_pk_fma_f32 v[230:231], v[46:47], v[46:47], v[230:231]\n"
    "ds_read_b128 v[220:223], v251 offset:12432\n"
    "ds_read_b128 a[252:255], v249 offset:2048\n"
    "v_lshlrev_b32_sdwa v201, 1, v171 dst_sel:DWORD dst_unused:UNUSED_PAD src0_sel:DWORD src1_sel:WORD_1\n"
    "v_mfma_f32_16x16x32_bf16 v[168:171], a[104:107], v[144:147], 0\n"
    "s_waitcnt lgkmcnt(8)\n"
    "ds_read_u16 v185, v185\n"
    "ds_read_u16_d16_hi v201, v201\n"
    "v_mfma_f32_16x16x32_bf16 v[168:171], a[152:155], v[148:151], v[168:171]\n"
    "s_nop 0\n"
    "v_lshlrev_b32_sdwa v186, 1, v172 dst_sel:DWORD dst_unused:UNUSED_PAD src0_sel:DWORD src1_sel:WORD_1\n"
    "v_lshlrev_b32_sdwa v202, 1, v173 dst_sel:DWORD dst_unused:UNUSED_PAD src0_sel:DWORD src1_sel:WORD_1\n"
    "v_lshlrev_b32_sdwa v187, 1, v174 dst_sel:DWORD dst_unused:UNUSED_PAD src0_sel:DWORD src1_sel:WORD_1\n"
    "v_lshlrev_b32_sdwa v203, 1, v175 dst_sel:DWORD dst_unused:UNUSED_PAD src0_sel:DWORD src1_sel:WORD_1\n"
    "v_rcp_f16_e64 v232, v234\n"
    "v_mfma_f32_16x16x32_bf16 v[168:171], a[64:67], v[152:155], v[168:171]\n"
    "ds_read_u16 v186, v186\n"
    "v_or_b32_e64 v198, v198, v206\n"
    "v_rcp_f16_sdwa v232, v234 dst_sel:WORD_1 dst_unused:UNUSED_PRESERVE src0_sel:WORD_1\n"
    "v_mfma_f32_16x16x32_bf16 v[172:175], a[108:111], v[144:147], 0\n"
    "ds_read_u16_d16_hi v202, v202\n"
    "v_cvt_pk_f16_f32 v188, v176, v177\n"
    "v_lshlrev_b32_sdwa v189, 1, v178 dst_sel:DWORD dst_unused:UNUSED_PAD src0_sel:DWORD src1_sel:WORD_1\n"
    "v_lshlrev_b32_sdwa v205, 1, v179 dst_sel:DWORD dst_unused:UNUSED_PAD src0_sel:DWORD src1_sel:WORD_1\n"
    "v_lshlrev_b32_sdwa v190, 1, v180 dst_sel:DWORD dst_unused:UNUSED_PAD src0_sel:DWORD src1_sel:WORD_1\n"
    "v_lshlrev_b32_sdwa v206, 1, v181 dst_sel:DWORD dst_unused:UNUSED_PAD src0_sel:DWORD src1_sel:WORD_1\n"
    "v_lshlrev_b32_sdwa v191, 1, v182 dst_sel:DWORD dst_unused:UNUSED_PAD src0_sel:DWORD src1_sel:WORD_1\n"
    "v_fma_mix_f32 v234, v192, v232, 0 op_sel_hi:[1,1,0]\n"
    "v_mfma_f32_16x16x32_bf16 v[172:175], a[156:159], v[148:151], v[172:175]\n"
    "s_waitcnt lgkmcnt(7)\n"
    "ds_read_b128 v[236:239], v244 offset:4096\n"
    "ds_read_u16 v187, v187\n"
    "s_waitcnt lgkmcnt(8)\n"
    "v_smfmac_f32_16x16x64_bf16 v[48:51], v[216:219], v[208:215], v240\n"
    "s_waitcnt lgkmcnt(7)\n"
    "ds_read_b128 v[216:219], v252 offset:12096\n"
    "v_smfmac_f32_16x16x64_bf16 v[52:55], v[220:223], v[208:215], v240 abid:1\n"
    "s_waitcnt lgkmcnt(7)\n"
    "ds_read_b128 v[208:211], v250 offset:800\n"
    "buffer_store_dwordx4 a[252:255], v255, s[44:47], s57 offen offset:64 sc1\n"
    "v_mfma_f32_16x16x32_bf16 v[172:175], a[68:71], v[152:155], v[172:175]\n"
    "ds_read_b128 v[212:215], v250 offset:1120\n"
    "ds_read_b128 v[220:223], v252 offset:12432\n"
    "v_or_b32_e64 v199, v199, v207\n"
    "v_lshlrev_b32_sdwa v207, 1, v183 dst_sel:DWORD dst_unused:UNUSED_PAD src0_sel:DWORD src1_sel:WORD_1\n"
    "v_fma_mix_f32 v232, v192, v232, 0 op_sel:[1,1,0] op_sel_hi:[1,1,0]\n"
    "v_mfma_f32_16x16x32_bf16 v[176:179], a[104:107], v[156:159], 0\n"
    "s_nop 0\n"
    "ds_read_u16_d16_hi v203, v203\n"
    "v_cvt_pk_bf16_f32 v192, v234, v232\n"
    "v_mfma_f32_16x16x32_bf16 v[176:179], a[152:155], v[160:163], v[176:179]\n"
    "s_waitcnt lgkmcnt(9)\n"
    "ds_read_b128 a[244:247], v244 offset:6144\n"
    "v_pk_fma_f16 v232, v196, v196, s48\n"
    "v_mfma_f32_16x16x32_bf16 v[176:179], a[64:67], v[164:167], v[176:179]\n"
    "ds_read_b128 a[252:255], v249 offset:4096\n"
    "v_pk_mul_f16 v232, v196, v232\n"
    "v_mfma_f32_16x16x32_bf16 v[180:183], a[108:111], v[156:159], 0\n"
    "ds_read_u16 v189, v189\n"
    "v_exp_f16_e32 v234, v232\n"
    "v_mfma_f32_16x16x32_bf16 v[180:183], a[156:159], v[160:163], v[180:183]\n"
    "v_exp_f16_sdwa v234, v232 dst_sel:WORD_1 dst_unused:UNUSED_PRESERVE src0_sel:WORD_1\n"
    "v_mfma_f32_16x16x32_bf16 v[180:183], a[68:71], v[164:167], v[180:183]\n"
    "s_waitcnt lgkmcnt(9)\n"
    "ds_read_u16_d16_hi v205, v205\n"
    "v_pk_fma_f16 v234, v234, s49, s49\n"
    "v_mfma_f32_16x16x32_bf16 v[96:99], a[16:19], v[192:195], v[96:99]\n"
    "ds_read_u16 v190, v190\n"
    "v_rcp_f16_e64 v232, v234\n"
    "v_mfma_f32_16x16x32_bf16 v[100:103], a[20:23], v[192:195], v[100:103]\n"
    "v_rcp_f16_sdwa v232, v234 dst_sel:WORD_1 dst_unused:UNUSED_PRESERVE src0_sel:WORD_1\n"
    "v_mfma_f32_16x16x32_bf16 v[112:115], a[24:27], v[192:195], v[112:115]\n"
    "ds_read_u16_d16_hi v206, v206\n"
    "v_or_b32_e64 v185, v185, v201\n"
    "s_waitcnt lgkmcnt(8)\n"
    "v_smfmac_f32_16x16x64_bf16 v[48:51], v[216:219], v[208:215], v240\n"
    "s_waitcnt lgkmcnt(7)\n"
    "ds_read_b128 v[216:219], v251 offset:12768\n"
    "v_smfmac_f32_16x16x64_bf16 v[52:55], v[220:223], v[208:215], v240 abid:1\n"
    "ds_read_b128 v[208:211], v250 offset:240\n"
    "v_mfma_f32_16x16x32_bf16 v[116:119], a[28:31], v[192:195], v[116:119]\n"
    "ds_read_b128 v[212:215], v250 offset:560\n"
    "v_fma_mix_f32 v234, v196, v232, 0 op_sel_hi:[1,1,0]\n"
    "v_mfma_f32_16x16x32_bf16 v[128:131], v[236:239], v[192:195], v[128:131]\n"
    "s_waitcnt lgkmcnt(8)\n"
    "ds_read_b128 v[220:223], v251 offset:13104\n"
    "v_fma_mix_f32 v232, v196, v232, 0 op_sel:[1,1,0] op_sel_hi:[1,1,0]\n"
    "v_or_b32_e32 v186, v186, v202\n"
    "v_cvt_pk_bf16_f32 v196, v234, v232\n"
    "v_pk_fma_f16 v232, v184, v184, s48\n"
    "v_pk_add_f32 v[224:225], v[224:225], v[48:49]\n"
    "v_pk_add_f32 v[226:227], v[226:227], v[50:51]\n"
    "v_pk_fma_f32 v[228:229], v[48:49], v[48:49], v[228:229]\n"
    "v_pk_fma_f32 v[230:231], v[50:51], v[50:51], v[230:231]\n"
    "v_pk_add_f32 v[224:225], v[224:225], v[52:53]\n"
    "v_pk_add_f32 v[226:227], v[226:227], v[54:55]\n"
    "v_pk_fma_f32 v[228:229], v[52:53], v[52:53], v[228:229]\n"
    "v_pk_fma_f32 v[230:231], v[54:55], v[54:55], v[230:231]\n"
    "v_mfma_f32_16x16x32_bf16 v[132:135], a[244:247], v[192:195], v[132:135]\n"
    "s_waitcnt lgkmcnt(8)\n"
    "ds_read_u16 v191, v191\n"
    "buffer_store_dwordx4 a[252:255], v255, s[44:47], s57 offen offset:128 sc1\n"
    "v_mfma_f32_16x16x32_bf16 v[104:107], a[16:19], v[196:199], v[104:107]\n"
    "s_waitcnt lgkmcnt(7)\n"
    "ds_read_u16_d16_hi v207, v207\n"
    "ds_read_b128 a[252:255], v249 offset:1024\n"
    "v_mfma_f32_16x16x32_bf16 v[108:111], a[20:23], v[196:199], v[108:111]\n"
    "ds_read_b128 a[240:243], v244 offset:34816\n"
    "v_or_b32_e64 v187, v187, v203\n"
    "v_mfma_f32_16x16x32_bf16 v[120:123], a[24:27], v[196:199], v[120:123]\n"
    "v_pk_mul_f16 v232, v184, v232\n"
    "v_or_b32_e64 v189, v189, v205\n"
    "v_mfma_f32_16x16x32_bf16 v[124:127], a[28:31], v[196:199], v[124:127]\n"
    "v_exp_f16_e32 v234, v232\n"
    "v_mfma_f32_16x16x32_bf16 v[136:139], v[236:239], v[196:199], v[136:139]\n"
    "s_waitcnt lgkmcnt(7)\n"
    "v_exp_f16_sdwa v234, v232 dst_sel:WORD_1 dst_unused:UNUSED_PRESERVE src0_sel:WORD_1\n"
    "v_mfma_f32_16x16x32_bf16 v[140:143], a[244:247], v[196:199], v[140:143]\n"
    "v_pk_fma_f16 v234, v234, s49, s49\n"
    "v_lshlrev_b32_sdwa v192, 1, v168 dst_sel:DWORD dst_unused:UNUSED_PAD src0_sel:DWORD src1_sel:WORD_1\n"
    "s_waitcnt lgkmcnt(5)\n"
    "v_smfmac_f32_16x16x64_bf16 v[56:59], v[216:219], v[208:215], v240\n"
    "s_waitcnt lgkmcnt(4)\n"
    "ds_read_b128 v[216:219], v252 offset:12768\n"
    "ds_read_u16 v192, v192\n"
    "v_smfmac_f32_16x16x64_bf16 v[60:63], v[220:223], v[208:215], v240 abid:1\n"
    "ds_read_b128 v[208:211], v250 offset:880\n"
    "ds_read_b128 v[212:215], v250 offset:1200\n"
    "ds_read_b128 v[220:223], v252 offset:13104\n"
    "v_lshlrev_b32_sdwa v200, 1, v169 dst_sel:DWORD dst_unused:UNUSED_PAD src0_sel:DWORD src1_sel:WORD_1\n"
    "v_lshlrev_b32_sdwa v193, 1, v170 dst_sel:DWORD dst_unused:UNUSED_PAD src0_sel:DWORD src1_sel:WORD_1\n"
    "v_lshlrev_b32_sdwa v201, 1, v171 dst_sel:DWORD dst_unused:UNUSED_PAD src0_sel:DWORD src1_sel:WORD_1\n"
    "v_mfma_f32_16x16x32_bf16 v[168:171], a[112:115], v[144:147], 0\n"
    "s_nop 0\n"
    "ds_read_u16_d16_hi v200, v200\n"
    "ds_read_u16 v193, v193\n"
    "v_mfma_f32_16x16x32_bf16 v[168:171], a[160:163], v[148:151], v[168:171]\n"
    "s_waitcnt lgkmcnt(8)\n"
    "buffer_store_dwordx4 a[252:255], v255, s[44:47], s88 offen offset:0 sc1\n"
    "ds_read_u16_d16_hi v201, v201\n"
    "v_lshlrev_b32_sdwa v194, 1, v172 dst_sel:DWORD dst_unused:UNUSED_PAD src0_sel:DWORD src1_sel:WORD_1\n"
    "v_lshlrev_b32_sdwa v202, 1, v173 dst_sel:DWORD dst_unused:UNUSED_PAD src0_sel:DWORD src1_sel:WORD_1\n"
    "v_lshlrev_b32_sdwa v195, 1, v174 dst_sel:DWORD dst_unused:UNUSED_PAD src0_sel:DWORD src1_sel:WORD_1\n"
    "v_lshlrev_b32_sdwa v203, 1, v175 dst_sel:DWORD dst_unused:UNUSED_PAD src0_sel:DWORD src1_sel:WORD_1\n"
    "v_rcp_f16_e64 v232, v234\n"
    "v_mfma_f32_16x16x32_bf16 v[168:171], a[72:75], v[152:155], v[168:171]\n"
    "ds_read_u16 v194, v194\n"
    "v_or_b32_e64 v190, v190, v206\n"
    "v_rcp_f16_sdwa v232, v234 dst_sel:WORD_1 dst_unused:UNUSED_PRESERVE src0_sel:WORD_1\n"
    "v_mfma_f32_16x16x32_bf16 v[172:175], a[116:119], v[144:147], 0\n"
    "ds_read_b128 v[236:239], v244 offset:8192\n"
    "v_lshlrev_b32_sdwa v196, 1, v176 dst_sel:DWORD dst_unused:UNUSED_PAD src0_sel:DWORD src1_sel:WORD_1\n"
    "v_lshlrev_b32_sdwa v204, 1, v177 dst_sel:DWORD dst_unused:UNUSED_PAD src0_sel:DWORD src1_sel:WORD_1\n"
    "v_lshlrev_b32_sdwa v197, 1, v178 dst_sel:DWORD dst_unused:UNUSED_PAD src0_sel:DWORD src1_sel:WORD_1\n"
    "v_lshlrev_b32_sdwa v205, 1, v179 dst_sel:DWORD dst_unused:UNUSED_PAD src0_sel:DWORD src1_sel:WORD_1\n"
    "v_lshlrev_b32_sdwa v198, 1, v180 dst_sel:DWORD dst_unused:UNUSED_PAD src0_sel:DWORD src1_sel:WORD_1\n"
    "v_lshlrev_b32_sdwa v206, 1, v181 dst_sel:DWORD dst_unused:UNUSED_PAD src0_sel:DWORD src1_sel:WORD_1\n"
    "v_lshlrev_b32_sdwa v199, 1, v182 dst_sel:DWORD dst_unused:UNUSED_PAD src0_sel:DWORD src1_sel:WORD_1\n"
    "v_fma_mix_f32 v234, v184, v232, 0 op_sel_hi:[1,1,0]\n"
    "v_mfma_f32_16x16x32_bf16 v[172:175], a[164:167], v[148:151], v[172:175]\n"
    "s_waitcnt lgkmcnt(6)\n"
    "ds_read_u16_d16_hi v202, v202\n"
    "ds_read_u16 v195, v195\n"
    "v_smfmac_f32_16x16x64_bf16 v[56:59], v[216:219], v[208:215], v240\n"
    "s_waitcnt lgkmcnt(7)\n"
    "ds_read_b128 a[244:247], v244 offset:10240\n"
    "v_smfmac_f32_16x16x64_bf16 v[60:63], v[220:223], v[208:215], v240 abid:1\n"
    "s_barrier\n"
    "v_mfma_f32_16x16x32_bf16 v[172:175], a[240:243], v[152:155], v[172:175]\n"
    "s_waitcnt lgkmcnt(7)\n"
    "ds_read_u16_d16_hi v203, v203\n"
    "ds_write2_b32 v248, v64, v65 offset0:0 offset1:20\n"
    "v_or_b32_e64 v191, v191, v207\n"
    "v_pk_add_f32 v[224:225], v[224:225], v[56:57]\n"
    "v_pk_add_f32 v[226:227], v[226:227], v[58:59]\n"
    "v_pk_fma_f32 v[228:229], v[56:57], v[56:57], v[228:229]\n"
    "v_pk_fma_f32 v[230:231], v[58:59], v[58:59], v[230:231]\n"
    "v_pk_add_f32 v[224:225], v[224:225], v[60:61]\n"
    "v_pk_add_f32 v[226:227], v[226:227], v[62:63]\n"
    "v_pk_fma_f32 v[228:229], v[60:61], v[60:61], v[228:229]\n"
    "v_pk_fma_f32 v[230:231], v[62:63], v[62:63], v[230:231]\n"
    "v_lshlrev_b32_sdwa v207, 1, v183 dst_sel:DWORD dst_unused:UNUSED_PAD src0_sel:DWORD src1_sel:WORD_1\n"
    "v_fma_mix_f32 v232, v184, v232, 0 op_sel:[1,1,0] op_sel_hi:[1,1,0]\n"
    "v_mfma_f32_16x16x32_bf16 v[176:179], a[112:115], v[156:159], 0\n"
    "ds_read_u16 v196, v196\n"
    "ds_write2_b32 v248, v66, v67 offset0:40 offset1:60\n"
    "v_add_u32_e32 v248, 1360, v248\n"
    "v_cvt_pk_bf16_f32 v184, v234, v232\n"
    "v_mfma_f32_16x16x32_bf16 v[176:179], a[160:163], v[160:163], v[176:179]\n"
    "v_pk_fma_f16 v232, v188, v188, s48\n"
    "v_or_b32_e32 v192, v192, v200\n"
    "v_mfma_f32_16x16x32_bf16 v[176:179], a[72:75], v[164:167], v[176:179]\n"
    "s_waitcnt lgkmcnt(7)\n"
    "ds_read_u16_d16_hi v204, v204\n"
    "s_waitcnt vmcnt(6)\n"
    "ds_write2_b32 v235, v84, v85 offset0:0 offset1:20\n"
    "v_mfma_f32_16x16x32_bf16 v[180:183], a[116:119], v[156:159], 0\n"
    "s_nop 0\n"
    "v_pk_mul_f16 v232, v188, v232\n"
    "v_or_b32_e64 v193, v193, v201\n"
    "v_mfma_f32_16x16x32_bf16 v[180:183], a[164:167], v[160:163], v[180:183]\n"
    "ds_read_u16 v197, v197\n"
    "ds_write2_b32 v235, v86, v87 offset0:40 offset1:60\n"
    "v_add_u32_e32 v235, 1360, v235\n"
    "v_exp_f16_e32 v234, v232\n"
    "v_mfma_f32_16x16x32_bf16 v[180:183], a[240:243], v[164:167], v[180:183]\n"
    "s_waitcnt lgkmcnt(6)\n"
    "ds_read_b128 a[252:255], v249 offset:3072\n"
    "ds_write2_b32 v248, v68, v69 offset0:0 offset1:20\n"
    "v_exp_f16_sdwa v234, v232 dst_sel:WORD_1 dst_unused:UNUSED_PRESERVE src0_sel:WORD_1\n"
    "v_lshlrev_b32_sdwa v201, 1, v171 dst_sel:DWORD dst_unused:UNUSED_PAD src0_sel:DWORD src1_sel:WORD_1\n"
    "v_pk_fma_f16 v234, v234, s49, s49\n"
    "v_mfma_f32_16x16x32_bf16 v[96:99], a[32:35], v[184:187], v[96:99]\n"
    "ds_read_u16_d16_hi v205, v205\n"
    "ds_write2_b32 v248, v70, v71 offset0:40 offset1:60\n"
    "v_mfma_f32_16x16x32_bf16 v[100:103], a[36:39], v[184:187], v[100:103]\n"
    "v_add_u32_e32 v248, 1360, v248\n"
    "v_rcp_f16_e32 v232, v234\n"
    "v_mfma_f32_16x16x32_bf16 v[112:115], a[40:43], v[184:187], v[112:115]\n"
    "ds_read_u16 v198, v198\n"
    "ds_read_u16_d16_hi v206, v206\n"
    "v_rcp_f16_sdwa v232, v234 dst_sel:WORD_1 dst_unused:UNUSED_PRESERVE src0_sel:WORD_1\n"
    "v_mfma_f32_16x16x32_bf16 v[116:119], a[44:47], v[184:187], v[116:119]\n"
    "s_waitcnt lgkmcnt(8)\n"
    "ds_read_u16 v199, v199\n"
    "ds_read_u16_d16_hi v207, v207\n"
    "v_fma_mix_f32 v234, v188, v232, 0 op_sel_hi:[1,1,0]\n"
    "v_mfma_f32_16x16x32_bf16 v[128:131], v[236:239], v[184:187], v[128:131]\n"
    "v_fma_mix_f32 v232, v188, v232, 0 op_sel:[1,1,0] op_sel_hi:[1,1,0]\n"
    "v_or_b32_e32 v194, v194, v202\n"
    "v_mfma_f32_16x16x32_bf16 v[132:135], a[244:247], v[184:187], v[132:135]\n"
    "s_waitcnt lgkmcnt(6)\n"
    "ds_read_b128 a[240:243], v244 offset:38912\n"
    "buffer_store_dwordx4 a[252:255], v255, s[44:47], s88 offen offset:64 sc1\n"
    "v_cvt_pk_bf16_f32 v188, v234, v232\n"
    "v_cvt_pk_f16_f32 v184, v168, v169\n"
    "v_lshlrev_b32_sdwa v185, 1, v170 dst_sel:DWORD dst_unused:UNUSED_PAD src0_sel:DWORD src1_sel:WORD_1\n"
    "v_mfma_f32_16x16x32_bf16 v[104:107], a[32:35], v[188:191], v[104:107]\n"
    "ds_read_u16 v185, v185\n"
    "s_waitcnt vmcnt(6)\n"
    "ds_write2_b32 v235, v88, v89 offset0:0 offset1:20\n"
    "v_mfma_f32_16x16x32_bf16 v[108:111], a[36:39], v[188:191], v[108:111]\n"
    "s_waitcnt lgkmcnt(7)\n"
    "v_lshlrev_b32_sdwa v186, 1, v172 dst_sel:DWORD dst_unused:UNUSED_PAD src0_sel:DWORD src1_sel:WORD_1\n"
    "v_lshlrev_b32_sdwa v202, 1, v173 dst_sel:DWORD dst_unused:UNUSED_PAD src0_sel:DWORD src1_sel:WORD_1\n"
    "v_mfma_f32_16x16x32_bf16 v[120:123], a[40:43], v[188:191], v[120:123]\n"
    "ds_read_b128 a[252:255], v249 offset:5120\n"
    "ds_write2_b32 v235, v90, v91 offset0:40 offset1:60\n"
    "v_mfma_f32_16x16x32_bf16 v[124:127], a[44:47], v[188:191], v[124:127]\n"
    "v_add_u32_e32 v235, -1360, v235\n"
    "v_lshlrev_b32_sdwa v187, 1, v174 dst_sel:DWORD dst_unused:UNUSED_PAD src0_sel:DWORD src1_sel:WORD_1\n"
    "v_mfma_f32_16x16x32_bf16 v[136:139], v[236:239], v[188:191], v[136:139]\n"
    "ds_read_u16_d16_hi v201, v201\n"
    "ds_write2_b32 v248, v72, v73 offset0:0 offset1:20\n"
    "v_mfma_f32_16x16x32_bf16 v[140:143], a[244:247], v[188:191], v[140:143]\n"
    "s_waitcnt lgkmcnt(9)\n"
    "v_cvt_pk_f16_f32 v188, v176, v177\n"
    "v_lshlrev_b32_sdwa v189, 1, v178 dst_sel:DWORD dst_unused:UNUSED_PAD src0_sel:DWORD src1_sel:WORD_1\n"
    "v_mfma_f32_16x16x32_bf16 v[168:171], a[120:123], v[144:147], 0\n"
    "ds_read_u16 v186, v186\n"
    "ds_write2_b32 v248, v74, v75 offset0:40 offset1:60\n"
    "v_mfma_f32_16x16x32_bf16 v[168:171], a[168:171], v[148:151], v[168:171]\n"
    "s_waitcnt lgkmcnt(8)\n"
    "v_add_u32_e32 v248, 1360, v248\n"
    "v_lshlrev_b32_sdwa v190, 1, v180 dst_sel:DWORD dst_unused:UNUSED_PAD src0_sel:DWORD src1_sel:WORD_1\n"
    "v_mfma_f32_16x16x32_bf16 v[168:171], a[76:79], v[152:155], v[168:171]\n"
    "ds_read_u16_d16_hi v202, v202\n"
    "ds_read_b128 v[236:239], v244 offset:12288\n"
    "v_lshlrev_b32_sdwa v191, 1, v182 dst_sel:DWORD dst_unused:UNUSED_PAD src0_sel:DWORD src1_sel:WORD_1\n"
    "v_or_b32_e64 v195, v195, v203\n"
    "v_lshlrev_b32_sdwa v203, 1, v175 dst_sel:DWORD dst_unused:UNUSED_PAD src0_sel:DWORD src1_sel:WORD_1\n"
    "v_mfma_f32_16x16x32_bf16 v[172:175], a[124:127], v[144:147], 0\n"
    "ds_read_u16 v187, v187\n"
    "v_or_b32_e32 v196, v196, v204\n"
    "v_mfma_f32_16x16x32_bf16 v[172:175], a[172:175], v[148:151], v[172:175]\n"
    "s_waitcnt lgkmcnt(7)\n"
    "ds_read_u16_d16_hi v203, v203\n"
    "buffer_store_dwordx4 a[252:255], v255, s[44:47], s88 offen offset:128 sc1\n"
    "v_mfma_f32_16x16x32_bf16 v[172:175], a[240:243], v[152:155], v[172:175]\n"
    "ds_read_b128 a[244:247], v244 offset:14336\n"
    "ds_write2_b32 v248, v76, v77 offset0:0 offset1:20\n"
    "v_or_b32_e64 v197, v197, v205\n"
    "v_lshlrev_b32_sdwa v205, 1, v179 dst_sel:DWORD dst_unused:UNUSED_PAD src0_sel:DWORD src1_sel:WORD_1\n"
    "v_mfma_f32_16x16x32_bf16 v[176:179], a[120:123], v[156:159], 0\n"
    "s_waitcnt lgkmcnt(8)\n"
    "v_or_b32_e32 v198, v198, v206\n"
    "v_lshlrev_b32_sdwa v206, 1, v181 dst_sel:DWORD dst_unused:UNUSED_PAD src0_sel:DWORD src1_sel:WORD_1\n"
    "v_mfma_f32_16x16x32_bf16 v[176:179], a[168:171], v[160:163], v[176:179]\n"
    "ds_read_u16 v189, v189\n"
    "ds_write2_b32 v248, v78, v79 offset0:40 offset1:60\n"
    "v_mfma_f32_16x16x32_bf16 v[176:179], a[76:79], v[164:167], v[176:179]\n"
    "v_add_u32_e32 v248, 1360, v248\n"
    "v_or_b32_e64 v199, v199, v207\n"
    "v_mfma_f32_16x16x32_bf16 v[96:99], a[176:179], v[192:195], v[96:99]\n"
    "ds_read_u16_d16_hi v205, v205\n"
    "ds_read_u16 v190, v190\n"
    "v_mfma_f32_16x16x32_bf16 v[100:103], a[180:183], v[192:195], v[100:103]\n"
    "v_lshlrev_b32_sdwa v207, 1, v183 dst_sel:DWORD dst_unused:UNUSED_PAD src0_sel:DWORD src1_sel:WORD_1\n"
    "v_or_b32_e32 v185, v185, v201\n"
    "v_mfma_f32_16x16x32_bf16 v[180:183], a[124:127], v[156:159], 0\n"
    "s_waitcnt lgkmcnt(8)\n"
    "ds_read_b128 v[144:147], v243 offset:0\n"
    "v_or_b32_e64 v186, v186, v202\n"
    "v_mfma_f32_16x16x32_bf16 v[180:183], a[172:175], v[160:163], v[180:183]\n"
    "ds_read_u16_d16_hi v206, v206\n"
    "v_pk_fma_f16 v232, v184, v184, s48\n"
    "v_mfma_f32_16x16x32_bf16 v[180:183], a[240:243], v[164:167], v[180:183]\n"
    "v_pk_mul_f16 v232, v184, v232\n"
    "v_lshlrev_b32_sdwa v201, 1, v171 dst_sel:DWORD dst_unused:UNUSED_PAD src0_sel:DWORD src1_sel:WORD_1\n"
    "v_mfma_f32_16x16x32_bf16 v[112:115], a[184:187], v[192:195], v[112:115]\n"
    "ds_read_b128 v[148:151], v243 offset:1024\n"
    "v_exp_f16_e32 v234, v232\n"
    "v_mfma_f32_16x16x32_bf16 v[116:119], a[188:191], v[192:195], v[116:119]\n"
    "s_waitcnt lgkmcnt(7)\n"
    "ds_read_u16 v191, v191\n"
    "v_or_b32_e64 v187, v187, v203\n"
    "v_mfma_f32_16x16x32_bf16 v[128:131], v[236:239], v[192:195], v[128:131]\n"
    "v_exp_f16_sdwa v234, v232 dst_sel:WORD_1 dst_unused:UNUSED_PRESERVE src0_sel:WORD_1\n"
    "v_mfma_f32_16x16x32_bf16 v[132:135], a[244:247], v[192:195], v[132:135]\n"
    "ds_read_b128 v[152:155], v243 offset:2048\n"
    "ds_write2_b32 v248, v80, v81 offset0:0 offset1:20\n"
    "v_mfma_f32_16x16x32_bf16 v[104:107], a[176:179], v[196:199], v[104:107]\n"
    "v_pk_fma_f16 v234, v234, s49, s49\n"
    "v_cvt_pk_f16_f32 v192, v168, v169\n"
    "v_mfma_f32_16x16x32_bf16 v[108:111], a[180:183], v[196:199], v[108:111]\n"
    "ds_read_u16_d16_hi v207, v207\n"
    "v_lshlrev_b32_sdwa v193, 1, v170 dst_sel:DWORD dst_unused:UNUSED_PAD src0_sel:DWORD src1_sel:WORD_1\n"
    "v_mfma_f32_16x16x32_bf16 v[120:123], a[184:187], v[196:199], v[120:123]\n"
    "ds_read_b128 v[156:159], v243 offset:3072\n"
    "v_lshlrev_b32_sdwa v194, 1, v172 dst_sel:DWORD dst_unused:UNUSED_PAD src0_sel:DWORD src1_sel:WORD_1\n"
    "v_mfma_f32_16x16x32_bf16 v[124:127], a[188:191], v[196:199], v[124:127]\n"
    "s_waitcnt lgkmcnt(9)\n"
    "ds_read_b128 v[160:163], v243 offset:4096\n"
    "v_or_b32_e32 v189, v189, v205\n"
    "v_mfma_f32_16x16x32_bf16 v[136:139], v[236:239], v[196:199], v[136:139]\n"
    "s_nop 0\n"
    "ds_read_b128 v[164:167], v243 offset:5120\n"
    "v_lshlrev_b32_sdwa v202, 1, v173 dst_sel:DWORD dst_unused:UNUSED_PAD src0_sel:DWORD src1_sel:WORD_1\n"
    "v_mfma_f32_16x16x32_bf16 v[140:143], a[244:247], v[196:199], v[140:143]\n"
    "s_waitcnt lgkmcnt(9)\n"
    "ds_read_b128 a[240:243], v244 offset:32768\n"
    "buffer_load_dwordx4 a[232:235], v245, s[40:43], s58 offen offset:64\n"
    "buffer_load_dwordx4 a[224:227], v245, s[40:43], s58 offen offset:0\n"
    "v_mfma_f32_16x16x32_bf16 v[168:171], a[80:83], v[144:147], 0\n"
    "ds_read_u16 v193, v193\n"
    "v_lshlrev_b32_sdwa v195, 1, v174 dst_sel:DWORD dst_unused:UNUSED_PAD src0_sel:DWORD src1_sel:WORD_1\n"
    "v_lshlrev_b32_sdwa v203, 1, v175 dst_sel:DWORD dst_unused:UNUSED_PAD src0_sel:DWORD src1_sel:WORD_1\n"
    "s_waitcnt lgkmcnt(9)\n"
    "v_mfma_f32_16x16x32_bf16 v[168:171], a[128:131], v[148:151], v[168:171]\n"
    "s_nop 0\n"
    "ds_read_u16_d16_hi v201, v201\n"
    "v_rcp_f16_e64 v232, v234\n"
    "v_or_b32_e32 v190, v190, v206\n"
    "v_rcp_f16_sdwa v232, v234 dst_sel:WORD_1 dst_unused:UNUSED_PRESERVE src0_sel:WORD_1\n"
    "v_mfma_f32_16x16x32_bf16 v[172:175], a[84:87], v[144:147], 0\n"
    "s_waitcnt lgkmcnt(8)\n"
    "ds_read_u16 v194, v194\n"
    "v_cvt_pk_f16_f32 v196, v176, v177\n"
    "v_mfma_f32_16x16x32_bf16 v[168:171], a[48:51], v[152:155], v[168:171]\n"
    "ds_read_u16_d16_hi v202, v202\n"
    "v_lshlrev_b32_sdwa v197, 1, v178 dst_sel:DWORD dst_unused:UNUSED_PAD src0_sel:DWORD src1_sel:WORD_1\n"
    "v_mfma_f32_16x16x32_bf16 v[172:175], a[132:135], v[148:151], v[172:175]\n"
    "v_lshlrev_b32_sdwa v205, 1, v179 dst_sel:DWORD dst_unused:UNUSED_PAD src0_sel:DWORD src1_sel:WORD_1\n"
    "v_lshlrev_b32_sdwa v198, 1, v180 dst_sel:DWORD dst_unused:UNUSED_PAD src0_sel:DWORD src1_sel:WORD_1\n"
    "v_lshlrev_b32_sdwa v206, 1, v181 dst_sel:DWORD dst_unused:UNUSED_PAD src0_sel:DWORD src1_sel:WORD_1\n"
    "v_lshlrev_b32_sdwa v199, 1, v182 dst_sel:DWORD dst_unused:UNUSED_PAD src0_sel:DWORD src1_sel:WORD_1\n"
    "v_fma_mix_f32 v234, v184, v232, 0 op_sel_hi:[1,1,0]\n"
    "v_fma_mix_f32 v232, v184, v232, 0 op_sel:[1,1,0] op_sel_hi:[1,1,0]\n"
    "v_lshlrev_b32_sdwa v200, 1, v169 dst_sel:DWORD dst_unused:UNUSED_PAD src0_sel:DWORD src1_sel:WORD_1\n"
    "s_waitcnt lgkmcnt(7)\n"
    "v_mfma_f32_16x16x32_bf16 v[176:179], a[80:83], v[156:159], 0\n"
    "s_nop 0\n"
    "ds_read_b128 v[236:239], v244 offset:16384\n"
    "ds_write2_b32 v248, v82, v83 offset0:40 offset1:60\n"
    "v_mfma_f32_32x32x1_2b_f32 v[64:95], v254, v254, 0\n"
    "v_add_u32_e32 v248, -5440, v248\n"
    "v_or_b32_e64 v191, v191, v207\n"
    "v_lshlrev_b32_sdwa v207, 1, v183 dst_sel:DWORD dst_unused:UNUSED_PAD src0_sel:DWORD src1_sel:WORD_1\n"
    "v_cvt_pk_bf16_f32 v184, v234, v232\n"
    "v_pk_fma_f16 v232, v188, v188, s48\n"
    "v_lshlrev_b32_e64 v254, 3, v244\n"
    "v_pk_mul_f16 v232, v188, v232\n"
    "v_exp_f16_e64 v234, v232\n"
    "ds_read_u16 v195, v195\n"
    "ds_read_b128 v[216:219], v251 offset:21504\n"
    "v_exp_f16_sdwa v234, v232 dst_sel:WORD_1 dst_unused:UNUSED_PRESERVE src0_sel:WORD_1\n"
    "s_waitcnt lgkmcnt(7)\n"
    "v_mfma_f32_16x16x32_bf16 v[172:175], a[240:243], v[152:155], v[172:175]\n"
    "ds_read_b128 v[220:223], v251 offset:21840\n"
    "ds_read_u16_d16_hi v203, v203\n"
    "v_mfma_f32_16x16x32_bf16 v[176:179], a[128:131], v[160:163], v[176:179]\n"
    "s_waitcnt lgkmcnt(8)\n"
    "ds_read_b128 a[244:247], v244 offset:18432\n"
    "ds_read_u16 v197, v197\n"
    "v_mfma_f32_16x16x32_bf16 v[176:179], a[48:51], v[164:167], v[176:179]\n"
    "s_mov_b32 exec_hi, 0\n"
    "buffer_load_dword v233, v254, s[72:75], s53 offen sc1\n"
    "s_mov_b32 exec_hi, -1\n"
    "v_mfma_f32_16x16x32_bf16 v[180:183], a[84:87], v[156:159], 0\n"
    "s_add_u32 s53, s53, 393216\n"
    "ds_read_u16_d16_hi v205, v205\n"
    "v_pk_fma_f16 v234, v234, s49, s49\n"
    "v_mfma_f32_16x16x32_bf16 v[180:183], a[132:135], v[160:163], v[180:183]\n"
    "v_rcp_f16_e32 v232, v234\n"
    "v_mfma_f32_16x16x32_bf16 v[180:183], a[240:243], v[164:167], v[180:183]\n"
    "s_waitcnt lgkmcnt(7)\n"
    "ds_read_u16 v198, v198\n"
    "v_rcp_f16_sdwa v232, v234 dst_sel:WORD_1 dst_unused:UNUSED_PRESERVE src0_sel:WORD_1\n"
    "v_mfma_f32_16x16x32_bf16 v[96:99], a[192:195], v[184:187], v[96:99]\n"
    "s_barrier\n"
    "v_mfma_f32_16x16x32_bf16 v[100:103], a[196:199], v[184:187], v[100:103]\n"
    "s_nop 0\n"
    "ds_read_b128 v[208:211], v250 offset:0\n"
    "ds_read_b128 v[212:215], v250 offset:320\n"
    "v_mfma_f32_16x16x32_bf16 v[112:115], a[200:203], v[184:187], v[112:115]\n"
    "ds_read_u16_d16_hi v206, v206\n"
    "v_or_b32_e64 v193, v193, v201\n"
    "v_mfma_f32_16x16x32_bf16 v[116:119], a[204:207], v[184:187], v[116:119]\n"
    "ds_read_u16 v199, v199\n"
    "v_fma_mix_f32 v234, v188, v232, 0 op_sel_hi:[1,1,0]\n"
    "v_mfma_f32_16x16x32_bf16 v[128:131], v[236:239], v[184:187], v[128:131]\n"
    "v_fma_mix_f32 v232, v188, v232, 0 op_sel:[1,1,0] op_sel_hi:[1,1,0]\n"
    "v_or_b32_e32 v194, v194, v202\n"
    "s_waitcnt lgkmcnt(7)\n"
    "v_mfma_f32_16x16x32_bf16 v[132:135], a[244:247], v[184:187], v[132:135]\n"
    "buffer_load_dwordx4 a[236:239], v245, s[40:43], s89 offen offset:64\n"
    "buffer_load_dwordx4 a[228:231], v245, s[40:43], s89 offen offset:0\n"
    "ds_read_u16_d16_hi v207, v207\n"
    "v_cvt_pk_bf16_f32 v188, v234, v232\n"
    "v_pk_fma_f16 v232, v192, v192, s48\n"
    "v_or_b32_e64 v195, v195, v203\n"
    "v_mfma_f32_16x16x32_bf16 v[104:107], a[192:195], v[188:191], v[104:107]\n"
    "ds_read_b128 a[240:243], v244 offset:36864\n"
    "ds_read_u16_d16_hi v200, v200\n"
    "v_mfma_f32_16x16x32_bf16 v[108:111], a[196:199], v[188:191], v[108:111]\n"
    "v_pk_mul_f16 v232, v192, v232\n"
    "v_lshlrev_b32_sdwa v184, 1, v168 dst_sel:DWORD dst_unused:UNUSED_PAD src0_sel:DWORD src1_sel:WORD_1\n"
    "v_mfma_f32_16x16x32_bf16 v[120:123], a[200:203], v[188:191], v[120:123]\n"
    "s_waitcnt lgkmcnt(8)\n"
    "ds_read_u16 v184, v184\n"
    "v_exp_f16_e32 v234, v232\n"
    "v_mfma_f32_16x16x32_bf16 v[124:127], a[204:207], v[188:191], v[124:127]\n"
    "v_exp_f16_sdwa v234, v232 dst_sel:WORD_1 dst_unused:UNUSED_PRESERVE src0_sel:WORD_1\n"
    "v_mfma_f32_16x16x32_bf16 v[136:139], v[236:239], v[188:191], v[136:139]\n"
    "ds_read_b128 v[236:239], v244 offset:20480\n"
    "v_or_b32_e32 v197, v197, v205\n"
    "s_waitcnt lgkmcnt(7)\n"
    "v_smfmac_f32_16x16x64_bf16 v[64:67], v[216:219], v[208:215], v240\n"
    "ds_read_b128 v[216:219], v252 offset:21504\n"
    "v_smfmac_f32_16x16x64_bf16 v[68:71], v[220:223], v[208:215], v240 abid:1\n"
    "s_nop 0\n"
    "ds_read_b128 v[208:211], v250 offset:640\n"
    "ds_read_b128 v[212:215], v250 offset:960\n"
    "v_mfma_f32_16x16x32_bf16 v[140:143], a[244:247], v[188:191], v[140:143]\n"
    "s_waitcnt lgkmcnt(9)\n"
    "ds_read_b128 v[220:223], v252 offset:21840\n"
    "v_pk_fma_f16 v234, v234, s49, s49\n"
    "v_lshlrev_b32_sdwa v185, 1, v170 dst_sel:DWORD dst_unused:UNUSED_PAD src0_sel:DWORD src1_sel:WORD_1\n"
    "v_lshlrev_b32_sdwa v201, 1, v171 dst_sel:DWORD dst_unused:UNUSED_PAD src0_sel:DWORD src1_sel:WORD_1\n"
    "v_mfma_f32_16x16x32_bf16 v[168:171], a[88:91], v[144:147], 0\n"
    "ds_read_u16 v185, v185\n"
    "v_lshlrev_b32_sdwa v186, 1, v172 dst_sel:DWORD dst_unused:UNUSED_PAD src0_sel:DWORD src1_sel:WORD_1\n"
    "v_mfma_f32_16x16x32_bf16 v[168:171], a[136:139], v[148:151], v[168:171]\n"
    "ds_read_u16_d16_hi v201, v201\n"
    "v_lshlrev_b32_sdwa v202, 1, v173 dst_sel:DWORD dst_unused:UNUSED_PAD src0_sel:DWORD src1_sel:WORD_1\n"
    "v_lshlrev_b32_sdwa v187, 1, v174 dst_sel:DWORD dst_unused:UNUSED_PAD src0_sel:DWORD src1_sel:WORD_1\n"
    "v_lshlrev_b32_sdwa v203, 1, v175 dst_sel:DWORD dst_unused:UNUSED_PAD src0_sel:DWORD src1_sel:WORD_1\n"
    "v_rcp_f16_e32 v232, v234\n"
    "v_mfma_f32_16x16x32_bf16 v[168:171], a[52:55], v[152:155], v[168:171]\n"
    "s_waitcnt lgkmcnt(9)\n"
    "ds_read_u16 v186, v186\n"
    "v_or_b32_e64 v198, v198, v206\n"
    "v_rcp_f16_sdwa v232, v234 dst_sel:WORD_1 dst_unused:UNUSED_PRESERVE src0_sel:WORD_1\n"
    "v_mfma_f32_16x16x32_bf16 v[172:175], a[92:95], v[144:147], 0\n"
    "ds_read_u16_d16_hi v202, v202\n"
    "v_lshlrev_b32_sdwa v188, 1, v176 dst_sel:DWORD dst_unused:UNUSED_PAD src0_sel:DWORD src1_sel:WORD_1\n"
    "v_mfma_f32_16x16x32_bf16 v[172:175], a[140:143], v[148:151], v[172:175]\n"
    "v_lshlrev_b32_sdwa v204, 1, v177 dst_sel:DWORD dst_unused:UNUSED_PAD src0_sel:DWORD src1_sel:WORD_1\n"
    "v_lshlrev_b32_sdwa v189, 1, v178 dst_sel:DWORD dst_unused:UNUSED_PAD src0_sel:DWORD src1_sel:WORD_1\n"
    "v_lshlrev_b32_sdwa v205, 1, v179 dst_sel:DWORD dst_unused:UNUSED_PAD src0_sel:DWORD src1_sel:WORD_1\n"
    "v_lshlrev_b32_sdwa v190, 1, v180 dst_sel:DWORD dst_unused:UNUSED_PAD src0_sel:DWORD src1_sel:WORD_1\n"
    "v_lshlrev_b32_sdwa v206, 1, v181 dst_sel:DWORD dst_unused:UNUSED_PAD src0_sel:DWORD src1_sel:WORD_1\n"
    "v_lshlrev_b32_sdwa v191, 1, v182 dst_sel:DWORD dst_unused:UNUSED_PAD src0_sel:DWORD src1_sel:WORD_1\n"
    "v_fma_mix_f32 v234, v192, v232, 0 op_sel_hi:[1,1,0]\n"
    "v_mfma_f32_16x16x32_bf16 v[172:175], a[240:243], v[152:155], v[172:175]\n"
    "s_waitcnt lgkmcnt(7)\n"
    "ds_read_u16 v187, v187\n"
    "ds_read_b128 a[244:247], v244 offset:22528\n"
    "v_or_b32_e32 v199, v199, v207\n"
    "v_lshlrev_b32_sdwa v207, 1, v183 dst_sel:DWORD dst_unused:UNUSED_PAD src0_sel:DWORD src1_sel:WORD_1\n"
    "v_fma_mix_f32 v232, v192, v232, 0 op_sel:[1,1,0] op_sel_hi:[1,1,0]\n"
    "s_waitcnt lgkmcnt(7)\n"
    "v_smfmac_f32_16x16x64_bf16 v[64:67], v[216:219], v[208:215], v240\n"
    "s_waitcnt lgkmcnt(6)\n"
    "ds_read_b128 v[216:219], v251 offset:22176\n"
    "ds_read_u16_d16_hi v203, v203\n"
    "v_smfmac_f32_16x16x64_bf16 v[68:71], v[220:223], v[208:215], v240 abid:1\n"
    "v_cvt_pk_bf16_f32 v192, v234, v232\n"
    "v_mfma_f32_16x16x32_bf16 v[176:179], a[88:91], v[156:159], 0\n"
    "ds_read_b128 v[208:211], v250 offset:80\n"
    "v_pk_fma_f16 v232, v196, v196, s48\n"
    "v_or_b32_e64 v184, v184, v200\n"
    "v_pk_mul_f16 v232, v196, v232\n"
    "v_lshlrev_b32_e64 v254, 3, v244\n"
    "v_pk_add_f32 v[224:225], v[224:225], v[64:65]\n"
    "v_pk_add_f32 v[226:227], v[226:227], v[66:67]\n"
    "v_pk_fma_f32 v[228:229], v[64:65], v[64:65], v[228:229]\n"
    "v_pk_fma_f32 v[230:231], v[66:67], v[66:67], v[230:231]\n"
    "v_pk_add_f32 v[224:225], v[224:225], v[68:69]\n"
    "v_pk_add_f32 v[226:227], v[226:227], v[70:71]\n"
    "v_pk_fma_f32 v[228:229], v[68:69], v[68:69], v[228:229]\n"
    "v_pk_fma_f32 v[230:231], v[70:71], v[70:71], v[230:231]\n"
    "v_mfma_f32_16x16x32_bf16 v[176:179], a[136:139], v[160:163], v[176:179]\n"
    "s_waitcnt lgkmcnt(7)\n"
    "ds_read_b128 v[212:215], v250 offset:400\n"
    "ds_read_b128 v[220:223], v251 offset:22512\n"
    "v_mfma_f32_16x16x32_bf16 v[176:179], a[52:55], v[164:167], v[176:179]\n"
    "s_nop 0\n"
    "ds_read_u16 v188, v188\n"
    "ds_read_u16_d16_hi v204, v204\n"
    "v_mfma_f32_16x16x32_bf16 v[180:183], a[92:95], v[156:159], 0\n"
    "v_exp_f16_e32 v234, v232\n"
    "v_mfma_f32_16x16x32_bf16 v[180:183], a[140:143], v[160:163], v[180:183]\n"
    "s_waitcnt lgkmcnt(9)\n"
    "ds_read_u16 v189, v189\n"
    "v_exp_f16_sdwa v234, v232 dst_sel:WORD_1 dst_unused:UNUSED_PRESERVE src0_sel:WORD_1\n"
    "v_mfma_f32_16x16x32_bf16 v[180:183], a[240:243], v[164:167], v[180:183]\n"
    "ds_read_u16_d16_hi v205, v205\n"
    "v_pk_fma_f16 v234, v234, s49, s49\n"
    "v_mfma_f32_16x16x32_bf16 v[96:99], a[208:211], v[192:195], v[96:99]\n"
    "v_rcp_f16_e64 v232, v234\n"
    "v_mfma_f32_16x16x32_bf16 v[100:103], a[212:215], v[192:195], v[100:103]\n"
    "ds_read_u16 v190, v190\n"
    "v_rcp_f16_sdwa v232, v234 dst_sel:WORD_1 dst_unused:UNUSED_PRESERVE src0_sel:WORD_1\n"
    "v_mfma_f32_16x16x32_bf16 v[112:115], a[216:219], v[192:195], v[112:115]\n"
    "ds_read_u16_d16_hi v206, v206\n"
    "v_fma_mix_f32 v234, v196, v232, 0 op_sel_hi:[1,1,0]\n"
    "v_mfma_f32_16x16x32_bf16 v[116:119], a[220:223], v[192:195], v[116:119]\n"
    "s_waitcnt lgkmcnt(8)\n"
    "v_fma_mix_f32 v232, v196, v232, 0 op_sel:[1,1,0] op_sel_hi:[1,1,0]\n"
    "v_or_b32_e32 v185, v185, v201\n"
    "s_waitcnt lgkmcnt(7)\n"
    "v_smfmac_f32_16x16x64_bf16 v[72:75], v[216:219], v[208:215], v240\n"
    "s_waitcnt lgkmcnt(6)\n"
    "ds_read_b128 v[216:219], v252 offset:22176\n"
    "s_mov_b32 exec_hi, 0\n"
    "buffer_load_dword v233, v254, s[72:75], s53 offen sc1\n"
    "s_mov_b32 exec_hi, -1\n"
    "v_smfmac_f32_16x16x64_bf16 v[76:79], v[220:223], v[208:215], v240 abid:1\n"
    "s_add_u32 s53, s53, 393216\n"
    "ds_read_b128 v[208:211], v250 offset:720\n"
    "ds_read_b128 v[212:215], v250 offset:1040\n"
    "v_mfma_f32_16x16x32_bf16 v[128:131], v[236:239], v[192:195], v[128:131]\n"
    "ds_read_b128 v[220:223], v252 offset:22512\n"
    "v_cvt_pk_bf16_f32 v196, v234, v232\n"
    "v_mfma_f32_16x16x32_bf16 v[132:135], a[244:247], v[192:195], v[132:135]\n"
    "s_nop 0\n"
    "ds_read_u16 v191, v191\n"
    "v_cvt_pk_f16_f32 v192, v168, v169\n"
    "v_mfma_f32_16x16x32_bf16 v[104:107], a[208:211], v[196:199], v[104:107]\n"
    "s_waitcnt lgkmcnt(9)\n"
    "v_lshlrev_b32_sdwa v193, 1, v170 dst_sel:DWORD dst_unused:UNUSED_PAD src0_sel:DWORD src1_sel:WORD_1\n"
    "v_lshlrev_b32_sdwa v201, 1, v171 dst_sel:DWORD dst_unused:UNUSED_PAD src0_sel:DWORD src1_sel:WORD_1\n"
    "v_mfma_f32_16x16x32_bf16 v[108:111], a[212:215], v[196:199], v[108:111]\n"
    "ds_read_u16_d16_hi v207, v207\n"
    "v_lshlrev_b32_sdwa v194, 1, v172 dst_sel:DWORD dst_unused:UNUSED_PAD src0_sel:DWORD src1_sel:WORD_1\n"
    "v_mfma_f32_16x16x32_bf16 v[120:123], a[216:219], v[196:199], v[120:123]\n"
    "ds_read_u16 v193, v193\n"
    "v_or_b32_e64 v186, v186, v202\n"
    "v_mfma_f32_16x16x32_bf16 v[124:127], a[220:223], v[196:199], v[124:127]\n"
    "v_lshlrev_b32_sdwa v202, 1, v173 dst_sel:DWORD dst_unused:UNUSED_PAD src0_sel:DWORD src1_sel:WORD_1\n"
    "v_lshlrev_b32_sdwa v195, 1, v174 dst_sel:DWORD dst_unused:UNUSED_PAD src0_sel:DWORD src1_sel:WORD_1\n"
    "v_mfma_f32_16x16x32_bf16 v[136:139], v[236:239], v[196:199], v[136:139]\n"
    "s_waitcnt lgkmcnt(9)\n"
    "ds_read_u16_d16_hi v201, v201\n"
    "v_or_b32_e32 v187, v187, v203\n"
    "v_mfma_f32_16x16x32_bf16 v[140:143], a[244:247], v[196:199], v[140:143]\n"
    "v_lshlrev_b32_sdwa v203, 1, v175 dst_sel:DWORD dst_unused:UNUSED_PAD src0_sel:DWORD src1_sel:WORD_1\n"
    "v_cvt_pk_f16_f32 v196, v176, v177\n"
    "v_mfma_f32_16x16x32_bf16 v[168:171], a[96:99], v[144:147], 0\n"
    "s_nop 0\n"
    "ds_read_u16 v194, v194\n"
    "v_lshlrev_b32_sdwa v197, 1, v178 dst_sel:DWORD dst_unused:UNUSED_PAD src0_sel:DWORD src1_sel:WORD_1\n"
    "v_mfma_f32_16x16x32_bf16 v[168:171], a[144:147], v[148:151], v[168:171]\n"
    "s_waitcnt lgkmcnt(8)\n"
    "v_lshlrev_b32_sdwa v198, 1, v180 dst_sel:DWORD dst_unused:UNUSED_PAD src0_sel:DWORD src1_sel:WORD_1\n"
    "v_lshlrev_b32_sdwa v199, 1, v182 dst_sel:DWORD dst_unused:UNUSED_PAD src0_sel:DWORD src1_sel:WORD_1\n"
    "v_mfma_f32_16x16x32_bf16 v[168:171], a[56:59], v[152:155], v[168:171]\n"
    "ds_read_u16_d16_hi v202, v202\n"
    "v_or_b32_e64 v188, v188, v204\n"
    "s_waitcnt lgkmcnt(7)\n"
    "v_smfmac_f32_16x16x64_bf16 v[72:75], v[216:219], v[208:215], v240\n"
    "s_waitcnt lgkmcnt(6)\n"
    "ds_read_b128 v[216:219], v251 offset:22848\n"
    "v_smfmac_f32_16x16x64_bf16 v[76:79], v[220:223], v[208:215], v240 abid:1\n"
    "ds_read_b128 v[208:211], v250 offset:160\n"
    "v_mfma_f32_16x16x32_bf16 v[172:175], a[100:103], v[144:147], 0\n"
    "ds_read_b128 v[212:215], v250 offset:480\n"
    "v_or_b32_e64 v189, v189, v205\n"
    "v_mfma_f32_16x16x32_bf16 v[172:175], a[148:151], v[148:151], v[172:175]\n"
    "ds_read_b128 v[220:223], v251 offset:23184\n"
    "v_lshlrev_b32_sdwa v205, 1, v179 dst_sel:DWORD dst_unused:UNUSED_PAD src0_sel:DWORD src1_sel:WORD_1\n"
    "v_cvt_pk_bf16_f32 v96, v96, v97\n"
    "v_cvt_pk_bf16_f32 v97, v98, v99\n"
    "v_cvt_pk_bf16_f32 v98, v100, v101\n"
    "v_pk_add_f32 v[224:225], v[224:225], v[72:73]\n"
    "v_pk_add_f32 v[226:227], v[226:227], v[74:75]\n"
    "v_pk_fma_f32 v[228:229], v[72:73], v[72:73], v[228:229]\n"
    "v_pk_fma_f32 v[230:231], v[74:75], v[74:75], v[230:231]\n"
    "v_pk_add_f32 v[224:225], v[224:225], v[76:77]\n"
    "v_pk_add_f32 v[226:227], v[226:227], v[78:79]\n"
    "v_pk_fma_f32 v[228:229], v[76:77], v[76:77], v[228:229]\n"
    "v_pk_fma_f32 v[230:231], v[78:79], v[78:79], v[230:231]\n"
    "v_mfma_f32_16x16x32_bf16 v[172:175], a[60:63], v[152:155], v[172:175]\n"
    "s_waitcnt lgkmcnt(7)\n"
    "ds_read_b128 v[236:239], v244 offset:0\n"
    "ds_read_u16 v195, v195\n"
    "v_mfma_f32_16x16x32_bf16 v[176:179], a[96:99], v[156:159], 0\n"
    "s_nop 0\n"
    "ds_read_u16_d16_hi v203, v203\n"
    "v_cvt_pk_bf16_f32 v99, v102, v103\n"
    "v_mfma_f32_16x16x32_bf16 v[176:179], a[144:147], v[160:163], v[176:179]\n"
    "s_waitcnt lgkmcnt(9)\n"
    "ds_read_b128 a[244:247], v244 offset:2048\n"
    "v_or_b32_e32 v190, v190, v206\n"
    "v_mfma_f32_16x16x32_bf16 v[176:179], a[56:59], v[164:167], v[176:179]\n"
    "ds_read_u16 v197, v197\n"
    "v_lshlrev_b32_sdwa v206, 1, v181 dst_sel:DWORD dst_unused:UNUSED_PAD src0_sel:DWORD src1_sel:WORD_1\n"
    "v_mfma_f32_16x16x32_bf16 v[100:103], a[4:7], v[184:187], 0\n"
    "s_nop 0\n"
    "v_cvt_pk_bf16_f32 v112, v112, v113\n"
    "ds_write_b128 v242, v[96:99] offset:24640\n"
    "v_mfma_f32_16x16x32_bf16 v[96:99], a[0:3], v[184:187], 0\n"
    "s_waitcnt lgkmcnt(8)\n"
    "v_cvt_pk_bf16_f32 v113, v114, v115\n"
    "v_cvt_pk_bf16_f32 v114, v116, v117\n"
    "s_waitcnt lgkmcnt(7)\n"
    "v_smfmac_f32_16x16x64_bf16 v[80:83], v[216:219], v[208:215], v240\n"
    "s_waitcnt lgkmcnt(6)\n"
    "ds_read_b128 v[216:219], v252 offset:22848\n"
    "ds_read_u16_d16_hi v205, v205\n"
    "v_smfmac_f32_16x16x64_bf16 v[84:87], v[220:223], v[208:215], v240 abid:1\n"
    "ds_read_b128 v[208:211], v250 offset:800\n"
    "v_cvt_pk_bf16_f32 v115, v118, v119\n"
    "v_cvt_pk_bf16_f32 v128, v128, v129\n"
    "ds_read_b128 v[212:215], v250 offset:1120\n"
    "v_cvt_pk_bf16_f32 v129, v130, v131\n"
    "ds_read_b128 v[220:223], v252 offset:23184\n"
    "v_or_b32_e64 v191, v191, v207\n"
    "v_mfma_f32_16x16x32_bf16 v[116:119], a[12:15], v[184:187], 0\n"
    "ds_read_u16 v198, v198\n"
    "v_lshlrev_b32_sdwa v207, 1, v183 dst_sel:DWORD dst_unused:UNUSED_PAD src0_sel:DWORD src1_sel:WORD_1\n"
    "v_mfma_f32_16x16x32_bf16 v[180:183], a[100:103], v[156:159], 0\n"
    "v_cvt_pk_bf16_f32 v130, v132, v133\n"
    "v_or_b32_e32 v193, v193, v201\n"
    "v_mfma_f32_16x16x32_bf16 v[180:183], a[148:151], v[160:163], v[180:183]\n"
    "s_waitcnt lgkmcnt(8)\n"
    "ds_read_u16_d16_hi v206, v206\n"
    "v_cvt_pk_bf16_f32 v131, v134, v135\n"
    "v_mfma_f32_16x16x32_bf16 v[180:183], a[60:63], v[164:167], v[180:183]\n"
    "ds_read_u16 v199, v199\n"
    "v_cvt_pk_bf16_f32 v104, v104, v105\n"
    "v_mfma_f32_16x16x32_bf16 v[132:135], a[244:247], v[184:187], 0\n"
    "v_cvt_pk_bf16_f32 v105, v106, v107\n"
    "v_cvt_pk_bf16_f32 v106, v108, v109\n"
    "v_cvt_pk_bf16_f32 v107, v110, v111\n"
    "v_mfma_f32_16x16x32_bf16 v[108:111], a[4:7], v[188:191], 0\n"
    "ds_read_u16_d16_hi v207, v207\n"
    "v_or_b32_e64 v194, v194, v202\n"
    "v_cvt_pk_bf16_f32 v120, v120, v121\n"
    "v_cvt_pk_bf16_f32 v121, v122, v123\n"
    "v_cvt_pk_bf16_f32 v122, v124, v125\n"
    "ds_write_b128 v242, v[112:115] offset:26688\n"
    "v_mfma_f32_16x16x32_bf16 v[112:115], a[8:11], v[184:187], 0\n"
    "s_waitcnt lgkmcnt(7)\n"
    "v_cvt_pk_bf16_f32 v123, v126, v127\n"
    "v_add_u32_e32 v254, s54, v242\n"
    "v_cvt_pk_bf16_f32 v136, v136, v137\n"
    "s_waitcnt lgkmcnt(6)\n"
    "v_smfmac_f32_16x16x64_bf16 v[80:83], v[216:219], v[208:215], v240\n"
    "s_waitcnt lgkmcnt(5)\n"
    "ds_read_b128 v[216:219], v251 offset:23520\n"
    "ds_write_b128 v242, v[128:131] offset:28736\n"
    "v_smfmac_f32_16x16x64_bf16 v[84:87], v[220:223], v[208:215], v240 abid:1\n"
    "v_pk_fma_f16 v232, v192, v192, s48\n"
    "v_mfma_f32_16x16x32_bf16 v[128:131], v[236:239], v[184:187], 0\n"
    "v_cvt_pk_bf16_f32 v137, v138, v139\n"
    "v_cvt_pk_bf16_f32 v138, v140, v141\n"
    "v_or_b32_e64 v195, v195, v203\n"
    "ds_read_b128 v[208:211], v250 offset:240\n"
    "ds_read_b128 v[212:215], v250 offset:560\n"
    "v_pk_add_f32 v[224:225], v[224:225], v[80:81]\n"
    "v_pk_add_f32 v[226:227], v[226:227], v[82:83]\n"
    "v_pk_fma_f32 v[228:229], v[80:81], v[80:81], v[228:229]\n"
    "v_pk_fma_f32 v[230:231], v[82:83], v[82:83], v[230:231]\n"
    "v_pk_add_f32 v[224:225], v[224:225], v[84:85]\n"
    "v_pk_add_f32 v[226:227], v[226:227], v[86:87]\n"
    "v_pk_fma_f32 v[228:229], v[84:85], v[84:85], v[228:229]\n"
    "v_pk_fma_f32 v[230:231], v[86:87], v[86:87], v[230:231]\n"
    "v_mfma_f32_16x16x32_bf16 v[124:127], a[12:15], v[188:191], 0\n"
    "s_waitcnt lgkmcnt(6)\n"
    "ds_read_b128 v[220:223], v251 offset:23856\n"
    "ds_write_b128 v242, v[104:107] offset:25664\n"
    "v_mfma_f32_16x16x32_bf16 v[104:107], a[0:3], v[188:191], 0\n"
    "s_waitcnt lgkmcnt(6)\n"
    "v_cvt_pk_bf16_f32 v139, v142, v143\n"
    "v_pk_mul_f16 v232, v192, v232\n"
    "v_or_b32_e64 v197, v197, v205\n"
    "v_exp_f16_e64 v234, v232\n"
    "v_mfma_f32_16x16x32_bf16 v[140:143], a[244:247], v[188:191], 0\n"
    "ds_write_b128 v242, v[120:123] offset:27712\n"
    "v_mfma_f32_16x16x32_bf16 v[120:123], a[8:11], v[188:191], 0\n"
    "v_exp_f16_sdwa v234, v232 dst_sel:WORD_1 dst_unused:UNUSED_PRESERVE src0_sel:WORD_1\n"
    "v_cvt_pk_f16_f32 v184, v168, v169\n"
    "v_pk_fma_f16 v234, v234, s49, s49\n"
    "v_lshlrev_b32_sdwa v185, 1, v170 dst_sel:DWORD dst_unused:UNUSED_PAD src0_sel:DWORD src1_sel:WORD_1\n"
    "s_waitcnt lgkmcnt(3)\n"
    "v_smfmac_f32_16x16x64_bf16 v[88:91], v[216:219], v[208:215], v240\n"
    "ds_read_b128 v[216:219], v252 offset:23520\n"
    "ds_write_b128 v242, v[136:139] offset:29760\n"
    "s_waitcnt lgkmcnt(3)\n"
    "v_smfmac_f32_16x16x64_bf16 v[92:95], v[220:223], v[208:215], v240 abid:1\n"
    "v_lshlrev_b32_sdwa v201, 1, v171 dst_sel:DWORD dst_unused:UNUSED_PAD src0_sel:DWORD src1_sel:WORD_1\n"
    "v_mfma_f32_16x16x32_bf16 v[136:139], v[236:239], v[188:191], 0\n"
    "v_lshlrev_b32_sdwa v186, 1, v172 dst_sel:DWORD dst_unused:UNUSED_PAD src0_sel:DWORD src1_sel:WORD_1\n"
    "v_lshlrev_b32_sdwa v202, 1, v173 dst_sel:DWORD dst_unused:UNUSED_PAD src0_sel:DWORD src1_sel:WORD_1\n"
    "v_mfma_f32_16x16x32_bf16 v[168:171], a[104:107], v[144:147], 0\n"
    "ds_read_b128 v[208:211], v250 offset:880\n"
    "ds_read_b128 v[212:215], v250 offset:1200\n"
    "v_mfma_f32_16x16x32_bf16 v[168:171], a[152:155], v[148:151], v[168:171]\n"
    "ds_read_b128 v[220:223], v252 offset:23856\n"
    "ds_read_u16 v185, v185\n"
    "v_lshlrev_b32_sdwa v187, 1, v174 dst_sel:DWORD dst_unused:UNUSED_PAD src0_sel:DWORD src1_sel:WORD_1\n"
    "v_lshlrev_b32_sdwa v203, 1, v175 dst_sel:DWORD dst_unused:UNUSED_PAD src0_sel:DWORD src1_sel:WORD_1\n"
    "v_rcp_f16_e64 v232, v234\n"
    "v_mfma_f32_16x16x32_bf16 v[168:171], a[64:67], v[152:155], v[168:171]\n"
    "ds_read_u16_d16_hi v201, v201\n"
    "ds_read_u16 v186, v186\n"
    "v_or_b32_e64 v198, v198, v206\n"
    "v_rcp_f16_sdwa v232, v234 dst_sel:WORD_1 dst_unused:UNUSED_PRESERVE src0_sel:WORD_1\n"
    "v_mfma_f32_16x16x32_bf16 v[172:175], a[108:111], v[144:147], 0\n"
    "ds_read_u16_d16_hi v202, v202\n"
    "v_cvt_pk_f16_f32 v188, v176, v177\n"
    "v_mfma_f32_16x16x32_bf16 v[172:175], a[156:159], v[148:151], v[172:175]\n"
    "v_lshlrev_b32_sdwa v189, 1, v178 dst_sel:DWORD dst_unused:UNUSED_PAD src0_sel:DWORD src1_sel:WORD_1\n"
    "v_lshlrev_b32_sdwa v205, 1, v179 dst_sel:DWORD dst_unused:UNUSED_PAD src0_sel:DWORD src1_sel:WORD_1\n"
    "v_lshlrev_b32_sdwa v190, 1, v180 dst_sel:DWORD dst_unused:UNUSED_PAD src0_sel:DWORD src1_sel:WORD_1\n"
    "v_lshlrev_b32_sdwa v206, 1, v181 dst_sel:DWORD dst_unused:UNUSED_PAD src0_sel:DWORD src1_sel:WORD_1\n"
    "v_lshlrev_b32_sdwa v191, 1, v182 dst_sel:DWORD dst_unused:UNUSED_PAD src0_sel:DWORD src1_sel:WORD_1\n"
    "v_fma_mix_f32 v234, v192, v232, 0 op_sel_hi:[1,1,0]\n"
    "v_mfma_f32_16x16x32_bf16 v[172:175], a[68:71], v[152:155], v[172:175]\n"
    "ds_read_b128 v[236:239], v244 offset:4096\n"
    "ds_read_u16 v187, v187\n"
    "v_or_b32_e64 v199, v199, v207\n"
    "v_lshlrev_b32_sdwa v207, 1, v183 dst_sel:DWORD dst_unused:UNUSED_PAD src0_sel:DWORD src1_sel:WORD_1\n"
    "v_fma_mix_f32 v232, v192, v232, 0 op_sel:[1,1,0] op_sel_hi:[1,1,0]\n"
    "s_waitcnt lgkmcnt(7)\n"
    "v_smfmac_f32_16x16x64_bf16 v[88:91], v[216:219], v[208:215], v240\n"
    "s_waitcnt lgkmcnt(6)\n"
    "ds_read_u16_d16_hi v203, v203\n"
    "v_smfmac_f32_16x16x64_bf16 v[92:95], v[220:223], v[208:215], v240 abid:1\n"
    "ds_read_b128 a[244:247], v244 offset:6144\n"
    "v_mfma_f32_16x16x32_bf16 v[176:179], a[104:107], v[156:159], 0\n"
    "ds_read_u16 v189, v189\n"
    "v_cvt_pk_bf16_f32 v192, v234, v232\n"
    "v_mfma_f32_16x16x32_bf16 v[176:179], a[152:155], v[160:163], v[176:179]\n"
    "v_pk_fma_f16 v232, v196, v196, s48\n"
    "v_lshlrev_b32_sdwa v200, 1, v169 dst_sel:DWORD dst_unused:UNUSED_PAD src0_sel:DWORD src1_sel:WORD_1\n"
    "v_pk_mul_f16 v232, v196, v232\n"
    "v_exp_f16_e64 v234, v232\n"
    "v_pk_add_f32 v[224:225], v[224:225], v[88:89]\n"
    "v_pk_add_f32 v[226:227], v[226:227], v[90:91]\n"
    "v_pk_fma_f32 v[228:229], v[88:89], v[88:89], v[228:229]\n"
    "v_pk_fma_f32 v[230:231], v[90:91], v[90:91], v[230:231]\n"
    "v_pk_add_f32 v[224:225], v[224:225], v[92:93]\n"
    "v_pk_add_f32 v[226:227], v[226:227], v[94:95]\n"
    "v_pk_fma_f32 v[228:229], v[92:93], v[92:93], v[228:229]\n"
    "v_pk_fma_f32 v[230:231], v[94:95], v[94:95], v[230:231]\n"
    "v_mfma_f32_16x16x32_bf16 v[176:179], a[64:67], v[164:167], v[176:179]\n"
    "s_waitcnt lgkmcnt(6)\n"
    "ds_read_u16_d16_hi v205, v205\n"
    "ds_write_b128 v254, v[224:227]\n"
    "v_mfma_f32_16x16x32_bf16 v[180:183], a[108:111], v[156:159], 0\n"
    "s_waitcnt lgkmcnt(6)\n"
    "v_exp_f16_sdwa v234, v232 dst_sel:WORD_1 dst_unused:UNUSED_PRESERVE src0_sel:WORD_1\n"
    "v_mfma_f32_16x16x32_bf16 v[180:183], a[156:159], v[160:163], v[180:183]\n"
    "v_pk_fma_f16 v234, v234, s49, s49\n"
    "v_or_b32_e64 v185, v185, v201\n"
    "v_mfma_f32_16x16x32_bf16 v[180:183], a[68:71], v[164:167], v[180:183]\n"
    "ds_read_u16 v190, v190\n"
    "ds_write_b128 v254, v[228:231] offset:1024\n"
    "v_mfma_f32_16x16x32_bf16 v[96:99], a[16:19], v[192:195], v[96:99]\n"
    "v_lshrrev_b32_e64 v254, 2, v242\n"
    "v_rcp_f16_e64 v232, v234\n"
    "v_mfma_f32_16x16x32_bf16 v[100:103], a[20:23], v[192:195], v[100:103]\n"
    "s_waitcnt lgkmcnt(5)\n"
    "v_add_u32_e32 v254, s55, v254\n"
    "v_rcp_f16_sdwa v232, v234 dst_sel:WORD_1 dst_unused:UNUSED_PRESERVE src0_sel:WORD_1\n"
    "v_mfma_f32_16x16x32_bf16 v[112:115], a[24:27], v[192:195], v[112:115]\n"
    "ds_read_u16_d16_hi v206, v206\n"
    "ds_read_u16 v191, v191\n"
    "v_mfma_f32_16x16x32_bf16 v[116:119], a[28:31], v[192:195], v[116:119]\n"
    "ds_read_u16_d16_hi v207, v207\n"
    "v_fma_mix_f32 v234, v196, v232, 0 op_sel_hi:[1,1,0]\n"
    "v_mfma_f32_16x16x32_bf16 v[128:131], v[236:239], v[192:195], v[128:131]\n"
    "ds_read_b128 a[240:243], v244 offset:34816\n"
    "v_fma_mix_f32 v232, v196, v232, 0 op_sel:[1,1,0] op_sel_hi:[1,1,0]\n"
    "v_mfma_f32_16x16x32_bf16 v[132:135], a[244:247], v[192:195], v[132:135]\n"
    "s_waitcnt lgkmcnt(6)\n"
    "v_or_b32_e32 v186, v186, v202\n"
    "v_cvt_pk_bf16_f32 v196, v234, v232\n"
    "v_pk_fma_f16 v232, v184, v184, s48\n"
    "v_or_b32_e64 v187, v187, v203\n"
    "v_mfma_f32_16x16x32_bf16 v[104:107], a[16:19], v[196:199], v[104:107]\n"
    "ds_read_u16_d16_hi v200, v200\n"
    "v_pk_mul_f16 v232, v184, v232\n"
    "v_mfma_f32_16x16x32_bf16 v[108:111], a[20:23], v[196:199], v[108:111]\n"
    "v_exp_f16_e64 v234, v232\n"
    "s_waitcnt lgkmcnt(5)\n"
    "s_barrier\n"
    "v_mfma_f32_16x16x32_bf16 v[120:123], a[24:27], v[196:199], v[120:123]\n"
    "ds_read_b32 v224, v254 offset:0\n"
    "ds_read_b32 v225, v254 offset:6752\n"
    "v_mfma_f32_16x16x32_bf16 v[124:127], a[28:31], v[196:199], v[124:127]\n"
    "s_nop 0\n"
    "ds_read_b32 v226, v254 offset:13504\n"
    "ds_read_b32 v227, v254 offset:20256\n"
    "v_mfma_f32_16x16x32_bf16 v[136:139], v[236:239], v[196:199], v[136:139]\n"
    "s_waitcnt lgkmcnt(8)\n"
    "ds_read_b32 v228, v254 offset:1024\n"
    "ds_read_b32 v229, v254 offset:7776\n"
    "v_mfma_f32_16x16x32_bf16 v[140:143], a[244:247], v[196:199], v[140:143]\n"
    "ds_read_b32 v230, v254 offset:14528\n"
    "ds_read_b32 v231, v254 offset:21280\n"
    "s_waitcnt vmcnt(2)\n"
    "v_smfmac_f32_32x32x32_bf16 v[112:127], a[248:251], a[232:239], v241\n"
    "v_exp_f16_sdwa v234, v232 dst_sel:WORD_1 dst_unused:UNUSED_PRESERVE src0_sel:WORD_1\n"
    "v_or_b32_e32 v189, v189, v205\n"
    "v_pk_fma_f16 v234, v234, s49, s49\n"
    "v_lshlrev_b32_sdwa v192, 1, v168 dst_sel:DWORD dst_unused:UNUSED_PAD src0_sel:DWORD src1_sel:WORD_1\n"
    "s_waitcnt vmcnt(1)\n"
    "v_smfmac_f32_32x32x32_bf16 v[96:111], a[248:251], a[224:231], v241\n"
    "s_waitcnt lgkmcnt(9)\n"
    "ds_read_u16 v192, v192\n"
    "ds_read_b128 v[236:239], v244 offset:8192\n"
    "v_lshlrev_b32_sdwa v193, 1, v170 dst_sel:DWORD dst_unused:UNUSED_PAD src0_sel:DWORD src1_sel:WORD_1\n"
    "v_lshlrev_b32_sdwa v201, 1, v171 dst_sel:DWORD dst_unused:UNUSED_PAD src0_sel:DWORD src1_sel:WORD_1\n"
    "v_lshlrev_b32_sdwa v194, 1, v172 dst_sel:DWORD dst_unused:UNUSED_PAD src0_sel:DWORD src1_sel:WORD_1\n"
    "v_mfma_f32_16x16x32_bf16 v[168:171], a[112:115], v[144:147], 0\n"
    "s_waitcnt lgkmcnt(7)\n"
    "ds_read_u16 v193, v193\n"
    "ds_read_u16_d16_hi v201, v201\n"
    "v_mfma_f32_16x16x32_bf16 v[168:171], a[160:163], v[148:151], v[168:171]\n"
    "s_waitcnt lgkmcnt(7)\n"
    "ds_read_u16 v194, v194\n"
    "buffer_load_dwordx4 a[224:227], v245, s[40:43], s58 offen offset:128\n"
    "v_add_f32_e64 v224, v224, v225\n"
    "v_add_f32_e32 v226, v226, v227\n"
    "s_waitcnt lgkmcnt(6)\n"
    "v_add_f32_e32 v228, v228, v229\n"
    "s_waitcnt lgkmcnt(5)\n"
    "v_add_f32_e64 v230, v230, v231\n"
    "v_add_f32_e64 v224, v224, v226\n"
    "v_add_f32_e64 v228, v228, v230\n"
    "v_mul_f32_e32 v224, 0x3c2aaaab, v224\n"
    "v_mul_f32_e32 v228, 0x3c2aaaab, v228\n"
    "v_lshlrev_b32_sdwa v202, 1, v173 dst_sel:DWORD dst_unused:UNUSED_PAD src0_sel:DWORD src1_sel:WORD_1\n"
    "v_fma_f32 v228, -v224, v224, v228\n"
    "v_lshlrev_b32_sdwa v195, 1, v174 dst_sel:DWORD dst_unused:UNUSED_PAD src0_sel:DWORD src1_sel:WORD_1\n"
    "v_max_f32_e64 v228, 0, v228\n"
    "v_lshlrev_b32_sdwa v203, 1, v175 dst_sel:DWORD dst_unused:UNUSED_PAD src0_sel:DWORD src1_sel:WORD_1\n"
    "v_add_f32_e32 v228, 0x3727c5ac, v228\n"
    "v_rcp_f16_e64 v232, v234\n"
    "v_mfma_f32_16x16x32_bf16 v[168:171], a[72:75], v[152:155], v[168:171]\n"
    "ds_read_b128 a[232:235], v243 offset:24640\n"
    "ds_read_u16_d16_hi v202, v202\n"
    "v_rsq_f32_e64 v228, v228\n"
    "v_or_b32_e64 v190, v190, v206\n"
    "v_mul_f32_e64 v228, s50, v228\n"
    "ds_write_b32 v254, v228\n"
    "ds_read_b128 a[236:239], v243 offset:25664\n"
    "v_mul_f32_e64 v224, v228, -v224\n"
    "ds_write_b32 v254, v224 offset:1024\n"
    "ds_read_u16 v195, v195\n"
    "v_add_u32_e64 v254, s56, v242\n"
    "v_rcp_f16_sdwa v232, v234 dst_sel:WORD_1 dst_unused:UNUSED_PRESERVE src0_sel:WORD_1\n"
    "v_mfma_f32_16x16x32_bf16 v[172:175], a[116:119], v[144:147], 0\n"
    "ds_read_b128 a[244:247], v244 offset:10240\n"
    "buffer_load_dwordx4 a[228:231], v245, s[40:43], s89 offen offset:128\n"
    "v_mfma_f32_16x16x32_bf16 v[172:175], a[164:167], v[148:151], v[172:175]\n"
    "ds_read_u16_d16_hi v203, v203\n"
    "v_lshlrev_b32_sdwa v196, 1, v176 dst_sel:DWORD dst_unused:UNUSED_PAD src0_sel:DWORD src1_sel:WORD_1\n"
    "v_lshlrev_b32_sdwa v204, 1, v177 dst_sel:DWORD dst_unused:UNUSED_PAD src0_sel:DWORD src1_sel:WORD_1\n"
    "v_lshlrev_b32_sdwa v197, 1, v178 dst_sel:DWORD dst_unused:UNUSED_PAD src0_sel:DWORD src1_sel:WORD_1\n"
    "v_lshlrev_b32_sdwa v205, 1, v179 dst_sel:DWORD dst_unused:UNUSED_PAD src0_sel:DWORD src1_sel:WORD_1\n"
    "v_lshlrev_b32_sdwa v198, 1, v180 dst_sel:DWORD dst_unused:UNUSED_PAD src0_sel:DWORD src1_sel:WORD_1\n"
    "v_lshlrev_b32_sdwa v206, 1, v181 dst_sel:DWORD dst_unused:UNUSED_PAD src0_sel:DWORD src1_sel:WORD_1\n"
    "v_lshlrev_b32_sdwa v199, 1, v182 dst_sel:DWORD dst_unused:UNUSED_PAD src0_sel:DWORD src1_sel:WORD_1\n"
    "v_fma_mix_f32 v234, v184, v232, 0 op_sel_hi:[1,1,0]\n"
    "v_mfma_f32_16x16x32_bf16 v[172:175], a[240:243], v[152:155], v[172:175]\n"
    "s_waitcnt lgkmcnt(7)\n"
    "ds_read_u16 v196, v196\n"
    "ds_read_u16_d16_hi v204, v204\n"
    "v_or_b32_e32 v191, v191, v207\n"
    "v_lshlrev_b32_sdwa v207, 1, v183 dst_sel:DWORD dst_unused:UNUSED_PAD src0_sel:DWORD src1_sel:WORD_1\n"
    "v_fma_mix_f32 v232, v184, v232, 0 op_sel:[1,1,0] op_sel_hi:[1,1,0]\n"
    "v_mfma_f32_16x16x32_bf16 v[176:179], a[112:115], v[156:159], 0\n"
    "s_waitcnt lgkmcnt(5)\n"
    "ds_read_u16 v197, v197\n"
    "v_cvt_pk_bf16_f32 v184, v234, v232\n"
    "s_barrier\n"
    "v_mfma_f32_16x16x32_bf16 v[176:179], a[160:163], v[160:163], v[176:179]\n"
    "ds_read_b128 v[224:227], v254\n"
    "ds_read_b128 v[228:231], v254 offset:1024\n"
    "v_smfmac_f32_32x32x32_bf16 v[96:111], a[248:251], a[232:239], v241\n"
    "ds_read_u16_d16_hi v205, v205\n"
    "v_lshlrev_b32_e64 v254, 3, v244\n"
    "v_pk_fma_f16 v232, v188, v188, s48\n"
    "v_or_b32_e64 v192, v192, v200\n"
    "v_pk_mul_f16 v232, v188, v232\n"
    "v_mfma_f32_16x16x32_bf16 v[176:179], a[72:75], v[164:167], v[176:179]\n"
    "s_mov_b32 exec_hi, 0\n"
    "buffer_load_dword v233, v254, s[72:75], s53 offen sc1\n"
    "s_mov_b32 exec_hi, -1\n"
    "v_mfma_f32_16x16x32_bf16 v[180:183], a[116:119], v[156:159], 0\n"
    "s_add_u32 s53, s53, 393216\n"
    "ds_read_u16 v198, v198\n"
    "ds_read_u16_d16_hi v206, v206\n"
    "v_mfma_f32_16x16x32_bf16 v[180:183], a[164:167], v[160:163], v[180:183]\n"
    "v_exp_f16_e64 v234, v232\n"
    "v_mfma_f32_16x16x32_bf16 v[180:183], a[240:243], v[164:167], v[180:183]\n"
    "ds_read_u16 v199, v199\n"
    "v_exp_f16_sdwa v234, v232 dst_sel:WORD_1 dst_unused:UNUSED_PRESERVE src0_sel:WORD_1\n"
    "v_mfma_f32_16x16x32_bf16 v[96:99], a[32:35], v[184:187], v[96:99]\n"
    "ds_read_u16_d16_hi v207, v207\n"
    "ds_read_b128 a[232:235], v243 offset:26688\n"
    "v_pk_fma_f16 v234, v234, s49, s49\n"
    "v_mfma_f32_16x16x32_bf16 v[100:103], a[36:39], v[184:187], v[100:103]\n"
    "s_waitcnt lgkmcnt(9)\n"
    "ds_read_b128 a[236:239], v243 offset:27712\n"
    "v_rcp_f16_e32 v232, v234\n"
    "v_mfma_f32_16x16x32_bf16 v[112:115], a[40:43], v[184:187], v[112:115]\n"
    "s_waitcnt lgkmcnt(7)\n"
    "v_rcp_f16_sdwa v232, v234 dst_sel:WORD_1 dst_unused:UNUSED_PRESERVE src0_sel:WORD_1\n"
    "v_or_b32_e32 v193, v193, v201\n"
    "v_fma_mix_f32 v234, v188, v232, 0 op_sel_hi:[1,1,0]\n"
    "v_fma_mix_f32 v232, v188, v232, 0 op_sel:[1,1,0] op_sel_hi:[1,1,0]\n"
    "v_pk_fma_f32 v[64:65], v[64:65], v[224:225], v[228:229]\n"
    "v_pk_fma_f32 v[66:67], v[66:67], v[226:227], v[230:231]\n"
    "v_pk_fma_f32 v[68:69], v[68:69], v[224:225], v[228:229]\n"
    "v_pk_fma_f32 v[70:71], v[70:71], v[226:227], v[230:231]\n"
    "v_pk_fma_f32 v[72:73], v[72:73], v[224:225], v[228:229]\n"
    "v_pk_fma_f32 v[74:75], v[74:75], v[226:227], v[230:231]\n"
    "v_pk_fma_f32 v[76:77], v[76:77], v[224:225], v[228:229]\n"
    "v_pk_fma_f32 v[78:79], v[78:79], v[226:227], v[230:231]\n"
    "v_pk_fma_f32 v[80:81], v[80:81], v[224:225], v[228:229]\n"
    "v_pk_fma_f32 v[82:83], v[82:83], v[226:227], v[230:231]\n"
    "v_pk_fma_f32 v[84:85], v[84:85], v[224:225], v[228:229]\n"
    "v_pk_fma_f32 v[86:87], v[86:87], v[226:227], v[230:231]\n"
    "v_pk_fma_f32 v[88:89], v[88:89], v[224:225], v[228:229]\n"
    "v_pk_fma_f32 v[90:91], v[90:91], v[226:227], v[230:231]\n"
    "v_pk_fma_f32 v[92:93], v[92:93], v[224:225], v[228:229]\n"
    "v_pk_fma_f32 v[94:95], v[94:95], v[226:227], v[230:231]\n"
    "v_pk_fma_f32 v[0:1], v[0:1], v[224:225], v[228:229]\n"
    "v_pk_fma_f32 v[2:3], v[2:3], v[226:227], v[230:231]\n"
    "v_pk_fma_f32 v[4:5], v[4:5], v[224:225], v[228:229]\n"
    "v_pk_fma_f32 v[6:7], v[6:7], v[226:227], v[230:231]\n"
    "v_pk_fma_f32 v[8:9], v[8:9], v[224:225], v[228:229]\n"
    "v_pk_fma_f32 v[10:11], v[10:11], v[226:227], v[230:231]\n"
    "v_pk_fma_f32 v[12:13], v[12:13], v[224:225], v[228:229]\n"
    "v_pk_fma_f32 v[14:15], v[14:15], v[226:227], v[230:231]\n"
    "v_pk_fma_f32 v[16:17], v[16:17], v[224:225], v[228:229]\n"
    "v_pk_fma_f32 v[18:19], v[18:19], v[226:227], v[230:231]\n"
    "v_pk_fma_f32 v[20:21], v[20:21], v[224:225], v[228:229]\n"
    "v_pk_fma_f32 v[22:23], v[22:23], v[226:227], v[230:231]\n"
    "v_pk_fma_f32 v[24:25], v[24:25], v[224:225], v[228:229]\n"
    "v_pk_fma_f32 v[26:27], v[26:27], v[226:227], v[230:231]\n"
    "v_pk_fma_f32 v[28:29], v[28:29], v[224:225], v[228:229]\n"
    "v_pk_fma_f32 v[30:31], v[30:31], v[226:227], v[230:231]\n"
    "v_pk_fma_f32 v[32:33], v[32:33], v[224:225], v[228:229]\n"
    "v_pk_fma_f32 v[34:35], v[34:35], v[226:227], v[230:231]\n"
    "v_pk_fma_f32 v[36:37], v[36:37], v[224:225], v[228:229]\n"
    "v_pk_fma_f32 v[38:39], v[38:39], v[226:227], v[230:231]\n"
    "v_pk_fma_f32 v[40:41], v[40:41], v[224:225], v[228:229]\n"
    "v_pk_fma_f32 v[42:43], v[42:43], v[226:227], v[230:231]\n"
    "v_pk_fma_f32 v[44:45], v[44:45], v[224:225], v[228:229]\n"
    "v_pk_fma_f32 v[46:47], v[46:47], v[226:227], v[230:231]\n"
    "v_pk_fma_f32 v[48:49], v[48:49], v[224:225], v[228:229]\n"
    "v_pk_fma_f32 v[50:51], v[50:51], v[226:227], v[230:231]\n"
    "v_pk_fma_f32 v[52:53], v[52:53], v[224:225], v[228:229]\n"
    "v_pk_fma_f32 v[54:55], v[54:55], v[226:227], v[230:231]\n"
    "v_pk_fma_f32 v[56:57], v[56:57], v[224:225], v[228:229]\n"
    "v_pk_fma_f32 v[58:59], v[58:59], v[226:227], v[230:231]\n"
    "v_pk_fma_f32 v[60:61], v[60:61], v[224:225], v[228:229]\n"
    "v_pk_fma_f32 v[62:63], v[62:63], v[226:227], v[230:231]\n"
    "v_mfma_f32_16x16x32_bf16 v[116:119], a[44:47], v[184:187], v[116:119]\n"
    "ds_read_b128 a[240:243], v244 offset:38912\n"
    "v_cvt_pk_bf16_f32 v208, v64, v68\n"
    "v_mfma_f32_16x16x32_bf16 v[128:131], v[236:239], v[184:187], v[128:131]\n"
    "v_cvt_pk_bf16_f32 v212, v65, v69\n"
    "v_cvt_pk_bf16_f32 v216, v66, v70\n"
    "v_mfma_f32_16x16x32_bf16 v[132:135], a[244:247], v[184:187], v[132:135]\n"
    "v_cvt_pk_bf16_f32 v220, v67, v71\n"
    "v_cvt_pk_bf16_f32 v209, v72, v76\n"
    "v_cvt_pk_bf16_f32 v213, v73, v77\n"
    "v_cvt_pk_bf16_f32 v217, v74, v78\n"
    "v_cvt_pk_bf16_f32 v221, v75, v79\n"
    "v_cvt_pk_bf16_f32 v210, v80, v84\n"
    "v_cvt_pk_bf16_f32 v214, v81, v85\n"
    "v_cvt_pk_bf16_f32 v218, v82, v86\n"
    "v_cvt_pk_bf16_f32 v222, v83, v87\n"
    "v_cvt_pk_bf16_f32 v211, v88, v92\n"
    "v_cvt_pk_bf16_f32 v215, v89, v93\n"
    "v_cvt_pk_bf16_f32 v219, v90, v94\n"
    "v_cvt_pk_bf16_f32 v223, v91, v95\n"
    "v_cvt_pk_bf16_f32 v80, v0, v4\n"
    "v_cvt_pk_bf16_f32 v84, v1, v5\n"
    "v_cvt_pk_bf16_f32 v188, v234, v232\n"
    "ds_write_b128 v253, v[208:211] offset:2048\n"
    "v_cvt_pk_bf16_f32 v88, v2, v6\n"
    "v_mfma_f32_16x16x32_bf16 v[104:107], a[32:35], v[188:191], v[104:107]\n"
    "s_waitcnt lgkmcnt(1)\n"
    "v_cvt_pk_bf16_f32 v92, v3, v7\n"
    "v_cvt_pk_bf16_f32 v81, v8, v12\n"
    "v_mfma_f32_16x16x32_bf16 v[108:111], a[36:39], v[188:191], v[108:111]\n"
    "s_nop 0\n"
    "ds_write_b128 v253, v[212:215] offset:5120\n"
    "v_mfma_f32_16x16x32_bf16 v[120:123], a[40:43], v[188:191], v[120:123]\n"
    "v_cvt_pk_bf16_f32 v85, v9, v13\n"
    "v_cvt_pk_bf16_f32 v89, v10, v14\n"
    "v_mfma_f32_16x16x32_bf16 v[124:127], a[44:47], v[188:191], v[124:127]\n"
    "v_cvt_pk_bf16_f32 v93, v11, v15\n"
    "v_cvt_pk_bf16_f32 v82, v16, v20\n"
    "v_mfma_f32_16x16x32_bf16 v[136:139], v[236:239], v[188:191], v[136:139]\n"
    "ds_read_b128 v[236:239], v244 offset:12288\n"
    "v_cvt_pk_bf16_f32 v86, v17, v21\n"
    "v_mfma_f32_16x16x32_bf16 v[140:143], a[244:247], v[188:191], v[140:143]\n"
    "v_cvt_pk_bf16_f32 v90, v18, v22\n"
    "v_cvt_pk_bf16_f32 v94, v19, v23\n"
    "v_smfmac_f32_32x32x32_bf16 v[112:127], a[248:251], a[232:239], v241\n"
    "v_cvt_pk_bf16_f32 v83, v24, v28\n"
    "v_cvt_pk_f16_f32 v184, v168, v169\n"
    "v_cvt_pk_bf16_f32 v87, v25, v29\n"
    "v_lshlrev_b32_sdwa v185, 1, v170 dst_sel:DWORD dst_unused:UNUSED_PAD src0_sel:DWORD src1_sel:WORD_1\n"
    "v_lshlrev_b32_sdwa v201, 1, v171 dst_sel:DWORD dst_unused:UNUSED_PAD src0_sel:DWORD src1_sel:WORD_1\n"
    "v_mfma_f32_16x16x32_bf16 v[168:171], a[120:123], v[144:147], 0\n"
    "ds_read_u16 v185, v185\n"
    "ds_write_b128 v253, v[80:83] offset:0\n"
    "v_lshlrev_b32_sdwa v186, 1, v172 dst_sel:DWORD dst_unused:UNUSED_PAD src0_sel:DWORD src1_sel:WORD_1\n"
    "v_cvt_pk_bf16_f32 v91, v26, v30\n"
    "v_or_b32_e64 v194, v194, v202\n"
    "v_mfma_f32_16x16x32_bf16 v[168:171], a[168:171], v[148:151], v[168:171]\n"
    "v_lshlrev_b32_sdwa v202, 1, v173 dst_sel:DWORD dst_unused:UNUSED_PAD src0_sel:DWORD src1_sel:WORD_1\n"
    "v_lshlrev_b32_sdwa v187, 1, v174 dst_sel:DWORD dst_unused:UNUSED_PAD src0_sel:DWORD src1_sel:WORD_1\n"
    "v_mfma_f32_16x16x32_bf16 v[168:171], a[76:79], v[152:155], v[168:171]\n"
    "ds_read_u16_d16_hi v201, v201\n"
    "ds_write_b128 v253, v[84:87] offset:3072\n"
    "v_cvt_pk_bf16_f32 v95, v27, v31\n"
    "v_cvt_pk_f16_f32 v188, v176, v177\n"
    "v_lshlrev_b32_sdwa v189, 1, v178 dst_sel:DWORD dst_unused:UNUSED_PAD src0_sel:DWORD src1_sel:WORD_1\n"
    "v_lshlrev_b32_sdwa v190, 1, v180 dst_sel:DWORD dst_unused:UNUSED_PAD src0_sel:DWORD src1_sel:WORD_1\n"
    "v_lshlrev_b32_sdwa v191, 1, v182 dst_sel:DWORD dst_unused:UNUSED_PAD src0_sel:DWORD src1_sel:WORD_1\n"
    "v_or_b32_e64 v195, v195, v203\n"
    "v_lshlrev_b32_sdwa v203, 1, v175 dst_sel:DWORD dst_unused:UNUSED_PAD src0_sel:DWORD src1_sel:WORD_1\n"
    "v_mfma_f32_16x16x32_bf16 v[172:175], a[124:127], v[144:147], 0\n"
    "ds_read_u16 v186, v186\n"
    "ds_read_u16_d16_hi v202, v202\n"
    "v_mfma_f32_16x16x32_bf16 v[172:175], a[172:175], v[148:151], v[172:175]\n"
    "ds_read_u16 v187, v187\n"
    "ds_read_u16_d16_hi v203, v203\n"
    "v_mfma_f32_16x16x32_bf16 v[172:175], a[240:243], v[152:155], v[172:175]\n"
    "v_cvt_pk_bf16_f32 v64, v32, v36\n"
    "v_cvt_pk_bf16_f32 v68, v33, v37\n"
    "v_cvt_pk_bf16_f32 v72, v34, v38\n"
    "v_cvt_pk_bf16_f32 v76, v35, v39\n"
    "v_cvt_pk_bf16_f32 v65, v40, v44\n"
    "v_or_b32_e64 v196, v196, v204\n"
    "v_cvt_pk_bf16_f32 v69, v41, v45\n"
    "v_cvt_pk_bf16_f32 v73, v42, v46\n"
    "v_cvt_pk_bf16_f32 v77, v43, v47\n"
    "v_cvt_pk_bf16_f32 v66, v48, v52\n"
    "v_cvt_pk_bf16_f32 v70, v49, v53\n"
    "v_or_b32_e64 v197, v197, v205\n"
    "v_lshlrev_b32_sdwa v205, 1, v179 dst_sel:DWORD dst_unused:UNUSED_PAD src0_sel:DWORD src1_sel:WORD_1\n"
    "v_mfma_f32_16x16x32_bf16 v[176:179], a[120:123], v[156:159], 0\n"
    "s_nop 0\n"
    "ds_read_b128 a[244:247], v244 offset:14336\n"
    "ds_read_u16 v189, v189\n"
    "v_mfma_f32_16x16x32_bf16 v[176:179], a[168:171], v[160:163], v[176:179]\n"
    "s_waitcnt lgkmcnt(6)\n"
    "ds_read_b128 a[232:235], v243 offset:28736\n"
    "ds_read_u16_d16_hi v205, v205\n"
    "v_mfma_f32_16x16x32_bf16 v[176:179], a[76:79], v[164:167], v[176:179]\n"
    "v_cvt_pk_bf16_f32 v74, v50, v54\n"
    "v_cvt_pk_bf16_f32 v78, v51, v55\n"
    "v_cvt_pk_bf16_f32 v67, v56, v60\n"
    "v_cvt_pk_bf16_f32 v71, v57, v61\n"
    "v_cvt_pk_bf16_f32 v75, v58, v62\n"
    "v_or_b32_e32 v198, v198, v206\n"
    "v_mfma_f32_16x16x32_bf16 v[96:99], a[176:179], v[192:195], v[96:99]\n"
    "ds_read_b128 a[236:239], v243 offset:29760\n"
    "ds_write_b128 v253, v[64:67] offset:1024\n"
    "v_mfma_f32_16x16x32_bf16 v[100:103], a[180:183], v[192:195], v[100:103]\n"
    "s_waitcnt lgkmcnt(8)\n"
    "v_lshlrev_b32_sdwa v206, 1, v181 dst_sel:DWORD dst_unused:UNUSED_PAD src0_sel:DWORD src1_sel:WORD_1\n"
    "v_cvt_pk_bf16_f32 v79, v59, v63\n"
    "v_mfma_f32_16x16x32_bf16 v[112:115], a[184:187], v[192:195], v[112:115]\n"
    "v_or_b32_e64 v199, v199, v207\n"
    "v_lshlrev_b32_sdwa v207, 1, v183 dst_sel:DWORD dst_unused:UNUSED_PAD src0_sel:DWORD src1_sel:WORD_1\n"
    "v_mfma_f32_16x16x32_bf16 v[180:183], a[124:127], v[156:159], 0\n"
    "ds_read_u16 v190, v190\n"
    "ds_write_b128 v253, v[68:71] offset:4096\n"
    "v_mfma_f32_16x16x32_bf16 v[180:183], a[172:175], v[160:163], v[180:183]\n"
    "s_waitcnt lgkmcnt(7)\n"
    "v_or_b32_e32 v185, v185, v201\n"
    "v_or_b32_e64 v186, v186, v202\n"
    "v_mfma_f32_16x16x32_bf16 v[180:183], a[240:243], v[164:167], v[180:183]\n"
    "v_pk_fma_f16 v232, v184, v184, s48\n"
    "v_or_b32_e64 v187, v187, v203\n"
    "v_mfma_f32_16x16x32_bf16 v[116:119], a[188:191], v[192:195], v[116:119]\n"
    "ds_read_u16_d16_hi v206, v206\n"
    "ds_read_u16 v191, v191\n"
    "v_mfma_f32_16x16x32_bf16 v[128:131], v[236:239], v[192:195], v[128:131]\n"
    "ds_read_u16_d16_hi v207, v207\n"
    "v_pk_mul_f16 v232, v184, v232\n"
    "v_mfma_f32_16x16x32_bf16 v[132:135], a[244:247], v[192:195], v[132:135]\n"
    "v_exp_f16_e32 v234, v232\n"
    "v_mfma_f32_16x16x32_bf16 v[104:107], a[176:179], v[196:199], v[104:107]\n"
    "s_waitcnt lgkmcnt(6)\n"
    "ds_read_b128 a[240:243], v244 offset:32768\n"
    "v_exp_f16_sdwa v234, v232 dst_sel:WORD_1 dst_unused:UNUSED_PRESERVE src0_sel:WORD_1\n"
    "v_mfma_f32_16x16x32_bf16 v[108:111], a[180:183], v[196:199], v[108:111]\n"
    "v_or_b32_e64 v189, v189, v205\n"
    "v_pk_fma_f16 v234, v234, s49, s49\n"
    "v_mfma_f32_16x16x32_bf16 v[120:123], a[184:187], v[196:199], v[120:123]\n"
    "v_cvt_pk_f16_f32 v192, v168, v169\n"
    "v_lshlrev_b32_sdwa v193, 1, v170 dst_sel:DWORD dst_unused:UNUSED_PAD src0_sel:DWORD src1_sel:WORD_1\n"
    "v_mfma_f32_16x16x32_bf16 v[124:127], a[188:191], v[196:199], v[124:127]\n"
    "ds_read_u16 v193, v193\n"
    "v_lshlrev_b32_sdwa v201, 1, v171 dst_sel:DWORD dst_unused:UNUSED_PAD src0_sel:DWORD src1_sel:WORD_1\n"
    "v_mfma_f32_16x16x32_bf16 v[136:139], v[236:239], v[196:199], v[136:139]\n"
    "s_nop 0\n"
    "ds_read_u16_d16_hi v201, v201\n"
    "ds_read_b128 v[236:239], v244 offset:16384\n"
    "v_mfma_f32_16x16x32_bf16 v[140:143], a[244:247], v[196:199], v[140:143]\n"
    "s_waitcnt lgkmcnt(7)\n"
    "v_lshlrev_b32_sdwa v194, 1, v172 dst_sel:DWORD dst_unused:UNUSED_PAD src0_sel:DWORD src1_sel:WORD_1\n"
    "v_lshlrev_b32_sdwa v202, 1, v173 dst_sel:DWORD dst_unused:UNUSED_PAD src0_sel:DWORD src1_sel:WORD_1\n"
    "v_lshlrev_b32_sdwa v195, 1, v174 dst_sel:DWORD dst_unused:UNUSED_PAD src0_sel:DWORD src1_sel:WORD_1\n"
    "v_lshlrev_b32_sdwa v203, 1, v175 dst_sel:DWORD dst_unused:UNUSED_PAD src0_sel:DWORD src1_sel:WORD_1\n"
    "v_rcp_f16_e64 v232, v234\n"
    "s_barrier\n"
    "v_smfmac_f32_32x32x32_bf16 v[128:143], a[248:251], a[232:239], v241\n"
    "s_waitcnt lgkmcnt(6)\n"
    "ds_read_b128 v[144:147], v242 offset:0\n"
    "ds_read_b128 v[148:151], v242 offset:1024\n"
    "v_or_b32_e64 v190, v190, v206\n"
    "v_mov_b32_e64 v64, v216\n"
    "v_rcp_f16_sdwa v232, v234 dst_sel:WORD_1 dst_unused:UNUSED_PRESERVE src0_sel:WORD_1\n"
    "v_cvt_pk_f16_f32 v196, v176, v177\n"
    "v_mov_b32_e64 v65, v217\n"
    "v_lshlrev_b32_sdwa v197, 1, v178 dst_sel:DWORD dst_unused:UNUSED_PAD src0_sel:DWORD src1_sel:WORD_1\n"
    "v_mov_b32_e64 v66, v218\n"
    "v_lshlrev_b32_sdwa v205, 1, v179 dst_sel:DWORD dst_unused:UNUSED_PAD src0_sel:DWORD src1_sel:WORD_1\n"
    "v_mov_b32_e64 v67, v219\n"
    "v_lshlrev_b32_sdwa v198, 1, v180 dst_sel:DWORD dst_unused:UNUSED_PAD src0_sel:DWORD src1_sel:WORD_1\n"
    "v_mov_b32_e64 v68, v220\n"
    "v_lshlrev_b32_sdwa v206, 1, v181 dst_sel:DWORD dst_unused:UNUSED_PAD src0_sel:DWORD src1_sel:WORD_1\n"
    "v_mov_b32_e64 v69, v221\n"
    "v_lshlrev_b32_sdwa v199, 1, v182 dst_sel:DWORD dst_unused:UNUSED_PAD src0_sel:DWORD src1_sel:WORD_1\n"
    "v_mov_b32_e64 v70, v222\n"
    "v_mov_b32_e64 v71, v223\n"
    "v_fma_mix_f32 v234, v184, v232, 0 op_sel_hi:[1,1,0]\n"
    "s_waitcnt lgkmcnt(5)\n"
    "v_or_b32_e32 v191, v191, v207\n"
    "v_lshlrev_b32_sdwa v207, 1, v183 dst_sel:DWORD dst_unused:UNUSED_PAD src0_sel:DWORD src1_sel:WORD_1\n"
    "v_fma_mix_f32 v232, v184, v232, 0 op_sel:[1,1,0] op_sel_hi:[1,1,0]\n"
    "v_cvt_pk_bf16_f32 v184, v234, v232\n"
    "ds_read_b128 v[152:155], v242 offset:2048\n"
    "ds_read_b128 v[156:159], v242 offset:3072\n"
    "v_pk_fma_f16 v232, v188, v188, s48\n"
    "s_waitcnt lgkmcnt(4)\n"
    "v_or_b32_e32 v193, v193, v201\n"
    "s_waitcnt lgkmcnt(3)\n"
    "v_mfma_f32_16x16x32_bf16 v[168:171], a[80:83], v[144:147], 0\n"
    "s_nop 0\n"
    "ds_read_b128 v[160:163], v242 offset:4096\n"
    "ds_read_b128 v[164:167], v242 offset:5120\n"
    "s_waitcnt lgkmcnt(4)\n"
    "v_mfma_f32_16x16x32_bf16 v[168:171], a[128:131], v[148:151], v[168:171]\n"
    "s_nop 0\n"
    "ds_read_u16 v194, v194\n"
    "ds_read_u16_d16_hi v202, v202\n"
    "v_mfma_f32_16x16x32_bf16 v[172:175], a[84:87], v[144:147], 0\n"
    "ds_read_u16 v195, v195\n"
    "v_pk_mul_f16 v232, v188, v232\n"
    "v_mfma_f32_16x16x32_bf16 v[172:175], a[132:135], v[148:151], v[172:175]\n"
    "ds_read_u16_d16_hi v203, v203\n"
    "v_exp_f16_e64 v234, v232\n"
    "ds_read_b128 a[244:247], v244 offset:18432\n"
    "v_exp_f16_sdwa v234, v232 dst_sel:WORD_1 dst_unused:UNUSED_PRESERVE src0_sel:WORD_1\n"
    "v_mfma_f32_16x16x32_bf16 v[96:99], a[192:195], v[184:187], v[96:99]\n"
    "s_waitcnt lgkmcnt(8)\n"
    "v_pk_fma_f16 v234, v234, s49, s49\n"
    "v_rcp_f16_e64 v232, v234\n"
    "v_mfma_f32_16x16x32_bf16 v[168:171], a[48:51], v[152:155], v[168:171]\n"
    "s_waitcnt lgkmcnt(7)\n"
    "ds_read_u16 v197, v197\n"
    "v_rcp_f16_sdwa v232, v234 dst_sel:WORD_1 dst_unused:UNUSED_PRESERVE src0_sel:WORD_1\n"
    "v_mfma_f32_16x16x32_bf16 v[172:175], a[240:243], v[152:155], v[172:175]\n"
    "s_nop 0\n"
    "ds_read_u16_d16_hi v205, v205\n"
    "v_fma_mix_f32 v234, v188, v232, 0 op_sel_hi:[1,1,0]\n"
    "v_mfma_f32_16x16x32_bf16 v[176:179], a[80:83], v[156:159], 0\n"
    "s_waitcnt lgkmcnt(7)\n"
    "v_fma_mix_f32 v232, v188, v232, 0 op_sel:[1,1,0] op_sel_hi:[1,1,0]\n"
    "v_cvt_pk_bf16_f32 v188, v234, v232\n"
    "v_mfma_f32_16x16x32_bf16 v[176:179], a[128:131], v[160:163], v[176:179]\n"
    "ds_read_u16 v198, v198\n"
    "v_pk_fma_f16 v232, v192, v192, s48\n"
    "v_mfma_f32_16x16x32_bf16 v[176:179], a[48:51], v[164:167], v[176:179]\n"
    "ds_read_u16_d16_hi v206, v206\n"
    "v_pk_mul_f16 v232, v192, v232\n"
    "v_mfma_f32_16x16x32_bf16 v[180:183], a[84:87], v[156:159], 0\n"
    "v_exp_f16_e64 v234, v232\n"
    "v_mfma_f32_16x16x32_bf16 v[180:183], a[132:135], v[160:163], v[180:183]\n"
    "ds_read_u16 v199, v199\n"
    "v_exp_f16_sdwa v234, v232 dst_sel:WORD_1 dst_unused:UNUSED_PRESERVE src0_sel:WORD_1\n"
    "v_mfma_f32_16x16x32_bf16 v[180:183], a[240:243], v[164:167], v[180:183]\n"
    "s_waitcnt lgkmcnt(8)\n"
    "v_or_b32_e32 v194, v194, v202\n"
    "v_pk_fma_f16 v234, v234, s49, s49\n"
    "v_mfma_f32_16x16x32_bf16 v[100:103], a[196:199], v[184:187], v[100:103]\n"
    "ds_read_u16_d16_hi v207, v207\n"
    "v_rcp_f16_e64 v232, v234\n"
    "v_mfma_f32_16x16x32_bf16 v[112:115], a[200:203], v[184:187], v[112:115]\n"
    "v_rcp_f16_sdwa v232, v234 dst_sel:WORD_1 dst_unused:UNUSED_PRESERVE src0_sel:WORD_1\n"
    "v_mfma_f32_16x16x32_bf16 v[116:119], a[204:207], v[184:187], v[116:119]\n"
    "s_waitcnt lgkmcnt(6)\n"
    "v_or_b32_e32 v195, v195, v203\n"
    "v_fma_mix_f32 v234, v192, v232, 0 op_sel_hi:[1,1,0]\n"
    "v_mfma_f32_16x16x32_bf16 v[128:131], v[236:239], v[184:187], v[128:131]\n"
    "v_fma_mix_f32 v232, v192, v232, 0 op_sel:[1,1,0] op_sel_hi:[1,1,0]\n"
    "v_cvt_pk_bf16_f32 v192, v234, v232\n"
    "v_mfma_f32_16x16x32_bf16 v[132:135], a[244:247], v[184:187], v[132:135]\n"
    "v_pk_fma_f16 v232, v196, v196, s48\n"
    "v_pk_mul_f16 v232, v196, v232\n"
    "v_mfma_f32_16x16x32_bf16 v[104:107], a[192:195], v[188:191], v[104:107]\n"
    "v_exp_f16_e32 v234, v232\n"
    "v_mfma_f32_16x16x32_bf16 v[108:111], a[196:199], v[188:191], v[108:111]\n"
    "s_waitcnt lgkmcnt(4)\n"
    "v_or_b32_e32 v197, v197, v205\n"
    "v_exp_f16_sdwa v234, v232 dst_sel:WORD_1 dst_unused:UNUSED_PRESERVE src0_sel:WORD_1\n"
    "v_mfma_f32_16x16x32_bf16 v[120:123], a[200:203], v[188:191], v[120:123]\n"
    "v_mfma_f32_16x16x32_bf16 v[124:127], a[204:207], v[188:191], v[124:127]\n"
    "v_mfma_f32_16x16x32_bf16 v[136:139], v[236:239], v[188:191], v[136:139]\n"
    "s_waitcnt lgkmcnt(2)\n"
    "v_or_b32_e32 v198, v198, v206\n"
    "s_waitcnt lgkmcnt(0)\n"
    "v_or_b32_e32 v199, v199, v207\n"
    "s_mov_b32 s59, s58\n"
    "s_add_u32 s57, s57, 1572864\n"
    "s_add_u32 s58, s58, 1572864\n"
    "s_mov_b32 s70, s46\n"
    "s_add_u32 s88, s57, 98304\n"
    "s_add_u32 s89, s58, 98304\n"
    "s_add_u32 s39, s59, 98304\n"
    "s_add_u32 s64, s64, 1\n"
    "s_cmp_lt_u32 s64, 2\n"
    "s_cselect_b64 s[72:73], s[72:73], s[76:77]\n"
    "s_cselect_b32 s74, s74, s78\n"
    "s_cselect_b32 s53, s53, s79\n"
    "s_cmp_lg_u32 s64, 3\n"
    "s_cbranch_scc1 1b\n"
    ".Lreplk_last:\n"
    "v_lshlrev_b32_sdwa v184, 1, v168 dst_sel:DWORD dst_unused:UNUSED_PAD src0_sel:DWORD src1_sel:WORD_1\n"
    "v_lshlrev_b32_sdwa v200, 1, v169 dst_sel:DWORD dst_unused:UNUSED_PAD src0_sel:DWORD src1_sel:WORD_1\n"
    "v_lshlrev_b32_sdwa v185, 1, v170 dst_sel:DWORD dst_unused:UNUSED_PAD src0_sel:DWORD src1_sel:WORD_1\n"
    "v_lshlrev_b32_sdwa v201, 1, v171 dst_sel:DWORD dst_unused:UNUSED_PAD src0_sel:DWORD src1_sel:WORD_1\n"
    "v_mfma_f32_16x16x32_bf16 v[140:143], a[244:247], v[188:191], v[140:143]\n"
    "ds_read_b128 v[56:59], v244 offset:36864\n"
    "ds_read_u16 v184, v184\n"
    "v_mfma_f32_16x16x32_bf16 v[168:171], a[88:91], v[144:147], 0\n"
    "ds_read_u16_d16_hi v200, v200\n"
    "ds_read_u16 v185, v185\n"
    "v_mfma_f32_16x16x32_bf16 v[168:171], a[136:139], v[148:151], v[168:171]\n"
    "v_lshlrev_b32_sdwa v186, 1, v172 dst_sel:DWORD dst_unused:UNUSED_PAD src0_sel:DWORD src1_sel:WORD_1\n"
    "v_lshlrev_b32_sdwa v202, 1, v173 dst_sel:DWORD dst_unused:UNUSED_PAD src0_sel:DWORD src1_sel:WORD_1\n"
    "v_mfma_f32_16x16x32_bf16 v[168:171], a[52:55], v[152:155], v[168:171]\n"
    "ds_read_u16_d16_hi v201, v201\n"
    "v_lshlrev_b32_sdwa v187, 1, v174 dst_sel:DWORD dst_unused:UNUSED_PAD src0_sel:DWORD src1_sel:WORD_1\n"
    "s_waitcnt vmcnt(1)\n"
    "v_smfmac_f32_32x32x32_bf16 v[128:143], a[248:251], a[224:231], v241\n"
    "s_nop 0\n"
    "ds_read_u16 v186, v186\n"
    "v_lshlrev_b32_sdwa v203, 1, v175 dst_sel:DWORD dst_unused:UNUSED_PAD src0_sel:DWORD src1_sel:WORD_1\n"
    "v_lshlrev_b32_sdwa v188, 1, v176 dst_sel:DWORD dst_unused:UNUSED_PAD src0_sel:DWORD src1_sel:WORD_1\n"
    "v_lshlrev_b32_sdwa v204, 1, v177 dst_sel:DWORD dst_unused:UNUSED_PAD src0_sel:DWORD src1_sel:WORD_1\n"
    "v_lshlrev_b32_sdwa v189, 1, v178 dst_sel:DWORD dst_unused:UNUSED_PAD src0_sel:DWORD src1_sel:WORD_1\n"
    "v_mfma_f32_16x16x32_bf16 v[172:175], a[92:95], v[144:147], 0\n"
    "ds_read_b128 v[236:239], v244 offset:20480\n"
    "v_lshlrev_b32_sdwa v205, 1, v179 dst_sel:DWORD dst_unused:UNUSED_PAD src0_sel:DWORD src1_sel:WORD_1\n"
    "v_mfma_f32_16x16x32_bf16 v[172:175], a[140:143], v[148:151], v[172:175]\n"
    "s_nop 0\n"
    "ds_read_u16_d16_hi v202, v202\n"
    "v_lshlrev_b32_sdwa v190, 1, v180 dst_sel:DWORD dst_unused:UNUSED_PAD src0_sel:DWORD src1_sel:WORD_1\n"
    "v_mfma_f32_16x16x32_bf16 v[176:179], a[88:91], v[156:159], 0\n"
    "s_waitcnt lgkmcnt(7)\n"
    "v_lshlrev_b32_sdwa v206, 1, v181 dst_sel:DWORD dst_unused:UNUSED_PAD src0_sel:DWORD src1_sel:WORD_1\n"
    "v_lshlrev_b32_sdwa v191, 1, v182 dst_sel:DWORD dst_unused:UNUSED_PAD src0_sel:DWORD src1_sel:WORD_1\n"
    "v_mfma_f32_16x16x32_bf16 v[172:175], v[56:59], v[152:155], v[172:175]\n"
    "ds_read_u16 v187, v187\n"
    "buffer_load_dwordx4 a[232:235], v245, s[40:43], s57 offen offset:64\n"
    "buffer_load_dwordx4 a[224:227], v245, s[40:43], s57 offen offset:0\n"
    "buffer_load_dwordx4 v[208:211], v245, s[40:43], s57 offen offset:128\n"
    "v_mfma_f32_16x16x32_bf16 v[176:179], a[136:139], v[160:163], v[176:179]\n"
    "ds_read_b128 a[244:247], v244 offset:22528\n"
    "v_lshlrev_b32_sdwa v207, 1, v183 dst_sel:DWORD dst_unused:UNUSED_PAD src0_sel:DWORD src1_sel:WORD_1\n"
    "v_mfma_f32_16x16x32_bf16 v[176:179], a[52:55], v[164:167], v[176:179]\n"
    "ds_read_u16_d16_hi v203, v203\n"
    "v_pk_fma_f16 v234, v234, s49, s49\n"
    "v_mfma_f32_16x16x32_bf16 v[180:183], a[92:95], v[156:159], 0\n"
    "s_waitcnt lgkmcnt(8)\n"
    "v_rcp_f16_e32 v232, v234\n"
    "v_mfma_f32_16x16x32_bf16 v[180:183], a[140:143], v[160:163], v[180:183]\n"
    "ds_read_u16 v188, v188\n"
    "v_rcp_f16_sdwa v232, v234 dst_sel:WORD_1 dst_unused:UNUSED_PRESERVE src0_sel:WORD_1\n"
    "v_mfma_f32_16x16x32_bf16 v[180:183], v[56:59], v[164:167], v[180:183]\n"
    "ds_read_u16_d16_hi v204, v204\n"
    "v_fma_mix_f32 v234, v196, v232, 0 op_sel_hi:[1,1,0]\n"
    "v_mfma_f32_16x16x32_bf16 v[96:99], a[208:211], v[192:195], v[96:99]\n"
    "v_fma_mix_f32 v232, v196, v232, 0 op_sel:[1,1,0] op_sel_hi:[1,1,0]\n"
    "v_or_b32_e32 v184, v184, v200\n"
    "v_mfma_f32_16x16x32_bf16 v[100:103], a[212:215], v[192:195], v[100:103]\n"
    "s_waitcnt lgkmcnt(8)\n"
    "ds_read_u16 v189, v189\n"
    "v_cvt_pk_bf16_f32 v196, v234, v232\n"
    "v_mfma_f32_16x16x32_bf16 v[112:115], a[216:219], v[192:195], v[112:115]\n"
    "s_waitcnt lgkmcnt(7)\n"
    "v_or_b32_e32 v185, v185, v201\n"
    "v_lshlrev_b32_sdwa v201, 1, v171 dst_sel:DWORD dst_unused:UNUSED_PAD src0_sel:DWORD src1_sel:WORD_1\n"
    "v_mfma_f32_16x16x32_bf16 v[116:119], a[220:223], v[192:195], v[116:119]\n"
    "ds_read_u16_d16_hi v205, v205\n"
    "v_cvt_pk_bf16_f32 v96, v96, v97\n"
    "v_mfma_f32_16x16x32_bf16 v[128:131], v[236:239], v[192:195], v[128:131]\n"
    "ds_read_u16 v190, v190\n"
    "v_cvt_pk_bf16_f32 v97, v98, v99\n"
    "v_mfma_f32_16x16x32_bf16 v[104:107], a[208:211], v[196:199], v[104:107]\n"
    "s_waitcnt lgkmcnt(8)\n"
    "v_or_b32_e32 v186, v186, v202\n"
    "v_lshlrev_b32_sdwa v202, 1, v173 dst_sel:DWORD dst_unused:UNUSED_PAD src0_sel:DWORD src1_sel:WORD_1\n"
    "v_mfma_f32_16x16x32_bf16 v[108:111], a[212:215], v[196:199], v[108:111]\n"
    "ds_read_u16_d16_hi v206, v206\n"
    "v_cvt_pk_bf16_f32 v98, v100, v101\n"
    "s_waitcnt lgkmcnt(7)\n"
    "v_mfma_f32_16x16x32_bf16 v[132:135], a[244:247], v[192:195], v[132:135]\n"
    "s_nop 0\n"
    "v_cvt_pk_f16_f32 v192, v168, v169\n"
    "v_lshlrev_b32_sdwa v193, 1, v170 dst_sel:DWORD dst_unused:UNUSED_PAD src0_sel:DWORD src1_sel:WORD_1\n"
    "v_mfma_f32_16x16x32_bf16 v[120:123], a[216:219], v[196:199], v[120:123]\n"
    "s_nop 0\n"
    "ds_read_u16 v191, v191\n"
    "v_lshlrev_b32_sdwa v194, 1, v172 dst_sel:DWORD dst_unused:UNUSED_PAD src0_sel:DWORD src1_sel:WORD_1\n"
    "v_mfma_f32_16x16x32_bf16 v[124:127], a[220:223], v[196:199], v[124:127]\n"
    "s_waitcnt lgkmcnt(7)\n"
    "ds_read_u16_d16_hi v207, v207\n"
    "buffer_load_dwordx4 a[236:239], v245, s[40:43], s88 offen offset:64\n"
    "buffer_load_dwordx4 a[228:231], v245, s[40:43], s88 offen offset:0\n"
    "buffer_load_dwordx4 v[212:215], v245, s[40:43], s88 offen offset:128\n"
    "v_mfma_f32_16x16x32_bf16 v[136:139], v[236:239], v[196:199], v[136:139]\n"
    "v_lshlrev_b32_sdwa v195, 1, v174 dst_sel:DWORD dst_unused:UNUSED_PAD src0_sel:DWORD src1_sel:WORD_1\n"
    "v_or_b32_e32 v187, v187, v203\n"
    "v_mfma_f32_16x16x32_bf16 v[140:143], a[244:247], v[196:199], v[140:143]\n"
    "ds_read_u16 v193, v193\n"
    "v_lshlrev_b32_sdwa v203, 1, v175 dst_sel:DWORD dst_unused:UNUSED_PAD src0_sel:DWORD src1_sel:WORD_1\n"
    "v_mfma_f32_16x16x32_bf16 v[168:171], a[96:99], v[144:147], 0\n"
    "s_waitcnt lgkmcnt(7)\n"
    "ds_read_u16_d16_hi v201, v201\n"
    "v_cvt_pk_f16_f32 v196, v176, v177\n"
    "v_mfma_f32_16x16x32_bf16 v[168:171], a[144:147], v[148:151], v[168:171]\n"
    "v_lshlrev_b32_sdwa v197, 1, v178 dst_sel:DWORD dst_unused:UNUSED_PAD src0_sel:DWORD src1_sel:WORD_1\n"
    "v_lshlrev_b32_sdwa v198, 1, v180 dst_sel:DWORD dst_unused:UNUSED_PAD src0_sel:DWORD src1_sel:WORD_1\n"
    "v_mfma_f32_16x16x32_bf16 v[168:171], a[56:59], v[152:155], v[168:171]\n"
    "ds_read_u16 v194, v194\n"
    "v_lshlrev_b32_sdwa v199, 1, v182 dst_sel:DWORD dst_unused:UNUSED_PAD src0_sel:DWORD src1_sel:WORD_1\n"
    "v_mfma_f32_16x16x32_bf16 v[172:175], a[100:103], v[144:147], 0\n"
    "v_or_b32_e64 v188, v188, v204\n"
    "v_cvt_pk_bf16_f32 v99, v102, v103\n"
    "v_mfma_f32_16x16x32_bf16 v[172:175], a[148:151], v[148:151], v[172:175]\n"
    "s_waitcnt lgkmcnt(7)\n"
    "ds_read_u16_d16_hi v202, v202\n"
    "v_or_b32_e32 v189, v189, v205\n"
    "v_mfma_f32_16x16x32_bf16 v[172:175], a[60:63], v[152:155], v[172:175]\n"
    "v_lshlrev_b32_sdwa v205, 1, v179 dst_sel:DWORD dst_unused:UNUSED_PAD src0_sel:DWORD src1_sel:WORD_1\n"
    "v_cvt_pk_bf16_f32 v112, v112, v113\n"
    "v_mfma_f32_16x16x32_bf16 v[176:179], a[96:99], v[156:159], 0\n"
    "ds_read_b128 v[0:3], v244 offset:0\n"
    "ds_write_b128 v243, v[96:99] offset:24640\n"
    "v_mfma_f32_16x16x32_bf16 v[176:179], a[144:147], v[160:163], v[176:179]\n"
    "s_waitcnt lgkmcnt(8)\n"
    "v_or_b32_e32 v190, v190, v206\n"
    "v_lshlrev_b32_sdwa v206, 1, v181 dst_sel:DWORD dst_unused:UNUSED_PAD src0_sel:DWORD src1_sel:WORD_1\n"
    "v_mfma_f32_16x16x32_bf16 v[176:179], a[56:59], v[164:167], v[176:179]\n"
    "v_cvt_pk_bf16_f32 v113, v114, v115\n"
    "v_cvt_pk_bf16_f32 v114, v116, v117\n"
    "v_mfma_f32_16x16x32_bf16 v[96:99], a[0:3], v[184:187], 0\n"
    "s_nop 0\n"
    "ds_read_b128 a[252:255], v249 offset:0\n"
    "ds_read_u16 v195, v195\n"
    "v_mfma_f32_16x16x32_bf16 v[100:103], a[4:7], v[184:187], 0\n"
    "s_waitcnt lgkmcnt(8)\n"
    "ds_read_u16_d16_hi v203, v203\n"
    "v_cvt_pk_bf16_f32 v115, v118, v119\n"
    "v_cvt_pk_bf16_f32 v128, v128, v129\n"
    "v_cvt_pk_bf16_f32 v129, v130, v131\n"
    "v_or_b32_e64 v191, v191, v207\n"
    "v_mfma_f32_16x16x32_bf16 v[116:119], a[12:15], v[184:187], 0\n"
    "ds_read_b128 v[4:7], v244 offset:2048\n"
    "v_lshlrev_b32_sdwa v207, 1, v183 dst_sel:DWORD dst_unused:UNUSED_PAD src0_sel:DWORD src1_sel:WORD_1\n"
    "v_mfma_f32_16x16x32_bf16 v[180:183], a[100:103], v[156:159], 0\n"
    "ds_read_u16 v197, v197\n"
    "v_cvt_pk_bf16_f32 v130, v132, v133\n"
    "v_mfma_f32_16x16x32_bf16 v[180:183], a[148:151], v[160:163], v[180:183]\n"
    "s_waitcnt lgkmcnt(9)\n"
    "v_or_b32_e32 v193, v193, v201\n"
    "v_cvt_pk_bf16_f32 v131, v134, v135\n"
    "v_mfma_f32_16x16x32_bf16 v[180:183], a[60:63], v[164:167], v[180:183]\n"
    "ds_read_u16_d16_hi v205, v205\n"
    "v_cvt_pk_bf16_f32 v104, v104, v105\n"
    "v_cvt_pk_bf16_f32 v105, v106, v107\n"
    "v_cvt_pk_bf16_f32 v106, v108, v109\n"
    "v_cvt_pk_bf16_f32 v107, v110, v111\n"
    "v_mfma_f32_16x16x32_bf16 v[108:111], a[4:7], v[188:191], 0\n"
    "s_waitcnt lgkmcnt(7)\n"
    "ds_read_u16 v198, v198\n"
    "v_or_b32_e32 v194, v194, v202\n"
    "v_cvt_pk_bf16_f32 v120, v120, v121\n"
    "v_cvt_pk_bf16_f32 v121, v122, v123\n"
    "v_cvt_pk_bf16_f32 v122, v124, v125\n"
    "v_cvt_pk_bf16_f32 v123, v126, v127\n"
    "ds_write_b128 v243, v[112:115] offset:26688\n"
    "s_waitcnt lgkmcnt(7)\n"
    "buffer_store_dwordx4 a[252:255], v255, s[68:71], s59 offen offset:0 sc1\n"
    "v_mfma_f32_16x16x32_bf16 v[112:115], a[8:11], v[184:187], 0\n"
    "ds_read_u16_d16_hi v206, v206\n"
    "ds_write_b128 v243, v[128:131] offset:28736\n"
    "v_mfma_f32_16x16x32_bf16 v[128:131], v[0:3], v[184:187], 0\n"
    "s_waitcnt lgkmcnt(6)\n"
    "v_pk_fma_f16 v232, v192, v192, s48\n"
    "v_cvt_pk_bf16_f32 v136, v136, v137\n"
    "v_mfma_f32_16x16x32_bf16 v[132:135], v[4:7], v[184:187], 0\n"
    "v_cvt_pk_bf16_f32 v137, v138, v139\n"
    "v_cvt_pk_bf16_f32 v138, v140, v141\n"
    "v_mfma_f32_16x16x32_bf16 v[124:127], a[12:15], v[188:191], 0\n"
    "ds_read_u16 v199, v199\n"
    "ds_write_b128 v243, v[104:107] offset:25664\n"
    "v_mfma_f32_16x16x32_bf16 v[104:107], a[0:3], v[188:191], 0\n"
    "v_or_b32_e64 v195, v195, v203\n"
    "v_cvt_pk_bf16_f32 v139, v142, v143\n"
    "v_pk_mul_f16 v232, v192, v232\n"
    "s_waitcnt lgkmcnt(6)\n"
    "v_or_b32_e32 v197, v197, v205\n"
    "v_exp_f16_e64 v234, v232\n"
    "v_mfma_f32_16x16x32_bf16 v[140:143], v[4:7], v[188:191], 0\n"
    "ds_read_u16_d16_hi v207, v207\n"
    "ds_write_b128 v243, v[120:123] offset:27712\n"
    "v_mfma_f32_16x16x32_bf16 v[120:123], a[8:11], v[188:191], 0\n"
    "v_exp_f16_sdwa v234, v232 dst_sel:WORD_1 dst_unused:UNUSED_PRESERVE src0_sel:WORD_1\n"
    "v_cvt_pk_f16_f32 v184, v168, v169\n"
    "v_pk_fma_f16 v234, v234, s49, s49\n"
    "v_lshlrev_b32_sdwa v185, 1, v170 dst_sel:DWORD dst_unused:UNUSED_PAD src0_sel:DWORD src1_sel:WORD_1\n"
    "v_lshlrev_b32_sdwa v201, 1, v171 dst_sel:DWORD dst_unused:UNUSED_PAD src0_sel:DWORD src1_sel:WORD_1\n"
    "v_mfma_f32_16x16x32_bf16 v[168:171], a[104:107], v[144:147], 0\n"
    "s_waitcnt lgkmcnt(4)\n"
    "ds_read_b128 a[252:255], v249 offset:2048\n"
    "ds_write_b128 v243, v[136:139] offset:29760\n"
    "v_mfma_f32_16x16x32_bf16 v[136:139], v[0:3], v[188:191], 0\n"
    "s_nop 0\n"
    "v_lshlrev_b32_sdwa v186, 1, v172 dst_sel:DWORD dst_unused:UNUSED_PAD src0_sel:DWORD src1_sel:WORD_1\n"
    "v_lshlrev_b32_sdwa v202, 1, v173 dst_sel:DWORD dst_unused:UNUSED_PAD src0_sel:DWORD src1_sel:WORD_1\n"
    "v_mfma_f32_16x16x32_bf16 v[168:171], a[152:155], v[148:151], v[168:171]\n"
    "v_lshlrev_b32_sdwa v187, 1, v174 dst_sel:DWORD dst_unused:UNUSED_PAD src0_sel:DWORD src1_sel:WORD_1\n"
    "v_lshlrev_b32_sdwa v203, 1, v175 dst_sel:DWORD dst_unused:UNUSED_PAD src0_sel:DWORD src1_sel:WORD_1\n"
    "v_rcp_f16_e64 v232, v234\n"
    "v_mfma_f32_16x16x32_bf16 v[168:171], a[64:67], v[152:155], v[168:171]\n"
    "ds_read_u16 v185, v185\n"
    "ds_read_u16_d16_hi v201, v201\n"
    "v_or_b32_e64 v198, v198, v206\n"
    "v_rcp_f16_sdwa v232, v234 dst_sel:WORD_1 dst_unused:UNUSED_PRESERVE src0_sel:WORD_1\n"
    "v_mfma_f32_16x16x32_bf16 v[172:175], a[108:111], v[144:147], 0\n"
    "ds_read_u16 v186, v186\n"
    "ds_read_u16_d16_hi v202, v202\n"
    "v_mfma_f32_16x16x32_bf16 v[172:175], a[156:159], v[148:151], v[172:175]\n"
    "v_cvt_pk_f16_f32 v188, v176, v177\n"
    "v_lshlrev_b32_sdwa v189, 1, v178 dst_sel:DWORD dst_unused:UNUSED_PAD src0_sel:DWORD src1_sel:WORD_1\n"
    "v_lshlrev_b32_sdwa v205, 1, v179 dst_sel:DWORD dst_unused:UNUSED_PAD src0_sel:DWORD src1_sel:WORD_1\n"
    "v_lshlrev_b32_sdwa v190, 1, v180 dst_sel:DWORD dst_unused:UNUSED_PAD src0_sel:DWORD src1_sel:WORD_1\n"
    "v_lshlrev_b32_sdwa v206, 1, v181 dst_sel:DWORD dst_unused:UNUSED_PAD src0_sel:DWORD src1_sel:WORD_1\n"
    "v_lshlrev_b32_sdwa v191, 1, v182 dst_sel:DWORD dst_unused:UNUSED_PAD src0_sel:DWORD src1_sel:WORD_1\n"
    "v_fma_mix_f32 v234, v192, v232, 0 op_sel_hi:[1,1,0]\n"
    "v_mfma_f32_16x16x32_bf16 v[172:175], a[68:71], v[152:155], v[172:175]\n"
    "s_waitcnt lgkmcnt(5)\n"
    "ds_read_b128 v[8:11], v244 offset:4096\n"
    "buffer_store_dwordx4 a[252:255], v255, s[68:71], s59 offen offset:64 sc1\n"
    "v_or_b32_e32 v199, v199, v207\n"
    "v_lshlrev_b32_sdwa v207, 1, v183 dst_sel:DWORD dst_unused:UNUSED_PAD src0_sel:DWORD src1_sel:WORD_1\n"
    "v_fma_mix_f32 v232, v192, v232, 0 op_sel:[1,1,0] op_sel_hi:[1,1,0]\n"
    "v_mfma_f32_16x16x32_bf16 v[176:179], a[104:107], v[156:159], 0\n"
    "ds_read_u16 v187, v187\n"
    "ds_read_u16_d16_hi v203, v203\n"
    "v_cvt_pk_bf16_f32 v192, v234, v232\n"
    "v_mfma_f32_16x16x32_bf16 v[176:179], a[152:155], v[160:163], v[176:179]\n"
    "ds_read_b128 v[12:15], v244 offset:6144\n"
    "ds_read_b128 a[252:255], v249 offset:4096\n"
    "v_mfma_f32_16x16x32_bf16 v[176:179], a[64:67], v[164:167], v[176:179]\n"
    "ds_read_u16 v189, v189\n"
    "v_pk_fma_f16 v232, v196, v196, s48\n"
    "v_mfma_f32_16x16x32_bf16 v[180:183], a[108:111], v[156:159], 0\n"
    "v_pk_mul_f16 v232, v196, v232\n"
    "v_lshlrev_b32_sdwa v200, 1, v169 dst_sel:DWORD dst_unused:UNUSED_PAD src0_sel:DWORD src1_sel:WORD_1\n"
    "v_mfma_f32_16x16x32_bf16 v[180:183], a[156:159], v[160:163], v[180:183]\n"
    "s_waitcnt lgkmcnt(8)\n"
    "ds_read_u16_d16_hi v205, v205\n"
    "v_exp_f16_e32 v234, v232\n"
    "v_mfma_f32_16x16x32_bf16 v[180:183], a[68:71], v[164:167], v[180:183]\n"
    "ds_read_u16 v190, v190\n"
    "v_exp_f16_sdwa v234, v232 dst_sel:WORD_1 dst_unused:UNUSED_PRESERVE src0_sel:WORD_1\n"
    "v_mfma_f32_16x16x32_bf16 v[96:99], a[16:19], v[192:195], v[96:99]\n"
    "v_pk_fma_f16 v234, v234, s49, s49\n"
    "v_or_b32_e32 v185, v185, v201\n"
    "v_mfma_f32_16x16x32_bf16 v[100:103], a[20:23], v[192:195], v[100:103]\n"
    "s_waitcnt lgkmcnt(7)\n"
    "ds_read_u16_d16_hi v206, v206\n"
    "v_rcp_f16_e64 v232, v234\n"
    "v_mfma_f32_16x16x32_bf16 v[112:115], a[24:27], v[192:195], v[112:115]\n"
    "ds_read_u16 v191, v191\n"
    "v_rcp_f16_sdwa v232, v234 dst_sel:WORD_1 dst_unused:UNUSED_PRESERVE src0_sel:WORD_1\n"
    "v_mfma_f32_16x16x32_bf16 v[116:119], a[28:31], v[192:195], v[116:119]\n"
    "v_fma_mix_f32 v234, v196, v232, 0 op_sel_hi:[1,1,0]\n"
    "v_fma_mix_f32 v232, v196, v232, 0 op_sel:[1,1,0] op_sel_hi:[1,1,0]\n"
    "v_mfma_f32_16x16x32_bf16 v[128:131], v[8:11], v[192:195], v[128:131]\n"
    "ds_read_u16_d16_hi v207, v207\n"
    "v_or_b32_e64 v186, v186, v202\n"
    "s_waitcnt lgkmcnt(7)\n"
    "v_mfma_f32_16x16x32_bf16 v[132:135], v[12:15], v[192:195], v[132:135]\n"
    "ds_read_b128 v[52:55], v244 offset:34816\n"
    "s_waitcnt lgkmcnt(7)\n"
    "buffer_store_dwordx4 a[252:255], v255, s[68:71], s59 offen offset:128 sc1\n"
    "v_cvt_pk_bf16_f32 v196, v234, v232\n"
    "v_pk_fma_f16 v232, v184, v184, s48\n"
    "v_or_b32_e64 v187, v187, v203\n"
    "v_mfma_f32_16x16x32_bf16 v[104:107], a[16:19], v[196:199], v[104:107]\n"
    "ds_read_b128 a[252:255], v249 offset:1024\n"
    "ds_read_u16_d16_hi v200, v200\n"
    "v_mfma_f32_16x16x32_bf16 v[108:111], a[20:23], v[196:199], v[108:111]\n"
    "v_pk_mul_f16 v232, v184, v232\n"
    "v_lshlrev_b32_sdwa v192, 1, v168 dst_sel:DWORD dst_unused:UNUSED_PAD src0_sel:DWORD src1_sel:WORD_1\n"
    "v_mfma_f32_16x16x32_bf16 v[120:123], a[24:27], v[196:199], v[120:123]\n"
    "s_waitcnt lgkmcnt(7)\n"
    "ds_read_u16 v192, v192\n"
    "v_exp_f16_e32 v234, v232\n"
    "v_mfma_f32_16x16x32_bf16 v[124:127], a[28:31], v[196:199], v[124:127]\n"
    "v_or_b32_e32 v189, v189, v205\n"
    "v_exp_f16_sdwa v234, v232 dst_sel:WORD_1 dst_unused:UNUSED_PRESERVE src0_sel:WORD_1\n"
    "v_mfma_f32_16x16x32_bf16 v[136:139], v[8:11], v[196:199], v[136:139]\n"
    "ds_read_b128 v[16:19], v244 offset:8192\n"
    "v_pk_fma_f16 v234, v234, s49, s49\n"
    "v_mfma_f32_16x16x32_bf16 v[140:143], v[12:15], v[196:199], v[140:143]\n"
    "s_waitcnt lgkmcnt(7)\n"
    "v_lshlrev_b32_sdwa v193, 1, v170 dst_sel:DWORD dst_unused:UNUSED_PAD src0_sel:DWORD src1_sel:WORD_1\n"
    "v_lshlrev_b32_sdwa v201, 1, v171 dst_sel:DWORD dst_unused:UNUSED_PAD src0_sel:DWORD src1_sel:WORD_1\n"
    "v_mfma_f32_16x16x32_bf16 v[168:171], a[112:115], v[144:147], 0\n"
    "ds_read_u16 v193, v193\n"
    "ds_read_u16_d16_hi v201, v201\n"
    "v_mfma_f32_16x16x32_bf16 v[168:171], a[160:163], v[148:151], v[168:171]\n"
    "v_lshlrev_b32_sdwa v194, 1, v172 dst_sel:DWORD dst_unused:UNUSED_PAD src0_sel:DWORD src1_sel:WORD_1\n"
    "v_lshlrev_b32_sdwa v202, 1, v173 dst_sel:DWORD dst_unused:UNUSED_PAD src0_sel:DWORD src1_sel:WORD_1\n"
    "v_lshlrev_b32_sdwa v195, 1, v174 dst_sel:DWORD dst_unused:UNUSED_PAD src0_sel:DWORD src1_sel:WORD_1\n"
    "v_lshlrev_b32_sdwa v203, 1, v175 dst_sel:DWORD dst_unused:UNUSED_PAD src0_sel:DWORD src1_sel:WORD_1\n"
    "v_rcp_f16_e32 v232, v234\n"
    "v_mfma_f32_16x16x32_bf16 v[168:171], a[72:75], v[152:155], v[168:171]\n"
    "s_waitcnt lgkmcnt(6)\n"
    "ds_read_u16 v194, v194\n"
    "s_waitcnt lgkmcnt(6)\n"
    "buffer_store_dwordx4 a[252:255], v255, s[68:71], s39 offen offset:0 sc1\n"
    "v_or_b32_e32 v190, v190, v206\n"
    "v_rcp_f16_sdwa v232, v234 dst_sel:WORD_1 dst_unused:UNUSED_PRESERVE src0_sel:WORD_1\n"
    "v_mfma_f32_16x16x32_bf16 v[172:175], a[116:119], v[144:147], 0\n"
    "ds_read_u16_d16_hi v202, v202\n"
    "ds_read_u16 v195, v195\n"
    "v_mfma_f32_16x16x32_bf16 v[172:175], a[164:167], v[148:151], v[172:175]\n"
    "ds_read_b128 v[20:23], v244 offset:10240\n"
    "ds_read_u16_d16_hi v203, v203\n"
    "v_lshlrev_b32_sdwa v196, 1, v176 dst_sel:DWORD dst_unused:UNUSED_PAD src0_sel:DWORD src1_sel:WORD_1\n"
    "v_lshlrev_b32_sdwa v204, 1, v177 dst_sel:DWORD dst_unused:UNUSED_PAD src0_sel:DWORD src1_sel:WORD_1\n"
    "v_lshlrev_b32_sdwa v197, 1, v178 dst_sel:DWORD dst_unused:UNUSED_PAD src0_sel:DWORD src1_sel:WORD_1\n"
    "v_lshlrev_b32_sdwa v205, 1, v179 dst_sel:DWORD dst_unused:UNUSED_PAD src0_sel:DWORD src1_sel:WORD_1\n"
    "v_lshlrev_b32_sdwa v198, 1, v180 dst_sel:DWORD dst_unused:UNUSED_PAD src0_sel:DWORD src1_sel:WORD_1\n"
    "v_lshlrev_b32_sdwa v206, 1, v181 dst_sel:DWORD dst_unused:UNUSED_PAD src0_sel:DWORD src1_sel:WORD_1\n"
    "v_lshlrev_b32_sdwa v199, 1, v182 dst_sel:DWORD dst_unused:UNUSED_PAD src0_sel:DWORD src1_sel:WORD_1\n"
    "v_fma_mix_f32 v234, v184, v232, 0 op_sel_hi:[1,1,0]\n"
    "v_mfma_f32_16x16x32_bf16 v[172:175], v[52:55], v[152:155], v[172:175]\n"
    "s_waitcnt lgkmcnt(7)\n"
    "ds_read_u16 v196, v196\n"
    "ds_read_u16_d16_hi v204, v204\n"
    "v_or_b32_e32 v191, v191, v207\n"
    "v_lshlrev_b32_sdwa v207, 1, v183 dst_sel:DWORD dst_unused:UNUSED_PAD src0_sel:DWORD src1_sel:WORD_1\n"
    "v_fma_mix_f32 v232, v184, v232, 0 op_sel:[1,1,0] op_sel_hi:[1,1,0]\n"
    "v_mfma_f32_16x16x32_bf16 v[176:179], a[112:115], v[156:159], 0\n"
    "ds_read_u16 v197, v197\n"
    "v_cvt_pk_bf16_f32 v184, v234, v232\n"
    "v_mfma_f32_16x16x32_bf16 v[176:179], a[160:163], v[160:163], v[176:179]\n"
    "v_pk_fma_f16 v232, v188, v188, s48\n"
    "v_or_b32_e32 v192, v192, v200\n"
    "v_mfma_f32_16x16x32_bf16 v[176:179], a[72:75], v[164:167], v[176:179]\n"
    "s_waitcnt lgkmcnt(8)\n"
    "ds_read_b128 a[252:255], v249 offset:3072\n"
    "v_pk_mul_f16 v232, v188, v232\n"
    "v_mfma_f32_16x16x32_bf16 v[180:183], a[116:119], v[156:159], 0\n"
    "ds_read_u16_d16_hi v205, v205\n"
    "v_exp_f16_e32 v234, v232\n"
    "v_mfma_f32_16x16x32_bf16 v[180:183], a[164:167], v[160:163], v[180:183]\n"
    "s_waitcnt lgkmcnt(8)\n"
    "v_exp_f16_sdwa v234, v232 dst_sel:WORD_1 dst_unused:UNUSED_PRESERVE src0_sel:WORD_1\n"
    "v_mfma_f32_16x16x32_bf16 v[180:183], v[52:55], v[164:167], v[180:183]\n"
    "ds_read_u16 v198, v198\n"
    "v_pk_fma_f16 v234, v234, s49, s49\n"
    "v_mfma_f32_16x16x32_bf16 v[96:99], a[32:35], v[184:187], v[96:99]\n"
    "ds_read_u16_d16_hi v206, v206\n"
    "v_rcp_f16_e64 v232, v234\n"
    "v_mfma_f32_16x16x32_bf16 v[100:103], a[36:39], v[184:187], v[100:103]\n"
    "v_rcp_f16_sdwa v232, v234 dst_sel:WORD_1 dst_unused:UNUSED_PRESERVE src0_sel:WORD_1\n"
    "v_mfma_f32_16x16x32_bf16 v[112:115], a[40:43], v[184:187], v[112:115]\n"
    "ds_read_u16 v199, v199\n"
    "v_fma_mix_f32 v234, v188, v232, 0 op_sel_hi:[1,1,0]\n"
    "v_mfma_f32_16x16x32_bf16 v[116:119], a[44:47], v[184:187], v[116:119]\n"
    "s_waitcnt lgkmcnt(8)\n"
    "v_fma_mix_f32 v232, v188, v232, 0 op_sel:[1,1,0] op_sel_hi:[1,1,0]\n"
    "v_or_b32_e32 v193, v193, v201\n"
    "v_mfma_f32_16x16x32_bf16 v[128:131], v[16:19], v[184:187], v[128:131]\n"
    "ds_read_u16_d16_hi v207, v207\n"
    "v_cvt_pk_bf16_f32 v188, v234, v232\n"
    "v_mfma_f32_16x16x32_bf16 v[132:135], v[20:23], v[184:187], v[132:135]\n"
    "s_nop 0\n"
    "ds_read_b128 v[60:63], v244 offset:38912\n"
    "v_cvt_pk_f16_f32 v184, v168, v169\n"
    "v_mfma_f32_16x16x32_bf16 v[104:107], a[32:35], v[188:191], v[104:107]\n"
    "s_waitcnt lgkmcnt(8)\n"
    "ds_read_b128 v[24:27], v244 offset:12288\n"
    "v_lshlrev_b32_sdwa v185, 1, v170 dst_sel:DWORD dst_unused:UNUSED_PAD src0_sel:DWORD src1_sel:WORD_1\n"
    "v_mfma_f32_16x16x32_bf16 v[108:111], a[36:39], v[188:191], v[108:111]\n"
    "ds_read_u16 v185, v185\n"
    "s_waitcnt lgkmcnt(8)\n"
    "buffer_store_dwordx4 a[252:255], v255, s[68:71], s39 offen offset:64 sc1\n"
    "v_mfma_f32_16x16x32_bf16 v[120:123], a[40:43], v[188:191], v[120:123]\n"
    "v_lshlrev_b32_sdwa v201, 1, v171 dst_sel:DWORD dst_unused:UNUSED_PAD src0_sel:DWORD src1_sel:WORD_1\n"
    "v_lshlrev_b32_sdwa v186, 1, v172 dst_sel:DWORD dst_unused:UNUSED_PAD src0_sel:DWORD src1_sel:WORD_1\n"
    "v_mfma_f32_16x16x32_bf16 v[124:127], a[44:47], v[188:191], v[124:127]\n"
    "s_waitcnt lgkmcnt(7)\n"
    "ds_read_b128 a[252:255], v249 offset:5120\n"
    "ds_read_u16_d16_hi v201, v201\n"
    "v_mfma_f32_16x16x32_bf16 v[136:139], v[16:19], v[188:191], v[136:139]\n"
    "v_or_b32_e64 v194, v194, v202\n"
    "v_lshlrev_b32_sdwa v202, 1, v173 dst_sel:DWORD dst_unused:UNUSED_PAD src0_sel:DWORD src1_sel:WORD_1\n"
    "v_mfma_f32_16x16x32_bf16 v[140:143], v[20:23], v[188:191], v[140:143]\n"
    "ds_read_u16 v186, v186\n"
    "v_lshlrev_b32_sdwa v187, 1, v174 dst_sel:DWORD dst_unused:UNUSED_PAD src0_sel:DWORD src1_sel:WORD_1\n"
    "v_mfma_f32_16x16x32_bf16 v[168:171], a[120:123], v[144:147], 0\n"
    "s_nop 0\n"
    "ds_read_u16_d16_hi v202, v202\n"
    "v_cvt_pk_f16_f32 v188, v176, v177\n"
    "v_mfma_f32_16x16x32_bf16 v[168:171], a[168:171], v[148:151], v[168:171]\n"
    "s_waitcnt lgkmcnt(9)\n"
    "v_lshlrev_b32_sdwa v189, 1, v178 dst_sel:DWORD dst_unused:UNUSED_PAD src0_sel:DWORD src1_sel:WORD_1\n"
    "v_lshlrev_b32_sdwa v190, 1, v180 dst_sel:DWORD dst_unused:UNUSED_PAD src0_sel:DWORD src1_sel:WORD_1\n"
    "v_mfma_f32_16x16x32_bf16 v[168:171], a[76:79], v[152:155], v[168:171]\n"
    "ds_read_u16 v187, v187\n"
    "v_lshlrev_b32_sdwa v191, 1, v182 dst_sel:DWORD dst_unused:UNUSED_PAD src0_sel:DWORD src1_sel:WORD_1\n"
    "v_or_b32_e64 v195, v195, v203\n"
    "v_lshlrev_b32_sdwa v203, 1, v175 dst_sel:DWORD dst_unused:UNUSED_PAD src0_sel:DWORD src1_sel:WORD_1\n"
    "v_mfma_f32_16x16x32_bf16 v[172:175], a[124:127], v[144:147], 0\n"
    "ds_read_u16_d16_hi v203, v203\n"
    "v_or_b32_e64 v196, v196, v204\n"
    "v_mfma_f32_16x16x32_bf16 v[172:175], a[172:175], v[148:151], v[172:175]\n"
    "s_waitcnt lgkmcnt(8)\n"
    "v_or_b32_e32 v197, v197, v205\n"
    "v_lshlrev_b32_sdwa v205, 1, v179 dst_sel:DWORD dst_unused:UNUSED_PAD src0_sel:DWORD src1_sel:WORD_1\n"
    "v_mfma_f32_16x16x32_bf16 v[172:175], v[60:63], v[152:155], v[172:175]\n"
    "ds_read_b128 v[28:31], v244 offset:14336\n"
    "v_or_b32_e64 v198, v198, v206\n"
    "v_mfma_f32_16x16x32_bf16 v[176:179], a[120:123], v[156:159], 0\n"
    "ds_read_u16 v189, v189\n"
    "v_lshlrev_b32_sdwa v206, 1, v181 dst_sel:DWORD dst_unused:UNUSED_PAD src0_sel:DWORD src1_sel:WORD_1\n"
    "v_mfma_f32_16x16x32_bf16 v[176:179], a[168:171], v[160:163], v[176:179]\n"
    "s_waitcnt lgkmcnt(7)\n"
    "buffer_store_dwordx4 a[252:255], v255, s[68:71], s39 offen offset:128 sc1\n"
    "ds_read_u16_d16_hi v205, v205\n"
    "v_mfma_f32_16x16x32_bf16 v[176:179], a[76:79], v[164:167], v[176:179]\n"
    "v_or_b32_e32 v199, v199, v207\n"
    "v_lshlrev_b32_sdwa v207, 1, v183 dst_sel:DWORD dst_unused:UNUSED_PAD src0_sel:DWORD src1_sel:WORD_1\n"
    "v_mfma_f32_16x16x32_bf16 v[180:183], a[124:127], v[156:159], 0\n"
    "s_waitcnt lgkmcnt(7)\n"
    "ds_read_u16 v190, v190\n"
    "v_or_b32_e32 v185, v185, v201\n"
    "v_mfma_f32_16x16x32_bf16 v[180:183], a[172:175], v[160:163], v[180:183]\n"
    "ds_read_b128 v[144:147], v243 offset:0\n"
    "v_pk_fma_f16 v232, v184, v184, s48\n"
    "v_mfma_f32_16x16x32_bf16 v[180:183], v[60:63], v[164:167], v[180:183]\n"
    "ds_read_u16_d16_hi v206, v206\n"
    "v_pk_mul_f16 v232, v184, v232\n"
    "v_mfma_f32_16x16x32_bf16 v[96:99], a[176:179], v[192:195], v[96:99]\n"
    "v_exp_f16_e32 v234, v232\n"
    "v_mfma_f32_16x16x32_bf16 v[100:103], a[180:183], v[192:195], v[100:103]\n"
    "s_waitcnt lgkmcnt(8)\n"
    "ds_read_b128 v[148:151], v243 offset:1024\n"
    "v_or_b32_e64 v186, v186, v202\n"
    "v_mfma_f32_16x16x32_bf16 v[112:115], a[184:187], v[192:195], v[112:115]\n"
    "ds_read_u16 v191, v191\n"
    "v_exp_f16_sdwa v234, v232 dst_sel:WORD_1 dst_unused:UNUSED_PRESERVE src0_sel:WORD_1\n"
    "v_mfma_f32_16x16x32_bf16 v[116:119], a[188:191], v[192:195], v[116:119]\n"
    "v_pk_fma_f16 v234, v234, s49, s49\n"
    "v_lshlrev_b32_sdwa v201, 1, v171 dst_sel:DWORD dst_unused:UNUSED_PAD src0_sel:DWORD src1_sel:WORD_1\n"
    "v_mfma_f32_16x16x32_bf16 v[128:131], v[24:27], v[192:195], v[128:131]\n"
    "s_waitcnt lgkmcnt(7)\n"
    "ds_read_b128 v[152:155], v243 offset:2048\n"
    "ds_read_u16_d16_hi v207, v207\n"
    "v_mfma_f32_16x16x32_bf16 v[132:135], v[28:31], v[192:195], v[132:135]\n"
    "v_or_b32_e32 v187, v187, v203\n"
    "v_cvt_pk_f16_f32 v192, v168, v169\n"
    "v_mfma_f32_16x16x32_bf16 v[104:107], a[176:179], v[196:199], v[104:107]\n"
    "ds_read_b128 v[156:159], v243 offset:3072\n"
    "v_lshlrev_b32_sdwa v193, 1, v170 dst_sel:DWORD dst_unused:UNUSED_PAD src0_sel:DWORD src1_sel:WORD_1\n"
    "v_mfma_f32_16x16x32_bf16 v[108:111], a[180:183], v[196:199], v[108:111]\n"
    "ds_read_b128 v[160:163], v243 offset:4096\n"
    "v_lshlrev_b32_sdwa v194, 1, v172 dst_sel:DWORD dst_unused:UNUSED_PAD src0_sel:DWORD src1_sel:WORD_1\n"
    "v_mfma_f32_16x16x32_bf16 v[120:123], a[184:187], v[196:199], v[120:123]\n"
    "ds_read_b128 v[164:167], v243 offset:5120\n"
    "s_waitcnt lgkmcnt(10)\n"
    "v_or_b32_e32 v189, v189, v205\n"
    "v_mfma_f32_16x16x32_bf16 v[124:127], a[188:191], v[196:199], v[124:127]\n"
    "s_nop 0\n"
    "ds_read_b128 v[48:51], v244 offset:32768\n"
    "v_lshlrev_b32_sdwa v202, 1, v173 dst_sel:DWORD dst_unused:UNUSED_PAD src0_sel:DWORD src1_sel:WORD_1\n"
    "v_mfma_f32_16x16x32_bf16 v[136:139], v[24:27], v[196:199], v[136:139]\n"
    "s_waitcnt lgkmcnt(9)\n"
    "ds_read_u16 v193, v193\n"
    "v_lshlrev_b32_sdwa v195, 1, v174 dst_sel:DWORD dst_unused:UNUSED_PAD src0_sel:DWORD src1_sel:WORD_1\n"
    "v_mfma_f32_16x16x32_bf16 v[140:143], v[28:31], v[196:199], v[140:143]\n"
    "v_lshlrev_b32_sdwa v203, 1, v175 dst_sel:DWORD dst_unused:UNUSED_PAD src0_sel:DWORD src1_sel:WORD_1\n"
    "v_rcp_f16_e32 v232, v234\n"
    "v_mfma_f32_16x16x32_bf16 v[168:171], a[80:83], v[144:147], 0\n"
    "s_waitcnt lgkmcnt(8)\n"
    "ds_read_u16_d16_hi v201, v201\n"
    "v_or_b32_e64 v190, v190, v206\n"
    "v_mfma_f32_16x16x32_bf16 v[168:171], a[128:131], v[148:151], v[168:171]\n"
    "ds_read_u16 v194, v194\n"
    "v_rcp_f16_sdwa v232, v234 dst_sel:WORD_1 dst_unused:UNUSED_PRESERVE src0_sel:WORD_1\n"
    "v_mfma_f32_16x16x32_bf16 v[172:175], a[84:87], v[144:147], 0\n"
    "v_cvt_pk_f16_f32 v196, v176, v177\n"
    "v_lshlrev_b32_sdwa v197, 1, v178 dst_sel:DWORD dst_unused:UNUSED_PAD src0_sel:DWORD src1_sel:WORD_1\n"
    "s_waitcnt lgkmcnt(8)\n"
    "v_mfma_f32_16x16x32_bf16 v[168:171], a[48:51], v[152:155], v[168:171]\n"
    "s_nop 0\n"
    "ds_read_u16_d16_hi v202, v202\n"
    "v_lshlrev_b32_sdwa v205, 1, v179 dst_sel:DWORD dst_unused:UNUSED_PAD src0_sel:DWORD src1_sel:WORD_1\n"
    "v_lshlrev_b32_sdwa v198, 1, v180 dst_sel:DWORD dst_unused:UNUSED_PAD src0_sel:DWORD src1_sel:WORD_1\n"
    "v_lshlrev_b32_sdwa v206, 1, v181 dst_sel:DWORD dst_unused:UNUSED_PAD src0_sel:DWORD src1_sel:WORD_1\n"
    "v_lshlrev_b32_sdwa v199, 1, v182 dst_sel:DWORD dst_unused:UNUSED_PAD src0_sel:DWORD src1_sel:WORD_1\n"
    "v_fma_mix_f32 v234, v184, v232, 0 op_sel_hi:[1,1,0]\n"
    "v_mfma_f32_16x16x32_bf16 v[172:175], a[132:135], v[148:151], v[172:175]\n"
    "ds_read_b128 v[32:35], v244 offset:16384\n"
    "ds_read_u16 v195, v195\n"
    "s_waitcnt lgkmcnt(9)\n"
    "v_or_b32_e32 v191, v191, v207\n"
    "v_lshlrev_b32_sdwa v207, 1, v183 dst_sel:DWORD dst_unused:UNUSED_PAD src0_sel:DWORD src1_sel:WORD_1\n"
    "v_fma_mix_f32 v232, v184, v232, 0 op_sel:[1,1,0] op_sel_hi:[1,1,0]\n"
    "v_mfma_f32_16x16x32_bf16 v[176:179], a[80:83], v[156:159], 0\n"
    "s_waitcnt lgkmcnt(7)\n"
    "ds_read_u16_d16_hi v203, v203\n"
    "v_cvt_pk_bf16_f32 v184, v234, v232\n"
    "v_mfma_f32_16x16x32_bf16 v[176:179], a[128:131], v[160:163], v[176:179]\n"
    "s_waitcnt lgkmcnt(7)\n"
    "v_pk_fma_f16 v232, v188, v188, s48\n"
    "v_lshlrev_b32_sdwa v200, 1, v169 dst_sel:DWORD dst_unused:UNUSED_PAD src0_sel:DWORD src1_sel:WORD_1\n"
    "v_mfma_f32_16x16x32_bf16 v[172:175], v[48:51], v[152:155], v[172:175]\n"
    "s_barrier\n"
    "v_mfma_f32_16x16x32_bf16 v[176:179], a[48:51], v[164:167], v[176:179]\n"
    "ds_read_b128 v[36:39], v244 offset:18432\n"
    "ds_write_b128 v253, v[64:67] offset:2048\n"
    "v_mfma_f32_16x16x32_bf16 v[180:183], a[84:87], v[156:159], 0\n"
    "s_waitcnt lgkmcnt(7)\n"
    "v_pk_mul_f16 v232, v188, v232\n"
    "v_or_b32_e64 v193, v193, v201\n"
    "v_mfma_f32_16x16x32_bf16 v[180:183], a[132:135], v[160:163], v[180:183]\n"
    "v_exp_f16_e64 v234, v232\n"
    "v_mfma_f32_16x16x32_bf16 v[180:183], v[48:51], v[164:167], v[180:183]\n"
    "ds_read_u16 v197, v197\n"
    "ds_write_b128 v253, v[68:71] offset:5120\n"
    "v_exp_f16_sdwa v234, v232 dst_sel:WORD_1 dst_unused:UNUSED_PRESERVE src0_sel:WORD_1\n"
    "v_mfma_f32_16x16x32_bf16 v[96:99], a[192:195], v[184:187], v[96:99]\n"
    "s_waitcnt lgkmcnt(6)\n"
    "v_pk_fma_f16 v234, v234, s49, s49\n"
    "v_or_b32_e32 v194, v194, v202\n"
    "v_mfma_f32_16x16x32_bf16 v[100:103], a[196:199], v[184:187], v[100:103]\n"
    "v_rcp_f16_e32 v232, v234\n"
    "v_mfma_f32_16x16x32_bf16 v[112:115], a[200:203], v[184:187], v[112:115]\n"
    "ds_read_u16_d16_hi v205, v205\n"
    "ds_write_b128 v253, v[88:91] offset:0\n"
    "v_rcp_f16_sdwa v232, v234 dst_sel:WORD_1 dst_unused:UNUSED_PRESERVE src0_sel:WORD_1\n"
    "v_mfma_f32_16x16x32_bf16 v[116:119], a[204:207], v[184:187], v[116:119]\n"
    "s_waitcnt lgkmcnt(4)\n"
    "v_fma_mix_f32 v234, v188, v232, 0 op_sel_hi:[1,1,0]\n"
    "v_fma_mix_f32 v232, v188, v232, 0 op_sel:[1,1,0] op_sel_hi:[1,1,0]\n"
    "v_mfma_f32_16x16x32_bf16 v[128:131], v[32:35], v[184:187], v[128:131]\n"
    "v_cvt_pk_bf16_f32 v188, v234, v232\n"
    "v_pk_fma_f16 v232, v192, v192, s48\n"
    "v_mfma_f32_16x16x32_bf16 v[132:135], v[36:39], v[184:187], v[132:135]\n"
    "ds_read_u16 v198, v198\n"
    "ds_write_b128 v253, v[92:95] offset:3072\n"
    "v_mfma_f32_16x16x32_bf16 v[104:107], a[192:195], v[188:191], v[104:107]\n"
    "v_or_b32_e64 v195, v195, v203\n"
    "v_pk_mul_f16 v232, v192, v232\n"
    "v_mfma_f32_16x16x32_bf16 v[108:111], a[196:199], v[188:191], v[108:111]\n"
    "v_exp_f16_e64 v234, v232\n"
    "v_mfma_f32_16x16x32_bf16 v[120:123], a[200:203], v[188:191], v[120:123]\n"
    "ds_read_u16_d16_hi v206, v206\n"
    "ds_read_u16 v199, v199\n"
    "v_mfma_f32_16x16x32_bf16 v[124:127], a[204:207], v[188:191], v[124:127]\n"
    "ds_read_u16_d16_hi v207, v207\n"
    "ds_write_b128 v253, v[72:75] offset:1024\n"
    "v_mfma_f32_16x16x32_bf16 v[136:139], v[32:35], v[188:191], v[136:139]\n"
    "s_waitcnt lgkmcnt(6)\n"
    "v_or_b32_e32 v197, v197, v205\n"
    "v_exp_f16_sdwa v234, v232 dst_sel:WORD_1 dst_unused:UNUSED_PRESERVE src0_sel:WORD_1\n"
    "v_mfma_f32_16x16x32_bf16 v[140:143], v[36:39], v[188:191], v[140:143]\n"
    "v_pk_fma_f16 v234, v234, s49, s49\n"
    "v_lshlrev_b32_sdwa v184, 1, v168 dst_sel:DWORD dst_unused:UNUSED_PAD src0_sel:DWORD src1_sel:WORD_1\n"
    "v_lshlrev_b32_sdwa v185, 1, v170 dst_sel:DWORD dst_unused:UNUSED_PAD src0_sel:DWORD src1_sel:WORD_1\n"
    "v_lshlrev_b32_sdwa v201, 1, v171 dst_sel:DWORD dst_unused:UNUSED_PAD src0_sel:DWORD src1_sel:WORD_1\n"
    "v_mfma_f32_16x16x32_bf16 v[168:171], a[88:91], v[144:147], 0\n"
    "ds_read_u16 v184, v184\n"
    "ds_write_b128 v253, v[76:79] offset:4096\n"
    "v_mfma_f32_16x16x32_bf16 v[168:171], a[136:139], v[148:151], v[168:171]\n"
    "v_lshlrev_b32_sdwa v186, 1, v172 dst_sel:DWORD dst_unused:UNUSED_PAD src0_sel:DWORD src1_sel:WORD_1\n"
    "v_lshlrev_b32_sdwa v202, 1, v173 dst_sel:DWORD dst_unused:UNUSED_PAD src0_sel:DWORD src1_sel:WORD_1\n"
    "v_lshlrev_b32_sdwa v187, 1, v174 dst_sel:DWORD dst_unused:UNUSED_PAD src0_sel:DWORD src1_sel:WORD_1\n"
    "v_lshlrev_b32_sdwa v203, 1, v175 dst_sel:DWORD dst_unused:UNUSED_PAD src0_sel:DWORD src1_sel:WORD_1\n"
    "v_rcp_f16_e64 v232, v234\n"
    "v_mfma_f32_16x16x32_bf16 v[168:171], a[52:55], v[152:155], v[168:171]\n"
    "ds_read_u16_d16_hi v200, v200\n"
    "ds_read_u16 v185, v185\n"
    "s_waitcnt lgkmcnt(7)\n"
    "v_or_b32_e32 v198, v198, v206\n"
    "v_rcp_f16_sdwa v232, v234 dst_sel:WORD_1 dst_unused:UNUSED_PRESERVE src0_sel:WORD_1\n"
    "v_mfma_f32_16x16x32_bf16 v[172:175], a[92:95], v[144:147], 0\n"
    "ds_read_u16_d16_hi v201, v201\n"
    "v_lshlrev_b32_sdwa v188, 1, v176 dst_sel:DWORD dst_unused:UNUSED_PAD src0_sel:DWORD src1_sel:WORD_1\n"
    "v_lshlrev_b32_sdwa v204, 1, v177 dst_sel:DWORD dst_unused:UNUSED_PAD src0_sel:DWORD src1_sel:WORD_1\n"
    "v_lshlrev_b32_sdwa v189, 1, v178 dst_sel:DWORD dst_unused:UNUSED_PAD src0_sel:DWORD src1_sel:WORD_1\n"
    "v_lshlrev_b32_sdwa v205, 1, v179 dst_sel:DWORD dst_unused:UNUSED_PAD src0_sel:DWORD src1_sel:WORD_1\n"
    "v_lshlrev_b32_sdwa v190, 1, v180 dst_sel:DWORD dst_unused:UNUSED_PAD src0_sel:DWORD src1_sel:WORD_1\n"
    "v_lshlrev_b32_sdwa v206, 1, v181 dst_sel:DWORD dst_unused:UNUSED_PAD src0_sel:DWORD src1_sel:WORD_1\n"
    "v_lshlrev_b32_sdwa v191, 1, v182 dst_sel:DWORD dst_unused:UNUSED_PAD src0_sel:DWORD src1_sel:WORD_1\n"
    "v_fma_mix_f32 v234, v192, v232, 0 op_sel_hi:[1,1,0]\n"
    "v_mfma_f32_16x16x32_bf16 v[172:175], a[140:143], v[148:151], v[172:175]\n"
    "s_waitcnt lgkmcnt(5)\n"
    "ds_read_u16 v186, v186\n"
    "ds_read_b128 v[40:43], v244 offset:20480\n"
    "v_mfma_f32_16x16x32_bf16 v[172:175], v[56:59], v[152:155], v[172:175]\n"
    "s_nop 0\n"
    "ds_read_u16_d16_hi v202, v202\n"
    "v_or_b32_e64 v199, v199, v207\n"
    "v_lshlrev_b32_sdwa v207, 1, v183 dst_sel:DWORD dst_unused:UNUSED_PAD src0_sel:DWORD src1_sel:WORD_1\n"
    "v_fma_mix_f32 v232, v192, v232, 0 op_sel:[1,1,0] op_sel_hi:[1,1,0]\n"
    "v_mfma_f32_16x16x32_bf16 v[176:179], a[88:91], v[156:159], 0\n"
    "ds_read_u16 v187, v187\n"
    "v_cvt_pk_bf16_f32 v192, v234, v232\n"
    "v_mfma_f32_16x16x32_bf16 v[176:179], a[136:139], v[160:163], v[176:179]\n"
    "v_pk_fma_f16 v232, v196, v196, s48\n"
    "v_pk_mul_f16 v232, v196, v232\n"
    "v_mfma_f32_16x16x32_bf16 v[176:179], a[52:55], v[164:167], v[176:179]\n"
    "ds_read_b128 v[44:47], v244 offset:22528\n"
    "v_exp_f16_e32 v234, v232\n"
    "v_mfma_f32_16x16x32_bf16 v[180:183], a[92:95], v[156:159], 0\n"
    "ds_read_u16_d16_hi v203, v203\n"
    "v_exp_f16_sdwa v234, v232 dst_sel:WORD_1 dst_unused:UNUSED_PRESERVE src0_sel:WORD_1\n"
    "v_mfma_f32_16x16x32_bf16 v[180:183], a[140:143], v[160:163], v[180:183]\n"
    "s_waitcnt lgkmcnt(8)\n"
    "ds_read_u16 v188, v188\n"
    "v_pk_fma_f16 v234, v234, s49, s49\n"
    "v_mfma_f32_16x16x32_bf16 v[180:183], v[56:59], v[164:167], v[180:183]\n"
    "v_rcp_f16_e32 v232, v234\n"
    "v_mfma_f32_16x16x32_bf16 v[96:99], a[208:211], v[192:195], v[96:99]\n"
    "ds_read_u16_d16_hi v204, v204\n"
    "v_rcp_f16_sdwa v232, v234 dst_sel:WORD_1 dst_unused:UNUSED_PRESERVE src0_sel:WORD_1\n"
    "v_mfma_f32_16x16x32_bf16 v[100:103], a[212:215], v[192:195], v[100:103]\n"
    "s_waitcnt lgkmcnt(8)\n"
    "ds_read_u16 v189, v189\n"
    "v_fma_mix_f32 v234, v196, v232, 0 op_sel_hi:[1,1,0]\n"
    "v_mfma_f32_16x16x32_bf16 v[112:115], a[216:219], v[192:195], v[112:115]\n"
    "v_fma_mix_f32 v232, v196, v232, 0 op_sel:[1,1,0] op_sel_hi:[1,1,0]\n"
    "v_or_b32_e32 v184, v184, v200\n"
    "v_mfma_f32_16x16x32_bf16 v[116:119], a[220:223], v[192:195], v[116:119]\n"
    "s_waitcnt lgkmcnt(7)\n"
    "ds_read_u16_d16_hi v205, v205\n"
    "v_cvt_pk_bf16_f32 v196, v234, v232\n"
    "v_mfma_f32_16x16x32_bf16 v[128:131], v[40:43], v[192:195], v[128:131]\n"
    "v_or_b32_e64 v185, v185, v201\n"
    "v_lshlrev_b32_sdwa v201, 1, v171 dst_sel:DWORD dst_unused:UNUSED_PAD src0_sel:DWORD src1_sel:WORD_1\n"
    "v_mfma_f32_16x16x32_bf16 v[104:107], a[208:211], v[196:199], v[104:107]\n"
    "ds_read_u16 v190, v190\n"
    "s_waitcnt lgkmcnt(8)\n"
    "v_or_b32_e32 v186, v186, v202\n"
    "v_mfma_f32_16x16x32_bf16 v[108:111], a[212:215], v[196:199], v[108:111]\n"
    "ds_read_u16_d16_hi v206, v206\n"
    "v_lshlrev_b32_sdwa v202, 1, v173 dst_sel:DWORD dst_unused:UNUSED_PAD src0_sel:DWORD src1_sel:WORD_1\n"
    "s_waitcnt lgkmcnt(7)\n"
    "v_mfma_f32_16x16x32_bf16 v[132:135], v[44:47], v[192:195], v[132:135]\n"
    "s_nop 0\n"
    "v_cvt_pk_f16_f32 v192, v168, v169\n"
    "v_lshlrev_b32_sdwa v193, 1, v170 dst_sel:DWORD dst_unused:UNUSED_PAD src0_sel:DWORD src1_sel:WORD_1\n"
    "v_mfma_f32_16x16x32_bf16 v[120:123], a[216:219], v[196:199], v[120:123]\n"
    "ds_read_u16 v191, v191\n"
    "v_lshlrev_b32_sdwa v194, 1, v172 dst_sel:DWORD dst_unused:UNUSED_PAD src0_sel:DWORD src1_sel:WORD_1\n"
    "v_mfma_f32_16x16x32_bf16 v[124:127], a[220:223], v[196:199], v[124:127]\n"
    "v_lshlrev_b32_sdwa v195, 1, v174 dst_sel:DWORD dst_unused:UNUSED_PAD src0_sel:DWORD src1_sel:WORD_1\n"
    "s_waitcnt lgkmcnt(7)\n"
    "v_or_b32_e32 v187, v187, v203\n"
    "v_mfma_f32_16x16x32_bf16 v[136:139], v[40:43], v[196:199], v[136:139]\n"
    "ds_read_u16_d16_hi v207, v207\n"
    "v_lshlrev_b32_sdwa v203, 1, v175 dst_sel:DWORD dst_unused:UNUSED_PAD src0_sel:DWORD src1_sel:WORD_1\n"
    "v_mfma_f32_16x16x32_bf16 v[140:143], v[44:47], v[196:199], v[140:143]\n"
    "ds_read_u16 v193, v193\n"
    "v_cvt_pk_f16_f32 v196, v176, v177\n"
    "v_mfma_f32_16x16x32_bf16 v[168:171], a[96:99], v[144:147], 0\n"
    "v_lshlrev_b32_sdwa v197, 1, v178 dst_sel:DWORD dst_unused:UNUSED_PAD src0_sel:DWORD src1_sel:WORD_1\n"
    "v_lshlrev_b32_sdwa v198, 1, v180 dst_sel:DWORD dst_unused:UNUSED_PAD src0_sel:DWORD src1_sel:WORD_1\n"
    "v_mfma_f32_16x16x32_bf16 v[168:171], a[144:147], v[148:151], v[168:171]\n"
    "s_waitcnt lgkmcnt(7)\n"
    "ds_read_u16_d16_hi v201, v201\n"
    "v_lshlrev_b32_sdwa v199, 1, v182 dst_sel:DWORD dst_unused:UNUSED_PAD src0_sel:DWORD src1_sel:WORD_1\n"
    "v_mfma_f32_16x16x32_bf16 v[168:171], a[56:59], v[152:155], v[168:171]\n"
    "v_or_b32_e32 v188, v188, v204\n"
    "v_cvt_pk_bf16_f32 v96, v96, v97\n"
    "v_mfma_f32_16x16x32_bf16 v[172:175], a[100:103], v[144:147], 0\n"
    "ds_read_u16 v194, v194\n"
    "v_cvt_pk_bf16_f32 v97, v98, v99\n"
    "v_mfma_f32_16x16x32_bf16 v[172:175], a[148:151], v[148:151], v[172:175]\n"
    "s_waitcnt lgkmcnt(7)\n"
    "ds_read_u16_d16_hi v202, v202\n"
    "v_or_b32_e32 v189, v189, v205\n"
    "v_mfma_f32_16x16x32_bf16 v[172:175], a[60:63], v[152:155], v[172:175]\n"
    "v_lshlrev_b32_sdwa v205, 1, v179 dst_sel:DWORD dst_unused:UNUSED_PAD src0_sel:DWORD src1_sel:WORD_1\n"
    "v_cvt_pk_bf16_f32 v98, v100, v101\n"
    "v_mfma_f32_16x16x32_bf16 v[176:179], a[96:99], v[156:159], 0\n"
    "ds_read_u16 v195, v195\n"
    "v_cvt_pk_bf16_f32 v99, v102, v103\n"
    "v_mfma_f32_16x16x32_bf16 v[176:179], a[144:147], v[160:163], v[176:179]\n"
    "v_cvt_pk_bf16_f32 v112, v112, v113\n"
    "v_cvt_pk_bf16_f32 v113, v114, v115\n"
    "v_mfma_f32_16x16x32_bf16 v[176:179], a[56:59], v[164:167], v[176:179]\n"
    "s_waitcnt lgkmcnt(7)\n"
    "ds_read_u16_d16_hi v203, v203\n"
    "v_or_b32_e32 v190, v190, v206\n"
    "v_mfma_f32_16x16x32_bf16 v[100:103], a[4:7], v[184:187], 0\n"
    "s_nop 0\n"
    "v_lshlrev_b32_sdwa v206, 1, v181 dst_sel:DWORD dst_unused:UNUSED_PAD src0_sel:DWORD src1_sel:WORD_1\n"
    "ds_write_b128 v242, v[96:99] offset:24640\n"
    "v_mfma_f32_16x16x32_bf16 v[96:99], a[0:3], v[184:187], 0\n"
    "s_waitcnt lgkmcnt(7)\n"
    "v_cvt_pk_bf16_f32 v114, v116, v117\n"
    "v_cvt_pk_bf16_f32 v115, v118, v119\n"
    "v_cvt_pk_bf16_f32 v128, v128, v129\n"
    "v_cvt_pk_bf16_f32 v129, v130, v131\n"
    "ds_write_b128 v242, v[112:115] offset:26688\n"
    "v_mfma_f32_16x16x32_bf16 v[112:115], a[8:11], v[184:187], 0\n"
    "s_waitcnt lgkmcnt(6)\n"
    "v_or_b32_e32 v191, v191, v207\n"
    "v_lshlrev_b32_sdwa v207, 1, v183 dst_sel:DWORD dst_unused:UNUSED_PAD src0_sel:DWORD src1_sel:WORD_1\n"
    "v_mfma_f32_16x16x32_bf16 v[180:183], a[100:103], v[156:159], 0\n"
    "ds_read_u16 v197, v197\n"
    "ds_read_u16_d16_hi v205, v205\n"
    "v_mfma_f32_16x16x32_bf16 v[180:183], a[148:151], v[160:163], v[180:183]\n"
    "ds_read_u16 v198, v198\n"
    "v_cvt_pk_bf16_f32 v130, v132, v133\n"
    "v_mfma_f32_16x16x32_bf16 v[180:183], a[60:63], v[164:167], v[180:183]\n"
    "v_or_b32_e32 v193, v193, v201\n"
    "v_cvt_pk_bf16_f32 v131, v134, v135\n"
    "v_mfma_f32_16x16x32_bf16 v[116:119], a[12:15], v[184:187], 0\n"
    "s_waitcnt lgkmcnt(7)\n"
    "ds_read_u16_d16_hi v206, v206\n"
    "v_cvt_pk_bf16_f32 v104, v104, v105\n"
    "v_mfma_f32_16x16x32_bf16 v[132:135], v[4:7], v[184:187], 0\n"
    "v_cvt_pk_bf16_f32 v105, v106, v107\n"
    "v_cvt_pk_bf16_f32 v106, v108, v109\n"
    "ds_write_b128 v242, v[128:131] offset:28736\n"
    "v_mfma_f32_16x16x32_bf16 v[128:131], v[0:3], v[184:187], 0\n"
    "s_waitcnt lgkmcnt(6)\n"
    "v_cvt_pk_bf16_f32 v107, v110, v111\n"
    "v_or_b32_e32 v194, v194, v202\n"
    "v_mfma_f32_16x16x32_bf16 v[108:111], a[4:7], v[188:191], 0\n"
    "ds_read_u16 v199, v199\n"
    "v_cvt_pk_bf16_f32 v120, v120, v121\n"
    "ds_write_b128 v242, v[104:107] offset:25664\n"
    "v_mfma_f32_16x16x32_bf16 v[104:107], a[0:3], v[188:191], 0\n"
    "v_cvt_pk_bf16_f32 v121, v122, v123\n"
    "v_cvt_pk_bf16_f32 v122, v124, v125\n"
    "v_cvt_pk_bf16_f32 v123, v126, v127\n"
    "v_cvt_pk_bf16_f32 v136, v136, v137\n"
    "ds_write_b128 v242, v[120:123] offset:27712\n"
    "v_mfma_f32_16x16x32_bf16 v[120:123], a[8:11], v[188:191], 0\n"
    "s_waitcnt lgkmcnt(6)\n"
    "v_pk_fma_f16 v232, v192, v192, s48\n"
    "v_cvt_pk_bf16_f32 v137, v138, v139\n"
    "v_mfma_f32_16x16x32_bf16 v[124:127], a[12:15], v[188:191], 0\n"
    "s_nop 0\n"
    "ds_read_u16_d16_hi v207, v207\n"
    "v_cvt_pk_bf16_f32 v138, v140, v141\n"
    "v_or_b32_e64 v195, v195, v203\n"
    "v_cvt_pk_bf16_f32 v139, v142, v143\n"
    "ds_write_b128 v242, v[136:139] offset:29760\n"
    "v_pk_mul_f16 v232, v192, v232\n"
    "v_mfma_f32_16x16x32_bf16 v[136:139], v[0:3], v[188:191], 0\n"
    "s_waitcnt lgkmcnt(5)\n"
    "v_exp_f16_e32 v234, v232\n"
    "v_mfma_f32_16x16x32_bf16 v[140:143], v[4:7], v[188:191], 0\n"
    "v_or_b32_e64 v197, v197, v205\n"
    "v_exp_f16_sdwa v234, v232 dst_sel:WORD_1 dst_unused:UNUSED_PRESERVE src0_sel:WORD_1\n"
    "v_cvt_pk_f16_f32 v184, v168, v169\n"
    "v_pk_fma_f16 v234, v234, s49, s49\n"
    "v_lshlrev_b32_sdwa v185, 1, v170 dst_sel:DWORD dst_unused:UNUSED_PAD src0_sel:DWORD src1_sel:WORD_1\n"
    "v_lshlrev_b32_sdwa v201, 1, v171 dst_sel:DWORD dst_unused:UNUSED_PAD src0_sel:DWORD src1_sel:WORD_1\n"
    "v_mfma_f32_16x16x32_bf16 v[168:171], a[104:107], v[144:147], 0\n"
    "ds_read_u16 v185, v185\n"
    "ds_read_u16_d16_hi v201, v201\n"
    "v_mfma_f32_16x16x32_bf16 v[168:171], a[152:155], v[148:151], v[168:171]\n"
    "v_lshlrev_b32_sdwa v186, 1, v172 dst_sel:DWORD dst_unused:UNUSED_PAD src0_sel:DWORD src1_sel:WORD_1\n"
    "v_lshlrev_b32_sdwa v202, 1, v173 dst_sel:DWORD dst_unused:UNUSED_PAD src0_sel:DWORD src1_sel:WORD_1\n"
    "v_lshlrev_b32_sdwa v187, 1, v174 dst_sel:DWORD dst_unused:UNUSED_PAD src0_sel:DWORD src1_sel:WORD_1\n"
    "v_lshlrev_b32_sdwa v203, 1, v175 dst_sel:DWORD dst_unused:UNUSED_PAD src0_sel:DWORD src1_sel:WORD_1\n"
    "v_rcp_f16_e64 v232, v234\n"
    "v_mfma_f32_16x16x32_bf16 v[168:171], a[64:67], v[152:155], v[168:171]\n"
    "ds_read_u16 v186, v186\n"
    "ds_read_u16_d16_hi v202, v202\n"
    "v_or_b32_e64 v198, v198, v206\n"
    "v_rcp_f16_sdwa v232, v234 dst_sel:WORD_1 dst_unused:UNUSED_PRESERVE src0_sel:WORD_1\n"
    "v_mfma_f32_16x16x32_bf16 v[172:175], a[108:111], v[144:147], 0\n"
    "s_waitcnt lgkmcnt(4)\n"
    "ds_read_u16 v187, v187\n"
    "ds_read_u16_d16_hi v203, v203\n"
    "v_mfma_f32_16x16x32_bf16 v[172:175], a[156:159], v[148:151], v[172:175]\n"
    "s_barrier\n"
    "v_cvt_pk_f16_f32 v188, v176, v177\n"
    "v_lshlrev_b32_sdwa v189, 1, v178 dst_sel:DWORD dst_unused:UNUSED_PAD src0_sel:DWORD src1_sel:WORD_1\n"
    "v_lshlrev_b32_sdwa v205, 1, v179 dst_sel:DWORD dst_unused:UNUSED_PAD src0_sel:DWORD src1_sel:WORD_1\n"
    "v_lshlrev_b32_sdwa v190, 1, v180 dst_sel:DWORD dst_unused:UNUSED_PAD src0_sel:DWORD src1_sel:WORD_1\n"
    "v_lshlrev_b32_sdwa v206, 1, v181 dst_sel:DWORD dst_unused:UNUSED_PAD src0_sel:DWORD src1_sel:WORD_1\n"
    "v_lshlrev_b32_sdwa v191, 1, v182 dst_sel:DWORD dst_unused:UNUSED_PAD src0_sel:DWORD src1_sel:WORD_1\n"
    "v_fma_mix_f32 v234, v192, v232, 0 op_sel_hi:[1,1,0]\n"
    "v_mfma_f32_16x16x32_bf16 v[172:175], a[68:71], v[152:155], v[172:175]\n"
    "ds_read_u16 v189, v189\n"
    "v_or_b32_e64 v199, v199, v207\n"
    "v_lshlrev_b32_sdwa v207, 1, v183 dst_sel:DWORD dst_unused:UNUSED_PAD src0_sel:DWORD src1_sel:WORD_1\n"
    "v_fma_mix_f32 v232, v192, v232, 0 op_sel:[1,1,0] op_sel_hi:[1,1,0]\n"
    "v_mfma_f32_16x16x32_bf16 v[176:179], a[104:107], v[156:159], 0\n"
    "s_nop 0\n"
    "ds_read_u16_d16_hi v205, v205\n"
    "v_cvt_pk_bf16_f32 v192, v234, v232\n"
    "v_mfma_f32_16x16x32_bf16 v[176:179], a[152:155], v[160:163], v[176:179]\n"
    "s_waitcnt lgkmcnt(6)\n"
    "ds_read_u16 v190, v190\n"
    "v_pk_fma_f16 v232, v196, v196, s48\n"
    "v_mfma_f32_16x16x32_bf16 v[176:179], a[64:67], v[164:167], v[176:179]\n"
    "ds_read_u16_d16_hi v206, v206\n"
    "v_pk_mul_f16 v232, v196, v232\n"
    "v_mfma_f32_16x16x32_bf16 v[180:183], a[108:111], v[156:159], 0\n"
    "ds_read_u16 v191, v191\n"
    "v_exp_f16_e32 v234, v232\n"
    "v_mfma_f32_16x16x32_bf16 v[180:183], a[156:159], v[160:163], v[180:183]\n"
    "s_waitcnt lgkmcnt(7)\n"
    "ds_read_u16_d16_hi v207, v207\n"
    "v_exp_f16_sdwa v234, v232 dst_sel:WORD_1 dst_unused:UNUSED_PRESERVE src0_sel:WORD_1\n"
    "v_mfma_f32_16x16x32_bf16 v[180:183], a[68:71], v[164:167], v[180:183]\n"
    "v_pk_fma_f16 v234, v234, s49, s49\n"
    "v_or_b32_e64 v185, v185, v201\n"
    "v_mfma_f32_16x16x32_bf16 v[96:99], a[16:19], v[192:195], v[96:99]\n"
    "v_rcp_f16_e32 v232, v234\n"
    "v_mfma_f32_16x16x32_bf16 v[100:103], a[20:23], v[192:195], v[100:103]\n"
    "v_rcp_f16_sdwa v232, v234 dst_sel:WORD_1 dst_unused:UNUSED_PRESERVE src0_sel:WORD_1\n"
    "v_mfma_f32_16x16x32_bf16 v[112:115], a[24:27], v[192:195], v[112:115]\n"
    "s_waitcnt lgkmcnt(6)\n"
    "v_fma_mix_f32 v234, v196, v232, 0 op_sel_hi:[1,1,0]\n"
    "v_fma_mix_f32 v232, v196, v232, 0 op_sel:[1,1,0] op_sel_hi:[1,1,0]\n"
    "v_mfma_f32_16x16x32_bf16 v[116:119], a[28:31], v[192:195], v[116:119]\n"
    "v_or_b32_e64 v186, v186, v202\n"
    "v_cvt_pk_bf16_f32 v196, v234, v232\n"
    "v_mfma_f32_16x16x32_bf16 v[128:131], v[8:11], v[192:195], v[128:131]\n"
    "v_pk_fma_f16 v232, v184, v184, s48\n"
    "v_or_b32_e32 v187, v187, v203\n"
    "v_mfma_f32_16x16x32_bf16 v[132:135], v[12:15], v[192:195], v[132:135]\n"
    "s_waitcnt lgkmcnt(4)\n"
    "v_pk_mul_f16 v232, v184, v232\n"
    "v_or_b32_e64 v189, v189, v205\n"
    "v_mfma_f32_16x16x32_bf16 v[104:107], a[16:19], v[196:199], v[104:107]\n"
    "v_exp_f16_e32 v234, v232\n"
    "v_mfma_f32_16x16x32_bf16 v[108:111], a[20:23], v[196:199], v[108:111]\n"
    "v_exp_f16_sdwa v234, v232 dst_sel:WORD_1 dst_unused:UNUSED_PRESERVE src0_sel:WORD_1\n"
    "v_mfma_f32_16x16x32_bf16 v[120:123], a[24:27], v[196:199], v[120:123]\n"
    "v_pk_fma_f16 v234, v234, s49, s49\n"
    "v_lshlrev_b32_sdwa v192, 1, v168 dst_sel:DWORD dst_unused:UNUSED_PAD src0_sel:DWORD src1_sel:WORD_1\n"
    "v_mfma_f32_16x16x32_bf16 v[124:127], a[28:31], v[196:199], v[124:127]\n"
    "s_waitcnt lgkmcnt(2)\n"
    "ds_read_u16 v192, v192\n"
    "v_lshlrev_b32_sdwa v200, 1, v169 dst_sel:DWORD dst_unused:UNUSED_PAD src0_sel:DWORD src1_sel:WORD_1\n"
    "v_mfma_f32_16x16x32_bf16 v[136:139], v[8:11], v[196:199], v[136:139]\n"
    "ds_read_u16_d16_hi v200, v200\n"
    "v_lshlrev_b32_sdwa v193, 1, v170 dst_sel:DWORD dst_unused:UNUSED_PAD src0_sel:DWORD src1_sel:WORD_1\n"
    "v_mfma_f32_16x16x32_bf16 v[140:143], v[12:15], v[196:199], v[140:143]\n"
    "ds_read_u16 v193, v193\n"
    "v_lshlrev_b32_sdwa v201, 1, v171 dst_sel:DWORD dst_unused:UNUSED_PAD src0_sel:DWORD src1_sel:WORD_1\n"
    "s_waitcnt vmcnt(8)\n"
    "v_smfmac_f32_32x32x32_bf16 v[112:127], a[248:251], a[232:239], v241\n"
    "s_waitcnt lgkmcnt(3)\n"
    "ds_read_u16_d16_hi v201, v201\n"
    "ds_read_b128 a[232:235], v243 offset:24640\n"
    "v_lshlrev_b32_sdwa v194, 1, v172 dst_sel:DWORD dst_unused:UNUSED_PAD src0_sel:DWORD src1_sel:WORD_1\n"
    "v_lshlrev_b32_sdwa v202, 1, v173 dst_sel:DWORD dst_unused:UNUSED_PAD src0_sel:DWORD src1_sel:WORD_1\n"
    "v_lshlrev_b32_sdwa v195, 1, v174 dst_sel:DWORD dst_unused:UNUSED_PAD src0_sel:DWORD src1_sel:WORD_1\n"
    "s_waitcnt vmcnt(7)\n"
    "v_smfmac_f32_32x32x32_bf16 v[96:111], a[248:251], a[224:231], v241\n"
    "s_nop 0\n"
    "ds_read_u16 v194, v194\n"
    "ds_read_u16_d16_hi v202, v202\n"
    "v_lshlrev_b32_sdwa v203, 1, v175 dst_sel:DWORD dst_unused:UNUSED_PAD src0_sel:DWORD src1_sel:WORD_1\n"
    "v_rcp_f16_e32 v232, v234\n"
    "s_waitcnt vmcnt(6)\n"
    "v_smfmac_f32_32x32x32_bf16 v[128:143], a[248:251], v[208:215], v241\n"
    "ds_read_b128 a[236:239], v243 offset:25664\n"
    "v_or_b32_e64 v190, v190, v206\n"
    "v_rcp_f16_sdwa v232, v234 dst_sel:WORD_1 dst_unused:UNUSED_PRESERVE src0_sel:WORD_1\n"
    "v_lshlrev_b32_sdwa v196, 1, v176 dst_sel:DWORD dst_unused:UNUSED_PAD src0_sel:DWORD src1_sel:WORD_1\n"
    "v_mfma_f32_16x16x32_bf16 v[168:171], a[112:115], v[144:147], 0\n"
    "ds_read_u16 v195, v195\n"
    "v_lshlrev_b32_sdwa v204, 1, v177 dst_sel:DWORD dst_unused:UNUSED_PAD src0_sel:DWORD src1_sel:WORD_1\n"
    "v_mfma_f32_16x16x32_bf16 v[168:171], a[160:163], v[148:151], v[168:171]\n"
    "ds_read_u16_d16_hi v203, v203\n"
    "v_lshlrev_b32_sdwa v197, 1, v178 dst_sel:DWORD dst_unused:UNUSED_PAD src0_sel:DWORD src1_sel:WORD_1\n"
    "v_mfma_f32_16x16x32_bf16 v[168:171], a[72:75], v[152:155], v[168:171]\n"
    "v_lshlrev_b32_sdwa v205, 1, v179 dst_sel:DWORD dst_unused:UNUSED_PAD src0_sel:DWORD src1_sel:WORD_1\n"
    "v_lshlrev_b32_sdwa v198, 1, v180 dst_sel:DWORD dst_unused:UNUSED_PAD src0_sel:DWORD src1_sel:WORD_1\n"
    "v_mfma_f32_16x16x32_bf16 v[172:175], a[116:119], v[144:147], 0\n"
    "v_lshlrev_b32_sdwa v206, 1, v181 dst_sel:DWORD dst_unused:UNUSED_PAD src0_sel:DWORD src1_sel:WORD_1\n"
    "v_lshlrev_b32_sdwa v199, 1, v182 dst_sel:DWORD dst_unused:UNUSED_PAD src0_sel:DWORD src1_sel:WORD_1\n"
    "v_fma_mix_f32 v234, v184, v232, 0 op_sel_hi:[1,1,0]\n"
    "v_mfma_f32_16x16x32_bf16 v[172:175], a[164:167], v[148:151], v[172:175]\n"
    "s_waitcnt lgkmcnt(7)\n"
    "ds_read_u16 v196, v196\n"
    "ds_read_u16_d16_hi v204, v204\n"
    "v_mfma_f32_16x16x32_bf16 v[172:175], v[52:55], v[152:155], v[172:175]\n"
    "v_or_b32_e32 v191, v191, v207\n"
    "v_lshlrev_b32_sdwa v207, 1, v183 dst_sel:DWORD dst_unused:UNUSED_PAD src0_sel:DWORD src1_sel:WORD_1\n"
    "v_fma_mix_f32 v232, v184, v232, 0 op_sel:[1,1,0] op_sel_hi:[1,1,0]\n"
    "v_mfma_f32_16x16x32_bf16 v[176:179], a[112:115], v[156:159], 0\n"
    "ds_read_u16 v197, v197\n"
    "v_cvt_pk_bf16_f32 v184, v234, v232\n"
    "v_mfma_f32_16x16x32_bf16 v[176:179], a[160:163], v[160:163], v[176:179]\n"
    "ds_read_u16_d16_hi v205, v205\n"
    "v_pk_fma_f16 v232, v188, v188, s48\n"
    "v_mfma_f32_16x16x32_bf16 v[176:179], a[72:75], v[164:167], v[176:179]\n"
    "s_waitcnt lgkmcnt(8)\n"
    "v_pk_mul_f16 v232, v188, v232\n"
    "v_or_b32_e32 v192, v192, v200\n"
    "v_mfma_f32_16x16x32_bf16 v[180:183], a[116:119], v[156:159], 0\n"
    "ds_read_u16 v198, v198\n"
    "v_exp_f16_e32 v234, v232\n"
    "s_waitcnt lgkmcnt(7)\n"
    "v_smfmac_f32_32x32x32_bf16 v[96:111], a[248:251], a[232:239], v241\n"
    "ds_read_u16_d16_hi v206, v206\n"
    "v_exp_f16_sdwa v234, v232 dst_sel:WORD_1 dst_unused:UNUSED_PRESERVE src0_sel:WORD_1\n"
    "v_or_b32_e64 v193, v193, v201\n"
    "v_pk_fma_f16 v234, v234, s49, s49\n"
    "v_mfma_f32_16x16x32_bf16 v[180:183], a[164:167], v[160:163], v[180:183]\n"
    "ds_read_u16 v199, v199\n"
    "v_rcp_f16_e32 v232, v234\n"
    "v_mfma_f32_16x16x32_bf16 v[180:183], v[52:55], v[164:167], v[180:183]\n"
    "v_rcp_f16_sdwa v232, v234 dst_sel:WORD_1 dst_unused:UNUSED_PRESERVE src0_sel:WORD_1\n"
    "v_mfma_f32_16x16x32_bf16 v[112:115], a[40:43], v[184:187], v[112:115]\n"
    "s_waitcnt lgkmcnt(7)\n"
    "ds_read_u16_d16_hi v207, v207\n"
    "v_fma_mix_f32 v234, v188, v232, 0 op_sel_hi:[1,1,0]\n"
    "v_mfma_f32_16x16x32_bf16 v[96:99], a[32:35], v[184:187], v[96:99]\n"
    "ds_read_b128 a[232:235], v243 offset:26688\n"
    "v_fma_mix_f32 v232, v188, v232, 0 op_sel:[1,1,0] op_sel_hi:[1,1,0]\n"
    "v_mfma_f32_16x16x32_bf16 v[100:103], a[36:39], v[184:187], v[100:103]\n"
    "ds_read_b128 a[236:239], v243 offset:27712\n"
    "v_cvt_pk_bf16_f32 v188, v234, v232\n"
    "v_mfma_f32_16x16x32_bf16 v[116:119], a[44:47], v[184:187], v[116:119]\n"
    "s_waitcnt lgkmcnt(8)\n"
    "v_lshlrev_b32_sdwa v201, 1, v171 dst_sel:DWORD dst_unused:UNUSED_PAD src0_sel:DWORD src1_sel:WORD_1\n"
    "v_or_b32_e32 v194, v194, v202\n"
    "v_mfma_f32_16x16x32_bf16 v[128:131], v[16:19], v[184:187], v[128:131]\n"
    "ds_read_u16_d16_hi v201, v201\n"
    "v_lshlrev_b32_sdwa v202, 1, v173 dst_sel:DWORD dst_unused:UNUSED_PAD src0_sel:DWORD src1_sel:WORD_1\n"
    "v_mfma_f32_16x16x32_bf16 v[132:135], v[20:23], v[184:187], v[132:135]\n"
    "ds_read_u16_d16_hi v202, v202\n"
    "v_cvt_pk_f16_f32 v184, v168, v169\n"
    "v_mfma_f32_16x16x32_bf16 v[104:107], a[32:35], v[188:191], v[104:107]\n"
    "v_lshlrev_b32_sdwa v185, 1, v170 dst_sel:DWORD dst_unused:UNUSED_PAD src0_sel:DWORD src1_sel:WORD_1\n"
    "v_lshlrev_b32_sdwa v186, 1, v172 dst_sel:DWORD dst_unused:UNUSED_PAD src0_sel:DWORD src1_sel:WORD_1\n"
    "v_mfma_f32_16x16x32_bf16 v[108:111], a[36:39], v[188:191], v[108:111]\n"
    "s_waitcnt lgkmcnt(8)\n"
    "ds_read_u16 v185, v185\n"
    "v_lshlrev_b32_sdwa v187, 1, v174 dst_sel:DWORD dst_unused:UNUSED_PAD src0_sel:DWORD src1_sel:WORD_1\n"
    "v_mfma_f32_16x16x32_bf16 v[120:123], a[40:43], v[188:191], v[120:123]\n"
    "v_or_b32_e32 v195, v195, v203\n"
    "v_lshlrev_b32_sdwa v203, 1, v175 dst_sel:DWORD dst_unused:UNUSED_PAD src0_sel:DWORD src1_sel:WORD_1\n"
    "v_mfma_f32_16x16x32_bf16 v[124:127], a[44:47], v[188:191], v[124:127]\n"
    "ds_read_u16 v186, v186\n"
    "v_or_b32_e32 v196, v196, v204\n"
    "v_mfma_f32_16x16x32_bf16 v[136:139], v[16:19], v[188:191], v[136:139]\n"
    "s_waitcnt lgkmcnt(8)\n"
    "ds_read_u16 v187, v187\n"
    "v_or_b32_e64 v197, v197, v205\n"
    "v_mfma_f32_16x16x32_bf16 v[140:143], v[20:23], v[188:191], v[140:143]\n"
    "v_cvt_pk_f16_f32 v188, v176, v177\n"
    "v_lshlrev_b32_sdwa v189, 1, v178 dst_sel:DWORD dst_unused:UNUSED_PAD src0_sel:DWORD src1_sel:WORD_1\n"
    "v_mfma_f32_16x16x32_bf16 v[168:171], a[120:123], v[144:147], 0\n"
    "ds_read_u16_d16_hi v203, v203\n"
    "v_lshlrev_b32_sdwa v205, 1, v179 dst_sel:DWORD dst_unused:UNUSED_PAD src0_sel:DWORD src1_sel:WORD_1\n"
    "v_mfma_f32_16x16x32_bf16 v[168:171], a[168:171], v[148:151], v[168:171]\n"
    "v_lshlrev_b32_sdwa v190, 1, v180 dst_sel:DWORD dst_unused:UNUSED_PAD src0_sel:DWORD src1_sel:WORD_1\n"
    "v_lshlrev_b32_sdwa v191, 1, v182 dst_sel:DWORD dst_unused:UNUSED_PAD src0_sel:DWORD src1_sel:WORD_1\n"
    "v_mfma_f32_16x16x32_bf16 v[168:171], a[76:79], v[152:155], v[168:171]\n"
    "s_waitcnt lgkmcnt(6)\n"
    "ds_read_u16 v189, v189\n"
    "v_or_b32_e32 v198, v198, v206\n"
    "v_smfmac_f32_32x32x32_bf16 v[112:127], a[248:251], a[232:239], v241\n"
    "ds_read_b128 a[232:235], v243 offset:28736\n"
    "v_lshlrev_b32_sdwa v206, 1, v181 dst_sel:DWORD dst_unused:UNUSED_PAD src0_sel:DWORD src1_sel:WORD_1\n"
    "v_or_b32_e64 v199, v199, v207\n"
    "v_lshlrev_b32_sdwa v207, 1, v183 dst_sel:DWORD dst_unused:UNUSED_PAD src0_sel:DWORD src1_sel:WORD_1\n"
    "v_pk_fma_f16 v232, v184, v184, s48\n"
    "v_mfma_f32_16x16x32_bf16 v[172:175], a[124:127], v[144:147], 0\n"
    "ds_read_u16_d16_hi v205, v205\n"
    "v_pk_mul_f16 v232, v184, v232\n"
    "v_mfma_f32_16x16x32_bf16 v[172:175], a[172:175], v[148:151], v[172:175]\n"
    "ds_read_b128 a[236:239], v243 offset:29760\n"
    "v_exp_f16_e64 v234, v232\n"
    "v_mfma_f32_16x16x32_bf16 v[172:175], v[60:63], v[152:155], v[172:175]\n"
    "ds_read_u16 v190, v190\n"
    "v_exp_f16_sdwa v234, v232 dst_sel:WORD_1 dst_unused:UNUSED_PRESERVE src0_sel:WORD_1\n"
    "v_mfma_f32_16x16x32_bf16 v[176:179], a[120:123], v[156:159], 0\n"
    "s_waitcnt lgkmcnt(8)\n"
    "v_or_b32_e32 v185, v185, v201\n"
    "v_pk_fma_f16 v234, v234, s49, s49\n"
    "v_mfma_f32_16x16x32_bf16 v[176:179], a[168:171], v[160:163], v[176:179]\n"
    "ds_read_b128 v[144:147], v242 offset:0\n"
    "ds_read_u16_d16_hi v206, v206\n"
    "v_mfma_f32_16x16x32_bf16 v[176:179], a[76:79], v[164:167], v[176:179]\n"
    "s_waitcnt lgkmcnt(9)\n"
    "v_or_b32_e32 v186, v186, v202\n"
    "v_lshlrev_b32_sdwa v201, 1, v171 dst_sel:DWORD dst_unused:UNUSED_PAD src0_sel:DWORD src1_sel:WORD_1\n"
    "v_mfma_f32_16x16x32_bf16 v[180:183], a[124:127], v[156:159], 0\n"
    "ds_read_b128 v[148:151], v242 offset:1024\n"
    "v_lshlrev_b32_sdwa v202, 1, v173 dst_sel:DWORD dst_unused:UNUSED_PAD src0_sel:DWORD src1_sel:WORD_1\n"
    "v_mfma_f32_16x16x32_bf16 v[180:183], a[172:175], v[160:163], v[180:183]\n"
    "ds_read_u16 v191, v191\n"
    "v_rcp_f16_e64 v232, v234\n"
    "v_mfma_f32_16x16x32_bf16 v[180:183], v[60:63], v[164:167], v[180:183]\n"
    "s_waitcnt lgkmcnt(9)\n"
    "v_or_b32_e32 v187, v187, v203\n"
    "v_lshlrev_b32_sdwa v203, 1, v175 dst_sel:DWORD dst_unused:UNUSED_PAD src0_sel:DWORD src1_sel:WORD_1\n"
    "v_mfma_f32_16x16x32_bf16 v[96:99], a[176:179], v[192:195], v[96:99]\n"
    "ds_read_b128 v[152:155], v242 offset:2048\n"
    "v_rcp_f16_sdwa v232, v234 dst_sel:WORD_1 dst_unused:UNUSED_PRESERVE src0_sel:WORD_1\n"
    "v_mfma_f32_16x16x32_bf16 v[100:103], a[180:183], v[192:195], v[100:103]\n"
    "ds_read_u16_d16_hi v207, v207\n"
    "v_fma_mix_f32 v234, v184, v232, 0 op_sel_hi:[1,1,0]\n"
    "v_mfma_f32_16x16x32_bf16 v[112:115], a[184:187], v[192:195], v[112:115]\n"
    "ds_read_b128 v[156:159], v242 offset:3072\n"
    "v_fma_mix_f32 v232, v184, v232, 0 op_sel:[1,1,0] op_sel_hi:[1,1,0]\n"
    "v_mfma_f32_16x16x32_bf16 v[116:119], a[188:191], v[192:195], v[116:119]\n"
    "ds_read_b128 v[160:163], v242 offset:4096\n"
    "s_waitcnt lgkmcnt(9)\n"
    "v_or_b32_e32 v189, v189, v205\n"
    "v_mfma_f32_16x16x32_bf16 v[128:131], v[24:27], v[192:195], v[128:131]\n"
    "ds_read_b128 v[164:167], v242 offset:5120\n"
    "v_lshlrev_b32_sdwa v205, 1, v179 dst_sel:DWORD dst_unused:UNUSED_PAD src0_sel:DWORD src1_sel:WORD_1\n"
    "v_mfma_f32_16x16x32_bf16 v[132:135], v[28:31], v[192:195], v[132:135]\n"
    "ds_read_u16_d16_hi v201, v201\n"
    "v_cvt_pk_f16_f32 v192, v168, v169\n"
    "v_mfma_f32_16x16x32_bf16 v[104:107], a[176:179], v[196:199], v[104:107]\n"
    "s_nop 0\n"
    "v_lshlrev_b32_sdwa v193, 1, v170 dst_sel:DWORD dst_unused:UNUSED_PAD src0_sel:DWORD src1_sel:WORD_1\n"
    "v_lshlrev_b32_sdwa v194, 1, v172 dst_sel:DWORD dst_unused:UNUSED_PAD src0_sel:DWORD src1_sel:WORD_1\n"
    "v_mfma_f32_16x16x32_bf16 v[108:111], a[180:183], v[196:199], v[108:111]\n"
    "s_waitcnt lgkmcnt(9)\n"
    "ds_read_u16 v193, v193\n"
    "v_lshlrev_b32_sdwa v195, 1, v174 dst_sel:DWORD dst_unused:UNUSED_PAD src0_sel:DWORD src1_sel:WORD_1\n"
    "v_mfma_f32_16x16x32_bf16 v[120:123], a[184:187], v[196:199], v[120:123]\n"
    "v_cvt_pk_bf16_f32 v184, v234, v232\n"
    "v_pk_fma_f16 v232, v188, v188, s48\n"
    "v_mfma_f32_16x16x32_bf16 v[124:127], a[188:191], v[196:199], v[124:127]\n"
    "s_waitcnt lgkmcnt(8)\n"
    "ds_read_u16 v194, v194\n"
    "v_or_b32_e32 v190, v190, v206\n"
    "v_mfma_f32_16x16x32_bf16 v[136:139], v[24:27], v[196:199], v[136:139]\n"
    "ds_read_u16_d16_hi v202, v202\n"
    "v_lshlrev_b32_sdwa v206, 1, v181 dst_sel:DWORD dst_unused:UNUSED_PAD src0_sel:DWORD src1_sel:WORD_1\n"
    "v_mfma_f32_16x16x32_bf16 v[140:143], v[28:31], v[196:199], v[140:143]\n"
    "v_cvt_pk_f16_f32 v196, v176, v177\n"
    "v_lshlrev_b32_sdwa v197, 1, v178 dst_sel:DWORD dst_unused:UNUSED_PAD src0_sel:DWORD src1_sel:WORD_1\n"
    "v_mfma_f32_16x16x32_bf16 v[168:171], a[80:83], v[144:147], 0\n"
    "ds_read_u16 v195, v195\n"
    "v_lshlrev_b32_sdwa v198, 1, v180 dst_sel:DWORD dst_unused:UNUSED_PAD src0_sel:DWORD src1_sel:WORD_1\n"
    "v_mfma_f32_16x16x32_bf16 v[168:171], a[128:131], v[148:151], v[168:171]\n"
    "s_waitcnt lgkmcnt(9)\n"
    "v_lshlrev_b32_sdwa v199, 1, v182 dst_sel:DWORD dst_unused:UNUSED_PAD src0_sel:DWORD src1_sel:WORD_1\n"
    "v_pk_mul_f16 v232, v188, v232\n"
    "v_smfmac_f32_32x32x32_bf16 v[128:143], a[248:251], a[232:239], v241\n"
    "s_waitcnt lgkmcnt(7)\n"
    "ds_read_u16_d16_hi v203, v203\n"
    "v_or_b32_e64 v191, v191, v207\n"
    "v_lshlrev_b32_sdwa v207, 1, v183 dst_sel:DWORD dst_unused:UNUSED_PAD src0_sel:DWORD src1_sel:WORD_1\n"
    "v_exp_f16_e64 v234, v232\n"
    "v_mfma_f32_16x16x32_bf16 v[168:171], a[48:51], v[152:155], v[168:171]\n"
    "ds_read_u16 v197, v197\n"
    "v_exp_f16_sdwa v234, v232 dst_sel:WORD_1 dst_unused:UNUSED_PRESERVE src0_sel:WORD_1\n"
    "v_mfma_f32_16x16x32_bf16 v[172:175], a[84:87], v[144:147], 0\n"
    "ds_read_u16_d16_hi v205, v205\n"
    "v_pk_fma_f16 v234, v234, s49, s49\n"
    "v_mfma_f32_16x16x32_bf16 v[172:175], a[132:135], v[148:151], v[172:175]\n"
    "v_rcp_f16_e64 v232, v234\n"
    "v_mfma_f32_16x16x32_bf16 v[172:175], v[48:51], v[152:155], v[172:175]\n"
    "ds_read_u16 v198, v198\n"
    "v_rcp_f16_sdwa v232, v234 dst_sel:WORD_1 dst_unused:UNUSED_PRESERVE src0_sel:WORD_1\n"
    "v_mfma_f32_16x16x32_bf16 v[176:179], a[80:83], v[156:159], 0\n"
    "s_waitcnt lgkmcnt(7)\n"
    "ds_read_u16_d16_hi v206, v206\n"
    "v_or_b32_e32 v193, v193, v201\n"
    "v_mfma_f32_16x16x32_bf16 v[176:179], a[128:131], v[160:163], v[176:179]\n"
    "v_fma_mix_f32 v234, v188, v232, 0 op_sel_hi:[1,1,0]\n"
    "v_fma_mix_f32 v232, v188, v232, 0 op_sel:[1,1,0] op_sel_hi:[1,1,0]\n"
    "v_mfma_f32_16x16x32_bf16 v[176:179], a[48:51], v[164:167], v[176:179]\n"
    "ds_read_u16 v199, v199\n"
    "v_cvt_pk_bf16_f32 v188, v234, v232\n"
    "v_mfma_f32_16x16x32_bf16 v[180:183], a[84:87], v[156:159], 0\n"
    "v_pk_fma_f16 v232, v192, v192, s48\n"
    "v_lshlrev_b32_sdwa v200, 1, v169 dst_sel:DWORD dst_unused:UNUSED_PAD src0_sel:DWORD src1_sel:WORD_1\n"
    "v_mfma_f32_16x16x32_bf16 v[180:183], a[132:135], v[160:163], v[180:183]\n"
    "s_waitcnt lgkmcnt(7)\n"
    "ds_read_u16_d16_hi v207, v207\n"
    "v_or_b32_e32 v194, v194, v202\n"
    "v_mfma_f32_16x16x32_bf16 v[180:183], v[48:51], v[164:167], v[180:183]\n"
    "v_pk_mul_f16 v232, v192, v232\n"
    "v_lshlrev_b32_sdwa v201, 1, v171 dst_sel:DWORD dst_unused:UNUSED_PAD src0_sel:DWORD src1_sel:WORD_1\n"
    "v_mfma_f32_16x16x32_bf16 v[96:99], a[192:195], v[184:187], v[96:99]\n"
    "ds_read_u16_d16_hi v200, v200\n"
    "v_exp_f16_e32 v234, v232\n"
    "v_mfma_f32_16x16x32_bf16 v[100:103], a[196:199], v[184:187], v[100:103]\n"
    "s_waitcnt lgkmcnt(7)\n"
    "ds_read_u16_d16_hi v201, v201\n"
    "v_or_b32_e64 v195, v195, v203\n"
    "v_mfma_f32_16x16x32_bf16 v[112:115], a[200:203], v[184:187], v[112:115]\n"
    "v_exp_f16_sdwa v234, v232 dst_sel:WORD_1 dst_unused:UNUSED_PRESERVE src0_sel:WORD_1\n"
    "v_mfma_f32_16x16x32_bf16 v[116:119], a[204:207], v[184:187], v[116:119]\n"
    "v_pk_fma_f16 v234, v234, s49, s49\n"
    "v_lshlrev_b32_sdwa v202, 1, v173 dst_sel:DWORD dst_unused:UNUSED_PAD src0_sel:DWORD src1_sel:WORD_1\n"
    "v_mfma_f32_16x16x32_bf16 v[128:131], v[32:35], v[184:187], v[128:131]\n"
    "ds_read_u16_d16_hi v202, v202\n"
    "s_waitcnt lgkmcnt(7)\n"
    "v_or_b32_e32 v197, v197, v205\n"
    "v_mfma_f32_16x16x32_bf16 v[132:135], v[36:39], v[184:187], v[132:135]\n"
    "v_lshlrev_b32_sdwa v184, 1, v168 dst_sel:DWORD dst_unused:UNUSED_PAD src0_sel:DWORD src1_sel:WORD_1\n"
    "v_lshlrev_b32_sdwa v185, 1, v170 dst_sel:DWORD dst_unused:UNUSED_PAD src0_sel:DWORD src1_sel:WORD_1\n"
    "v_mfma_f32_16x16x32_bf16 v[104:107], a[192:195], v[188:191], v[104:107]\n"
    "ds_read_u16 v184, v184\n"
    "ds_read_u16 v185, v185\n"
    "v_mfma_f32_16x16x32_bf16 v[108:111], a[196:199], v[188:191], v[108:111]\n"
    "v_lshlrev_b32_sdwa v186, 1, v172 dst_sel:DWORD dst_unused:UNUSED_PAD src0_sel:DWORD src1_sel:WORD_1\n"
    "v_lshlrev_b32_sdwa v187, 1, v174 dst_sel:DWORD dst_unused:UNUSED_PAD src0_sel:DWORD src1_sel:WORD_1\n"
    "v_mfma_f32_16x16x32_bf16 v[120:123], a[200:203], v[188:191], v[120:123]\n"
    "s_waitcnt lgkmcnt(7)\n"
    "ds_read_u16 v186, v186\n"
    "v_lshlrev_b32_sdwa v203, 1, v175 dst_sel:DWORD dst_unused:UNUSED_PAD src0_sel:DWORD src1_sel:WORD_1\n"
    "v_mfma_f32_16x16x32_bf16 v[124:127], a[204:207], v[188:191], v[124:127]\n"
    "v_rcp_f16_e32 v232, v234\n"
    "v_mfma_f32_16x16x32_bf16 v[136:139], v[32:35], v[188:191], v[136:139]\n"
    "ds_read_u16 v187, v187\n"
    "v_or_b32_e32 v198, v198, v206\n"
    "v_mfma_f32_16x16x32_bf16 v[140:143], v[36:39], v[188:191], v[140:143]\n"
    "s_waitcnt lgkmcnt(7)\n"
    "v_rcp_f16_sdwa v232, v234 dst_sel:WORD_1 dst_unused:UNUSED_PRESERVE src0_sel:WORD_1\n"
    "v_mfma_f32_16x16x32_bf16 v[168:171], a[88:91], v[144:147], 0\n"
    "ds_read_u16_d16_hi v203, v203\n"
    "v_lshlrev_b32_sdwa v188, 1, v176 dst_sel:DWORD dst_unused:UNUSED_PAD src0_sel:DWORD src1_sel:WORD_1\n"
    "v_mfma_f32_16x16x32_bf16 v[168:171], a[136:139], v[148:151], v[168:171]\n"
    "v_lshlrev_b32_sdwa v204, 1, v177 dst_sel:DWORD dst_unused:UNUSED_PAD src0_sel:DWORD src1_sel:WORD_1\n"
    "v_lshlrev_b32_sdwa v189, 1, v178 dst_sel:DWORD dst_unused:UNUSED_PAD src0_sel:DWORD src1_sel:WORD_1\n"
    "v_mfma_f32_16x16x32_bf16 v[168:171], a[52:55], v[152:155], v[168:171]\n"
    "v_lshlrev_b32_sdwa v205, 1, v179 dst_sel:DWORD dst_unused:UNUSED_PAD src0_sel:DWORD src1_sel:WORD_1\n"
    "v_lshlrev_b32_sdwa v190, 1, v180 dst_sel:DWORD dst_unused:UNUSED_PAD src0_sel:DWORD src1_sel:WORD_1\n"
    "v_mfma_f32_16x16x32_bf16 v[172:175], a[92:95], v[144:147], 0\n"
    "v_lshlrev_b32_sdwa v206, 1, v181 dst_sel:DWORD dst_unused:UNUSED_PAD src0_sel:DWORD src1_sel:WORD_1\n"
    "v_lshlrev_b32_sdwa v191, 1, v182 dst_sel:DWORD dst_unused:UNUSED_PAD src0_sel:DWORD src1_sel:WORD_1\n"
    "v_fma_mix_f32 v234, v192, v232, 0 op_sel_hi:[1,1,0]\n"
    "v_mfma_f32_16x16x32_bf16 v[172:175], a[140:143], v[148:151], v[172:175]\n"
    "ds_read_u16 v188, v188\n"
    "ds_read_u16_d16_hi v204, v204\n"
    "v_mfma_f32_16x16x32_bf16 v[172:175], v[56:59], v[152:155], v[172:175]\n"
    "v_or_b32_e64 v199, v199, v207\n"
    "v_lshlrev_b32_sdwa v207, 1, v183 dst_sel:DWORD dst_unused:UNUSED_PAD src0_sel:DWORD src1_sel:WORD_1\n"
    "v_fma_mix_f32 v232, v192, v232, 0 op_sel:[1,1,0] op_sel_hi:[1,1,0]\n"
    "v_mfma_f32_16x16x32_bf16 v[176:179], a[88:91], v[156:159], 0\n"
    "s_waitcnt lgkmcnt(6)\n"
    "ds_read_u16 v189, v189\n"
    "v_cvt_pk_bf16_f32 v192, v234, v232\n"
    "v_mfma_f32_16x16x32_bf16 v[176:179], a[136:139], v[160:163], v[176:179]\n"
    "ds_read_u16_d16_hi v205, v205\n"
    "v_pk_fma_f16 v232, v196, v196, s48\n"
    "v_mfma_f32_16x16x32_bf16 v[176:179], a[52:55], v[164:167], v[176:179]\n"
    "s_waitcnt lgkmcnt(7)\n"
    "ds_read_u16 v190, v190\n"
    "v_pk_mul_f16 v232, v196, v232\n"
    "v_mfma_f32_16x16x32_bf16 v[180:183], a[92:95], v[156:159], 0\n"
    "ds_read_u16_d16_hi v206, v206\n"
    "v_exp_f16_e32 v234, v232\n"
    "v_mfma_f32_16x16x32_bf16 v[180:183], a[140:143], v[160:163], v[180:183]\n"
    "s_waitcnt lgkmcnt(8)\n"
    "buffer_load_dwordx4 a[232:235], v245, s[40:43], s58 offen offset:64\n"
    "buffer_load_dwordx4 a[224:227], v245, s[40:43], s58 offen offset:0\n"
    "buffer_load_dwordx4 v[208:211], v245, s[40:43], s58 offen offset:128\n"
    "ds_read_u16 v191, v191\n"
    "v_mfma_f32_16x16x32_bf16 v[180:183], v[56:59], v[164:167], v[180:183]\n"
    "v_exp_f16_sdwa v234, v232 dst_sel:WORD_1 dst_unused:UNUSED_PRESERVE src0_sel:WORD_1\n"
    "v_or_b32_e64 v184, v184, v200\n"
    "v_pk_fma_f16 v234, v234, s49, s49\n"
    "v_mfma_f32_16x16x32_bf16 v[96:99], a[208:211], v[192:195], v[96:99]\n"
    "s_waitcnt lgkmcnt(7)\n"
    "ds_read_u16_d16_hi v207, v207\n"
    "v_rcp_f16_e32 v232, v234\n"
    "v_mfma_f32_16x16x32_bf16 v[100:103], a[212:215], v[192:195], v[100:103]\n"
    "v_rcp_f16_sdwa v232, v234 dst_sel:WORD_1 dst_unused:UNUSED_PRESERVE src0_sel:WORD_1\n"
    "v_mfma_f32_16x16x32_bf16 v[112:115], a[216:219], v[192:195], v[112:115]\n"
    "v_fma_mix_f32 v234, v196, v232, 0 op_sel_hi:[1,1,0]\n"
    "v_fma_mix_f32 v232, v196, v232, 0 op_sel:[1,1,0] op_sel_hi:[1,1,0]\n"
    "v_mfma_f32_16x16x32_bf16 v[116:119], a[220:223], v[192:195], v[116:119]\n"
    "s_waitcnt lgkmcnt(6)\n"
    "v_cvt_pk_bf16_f32 v196, v234, v232\n"
    "v_or_b32_e32 v185, v185, v201\n"
    "v_mfma_f32_16x16x32_bf16 v[128:131], v[40:43], v[192:195], v[128:131]\n"
    "v_lshlrev_b32_sdwa v201, 1, v171 dst_sel:DWORD dst_unused:UNUSED_PAD src0_sel:DWORD src1_sel:WORD_1\n"
    "v_or_b32_e32 v186, v186, v202\n"
    "v_mfma_f32_16x16x32_bf16 v[132:135], v[44:47], v[192:195], v[132:135]\n"
    "ds_read_u16_d16_hi v201, v201\n"
    "v_cvt_pk_f16_f32 v192, v168, v169\n"
    "v_mfma_f32_16x16x32_bf16 v[104:107], a[208:211], v[196:199], v[104:107]\n"
    "s_waitcnt lgkmcnt(5)\n"
    "v_lshlrev_b32_sdwa v193, 1, v170 dst_sel:DWORD dst_unused:UNUSED_PAD src0_sel:DWORD src1_sel:WORD_1\n"
    "v_lshlrev_b32_sdwa v194, 1, v172 dst_sel:DWORD dst_unused:UNUSED_PAD src0_sel:DWORD src1_sel:WORD_1\n"
    "v_mfma_f32_16x16x32_bf16 v[108:111], a[212:215], v[196:199], v[108:111]\n"
    "ds_read_u16 v193, v193\n"
    "buffer_load_dwordx4 a[236:239], v245, s[40:43], s89 offen offset:64\n"
    "buffer_load_dwordx4 a[228:231], v245, s[40:43], s89 offen offset:0\n"
    "buffer_load_dwordx4 v[212:215], v245, s[40:43], s89 offen offset:128\n"
    "v_mfma_f32_16x16x32_bf16 v[120:123], a[216:219], v[196:199], v[120:123]\n"
    "s_nop 0\n"
    "ds_read_u16 v194, v194\n"
    "v_lshlrev_b32_sdwa v202, 1, v173 dst_sel:DWORD dst_unused:UNUSED_PAD src0_sel:DWORD src1_sel:WORD_1\n"
    "v_mfma_f32_16x16x32_bf16 v[124:127], a[220:223], v[196:199], v[124:127]\n"
    "s_waitcnt lgkmcnt(5)\n"
    "ds_read_u16_d16_hi v202, v202\n"
    "v_lshlrev_b32_sdwa v195, 1, v174 dst_sel:DWORD dst_unused:UNUSED_PAD src0_sel:DWORD src1_sel:WORD_1\n"
    "v_mfma_f32_16x16x32_bf16 v[136:139], v[40:43], v[196:199], v[136:139]\n"
    "ds_read_u16 v195, v195\n"
    "v_or_b32_e64 v187, v187, v203\n"
    "v_mfma_f32_16x16x32_bf16 v[140:143], v[44:47], v[196:199], v[140:143]\n"
    "v_lshlrev_b32_sdwa v203, 1, v175 dst_sel:DWORD dst_unused:UNUSED_PAD src0_sel:DWORD src1_sel:WORD_1\n"
    "v_cvt_pk_f16_f32 v196, v176, v177\n"
    "v_mfma_f32_16x16x32_bf16 v[168:171], a[96:99], v[144:147], 0\n"
    "s_nop 0\n"
    "ds_read_u16_d16_hi v203, v203\n"
    "v_lshlrev_b32_sdwa v197, 1, v178 dst_sel:DWORD dst_unused:UNUSED_PAD src0_sel:DWORD src1_sel:WORD_1\n"
    "v_mfma_f32_16x16x32_bf16 v[168:171], a[144:147], v[148:151], v[168:171]\n"
    "s_waitcnt lgkmcnt(6)\n"
    "ds_read_u16 v197, v197\n"
    "v_lshlrev_b32_sdwa v198, 1, v180 dst_sel:DWORD dst_unused:UNUSED_PAD src0_sel:DWORD src1_sel:WORD_1\n"
    "v_mfma_f32_16x16x32_bf16 v[168:171], a[56:59], v[152:155], v[168:171]\n"
    "v_lshlrev_b32_sdwa v199, 1, v182 dst_sel:DWORD dst_unused:UNUSED_PAD src0_sel:DWORD src1_sel:WORD_1\n"
    "v_or_b32_e64 v188, v188, v204\n"
    "v_mfma_f32_16x16x32_bf16 v[172:175], a[100:103], v[144:147], 0\n"
    "ds_read_u16 v198, v198\n"
    "v_or_b32_e32 v189, v189, v205\n"
    "v_mfma_f32_16x16x32_bf16 v[172:175], a[148:151], v[148:151], v[172:175]\n"
    "ds_read_u16 v199, v199\n"
    "v_lshlrev_b32_sdwa v205, 1, v179 dst_sel:DWORD dst_unused:UNUSED_PAD src0_sel:DWORD src1_sel:WORD_1\n"
    "v_mfma_f32_16x16x32_bf16 v[172:175], a[60:63], v[152:155], v[172:175]\n"
    "s_waitcnt lgkmcnt(7)\n"
    "ds_read_u16_d16_hi v205, v205\n"
    "v_cvt_pk_bf16_f32 v96, v96, v97\n"
    "v_mfma_f32_16x16x32_bf16 v[176:179], a[96:99], v[156:159], 0\n"
    "v_cvt_pk_bf16_f32 v97, v98, v99\n"
    "v_cvt_pk_bf16_f32 v98, v100, v101\n"
    "v_mfma_f32_16x16x32_bf16 v[176:179], a[144:147], v[160:163], v[176:179]\n"
    "v_cvt_pk_bf16_f32 v99, v102, v103\n"
    "v_or_b32_e32 v190, v190, v206\n"
    "v_mfma_f32_16x16x32_bf16 v[176:179], a[56:59], v[164:167], v[176:179]\n"
    "s_waitcnt lgkmcnt(6)\n"
    "ds_write_b128 v243, v[96:99] offset:24640\n"
    "v_mfma_f32_16x16x32_bf16 v[96:99], a[0:3], v[184:187], 0\n"
    "v_lshlrev_b32_sdwa v206, 1, v181 dst_sel:DWORD dst_unused:UNUSED_PAD src0_sel:DWORD src1_sel:WORD_1\n"
    "v_cvt_pk_bf16_f32 v112, v112, v113\n"
    "v_mfma_f32_16x16x32_bf16 v[100:103], a[4:7], v[184:187], 0\n"
    "v_cvt_pk_bf16_f32 v113, v114, v115\n"
    "v_cvt_pk_bf16_f32 v114, v116, v117\n"
    "v_cvt_pk_bf16_f32 v115, v118, v119\n"
    "ds_write_b128 v243, v[112:115] offset:26688\n"
    "v_mfma_f32_16x16x32_bf16 v[112:115], a[8:11], v[184:187], 0\n"
    "s_waitcnt lgkmcnt(5)\n"
    "v_cvt_pk_bf16_f32 v128, v128, v129\n"
    "v_cvt_pk_bf16_f32 v129, v130, v131\n"
    "v_or_b32_e32 v191, v191, v207\n"
    "v_mfma_f32_16x16x32_bf16 v[116:119], a[12:15], v[184:187], 0\n"
    "ds_read_b128 a[252:255], v249 offset:0\n"
    "ds_read_u16_d16_hi v206, v206\n"
    "v_lshlrev_b32_sdwa v207, 1, v183 dst_sel:DWORD dst_unused:UNUSED_PAD src0_sel:DWORD src1_sel:WORD_1\n"
    "v_mfma_f32_16x16x32_bf16 v[180:183], a[100:103], v[156:159], 0\n"
    "ds_read_u16_d16_hi v207, v207\n"
    "v_cvt_pk_bf16_f32 v130, v132, v133\n"
    "v_mfma_f32_16x16x32_bf16 v[180:183], a[148:151], v[160:163], v[180:183]\n"
    "v_or_b32_e64 v193, v193, v201\n"
    "v_cvt_pk_bf16_f32 v131, v134, v135\n"
    "v_mfma_f32_16x16x32_bf16 v[180:183], a[60:63], v[164:167], v[180:183]\n"
    "s_nop 0\n"
    "ds_write_b128 v243, v[128:131] offset:28736\n"
    "v_mfma_f32_16x16x32_bf16 v[128:131], v[0:3], v[184:187], 0\n"
    "s_waitcnt lgkmcnt(5)\n"
    "v_cvt_pk_bf16_f32 v104, v104, v105\n"
    "v_cvt_pk_bf16_f32 v105, v106, v107\n"
    "v_mfma_f32_16x16x32_bf16 v[132:135], v[4:7], v[184:187], 0\n"
    "v_cvt_pk_bf16_f32 v106, v108, v109\n"
    "v_cvt_pk_bf16_f32 v107, v110, v111\n"
    "v_mfma_f32_16x16x32_bf16 v[108:111], a[4:7], v[188:191], 0\n"
    "ds_write_b128 v243, v[104:107] offset:25664\n"
    "s_waitcnt lgkmcnt(4)\n"
    "buffer_store_dwordx4 a[252:255], v255, s[44:47], s57 offen offset:0 sc1\n"
    "v_mfma_f32_16x16x32_bf16 v[104:107], a[0:3], v[188:191], 0\n"
    "v_or_b32_e32 v194, v194, v202\n"
    "v_cvt_pk_bf16_f32 v120, v120, v121\n"
    "v_cvt_pk_bf16_f32 v121, v122, v123\n"
    "v_cvt_pk_bf16_f32 v122, v124, v125\n"
    "v_cvt_pk_bf16_f32 v123, v126, v127\n"
    "ds_write_b128 v243, v[120:123] offset:27712\n"
    "v_mfma_f32_16x16x32_bf16 v[120:123], a[8:11], v[188:191], 0\n"
    "s_waitcnt lgkmcnt(2)\n"
    "v_pk_fma_f16 v232, v192, v192, s48\n"
    "v_cvt_pk_bf16_f32 v136, v136, v137\n"
    "v_mfma_f32_16x16x32_bf16 v[124:127], a[12:15], v[188:191], 0\n"
    "s_nop 0\n"
    "ds_read_b128 a[252:255], v249 offset:2048\n"
    "v_cvt_pk_bf16_f32 v137, v138, v139\n"
    "v_cvt_pk_bf16_f32 v138, v140, v141\n"
    "v_or_b32_e64 v195, v195, v203\n"
    "v_cvt_pk_bf16_f32 v139, v142, v143\n"
    "ds_write_b128 v243, v[136:139] offset:29760\n"
    "v_pk_mul_f16 v232, v192, v232\n"
    "v_mfma_f32_16x16x32_bf16 v[136:139], v[0:3], v[188:191], 0\n"
    "v_exp_f16_e64 v234, v232\n"
    "v_mfma_f32_16x16x32_bf16 v[140:143], v[4:7], v[188:191], 0\n"
    "v_or_b32_e64 v197, v197, v205\n"
    "v_exp_f16_sdwa v234, v232 dst_sel:WORD_1 dst_unused:UNUSED_PRESERVE src0_sel:WORD_1\n"
    "v_cvt_pk_f16_f32 v184, v168, v169\n"
    "v_pk_fma_f16 v234, v234, s49, s49\n"
    "v_lshlrev_b32_sdwa v185, 1, v170 dst_sel:DWORD dst_unused:UNUSED_PAD src0_sel:DWORD src1_sel:WORD_1\n"
    "v_lshlrev_b32_sdwa v201, 1, v171 dst_sel:DWORD dst_unused:UNUSED_PAD src0_sel:DWORD src1_sel:WORD_1\n"
    "v_mfma_f32_16x16x32_bf16 v[168:171], a[104:107], v[144:147], 0\n"
    "s_nop 0\n"
    "ds_read_u16 v185, v185\n"
    "ds_read_u16_d16_hi v201, v201\n"
    "v_mfma_f32_16x16x32_bf16 v[168:171], a[152:155], v[148:151], v[168:171]\n"
    "s_waitcnt lgkmcnt(3)\n"
    "buffer_store_dwordx4 a[252:255], v255, s[44:47], s57 offen offset:64 sc1\n"
    "v_lshlrev_b32_sdwa v186, 1, v172 dst_sel:DWORD dst_unused:UNUSED_PAD src0_sel:DWORD src1_sel:WORD_1\n"
    "v_lshlrev_b32_sdwa v202, 1, v173 dst_sel:DWORD dst_unused:UNUSED_PAD src0_sel:DWORD src1_sel:WORD_1\n"
    "v_lshlrev_b32_sdwa v187, 1, v174 dst_sel:DWORD dst_unused:UNUSED_PAD src0_sel:DWORD src1_sel:WORD_1\n"
    "v_lshlrev_b32_sdwa v203, 1, v175 dst_sel:DWORD dst_unused:UNUSED_PAD src0_sel:DWORD src1_sel:WORD_1\n"
    "v_rcp_f16_e64 v232, v234\n"
    "v_mfma_f32_16x16x32_bf16 v[168:171], a[64:67], v[152:155], v[168:171]\n"
    "ds_read_u16 v186, v186\n"
    "ds_read_u16_d16_hi v202, v202\n"
    "v_or_b32_e64 v198, v198, v206\n"
    "v_rcp_f16_sdwa v232, v234 dst_sel:WORD_1 dst_unused:UNUSED_PRESERVE src0_sel:WORD_1\n"
    "v_mfma_f32_16x16x32_bf16 v[172:175], a[108:111], v[144:147], 0\n"
    "ds_read_u16 v187, v187\n"
    "ds_read_u16_d16_hi v203, v203\n"
    "v_cvt_pk_f16_f32 v188, v176, v177\n"
    "v_lshlrev_b32_sdwa v189, 1, v178 dst_sel:DWORD dst_unused:UNUSED_PAD src0_sel:DWORD src1_sel:WORD_1\n"
    "v_lshlrev_b32_sdwa v205, 1, v179 dst_sel:DWORD dst_unused:UNUSED_PAD src0_sel:DWORD src1_sel:WORD_1\n"
    "v_lshlrev_b32_sdwa v190, 1, v180 dst_sel:DWORD dst_unused:UNUSED_PAD src0_sel:DWORD src1_sel:WORD_1\n"
    "v_lshlrev_b32_sdwa v206, 1, v181 dst_sel:DWORD dst_unused:UNUSED_PAD src0_sel:DWORD src1_sel:WORD_1\n"
    "v_lshlrev_b32_sdwa v191, 1, v182 dst_sel:DWORD dst_unused:UNUSED_PAD src0_sel:DWORD src1_sel:WORD_1\n"
    "v_fma_mix_f32 v234, v192, v232, 0 op_sel_hi:[1,1,0]\n"
    "v_mfma_f32_16x16x32_bf16 v[172:175], a[156:159], v[148:151], v[172:175]\n"
    "ds_read_b128 a[252:255], v249 offset:4096\n"
    "ds_read_u16 v189, v189\n"
    "v_mfma_f32_16x16x32_bf16 v[172:175], a[68:71], v[152:155], v[172:175]\n"
    "v_or_b32_e64 v199, v199, v207\n"
    "v_lshlrev_b32_sdwa v207, 1, v183 dst_sel:DWORD dst_unused:UNUSED_PAD src0_sel:DWORD src1_sel:WORD_1\n"
    "v_fma_mix_f32 v232, v192, v232, 0 op_sel:[1,1,0] op_sel_hi:[1,1,0]\n"
    "v_mfma_f32_16x16x32_bf16 v[176:179], a[104:107], v[156:159], 0\n"
    "s_nop 0\n"
    "ds_read_u16_d16_hi v205, v205\n"
    "v_cvt_pk_bf16_f32 v192, v234, v232\n"
    "v_mfma_f32_16x16x32_bf16 v[176:179], a[152:155], v[160:163], v[176:179]\n"
    "s_waitcnt lgkmcnt(7)\n"
    "ds_read_u16 v190, v190\n"
    "v_pk_fma_f16 v232, v196, v196, s48\n"
    "v_mfma_f32_16x16x32_bf16 v[176:179], a[64:67], v[164:167], v[176:179]\n"
    "v_pk_mul_f16 v232, v196, v232\n"
    "v_or_b32_e64 v185, v185, v201\n"
    "v_mfma_f32_16x16x32_bf16 v[180:183], a[108:111], v[156:159], 0\n"
    "ds_read_u16_d16_hi v206, v206\n"
    "v_exp_f16_e64 v234, v232\n"
    "v_mfma_f32_16x16x32_bf16 v[180:183], a[156:159], v[160:163], v[180:183]\n"
    "ds_read_u16 v191, v191\n"
    "v_exp_f16_sdwa v234, v232 dst_sel:WORD_1 dst_unused:UNUSED_PRESERVE src0_sel:WORD_1\n"
    "v_mfma_f32_16x16x32_bf16 v[180:183], a[68:71], v[164:167], v[180:183]\n"
    "s_waitcnt lgkmcnt(8)\n"
    "v_pk_fma_f16 v234, v234, s49, s49\n"
    "v_or_b32_e32 v186, v186, v202\n"
    "v_mfma_f32_16x16x32_bf16 v[96:99], a[16:19], v[192:195], v[96:99]\n"
    "ds_read_u16_d16_hi v207, v207\n"
    "v_rcp_f16_e32 v232, v234\n"
    "v_mfma_f32_16x16x32_bf16 v[100:103], a[20:23], v[192:195], v[100:103]\n"
    "v_rcp_f16_sdwa v232, v234 dst_sel:WORD_1 dst_unused:UNUSED_PRESERVE src0_sel:WORD_1\n"
    "v_mfma_f32_16x16x32_bf16 v[112:115], a[24:27], v[192:195], v[112:115]\n"
    "s_waitcnt lgkmcnt(6)\n"
    "buffer_store_dwordx4 a[252:255], v255, s[44:47], s57 offen offset:128 sc1\n"
    "v_mfma_f32_16x16x32_bf16 v[116:119], a[28:31], v[192:195], v[116:119]\n"
    "v_fma_mix_f32 v234, v196, v232, 0 op_sel_hi:[1,1,0]\n"
    "v_fma_mix_f32 v232, v196, v232, 0 op_sel:[1,1,0] op_sel_hi:[1,1,0]\n"
    "v_mfma_f32_16x16x32_bf16 v[128:131], v[8:11], v[192:195], v[128:131]\n"
    "ds_read_b128 a[252:255], v249 offset:1024\n"
    "v_cvt_pk_bf16_f32 v196, v234, v232\n"
    "v_mfma_f32_16x16x32_bf16 v[132:135], v[12:15], v[192:195], v[132:135]\n"
    "v_pk_fma_f16 v232, v184, v184, s48\n"
    "v_or_b32_e32 v187, v187, v203\n"
    "v_mfma_f32_16x16x32_bf16 v[104:107], a[16:19], v[196:199], v[104:107]\n"
    "s_waitcnt lgkmcnt(5)\n"
    "v_pk_mul_f16 v232, v184, v232\n"
    "v_or_b32_e64 v189, v189, v205\n"
    "v_mfma_f32_16x16x32_bf16 v[108:111], a[20:23], v[196:199], v[108:111]\n"
    "v_exp_f16_e32 v234, v232\n"
    "v_mfma_f32_16x16x32_bf16 v[120:123], a[24:27], v[196:199], v[120:123]\n"
    "v_exp_f16_sdwa v234, v232 dst_sel:WORD_1 dst_unused:UNUSED_PRESERVE src0_sel:WORD_1\n"
    "v_mfma_f32_16x16x32_bf16 v[124:127], a[28:31], v[196:199], v[124:127]\n"
    "s_waitcnt lgkmcnt(3)\n"
    "v_pk_fma_f16 v234, v234, s49, s49\n"
    "v_lshlrev_b32_sdwa v192, 1, v168 dst_sel:DWORD dst_unused:UNUSED_PAD src0_sel:DWORD src1_sel:WORD_1\n"
    "v_mfma_f32_16x16x32_bf16 v[136:139], v[8:11], v[196:199], v[136:139]\n"
    "ds_read_u16 v192, v192\n"
    "v_lshlrev_b32_sdwa v200, 1, v169 dst_sel:DWORD dst_unused:UNUSED_PAD src0_sel:DWORD src1_sel:WORD_1\n"
    "v_mfma_f32_16x16x32_bf16 v[140:143], v[12:15], v[196:199], v[140:143]\n"
    "ds_read_u16_d16_hi v200, v200\n"
    "v_lshlrev_b32_sdwa v193, 1, v170 dst_sel:DWORD dst_unused:UNUSED_PAD src0_sel:DWORD src1_sel:WORD_1\n"
    "v_lshlrev_b32_sdwa v201, 1, v171 dst_sel:DWORD dst_unused:UNUSED_PAD src0_sel:DWORD src1_sel:WORD_1\n"
    "v_mfma_f32_16x16x32_bf16 v[168:171], a[112:115], v[144:147], 0\n"
    "s_waitcnt lgkmcnt(2)\n"
    "ds_read_u16 v193, v193\n"
    "ds_read_u16_d16_hi v201, v201\n"
    "v_mfma_f32_16x16x32_bf16 v[168:171], a[160:163], v[148:151], v[168:171]\n"
    "s_nop 0\n"
    "buffer_store_dwordx4 a[252:255], v255, s[44:47], s88 offen offset:0 sc1\n"
    "v_lshlrev_b32_sdwa v194, 1, v172 dst_sel:DWORD dst_unused:UNUSED_PAD src0_sel:DWORD src1_sel:WORD_1\n"
    "v_lshlrev_b32_sdwa v202, 1, v173 dst_sel:DWORD dst_unused:UNUSED_PAD src0_sel:DWORD src1_sel:WORD_1\n"
    "v_lshlrev_b32_sdwa v195, 1, v174 dst_sel:DWORD dst_unused:UNUSED_PAD src0_sel:DWORD src1_sel:WORD_1\n"
    "v_lshlrev_b32_sdwa v203, 1, v175 dst_sel:DWORD dst_unused:UNUSED_PAD src0_sel:DWORD src1_sel:WORD_1\n"
    "v_rcp_f16_e64 v232, v234\n"
    "v_mfma_f32_16x16x32_bf16 v[168:171], a[72:75], v[152:155], v[168:171]\n"
    "ds_read_u16 v194, v194\n"
    "ds_read_u16_d16_hi v202, v202\n"
    "v_or_b32_e64 v190, v190, v206\n"
    "v_rcp_f16_sdwa v232, v234 dst_sel:WORD_1 dst_unused:UNUSED_PRESERVE src0_sel:WORD_1\n"
    "v_mfma_f32_16x16x32_bf16 v[172:175], a[116:119], v[144:147], 0\n"
    "ds_read_u16 v195, v195\n"
    "ds_read_u16_d16_hi v203, v203\n"
    "v_lshlrev_b32_sdwa v196, 1, v176 dst_sel:DWORD dst_unused:UNUSED_PAD src0_sel:DWORD src1_sel:WORD_1\n"
    "v_lshlrev_b32_sdwa v204, 1, v177 dst_sel:DWORD dst_unused:UNUSED_PAD src0_sel:DWORD src1_sel:WORD_1\n"
    "v_lshlrev_b32_sdwa v197, 1, v178 dst_sel:DWORD dst_unused:UNUSED_PAD src0_sel:DWORD src1_sel:WORD_1\n"
    "v_lshlrev_b32_sdwa v205, 1, v179 dst_sel:DWORD dst_unused:UNUSED_PAD src0_sel:DWORD src1_sel:WORD_1\n"
    "v_lshlrev_b32_sdwa v198, 1, v180 dst_sel:DWORD dst_unused:UNUSED_PAD src0_sel:DWORD src1_sel:WORD_1\n"
    "v_lshlrev_b32_sdwa v206, 1, v181 dst_sel:DWORD dst_unused:UNUSED_PAD src0_sel:DWORD src1_sel:WORD_1\n"
    "v_lshlrev_b32_sdwa v199, 1, v182 dst_sel:DWORD dst_unused:UNUSED_PAD src0_sel:DWORD src1_sel:WORD_1\n"
    "v_fma_mix_f32 v234, v184, v232, 0 op_sel_hi:[1,1,0]\n"
    "v_mfma_f32_16x16x32_bf16 v[172:175], a[164:167], v[148:151], v[172:175]\n"
    "ds_read_u16 v196, v196\n"
    "v_or_b32_e32 v191, v191, v207\n"
    "v_mfma_f32_16x16x32_bf16 v[172:175], v[52:55], v[152:155], v[172:175]\n"
    "s_waitcnt lgkmcnt(7)\n"
    "ds_read_u16_d16_hi v204, v204\n"
    "v_lshlrev_b32_sdwa v207, 1, v183 dst_sel:DWORD dst_unused:UNUSED_PAD src0_sel:DWORD src1_sel:WORD_1\n"
    "v_fma_mix_f32 v232, v184, v232, 0 op_sel:[1,1,0] op_sel_hi:[1,1,0]\n"
    "v_mfma_f32_16x16x32_bf16 v[176:179], a[112:115], v[156:159], 0\n"
    "ds_read_u16 v197, v197\n"
    "v_cvt_pk_bf16_f32 v184, v234, v232\n"
    "v_mfma_f32_16x16x32_bf16 v[176:179], a[160:163], v[160:163], v[176:179]\n"
    "v_pk_fma_f16 v232, v188, v188, s48\n"
    "v_or_b32_e32 v192, v192, v200\n"
    "v_mfma_f32_16x16x32_bf16 v[176:179], a[72:75], v[164:167], v[176:179]\n"
    "s_waitcnt lgkmcnt(7)\n"
    "ds_read_b128 a[252:255], v249 offset:3072\n"
    "v_pk_mul_f16 v232, v188, v232\n"
    "v_mfma_f32_16x16x32_bf16 v[180:183], a[116:119], v[156:159], 0\n"
    "ds_read_u16_d16_hi v205, v205\n"
    "v_exp_f16_e32 v234, v232\n"
    "v_mfma_f32_16x16x32_bf16 v[180:183], a[164:167], v[160:163], v[180:183]\n"
    "v_exp_f16_sdwa v234, v232 dst_sel:WORD_1 dst_unused:UNUSED_PRESERVE src0_sel:WORD_1\n"
    "v_mfma_f32_16x16x32_bf16 v[180:183], v[52:55], v[164:167], v[180:183]\n"
    "s_waitcnt lgkmcnt(7)\n"
    "ds_read_u16 v198, v198\n"
    "v_pk_fma_f16 v234, v234, s49, s49\n"
    "v_mfma_f32_16x16x32_bf16 v[96:99], a[32:35], v[184:187], v[96:99]\n"
    "ds_read_u16_d16_hi v206, v206\n"
    "v_rcp_f16_e64 v232, v234\n"
    "v_mfma_f32_16x16x32_bf16 v[100:103], a[36:39], v[184:187], v[100:103]\n"
    "v_rcp_f16_sdwa v232, v234 dst_sel:WORD_1 dst_unused:UNUSED_PRESERVE src0_sel:WORD_1\n"
    "v_mfma_f32_16x16x32_bf16 v[112:115], a[40:43], v[184:187], v[112:115]\n"
    "ds_read_u16 v199, v199\n"
    "v_fma_mix_f32 v234, v188, v232, 0 op_sel_hi:[1,1,0]\n"
    "v_mfma_f32_16x16x32_bf16 v[116:119], a[44:47], v[184:187], v[116:119]\n"
    "s_waitcnt lgkmcnt(8)\n"
    "v_fma_mix_f32 v232, v188, v232, 0 op_sel:[1,1,0] op_sel_hi:[1,1,0]\n"
    "v_or_b32_e32 v193, v193, v201\n"
    "v_mfma_f32_16x16x32_bf16 v[128:131], v[16:19], v[184:187], v[128:131]\n"
    "ds_read_u16_d16_hi v207, v207\n"
    "v_cvt_pk_bf16_f32 v188, v234, v232\n"
    "v_mfma_f32_16x16x32_bf16 v[132:135], v[20:23], v[184:187], v[132:135]\n"
    "v_cvt_pk_f16_f32 v184, v168, v169\n"
    "v_lshlrev_b32_sdwa v185, 1, v170 dst_sel:DWORD dst_unused:UNUSED_PAD src0_sel:DWORD src1_sel:WORD_1\n"
    "v_mfma_f32_16x16x32_bf16 v[104:107], a[32:35], v[188:191], v[104:107]\n"
    "s_waitcnt lgkmcnt(7)\n"
    "ds_read_u16 v185, v185\n"
    "v_lshlrev_b32_sdwa v201, 1, v171 dst_sel:DWORD dst_unused:UNUSED_PAD src0_sel:DWORD src1_sel:WORD_1\n"
    "v_mfma_f32_16x16x32_bf16 v[108:111], a[36:39], v[188:191], v[108:111]\n"
    "ds_read_u16_d16_hi v201, v201\n"
    "s_waitcnt lgkmcnt(7)\n"
    "buffer_store_dwordx4 a[252:255], v255, s[44:47], s88 offen offset:64 sc1\n"
    "v_mfma_f32_16x16x32_bf16 v[120:123], a[40:43], v[188:191], v[120:123]\n"
    "v_lshlrev_b32_sdwa v186, 1, v172 dst_sel:DWORD dst_unused:UNUSED_PAD src0_sel:DWORD src1_sel:WORD_1\n"
    "v_or_b32_e32 v194, v194, v202\n"
    "v_mfma_f32_16x16x32_bf16 v[124:127], a[44:47], v[188:191], v[124:127]\n"
    "s_waitcnt lgkmcnt(6)\n"
    "ds_read_b128 a[252:255], v249 offset:5120\n"
    "ds_read_u16 v186, v186\n"
    "v_mfma_f32_16x16x32_bf16 v[136:139], v[16:19], v[188:191], v[136:139]\n"
    "v_lshlrev_b32_sdwa v202, 1, v173 dst_sel:DWORD dst_unused:UNUSED_PAD src0_sel:DWORD src1_sel:WORD_1\n"
    "v_lshlrev_b32_sdwa v187, 1, v174 dst_sel:DWORD dst_unused:UNUSED_PAD src0_sel:DWORD src1_sel:WORD_1\n"
    "v_mfma_f32_16x16x32_bf16 v[140:143], v[20:23], v[188:191], v[140:143]\n"
    "ds_read_u16_d16_hi v202, v202\n"
    "v_cvt_pk_f16_f32 v188, v176, v177\n"
    "v_mfma_f32_16x16x32_bf16 v[168:171], a[120:123], v[144:147], 0\n"
    "ds_read_u16 v187, v187\n"
    "v_lshlrev_b32_sdwa v189, 1, v178 dst_sel:DWORD dst_unused:UNUSED_PAD src0_sel:DWORD src1_sel:WORD_1\n"
    "v_mfma_f32_16x16x32_bf16 v[168:171], a[168:171], v[148:151], v[168:171]\n"
    "s_waitcnt lgkmcnt(8)\n"
    "v_lshlrev_b32_sdwa v190, 1, v180 dst_sel:DWORD dst_unused:UNUSED_PAD src0_sel:DWORD src1_sel:WORD_1\n"
    "v_lshlrev_b32_sdwa v191, 1, v182 dst_sel:DWORD dst_unused:UNUSED_PAD src0_sel:DWORD src1_sel:WORD_1\n"
    "v_mfma_f32_16x16x32_bf16 v[168:171], a[76:79], v[152:155], v[168:171]\n"
    "v_or_b32_e32 v195, v195, v203\n"
    "v_lshlrev_b32_sdwa v203, 1, v175 dst_sel:DWORD dst_unused:UNUSED_PAD src0_sel:DWORD src1_sel:WORD_1\n"
    "v_mfma_f32_16x16x32_bf16 v[172:175], a[124:127], v[144:147], 0\n"
    "ds_read_u16_d16_hi v203, v203\n"
    "ds_read_u16 v189, v189\n"
    "v_mfma_f32_16x16x32_bf16 v[172:175], a[172:175], v[148:151], v[172:175]\n"
    "s_waitcnt lgkmcnt(8)\n"
    "v_or_b32_e32 v196, v196, v204\n"
    "v_or_b32_e64 v197, v197, v205\n"
    "v_mfma_f32_16x16x32_bf16 v[172:175], v[60:63], v[152:155], v[172:175]\n"
    "ds_read_u16 v190, v190\n"
    "v_lshlrev_b32_sdwa v205, 1, v179 dst_sel:DWORD dst_unused:UNUSED_PAD src0_sel:DWORD src1_sel:WORD_1\n"
    "v_mfma_f32_16x16x32_bf16 v[176:179], a[120:123], v[156:159], 0\n"
    "v_or_b32_e64 v198, v198, v206\n"
    "v_lshlrev_b32_sdwa v206, 1, v181 dst_sel:DWORD dst_unused:UNUSED_PAD src0_sel:DWORD src1_sel:WORD_1\n"
    "v_mfma_f32_16x16x32_bf16 v[176:179], a[168:171], v[160:163], v[176:179]\n"
    "ds_read_u16_d16_hi v205, v205\n"
    "s_waitcnt lgkmcnt(7)\n"
    "buffer_store_dwordx4 a[252:255], v255, s[44:47], s88 offen offset:128 sc1\n"
    "v_mfma_f32_16x16x32_bf16 v[176:179], a[76:79], v[164:167], v[176:179]\n"
    "s_nop 0\n"
    "ds_read_b128 v[144:147], v243 offset:0\n"
    "ds_read_u16_d16_hi v206, v206\n"
    "v_mfma_f32_16x16x32_bf16 v[96:99], a[176:179], v[192:195], v[96:99]\n"
    "v_or_b32_e64 v199, v199, v207\n"
    "v_lshlrev_b32_sdwa v207, 1, v183 dst_sel:DWORD dst_unused:UNUSED_PAD src0_sel:DWORD src1_sel:WORD_1\n"
    "v_mfma_f32_16x16x32_bf16 v[180:183], a[124:127], v[156:159], 0\n"
    "ds_read_b128 v[148:151], v243 offset:1024\n"
    "v_or_b32_e32 v185, v185, v201\n"
    "v_mfma_f32_16x16x32_bf16 v[180:183], a[172:175], v[160:163], v[180:183]\n"
    "s_waitcnt lgkmcnt(8)\n"
    "ds_read_u16 v191, v191\n"
    "v_or_b32_e64 v186, v186, v202\n"
    "v_mfma_f32_16x16x32_bf16 v[180:183], v[60:63], v[164:167], v[180:183]\n"
    "v_pk_fma_f16 v232, v184, v184, s48\n"
    "v_lshlrev_b32_sdwa v201, 1, v171 dst_sel:DWORD dst_unused:UNUSED_PAD src0_sel:DWORD src1_sel:WORD_1\n"
    "v_mfma_f32_16x16x32_bf16 v[100:103], a[180:183], v[192:195], v[100:103]\n"
    "ds_read_b128 v[152:155], v243 offset:2048\n"
    "v_pk_mul_f16 v232, v184, v232\n"
    "v_mfma_f32_16x16x32_bf16 v[112:115], a[184:187], v[192:195], v[112:115]\n"
    "ds_read_u16_d16_hi v207, v207\n"
    "s_waitcnt lgkmcnt(9)\n"
    "v_or_b32_e32 v187, v187, v203\n"
    "v_mfma_f32_16x16x32_bf16 v[116:119], a[188:191], v[192:195], v[116:119]\n"
    "v_exp_f16_e64 v234, v232\n"
    "v_mfma_f32_16x16x32_bf16 v[128:131], v[24:27], v[192:195], v[128:131]\n"
    "ds_read_b128 v[156:159], v243 offset:3072\n"
    "ds_read_b128 v[160:163], v243 offset:4096\n"
    "v_mfma_f32_16x16x32_bf16 v[132:135], v[28:31], v[192:195], v[132:135]\n"
    "v_exp_f16_sdwa v234, v232 dst_sel:WORD_1 dst_unused:UNUSED_PRESERVE src0_sel:WORD_1\n"
    "v_mfma_f32_16x16x32_bf16 v[104:107], a[176:179], v[196:199], v[104:107]\n"
    "ds_read_b128 v[164:167], v243 offset:5120\n"
    "ds_read_u16_d16_hi v201, v201\n"
    "v_mfma_f32_16x16x32_bf16 v[108:111], a[180:183], v[196:199], v[108:111]\n"
    "v_pk_fma_f16 v234, v234, s49, s49\n"
    "s_waitcnt lgkmcnt(9)\n"
    "v_or_b32_e32 v189, v189, v205\n"
    "v_mfma_f32_16x16x32_bf16 v[120:123], a[184:187], v[196:199], v[120:123]\n"
    "v_cvt_pk_f16_f32 v192, v168, v169\n"
    "v_lshlrev_b32_sdwa v193, 1, v170 dst_sel:DWORD dst_unused:UNUSED_PAD src0_sel:DWORD src1_sel:WORD_1\n"
    "v_mfma_f32_16x16x32_bf16 v[124:127], a[188:191], v[196:199], v[124:127]\n"
    "s_nop 0\n"
    "ds_read_u16 v193, v193\n"
    "v_lshlrev_b32_sdwa v194, 1, v172 dst_sel:DWORD dst_unused:UNUSED_PAD src0_sel:DWORD src1_sel:WORD_1\n"
    "v_mfma_f32_16x16x32_bf16 v[136:139], v[24:27], v[196:199], v[136:139]\n"
    "s_waitcnt lgkmcnt(8)\n"
    "ds_read_u16 v194, v194\n"
    "v_lshlrev_b32_sdwa v202, 1, v173 dst_sel:DWORD dst_unused:UNUSED_PAD src0_sel:DWORD src1_sel:WORD_1\n"
    "v_mfma_f32_16x16x32_bf16 v[140:143], v[28:31], v[196:199], v[140:143]\n"
    "v_lshlrev_b32_sdwa v195, 1, v174 dst_sel:DWORD dst_unused:UNUSED_PAD src0_sel:DWORD src1_sel:WORD_1\n"
    "v_lshlrev_b32_sdwa v203, 1, v175 dst_sel:DWORD dst_unused:UNUSED_PAD src0_sel:DWORD src1_sel:WORD_1\n"
    "v_mfma_f32_16x16x32_bf16 v[168:171], a[80:83], v[144:147], 0\n"
    "ds_read_u16_d16_hi v202, v202\n"
    "v_rcp_f16_e32 v232, v234\n"
    "v_mfma_f32_16x16x32_bf16 v[168:171], a[128:131], v[148:151], v[168:171]\n"
    "s_waitcnt lgkmcnt(8)\n"
    "ds_read_u16 v195, v195\n"
    "v_or_b32_e32 v190, v190, v206\n"
    "v_mfma_f32_16x16x32_bf16 v[168:171], a[48:51], v[152:155], v[168:171]\n"
    "v_rcp_f16_sdwa v232, v234 dst_sel:WORD_1 dst_unused:UNUSED_PRESERVE src0_sel:WORD_1\n"
    "v_mfma_f32_16x16x32_bf16 v[172:175], a[84:87], v[144:147], 0\n"
    "s_waitcnt lgkmcnt(7)\n"
    "ds_read_u16_d16_hi v203, v203\n"
    "v_cvt_pk_f16_f32 v196, v176, v177\n"
    "v_mfma_f32_16x16x32_bf16 v[172:175], a[132:135], v[148:151], v[172:175]\n"
    "v_lshlrev_b32_sdwa v197, 1, v178 dst_sel:DWORD dst_unused:UNUSED_PAD src0_sel:DWORD src1_sel:WORD_1\n"
    "v_lshlrev_b32_sdwa v205, 1, v179 dst_sel:DWORD dst_unused:UNUSED_PAD src0_sel:DWORD src1_sel:WORD_1\n"
    "v_lshlrev_b32_sdwa v198, 1, v180 dst_sel:DWORD dst_unused:UNUSED_PAD src0_sel:DWORD src1_sel:WORD_1\n"
    "v_lshlrev_b32_sdwa v206, 1, v181 dst_sel:DWORD dst_unused:UNUSED_PAD src0_sel:DWORD src1_sel:WORD_1\n"
    "v_lshlrev_b32_sdwa v199, 1, v182 dst_sel:DWORD dst_unused:UNUSED_PAD src0_sel:DWORD src1_sel:WORD_1\n"
    "v_fma_mix_f32 v234, v184, v232, 0 op_sel_hi:[1,1,0]\n"
    "v_mfma_f32_16x16x32_bf16 v[172:175], v[48:51], v[152:155], v[172:175]\n"
    "s_waitcnt lgkmcnt(6)\n"
    "ds_read_u16 v197, v197\n"
    "v_or_b32_e32 v191, v191, v207\n"
    "v_lshlrev_b32_sdwa v207, 1, v183 dst_sel:DWORD dst_unused:UNUSED_PAD src0_sel:DWORD src1_sel:WORD_1\n"
    "v_fma_mix_f32 v232, v184, v232, 0 op_sel:[1,1,0] op_sel_hi:[1,1,0]\n"
    "v_mfma_f32_16x16x32_bf16 v[176:179], a[80:83], v[156:159], 0\n"
    "s_barrier\n"
    "v_cvt_pk_bf16_f32 v184, v234, v232\n"
    "v_mfma_f32_16x16x32_bf16 v[176:179], a[128:131], v[160:163], v[176:179]\n"
    "s_waitcnt lgkmcnt(5)\n"
    "ds_read_u16_d16_hi v205, v205\n"
    "ds_read_u16 v198, v198\n"
    "v_mfma_f32_16x16x32_bf16 v[176:179], a[48:51], v[164:167], v[176:179]\n"
    "v_pk_fma_f16 v232, v188, v188, s48\n"
    "v_or_b32_e64 v193, v193, v201\n"
    "v_mfma_f32_16x16x32_bf16 v[180:183], a[84:87], v[156:159], 0\n"
    "ds_read_u16_d16_hi v206, v206\n"
    "v_pk_mul_f16 v232, v188, v232\n"
    "v_mfma_f32_16x16x32_bf16 v[180:183], a[132:135], v[160:163], v[180:183]\n"
    "ds_read_u16 v199, v199\n"
    "v_exp_f16_e32 v234, v232\n"
    "v_mfma_f32_16x16x32_bf16 v[180:183], v[48:51], v[164:167], v[180:183]\n"
    "s_waitcnt lgkmcnt(7)\n"
    "ds_read_u16_d16_hi v207, v207\n"
    "v_exp_f16_sdwa v234, v232 dst_sel:WORD_1 dst_unused:UNUSED_PRESERVE src0_sel:WORD_1\n"
    "v_mfma_f32_16x16x32_bf16 v[96:99], a[192:195], v[184:187], v[96:99]\n"
    "v_pk_fma_f16 v234, v234, s49, s49\n"
    "v_or_b32_e64 v194, v194, v202\n"
    "v_mfma_f32_16x16x32_bf16 v[100:103], a[196:199], v[184:187], v[100:103]\n"
    "v_rcp_f16_e32 v232, v234\n"
    "v_mfma_f32_16x16x32_bf16 v[112:115], a[200:203], v[184:187], v[112:115]\n"
    "s_waitcnt lgkmcnt(6)\n"
    "v_rcp_f16_sdwa v232, v234 dst_sel:WORD_1 dst_unused:UNUSED_PRESERVE src0_sel:WORD_1\n"
    "v_mfma_f32_16x16x32_bf16 v[116:119], a[204:207], v[184:187], v[116:119]\n"
    "v_fma_mix_f32 v234, v188, v232, 0 op_sel_hi:[1,1,0]\n"
    "v_fma_mix_f32 v232, v188, v232, 0 op_sel:[1,1,0] op_sel_hi:[1,1,0]\n"
    "v_mfma_f32_16x16x32_bf16 v[128:131], v[32:35], v[184:187], v[128:131]\n"
    "v_cvt_pk_bf16_f32 v188, v234, v232\n"
    "v_pk_fma_f16 v232, v192, v192, s48\n"
    "v_mfma_f32_16x16x32_bf16 v[132:135], v[36:39], v[184:187], v[132:135]\n"
    "s_waitcnt lgkmcnt(4)\n"
    "v_or_b32_e32 v195, v195, v203\n"
    "v_pk_mul_f16 v232, v192, v232\n"
    "v_mfma_f32_16x16x32_bf16 v[104:107], a[192:195], v[188:191], v[104:107]\n"
    "v_exp_f16_e64 v234, v232\n"
    "v_mfma_f32_16x16x32_bf16 v[108:111], a[196:199], v[188:191], v[108:111]\n"
    "v_exp_f16_sdwa v234, v232 dst_sel:WORD_1 dst_unused:UNUSED_PRESERVE src0_sel:WORD_1\n"
    "v_mfma_f32_16x16x32_bf16 v[120:123], a[200:203], v[188:191], v[120:123]\n"
    "v_or_b32_e32 v197, v197, v205\n"
    "v_pk_fma_f16 v234, v234, s49, s49\n"
    "v_mfma_f32_16x16x32_bf16 v[124:127], a[204:207], v[188:191], v[124:127]\n"
    "s_waitcnt lgkmcnt(2)\n"
    "v_lshlrev_b32_sdwa v184, 1, v168 dst_sel:DWORD dst_unused:UNUSED_PAD src0_sel:DWORD src1_sel:WORD_1\n"
    "v_lshlrev_b32_sdwa v200, 1, v169 dst_sel:DWORD dst_unused:UNUSED_PAD src0_sel:DWORD src1_sel:WORD_1\n"
    "v_mfma_f32_16x16x32_bf16 v[136:139], v[32:35], v[188:191], v[136:139]\n"
    "ds_read_u16 v184, v184\n"
    "ds_read_u16_d16_hi v200, v200\n"
    "v_mfma_f32_16x16x32_bf16 v[140:143], v[36:39], v[188:191], v[140:143]\n"
    "s_nop 0\n"
    "v_lshlrev_b32_sdwa v185, 1, v170 dst_sel:DWORD dst_unused:UNUSED_PAD src0_sel:DWORD src1_sel:WORD_1\n"
    "v_lshlrev_b32_sdwa v201, 1, v171 dst_sel:DWORD dst_unused:UNUSED_PAD src0_sel:DWORD src1_sel:WORD_1\n"
    "v_mfma_f32_16x16x32_bf16 v[168:171], a[88:91], v[144:147], 0\n"
    "s_waitcnt lgkmcnt(2)\n"
    "ds_read_u16 v185, v185\n"
    "ds_read_u16_d16_hi v201, v201\n"
    "v_mfma_f32_16x16x32_bf16 v[168:171], a[136:139], v[148:151], v[168:171]\n"
    "v_lshlrev_b32_sdwa v186, 1, v172 dst_sel:DWORD dst_unused:UNUSED_PAD src0_sel:DWORD src1_sel:WORD_1\n"
    "v_lshlrev_b32_sdwa v202, 1, v173 dst_sel:DWORD dst_unused:UNUSED_PAD src0_sel:DWORD src1_sel:WORD_1\n"
    "v_lshlrev_b32_sdwa v187, 1, v174 dst_sel:DWORD dst_unused:UNUSED_PAD src0_sel:DWORD src1_sel:WORD_1\n"
    "v_lshlrev_b32_sdwa v203, 1, v175 dst_sel:DWORD dst_unused:UNUSED_PAD src0_sel:DWORD src1_sel:WORD_1\n"
    "v_rcp_f16_e64 v232, v234\n"
    "v_mfma_f32_16x16x32_bf16 v[168:171], a[52:55], v[152:155], v[168:171]\n"
    "ds_read_u16 v186, v186\n"
    "ds_read_u16_d16_hi v202, v202\n"
    "v_or_b32_e64 v198, v198, v206\n"
    "v_rcp_f16_sdwa v232, v234 dst_sel:WORD_1 dst_unused:UNUSED_PRESERVE src0_sel:WORD_1\n"
    "v_mfma_f32_16x16x32_bf16 v[172:175], a[92:95], v[144:147], 0\n"
    "ds_read_u16 v187, v187\n"
    "v_lshlrev_b32_sdwa v188, 1, v176 dst_sel:DWORD dst_unused:UNUSED_PAD src0_sel:DWORD src1_sel:WORD_1\n"
    "v_mfma_f32_16x16x32_bf16 v[172:175], a[140:143], v[148:151], v[172:175]\n"
    "v_lshlrev_b32_sdwa v204, 1, v177 dst_sel:DWORD dst_unused:UNUSED_PAD src0_sel:DWORD src1_sel:WORD_1\n"
    "v_lshlrev_b32_sdwa v189, 1, v178 dst_sel:DWORD dst_unused:UNUSED_PAD src0_sel:DWORD src1_sel:WORD_1\n"
    "v_lshlrev_b32_sdwa v205, 1, v179 dst_sel:DWORD dst_unused:UNUSED_PAD src0_sel:DWORD src1_sel:WORD_1\n"
    "v_lshlrev_b32_sdwa v190, 1, v180 dst_sel:DWORD dst_unused:UNUSED_PAD src0_sel:DWORD src1_sel:WORD_1\n"
    "v_lshlrev_b32_sdwa v206, 1, v181 dst_sel:DWORD dst_unused:UNUSED_PAD src0_sel:DWORD src1_sel:WORD_1\n"
    "v_lshlrev_b32_sdwa v191, 1, v182 dst_sel:DWORD dst_unused:UNUSED_PAD src0_sel:DWORD src1_sel:WORD_1\n"
    "v_fma_mix_f32 v234, v192, v232, 0 op_sel_hi:[1,1,0]\n"
    "v_mfma_f32_16x16x32_bf16 v[172:175], v[56:59], v[152:155], v[172:175]\n"
    "ds_read_u16_d16_hi v203, v203\n"
    "ds_read_u16 v188, v188\n"
    "v_or_b32_e32 v199, v199, v207\n"
    "v_lshlrev_b32_sdwa v207, 1, v183 dst_sel:DWORD dst_unused:UNUSED_PAD src0_sel:DWORD src1_sel:WORD_1\n"
    "v_fma_mix_f32 v232, v192, v232, 0 op_sel:[1,1,0] op_sel_hi:[1,1,0]\n"
    "v_mfma_f32_16x16x32_bf16 v[176:179], a[88:91], v[156:159], 0\n"
    "s_waitcnt lgkmcnt(7)\n"
    "ds_read_u16_d16_hi v204, v204\n"
    "v_cvt_pk_bf16_f32 v192, v234, v232\n"
    "v_mfma_f32_16x16x32_bf16 v[176:179], a[136:139], v[160:163], v[176:179]\n"
    "v_pk_fma_f16 v232, v196, v196, s48\n"
    "v_or_b32_e64 v184, v184, v200\n"
    "v_mfma_f32_16x16x32_bf16 v[176:179], a[52:55], v[164:167], v[176:179]\n"
    "ds_read_u16 v189, v189\n"
    "v_pk_mul_f16 v232, v196, v232\n"
    "v_mfma_f32_16x16x32_bf16 v[180:183], a[92:95], v[156:159], 0\n"
    "v_exp_f16_e32 v234, v232\n"
    "v_mfma_f32_16x16x32_bf16 v[180:183], a[140:143], v[160:163], v[180:183]\n"
    "s_waitcnt lgkmcnt(7)\n"
    "ds_read_u16_d16_hi v205, v205\n"
    "v_exp_f16_sdwa v234, v232 dst_sel:WORD_1 dst_unused:UNUSED_PRESERVE src0_sel:WORD_1\n"
    "v_mfma_f32_16x16x32_bf16 v[180:183], v[56:59], v[164:167], v[180:183]\n"
    "ds_read_u16 v190, v190\n"
    "v_pk_fma_f16 v234, v234, s49, s49\n"
    "v_mfma_f32_16x16x32_bf16 v[96:99], a[208:211], v[192:195], v[96:99]\n"
    "v_rcp_f16_e32 v232, v234\n"
    "v_mfma_f32_16x16x32_bf16 v[100:103], a[212:215], v[192:195], v[100:103]\n"
    "s_waitcnt lgkmcnt(7)\n"
    "ds_read_u16_d16_hi v206, v206\n"
    "v_rcp_f16_sdwa v232, v234 dst_sel:WORD_1 dst_unused:UNUSED_PRESERVE src0_sel:WORD_1\n"
    "v_mfma_f32_16x16x32_bf16 v[112:115], a[216:219], v[192:195], v[112:115]\n"
    "v_fma_mix_f32 v234, v196, v232, 0 op_sel_hi:[1,1,0]\n"
    "v_fma_mix_f32 v232, v196, v232, 0 op_sel:[1,1,0] op_sel_hi:[1,1,0]\n"
    "v_mfma_f32_16x16x32_bf16 v[116:119], a[220:223], v[192:195], v[116:119]\n"
    "ds_read_u16 v191, v191\n"
    "v_cvt_pk_bf16_f32 v196, v234, v232\n"
    "v_mfma_f32_16x16x32_bf16 v[128:131], v[40:43], v[192:195], v[128:131]\n"
    "s_waitcnt lgkmcnt(7)\n"
    "ds_read_u16_d16_hi v207, v207\n"
    "v_or_b32_e32 v185, v185, v201\n"
    "v_mfma_f32_16x16x32_bf16 v[132:135], v[44:47], v[192:195], v[132:135]\n"
    "v_cvt_pk_f16_f32 v192, v168, v169\n"
    "v_lshlrev_b32_sdwa v193, 1, v170 dst_sel:DWORD dst_unused:UNUSED_PAD src0_sel:DWORD src1_sel:WORD_1\n"
    "v_mfma_f32_16x16x32_bf16 v[104:107], a[208:211], v[196:199], v[104:107]\n"
    "ds_read_u16 v193, v193\n"
    "v_lshlrev_b32_sdwa v201, 1, v171 dst_sel:DWORD dst_unused:UNUSED_PAD src0_sel:DWORD src1_sel:WORD_1\n"
    "v_mfma_f32_16x16x32_bf16 v[108:111], a[212:215], v[196:199], v[108:111]\n"
    "v_lshlrev_b32_sdwa v194, 1, v172 dst_sel:DWORD dst_unused:UNUSED_PAD src0_sel:DWORD src1_sel:WORD_1\n"
    "v_or_b32_e32 v186, v186, v202\n"
    "v_mfma_f32_16x16x32_bf16 v[120:123], a[216:219], v[196:199], v[120:123]\n"
    "s_waitcnt lgkmcnt(7)\n"
    "ds_read_u16_d16_hi v201, v201\n"
    "v_lshlrev_b32_sdwa v202, 1, v173 dst_sel:DWORD dst_unused:UNUSED_PAD src0_sel:DWORD src1_sel:WORD_1\n"
    "v_mfma_f32_16x16x32_bf16 v[124:127], a[220:223], v[196:199], v[124:127]\n"
    "v_lshlrev_b32_sdwa v195, 1, v174 dst_sel:DWORD dst_unused:UNUSED_PAD src0_sel:DWORD src1_sel:WORD_1\n"
    "v_or_b32_e32 v187, v187, v203\n"
    "v_mfma_f32_16x16x32_bf16 v[136:139], v[40:43], v[196:199], v[136:139]\n"
    "ds_read_u16 v194, v194\n"
    "v_lshlrev_b32_sdwa v203, 1, v175 dst_sel:DWORD dst_unused:UNUSED_PAD src0_sel:DWORD src1_sel:WORD_1\n"
    "v_mfma_f32_16x16x32_bf16 v[140:143], v[44:47], v[196:199], v[140:143]\n"
    "s_waitcnt lgkmcnt(7)\n"
    "ds_read_u16_d16_hi v202, v202\n"
    "v_cvt_pk_f16_f32 v196, v176, v177\n"
    "v_mfma_f32_16x16x32_bf16 v[168:171], a[96:99], v[144:147], 0\n"
    "v_lshlrev_b32_sdwa v197, 1, v178 dst_sel:DWORD dst_unused:UNUSED_PAD src0_sel:DWORD src1_sel:WORD_1\n"
    "v_lshlrev_b32_sdwa v198, 1, v180 dst_sel:DWORD dst_unused:UNUSED_PAD src0_sel:DWORD src1_sel:WORD_1\n"
    "v_mfma_f32_16x16x32_bf16 v[168:171], a[144:147], v[148:151], v[168:171]\n"
    "ds_read_u16 v195, v195\n"
    "v_lshlrev_b32_sdwa v199, 1, v182 dst_sel:DWORD dst_unused:UNUSED_PAD src0_sel:DWORD src1_sel:WORD_1\n"
    "v_mfma_f32_16x16x32_bf16 v[168:171], a[56:59], v[152:155], v[168:171]\n"
    "v_or_b32_e64 v188, v188, v204\n"
    "v_or_b32_e32 v189, v189, v205\n"
    "v_mfma_f32_16x16x32_bf16 v[172:175], a[100:103], v[144:147], 0\n"
    "s_waitcnt lgkmcnt(7)\n"
    "ds_read_u16_d16_hi v203, v203\n"
    "v_lshlrev_b32_sdwa v205, 1, v179 dst_sel:DWORD dst_unused:UNUSED_PAD src0_sel:DWORD src1_sel:WORD_1\n"
    "v_mfma_f32_16x16x32_bf16 v[172:175], a[148:151], v[148:151], v[172:175]\n"
    "v_cvt_pk_bf16_f32 v96, v96, v97\n"
    "v_cvt_pk_bf16_f32 v97, v98, v99\n"
    "v_mfma_f32_16x16x32_bf16 v[172:175], a[60:63], v[152:155], v[172:175]\n"
    "ds_read_u16 v197, v197\n"
    "v_cvt_pk_bf16_f32 v98, v100, v101\n"
    "v_mfma_f32_16x16x32_bf16 v[176:179], a[96:99], v[156:159], 0\n"
    "s_waitcnt lgkmcnt(7)\n"
    "ds_read_u16_d16_hi v205, v205\n"
    "v_cvt_pk_bf16_f32 v99, v102, v103\n"
    "v_mfma_f32_16x16x32_bf16 v[176:179], a[144:147], v[160:163], v[176:179]\n"
    "v_or_b32_e32 v190, v190, v206\n"
    "v_lshlrev_b32_sdwa v206, 1, v181 dst_sel:DWORD dst_unused:UNUSED_PAD src0_sel:DWORD src1_sel:WORD_1\n"
    "v_mfma_f32_16x16x32_bf16 v[176:179], a[56:59], v[164:167], v[176:179]\n"
    "ds_read_u16 v198, v198\n"
    "v_cvt_pk_bf16_f32 v112, v112, v113\n"
    "v_mfma_f32_16x16x32_bf16 v[100:103], a[4:7], v[184:187], 0\n"
    "s_nop 0\n"
    "v_cvt_pk_bf16_f32 v113, v114, v115\n"
    "v_cvt_pk_bf16_f32 v114, v116, v117\n"
    "ds_write_b128 v242, v[96:99] offset:24640\n"
    "v_mfma_f32_16x16x32_bf16 v[96:99], a[0:3], v[184:187], 0\n"
    "s_waitcnt lgkmcnt(7)\n"
    "v_cvt_pk_bf16_f32 v115, v118, v119\n"
    "v_cvt_pk_bf16_f32 v128, v128, v129\n"
    "v_cvt_pk_bf16_f32 v129, v130, v131\n"
    "v_or_b32_e32 v191, v191, v207\n"
    "v_mfma_f32_16x16x32_bf16 v[116:119], a[12:15], v[184:187], 0\n"
    "s_waitcnt lgkmcnt(6)\n"
    "ds_read_u16_d16_hi v206, v206\n"
    "ds_write_b128 v242, v[112:115] offset:26688\n"
    "v_mfma_f32_16x16x32_bf16 v[112:115], a[8:11], v[184:187], 0\n"
    "v_lshlrev_b32_sdwa v207, 1, v183 dst_sel:DWORD dst_unused:UNUSED_PAD src0_sel:DWORD src1_sel:WORD_1\n"
    "v_cvt_pk_bf16_f32 v130, v132, v133\n"
    "v_mfma_f32_16x16x32_bf16 v[180:183], a[100:103], v[156:159], 0\n"
    "v_or_b32_e64 v193, v193, v201\n"
    "v_cvt_pk_bf16_f32 v131, v134, v135\n"
    "v_mfma_f32_16x16x32_bf16 v[180:183], a[148:151], v[160:163], v[180:183]\n"
    "s_waitcnt lgkmcnt(6)\n"
    "ds_read_u16 v199, v199\n"
    "ds_write_b128 v242, v[128:131] offset:28736\n"
    "v_mfma_f32_16x16x32_bf16 v[180:183], a[60:63], v[164:167], v[180:183]\n"
    "v_cvt_pk_bf16_f32 v104, v104, v105\n"
    "v_cvt_pk_bf16_f32 v105, v106, v107\n"
    "v_mfma_f32_16x16x32_bf16 v[128:131], v[0:3], v[184:187], 0\n"
    "s_waitcnt lgkmcnt(6)\n"
    "v_cvt_pk_bf16_f32 v106, v108, v109\n"
    "v_cvt_pk_bf16_f32 v107, v110, v111\n"
    "v_mfma_f32_16x16x32_bf16 v[132:135], v[4:7], v[184:187], 0\n"
    "ds_read_u16_d16_hi v207, v207\n"
    "ds_write_b128 v242, v[104:107] offset:25664\n"
    "v_mfma_f32_16x16x32_bf16 v[104:107], a[0:3], v[188:191], 0\n"
    "v_or_b32_e64 v194, v194, v202\n"
    "v_cvt_pk_bf16_f32 v120, v120, v121\n"
    "v_mfma_f32_16x16x32_bf16 v[108:111], a[4:7], v[188:191], 0\n"
    "v_cvt_pk_bf16_f32 v121, v122, v123\n"
    "v_cvt_pk_bf16_f32 v122, v124, v125\n"
    "v_cvt_pk_bf16_f32 v123, v126, v127\n"
    "ds_write_b128 v242, v[120:123] offset:27712\n"
    "v_mfma_f32_16x16x32_bf16 v[120:123], a[8:11], v[188:191], 0\n"
    "s_waitcnt lgkmcnt(5)\n"
    "v_cvt_pk_bf16_f32 v136, v136, v137\n"
    "v_pk_fma_f16 v232, v192, v192, s48\n"
    "v_mfma_f32_16x16x32_bf16 v[124:127], a[12:15], v[188:191], 0\n"
    "s_nop 0\n"
    "v_cvt_pk_bf16_f32 v137, v138, v139\n"
    "v_cvt_pk_bf16_f32 v138, v140, v141\n"
    "v_or_b32_e64 v195, v195, v203\n"
    "v_cvt_pk_bf16_f32 v139, v142, v143\n"
    "ds_write_b128 v242, v[136:139] offset:29760\n"
    "v_pk_mul_f16 v232, v192, v232\n"
    "v_mfma_f32_16x16x32_bf16 v[136:139], v[0:3], v[188:191], 0\n"
    "v_exp_f16_e64 v234, v232\n"
    "v_mfma_f32_16x16x32_bf16 v[140:143], v[4:7], v[188:191], 0\n"
    "v_exp_f16_sdwa v234, v232 dst_sel:WORD_1 dst_unused:UNUSED_PRESERVE src0_sel:WORD_1\n"
    "v_or_b32_e64 v197, v197, v205\n"
    "v_pk_fma_f16 v234, v234, s49, s49\n"
    "v_cvt_pk_f16_f32 v184, v168, v169\n"
    "v_lshlrev_b32_sdwa v185, 1, v170 dst_sel:DWORD dst_unused:UNUSED_PAD src0_sel:DWORD src1_sel:WORD_1\n"
    "v_lshlrev_b32_sdwa v201, 1, v171 dst_sel:DWORD dst_unused:UNUSED_PAD src0_sel:DWORD src1_sel:WORD_1\n"
    "v_mfma_f32_16x16x32_bf16 v[168:171], a[104:107], v[144:147], 0\n"
    "s_waitcnt lgkmcnt(1)\n"
    "ds_read_u16 v185, v185\n"
    "ds_read_u16_d16_hi v201, v201\n"
    "v_mfma_f32_16x16x32_bf16 v[168:171], a[152:155], v[148:151], v[168:171]\n"
    "s_nop 0\n"
    "v_lshlrev_b32_sdwa v186, 1, v172 dst_sel:DWORD dst_unused:UNUSED_PAD src0_sel:DWORD src1_sel:WORD_1\n"
    "v_lshlrev_b32_sdwa v202, 1, v173 dst_sel:DWORD dst_unused:UNUSED_PAD src0_sel:DWORD src1_sel:WORD_1\n"
    "v_lshlrev_b32_sdwa v187, 1, v174 dst_sel:DWORD dst_unused:UNUSED_PAD src0_sel:DWORD src1_sel:WORD_1\n"
    "v_lshlrev_b32_sdwa v203, 1, v175 dst_sel:DWORD dst_unused:UNUSED_PAD src0_sel:DWORD src1_sel:WORD_1\n"
    "v_rcp_f16_e32 v232, v234\n"
    "v_mfma_f32_16x16x32_bf16 v[168:171], a[64:67], v[152:155], v[168:171]\n"
    "s_waitcnt lgkmcnt(2)\n"
    "ds_read_u16 v186, v186\n"
    "ds_read_u16_d16_hi v202, v202\n"
    "v_or_b32_e32 v198, v198, v206\n"
    "v_rcp_f16_sdwa v232, v234 dst_sel:WORD_1 dst_unused:UNUSED_PRESERVE src0_sel:WORD_1\n"
    "v_mfma_f32_16x16x32_bf16 v[172:175], a[108:111], v[144:147], 0\n"
    "s_barrier\n"
    "v_mfma_f32_16x16x32_bf16 v[172:175], a[156:159], v[148:151], v[172:175]\n"
    "ds_read_u16 v187, v187\n"
    "ds_read_u16_d16_hi v203, v203\n"
    "v_cvt_pk_f16_f32 v188, v176, v177\n"
    "v_lshlrev_b32_sdwa v189, 1, v178 dst_sel:DWORD dst_unused:UNUSED_PAD src0_sel:DWORD src1_sel:WORD_1\n"
    "v_lshlrev_b32_sdwa v205, 1, v179 dst_sel:DWORD dst_unused:UNUSED_PAD src0_sel:DWORD src1_sel:WORD_1\n"
    "v_lshlrev_b32_sdwa v190, 1, v180 dst_sel:DWORD dst_unused:UNUSED_PAD src0_sel:DWORD src1_sel:WORD_1\n"
    "v_lshlrev_b32_sdwa v206, 1, v181 dst_sel:DWORD dst_unused:UNUSED_PAD src0_sel:DWORD src1_sel:WORD_1\n"
    "v_lshlrev_b32_sdwa v191, 1, v182 dst_sel:DWORD dst_unused:UNUSED_PAD src0_sel:DWORD src1_sel:WORD_1\n"
    "v_fma_mix_f32 v234, v192, v232, 0 op_sel_hi:[1,1,0]\n"
    "v_mfma_f32_16x16x32_bf16 v[172:175], a[68:71], v[152:155], v[172:175]\n"
    "ds_read_u16 v189, v189\n"
    "v_or_b32_e64 v199, v199, v207\n"
    "v_lshlrev_b32_sdwa v207, 1, v183 dst_sel:DWORD dst_unused:UNUSED_PAD src0_sel:DWORD src1_sel:WORD_1\n"
    "v_fma_mix_f32 v232, v192, v232, 0 op_sel:[1,1,0] op_sel_hi:[1,1,0]\n"
    "v_mfma_f32_16x16x32_bf16 v[176:179], a[104:107], v[156:159], 0\n"
    "s_nop 0\n"
    "ds_read_u16_d16_hi v205, v205\n"
    "v_cvt_pk_bf16_f32 v192, v234, v232\n"
    "v_mfma_f32_16x16x32_bf16 v[176:179], a[152:155], v[160:163], v[176:179]\n"
    "s_waitcnt lgkmcnt(6)\n"
    "ds_read_u16 v190, v190\n"
    "v_pk_fma_f16 v232, v196, v196, s48\n"
    "v_mfma_f32_16x16x32_bf16 v[176:179], a[64:67], v[164:167], v[176:179]\n"
    "ds_read_u16_d16_hi v206, v206\n"
    "v_pk_mul_f16 v232, v196, v232\n"
    "v_mfma_f32_16x16x32_bf16 v[180:183], a[108:111], v[156:159], 0\n"
    "ds_read_u16 v191, v191\n"
    "v_exp_f16_e32 v234, v232\n"
    "v_mfma_f32_16x16x32_bf16 v[180:183], a[156:159], v[160:163], v[180:183]\n"
    "s_waitcnt lgkmcnt(7)\n"
    "ds_read_u16_d16_hi v207, v207\n"
    "v_exp_f16_sdwa v234, v232 dst_sel:WORD_1 dst_unused:UNUSED_PRESERVE src0_sel:WORD_1\n"
    "v_mfma_f32_16x16x32_bf16 v[180:183], a[68:71], v[164:167], v[180:183]\n"
    "v_pk_fma_f16 v234, v234, s49, s49\n"
    "v_or_b32_e64 v185, v185, v201\n"
    "v_mfma_f32_16x16x32_bf16 v[96:99], a[16:19], v[192:195], v[96:99]\n"
    "v_rcp_f16_e32 v232, v234\n"
    "v_mfma_f32_16x16x32_bf16 v[100:103], a[20:23], v[192:195], v[100:103]\n"
    "v_rcp_f16_sdwa v232, v234 dst_sel:WORD_1 dst_unused:UNUSED_PRESERVE src0_sel:WORD_1\n"
    "v_mfma_f32_16x16x32_bf16 v[112:115], a[24:27], v[192:195], v[112:115]\n"
    "s_waitcnt lgkmcnt(6)\n"
    "v_fma_mix_f32 v234, v196, v232, 0 op_sel_hi:[1,1,0]\n"
    "v_fma_mix_f32 v232, v196, v232, 0 op_sel:[1,1,0] op_sel_hi:[1,1,0]\n"
    "v_mfma_f32_16x16x32_bf16 v[116:119], a[28:31], v[192:195], v[116:119]\n"
    "v_or_b32_e64 v186, v186, v202\n"
    "v_cvt_pk_bf16_f32 v196, v234, v232\n"
    "v_mfma_f32_16x16x32_bf16 v[128:131], v[8:11], v[192:195], v[128:131]\n"
    "v_pk_fma_f16 v232, v184, v184, s48\n"
    "v_or_b32_e32 v187, v187, v203\n"
    "v_mfma_f32_16x16x32_bf16 v[132:135], v[12:15], v[192:195], v[132:135]\n"
    "s_waitcnt lgkmcnt(4)\n"
    "v_pk_mul_f16 v232, v184, v232\n"
    "v_or_b32_e64 v189, v189, v205\n"
    "v_mfma_f32_16x16x32_bf16 v[104:107], a[16:19], v[196:199], v[104:107]\n"
    "v_exp_f16_e32 v234, v232\n"
    "v_mfma_f32_16x16x32_bf16 v[108:111], a[20:23], v[196:199], v[108:111]\n"
    "v_exp_f16_sdwa v234, v232 dst_sel:WORD_1 dst_unused:UNUSED_PRESERVE src0_sel:WORD_1\n"
    "v_mfma_f32_16x16x32_bf16 v[120:123], a[24:27], v[196:199], v[120:123]\n"
    "v_pk_fma_f16 v234, v234, s49, s49\n"
    "v_lshlrev_b32_sdwa v192, 1, v168 dst_sel:DWORD dst_unused:UNUSED_PAD src0_sel:DWORD src1_sel:WORD_1\n"
    "v_mfma_f32_16x16x32_bf16 v[124:127], a[28:31], v[196:199], v[124:127]\n"
    "s_waitcnt lgkmcnt(2)\n"
    "ds_read_u16 v192, v192\n"
    "v_lshlrev_b32_sdwa v200, 1, v169 dst_sel:DWORD dst_unused:UNUSED_PAD src0_sel:DWORD src1_sel:WORD_1\n"
    "v_mfma_f32_16x16x32_bf16 v[136:139], v[8:11], v[196:199], v[136:139]\n"
    "ds_read_u16_d16_hi v200, v200\n"
    "v_lshlrev_b32_sdwa v193, 1, v170 dst_sel:DWORD dst_unused:UNUSED_PAD src0_sel:DWORD src1_sel:WORD_1\n"
    "v_mfma_f32_16x16x32_bf16 v[140:143], v[12:15], v[196:199], v[140:143]\n"
    "ds_read_u16 v193, v193\n"
    "v_lshlrev_b32_sdwa v201, 1, v171 dst_sel:DWORD dst_unused:UNUSED_PAD src0_sel:DWORD src1_sel:WORD_1\n"
    "s_waitcnt vmcnt(8)\n"
    "v_smfmac_f32_32x32x32_bf16 v[112:127], a[248:251], a[232:239], v241\n"
    "s_waitcnt lgkmcnt(3)\n"
    "ds_read_u16_d16_hi v201, v201\n"
    "ds_read_b128 a[232:235], v243 offset:24640\n"
    "v_lshlrev_b32_sdwa v194, 1, v172 dst_sel:DWORD dst_unused:UNUSED_PAD src0_sel:DWORD src1_sel:WORD_1\n"
    "v_lshlrev_b32_sdwa v202, 1, v173 dst_sel:DWORD dst_unused:UNUSED_PAD src0_sel:DWORD src1_sel:WORD_1\n"
    "v_lshlrev_b32_sdwa v195, 1, v174 dst_sel:DWORD dst_unused:UNUSED_PAD src0_sel:DWORD src1_sel:WORD_1\n"
    "s_waitcnt vmcnt(7)\n"
    "v_smfmac_f32_32x32x32_bf16 v[96:111], a[248:251], a[224:231], v241\n"
    "s_nop 0\n"
    "ds_read_u16 v194, v194\n"
    "ds_read_u16_d16_hi v202, v202\n"
    "v_lshlrev_b32_sdwa v203, 1, v175 dst_sel:DWORD dst_unused:UNUSED_PAD src0_sel:DWORD src1_sel:WORD_1\n"
    "v_rcp_f16_e32 v232, v234\n"
    "s_waitcnt vmcnt(6)\n"
    "v_smfmac_f32_32x32x32_bf16 v[128:143], a[248:251], v[208:215], v241\n"
    "ds_read_b128 a[236:239], v243 offset:25664\n"
    "v_or_b32_e64 v190, v190, v206\n"
    "v_rcp_f16_sdwa v232, v234 dst_sel:WORD_1 dst_unused:UNUSED_PRESERVE src0_sel:WORD_1\n"
    "v_lshlrev_b32_sdwa v196, 1, v176 dst_sel:DWORD dst_unused:UNUSED_PAD src0_sel:DWORD src1_sel:WORD_1\n"
    "v_mfma_f32_16x16x32_bf16 v[168:171], a[112:115], v[144:147], 0\n"
    "ds_read_u16 v195, v195\n"
    "v_lshlrev_b32_sdwa v204, 1, v177 dst_sel:DWORD dst_unused:UNUSED_PAD src0_sel:DWORD src1_sel:WORD_1\n"
    "v_mfma_f32_16x16x32_bf16 v[168:171], a[160:163], v[148:151], v[168:171]\n"
    "ds_read_u16_d16_hi v203, v203\n"
    "v_lshlrev_b32_sdwa v197, 1, v178 dst_sel:DWORD dst_unused:UNUSED_PAD src0_sel:DWORD src1_sel:WORD_1\n"
    "v_mfma_f32_16x16x32_bf16 v[168:171], a[72:75], v[152:155], v[168:171]\n"
    "v_lshlrev_b32_sdwa v205, 1, v179 dst_sel:DWORD dst_unused:UNUSED_PAD src0_sel:DWORD src1_sel:WORD_1\n"
    "v_lshlrev_b32_sdwa v198, 1, v180 dst_sel:DWORD dst_unused:UNUSED_PAD src0_sel:DWORD src1_sel:WORD_1\n"
    "v_mfma_f32_16x16x32_bf16 v[172:175], a[116:119], v[144:147], 0\n"
    "ds_read_u16 v196, v196\n"
    "v_lshlrev_b32_sdwa v206, 1, v181 dst_sel:DWORD dst_unused:UNUSED_PAD src0_sel:DWORD src1_sel:WORD_1\n"
    "v_mfma_f32_16x16x32_bf16 v[172:175], a[164:167], v[148:151], v[172:175]\n"
    "v_lshlrev_b32_sdwa v199, 1, v182 dst_sel:DWORD dst_unused:UNUSED_PAD src0_sel:DWORD src1_sel:WORD_1\n"
    "v_fma_mix_f32 v234, v184, v232, 0 op_sel_hi:[1,1,0]\n"
    "v_mfma_f32_16x16x32_bf16 v[172:175], v[52:55], v[152:155], v[172:175]\n"
    "s_waitcnt lgkmcnt(8)\n"
    "ds_read_u16_d16_hi v204, v204\n"
    "v_or_b32_e32 v191, v191, v207\n"
    "v_lshlrev_b32_sdwa v207, 1, v183 dst_sel:DWORD dst_unused:UNUSED_PAD src0_sel:DWORD src1_sel:WORD_1\n"
    "v_fma_mix_f32 v232, v184, v232, 0 op_sel:[1,1,0] op_sel_hi:[1,1,0]\n"
    "v_mfma_f32_16x16x32_bf16 v[176:179], a[112:115], v[156:159], 0\n"
    "ds_read_u16 v197, v197\n"
    "v_cvt_pk_bf16_f32 v184, v234, v232\n"
    "v_mfma_f32_16x16x32_bf16 v[176:179], a[160:163], v[160:163], v[176:179]\n"
    "ds_read_u16_d16_hi v205, v205\n"
    "v_pk_fma_f16 v232, v188, v188, s48\n"
    "v_mfma_f32_16x16x32_bf16 v[176:179], a[72:75], v[164:167], v[176:179]\n"
    "s_waitcnt lgkmcnt(8)\n"
    "v_pk_mul_f16 v232, v188, v232\n"
    "v_or_b32_e32 v192, v192, v200\n"
    "v_mfma_f32_16x16x32_bf16 v[180:183], a[116:119], v[156:159], 0\n"
    "ds_read_u16 v198, v198\n"
    "v_exp_f16_e32 v234, v232\n"
    "s_waitcnt lgkmcnt(7)\n"
    "v_smfmac_f32_32x32x32_bf16 v[96:111], a[248:251], a[232:239], v241\n"
    "ds_read_u16_d16_hi v206, v206\n"
    "v_exp_f16_sdwa v234, v232 dst_sel:WORD_1 dst_unused:UNUSED_PRESERVE src0_sel:WORD_1\n"
    "v_or_b32_e64 v193, v193, v201\n"
    "v_pk_fma_f16 v234, v234, s49, s49\n"
    "v_mfma_f32_16x16x32_bf16 v[180:183], a[164:167], v[160:163], v[180:183]\n"
    "ds_read_u16 v199, v199\n"
    "v_rcp_f16_e32 v232, v234\n"
    "v_mfma_f32_16x16x32_bf16 v[180:183], v[52:55], v[164:167], v[180:183]\n"
    "v_rcp_f16_sdwa v232, v234 dst_sel:WORD_1 dst_unused:UNUSED_PRESERVE src0_sel:WORD_1\n"
    "v_mfma_f32_16x16x32_bf16 v[112:115], a[40:43], v[184:187], v[112:115]\n"
    "s_waitcnt lgkmcnt(7)\n"
    "ds_read_u16_d16_hi v207, v207\n"
    "v_fma_mix_f32 v234, v188, v232, 0 op_sel_hi:[1,1,0]\n"
    "v_mfma_f32_16x16x32_bf16 v[96:99], a[32:35], v[184:187], v[96:99]\n"
    "ds_read_b128 a[232:235], v243 offset:26688\n"
    "v_fma_mix_f32 v232, v188, v232, 0 op_sel:[1,1,0] op_sel_hi:[1,1,0]\n"
    "v_mfma_f32_16x16x32_bf16 v[100:103], a[36:39], v[184:187], v[100:103]\n"
    "ds_read_b128 a[236:239], v243 offset:27712\n"
    "v_cvt_pk_bf16_f32 v188, v234, v232\n"
    "v_mfma_f32_16x16x32_bf16 v[116:119], a[44:47], v[184:187], v[116:119]\n"
    "s_waitcnt lgkmcnt(8)\n"
    "v_lshlrev_b32_sdwa v201, 1, v171 dst_sel:DWORD dst_unused:UNUSED_PAD src0_sel:DWORD src1_sel:WORD_1\n"
    "v_or_b32_e32 v194, v194, v202\n"
    "v_mfma_f32_16x16x32_bf16 v[128:131], v[16:19], v[184:187], v[128:131]\n"
    "ds_read_u16_d16_hi v201, v201\n"
    "v_lshlrev_b32_sdwa v202, 1, v173 dst_sel:DWORD dst_unused:UNUSED_PAD src0_sel:DWORD src1_sel:WORD_1\n"
    "v_mfma_f32_16x16x32_bf16 v[132:135], v[20:23], v[184:187], v[132:135]\n"
    "ds_read_u16_d16_hi v202, v202\n"
    "v_cvt_pk_f16_f32 v184, v168, v169\n"
    "v_mfma_f32_16x16x32_bf16 v[104:107], a[32:35], v[188:191], v[104:107]\n"
    "v_lshlrev_b32_sdwa v185, 1, v170 dst_sel:DWORD dst_unused:UNUSED_PAD src0_sel:DWORD src1_sel:WORD_1\n"
    "v_lshlrev_b32_sdwa v186, 1, v172 dst_sel:DWORD dst_unused:UNUSED_PAD src0_sel:DWORD src1_sel:WORD_1\n"
    "v_mfma_f32_16x16x32_bf16 v[108:111], a[36:39], v[188:191], v[108:111]\n"
    "s_waitcnt lgkmcnt(8)\n"
    "ds_read_u16 v185, v185\n"
    "v_lshlrev_b32_sdwa v187, 1, v174 dst_sel:DWORD dst_unused:UNUSED_PAD src0_sel:DWORD src1_sel:WORD_1\n"
    "v_mfma_f32_16x16x32_bf16 v[120:123], a[40:43], v[188:191], v[120:123]\n"
    "v_or_b32_e32 v195, v195, v203\n"
    "v_lshlrev_b32_sdwa v203, 1, v175 dst_sel:DWORD dst_unused:UNUSED_PAD src0_sel:DWORD src1_sel:WORD_1\n"
    "v_mfma_f32_16x16x32_bf16 v[124:127], a[44:47], v[188:191], v[124:127]\n"
    "ds_read_u16 v186, v186\n"
    "v_or_b32_e32 v196, v196, v204\n"
    "v_mfma_f32_16x16x32_bf16 v[136:139], v[16:19], v[188:191], v[136:139]\n"
    "s_waitcnt lgkmcnt(8)\n"
    "ds_read_u16 v187, v187\n"
    "v_or_b32_e64 v197, v197, v205\n"
    "v_mfma_f32_16x16x32_bf16 v[140:143], v[20:23], v[188:191], v[140:143]\n"
    "v_cvt_pk_f16_f32 v188, v176, v177\n"
    "v_lshlrev_b32_sdwa v189, 1, v178 dst_sel:DWORD dst_unused:UNUSED_PAD src0_sel:DWORD src1_sel:WORD_1\n"
    "v_mfma_f32_16x16x32_bf16 v[168:171], a[120:123], v[144:147], 0\n"
    "ds_read_u16_d16_hi v203, v203\n"
    "v_lshlrev_b32_sdwa v205, 1, v179 dst_sel:DWORD dst_unused:UNUSED_PAD src0_sel:DWORD src1_sel:WORD_1\n"
    "v_mfma_f32_16x16x32_bf16 v[168:171], a[168:171], v[148:151], v[168:171]\n"
    "v_lshlrev_b32_sdwa v190, 1, v180 dst_sel:DWORD dst_unused:UNUSED_PAD src0_sel:DWORD src1_sel:WORD_1\n"
    "v_lshlrev_b32_sdwa v191, 1, v182 dst_sel:DWORD dst_unused:UNUSED_PAD src0_sel:DWORD src1_sel:WORD_1\n"
    "v_mfma_f32_16x16x32_bf16 v[168:171], a[76:79], v[152:155], v[168:171]\n"
    "s_waitcnt lgkmcnt(6)\n"
    "ds_read_u16 v189, v189\n"
    "v_or_b32_e32 v198, v198, v206\n"
    "v_smfmac_f32_32x32x32_bf16 v[112:127], a[248:251], a[232:239], v241\n"
    "ds_read_b128 a[232:235], v243 offset:28736\n"
    "v_lshlrev_b32_sdwa v206, 1, v181 dst_sel:DWORD dst_unused:UNUSED_PAD src0_sel:DWORD src1_sel:WORD_1\n"
    "v_or_b32_e64 v199, v199, v207\n"
    "v_lshlrev_b32_sdwa v207, 1, v183 dst_sel:DWORD dst_unused:UNUSED_PAD src0_sel:DWORD src1_sel:WORD_1\n"
    "v_pk_fma_f16 v232, v184, v184, s48\n"
    "v_mfma_f32_16x16x32_bf16 v[172:175], a[124:127], v[144:147], 0\n"
    "ds_read_u16_d16_hi v205, v205\n"
    "v_pk_mul_f16 v232, v184, v232\n"
    "v_mfma_f32_16x16x32_bf16 v[172:175], a[172:175], v[148:151], v[172:175]\n"
    "ds_read_b128 a[236:239], v243 offset:29760\n"
    "v_exp_f16_e64 v234, v232\n"
    "v_mfma_f32_16x16x32_bf16 v[172:175], v[60:63], v[152:155], v[172:175]\n"
    "ds_read_u16 v190, v190\n"
    "v_exp_f16_sdwa v234, v232 dst_sel:WORD_1 dst_unused:UNUSED_PRESERVE src0_sel:WORD_1\n"
    "v_mfma_f32_16x16x32_bf16 v[176:179], a[120:123], v[156:159], 0\n"
    "s_waitcnt lgkmcnt(8)\n"
    "v_or_b32_e32 v185, v185, v201\n"
    "v_pk_fma_f16 v234, v234, s49, s49\n"
    "v_mfma_f32_16x16x32_bf16 v[176:179], a[168:171], v[160:163], v[176:179]\n"
    "ds_read_u16_d16_hi v206, v206\n"
    "v_lshlrev_b32_sdwa v201, 1, v171 dst_sel:DWORD dst_unused:UNUSED_PAD src0_sel:DWORD src1_sel:WORD_1\n"
    "v_mfma_f32_16x16x32_bf16 v[176:179], a[76:79], v[164:167], v[176:179]\n"
    "s_waitcnt lgkmcnt(8)\n"
    "ds_read_u16 v191, v191\n"
    "v_or_b32_e32 v186, v186, v202\n"
    "v_mfma_f32_16x16x32_bf16 v[180:183], a[124:127], v[156:159], 0\n"
    "v_lshlrev_b32_sdwa v202, 1, v173 dst_sel:DWORD dst_unused:UNUSED_PAD src0_sel:DWORD src1_sel:WORD_1\n"
    "v_rcp_f16_e64 v232, v234\n"
    "v_mfma_f32_16x16x32_bf16 v[180:183], a[172:175], v[160:163], v[180:183]\n"
    "ds_read_u16_d16_hi v207, v207\n"
    "v_rcp_f16_sdwa v232, v234 dst_sel:WORD_1 dst_unused:UNUSED_PRESERVE src0_sel:WORD_1\n"
    "v_mfma_f32_16x16x32_bf16 v[180:183], v[60:63], v[164:167], v[180:183]\n"
    "s_waitcnt lgkmcnt(8)\n"
    "ds_read_u16_d16_hi v201, v201\n"
    "v_or_b32_e32 v187, v187, v203\n"
    "v_mfma_f32_16x16x32_bf16 v[96:99], a[176:179], v[192:195], v[96:99]\n"
    "v_lshlrev_b32_sdwa v203, 1, v175 dst_sel:DWORD dst_unused:UNUSED_PAD src0_sel:DWORD src1_sel:WORD_1\n"
    "v_fma_mix_f32 v234, v184, v232, 0 op_sel_hi:[1,1,0]\n"
    "v_mfma_f32_16x16x32_bf16 v[100:103], a[180:183], v[192:195], v[100:103]\n"
    "ds_read_u16_d16_hi v202, v202\n"
    "v_fma_mix_f32 v232, v184, v232, 0 op_sel:[1,1,0] op_sel_hi:[1,1,0]\n"
    "v_mfma_f32_16x16x32_bf16 v[112:115], a[184:187], v[192:195], v[112:115]\n"
    "v_cvt_pk_bf16_f32 v184, v234, v232\n"
    "v_pk_fma_f16 v232, v188, v188, s48\n"
    "v_mfma_f32_16x16x32_bf16 v[116:119], a[188:191], v[192:195], v[116:119]\n"
    "ds_read_u16_d16_hi v203, v203\n"
    "s_waitcnt lgkmcnt(7)\n"
    "v_or_b32_e32 v189, v189, v205\n"
    "v_mfma_f32_16x16x32_bf16 v[128:131], v[24:27], v[192:195], v[128:131]\n"
    "v_lshlrev_b32_sdwa v205, 1, v179 dst_sel:DWORD dst_unused:UNUSED_PAD src0_sel:DWORD src1_sel:WORD_1\n"
    "v_pk_mul_f16 v232, v188, v232\n"
    "v_mfma_f32_16x16x32_bf16 v[132:135], v[28:31], v[192:195], v[132:135]\n"
    "ds_read_u16_d16_hi v205, v205\n"
    "v_cvt_pk_f16_f32 v192, v168, v169\n"
    "v_mfma_f32_16x16x32_bf16 v[104:107], a[176:179], v[196:199], v[104:107]\n"
    "v_lshlrev_b32_sdwa v193, 1, v170 dst_sel:DWORD dst_unused:UNUSED_PAD src0_sel:DWORD src1_sel:WORD_1\n"
    "v_lshlrev_b32_sdwa v194, 1, v172 dst_sel:DWORD dst_unused:UNUSED_PAD src0_sel:DWORD src1_sel:WORD_1\n"
    "v_mfma_f32_16x16x32_bf16 v[108:111], a[180:183], v[196:199], v[108:111]\n"
    "ds_read_u16 v193, v193\n"
    "v_lshlrev_b32_sdwa v195, 1, v174 dst_sel:DWORD dst_unused:UNUSED_PAD src0_sel:DWORD src1_sel:WORD_1\n"
    "v_mfma_f32_16x16x32_bf16 v[120:123], a[184:187], v[196:199], v[120:123]\n"
    "s_waitcnt lgkmcnt(7)\n"
    "ds_read_u16 v194, v194\n"
    "v_or_b32_e32 v190, v190, v206\n"
    "v_mfma_f32_16x16x32_bf16 v[124:127], a[188:191], v[196:199], v[124:127]\n"
    "v_lshlrev_b32_sdwa v206, 1, v181 dst_sel:DWORD dst_unused:UNUSED_PAD src0_sel:DWORD src1_sel:WORD_1\n"
    "v_exp_f16_e32 v234, v232\n"
    "v_mfma_f32_16x16x32_bf16 v[136:139], v[24:27], v[196:199], v[136:139]\n"
    "ds_read_u16 v195, v195\n"
    "v_exp_f16_sdwa v234, v232 dst_sel:WORD_1 dst_unused:UNUSED_PRESERVE src0_sel:WORD_1\n"
    "v_mfma_f32_16x16x32_bf16 v[140:143], v[28:31], v[196:199], v[140:143]\n"
    "s_waitcnt lgkmcnt(7)\n"
    "ds_read_u16_d16_hi v206, v206\n"
    "v_cvt_pk_f16_f32 v196, v176, v177\n"
    "v_mfma_f32_16x16x32_bf16 v[96:99], a[192:195], v[184:187], v[96:99]\n"
    "v_lshlrev_b32_sdwa v197, 1, v178 dst_sel:DWORD dst_unused:UNUSED_PAD src0_sel:DWORD src1_sel:WORD_1\n"
    "v_lshlrev_b32_sdwa v198, 1, v180 dst_sel:DWORD dst_unused:UNUSED_PAD src0_sel:DWORD src1_sel:WORD_1\n"
    "v_smfmac_f32_32x32x32_bf16 v[128:143], a[248:251], a[232:239], v241\n"
    "ds_read_u16 v197, v197\n"
    "v_lshlrev_b32_sdwa v199, 1, v182 dst_sel:DWORD dst_unused:UNUSED_PAD src0_sel:DWORD src1_sel:WORD_1\n"
    "v_or_b32_e64 v191, v191, v207\n"
    "v_lshlrev_b32_sdwa v207, 1, v183 dst_sel:DWORD dst_unused:UNUSED_PAD src0_sel:DWORD src1_sel:WORD_1\n"
    "v_pk_fma_f16 v234, v234, s49, s49\n"
    "v_mfma_f32_16x16x32_bf16 v[100:103], a[196:199], v[184:187], v[100:103]\n"
    "ds_read_u16 v198, v198\n"
    "v_rcp_f16_e64 v232, v234\n"
    "v_mfma_f32_16x16x32_bf16 v[112:115], a[200:203], v[184:187], v[112:115]\n"
    "ds_read_u16 v199, v199\n"
    "v_rcp_f16_sdwa v232, v234 dst_sel:WORD_1 dst_unused:UNUSED_PRESERVE src0_sel:WORD_1\n"
    "v_mfma_f32_16x16x32_bf16 v[116:119], a[204:207], v[184:187], v[116:119]\n"
    "ds_read_u16_d16_hi v207, v207\n"
    "v_fma_mix_f32 v234, v188, v232, 0 op_sel_hi:[1,1,0]\n"
    "v_mfma_f32_16x16x32_bf16 v[128:131], v[32:35], v[184:187], v[128:131]\n"
    "v_fma_mix_f32 v232, v188, v232, 0 op_sel:[1,1,0] op_sel_hi:[1,1,0]\n"
    "v_cvt_pk_bf16_f32 v188, v234, v232\n"
    "v_mfma_f32_16x16x32_bf16 v[132:135], v[36:39], v[184:187], v[132:135]\n"
    "s_waitcnt lgkmcnt(7)\n"
    "v_or_b32_e32 v193, v193, v201\n"
    "v_pk_fma_f16 v232, v192, v192, s48\n"
    "v_mfma_f32_16x16x32_bf16 v[104:107], a[192:195], v[188:191], v[104:107]\n"
    "v_pk_mul_f16 v232, v192, v232\n"
    "v_exp_f16_e64 v234, v232\n"
    "v_mfma_f32_16x16x32_bf16 v[108:111], a[196:199], v[188:191], v[108:111]\n"
    "s_waitcnt lgkmcnt(6)\n"
    "v_or_b32_e32 v194, v194, v202\n"
    "v_exp_f16_sdwa v234, v232 dst_sel:WORD_1 dst_unused:UNUSED_PRESERVE src0_sel:WORD_1\n"
    "v_mfma_f32_16x16x32_bf16 v[120:123], a[200:203], v[188:191], v[120:123]\n"
    "s_waitcnt lgkmcnt(5)\n"
    "v_or_b32_e32 v195, v195, v203\n"
    "v_pk_fma_f16 v234, v234, s49, s49\n"
    "v_mfma_f32_16x16x32_bf16 v[124:127], a[204:207], v[188:191], v[124:127]\n"
    "v_rcp_f16_e64 v232, v234\n"
    "v_mfma_f32_16x16x32_bf16 v[136:139], v[32:35], v[188:191], v[136:139]\n"
    "v_rcp_f16_sdwa v232, v234 dst_sel:WORD_1 dst_unused:UNUSED_PRESERVE src0_sel:WORD_1\n"
    "v_mfma_f32_16x16x32_bf16 v[140:143], v[36:39], v[188:191], v[140:143]\n"
    "v_fma_mix_f32 v234, v192, v232, 0 op_sel_hi:[1,1,0]\n"
    "s_waitcnt lgkmcnt(3)\n"
    "v_or_b32_e32 v197, v197, v205\n"
    "v_fma_mix_f32 v232, v192, v232, 0 op_sel:[1,1,0] op_sel_hi:[1,1,0]\n"
    "v_cvt_pk_bf16_f32 v192, v234, v232\n"
    "v_pk_fma_f16 v232, v196, v196, s48\n"
    "v_pk_mul_f16 v232, v196, v232\n"
    "v_exp_f16_e32 v234, v232\n"
    "s_waitcnt lgkmcnt(2)\n"
    "v_or_b32_e64 v198, v198, v206\n"
    "v_exp_f16_sdwa v234, v232 dst_sel:WORD_1 dst_unused:UNUSED_PRESERVE src0_sel:WORD_1\n"
    "v_mfma_f32_16x16x32_bf16 v[96:99], a[208:211], v[192:195], v[96:99]\n"
    "v_pk_fma_f16 v234, v234, s49, s49\n"
    "v_rcp_f16_e64 v232, v234\n"
    "v_mfma_f32_16x16x32_bf16 v[100:103], a[212:215], v[192:195], v[100:103]\n"
    "v_rcp_f16_sdwa v232, v234 dst_sel:WORD_1 dst_unused:UNUSED_PRESERVE src0_sel:WORD_1\n"
    "v_mfma_f32_16x16x32_bf16 v[112:115], a[216:219], v[192:195], v[112:115]\n"
    "s_waitcnt lgkmcnt(0)\n"
    "v_or_b32_e32 v199, v199, v207\n"
    "v_fma_mix_f32 v234, v196, v232, 0 op_sel_hi:[1,1,0]\n"
    "v_mfma_f32_16x16x32_bf16 v[116:119], a[220:223], v[192:195], v[116:119]\n"
    "v_fma_mix_f32 v232, v196, v232, 0 op_sel:[1,1,0] op_sel_hi:[1,1,0]\n"
    "v_cvt_pk_bf16_f32 v96, v96, v97\n"
    "v_mfma_f32_16x16x32_bf16 v[128:131], v[40:43], v[192:195], v[128:131]\n"
    "v_cvt_pk_bf16_f32 v196, v234, v232\n"
    "v_cvt_pk_bf16_f32 v97, v98, v99\n"
    "v_mfma_f32_16x16x32_bf16 v[132:135], v[44:47], v[192:195], v[132:135]\n"
    "v_cvt_pk_bf16_f32 v98, v100, v101\n"
    "v_cvt_pk_bf16_f32 v99, v102, v103\n"
    "v_mfma_f32_16x16x32_bf16 v[104:107], a[208:211], v[196:199], v[104:107]\n"
    "ds_write_b128 v243, v[96:99] offset:24640\n"
    "v_mfma_f32_16x16x32_bf16 v[108:111], a[212:215], v[196:199], v[108:111]\n"
    "v_cvt_pk_bf16_f32 v112, v112, v113\n"
    "v_cvt_pk_bf16_f32 v113, v114, v115\n"
    "v_mfma_f32_16x16x32_bf16 v[120:123], a[216:219], v[196:199], v[120:123]\n"
    "v_cvt_pk_bf16_f32 v114, v116, v117\n"
    "v_cvt_pk_bf16_f32 v115, v118, v119\n"
    "v_mfma_f32_16x16x32_bf16 v[124:127], a[220:223], v[196:199], v[124:127]\n"
    "ds_read_b128 a[224:227], v249 offset:0\n"
    "ds_write_b128 v243, v[112:115] offset:26688\n"
    "v_mfma_f32_16x16x32_bf16 v[136:139], v[40:43], v[196:199], v[136:139]\n"
    "v_cvt_pk_bf16_f32 v128, v128, v129\n"
    "v_cvt_pk_bf16_f32 v129, v130, v131\n"
    "v_mfma_f32_16x16x32_bf16 v[140:143], v[44:47], v[196:199], v[140:143]\n"
    "v_cvt_pk_bf16_f32 v130, v132, v133\n"
    "v_cvt_pk_bf16_f32 v131, v134, v135\n"
    "v_cvt_pk_bf16_f32 v104, v104, v105\n"
    "ds_read_b128 a[228:231], v249 offset:2048\n"
    "ds_write_b128 v243, v[128:131] offset:28736\n"
    "v_cvt_pk_bf16_f32 v105, v106, v107\n"
    "v_cvt_pk_bf16_f32 v106, v108, v109\n"
    "v_cvt_pk_bf16_f32 v107, v110, v111\n"
    "v_cvt_pk_bf16_f32 v120, v120, v121\n"
    "v_cvt_pk_bf16_f32 v121, v122, v123\n"
    "v_cvt_pk_bf16_f32 v122, v124, v125\n"
    "v_cvt_pk_bf16_f32 v123, v126, v127\n"
    "v_cvt_pk_bf16_f32 v136, v136, v137\n"
    "ds_read_b128 a[232:235], v249 offset:4096\n"
    "s_nop 0\n"
    "s_waitcnt lgkmcnt(4)\n"
    "buffer_store_dwordx4 a[224:227], v255, s[44:47], s58 offen offset:0 sc1\n"
    "ds_write_b128 v243, v[104:107] offset:25664\n"
    "v_cvt_pk_bf16_f32 v137, v138, v139\n"
    "v_cvt_pk_bf16_f32 v138, v140, v141\n"
    "v_cvt_pk_bf16_f32 v139, v142, v143\n"
    "ds_read_b128 a[236:239], v249 offset:1024\n"
    "ds_write_b128 v243, v[120:123] offset:27712\n"
    "ds_read_b128 a[252:255], v249 offset:3072\n"
    "ds_write_b128 v243, v[136:139] offset:29760\n"
    "s_waitcnt lgkmcnt(4)\n"
    "buffer_store_dwordx4 a[228:231], v255, s[44:47], s58 offen offset:64 sc1\n"
    "ds_read_b128 a[240:243], v249 offset:5120\n"
    "buffer_store_dwordx4 a[232:235], v255, s[44:47], s58 offen offset:128 sc1\n"
    "s_waitcnt lgkmcnt(3)\n"
    "buffer_store_dwordx4 a[236:239], v255, s[44:47], s89 offen offset:0 sc1\n"
    "s_waitcnt lgkmcnt(2)\n"
    "buffer_store_dwordx4 a[252:255], v255, s[44:47], s89 offen offset:64 sc1\n"
    "s_waitcnt lgkmcnt(0)\n"
    "buffer_store_dwordx4 a[240:243], v255, s[44:47], s89 offen offset:128 sc1\n"
    "97:\n"
    "98:\n"
    "s_waitcnt vmcnt(0) lgkmcnt(0)\n"
    ".Lreplk_end:\n"
    : "+{s66}"(flg), "+{v208}"(tid), "+{s65}"(blk), "+{s[98:99]}"(ka)
    : "{s[96:97]}"(CTAB)
    : "memory", "scc", "vcc", "m0", "v0", "v1", "v2", "v3", "v4", "v5", "v6", "v7", "v8", "v9", "v10", "v11", "v12", "v13", "v14", "v15", "v16", "v17", "v18", "v19", "v20", "v21", "v22", "v23", "v24", "v25", "v26", "v27", "v28", "v29", "v30", "v31", "v32", "v33", "v34", "v35", "v36", "v37", "v38", "v39", "v40", "v41", "v42", "v43", "v44", "v45", "v46", "v47", "v48", "v49", "v50", "v51", "v52", "v53", "v54", "v55", "v56", "v57", "v58", "v59", "v60", "v61", "v62", "v63", "v64", "v65", "v66", "v67", "v68", "v69", "v70", "v71", "v72", "v73", "v74", "v75", "v76", "v77", "v78", "v79", "v80", "v81", "v82", "v83", "v84", "v85", "v86", "v87", "v88", "v89", "v90", "v91", "v92", "v93", "v94", "v95", "v96", "v97", "v98", "v99", "v100", "v101", "v102", "v103", "v104", "v105", "v106", "v107", "v108", "v109", "v110", "v111", "v112", "v113", "v114", "v115", "v116", "v117", "v118", "v119", "v120", "v121", "v122", "v123", "v124", "v125", "v126", "v127", "v128", "v129", "v130", "v131", "v132", "v133", "v134", "v135", "v136", "v137", "v138", "v139", "v140", "v141", "v142", "v143", "v144", "v145", "v146", "v147", "v148", "v149", "v150", "v151", "v152", "v153", "v154", "v155", "v156", "v157", "v158", "v159", "v160", "v161", "v162", "v163", "v164", "v165", "v166", "v167", "v168", "v169", "v170", "v171", "v172", "v173", "v174", "v175", "v176", "v177", "v178", "v179", "v180", "v181", "v182", "v183", "v184", "v185", "v186", "v187", "v188", "v189", "v190", "v191", "v192", "v193", "v194", "v195", "v196", "v197", "v198", "v199", "v200", "v201", "v202", "v203", "v204", "v205", "v206", "v207", "v209", "v210", "v211", "v212", "v213", "v214", "v215", "v216", "v217", "v218", "v219", "v220", "v221", "v222", "v223", "v224", "v225", "v226", "v227", "v228", "v229", "v230", "v231", "v232", "v233", "v234", "v235", "v236", "v237", "v238", "v239", "v240", "v241", "v242", "v243", "v244", "v245", "v246", "v247", "v248", "v249", "v250", "v251", "v252", "v253", "v254", "v255", "a0", "a1", "a2", "a3", "a4", "a5", "a6", "a7", "a8", "a9", "a10", "a11", "a12", "a13", "a14", "a15", "a16", "a17", "a18", "a19", "a20", "a21", "a22", "a23", "a24", "a25", "a26", "a27", "a28", "a29", "a30", "a31", "a32", "a33", "a34", "a35", "a36", "a37", "a38", "a39", "a40", "a41", "a42", "a43", "a44", "a45", "a46", "a47", "a48", "a49", "a50", "a51", "a52", "a53", "a54", "a55", "a56", "a57", "a58", "a59", "a60", "a61", "a62", "a63", "a64", "a65", "a66", "a67", "a68", "a69", "a70", "a71", "a72", "a73", "a74", "a75", "a76", "a77", "a78", "a79", "a80", "a81", "a82", "a83", "a84", "a85", "a86", "a87", "a88", "a89", "a90", "a91", "a92", "a93", "a94", "a95", "a96", "a97", "a98", "a99", "a100", "a101", "a102", "a103", "a104", "a105", "a106", "a107", "a108", "a109", "a110", "a111", "a112", "a113", "a114", "a115", "a116", "a117", "a118", "a119", "a120", "a121", "a122", "a123", "a124", "a125", "a126", "a127", "a128", "a129", "a130", "a131", "a132", "a133", "a134", "a135", "a136", "a137", "a138", "a139", "a140", "a141", "a142", "a143", "a144", "a145", "a146", "a147", "a148", "a149", "a150", "a151", "a152", "a153", "a154", "a155", "a156", "a157", "a158", "a159", "a160", "a161", "a162", "a163", "a164", "a165", "a166", "a167", "a168", "a169", "a170", "a171", "a172", "a173", "a174", "a175", "a176", "a177", "a178", "a179", "a180", "a181", "a182", "a183", "a184", "a185", "a186", "a187", "a188", "a189", "a190", "a191", "a192", "a193", "a194", "a195", "a196", "a197", "a198", "a199", "a200", "a201", "a202", "a203", "a204", "a205", "a206", "a207", "a208", "a209", "a210", "a211", "a212", "a213", "a214", "a215", "a216", "a217", "a218", "a219", "a220", "a221", "a222", "a223", "a224", "a225", "a226", "a227", "a228", "a229", "a230", "a231", "a232", "a233", "a234", "a235", "a236", "a237", "a238", "a239", "a240", "a241", "a242", "a243", "a244", "a245", "a246", "a247", "a248", "a249", "a250", "a251", "a252", "a253", "a254", "a255", "s36", "s37", "s38", "s39", "s40", "s41", "s42", "s43", "s44", "s45", "s46", "s47", "s48", "s49", "s50", "s51", "s52", "s53", "s54", "s55", "s56", "s57", "s58", "s59", "s60", "s61", "s62", "s63", "s64", "s67", "s68", "s69", "s70", "s71", "s72", "s73", "s74", "s75", "s76", "s77", "s78", "s79", "s80", "s81", "s82", "s83", "s84", "s85", "s86", "s87", "s88", "s89", "s90", "s91", "s92", "s93", "s94", "s95");
  if (flg != 2u) {
    const int l2 = __builtin_amdgcn_mbcnt_hi(~0u, __builtin_amdgcn_mbcnt_lo(~0u, 0u));
    slow_path(x, dwl, dws, w1, w2, out, (float*)smem, w * 64 + l2, blockIdx.x >> 3, (blockIdx.x & 7) * 4);
  }
}

extern "C" void kernel_launch(void* const* d_in, const int* in_sizes, int n_in, void* d_out, int out_size, void* d_ws,
                              size_t ws_size, hipStream_t stream) {
  replk_kernel<<<256, 256, 0, stream>>>((const u16*)d_in[0], (const u16*)d_in[1], (const u16*)d_in[2], (const u16*)d_in[3],
                                        (const u16*)d_in[4], (u16*)d_out
                                        );
}
```

```cpp
#include <hip/hip_runtime.h>
#include <hip/hip_bf16.h>

typedef unsigned u32;
typedef unsigned short u16;
typedef unsigned long long u64;
typedef __bf16 bf16x2 __attribute__((ext_vector_type(2)));
typedef float f32x2 __attribute__((ext_vector_type(2)));
#define DI __device__ __forceinline__

constexpr int IMG = 512;
constexpr int CH = 96;
constexpr int HID = 384;
constexpr int PXB = 192;
constexpr int ROWB = 98304;
constexpr int TOEP_CS = 336;
constexpr int XT_RS = 320;
constexpr int XT_PP = 1360;
constexpr int XT_CG = 6752;
constexpr int GS = 6160;
constexpr int L_LUT = 0;
constexpr int L_TOEP = 8208;
constexpr int L_W = 40960;
constexpr int L_LUTN = 65536;
constexpr int L_XT = 81920;
constexpr int L_H = 108928;
constexpr int L_EX = 133568;
constexpr int XT_Q = 27024;
constexpr int L_MISC = 162992;
constexpr int L_TOTAL = 163056;
constexpr int LUTK = 106;
constexpr float GC1 = -2.302208198144325f, GC3 = -0.1029432395800235f;
__device__ const u32 CTAB[3 * 1024] = {
671109120,671098880,134490112,673187840,671372952,671229956,145500164,673318996,671636784,671361032,156510216,673450152,671900616,671492108,167520268,673581308,672164452,684206128,178530352,686295376,672428284,684337204,189540404,686426532,672692116,684468280,200550456,686557688,672955948,684599356,211560508,686688844,673219784,697313376,222570592,699402912,673483616,697444452,233580644,699534068,673747448,697575528,244590696,699665224,674011280,697706604,255600748,699796380,674254849,710420624,266610832,712510448,674518681,710551700,277620884,712641604,674782513,710682776,288630936,712772760,675046345,710813852,299640988,712903916,
684223077,723527872,310651072,721423680,684486909,723658948,321661124,721554836,683702165,723790024,332671176,721685992,683965997,723921100,343681228,721817148,685278409,736635120,354691312,734531216,685542241,736766196,365701364,734662372,684757497,736897272,376711416,734793528,685021329,737028348,387721468,734924684,686313474,749742368,398731552,747638752,686577306,749873444,409741604,747769908,685792562,750004520,420751656,747901064,686056394,750135596,431761708,748032220,687368806,762849616,442771792,760746288,687632638,762980692,453781844,760877444,686847894,763111768,464791896,761008600,687111726,763242844,475801948,761139756,
697337034,771762560,486812032,773853824,697600866,771893636,497822084,773984980,697864698,772024712,508832136,774116136,698128530,772155788,519842188,774247292,696274947,784869808,530852272,786961360,696538779,785000884,541862324,787092516,696802611,785131960,552872376,787223672,697066443,785263036,563882428,787354828,699427431,797977056,574892512,800068896,699691263,798108132,585902564,800200052,699955095,798239208,596912616,800331208,700218927,798370284,607922668,800462364,698385611,811084304,618932752,813176432,698649443,811215380,629942804,813307588,698913275,811346456,640952856,813438744,699177107,811477532,651962908,813569900,
710430724,824191552,135539264,822089664,710694556,824322628,146549316,822220820,709909812,824453704,157559368,822351976,710173644,824584780,168569420,822483132,709388904,837298800,179579504,835197200,709652736,837429876,190589556,835328356,708867992,837560952,201599608,835459512,709131824,837692028,212609660,835590668,712541388,850406048,223619744,848304736,712805220,850537124,234629796,848435892,712020476,850668200,245639848,848567048,712284308,850799276,256649900,848698204,711479301,863513296,267659984,861412272,711743133,863644372,278670036,861543428,710958389,863775448,289680088,861674584,711222221,863906524,300690140,861805740,
723544681,677394944,311704064,679484672,723808513,677526020,322714116,679615828,724072345,677657096,333724168,679746984,724336177,677788172,344734220,679878140,724600013,690502192,355744304,692592208,724863845,690633268,366754356,692723364,725127677,690764344,377764408,692854520,725391509,690895420,388774460,692985676,721440774,703609440,399784544,705699744,721704606,703740516,410794596,705830900,721968438,703871592,421804648,705962056,722232270,704002668,432814700,706093212,722496106,716716688,443824784,718807280,722759938,716847764,454834836,718938436,723023770,716978840,465844888,719069592,723287602,717109916,476854940,719200748,
736658638,725629632,487865024,731914816,736922470,725760708,498875076,732045972,736137726,725891784,509885128,732177128,736401558,726022860,520895180,732308284,737693703,738736880,531905264,745022352,737957535,738867956,542915316,745153508,737172791,738999032,553925368,745284664,737436623,739130108,564935420,745415820,734554731,751844128,575945504,758129888,734818563,751975204,586955556,758261044,734033819,752106280,597965608,758392200,734297651,752237356,608975660,758523356,735610063,764951376,619985744,771237424,735873895,765082452,630995796,771368580,735089151,765213528,642005848,771499736,735352983,765344604,653015900,771630892,
749752328,778058624,136592256,780150656,750016160,778189700,147602308,780281812,750279992,778320776,158612360,780443436,750543824,778451852,169622412,780574508,748710508,791165872,180632496,793288492,748974340,791296948,191642548,793419564,749238172,791428024,202652600,793550636,749502004,791559100,213662652,793681708,747668688,804273120,224672736,806395692,747932520,804404196,235682788,806526764,748196352,804535272,246692840,806657836,748460184,804666348,257702892,806788908,746606601,817380368,268712976,819502892,746870433,817511444,279723028,819633964,747134265,817642520,290733080,819765036,747398097,817773596,301743132,819896108,
762866285,826293312,312753216,832610092,763130117,826424388,323763268,832741164,762345373,826555464,334773320,832872236,762609205,826686540,345783372,833003308,761824465,839400560,356793456,845717292,762088297,839531636,367803508,845848364,761303553,839662712,378813560,845979436,761567385,839793788,389823612,846110508,760762378,852507808,400833696,858824492,761026210,852638884,411843748,858955564,760241466,852769960,422853800,859086636,760505298,852901036,433863852,859217708,759720558,865615056,444873936,871931692,759984390,865746132,455883988,872062764,759199646,865877208,466894040,872193836,759463478,866008284,477904092,872324908,
771785938,671098882,488908800,673226540,772049770,671229958,499918852,673357612,772313602,671361034,510928904,673488684,772577434,671492110,521938956,673619756,772821003,684206130,532949040,686333740,773084835,684337206,543959092,686464812,773348667,684468282,554969144,686595884,773612499,684599358,565979196,686726956,773876335,697313378,576989280,699440940,774140167,697444454,587999332,699572012,774403999,697575530,599009384,699703084,774667831,697706606,610019436,699834156,774931667,710420626,621029520,712548140,775195499,710551702,632039572,712679212,775459331,710682778,643049624,712810284,775723163,710813854,654059676,712941356,
784879628,723527874,137636032,721461036,785143460,723658950,148646084,721592108,784358716,723790026,159656136,721723180,784622548,723921102,170666188,721854252,785934960,736635122,181676272,734568236,786198792,736766198,192686324,734699308,785414048,736897274,203696376,734830380,785677880,737028350,214706428,734961452,786990292,749742370,225716512,747675436,787254124,749873446,236726564,747806508,786469380,750004522,247736616,747937580,786733212,750135598,258746668,748068652,788025357,762849618,269756752,760782636,788289189,762980694,280766804,760913708,787504445,763111770,291776856,761044780,787768277,763242846,302786908,761175852,
797993585,771762562,313796992,773889836,798257417,771893638,324807044,774020908,798521249,772024714,335817096,774151980,798785081,772155790,346827148,774283052,796951765,784869810,357837232,786997036,797215597,785000886,368847284,787128108,797479429,785131962,379857336,787259180,797743261,785263038,390867388,787390252,800083982,797977058,401877472,800104236,800347814,798108134,412887524,800235308,800611646,798239210,423897576,800366380,800875478,798370286,434907628,800497452,799042162,811084306,445917712,813211436,799305994,811215382,456927764,813342508,799569826,811346458,467937816,813473580,799833658,811477534,478947868,813604652,
811107542,824191554,489957952,822124332,811371374,824322630,500968004,822255404,810586630,824453706,511978056,822386476,810850462,824584782,522988108,822517548,810045455,837298802,533998192,835231532,810309287,837429878,545008244,835362604,809524543,837560954,556018296,835493676,809788375,837692030,567028348,835624748,813197939,850406050,578038432,848338732,813461771,850537126,589048484,848469804,812677027,850668202,600058536,848600876,812940859,850799278,611068588,848731948,812156119,863513298,622078672,861445932,812419951,863644374,633088724,861577004,811635207,863775450,644098776,861708076,811899039,863906526,655108828,861839148,
824201232,677394946,138689024,679517996,824465064,677526022,149699076,679649068,824728896,677657098,160709128,679780140,824992728,677788174,171719180,679911212,825256564,690502194,182729264,692625196,825520396,690633270,193739316,692756268,825784228,690764346,204749368,692887340,826048060,690895422,215759420,693018412,822117592,703609442,226769504,705732396,822381424,703740518,237779556,705863468,822645256,703871594,248789608,705994540,822909088,704002670,259799660,706125612,823152657,716716690,270809744,718839596,823416489,716847766,281819796,718970668,823680321,716978842,292829848,719101740,823944153,717109918,303839900,719232812,
837315189,725629634,314849984,731946796,837579021,725760710,325860036,732077868,836794277,725891786,336870088,732208940,837058109,726022862,347880140,732340012,838370521,738736882,358890224,745053996,838634353,738867958,369900276,745185068,837849609,738999034,380910328,745316140,838113441,739130110,391920380,745447212,835211282,751844130,402930464,758161196,835475114,751975206,413940516,758292268,834690370,752106282,424950568,758423340,834954202,752237358,435960620,758554412,836266614,764951378,446970704,771268396,836530446,765082454,457980756,771399468,835745702,765213530,468990808,771530540,836009534,765344606,480000860,771661612,
850429146,778058626,491010944,780181292,850692978,778189702,502020996,780312364,850956810,778320778,513031048,780443436,851220642,778451854,524041100,780574508,849367059,791165874,535051184,793288492,849630891,791296950,546061236,793419564,849894723,791428026,557071288,793550636,850158555,791559102,568081340,793681708,848325239,804273122,579091424,806395692,848589071,804404198,590101476,806526764,848852903,804535274,601111528,806657836,849116735,804666350,612121580,806788908,847283419,817380370,623131664,819502892,847547251,817511446,634141716,819633964,847811083,817642522,645151768,819765036,848074915,817773598,656161820,819896108,
863541411,826293314,656161856,832610092,863803555,826424390,656161860,832741164,863017123,826555466,656161864,832872236,863279267,826686542,656161868,833003308,862492835,839400562,656161904,845717292,862754979,839531638,656161908,845848364,861968547,839662714,656161912,845979436,862230691,839793790,656161916,846110508,861444259,852507810,656161952,858824492,861706403,852638886,656161956,858955564,860919971,852769962,656161960,859086636,861182115,852901038,656161964,859217708,860395683,865615058,656162000,871931692,860657827,865746134,656162004,872062764,859871395,865877210,656162008,872193836,860133539,866008286,656162012,872324908,
2152998912u,1885629024,1342187520,1342212212,2152998916u,1885891172,1452812292,1364494564,2151688200u,1886153320,1563437064,1386776836,2218797068u,1886415468,1674061836,1409059156,2164598800u,1886677616,1342253072,1342474360,2164598804u,1886939764,1452877844,1364756712,2164271128u,1887201912,1563502616,1387038984,2231379996u,1887464060,1674127388,1409321304,1083254816,1887726208,1342318624,1342998652,1083254820,1887988356,1452943396,1365281004,1082140712,1888250504,1563568168,1387563276,1149249580,1888512652,1674192940,1409845596,1108617264,1888774800,1342384176,1342736512,1108617268,1889036948,1453008948,1365018864,1107306552,1889299096,1563633720,1387301136,1174415420,1889561244,1674258492,1409583456,
269756480,1892969168,1342449728,1342474372,269756484,1893231316,1453074500,1364756692,268445768,1893493464,1563699272,1387038964,335554636,1893755612,1674324044,1409321316,268773456,1891920576,1342515280,1342736520,268773460,1892182724,1453140052,1365018840,268445784,1892444872,1563764824,1387301112,335554652,1892707020,1674389596,1409583464,1343301728,1890871984,1342580832,1343260812,1343301732,1891134132,1453205604,1365543132,1342187624,1891396280,1563830376,1387825404,1409296492,1891658428,1674455148,1410107756,1343498352,1889823392,1342646384,1342998672,1343498356,1890085540,1453271156,1365280992,1342187640,1890347688,1563895928,1387563264,1409296508,1890609836,1674520700,1409845616,
538192000,1895066352,1342711936,1364494484,538192004,1895328500,1453336708,1386776772,536881288,1895590648,1563961480,1409059108,603990156,1895852796,1674586252,1431341364,537208976,1894017760,1342777488,1364756632,537208980,1894279908,1453402260,1387038920,536881304,1894542056,1564027032,1409321256,603990172,1894804204,1674651804,1431603512,1611737248,1897163536,1342843040,1365280924,1611737252,1897425684,1453467812,1387563212,1610623144,1897687832,1564092584,1409845548,1677732012,1897949980,1674717356,1432127804,1611933872,1896114944,1342908592,1365018784,1611933876,1896377092,1453533364,1387301072,1610623160,1896639240,1564158136,1409583408,1677732028,1896901388,1674782908,1431865664,
810821824,1900309312,1342974144,1364756644,810821828,1900571460,1453598916,1387038900,809511112,1900833608,1564223688,1409321236,876619980,1901095756,1674848460,1431603524,822421712,1901357904,1343039696,1365018792,822421716,1901620052,1453664468,1387301048,822094040,1901882200,1564289240,1409583384,889202908,1902144348,1674914012,1431865672,1888561376,1898212128,1343105248,1365543084,1888561380,1898474276,1453730020,1387825340,1887447272,1898736424,1564354792,1410107676,1954556140,1898998572,1674979564,1432389964,1913923824,1899260720,1343170800,1365280944,1913923828,1899522868,1453795572,1387563200,1912613112,1899785016,1564420344,1409845536,1979721980,1900047164,1675045116,1432127824,
2152999168u,1784705124,1347430656,1452835440,2152999172u,1784967272,1458055428,1475117792,2151688456u,1785229420,1568680200,1497400064,2218797324u,1785491568,1679304972,1519682384,2164599056u,1785753716,1347496208,1453097588,2164599060u,1786015864,1458120980,1475379940,2164271384u,1786278012,1568745752,1497662212,2231380252u,1786540160,1679370524,1519944532,1083255072,1786802308,1347561760,1453621880,1083255076,1787064456,1458186532,1475904232,1082140968,1787326604,1568811304,1498186504,1149249836,1787588752,1679436076,1520468824,1108617520,1787850900,1347627312,1453359740,1108617524,1788113048,1458252084,1475642092,1107306808,1788375196,1568876856,1497924364,1174415676,1788637344,1679501628,1520206684,
269756736,1792045268,1347692864,1453097600,269756740,1792307416,1458317636,1475379920,268446024,1792569564,1568942408,1497662192,335554892,1792831712,1679567180,1519944544,268773712,1790996676,1347758416,1453359748,268773716,1791258824,1458383188,1475642068,268446040,1791520972,1569007960,1497924340,335554908,1791783120,1679632732,1520206692,1343301984,1789948084,1347823968,1453884040,1343301988,1790210232,1458448740,1476166360,1342187880,1790472380,1569073512,1498448632,1409296748,1790734528,1679698284,1520730984,1343498608,1788899492,1347889520,1453621900,1343498612,1789161640,1458514292,1475904220,1342187896,1789423788,1569139064,1498186492,1409296764,1789685936,1679763836,1520468844,
538192256,1794142452,1347955072,1475117712,538192260,1794404600,1458579844,1497400000,536881544,1794666748,1569204616,1519682336,603990412,1794928896,1679829388,1541964592,537209232,1793093860,1348020624,1475379860,537209236,1793356008,1458645396,1497662148,536881560,1793618156,1569270168,1519944484,603990428,1793880304,1679894940,1542226740,1611737504,1796239636,1348086176,1475904152,1611737508,1796501784,1458710948,1498186440,1610623400,1796763932,1569335720,1520468776,1677732268,1797026080,1679960492,1542751032,1611934128,1795191044,1348151728,1475642012,1611934132,1795453192,1458776500,1497924300,1610623416,1795715340,1569401272,1520206636,1677732284,1795977488,1680026044,1542488892,
810822080,1799385412,1348217280,1475379872,810822084,1799647560,1458842052,1497662128,809511368,1799909708,1569466824,1519944464,876620236,1800171856,1680091596,1542226752,822421968,1800434004,1348282832,1475642020,822421972,1800696152,1458907604,1497924276,822094296,1800958300,1569532376,1520206612,889203164,1801220448,1680157148,1542488900,1888561632,1797288228,1348348384,1476166312,1888561636,1797550376,1458973156,1498448568,1887447528,1797812524,1569597928,1520730904,1954556396,1798074672,1680222700,1543013192,1913924080,1798336820,1348413936,1475904172,1913924084,1798598968,1459038708,1498186428,1912613368,1798861116,1569663480,1520468764,1979722236,1799123264,1680288252,1542751052,
2152999424u,2087482984,1352673280,1563464828,2152999428u,2087745132,1463298052,1585747180,2151688712u,2088007280,1573922824,1608029452,2218797580u,2088269428,1684547596,1630311772,2164599312u,2088531576,1352738832,1563726976,2164599316u,2088793724,1463363604,1586009328,2164271640u,2089055872,1573988376,1608291600,2231380508u,2089318020,1684613148,1630573920,1083255328,2089580168,1352804384,1564251268,1083255332,2089842316,1463429156,1586533620,1082141224,2090104464,1574053928,1608815892,1149250092,2090366612,1684678700,1631098212,1108617776,2090628760,1352869936,1563989128,1108617780,2090890908,1463494708,1586271480,1107307064,2091153056,1574119480,1608553752,1174415932,2091415204,1684744252,1630836072,
269756992,2094823128,1352935488,1563726988,269756996,2095085276,1463560260,1586009308,268446280,2095347424,1574185032,1608291580,335555148,2095609572,1684809804,1630573932,268773968,2093774536,1353001040,1563989136,268773972,2094036684,1463625812,1586271456,268446296,2094298832,1574250584,1608553728,335555164,2094560980,1684875356,1630836080,1343302240,2092725944,1353066592,1564513428,1343302244,2092988092,1463691364,1586795748,1342188136,2093250240,1574316136,1609078020,1409297004,2093512388,1684940908,1631360372,1343498864,2091677352,1353132144,1564251288,1343498868,2091939500,1463756916,1586533608,1342188152,2092201648,1574381688,1608815880,1409297020,2092463796,1685006460,1631098232,
538192512,2096920312,1353197696,1585747100,538192516,2097182460,1463822468,1608029388,536881800,2097444608,1574447240,1630311724,603990668,2097706756,1685072012,1652593980,537209488,2095871720,1353263248,1586009248,537209492,2096133868,1463888020,1608291536,536881816,2096396016,1574512792,1630573872,603990684,2096658164,1685137564,1652856128,1611737760,2099017496,1353328800,1586533540,1611737764,2099279644,1463953572,1608815828,1610623656,2099541792,1574578344,1631098164,1677732524,2099803940,1685203116,1653380420,1611934384,2097968904,1353394352,1586271400,1611934388,2098231052,1464019124,1608553688,1610623672,2098493200,1574643896,1630836024,1677732540,2098755348,1685268668,1653118280,
810822336,2102163272,1353459904,1586009260,810822340,2102425420,1464084676,1608291516,809511624,2102687568,1574709448,1630573852,876620492,2102949716,1685334220,1652856140,822422224,2103211864,1353525456,1586271408,822422228,2103474012,1464150228,1608553664,822094552,2103736160,1574775000,1630836000,889203420,2103998308,1685399772,1653118288,1888561888,2100066088,1353591008,1586795700,1888561892,2100328236,1464215780,1609077956,1887447784,2100590384,1574840552,1631360292,1954556652,2100852532,1685465324,1653642580,1913924336,2101114680,1353656560,1586533560,1913924340,2101376828,1464281332,1608815816,1912613624,2101638976,1574906104,1631098152,1979722492,2101901124,1685530876,1653380440,
2152999680u,1986559084,1357916416,1674088056,2152999684u,1986821232,1468541188,1696370408,2151688968u,1987083380,1579165960,1718652680,2218797836u,1987345528,1689790732,1740935000,2164599568u,1987607676,1357981968,1674350204,2164599572u,1987869824,1468606740,1696632556,2164271896u,1988131972,1579231512,1718914828,2231380764u,1988394120,1689856284,1741197148,1083255584,1988656268,1358047520,1674874496,1083255588,1988918416,1468672292,1697156848,1082141480,1989180564,1579297064,1719439120,1149250348,1989442712,1689921836,1741721440,1108618032,1989704860,1358113072,1674612356,1108618036,1989967008,1468737844,1696894708,1107307320,1990229156,1579362616,1719176980,1174416188,1990491304,1689987388,1741459300,
269757248,1993899228,1358178624,1674350216,269757252,1994161376,1468803396,1696632536,268446536,1994423524,1579428168,1718914808,335555404,1994685672,1690052940,1741197160,268774224,1992850636,1358244176,1674612364,268774228,1993112784,1468868948,1696894684,268446552,1993374932,1579493720,1719176956,335555420,1993637080,1690118492,1741459308,1343302496,1991802044,1358309728,1675136656,1343302500,1992064192,1468934500,1697418976,1342188392,1992326340,1579559272,1719701248,1409297260,1992588488,1690184044,1741983600,1343499120,1990753452,1358375280,1674874516,1343499124,1991015600,1469000052,1697156836,1342188408,1991277748,1579624824,1719439108,1409297276,1991539896,1690249596,1741721460,
538192768,1995996412,1358440832,1696370328,538192772,1996258560,1469065604,1718652616,536882056,1996520708,1579690376,1740934952,603990924,1996782856,1690315148,1763217208,537209744,1994947820,1358506384,1696632476,537209748,1995209968,1469131156,1718914764,536882072,1995472116,1579755928,1741197100,603990940,1995734264,1690380700,1763479356,1611738016,1998093596,1358571936,1697156768,1611738020,1998355744,1469196708,1719439056,1610623912,1998617892,1579821480,1741721392,1677732780,1998880040,1690446252,1764003648,1611934640,1997045004,1358637488,1696894628,1611934644,1997307152,1469262260,1719176916,1610623928,1997569300,1579887032,1741459252,1677732796,1997831448,1690511804,1763741508,
810822592,2001239372,1358703040,1696632488,810822596,2001501520,1469327812,1718914744,809511880,2001763668,1579952584,1741197080,876620748,2002025816,1690577356,1763479368,822422480,2002287964,1358768592,1696894636,822422484,2002550112,1469393364,1719176892,822094808,2002812260,1580018136,1741459228,889203676,2003074408,1690642908,1763741516,1888562144,1999142188,1358834144,1697418928,1888562148,1999404336,1469458916,1719701184,1887448040,1999666484,1580083688,1741983520,1954556908,1999928632,1690708460,1764265808,1913924592,2000190780,1358899696,1697156788,1913924596,2000452928,1469524468,1719439044,1912613880,2000715076,1580149240,1741721380,1979722748,2000977224,1690774012,1764003668,
136579076,1343253088,52428,12582912,135530496,1453879396,843229,12648449,134481920,1564505704,1634030,12713986,145229824,1675132012,2411724,12779523,136841224,1343318640,3198156,13369356,135792808,1453944948,3988957,13434893,134744232,1564571256,4779758,13500430,139724968,1675197564,5557452,13565967,137103372,1343384208,6343884,14155800,136054952,1454010516,7134685,14221337,135006376,1564636824,7925486,14286874,139724968,1675263132,8703180,14352411,137365520,1343449728,9489612,14942244,136316928,1454076036,10280413,15007781,135268352,1564702344,11071214,15073318,145229824,1675328652,11848908,15138855,
139724868,1365535332,117964,12582960,139724852,1476161640,908765,12648497,138676260,1586787948,1699566,12714034,137627668,1697414256,2477260,12779571,145229896,1365600884,3263692,13369404,145229880,1476227192,4054493,13434941,138938408,1586853500,4845294,13500478,137889816,1697479808,5622988,13566015,145229900,1365666452,6409420,14155848,145229884,1476292760,7200221,14221385,139200556,1586919068,7991022,14286922,138151964,1697545376,8768716,14352459,139724880,1365731972,9555148,14942292,139724864,1476358280,10345949,15007829,139462704,1586984588,11136750,15073366,138414112,1697610896,11914444,15138903,
137627668,1387817576,183500,12583008,136579076,1498443884,974301,12648545,135530496,1609070192,1765102,12714082,134481920,1719696500,2542796,12779619,137889816,1387883128,3329228,13369452,136841224,1498509436,4120029,13434989,135792808,1609135744,4910830,13500526,134744232,1719762052,5688524,13566063,138151964,1387948696,6474956,14155896,137103372,1498575004,7265757,14221433,136054952,1609201312,8056558,14286970,135006376,1719827620,8834252,14352507,138414112,1388014216,9620684,14942340,137365520,1498640524,10411485,15007877,136316928,1609266832,11202286,15073414,135268352,1719893140,11979980,15138951,
139724968,1410099820,249036,12583056,139724868,1520726128,1039837,12648593,139724852,1631352436,1830638,12714130,138676260,1741978744,2608332,12779667,145229824,1410165372,3394764,13369500,145229896,1520791680,4185565,13435037,145229880,1631417988,4976366,13500574,138938408,1742044296,5754060,13566111,145229824,1410230940,6540492,14155944,145229900,1520857248,7331293,14221481,145229884,1631483556,8122094,14287018,139200556,1742109864,8899788,14352555,139724968,1410296460,9686220,14942388,139724880,1520922768,10477021,15007925,139724864,1631549076,11267822,15073462,139462704,1742175384,12045516,15138999,
180619940,1348496080,52428,12582912,179571360,1459122388,843229,12648449,178522784,1569748696,1634030,12713986,189270688,1680375004,2411724,12779523,180882088,1348561600,3198156,13369356,179833672,1459187908,3988957,13434893,178785096,1569814216,4779758,13500430,183765832,1680440524,5557452,13565967,181144236,1348627104,6343884,14155800,180095816,1459253412,7134685,14221337,179047240,1569879720,7925486,14286874,183765832,1680506028,8703180,14352411,181406384,1348692656,9489612,14942244,180357792,1459318964,10280413,15007781,179309216,1569945272,11071214,15073318,189270688,1680571580,11848908,15138855,
183765732,1370778324,117964,12582960,183765716,1481404632,908765,12648497,182717124,1592030940,1699566,12714034,181668532,1702657248,2477260,12779571,189270760,1370843844,3263692,13369404,189270744,1481470152,4054493,13434941,182979272,1592096460,4845294,13500478,181930680,1702722768,5622988,13566015,189270764,1370909348,6409420,14155848,189270748,1481535656,7200221,14221385,183241420,1592161964,7991022,14286922,182192828,1702788272,8768716,14352459,183765744,1370974900,9555148,14942292,183765728,1481601208,10345949,15007829,183503568,1592227516,11136750,15073366,182454976,1702853824,11914444,15138903,
181668532,1393060568,183500,12583008,180619940,1503686876,974301,12648545,179571360,1614313184,1765102,12714082,178522784,1724939492,2542796,12779619,181930680,1393126088,3329228,13369452,180882088,1503752396,4120029,13434989,179833672,1614378704,4910830,13500526,178785096,1725005012,5688524,13566063,182192828,1393191592,6474956,14155896,181144236,1503817900,7265757,14221433,180095816,1614444208,8056558,14286970,179047240,1725070516,8834252,14352507,182454976,1393257144,9620684,14942340,181406384,1503883452,10411485,15007877,180357792,1614509760,11202286,15073414,179309216,1725136068,11979980,15138951,
183765832,1415342812,249036,12583056,183765732,1525969120,1039837,12648593,183765716,1636595428,1830638,12714130,182717124,1747221736,2608332,12779667,189270688,1415408332,3394764,13369500,189270760,1526034640,4185565,13435037,189270744,1636660948,4976366,13500574,182979272,1747287256,5754060,13566111,189270688,1415473836,6540492,14155944,189270764,1526100144,7331293,14221481,189270748,1636726452,8122094,14287018,183241420,1747352760,8899788,14352555,183765832,1415539388,9686220,14942388,183765744,1526165696,10477021,15007925,183765728,1636792004,11267822,15073462,183503568,1747418312,12045516,15138999,
224660804,1353738992,52428,12582912,223612224,1464365300,843229,12648449,222563648,1574991608,1634030,12713986,233311552,1685617916,2411724,12779523,224922952,1353804512,3198156,13369356,223874536,1464430820,3988957,13434893,222825960,1575057128,4779758,13500430,227806696,1685683436,5557452,13565967,225185100,1353870080,6343884,14155800,224136680,1464496388,7134685,14221337,223088104,1575122696,7925486,14286874,227806696,1685749004,8703180,14352411,225447248,1353935632,9489612,14942244,224398656,1464561940,10280413,15007781,223350080,1575188248,11071214,15073318,233311552,1685814556,11848908,15138855,
227806596,1376021236,117964,12582960,227806580,1486647544,908765,12648497,226757988,1597273852,1699566,12714034,225709396,1707900160,2477260,12779571,233311624,1376086756,3263692,13369404,233311608,1486713064,4054493,13434941,227020136,1597339372,4845294,13500478,225971544,1707965680,5622988,13566015,233311628,1376152324,6409420,14155848,233311612,1486778632,7200221,14221385,227282284,1597404940,7991022,14286922,226233692,1708031248,8768716,14352459,227806608,1376217876,9555148,14942292,227806592,1486844184,10345949,15007829,227544432,1597470492,11136750,15073366,226495840,1708096800,11914444,15138903,
225709396,1398303480,183500,12583008,224660804,1508929788,974301,12648545,223612224,1619556096,1765102,12714082,222563648,1730182404,2542796,12779619,225971544,1398369000,3329228,13369452,224922952,1508995308,4120029,13434989,223874536,1619621616,4910830,13500526,222825960,1730247924,5688524,13566063,226233692,1398434568,6474956,14155896,225185100,1509060876,7265757,14221433,224136680,1619687184,8056558,14286970,223088104,1730313492,8834252,14352507,226495840,1398500120,9620684,14942340,225447248,1509126428,10411485,15007877,224398656,1619752736,11202286,15073414,223350080,1730379044,11979980,15138951,
227806696,1420585724,249036,12583056,227806596,1531212032,1039837,12648593,227806580,1641838340,1830638,12714130,226757988,1752464648,2608332,12779667,233311552,1420651244,3394764,13369500,233311624,1531277552,4185565,13435037,233311608,1641903860,4976366,13500574,227020136,1752530168,5754060,13566111,233311552,1420716812,6540492,14155944,233311628,1531343120,7331293,14221481,233311612,1641969428,8122094,14287018,227282284,1752595736,8899788,14352555,227806696,1420782364,9686220,14942388,227806608,1531408672,10477021,15007925,227806592,1642034980,11267822,15073462,227544432,1752661288,12045516,15138999,
268701668,1358981952,52428,12582912,267653088,1469608260,843229,12648449,266604512,1580234568,1634030,12713986,277352416,1690860876,2411724,12779523,268963816,1359047504,3198156,13369356,267915400,1469673812,3988957,13434893,266866824,1580300120,4779758,13500430,271847560,1690926428,5557452,13565967,269225964,1359113008,6343884,14155800,268177544,1469739316,7134685,14221337,267128968,1580365624,7925486,14286874,271847560,1690991932,8703180,14352411,269488112,1359178528,9489612,14942244,268439520,1469804836,10280413,15007781,267390944,1580431144,11071214,15073318,277352416,1691057452,11848908,15138855,
271847460,1381264196,117964,12582960,271847444,1491890504,908765,12648497,270798852,1602516812,1699566,12714034,269750260,1713143120,2477260,12779571,277352488,1381329748,3263692,13369404,277352472,1491956056,4054493,13434941,271061000,1602582364,4845294,13500478,270012408,1713208672,5622988,13566015,277352492,1381395252,6409420,14155848,277352476,1492021560,7200221,14221385,271323148,1602647868,7991022,14286922,270274556,1713274176,8768716,14352459,271847472,1381460772,9555148,14942292,271847456,1492087080,10345949,15007829,271585296,1602713388,11136750,15073366,270536704,1713339696,11914444,15138903,
269750260,1403546440,183500,12583008,268701668,1514172748,974301,12648545,267653088,1624799056,1765102,12714082,266604512,1735425364,2542796,12779619,270012408,1403611992,3329228,13369452,268963816,1514238300,4120029,13434989,267915400,1624864608,4910830,13500526,266866824,1735490916,5688524,13566063,270274556,1403677496,6474956,14155896,269225964,1514303804,7265757,14221433,268177544,1624930112,8056558,14286970,267128968,1735556420,8834252,14352507,270536704,1403743016,9620684,14942340,269488112,1514369324,10411485,15007877,268439520,1624995632,11202286,15073414,267390944,1735621940,11979980,15138951,
271847560,1425828684,249036,12583056,271847460,1536454992,1039837,12648593,271847444,1647081300,1830638,12714130,270798852,1757707608,2608332,12779667,277352416,1425894236,3394764,13369500,277352488,1536520544,4185565,13435037,277352472,1647146852,4976366,13500574,271061000,1757773160,5754060,13566111,277352416,1425959740,6540492,14155944,277352492,1536586048,7331293,14221481,277352476,1647212356,8122094,14287018,271323148,1757838664,8899788,14352555,271847560,1426025260,9686220,14942388,271847472,1536651568,10477021,15007925,271847456,1647277876,11267822,15073462,271585296,1757904184,12045516,15138999};

DI float bf_lo(u32 v) { return __uint_as_float(v << 16); }
DI u32 pk_bf16(float a, float b) { f32x2 t = {a, b}; return __builtin_bit_cast(u32, __builtin_convertvector(t, bf16x2)); }

DI void slow_path(const u16* __restrict__ x, const u16* __restrict__ dwl, const u16* __restrict__ dws, const u16* __restrict__ w1,
                  const u16* __restrict__ w2, u16* __restrict__ out, float* sm, int t, int tx, int ty0) {
  float* o = sm;
  float* g = sm + 8 * CH;
  float* st = g + 8 * HID;
#pragma unroll 1
  for (int b = 0; b < 128; ++b) {
    const int y = ty0 * 16 + (b >> 1), x0 = tx * 16 + (b & 1) * 8;
    __syncthreads();
#pragma unroll 1
    for (int i = t; i < 8 * CH; i += 256) {
      const int p = i / CH, c = i - p * CH;
      float acc = 0.f;
#pragma unroll 1
      for (int a = 0; a < 5; ++a)
#pragma unroll 1
        for (int e = 0; e < 5; ++e) {
          const int yy = y + a - 2, xc = x0 + p + e - 2;
          if (yy < 0 || yy >= IMG || xc < 0 || xc >= IMG) continue;
          const float xv = bf_lo(x[(size_t)(yy * IMG + xc) * CH + c]);
          acc = __builtin_fmaf(bf_lo(dws[(a * 5 + e) * CH + c]), xv, acc);
          if (a >= 1 && a <= 3 && e >= 1 && e <= 3) acc = __builtin_fmaf(bf_lo(dwl[((a - 1) * 3 + (e - 1)) * CH + c]), xv, acc);
        }
      o[i] = acc;
    }
    __syncthreads();
    if (t < 8) {
      float m = 0.f, v = 0.f;
#pragma unroll 1
      for (int c = 0; c < CH; ++c) m += o[t * CH + c];
      m *= (1.0f / CH);
#pragma unroll 1
      for (int c = 0; c < CH; ++c) { const float d = o[t * CH + c] - m; v = __builtin_fmaf(d, d, v); }
      st[2 * t] = m; st[2 * t + 1] = __builtin_amdgcn_rsqf(v * (1.0f / CH) + 1e-5f);
    }
    __syncthreads();
#pragma unroll 1
    for (int i = t; i < 8 * HID; i += 256) {
      const int p = i / HID, e = i - p * HID;
      const float m = st[2 * p], r = st[2 * p + 1];
      float u = 0.f;
#pragma unroll 1
      for (int c = 0; c < CH; ++c) u = __builtin_fmaf((o[p * CH + c] - m) * r, bf_lo(w1[c * HID + e]), u);
      const float z = u * __builtin_fmaf(u * u, GC3, GC1);
      g[i] = u * __builtin_amdgcn_rcpf(1.0f + __builtin_amdgcn_exp2f(z));
    }
    __syncthreads();
#pragma unroll 1
    for (int i = t; i < 8 * CH; i += 256) {
      const int p = i / CH, c = i - p * CH;
      float acc = 0.f;
#pragma unroll 1
      for (int e = 0; e < HID; ++e) acc = __builtin_fmaf(g[p * HID + e], bf_lo(w2[e * CH + c]), acc);
      const size_t idx = (size_t)(y * IMG + x0 + p) * CH + c;
      out[idx] = (u16)pk_bf16(bf_lo(x[idx]) + acc, 0.f);
    }
  }
}

__global__ __launch_bounds__(256, 1) void replk_kernel(const u16* __restrict__ x, const u16* __restrict__ dwl,
    const u16* __restrict__ dws, const u16* __restrict__ w1, const u16* __restrict__ w2, u16* __restrict__ out
    ) {
  __shared__ __attribute__((aligned(16))) char smem[L_TOTAL];
  __attribute__((address_space(3))) char* smem3 = (__attribute__((address_space(3))) char*)smem;
  const int w = __builtin_amdgcn_readfirstlane(threadIdx.x >> 6);
  u32 tid = threadIdx.x, blk = blockIdx.x;
  u32 flg = (u32)(size_t)smem3 ? 1u : 0u;
  u64 ka = (u64)__builtin_amdgcn_kernarg_segment_ptr();
  asm volatile(
    ".p2align 3\n"
    "s_load_dwordx2 s[40:41], s[98:99], 0x0\n"
    "s_load_dwordx2 s[88:89], s[98:99], 0x10\n"
    "s_load_dwordx2 s[92:93], s[98:99], 0x8\n"
    "s_load_dwordx2 s[84:85], s[98:99], 0x20\n"
    "s_load_dwordx2 s[80:81], s[98:99], 0x18\n"
    "s_load_dwordx2 s[44:45], s[98:99], 0x28\n"
    "v_lshlrev_b32_e32 v213, 4, v208\n"
    "v_lshrrev_b32_e32 v0, 6, v208\n"
    "s_add_u32 vcc_lo, s96, 0\n"
    "s_addc_u32 vcc_hi, s97, 0\n"
    "global_load_dwordx4 v[168:171], v213, vcc\n"
    "s_add_u32 vcc_lo, s96, 4096\n"
    "s_addc_u32 vcc_hi, s97, 0\n"
    "global_load_dwordx4 v[172:175], v213, vcc\n"
    "s_add_u32 vcc_lo, s96, 8192\n"
    "s_addc_u32 vcc_hi, s97, 0\n"
    "global_load_dwordx4 v[176:179], v213, vcc\n"
    "s_getpc_b64 s[76:77]\n"
    ".Lreplk_pc0:\n"
    "s_bfe_u32 s63, s65, 0x30003\n"
    "v_lshrrev_b32_e32 v5, 2, v208\n"
    "s_lshl_b32 s63, s63, 6\n"
    "v_add_lshl_u32 v5, s63, v5, 7\n"
    "v_min_u32_e32 v5, .Lreplk_end-.Lreplk_pc0-4, v5\n"
    "global_load_dword v233, v5, s[76:77]\n"
    "v_readfirstlane_b32 s60, v0\n"
    "v_min_u32_e32 v1, 239, v208\n"
    "v_mul_u32_u24_e32 v2, 1366, v1\n"
    "s_lshr_b32 s61, s65, 3\n"
    "s_and_b32 s62, s65, 7\n"
    "v_lshrrev_b32_e32 v2, 16, v2\n"
    "v_lshlrev_b32_e32 v3, 2, v1\n"
    "s_lshl_b32 s62, s62, 6\n"
    "s_movk_i32 s63, 768\n"
    "v_mad_u32_u24 v211, v2, s63, v3\n"
    "v_mul_u32_u24_e32 v4, 384, v2\n"
    "v_add_u32_e32 v2, -1, v2\n"
    "s_movk_i32 s90, 4800\n"
    "s_mov_b32 s91, 0x20000\n"
    "v_add_u32_e32 v3, -576, v3\n"
    "v_cmp_gt_u32_e32 vcc, 3, v2\n"
    "v_add_u32_e32 v3, v3, v4\n"
    "v_bfrev_b32_e32 v4, 1\n"
    "s_movk_i32 s94, 1728\n"
    "s_mov_b32 s95, 0x20000\n"
    "v_cndmask_b32_e32 v212, v4, v3, vcc\n"
    "s_lshl_b32 s99, s62, 5\n"
    "s_add_u32 s99, s99, s61\n"
    "s_mulk_i32 s99, 3072\n"
    "s_lshr_b32 s51, s60, 1\n"
    "s_mul_i32 s63, s51, 18432\n"
    "s_lshl_b32 s52, s60, 13\n"
    "s_add_u32 s63, s63, s52\n"
    "v_add_u32_e32 v210, s63, v213\n"
    "s_mul_i32 s38, s60, 9216\n"
    "s_add_u32 s38, s38, 40960\n"
    "s_mov_b32 s86, 73728\n"
    "s_mov_b32 s87, 0x20000\n"
    "v_and_b32_e32 v5, 63, v208\n"
    "v_min_u32_e32 v5, 8, v5\n"
    "s_and_b32 s52, s60, 1\n"
    "s_lshl_b32 s53, s61, 1\n"
    "s_add_u32 s52, s52, s53\n"
    "s_mul_i32 s52, s52, 9\n"
    "v_add_lshl_u32 v214, s52, v5, 7\n"
    "s_cmp_eq_u32 s61, 0\n"
    "s_cselect_b32 s52, 24, 0\n"
    "s_cmp_eq_u32 s61, 31\n"
    "s_cselect_b32 s53, 40, s52\n"
    "s_sub_u32 s53, 256, s53\n"
    "v_subrev_u32_e32 v6, s52, v208\n"
    "s_sub_u32 s63, s99, 384\n"
    "v_cmp_gt_u32_e32 vcc, s53, v6\n"
    "v_lshl_add_u32 v7, v1, 4, s63\n"
    "v_bfe_u32 v8, v208, 4, 2\n"
    "v_lshl_or_b32 v8, v0, 3, v8\n"
    "v_and_b32_e32 v9, 0xf0, v213\n"
    "v_cndmask_b32_e32 v208, v4, v7, vcc\n"
    "v_mul_u32_u24_e32 v8, 768, v8\n"
    "s_mov_b32 s42, 50331648\n"
    "s_mov_b32 s43, 0x20000\n"
    "v_or_b32_e32 v209, v8, v9\n"
    "s_mov_b32 s82, 73728\n"
    "s_mov_b32 s83, 0x20000\n"
    "s_cmp_eq_u32 s62, 0\n"
    "s_cselect_b32 s78, 0, s42\n"
    "s_mov_b32 s79, 0x20000\n"
    "s_waitcnt lgkmcnt(0)\n"
    "s_cmp_lt_u32 s60, 2\n"
    "s_nop 0\n"
    "s_cselect_b64 s[70:71], s[80:81], s[84:85]\n"
    "s_sub_u32 s76, s40, 196608\n"
    "s_subb_u32 s77, s41, 0\n"
    "s_mov_b32 s65, 0\n"
    "buffer_load_dwordx4 a[224:227], v208, s[76:79], s65 offen\n"
    "s_mov_b32 s65, 98304\n"
    "buffer_load_dwordx4 a[228:231], v208, s[76:79], s65 offen\n"
    "s_mov_b32 s65, 196608\n"
    "buffer_load_dwordx4 a[232:235], v208, s[40:43], s65 offen\n"
    "s_mov_b32 s65, 294912\n"
    "buffer_load_dwordx4 a[236:239], v208, s[40:43], s65 offen\n"
    "buffer_load_dword v224, v211, s[88:91], 0 offen offset:0\n"
    "buffer_load_dword v225, v211, s[88:91], 0 offen offset:192\n"
    "buffer_load_dword v226, v211, s[88:91], 0 offen offset:384\n"
    "buffer_load_dword v227, v211, s[88:91], 0 offen offset:576\n"
    "buffer_load_dword v228, v211, s[88:91], 0 offen offset:768\n"
    "v_mov_b32_e64 v88, 0\n"
    "v_mov_b32_e32 v240, 0xdddd8888\n"
    "v_mov_b32_e64 v89, 0\n"
    "v_mov_b32_e64 v200, 0\n"
    "v_mov_b32_e64 v90, 0\n"
    "v_mov_b32_e64 v91, 0\n"
    "v_mov_b32_e64 v59, 0\n"
    "v_mov_b32_e64 v60, 0\n"
    "v_mov_b32_e64 v67, 0\n"
    "v_mov_b32_e64 v68, 0\n"
    "v_mov_b32_e64 v75, 0\n"
    "v_mov_b32_e64 v76, 0\n"
    "v_mov_b32_e64 v83, 0\n"
    "v_mov_b32_e64 v84, 0\n"
    "v_mov_b32_e32 v36, 0x75089ba4\n"
    "v_mov_b32_e32 v38, 0xbdd2d3e8\n"
    "v_mov_b32_e32 v32, 0xc0135761\n"
    "v_lshlrev_b32_e64 v34, 16, v213\n"
    "v_lshlrev_b32_e64 v234, 1, v213\n"
    "v_or_b32_e32 v0, 0x8000, v34\n"
    "v_or_b32_e32 v1, 0x18000, v34\n"
    "v_or_b32_e32 v2, 0x28000, v34\n"
    "v_or_b32_e32 v3, 0x38000, v34\n"
    "v_or_b32_e32 v4, 0x48000, v34\n"
    "v_or_b32_e32 v5, 0x58000, v34\n"
    "v_or_b32_e32 v6, 0x68000, v34\n"
    "v_or_b32_e32 v7, 0x78000, v34\n"
    "v_cmp_ne_u32_e64 s[72:73], 0, v34\n"
    "s_nop 0\n"
    "s_nop 1\n"
    "v_cndmask_b32_e64 v0, 0, v0, s[72:73]\n"
    "v_pk_mul_f32 v[0:1], v[0:1], v[36:37] op_sel_hi:[1,0]\n"
    "v_pk_mul_f32 v[2:3], v[2:3], v[36:37] op_sel_hi:[1,0]\n"
    "v_pk_mul_f32 v[4:5], v[4:5], v[36:37] op_sel_hi:[1,0]\n"
    "v_pk_mul_f32 v[6:7], v[6:7], v[36:37] op_sel_hi:[1,0]\n"
    "v_pk_mul_f32 v[8:9], v[0:1], v[0:1]\n"
    "v_pk_mul_f32 v[10:11], v[2:3], v[2:3]\n"
    "v_pk_mul_f32 v[12:13], v[4:5], v[4:5]\n"
    "v_pk_mul_f32 v[14:15], v[6:7], v[6:7]\n"
    "v_pk_fma_f32 v[8:9], v[8:9], v[38:39], v[32:33] op_sel_hi:[1,0,0]\n"
    "v_pk_fma_f32 v[10:11], v[10:11], v[38:39], v[32:33] op_sel_hi:[1,0,0]\n"
    "v_pk_fma_f32 v[12:13], v[12:13], v[38:39], v[32:33] op_sel_hi:[1,0,0]\n"
    "v_pk_fma_f32 v[14:15], v[14:15], v[38:39], v[32:33] op_sel_hi:[1,0,0]\n"
    "v_pk_mul_f32 v[8:9], v[8:9], v[0:1]\n"
    "v_pk_mul_f32 v[10:11], v[10:11], v[2:3]\n"
    "v_pk_mul_f32 v[12:13], v[12:13], v[4:5]\n"
    "v_pk_mul_f32 v[14:15], v[14:15], v[6:7]\n"
    "v_exp_f32_e64 v8, v8\n"
    "v_exp_f32_e64 v9, v9\n"
    "v_exp_f32_e64 v10, v10\n"
    "v_exp_f32_e64 v11, v11\n"
    "v_exp_f32_e64 v12, v12\n"
    "v_exp_f32_e64 v13, v13\n"
    "v_exp_f32_e64 v14, v14\n"
    "v_exp_f32_e64 v15, v15\n"
    "v_pk_add_f32 v[16:17], v[8:9], 1.0 op_sel_hi:[1,0]\n"
    "v_pk_add_f32 v[18:19], v[10:11], 1.0 op_sel_hi:[1,0]\n"
    "v_pk_add_f32 v[20:21], v[12:13], 1.0 op_sel_hi:[1,0]\n"
    "v_pk_add_f32 v[22:23], v[14:15], 1.0 op_sel_hi:[1,0]\n"
    "v_rcp_f32_e64 v16, v16\n"
    "v_rcp_f32_e64 v17, v17\n"
    "v_rcp_f32_e64 v18, v18\n"
    "v_rcp_f32_e64 v19, v19\n"
    "v_rcp_f32_e64 v20, v20\n"
    "v_rcp_f32_e64 v21, v21\n"
    "v_rcp_f32_e64 v22, v22\n"
    "v_rcp_f32_e64 v23, v23\n"
    "v_pk_mul_f32 v[16:17], v[16:17], v[0:1]\n"
    "v_pk_mul_f32 v[18:19], v[18:19], v[2:3]\n"
    "v_pk_mul_f32 v[20:21], v[20:21], v[4:5]\n"
    "v_pk_mul_f32 v[22:23], v[22:23], v[6:7]\n"
    "v_pk_mul_f32 v[8:9], v[8:9], v[16:17]\n"
    "v_pk_mul_f32 v[10:11], v[10:11], v[18:19]\n"
    "v_pk_mul_f32 v[12:13], v[12:13], v[20:21]\n"
    "v_pk_mul_f32 v[14:15], v[14:15], v[22:23]\n"
    "v_cvt_pk_bf16_f32 v188, v16, v17\n"
    "v_cvt_pk_bf16_f32 v189, v18, v19\n"
    "v_cvt_pk_bf16_f32 v190, v20, v21\n"
    "v_cvt_pk_bf16_f32 v191, v22, v23\n"
    "v_cvt_pk_bf16_f32 v24, -v8, -v9\n"
    "v_cvt_pk_bf16_f32 v25, -v10, -v11\n"
    "v_cvt_pk_bf16_f32 v26, -v12, -v13\n"
    "v_cvt_pk_bf16_f32 v27, -v14, -v15\n"
    "ds_write_b128 v234, v[24:27] offset:0\n"
    "v_or_b32_e32 v0, 0x88000, v34\n"
    "v_or_b32_e32 v1, 0x98000, v34\n"
    "v_or_b32_e32 v2, 0xa8000, v34\n"
    "v_or_b32_e32 v3, 0xb8000, v34\n"
    "v_or_b32_e32 v4, 0xc8000, v34\n"
    "v_or_b32_e32 v5, 0xd8000, v34\n"
    "v_or_b32_e32 v6, 0xe8000, v34\n"
    "v_or_b32_e32 v7, 0xf8000, v34\n"
    "v_pk_mul_f32 v[0:1], v[0:1], v[36:37] op_sel_hi:[1,0]\n"
    "v_pk_mul_f32 v[2:3], v[2:3], v[36:37] op_sel_hi:[1,0]\n"
    "v_pk_mul_f32 v[4:5], v[4:5], v[36:37] op_sel_hi:[1,0]\n"
    "v_pk_mul_f32 v[6:7], v[6:7], v[36:37] op_sel_hi:[1,0]\n"
    "v_pk_mul_f32 v[8:9], v[0:1], v[0:1]\n"
    "v_pk_mul_f32 v[10:11], v[2:3], v[2:3]\n"
    "v_pk_mul_f32 v[12:13], v[4:5], v[4:5]\n"
    "v_pk_mul_f32 v[14:15], v[6:7], v[6:7]\n"
    "v_pk_fma_f32 v[8:9], v[8:9], v[38:39], v[32:33] op_sel_hi:[1,0,0]\n"
    "v_pk_fma_f32 v[10:11], v[10:11], v[38:39], v[32:33] op_sel_hi:[1,0,0]\n"
    "v_pk_fma_f32 v[12:13], v[12:13], v[38:39], v[32:33] op_sel_hi:[1,0,0]\n"
    "v_pk_fma_f32 v[14:15], v[14:15], v[38:39], v[32:33] op_sel_hi:[1,0,0]\n"
    "v_pk_mul_f32 v[8:9], v[8:9], v[0:1]\n"
    "v_pk_mul_f32 v[10:11], v[10:11], v[2:3]\n"
    "v_pk_mul_f32 v[12:13], v[12:13], v[4:5]\n"
    "v_pk_mul_f32 v[14:15], v[14:15], v[6:7]\n"
    "v_exp_f32_e64 v8, v8\n"
    "v_exp_f32_e64 v9, v9\n"
    "v_exp_f32_e64 v10, v10\n"
    "v_exp_f32_e64 v11, v11\n"
    "v_exp_f32_e64 v12, v12\n"
    "v_exp_f32_e64 v13, v13\n"
    "v_exp_f32_e64 v14, v14\n"
    "v_exp_f32_e64 v15, v15\n"
    "v_pk_add_f32 v[16:17], v[8:9], 1.0 op_sel_hi:[1,0]\n"
    "v_pk_add_f32 v[18:19], v[10:11], 1.0 op_sel_hi:[1,0]\n"
    "v_pk_add_f32 v[20:21], v[12:13], 1.0 op_sel_hi:[1,0]\n"
    "v_pk_add_f32 v[22:23], v[14:15], 1.0 op_sel_hi:[1,0]\n"
    "v_rcp_f32_e64 v16, v16\n"
    "v_rcp_f32_e64 v17, v17\n"
    "v_rcp_f32_e64 v18, v18\n"
    "v_rcp_f32_e64 v19, v19\n"
    "v_rcp_f32_e64 v20, v20\n"
    "v_rcp_f32_e64 v21, v21\n"
    "v_rcp_f32_e64 v22, v22\n"
    "v_rcp_f32_e64 v23, v23\n"
    "v_pk_mul_f32 v[16:17], v[16:17], v[0:1]\n"
    "v_pk_mul_f32 v[18:19], v[18:19], v[2:3]\n"
    "v_pk_mul_f32 v[20:21], v[20:21], v[4:5]\n"
    "v_pk_mul_f32 v[22:23], v[22:23], v[6:7]\n"
    "v_pk_mul_f32 v[8:9], v[8:9], v[16:17]\n"
    "v_pk_mul_f32 v[10:11], v[10:11], v[18:19]\n"
    "v_pk_mul_f32 v[12:13], v[12:13], v[20:21]\n"
    "v_pk_mul_f32 v[14:15], v[14:15], v[22:23]\n"
    "v_cvt_pk_bf16_f32 v192, v16, v17\n"
    "v_cvt_pk_bf16_f32 v193, v18, v19\n"
    "v_cvt_pk_bf16_f32 v194, v20, v21\n"
    "v_cvt_pk_bf16_f32 v195, v22, v23\n"
    "v_cvt_pk_bf16_f32 v24, -v8, -v9\n"
    "v_cvt_pk_bf16_f32 v25, -v10, -v11\n"
    "v_cvt_pk_bf16_f32 v26, -v12, -v13\n"
    "v_cvt_pk_bf16_f32 v27, -v14, -v15\n"
    "ds_write_b128 v234, v[24:27] offset:16\n"
    "v_add_u32_e32 v234, 65536, v234\n"
    "buffer_load_dword v229, v212, s[92:95], 0 offen offset:0\n"
    "s_nop 0\n"
    "s_waitcnt vmcnt(13)\n"
    "v_lshlrev_b32_sdwa v216, 2, v168 dst_sel:DWORD dst_unused:UNUSED_PAD src0_sel:DWORD src1_sel:WORD_0\n"
    "v_lshlrev_b32_sdwa v217, 2, v168 dst_sel:DWORD dst_unused:UNUSED_PAD src0_sel:DWORD src1_sel:WORD_1\n"
    "v_lshlrev_b32_sdwa v218, 2, v169 dst_sel:DWORD dst_unused:UNUSED_PAD src0_sel:DWORD src1_sel:WORD_0\n"
    "v_lshlrev_b32_sdwa v219, 2, v169 dst_sel:DWORD dst_unused:UNUSED_PAD src0_sel:DWORD src1_sel:WORD_1\n"
    "v_lshlrev_b32_sdwa v220, 2, v170 dst_sel:DWORD dst_unused:UNUSED_PAD src0_sel:DWORD src1_sel:WORD_0\n"
    "v_lshlrev_b32_sdwa v221, 2, v170 dst_sel:DWORD dst_unused:UNUSED_PAD src0_sel:DWORD src1_sel:WORD_1\n"
    "v_lshlrev_b32_sdwa v222, 2, v171 dst_sel:DWORD dst_unused:UNUSED_PAD src0_sel:DWORD src1_sel:WORD_0\n"
    "v_lshlrev_b32_sdwa v223, 2, v171 dst_sel:DWORD dst_unused:UNUSED_PAD src0_sel:DWORD src1_sel:WORD_1\n"
    "ds_write_b128 v222, v[88:91]\n"
    "buffer_load_dword v230, v212, s[92:95], 0 offen offset:192\n"
    "s_nop 0\n"
    "s_waitcnt vmcnt(13)\n"
    "v_lshlrev_b32_sdwa v207, 2, v172 dst_sel:DWORD dst_unused:UNUSED_PAD src0_sel:DWORD src1_sel:WORD_0\n"
    "v_lshlrev_b32_sdwa v242, 2, v173 dst_sel:DWORD dst_unused:UNUSED_PAD src0_sel:DWORD src1_sel:WORD_0\n"
    "v_lshlrev_b32_sdwa v243, 2, v173 dst_sel:DWORD dst_unused:UNUSED_PAD src0_sel:DWORD src1_sel:WORD_1\n"
    "v_lshlrev_b32_sdwa v244, 2, v174 dst_sel:DWORD dst_unused:UNUSED_PAD src0_sel:DWORD src1_sel:WORD_0\n"
    "v_lshlrev_b32_sdwa v248, 2, v174 dst_sel:DWORD dst_unused:UNUSED_PAD src0_sel:DWORD src1_sel:WORD_1\n"
    "v_lshlrev_b32_sdwa v249, 2, v175 dst_sel:DWORD dst_unused:UNUSED_PAD src0_sel:DWORD src1_sel:WORD_0\n"
    "v_lshlrev_b32_sdwa v250, 2, v175 dst_sel:DWORD dst_unused:UNUSED_PAD src0_sel:DWORD src1_sel:WORD_1\n"
    "v_bfe_i32 v201, v172, 16, 1\n"
    "v_and_b32_e32 v201, 0x35003500, v201\n"
    "v_bfe_i32 v203, v172, 18, 1\n"
    "v_and_b32_e32 v203, 0x35003500, v203\n"
    "v_bfe_i32 v205, v172, 20, 1\n"
    "v_and_b32_e32 v205, 0x35003500, v205\n"
    "v_bfe_i32 v92, v172, 26, 1\n"
    "v_xor_b32_e32 v92, 0xffff, v92\n"
    "v_and_b32_e32 v92, 0x3f803f80, v92\n"
    "v_bfe_i32 v236, v172, 22, 1\n"
    "v_and_b32_e64 v236, v92, v236\n"
    "v_bfe_i32 v237, v172, 23, 1\n"
    "v_and_b32_e32 v237, v92, v237\n"
    "buffer_load_dword v231, v212, s[92:95], 0 offen offset:384\n"
    "s_waitcnt vmcnt(13)\n"
    "v_lshlrev_b32_sdwa v251, 2, v176 dst_sel:DWORD dst_unused:UNUSED_PAD src0_sel:DWORD src1_sel:WORD_0\n"
    "v_lshlrev_b32_sdwa v252, 2, v176 dst_sel:DWORD dst_unused:UNUSED_PAD src0_sel:DWORD src1_sel:WORD_1\n"
    "v_lshlrev_b32_sdwa v253, 2, v177 dst_sel:DWORD dst_unused:UNUSED_PAD src0_sel:DWORD src1_sel:WORD_0\n"
    "v_lshlrev_b32_sdwa v254, 2, v177 dst_sel:DWORD dst_unused:UNUSED_PAD src0_sel:DWORD src1_sel:WORD_1\n"
    "v_lshlrev_b32_sdwa v241, 0, v178 dst_sel:DWORD dst_unused:UNUSED_PAD src0_sel:DWORD src1_sel:WORD_0\n"
    "v_lshlrev_b32_sdwa v245, 4, v178 dst_sel:DWORD dst_unused:UNUSED_PAD src0_sel:DWORD src1_sel:WORD_1\n"
    "v_lshlrev_b32_sdwa v255, 4, v179 dst_sel:DWORD dst_unused:UNUSED_PAD src0_sel:DWORD src1_sel:WORD_0\n"
    "v_lshlrev_b32_sdwa v247, 4, v179 dst_sel:DWORD dst_unused:UNUSED_PAD src0_sel:DWORD src1_sel:WORD_1\n"
    "v_bfe_i32 v238, v172, 24, 1\n"
    "v_and_b32_e64 v238, v92, v238\n"
    "v_bfe_i32 v239, v172, 25, 1\n"
    "v_and_b32_e64 v239, v92, v239\n"
    "v_bfe_u32 v93, v172, 28, 2\n"
    "v_mul_u32_u24_e32 v93, 393216, v93\n"
    "v_mov_b32_e64 v222, v254\n"
    "v_mov_b32_e32 v254, 0\n"
    "s_nop 1\n"
    "v_mfma_f32_32x32x1_2b_f32 v[0:31], v254, v254, 0\n"
    "global_load_dword v233, v214, s[70:71]\n"
    "s_mov_b32 s48, 0x44ea44ea\n"
    "s_mov_b32 s49, 0xb77fb77f\n"
    "s_mov_b32 s50, 0x89efde6e\n"
    "s_mov_b32 s46, s42\n"
    "s_mov_b32 s47, s43\n"
    "s_mul_i32 s54, s60, 592\n"
    "s_add_u32 s54, s54, -27008\n"
    "s_mul_i32 s55, s60, -1284\n"
    "s_add_u32 s55, s55, 54688\n"
    "s_mul_i32 s56, s60, -6160\n"
    "s_add_u32 s56, s56, -27008\n"
    "s_add_u32 s98, s99, 1375872\n"
    "s_and_b32 s36, s60, 1\n"
    "s_lshl_b32 s36, s36, 13\n"
    "s_add_u32 s36, s36, 327680\n"
    "s_mul_i32 s53, s60, 98304\n"
    "s_add_u32 s98, s53, s98\n"
    "s_xor_b32 s53, s60, 1\n"
    "s_mul_i32 s53, s53, 393216\n"
    "s_add_u32 s99, s99, s53\n"
    "s_add_u32 s53, s98, 393216\n"
    "s_sub_u32 s53, s53, s36\n"
    "s_and_b32 s37, s61, 1\n"
    "s_lshl_b32 s37, s37, 2\n"
    "s_add_u32 s37, s37, s60\n"
    "s_lshl_b32 s37, s37, 12\n"
    "s_cmp_eq_u32 s61, 0\n"
    "s_cselect_b32 s67, 2, 0\n"
    "s_cmp_eq_u32 s61, 31\n"
    "s_cselect_b32 s67, 1, s67\n"
    "s_lshl_b32 s67, s67, 30\n"
    "s_mov_b64 s[68:69], s[44:45]\n"
    "s_mov_b32 s70, 0\n"
    "s_mov_b32 s71, s47\n"
    "s_mov_b64 s[72:73], s[40:41]\n"
    "s_mov_b64 s[74:75], s[42:43]\n"
    "s_mov_b32 s57, 0\n"
    "s_mov_b32 s58, 196608\n"
    "s_mov_b32 s60, 0\n"
    "s_mov_b32 s61, 393216\n"
    "s_mov_b32 s62, 786432\n"
    "s_mov_b32 s63, 1179648\n"
    "s_mov_b32 s52, 1572864\n"
    "s_mov_b32 s88, 98304\n"
    "s_mov_b32 s89, 294912\n"
    "s_mov_b32 s39, 0\n"
    "s_getpc_b64 s[76:77]\n"
    ".Lreplk_pc1:\n"
    "s_add_u32 s76, s76, .Lreplk_last-.Lreplk_pc1\n"
    "s_addc_u32 s77, s77, 0\n"
    "s_sub_u32 s76, s76, s36\n"
    "s_subb_u32 s77, s77, 0\n"
    "s_add_u32 s78, s36, .Lreplk_end-.Lreplk_last\n"
    "s_mov_b32 s79, s37\n"
    "s_add_u32 m0, s38, 0\n"
    "s_mov_b32 s64, 0\n"
    "buffer_load_dwordx4 v210, s[84:87], s64 offen offset:0 lds\n"
    "s_waitcnt vmcnt(13)\n"
    "ds_write2_b32 v216, a224, a225 offset0:0 offset1:20\n"
    "v_add3_u32 v247, v247, v93, s98\n"
    "v_add_u32_e32 v246, s98, v255\n"
    "v_add_u32_e64 v245, s99, v245\n"
    "v_add_u32_e64 v255, s99, v255\n"
    "ds_write2_b32 v216, a226, a227 offset0:40 offset1:60\n"
    "v_and_b32_e64 v93, s67, v172\n"
    "v_and_b32_e32 v94, 0x80000000, v93\n"
    "s_waitcnt vmcnt(12)\n"
    "ds_write2_b32 v216, a228, a229 offset0:80 offset1:100\n"
    "v_and_b32_e32 v93, 0x40000000, v93\n"
    "v_or_b32_e64 v246, v246, v94\n"
    "v_or_b32_e64 v247, v247, v93\n"
    "ds_write2_b32 v216, a230, a231 offset0:120 offset1:140\n"
    "v_add_u32_e32 v216, 1360, v216\n"
    "buffer_load_dwordx4 v210, s[84:87], s64 offen offset:1024 lds\n"
    "s_waitcnt vmcnt(12)\n"
    "ds_write2_b32 v216, a232, a233 offset0:0 offset1:20\n"
    "ds_write2_b32 v216, a234, a235 offset0:40 offset1:60\n"
    "buffer_load_dwordx4 v210, s[84:87], s64 offen offset:2048 lds\n"
    "s_waitcnt vmcnt(12)\n"
    "ds_write2_b32 v216, a236, a237 offset0:80 offset1:100\n"
    "s_add_u32 m0, s38, 3072\n"
    "s_mov_b32 s64, 3072\n"
    "buffer_load_dwordx4 v210, s[84:87], s64 offen offset:0 lds\n"
    "buffer_load_dwordx4 v210, s[84:87], s64 offen offset:1024 lds\n"
    "ds_write2_b32 v216, a238, a239 offset0:120 offset1:140\n"
    "buffer_load_dwordx4 v210, s[84:87], s64 offen offset:2048 lds\n"
    "s_add_u32 m0, s38, 6144\n"
    "s_mov_b32 s64, 6144\n"
    "buffer_load_dwordx4 v210, s[84:87], s64 offen offset:0 lds\n"
    "buffer_load_dwordx4 v210, s[84:87], s64 offen offset:1024 lds\n"
    "v_add_u32_e32 v216, 1360, v216\n"
    "s_waitcnt vmcnt(16)\n"
    "v_lshlrev_b32_e32 v40, 16, v224\n"
    "buffer_load_dwordx4 v210, s[84:87], s64 offen offset:2048 lds\n"
    "s_mov_b32 s65, 589824\n"
    "buffer_load_dwordx4 a[240:243], v208, s[40:43], s65 offen\n"
    "s_waitcnt vmcnt(17)\n"
    "v_lshlrev_b32_e32 v41, 16, v225\n"
    "s_mov_b32 s65, 688128\n"
    "buffer_load_dwordx4 a[244:247], v208, s[40:43], s65 offen\n"
    "s_mov_b32 s59, 49152\n"
    "buffer_load_dwordx4 v[96:99], v209, s[80:83], s59 offen offset:0\n"
    "s_waitcnt vmcnt(18)\n"
    "v_lshlrev_b32_e32 v42, 16, v226\n"
    "s_waitcnt vmcnt(17)\n"
    "v_lshlrev_b32_e32 v43, 16, v227\n"
    "s_waitcnt vmcnt(16)\n"
    "v_lshlrev_b32_e32 v44, 16, v228\n"
    "s_waitcnt vmcnt(15)\n"
    "v_lshlrev_b32_e32 v45, 16, v229\n"
    "s_waitcnt vmcnt(14)\n"
    "v_lshlrev_b32_e32 v46, 16, v230\n"
    "s_waitcnt vmcnt(13)\n"
    "v_lshlrev_b32_e32 v47, 16, v231\n"
    "v_add_f32_e64 v41, v41, v45\n"
    "v_add_f32_e64 v42, v42, v46\n"
    "v_add_f32_e64 v43, v43, v47\n"
    "v_cvt_pk_bf16_f32 v56, v40, v41\n"
    "v_cvt_pk_bf16_f32 v57, v42, v43\n"
    "v_cvt_pk_bf16_f32 v58, v44, 0\n"
    "v_cvt_pk_bf16_f32 v64, 0, v40\n"
    "buffer_load_dwordx4 v[100:103], v209, s[80:83], s59 offen offset:256\n"
    "ds_write_b128 v221, v[56:59] offset:0\n"
    "v_cvt_pk_bf16_f32 v65, v41, v42\n"
    "v_cvt_pk_bf16_f32 v66, v43, v44\n"
    "v_cvt_pk_bf16_f32 v61, v40, v41\n"
    "v_cvt_pk_bf16_f32 v62, v42, v43\n"
    "v_cvt_pk_bf16_f32 v63, v44, 0\n"
    "v_cvt_pk_bf16_f32 v69, 0, v40\n"
    "v_cvt_pk_bf16_f32 v70, v41, v42\n"
    "v_cvt_pk_bf16_f32 v71, v43, v44\n"
    "ds_write_b128 v221, v[64:67] offset:16\n"
    "buffer_load_dwordx4 v[104:107], v209, s[80:83], s59 offen offset:512\n"
    "v_and_b32_e32 v48, 0xffff0000, v224\n"
    "v_and_b32_e32 v49, 0xffff0000, v225\n"
    "v_and_b32_e32 v50, 0xffff0000, v226\n"
    "v_and_b32_e32 v51, 0xffff0000, v227\n"
    "v_and_b32_e32 v52, 0xffff0000, v228\n"
    "ds_write_b128 v221, v[60:63] offset:32\n"
    "v_and_b32_e32 v53, 0xffff0000, v229\n"
    "v_and_b32_e32 v54, 0xffff0000, v230\n"
    "v_and_b32_e32 v55, 0xffff0000, v231\n"
    "v_add_f32_e64 v49, v49, v53\n"
    "v_add_f32_e64 v50, v50, v54\n"
    "v_add_f32_e64 v51, v51, v55\n"
    "ds_write_b128 v221, v[68:71] offset:48\n"
    "s_mov_b32 s65, 983040\n"
    "buffer_load_dwordx4 a[248:251], v208, s[40:43], s65 offen\n"
    "v_cvt_pk_bf16_f32 v72, v48, v49\n"
    "v_cvt_pk_bf16_f32 v73, v50, v51\n"
    "v_cvt_pk_bf16_f32 v74, v52, 0\n"
    "v_cvt_pk_bf16_f32 v80, 0, v48\n"
    "v_cvt_pk_bf16_f32 v81, v49, v50\n"
    "v_cvt_pk_bf16_f32 v82, v51, v52\n"
    "v_cvt_pk_bf16_f32 v77, v48, v49\n"
    "v_cvt_pk_bf16_f32 v78, v50, v51\n"
    "v_cvt_pk_bf16_f32 v79, v52, 0\n"
    "v_cvt_pk_bf16_f32 v85, 0, v48\n"
    "v_cvt_pk_bf16_f32 v86, v49, v50\n"
    "v_cvt_pk_bf16_f32 v87, v51, v52\n"
    "ds_write_b128 v221, v[72:75] offset:336\n"
    "s_mov_b32 s65, 1081344\n"
    "buffer_load_dwordx4 a[252:255], v208, s[40:43], s65 offen\n"
    "v_xor_b32_e32 v224, 0, v219\n"
    "v_xor_b32_e32 v225, 32, v219\n"
    "v_xor_b32_e32 v226, 64, v219\n"
    "v_xor_b32_e32 v227, 96, v219\n"
    "ds_write_b128 v221, v[80:83] offset:352\n"
    "v_xor_b32_e32 v228, 0, v223\n"
    "v_xor_b32_e32 v229, 32, v223\n"
    "s_mov_b32 s59, 52224\n"
    "buffer_load_dwordx4 v[108:111], v209, s[80:83], s59 offen offset:0\n"
    "v_xor_b32_e32 v230, 64, v223\n"
    "v_xor_b32_e32 v231, 96, v223\n"
    "ds_write_b128 v221, v[76:79] offset:368\n"
    "v_lshl_add_u32 v196, s51, 8, v225\n"
    "v_lshl_add_u32 v197, s51, 8, v227\n"
    "v_mfma_f32_32x32x1_2b_f32 v[32:63], v254, v254, 0\n"
    "ds_write_b128 v221, v[84:87] offset:384\n"
    "buffer_load_dwordx4 v[112:115], v209, s[80:83], s59 offen offset:256\n"
    "v_mfma_f32_32x32x1_2b_f32 v[64:95], v254, v254, 0\n"
    "v_add_u32_e64 v254, s54, v242\n"
    "buffer_load_dwordx4 v[116:119], v209, s[80:83], s59 offen offset:512\n"
    "s_mov_b32 s65, 1376256\n"
    "buffer_load_dwordx4 a[176:179], v208, s[40:43], s65 offen\n"
    "s_mov_b32 s65, 1474560\n"
    "buffer_load_dwordx4 a[180:183], v208, s[40:43], s65 offen\n"
    "s_waitcnt vmcnt(12)\n"
    "s_barrier\n"
    "ds_read_b64_tr_b16 a[0:1], v220 offset:0\n"
    "ds_read_b64_tr_b16 a[2:3], v220 offset:3072\n"
    "ds_read_b64_tr_b16 a[4:5], v220 offset:8\n"
    "ds_read_b64_tr_b16 a[6:7], v220 offset:3080\n"
    "ds_read_b64_tr_b16 a[8:9], v220 offset:64\n"
    "ds_read_b64_tr_b16 a[10:11], v220 offset:3136\n"
    "ds_read_b64_tr_b16 a[12:13], v220 offset:72\n"
    "ds_read_b64_tr_b16 a[14:15], v220 offset:3144\n"
    "ds_read_b64_tr_b16 v[168:169], v218 offset:128\n"
    "ds_read_b64_tr_b16 v[170:171], v218 offset:3200\n"
    "ds_read_b64_tr_b16 a[16:17], v220 offset:6144\n"
    "ds_read_b64_tr_b16 a[18:19], v220 offset:9216\n"
    "ds_read_b64_tr_b16 a[20:21], v220 offset:6152\n"
    "ds_read_b64_tr_b16 a[22:23], v220 offset:9224\n"
    "ds_read_b64_tr_b16 a[24:25], v220 offset:6208\n"
    "ds_read_b64_tr_b16 a[26:27], v220 offset:9280\n"
    "ds_read_b64_tr_b16 a[28:29], v220 offset:6216\n"
    "ds_read_b64_tr_b16 a[30:31], v220 offset:9288\n"
    "ds_read_b64_tr_b16 v[172:173], v218 offset:6272\n"
    "s_nop 0\n"
    "s_mov_b32 s59, 0\n"
    "buffer_load_dwordx4 v[120:123], v209, s[80:83], s59 offen offset:0\n"
    "ds_read_b64_tr_b16 v[174:175], v218 offset:9344\n"
    "ds_read_b64_tr_b16 a[32:33], v220 offset:12288\n"
    "ds_read_b64_tr_b16 a[34:35], v220 offset:15360\n"
    "ds_read_b64_tr_b16 a[36:37], v220 offset:12296\n"
    "ds_read_b64_tr_b16 a[38:39], v220 offset:15368\n"
    "ds_read_b64_tr_b16 a[40:41], v220 offset:12352\n"
    "ds_read_b64_tr_b16 a[42:43], v220 offset:15424\n"
    "ds_read_b64_tr_b16 a[44:45], v220 offset:12360\n"
    "ds_read_b64_tr_b16 a[46:47], v220 offset:15432\n"
    "ds_read_b64_tr_b16 v[176:177], v218 offset:12416\n"
    "ds_read_b64_tr_b16 v[178:179], v218 offset:15488\n"
    "s_waitcnt vmcnt(12)\n"
    "ds_write2_b32 v216, a240, a241 offset0:0 offset1:20\n"
    "ds_write2_b32 v216, a242, a243 offset0:40 offset1:60\n"
    "buffer_load_dwordx4 v[124:127], v209, s[80:83], s59 offen offset:256\n"
    "s_waitcnt vmcnt(12)\n"
    "ds_write2_b32 v216, a244, a245 offset0:80 offset1:100\n"
    "s_waitcnt lgkmcnt(2)\n"
    "s_barrier\n"
    "ds_write2_b32 v216, a246, a247 offset0:120 offset1:140\n"
    "buffer_load_dwordx4 v[128:131], v209, s[80:83], s59 offen offset:512\n"
    "v_add_u32_e32 v216, 1360, v216\n"
    "s_waitcnt vmcnt(12)\n"
    "v_or3_b32 v200, v200, v96, v97\n"
    "v_or3_b32 v200, v200, v98, v99\n"
    "v_and_b32_e32 v232, 0x7fff7fff, v96\n"
    "v_add_u32_e32 v96, v201, v96\n"
    "v_add_u32_e64 v98, v201, v98\n"
    "ds_write_b128 v217, v[96:99] offset:24576\n"
    "s_mov_b32 s65, 0\n"
    "buffer_load_dwordx4 a[184:187], v208, s[40:43], s65 offen\n"
    "s_waitcnt vmcnt(12)\n"
    "v_or3_b32 v200, v200, v100, v101\n"
    "v_or3_b32 v200, v200, v102, v103\n"
    "v_add_u32_e64 v100, v203, v100\n"
    "v_add_u32_e64 v102, v203, v102\n"
    "ds_write_b128 v217, v[100:103] offset:24832\n"
    "s_mov_b32 s65, 98304\n"
    "buffer_load_dwordx4 a[188:191], v208, s[40:43], s65 offen\n"
    "s_waitcnt vmcnt(12)\n"
    "v_or3_b32 v200, v200, v104, v105\n"
    "v_or3_b32 v200, v200, v106, v107\n"
    "v_add_u32_e32 v104, v205, v104\n"
    "v_add_u32_e64 v106, v205, v106\n"
    "ds_write_b128 v217, v[104:107] offset:25088\n"
    "s_waitcnt lgkmcnt(0)\n"
    "s_barrier\n"
    "s_mov_b32 s59, 3072\n"
    "buffer_load_dwordx4 v[132:135], v209, s[80:83], s59 offen offset:0\n"
    "ds_read_b64_tr_b16 a[48:49], v224 offset:24576\n"
    "ds_read_b64_tr_b16 v[180:181], v196 offset:24576\n"
    "ds_read_b64_tr_b16 a[52:53], v226 offset:24576\n"
    "ds_read_b64_tr_b16 v[184:185], v197 offset:24576\n"
    "ds_read_b64_tr_b16 a[56:57], v228 offset:24576\n"
    "ds_read_b64_tr_b16 a[60:61], v229 offset:24576\n"
    "ds_read_b64_tr_b16 a[64:65], v230 offset:24576\n"
    "ds_read_b64_tr_b16 a[68:69], v231 offset:24576\n"
    "ds_read_b64_tr_b16 a[72:73], v224 offset:24832\n"
    "ds_read_b64_tr_b16 a[76:77], v226 offset:24832\n"
    "s_waitcnt vmcnt(12)\n"
    "ds_write2_b32 v216, a248, a249 offset0:0 offset1:20\n"
    "ds_write2_b32 v216, a250, a251 offset0:40 offset1:60\n"
    "buffer_load_dwordx4 v[136:139], v209, s[80:83], s59 offen offset:256\n"
    "s_waitcnt vmcnt(12)\n"
    "ds_write2_b32 v216, a252, a253 offset0:80 offset1:100\n"
    "ds_write2_b32 v216, a254, a255 offset0:120 offset1:140\n"
    "v_add_u32_e32 v216, 1360, v216\n"
    "buffer_load_dwordx4 v[140:143], v209, s[80:83], s59 offen offset:512\n"
    "s_waitcnt vmcnt(12)\n"
    "v_or3_b32 v200, v200, v108, v109\n"
    "v_or3_b32 v200, v200, v110, v111\n"
    "v_add_u32_e32 v108, v201, v108\n"
    "v_add_u32_e64 v110, v201, v110\n"
    "ds_write_b128 v217, v[108:111] offset:0\n"
    "s_mov_b32 s65, 393216\n"
    "buffer_load_dwordx4 a[192:195], v208, s[40:43], s65 offen\n"
    "s_waitcnt vmcnt(12)\n"
    "v_or3_b32 v200, v200, v112, v113\n"
    "v_or3_b32 v200, v200, v114, v115\n"
    "v_add_u32_e32 v112, v203, v112\n"
    "v_add_u32_e64 v114, v203, v114\n"
    "ds_write_b128 v217, v[112:115] offset:256\n"
    "v_mov_b32_e64 v112, v188\n"
    "v_mov_b32_e64 v113, v189\n"
    "v_mov_b32_e64 v114, v190\n"
    "v_mov_b32_e32 v115, v191\n"
    "s_mov_b32 s65, 491520\n"
    "buffer_load_dwordx4 a[196:199], v208, s[40:43], s65 offen\n"
    "s_waitcnt vmcnt(12)\n"
    "v_or3_b32 v200, v200, v116, v117\n"
    "v_or3_b32 v200, v200, v118, v119\n"
    "v_add_u32_e64 v116, v205, v116\n"
    "v_add_u32_e64 v118, v205, v118\n"
    "ds_write_b128 v217, v[116:119] offset:512\n"
    "v_mov_b32_e64 v116, v192\n"
    "v_mov_b32_e64 v117, v193\n"
    "v_mov_b32_e64 v118, v194\n"
    "v_mov_b32_e64 v119, v195\n"
    "s_waitcnt lgkmcnt(0)\n"
    "s_barrier\n"
    "s_mov_b32 s59, 24576\n"
    "buffer_load_dwordx4 v[144:147], v209, s[80:83], s59 offen offset:0\n"
    "ds_read_b64_tr_b16 a[50:51], v224 offset:0\n"
    "ds_read_b64_tr_b16 v[182:183], v196 offset:0\n"
    "ds_read_b64_tr_b16 a[54:55], v226 offset:0\n"
    "ds_read_b64_tr_b16 v[186:187], v197 offset:0\n"
    "ds_read_b64_tr_b16 a[58:59], v228 offset:0\n"
    "ds_read_b64_tr_b16 a[62:63], v229 offset:0\n"
    "ds_read_b64_tr_b16 a[66:67], v230 offset:0\n"
    "ds_read_b64_tr_b16 a[70:71], v231 offset:0\n"
    "ds_read_b64_tr_b16 a[74:75], v224 offset:256\n"
    "ds_read_b64_tr_b16 a[78:79], v226 offset:256\n"
    "s_waitcnt vmcnt(12)\n"
    "ds_write2_b32 v216, a176, a177 offset0:0 offset1:20\n"
    "ds_write2_b32 v216, a178, a179 offset0:40 offset1:60\n"
    "s_waitcnt lgkmcnt(8)\n"
    "ds_write_b128 v207, v[184:187] offset:36864\n"
    "buffer_load_dwordx4 v[148:151], v209, s[80:83], s59 offen offset:256\n"
    "s_waitcnt vmcnt(12)\n"
    "ds_write2_b32 v216, a180, a181 offset0:80 offset1:100\n"
    "ds_write2_b32 v216, a182, a183 offset0:120 offset1:140\n"
    "v_add_u32_e32 v216, -4800, v216\n"
    "buffer_load_dwordx4 v[152:155], v209, s[80:83], s59 offen offset:512\n"
    "s_waitcnt vmcnt(12)\n"
    "v_or3_b32 v200, v200, v120, v121\n"
    "v_or3_b32 v200, v200, v122, v123\n"
    "v_add_u32_e64 v120, v201, v120\n"
    "v_add_u32_e64 v122, v201, v122\n"
    "ds_write_b128 v217, v[120:123] offset:12288\n"
    "s_waitcnt lgkmcnt(1)\n"
    "s_barrier\n"
    "s_mov_b32 s65, 786432\n"
    "buffer_load_dwordx4 a[200:203], v208, s[40:43], s65 offen\n"
    "s_waitcnt vmcnt(12)\n"
    "v_or3_b32 v200, v200, v124, v125\n"
    "v_or3_b32 v200, v200, v126, v127\n"
    "v_add_u32_e32 v124, v203, v124\n"
    "v_add_u32_e64 v126, v203, v126\n"
    "ds_write_b128 v217, v[124:127] offset:12544\n"
    "s_mov_b32 s65, 884736\n"
    "buffer_load_dwordx4 a[204:207], v208, s[40:43], s65 offen\n"
    "s_waitcnt vmcnt(12)\n"
    "v_or3_b32 v200, v200, v128, v129\n"
    "v_or3_b32 v200, v200, v130, v131\n"
    "v_add_u32_e32 v128, v205, v128\n"
    "v_add_u32_e64 v130, v205, v130\n"
    "ds_write_b128 v217, v[128:131] offset:12800\n"
    "ds_read_b128 a[224:227], v250 offset:0\n"
    "ds_read_b128 a[228:231], v250 offset:320\n"
    "ds_read_b128 a[232:235], v251 offset:0\n"
    "ds_read_b128 a[236:239], v251 offset:336\n"
    "ds_read_b128 a[240:243], v250 offset:80\n"
    "ds_read_b128 a[244:247], v250 offset:400\n"
    "ds_read_b128 a[248:251], v251 offset:672\n"
    "ds_read_b128 a[252:255], v251 offset:1008\n"
    "s_waitcnt lgkmcnt(8)\n"
    "s_barrier\n"
    "s_mov_b32 s59, 27648\n"
    "buffer_load_dwordx4 v[156:159], v209, s[80:83], s59 offen offset:0\n"
    "ds_read_b64_tr_b16 a[80:81], v224 offset:12288\n"
    "ds_read_b64_tr_b16 a[84:85], v225 offset:12288\n"
    "ds_read_b64_tr_b16 a[88:89], v226 offset:12288\n"
    "ds_read_b64_tr_b16 a[92:93], v227 offset:12288\n"
    "s_waitcnt lgkmcnt(9)\n"
    "v_smfmac_f32_16x16x64_bf16 v[0:3], a[232:235], a[224:231], v240\n"
    "s_waitcnt lgkmcnt(8)\n"
    "ds_read_b64_tr_b16 a[96:97], v228 offset:12288\n"
    "ds_read_b64_tr_b16 a[100:101], v229 offset:12288\n"
    "v_smfmac_f32_16x16x64_bf16 v[4:7], a[236:239], a[224:231], v240 abid:1\n"
    "ds_read_b64_tr_b16 a[104:105], v230 offset:12288\n"
    "ds_read_b64_tr_b16 a[108:109], v231 offset:12288\n"
    "ds_read_b64_tr_b16 a[112:113], v224 offset:12544\n"
    "ds_read_b64_tr_b16 a[116:117], v225 offset:12544\n"
    "ds_read_b64_tr_b16 a[120:121], v226 offset:12544\n"
    "s_waitcnt lgkmcnt(12)\n"
    "v_smfmac_f32_16x16x64_bf16 v[8:11], a[248:251], a[240:247], v240\n"
    "s_waitcnt lgkmcnt(11)\n"
    "ds_read_b64_tr_b16 a[124:125], v227 offset:12544\n"
    "s_waitcnt vmcnt(12)\n"
    "ds_write2_b32 v216, a184, a185 offset0:0 offset1:20\n"
    "v_smfmac_f32_16x16x64_bf16 v[12:15], a[252:255], a[240:247], v240 abid:1\n"
    "s_nop 0\n"
    "ds_write2_b32 v216, a186, a187 offset0:40 offset1:60\n"
    "ds_read_b128 a[224:227], v250 offset:160\n"
    "ds_read_b128 a[228:231], v250 offset:480\n"
    "ds_read_b128 a[232:235], v251 offset:1344\n"
    "ds_read_b128 a[236:239], v251 offset:1680\n"
    "buffer_load_dwordx4 v[160:163], v209, s[80:83], s59 offen offset:256\n"
    "s_waitcnt vmcnt(12)\n"
    "ds_write2_b32 v216, a188, a189 offset0:80 offset1:100\n"
    "ds_write2_b32 v216, a190, a191 offset0:120 offset1:140\n"
    "v_add_u32_e32 v216, 1360, v216\n"
    "ds_read_b128 a[240:243], v250 offset:240\n"
    "ds_read_b128 a[244:247], v250 offset:560\n"
    "buffer_load_dwordx4 v[164:167], v209, s[80:83], s59 offen offset:512\n"
    "s_waitcnt vmcnt(12)\n"
    "v_or3_b32 v200, v200, v132, v133\n"
    "v_or3_b32 v200, v200, v134, v135\n"
    "v_add_u32_e64 v132, v201, v132\n"
    "v_add_u32_e64 v134, v201, v134\n"
    "ds_write_b128 v217, v[132:135] offset:24576\n"
    "s_waitcnt lgkmcnt(6)\n"
    "v_smfmac_f32_16x16x64_bf16 v[16:19], a[232:235], a[224:231], v240\n"
    "s_waitcnt lgkmcnt(5)\n"
    "v_smfmac_f32_16x16x64_bf16 v[20:23], a[236:239], a[224:231], v240 abid:1\n"
    "s_mov_b32 s65, 1179648\n"
    "buffer_load_dwordx4 a[208:211], v208, s[40:43], s65 offen\n"
    "s_waitcnt vmcnt(12)\n"
    "v_or3_b32 v200, v200, v136, v137\n"
    "v_or3_b32 v200, v200, v138, v139\n"
    "v_add_u32_e32 v136, v203, v136\n"
    "v_add_u32_e64 v138, v203, v138\n"
    "ds_write_b128 v217, v[136:139] offset:24832\n"
    "ds_read_b128 a[248:251], v251 offset:2016\n"
    "ds_read_b128 a[252:255], v251 offset:2352\n"
    "s_mov_b32 s65, 1277952\n"
    "buffer_load_dwordx4 a[212:215], v208, s[40:43], s65 offen\n"
    "s_waitcnt vmcnt(12)\n"
    "v_or3_b32 v200, v200, v140, v141\n"
    "v_or3_b32 v200, v200, v142, v143\n"
    "v_add_u32_e32 v140, v205, v140\n"
    "v_add_u32_e64 v142, v205, v142\n"
    "ds_write_b128 v217, v[140:143] offset:25088\n"
    "ds_read_b128 a[224:227], v250 offset:27024\n"
    "ds_read_b128 a[228:231], v250 offset:27344\n"
    "ds_read_b128 a[232:235], v251 offset:10752\n"
    "ds_read_b128 a[236:239], v251 offset:11088\n"
    "ds_read_b128 v[136:139], v252 offset:1344\n"
    "ds_read_b128 v[140:143], v252 offset:1680\n"
    "s_waitcnt lgkmcnt(8)\n"
    "v_smfmac_f32_16x16x64_bf16 v[24:27], a[248:251], a[240:247], v240\n"
    "ds_read_b128 a[248:251], v251 offset:11424\n"
    "s_waitcnt lgkmcnt(8)\n"
    "v_smfmac_f32_16x16x64_bf16 v[28:31], a[252:255], a[240:247], v240 abid:1\n"
    "ds_read_b128 a[240:243], v250 offset:27104\n"
    "ds_read_b128 a[244:247], v250 offset:27424\n"
    "s_waitcnt lgkmcnt(9)\n"
    "s_barrier\n"
    "s_mov_b32 s65, 1572864\n"
    "buffer_load_dwordx4 a[216:219], v208, s[40:43], s65 offen\n"
    "ds_read_b64_tr_b16 a[82:83], v224 offset:24576\n"
    "ds_read_b64_tr_b16 a[86:87], v225 offset:24576\n"
    "ds_read_b64_tr_b16 a[90:91], v226 offset:24576\n"
    "s_waitcnt lgkmcnt(9)\n"
    "v_smfmac_f32_16x16x64_bf16 v[32:35], a[232:235], a[224:231], v240\n"
    "s_nop 0\n"
    "ds_read_b64_tr_b16 a[94:95], v227 offset:24576\n"
    "ds_read_b64_tr_b16 a[98:99], v228 offset:24576\n"
    "s_waitcnt lgkmcnt(10)\n"
    "v_smfmac_f32_16x16x64_bf16 v[36:39], a[236:239], a[224:231], v240 abid:1\n"
    "s_nop 0\n"
    "ds_read_b64_tr_b16 a[102:103], v229 offset:24576\n"
    "ds_read_b64_tr_b16 a[106:107], v230 offset:24576\n"
    "ds_read_b64_tr_b16 a[110:111], v231 offset:24576\n"
    "ds_read_b64_tr_b16 a[114:115], v224 offset:24832\n"
    "ds_read_b64_tr_b16 a[118:119], v225 offset:24832\n"
    "ds_read_b64_tr_b16 a[122:123], v226 offset:24832\n"
    "ds_read_b64_tr_b16 a[126:127], v227 offset:24832\n"
    "s_waitcnt vmcnt(12)\n"
    "ds_write2_b32 v216, a192, a193 offset0:0 offset1:20\n"
    "s_waitcnt lgkmcnt(13)\n"
    "v_smfmac_f32_16x16x64_bf16 v[40:43], a[248:251], a[240:247], v240\n"
    "ds_write2_b32 v216, a194, a195 offset0:40 offset1:60\n"
    "ds_read_b128 a[252:255], v251 offset:11760\n"
    "ds_read_b128 a[224:227], v250 offset:27184\n"
    "ds_read_b128 a[228:231], v250 offset:27504\n"
    "ds_read_b128 a[232:235], v251 offset:12096\n"
    "s_mov_b32 s65, 1671168\n"
    "buffer_load_dwordx4 a[220:223], v208, s[40:43], s65 offen\n"
    "s_waitcnt vmcnt(12)\n"
    "ds_write2_b32 v216, a196, a197 offset0:80 offset1:100\n"
    "ds_write2_b32 v216, a198, a199 offset0:120 offset1:140\n"
    "v_add_u32_e32 v216, 1360, v216\n"
    "ds_read_b128 a[236:239], v251 offset:12432\n"
    "ds_read_b128 a[248:251], v251 offset:12768\n"
    "s_waitcnt vmcnt(11)\n"
    "v_or3_b32 v200, v200, v144, v145\n"
    "v_or3_b32 v200, v200, v146, v147\n"
    "v_add_u32_e64 v144, v201, v144\n"
    "v_add_u32_e32 v146, v201, v146\n"
    "ds_write_b128 v217, v[144:147] offset:0\n"
    "s_waitcnt lgkmcnt(7)\n"
    "v_smfmac_f32_16x16x64_bf16 v[44:47], a[252:255], a[240:247], v240 abid:1\n"
    "s_waitcnt lgkmcnt(5)\n"
    "v_smfmac_f32_16x16x64_bf16 v[48:51], a[232:235], a[224:231], v240\n"
    "ds_read_b128 a[240:243], v250 offset:27264\n"
    "s_waitcnt vmcnt(10)\n"
    "v_or3_b32 v200, v200, v148, v149\n"
    "v_or3_b32 v200, v200, v150, v151\n"
    "v_add_u32_e64 v148, v203, v148\n"
    "v_add_u32_e32 v150, v203, v150\n"
    "ds_write_b128 v217, v[148:151] offset:256\n"
    "s_waitcnt lgkmcnt(3)\n"
    "v_smfmac_f32_16x16x64_bf16 v[52:55], a[236:239], a[224:231], v240 abid:1\n"
    "ds_read_b128 a[244:247], v250 offset:27584\n"
    "ds_read_b128 a[252:255], v251 offset:13104\n"
    "ds_read_b128 a[224:227], v250 offset:54048\n"
    "s_waitcnt vmcnt(9)\n"
    "v_or3_b32 v200, v200, v152, v153\n"
    "v_or3_b32 v200, v200, v154, v155\n"
    "v_add_u32_e32 v152, v205, v152\n"
    "v_add_u32_e64 v154, v205, v154\n"
    "ds_write_b128 v217, v[152:155] offset:512\n"
    "ds_read_b128 a[228:231], v250 offset:54368\n"
    "ds_read_b128 a[232:235], v251 offset:21504\n"
    "ds_read_b128 a[236:239], v251 offset:21840\n"
    "s_waitcnt lgkmcnt(6)\n"
    "v_smfmac_f32_16x16x64_bf16 v[56:59], a[248:251], a[240:247], v240\n"
    "ds_read_b128 a[248:251], v251 offset:22176\n"
    "s_waitcnt lgkmcnt(6)\n"
    "v_smfmac_f32_16x16x64_bf16 v[60:63], a[252:255], a[240:247], v240 abid:1\n"
    "ds_read_b128 a[240:243], v250 offset:54128\n"
    "ds_read_b128 a[244:247], v250 offset:54448\n"
    "ds_read_b128 a[252:255], v251 offset:22512\n"
    "s_waitcnt lgkmcnt(7)\n"
    "s_barrier\n"
    "ds_read_b64_tr_b16 a[128:129], v224 offset:0\n"
    "ds_read_b64_tr_b16 a[132:133], v225 offset:0\n"
    "s_waitcnt lgkmcnt(7)\n"
    "v_smfmac_f32_16x16x64_bf16 v[64:67], a[232:235], a[224:231], v240\n"
    "s_waitcnt lgkmcnt(6)\n"
    "ds_read_b64_tr_b16 a[136:137], v226 offset:0\n"
    "ds_read_b64_tr_b16 a[140:141], v227 offset:0\n"
    "v_smfmac_f32_16x16x64_bf16 v[68:71], a[236:239], a[224:231], v240 abid:1\n"
    "ds_read_b64_tr_b16 a[144:145], v228 offset:0\n"
    "ds_read_b64_tr_b16 a[148:149], v229 offset:0\n"
    "ds_read_b64_tr_b16 a[152:153], v230 offset:0\n"
    "ds_read_b64_tr_b16 a[156:157], v231 offset:0\n"
    "ds_read_b64_tr_b16 a[160:161], v224 offset:256\n"
    "s_waitcnt lgkmcnt(10)\n"
    "v_smfmac_f32_16x16x64_bf16 v[72:75], a[248:251], a[240:247], v240\n"
    "s_waitcnt lgkmcnt(9)\n"
    "ds_read_b64_tr_b16 a[164:165], v225 offset:256\n"
    "ds_read_b64_tr_b16 a[168:169], v226 offset:256\n"
    "v_smfmac_f32_16x16x64_bf16 v[76:79], a[252:255], a[240:247], v240 abid:1\n"
    "s_nop 0\n"
    "ds_read_b64_tr_b16 a[172:173], v227 offset:256\n"
    "s_waitcnt vmcnt(8)\n"
    "ds_write2_b32 v216, a200, a201 offset0:0 offset1:20\n"
    "ds_write2_b32 v216, a202, a203 offset0:40 offset1:60\n"
    "ds_read_b128 a[224:227], v250 offset:54208\n"
    "ds_read_b128 a[228:231], v250 offset:54528\n"
    "ds_read_b128 a[232:235], v251 offset:22848\n"
    "ds_read_b128 a[236:239], v251 offset:23184\n"
    "s_waitcnt vmcnt(7)\n"
    "ds_write2_b32 v216, a204, a205 offset0:80 offset1:100\n"
    "ds_write2_b32 v216, a206, a207 offset0:120 offset1:140\n"
    "v_add_u32_e32 v216, 1360, v216\n"
    "ds_read_b128 a[240:243], v250 offset:54288\n"
    "ds_read_b128 a[244:247], v250 offset:54608\n"
    "s_waitcnt vmcnt(6)\n"
    "v_or3_b32 v200, v200, v156, v157\n"
    "v_or3_b32 v200, v200, v158, v159\n"
    "v_add_u32_e64 v156, v201, v156\n"
    "v_add_u32_e32 v158, v201, v158\n"
    "ds_write_b128 v217, v[156:159] offset:12288\n"
    "s_waitcnt lgkmcnt(6)\n"
    "v_smfmac_f32_16x16x64_bf16 v[80:83], a[232:235], a[224:231], v240\n"
    "s_waitcnt lgkmcnt(5)\n"
    "v_smfmac_f32_16x16x64_bf16 v[84:87], a[236:239], a[224:231], v240 abid:1\n"
    "ds_read_b128 a[248:251], v251 offset:23520\n"
    "s_waitcnt vmcnt(5)\n"
    "v_or3_b32 v200, v200, v160, v161\n"
    "v_or3_b32 v200, v200, v162, v163\n"
    "v_add_u32_e64 v160, v203, v160\n"
    "v_add_u32_e64 v162, v203, v162\n"
    "ds_write_b128 v217, v[160:163] offset:12544\n"
    "ds_read_b128 a[252:255], v251 offset:23856\n"
    "ds_read_b128 a[232:235], v252 offset:0\n"
    "ds_read_b128 a[236:239], v252 offset:336\n"
    "s_waitcnt vmcnt(4)\n"
    "v_or3_b32 v200, v200, v164, v165\n"
    "v_or3_b32 v200, v200, v166, v167\n"
    "v_add_u32_e32 v164, v205, v164\n"
    "v_add_u32_e64 v166, v205, v166\n"
    "ds_write_b128 v217, v[164:167] offset:12800\n"
    "v_and_b32_e32 v200, 0x40004000, v200\n"
    "s_waitcnt lgkmcnt(5)\n"
    "v_smfmac_f32_16x16x64_bf16 v[88:91], a[248:251], a[240:247], v240\n"
    "v_cmp_le_u32_e32 vcc, 1, v200\n"
    "s_nop 1\n"
    "s_cmp_lg_u64 vcc, 0\n"
    "s_cselect_b32 vcc_lo, 1, 0\n"
    "s_or_b32 s66, s66, vcc_lo\n"
    "v_cmp_le_u32_e32 vcc, 981467136, v232\n"
    "s_nop 1\n"
    "s_cmp_lg_u64 vcc, 0\n"
    "s_cselect_b32 vcc_lo, 2, 0\n"
    "s_or_b32 s66, s66, vcc_lo\n"
    "v_mov_b32_e32 v200, s66\n"
    "v_mov_b32_e32 v232, 162992\n"
    "s_mov_b64 exec, 1\n"
    "ds_or_b32 v232, v200\n"
    "s_mov_b64 exec, -1\n"
    "s_waitcnt lgkmcnt(3)\n"
    "v_smfmac_f32_16x16x64_bf16 v[92:95], a[252:255], a[240:247], v240 abid:1\n"
    "ds_read_b128 a[248:251], v252 offset:672\n"
    "ds_read_b128 a[252:255], v252 offset:1008\n"
    "s_waitcnt lgkmcnt(3)\n"
    "s_barrier\n"
    "ds_read_b64_tr_b16 a[130:131], v224 offset:12288\n"
    "ds_read_b64_tr_b16 a[134:135], v225 offset:12288\n"
    "ds_read_b64_tr_b16 a[138:139], v226 offset:12288\n"
    "ds_read_b64_tr_b16 a[142:143], v227 offset:12288\n"
    "ds_read_b64_tr_b16 a[146:147], v228 offset:12288\n"
    "ds_read_b64_tr_b16 a[150:151], v229 offset:12288\n"
    "ds_read_b64_tr_b16 a[154:155], v230 offset:12288\n"
    "ds_read_b64_tr_b16 a[158:159], v231 offset:12288\n"
    "ds_read_b64_tr_b16 a[162:163], v224 offset:12544\n"
    "ds_read_b64_tr_b16 a[166:167], v225 offset:12544\n"
    "ds_read_b64_tr_b16 a[170:171], v226 offset:12544\n"
    "ds_read_b64_tr_b16 a[174:175], v227 offset:12544\n"
    "s_nop 0\n"
    "s_waitcnt vmcnt(3)\n"
    "ds_write2_b32 v216, a208, a209 offset0:0 offset1:20\n"
    "ds_write2_b32 v216, a210, a211 offset0:40 offset1:60\n"
    "s_waitcnt vmcnt(2)\n"
    "ds_write2_b32 v216, a212, a213 offset0:80 offset1:100\n"
    "s_waitcnt lgkmcnt(3)\n"
    "s_barrier\n"
    "s_add_u32 m0, s38, 0\n"
    "s_mov_b32 s64, 18432\n"
    "buffer_load_dwordx4 v210, s[84:87], s64 offen offset:0 lds\n"
    "buffer_load_dwordx4 v210, s[84:87], s64 offen offset:1024 lds\n"
    "buffer_load_dwordx4 v210, s[84:87], s64 offen offset:2048 lds\n"
    "s_add_u32 m0, s38, 3072\n"
    "s_mov_b32 s64, 21504\n"
    "buffer_load_dwordx4 v210, s[84:87], s64 offen offset:0 lds\n"
    "buffer_load_dwordx4 v210, s[84:87], s64 offen offset:1024 lds\n"
    "buffer_load_dwordx4 v210, s[84:87], s64 offen offset:2048 lds\n"
    "s_add_u32 m0, s38, 6144\n"
    "s_mov_b32 s64, 24576\n"
    "buffer_load_dwordx4 v210, s[84:87], s64 offen offset:0 lds\n"
    "buffer_load_dwordx4 v210, s[84:87], s64 offen offset:1024 lds\n"
    "buffer_load_dwordx4 v210, s[84:87], s64 offen offset:2048 lds\n"
    "v_lshlrev_b32_e64 v235, 3, v244\n"
    "s_mov_b32 exec_hi, 0\n"
    "buffer_load_dword v233, v235, s[40:43], s53 offen sc1\n"
    "s_mov_b32 exec_hi, -1\n"
    "s_add_u32 s53, s53, 393216\n"
    "ds_write2_b32 v216, a214, a215 offset0:120 offset1:140\n"
    "v_add_u32_e32 v216, 1360, v216\n"
    "s_mov_b32 s64, 0\n"
    "s_mov_b32 s59, 0\n"
    "ds_read_b32 v232, v232\n"
    "v_lshlrev_b32_e32 v235, 3, v244\n"
    "s_mov_b32 exec_hi, 0\n"
    "buffer_load_dword v233, v235, s[40:43], s53 offen sc1\n"
    "s_mov_b32 exec_hi, -1\n"
    "s_add_u32 s53, s53, 393216\n"
    "s_waitcnt vmcnt(12)\n"
    "ds_write2_b32 v216, a216, a217 offset0:0 offset1:20\n"
    "ds_write2_b32 v216, a218, a219 offset0:40 offset1:60\n"
    "v_lshlrev_b32_e32 v235, 3, v244\n"
    "s_mov_b32 exec_hi, 0\n"
    "buffer_load_dword v233, v235, s[40:43], s53 offen sc1\n"
    "s_mov_b32 exec_hi, -1\n"
    "s_add_u32 s53, s53, 393216\n"
    "s_waitcnt vmcnt(12)\n"
    "ds_write2_b32 v216, a220, a221 offset0:80 offset1:100\n"
    "ds_write2_b32 v216, a222, a223 offset0:120 offset1:140\n"
    "v_lshlrev_b32_e64 v235, 3, v244\n"
    "s_mov_b32 exec_hi, 0\n"
    "buffer_load_dword v233, v235, s[40:43], s53 offen sc1\n"
    "s_mov_b32 exec_hi, -1\n"
    "s_add_u32 s53, s53, 393216\n"
    "v_mov_b32_e64 v235, v222\n"
    "s_waitcnt lgkmcnt(0)\n"
    "s_barrier\n"
    "ds_read_b128 a[224:227], v250 offset:640\n"
    "ds_read_b128 a[228:231], v250 offset:960\n"
    "ds_read_b128 a[240:243], v250 offset:720\n"
    "ds_read_b128 a[244:247], v250 offset:1040\n"
    "ds_read_b128 v[128:131], v250 offset:800\n"
    "ds_read_b128 v[132:135], v250 offset:1120\n"
    "s_waitcnt lgkmcnt(4)\n"
    "v_smfmac_f32_16x16x64_bf16 v[4:7], a[236:239], a[224:231], v240 abid:1\n"
    "s_nop 0\n"
    "ds_read_b128 a[236:239], v252 offset:2352\n"
    "v_smfmac_f32_16x16x64_bf16 v[0:3], a[232:235], a[224:231], v240\n"
    "ds_read_b128 a[224:227], v250 offset:880\n"
    "s_waitcnt lgkmcnt(4)\n"
    "v_smfmac_f32_16x16x64_bf16 v[8:11], a[248:251], a[240:247], v240\n"
    "ds_read_b128 a[228:231], v250 offset:1200\n"
    "v_smfmac_f32_16x16x64_bf16 v[12:15], a[252:255], a[240:247], v240 abid:1\n"
    "s_waitcnt lgkmcnt(3)\n"
    "ds_read_b128 a[232:235], v252 offset:2016\n"
    "v_smfmac_f32_16x16x64_bf16 v[16:19], v[136:139], v[128:135], v240\n"
    "ds_read_b128 a[240:243], v250 offset:27664\n"
    "v_smfmac_f32_16x16x64_bf16 v[20:23], v[140:143], v[128:135], v240 abid:1\n"
    "ds_read_b128 a[244:247], v250 offset:27984\n"
    "ds_read_b128 a[248:251], v252 offset:10752\n"
    "ds_read_b128 a[252:255], v252 offset:11088\n"
    "v_pk_mov_b32 v[224:225], v[0:1], v[0:1] op_sel:[0,1]\n"
    "v_pk_mov_b32 v[226:227], v[2:3], v[2:3] op_sel:[0,1]\n"
    "v_pk_mul_f32 v[228:229], v[0:1], v[0:1]\n"
    "v_pk_mul_f32 v[230:231], v[2:3], v[2:3]\n"
    "v_pk_add_f32 v[224:225], v[224:225], v[4:5]\n"
    "v_pk_add_f32 v[226:227], v[226:227], v[6:7]\n"
    "v_pk_fma_f32 v[228:229], v[4:5], v[4:5], v[228:229]\n"
    "v_pk_fma_f32 v[230:231], v[6:7], v[6:7], v[230:231]\n"
    "v_pk_add_f32 v[224:225], v[224:225], v[8:9]\n"
    "v_pk_add_f32 v[226:227], v[226:227], v[10:11]\n"
    "v_pk_fma_f32 v[228:229], v[8:9], v[8:9], v[228:229]\n"
    "v_pk_fma_f32 v[230:231], v[10:11], v[10:11], v[230:231]\n"
    "v_pk_add_f32 v[224:225], v[224:225], v[12:13]\n"
    "v_pk_add_f32 v[226:227], v[226:227], v[14:15]\n"
    "v_pk_fma_f32 v[228:229], v[12:13], v[12:13], v[228:229]\n"
    "v_pk_fma_f32 v[230:231], v[14:15], v[14:15], v[230:231]\n"
    "v_pk_add_f32 v[224:225], v[224:225], v[16:17]\n"
    "v_pk_add_f32 v[226:227], v[226:227], v[18:19]\n"
    "v_pk_fma_f32 v[228:229], v[16:17], v[16:17], v[228:229]\n"
    "v_pk_fma_f32 v[230:231], v[18:19], v[18:19], v[230:231]\n"
    "v_pk_add_f32 v[224:225], v[224:225], v[20:21]\n"
    "v_pk_add_f32 v[226:227], v[226:227], v[22:23]\n"
    "v_pk_fma_f32 v[228:229], v[20:21], v[20:21], v[228:229]\n"
    "v_pk_fma_f32 v[230:231], v[22:23], v[22:23], v[230:231]\n"
    "s_waitcnt lgkmcnt(3)\n"
    "v_smfmac_f32_16x16x64_bf16 v[24:27], a[232:235], a[224:231], v240\n"
    "s_nop 0\n"
    "ds_read_b128 v[128:131], v250 offset:27744\n"
    "v_smfmac_f32_16x16x64_bf16 v[28:31], a[236:239], a[224:231], v240 abid:1\n"
    "s_waitcnt lgkmcnt(2)\n"
    "ds_read_b128 v[132:135], v250 offset:28064\n"
    "v_smfmac_f32_16x16x64_bf16 v[32:35], a[248:251], a[240:247], v240\n"
    "s_waitcnt lgkmcnt(2)\n"
    "ds_read_b128 v[136:139], v252 offset:11424\n"
    "v_smfmac_f32_16x16x64_bf16 v[36:39], a[252:255], a[240:247], v240 abid:1\n"
    "ds_read_b128 v[140:143], v252 offset:11760\n"
    "ds_read_b128 a[224:227], v250 offset:27824\n"
    "ds_read_b128 a[228:231], v250 offset:28144\n"
    "v_pk_add_f32 v[224:225], v[224:225], v[24:25]\n"
    "v_pk_add_f32 v[226:227], v[226:227], v[26:27]\n"
    "v_pk_fma_f32 v[228:229], v[24:25], v[24:25], v[228:229]\n"
    "v_pk_fma_f32 v[230:231], v[26:27], v[26:27], v[230:231]\n"
    "v_pk_add_f32 v[224:225], v[224:225], v[28:29]\n"
    "v_pk_add_f32 v[226:227], v[226:227], v[30:31]\n"
    "v_pk_fma_f32 v[228:229], v[28:29], v[28:29], v[228:229]\n"
    "v_pk_fma_f32 v[230:231], v[30:31], v[30:31], v[230:231]\n"
    "v_pk_add_f32 v[224:225], v[224:225], v[32:33]\n"
    "v_pk_add_f32 v[226:227], v[226:227], v[34:35]\n"
    "v_pk_fma_f32 v[228:229], v[32:33], v[32:33], v[228:229]\n"
    "v_pk_fma_f32 v[230:231], v[34:35], v[34:35], v[230:231]\n"
    "v_pk_add_f32 v[224:225], v[224:225], v[36:37]\n"
    "v_pk_add_f32 v[226:227], v[226:227], v[38:39]\n"
    "v_pk_fma_f32 v[228:229], v[36:37], v[36:37], v[228:229]\n"
    "v_pk_fma_f32 v[230:231], v[38:39], v[38:39], v[230:231]\n"
    "ds_read_b128 a[232:235], v252 offset:12096\n"
    "ds_read_b128 a[236:239], v252 offset:12432\n"
    "ds_read_b128 a[240:243], v250 offset:27904\n"
    "ds_read_b128 a[244:247], v250 offset:28224\n"
    "ds_read_b128 a[248:251], v252 offset:12768\n"
    "s_waitcnt lgkmcnt(8)\n"
    "v_smfmac_f32_16x16x64_bf16 v[40:43], v[136:139], v[128:135], v240\n"
    "s_waitcnt lgkmcnt(7)\n"
    "ds_read_b128 a[252:255], v252 offset:13104\n"
    "v_smfmac_f32_16x16x64_bf16 v[44:47], v[140:143], v[128:135], v240 abid:1\n"
    "ds_read_b128 v[128:131], v250 offset:54688\n"
    "ds_read_b128 v[132:135], v250 offset:55008\n"
    "ds_read_b128 v[136:139], v252 offset:21504\n"
    "ds_read_b128 v[140:143], v252 offset:21840\n"
    "s_nop 0\n"
    "s_nop 0\n"
    "v_pk_add_f32 v[224:225], v[224:225], v[40:41]\n"
    "v_pk_add_f32 v[226:227], v[226:227], v[42:43]\n"
    "v_pk_fma_f32 v[228:229], v[40:41], v[40:41], v[228:229]\n"
    "v_pk_fma_f32 v[230:231], v[42:43], v[42:43], v[230:231]\n"
    "v_pk_add_f32 v[224:225], v[224:225], v[44:45]\n"
    "v_pk_add_f32 v[226:227], v[226:227], v[46:47]\n"
    "v_pk_fma_f32 v[228:229], v[44:45], v[44:45], v[228:229]\n"
    "v_pk_fma_f32 v[230:231], v[46:47], v[46:47], v[230:231]\n"
    "s_waitcnt lgkmcnt(9)\n"
    "v_smfmac_f32_16x16x64_bf16 v[48:51], a[232:235], a[224:231], v240\n"
    "s_nop 0\n"
    "ds_read_b128 a[232:235], v252 offset:22176\n"
    "s_waitcnt lgkmcnt(9)\n"
    "v_smfmac_f32_16x16x64_bf16 v[52:55], a[236:239], a[224:231], v240 abid:1\n"
    "ds_read_b128 a[224:227], v250 offset:54768\n"
    "ds_read_b128 a[228:231], v250 offset:55088\n"
    "ds_read_b128 a[236:239], v252 offset:22512\n"
    "s_nop 1\n"
    "v_pk_add_f32 v[224:225], v[224:225], v[48:49]\n"
    "v_pk_add_f32 v[226:227], v[226:227], v[50:51]\n"
    "v_pk_fma_f32 v[228:229], v[48:49], v[48:49], v[228:229]\n"
    "v_pk_fma_f32 v[230:231], v[50:51], v[50:51], v[230:231]\n"
    "v_pk_add_f32 v[224:225], v[224:225], v[52:53]\n"
    "v_pk_add_f32 v[226:227], v[226:227], v[54:55]\n"
    "v_pk_fma_f32 v[228:229], v[52:53], v[52:53], v[228:229]\n"
    "v_pk_fma_f32 v[230:231], v[54:55], v[54:55], v[230:231]\n"
    "s_waitcnt lgkmcnt(8)\n"
    "v_smfmac_f32_16x16x64_bf16 v[56:59], a[248:251], a[240:247], v240\n"
    "s_nop 0\n"
    "ds_read_b128 a[248:251], v252 offset:22848\n"
    "v_smfmac_f32_16x16x64_bf16 v[60:63], a[252:255], a[240:247], v240 abid:1\n"
    "ds_read_b128 a[240:243], v250 offset:54848\n"
    "ds_read_b128 a[244:247], v250 offset:55168\n"
    "s_waitcnt vmcnt(4)\n"
    "s_barrier\n"
    "s_waitcnt lgkmcnt(5)\n"
    "v_smfmac_f32_16x16x64_bf16 v[64:67], v[136:139], v[128:135], v240\n"
    "s_waitcnt lgkmcnt(4)\n"
    "ds_read_b128 a[252:255], v252 offset:23184\n"
    "v_smfmac_f32_16x16x64_bf16 v[68:71], v[140:143], v[128:135], v240 abid:1\n"
    "s_waitcnt lgkmcnt(4)\n"
    "ds_read_b128 v[128:131], v250 offset:54928\n"
    "v_smfmac_f32_16x16x64_bf16 v[72:75], a[232:235], a[224:231], v240\n"
    "s_nop 0\n"
    "ds_read_b128 v[132:135], v250 offset:55248\n"
    "v_smfmac_f32_16x16x64_bf16 v[76:79], a[236:239], a[224:231], v240 abid:1\n"
    "ds_read_b128 v[136:139], v252 offset:23520\n"
    "ds_read_b128 v[140:143], v252 offset:23856\n"
    "ds_read_b64_tr_b16 a[176:177], v220 offset:0\n"
    "s_waitcnt lgkmcnt(6)\n"
    "v_smfmac_f32_16x16x64_bf16 v[80:83], a[248:251], a[240:247], v240\n"
    "s_nop 0\n"
    "v_accvgpr_write_b32 a248, v236\n"
    "v_accvgpr_write_b32 a249, v237\n"
    "v_accvgpr_write_b32 a250, v238\n"
    "v_accvgpr_write_b32 a251, v239\n"
    "v_pk_add_f32 v[224:225], v[224:225], v[56:57]\n"
    "v_pk_add_f32 v[226:227], v[226:227], v[58:59]\n"
    "v_pk_fma_f32 v[228:229], v[56:57], v[56:57], v[228:229]\n"
    "v_pk_fma_f32 v[230:231], v[58:59], v[58:59], v[230:231]\n"
    "v_pk_add_f32 v[224:225], v[224:225], v[60:61]\n"
    "v_pk_add_f32 v[226:227], v[226:227], v[62:63]\n"
    "v_pk_fma_f32 v[228:229], v[60:61], v[60:61], v[228:229]\n"
    "v_pk_fma_f32 v[230:231], v[62:63], v[62:63], v[230:231]\n"
    "v_pk_add_f32 v[224:225], v[224:225], v[64:65]\n"
    "v_pk_add_f32 v[226:227], v[226:227], v[66:67]\n"
    "v_pk_fma_f32 v[228:229], v[64:65], v[64:65], v[228:229]\n"
    "v_pk_fma_f32 v[230:231], v[66:67], v[66:67], v[230:231]\n"
    "v_pk_add_f32 v[224:225], v[224:225], v[68:69]\n"
    "v_pk_add_f32 v[226:227], v[226:227], v[70:71]\n"
    "v_pk_fma_f32 v[228:229], v[68:69], v[68:69], v[228:229]\n"
    "v_pk_fma_f32 v[230:231], v[70:71], v[70:71], v[230:231]\n"
    "v_pk_add_f32 v[224:225], v[224:225], v[72:73]\n"
    "v_pk_add_f32 v[226:227], v[226:227], v[74:75]\n"
    "v_pk_fma_f32 v[228:229], v[72:73], v[72:73], v[228:229]\n"
    "v_pk_fma_f32 v[230:231], v[74:75], v[74:75], v[230:231]\n"
    "v_pk_add_f32 v[224:225], v[224:225], v[76:77]\n"
    "v_pk_add_f32 v[226:227], v[226:227], v[78:79]\n"
    "v_pk_fma_f32 v[228:229], v[76:77], v[76:77], v[228:229]\n"
    "v_pk_fma_f32 v[230:231], v[78:79], v[78:79], v[230:231]\n"
    "s_waitcnt lgkmcnt(2)\n"
    "v_smfmac_f32_16x16x64_bf16 v[84:87], a[252:255], a[240:247], v240 abid:1\n"
    "s_waitcnt lgkmcnt(0)\n"
    "ds_read_b64_tr_b16 a[178:179], v220 offset:3072\n"
    "v_smfmac_f32_16x16x64_bf16 v[88:91], v[136:139], v[128:135], v240\n"
    "ds_read_b64_tr_b16 a[180:181], v220 offset:8\n"
    "v_smfmac_f32_16x16x64_bf16 v[92:95], v[140:143], v[128:135], v240 abid:1\n"
    "ds_read_b64_tr_b16 a[182:183], v220 offset:3080\n"
    "ds_read_b64_tr_b16 a[184:185], v220 offset:64\n"
    "ds_read_b64_tr_b16 a[186:187], v220 offset:3136\n"
    "v_pk_add_f32 v[224:225], v[224:225], v[80:81]\n"
    "v_pk_add_f32 v[226:227], v[226:227], v[82:83]\n"
    "v_pk_fma_f32 v[228:229], v[80:81], v[80:81], v[228:229]\n"
    "v_pk_fma_f32 v[230:231], v[82:83], v[82:83], v[230:231]\n"
    "v_pk_add_f32 v[224:225], v[224:225], v[84:85]\n"
    "v_pk_add_f32 v[226:227], v[226:227], v[86:87]\n"
    "v_pk_fma_f32 v[228:229], v[84:85], v[84:85], v[228:229]\n"
    "v_pk_fma_f32 v[230:231], v[86:87], v[86:87], v[230:231]\n"
    "v_pk_add_f32 v[224:225], v[224:225], v[88:89]\n"
    "v_pk_add_f32 v[226:227], v[226:227], v[90:91]\n"
    "v_pk_fma_f32 v[228:229], v[88:89], v[88:89], v[228:229]\n"
    "v_pk_fma_f32 v[230:231], v[90:91], v[90:91], v[230:231]\n"
    "v_pk_add_f32 v[224:225], v[224:225], v[92:93]\n"
    "v_pk_add_f32 v[226:227], v[226:227], v[94:95]\n"
    "v_pk_fma_f32 v[228:229], v[92:93], v[92:93], v[228:229]\n"
    "v_pk_fma_f32 v[230:231], v[94:95], v[94:95], v[230:231]\n"
    "ds_write_b128 v254, v[224:227]\n"
    "ds_write_b128 v254, v[228:231] offset:1024\n"
    "v_lshrrev_b32_e64 v254, 2, v242\n"
    "v_add_u32_e64 v254, s55, v254\n"
    "ds_read_b64_tr_b16 a[188:189], v220 offset:72\n"
    "ds_read_b64_tr_b16 a[190:191], v220 offset:3144\n"
    "ds_read_b64_tr_b16 v[188:189], v218 offset:128\n"
    "ds_read_b64_tr_b16 v[190:191], v218 offset:3200\n"
    "ds_read_b64_tr_b16 a[192:193], v220 offset:6144\n"
    "ds_read_b64_tr_b16 a[194:195], v220 offset:9216\n"
    "ds_read_b64_tr_b16 a[196:197], v220 offset:6152\n"
    "ds_read_b64_tr_b16 a[198:199], v220 offset:9224\n"
    "ds_read_b64_tr_b16 a[200:201], v220 offset:6208\n"
    "ds_read_b64_tr_b16 a[202:203], v220 offset:9280\n"
    "ds_read_b64_tr_b16 a[204:205], v220 offset:6216\n"
    "s_waitcnt lgkmcnt(11)\n"
    "s_barrier\n"
    "ds_read_b32 v224, v254 offset:0\n"
    "ds_read_b32 v225, v254 offset:6752\n"
    "ds_read_b32 v226, v254 offset:13504\n"
    "ds_read_b32 v227, v254 offset:20256\n"
    "ds_read_b32 v228, v254 offset:1024\n"
    "ds_read_b32 v229, v254 offset:7776\n"
    "ds_read_b32 v230, v254 offset:14528\n"
    "ds_read_b32 v231, v254 offset:21280\n"
    "ds_read_b64_tr_b16 a[206:207], v220 offset:9288\n"
    "ds_read_b64_tr_b16 v[192:193], v218 offset:6272\n"
    "ds_read_b64_tr_b16 v[194:195], v218 offset:9344\n"
    "ds_read_b64_tr_b16 a[208:209], v220 offset:12288\n"
    "ds_read_b64_tr_b16 a[210:211], v220 offset:15360\n"
    "ds_read_b64_tr_b16 a[212:213], v220 offset:12296\n"
    "ds_read_b64_tr_b16 a[214:215], v220 offset:15368\n"
    "ds_read_b64_tr_b16 a[216:217], v220 offset:12352\n"
    "ds_read_b64_tr_b16 a[218:219], v220 offset:15424\n"
    "s_waitcnt lgkmcnt(15)\n"
    "v_add_f32_e32 v224, v224, v225\n"
    "ds_read_b64_tr_b16 a[220:221], v220 offset:12360\n"
    "s_waitcnt lgkmcnt(14)\n"
    "v_add_f32_e32 v226, v226, v227\n"
    "v_add_f32_e64 v224, v224, v226\n"
    "v_mul_f32_e32 v224, 0x3c2aaaab, v224\n"
    "s_waitcnt lgkmcnt(12)\n"
    "v_add_f32_e32 v228, v228, v229\n"
    "ds_read_b64_tr_b16 a[222:223], v220 offset:15432\n"
    "s_waitcnt lgkmcnt(11)\n"
    "v_add_f32_e32 v230, v230, v231\n"
    "v_add_f32_e64 v228, v228, v230\n"
    "v_mul_f32_e32 v228, 0x3c2aaaab, v228\n"
    "v_fma_f32 v228, -v224, v224, v228\n"
    "v_max_f32_e64 v228, 0, v228\n"
    "v_add_f32_e32 v228, 0x3727c5ac, v228\n"
    "v_rsq_f32_e64 v228, v228\n"
    "ds_read_b64_tr_b16 v[196:197], v218 offset:12416\n"
    "v_mul_f32_e64 v228, s50, v228\n"
    "v_mul_f32_e64 v224, v228, -v224\n"
    "ds_write_b32 v254, v228\n"
    "ds_write_b32 v254, v224 offset:1024\n"
    "v_add_u32_e32 v254, s56, v242\n"
    "ds_read_b64_tr_b16 v[198:199], v218 offset:15488\n"
    "s_waitcnt lgkmcnt(1)\n"
    "s_barrier\n"
    "ds_read_b128 v[224:227], v254\n"
    "ds_read_b128 v[228:231], v254 offset:1024\n"
    "s_waitcnt lgkmcnt(2)\n"
    "s_barrier\n"
    "ds_write_b128 v234, v[112:115] offset:0\n"
    "ds_write_b128 v234, v[116:119] offset:16\n"
    "s_waitcnt lgkmcnt(2)\n"
    "v_pk_fma_f32 v[64:65], v[64:65], v[224:225], v[228:229]\n"
    "v_pk_fma_f32 v[66:67], v[66:67], v[226:227], v[230:231]\n"
    "v_pk_fma_f32 v[68:69], v[68:69], v[224:225], v[228:229]\n"
    "v_pk_fma_f32 v[70:71], v[70:71], v[226:227], v[230:231]\n"
    "v_pk_fma_f32 v[72:73], v[72:73], v[224:225], v[228:229]\n"
    "v_pk_fma_f32 v[74:75], v[74:75], v[226:227], v[230:231]\n"
    "v_pk_fma_f32 v[76:77], v[76:77], v[224:225], v[228:229]\n"
    "v_pk_fma_f32 v[78:79], v[78:79], v[226:227], v[230:231]\n"
    "v_pk_fma_f32 v[80:81], v[80:81], v[224:225], v[228:229]\n"
    "v_pk_fma_f32 v[82:83], v[82:83], v[226:227], v[230:231]\n"
    "v_pk_fma_f32 v[84:85], v[84:85], v[224:225], v[228:229]\n"
    "v_pk_fma_f32 v[86:87], v[86:87], v[226:227], v[230:231]\n"
    "v_pk_fma_f32 v[88:89], v[88:89], v[224:225], v[228:229]\n"
    "v_pk_fma_f32 v[90:91], v[90:91], v[226:227], v[230:231]\n"
    "v_pk_fma_f32 v[92:93], v[92:93], v[224:225], v[228:229]\n"
    "v_pk_fma_f32 v[94:95], v[94:95], v[226:227], v[230:231]\n"
    "v_cvt_pk_bf16_f32 v96, v64, v68\n"
    "v_cvt_pk_bf16_f32 v100, v65, v69\n"
    "v_cvt_pk_bf16_f32 v104, v66, v70\n"
    "v_cvt_pk_bf16_f32 v108, v67, v71\n"
    "v_cvt_pk_bf16_f32 v97, v72, v76\n"
    "v_cvt_pk_bf16_f32 v101, v73, v77\n"
    "v_cvt_pk_bf16_f32 v105, v74, v78\n"
    "v_cvt_pk_bf16_f32 v109, v75, v79\n"
    "v_cvt_pk_bf16_f32 v98, v80, v84\n"
    "v_cvt_pk_bf16_f32 v102, v81, v85\n"
    "v_cvt_pk_bf16_f32 v106, v82, v86\n"
    "v_cvt_pk_bf16_f32 v110, v83, v87\n"
    "v_cvt_pk_bf16_f32 v99, v88, v92\n"
    "ds_write_b128 v253, v[96:99] offset:2048\n"
    "v_cvt_pk_bf16_f32 v103, v89, v93\n"
    "v_cvt_pk_bf16_f32 v107, v90, v94\n"
    "v_cvt_pk_bf16_f32 v111, v91, v95\n"
    "v_pk_fma_f32 v[0:1], v[0:1], v[224:225], v[228:229]\n"
    "v_pk_fma_f32 v[2:3], v[2:3], v[226:227], v[230:231]\n"
    "v_pk_fma_f32 v[4:5], v[4:5], v[224:225], v[228:229]\n"
    "ds_write_b128 v253, v[100:103] offset:5120\n"
    "v_pk_fma_f32 v[6:7], v[6:7], v[226:227], v[230:231]\n"
    "v_pk_fma_f32 v[8:9], v[8:9], v[224:225], v[228:229]\n"
    "v_pk_fma_f32 v[10:11], v[10:11], v[226:227], v[230:231]\n"
    "v_pk_fma_f32 v[12:13], v[12:13], v[224:225], v[228:229]\n"
    "v_pk_fma_f32 v[14:15], v[14:15], v[226:227], v[230:231]\n"
    "v_pk_fma_f32 v[16:17], v[16:17], v[224:225], v[228:229]\n"
    "v_pk_fma_f32 v[18:19], v[18:19], v[226:227], v[230:231]\n"
    "v_pk_fma_f32 v[20:21], v[20:21], v[224:225], v[228:229]\n"
    "v_pk_fma_f32 v[22:23], v[22:23], v[226:227], v[230:231]\n"
    "v_pk_fma_f32 v[24:25], v[24:25], v[224:225], v[228:229]\n"
    "v_pk_fma_f32 v[26:27], v[26:27], v[226:227], v[230:231]\n"
    "v_pk_fma_f32 v[28:29], v[28:29], v[224:225], v[228:229]\n"
    "v_pk_fma_f32 v[30:31], v[30:31], v[226:227], v[230:231]\n"
    "v_cvt_pk_bf16_f32 v80, v0, v4\n"
    "v_cvt_pk_bf16_f32 v84, v1, v5\n"
    "v_cvt_pk_bf16_f32 v88, v2, v6\n"
    "v_cvt_pk_bf16_f32 v92, v3, v7\n"
    "v_cvt_pk_bf16_f32 v81, v8, v12\n"
    "v_cvt_pk_bf16_f32 v85, v9, v13\n"
    "v_cvt_pk_bf16_f32 v89, v10, v14\n"
    "v_cvt_pk_bf16_f32 v93, v11, v15\n"
    "v_cvt_pk_bf16_f32 v82, v16, v20\n"
    "v_cvt_pk_bf16_f32 v86, v17, v21\n"
    "v_cvt_pk_bf16_f32 v90, v18, v22\n"
    "v_cvt_pk_bf16_f32 v94, v19, v23\n"
    "v_cvt_pk_bf16_f32 v83, v24, v28\n"
    "ds_write_b128 v253, v[80:83] offset:0\n"
    "v_cvt_pk_bf16_f32 v87, v25, v29\n"
    "v_cvt_pk_bf16_f32 v91, v26, v30\n"
    "v_cvt_pk_bf16_f32 v95, v27, v31\n"
    "v_pk_fma_f32 v[32:33], v[32:33], v[224:225], v[228:229]\n"
    "v_pk_fma_f32 v[34:35], v[34:35], v[226:227], v[230:231]\n"
    "v_pk_fma_f32 v[36:37], v[36:37], v[224:225], v[228:229]\n"
    "ds_write_b128 v253, v[84:87] offset:3072\n"
    "v_pk_fma_f32 v[38:39], v[38:39], v[226:227], v[230:231]\n"
    "v_pk_fma_f32 v[40:41], v[40:41], v[224:225], v[228:229]\n"
    "v_pk_fma_f32 v[42:43], v[42:43], v[226:227], v[230:231]\n"
    "v_pk_fma_f32 v[44:45], v[44:45], v[224:225], v[228:229]\n"
    "v_pk_fma_f32 v[46:47], v[46:47], v[226:227], v[230:231]\n"
    "v_pk_fma_f32 v[48:49], v[48:49], v[224:225], v[228:229]\n"
    "v_pk_fma_f32 v[50:51], v[50:51], v[226:227], v[230:231]\n"
    "v_pk_fma_f32 v[52:53], v[52:53], v[224:225], v[228:229]\n"
    "v_pk_fma_f32 v[54:55], v[54:55], v[226:227], v[230:231]\n"
    "v_pk_fma_f32 v[56:57], v[56:57], v[224:225], v[228:229]\n"
    "v_pk_fma_f32 v[58:59], v[58:59], v[226:227], v[230:231]\n"
    "v_pk_fma_f32 v[60:61], v[60:61], v[224:225], v[228:229]\n"
    "v_pk_fma_f32 v[62:63], v[62:63], v[226:227], v[230:231]\n"
    "v_cvt_pk_bf16_f32 v64, v32, v36\n"
    "v_cvt_pk_bf16_f32 v68, v33, v37\n"
    "v_cvt_pk_bf16_f32 v72, v34, v38\n"
    "v_cvt_pk_bf16_f32 v76, v35, v39\n"
    "v_cvt_pk_bf16_f32 v65, v40, v44\n"
    "v_cvt_pk_bf16_f32 v69, v41, v45\n"
    "v_cvt_pk_bf16_f32 v73, v42, v46\n"
    "v_cvt_pk_bf16_f32 v77, v43, v47\n"
    "v_cvt_pk_bf16_f32 v66, v48, v52\n"
    "v_cvt_pk_bf16_f32 v70, v49, v53\n"
    "v_cvt_pk_bf16_f32 v74, v50, v54\n"
    "v_cvt_pk_bf16_f32 v78, v51, v55\n"
    "v_cvt_pk_bf16_f32 v67, v56, v60\n"
    "ds_write_b128 v253, v[64:67] offset:1024\n"
    "v_cvt_pk_bf16_f32 v71, v57, v61\n"
    "v_cvt_pk_bf16_f32 v75, v58, v62\n"
    "v_cvt_pk_bf16_f32 v79, v59, v63\n"
    "v_mov_b32_e64 v64, v104\n"
    "v_mov_b32_e64 v65, v105\n"
    "v_mov_b32_e64 v66, v106\n"
    "ds_write_b128 v253, v[68:71] offset:4096\n"
    "v_mov_b32_e64 v67, v107\n"
    "v_mov_b32_e64 v68, v108\n"
    "v_mov_b32_e64 v69, v109\n"
    "v_mov_b32_e64 v70, v110\n"
    "v_mov_b32_e64 v71, v111\n"
    "ds_write_b128 v207, v[180:183] offset:32768\n"
    "ds_write_b128 v207, v[168:171] offset:0\n"
    "ds_write_b128 v207, v[172:175] offset:4096\n"
    "ds_write_b128 v207, v[176:179] offset:8192\n"
    "ds_write_b128 v207, v[188:191] offset:12288\n"
    "ds_write_b128 v207, v[192:195] offset:16384\n"
    "s_waitcnt lgkmcnt(2)\n"
    "s_barrier\n"
    "ds_write_b128 v207, v[196:199] offset:20480\n"
    "ds_read_b128 v[144:147], v242 offset:0\n"
    "ds_read_b128 v[148:151], v242 offset:1024\n"
    "ds_read_b128 v[152:155], v242 offset:2048\n"
    "ds_read_b128 v[156:159], v242 offset:3072\n"
    "ds_read_b128 v[160:163], v242 offset:4096\n"
    "ds_read_b128 v[164:167], v242 offset:5120\n"
    "ds_read_b128 a[240:243], v244 offset:32768\n"
    "s_waitcnt lgkmcnt(7)\n"
    "s_barrier\n"
    "s_waitcnt lgkmcnt(6)\n"
    "v_mfma_f32_16x16x32_bf16 v[168:171], a[80:83], v[144:147], 0\n"
    "s_waitcnt lgkmcnt(5)\n"
    "v_mfma_f32_16x16x32_bf16 v[168:171], a[128:131], v[148:151], v[168:171]\n"
    "s_waitcnt lgkmcnt(4)\n"
    "v_mfma_f32_16x16x32_bf16 v[168:171], a[48:51], v[152:155], v[168:171]\n"
    "s_waitcnt lgkmcnt(3)\n"
    "v_mfma_f32_16x16x32_bf16 v[172:175], a[84:87], v[144:147], 0\n"
    "v_mfma_f32_16x16x32_bf16 v[172:175], a[132:135], v[148:151], v[172:175]\n"
    "v_mfma_f32_16x16x32_bf16 v[176:179], a[80:83], v[156:159], 0\n"
    "s_waitcnt lgkmcnt(0)\n"
    "v_mfma_f32_16x16x32_bf16 v[172:175], a[240:243], v[152:155], v[172:175]\n"
    "v_mfma_f32_16x16x32_bf16 v[176:179], a[128:131], v[160:163], v[176:179]\n"
    "v_mfma_f32_16x16x32_bf16 v[176:179], a[48:51], v[164:167], v[176:179]\n"
    "v_mfma_f32_16x16x32_bf16 v[180:183], a[84:87], v[156:159], 0\n"
    "v_mfma_f32_16x16x32_bf16 v[180:183], a[132:135], v[160:163], v[180:183]\n"
    "v_mfma_f32_16x16x32_bf16 v[180:183], a[240:243], v[164:167], v[180:183]\n"
    "v_readfirstlane_b32 s66, v232\n"
    "s_nop 1\n"
    "s_cmp_lg_u32 s66, 2\n"
    "s_cbranch_scc1 98f\n"
    ".p2align 6\n"
    "1:\n"
    "v_lshlrev_b32_sdwa v184, 1, v168 dst_sel:DWORD dst_unused:UNUSED_PAD src0_sel:DWORD src1_sel:WORD_1\n"
    "v_lshlrev_b32_sdwa v200, 1, v169 dst_sel:DWORD dst_unused:UNUSED_PAD src0_sel:DWORD src1_sel:WORD_1\n"
    "v_lshlrev_b32_sdwa v185, 1, v170 dst_sel:DWORD dst_unused:UNUSED_PAD src0_sel:DWORD src1_sel:WORD_1\n"
    "v_lshlrev_b32_sdwa v201, 1, v171 dst_sel:DWORD dst_unused:UNUSED_PAD src0_sel:DWORD src1_sel:WORD_1\n"
    "v_mfma_f32_16x16x32_bf16 v[140:143], a[244:247], v[188:191], v[140:143]\n"
    "s_waitcnt vmcnt(1)\n"
    "ds_read_b128 a[240:243], v244 offset:36864\n"
    "ds_read_u16 v184, v184\n"
    "v_mfma_f32_16x16x32_bf16 v[168:171], a[88:91], v[144:147], 0\n"
    "s_nop 0\n"
    "ds_read_u16_d16_hi v200, v200\n"
    "ds_read_u16 v185, v185\n"
    "v_mfma_f32_16x16x32_bf16 v[168:171], a[136:139], v[148:151], v[168:171]\n"
    "buffer_load_dwordx4 v[0:3], v246, s[40:43], s60 offen offset:0\n"
    "buffer_load_dwordx4 v[32:35], v246, s[40:43], s60 offen offset:64\n"
    "ds_read_u16_d16_hi v201, v201\n"
    "v_mfma_f32_16x16x32_bf16 v[168:171], a[52:55], v[152:155], v[168:171]\n"
    "v_lshlrev_b32_sdwa v186, 1, v172 dst_sel:DWORD dst_unused:UNUSED_PAD src0_sel:DWORD src1_sel:WORD_1\n"
    "v_lshlrev_b32_sdwa v202, 1, v173 dst_sel:DWORD dst_unused:UNUSED_PAD src0_sel:DWORD src1_sel:WORD_1\n"
    "v_smfmac_f32_32x32x32_bf16 v[128:143], a[248:251], a[224:231], v241\n"
    "ds_read_u16 v186, v186\n"
    "v_lshlrev_b32_sdwa v187, 1, v174 dst_sel:DWORD dst_unused:UNUSED_PAD src0_sel:DWORD src1_sel:WORD_1\n"
    "v_lshlrev_b32_sdwa v203, 1, v175 dst_sel:DWORD dst_unused:UNUSED_PAD src0_sel:DWORD src1_sel:WORD_1\n"
    "v_lshlrev_b32_sdwa v188, 1, v176 dst_sel:DWORD dst_unused:UNUSED_PAD src0_sel:DWORD src1_sel:WORD_1\n"
    "v_lshlrev_b32_sdwa v204, 1, v177 dst_sel:DWORD dst_unused:UNUSED_PAD src0_sel:DWORD src1_sel:WORD_1\n"
    "v_mfma_f32_16x16x32_bf16 v[172:175], a[92:95], v[144:147], 0\n"
    "ds_read_b128 v[236:239], v244 offset:20480\n"
    "v_lshlrev_b32_sdwa v189, 1, v178 dst_sel:DWORD dst_unused:UNUSED_PAD src0_sel:DWORD src1_sel:WORD_1\n"
    "v_mfma_f32_16x16x32_bf16 v[172:175], a[140:143], v[148:151], v[172:175]\n"
    "s_nop 0\n"
    "ds_read_u16_d16_hi v202, v202\n"
    "v_lshlrev_b32_sdwa v205, 1, v179 dst_sel:DWORD dst_unused:UNUSED_PAD src0_sel:DWORD src1_sel:WORD_1\n"
    "v_mfma_f32_16x16x32_bf16 v[176:179], a[88:91], v[156:159], 0\n"
    "s_waitcnt lgkmcnt(7)\n"
    "v_lshlrev_b32_sdwa v190, 1, v180 dst_sel:DWORD dst_unused:UNUSED_PAD src0_sel:DWORD src1_sel:WORD_1\n"
    "v_lshlrev_b32_sdwa v206, 1, v181 dst_sel:DWORD dst_unused:UNUSED_PAD src0_sel:DWORD src1_sel:WORD_1\n"
    "v_mfma_f32_16x16x32_bf16 v[172:175], a[240:243], v[152:155], v[172:175]\n"
    "ds_read_u16 v187, v187\n"
    "v_lshlrev_b32_sdwa v191, 1, v182 dst_sel:DWORD dst_unused:UNUSED_PAD src0_sel:DWORD src1_sel:WORD_1\n"
    "v_mfma_f32_16x16x32_bf16 v[176:179], a[136:139], v[160:163], v[176:179]\n"
    "ds_read_b128 a[244:247], v244 offset:22528\n"
    "v_lshlrev_b32_sdwa v207, 1, v183 dst_sel:DWORD dst_unused:UNUSED_PAD src0_sel:DWORD src1_sel:WORD_1\n"
    "v_mfma_f32_16x16x32_bf16 v[176:179], a[52:55], v[164:167], v[176:179]\n"
    "ds_read_u16_d16_hi v203, v203\n"
    "v_pk_fma_f16 v234, v234, s49, s49\n"
    "v_mfma_f32_16x16x32_bf16 v[180:183], a[92:95], v[156:159], 0\n"
    "v_rcp_f16_e32 v232, v234\n"
    "v_mfma_f32_16x16x32_bf16 v[180:183], a[140:143], v[160:163], v[180:183]\n"
    "s_waitcnt lgkmcnt(8)\n"
    "ds_read_u16 v188, v188\n"
    "v_rcp_f16_sdwa v232, v234 dst_sel:WORD_1 dst_unused:UNUSED_PRESERVE src0_sel:WORD_1\n"
    "v_mfma_f32_16x16x32_bf16 v[180:183], a[240:243], v[164:167], v[180:183]\n"
    "v_fma_mix_f32 v234, v196, v232, 0 op_sel_hi:[1,1,0]\n"
    "v_fma_mix_f32 v232, v196, v232, 0 op_sel:[1,1,0] op_sel_hi:[1,1,0]\n"
    "v_mfma_f32_16x16x32_bf16 v[96:99], a[208:211], v[192:195], v[96:99]\n"
    "ds_read_u16_d16_hi v204, v204\n"
    "v_cvt_pk_bf16_f32 v196, v234, v232\n"
    "v_mfma_f32_16x16x32_bf16 v[100:103], a[212:215], v[192:195], v[100:103]\n"
    "s_waitcnt lgkmcnt(8)\n"
    "ds_read_u16 v189, v189\n"
    "v_or_b32_e32 v184, v184, v200\n"
    "v_mfma_f32_16x16x32_bf16 v[112:115], a[216:219], v[192:195], v[112:115]\n"
    "v_or_b32_e32 v185, v185, v201\n"
    "v_lshlrev_b32_sdwa v201, 1, v171 dst_sel:DWORD dst_unused:UNUSED_PAD src0_sel:DWORD src1_sel:WORD_1\n"
    "v_mfma_f32_16x16x32_bf16 v[116:119], a[220:223], v[192:195], v[116:119]\n"
    "s_waitcnt lgkmcnt(7)\n"
    "ds_read_u16_d16_hi v205, v205\n"
    "v_cvt_pk_bf16_f32 v96, v96, v97\n"
    "v_mfma_f32_16x16x32_bf16 v[128:131], v[236:239], v[192:195], v[128:131]\n"
    "buffer_load_dwordx4 v[20:23], v247, s[40:43], s60 offen offset:0\n"
    "buffer_load_dwordx4 v[52:55], v247, s[40:43], s60 offen offset:64\n"
    "ds_read_u16 v190, v190\n"
    "v_mfma_f32_16x16x32_bf16 v[104:107], a[208:211], v[196:199], v[104:107]\n"
    "s_waitcnt lgkmcnt(8)\n"
    "v_or_b32_e32 v186, v186, v202\n"
    "v_lshlrev_b32_sdwa v202, 1, v173 dst_sel:DWORD dst_unused:UNUSED_PAD src0_sel:DWORD src1_sel:WORD_1\n"
    "v_mfma_f32_16x16x32_bf16 v[108:111], a[212:215], v[196:199], v[108:111]\n"
    "s_nop 0\n"
    "ds_read_u16_d16_hi v206, v206\n"
    "v_cvt_pk_bf16_f32 v97, v98, v99\n"
    "v_mfma_f32_16x16x32_bf16 v[120:123], a[216:219], v[196:199], v[120:123]\n"
    "s_waitcnt lgkmcnt(7)\n"
    "v_cvt_pk_bf16_f32 v98, v100, v101\n"
    "v_cvt_pk_bf16_f32 v99, v102, v103\n"
    "v_mfma_f32_16x16x32_bf16 v[132:135], a[244:247], v[192:195], v[132:135]\n"
    "v_cvt_pk_f16_f32 v192, v168, v169\n"
    "v_lshlrev_b32_sdwa v193, 1, v170 dst_sel:DWORD dst_unused:UNUSED_PAD src0_sel:DWORD src1_sel:WORD_1\n"
    "v_mfma_f32_16x16x32_bf16 v[124:127], a[220:223], v[196:199], v[124:127]\n"
    "v_lshlrev_b32_sdwa v194, 1, v172 dst_sel:DWORD dst_unused:UNUSED_PAD src0_sel:DWORD src1_sel:WORD_1\n"
    "v_lshlrev_b32_sdwa v195, 1, v174 dst_sel:DWORD dst_unused:UNUSED_PAD src0_sel:DWORD src1_sel:WORD_1\n"
    "v_mfma_f32_16x16x32_bf16 v[136:139], v[236:239], v[196:199], v[136:139]\n"
    "s_waitcnt lgkmcnt(6)\n"
    "ds_write_b128 v243, v[96:99] offset:24640\n"
    "v_mfma_f32_16x16x32_bf16 v[140:143], a[244:247], v[196:199], v[140:143]\n"
    "s_nop 0\n"
    "v_cvt_pk_f16_f32 v196, v176, v177\n"
    "v_lshlrev_b32_sdwa v197, 1, v178 dst_sel:DWORD dst_unused:UNUSED_PAD src0_sel:DWORD src1_sel:WORD_1\n"
    "v_mfma_f32_16x16x32_bf16 v[168:171], a[96:99], v[144:147], 0\n"
    "s_nop 0\n"
    "v_lshlrev_b32_sdwa v198, 1, v180 dst_sel:DWORD dst_unused:UNUSED_PAD src0_sel:DWORD src1_sel:WORD_1\n"
    "v_lshlrev_b32_sdwa v199, 1, v182 dst_sel:DWORD dst_unused:UNUSED_PAD src0_sel:DWORD src1_sel:WORD_1\n"
    "v_mfma_f32_16x16x32_bf16 v[168:171], a[144:147], v[148:151], v[168:171]\n"
    "s_waitcnt lgkmcnt(5)\n"
    "ds_read_u16 v191, v191\n"
    "ds_read_u16_d16_hi v207, v207\n"
    "v_mfma_f32_16x16x32_bf16 v[168:171], a[56:59], v[152:155], v[168:171]\n"
    "ds_read_u16 v193, v193\n"
    "ds_read_u16_d16_hi v201, v201\n"
    "v_or_b32_e64 v187, v187, v203\n"
    "v_lshlrev_b32_sdwa v203, 1, v175 dst_sel:DWORD dst_unused:UNUSED_PAD src0_sel:DWORD src1_sel:WORD_1\n"
    "v_mfma_f32_16x16x32_bf16 v[172:175], a[100:103], v[144:147], 0\n"
    "v_or_b32_e64 v188, v188, v204\n"
    "v_cvt_pk_bf16_f32 v112, v112, v113\n"
    "v_mfma_f32_16x16x32_bf16 v[172:175], a[148:151], v[148:151], v[172:175]\n"
    "s_waitcnt lgkmcnt(7)\n"
    "ds_read_u16 v194, v194\n"
    "v_or_b32_e32 v189, v189, v205\n"
    "v_mfma_f32_16x16x32_bf16 v[172:175], a[60:63], v[152:155], v[172:175]\n"
    "v_lshlrev_b32_sdwa v205, 1, v179 dst_sel:DWORD dst_unused:UNUSED_PAD src0_sel:DWORD src1_sel:WORD_1\n"
    "v_cvt_pk_bf16_f32 v113, v114, v115\n"
    "v_mfma_f32_16x16x32_bf16 v[176:179], a[96:99], v[156:159], 0\n"
    "ds_read_u16_d16_hi v202, v202\n"
    "v_cvt_pk_bf16_f32 v114, v116, v117\n"
    "v_mfma_f32_16x16x32_bf16 v[176:179], a[144:147], v[160:163], v[176:179]\n"
    "s_waitcnt lgkmcnt(6)\n"
    "ds_read_b128 v[236:239], v244 offset:0\n"
    "v_or_b32_e32 v190, v190, v206\n"
    "v_mfma_f32_16x16x32_bf16 v[176:179], a[56:59], v[164:167], v[176:179]\n"
    "ds_read_b128 a[252:255], v249 offset:0\n"
    "v_lshlrev_b32_sdwa v206, 1, v181 dst_sel:DWORD dst_unused:UNUSED_PAD src0_sel:DWORD src1_sel:WORD_1\n"
    "v_mfma_f32_16x16x32_bf16 v[96:99], a[0:3], v[184:187], 0\n"
    "ds_read_u16 v195, v195\n"
    "v_cvt_pk_bf16_f32 v115, v118, v119\n"
    "v_mfma_f32_16x16x32_bf16 v[100:103], a[4:7], v[184:187], 0\n"
    "v_cvt_pk_bf16_f32 v128, v128, v129\n"
    "v_cvt_pk_bf16_f32 v129, v130, v131\n"
    "v_cvt_pk_bf16_f32 v130, v132, v133\n"
    "v_cvt_pk_bf16_f32 v131, v134, v135\n"
    "v_cvt_pk_bf16_f32 v104, v104, v105\n"
    "v_cvt_pk_bf16_f32 v105, v106, v107\n"
    "v_cvt_pk_bf16_f32 v106, v108, v109\n"
    "v_cvt_pk_bf16_f32 v107, v110, v111\n"
    "v_cvt_pk_bf16_f32 v120, v120, v121\n"
    "ds_write_b128 v243, v[112:115] offset:26688\n"
    "buffer_load_dwordx4 v[4:7], v246, s[40:43], s61 offen offset:0\n"
    "buffer_load_dwordx4 v[36:39], v246, s[40:43], s61 offen offset:64\n"
    "v_mfma_f32_16x16x32_bf16 v[112:115], a[8:11], v[184:187], 0\n"
    "s_waitcnt lgkmcnt(7)\n"
    "v_or_b32_e32 v191, v191, v207\n"
    "v_lshlrev_b32_sdwa v207, 1, v183 dst_sel:DWORD dst_unused:UNUSED_PAD src0_sel:DWORD src1_sel:WORD_1\n"
    "v_mfma_f32_16x16x32_bf16 v[180:183], a[100:103], v[156:159], 0\n"
    "s_waitcnt lgkmcnt(6)\n"
    "ds_read_u16_d16_hi v203, v203\n"
    "ds_write_b128 v243, v[128:131] offset:28736\n"
    "v_mfma_f32_16x16x32_bf16 v[180:183], a[148:151], v[160:163], v[180:183]\n"
    "v_or_b32_e32 v193, v193, v201\n"
    "v_cvt_pk_bf16_f32 v121, v122, v123\n"
    "v_mfma_f32_16x16x32_bf16 v[180:183], a[60:63], v[164:167], v[180:183]\n"
    "s_waitcnt lgkmcnt(5)\n"
    "v_or_b32_e32 v194, v194, v202\n"
    "v_cvt_pk_bf16_f32 v122, v124, v125\n"
    "v_mfma_f32_16x16x32_bf16 v[116:119], a[12:15], v[184:187], 0\n"
    "s_waitcnt lgkmcnt(4)\n"
    "ds_read_b128 a[244:247], v244 offset:2048\n"
    "ds_write_b128 v243, v[104:107] offset:25664\n"
    "v_mfma_f32_16x16x32_bf16 v[128:131], v[236:239], v[184:187], 0\n"
    "s_nop 0\n"
    "v_cvt_pk_bf16_f32 v123, v126, v127\n"
    "v_pk_fma_f16 v232, v192, v192, s48\n"
    "v_mfma_f32_16x16x32_bf16 v[104:107], a[0:3], v[188:191], 0\n"
    "v_cvt_pk_bf16_f32 v136, v136, v137\n"
    "v_cvt_pk_bf16_f32 v137, v138, v139\n"
    "v_mfma_f32_16x16x32_bf16 v[108:111], a[4:7], v[188:191], 0\n"
    "ds_read_u16 v197, v197\n"
    "ds_write_b128 v243, v[120:123] offset:27712\n"
    "v_mfma_f32_16x16x32_bf16 v[120:123], a[8:11], v[188:191], 0\n"
    "s_nop 0\n"
    "buffer_store_dwordx4 a[252:255], v255, s[68:71], s59 offen offset:0 sc1\n"
    "v_mfma_f32_16x16x32_bf16 v[124:127], a[12:15], v[188:191], 0\n"
    "s_waitcnt lgkmcnt(4)\n"
    "ds_read_u16_d16_hi v205, v205\n"
    "ds_read_u16 v198, v198\n"
    "v_cvt_pk_bf16_f32 v138, v140, v141\n"
    "s_waitcnt lgkmcnt(5)\n"
    "v_mfma_f32_16x16x32_bf16 v[132:135], a[244:247], v[184:187], 0\n"
    "s_nop 0\n"
    "ds_read_u16_d16_hi v206, v206\n"
    "ds_read_u16 v199, v199\n"
    "v_or_b32_e64 v195, v195, v203\n"
    "v_cvt_pk_bf16_f32 v139, v142, v143\n"
    "v_pk_mul_f16 v232, v192, v232\n"
    "v_cvt_pk_f16_f32 v184, v168, v169\n"
    "v_exp_f16_e64 v234, v232\n"
    "v_mfma_f32_16x16x32_bf16 v[140:143], a[244:247], v[188:191], 0\n"
    "v_exp_f16_sdwa v234, v232 dst_sel:WORD_1 dst_unused:UNUSED_PRESERVE src0_sel:WORD_1\n"
    "ds_write_b128 v243, v[136:139] offset:29760\n"
    "v_mfma_f32_16x16x32_bf16 v[136:139], v[236:239], v[188:191], 0\n"
    "v_pk_fma_f16 v234, v234, s49, s49\n"
    "v_lshlrev_b32_sdwa v185, 1, v170 dst_sel:DWORD dst_unused:UNUSED_PAD src0_sel:DWORD src1_sel:WORD_1\n"
    "v_lshlrev_b32_sdwa v201, 1, v171 dst_sel:DWORD dst_unused:UNUSED_PAD src0_sel:DWORD src1_sel:WORD_1\n"
    "v_mfma_f32_16x16x32_bf16 v[168:171], a[104:107], v[144:147], 0\n"
    "ds_read_u16_d16_hi v207, v207\n"
    "ds_read_b128 a[252:255], v249 offset:2048\n"
    "v_mfma_f32_16x16x32_bf16 v[168:171], a[152:155], v[148:151], v[168:171]\n"
    "buffer_load_dwordx4 v[24:27], v247, s[40:43], s61 offen offset:0\n"
    "buffer_load_dwordx4 v[56:59], v247, s[40:43], s61 offen offset:64\n"
    "ds_read_u16 v185, v185\n"
    "v_lshlrev_b32_sdwa v186, 1, v172 dst_sel:DWORD dst_unused:UNUSED_PAD src0_sel:DWORD src1_sel:WORD_1\n"
    "s_waitcnt lgkmcnt(7)\n"
    "v_or_b32_e32 v197, v197, v205\n"
    "v_lshlrev_b32_sdwa v202, 1, v173 dst_sel:DWORD dst_unused:UNUSED_PAD src0_sel:DWORD src1_sel:WORD_1\n"
    "v_lshlrev_b32_sdwa v187, 1, v174 dst_sel:DWORD dst_unused:UNUSED_PAD src0_sel:DWORD src1_sel:WORD_1\n"
    "v_lshlrev_b32_sdwa v203, 1, v175 dst_sel:DWORD dst_unused:UNUSED_PAD src0_sel:DWORD src1_sel:WORD_1\n"
    "v_rcp_f16_e64 v232, v234\n"
    "v_mfma_f32_16x16x32_bf16 v[168:171], a[64:67], v[152:155], v[168:171]\n"
    "ds_read_u16_d16_hi v201, v201\n"
    "ds_read_u16 v186, v186\n"
    "v_rcp_f16_sdwa v232, v234 dst_sel:WORD_1 dst_unused:UNUSED_PRESERVE src0_sel:WORD_1\n"
    "v_mfma_f32_16x16x32_bf16 v[172:175], a[108:111], v[144:147], 0\n"
    "s_waitcnt lgkmcnt(7)\n"
    "ds_read_u16_d16_hi v202, v202\n"
    "v_or_b32_e32 v198, v198, v206\n"
    "v_mfma_f32_16x16x32_bf16 v[172:175], a[156:159], v[148:151], v[172:175]\n"
    "ds_read_b128 v[236:239], v244 offset:4096\n"
    "v_cvt_pk_f16_f32 v188, v176, v177\n"
    "v_lshlrev_b32_sdwa v189, 1, v178 dst_sel:DWORD dst_unused:UNUSED_PAD src0_sel:DWORD src1_sel:WORD_1\n"
    "v_lshlrev_b32_sdwa v205, 1, v179 dst_sel:DWORD dst_unused:UNUSED_PAD src0_sel:DWORD src1_sel:WORD_1\n"
    "v_lshlrev_b32_sdwa v190, 1, v180 dst_sel:DWORD dst_unused:UNUSED_PAD src0_sel:DWORD src1_sel:WORD_1\n"
    "v_lshlrev_b32_sdwa v206, 1, v181 dst_sel:DWORD dst_unused:UNUSED_PAD src0_sel:DWORD src1_sel:WORD_1\n"
    "v_lshlrev_b32_sdwa v191, 1, v182 dst_sel:DWORD dst_unused:UNUSED_PAD src0_sel:DWORD src1_sel:WORD_1\n"
    "v_fma_mix_f32 v234, v192, v232, 0 op_sel_hi:[1,1,0]\n"
    "v_mfma_f32_16x16x32_bf16 v[172:175], a[68:71], v[152:155], v[172:175]\n"
    "ds_read_u16 v187, v187\n"
    "ds_read_u16_d16_hi v203, v203\n"
    "v_fma_mix_f32 v232, v192, v232, 0 op_sel:[1,1,0] op_sel_hi:[1,1,0]\n"
    "v_mfma_f32_16x16x32_bf16 v[176:179], a[104:107], v[156:159], 0\n"
    "v_cvt_pk_bf16_f32 v192, v234, v232\n"
    "v_pk_fma_f16 v232, v196, v196, s48\n"
    "v_mfma_f32_16x16x32_bf16 v[176:179], a[152:155], v[160:163], v[176:179]\n"
    "s_waitcnt lgkmcnt(7)\n"
    "ds_read_b128 a[244:247], v244 offset:6144\n"
    "buffer_store_dwordx4 a[252:255], v255, s[68:71], s59 offen offset:64 sc1\n"
    "v_mfma_f32_16x16x32_bf16 v[176:179], a[64:67], v[164:167], v[176:179]\n"
    "s_nop 0\n"
    "ds_read_u16 v189, v189\n"
    "v_or_b32_e64 v199, v199, v207\n"
    "v_lshlrev_b32_sdwa v207, 1, v183 dst_sel:DWORD dst_unused:UNUSED_PAD src0_sel:DWORD src1_sel:WORD_1\n"
    "v_mfma_f32_16x16x32_bf16 v[180:183], a[108:111], v[156:159], 0\n"
    "ds_read_b128 a[252:255], v249 offset:4096\n"
    "ds_read_u16_d16_hi v205, v205\n"
    "v_pk_mul_f16 v232, v196, v232\n"
    "v_mfma_f32_16x16x32_bf16 v[180:183], a[156:159], v[160:163], v[180:183]\n"
    "s_waitcnt lgkmcnt(9)\n"
    "v_exp_f16_e32 v234, v232\n"
    "v_mfma_f32_16x16x32_bf16 v[180:183], a[68:71], v[164:167], v[180:183]\n"
    "ds_read_u16 v190, v190\n"
    "v_exp_f16_sdwa v234, v232 dst_sel:WORD_1 dst_unused:UNUSED_PRESERVE src0_sel:WORD_1\n"
    "v_mfma_f32_16x16x32_bf16 v[96:99], a[16:19], v[192:195], v[96:99]\n"
    "v_pk_fma_f16 v234, v234, s49, s49\n"
    "v_or_b32_e32 v185, v185, v201\n"
    "v_mfma_f32_16x16x32_bf16 v[100:103], a[20:23], v[192:195], v[100:103]\n"
    "s_waitcnt lgkmcnt(7)\n"
    "ds_read_u16_d16_hi v206, v206\n"
    "v_rcp_f16_e64 v232, v234\n"
    "v_mfma_f32_16x16x32_bf16 v[112:115], a[24:27], v[192:195], v[112:115]\n"
    "ds_read_u16 v191, v191\n"
    "v_rcp_f16_sdwa v232, v234 dst_sel:WORD_1 dst_unused:UNUSED_PRESERVE src0_sel:WORD_1\n"
    "v_mfma_f32_16x16x32_bf16 v[116:119], a[28:31], v[192:195], v[116:119]\n"
    "buffer_load_dwordx4 v[8:11], v246, s[40:43], s62 offen offset:0\n"
    "buffer_load_dwordx4 v[40:43], v246, s[40:43], s62 offen offset:64\n"
    "ds_read_u16_d16_hi v207, v207\n"
    "v_mfma_f32_16x16x32_bf16 v[128:131], v[236:239], v[192:195], v[128:131]\n"
    "v_fma_mix_f32 v234, v196, v232, 0 op_sel_hi:[1,1,0]\n"
    "v_fma_mix_f32 v232, v196, v232, 0 op_sel:[1,1,0] op_sel_hi:[1,1,0]\n"
    "s_waitcnt lgkmcnt(7)\n"
    "v_mfma_f32_16x16x32_bf16 v[132:135], a[244:247], v[192:195], v[132:135]\n"
    "s_nop 0\n"
    "ds_read_b128 a[240:243], v244 offset:34816\n"
    "v_or_b32_e64 v186, v186, v202\n"
    "v_cvt_pk_bf16_f32 v196, v234, v232\n"
    "v_pk_fma_f16 v232, v184, v184, s48\n"
    "v_or_b32_e32 v187, v187, v203\n"
    "v_mfma_f32_16x16x32_bf16 v[104:107], a[16:19], v[196:199], v[104:107]\n"
    "s_waitcnt lgkmcnt(6)\n"
    "v_pk_mul_f16 v232, v184, v232\n"
    "v_lshlrev_b32_sdwa v192, 1, v168 dst_sel:DWORD dst_unused:UNUSED_PAD src0_sel:DWORD src1_sel:WORD_1\n"
    "v_mfma_f32_16x16x32_bf16 v[108:111], a[20:23], v[196:199], v[108:111]\n"
    "ds_read_u16 v192, v192\n"
    "v_exp_f16_e64 v234, v232\n"
    "v_mfma_f32_16x16x32_bf16 v[120:123], a[24:27], v[196:199], v[120:123]\n"
    "v_exp_f16_sdwa v234, v232 dst_sel:WORD_1 dst_unused:UNUSED_PRESERVE src0_sel:WORD_1\n"
    "v_mfma_f32_16x16x32_bf16 v[124:127], a[28:31], v[196:199], v[124:127]\n"
    "s_waitcnt lgkmcnt(6)\n"
    "v_or_b32_e32 v189, v189, v205\n"
    "v_pk_fma_f16 v234, v234, s49, s49\n"
    "v_mfma_f32_16x16x32_bf16 v[136:139], v[236:239], v[196:199], v[136:139]\n"
    "ds_read_b128 v[236:239], v244 offset:8192\n"
    "v_lshlrev_b32_sdwa v200, 1, v169 dst_sel:DWORD dst_unused:UNUSED_PAD src0_sel:DWORD src1_sel:WORD_1\n"
    "v_mfma_f32_16x16x32_bf16 v[140:143], a[244:247], v[196:199], v[140:143]\n"
    "ds_read_u16_d16_hi v200, v200\n"
    "buffer_store_dwordx4 a[252:255], v255, s[68:71], s59 offen offset:128 sc1\n"
    "v_lshlrev_b32_sdwa v193, 1, v170 dst_sel:DWORD dst_unused:UNUSED_PAD src0_sel:DWORD src1_sel:WORD_1\n"
    "v_lshlrev_b32_sdwa v201, 1, v171 dst_sel:DWORD dst_unused:UNUSED_PAD src0_sel:DWORD src1_sel:WORD_1\n"
    "v_mfma_f32_16x16x32_bf16 v[168:171], a[112:115], v[144:147], 0\n"
    "s_waitcnt lgkmcnt(6)\n"
    "ds_read_b128 a[252:255], v249 offset:1024\n"
    "ds_read_u16 v193, v193\n"
    "v_mfma_f32_16x16x32_bf16 v[168:171], a[160:163], v[148:151], v[168:171]\n"
    "s_nop 0\n"
    "ds_read_u16_d16_hi v201, v201\n"
    "v_lshlrev_b32_sdwa v194, 1, v172 dst_sel:DWORD dst_unused:UNUSED_PAD src0_sel:DWORD src1_sel:WORD_1\n"
    "v_lshlrev_b32_sdwa v202, 1, v173 dst_sel:DWORD dst_unused:UNUSED_PAD src0_sel:DWORD src1_sel:WORD_1\n"
    "v_lshlrev_b32_sdwa v195, 1, v174 dst_sel:DWORD dst_unused:UNUSED_PAD src0_sel:DWORD src1_sel:WORD_1\n"
    "v_lshlrev_b32_sdwa v203, 1, v175 dst_sel:DWORD dst_unused:UNUSED_PAD src0_sel:DWORD src1_sel:WORD_1\n"
    "v_rcp_f16_e32 v232, v234\n"
    "v_mfma_f32_16x16x32_bf16 v[168:171], a[72:75], v[152:155], v[168:171]\n"
    "s_waitcnt lgkmcnt(6)\n"
    "ds_read_u16 v194, v194\n"
    "ds_read_u16_d16_hi v202, v202\n"
    "v_or_b32_e64 v190, v190, v206\n"
    "v_rcp_f16_sdwa v232, v234 dst_sel:WORD_1 dst_unused:UNUSED_PRESERVE src0_sel:WORD_1\n"
    "v_mfma_f32_16x16x32_bf16 v[172:175], a[116:119], v[144:147], 0\n"
    "ds_read_u16 v195, v195\n"
    "v_lshlrev_b32_sdwa v196, 1, v176 dst_sel:DWORD dst_unused:UNUSED_PAD src0_sel:DWORD src1_sel:WORD_1\n"
    "v_mfma_f32_16x16x32_bf16 v[172:175], a[164:167], v[148:151], v[172:175]\n"
    "ds_read_b128 a[244:247], v244 offset:10240\n"
    "v_lshlrev_b32_sdwa v204, 1, v177 dst_sel:DWORD dst_unused:UNUSED_PAD src0_sel:DWORD src1_sel:WORD_1\n"
    "v_lshlrev_b32_sdwa v197, 1, v178 dst_sel:DWORD dst_unused:UNUSED_PAD src0_sel:DWORD src1_sel:WORD_1\n"
    "v_lshlrev_b32_sdwa v205, 1, v179 dst_sel:DWORD dst_unused:UNUSED_PAD src0_sel:DWORD src1_sel:WORD_1\n"
    "v_lshlrev_b32_sdwa v198, 1, v180 dst_sel:DWORD dst_unused:UNUSED_PAD src0_sel:DWORD src1_sel:WORD_1\n"
    "v_lshlrev_b32_sdwa v206, 1, v181 dst_sel:DWORD dst_unused:UNUSED_PAD src0_sel:DWORD src1_sel:WORD_1\n"
    "v_lshlrev_b32_sdwa v199, 1, v182 dst_sel:DWORD dst_unused:UNUSED_PAD src0_sel:DWORD src1_sel:WORD_1\n"
    "v_fma_mix_f32 v234, v184, v232, 0 op_sel_hi:[1,1,0]\n"
    "v_mfma_f32_16x16x32_bf16 v[172:175], a[240:243], v[152:155], v[172:175]\n"
    "s_waitcnt lgkmcnt(6)\n"
    "buffer_load_dwordx4 v[12:15], v246, s[40:43], s63 offen offset:0\n"
    "buffer_load_dwordx4 v[44:47], v246, s[40:43], s63 offen offset:64\n"
    "ds_read_u16_d16_hi v203, v203\n"
    "v_or_b32_e32 v191, v191, v207\n"
    "v_lshlrev_b32_sdwa v207, 1, v183 dst_sel:DWORD dst_unused:UNUSED_PAD src0_sel:DWORD src1_sel:WORD_1\n"
    "v_fma_mix_f32 v232, v184, v232, 0 op_sel:[1,1,0] op_sel_hi:[1,1,0]\n"
    "v_mfma_f32_16x16x32_bf16 v[176:179], a[112:115], v[156:159], 0\n"
    "ds_read_u16 v196, v196\n"
    "ds_read_u16_d16_hi v204, v204\n"
    "v_cvt_pk_bf16_f32 v184, v234, v232\n"
    "v_mfma_f32_16x16x32_bf16 v[176:179], a[160:163], v[160:163], v[176:179]\n"
    "v_pk_fma_f16 v232, v188, v188, s48\n"
    "v_or_b32_e32 v192, v192, v200\n"
    "v_mfma_f32_16x16x32_bf16 v[176:179], a[72:75], v[164:167], v[176:179]\n"
    "s_waitcnt lgkmcnt(7)\n"
    "ds_read_u16 v197, v197\n"
    "v_pk_mul_f16 v232, v188, v232\n"
    "v_mfma_f32_16x16x32_bf16 v[180:183], a[116:119], v[156:159], 0\n"
    "ds_read_u16_d16_hi v205, v205\n"
    "v_exp_f16_e64 v234, v232\n"
    "v_mfma_f32_16x16x32_bf16 v[180:183], a[164:167], v[160:163], v[180:183]\n"
    "v_exp_f16_sdwa v234, v232 dst_sel:WORD_1 dst_unused:UNUSED_PRESERVE src0_sel:WORD_1\n"
    "v_mfma_f32_16x16x32_bf16 v[180:183], a[240:243], v[164:167], v[180:183]\n"
    "ds_read_u16 v198, v198\n"
    "v_pk_fma_f16 v234, v234, s49, s49\n"
    "v_mfma_f32_16x16x32_bf16 v[96:99], a[32:35], v[184:187], v[96:99]\n"
    "s_waitcnt lgkmcnt(8)\n"
    "v_rcp_f16_e32 v232, v234\n"
    "v_mfma_f32_16x16x32_bf16 v[100:103], a[36:39], v[184:187], v[100:103]\n"
    "ds_read_u16_d16_hi v206, v206\n"
    "buffer_store_dwordx4 a[252:255], v255, s[68:71], s39 offen offset:0 sc1\n"
    "v_mfma_f32_16x16x32_bf16 v[112:115], a[40:43], v[184:187], v[112:115]\n"
    "s_nop 0\n"
    "ds_read_u16 v199, v199\n"
    "v_rcp_f16_sdwa v232, v234 dst_sel:WORD_1 dst_unused:UNUSED_PRESERVE src0_sel:WORD_1\n"
    "v_mfma_f32_16x16x32_bf16 v[116:119], a[44:47], v[184:187], v[116:119]\n"
    "s_waitcnt lgkmcnt(7)\n"
    "ds_read_b128 a[252:255], v249 offset:3072\n"
    "v_fma_mix_f32 v234, v188, v232, 0 op_sel_hi:[1,1,0]\n"
    "v_mfma_f32_16x16x32_bf16 v[128:131], v[236:239], v[184:187], v[128:131]\n"
    "ds_read_u16_d16_hi v207, v207\n"
    "v_fma_mix_f32 v232, v188, v232, 0 op_sel:[1,1,0] op_sel_hi:[1,1,0]\n"
    "v_mfma_f32_16x16x32_bf16 v[132:135], a[244:247], v[184:187], v[132:135]\n"
    "v_cvt_pk_bf16_f32 v188, v234, v232\n"
    "v_or_b32_e32 v193, v193, v201\n"
    "v_cvt_pk_f16_f32 v184, v168, v169\n"
    "v_mfma_f32_16x16x32_bf16 v[104:107], a[32:35], v[188:191], v[104:107]\n"
    "s_waitcnt lgkmcnt(7)\n"
    "ds_read_b128 a[240:243], v244 offset:38912\n"
    "v_lshlrev_b32_sdwa v185, 1, v170 dst_sel:DWORD dst_unused:UNUSED_PAD src0_sel:DWORD src1_sel:WORD_1\n"
    "v_mfma_f32_16x16x32_bf16 v[108:111], a[36:39], v[188:191], v[108:111]\n"
    "ds_read_u16 v185, v185\n"
    "v_lshlrev_b32_sdwa v201, 1, v171 dst_sel:DWORD dst_unused:UNUSED_PAD src0_sel:DWORD src1_sel:WORD_1\n"
    "v_mfma_f32_16x16x32_bf16 v[120:123], a[40:43], v[188:191], v[120:123]\n"
    "ds_read_u16_d16_hi v201, v201\n"
    "v_lshlrev_b32_sdwa v186, 1, v172 dst_sel:DWORD dst_unused:UNUSED_PAD src0_sel:DWORD src1_sel:WORD_1\n"
    "v_mfma_f32_16x16x32_bf16 v[124:127], a[44:47], v[188:191], v[124:127]\n"
    "v_or_b32_e32 v194, v194, v202\n"
    "v_lshlrev_b32_sdwa v202, 1, v173 dst_sel:DWORD dst_unused:UNUSED_PAD src0_sel:DWORD src1_sel:WORD_1\n"
    "v_mfma_f32_16x16x32_bf16 v[136:139], v[236:239], v[188:191], v[136:139]\n"
    "s_waitcnt lgkmcnt(8)\n"
    "buffer_load_dwordx4 v[16:19], v246, s[40:43], s52 offen offset:0\n"
    "buffer_load_dwordx4 v[48:51], v246, s[40:43], s52 offen offset:64\n"
    "ds_read_u16 v186, v186\n"
    "v_mfma_f32_16x16x32_bf16 v[140:143], a[244:247], v[188:191], v[140:143]\n"
    "ds_read_u16_d16_hi v202, v202\n"
    "v_lshlrev_b32_sdwa v187, 1, v174 dst_sel:DWORD dst_unused:UNUSED_PAD src0_sel:DWORD src1_sel:WORD_1\n"
    "v_mfma_f32_16x16x32_bf16 v[168:171], a[120:123], v[144:147], 0\n"
    "v_cvt_pk_f16_f32 v188, v176, v177\n"
    "v_lshlrev_b32_sdwa v189, 1, v178 dst_sel:DWORD dst_unused:UNUSED_PAD src0_sel:DWORD src1_sel:WORD_1\n"
    "v_mfma_f32_16x16x32_bf16 v[168:171], a[168:171], v[148:151], v[168:171]\n"
    "s_waitcnt lgkmcnt(8)\n"
    "ds_read_b128 v[236:239], v244 offset:12288\n"
    "v_lshlrev_b32_sdwa v190, 1, v180 dst_sel:DWORD dst_unused:UNUSED_PAD src0_sel:DWORD src1_sel:WORD_1\n"
    "v_mfma_f32_16x16x32_bf16 v[168:171], a[76:79], v[152:155], v[168:171]\n"
    "s_waitcnt lgkmcnt(7)\n"
    "ds_read_u16 v187, v187\n"
    "v_lshlrev_b32_sdwa v191, 1, v182 dst_sel:DWORD dst_unused:UNUSED_PAD src0_sel:DWORD src1_sel:WORD_1\n"
    "v_or_b32_e64 v195, v195, v203\n"
    "v_lshlrev_b32_sdwa v203, 1, v175 dst_sel:DWORD dst_unused:UNUSED_PAD src0_sel:DWORD src1_sel:WORD_1\n"
    "v_mfma_f32_16x16x32_bf16 v[172:175], a[124:127], v[144:147], 0\n"
    "ds_read_u16_d16_hi v203, v203\n"
    "v_or_b32_e64 v196, v196, v204\n"
    "v_mfma_f32_16x16x32_bf16 v[172:175], a[172:175], v[148:151], v[172:175]\n"
    "v_or_b32_e64 v197, v197, v205\n"
    "v_lshlrev_b32_sdwa v205, 1, v179 dst_sel:DWORD dst_unused:UNUSED_PAD src0_sel:DWORD src1_sel:WORD_1\n"
    "v_mfma_f32_16x16x32_bf16 v[176:179], a[120:123], v[156:159], 0\n"
    "s_waitcnt lgkmcnt(7)\n"
    "ds_read_b128 a[244:247], v244 offset:14336\n"
    "v_or_b32_e32 v198, v198, v206\n"
    "v_mfma_f32_16x16x32_bf16 v[172:175], a[240:243], v[152:155], v[172:175]\n"
    "ds_read_u16 v189, v189\n"
    "v_lshlrev_b32_sdwa v206, 1, v181 dst_sel:DWORD dst_unused:UNUSED_PAD src0_sel:DWORD src1_sel:WORD_1\n"
    "v_mfma_f32_16x16x32_bf16 v[176:179], a[168:171], v[160:163], v[176:179]\n"
    "buffer_store_dwordx4 a[252:255], v255, s[68:71], s39 offen offset:64 sc1\n"
    "ds_read_u16_d16_hi v205, v205\n"
    "v_mfma_f32_16x16x32_bf16 v[176:179], a[76:79], v[164:167], v[176:179]\n"
    "s_waitcnt lgkmcnt(8)\n"
    "v_or_b32_e32 v199, v199, v207\n"
    "v_lshlrev_b32_sdwa v207, 1, v183 dst_sel:DWORD dst_unused:UNUSED_PAD src0_sel:DWORD src1_sel:WORD_1\n"
    "v_mfma_f32_16x16x32_bf16 v[180:183], a[124:127], v[156:159], 0\n"
    "ds_read_b128 a[252:255], v249 offset:5120\n"
    "ds_read_u16 v190, v190\n"
    "v_mfma_f32_16x16x32_bf16 v[180:183], a[172:175], v[160:163], v[180:183]\n"
    "v_or_b32_e64 v185, v185, v201\n"
    "v_pk_fma_f16 v232, v184, v184, s48\n"
    "v_mfma_f32_16x16x32_bf16 v[180:183], a[240:243], v[164:167], v[180:183]\n"
    "ds_read_b128 v[144:147], v243 offset:0\n"
    "v_pk_mul_f16 v232, v184, v232\n"
    "v_mfma_f32_16x16x32_bf16 v[96:99], a[176:179], v[192:195], v[96:99]\n"
    "s_waitcnt lgkmcnt(8)\n"
    "ds_read_u16_d16_hi v206, v206\n"
    "v_or_b32_e32 v186, v186, v202\n"
    "v_mfma_f32_16x16x32_bf16 v[100:103], a[180:183], v[192:195], v[100:103]\n"
    "v_exp_f16_e64 v234, v232\n"
    "v_mfma_f32_16x16x32_bf16 v[112:115], a[184:187], v[192:195], v[112:115]\n"
    "ds_read_b128 v[148:151], v243 offset:1024\n"
    "v_exp_f16_sdwa v234, v232 dst_sel:WORD_1 dst_unused:UNUSED_PRESERVE src0_sel:WORD_1\n"
    "v_mfma_f32_16x16x32_bf16 v[116:119], a[188:191], v[192:195], v[116:119]\n"
    "ds_read_u16 v191, v191\n"
    "v_pk_fma_f16 v234, v234, s49, s49\n"
    "v_mfma_f32_16x16x32_bf16 v[128:131], v[236:239], v[192:195], v[128:131]\n"
    "s_waitcnt lgkmcnt(8)\n"
    "v_or_b32_e32 v187, v187, v203\n"
    "v_lshlrev_b32_sdwa v201, 1, v171 dst_sel:DWORD dst_unused:UNUSED_PAD src0_sel:DWORD src1_sel:WORD_1\n"
    "v_mfma_f32_16x16x32_bf16 v[132:135], a[244:247], v[192:195], v[132:135]\n"
    "ds_read_b128 v[152:155], v243 offset:2048\n"
    "ds_read_u16_d16_hi v207, v207\n"
    "v_mfma_f32_16x16x32_bf16 v[104:107], a[176:179], v[196:199], v[104:107]\n"
    "v_cvt_pk_f16_f32 v192, v168, v169\n"
    "v_lshlrev_b32_sdwa v193, 1, v170 dst_sel:DWORD dst_unused:UNUSED_PAD src0_sel:DWORD src1_sel:WORD_1\n"
    "v_mfma_f32_16x16x32_bf16 v[108:111], a[180:183], v[196:199], v[108:111]\n"
    "ds_read_b128 v[156:159], v243 offset:3072\n"
    "v_lshlrev_b32_sdwa v194, 1, v172 dst_sel:DWORD dst_unused:UNUSED_PAD src0_sel:DWORD src1_sel:WORD_1\n"
    "v_mfma_f32_16x16x32_bf16 v[120:123], a[184:187], v[196:199], v[120:123]\n"
    "s_waitcnt lgkmcnt(8)\n"
    "ds_read_b128 v[160:163], v243 offset:4096\n"
    "buffer_store_dwordx4 a[252:255], v255, s[68:71], s39 offen offset:128 sc1\n"
    "v_mfma_f32_16x16x32_bf16 v[124:127], a[188:191], v[196:199], v[124:127]\n"
    "s_nop 0\n"
    "ds_read_b128 v[164:167], v243 offset:5120\n"
    "ds_read_b128 a[240:243], v244 offset:32768\n"
    "v_mfma_f32_16x16x32_bf16 v[136:139], v[236:239], v[196:199], v[136:139]\n"
    "s_waitcnt lgkmcnt(9)\n"
    "ds_read_u16 v193, v193\n"
    "v_or_b32_e32 v189, v189, v205\n"
    "v_mfma_f32_16x16x32_bf16 v[140:143], a[244:247], v[196:199], v[140:143]\n"
    "v_lshlrev_b32_sdwa v202, 1, v173 dst_sel:DWORD dst_unused:UNUSED_PAD src0_sel:DWORD src1_sel:WORD_1\n"
    "v_lshlrev_b32_sdwa v195, 1, v174 dst_sel:DWORD dst_unused:UNUSED_PAD src0_sel:DWORD src1_sel:WORD_1\n"
    "v_mfma_f32_16x16x32_bf16 v[168:171], a[80:83], v[144:147], 0\n"
    "ds_read_u16_d16_hi v201, v201\n"
    "v_lshlrev_b32_sdwa v203, 1, v175 dst_sel:DWORD dst_unused:UNUSED_PAD src0_sel:DWORD src1_sel:WORD_1\n"
    "s_waitcnt lgkmcnt(9)\n"
    "v_mfma_f32_16x16x32_bf16 v[168:171], a[128:131], v[148:151], v[168:171]\n"
    "v_rcp_f16_e32 v232, v234\n"
    "v_or_b32_e64 v190, v190, v206\n"
    "v_rcp_f16_sdwa v232, v234 dst_sel:WORD_1 dst_unused:UNUSED_PRESERVE src0_sel:WORD_1\n"
    "v_mfma_f32_16x16x32_bf16 v[172:175], a[84:87], v[144:147], 0\n"
    "ds_read_u16 v194, v194\n"
    "ds_read_u16_d16_hi v202, v202\n"
    "s_waitcnt lgkmcnt(9)\n"
    "v_mfma_f32_16x16x32_bf16 v[168:171], a[48:51], v[152:155], v[168:171]\n"
    "s_nop 0\n"
    "v_cvt_pk_f16_f32 v196, v176, v177\n"
    "v_lshlrev_b32_sdwa v197, 1, v178 dst_sel:DWORD dst_unused:UNUSED_PAD src0_sel:DWORD src1_sel:WORD_1\n"
    "v_lshlrev_b32_sdwa v205, 1, v179 dst_sel:DWORD dst_unused:UNUSED_PAD src0_sel:DWORD src1_sel:WORD_1\n"
    "v_lshlrev_b32_sdwa v198, 1, v180 dst_sel:DWORD dst_unused:UNUSED_PAD src0_sel:DWORD src1_sel:WORD_1\n"
    "v_lshlrev_b32_sdwa v206, 1, v181 dst_sel:DWORD dst_unused:UNUSED_PAD src0_sel:DWORD src1_sel:WORD_1\n"
    "v_lshlrev_b32_sdwa v199, 1, v182 dst_sel:DWORD dst_unused:UNUSED_PAD src0_sel:DWORD src1_sel:WORD_1\n"
    "v_fma_mix_f32 v234, v184, v232, 0 op_sel_hi:[1,1,0]\n"
    "v_mfma_f32_16x16x32_bf16 v[172:175], a[132:135], v[148:151], v[172:175]\n"
    "s_waitcnt lgkmcnt(7)\n"
    "ds_read_b128 v[236:239], v244 offset:16384\n"
    "ds_read_u16 v195, v195\n"
    "v_or_b32_e32 v191, v191, v207\n"
    "v_lshlrev_b32_sdwa v207, 1, v183 dst_sel:DWORD dst_unused:UNUSED_PAD src0_sel:DWORD src1_sel:WORD_1\n"
    "v_fma_mix_f32 v232, v184, v232, 0 op_sel:[1,1,0] op_sel_hi:[1,1,0]\n"
    "v_mfma_f32_16x16x32_bf16 v[176:179], a[80:83], v[156:159], 0\n"
    "s_waitcnt lgkmcnt(7)\n"
    "ds_read_u16_d16_hi v203, v203\n"
    "v_cvt_pk_bf16_f32 v184, v234, v232\n"
    "v_mfma_f32_16x16x32_bf16 v[176:179], a[128:131], v[160:163], v[176:179]\n"
    "s_waitcnt lgkmcnt(7)\n"
    "v_pk_fma_f16 v232, v188, v188, s48\n"
    "v_lshlrev_b32_sdwa v200, 1, v169 dst_sel:DWORD dst_unused:UNUSED_PAD src0_sel:DWORD src1_sel:WORD_1\n"
    "v_mfma_f32_16x16x32_bf16 v[172:175], a[240:243], v[152:155], v[172:175]\n"
    "s_barrier\n"
    "v_mfma_f32_16x16x32_bf16 v[176:179], a[48:51], v[164:167], v[176:179]\n"
    "s_waitcnt lgkmcnt(5)\n"
    "ds_read_b128 a[244:247], v244 offset:18432\n"
    "ds_write_b128 v253, v[64:67] offset:2048\n"
    "v_mfma_f32_16x16x32_bf16 v[180:183], a[84:87], v[156:159], 0\n"
    "v_pk_mul_f16 v232, v188, v232\n"
    "v_or_b32_e64 v193, v193, v201\n"
    "v_mfma_f32_16x16x32_bf16 v[180:183], a[132:135], v[160:163], v[180:183]\n"
    "v_exp_f16_e32 v234, v232\n"
    "v_mfma_f32_16x16x32_bf16 v[180:183], a[240:243], v[164:167], v[180:183]\n"
    "s_waitcnt lgkmcnt(4)\n"
    "ds_read_u16 v197, v197\n"
    "ds_write_b128 v253, v[68:71] offset:5120\n"
    "v_exp_f16_sdwa v234, v232 dst_sel:WORD_1 dst_unused:UNUSED_PRESERVE src0_sel:WORD_1\n"
    "v_mfma_f32_16x16x32_bf16 v[96:99], a[192:195], v[184:187], v[96:99]\n"
    "v_pk_fma_f16 v234, v234, s49, s49\n"
    "v_or_b32_e64 v194, v194, v202\n"
    "v_mfma_f32_16x16x32_bf16 v[100:103], a[196:199], v[184:187], v[100:103]\n"
    "v_rcp_f16_e32 v232, v234\n"
    "v_mfma_f32_16x16x32_bf16 v[112:115], a[200:203], v[184:187], v[112:115]\n"
    "s_waitcnt lgkmcnt(3)\n"
    "ds_read_u16_d16_hi v205, v205\n"
    "ds_write_b128 v253, v[88:91] offset:0\n"
    "v_rcp_f16_sdwa v232, v234 dst_sel:WORD_1 dst_unused:UNUSED_PRESERVE src0_sel:WORD_1\n"
    "v_mfma_f32_16x16x32_bf16 v[116:119], a[204:207], v[184:187], v[116:119]\n"
    "v_fma_mix_f32 v234, v188, v232, 0 op_sel_hi:[1,1,0]\n"
    "v_fma_mix_f32 v232, v188, v232, 0 op_sel:[1,1,0] op_sel_hi:[1,1,0]\n"
    "v_mfma_f32_16x16x32_bf16 v[128:131], v[236:239], v[184:187], v[128:131]\n"
    "v_cvt_pk_bf16_f32 v188, v234, v232\n"
    "v_pk_fma_f16 v232, v192, v192, s48\n"
    "v_mfma_f32_16x16x32_bf16 v[132:135], a[244:247], v[184:187], v[132:135]\n"
    "buffer_load_dwordx4 a[232:235], v245, s[40:43], s57 offen offset:64\n"
    "buffer_load_dwordx4 a[224:227], v245, s[40:43], s57 offen offset:0\n"
    "ds_read_u16 v198, v198\n"
    "v_mfma_f32_16x16x32_bf16 v[104:107], a[192:195], v[188:191], v[104:107]\n"
    "ds_read_u16_d16_hi v206, v206\n"
    "ds_write_b128 v253, v[92:95] offset:3072\n"
    "v_mfma_f32_16x16x32_bf16 v[108:111], a[196:199], v[188:191], v[108:111]\n"
    "v_or_b32_e64 v195, v195, v203\n"
    "v_pk_mul_f16 v232, v192, v232\n"
    "v_mfma_f32_16x16x32_bf16 v[120:123], a[200:203], v[188:191], v[120:123]\n"
    "v_exp_f16_e64 v234, v232\n"
    "v_mfma_f32_16x16x32_bf16 v[124:127], a[204:207], v[188:191], v[124:127]\n"
    "ds_read_u16 v199, v199\n"
    "ds_write_b128 v253, v[72:75] offset:1024\n"
    "v_mfma_f32_16x16x32_bf16 v[136:139], v[236:239], v[188:191], v[136:139]\n"
    "s_waitcnt lgkmcnt(5)\n"
    "v_or_b32_e32 v197, v197, v205\n"
    "v_exp_f16_sdwa v234, v232 dst_sel:WORD_1 dst_unused:UNUSED_PRESERVE src0_sel:WORD_1\n"
    "v_mfma_f32_16x16x32_bf16 v[140:143], a[244:247], v[188:191], v[140:143]\n"
    "v_pk_fma_f16 v234, v234, s49, s49\n"
    "v_lshlrev_b32_sdwa v184, 1, v168 dst_sel:DWORD dst_unused:UNUSED_PAD src0_sel:DWORD src1_sel:WORD_1\n"
    "v_lshlrev_b32_sdwa v185, 1, v170 dst_sel:DWORD dst_unused:UNUSED_PAD src0_sel:DWORD src1_sel:WORD_1\n"
    "v_lshlrev_b32_sdwa v201, 1, v171 dst_sel:DWORD dst_unused:UNUSED_PAD src0_sel:DWORD src1_sel:WORD_1\n"
    "v_mfma_f32_16x16x32_bf16 v[168:171], a[88:91], v[144:147], 0\n"
    "ds_read_u16_d16_hi v207, v207\n"
    "ds_write_b128 v253, v[76:79] offset:4096\n"
    "v_mfma_f32_16x16x32_bf16 v[168:171], a[136:139], v[148:151], v[168:171]\n"
    "v_lshlrev_b32_sdwa v186, 1, v172 dst_sel:DWORD dst_unused:UNUSED_PAD src0_sel:DWORD src1_sel:WORD_1\n"
    "v_lshlrev_b32_sdwa v202, 1, v173 dst_sel:DWORD dst_unused:UNUSED_PAD src0_sel:DWORD src1_sel:WORD_1\n"
    "v_lshlrev_b32_sdwa v187, 1, v174 dst_sel:DWORD dst_unused:UNUSED_PAD src0_sel:DWORD src1_sel:WORD_1\n"
    "v_lshlrev_b32_sdwa v203, 1, v175 dst_sel:DWORD dst_unused:UNUSED_PAD src0_sel:DWORD src1_sel:WORD_1\n"
    "v_rcp_f16_e32 v232, v234\n"
    "v_mfma_f32_16x16x32_bf16 v[168:171], a[52:55], v[152:155], v[168:171]\n"
    "s_waitcnt lgkmcnt(4)\n"
    "ds_read_b128 a[240:243], v244 offset:36864\n"
    "ds_read_u16 v184, v184\n"
    "v_or_b32_e64 v198, v198, v206\n"
    "v_rcp_f16_sdwa v232, v234 dst_sel:WORD_1 dst_unused:UNUSED_PRESERVE src0_sel:WORD_1\n"
    "v_mfma_f32_16x16x32_bf16 v[172:175], a[92:95], v[144:147], 0\n"
    "ds_read_u16_d16_hi v200, v200\n"
    "ds_read_u16 v185, v185\n"
    "v_lshlrev_b32_sdwa v188, 1, v176 dst_sel:DWORD dst_unused:UNUSED_PAD src0_sel:DWORD src1_sel:WORD_1\n"
    "v_lshlrev_b32_sdwa v204, 1, v177 dst_sel:DWORD dst_unused:UNUSED_PAD src0_sel:DWORD src1_sel:WORD_1\n"
    "v_lshlrev_b32_sdwa v189, 1, v178 dst_sel:DWORD dst_unused:UNUSED_PAD src0_sel:DWORD src1_sel:WORD_1\n"
    "v_lshlrev_b32_sdwa v205, 1, v179 dst_sel:DWORD dst_unused:UNUSED_PAD src0_sel:DWORD src1_sel:WORD_1\n"
    "v_lshlrev_b32_sdwa v190, 1, v180 dst_sel:DWORD dst_unused:UNUSED_PAD src0_sel:DWORD src1_sel:WORD_1\n"
    "v_lshlrev_b32_sdwa v206, 1, v181 dst_sel:DWORD dst_unused:UNUSED_PAD src0_sel:DWORD src1_sel:WORD_1\n"
    "v_lshlrev_b32_sdwa v191, 1, v182 dst_sel:DWORD dst_unused:UNUSED_PAD src0_sel:DWORD src1_sel:WORD_1\n"
    "v_fma_mix_f32 v234, v192, v232, 0 op_sel_hi:[1,1,0]\n"
    "v_mfma_f32_16x16x32_bf16 v[172:175], a[140:143], v[148:151], v[172:175]\n"
    "ds_read_u16_d16_hi v201, v201\n"
    "ds_read_u16 v186, v186\n"
    "buffer_load_dwordx4 a[236:239], v245, s[40:43], s88 offen offset:64\n"
    "buffer_load_dwordx4 a[228:231], v245, s[40:43], s88 offen offset:0\n"
    "v_fma_mix_f32 v232, v192, v232, 0 op_sel:[1,1,0] op_sel_hi:[1,1,0]\n"
    "v_mfma_f32_16x16x32_bf16 v[176:179], a[88:91], v[156:159], 0\n"
    "s_waitcnt lgkmcnt(6)\n"
    "ds_read_b128 v[236:239], v244 offset:20480\n"
    "ds_read_u16_d16_hi v202, v202\n"
    "v_or_b32_e32 v199, v199, v207\n"
    "v_lshlrev_b32_sdwa v207, 1, v183 dst_sel:DWORD dst_unused:UNUSED_PAD src0_sel:DWORD src1_sel:WORD_1\n"
    "v_cvt_pk_bf16_f32 v192, v234, v232\n"
    "v_mfma_f32_16x16x32_bf16 v[176:179], a[136:139], v[160:163], v[176:179]\n"
    "s_waitcnt lgkmcnt(7)\n"
    "v_pk_fma_f16 v232, v196, v196, s48\n"
    "v_mov_b32_e32 v254, 0\n"
    "v_mfma_f32_16x16x32_bf16 v[172:175], a[240:243], v[152:155], v[172:175]\n"
    "ds_read_u16 v187, v187\n"
    "v_pk_mul_f16 v232, v196, v232\n"
    "v_mfma_f32_16x16x32_bf16 v[176:179], a[52:55], v[164:167], v[176:179]\n"
    "ds_read_b128 a[244:247], v244 offset:22528\n"
    "v_exp_f16_e64 v234, v232\n"
    "v_mfma_f32_16x16x32_bf16 v[180:183], a[92:95], v[156:159], 0\n"
    "ds_read_u16_d16_hi v203, v203\n"
    "v_exp_f16_sdwa v234, v232 dst_sel:WORD_1 dst_unused:UNUSED_PRESERVE src0_sel:WORD_1\n"
    "v_mfma_f32_16x16x32_bf16 v[180:183], a[140:143], v[160:163], v[180:183]\n"
    "s_waitcnt lgkmcnt(8)\n"
    "v_pk_fma_f16 v234, v234, s49, s49\n"
    "v_or_b32_e32 v184, v184, v200\n"
    "v_mfma_f32_16x16x32_bf16 v[180:183], a[240:243], v[164:167], v[180:183]\n"
    "ds_read_u16 v188, v188\n"
    "v_rcp_f16_e32 v232, v234\n"
    "v_mfma_f32_16x16x32_bf16 v[96:99], a[208:211], v[192:195], v[96:99]\n"
    "ds_read_u16_d16_hi v204, v204\n"
    "v_rcp_f16_sdwa v232, v234 dst_sel:WORD_1 dst_unused:UNUSED_PRESERVE src0_sel:WORD_1\n"
    "v_mfma_f32_16x16x32_bf16 v[100:103], a[212:215], v[192:195], v[100:103]\n"
    "s_waitcnt lgkmcnt(8)\n"
    "v_fma_mix_f32 v234, v196, v232, 0 op_sel_hi:[1,1,0]\n"
    "v_fma_mix_f32 v232, v196, v232, 0 op_sel:[1,1,0] op_sel_hi:[1,1,0]\n"
    "v_mfma_f32_16x16x32_bf16 v[112:115], a[216:219], v[192:195], v[112:115]\n"
    "ds_read_u16 v189, v189\n"
    "v_cvt_pk_bf16_f32 v196, v234, v232\n"
    "v_mfma_f32_16x16x32_bf16 v[116:119], a[220:223], v[192:195], v[116:119]\n"
    "s_waitcnt lgkmcnt(7)\n"
    "ds_read_u16_d16_hi v205, v205\n"
    "v_or_b32_e64 v185, v185, v201\n"
    "v_mfma_f32_16x16x32_bf16 v[128:131], v[236:239], v[192:195], v[128:131]\n"
    "s_waitcnt vmcnt(21)\n"
    "v_lshlrev_b32_sdwa v201, 1, v171 dst_sel:DWORD dst_unused:UNUSED_PAD src0_sel:DWORD src1_sel:WORD_1\n"
    "v_cvt_pk_bf16_f32 v96, v96, v97\n"
    "v_mfma_f32_16x16x32_bf16 v[104:107], a[208:211], v[196:199], v[104:107]\n"
    "v_cvt_pk_bf16_f32 v97, v98, v99\n"
    "v_cvt_pk_bf16_f32 v98, v100, v101\n"
    "v_mfma_f32_16x16x32_bf16 v[108:111], a[212:215], v[196:199], v[108:111]\n"
    "s_waitcnt lgkmcnt(7)\n"
    "v_or_b32_e32 v186, v186, v202\n"
    "v_lshlrev_b32_sdwa v202, 1, v173 dst_sel:DWORD dst_unused:UNUSED_PAD src0_sel:DWORD src1_sel:WORD_1\n"
    "v_mfma_f32_16x16x32_bf16 v[120:123], a[216:219], v[196:199], v[120:123]\n"
    "s_waitcnt lgkmcnt(5)\n"
    "ds_write2_b32 v248, v0, v1 offset0:0 offset1:20\n"
    "v_mfma_f32_16x16x32_bf16 v[132:135], a[244:247], v[192:195], v[132:135]\n"
    "s_nop 0\n"
    "v_cvt_pk_f16_f32 v192, v168, v169\n"
    "v_lshlrev_b32_sdwa v193, 1, v170 dst_sel:DWORD dst_unused:UNUSED_PAD src0_sel:DWORD src1_sel:WORD_1\n"
    "v_mfma_f32_16x16x32_bf16 v[124:127], a[220:223], v[196:199], v[124:127]\n"
    "ds_read_u16 v190, v190\n"
    "ds_write2_b32 v248, v2, v3 offset0:40 offset1:60\n"
    "v_mfma_f32_16x16x32_bf16 v[136:139], v[236:239], v[196:199], v[136:139]\n"
    "s_waitcnt lgkmcnt(7)\n"
    "v_add_u32_e32 v248, 1360, v248\n"
    "v_lshlrev_b32_sdwa v194, 1, v172 dst_sel:DWORD dst_unused:UNUSED_PAD src0_sel:DWORD src1_sel:WORD_1\n"
    "v_mfma_f32_16x16x32_bf16 v[140:143], a[244:247], v[196:199], v[140:143]\n"
    "s_waitcnt vmcnt(19)\n"
    "ds_read_u16_d16_hi v206, v206\n"
    "ds_write2_b32 v235, v20, v21 offset0:0 offset1:20\n"
    "v_mfma_f32_16x16x32_bf16 v[168:171], a[96:99], v[144:147], 0\n"
    "v_lshlrev_b32_sdwa v195, 1, v174 dst_sel:DWORD dst_unused:UNUSED_PAD src0_sel:DWORD src1_sel:WORD_1\n"
    "v_cvt_pk_f16_f32 v196, v176, v177\n"
    "v_mfma_f32_16x16x32_bf16 v[168:171], a[144:147], v[148:151], v[168:171]\n"
    "s_waitcnt lgkmcnt(7)\n"
    "ds_write2_b32 v235, v22, v23 offset0:40 offset1:60\n"
    "v_mfma_f32_16x16x32_bf16 v[168:171], a[56:59], v[152:155], v[168:171]\n"
    "s_nop 0\n"
    "v_add_u32_e32 v235, 1360, v235\n"
    "v_lshlrev_b32_sdwa v197, 1, v178 dst_sel:DWORD dst_unused:UNUSED_PAD src0_sel:DWORD src1_sel:WORD_1\n"
    "ds_write2_b32 v248, v4, v5 offset0:0 offset1:20\n"
    "v_lshlrev_b32_sdwa v198, 1, v180 dst_sel:DWORD dst_unused:UNUSED_PAD src0_sel:DWORD src1_sel:WORD_1\n"
    "v_lshlrev_b32_sdwa v199, 1, v182 dst_sel:DWORD dst_unused:UNUSED_PAD src0_sel:DWORD src1_sel:WORD_1\n"
    "v_or_b32_e32 v187, v187, v203\n"
    "v_lshlrev_b32_sdwa v203, 1, v175 dst_sel:DWORD dst_unused:UNUSED_PAD src0_sel:DWORD src1_sel:WORD_1\n"
    "v_mfma_f32_16x16x32_bf16 v[172:175], a[100:103], v[144:147], 0\n"
    "s_waitcnt vmcnt(16) lgkmcnt(6)\n"
    "ds_read_u16 v191, v191\n"
    "ds_write2_b32 v248, v6, v7 offset0:40 offset1:60\n"
    "v_mfma_f32_16x16x32_bf16 v[172:175], a[148:151], v[148:151], v[172:175]\n"
    "v_add_u32_e32 v248, 1360, v248\n"
    "v_or_b32_e32 v188, v188, v204\n"
    "v_mfma_f32_16x16x32_bf16 v[172:175], a[60:63], v[152:155], v[172:175]\n"
    "s_waitcnt vmcnt(13)\n"
    "ds_read_u16_d16_hi v207, v207\n"
    "ds_write2_b32 v235, v24, v25 offset0:0 offset1:20\n"
    "v_or_b32_e64 v189, v189, v205\n"
    "v_lshlrev_b32_sdwa v205, 1, v179 dst_sel:DWORD dst_unused:UNUSED_PAD src0_sel:DWORD src1_sel:WORD_1\n"
    "v_mfma_f32_16x16x32_bf16 v[176:179], a[96:99], v[156:159], 0\n"
    "s_waitcnt lgkmcnt(5)\n"
    "v_cvt_pk_bf16_f32 v99, v102, v103\n"
    "v_or_b32_e32 v190, v190, v206\n"
    "v_mfma_f32_16x16x32_bf16 v[176:179], a[144:147], v[160:163], v[176:179]\n"
    "ds_read_u16 v193, v193\n"
    "ds_write_b128 v242, v[96:99] offset:24640\n"
    "v_mfma_f32_16x16x32_bf16 v[176:179], a[56:59], v[164:167], v[176:179]\n"
    "buffer_load_dwordx4 v[64:67], v246, s[40:43], s60 offen offset:128\n"
    "v_lshlrev_b32_sdwa v206, 1, v181 dst_sel:DWORD dst_unused:UNUSED_PAD src0_sel:DWORD src1_sel:WORD_1\n"
    "v_mfma_f32_16x16x32_bf16 v[96:99], a[0:3], v[184:187], 0\n"
    "v_cvt_pk_bf16_f32 v112, v112, v113\n"
    "v_cvt_pk_bf16_f32 v113, v114, v115\n"
    "v_mfma_f32_16x16x32_bf16 v[100:103], a[4:7], v[184:187], 0\n"
    "ds_read_u16_d16_hi v201, v201\n"
    "ds_write2_b32 v235, v26, v27 offset0:40 offset1:60\n"
    "v_add_u32_e32 v235, -1360, v235\n"
    "v_cvt_pk_bf16_f32 v114, v116, v117\n"
    "v_cvt_pk_bf16_f32 v115, v118, v119\n"
    "v_cvt_pk_bf16_f32 v128, v128, v129\n"
    "v_cvt_pk_bf16_f32 v129, v130, v131\n"
    "ds_write_b128 v242, v[112:115] offset:26688\n"
    "v_mfma_f32_16x16x32_bf16 v[112:115], a[8:11], v[184:187], 0\n"
    "s_waitcnt lgkmcnt(5)\n"
    "v_or_b32_e32 v191, v191, v207\n"
    "v_lshlrev_b32_sdwa v207, 1, v183 dst_sel:DWORD dst_unused:UNUSED_PAD src0_sel:DWORD src1_sel:WORD_1\n"
    "v_mfma_f32_16x16x32_bf16 v[180:183], a[100:103], v[156:159], 0\n"
    "ds_read_u16 v194, v194\n"
    "ds_write2_b32 v248, v8, v9 offset0:0 offset1:20\n"
    "v_mfma_f32_16x16x32_bf16 v[180:183], a[148:151], v[160:163], v[180:183]\n"
    "v_cvt_pk_bf16_f32 v130, v132, v133\n"
    "v_cvt_pk_bf16_f32 v131, v134, v135\n"
    "v_mfma_f32_16x16x32_bf16 v[180:183], a[60:63], v[164:167], v[180:183]\n"
    "ds_read_u16_d16_hi v202, v202\n"
    "ds_write_b128 v242, v[128:131] offset:28736\n"
    "v_mfma_f32_16x16x32_bf16 v[116:119], a[12:15], v[184:187], 0\n"
    "s_waitcnt lgkmcnt(5)\n"
    "v_or_b32_e32 v193, v193, v201\n"
    "v_cvt_pk_bf16_f32 v104, v104, v105\n"
    "v_cvt_pk_bf16_f32 v105, v106, v107\n"
    "v_cvt_pk_bf16_f32 v106, v108, v109\n"
    "v_cvt_pk_bf16_f32 v107, v110, v111\n"
    "v_mfma_f32_16x16x32_bf16 v[108:111], a[4:7], v[188:191], 0\n"
    "ds_read_b128 v[236:239], v244 offset:0\n"
    "ds_write_b128 v242, v[104:107] offset:25664\n"
    "v_mfma_f32_16x16x32_bf16 v[104:107], a[0:3], v[188:191], 0\n"
    "v_cvt_pk_bf16_f32 v120, v120, v121\n"
    "v_cvt_pk_bf16_f32 v121, v122, v123\n"
    "v_cvt_pk_bf16_f32 v122, v124, v125\n"
    "v_cvt_pk_bf16_f32 v123, v126, v127\n"
    "v_cvt_pk_bf16_f32 v136, v136, v137\n"
    "v_pk_fma_f16 v232, v192, v192, s48\n"
    "v_mfma_f32_16x16x32_bf16 v[124:127], a[12:15], v[188:191], 0\n"
    "ds_read_u16 v195, v195\n"
    "ds_write_b128 v242, v[120:123] offset:27712\n"
    "v_mfma_f32_16x16x32_bf16 v[120:123], a[8:11], v[188:191], 0\n"
    "v_cvt_pk_bf16_f32 v137, v138, v139\n"
    "v_cvt_pk_bf16_f32 v138, v140, v141\n"
    "s_waitcnt lgkmcnt(4)\n"
    "v_or_b32_e32 v194, v194, v202\n"
    "v_cvt_pk_bf16_f32 v139, v142, v143\n"
    "v_pk_mul_f16 v232, v192, v232\n"
    "v_lshlrev_b32_sdwa v201, 1, v171 dst_sel:DWORD dst_unused:UNUSED_PAD src0_sel:DWORD src1_sel:WORD_1\n"
    "ds_write2_b32 v248, v10, v11 offset0:40 offset1:60\n"
    "buffer_load_dwordx4 v[68:71], v246, s[40:43], s61 offen offset:128\n"
    "s_waitcnt lgkmcnt(4)\n"
    "v_mfma_f32_16x16x32_bf16 v[128:131], v[236:239], v[184:187], 0\n"
    "s_nop 0\n"
    "ds_read_u16_d16_hi v203, v203\n"
    "ds_write_b128 v242, v[136:139] offset:29760\n"
    "v_mfma_f32_16x16x32_bf16 v[136:139], v[236:239], v[188:191], 0\n"
    "v_add_u32_e32 v248, 1360, v248\n"
    "v_exp_f16_e32 v234, v232\n"
    "v_lshlrev_b32_sdwa v202, 1, v173 dst_sel:DWORD dst_unused:UNUSED_PAD src0_sel:DWORD src1_sel:WORD_1\n"
    "v_exp_f16_sdwa v234, v232 dst_sel:WORD_1 dst_unused:UNUSED_PRESERVE src0_sel:WORD_1\n"
    "s_waitcnt vmcnt(12)\n"
    "ds_write2_b32 v248, v12, v13 offset0:0 offset1:20\n"
    "v_pk_fma_f16 v234, v234, s49, s49\n"
    "v_rcp_f16_e64 v232, v234\n"
    "ds_read_b128 a[244:247], v244 offset:2048\n"
    "ds_write2_b32 v248, v14, v15 offset0:40 offset1:60\n"
    "v_add_u32_e32 v248, 1360, v248\n"
    "v_rcp_f16_sdwa v232, v234 dst_sel:WORD_1 dst_unused:UNUSED_PRESERVE src0_sel:WORD_1\n"
    "ds_read_u16 v197, v197\n"
    "s_waitcnt vmcnt(9)\n"
    "ds_write2_b32 v248, v16, v17 offset0:0 offset1:20\n"
    "v_fma_mix_f32 v234, v192, v232, 0 op_sel_hi:[1,1,0]\n"
    "s_waitcnt lgkmcnt(5)\n"
    "v_or_b32_e64 v195, v195, v203\n"
    "v_lshlrev_b32_sdwa v203, 1, v175 dst_sel:DWORD dst_unused:UNUSED_PAD src0_sel:DWORD src1_sel:WORD_1\n"
    "ds_read_u16_d16_hi v205, v205\n"
    "ds_write2_b32 v248, v18, v19 offset0:40 offset1:60\n"
    "v_mfma_f32_32x32x1_2b_f32 v[0:31], v254, v254, 0\n"
    "v_add_u32_e32 v248, -5440, v248\n"
    "v_fma_mix_f32 v232, v192, v232, 0 op_sel:[1,1,0] op_sel_hi:[1,1,0]\n"
    "ds_read_u16 v198, v198\n"
    "ds_read_u16_d16_hi v206, v206\n"
    "ds_read_u16 v199, v199\n"
    "v_cvt_pk_bf16_f32 v192, v234, v232\n"
    "ds_read_u16_d16_hi v207, v207\n"
    "v_pk_fma_f16 v232, v196, v196, s48\n"
    "v_pk_mul_f16 v232, v196, v232\n"
    "v_exp_f16_e32 v234, v232\n"
    "s_waitcnt lgkmcnt(8)\n"
    "v_mfma_f32_16x16x32_bf16 v[132:135], a[244:247], v[184:187], 0\n"
    "ds_read_b128 v[216:219], v251 offset:0\n"
    "ds_read_b128 v[220:223], v251 offset:336\n"
    "v_mfma_f32_16x16x32_bf16 v[140:143], a[244:247], v[188:191], 0\n"
    "v_cvt_pk_f16_f32 v184, v168, v169\n"
    "v_lshlrev_b32_sdwa v185, 1, v170 dst_sel:DWORD dst_unused:UNUSED_PAD src0_sel:DWORD src1_sel:WORD_1\n"
    "v_mfma_f32_16x16x32_bf16 v[168:171], a[104:107], v[144:147], 0\n"
    "ds_read_u16 v185, v185\n"
    "v_lshlrev_b32_sdwa v186, 1, v172 dst_sel:DWORD dst_unused:UNUSED_PAD src0_sel:DWORD src1_sel:WORD_1\n"
    "v_mfma_f32_16x16x32_bf16 v[168:171], a[152:155], v[148:151], v[168:171]\n"
    "s_waitcnt lgkmcnt(7)\n"
    "ds_read_u16_d16_hi v201, v201\n"
    "v_or_b32_e32 v197, v197, v205\n"
    "v_mfma_f32_16x16x32_bf16 v[168:171], a[64:67], v[152:155], v[168:171]\n"
    "s_barrier\n"
    "buffer_load_dwordx4 v[72:75], v246, s[40:43], s62 offen offset:128\n"
    "ds_read_b128 v[208:211], v250 offset:0\n"
    "v_lshlrev_b32_sdwa v187, 1, v174 dst_sel:DWORD dst_unused:UNUSED_PAD src0_sel:DWORD src1_sel:WORD_1\n"
    "v_mfma_f32_16x16x32_bf16 v[172:175], a[108:111], v[144:147], 0\n"
    "s_waitcnt lgkmcnt(7)\n"
    "ds_read_b128 v[212:215], v250 offset:320\n"
    "ds_read_u16 v186, v186\n"
    "v_mfma_f32_16x16x32_bf16 v[172:175], a[156:159], v[148:151], v[172:175]\n"
    "ds_read_u16_d16_hi v202, v202\n"
    "v_or_b32_e64 v198, v198, v206\n"
    "v_mfma_f32_16x16x32_bf16 v[172:175], a[68:71], v[152:155], v[172:175]\n"
    "ds_read_b128 v[236:239], v244 offset:4096\n"
    "v_cvt_pk_f16_f32 v188, v176, v177\n"
    "v_lshlrev_b32_sdwa v189, 1, v178 dst_sel:DWORD dst_unused:UNUSED_PAD src0_sel:DWORD src1_sel:WORD_1\n"
    "v_lshlrev_b32_sdwa v205, 1, v179 dst_sel:DWORD dst_unused:UNUSED_PAD src0_sel:DWORD src1_sel:WORD_1\n"
    "v_mfma_f32_16x16x32_bf16 v[176:179], a[104:107], v[156:159], 0\n"
    "s_waitcnt lgkmcnt(8)\n"
    "ds_read_u16 v187, v187\n"
    "v_lshlrev_b32_sdwa v190, 1, v180 dst_sel:DWORD dst_unused:UNUSED_PAD src0_sel:DWORD src1_sel:WORD_1\n"
    "v_mfma_f32_16x16x32_bf16 v[176:179], a[152:155], v[160:163], v[176:179]\n"
    "s_nop 0\n"
    "v_lshlrev_b32_sdwa v206, 1, v181 dst_sel:DWORD dst_unused:UNUSED_PAD src0_sel:DWORD src1_sel:WORD_1\n"
    "v_lshlrev_b32_sdwa v191, 1, v182 dst_sel:DWORD dst_unused:UNUSED_PAD src0_sel:DWORD src1_sel:WORD_1\n"
    "v_mfma_f32_16x16x32_bf16 v[176:179], a[64:67], v[164:167], v[176:179]\n"
    "ds_read_u16_d16_hi v203, v203\n"
    "v_or_b32_e64 v199, v199, v207\n"
    "v_lshlrev_b32_sdwa v207, 1, v183 dst_sel:DWORD dst_unused:UNUSED_PAD src0_sel:DWORD src1_sel:WORD_1\n"
    "v_mfma_f32_16x16x32_bf16 v[180:183], a[108:111], v[156:159], 0\n"
    "ds_read_b128 a[244:247], v244 offset:6144\n"
    "v_exp_f16_sdwa v234, v232 dst_sel:WORD_1 dst_unused:UNUSED_PRESERVE src0_sel:WORD_1\n"
    "v_mfma_f32_16x16x32_bf16 v[180:183], a[156:159], v[160:163], v[180:183]\n"
    "ds_read_u16 v189, v189\n"
    "v_pk_fma_f16 v234, v234, s49, s49\n"
    "v_mfma_f32_16x16x32_bf16 v[180:183], a[68:71], v[164:167], v[180:183]\n"
    "s_waitcnt lgkmcnt(8)\n"
    "v_rcp_f16_e64 v232, v234\n"
    "v_mfma_f32_16x16x32_bf16 v[96:99], a[16:19], v[192:195], v[96:99]\n"
    "s_waitcnt lgkmcnt(7)\n"
    "ds_read_u16_d16_hi v205, v205\n"
    "v_rcp_f16_sdwa v232, v234 dst_sel:WORD_1 dst_unused:UNUSED_PRESERVE src0_sel:WORD_1\n"
    "v_smfmac_f32_16x16x64_bf16 v[0:3], v[216:219], v[208:215], v240\n"
    "ds_read_b128 v[216:219], v252 offset:0\n"
    "v_smfmac_f32_16x16x64_bf16 v[4:7], v[220:223], v[208:215], v240 abid:1\n"
    "ds_read_b128 v[208:211], v250 offset:640\n"
    "v_mfma_f32_16x16x32_bf16 v[100:103], a[20:23], v[192:195], v[100:103]\n"
    "ds_read_b128 v[212:215], v250 offset:960\n"
    "v_or_b32_e32 v185, v185, v201\n"
    "v_mfma_f32_16x16x32_bf16 v[112:115], a[24:27], v[192:195], v[112:115]\n"
    "s_waitcnt lgkmcnt(8)\n"
    "ds_read_b128 v[220:223], v252 offset:336\n"
    "v_fma_mix_f32 v234, v196, v232, 0 op_sel_hi:[1,1,0]\n"
    "v_mfma_f32_16x16x32_bf16 v[116:119], a[28:31], v[192:195], v[116:119]\n"
    "ds_read_u16 v190, v190\n"
    "v_fma_mix_f32 v232, v196, v232, 0 op_sel:[1,1,0] op_sel_hi:[1,1,0]\n"
    "v_mfma_f32_16x16x32_bf16 v[128:131], v[236:239], v[192:195], v[128:131]\n"
    "v_or_b32_e64 v186, v186, v202\n"
    "v_cvt_pk_bf16_f32 v196, v234, v232\n"
    "v_pk_fma_f16 v232, v184, v184, s48\n"
    "v_lshlrev_b32_sdwa v200, 1, v169 dst_sel:DWORD dst_unused:UNUSED_PAD src0_sel:DWORD src1_sel:WORD_1\n"
    "v_mfma_f32_16x16x32_bf16 v[104:107], a[16:19], v[196:199], v[104:107]\n"
    "ds_read_u16_d16_hi v206, v206\n"
    "v_pk_mul_f16 v232, v184, v232\n"
    "v_mfma_f32_16x16x32_bf16 v[108:111], a[20:23], v[196:199], v[108:111]\n"
    "s_waitcnt lgkmcnt(8)\n"
    "ds_read_u16 v191, v191\n"
    "v_or_b32_e32 v187, v187, v203\n"
    "v_mfma_f32_16x16x32_bf16 v[132:135], a[244:247], v[192:195], v[132:135]\n"
    "v_exp_f16_e64 v234, v232\n"
    "v_mfma_f32_16x16x32_bf16 v[120:123], a[24:27], v[196:199], v[120:123]\n"
    "buffer_load_dwordx4 v[76:79], v246, s[40:43], s63 offen offset:128\n"
    "ds_read_u16_d16_hi v207, v207\n"
    "v_mfma_f32_16x16x32_bf16 v[124:127], a[28:31], v[196:199], v[124:127]\n"
    "v_exp_f16_sdwa v234, v232 dst_sel:WORD_1 dst_unused:UNUSED_PRESERVE src0_sel:WORD_1\n"
    "v_mfma_f32_16x16x32_bf16 v[136:139], v[236:239], v[196:199], v[136:139]\n"
    "ds_read_b128 a[240:243], v244 offset:34816\n"
    "ds_read_u16_d16_hi v200, v200\n"
    "v_mfma_f32_16x16x32_bf16 v[140:143], a[244:247], v[196:199], v[140:143]\n"
    "s_waitcnt lgkmcnt(9)\n"
    "v_or_b32_e32 v189, v189, v205\n"
    "v_pk_fma_f16 v234, v234, s49, s49\n"
    "v_lshlrev_b32_sdwa v192, 1, v168 dst_sel:DWORD dst_unused:UNUSED_PAD src0_sel:DWORD src1_sel:WORD_1\n"
    "v_lshlrev_b32_sdwa v193, 1, v170 dst_sel:DWORD dst_unused:UNUSED_PAD src0_sel:DWORD src1_sel:WORD_1\n"
    "v_lshlrev_b32_sdwa v201, 1, v171 dst_sel:DWORD dst_unused:UNUSED_PAD src0_sel:DWORD src1_sel:WORD_1\n"
    "v_mfma_f32_16x16x32_bf16 v[168:171], a[112:115], v[144:147], 0\n"
    "s_waitcnt lgkmcnt(7)\n"
    "ds_read_u16 v192, v192\n"
    "v_lshlrev_b32_sdwa v194, 1, v172 dst_sel:DWORD dst_unused:UNUSED_PAD src0_sel:DWORD src1_sel:WORD_1\n"
    "v_smfmac_f32_16x16x64_bf16 v[0:3], v[216:219], v[208:215], v240\n"
    "s_waitcnt lgkmcnt(7)\n"
    "ds_read_b128 v[216:219], v251 offset:672\n"
    "v_smfmac_f32_16x16x64_bf16 v[4:7], v[220:223], v[208:215], v240 abid:1\n"
    "ds_read_b128 v[208:211], v250 offset:80\n"
    "v_mfma_f32_16x16x32_bf16 v[168:171], a[160:163], v[148:151], v[168:171]\n"
    "ds_read_b128 v[212:215], v250 offset:400\n"
    "v_lshlrev_b32_sdwa v202, 1, v173 dst_sel:DWORD dst_unused:UNUSED_PAD src0_sel:DWORD src1_sel:WORD_1\n"
    "v_lshlrev_b32_sdwa v195, 1, v174 dst_sel:DWORD dst_unused:UNUSED_PAD src0_sel:DWORD src1_sel:WORD_1\n"
    "v_lshlrev_b32_sdwa v203, 1, v175 dst_sel:DWORD dst_unused:UNUSED_PAD src0_sel:DWORD src1_sel:WORD_1\n"
    "v_rcp_f16_e64 v232, v234\n"
    "v_pk_mov_b32 v[224:225], v[0:1], v[0:1] op_sel:[0,1]\n"
    "v_pk_mov_b32 v[226:227], v[2:3], v[2:3] op_sel:[0,1]\n"
    "v_pk_mul_f32 v[228:229], v[0:1], v[0:1]\n"
    "v_pk_mul_f32 v[230:231], v[2:3], v[2:3]\n"
    "v_pk_add_f32 v[224:225], v[224:225], v[4:5]\n"
    "v_pk_add_f32 v[226:227], v[226:227], v[6:7]\n"
    "v_pk_fma_f32 v[228:229], v[4:5], v[4:5], v[228:229]\n"
    "v_pk_fma_f32 v[230:231], v[6:7], v[6:7], v[230:231]\n"
    "v_mfma_f32_16x16x32_bf16 v[168:171], a[72:75], v[152:155], v[168:171]\n"
    "s_waitcnt lgkmcnt(7)\n"
    "ds_read_b128 v[220:223], v251 offset:1008\n"
    "ds_read_u16 v193, v193\n"
    "v_or_b32_e64 v190, v190, v206\n"
    "v_rcp_f16_sdwa v232, v234 dst_sel:WORD_1 dst_unused:UNUSED_PRESERVE src0_sel:WORD_1\n"
    "v_mfma_f32_16x16x32_bf16 v[172:175], a[116:119], v[144:147], 0\n"
    "s_waitcnt lgkmcnt(7)\n"
    "ds_read_u16_d16_hi v201, v201\n"
    "ds_read_u16 v194, v194\n"
    "v_lshlrev_b32_sdwa v196, 1, v176 dst_sel:DWORD dst_unused:UNUSED_PAD src0_sel:DWORD src1_sel:WORD_1\n"
    "v_lshlrev_b32_sdwa v204, 1, v177 dst_sel:DWORD dst_unused:UNUSED_PAD src0_sel:DWORD src1_sel:WORD_1\n"
    "v_lshlrev_b32_sdwa v197, 1, v178 dst_sel:DWORD dst_unused:UNUSED_PAD src0_sel:DWORD src1_sel:WORD_1\n"
    "v_lshlrev_b32_sdwa v205, 1, v179 dst_sel:DWORD dst_unused:UNUSED_PAD src0_sel:DWORD src1_sel:WORD_1\n"
    "v_lshlrev_b32_sdwa v198, 1, v180 dst_sel:DWORD dst_unused:UNUSED_PAD src0_sel:DWORD src1_sel:WORD_1\n"
    "v_lshlrev_b32_sdwa v206, 1, v181 dst_sel:DWORD dst_unused:UNUSED_PAD src0_sel:DWORD src1_sel:WORD_1\n"
    "v_lshlrev_b32_sdwa v199, 1, v182 dst_sel:DWORD dst_unused:UNUSED_PAD src0_sel:DWORD src1_sel:WORD_1\n"
    "v_fma_mix_f32 v234, v184, v232, 0 op_sel_hi:[1,1,0]\n"
    "v_mfma_f32_16x16x32_bf16 v[172:175], a[164:167], v[148:151], v[172:175]\n"
    "ds_read_b128 v[236:239], v244 offset:8192\n"
    "ds_read_u16_d16_hi v202, v202\n"
    "v_mfma_f32_16x16x32_bf16 v[172:175], a[240:243], v[152:155], v[172:175]\n"
    "v_or_b32_e64 v191, v191, v207\n"
    "v_lshlrev_b32_sdwa v207, 1, v183 dst_sel:DWORD dst_unused:UNUSED_PAD src0_sel:DWORD src1_sel:WORD_1\n"
    "v_fma_mix_f32 v232, v184, v232, 0 op_sel:[1,1,0] op_sel_hi:[1,1,0]\n"
    "v_mfma_f32_16x16x32_bf16 v[176:179], a[112:115], v[156:159], 0\n"
    "s_waitcnt vmcnt(5) lgkmcnt(7)\n"
    "ds_read_u16 v195, v195\n"
    "v_cvt_pk_bf16_f32 v184, v234, v232\n"
    "v_smfmac_f32_32x32x32_bf16 v[112:127], a[248:251], a[232:239], v241\n"
    "s_waitcnt vmcnt(4) lgkmcnt(7)\n"
    "ds_read_b128 a[232:235], v243 offset:24640\n"
    "v_pk_fma_f16 v232, v188, v188, s48\n"
    "v_or_b32_e64 v192, v192, v200\n"
    "v_pk_mul_f16 v232, v188, v232\n"
    "v_exp_f16_e32 v234, v232\n"
    "v_smfmac_f32_16x16x64_bf16 v[8:11], v[216:219], v[208:215], v240\n"
    "s_waitcnt lgkmcnt(7)\n"
    "ds_read_b128 v[216:219], v252 offset:672\n"
    "ds_read_b128 a[236:239], v243 offset:25664\n"
    "v_smfmac_f32_16x16x64_bf16 v[12:15], v[220:223], v[208:215], v240 abid:1\n"
    "ds_read_b128 v[208:211], v250 offset:720\n"
    "ds_read_b128 v[212:215], v250 offset:1040\n"
    "v_smfmac_f32_32x32x32_bf16 v[96:111], a[248:251], a[224:231], v241\n"
    "ds_read_b128 v[220:223], v252 offset:1008\n"
    "buffer_load_dwordx4 v[80:83], v246, s[40:43], s52 offen offset:128\n"
    "v_exp_f16_sdwa v234, v232 dst_sel:WORD_1 dst_unused:UNUSED_PRESERVE src0_sel:WORD_1\n"
    "ds_read_b128 a[244:247], v244 offset:10240\n"
    "ds_read_u16_d16_hi v203, v203\n"
    "v_mfma_f32_16x16x32_bf16 v[176:179], a[160:163], v[160:163], v[176:179]\n"
    "s_waitcnt lgkmcnt(10)\n"
    "v_pk_fma_f16 v234, v234, s49, s49\n"
    "v_or_b32_e32 v193, v193, v201\n"
    "v_mfma_f32_16x16x32_bf16 v[176:179], a[72:75], v[164:167], v[176:179]\n"
    "ds_read_u16 v196, v196\n"
    "v_rcp_f16_e32 v232, v234\n"
    "v_mfma_f32_16x16x32_bf16 v[180:183], a[116:119], v[156:159], 0\n"
    "v_rcp_f16_sdwa v232, v234 dst_sel:WORD_1 dst_unused:UNUSED_PRESERVE src0_sel:WORD_1\n"
    "v_mfma_f32_16x16x32_bf16 v[180:183], a[164:167], v[160:163], v[180:183]\n"
    "s_waitcnt lgkmcnt(10)\n"
    "ds_read_u16_d16_hi v204, v204\n"
    "v_fma_mix_f32 v234, v188, v232, 0 op_sel_hi:[1,1,0]\n"
    "v_mfma_f32_16x16x32_bf16 v[180:183], a[240:243], v[164:167], v[180:183]\n"
    "v_fma_mix_f32 v232, v188, v232, 0 op_sel:[1,1,0] op_sel_hi:[1,1,0]\n"
    "v_lshlrev_b32_sdwa v201, 1, v171 dst_sel:DWORD dst_unused:UNUSED_PAD src0_sel:DWORD src1_sel:WORD_1\n"
    "v_mfma_f32_16x16x32_bf16 v[112:115], a[40:43], v[184:187], v[112:115]\n"
    "ds_read_u16 v197, v197\n"
    "v_cvt_pk_bf16_f32 v188, v234, v232\n"
    "v_mfma_f32_16x16x32_bf16 v[116:119], a[44:47], v[184:187], v[116:119]\n"
    "s_waitcnt lgkmcnt(8)\n"
    "ds_read_u16_d16_hi v205, v205\n"
    "v_or_b32_e32 v194, v194, v202\n"
    "v_smfmac_f32_32x32x32_bf16 v[96:111], a[248:251], a[232:239], v241\n"
    "buffer_load_dwordx4 a[224:227], v245, s[40:43], s57 offen offset:128\n"
    "v_lshlrev_b32_sdwa v202, 1, v173 dst_sel:DWORD dst_unused:UNUSED_PAD src0_sel:DWORD src1_sel:WORD_1\n"
    "ds_read_u16 v198, v198\n"
    "v_mfma_f32_16x16x32_bf16 v[128:131], v[236:239], v[184:187], v[128:131]\n"
    "s_waitcnt lgkmcnt(7)\n"
    "v_smfmac_f32_16x16x64_bf16 v[8:11], v[216:219], v[208:215], v240\n"
    "s_waitcnt lgkmcnt(6)\n"
    "ds_read_b128 v[216:219], v251 offset:1344\n"
    "ds_read_u16_d16_hi v206, v206\n"
    "v_smfmac_f32_16x16x64_bf16 v[12:15], v[220:223], v[208:215], v240 abid:1\n"
    "v_mfma_f32_16x16x32_bf16 v[96:99], a[32:35], v[184:187], v[96:99]\n"
    "ds_read_b128 v[208:211], v250 offset:160\n"
    "v_mfma_f32_16x16x32_bf16 v[100:103], a[36:39], v[184:187], v[100:103]\n"
    "s_waitcnt lgkmcnt(8)\n"
    "ds_read_b128 v[212:215], v250 offset:480\n"
    "v_or_b32_e32 v195, v195, v203\n"
    "v_lshlrev_b32_sdwa v203, 1, v175 dst_sel:DWORD dst_unused:UNUSED_PAD src0_sel:DWORD src1_sel:WORD_1\n"
    "v_mfma_f32_16x16x32_bf16 v[132:135], a[244:247], v[184:187], v[132:135]\n"
    "ds_read_b128 v[220:223], v251 offset:1680\n"
    "v_cvt_pk_f16_f32 v184, v168, v169\n"
    "v_lshlrev_b32_sdwa v185, 1, v170 dst_sel:DWORD dst_unused:UNUSED_PAD src0_sel:DWORD src1_sel:WORD_1\n"
    "v_lshlrev_b32_sdwa v186, 1, v172 dst_sel:DWORD dst_unused:UNUSED_PAD src0_sel:DWORD src1_sel:WORD_1\n"
    "v_lshlrev_b32_sdwa v187, 1, v174 dst_sel:DWORD dst_unused:UNUSED_PAD src0_sel:DWORD src1_sel:WORD_1\n"
    "v_pk_add_f32 v[224:225], v[224:225], v[8:9]\n"
    "v_pk_add_f32 v[226:227], v[226:227], v[10:11]\n"
    "v_pk_fma_f32 v[228:229], v[8:9], v[8:9], v[228:229]\n"
    "v_pk_fma_f32 v[230:231], v[10:11], v[10:11], v[230:231]\n"
    "v_pk_add_f32 v[224:225], v[224:225], v[12:13]\n"
    "v_pk_add_f32 v[226:227], v[226:227], v[14:15]\n"
    "v_pk_fma_f32 v[228:229], v[12:13], v[12:13], v[228:229]\n"
    "v_pk_fma_f32 v[230:231], v[14:15], v[14:15], v[230:231]\n"
    "v_mfma_f32_16x16x32_bf16 v[104:107], a[32:35], v[188:191], v[104:107]\n"
    "s_waitcnt lgkmcnt(7)\n"
    "buffer_load_dwordx4 a[228:231], v245, s[40:43], s88 offen offset:128\n"
    "ds_read_u16 v199, v199\n"
    "v_mfma_f32_16x16x32_bf16 v[108:111], a[36:39], v[188:191], v[108:111]\n"
    "s_nop 0\n"
    "ds_read_u16_d16_hi v207, v207\n"
    "ds_read_b128 a[232:235], v243 offset:26688\n"
    "v_mfma_f32_16x16x32_bf16 v[120:123], a[40:43], v[188:191], v[120:123]\n"
    "s_waitcnt lgkmcnt(9)\n"
    "ds_read_b128 a[236:239], v243 offset:27712\n"
    "v_or_b32_e32 v196, v196, v204\n"
    "v_mfma_f32_16x16x32_bf16 v[124:127], a[44:47], v[188:191], v[124:127]\n"
    "ds_read_b128 a[240:243], v244 offset:38912\n"
    "v_or_b32_e64 v197, v197, v205\n"
    "v_mfma_f32_16x16x32_bf16 v[136:139], v[236:239], v[188:191], v[136:139]\n"
    "ds_read_u16 v185, v185\n"
    "v_lshlrev_b32_sdwa v205, 1, v179 dst_sel:DWORD dst_unused:UNUSED_PAD src0_sel:DWORD src1_sel:WORD_1\n"
    "v_mfma_f32_16x16x32_bf16 v[140:143], a[244:247], v[188:191], v[140:143]\n"
    "v_cvt_pk_f16_f32 v188, v176, v177\n"
    "v_lshlrev_b32_sdwa v189, 1, v178 dst_sel:DWORD dst_unused:UNUSED_PAD src0_sel:DWORD src1_sel:WORD_1\n"
    "v_mfma_f32_16x16x32_bf16 v[168:171], a[120:123], v[144:147], 0\n"
    "ds_read_u16_d16_hi v201, v201\n"
    "v_lshlrev_b32_sdwa v190, 1, v180 dst_sel:DWORD dst_unused:UNUSED_PAD src0_sel:DWORD src1_sel:WORD_1\n"
    "v_mfma_f32_16x16x32_bf16 v[168:171], a[168:171], v[148:151], v[168:171]\n"
    "s_waitcnt lgkmcnt(8)\n"
    "ds_read_u16 v186, v186\n"
    "v_lshlrev_b32_sdwa v191, 1, v182 dst_sel:DWORD dst_unused:UNUSED_PAD src0_sel:DWORD src1_sel:WORD_1\n"
    "v_smfmac_f32_16x16x64_bf16 v[16:19], v[216:219], v[208:215], v240\n"
    "s_waitcnt lgkmcnt(8)\n"
    "v_or_b32_e64 v198, v198, v206\n"
    "v_smfmac_f32_16x16x64_bf16 v[20:23], v[220:223], v[208:215], v240 abid:1\n"
    "ds_read_b128 v[208:211], v250 offset:800\n"
    "buffer_load_dwordx4 v[84:87], v247, s[40:43], s60 offen offset:128\n"
    "v_mfma_f32_16x16x32_bf16 v[168:171], a[76:79], v[152:155], v[168:171]\n"
    "ds_read_b128 v[212:215], v250 offset:1120\n"
    "v_lshlrev_b32_sdwa v206, 1, v181 dst_sel:DWORD dst_unused:UNUSED_PAD src0_sel:DWORD src1_sel:WORD_1\n"
    "v_mfma_f32_16x16x32_bf16 v[172:175], a[124:127], v[144:147], 0\n"
    "ds_read_b128 v[216:219], v252 offset:1344\n"
    "v_pk_fma_f16 v232, v184, v184, s48\n"
    "v_mfma_f32_16x16x32_bf16 v[172:175], a[172:175], v[148:151], v[172:175]\n"
    "ds_read_b128 v[220:223], v252 offset:1680\n"
    "v_pk_mul_f16 v232, v184, v232\n"
    "v_mfma_f32_16x16x32_bf16 v[176:179], a[120:123], v[156:159], 0\n"
    "s_waitcnt lgkmcnt(9)\n"
    "ds_read_u16_d16_hi v202, v202\n"
    "v_or_b32_e64 v199, v199, v207\n"
    "v_mfma_f32_16x16x32_bf16 v[176:179], a[168:171], v[160:163], v[176:179]\n"
    "s_waitcnt lgkmcnt(9)\n"
    "v_lshlrev_b32_sdwa v207, 1, v183 dst_sel:DWORD dst_unused:UNUSED_PAD src0_sel:DWORD src1_sel:WORD_1\n"
    "v_exp_f16_e32 v234, v232\n"
    "v_smfmac_f32_32x32x32_bf16 v[112:127], a[248:251], a[232:239], v241\n"
    "s_waitcnt lgkmcnt(8)\n"
    "ds_read_b128 v[236:239], v244 offset:12288\n"
    "ds_read_u16 v187, v187\n"
    "v_exp_f16_sdwa v234, v232 dst_sel:WORD_1 dst_unused:UNUSED_PRESERVE src0_sel:WORD_1\n"
    "v_mfma_f32_16x16x32_bf16 v[172:175], a[240:243], v[152:155], v[172:175]\n"
    "ds_read_u16_d16_hi v203, v203\n"
    "v_pk_fma_f16 v234, v234, s49, s49\n"
    "v_mfma_f32_16x16x32_bf16 v[176:179], a[76:79], v[164:167], v[176:179]\n"
    "v_rcp_f16_e32 v232, v234\n"
    "v_mfma_f32_16x16x32_bf16 v[180:183], a[124:127], v[156:159], 0\n"
    "s_waitcnt lgkmcnt(9)\n"
    "ds_read_b128 a[244:247], v244 offset:14336\n"
    "v_or_b32_e64 v185, v185, v201\n"
    "v_mfma_f32_16x16x32_bf16 v[180:183], a[172:175], v[160:163], v[180:183]\n"
    "ds_read_u16 v189, v189\n"
    "v_lshlrev_b32_e64 v254, 3, v244\n"
    "v_mfma_f32_16x16x32_bf16 v[180:183], a[240:243], v[164:167], v[180:183]\n"
    "v_lshlrev_b32_sdwa v201, 1, v171 dst_sel:DWORD dst_unused:UNUSED_PAD src0_sel:DWORD src1_sel:WORD_1\n"
    "v_rcp_f16_sdwa v232, v234 dst_sel:WORD_1 dst_unused:UNUSED_PRESERVE src0_sel:WORD_1\n"
    "v_mfma_f32_16x16x32_bf16 v[96:99], a[176:179], v[192:195], v[96:99]\n"
    "s_waitcnt lgkmcnt(7)\n"
    "ds_read_b128 a[232:235], v243 offset:28736\n"
    "ds_read_u16_d16_hi v205, v205\n"
    "v_smfmac_f32_16x16x64_bf16 v[16:19], v[216:219], v[208:215], v240\n"
    "s_waitcnt lgkmcnt(8)\n"
    "v_fma_mix_f32 v234, v184, v232, 0 op_sel_hi:[1,1,0]\n"
    "v_smfmac_f32_16x16x64_bf16 v[20:23], v[220:223], v[208:215], v240 abid:1\n"
    "ds_read_b128 v[208:211], v250 offset:240\n"
    "v_mfma_f32_16x16x32_bf16 v[100:103], a[180:183], v[192:195], v[100:103]\n"
    "ds_read_b128 v[212:215], v250 offset:560\n"
    "buffer_load_dwordx4 v[88:91], v247, s[40:43], s61 offen offset:128\n"
    "v_mfma_f32_16x16x32_bf16 v[112:115], a[184:187], v[192:195], v[112:115]\n"
    "s_waitcnt lgkmcnt(8)\n"
    "ds_read_b128 v[216:219], v251 offset:2016\n"
    "v_or_b32_e32 v186, v186, v202\n"
    "v_lshlrev_b32_sdwa v202, 1, v173 dst_sel:DWORD dst_unused:UNUSED_PAD src0_sel:DWORD src1_sel:WORD_1\n"
    "ds_read_b128 v[220:223], v251 offset:2352\n"
    "v_pk_add_f32 v[224:225], v[224:225], v[16:17]\n"
    "v_pk_add_f32 v[226:227], v[226:227], v[18:19]\n"
    "v_pk_fma_f32 v[228:229], v[16:17], v[16:17], v[228:229]\n"
    "v_pk_fma_f32 v[230:231], v[18:19], v[18:19], v[230:231]\n"
    "v_pk_add_f32 v[224:225], v[224:225], v[20:21]\n"
    "v_pk_add_f32 v[226:227], v[226:227], v[22:23]\n"
    "v_pk_fma_f32 v[228:229], v[20:21], v[20:21], v[228:229]\n"
    "v_pk_fma_f32 v[230:231], v[22:23], v[22:23], v[230:231]\n"
    "v_mfma_f32_16x16x32_bf16 v[116:119], a[188:191], v[192:195], v[116:119]\n"
    "ds_read_b128 a[236:239], v243 offset:29760\n"
    "ds_read_u16 v190, v190\n"
    "v_mfma_f32_16x16x32_bf16 v[128:131], v[236:239], v[192:195], v[128:131]\n"
    "s_waitcnt lgkmcnt(9)\n"
    "ds_read_b128 v[144:147], v242 offset:0\n"
    "v_or_b32_e32 v187, v187, v203\n"
    "v_mfma_f32_16x16x32_bf16 v[132:135], a[244:247], v[192:195], v[132:135]\n"
    "ds_read_u16_d16_hi v206, v206\n"
    "v_cvt_pk_f16_f32 v192, v168, v169\n"
    "v_mfma_f32_16x16x32_bf16 v[104:107], a[176:179], v[196:199], v[104:107]\n"
    "v_lshlrev_b32_sdwa v193, 1, v170 dst_sel:DWORD dst_unused:UNUSED_PAD src0_sel:DWORD src1_sel:WORD_1\n"
    "v_lshlrev_b32_sdwa v194, 1, v172 dst_sel:DWORD dst_unused:UNUSED_PAD src0_sel:DWORD src1_sel:WORD_1\n"
    "v_mfma_f32_16x16x32_bf16 v[108:111], a[180:183], v[196:199], v[108:111]\n"
    "ds_read_b128 v[148:151], v242 offset:1024\n"
    "ds_read_u16 v191, v191\n"
    "v_mfma_f32_16x16x32_bf16 v[120:123], a[184:187], v[196:199], v[120:123]\n"
    "s_mov_b32 exec_hi, 0\n"
    "buffer_load_dword v233, v254, s[72:75], s53 offen sc1\n"
    "s_mov_b32 exec_hi, -1\n"
    "v_mfma_f32_16x16x32_bf16 v[124:127], a[188:191], v[196:199], v[124:127]\n"
    "s_waitcnt lgkmcnt(9)\n"
    "ds_read_b128 v[152:155], v242 offset:2048\n"
    "v_or_b32_e32 v189, v189, v205\n"
    "v_mfma_f32_16x16x32_bf16 v[136:139], v[236:239], v[196:199], v[136:139]\n"
    "s_nop 0\n"
    "ds_read_u16_d16_hi v207, v207\n"
    "v_lshlrev_b32_sdwa v195, 1, v174 dst_sel:DWORD dst_unused:UNUSED_PAD src0_sel:DWORD src1_sel:WORD_1\n"
    "v_mfma_f32_16x16x32_bf16 v[140:143], a[244:247], v[196:199], v[140:143]\n"
    "s_waitcnt lgkmcnt(9)\n"
    "v_lshlrev_b32_sdwa v203, 1, v175 dst_sel:DWORD dst_unused:UNUSED_PAD src0_sel:DWORD src1_sel:WORD_1\n"
    "v_cvt_pk_f16_f32 v196, v176, v177\n"
    "v_smfmac_f32_16x16x64_bf16 v[24:27], v[216:219], v[208:215], v240\n"
    "s_waitcnt lgkmcnt(8)\n"
    "ds_read_b128 v[216:219], v252 offset:2016\n"
    "v_smfmac_f32_16x16x64_bf16 v[28:31], v[220:223], v[208:215], v240 abid:1\n"
    "s_waitcnt lgkmcnt(8)\n"
    "ds_read_b128 v[208:211], v250 offset:880\n"
    "v_smfmac_f32_32x32x32_bf16 v[128:143], a[248:251], a[232:239], v241\n"
    "ds_read_b128 v[212:215], v250 offset:1200\n"
    "v_lshlrev_b32_sdwa v197, 1, v178 dst_sel:DWORD dst_unused:UNUSED_PAD src0_sel:DWORD src1_sel:WORD_1\n"
    "ds_read_b128 v[220:223], v252 offset:2352\n"
    "s_waitcnt lgkmcnt(9)\n"
    "v_mfma_f32_16x16x32_bf16 v[168:171], a[80:83], v[144:147], 0\n"
    "s_nop 0\n"
    "ds_read_b128 v[156:159], v242 offset:3072\n"
    "v_lshlrev_b32_sdwa v205, 1, v179 dst_sel:DWORD dst_unused:UNUSED_PAD src0_sel:DWORD src1_sel:WORD_1\n"
    "v_mfma_f32_16x16x32_bf16 v[172:175], a[84:87], v[144:147], 0\n"
    "ds_read_b128 v[160:163], v242 offset:4096\n"
    "v_lshlrev_b32_sdwa v198, 1, v180 dst_sel:DWORD dst_unused:UNUSED_PAD src0_sel:DWORD src1_sel:WORD_1\n"
    "v_lshlrev_b32_sdwa v199, 1, v182 dst_sel:DWORD dst_unused:UNUSED_PAD src0_sel:DWORD src1_sel:WORD_1\n"
    "v_fma_mix_f32 v232, v184, v232, 0 op_sel:[1,1,0] op_sel_hi:[1,1,0]\n"
    "s_waitcnt lgkmcnt(9)\n"
    "v_mfma_f32_16x16x32_bf16 v[168:171], a[128:131], v[148:151], v[168:171]\n"
    "s_nop 0\n"
    "ds_read_b128 v[164:167], v242 offset:5120\n"
    "ds_read_b128 a[240:243], v244 offset:32768\n"
    "v_mfma_f32_16x16x32_bf16 v[172:175], a[132:135], v[148:151], v[172:175]\n"
    "ds_read_u16 v193, v193\n"
    "v_or_b32_e64 v190, v190, v206\n"
    "v_lshlrev_b32_sdwa v206, 1, v181 dst_sel:DWORD dst_unused:UNUSED_PAD src0_sel:DWORD src1_sel:WORD_1\n"
    "v_cvt_pk_bf16_f32 v184, v234, v232\n"
    "s_waitcnt lgkmcnt(10)\n"
    "v_mfma_f32_16x16x32_bf16 v[168:171], a[48:51], v[152:155], v[168:171]\n"
    "s_nop 0\n"
    "ds_read_u16_d16_hi v201, v201\n"
    "v_pk_fma_f16 v232, v188, v188, s48\n"
    "v_mov_b32_e64 v254, 0\n"
    "v_pk_mul_f16 v232, v188, v232\n"
    "v_exp_f16_e64 v234, v232\n"
    "ds_read_u16 v194, v194\n"
    "s_waitcnt lgkmcnt(10)\n"
    "v_or_b32_e32 v191, v191, v207\n"
    "v_lshlrev_b32_sdwa v207, 1, v183 dst_sel:DWORD dst_unused:UNUSED_PAD src0_sel:DWORD src1_sel:WORD_1\n"
    "v_exp_f16_sdwa v234, v232 dst_sel:WORD_1 dst_unused:UNUSED_PRESERVE src0_sel:WORD_1\n"
    "v_lshlrev_b32_sdwa v200, 1, v169 dst_sel:DWORD dst_unused:UNUSED_PAD src0_sel:DWORD src1_sel:WORD_1\n"
    "v_pk_fma_f16 v234, v234, s49, s49\n"
    "v_mfma_f32_16x16x32_bf16 v[96:99], a[192:195], v[184:187], v[96:99]\n"
    "s_waitcnt lgkmcnt(8)\n"
    "s_barrier\n"
    "v_smfmac_f32_16x16x64_bf16 v[24:27], v[216:219], v[208:215], v240\n"
    "s_waitcnt lgkmcnt(4)\n"
    "ds_read_u16_d16_hi v202, v202\n"
    "ds_write2_b32 v248, v32, v33 offset0:0 offset1:20\n"
    "v_smfmac_f32_16x16x64_bf16 v[28:31], v[220:223], v[208:215], v240 abid:1\n"
    "s_waitcnt lgkmcnt(5)\n"
    "v_rcp_f16_e64 v232, v234\n"
    "v_mfma_f32_16x16x32_bf16 v[172:175], a[240:243], v[152:155], v[172:175]\n"
    "ds_read_b128 v[236:239], v244 offset:16384\n"
    "ds_write2_b32 v248, v34, v35 offset0:40 offset1:60\n"
    "v_add_u32_e32 v248, 1360, v248\n"
    "v_pk_add_f32 v[224:225], v[224:225], v[24:25]\n"
    "v_pk_add_f32 v[226:227], v[226:227], v[26:27]\n"
    "v_pk_fma_f32 v[228:229], v[24:25], v[24:25], v[228:229]\n"
    "v_pk_fma_f32 v[230:231], v[26:27], v[26:27], v[230:231]\n"
    "v_pk_add_f32 v[224:225], v[224:225], v[28:29]\n"
    "v_pk_add_f32 v[226:227], v[226:227], v[30:31]\n"
    "v_pk_fma_f32 v[228:229], v[28:29], v[28:29], v[228:229]\n"
    "v_pk_fma_f32 v[230:231], v[30:31], v[30:31], v[230:231]\n"
    "v_mfma_f32_16x16x32_bf16 v[176:179], a[80:83], v[156:159], 0\n"
    "s_waitcnt lgkmcnt(5)\n"
    "ds_read_u16 v195, v195\n"
    "ds_write2_b32 v235, v52, v53 offset0:0 offset1:20\n"
    "v_mfma_f32_16x16x32_bf16 v[176:179], a[128:131], v[160:163], v[176:179]\n"
    "v_rcp_f16_sdwa v232, v234 dst_sel:WORD_1 dst_unused:UNUSED_PRESERVE src0_sel:WORD_1\n"
    "v_mfma_f32_16x16x32_bf16 v[176:179], a[48:51], v[164:167], v[176:179]\n"
    "ds_read_u16_d16_hi v203, v203\n"
    "ds_write2_b32 v235, v54, v55 offset0:40 offset1:60\n"
    "v_mfma_f32_16x16x32_bf16 v[180:183], a[84:87], v[156:159], 0\n"
    "s_waitcnt lgkmcnt(6)\n"
    "v_add_u32_e32 v235, 1360, v235\n"
    "v_or_b32_e64 v193, v193, v201\n"
    "v_mfma_f32_16x16x32_bf16 v[180:183], a[132:135], v[160:163], v[180:183]\n"
    "ds_read_b128 a[244:247], v244 offset:18432\n"
    "ds_write2_b32 v248, v36, v37 offset0:0 offset1:20\n"
    "v_mfma_f32_16x16x32_bf16 v[180:183], a[240:243], v[164:167], v[180:183]\n"
    "v_fma_mix_f32 v234, v188, v232, 0 op_sel_hi:[1,1,0]\n"
    "v_fma_mix_f32 v232, v188, v232, 0 op_sel:[1,1,0] op_sel_hi:[1,1,0]\n"
    "v_mfma_f32_16x16x32_bf16 v[100:103], a[196:199], v[184:187], v[100:103]\n"
    "s_waitcnt lgkmcnt(6)\n"
    "ds_read_u16 v197, v197\n"
    "ds_write2_b32 v248, v38, v39 offset0:40 offset1:60\n"
    "v_mfma_f32_16x16x32_bf16 v[112:115], a[200:203], v[184:187], v[112:115]\n"
    "s_nop 0\n"
    "v_add_u32_e32 v248, 1360, v248\n"
    "v_or_b32_e64 v194, v194, v202\n"
    "v_mfma_f32_16x16x32_bf16 v[116:119], a[204:207], v[184:187], v[116:119]\n"
    "ds_read_u16_d16_hi v205, v205\n"
    "ds_write2_b32 v235, v56, v57 offset0:0 offset1:20\n"
    "v_mfma_f32_16x16x32_bf16 v[128:131], v[236:239], v[184:187], v[128:131]\n"
    "v_cvt_pk_bf16_f32 v188, v234, v232\n"
    "v_pk_fma_f16 v232, v192, v192, s48\n"
    "v_lshlrev_b32_sdwa v201, 1, v171 dst_sel:DWORD dst_unused:UNUSED_PAD src0_sel:DWORD src1_sel:WORD_1\n"
    "v_mfma_f32_16x16x32_bf16 v[104:107], a[192:195], v[188:191], v[104:107]\n"
    "s_waitcnt lgkmcnt(6)\n"
    "ds_read_u16 v198, v198\n"
    "ds_write2_b32 v235, v58, v59 offset0:40 offset1:60\n"
    "v_mfma_f32_16x16x32_bf16 v[108:111], a[196:199], v[188:191], v[108:111]\n"
    "s_waitcnt lgkmcnt(6)\n"
    "v_add_u32_e32 v235, -1360, v235\n"
    "v_or_b32_e64 v195, v195, v203\n"
    "v_mfma_f32_16x16x32_bf16 v[132:135], a[244:247], v[184:187], v[132:135]\n"
    "ds_read_u16_d16_hi v206, v206\n"
    "ds_write2_b32 v248, v40, v41 offset0:0 offset1:20\n"
    "v_mfma_f32_16x16x32_bf16 v[120:123], a[200:203], v[188:191], v[120:123]\n"
    "v_pk_mul_f16 v232, v192, v232\n"
    "v_lshlrev_b32_sdwa v184, 1, v168 dst_sel:DWORD dst_unused:UNUSED_PAD src0_sel:DWORD src1_sel:WORD_1\n"
    "v_mfma_f32_16x16x32_bf16 v[124:127], a[204:207], v[188:191], v[124:127]\n"
    "ds_read_u16 v199, v199\n"
    "ds_write2_b32 v248, v42, v43 offset0:40 offset1:60\n"
    "v_mfma_f32_16x16x32_bf16 v[136:139], v[236:239], v[188:191], v[136:139]\n"
    "v_add_u32_e32 v248, 1360, v248\n"
    "v_exp_f16_e32 v234, v232\n"
    "v_mfma_f32_16x16x32_bf16 v[140:143], a[244:247], v[188:191], v[140:143]\n"
    "s_waitcnt lgkmcnt(6)\n"
    "ds_read_u16_d16_hi v207, v207\n"
    "ds_write2_b32 v248, v44, v45 offset0:0 offset1:20\n"
    "v_or_b32_e64 v197, v197, v205\n"
    "v_exp_f16_sdwa v234, v232 dst_sel:WORD_1 dst_unused:UNUSED_PRESERVE src0_sel:WORD_1\n"
    "v_lshlrev_b32_sdwa v185, 1, v170 dst_sel:DWORD dst_unused:UNUSED_PAD src0_sel:DWORD src1_sel:WORD_1\n"
    "v_pk_fma_f16 v234, v234, s49, s49\n"
    "v_mfma_f32_16x16x32_bf16 v[168:171], a[88:91], v[144:147], 0\n"
    "s_add_u32 s60, s60, 1572864\n"
    "ds_read_b128 a[240:243], v244 offset:36864\n"
    "ds_write2_b32 v248, v46, v47 offset0:40 offset1:60\n"
    "v_mfma_f32_16x16x32_bf16 v[168:171], a[136:139], v[148:151], v[168:171]\n"
    "v_add_u32_e32 v248, 1360, v248\n"
    "v_lshlrev_b32_sdwa v186, 1, v172 dst_sel:DWORD dst_unused:UNUSED_PAD src0_sel:DWORD src1_sel:WORD_1\n"
    "v_lshlrev_b32_sdwa v202, 1, v173 dst_sel:DWORD dst_unused:UNUSED_PAD src0_sel:DWORD src1_sel:WORD_1\n"
    "v_lshlrev_b32_sdwa v187, 1, v174 dst_sel:DWORD dst_unused:UNUSED_PAD src0_sel:DWORD src1_sel:WORD_1\n"
    "v_lshlrev_b32_sdwa v203, 1, v175 dst_sel:DWORD dst_unused:UNUSED_PAD src0_sel:DWORD src1_sel:WORD_1\n"
    "v_rcp_f16_e32 v232, v234\n"
    "v_mfma_f32_16x16x32_bf16 v[168:171], a[52:55], v[152:155], v[168:171]\n"
    "s_waitcnt lgkmcnt(6)\n"
    "ds_read_u16 v184, v184\n"
    "ds_write2_b32 v248, v48, v49 offset0:0 offset1:20\n"
    "v_or_b32_e64 v198, v198, v206\n"
    "v_rcp_f16_sdwa v232, v234 dst_sel:WORD_1 dst_unused:UNUSED_PRESERVE src0_sel:WORD_1\n"
    "v_mfma_f32_16x16x32_bf16 v[172:175], a[92:95], v[144:147], 0\n"
    "s_add_u32 s61, s61, 1572864\n"
    "ds_read_u16_d16_hi v200, v200\n"
    "ds_write2_b32 v248, v50, v51 offset0:40 offset1:60\n"
    "v_mfma_f32_32x32x1_2b_f32 v[32:63], v254, v254, 0\n"
    "v_add_u32_e32 v248, -5440, v248\n"
    "v_lshlrev_b32_sdwa v188, 1, v176 dst_sel:DWORD dst_unused:UNUSED_PAD src0_sel:DWORD src1_sel:WORD_1\n"
    "v_lshlrev_b32_sdwa v204, 1, v177 dst_sel:DWORD dst_unused:UNUSED_PAD src0_sel:DWORD src1_sel:WORD_1\n"
    "v_lshlrev_b32_sdwa v189, 1, v178 dst_sel:DWORD dst_unused:UNUSED_PAD src0_sel:DWORD src1_sel:WORD_1\n"
    "v_lshlrev_b32_sdwa v205, 1, v179 dst_sel:DWORD dst_unused:UNUSED_PAD src0_sel:DWORD src1_sel:WORD_1\n"
    "v_lshlrev_b32_sdwa v190, 1, v180 dst_sel:DWORD dst_unused:UNUSED_PAD src0_sel:DWORD src1_sel:WORD_1\n"
    "v_lshlrev_b32_sdwa v206, 1, v181 dst_sel:DWORD dst_unused:UNUSED_PAD src0_sel:DWORD src1_sel:WORD_1\n"
    "v_lshlrev_b32_sdwa v191, 1, v182 dst_sel:DWORD dst_unused:UNUSED_PAD src0_sel:DWORD src1_sel:WORD_1\n"
    "v_fma_mix_f32 v234, v192, v232, 0 op_sel_hi:[1,1,0]\n"
    "v_fma_mix_f32 v232, v192, v232, 0 op_sel:[1,1,0] op_sel_hi:[1,1,0]\n"
    "v_mov_b32_e32 v254, 0\n"
    "v_cvt_pk_bf16_f32 v192, v234, v232\n"
    "v_pk_fma_f16 v232, v196, v196, s48\n"
    "v_pk_mul_f16 v232, v196, v232\n"
    "v_mfma_f32_16x16x32_bf16 v[172:175], a[140:143], v[148:151], v[172:175]\n"
    "s_waitcnt lgkmcnt(4)\n"
    "ds_read_u16 v185, v185\n"
    "ds_read_u16_d16_hi v201, v201\n"
    "v_mfma_f32_16x16x32_bf16 v[172:175], a[240:243], v[152:155], v[172:175]\n"
    "s_add_u32 s62, s62, 1572864\n"
    "ds_read_u16 v186, v186\n"
    "ds_read_b128 v[236:239], v244 offset:20480\n"
    "v_mfma_f32_16x16x32_bf16 v[176:179], a[88:91], v[156:159], 0\n"
    "s_add_u32 s63, s63, 1572864\n"
    "v_or_b32_e64 v199, v199, v207\n"
    "v_lshlrev_b32_sdwa v207, 1, v183 dst_sel:DWORD dst_unused:UNUSED_PAD src0_sel:DWORD src1_sel:WORD_1\n"
    "v_mfma_f32_16x16x32_bf16 v[176:179], a[136:139], v[160:163], v[176:179]\n"
    "ds_read_u16_d16_hi v202, v202\n"
    "v_exp_f16_e64 v234, v232\n"
    "v_mfma_f32_16x16x32_bf16 v[176:179], a[52:55], v[164:167], v[176:179]\n"
    "s_waitcnt lgkmcnt(5)\n"
    "ds_read_b128 v[216:219], v251 offset:10752\n"
    "v_exp_f16_sdwa v234, v232 dst_sel:WORD_1 dst_unused:UNUSED_PRESERVE src0_sel:WORD_1\n"
    "v_mfma_f32_16x16x32_bf16 v[180:183], a[92:95], v[156:159], 0\n"
    "s_barrier\n"
    "v_mfma_f32_16x16x32_bf16 v[180:183], a[140:143], v[160:163], v[180:183]\n"
    "s_add_u32 s52, s52, 1572864\n"
    "s_add_u32 s53, s53, 393216\n"
    "ds_read_b128 v[208:211], v250 offset:0\n"
    "ds_read_b128 v[212:215], v250 offset:320\n"
    "v_mfma_f32_16x16x32_bf16 v[180:183], a[240:243], v[164:167], v[180:183]\n"
    "s_waitcnt vmcnt(3)\n"
    "ds_read_b128 v[220:223], v251 offset:11088\n"
    "ds_read_u16 v187, v187\n"
    "v_smfmac_f32_32x32x32_bf16 v[128:143], a[248:251], a[224:231], v241\n"
    "s_nop 0\n"
    "ds_read_b128 a[244:247], v244 offset:22528\n"
    "ds_read_u16_d16_hi v203, v203\n"
    "v_pk_fma_f16 v234, v234, s49, s49\n"
    "v_or_b32_e64 v184, v184, v200\n"
    "v_rcp_f16_e32 v232, v234\n"
    "v_mfma_f32_16x16x32_bf16 v[96:99], a[208:211], v[192:195], v[96:99]\n"
    "s_waitcnt lgkmcnt(8)\n"
    "ds_read_u16 v188, v188\n"
    "v_rcp_f16_sdwa v232, v234 dst_sel:WORD_1 dst_unused:UNUSED_PRESERVE src0_sel:WORD_1\n"
    "v_mfma_f32_16x16x32_bf16 v[100:103], a[212:215], v[192:195], v[100:103]\n"
    "ds_read_u16_d16_hi v204, v204\n"
    "v_fma_mix_f32 v234, v196, v232, 0 op_sel_hi:[1,1,0]\n"
    "v_mfma_f32_16x16x32_bf16 v[112:115], a[216:219], v[192:195], v[112:115]\n"
    "v_fma_mix_f32 v232, v196, v232, 0 op_sel:[1,1,0] op_sel_hi:[1,1,0]\n"
    "v_or_b32_e32 v185, v185, v201\n"
    "v_mfma_f32_16x16x32_bf16 v[116:119], a[220:223], v[192:195], v[116:119]\n"
    "s_waitcnt lgkmcnt(8)\n"
    "ds_read_u16 v189, v189\n"
    "v_cvt_pk_bf16_f32 v196, v234, v232\n"
    "v_mfma_f32_16x16x32_bf16 v[128:131], v[236:239], v[192:195], v[128:131]\n"
    "v_lshlrev_b32_sdwa v201, 1, v171 dst_sel:DWORD dst_unused:UNUSED_PAD src0_sel:DWORD src1_sel:WORD_1\n"
    "v_or_b32_e32 v186, v186, v202\n"
    "v_mfma_f32_16x16x32_bf16 v[104:107], a[208:211], v[196:199], v[104:107]\n"
    "s_waitcnt lgkmcnt(7)\n"
    "ds_read_u16_d16_hi v205, v205\n"
    "v_lshlrev_b32_sdwa v202, 1, v173 dst_sel:DWORD dst_unused:UNUSED_PAD src0_sel:DWORD src1_sel:WORD_1\n"
    "v_smfmac_f32_16x16x64_bf16 v[32:35], v[216:219], v[208:215], v240\n"
    "s_waitcnt lgkmcnt(7)\n"
    "ds_read_u16 v190, v190\n"
    "v_smfmac_f32_16x16x64_bf16 v[36:39], v[220:223], v[208:215], v240 abid:1\n"
    "s_nop 0\n"
    "v_cvt_pk_bf16_f32 v96, v96, v97\n"
    "v_mfma_f32_16x16x32_bf16 v[108:111], a[212:215], v[196:199], v[108:111]\n"
    "s_nop 0\n"
    "ds_read_b128 v[208:211], v250 offset:640\n"
    "v_cvt_pk_bf16_f32 v97, v98, v99\n"
    "v_mfma_f32_16x16x32_bf16 v[120:123], a[216:219], v[196:199], v[120:123]\n"
    "s_waitcnt lgkmcnt(7)\n"
    "ds_read_b128 v[212:215], v250 offset:960\n"
    "v_cvt_pk_bf16_f32 v98, v100, v101\n"
    "v_mfma_f32_16x16x32_bf16 v[132:135], a[244:247], v[192:195], v[132:135]\n"
    "ds_read_b128 v[216:219], v252 offset:10752\n"
    "v_cvt_pk_f16_f32 v192, v168, v169\n"
    "v_mfma_f32_16x16x32_bf16 v[124:127], a[220:223], v[196:199], v[124:127]\n"
    "s_nop 0\n"
    "ds_read_b128 v[220:223], v252 offset:11088\n"
    "v_lshlrev_b32_sdwa v193, 1, v170 dst_sel:DWORD dst_unused:UNUSED_PAD src0_sel:DWORD src1_sel:WORD_1\n"
    "v_mfma_f32_16x16x32_bf16 v[136:139], v[236:239], v[196:199], v[136:139]\n"
    "s_waitcnt lgkmcnt(9)\n"
    "ds_read_u16_d16_hi v206, v206\n"
    "v_lshlrev_b32_sdwa v194, 1, v172 dst_sel:DWORD dst_unused:UNUSED_PAD src0_sel:DWORD src1_sel:WORD_1\n"
    "v_mfma_f32_16x16x32_bf16 v[140:143], a[244:247], v[196:199], v[140:143]\n"
    "v_lshlrev_b32_sdwa v195, 1, v174 dst_sel:DWORD dst_unused:UNUSED_PAD src0_sel:DWORD src1_sel:WORD_1\n"
    "v_cvt_pk_f16_f32 v196, v176, v177\n"
    "v_mfma_f32_16x16x32_bf16 v[168:171], a[96:99], v[144:147], 0\n"
    "s_nop 0\n"
    "ds_read_u16 v191, v191\n"
    "v_lshlrev_b32_sdwa v197, 1, v178 dst_sel:DWORD dst_unused:UNUSED_PAD src0_sel:DWORD src1_sel:WORD_1\n"
    "v_mfma_f32_16x16x32_bf16 v[168:171], a[144:147], v[148:151], v[168:171]\n"
    "s_waitcnt lgkmcnt(9)\n"
    "v_lshlrev_b32_sdwa v198, 1, v180 dst_sel:DWORD dst_unused:UNUSED_PAD src0_sel:DWORD src1_sel:WORD_1\n"
    "v_lshlrev_b32_sdwa v199, 1, v182 dst_sel:DWORD dst_unused:UNUSED_PAD src0_sel:DWORD src1_sel:WORD_1\n"
    "v_mfma_f32_16x16x32_bf16 v[168:171], a[56:59], v[152:155], v[168:171]\n"
    "ds_read_u16_d16_hi v207, v207\n"
    "v_or_b32_e64 v187, v187, v203\n"
    "v_lshlrev_b32_sdwa v203, 1, v175 dst_sel:DWORD dst_unused:UNUSED_PAD src0_sel:DWORD src1_sel:WORD_1\n"
    "v_mfma_f32_16x16x32_bf16 v[172:175], a[100:103], v[144:147], 0\n"
    "ds_read_u16 v193, v193\n"
    "v_or_b32_e64 v188, v188, v204\n"
    "v_mfma_f32_16x16x32_bf16 v[172:175], a[148:151], v[148:151], v[172:175]\n"
    "s_waitcnt lgkmcnt(9)\n"
    "v_or_b32_e32 v189, v189, v205\n"
    "v_lshlrev_b32_sdwa v205, 1, v179 dst_sel:DWORD dst_unused:UNUSED_PAD src0_sel:DWORD src1_sel:WORD_1\n"
    "v_mfma_f32_16x16x32_bf16 v[172:175], a[60:63], v[152:155], v[172:175]\n"
    "ds_read_u16_d16_hi v201, v201\n"
    "v_cvt_pk_bf16_f32 v99, v102, v103\n"
    "v_mfma_f32_16x16x32_bf16 v[176:179], a[96:99], v[156:159], 0\n"
    "v_cvt_pk_bf16_f32 v112, v112, v113\n"
    "v_cvt_pk_bf16_f32 v113, v114, v115\n"
    "v_mfma_f32_16x16x32_bf16 v[176:179], a[144:147], v[160:163], v[176:179]\n"
    "v_cvt_pk_bf16_f32 v114, v116, v117\n"
    "v_cvt_pk_bf16_f32 v115, v118, v119\n"
    "v_mfma_f32_16x16x32_bf16 v[176:179], a[56:59], v[164:167], v[176:179]\n"
    "s_waitcnt lgkmcnt(6)\n"
    "ds_write_b128 v243, v[96:99] offset:24640\n"
    "v_smfmac_f32_16x16x64_bf16 v[32:35], v[216:219], v[208:215], v240\n"
    "s_waitcnt lgkmcnt(6)\n"
    "v_cvt_pk_bf16_f32 v128, v128, v129\n"
    "v_smfmac_f32_16x16x64_bf16 v[36:39], v[220:223], v[208:215], v240 abid:1\n"
    "v_cvt_pk_bf16_f32 v129, v130, v131\n"
    "ds_write_b128 v243, v[112:115] offset:26688\n"
    "s_waitcnt lgkmcnt(6)\n"
    "v_or_b32_e32 v190, v190, v206\n"
    "v_mfma_f32_16x16x32_bf16 v[96:99], a[0:3], v[184:187], 0\n"
    "v_lshlrev_b32_sdwa v206, 1, v181 dst_sel:DWORD dst_unused:UNUSED_PAD src0_sel:DWORD src1_sel:WORD_1\n"
    "v_cvt_pk_bf16_f32 v130, v132, v133\n"
    "ds_read_b128 v[208:211], v250 offset:80\n"
    "ds_read_b128 v[212:215], v250 offset:400\n"
    "v_pk_add_f32 v[224:225], v[224:225], v[32:33]\n"
    "v_pk_add_f32 v[226:227], v[226:227], v[34:35]\n"
    "v_pk_fma_f32 v[228:229], v[32:33], v[32:33], v[228:229]\n"
    "v_pk_fma_f32 v[230:231], v[34:35], v[34:35], v[230:231]\n"
    "v_pk_add_f32 v[224:225], v[224:225], v[36:37]\n"
    "v_pk_add_f32 v[226:227], v[226:227], v[38:39]\n"
    "v_pk_fma_f32 v[228:229], v[36:37], v[36:37], v[228:229]\n"
    "v_pk_fma_f32 v[230:231], v[38:39], v[38:39], v[230:231]\n"
    "v_mfma_f32_16x16x32_bf16 v[100:103], a[4:7], v[184:187], 0\n"
    "s_waitcnt lgkmcnt(5)\n"
    "ds_read_b128 v[216:219], v251 offset:11424\n"
    "ds_read_b128 v[220:223], v251 offset:11760\n"
    "v_mfma_f32_16x16x32_bf16 v[112:115], a[8:11], v[184:187], 0\n"
    "s_waitcnt lgkmcnt(5)\n"
    "ds_read_u16 v194, v194\n"
    "ds_read_u16_d16_hi v202, v202\n"
    "v_or_b32_e64 v191, v191, v207\n"
    "v_mfma_f32_16x16x32_bf16 v[116:119], a[12:15], v[184:187], 0\n"
    "ds_read_b128 v[236:239], v244 offset:0\n"
    "ds_read_b128 a[252:255], v249 offset:0\n"
    "v_lshlrev_b32_sdwa v207, 1, v183 dst_sel:DWORD dst_unused:UNUSED_PAD src0_sel:DWORD src1_sel:WORD_1\n"
    "v_mfma_f32_16x16x32_bf16 v[180:183], a[100:103], v[156:159], 0\n"
    "ds_read_u16 v195, v195\n"
    "v_or_b32_e64 v193, v193, v201\n"
    "v_mfma_f32_16x16x32_bf16 v[180:183], a[148:151], v[160:163], v[180:183]\n"
    "v_cvt_pk_bf16_f32 v131, v134, v135\n"
    "v_cvt_pk_bf16_f32 v104, v104, v105\n"
    "v_mfma_f32_16x16x32_bf16 v[180:183], a[60:63], v[164:167], v[180:183]\n"
    "v_cvt_pk_bf16_f32 v105, v106, v107\n"
    "v_cvt_pk_bf16_f32 v106, v108, v109\n"
    "v_cvt_pk_bf16_f32 v107, v110, v111\n"
    "v_mfma_f32_16x16x32_bf16 v[108:111], a[4:7], v[188:191], 0\n"
    "ds_write_b128 v243, v[128:131] offset:28736\n"
    "s_waitcnt lgkmcnt(7)\n"
    "v_smfmac_f32_16x16x64_bf16 v[40:43], v[216:219], v[208:215], v240\n"
    "s_waitcnt lgkmcnt(6)\n"
    "v_cvt_pk_bf16_f32 v120, v120, v121\n"
    "v_smfmac_f32_16x16x64_bf16 v[44:47], v[220:223], v[208:215], v240 abid:1\n"
    "ds_write_b128 v243, v[104:107] offset:25664\n"
    "v_mfma_f32_16x16x32_bf16 v[104:107], a[0:3], v[188:191], 0\n"
    "v_cvt_pk_bf16_f32 v121, v122, v123\n"
    "v_cvt_pk_bf16_f32 v122, v124, v125\n"
    "v_cvt_pk_bf16_f32 v123, v126, v127\n"
    "v_pk_fma_f16 v232, v192, v192, s48\n"
    "v_mfma_f32_16x16x32_bf16 v[124:127], a[12:15], v[188:191], 0\n"
    "ds_read_b128 v[208:211], v250 offset:720\n"
    "ds_write_b128 v243, v[120:123] offset:27712\n"
    "v_mfma_f32_16x16x32_bf16 v[120:123], a[8:11], v[188:191], 0\n"
    "s_waitcnt lgkmcnt(5)\n"
    "buffer_store_dwordx4 a[252:255], v255, s[44:47], s57 offen offset:0 sc1\n"
    "v_mfma_f32_16x16x32_bf16 v[128:131], v[236:239], v[184:187], 0\n"
    "s_nop 0\n"
    "ds_read_b128 v[212:215], v250 offset:1040\n"
    "ds_read_b128 v[216:219], v252 offset:11424\n"
    "v_or_b32_e64 v194, v194, v202\n"
    "ds_read_b128 v[220:223], v252 offset:11760\n"
    "ds_read_u16_d16_hi v203, v203\n"
    "v_cvt_pk_bf16_f32 v136, v136, v137\n"
    "v_cvt_pk_bf16_f32 v137, v138, v139\n"
    "v_cvt_pk_bf16_f32 v138, v140, v141\n"
    "v_cvt_pk_bf16_f32 v139, v142, v143\n"
    "ds_write_b128 v243, v[136:139] offset:29760\n"
    "v_pk_mul_f16 v232, v192, v232\n"
    "v_mfma_f32_16x16x32_bf16 v[136:139], v[236:239], v[188:191], 0\n"
    "v_exp_f16_e64 v234, v232\n"
    "v_lshlrev_b32_sdwa v201, 1, v171 dst_sel:DWORD dst_unused:UNUSED_PAD src0_sel:DWORD src1_sel:WORD_1\n"
    "v_exp_f16_sdwa v234, v232 dst_sel:WORD_1 dst_unused:UNUSED_PRESERVE src0_sel:WORD_1\n"
    "ds_read_b128 a[244:247], v244 offset:2048\n"
    "ds_read_u16 v197, v197\n"
    "v_pk_fma_f16 v234, v234, s49, s49\n"
    "v_lshlrev_b32_sdwa v202, 1, v173 dst_sel:DWORD dst_unused:UNUSED_PAD src0_sel:DWORD src1_sel:WORD_1\n"
    "v_rcp_f16_e64 v232, v234\n"
    "ds_read_u16_d16_hi v205, v205\n"
    "ds_read_u16 v198, v198\n"
    "s_waitcnt lgkmcnt(7)\n"
    "v_smfmac_f32_16x16x64_bf16 v[40:43], v[216:219], v[208:215], v240\n"
    "s_waitcnt lgkmcnt(6)\n"
    "ds_read_b128 v[216:219], v251 offset:12096\n"
    "ds_read_u16_d16_hi v206, v206\n"
    "v_smfmac_f32_16x16x64_bf16 v[44:47], v[220:223], v[208:215], v240 abid:1\n"
    "ds_read_b128 v[208:211], v250 offset:160\n"
    "v_rcp_f16_sdwa v232, v234 dst_sel:WORD_1 dst_unused:UNUSED_PRESERVE src0_sel:WORD_1\n"
    "ds_read_b128 v[212:215], v250 offset:480\n"
    "s_waitcnt lgkmcnt(8)\n"
    "v_or_b32_e32 v195, v195, v203\n"
    "v_pk_add_f32 v[224:225], v[224:225], v[40:41]\n"
    "v_pk_add_f32 v[226:227], v[226:227], v[42:43]\n"
    "v_pk_fma_f32 v[228:229], v[40:41], v[40:41], v[228:229]\n"
    "v_pk_fma_f32 v[230:231], v[42:43], v[42:43], v[230:231]\n"
    "v_pk_add_f32 v[224:225], v[224:225], v[44:45]\n"
    "v_pk_add_f32 v[226:227], v[226:227], v[46:47]\n"
    "v_pk_fma_f32 v[228:229], v[44:45], v[44:45], v[228:229]\n"
    "v_pk_fma_f32 v[230:231], v[46:47], v[46:47], v[230:231]\n"
    "s_waitcnt lgkmcnt(7)\n"
    "v_mfma_f32_16x16x32_bf16 v[132:135], a[244:247], v[184:187], 0\n"
    "s_nop 0\n"
    "ds_read_b128 v[220:223], v251 offset:12432\n"
    "ds_read_u16 v199, v199\n"
    "v_mfma_f32_16x16x32_bf16 v[140:143], a[244:247], v[188:191], 0\n"
    "ds_read_u16_d16_hi v207, v207\n"
    "v_cvt_pk_f16_f32 v184, v168, v169\n"
    "v_lshlrev_b32_sdwa v185, 1, v170 dst_sel:DWORD dst_unused:UNUSED_PAD src0_sel:DWORD src1_sel:WORD_1\n"
    "v_mfma_f32_16x16x32_bf16 v[168:171], a[104:107], v[144:147], 0\n"
    "ds_read_b128 a[252:255], v249 offset:2048\n"
    "v_lshlrev_b32_sdwa v186, 1, v172 dst_sel:DWORD dst_unused:UNUSED_PAD src0_sel:DWORD src1_sel:WORD_1\n"
    "v_mfma_f32_16x16x32_bf16 v[168:171], a[152:155], v[148:151], v[168:171]\n"
    "s_waitcnt lgkmcnt(9)\n"
    "ds_read_u16 v185, v185\n"
    "v_or_b32_e32 v197, v197, v205\n"
    "v_mfma_f32_16x16x32_bf16 v[168:171], a[64:67], v[152:155], v[168:171]\n"
    "v_lshlrev_b32_sdwa v187, 1, v174 dst_sel:DWORD dst_unused:UNUSED_PAD src0_sel:DWORD src1_sel:WORD_1\n"
    "v_lshlrev_b32_sdwa v203, 1, v175 dst_sel:DWORD dst_unused:UNUSED_PAD src0_sel:DWORD src1_sel:WORD_1\n"
    "v_mfma_f32_16x16x32_bf16 v[172:175], a[108:111], v[144:147], 0\n"
    "ds_read_u16_d16_hi v201, v201\n"
    "v_cvt_pk_f16_f32 v188, v176, v177\n"
    "v_lshlrev_b32_sdwa v189, 1, v178 dst_sel:DWORD dst_unused:UNUSED_PAD src0_sel:DWORD src1_sel:WORD_1\n"
    "v_lshlrev_b32_sdwa v205, 1, v179 dst_sel:DWORD dst_unused:UNUSED_PAD src0_sel:DWORD src1_sel:WORD_1\n"
    "v_lshlrev_b32_sdwa v190, 1, v180 dst_sel:DWORD dst_unused:UNUSED_PAD src0_sel:DWORD src1_sel:WORD_1\n"
    "v_lshlrev_b32_sdwa v191, 1, v182 dst_sel:DWORD dst_unused:UNUSED_PAD src0_sel:DWORD src1_sel:WORD_1\n"
    "v_fma_mix_f32 v234, v192, v232, 0 op_sel_hi:[1,1,0]\n"
    "v_mfma_f32_16x16x32_bf16 v[172:175], a[156:159], v[148:151], v[172:175]\n"
    "s_waitcnt lgkmcnt(7)\n"
    "ds_read_u16 v186, v186\n"
    "v_or_b32_e32 v198, v198, v206\n"
    "v_mfma_f32_16x16x32_bf16 v[172:175], a[68:71], v[152:155], v[172:175]\n"
    "s_waitcnt lgkmcnt(7)\n"
    "ds_read_u16_d16_hi v202, v202\n"
    "v_lshlrev_b32_sdwa v206, 1, v181 dst_sel:DWORD dst_unused:UNUSED_PAD src0_sel:DWORD src1_sel:WORD_1\n"
    "v_smfmac_f32_16x16x64_bf16 v[48:51], v[216:219], v[208:215], v240\n"
    "s_waitcnt lgkmcnt(7)\n"
    "ds_read_b128 v[216:219], v252 offset:12096\n"
    "v_smfmac_f32_16x16x64_bf16 v[52:55], v[220:223], v[208:215], v240 abid:1\n"
    "ds_read_b128 v[208:211], v250 offset:800\n"
    "v_fma_mix_f32 v232, v192, v232, 0 op_sel:[1,1,0] op_sel_hi:[1,1,0]\n"
    "v_mfma_f32_16x16x32_bf16 v[176:179], a[104:107], v[156:159], 0\n"
    "ds_read_b128 v[212:215], v250 offset:1120\n"
    "v_cvt_pk_bf16_f32 v192, v234, v232\n"
    "v_mfma_f32_16x16x32_bf16 v[176:179], a[152:155], v[160:163], v[176:179]\n"
    "ds_read_b128 v[220:223], v252 offset:12432\n"
    "v_pk_fma_f16 v232, v196, v196, s48\n"
    "v_mfma_f32_16x16x32_bf16 v[176:179], a[64:67], v[164:167], v[176:179]\n"
    "s_waitcnt lgkmcnt(8)\n"
    "ds_read_b128 v[236:239], v244 offset:4096\n"
    "buffer_store_dwordx4 a[252:255], v255, s[44:47], s57 offen offset:64 sc1\n"
    "v_or_b32_e32 v199, v199, v207\n"
    "v_lshlrev_b32_sdwa v207, 1, v183 dst_sel:DWORD dst_unused:UNUSED_PAD src0_sel:DWORD src1_sel:WORD_1\n"
    "v_mfma_f32_16x16x32_bf16 v[180:183], a[108:111], v[156:159], 0\n"
    "ds_read_u16 v187, v187\n"
    "ds_read_u16_d16_hi v203, v203\n"
    "v_pk_mul_f16 v232, v196, v232\n"
    "v_mfma_f32_16x16x32_bf16 v[180:183], a[156:159], v[160:163], v[180:183]\n"
    "v_exp_f16_e32 v234, v232\n"
    "v_mfma_f32_16x16x32_bf16 v[180:183], a[68:71], v[164:167], v[180:183]\n"
    "s_waitcnt lgkmcnt(9)\n"
    "ds_read_b128 a[244:247], v244 offset:6144\n"
    "v_exp_f16_sdwa v234, v232 dst_sel:WORD_1 dst_unused:UNUSED_PRESERVE src0_sel:WORD_1\n"
    "v_or_b32_e64 v185, v185, v201\n"
    "v_pk_fma_f16 v234, v234, s49, s49\n"
    "v_mfma_f32_16x16x32_bf16 v[96:99], a[16:19], v[192:195], v[96:99]\n"
    "ds_read_b128 a[252:255], v249 offset:4096\n"
    "ds_read_u16 v189, v189\n"
    "v_mfma_f32_16x16x32_bf16 v[100:103], a[20:23], v[192:195], v[100:103]\n"
    "v_rcp_f16_e32 v232, v234\n"
    "v_mfma_f32_16x16x32_bf16 v[112:115], a[24:27], v[192:195], v[112:115]\n"
    "s_waitcnt lgkmcnt(9)\n"
    "ds_read_u16_d16_hi v205, v205\n"
    "v_rcp_f16_sdwa v232, v234 dst_sel:WORD_1 dst_unused:UNUSED_PRESERVE src0_sel:WORD_1\n"
    "v_mfma_f32_16x16x32_bf16 v[116:119], a[28:31], v[192:195], v[116:119]\n"
    "v_fma_mix_f32 v234, v196, v232, 0 op_sel_hi:[1,1,0]\n"
    "v_fma_mix_f32 v232, v196, v232, 0 op_sel:[1,1,0] op_sel_hi:[1,1,0]\n"
    "s_waitcnt lgkmcnt(8)\n"
    "v_smfmac_f32_16x16x64_bf16 v[48:51], v[216:219], v[208:215], v240\n"
    "s_waitcnt lgkmcnt(7)\n"
    "ds_read_b128 v[216:219], v251 offset:12768\n"
    "ds_read_u16 v190, v190\n"
    "v_smfmac_f32_16x16x64_bf16 v[52:55], v[220:223], v[208:215], v240 abid:1\n"
    "s_waitcnt lgkmcnt(8)\n"
    "v_or_b32_e32 v186, v186, v202\n"
    "v_mfma_f32_16x16x32_bf16 v[128:131], v[236:239], v[192:195], v[128:131]\n"
    "ds_read_b128 v[208:211], v250 offset:240\n"
    "v_cvt_pk_bf16_f32 v196, v234, v232\n"
    "ds_read_b128 v[212:215], v250 offset:560\n"
    "v_pk_add_f32 v[224:225], v[224:225], v[48:49]\n"
    "v_pk_add_f32 v[226:227], v[226:227], v[50:51]\n"
    "v_pk_fma_f32 v[228:229], v[48:49], v[48:49], v[228:229]\n"
    "v_pk_fma_f32 v[230:231], v[50:51], v[50:51], v[230:231]\n"
    "v_pk_add_f32 v[224:225], v[224:225], v[52:53]\n"
    "v_pk_add_f32 v[226:227], v[226:227], v[54:55]\n"
    "v_pk_fma_f32 v[228:229], v[52:53], v[52:53], v[228:229]\n"
    "v_pk_fma_f32 v[230:231], v[54:55], v[54:55], v[230:231]\n"
    "v_mfma_f32_16x16x32_bf16 v[104:107], a[16:19], v[196:199], v[104:107]\n"
    "s_waitcnt lgkmcnt(7)\n"
    "ds_read_b128 v[220:223], v251 offset:13104\n"
    "ds_read_u16_d16_hi v206, v206\n"
    "v_mfma_f32_16x16x32_bf16 v[132:135], a[244:247], v[192:195], v[132:135]\n"
    "s_waitcnt lgkmcnt(8)\n"
    "ds_read_u16 v191, v191\n"
    "buffer_store_dwordx4 a[252:255], v255, s[44:47], s57 offen offset:128 sc1\n"
    "v_mfma_f32_16x16x32_bf16 v[108:111], a[20:23], v[196:199], v[108:111]\n"
    "ds_read_u16_d16_hi v207, v207\n"
    "v_pk_fma_f16 v232, v184, v184, s48\n"
    "v_mfma_f32_16x16x32_bf16 v[120:123], a[24:27], v[196:199], v[120:123]\n"
    "ds_read_b128 a[252:255], v249 offset:1024\n"
    "v_or_b32_e64 v187, v187, v203\n"
    "v_mfma_f32_16x16x32_bf16 v[124:127], a[28:31], v[196:199], v[124:127]\n"
    "ds_read_b128 a[240:243], v244 offset:34816\n"
    "v_pk_mul_f16 v232, v184, v232\n"
    "v_mfma_f32_16x16x32_bf16 v[136:139], v[236:239], v[196:199], v[136:139]\n"
    "s_waitcnt lgkmcnt(9)\n"
    "v_exp_f16_e32 v234, v232\n"
    "v_mfma_f32_16x16x32_bf16 v[140:143], a[244:247], v[196:199], v[140:143]\n"
    "v_or_b32_e64 v189, v189, v205\n"
    "v_exp_f16_sdwa v234, v232 dst_sel:WORD_1 dst_unused:UNUSED_PRESERVE src0_sel:WORD_1\n"
    "v_lshlrev_b32_sdwa v192, 1, v168 dst_sel:DWORD dst_unused:UNUSED_PAD src0_sel:DWORD src1_sel:WORD_1\n"
    "v_pk_fma_f16 v234, v234, s49, s49\n"
    "v_lshlrev_b32_sdwa v200, 1, v169 dst_sel:DWORD dst_unused:UNUSED_PAD src0_sel:DWORD src1_sel:WORD_1\n"
    "v_lshlrev_b32_sdwa v193, 1, v170 dst_sel:DWORD dst_unused:UNUSED_PAD src0_sel:DWORD src1_sel:WORD_1\n"
    "s_waitcnt lgkmcnt(6)\n"
    "v_smfmac_f32_16x16x64_bf16 v[56:59], v[216:219], v[208:215], v240\n"
    "s_waitcnt lgkmcnt(5)\n"
    "ds_read_b128 v[216:219], v252 offset:12768\n"
    "ds_read_u16 v192, v192\n"
    "v_smfmac_f32_16x16x64_bf16 v[60:63], v[220:223], v[208:215], v240 abid:1\n"
    "ds_read_b128 v[208:211], v250 offset:880\n"
    "ds_read_b128 v[212:215], v250 offset:1200\n"
    "v_lshlrev_b32_sdwa v201, 1, v171 dst_sel:DWORD dst_unused:UNUSED_PAD src0_sel:DWORD src1_sel:WORD_1\n"
    "v_mfma_f32_16x16x32_bf16 v[168:171], a[112:115], v[144:147], 0\n"
    "s_nop 0\n"
    "ds_read_b128 v[220:223], v252 offset:13104\n"
    "ds_read_u16_d16_hi v200, v200\n"
    "v_mfma_f32_16x16x32_bf16 v[168:171], a[160:163], v[148:151], v[168:171]\n"
    "s_waitcnt lgkmcnt(10)\n"
    "v_lshlrev_b32_sdwa v194, 1, v172 dst_sel:DWORD dst_unused:UNUSED_PAD src0_sel:DWORD src1_sel:WORD_1\n"
    "v_lshlrev_b32_sdwa v202, 1, v173 dst_sel:DWORD dst_unused:UNUSED_PAD src0_sel:DWORD src1_sel:WORD_1\n"
    "v_lshlrev_b32_sdwa v195, 1, v174 dst_sel:DWORD dst_unused:UNUSED_PAD src0_sel:DWORD src1_sel:WORD_1\n"
    "v_lshlrev_b32_sdwa v203, 1, v175 dst_sel:DWORD dst_unused:UNUSED_PAD src0_sel:DWORD src1_sel:WORD_1\n"
    "v_rcp_f16_e64 v232, v234\n"
    "v_mfma_f32_16x16x32_bf16 v[168:171], a[72:75], v[152:155], v[168:171]\n"
    "ds_read_u16 v193, v193\n"
    "ds_read_u16_d16_hi v201, v201\n"
    "v_or_b32_e32 v190, v190, v206\n"
    "v_rcp_f16_sdwa v232, v234 dst_sel:WORD_1 dst_unused:UNUSED_PRESERVE src0_sel:WORD_1\n"
    "v_mfma_f32_16x16x32_bf16 v[172:175], a[116:119], v[144:147], 0\n"
    "s_waitcnt lgkmcnt(8)\n"
    "ds_read_u16 v194, v194\n"
    "buffer_store_dwordx4 a[252:255], v255, s[44:47], s88 offen offset:0 sc1\n"
    "v_lshlrev_b32_sdwa v196, 1, v176 dst_sel:DWORD dst_unused:UNUSED_PAD src0_sel:DWORD src1_sel:WORD_1\n"
    "v_lshlrev_b32_sdwa v204, 1, v177 dst_sel:DWORD dst_unused:UNUSED_PAD src0_sel:DWORD src1_sel:WORD_1\n"
    "v_lshlrev_b32_sdwa v197, 1, v178 dst_sel:DWORD dst_unused:UNUSED_PAD src0_sel:DWORD src1_sel:WORD_1\n"
    "v_lshlrev_b32_sdwa v205, 1, v179 dst_sel:DWORD dst_unused:UNUSED_PAD src0_sel:DWORD src1_sel:WORD_1\n"
    "v_lshlrev_b32_sdwa v198, 1, v180 dst_sel:DWORD dst_unused:UNUSED_PAD src0_sel:DWORD src1_sel:WORD_1\n"
    "v_lshlrev_b32_sdwa v206, 1, v181 dst_sel:DWORD dst_unused:UNUSED_PAD src0_sel:DWORD src1_sel:WORD_1\n"
    "v_lshlrev_b32_sdwa v199, 1, v182 dst_sel:DWORD dst_unused:UNUSED_PAD src0_sel:DWORD src1_sel:WORD_1\n"
    "v_fma_mix_f32 v234, v184, v232, 0 op_sel_hi:[1,1,0]\n"
    "v_mfma_f32_16x16x32_bf16 v[172:175], a[164:167], v[148:151], v[172:175]\n"
    "ds_read_b128 v[236:239], v244 offset:8192\n"
    "ds_read_u16_d16_hi v202, v202\n"
    "v_mfma_f32_16x16x32_bf16 v[172:175], a[240:243], v[152:155], v[172:175]\n"
    "s_waitcnt lgkmcnt(7)\n"
    "ds_read_u16 v195, v195\n"
    "v_or_b32_e64 v191, v191, v207\n"
    "v_smfmac_f32_16x16x64_bf16 v[56:59], v[216:219], v[208:215], v240\n"
    "s_waitcnt lgkmcnt(7)\n"
    "ds_read_b128 a[244:247], v244 offset:10240\n"
    "v_smfmac_f32_16x16x64_bf16 v[60:63], v[220:223], v[208:215], v240 abid:1\n"
    "s_barrier\n"
    "v_lshlrev_b32_sdwa v207, 1, v183 dst_sel:DWORD dst_unused:UNUSED_PAD src0_sel:DWORD src1_sel:WORD_1\n"
    "v_fma_mix_f32 v232, v184, v232, 0 op_sel:[1,1,0] op_sel_hi:[1,1,0]\n"
    "v_mfma_f32_16x16x32_bf16 v[176:179], a[112:115], v[156:159], 0\n"
    "s_waitcnt vmcnt(6) lgkmcnt(6)\n"
    "ds_read_u16_d16_hi v203, v203\n"
    "ds_write2_b32 v248, v64, v65 offset0:0 offset1:20\n"
    "v_cvt_pk_bf16_f32 v184, v234, v232\n"
    "v_pk_add_f32 v[224:225], v[224:225], v[56:57]\n"
    "v_pk_add_f32 v[226:227], v[226:227], v[58:59]\n"
    "v_pk_fma_f32 v[228:229], v[56:57], v[56:57], v[228:229]\n"
    "v_pk_fma_f32 v[230:231], v[58:59], v[58:59], v[230:231]\n"
    "v_pk_add_f32 v[224:225], v[224:225], v[60:61]\n"
    "v_pk_add_f32 v[226:227], v[226:227], v[62:63]\n"
    "v_pk_fma_f32 v[228:229], v[60:61], v[60:61], v[228:229]\n"
    "v_pk_fma_f32 v[230:231], v[62:63], v[62:63], v[230:231]\n"
    "v_mfma_f32_16x16x32_bf16 v[176:179], a[160:163], v[160:163], v[176:179]\n"
    "ds_read_u16 v196, v196\n"
    "ds_write2_b32 v248, v66, v67 offset0:40 offset1:60\n"
    "v_mfma_f32_16x16x32_bf16 v[176:179], a[72:75], v[164:167], v[176:179]\n"
    "v_add_u32_e32 v248, 1360, v248\n"
    "v_pk_fma_f16 v232, v188, v188, s48\n"
    "v_mfma_f32_16x16x32_bf16 v[180:183], a[116:119], v[156:159], 0\n"
    "s_waitcnt lgkmcnt(6)\n"
    "ds_read_u16_d16_hi v204, v204\n"
    "ds_write2_b32 v235, v84, v85 offset0:0 offset1:20\n"
    "v_pk_mul_f16 v232, v188, v232\n"
    "v_mfma_f32_16x16x32_bf16 v[180:183], a[164:167], v[160:163], v[180:183]\n"
    "v_exp_f16_e32 v234, v232\n"
    "v_mfma_f32_16x16x32_bf16 v[180:183], a[240:243], v[164:167], v[180:183]\n"
    "ds_read_u16 v197, v197\n"
    "ds_write2_b32 v235, v86, v87 offset0:40 offset1:60\n"
    "v_add_u32_e32 v235, 1360, v235\n"
    "v_exp_f16_sdwa v234, v232 dst_sel:WORD_1 dst_unused:UNUSED_PRESERVE src0_sel:WORD_1\n"
    "v_or_b32_e32 v192, v192, v200\n"
    "v_pk_fma_f16 v234, v234, s49, s49\n"
    "v_mfma_f32_16x16x32_bf16 v[96:99], a[32:35], v[184:187], v[96:99]\n"
    "s_waitcnt vmcnt(5) lgkmcnt(6)\n"
    "ds_read_b128 a[252:255], v249 offset:3072\n"
    "ds_write2_b32 v248, v68, v69 offset0:0 offset1:20\n"
    "v_mfma_f32_16x16x32_bf16 v[100:103], a[36:39], v[184:187], v[100:103]\n"
    "v_rcp_f16_e32 v232, v234\n"
    "v_mfma_f32_16x16x32_bf16 v[112:115], a[40:43], v[184:187], v[112:115]\n"
    "ds_read_u16_d16_hi v205, v205\n"
    "ds_write2_b32 v248, v70, v71 offset0:40 offset1:60\n"
    "v_add_u32_e32 v248, 1360, v248\n"
    "v_rcp_f16_sdwa v232, v234 dst_sel:WORD_1 dst_unused:UNUSED_PRESERVE src0_sel:WORD_1\n"
    "v_mfma_f32_16x16x32_bf16 v[116:119], a[44:47], v[184:187], v[116:119]\n"
    "s_waitcnt lgkmcnt(6)\n"
    "ds_read_u16 v198, v198\n"
    "ds_read_u16_d16_hi v206, v206\n"
    "v_fma_mix_f32 v234, v188, v232, 0 op_sel_hi:[1,1,0]\n"
    "v_mfma_f32_16x16x32_bf16 v[128:131], v[236:239], v[184:187], v[128:131]\n"
    "ds_read_u16 v199, v199\n"
    "v_fma_mix_f32 v232, v188, v232, 0 op_sel:[1,1,0] op_sel_hi:[1,1,0]\n"
    "v_mfma_f32_16x16x32_bf16 v[132:135], a[244:247], v[184:187], v[132:135]\n"
    "ds_read_u16_d16_hi v207, v207\n"
    "v_cvt_pk_bf16_f32 v188, v234, v232\n"
    "v_or_b32_e32 v193, v193, v201\n"
    "v_cvt_pk_f16_f32 v184, v168, v169\n"
    "v_mfma_f32_16x16x32_bf16 v[104:107], a[32:35], v[188:191], v[104:107]\n"
    "s_waitcnt lgkmcnt(6)\n"
    "ds_read_b128 a[240:243], v244 offset:38912\n"
    "buffer_store_dwordx4 a[252:255], v255, s[44:47], s88 offen offset:64 sc1\n"
    "v_mfma_f32_16x16x32_bf16 v[108:111], a[36:39], v[188:191], v[108:111]\n"
    "v_lshlrev_b32_sdwa v185, 1, v170 dst_sel:DWORD dst_unused:UNUSED_PAD src0_sel:DWORD src1_sel:WORD_1\n"
    "v_lshlrev_b32_sdwa v201, 1, v171 dst_sel:DWORD dst_unused:UNUSED_PAD src0_sel:DWORD src1_sel:WORD_1\n"
    "v_mfma_f32_16x16x32_bf16 v[120:123], a[40:43], v[188:191], v[120:123]\n"
    "ds_read_u16 v185, v185\n"
    "ds_read_b128 a[252:255], v249 offset:5120\n"
    "v_mfma_f32_16x16x32_bf16 v[124:127], a[44:47], v[188:191], v[124:127]\n"
    "s_waitcnt lgkmcnt(7)\n"
    "ds_read_u16_d16_hi v201, v201\n"
    "v_lshlrev_b32_sdwa v186, 1, v172 dst_sel:DWORD dst_unused:UNUSED_PAD src0_sel:DWORD src1_sel:WORD_1\n"
    "v_mfma_f32_16x16x32_bf16 v[136:139], v[236:239], v[188:191], v[136:139]\n"
    "v_or_b32_e32 v194, v194, v202\n"
    "v_lshlrev_b32_sdwa v202, 1, v173 dst_sel:DWORD dst_unused:UNUSED_PAD src0_sel:DWORD src1_sel:WORD_1\n"
    "v_mfma_f32_16x16x32_bf16 v[140:143], a[244:247], v[188:191], v[140:143]\n"
    "v_lshlrev_b32_sdwa v187, 1, v174 dst_sel:DWORD dst_unused:UNUSED_PAD src0_sel:DWORD src1_sel:WORD_1\n"
    "v_cvt_pk_f16_f32 v188, v176, v177\n"
    "v_mfma_f32_16x16x32_bf16 v[168:171], a[120:123], v[144:147], 0\n"
    "ds_write2_b32 v235, v88, v89 offset0:0 offset1:20\n"
    "v_mfma_f32_16x16x32_bf16 v[168:171], a[168:171], v[148:151], v[168:171]\n"
    "s_nop 0\n"
    "v_lshlrev_b32_sdwa v189, 1, v178 dst_sel:DWORD dst_unused:UNUSED_PAD src0_sel:DWORD src1_sel:WORD_1\n"
    "v_lshlrev_b32_sdwa v190, 1, v180 dst_sel:DWORD dst_unused:UNUSED_PAD src0_sel:DWORD src1_sel:WORD_1\n"
    "v_mfma_f32_16x16x32_bf16 v[168:171], a[76:79], v[152:155], v[168:171]\n"
    "s_waitcnt lgkmcnt(7)\n"
    "ds_read_u16 v186, v186\n"
    "ds_write2_b32 v235, v90, v91 offset0:40 offset1:60\n"
    "v_add_u32_e32 v235, -1360, v235\n"
    "v_lshlrev_b32_sdwa v191, 1, v182 dst_sel:DWORD dst_unused:UNUSED_PAD src0_sel:DWORD src1_sel:WORD_1\n"
    "v_or_b32_e64 v195, v195, v203\n"
    "v_lshlrev_b32_sdwa v203, 1, v175 dst_sel:DWORD dst_unused:UNUSED_PAD src0_sel:DWORD src1_sel:WORD_1\n"
    "v_mfma_f32_16x16x32_bf16 v[172:175], a[124:127], v[144:147], 0\n"
    "ds_read_u16_d16_hi v202, v202\n"
    "ds_read_b128 v[236:239], v244 offset:12288\n"
    "v_mfma_f32_16x16x32_bf16 v[172:175], a[172:175], v[148:151], v[172:175]\n"
    "s_waitcnt lgkmcnt(8)\n"
    "ds_read_u16 v187, v187\n"
    "v_or_b32_e32 v196, v196, v204\n"
    "v_mfma_f32_16x16x32_bf16 v[172:175], a[240:243], v[152:155], v[172:175]\n"
    "v_or_b32_e64 v197, v197, v205\n"
    "v_lshlrev_b32_sdwa v205, 1, v179 dst_sel:DWORD dst_unused:UNUSED_PAD src0_sel:DWORD src1_sel:WORD_1\n"
    "v_mfma_f32_16x16x32_bf16 v[176:179], a[120:123], v[156:159], 0\n"
    "s_waitcnt lgkmcnt(7)\n"
    "buffer_store_dwordx4 a[252:255], v255, s[44:47], s88 offen offset:128 sc1\n"
    "ds_write2_b32 v248, v72, v73 offset0:0 offset1:20\n"
    "v_mfma_f32_16x16x32_bf16 v[176:179], a[168:171], v[160:163], v[176:179]\n"
    "s_nop 0\n"
    "v_or_b32_e32 v198, v198, v206\n"
    "v_lshlrev_b32_sdwa v206, 1, v181 dst_sel:DWORD dst_unused:UNUSED_PAD src0_sel:DWORD src1_sel:WORD_1\n"
    "v_mfma_f32_16x16x32_bf16 v[176:179], a[76:79], v[164:167], v[176:179]\n"
    "s_waitcnt lgkmcnt(6)\n"
    "ds_read_u16_d16_hi v203, v203\n"
    "ds_write2_b32 v248, v74, v75 offset0:40 offset1:60\n"
    "v_mfma_f32_16x16x32_bf16 v[96:99], a[176:179], v[192:195], v[96:99]\n"
    "v_add_u32_e32 v248, 1360, v248\n"
    "v_or_b32_e64 v199, v199, v207\n"
    "v_mfma_f32_16x16x32_bf16 v[100:103], a[180:183], v[192:195], v[100:103]\n"
    "ds_read_b128 a[244:247], v244 offset:14336\n"
    "ds_write2_b32 v248, v76, v77 offset0:0 offset1:20\n"
    "v_mfma_f32_16x16x32_bf16 v[112:115], a[184:187], v[192:195], v[112:115]\n"
    "v_lshlrev_b32_sdwa v207, 1, v183 dst_sel:DWORD dst_unused:UNUSED_PAD src0_sel:DWORD src1_sel:WORD_1\n"
    "v_or_b32_e32 v185, v185, v201\n"
    "v_mfma_f32_16x16x32_bf16 v[180:183], a[124:127], v[156:159], 0\n"
    "s_waitcnt lgkmcnt(6)\n"
    "ds_read_u16 v189, v189\n"
    "ds_write2_b32 v248, v78, v79 offset0:40 offset1:60\n"
    "v_mfma_f32_16x16x32_bf16 v[180:183], a[172:175], v[160:163], v[180:183]\n"
    "v_add_u32_e32 v248, 1360, v248\n"
    "v_or_b32_e64 v186, v186, v202\n"
    "v_mfma_f32_16x16x32_bf16 v[180:183], a[240:243], v[164:167], v[180:183]\n"
    "ds_read_u16_d16_hi v205, v205\n"
    "ds_read_u16 v190, v190\n"
    "v_mfma_f32_16x16x32_bf16 v[116:119], a[188:191], v[192:195], v[116:119]\n"
    "ds_read_b128 v[144:147], v243 offset:0\n"
    "ds_read_u16_d16_hi v206, v206\n"
    "v_mfma_f32_16x16x32_bf16 v[128:131], v[236:239], v[192:195], v[128:131]\n"
    "v_pk_fma_f16 v232, v184, v184, s48\n"
    "v_lshlrev_b32_sdwa v201, 1, v171 dst_sel:DWORD dst_unused:UNUSED_PAD src0_sel:DWORD src1_sel:WORD_1\n"
    "v_mfma_f32_16x16x32_bf16 v[104:107], a[176:179], v[196:199], v[104:107]\n"
    "s_waitcnt lgkmcnt(8)\n"
    "ds_read_b128 v[148:151], v243 offset:1024\n"
    "v_or_b32_e64 v187, v187, v203\n"
    "v_mfma_f32_16x16x32_bf16 v[108:111], a[180:183], v[196:199], v[108:111]\n"
    "s_waitcnt lgkmcnt(7)\n"
    "ds_read_u16 v191, v191\n"
    "v_pk_mul_f16 v232, v184, v232\n"
    "v_mfma_f32_16x16x32_bf16 v[132:135], a[244:247], v[192:195], v[132:135]\n"
    "v_exp_f16_e64 v234, v232\n"
    "v_mfma_f32_16x16x32_bf16 v[120:123], a[184:187], v[196:199], v[120:123]\n"
    "v_exp_f16_sdwa v234, v232 dst_sel:WORD_1 dst_unused:UNUSED_PRESERVE src0_sel:WORD_1\n"
    "v_mfma_f32_16x16x32_bf16 v[124:127], a[188:191], v[196:199], v[124:127]\n"
    "ds_write2_b32 v248, v80, v81 offset0:0 offset1:20\n"
    "v_mfma_f32_16x16x32_bf16 v[136:139], v[236:239], v[196:199], v[136:139]\n"
    "v_pk_fma_f16 v234, v234, s49, s49\n"
    "v_cvt_pk_f16_f32 v192, v168, v169\n"
    "v_mfma_f32_16x16x32_bf16 v[140:143], a[244:247], v[196:199], v[140:143]\n"
    "ds_read_b128 v[152:155], v243 offset:2048\n"
    "ds_read_u16_d16_hi v207, v207\n"
    "v_lshlrev_b32_sdwa v193, 1, v170 dst_sel:DWORD dst_unused:UNUSED_PAD src0_sel:DWORD src1_sel:WORD_1\n"
    "v_lshlrev_b32_sdwa v194, 1, v172 dst_sel:DWORD dst_unused:UNUSED_PAD src0_sel:DWORD src1_sel:WORD_1\n"
    "s_waitcnt lgkmcnt(8)\n"
    "v_or_b32_e32 v189, v189, v205\n"
    "v_lshlrev_b32_sdwa v202, 1, v173 dst_sel:DWORD dst_unused:UNUSED_PAD src0_sel:DWORD src1_sel:WORD_1\n"
    "v_lshlrev_b32_sdwa v195, 1, v174 dst_sel:DWORD dst_unused:UNUSED_PAD src0_sel:DWORD src1_sel:WORD_1\n"
    "v_lshlrev_b32_sdwa v203, 1, v175 dst_sel:DWORD dst_unused:UNUSED_PAD src0_sel:DWORD src1_sel:WORD_1\n"
    "v_rcp_f16_e32 v232, v234\n"
    "s_waitcnt lgkmcnt(6)\n"
    "v_mfma_f32_16x16x32_bf16 v[168:171], a[80:83], v[144:147], 0\n"
    "ds_read_b128 v[156:159], v243 offset:3072\n"
    "ds_write2_b32 v248, v82, v83 offset0:40 offset1:60\n"
    "v_mfma_f32_32x32x1_2b_f32 v[64:95], v254, v254, 0\n"
    "buffer_load_dwordx4 a[232:235], v245, s[40:43], s58 offen offset:64\n"
    "buffer_load_dwordx4 a[224:227], v245, s[40:43], s58 offen offset:0\n"
    "v_add_u32_e32 v248, -5440, v248\n"
    "s_waitcnt lgkmcnt(6)\n"
    "v_or_b32_e32 v190, v190, v206\n"
    "v_rcp_f16_sdwa v232, v234 dst_sel:WORD_1 dst_unused:UNUSED_PRESERVE src0_sel:WORD_1\n"
    "v_cvt_pk_f16_f32 v196, v176, v177\n"
    "v_lshlrev_b32_sdwa v197, 1, v178 dst_sel:DWORD dst_unused:UNUSED_PAD src0_sel:DWORD src1_sel:WORD_1\n"
    "v_lshlrev_b32_sdwa v205, 1, v179 dst_sel:DWORD dst_unused:UNUSED_PAD src0_sel:DWORD src1_sel:WORD_1\n"
    "v_lshlrev_b32_sdwa v198, 1, v180 dst_sel:DWORD dst_unused:UNUSED_PAD src0_sel:DWORD src1_sel:WORD_1\n"
    "v_lshlrev_b32_sdwa v206, 1, v181 dst_sel:DWORD dst_unused:UNUSED_PAD src0_sel:DWORD src1_sel:WORD_1\n"
    "v_lshlrev_b32_sdwa v199, 1, v182 dst_sel:DWORD dst_unused:UNUSED_PAD src0_sel:DWORD src1_sel:WORD_1\n"
    "v_fma_mix_f32 v234, v184, v232, 0 op_sel_hi:[1,1,0]\n"
    "v_mfma_f32_16x16x32_bf16 v[168:171], a[128:131], v[148:151], v[168:171]\n"
    "s_waitcnt lgkmcnt(3)\n"
    "ds_read_b128 v[160:163], v243 offset:4096\n"
    "ds_read_b128 v[164:167], v243 offset:5120\n"
    "v_mfma_f32_16x16x32_bf16 v[168:171], a[48:51], v[152:155], v[168:171]\n"
    "s_nop 0\n"
    "ds_read_b128 a[240:243], v244 offset:32768\n"
    "ds_read_u16 v193, v193\n"
    "v_mfma_f32_16x16x32_bf16 v[172:175], a[84:87], v[144:147], 0\n"
    "ds_read_u16_d16_hi v201, v201\n"
    "v_fma_mix_f32 v232, v184, v232, 0 op_sel:[1,1,0] op_sel_hi:[1,1,0]\n"
    "v_mfma_f32_16x16x32_bf16 v[172:175], a[132:135], v[148:151], v[172:175]\n"
    "s_waitcnt lgkmcnt(5)\n"
    "ds_read_u16 v194, v194\n"
    "v_or_b32_e32 v191, v191, v207\n"
    "v_mfma_f32_16x16x32_bf16 v[176:179], a[80:83], v[156:159], 0\n"
    "v_lshlrev_b32_sdwa v207, 1, v183 dst_sel:DWORD dst_unused:UNUSED_PAD src0_sel:DWORD src1_sel:WORD_1\n"
    "v_cvt_pk_bf16_f32 v184, v234, v232\n"
    "v_pk_fma_f16 v232, v188, v188, s48\n"
    "v_mfma_f32_16x16x32_bf16 v[180:183], a[84:87], v[156:159], 0\n"
    "ds_read_u16_d16_hi v202, v202\n"
    "v_pk_mul_f16 v232, v188, v232\n"
    "v_lshlrev_b32_e64 v254, 3, v244\n"
    "v_exp_f16_e64 v234, v232\n"
    "v_lshlrev_b32_sdwa v200, 1, v169 dst_sel:DWORD dst_unused:UNUSED_PAD src0_sel:DWORD src1_sel:WORD_1\n"
    "v_exp_f16_sdwa v234, v232 dst_sel:WORD_1 dst_unused:UNUSED_PRESERVE src0_sel:WORD_1\n"
    "v_mfma_f32_16x16x32_bf16 v[96:99], a[192:195], v[184:187], v[96:99]\n"
    "s_nop 0\n"
    "ds_read_b128 v[236:239], v244 offset:16384\n"
    "ds_read_u16 v195, v195\n"
    "s_waitcnt lgkmcnt(8)\n"
    "v_mfma_f32_16x16x32_bf16 v[176:179], a[128:131], v[160:163], v[176:179]\n"
    "s_waitcnt lgkmcnt(7)\n"
    "ds_read_b128 v[216:219], v251 offset:21504\n"
    "s_mov_b32 exec_hi, 0\n"
    "buffer_load_dword v233, v254, s[72:75], s53 offen sc1\n"
    "s_mov_b32 exec_hi, -1\n"
    "v_mfma_f32_16x16x32_bf16 v[176:179], a[48:51], v[164:167], v[176:179]\n"
    "s_waitcnt lgkmcnt(7)\n"
    "ds_read_b128 v[220:223], v251 offset:21840\n"
    "ds_read_u16_d16_hi v203, v203\n"
    "v_mfma_f32_16x16x32_bf16 v[172:175], a[240:243], v[152:155], v[172:175]\n"
    "s_barrier\n"
    "v_mfma_f32_16x16x32_bf16 v[180:183], a[132:135], v[160:163], v[180:183]\n"
    "s_waitcnt lgkmcnt(7)\n"
    "ds_read_b128 v[208:211], v250 offset:0\n"
    "ds_read_b128 v[212:215], v250 offset:320\n"
    "v_mfma_f32_16x16x32_bf16 v[180:183], a[240:243], v[164:167], v[180:183]\n"
    "s_add_u32 s53, s53, 393216\n"
    "ds_read_b128 a[244:247], v244 offset:18432\n"
    "ds_read_u16 v197, v197\n"
    "v_pk_fma_f16 v234, v234, s49, s49\n"
    "v_mfma_f32_16x16x32_bf16 v[100:103], a[196:199], v[184:187], v[100:103]\n"
    "ds_read_u16_d16_hi v205, v205\n"
    "v_rcp_f16_e32 v232, v234\n"
    "v_mfma_f32_16x16x32_bf16 v[112:115], a[200:203], v[184:187], v[112:115]\n"
    "s_waitcnt lgkmcnt(9)\n"
    "v_rcp_f16_sdwa v232, v234 dst_sel:WORD_1 dst_unused:UNUSED_PRESERVE src0_sel:WORD_1\n"
    "v_mfma_f32_16x16x32_bf16 v[116:119], a[204:207], v[184:187], v[116:119]\n"
    "ds_read_u16 v198, v198\n"
    "buffer_load_dwordx4 a[236:239], v245, s[40:43], s89 offen offset:64\n"
    "buffer_load_dwordx4 a[228:231], v245, s[40:43], s89 offen offset:0\n"
    "v_mfma_f32_16x16x32_bf16 v[128:131], v[236:239], v[184:187], v[128:131]\n"
    "ds_read_u16_d16_hi v206, v206\n"
    "v_or_b32_e64 v193, v193, v201\n"
    "v_fma_mix_f32 v234, v188, v232, 0 op_sel_hi:[1,1,0]\n"
    "v_fma_mix_f32 v232, v188, v232, 0 op_sel:[1,1,0] op_sel_hi:[1,1,0]\n"
    "v_or_b32_e64 v194, v194, v202\n"
    "v_cvt_pk_bf16_f32 v188, v234, v232\n"
    "v_pk_fma_f16 v232, v192, v192, s48\n"
    "v_lshlrev_b32_sdwa v201, 1, v171 dst_sel:DWORD dst_unused:UNUSED_PAD src0_sel:DWORD src1_sel:WORD_1\n"
    "v_mfma_f32_16x16x32_bf16 v[104:107], a[192:195], v[188:191], v[104:107]\n"
    "ds_read_u16 v199, v199\n"
    "v_pk_mul_f16 v232, v192, v232\n"
    "v_mfma_f32_16x16x32_bf16 v[108:111], a[196:199], v[188:191], v[108:111]\n"
    "ds_read_u16_d16_hi v207, v207\n"
    "v_exp_f16_e32 v234, v232\n"
    "v_mfma_f32_16x16x32_bf16 v[120:123], a[200:203], v[188:191], v[120:123]\n"
    "s_waitcnt lgkmcnt(7)\n"
    "v_or_b32_e32 v195, v195, v203\n"
    "v_exp_f16_sdwa v234, v232 dst_sel:WORD_1 dst_unused:UNUSED_PRESERVE src0_sel:WORD_1\n"
    "v_smfmac_f32_16x16x64_bf16 v[64:67], v[216:219], v[208:215], v240\n"
    "s_waitcnt lgkmcnt(6)\n"
    "ds_read_b128 v[216:219], v252 offset:21504\n"
    "ds_read_b128 a[240:243], v244 offset:36864\n"
    "v_smfmac_f32_16x16x64_bf16 v[68:71], v[220:223], v[208:215], v240 abid:1\n"
    "ds_read_b128 v[208:211], v250 offset:640\n"
    "v_mfma_f32_16x16x32_bf16 v[132:135], a[244:247], v[184:187], v[132:135]\n"
    "ds_read_b128 v[212:215], v250 offset:960\n"
    "v_pk_fma_f16 v234, v234, s49, s49\n"
    "v_mfma_f32_16x16x32_bf16 v[124:127], a[204:207], v[188:191], v[124:127]\n"
    "ds_read_b128 v[220:223], v252 offset:21840\n"
    "v_lshlrev_b32_sdwa v184, 1, v168 dst_sel:DWORD dst_unused:UNUSED_PAD src0_sel:DWORD src1_sel:WORD_1\n"
    "v_mfma_f32_16x16x32_bf16 v[136:139], v[236:239], v[188:191], v[136:139]\n"
    "ds_read_u16 v184, v184\n"
    "v_lshlrev_b32_sdwa v185, 1, v170 dst_sel:DWORD dst_unused:UNUSED_PAD src0_sel:DWORD src1_sel:WORD_1\n"
    "v_mfma_f32_16x16x32_bf16 v[140:143], a[244:247], v[188:191], v[140:143]\n"
    "s_waitcnt lgkmcnt(10)\n"
    "v_or_b32_e32 v197, v197, v205\n"
    "v_lshlrev_b32_sdwa v186, 1, v172 dst_sel:DWORD dst_unused:UNUSED_PAD src0_sel:DWORD src1_sel:WORD_1\n"
    "v_mfma_f32_16x16x32_bf16 v[168:171], a[88:91], v[144:147], 0\n"
    "ds_read_u16_d16_hi v200, v200\n"
    "v_lshlrev_b32_sdwa v202, 1, v173 dst_sel:DWORD dst_unused:UNUSED_PAD src0_sel:DWORD src1_sel:WORD_1\n"
    "v_mfma_f32_16x16x32_bf16 v[168:171], a[136:139], v[148:151], v[168:171]\n"
    "v_lshlrev_b32_sdwa v187, 1, v174 dst_sel:DWORD dst_unused:UNUSED_PAD src0_sel:DWORD src1_sel:WORD_1\n"
    "v_lshlrev_b32_sdwa v203, 1, v175 dst_sel:DWORD dst_unused:UNUSED_PAD src0_sel:DWORD src1_sel:WORD_1\n"
    "v_rcp_f16_e32 v232, v234\n"
    "v_mfma_f32_16x16x32_bf16 v[168:171], a[52:55], v[152:155], v[168:171]\n"
    "s_waitcnt lgkmcnt(9)\n"
    "ds_read_u16 v185, v185\n"
    "v_or_b32_e64 v198, v198, v206\n"
    "v_rcp_f16_sdwa v232, v234 dst_sel:WORD_1 dst_unused:UNUSED_PRESERVE src0_sel:WORD_1\n"
    "v_mfma_f32_16x16x32_bf16 v[172:175], a[92:95], v[144:147], 0\n"
    "ds_read_u16_d16_hi v201, v201\n"
    "v_lshlrev_b32_sdwa v188, 1, v176 dst_sel:DWORD dst_unused:UNUSED_PAD src0_sel:DWORD src1_sel:WORD_1\n"
    "v_mfma_f32_16x16x32_bf16 v[172:175], a[140:143], v[148:151], v[172:175]\n"
    "ds_read_u16 v186, v186\n"
    "v_lshlrev_b32_sdwa v204, 1, v177 dst_sel:DWORD dst_unused:UNUSED_PAD src0_sel:DWORD src1_sel:WORD_1\n"
    "v_lshlrev_b32_sdwa v189, 1, v178 dst_sel:DWORD dst_unused:UNUSED_PAD src0_sel:DWORD src1_sel:WORD_1\n"
    "v_lshlrev_b32_sdwa v205, 1, v179 dst_sel:DWORD dst_unused:UNUSED_PAD src0_sel:DWORD src1_sel:WORD_1\n"
    "v_lshlrev_b32_sdwa v190, 1, v180 dst_sel:DWORD dst_unused:UNUSED_PAD src0_sel:DWORD src1_sel:WORD_1\n"
    "v_lshlrev_b32_sdwa v206, 1, v181 dst_sel:DWORD dst_unused:UNUSED_PAD src0_sel:DWORD src1_sel:WORD_1\n"
    "v_lshlrev_b32_sdwa v191, 1, v182 dst_sel:DWORD dst_unused:UNUSED_PAD src0_sel:DWORD src1_sel:WORD_1\n"
    "v_fma_mix_f32 v234, v192, v232, 0 op_sel_hi:[1,1,0]\n"
    "s_waitcnt lgkmcnt(7)\n"
    "v_mfma_f32_16x16x32_bf16 v[172:175], a[240:243], v[152:155], v[172:175]\n"
    "s_nop 0\n"
    "ds_read_b128 v[236:239], v244 offset:20480\n"
    "ds_read_u16_d16_hi v202, v202\n"
    "s_waitcnt lgkmcnt(8)\n"
    "v_smfmac_f32_16x16x64_bf16 v[64:67], v[216:219], v[208:215], v240\n"
    "s_waitcnt lgkmcnt(7)\n"
    "v_or_b32_e64 v199, v199, v207\n"
    "v_smfmac_f32_16x16x64_bf16 v[68:71], v[220:223], v[208:215], v240 abid:1\n"
    "ds_read_b128 v[208:211], v250 offset:80\n"
    "ds_read_b128 v[212:215], v250 offset:400\n"
    "v_lshlrev_b32_sdwa v207, 1, v183 dst_sel:DWORD dst_unused:UNUSED_PAD src0_sel:DWORD src1_sel:WORD_1\n"
    "v_fma_mix_f32 v232, v192, v232, 0 op_sel:[1,1,0] op_sel_hi:[1,1,0]\n"
    "v_mfma_f32_16x16x32_bf16 v[176:179], a[88:91], v[156:159], 0\n"
    "ds_read_b128 v[216:219], v251 offset:22176\n"
    "v_cvt_pk_bf16_f32 v192, v234, v232\n"
    "v_pk_fma_f16 v232, v196, v196, s48\n"
    "v_lshlrev_b32_e64 v254, 3, v244\n"
    "v_pk_mul_f16 v232, v196, v232\n"
    "v_pk_add_f32 v[224:225], v[224:225], v[64:65]\n"
    "v_pk_add_f32 v[226:227], v[226:227], v[66:67]\n"
    "v_pk_fma_f32 v[228:229], v[64:65], v[64:65], v[228:229]\n"
    "v_pk_fma_f32 v[230:231], v[66:67], v[66:67], v[230:231]\n"
    "v_pk_add_f32 v[224:225], v[224:225], v[68:69]\n"
    "v_pk_add_f32 v[226:227], v[226:227], v[70:71]\n"
    "v_pk_fma_f32 v[228:229], v[68:69], v[68:69], v[228:229]\n"
    "v_pk_fma_f32 v[230:231], v[70:71], v[70:71], v[230:231]\n"
    "v_mfma_f32_16x16x32_bf16 v[176:179], a[136:139], v[160:163], v[176:179]\n"
    "s_waitcnt lgkmcnt(7)\n"
    "ds_read_b128 v[220:223], v251 offset:22512\n"
    "ds_read_u16 v187, v187\n"
    "v_mfma_f32_16x16x32_bf16 v[176:179], a[52:55], v[164:167], v[176:179]\n"
    "s_nop 0\n"
    "ds_read_b128 a[244:247], v244 offset:22528\n"
    "ds_read_u16_d16_hi v203, v203\n"
    "v_mfma_f32_16x16x32_bf16 v[180:183], a[92:95], v[156:159], 0\n"
    "s_waitcnt lgkmcnt(10)\n"
    "v_exp_f16_e32 v234, v232\n"
    "v_mfma_f32_16x16x32_bf16 v[180:183], a[140:143], v[160:163], v[180:183]\n"
    "ds_read_u16 v188, v188\n"
    "v_exp_f16_sdwa v234, v232 dst_sel:WORD_1 dst_unused:UNUSED_PRESERVE src0_sel:WORD_1\n"
    "v_mfma_f32_16x16x32_bf16 v[180:183], a[240:243], v[164:167], v[180:183]\n"
    "ds_read_u16_d16_hi v204, v204\n"
    "v_pk_fma_f16 v234, v234, s49, s49\n"
    "v_mfma_f32_16x16x32_bf16 v[96:99], a[208:211], v[192:195], v[96:99]\n"
    "v_rcp_f16_e32 v232, v234\n"
    "v_mfma_f32_16x16x32_bf16 v[100:103], a[212:215], v[192:195], v[100:103]\n"
    "s_waitcnt lgkmcnt(8)\n"
    "ds_read_u16 v189, v189\n"
    "v_rcp_f16_sdwa v232, v234 dst_sel:WORD_1 dst_unused:UNUSED_PRESERVE src0_sel:WORD_1\n"
    "v_mfma_f32_16x16x32_bf16 v[112:115], a[216:219], v[192:195], v[112:115]\n"
    "s_mov_b32 exec_hi, 0\n"
    "buffer_load_dword v233, v254, s[72:75], s53 offen sc1\n"
    "s_mov_b32 exec_hi, -1\n"
    "v_mfma_f32_16x16x32_bf16 v[116:119], a[220:223], v[192:195], v[116:119]\n"
    "s_waitcnt lgkmcnt(7)\n"
    "ds_read_u16_d16_hi v205, v205\n"
    "v_fma_mix_f32 v234, v196, v232, 0 op_sel_hi:[1,1,0]\n"
    "v_smfmac_f32_16x16x64_bf16 v[72:75], v[216:219], v[208:215], v240\n"
    "s_waitcnt lgkmcnt(7)\n"
    "ds_read_b128 v[216:219], v252 offset:22176\n"
    "v_smfmac_f32_16x16x64_bf16 v[76:79], v[220:223], v[208:215], v240 abid:1\n"
    "s_add_u32 s53, s53, 393216\n"
    "ds_read_b128 v[208:211], v250 offset:720\n"
    "v_mfma_f32_16x16x32_bf16 v[128:131], v[236:239], v[192:195], v[128:131]\n"
    "ds_read_b128 v[212:215], v250 offset:1040\n"
    "v_fma_mix_f32 v232, v196, v232, 0 op_sel:[1,1,0] op_sel_hi:[1,1,0]\n"
    "s_waitcnt lgkmcnt(8)\n"
    "v_mfma_f32_16x16x32_bf16 v[132:135], a[244:247], v[192:195], v[132:135]\n"
    "s_nop 0\n"
    "ds_read_b128 v[220:223], v252 offset:22512\n"
    "v_cvt_pk_bf16_f32 v196, v234, v232\n"
    "v_or_b32_e64 v184, v184, v200\n"
    "v_or_b32_e32 v185, v185, v201\n"
    "v_mfma_f32_16x16x32_bf16 v[104:107], a[208:211], v[196:199], v[104:107]\n"
    "ds_read_u16 v190, v190\n"
    "v_cvt_pk_f16_f32 v192, v168, v169\n"
    "v_mfma_f32_16x16x32_bf16 v[108:111], a[212:215], v[196:199], v[108:111]\n"
    "s_waitcnt lgkmcnt(9)\n"
    "ds_read_u16_d16_hi v206, v206\n"
    "v_lshlrev_b32_sdwa v193, 1, v170 dst_sel:DWORD dst_unused:UNUSED_PAD src0_sel:DWORD src1_sel:WORD_1\n"
    "v_mfma_f32_16x16x32_bf16 v[120:123], a[216:219], v[196:199], v[120:123]\n"
    "v_lshlrev_b32_sdwa v201, 1, v171 dst_sel:DWORD dst_unused:UNUSED_PAD src0_sel:DWORD src1_sel:WORD_1\n"
    "v_lshlrev_b32_sdwa v194, 1, v172 dst_sel:DWORD dst_unused:UNUSED_PAD src0_sel:DWORD src1_sel:WORD_1\n"
    "v_mfma_f32_16x16x32_bf16 v[124:127], a[220:223], v[196:199], v[124:127]\n"
    "ds_read_u16 v191, v191\n"
    "v_or_b32_e32 v186, v186, v202\n"
    "v_mfma_f32_16x16x32_bf16 v[136:139], v[236:239], v[196:199], v[136:139]\n"
    "s_waitcnt lgkmcnt(9)\n"
    "v_lshlrev_b32_sdwa v202, 1, v173 dst_sel:DWORD dst_unused:UNUSED_PAD src0_sel:DWORD src1_sel:WORD_1\n"
    "v_lshlrev_b32_sdwa v195, 1, v174 dst_sel:DWORD dst_unused:UNUSED_PAD src0_sel:DWORD src1_sel:WORD_1\n"
    "v_mfma_f32_16x16x32_bf16 v[140:143], a[244:247], v[196:199], v[140:143]\n"
    "ds_read_u16_d16_hi v207, v207\n"
    "v_cvt_pk_f16_f32 v196, v176, v177\n"
    "v_mfma_f32_16x16x32_bf16 v[168:171], a[96:99], v[144:147], 0\n"
    "v_lshlrev_b32_sdwa v197, 1, v178 dst_sel:DWORD dst_unused:UNUSED_PAD src0_sel:DWORD src1_sel:WORD_1\n"
    "v_lshlrev_b32_sdwa v198, 1, v180 dst_sel:DWORD dst_unused:UNUSED_PAD src0_sel:DWORD src1_sel:WORD_1\n"
    "v_mfma_f32_16x16x32_bf16 v[168:171], a[144:147], v[148:151], v[168:171]\n"
    "ds_read_u16 v193, v193\n"
    "v_lshlrev_b32_sdwa v199, 1, v182 dst_sel:DWORD dst_unused:UNUSED_PAD src0_sel:DWORD src1_sel:WORD_1\n"
    "v_mfma_f32_16x16x32_bf16 v[168:171], a[56:59], v[152:155], v[168:171]\n"
    "s_waitcnt lgkmcnt(8)\n"
    "ds_read_u16_d16_hi v201, v201\n"
    "v_or_b32_e32 v187, v187, v203\n"
    "v_lshlrev_b32_sdwa v203, 1, v175 dst_sel:DWORD dst_unused:UNUSED_PAD src0_sel:DWORD src1_sel:WORD_1\n"
    "v_mfma_f32_16x16x32_bf16 v[172:175], a[100:103], v[144:147], 0\n"
    "s_waitcnt lgkmcnt(7)\n"
    "v_or_b32_e32 v188, v188, v204\n"
    "v_or_b32_e32 v189, v189, v205\n"
    "v_smfmac_f32_16x16x64_bf16 v[72:75], v[216:219], v[208:215], v240\n"
    "s_waitcnt lgkmcnt(6)\n"
    "ds_read_b128 v[216:219], v251 offset:22848\n"
    "ds_read_u16 v194, v194\n"
    "v_smfmac_f32_16x16x64_bf16 v[76:79], v[220:223], v[208:215], v240 abid:1\n"
    "v_lshlrev_b32_sdwa v205, 1, v179 dst_sel:DWORD dst_unused:UNUSED_PAD src0_sel:DWORD src1_sel:WORD_1\n"
    "v_mfma_f32_16x16x32_bf16 v[172:175], a[148:151], v[148:151], v[172:175]\n"
    "ds_read_b128 v[208:211], v250 offset:160\n"
    "v_cvt_pk_bf16_f32 v96, v96, v97\n"
    "v_cvt_pk_bf16_f32 v97, v98, v99\n"
    "v_cvt_pk_bf16_f32 v98, v100, v101\n"
    "ds_read_b128 v[212:215], v250 offset:480\n"
    "v_pk_add_f32 v[224:225], v[224:225], v[72:73]\n"
    "v_pk_add_f32 v[226:227], v[226:227], v[74:75]\n"
    "v_pk_fma_f32 v[228:229], v[72:73], v[72:73], v[228:229]\n"
    "v_pk_fma_f32 v[230:231], v[74:75], v[74:75], v[230:231]\n"
    "v_pk_add_f32 v[224:225], v[224:225], v[76:77]\n"
    "v_pk_add_f32 v[226:227], v[226:227], v[78:79]\n"
    "v_pk_fma_f32 v[228:229], v[76:77], v[76:77], v[228:229]\n"
    "v_pk_fma_f32 v[230:231], v[78:79], v[78:79], v[230:231]\n"
    "v_mfma_f32_16x16x32_bf16 v[172:175], a[60:63], v[152:155], v[172:175]\n"
    "s_waitcnt lgkmcnt(7)\n"
    "ds_read_b128 v[220:223], v251 offset:23184\n"
    "ds_read_u16_d16_hi v202, v202\n"
    "v_mfma_f32_16x16x32_bf16 v[176:179], a[96:99], v[156:159], 0\n"
    "s_nop 0\n"
    "ds_read_b128 v[236:239], v244 offset:0\n"
    "v_cvt_pk_bf16_f32 v99, v102, v103\n"
    "v_mfma_f32_16x16x32_bf16 v[176:179], a[144:147], v[160:163], v[176:179]\n"
    "s_waitcnt lgkmcnt(9)\n"
    "v_or_b32_e32 v190, v190, v206\n"
    "v_lshlrev_b32_sdwa v206, 1, v181 dst_sel:DWORD dst_unused:UNUSED_PAD src0_sel:DWORD src1_sel:WORD_1\n"
    "v_mfma_f32_16x16x32_bf16 v[176:179], a[56:59], v[164:167], v[176:179]\n"
    "ds_write_b128 v242, v[96:99] offset:24640\n"
    "v_mfma_f32_16x16x32_bf16 v[96:99], a[0:3], v[184:187], 0\n"
    "s_nop 0\n"
    "v_cvt_pk_bf16_f32 v112, v112, v113\n"
    "v_cvt_pk_bf16_f32 v113, v114, v115\n"
    "v_mfma_f32_16x16x32_bf16 v[100:103], a[4:7], v[184:187], 0\n"
    "s_waitcnt lgkmcnt(7)\n"
    "v_cvt_pk_bf16_f32 v114, v116, v117\n"
    "v_cvt_pk_bf16_f32 v115, v118, v119\n"
    "ds_write_b128 v242, v[112:115] offset:26688\n"
    "v_mfma_f32_16x16x32_bf16 v[112:115], a[8:11], v[184:187], 0\n"
    "v_cvt_pk_bf16_f32 v128, v128, v129\n"
    "v_cvt_pk_bf16_f32 v129, v130, v131\n"
    "s_waitcnt lgkmcnt(5)\n"
    "v_smfmac_f32_16x16x64_bf16 v[80:83], v[216:219], v[208:215], v240\n"
    "s_waitcnt lgkmcnt(4)\n"
    "ds_read_b128 v[216:219], v252 offset:22848\n"
    "ds_read_u16 v195, v195\n"
    "v_smfmac_f32_16x16x64_bf16 v[84:87], v[220:223], v[208:215], v240 abid:1\n"
    "ds_read_b128 v[208:211], v250 offset:800\n"
    "ds_read_b128 v[212:215], v250 offset:1120\n"
    "v_or_b32_e64 v191, v191, v207\n"
    "v_mfma_f32_16x16x32_bf16 v[116:119], a[12:15], v[184:187], 0\n"
    "ds_read_b128 v[220:223], v252 offset:23184\n"
    "ds_read_u16_d16_hi v203, v203\n"
    "v_lshlrev_b32_sdwa v207, 1, v183 dst_sel:DWORD dst_unused:UNUSED_PAD src0_sel:DWORD src1_sel:WORD_1\n"
    "v_mfma_f32_16x16x32_bf16 v[180:183], a[100:103], v[156:159], 0\n"
    "s_waitcnt lgkmcnt(7)\n"
    "ds_read_b128 a[244:247], v244 offset:2048\n"
    "v_cvt_pk_bf16_f32 v130, v132, v133\n"
    "v_mfma_f32_16x16x32_bf16 v[180:183], a[148:151], v[160:163], v[180:183]\n"
    "s_nop 0\n"
    "ds_read_u16 v197, v197\n"
    "v_or_b32_e64 v193, v193, v201\n"
    "v_mfma_f32_16x16x32_bf16 v[180:183], a[60:63], v[164:167], v[180:183]\n"
    "v_cvt_pk_bf16_f32 v131, v134, v135\n"
    "v_cvt_pk_bf16_f32 v104, v104, v105\n"
    "v_cvt_pk_bf16_f32 v105, v106, v107\n"
    "v_cvt_pk_bf16_f32 v106, v108, v109\n"
    "v_cvt_pk_bf16_f32 v107, v110, v111\n"
    "v_mfma_f32_16x16x32_bf16 v[108:111], a[4:7], v[188:191], 0\n"
    "v_or_b32_e64 v194, v194, v202\n"
    "ds_write_b128 v242, v[128:131] offset:28736\n"
    "v_mfma_f32_16x16x32_bf16 v[128:131], v[236:239], v[184:187], 0\n"
    "v_cvt_pk_bf16_f32 v120, v120, v121\n"
    "v_cvt_pk_bf16_f32 v121, v122, v123\n"
    "v_cvt_pk_bf16_f32 v122, v124, v125\n"
    "v_cvt_pk_bf16_f32 v123, v126, v127\n"
    "ds_write_b128 v242, v[104:107] offset:25664\n"
    "v_mfma_f32_16x16x32_bf16 v[104:107], a[0:3], v[188:191], 0\n"
    "s_waitcnt lgkmcnt(5)\n"
    "v_add_u32_e32 v254, s54, v242\n"
    "v_cvt_pk_bf16_f32 v136, v136, v137\n"
    "v_smfmac_f32_16x16x64_bf16 v[80:83], v[216:219], v[208:215], v240\n"
    "ds_read_b128 v[216:219], v251 offset:23520\n"
    "ds_write_b128 v242, v[120:123] offset:27712\n"
    "v_smfmac_f32_16x16x64_bf16 v[84:87], v[220:223], v[208:215], v240 abid:1\n"
    "s_waitcnt lgkmcnt(5)\n"
    "v_pk_fma_f16 v232, v192, v192, s48\n"
    "v_mfma_f32_16x16x32_bf16 v[132:135], a[244:247], v[184:187], 0\n"
    "s_nop 0\n"
    "v_cvt_pk_bf16_f32 v137, v138, v139\n"
    "v_cvt_pk_bf16_f32 v138, v140, v141\n"
    "v_or_b32_e64 v195, v195, v203\n"
    "ds_read_b128 v[208:211], v250 offset:240\n"
    "ds_read_b128 v[212:215], v250 offset:560\n"
    "v_pk_add_f32 v[224:225], v[224:225], v[80:81]\n"
    "v_pk_add_f32 v[226:227], v[226:227], v[82:83]\n"
    "v_pk_fma_f32 v[228:229], v[80:81], v[80:81], v[228:229]\n"
    "v_pk_fma_f32 v[230:231], v[82:83], v[82:83], v[230:231]\n"
    "v_pk_add_f32 v[224:225], v[224:225], v[84:85]\n"
    "v_pk_add_f32 v[226:227], v[226:227], v[86:87]\n"
    "v_pk_fma_f32 v[228:229], v[84:85], v[84:85], v[228:229]\n"
    "v_pk_fma_f32 v[230:231], v[86:87], v[86:87], v[230:231]\n"
    "v_mfma_f32_16x16x32_bf16 v[120:123], a[8:11], v[188:191], 0\n"
    "ds_read_b128 v[220:223], v251 offset:23856\n"
    "ds_read_u16_d16_hi v205, v205\n"
    "v_mfma_f32_16x16x32_bf16 v[124:127], a[12:15], v[188:191], 0\n"
    "ds_read_u16 v198, v198\n"
    "ds_read_u16_d16_hi v206, v206\n"
    "v_cvt_pk_bf16_f32 v139, v142, v143\n"
    "v_pk_mul_f16 v232, v192, v232\n"
    "v_cvt_pk_f16_f32 v184, v168, v169\n"
    "v_exp_f16_e32 v234, v232\n"
    "v_mfma_f32_16x16x32_bf16 v[140:143], a[244:247], v[188:191], 0\n"
    "v_exp_f16_sdwa v234, v232 dst_sel:WORD_1 dst_unused:UNUSED_PRESERVE src0_sel:WORD_1\n"
    "ds_write_b128 v242, v[136:139] offset:29760\n"
    "v_mfma_f32_16x16x32_bf16 v[136:139], v[236:239], v[188:191], 0\n"
    "s_waitcnt lgkmcnt(5)\n"
    "v_pk_fma_f16 v234, v234, s49, s49\n"
    "v_lshlrev_b32_sdwa v185, 1, v170 dst_sel:DWORD dst_unused:UNUSED_PAD src0_sel:DWORD src1_sel:WORD_1\n"
    "v_smfmac_f32_16x16x64_bf16 v[88:91], v[216:219], v[208:215], v240\n"
    "ds_read_b128 v[216:219], v252 offset:23520\n"
    "ds_read_u16 v199, v199\n"
    "v_lshlrev_b32_sdwa v201, 1, v171 dst_sel:DWORD dst_unused:UNUSED_PAD src0_sel:DWORD src1_sel:WORD_1\n"
    "v_mfma_f32_16x16x32_bf16 v[168:171], a[104:107], v[144:147], 0\n"
    "s_waitcnt lgkmcnt(6)\n"
    "ds_read_u16_d16_hi v207, v207\n"
    "ds_read_u16 v185, v185\n"
    "v_smfmac_f32_16x16x64_bf16 v[92:95], v[220:223], v[208:215], v240 abid:1\n"
    "s_nop 0\n"
    "v_lshlrev_b32_sdwa v186, 1, v172 dst_sel:DWORD dst_unused:UNUSED_PAD src0_sel:DWORD src1_sel:WORD_1\n"
    "v_mfma_f32_16x16x32_bf16 v[168:171], a[152:155], v[148:151], v[168:171]\n"
    "ds_read_b128 v[208:211], v250 offset:880\n"
    "v_lshlrev_b32_sdwa v202, 1, v173 dst_sel:DWORD dst_unused:UNUSED_PAD src0_sel:DWORD src1_sel:WORD_1\n"
    "s_waitcnt lgkmcnt(8)\n"
    "v_or_b32_e32 v197, v197, v205\n"
    "ds_read_b128 v[212:215], v250 offset:1200\n"
    "v_lshlrev_b32_sdwa v187, 1, v174 dst_sel:DWORD dst_unused:UNUSED_PAD src0_sel:DWORD src1_sel:WORD_1\n"
    "ds_read_b128 v[220:223], v252 offset:23856\n"
    "v_lshlrev_b32_sdwa v203, 1, v175 dst_sel:DWORD dst_unused:UNUSED_PAD src0_sel:DWORD src1_sel:WORD_1\n"
    "v_rcp_f16_e32 v232, v234\n"
    "v_mfma_f32_16x16x32_bf16 v[168:171], a[64:67], v[152:155], v[168:171]\n"
    "s_waitcnt lgkmcnt(7)\n"
    "ds_read_u16_d16_hi v201, v201\n"
    "v_or_b32_e64 v198, v198, v206\n"
    "v_rcp_f16_sdwa v232, v234 dst_sel:WORD_1 dst_unused:UNUSED_PRESERVE src0_sel:WORD_1\n"
    "v_mfma_f32_16x16x32_bf16 v[172:175], a[108:111], v[144:147], 0\n"
    "ds_read_u16 v186, v186\n"
    "v_cvt_pk_f16_f32 v188, v176, v177\n"
    "v_mfma_f32_16x16x32_bf16 v[172:175], a[156:159], v[148:151], v[172:175]\n"
    "ds_read_u16_d16_hi v202, v202\n"
    "v_lshlrev_b32_sdwa v189, 1, v178 dst_sel:DWORD dst_unused:UNUSED_PAD src0_sel:DWORD src1_sel:WORD_1\n"
    "v_lshlrev_b32_sdwa v205, 1, v179 dst_sel:DWORD dst_unused:UNUSED_PAD src0_sel:DWORD src1_sel:WORD_1\n"
    "v_lshlrev_b32_sdwa v190, 1, v180 dst_sel:DWORD dst_unused:UNUSED_PAD src0_sel:DWORD src1_sel:WORD_1\n"
    "v_lshlrev_b32_sdwa v206, 1, v181 dst_sel:DWORD dst_unused:UNUSED_PAD src0_sel:DWORD src1_sel:WORD_1\n"
    "v_lshlrev_b32_sdwa v191, 1, v182 dst_sel:DWORD dst_unused:UNUSED_PAD src0_sel:DWORD src1_sel:WORD_1\n"
    "v_fma_mix_f32 v234, v192, v232, 0 op_sel_hi:[1,1,0]\n"
    "v_mfma_f32_16x16x32_bf16 v[172:175], a[68:71], v[152:155], v[172:175]\n"
    "ds_read_b128 v[236:239], v244 offset:4096\n"
    "v_fma_mix_f32 v232, v192, v232, 0 op_sel:[1,1,0] op_sel_hi:[1,1,0]\n"
    "v_mfma_f32_16x16x32_bf16 v[176:179], a[104:107], v[156:159], 0\n"
    "ds_read_u16 v187, v187\n"
    "v_cvt_pk_bf16_f32 v192, v234, v232\n"
    "v_mfma_f32_16x16x32_bf16 v[176:179], a[152:155], v[160:163], v[176:179]\n"
    "s_waitcnt lgkmcnt(9)\n"
    "v_or_b32_e32 v199, v199, v207\n"
    "v_lshlrev_b32_sdwa v207, 1, v183 dst_sel:DWORD dst_unused:UNUSED_PAD src0_sel:DWORD src1_sel:WORD_1\n"
    "v_mfma_f32_16x16x32_bf16 v[176:179], a[64:67], v[164:167], v[176:179]\n"
    "ds_read_u16_d16_hi v203, v203\n"
    "v_pk_fma_f16 v232, v196, v196, s48\n"
    "v_mfma_f32_16x16x32_bf16 v[180:183], a[108:111], v[156:159], 0\n"
    "ds_read_b128 a[244:247], v244 offset:6144\n"
    "v_pk_mul_f16 v232, v196, v232\n"
    "v_mfma_f32_16x16x32_bf16 v[180:183], a[156:159], v[160:163], v[180:183]\n"
    "s_waitcnt lgkmcnt(8)\n"
    "ds_read_u16 v189, v189\n"
    "v_exp_f16_e32 v234, v232\n"
    "v_smfmac_f32_16x16x64_bf16 v[88:91], v[216:219], v[208:215], v240\n"
    "s_waitcnt lgkmcnt(8)\n"
    "v_exp_f16_sdwa v234, v232 dst_sel:WORD_1 dst_unused:UNUSED_PRESERVE src0_sel:WORD_1\n"
    "v_smfmac_f32_16x16x64_bf16 v[92:95], v[220:223], v[208:215], v240 abid:1\n"
    "ds_read_u16_d16_hi v205, v205\n"
    "v_mfma_f32_16x16x32_bf16 v[180:183], a[68:71], v[164:167], v[180:183]\n"
    "s_waitcnt lgkmcnt(8)\n"
    "ds_read_u16 v190, v190\n"
    "v_pk_fma_f16 v234, v234, s49, s49\n"
    "v_or_b32_e64 v185, v185, v201\n"
    "v_rcp_f16_e64 v232, v234\n"
    "v_pk_add_f32 v[224:225], v[224:225], v[88:89]\n"
    "v_pk_add_f32 v[226:227], v[226:227], v[90:91]\n"
    "v_pk_fma_f32 v[228:229], v[88:89], v[88:89], v[228:229]\n"
    "v_pk_fma_f32 v[230:231], v[90:91], v[90:91], v[230:231]\n"
    "v_pk_add_f32 v[224:225], v[224:225], v[92:93]\n"
    "v_pk_add_f32 v[226:227], v[226:227], v[94:95]\n"
    "v_pk_fma_f32 v[228:229], v[92:93], v[92:93], v[228:229]\n"
    "v_pk_fma_f32 v[230:231], v[94:95], v[94:95], v[230:231]\n"
    "v_mfma_f32_16x16x32_bf16 v[96:99], a[16:19], v[192:195], v[96:99]\n"
    "s_waitcnt lgkmcnt(6)\n"
    "ds_write_b128 v254, v[224:227]\n"
    "v_mfma_f32_16x16x32_bf16 v[100:103], a[20:23], v[192:195], v[100:103]\n"
    "s_nop 0\n"
    "v_rcp_f16_sdwa v232, v234 dst_sel:WORD_1 dst_unused:UNUSED_PRESERVE src0_sel:WORD_1\n"
    "v_mfma_f32_16x16x32_bf16 v[112:115], a[24:27], v[192:195], v[112:115]\n"
    "v_fma_mix_f32 v234, v196, v232, 0 op_sel_hi:[1,1,0]\n"
    "v_fma_mix_f32 v232, v196, v232, 0 op_sel:[1,1,0] op_sel_hi:[1,1,0]\n"
    "v_mfma_f32_16x16x32_bf16 v[116:119], a[28:31], v[192:195], v[116:119]\n"
    "s_waitcnt lgkmcnt(4)\n"
    "ds_read_u16_d16_hi v206, v206\n"
    "ds_write_b128 v254, v[228:231] offset:1024\n"
    "v_mfma_f32_16x16x32_bf16 v[128:131], v[236:239], v[192:195], v[128:131]\n"
    "v_lshrrev_b32_e32 v254, 2, v242\n"
    "v_or_b32_e64 v186, v186, v202\n"
    "v_mfma_f32_16x16x32_bf16 v[132:135], a[244:247], v[192:195], v[132:135]\n"
    "v_add_u32_e64 v254, s55, v254\n"
    "v_cvt_pk_bf16_f32 v196, v234, v232\n"
    "v_pk_fma_f16 v232, v184, v184, s48\n"
    "v_or_b32_e64 v187, v187, v203\n"
    "v_mfma_f32_16x16x32_bf16 v[104:107], a[16:19], v[196:199], v[104:107]\n"
    "ds_read_u16 v191, v191\n"
    "ds_read_u16_d16_hi v207, v207\n"
    "v_mfma_f32_16x16x32_bf16 v[108:111], a[20:23], v[196:199], v[108:111]\n"
    "s_waitcnt lgkmcnt(6)\n"
    "ds_read_b128 a[240:243], v244 offset:34816\n"
    "v_pk_mul_f16 v232, v184, v232\n"
    "v_mfma_f32_16x16x32_bf16 v[120:123], a[24:27], v[196:199], v[120:123]\n"
    "v_exp_f16_e32 v234, v232\n"
    "v_mfma_f32_16x16x32_bf16 v[124:127], a[28:31], v[196:199], v[124:127]\n"
    "s_waitcnt vmcnt(2)\n"
    "v_exp_f16_sdwa v234, v232 dst_sel:WORD_1 dst_unused:UNUSED_PRESERVE src0_sel:WORD_1\n"
    "v_mfma_f32_16x16x32_bf16 v[136:139], v[236:239], v[196:199], v[136:139]\n"
    "s_waitcnt vmcnt(1)\n"
    "ds_read_b128 v[236:239], v244 offset:8192\n"
    "v_or_b32_e64 v189, v189, v205\n"
    "v_mfma_f32_16x16x32_bf16 v[140:143], a[244:247], v[196:199], v[140:143]\n"
    "v_pk_fma_f16 v234, v234, s49, s49\n"
    "v_lshlrev_b32_sdwa v192, 1, v168 dst_sel:DWORD dst_unused:UNUSED_PAD src0_sel:DWORD src1_sel:WORD_1\n"
    "v_smfmac_f32_32x32x32_bf16 v[112:127], a[248:251], a[232:239], v241\n"
    "s_waitcnt lgkmcnt(4)\n"
    "s_barrier\n"
    "v_smfmac_f32_32x32x32_bf16 v[96:111], a[248:251], a[224:231], v241\n"
    "ds_read_b32 v224, v254 offset:0\n"
    "ds_read_b32 v225, v254 offset:6752\n"
    "v_lshlrev_b32_sdwa v200, 1, v169 dst_sel:DWORD dst_unused:UNUSED_PAD src0_sel:DWORD src1_sel:WORD_1\n"
    "ds_read_b32 v226, v254 offset:13504\n"
    "ds_read_b32 v227, v254 offset:20256\n"
    "ds_read_b32 v228, v254 offset:1024\n"
    "v_lshlrev_b32_sdwa v193, 1, v170 dst_sel:DWORD dst_unused:UNUSED_PAD src0_sel:DWORD src1_sel:WORD_1\n"
    "ds_read_b32 v229, v254 offset:7776\n"
    "ds_read_b32 v230, v254 offset:14528\n"
    "ds_read_b32 v231, v254 offset:21280\n"
    "v_lshlrev_b32_sdwa v201, 1, v171 dst_sel:DWORD dst_unused:UNUSED_PAD src0_sel:DWORD src1_sel:WORD_1\n"
    "v_mfma_f32_16x16x32_bf16 v[168:171], a[112:115], v[144:147], 0\n"
    "s_waitcnt lgkmcnt(8)\n"
    "ds_read_u16 v192, v192\n"
    "ds_read_u16_d16_hi v200, v200\n"
    "v_mfma_f32_16x16x32_bf16 v[168:171], a[160:163], v[148:151], v[168:171]\n"
    "s_nop 0\n"
    "ds_read_u16 v193, v193\n"
    "ds_read_u16_d16_hi v201, v201\n"
    "v_lshlrev_b32_sdwa v194, 1, v172 dst_sel:DWORD dst_unused:UNUSED_PAD src0_sel:DWORD src1_sel:WORD_1\n"
    "v_lshlrev_b32_sdwa v202, 1, v173 dst_sel:DWORD dst_unused:UNUSED_PAD src0_sel:DWORD src1_sel:WORD_1\n"
    "v_lshlrev_b32_sdwa v195, 1, v174 dst_sel:DWORD dst_unused:UNUSED_PAD src0_sel:DWORD src1_sel:WORD_1\n"
    "v_lshlrev_b32_sdwa v203, 1, v175 dst_sel:DWORD dst_unused:UNUSED_PAD src0_sel:DWORD src1_sel:WORD_1\n"
    "v_rcp_f16_e64 v232, v234\n"
    "v_mfma_f32_16x16x32_bf16 v[168:171], a[72:75], v[152:155], v[168:171]\n"
    "ds_read_u16 v194, v194\n"
    "buffer_load_dwordx4 a[224:227], v245, s[40:43], s58 offen offset:128\n"
    "s_waitcnt lgkmcnt(11)\n"
    "v_add_f32_e32 v224, v224, v225\n"
    "v_or_b32_e32 v190, v190, v206\n"
    "s_waitcnt lgkmcnt(9)\n"
    "v_add_f32_e32 v226, v226, v227\n"
    "v_rcp_f16_sdwa v232, v234 dst_sel:WORD_1 dst_unused:UNUSED_PRESERVE src0_sel:WORD_1\n"
    "v_mfma_f32_16x16x32_bf16 v[172:175], a[116:119], v[144:147], 0\n"
    "s_waitcnt lgkmcnt(6)\n"
    "ds_read_b128 a[232:235], v243 offset:24640\n"
    "ds_read_u16_d16_hi v202, v202\n"
    "v_mfma_f32_16x16x32_bf16 v[172:175], a[164:167], v[148:151], v[172:175]\n"
    "s_waitcnt lgkmcnt(7)\n"
    "v_add_f32_e32 v228, v228, v229\n"
    "v_add_f32_e64 v230, v230, v231\n"
    "v_add_f32_e64 v224, v224, v226\n"
    "v_add_f32_e64 v228, v228, v230\n"
    "v_mul_f32_e32 v224, 0x3c2aaaab, v224\n"
    "v_mul_f32_e32 v228, 0x3c2aaaab, v228\n"
    "v_lshlrev_b32_sdwa v196, 1, v176 dst_sel:DWORD dst_unused:UNUSED_PAD src0_sel:DWORD src1_sel:WORD_1\n"
    "v_fma_f32 v228, -v224, v224, v228\n"
    "v_lshlrev_b32_sdwa v204, 1, v177 dst_sel:DWORD dst_unused:UNUSED_PAD src0_sel:DWORD src1_sel:WORD_1\n"
    "v_max_f32_e64 v228, 0, v228\n"
    "v_lshlrev_b32_sdwa v197, 1, v178 dst_sel:DWORD dst_unused:UNUSED_PAD src0_sel:DWORD src1_sel:WORD_1\n"
    "v_add_f32_e32 v228, 0x3727c5ac, v228\n"
    "v_lshlrev_b32_sdwa v205, 1, v179 dst_sel:DWORD dst_unused:UNUSED_PAD src0_sel:DWORD src1_sel:WORD_1\n"
    "v_rsq_f32_e64 v228, v228\n"
    "v_lshlrev_b32_sdwa v198, 1, v180 dst_sel:DWORD dst_unused:UNUSED_PAD src0_sel:DWORD src1_sel:WORD_1\n"
    "v_mul_f32_e64 v228, s50, v228\n"
    "ds_write_b32 v254, v228\n"
    "ds_read_b128 a[236:239], v243 offset:25664\n"
    "v_mul_f32_e64 v224, v228, -v224\n"
    "ds_write_b32 v254, v224 offset:1024\n"
    "ds_read_u16 v195, v195\n"
    "v_add_u32_e64 v254, s56, v242\n"
    "v_lshlrev_b32_sdwa v206, 1, v181 dst_sel:DWORD dst_unused:UNUSED_PAD src0_sel:DWORD src1_sel:WORD_1\n"
    "v_lshlrev_b32_sdwa v199, 1, v182 dst_sel:DWORD dst_unused:UNUSED_PAD src0_sel:DWORD src1_sel:WORD_1\n"
    "v_fma_mix_f32 v234, v184, v232, 0 op_sel_hi:[1,1,0]\n"
    "v_mfma_f32_16x16x32_bf16 v[172:175], a[240:243], v[152:155], v[172:175]\n"
    "s_waitcnt lgkmcnt(7)\n"
    "ds_read_b128 a[244:247], v244 offset:10240\n"
    "buffer_load_dwordx4 a[228:231], v245, s[40:43], s89 offen offset:128\n"
    "v_or_b32_e32 v191, v191, v207\n"
    "v_lshlrev_b32_sdwa v207, 1, v183 dst_sel:DWORD dst_unused:UNUSED_PAD src0_sel:DWORD src1_sel:WORD_1\n"
    "v_fma_mix_f32 v232, v184, v232, 0 op_sel:[1,1,0] op_sel_hi:[1,1,0]\n"
    "v_mfma_f32_16x16x32_bf16 v[176:179], a[112:115], v[156:159], 0\n"
    "ds_read_u16_d16_hi v203, v203\n"
    "ds_read_u16 v196, v196\n"
    "v_cvt_pk_bf16_f32 v184, v234, v232\n"
    "v_mfma_f32_16x16x32_bf16 v[176:179], a[160:163], v[160:163], v[176:179]\n"
    "ds_read_u16_d16_hi v204, v204\n"
    "ds_read_u16 v197, v197\n"
    "v_mfma_f32_16x16x32_bf16 v[176:179], a[72:75], v[164:167], v[176:179]\n"
    "v_pk_fma_f16 v232, v188, v188, s48\n"
    "v_or_b32_e64 v192, v192, v200\n"
    "v_mfma_f32_16x16x32_bf16 v[180:183], a[116:119], v[156:159], 0\n"
    "s_waitcnt lgkmcnt(6)\n"
    "s_barrier\n"
    "v_smfmac_f32_32x32x32_bf16 v[96:111], a[248:251], a[232:239], v241\n"
    "ds_read_b128 v[224:227], v254\n"
    "ds_read_b128 v[228:231], v254 offset:1024\n"
    "v_lshlrev_b32_e64 v254, 3, v244\n"
    "v_pk_mul_f16 v232, v188, v232\n"
    "v_or_b32_e32 v193, v193, v201\n"
    "v_mfma_f32_16x16x32_bf16 v[180:183], a[164:167], v[160:163], v[180:183]\n"
    "s_mov_b32 exec_hi, 0\n"
    "buffer_load_dword v233, v254, s[72:75], s53 offen sc1\n"
    "s_mov_b32 exec_hi, -1\n"
    "v_exp_f16_e32 v234, v232\n"
    "v_mfma_f32_16x16x32_bf16 v[180:183], a[240:243], v[164:167], v[180:183]\n"
    "s_add_u32 s53, s53, 393216\n"
    "ds_read_u16_d16_hi v205, v205\n"
    "ds_read_u16 v198, v198\n"
    "v_exp_f16_sdwa v234, v232 dst_sel:WORD_1 dst_unused:UNUSED_PRESERVE src0_sel:WORD_1\n"
    "v_mfma_f32_16x16x32_bf16 v[96:99], a[32:35], v[184:187], v[96:99]\n"
    "ds_read_u16_d16_hi v206, v206\n"
    "ds_read_u16 v199, v199\n"
    "v_pk_fma_f16 v234, v234, s49, s49\n"
    "v_mfma_f32_16x16x32_bf16 v[100:103], a[36:39], v[184:187], v[100:103]\n"
    "s_waitcnt lgkmcnt(8)\n"
    "v_rcp_f16_e32 v232, v234\n"
    "v_mfma_f32_16x16x32_bf16 v[112:115], a[40:43], v[184:187], v[112:115]\n"
    "ds_read_u16_d16_hi v207, v207\n"
    "v_rcp_f16_sdwa v232, v234 dst_sel:WORD_1 dst_unused:UNUSED_PRESERVE src0_sel:WORD_1\n"
    "v_mfma_f32_16x16x32_bf16 v[116:119], a[44:47], v[184:187], v[116:119]\n"
    "s_waitcnt lgkmcnt(6)\n"
    "ds_read_b128 a[232:235], v243 offset:26688\n"
    "v_fma_mix_f32 v234, v188, v232, 0 op_sel_hi:[1,1,0]\n"
    "v_mfma_f32_16x16x32_bf16 v[128:131], v[236:239], v[184:187], v[128:131]\n"
    "s_waitcnt lgkmcnt(6)\n"
    "ds_read_b128 a[236:239], v243 offset:27712\n"
    "v_fma_mix_f32 v232, v188, v232, 0 op_sel:[1,1,0] op_sel_hi:[1,1,0]\n"
    "v_lshlrev_b32_sdwa v201, 1, v171 dst_sel:DWORD dst_unused:UNUSED_PAD src0_sel:DWORD src1_sel:WORD_1\n"
    "v_cvt_pk_bf16_f32 v188, v234, v232\n"
    "v_or_b32_e64 v194, v194, v202\n"
    "v_pk_fma_f32 v[64:65], v[64:65], v[224:225], v[228:229]\n"
    "v_pk_fma_f32 v[66:67], v[66:67], v[226:227], v[230:231]\n"
    "v_pk_fma_f32 v[68:69], v[68:69], v[224:225], v[228:229]\n"
    "v_pk_fma_f32 v[70:71], v[70:71], v[226:227], v[230:231]\n"
    "v_pk_fma_f32 v[72:73], v[72:73], v[224:225], v[228:229]\n"
    "v_pk_fma_f32 v[74:75], v[74:75], v[226:227], v[230:231]\n"
    "v_pk_fma_f32 v[76:77], v[76:77], v[224:225], v[228:229]\n"
    "v_pk_fma_f32 v[78:79], v[78:79], v[226:227], v[230:231]\n"
    "v_pk_fma_f32 v[80:81], v[80:81], v[224:225], v[228:229]\n"
    "v_pk_fma_f32 v[82:83], v[82:83], v[226:227], v[230:231]\n"
    "v_pk_fma_f32 v[84:85], v[84:85], v[224:225], v[228:229]\n"
    "v_pk_fma_f32 v[86:87], v[86:87], v[226:227], v[230:231]\n"
    "v_pk_fma_f32 v[88:89], v[88:89], v[224:225], v[228:229]\n"
    "v_pk_fma_f32 v[90:91], v[90:91], v[226:227], v[230:231]\n"
    "v_pk_fma_f32 v[92:93], v[92:93], v[224:225], v[228:229]\n"
    "v_pk_fma_f32 v[94:95], v[94:95], v[226:227], v[230:231]\n"
    "v_pk_fma_f32 v[0:1], v[0:1], v[224:225], v[228:229]\n"
    "v_pk_fma_f32 v[2:3], v[2:3], v[226:227], v[230:231]\n"
    "v_pk_fma_f32 v[4:5], v[4:5], v[224:225], v[228:229]\n"
    "v_pk_fma_f32 v[6:7], v[6:7], v[226:227], v[230:231]\n"
    "v_pk_fma_f32 v[8:9], v[8:9], v[224:225], v[228:229]\n"
    "v_pk_fma_f32 v[10:11], v[10:11], v[226:227], v[230:231]\n"
    "v_pk_fma_f32 v[12:13], v[12:13], v[224:225], v[228:229]\n"
    "v_pk_fma_f32 v[14:15], v[14:15], v[226:227], v[230:231]\n"
    "v_pk_fma_f32 v[16:17], v[16:17], v[224:225], v[228:229]\n"
    "v_pk_fma_f32 v[18:19], v[18:19], v[226:227], v[230:231]\n"
    "v_pk_fma_f32 v[20:21], v[20:21], v[224:225], v[228:229]\n"
    "v_pk_fma_f32 v[22:23], v[22:23], v[226:227], v[230:231]\n"
    "v_pk_fma_f32 v[24:25], v[24:25], v[224:225], v[228:229]\n"
    "v_pk_fma_f32 v[26:27], v[26:27], v[226:227], v[230:231]\n"
    "v_pk_fma_f32 v[28:29], v[28:29], v[224:225], v[228:229]\n"
    "v_pk_fma_f32 v[30:31], v[30:31], v[226:227], v[230:231]\n"
    "v_pk_fma_f32 v[32:33], v[32:33], v[224:225], v[228:229]\n"
    "v_pk_fma_f32 v[34:35], v[34:35], v[226:227], v[230:231]\n"
    "v_pk_fma_f32 v[36:37], v[36:37], v[224:225], v[228:229]\n"
    "v_pk_fma_f32 v[38:39], v[38:39], v[226:227], v[230:231]\n"
    "v_pk_fma_f32 v[40:41], v[40:41], v[224:225], v[228:229]\n"
    "v_pk_fma_f32 v[42:43], v[42:43], v[226:227], v[230:231]\n"
    "v_pk_fma_f32 v[44:45], v[44:45], v[224:225], v[228:229]\n"
    "v_pk_fma_f32 v[46:47], v[46:47], v[226:227], v[230:231]\n"
    "v_pk_fma_f32 v[48:49], v[48:49], v[224:225], v[228:229]\n"
    "v_pk_fma_f32 v[50:51], v[50:51], v[226:227], v[230:231]\n"
    "v_pk_fma_f32 v[52:53], v[52:53], v[224:225], v[228:229]\n"
    "v_pk_fma_f32 v[54:55], v[54:55], v[226:227], v[230:231]\n"
    "v_pk_fma_f32 v[56:57], v[56:57], v[224:225], v[228:229]\n"
    "v_pk_fma_f32 v[58:59], v[58:59], v[226:227], v[230:231]\n"
    "v_pk_fma_f32 v[60:61], v[60:61], v[224:225], v[228:229]\n"
    "v_pk_fma_f32 v[62:63], v[62:63], v[226:227], v[230:231]\n"
    "v_mfma_f32_16x16x32_bf16 v[132:135], a[244:247], v[184:187], v[132:135]\n"
    "s_waitcnt lgkmcnt(0)\n"
    "ds_read_b128 a[240:243], v244 offset:38912\n"
    "ds_read_u16_d16_hi v201, v201\n"
    "v_mfma_f32_16x16x32_bf16 v[104:107], a[32:35], v[188:191], v[104:107]\n"
    "s_nop 0\n"
    "v_cvt_pk_bf16_f32 v208, v64, v68\n"
    "v_cvt_pk_bf16_f32 v212, v65, v69\n"
    "v_mfma_f32_16x16x32_bf16 v[108:111], a[36:39], v[188:191], v[108:111]\n"
    "v_cvt_pk_bf16_f32 v216, v66, v70\n"
    "v_cvt_pk_bf16_f32 v220, v67, v71\n"
    "v_mfma_f32_16x16x32_bf16 v[120:123], a[40:43], v[188:191], v[120:123]\n"
    "v_cvt_pk_bf16_f32 v209, v72, v76\n"
    "v_cvt_pk_bf16_f32 v213, v73, v77\n"
    "v_mfma_f32_16x16x32_bf16 v[124:127], a[44:47], v[188:191], v[124:127]\n"
    "v_cvt_pk_bf16_f32 v217, v74, v78\n"
    "v_cvt_pk_bf16_f32 v221, v75, v79\n"
    "v_mfma_f32_16x16x32_bf16 v[136:139], v[236:239], v[188:191], v[136:139]\n"
    "ds_read_b128 v[236:239], v244 offset:12288\n"
    "v_cvt_pk_bf16_f32 v210, v80, v84\n"
    "v_mfma_f32_16x16x32_bf16 v[140:143], a[244:247], v[188:191], v[140:143]\n"
    "ds_read_b128 a[244:247], v244 offset:14336\n"
    "v_cvt_pk_bf16_f32 v214, v81, v85\n"
    "v_smfmac_f32_32x32x32_bf16 v[112:127], a[248:251], a[232:239], v241\n"
    "v_cvt_pk_bf16_f32 v218, v82, v86\n"
    "v_cvt_pk_bf16_f32 v222, v83, v87\n"
    "v_cvt_pk_bf16_f32 v211, v88, v92\n"
    "ds_write_b128 v253, v[208:211] offset:2048\n"
    "v_cvt_pk_bf16_f32 v215, v89, v93\n"
    "v_cvt_pk_bf16_f32 v219, v90, v94\n"
    "v_cvt_pk_bf16_f32 v223, v91, v95\n"
    "v_cvt_pk_bf16_f32 v80, v0, v4\n"
    "v_cvt_pk_bf16_f32 v84, v1, v5\n"
    "v_cvt_pk_bf16_f32 v88, v2, v6\n"
    "ds_write_b128 v253, v[212:215] offset:5120\n"
    "v_cvt_pk_bf16_f32 v92, v3, v7\n"
    "v_cvt_pk_bf16_f32 v81, v8, v12\n"
    "v_cvt_pk_bf16_f32 v85, v9, v13\n"
    "v_cvt_pk_bf16_f32 v89, v10, v14\n"
    "v_cvt_pk_bf16_f32 v93, v11, v15\n"
    "v_cvt_pk_bf16_f32 v82, v16, v20\n"
    "v_cvt_pk_bf16_f32 v86, v17, v21\n"
    "v_cvt_pk_bf16_f32 v90, v18, v22\n"
    "v_cvt_pk_bf16_f32 v94, v19, v23\n"
    "v_cvt_pk_bf16_f32 v83, v24, v28\n"
    "v_cvt_pk_f16_f32 v184, v168, v169\n"
    "v_cvt_pk_bf16_f32 v87, v25, v29\n"
    "v_lshlrev_b32_sdwa v185, 1, v170 dst_sel:DWORD dst_unused:UNUSED_PAD src0_sel:DWORD src1_sel:WORD_1\n"
    "v_mfma_f32_16x16x32_bf16 v[168:171], a[120:123], v[144:147], 0\n"
    "ds_read_u16 v185, v185\n"
    "ds_write_b128 v253, v[80:83] offset:0\n"
    "v_mfma_f32_16x16x32_bf16 v[168:171], a[168:171], v[148:151], v[168:171]\n"
    "v_lshlrev_b32_sdwa v186, 1, v172 dst_sel:DWORD dst_unused:UNUSED_PAD src0_sel:DWORD src1_sel:WORD_1\n"
    "v_cvt_pk_bf16_f32 v91, v26, v30\n"
    "v_mfma_f32_16x16x32_bf16 v[168:171], a[76:79], v[152:155], v[168:171]\n"
    "v_lshlrev_b32_sdwa v202, 1, v173 dst_sel:DWORD dst_unused:UNUSED_PAD src0_sel:DWORD src1_sel:WORD_1\n"
    "v_lshlrev_b32_sdwa v187, 1, v174 dst_sel:DWORD dst_unused:UNUSED_PAD src0_sel:DWORD src1_sel:WORD_1\n"
    "v_cvt_pk_bf16_f32 v95, v27, v31\n"
    "v_cvt_pk_f16_f32 v188, v176, v177\n"
    "v_lshlrev_b32_sdwa v189, 1, v178 dst_sel:DWORD dst_unused:UNUSED_PAD src0_sel:DWORD src1_sel:WORD_1\n"
    "v_lshlrev_b32_sdwa v190, 1, v180 dst_sel:DWORD dst_unused:UNUSED_PAD src0_sel:DWORD src1_sel:WORD_1\n"
    "v_lshlrev_b32_sdwa v191, 1, v182 dst_sel:DWORD dst_unused:UNUSED_PAD src0_sel:DWORD src1_sel:WORD_1\n"
    "v_or_b32_e64 v195, v195, v203\n"
    "v_lshlrev_b32_sdwa v203, 1, v175 dst_sel:DWORD dst_unused:UNUSED_PAD src0_sel:DWORD src1_sel:WORD_1\n"
    "v_mfma_f32_16x16x32_bf16 v[172:175], a[124:127], v[144:147], 0\n"
    "s_nop 0\n"
    "ds_read_u16 v186, v186\n"
    "ds_write_b128 v253, v[84:87] offset:3072\n"
    "v_mfma_f32_16x16x32_bf16 v[172:175], a[172:175], v[148:151], v[172:175]\n"
    "s_waitcnt lgkmcnt(4)\n"
    "v_cvt_pk_bf16_f32 v64, v32, v36\n"
    "v_cvt_pk_bf16_f32 v68, v33, v37\n"
    "v_mfma_f32_16x16x32_bf16 v[172:175], a[240:243], v[152:155], v[172:175]\n"
    "v_cvt_pk_bf16_f32 v72, v34, v38\n"
    "v_cvt_pk_bf16_f32 v76, v35, v39\n"
    "v_cvt_pk_bf16_f32 v65, v40, v44\n"
    "v_or_b32_e64 v196, v196, v204\n"
    "v_cvt_pk_bf16_f32 v69, v41, v45\n"
    "v_cvt_pk_bf16_f32 v73, v42, v46\n"
    "v_cvt_pk_bf16_f32 v77, v43, v47\n"
    "v_cvt_pk_bf16_f32 v66, v48, v52\n"
    "v_cvt_pk_bf16_f32 v70, v49, v53\n"
    "v_or_b32_e32 v197, v197, v205\n"
    "v_lshlrev_b32_sdwa v205, 1, v179 dst_sel:DWORD dst_unused:UNUSED_PAD src0_sel:DWORD src1_sel:WORD_1\n"
    "v_mfma_f32_16x16x32_bf16 v[176:179], a[120:123], v[156:159], 0\n"
    "s_waitcnt lgkmcnt(2)\n"
    "ds_read_u16_d16_hi v202, v202\n"
    "ds_read_u16 v187, v187\n"
    "v_mfma_f32_16x16x32_bf16 v[176:179], a[168:171], v[160:163], v[176:179]\n"
    "ds_read_u16_d16_hi v203, v203\n"
    "ds_read_u16 v189, v189\n"
    "v_mfma_f32_16x16x32_bf16 v[176:179], a[76:79], v[164:167], v[176:179]\n"
    "v_cvt_pk_bf16_f32 v74, v50, v54\n"
    "v_cvt_pk_bf16_f32 v78, v51, v55\n"
    "v_cvt_pk_bf16_f32 v67, v56, v60\n"
    "v_cvt_pk_bf16_f32 v71, v57, v61\n"
    "v_cvt_pk_bf16_f32 v75, v58, v62\n"
    "v_or_b32_e64 v198, v198, v206\n"
    "v_mfma_f32_16x16x32_bf16 v[96:99], a[176:179], v[192:195], v[96:99]\n"
    "v_lshlrev_b32_sdwa v206, 1, v181 dst_sel:DWORD dst_unused:UNUSED_PAD src0_sel:DWORD src1_sel:WORD_1\n"
    "v_cvt_pk_bf16_f32 v79, v59, v63\n"
    "v_mfma_f32_16x16x32_bf16 v[100:103], a[180:183], v[192:195], v[100:103]\n"
    "ds_write_b128 v253, v[64:67] offset:1024\n"
    "v_mfma_f32_16x16x32_bf16 v[112:115], a[184:187], v[192:195], v[112:115]\n"
    "v_or_b32_e64 v199, v199, v207\n"
    "v_lshlrev_b32_sdwa v207, 1, v183 dst_sel:DWORD dst_unused:UNUSED_PAD src0_sel:DWORD src1_sel:WORD_1\n"
    "v_mfma_f32_16x16x32_bf16 v[180:183], a[124:127], v[156:159], 0\n"
    "v_or_b32_e64 v185, v185, v201\n"
    "v_pk_fma_f16 v232, v184, v184, s48\n"
    "v_mfma_f32_16x16x32_bf16 v[180:183], a[172:175], v[160:163], v[180:183]\n"
    "ds_read_b128 a[232:235], v243 offset:28736\n"
    "ds_write_b128 v253, v[68:71] offset:4096\n"
    "v_mfma_f32_16x16x32_bf16 v[180:183], a[240:243], v[164:167], v[180:183]\n"
    "s_waitcnt lgkmcnt(6)\n"
    "v_or_b32_e32 v186, v186, v202\n"
    "v_pk_mul_f16 v232, v184, v232\n"
    "v_mfma_f32_16x16x32_bf16 v[116:119], a[188:191], v[192:195], v[116:119]\n"
    "v_exp_f16_e64 v234, v232\n"
    "v_mfma_f32_16x16x32_bf16 v[128:131], v[236:239], v[192:195], v[128:131]\n"
    "ds_read_u16_d16_hi v205, v205\n"
    "ds_read_b128 a[236:239], v243 offset:29760\n"
    "v_mfma_f32_16x16x32_bf16 v[132:135], a[244:247], v[192:195], v[132:135]\n"
    "s_waitcnt lgkmcnt(6)\n"
    "ds_read_u16 v190, v190\n"
    "ds_read_u16_d16_hi v206, v206\n"
    "v_mfma_f32_16x16x32_bf16 v[104:107], a[176:179], v[196:199], v[104:107]\n"
    "v_or_b32_e32 v187, v187, v203\n"
    "v_exp_f16_sdwa v234, v232 dst_sel:WORD_1 dst_unused:UNUSED_PRESERVE src0_sel:WORD_1\n"
    "v_mfma_f32_16x16x32_bf16 v[108:111], a[180:183], v[196:199], v[108:111]\n"
    "ds_read_u16 v191, v191\n"
    "v_pk_fma_f16 v234, v234, s49, s49\n"
    "v_mfma_f32_16x16x32_bf16 v[120:123], a[184:187], v[196:199], v[120:123]\n"
    "ds_read_u16_d16_hi v207, v207\n"
    "v_cvt_pk_f16_f32 v192, v168, v169\n"
    "v_mfma_f32_16x16x32_bf16 v[124:127], a[188:191], v[196:199], v[124:127]\n"
    "v_lshlrev_b32_sdwa v193, 1, v170 dst_sel:DWORD dst_unused:UNUSED_PAD src0_sel:DWORD src1_sel:WORD_1\n"
    "v_lshlrev_b32_sdwa v201, 1, v171 dst_sel:DWORD dst_unused:UNUSED_PAD src0_sel:DWORD src1_sel:WORD_1\n"
    "v_mfma_f32_16x16x32_bf16 v[136:139], v[236:239], v[196:199], v[136:139]\n"
    "s_waitcnt lgkmcnt(6)\n"
    "ds_read_b128 a[240:243], v244 offset:32768\n"
    "v_lshlrev_b32_sdwa v194, 1, v172 dst_sel:DWORD dst_unused:UNUSED_PAD src0_sel:DWORD src1_sel:WORD_1\n"
    "v_mfma_f32_16x16x32_bf16 v[140:143], a[244:247], v[196:199], v[140:143]\n"
    "s_barrier\n"
    "s_waitcnt lgkmcnt(5)\n"
    "v_or_b32_e32 v189, v189, v205\n"
    "v_lshlrev_b32_sdwa v202, 1, v173 dst_sel:DWORD dst_unused:UNUSED_PAD src0_sel:DWORD src1_sel:WORD_1\n"
    "v_lshlrev_b32_sdwa v195, 1, v174 dst_sel:DWORD dst_unused:UNUSED_PAD src0_sel:DWORD src1_sel:WORD_1\n"
    "v_lshlrev_b32_sdwa v203, 1, v175 dst_sel:DWORD dst_unused:UNUSED_PAD src0_sel:DWORD src1_sel:WORD_1\n"
    "v_smfmac_f32_32x32x32_bf16 v[128:143], a[248:251], a[232:239], v241\n"
    "ds_read_b128 v[144:147], v242 offset:0\n"
    "ds_read_b128 v[148:151], v242 offset:1024\n"
    "v_rcp_f16_e64 v232, v234\n"
    "v_mov_b32_e64 v64, v216\n"
    "v_rcp_f16_sdwa v232, v234 dst_sel:WORD_1 dst_unused:UNUSED_PRESERVE src0_sel:WORD_1\n"
    "v_cvt_pk_f16_f32 v196, v176, v177\n"
    "v_mov_b32_e32 v65, v217\n"
    "s_waitcnt lgkmcnt(5)\n"
    "v_or_b32_e64 v190, v190, v206\n"
    "v_lshlrev_b32_sdwa v197, 1, v178 dst_sel:DWORD dst_unused:UNUSED_PAD src0_sel:DWORD src1_sel:WORD_1\n"
    "v_mov_b32_e64 v66, v218\n"
    "v_lshlrev_b32_sdwa v205, 1, v179 dst_sel:DWORD dst_unused:UNUSED_PAD src0_sel:DWORD src1_sel:WORD_1\n"
    "v_mov_b32_e64 v67, v219\n"
    "v_lshlrev_b32_sdwa v198, 1, v180 dst_sel:DWORD dst_unused:UNUSED_PAD src0_sel:DWORD src1_sel:WORD_1\n"
    "v_mov_b32_e64 v68, v220\n"
    "v_lshlrev_b32_sdwa v206, 1, v181 dst_sel:DWORD dst_unused:UNUSED_PAD src0_sel:DWORD src1_sel:WORD_1\n"
    "v_mov_b32_e64 v69, v221\n"
    "v_lshlrev_b32_sdwa v199, 1, v182 dst_sel:DWORD dst_unused:UNUSED_PAD src0_sel:DWORD src1_sel:WORD_1\n"
    "v_mov_b32_e64 v70, v222\n"
    "v_mov_b32_e64 v71, v223\n"
    "v_fma_mix_f32 v234, v184, v232, 0 op_sel_hi:[1,1,0]\n"
    "s_waitcnt lgkmcnt(2)\n"
    "v_or_b32_e32 v191, v191, v207\n"
    "v_lshlrev_b32_sdwa v207, 1, v183 dst_sel:DWORD dst_unused:UNUSED_PAD src0_sel:DWORD src1_sel:WORD_1\n"
    "v_fma_mix_f32 v232, v184, v232, 0 op_sel:[1,1,0] op_sel_hi:[1,1,0]\n"
    "v_cvt_pk_bf16_f32 v184, v234, v232\n"
    "ds_read_b128 v[152:155], v242 offset:2048\n"
    "ds_read_b128 v[156:159], v242 offset:3072\n"
    "s_waitcnt lgkmcnt(3)\n"
    "v_mfma_f32_16x16x32_bf16 v[168:171], a[80:83], v[144:147], 0\n"
    "ds_read_b128 v[160:163], v242 offset:4096\n"
    "ds_read_b128 v[164:167], v242 offset:5120\n"
    "v_mfma_f32_16x16x32_bf16 v[172:175], a[84:87], v[144:147], 0\n"
    "s_waitcnt lgkmcnt(4)\n"
    "ds_read_u16 v193, v193\n"
    "ds_read_u16_d16_hi v201, v201\n"
    "v_mfma_f32_16x16x32_bf16 v[168:171], a[128:131], v[148:151], v[168:171]\n"
    "ds_read_u16 v194, v194\n"
    "v_pk_fma_f16 v232, v188, v188, s48\n"
    "v_mfma_f32_16x16x32_bf16 v[172:175], a[132:135], v[148:151], v[172:175]\n"
    "v_pk_mul_f16 v232, v188, v232\n"
    "v_exp_f16_e64 v234, v232\n"
    "ds_read_u16_d16_hi v202, v202\n"
    "v_exp_f16_sdwa v234, v232 dst_sel:WORD_1 dst_unused:UNUSED_PRESERVE src0_sel:WORD_1\n"
    "v_mfma_f32_16x16x32_bf16 v[96:99], a[192:195], v[184:187], v[96:99]\n"
    "ds_read_b128 v[236:239], v244 offset:16384\n"
    "v_pk_fma_f16 v234, v234, s49, s49\n"
    "v_mfma_f32_16x16x32_bf16 v[100:103], a[196:199], v[184:187], v[100:103]\n"
    "s_waitcnt lgkmcnt(8)\n"
    "ds_read_u16 v195, v195\n"
    "v_rcp_f16_e32 v232, v234\n"
    "v_mfma_f32_16x16x32_bf16 v[168:171], a[48:51], v[152:155], v[168:171]\n"
    "s_waitcnt lgkmcnt(8)\n"
    "v_rcp_f16_sdwa v232, v234 dst_sel:WORD_1 dst_unused:UNUSED_PRESERVE src0_sel:WORD_1\n"
    "v_mfma_f32_16x16x32_bf16 v[172:175], a[240:243], v[152:155], v[172:175]\n"
    "ds_read_u16_d16_hi v203, v203\n"
    "v_fma_mix_f32 v234, v188, v232, 0 op_sel_hi:[1,1,0]\n"
    "v_mfma_f32_16x16x32_bf16 v[176:179], a[80:83], v[156:159], 0\n"
    "s_waitcnt lgkmcnt(7)\n"
    "ds_read_b128 a[244:247], v244 offset:18432\n"
    "v_fma_mix_f32 v232, v188, v232, 0 op_sel:[1,1,0] op_sel_hi:[1,1,0]\n"
    "v_mfma_f32_16x16x32_bf16 v[176:179], a[128:131], v[160:163], v[176:179]\n"
    "ds_read_u16 v197, v197\n"
    "v_cvt_pk_bf16_f32 v188, v234, v232\n"
    "v_mfma_f32_16x16x32_bf16 v[176:179], a[48:51], v[164:167], v[176:179]\n"
    "v_pk_fma_f16 v232, v192, v192, s48\n"
    "v_pk_mul_f16 v232, v192, v232\n"
    "v_mfma_f32_16x16x32_bf16 v[180:183], a[84:87], v[156:159], 0\n"
    "ds_read_u16_d16_hi v205, v205\n"
    "v_exp_f16_e64 v234, v232\n"
    "v_mfma_f32_16x16x32_bf16 v[180:183], a[132:135], v[160:163], v[180:183]\n"
    "s_waitcnt lgkmcnt(8)\n"
    "v_or_b32_e32 v193, v193, v201\n"
    "v_exp_f16_sdwa v234, v232 dst_sel:WORD_1 dst_unused:UNUSED_PRESERVE src0_sel:WORD_1\n"
    "v_mfma_f32_16x16x32_bf16 v[180:183], a[240:243], v[164:167], v[180:183]\n"
    "ds_read_u16 v198, v198\n"
    "v_pk_fma_f16 v234, v234, s49, s49\n"
    "v_mfma_f32_16x16x32_bf16 v[112:115], a[200:203], v[184:187], v[112:115]\n"
    "ds_read_u16_d16_hi v206, v206\n"
    "v_rcp_f16_e64 v232, v234\n"
    "v_mfma_f32_16x16x32_bf16 v[116:119], a[204:207], v[184:187], v[116:119]\n"
    "s_waitcnt lgkmcnt(7)\n"
    "v_or_b32_e32 v194, v194, v202\n"
    "v_rcp_f16_sdwa v232, v234 dst_sel:WORD_1 dst_unused:UNUSED_PRESERVE src0_sel:WORD_1\n"
    "v_mfma_f32_16x16x32_bf16 v[128:131], v[236:239], v[184:187], v[128:131]\n"
    "ds_read_u16 v199, v199\n"
    "v_fma_mix_f32 v234, v192, v232, 0 op_sel_hi:[1,1,0]\n"
    "v_mfma_f32_16x16x32_bf16 v[104:107], a[192:195], v[188:191], v[104:107]\n"
    "ds_read_u16_d16_hi v207, v207\n"
    "v_fma_mix_f32 v232, v192, v232, 0 op_sel:[1,1,0] op_sel_hi:[1,1,0]\n"
    "v_mfma_f32_16x16x32_bf16 v[108:111], a[196:199], v[188:191], v[108:111]\n"
    "v_cvt_pk_bf16_f32 v192, v234, v232\n"
    "v_pk_fma_f16 v232, v196, v196, s48\n"
    "v_mfma_f32_16x16x32_bf16 v[120:123], a[200:203], v[188:191], v[120:123]\n"
    "s_waitcnt lgkmcnt(6)\n"
    "v_or_b32_e32 v195, v195, v203\n"
    "v_pk_mul_f16 v232, v196, v232\n"
    "v_mfma_f32_16x16x32_bf16 v[132:135], a[244:247], v[184:187], v[132:135]\n"
    "v_exp_f16_e32 v234, v232\n"
    "v_mfma_f32_16x16x32_bf16 v[124:127], a[204:207], v[188:191], v[124:127]\n"
    "v_exp_f16_sdwa v234, v232 dst_sel:WORD_1 dst_unused:UNUSED_PRESERVE src0_sel:WORD_1\n"
    "v_mfma_f32_16x16x32_bf16 v[136:139], v[236:239], v[188:191], v[136:139]\n"
    "s_waitcnt lgkmcnt(4)\n"
    "v_or_b32_e32 v197, v197, v205\n"
    "s_waitcnt lgkmcnt(2)\n"
    "v_or_b32_e32 v198, v198, v206\n"
    "s_waitcnt lgkmcnt(0)\n"
    "v_or_b32_e32 v199, v199, v207\n"
    "s_mov_b32 s59, s58\n"
    "s_add_u32 s57, s57, 1572864\n"
    "s_add_u32 s58, s58, 1572864\n"
    "s_mov_b32 s70, s46\n"
    "s_add_u32 s88, s57, 98304\n"
    "s_add_u32 s89, s58, 98304\n"
    "s_add_u32 s39, s59, 98304\n"
    "s_add_u32 s64, s64, 1\n"
    "s_cmp_lt_u32 s64, 2\n"
    "s_cselect_b64 s[72:73], s[72:73], s[76:77]\n"
    "s_cselect_b32 s74, s74, s78\n"
    "s_cselect_b32 s53, s53, s79\n"
    "s_cmp_lg_u32 s64, 3\n"
    "s_cbranch_scc1 1b\n"
    ".Lreplk_last:\n"
    "v_lshlrev_b32_sdwa v184, 1, v168 dst_sel:DWORD dst_unused:UNUSED_PAD src0_sel:DWORD src1_sel:WORD_1\n"
    "v_lshlrev_b32_sdwa v200, 1, v169 dst_sel:DWORD dst_unused:UNUSED_PAD src0_sel:DWORD src1_sel:WORD_1\n"
    "v_lshlrev_b32_sdwa v185, 1, v170 dst_sel:DWORD dst_unused:UNUSED_PAD src0_sel:DWORD src1_sel:WORD_1\n"
    "v_lshlrev_b32_sdwa v201, 1, v171 dst_sel:DWORD dst_unused:UNUSED_PAD src0_sel:DWORD src1_sel:WORD_1\n"
    "v_mfma_f32_16x16x32_bf16 v[140:143], a[244:247], v[188:191], v[140:143]\n"
    "s_waitcnt vmcnt(1)\n"
    "ds_read_b128 v[56:59], v244 offset:36864\n"
    "ds_read_u16 v184, v184\n"
    "v_mfma_f32_16x16x32_bf16 v[168:171], a[88:91], v[144:147], 0\n"
    "s_nop 0\n"
    "ds_read_u16_d16_hi v200, v200\n"
    "ds_read_u16 v185, v185\n"
    "v_mfma_f32_16x16x32_bf16 v[168:171], a[136:139], v[148:151], v[168:171]\n"
    "v_lshlrev_b32_sdwa v186, 1, v172 dst_sel:DWORD dst_unused:UNUSED_PAD src0_sel:DWORD src1_sel:WORD_1\n"
    "v_lshlrev_b32_sdwa v202, 1, v173 dst_sel:DWORD dst_unused:UNUSED_PAD src0_sel:DWORD src1_sel:WORD_1\n"
    "v_mfma_f32_16x16x32_bf16 v[168:171], a[52:55], v[152:155], v[168:171]\n"
    "ds_read_u16_d16_hi v201, v201\n"
    "v_lshlrev_b32_sdwa v187, 1, v174 dst_sel:DWORD dst_unused:UNUSED_PAD src0_sel:DWORD src1_sel:WORD_1\n"
    "v_smfmac_f32_32x32x32_bf16 v[128:143], a[248:251], a[224:231], v241\n"
    "ds_read_u16 v186, v186\n"
    "v_lshlrev_b32_sdwa v203, 1, v175 dst_sel:DWORD dst_unused:UNUSED_PAD src0_sel:DWORD src1_sel:WORD_1\n"
    "v_lshlrev_b32_sdwa v188, 1, v176 dst_sel:DWORD dst_unused:UNUSED_PAD src0_sel:DWORD src1_sel:WORD_1\n"
    "v_lshlrev_b32_sdwa v204, 1, v177 dst_sel:DWORD dst_unused:UNUSED_PAD src0_sel:DWORD src1_sel:WORD_1\n"
    "v_lshlrev_b32_sdwa v189, 1, v178 dst_sel:DWORD dst_unused:UNUSED_PAD src0_sel:DWORD src1_sel:WORD_1\n"
    "v_mfma_f32_16x16x32_bf16 v[172:175], a[92:95], v[144:147], 0\n"
    "ds_read_b128 v[236:239], v244 offset:20480\n"
    "v_lshlrev_b32_sdwa v205, 1, v179 dst_sel:DWORD dst_unused:UNUSED_PAD src0_sel:DWORD src1_sel:WORD_1\n"
    "v_mfma_f32_16x16x32_bf16 v[172:175], a[140:143], v[148:151], v[172:175]\n"
    "s_nop 0\n"
    "ds_read_u16_d16_hi v202, v202\n"
    "v_lshlrev_b32_sdwa v190, 1, v180 dst_sel:DWORD dst_unused:UNUSED_PAD src0_sel:DWORD src1_sel:WORD_1\n"
    "v_mfma_f32_16x16x32_bf16 v[176:179], a[88:91], v[156:159], 0\n"
    "s_waitcnt lgkmcnt(7)\n"
    "v_lshlrev_b32_sdwa v206, 1, v181 dst_sel:DWORD dst_unused:UNUSED_PAD src0_sel:DWORD src1_sel:WORD_1\n"
    "v_lshlrev_b32_sdwa v191, 1, v182 dst_sel:DWORD dst_unused:UNUSED_PAD src0_sel:DWORD src1_sel:WORD_1\n"
    "v_mfma_f32_16x16x32_bf16 v[172:175], v[56:59], v[152:155], v[172:175]\n"
    "ds_read_u16 v187, v187\n"
    "buffer_load_dwordx4 a[232:235], v245, s[40:43], s57 offen offset:64\n"
    "buffer_load_dwordx4 a[224:227], v245, s[40:43], s57 offen offset:0\n"
    "buffer_load_dwordx4 v[208:211], v245, s[40:43], s57 offen offset:128\n"
    "v_mfma_f32_16x16x32_bf16 v[176:179], a[136:139], v[160:163], v[176:179]\n"
    "ds_read_b128 a[244:247], v244 offset:22528\n"
    "v_lshlrev_b32_sdwa v207, 1, v183 dst_sel:DWORD dst_unused:UNUSED_PAD src0_sel:DWORD src1_sel:WORD_1\n"
    "v_mfma_f32_16x16x32_bf16 v[176:179], a[52:55], v[164:167], v[176:179]\n"
    "ds_read_u16_d16_hi v203, v203\n"
    "v_pk_fma_f16 v234, v234, s49, s49\n"
    "v_mfma_f32_16x16x32_bf16 v[180:183], a[92:95], v[156:159], 0\n"
    "v_rcp_f16_e32 v232, v234\n"
    "v_mfma_f32_16x16x32_bf16 v[180:183], a[140:143], v[160:163], v[180:183]\n"
    "s_waitcnt lgkmcnt(8)\n"
    "ds_read_u16 v188, v188\n"
    "v_rcp_f16_sdwa v232, v234 dst_sel:WORD_1 dst_unused:UNUSED_PRESERVE src0_sel:WORD_1\n"
    "v_mfma_f32_16x16x32_bf16 v[180:183], v[56:59], v[164:167], v[180:183]\n"
    "ds_read_u16_d16_hi v204, v204\n"
    "v_fma_mix_f32 v234, v196, v232, 0 op_sel_hi:[1,1,0]\n"
    "v_mfma_f32_16x16x32_bf16 v[96:99], a[208:211], v[192:195], v[96:99]\n"
    "v_fma_mix_f32 v232, v196, v232, 0 op_sel:[1,1,0] op_sel_hi:[1,1,0]\n"
    "v_or_b32_e32 v184, v184, v200\n"
    "v_mfma_f32_16x16x32_bf16 v[100:103], a[212:215], v[192:195], v[100:103]\n"
    "s_waitcnt lgkmcnt(8)\n"
    "ds_read_u16 v189, v189\n"
    "v_cvt_pk_bf16_f32 v196, v234, v232\n"
    "v_mfma_f32_16x16x32_bf16 v[112:115], a[216:219], v[192:195], v[112:115]\n"
    "s_waitcnt lgkmcnt(7)\n"
    "v_or_b32_e32 v185, v185, v201\n"
    "v_lshlrev_b32_sdwa v201, 1, v171 dst_sel:DWORD dst_unused:UNUSED_PAD src0_sel:DWORD src1_sel:WORD_1\n"
    "v_mfma_f32_16x16x32_bf16 v[116:119], a[220:223], v[192:195], v[116:119]\n"
    "ds_read_u16_d16_hi v205, v205\n"
    "v_cvt_pk_bf16_f32 v96, v96, v97\n"
    "v_mfma_f32_16x16x32_bf16 v[128:131], v[236:239], v[192:195], v[128:131]\n"
    "v_cvt_pk_bf16_f32 v97, v98, v99\n"
    "v_cvt_pk_bf16_f32 v98, v100, v101\n"
    "v_mfma_f32_16x16x32_bf16 v[104:107], a[208:211], v[196:199], v[104:107]\n"
    "s_waitcnt lgkmcnt(7)\n"
    "ds_read_u16 v190, v190\n"
    "v_or_b32_e32 v186, v186, v202\n"
    "v_mfma_f32_16x16x32_bf16 v[108:111], a[212:215], v[196:199], v[108:111]\n"
    "s_nop 0\n"
    "ds_read_u16_d16_hi v206, v206\n"
    "v_lshlrev_b32_sdwa v202, 1, v173 dst_sel:DWORD dst_unused:UNUSED_PAD src0_sel:DWORD src1_sel:WORD_1\n"
    "v_mfma_f32_16x16x32_bf16 v[120:123], a[216:219], v[196:199], v[120:123]\n"
    "s_waitcnt lgkmcnt(7)\n"
    "v_cvt_pk_bf16_f32 v99, v102, v103\n"
    "v_cvt_pk_bf16_f32 v112, v112, v113\n"
    "v_mfma_f32_16x16x32_bf16 v[132:135], a[244:247], v[192:195], v[132:135]\n"
    "v_cvt_pk_f16_f32 v192, v168, v169\n"
    "v_lshlrev_b32_sdwa v193, 1, v170 dst_sel:DWORD dst_unused:UNUSED_PAD src0_sel:DWORD src1_sel:WORD_1\n"
    "v_mfma_f32_16x16x32_bf16 v[124:127], a[220:223], v[196:199], v[124:127]\n"
    "buffer_load_dwordx4 a[236:239], v245, s[40:43], s88 offen offset:64\n"
    "buffer_load_dwordx4 a[228:231], v245, s[40:43], s88 offen offset:0\n"
    "buffer_load_dwordx4 v[212:215], v245, s[40:43], s88 offen offset:128\n"
    "ds_write_b128 v243, v[96:99] offset:24640\n"
    "v_mfma_f32_16x16x32_bf16 v[136:139], v[236:239], v[196:199], v[136:139]\n"
    "s_waitcnt lgkmcnt(6)\n"
    "v_lshlrev_b32_sdwa v194, 1, v172 dst_sel:DWORD dst_unused:UNUSED_PAD src0_sel:DWORD src1_sel:WORD_1\n"
    "v_lshlrev_b32_sdwa v195, 1, v174 dst_sel:DWORD dst_unused:UNUSED_PAD src0_sel:DWORD src1_sel:WORD_1\n"
    "v_mfma_f32_16x16x32_bf16 v[140:143], a[244:247], v[196:199], v[140:143]\n"
    "v_cvt_pk_f16_f32 v196, v176, v177\n"
    "v_lshlrev_b32_sdwa v197, 1, v178 dst_sel:DWORD dst_unused:UNUSED_PAD src0_sel:DWORD src1_sel:WORD_1\n"
    "v_mfma_f32_16x16x32_bf16 v[168:171], a[96:99], v[144:147], 0\n"
    "s_waitcnt lgkmcnt(5)\n"
    "ds_read_u16 v191, v191\n"
    "ds_read_u16_d16_hi v207, v207\n"
    "v_mfma_f32_16x16x32_bf16 v[168:171], a[144:147], v[148:151], v[168:171]\n"
    "ds_read_u16 v193, v193\n"
    "v_lshlrev_b32_sdwa v198, 1, v180 dst_sel:DWORD dst_unused:UNUSED_PAD src0_sel:DWORD src1_sel:WORD_1\n"
    "v_mfma_f32_16x16x32_bf16 v[168:171], a[56:59], v[152:155], v[168:171]\n"
    "ds_read_u16_d16_hi v201, v201\n"
    "v_lshlrev_b32_sdwa v199, 1, v182 dst_sel:DWORD dst_unused:UNUSED_PAD src0_sel:DWORD src1_sel:WORD_1\n"
    "v_or_b32_e64 v187, v187, v203\n"
    "v_lshlrev_b32_sdwa v203, 1, v175 dst_sel:DWORD dst_unused:UNUSED_PAD src0_sel:DWORD src1_sel:WORD_1\n"
    "v_mfma_f32_16x16x32_bf16 v[172:175], a[100:103], v[144:147], 0\n"
    "s_waitcnt lgkmcnt(7)\n"
    "ds_read_u16 v194, v194\n"
    "v_or_b32_e32 v188, v188, v204\n"
    "v_mfma_f32_16x16x32_bf16 v[172:175], a[148:151], v[148:151], v[172:175]\n"
    "v_or_b32_e64 v189, v189, v205\n"
    "v_lshlrev_b32_sdwa v205, 1, v179 dst_sel:DWORD dst_unused:UNUSED_PAD src0_sel:DWORD src1_sel:WORD_1\n"
    "v_mfma_f32_16x16x32_bf16 v[172:175], a[60:63], v[152:155], v[172:175]\n"
    "ds_read_u16_d16_hi v202, v202\n"
    "v_cvt_pk_bf16_f32 v113, v114, v115\n"
    "v_mfma_f32_16x16x32_bf16 v[176:179], a[96:99], v[156:159], 0\n"
    "s_waitcnt lgkmcnt(6)\n"
    "v_or_b32_e32 v190, v190, v206\n"
    "v_lshlrev_b32_sdwa v206, 1, v181 dst_sel:DWORD dst_unused:UNUSED_PAD src0_sel:DWORD src1_sel:WORD_1\n"
    "v_mfma_f32_16x16x32_bf16 v[176:179], a[144:147], v[160:163], v[176:179]\n"
    "ds_read_b128 v[0:3], v244 offset:0\n"
    "ds_read_b128 a[252:255], v249 offset:0\n"
    "v_mfma_f32_16x16x32_bf16 v[176:179], a[56:59], v[164:167], v[176:179]\n"
    "v_cvt_pk_bf16_f32 v114, v116, v117\n"
    "v_cvt_pk_bf16_f32 v115, v118, v119\n"
    "v_mfma_f32_16x16x32_bf16 v[96:99], a[0:3], v[184:187], 0\n"
    "v_cvt_pk_bf16_f32 v128, v128, v129\n"
    "v_cvt_pk_bf16_f32 v129, v130, v131\n"
    "v_mfma_f32_16x16x32_bf16 v[100:103], a[4:7], v[184:187], 0\n"
    "ds_write_b128 v243, v[112:115] offset:26688\n"
    "v_mfma_f32_16x16x32_bf16 v[112:115], a[8:11], v[184:187], 0\n"
    "s_waitcnt lgkmcnt(7)\n"
    "v_or_b32_e32 v191, v191, v207\n"
    "v_lshlrev_b32_sdwa v207, 1, v183 dst_sel:DWORD dst_unused:UNUSED_PAD src0_sel:DWORD src1_sel:WORD_1\n"
    "v_mfma_f32_16x16x32_bf16 v[180:183], a[100:103], v[156:159], 0\n"
    "v_cvt_pk_bf16_f32 v130, v132, v133\n"
    "v_cvt_pk_bf16_f32 v131, v134, v135\n"
    "v_mfma_f32_16x16x32_bf16 v[180:183], a[148:151], v[160:163], v[180:183]\n"
    "ds_read_u16 v195, v195\n"
    "ds_write_b128 v243, v[128:131] offset:28736\n"
    "v_mfma_f32_16x16x32_bf16 v[180:183], a[60:63], v[164:167], v[180:183]\n"
    "s_waitcnt lgkmcnt(7)\n"
    "v_or_b32_e32 v193, v193, v201\n"
    "v_cvt_pk_bf16_f32 v104, v104, v105\n"
    "v_mfma_f32_16x16x32_bf16 v[116:119], a[12:15], v[184:187], 0\n"
    "v_cvt_pk_bf16_f32 v105, v106, v107\n"
    "v_cvt_pk_bf16_f32 v106, v108, v109\n"
    "v_cvt_pk_bf16_f32 v107, v110, v111\n"
    "v_mfma_f32_16x16x32_bf16 v[108:111], a[4:7], v[188:191], 0\n"
    "ds_read_u16_d16_hi v203, v203\n"
    "ds_write_b128 v243, v[104:107] offset:25664\n"
    "v_mfma_f32_16x16x32_bf16 v[104:107], a[0:3], v[188:191], 0\n"
    "s_waitcnt lgkmcnt(5)\n"
    "buffer_store_dwordx4 a[252:255], v255, s[68:71], s59 offen offset:0 sc1\n"
    "v_mfma_f32_16x16x32_bf16 v[128:131], v[0:3], v[184:187], 0\n"
    "s_nop 0\n"
    "ds_read_b128 v[4:7], v244 offset:2048\n"
    "ds_read_u16 v197, v197\n"
    "v_or_b32_e64 v194, v194, v202\n"
    "v_cvt_pk_bf16_f32 v120, v120, v121\n"
    "v_cvt_pk_bf16_f32 v121, v122, v123\n"
    "v_cvt_pk_bf16_f32 v122, v124, v125\n"
    "v_cvt_pk_bf16_f32 v123, v126, v127\n"
    "ds_write_b128 v243, v[120:123] offset:27712\n"
    "v_pk_fma_f16 v232, v192, v192, s48\n"
    "v_mfma_f32_16x16x32_bf16 v[120:123], a[8:11], v[188:191], 0\n"
    "v_cvt_pk_bf16_f32 v136, v136, v137\n"
    "v_cvt_pk_bf16_f32 v137, v138, v139\n"
    "v_mfma_f32_16x16x32_bf16 v[124:127], a[12:15], v[188:191], 0\n"
    "ds_read_u16_d16_hi v205, v205\n"
    "ds_read_u16 v198, v198\n"
    "v_cvt_pk_bf16_f32 v138, v140, v141\n"
    "v_cvt_pk_bf16_f32 v139, v142, v143\n"
    "ds_write_b128 v243, v[136:139] offset:29760\n"
    "s_waitcnt lgkmcnt(5)\n"
    "v_mfma_f32_16x16x32_bf16 v[132:135], v[4:7], v[184:187], 0\n"
    "v_or_b32_e32 v195, v195, v203\n"
    "v_pk_mul_f16 v232, v192, v232\n"
    "v_mfma_f32_16x16x32_bf16 v[136:139], v[0:3], v[188:191], 0\n"
    "ds_read_u16_d16_hi v206, v206\n"
    "ds_read_u16 v199, v199\n"
    "v_exp_f16_e64 v234, v232\n"
    "v_mfma_f32_16x16x32_bf16 v[140:143], v[4:7], v[188:191], 0\n"
    "ds_read_u16_d16_hi v207, v207\n"
    "v_exp_f16_sdwa v234, v232 dst_sel:WORD_1 dst_unused:UNUSED_PRESERVE src0_sel:WORD_1\n"
    "v_cvt_pk_f16_f32 v184, v168, v169\n"
    "v_pk_fma_f16 v234, v234, s49, s49\n"
    "v_lshlrev_b32_sdwa v185, 1, v170 dst_sel:DWORD dst_unused:UNUSED_PAD src0_sel:DWORD src1_sel:WORD_1\n"
    "v_lshlrev_b32_sdwa v201, 1, v171 dst_sel:DWORD dst_unused:UNUSED_PAD src0_sel:DWORD src1_sel:WORD_1\n"
    "v_mfma_f32_16x16x32_bf16 v[168:171], a[104:107], v[144:147], 0\n"
    "ds_read_b128 a[252:255], v249 offset:2048\n"
    "ds_read_u16 v185, v185\n"
    "v_mfma_f32_16x16x32_bf16 v[168:171], a[152:155], v[148:151], v[168:171]\n"
    "v_lshlrev_b32_sdwa v186, 1, v172 dst_sel:DWORD dst_unused:UNUSED_PAD src0_sel:DWORD src1_sel:WORD_1\n"
    "v_lshlrev_b32_sdwa v202, 1, v173 dst_sel:DWORD dst_unused:UNUSED_PAD src0_sel:DWORD src1_sel:WORD_1\n"
    "v_lshlrev_b32_sdwa v187, 1, v174 dst_sel:DWORD dst_unused:UNUSED_PAD src0_sel:DWORD src1_sel:WORD_1\n"
    "s_waitcnt lgkmcnt(7)\n"
    "v_or_b32_e32 v197, v197, v205\n"
    "v_lshlrev_b32_sdwa v203, 1, v175 dst_sel:DWORD dst_unused:UNUSED_PAD src0_sel:DWORD src1_sel:WORD_1\n"
    "v_rcp_f16_e64 v232, v234\n"
    "v_mfma_f32_16x16x32_bf16 v[168:171], a[64:67], v[152:155], v[168:171]\n"
    "ds_read_u16_d16_hi v201, v201\n"
    "ds_read_u16 v186, v186\n"
    "v_rcp_f16_sdwa v232, v234 dst_sel:WORD_1 dst_unused:UNUSED_PRESERVE src0_sel:WORD_1\n"
    "v_mfma_f32_16x16x32_bf16 v[172:175], a[108:111], v[144:147], 0\n"
    "ds_read_u16_d16_hi v202, v202\n"
    "v_cvt_pk_f16_f32 v188, v176, v177\n"
    "v_mfma_f32_16x16x32_bf16 v[172:175], a[156:159], v[148:151], v[172:175]\n"
    "v_lshlrev_b32_sdwa v189, 1, v178 dst_sel:DWORD dst_unused:UNUSED_PAD src0_sel:DWORD src1_sel:WORD_1\n"
    "v_lshlrev_b32_sdwa v205, 1, v179 dst_sel:DWORD dst_unused:UNUSED_PAD src0_sel:DWORD src1_sel:WORD_1\n"
    "v_lshlrev_b32_sdwa v190, 1, v180 dst_sel:DWORD dst_unused:UNUSED_PAD src0_sel:DWORD src1_sel:WORD_1\n"
    "v_lshlrev_b32_sdwa v191, 1, v182 dst_sel:DWORD dst_unused:UNUSED_PAD src0_sel:DWORD src1_sel:WORD_1\n"
    "v_fma_mix_f32 v234, v192, v232, 0 op_sel_hi:[1,1,0]\n"
    "v_mfma_f32_16x16x32_bf16 v[172:175], a[68:71], v[152:155], v[172:175]\n"
    "s_waitcnt lgkmcnt(7)\n"
    "ds_read_b128 v[8:11], v244 offset:4096\n"
    "ds_read_u16 v187, v187\n"
    "v_or_b32_e32 v198, v198, v206\n"
    "v_lshlrev_b32_sdwa v206, 1, v181 dst_sel:DWORD dst_unused:UNUSED_PAD src0_sel:DWORD src1_sel:WORD_1\n"
    "v_fma_mix_f32 v232, v192, v232, 0 op_sel:[1,1,0] op_sel_hi:[1,1,0]\n"
    "v_mfma_f32_16x16x32_bf16 v[176:179], a[104:107], v[156:159], 0\n"
    "ds_read_u16_d16_hi v203, v203\n"
    "v_cvt_pk_bf16_f32 v192, v234, v232\n"
    "v_mfma_f32_16x16x32_bf16 v[176:179], a[152:155], v[160:163], v[176:179]\n"
    "s_waitcnt lgkmcnt(7)\n"
    "buffer_store_dwordx4 a[252:255], v255, s[68:71], s59 offen offset:64 sc1\n"
    "ds_read_b128 v[12:15], v244 offset:6144\n"
    "v_mfma_f32_16x16x32_bf16 v[176:179], a[64:67], v[164:167], v[176:179]\n"
    "s_nop 0\n"
    "ds_read_b128 a[252:255], v249 offset:4096\n"
    "v_or_b32_e64 v199, v199, v207\n"
    "v_lshlrev_b32_sdwa v207, 1, v183 dst_sel:DWORD dst_unused:UNUSED_PAD src0_sel:DWORD src1_sel:WORD_1\n"
    "v_pk_fma_f16 v232, v196, v196, s48\n"
    "v_mfma_f32_16x16x32_bf16 v[180:183], a[108:111], v[156:159], 0\n"
    "ds_read_u16 v189, v189\n"
    "v_pk_mul_f16 v232, v196, v232\n"
    "v_mfma_f32_16x16x32_bf16 v[180:183], a[156:159], v[160:163], v[180:183]\n"
    "ds_read_u16_d16_hi v205, v205\n"
    "v_exp_f16_e32 v234, v232\n"
    "v_mfma_f32_16x16x32_bf16 v[180:183], a[68:71], v[164:167], v[180:183]\n"
    "s_waitcnt lgkmcnt(9)\n"
    "v_exp_f16_sdwa v234, v232 dst_sel:WORD_1 dst_unused:UNUSED_PRESERVE src0_sel:WORD_1\n"
    "v_mfma_f32_16x16x32_bf16 v[96:99], a[16:19], v[192:195], v[96:99]\n"
    "ds_read_u16 v190, v190\n"
    "v_pk_fma_f16 v234, v234, s49, s49\n"
    "v_mfma_f32_16x16x32_bf16 v[100:103], a[20:23], v[192:195], v[100:103]\n"
    "ds_read_u16_d16_hi v206, v206\n"
    "v_rcp_f16_e32 v232, v234\n"
    "v_mfma_f32_16x16x32_bf16 v[112:115], a[24:27], v[192:195], v[112:115]\n"
    "s_waitcnt lgkmcnt(8)\n"
    "v_rcp_f16_sdwa v232, v234 dst_sel:WORD_1 dst_unused:UNUSED_PRESERVE src0_sel:WORD_1\n"
    "v_mfma_f32_16x16x32_bf16 v[116:119], a[28:31], v[192:195], v[116:119]\n"
    "ds_read_u16 v191, v191\n"
    "v_or_b32_e64 v185, v185, v201\n"
    "v_mfma_f32_16x16x32_bf16 v[128:131], v[8:11], v[192:195], v[128:131]\n"
    "v_fma_mix_f32 v234, v196, v232, 0 op_sel_hi:[1,1,0]\n"
    "v_fma_mix_f32 v232, v196, v232, 0 op_sel:[1,1,0] op_sel_hi:[1,1,0]\n"
    "v_or_b32_e64 v186, v186, v202\n"
    "v_cvt_pk_bf16_f32 v196, v234, v232\n"
    "v_pk_fma_f16 v232, v184, v184, s48\n"
    "v_lshlrev_b32_sdwa v200, 1, v169 dst_sel:DWORD dst_unused:UNUSED_PAD src0_sel:DWORD src1_sel:WORD_1\n"
    "v_mfma_f32_16x16x32_bf16 v[104:107], a[16:19], v[196:199], v[104:107]\n"
    "s_waitcnt lgkmcnt(6)\n"
    "ds_read_u16_d16_hi v207, v207\n"
    "ds_read_b128 v[52:55], v244 offset:34816\n"
    "v_mfma_f32_16x16x32_bf16 v[132:135], v[12:15], v[192:195], v[132:135]\n"
    "s_waitcnt lgkmcnt(7)\n"
    "ds_read_u16_d16_hi v200, v200\n"
    "buffer_store_dwordx4 a[252:255], v255, s[68:71], s59 offen offset:128 sc1\n"
    "v_mfma_f32_16x16x32_bf16 v[108:111], a[20:23], v[196:199], v[108:111]\n"
    "v_or_b32_e64 v187, v187, v203\n"
    "v_pk_mul_f16 v232, v184, v232\n"
    "v_mfma_f32_16x16x32_bf16 v[120:123], a[24:27], v[196:199], v[120:123]\n"
    "ds_read_b128 a[252:255], v249 offset:1024\n"
    "v_exp_f16_e64 v234, v232\n"
    "v_mfma_f32_16x16x32_bf16 v[124:127], a[28:31], v[196:199], v[124:127]\n"
    "s_waitcnt lgkmcnt(7)\n"
    "v_or_b32_e32 v189, v189, v205\n"
    "v_exp_f16_sdwa v234, v232 dst_sel:WORD_1 dst_unused:UNUSED_PRESERVE src0_sel:WORD_1\n"
    "v_mfma_f32_16x16x32_bf16 v[136:139], v[8:11], v[196:199], v[136:139]\n"
    "ds_read_b128 v[16:19], v244 offset:8192\n"
    "v_lshlrev_b32_sdwa v192, 1, v168 dst_sel:DWORD dst_unused:UNUSED_PAD src0_sel:DWORD src1_sel:WORD_1\n"
    "v_mfma_f32_16x16x32_bf16 v[140:143], v[12:15], v[196:199], v[140:143]\n"
    "ds_read_u16 v192, v192\n"
    "v_pk_fma_f16 v234, v234, s49, s49\n"
    "v_lshlrev_b32_sdwa v193, 1, v170 dst_sel:DWORD dst_unused:UNUSED_PAD src0_sel:DWORD src1_sel:WORD_1\n"
    "v_lshlrev_b32_sdwa v201, 1, v171 dst_sel:DWORD dst_unused:UNUSED_PAD src0_sel:DWORD src1_sel:WORD_1\n"
    "v_mfma_f32_16x16x32_bf16 v[168:171], a[112:115], v[144:147], 0\n"
    "s_waitcnt lgkmcnt(7)\n"
    "ds_read_u16 v193, v193\n"
    "ds_read_u16_d16_hi v201, v201\n"
    "v_mfma_f32_16x16x32_bf16 v[168:171], a[160:163], v[148:151], v[168:171]\n"
    "s_nop 0\n"
    "v_lshlrev_b32_sdwa v194, 1, v172 dst_sel:DWORD dst_unused:UNUSED_PAD src0_sel:DWORD src1_sel:WORD_1\n"
    "v_lshlrev_b32_sdwa v202, 1, v173 dst_sel:DWORD dst_unused:UNUSED_PAD src0_sel:DWORD src1_sel:WORD_1\n"
    "v_lshlrev_b32_sdwa v195, 1, v174 dst_sel:DWORD dst_unused:UNUSED_PAD src0_sel:DWORD src1_sel:WORD_1\n"
    "v_lshlrev_b32_sdwa v203, 1, v175 dst_sel:DWORD dst_unused:UNUSED_PAD src0_sel:DWORD src1_sel:WORD_1\n"
    "v_rcp_f16_e64 v232, v234\n"
    "v_mfma_f32_16x16x32_bf16 v[168:171], a[72:75], v[152:155], v[168:171]\n"
    "ds_read_u16 v194, v194\n"
    "ds_read_u16_d16_hi v202, v202\n"
    "v_or_b32_e64 v190, v190, v206\n"
    "v_rcp_f16_sdwa v232, v234 dst_sel:WORD_1 dst_unused:UNUSED_PRESERVE src0_sel:WORD_1\n"
    "v_mfma_f32_16x16x32_bf16 v[172:175], a[116:119], v[144:147], 0\n"
    "s_waitcnt lgkmcnt(8)\n"
    "ds_read_u16 v195, v195\n"
    "v_lshlrev_b32_sdwa v196, 1, v176 dst_sel:DWORD dst_unused:UNUSED_PAD src0_sel:DWORD src1_sel:WORD_1\n"
    "v_mfma_f32_16x16x32_bf16 v[172:175], a[164:167], v[148:151], v[172:175]\n"
    "s_waitcnt lgkmcnt(7)\n"
    "buffer_store_dwordx4 a[252:255], v255, s[68:71], s39 offen offset:0 sc1\n"
    "ds_read_b128 v[20:23], v244 offset:10240\n"
    "v_lshlrev_b32_sdwa v204, 1, v177 dst_sel:DWORD dst_unused:UNUSED_PAD src0_sel:DWORD src1_sel:WORD_1\n"
    "v_lshlrev_b32_sdwa v197, 1, v178 dst_sel:DWORD dst_unused:UNUSED_PAD src0_sel:DWORD src1_sel:WORD_1\n"
    "v_lshlrev_b32_sdwa v205, 1, v179 dst_sel:DWORD dst_unused:UNUSED_PAD src0_sel:DWORD src1_sel:WORD_1\n"
    "v_lshlrev_b32_sdwa v198, 1, v180 dst_sel:DWORD dst_unused:UNUSED_PAD src0_sel:DWORD src1_sel:WORD_1\n"
    "v_lshlrev_b32_sdwa v206, 1, v181 dst_sel:DWORD dst_unused:UNUSED_PAD src0_sel:DWORD src1_sel:WORD_1\n"
    "v_lshlrev_b32_sdwa v199, 1, v182 dst_sel:DWORD dst_unused:UNUSED_PAD src0_sel:DWORD src1_sel:WORD_1\n"
    "v_fma_mix_f32 v234, v184, v232, 0 op_sel_hi:[1,1,0]\n"
    "v_mfma_f32_16x16x32_bf16 v[172:175], v[52:55], v[152:155], v[172:175]\n"
    "ds_read_u16_d16_hi v203, v203\n"
    "ds_read_u16 v196, v196\n"
    "v_or_b32_e32 v191, v191, v207\n"
    "v_lshlrev_b32_sdwa v207, 1, v183 dst_sel:DWORD dst_unused:UNUSED_PAD src0_sel:DWORD src1_sel:WORD_1\n"
    "v_fma_mix_f32 v232, v184, v232, 0 op_sel:[1,1,0] op_sel_hi:[1,1,0]\n"
    "v_mfma_f32_16x16x32_bf16 v[176:179], a[112:115], v[156:159], 0\n"
    "s_waitcnt lgkmcnt(8)\n"
    "ds_read_u16_d16_hi v204, v204\n"
    "v_cvt_pk_bf16_f32 v184, v234, v232\n"
    "v_mfma_f32_16x16x32_bf16 v[176:179], a[160:163], v[160:163], v[176:179]\n"
    "v_pk_fma_f16 v232, v188, v188, s48\n"
    "v_or_b32_e32 v192, v192, v200\n"
    "v_mfma_f32_16x16x32_bf16 v[176:179], a[72:75], v[164:167], v[176:179]\n"
    "s_waitcnt lgkmcnt(7)\n"
    "ds_read_u16 v197, v197\n"
    "v_pk_mul_f16 v232, v188, v232\n"
    "v_mfma_f32_16x16x32_bf16 v[180:183], a[116:119], v[156:159], 0\n"
    "ds_read_b128 a[252:255], v249 offset:3072\n"
    "v_exp_f16_e64 v234, v232\n"
    "v_mfma_f32_16x16x32_bf16 v[180:183], a[164:167], v[160:163], v[180:183]\n"
    "ds_read_u16_d16_hi v205, v205\n"
    "v_exp_f16_sdwa v234, v232 dst_sel:WORD_1 dst_unused:UNUSED_PRESERVE src0_sel:WORD_1\n"
    "v_mfma_f32_16x16x32_bf16 v[180:183], v[52:55], v[164:167], v[180:183]\n"
    "s_waitcnt lgkmcnt(8)\n"
    "v_pk_fma_f16 v234, v234, s49, s49\n"
    "v_or_b32_e32 v193, v193, v201\n"
    "v_mfma_f32_16x16x32_bf16 v[96:99], a[32:35], v[184:187], v[96:99]\n"
    "ds_read_u16 v198, v198\n"
    "v_rcp_f16_e64 v232, v234\n"
    "v_mfma_f32_16x16x32_bf16 v[100:103], a[36:39], v[184:187], v[100:103]\n"
    "ds_read_u16_d16_hi v206, v206\n"
    "v_rcp_f16_sdwa v232, v234 dst_sel:WORD_1 dst_unused:UNUSED_PRESERVE src0_sel:WORD_1\n"
    "v_mfma_f32_16x16x32_bf16 v[112:115], a[40:43], v[184:187], v[112:115]\n"
    "s_nop 0\n"
    "v_fma_mix_f32 v234, v188, v232, 0 op_sel_hi:[1,1,0]\n"
    "v_fma_mix_f32 v232, v188, v232, 0 op_sel:[1,1,0] op_sel_hi:[1,1,0]\n"
    "v_mfma_f32_16x16x32_bf16 v[116:119], a[44:47], v[184:187], v[116:119]\n"
    "s_waitcnt lgkmcnt(7)\n"
    "ds_read_u16 v199, v199\n"
    "v_cvt_pk_bf16_f32 v188, v234, v232\n"
    "v_mfma_f32_16x16x32_bf16 v[128:131], v[16:19], v[184:187], v[128:131]\n"
    "ds_read_u16_d16_hi v207, v207\n"
    "v_lshlrev_b32_sdwa v201, 1, v171 dst_sel:DWORD dst_unused:UNUSED_PAD src0_sel:DWORD src1_sel:WORD_1\n"
    "v_mfma_f32_16x16x32_bf16 v[132:135], v[20:23], v[184:187], v[132:135]\n"
    "v_cvt_pk_f16_f32 v184, v168, v169\n"
    "v_lshlrev_b32_sdwa v185, 1, v170 dst_sel:DWORD dst_unused:UNUSED_PAD src0_sel:DWORD src1_sel:WORD_1\n"
    "v_mfma_f32_16x16x32_bf16 v[104:107], a[32:35], v[188:191], v[104:107]\n"
    "ds_read_b128 v[60:63], v244 offset:38912\n"
    "v_lshlrev_b32_sdwa v186, 1, v172 dst_sel:DWORD dst_unused:UNUSED_PAD src0_sel:DWORD src1_sel:WORD_1\n"
    "v_mfma_f32_16x16x32_bf16 v[108:111], a[36:39], v[188:191], v[108:111]\n"
    "s_waitcnt lgkmcnt(8)\n"
    "ds_read_u16 v185, v185\n"
    "v_or_b32_e32 v194, v194, v202\n"
    "v_mfma_f32_16x16x32_bf16 v[120:123], a[40:43], v[188:191], v[120:123]\n"
    "s_waitcnt lgkmcnt(7)\n"
    "buffer_store_dwordx4 a[252:255], v255, s[68:71], s39 offen offset:64 sc1\n"
    "ds_read_u16_d16_hi v201, v201\n"
    "v_mfma_f32_16x16x32_bf16 v[124:127], a[44:47], v[188:191], v[124:127]\n"
    "v_lshlrev_b32_sdwa v202, 1, v173 dst_sel:DWORD dst_unused:UNUSED_PAD src0_sel:DWORD src1_sel:WORD_1\n"
    "v_lshlrev_b32_sdwa v187, 1, v174 dst_sel:DWORD dst_unused:UNUSED_PAD src0_sel:DWORD src1_sel:WORD_1\n"
    "v_mfma_f32_16x16x32_bf16 v[136:139], v[16:19], v[188:191], v[136:139]\n"
    "s_waitcnt lgkmcnt(7)\n"
    "ds_read_b128 a[252:255], v249 offset:5120\n"
    "ds_read_u16 v186, v186\n"
    "v_mfma_f32_16x16x32_bf16 v[140:143], v[20:23], v[188:191], v[140:143]\n"
    "v_cvt_pk_f16_f32 v188, v176, v177\n"
    "v_lshlrev_b32_sdwa v189, 1, v178 dst_sel:DWORD dst_unused:UNUSED_PAD src0_sel:DWORD src1_sel:WORD_1\n"
    "v_mfma_f32_16x16x32_bf16 v[168:171], a[120:123], v[144:147], 0\n"
    "ds_read_u16_d16_hi v202, v202\n"
    "v_lshlrev_b32_sdwa v190, 1, v180 dst_sel:DWORD dst_unused:UNUSED_PAD src0_sel:DWORD src1_sel:WORD_1\n"
    "v_mfma_f32_16x16x32_bf16 v[168:171], a[168:171], v[148:151], v[168:171]\n"
    "v_lshlrev_b32_sdwa v191, 1, v182 dst_sel:DWORD dst_unused:UNUSED_PAD src0_sel:DWORD src1_sel:WORD_1\n"
    "v_or_b32_e32 v195, v195, v203\n"
    "v_mfma_f32_16x16x32_bf16 v[168:171], a[76:79], v[152:155], v[168:171]\n"
    "s_waitcnt lgkmcnt(8)\n"
    "ds_read_b128 v[24:27], v244 offset:12288\n"
    "v_lshlrev_b32_sdwa v203, 1, v175 dst_sel:DWORD dst_unused:UNUSED_PAD src0_sel:DWORD src1_sel:WORD_1\n"
    "v_mfma_f32_16x16x32_bf16 v[172:175], a[124:127], v[144:147], 0\n"
    "ds_read_u16 v187, v187\n"
    "v_or_b32_e64 v196, v196, v204\n"
    "v_mfma_f32_16x16x32_bf16 v[172:175], a[172:175], v[148:151], v[172:175]\n"
    "v_or_b32_e64 v197, v197, v205\n"
    "v_lshlrev_b32_sdwa v205, 1, v179 dst_sel:DWORD dst_unused:UNUSED_PAD src0_sel:DWORD src1_sel:WORD_1\n"
    "v_mfma_f32_16x16x32_bf16 v[176:179], a[120:123], v[156:159], 0\n"
    "s_waitcnt lgkmcnt(7)\n"
    "ds_read_u16_d16_hi v203, v203\n"
    "v_or_b32_e32 v198, v198, v206\n"
    "v_mfma_f32_16x16x32_bf16 v[172:175], v[60:63], v[152:155], v[172:175]\n"
    "v_lshlrev_b32_sdwa v206, 1, v181 dst_sel:DWORD dst_unused:UNUSED_PAD src0_sel:DWORD src1_sel:WORD_1\n"
    "v_or_b32_e64 v199, v199, v207\n"
    "v_mfma_f32_16x16x32_bf16 v[176:179], a[168:171], v[160:163], v[176:179]\n"
    "ds_read_b128 v[28:31], v244 offset:14336\n"
    "ds_read_u16 v189, v189\n"
    "v_mfma_f32_16x16x32_bf16 v[176:179], a[76:79], v[164:167], v[176:179]\n"
    "v_lshlrev_b32_sdwa v207, 1, v183 dst_sel:DWORD dst_unused:UNUSED_PAD src0_sel:DWORD src1_sel:WORD_1\n"
    "v_pk_fma_f16 v232, v184, v184, s48\n"
    "v_mfma_f32_16x16x32_bf16 v[180:183], a[124:127], v[156:159], 0\n"
    "ds_read_u16_d16_hi v205, v205\n"
    "v_pk_mul_f16 v232, v184, v232\n"
    "v_mfma_f32_16x16x32_bf16 v[180:183], a[172:175], v[160:163], v[180:183]\n"
    "s_waitcnt lgkmcnt(8)\n"
    "buffer_store_dwordx4 a[252:255], v255, s[68:71], s39 offen offset:128 sc1\n"
    "ds_read_u16 v190, v190\n"
    "v_mfma_f32_16x16x32_bf16 v[180:183], v[60:63], v[164:167], v[180:183]\n"
    "v_or_b32_e32 v185, v185, v201\n"
    "v_exp_f16_e64 v234, v232\n"
    "v_mfma_f32_16x16x32_bf16 v[96:99], a[176:179], v[192:195], v[96:99]\n"
    "ds_read_b128 v[144:147], v243 offset:0\n"
    "ds_read_u16_d16_hi v206, v206\n"
    "v_mfma_f32_16x16x32_bf16 v[100:103], a[180:183], v[192:195], v[100:103]\n"
    "s_waitcnt lgkmcnt(8)\n"
    "v_or_b32_e32 v186, v186, v202\n"
    "v_exp_f16_sdwa v234, v232 dst_sel:WORD_1 dst_unused:UNUSED_PRESERVE src0_sel:WORD_1\n"
    "v_mfma_f32_16x16x32_bf16 v[112:115], a[184:187], v[192:195], v[112:115]\n"
    "ds_read_b128 v[148:151], v243 offset:1024\n"
    "v_pk_fma_f16 v234, v234, s49, s49\n"
    "v_mfma_f32_16x16x32_bf16 v[116:119], a[188:191], v[192:195], v[116:119]\n"
    "ds_read_u16 v191, v191\n"
    "v_lshlrev_b32_sdwa v201, 1, v171 dst_sel:DWORD dst_unused:UNUSED_PAD src0_sel:DWORD src1_sel:WORD_1\n"
    "v_mfma_f32_16x16x32_bf16 v[128:131], v[24:27], v[192:195], v[128:131]\n"
    "v_lshlrev_b32_sdwa v202, 1, v173 dst_sel:DWORD dst_unused:UNUSED_PAD src0_sel:DWORD src1_sel:WORD_1\n"
    "v_rcp_f16_e32 v232, v234\n"
    "v_mfma_f32_16x16x32_bf16 v[104:107], a[176:179], v[196:199], v[104:107]\n"
    "s_waitcnt lgkmcnt(7)\n"
    "ds_read_b128 v[152:155], v243 offset:2048\n"
    "ds_read_u16_d16_hi v207, v207\n"
    "v_mfma_f32_16x16x32_bf16 v[132:135], v[28:31], v[192:195], v[132:135]\n"
    "v_or_b32_e64 v187, v187, v203\n"
    "v_cvt_pk_f16_f32 v192, v168, v169\n"
    "v_mfma_f32_16x16x32_bf16 v[108:111], a[180:183], v[196:199], v[108:111]\n"
    "ds_read_b128 v[156:159], v243 offset:3072\n"
    "v_lshlrev_b32_sdwa v193, 1, v170 dst_sel:DWORD dst_unused:UNUSED_PAD src0_sel:DWORD src1_sel:WORD_1\n"
    "v_mfma_f32_16x16x32_bf16 v[120:123], a[184:187], v[196:199], v[120:123]\n"
    "ds_read_b128 v[160:163], v243 offset:4096\n"
    "v_lshlrev_b32_sdwa v194, 1, v172 dst_sel:DWORD dst_unused:UNUSED_PAD src0_sel:DWORD src1_sel:WORD_1\n"
    "v_mfma_f32_16x16x32_bf16 v[124:127], a[188:191], v[196:199], v[124:127]\n"
    "ds_read_b128 v[164:167], v243 offset:5120\n"
    "v_lshlrev_b32_sdwa v195, 1, v174 dst_sel:DWORD dst_unused:UNUSED_PAD src0_sel:DWORD src1_sel:WORD_1\n"
    "v_mfma_f32_16x16x32_bf16 v[136:139], v[24:27], v[196:199], v[136:139]\n"
    "s_waitcnt lgkmcnt(10)\n"
    "ds_read_b128 v[48:51], v244 offset:32768\n"
    "v_or_b32_e64 v189, v189, v205\n"
    "v_mfma_f32_16x16x32_bf16 v[140:143], v[28:31], v[196:199], v[140:143]\n"
    "s_waitcnt lgkmcnt(9)\n"
    "ds_read_u16 v193, v193\n"
    "v_lshlrev_b32_sdwa v203, 1, v175 dst_sel:DWORD dst_unused:UNUSED_PAD src0_sel:DWORD src1_sel:WORD_1\n"
    "v_mfma_f32_16x16x32_bf16 v[168:171], a[80:83], v[144:147], 0\n"
    "v_rcp_f16_sdwa v232, v234 dst_sel:WORD_1 dst_unused:UNUSED_PRESERVE src0_sel:WORD_1\n"
    "v_mfma_f32_16x16x32_bf16 v[172:175], a[84:87], v[144:147], 0\n"
    "ds_read_u16_d16_hi v201, v201\n"
    "v_cvt_pk_f16_f32 v196, v176, v177\n"
    "s_waitcnt lgkmcnt(9)\n"
    "v_mfma_f32_16x16x32_bf16 v[168:171], a[128:131], v[148:151], v[168:171]\n"
    "v_or_b32_e32 v190, v190, v206\n"
    "v_lshlrev_b32_sdwa v197, 1, v178 dst_sel:DWORD dst_unused:UNUSED_PAD src0_sel:DWORD src1_sel:WORD_1\n"
    "v_lshlrev_b32_sdwa v205, 1, v179 dst_sel:DWORD dst_unused:UNUSED_PAD src0_sel:DWORD src1_sel:WORD_1\n"
    "v_lshlrev_b32_sdwa v198, 1, v180 dst_sel:DWORD dst_unused:UNUSED_PAD src0_sel:DWORD src1_sel:WORD_1\n"
    "v_lshlrev_b32_sdwa v206, 1, v181 dst_sel:DWORD dst_unused:UNUSED_PAD src0_sel:DWORD src1_sel:WORD_1\n"
    "v_lshlrev_b32_sdwa v199, 1, v182 dst_sel:DWORD dst_unused:UNUSED_PAD src0_sel:DWORD src1_sel:WORD_1\n"
    "v_fma_mix_f32 v234, v184, v232, 0 op_sel_hi:[1,1,0]\n"
    "v_mfma_f32_16x16x32_bf16 v[172:175], a[132:135], v[148:151], v[172:175]\n"
    "s_waitcnt lgkmcnt(7)\n"
    "ds_read_u16 v194, v194\n"
    "ds_read_u16_d16_hi v202, v202\n"
    "v_mfma_f32_16x16x32_bf16 v[168:171], a[48:51], v[152:155], v[168:171]\n"
    "s_nop 0\n"
    "v_fma_mix_f32 v232, v184, v232, 0 op_sel:[1,1,0] op_sel_hi:[1,1,0]\n"
    "v_cvt_pk_bf16_f32 v184, v234, v232\n"
    "v_pk_fma_f16 v232, v188, v188, s48\n"
    "v_pk_mul_f16 v232, v188, v232\n"
    "v_exp_f16_e32 v234, v232\n"
    "s_waitcnt lgkmcnt(7)\n"
    "v_mfma_f32_16x16x32_bf16 v[176:179], a[80:83], v[156:159], 0\n"
    "ds_read_b128 v[32:35], v244 offset:16384\n"
    "ds_read_u16 v195, v195\n"
    "s_waitcnt lgkmcnt(8)\n"
    "v_mfma_f32_16x16x32_bf16 v[176:179], a[128:131], v[160:163], v[176:179]\n"
    "s_waitcnt lgkmcnt(7)\n"
    "ds_read_u16_d16_hi v203, v203\n"
    "v_or_b32_e32 v191, v191, v207\n"
    "v_mfma_f32_16x16x32_bf16 v[176:179], a[48:51], v[164:167], v[176:179]\n"
    "s_waitcnt lgkmcnt(7)\n"
    "v_lshlrev_b32_sdwa v207, 1, v183 dst_sel:DWORD dst_unused:UNUSED_PAD src0_sel:DWORD src1_sel:WORD_1\n"
    "v_exp_f16_sdwa v234, v232 dst_sel:WORD_1 dst_unused:UNUSED_PRESERVE src0_sel:WORD_1\n"
    "v_mfma_f32_16x16x32_bf16 v[172:175], v[48:51], v[152:155], v[172:175]\n"
    "s_barrier\n"
    "v_mfma_f32_16x16x32_bf16 v[180:183], a[84:87], v[156:159], 0\n"
    "s_waitcnt lgkmcnt(5)\n"
    "ds_read_b128 v[36:39], v244 offset:18432\n"
    "ds_write_b128 v253, v[64:67] offset:2048\n"
    "v_mfma_f32_16x16x32_bf16 v[180:183], a[132:135], v[160:163], v[180:183]\n"
    "v_pk_fma_f16 v234, v234, s49, s49\n"
    "v_or_b32_e64 v193, v193, v201\n"
    "v_mfma_f32_16x16x32_bf16 v[180:183], v[48:51], v[164:167], v[180:183]\n"
    "v_rcp_f16_e32 v232, v234\n"
    "v_mfma_f32_16x16x32_bf16 v[96:99], a[192:195], v[184:187], v[96:99]\n"
    "s_waitcnt lgkmcnt(4)\n"
    "ds_read_u16 v197, v197\n"
    "ds_write_b128 v253, v[68:71] offset:5120\n"
    "v_mfma_f32_16x16x32_bf16 v[100:103], a[196:199], v[184:187], v[100:103]\n"
    "v_rcp_f16_sdwa v232, v234 dst_sel:WORD_1 dst_unused:UNUSED_PRESERVE src0_sel:WORD_1\n"
    "v_mfma_f32_16x16x32_bf16 v[112:115], a[200:203], v[184:187], v[112:115]\n"
    "v_fma_mix_f32 v234, v188, v232, 0 op_sel_hi:[1,1,0]\n"
    "v_fma_mix_f32 v232, v188, v232, 0 op_sel:[1,1,0] op_sel_hi:[1,1,0]\n"
    "v_mfma_f32_16x16x32_bf16 v[116:119], a[204:207], v[184:187], v[116:119]\n"
    "s_waitcnt lgkmcnt(3)\n"
    "ds_read_u16_d16_hi v205, v205\n"
    "ds_write_b128 v253, v[88:91] offset:0\n"
    "v_mfma_f32_16x16x32_bf16 v[128:131], v[32:35], v[184:187], v[128:131]\n"
    "v_or_b32_e32 v194, v194, v202\n"
    "v_cvt_pk_bf16_f32 v188, v234, v232\n"
    "v_mfma_f32_16x16x32_bf16 v[132:135], v[36:39], v[184:187], v[132:135]\n"
    "v_pk_fma_f16 v232, v192, v192, s48\n"
    "v_or_b32_e64 v195, v195, v203\n"
    "v_mfma_f32_16x16x32_bf16 v[104:107], a[192:195], v[188:191], v[104:107]\n"
    "ds_read_u16 v198, v198\n"
    "ds_write_b128 v253, v[92:95] offset:3072\n"
    "v_mfma_f32_16x16x32_bf16 v[108:111], a[196:199], v[188:191], v[108:111]\n"
    "v_pk_mul_f16 v232, v192, v232\n"
    "v_lshlrev_b32_sdwa v184, 1, v168 dst_sel:DWORD dst_unused:UNUSED_PAD src0_sel:DWORD src1_sel:WORD_1\n"
    "v_mfma_f32_16x16x32_bf16 v[120:123], a[200:203], v[188:191], v[120:123]\n"
    "v_exp_f16_e64 v234, v232\n"
    "v_mfma_f32_16x16x32_bf16 v[124:127], a[204:207], v[188:191], v[124:127]\n"
    "ds_read_u16_d16_hi v206, v206\n"
    "ds_read_u16 v199, v199\n"
    "v_mfma_f32_16x16x32_bf16 v[136:139], v[32:35], v[188:191], v[136:139]\n"
    "ds_read_u16_d16_hi v207, v207\n"
    "ds_read_u16 v184, v184\n"
    "v_mfma_f32_16x16x32_bf16 v[140:143], v[36:39], v[188:191], v[140:143]\n"
    "s_waitcnt lgkmcnt(6)\n"
    "v_or_b32_e32 v197, v197, v205\n"
    "v_exp_f16_sdwa v234, v232 dst_sel:WORD_1 dst_unused:UNUSED_PRESERVE src0_sel:WORD_1\n"
    "v_lshlrev_b32_sdwa v200, 1, v169 dst_sel:DWORD dst_unused:UNUSED_PAD src0_sel:DWORD src1_sel:WORD_1\n"
    "v_pk_fma_f16 v234, v234, s49, s49\n"
    "v_lshlrev_b32_sdwa v185, 1, v170 dst_sel:DWORD dst_unused:UNUSED_PAD src0_sel:DWORD src1_sel:WORD_1\n"
    "v_lshlrev_b32_sdwa v201, 1, v171 dst_sel:DWORD dst_unused:UNUSED_PAD src0_sel:DWORD src1_sel:WORD_1\n"
    "v_mfma_f32_16x16x32_bf16 v[168:171], a[88:91], v[144:147], 0\n"
    "v_lshlrev_b32_sdwa v186, 1, v172 dst_sel:DWORD dst_unused:UNUSED_PAD src0_sel:DWORD src1_sel:WORD_1\n"
    "v_lshlrev_b32_sdwa v202, 1, v173 dst_sel:DWORD dst_unused:UNUSED_PAD src0_sel:DWORD src1_sel:WORD_1\n"
    "v_mfma_f32_16x16x32_bf16 v[168:171], a[136:139], v[148:151], v[168:171]\n"
    "ds_write_b128 v253, v[72:75] offset:1024\n"
    "v_lshlrev_b32_sdwa v187, 1, v174 dst_sel:DWORD dst_unused:UNUSED_PAD src0_sel:DWORD src1_sel:WORD_1\n"
    "v_lshlrev_b32_sdwa v203, 1, v175 dst_sel:DWORD dst_unused:UNUSED_PAD src0_sel:DWORD src1_sel:WORD_1\n"
    "v_rcp_f16_e64 v232, v234\n"
    "v_mfma_f32_16x16x32_bf16 v[168:171], a[52:55], v[152:155], v[168:171]\n"
    "v_rcp_f16_sdwa v232, v234 dst_sel:WORD_1 dst_unused:UNUSED_PRESERVE src0_sel:WORD_1\n"
    "v_mfma_f32_16x16x32_bf16 v[172:175], a[92:95], v[144:147], 0\n"
    "ds_read_u16_d16_hi v200, v200\n"
    "ds_write_b128 v253, v[76:79] offset:4096\n"
    "s_waitcnt lgkmcnt(6)\n"
    "v_or_b32_e32 v198, v198, v206\n"
    "v_lshlrev_b32_sdwa v188, 1, v176 dst_sel:DWORD dst_unused:UNUSED_PAD src0_sel:DWORD src1_sel:WORD_1\n"
    "v_lshlrev_b32_sdwa v204, 1, v177 dst_sel:DWORD dst_unused:UNUSED_PAD src0_sel:DWORD src1_sel:WORD_1\n"
    "v_lshlrev_b32_sdwa v189, 1, v178 dst_sel:DWORD dst_unused:UNUSED_PAD src0_sel:DWORD src1_sel:WORD_1\n"
    "v_lshlrev_b32_sdwa v205, 1, v179 dst_sel:DWORD dst_unused:UNUSED_PAD src0_sel:DWORD src1_sel:WORD_1\n"
    "v_lshlrev_b32_sdwa v190, 1, v180 dst_sel:DWORD dst_unused:UNUSED_PAD src0_sel:DWORD src1_sel:WORD_1\n"
    "v_lshlrev_b32_sdwa v206, 1, v181 dst_sel:DWORD dst_unused:UNUSED_PAD src0_sel:DWORD src1_sel:WORD_1\n"
    "v_lshlrev_b32_sdwa v191, 1, v182 dst_sel:DWORD dst_unused:UNUSED_PAD src0_sel:DWORD src1_sel:WORD_1\n"
    "v_fma_mix_f32 v234, v192, v232, 0 op_sel_hi:[1,1,0]\n"
    "v_mfma_f32_16x16x32_bf16 v[172:175], a[140:143], v[148:151], v[172:175]\n"
    "s_waitcnt lgkmcnt(4)\n"
    "ds_read_u16 v185, v185\n"
    "ds_read_u16_d16_hi v201, v201\n"
    "v_mfma_f32_16x16x32_bf16 v[172:175], v[56:59], v[152:155], v[172:175]\n"
    "s_nop 0\n"
    "ds_read_u16 v186, v186\n"
    "ds_read_b128 v[40:43], v244 offset:20480\n"
    "v_or_b32_e64 v199, v199, v207\n"
    "v_lshlrev_b32_sdwa v207, 1, v183 dst_sel:DWORD dst_unused:UNUSED_PAD src0_sel:DWORD src1_sel:WORD_1\n"
    "v_fma_mix_f32 v232, v192, v232, 0 op_sel:[1,1,0] op_sel_hi:[1,1,0]\n"
    "v_mfma_f32_16x16x32_bf16 v[176:179], a[88:91], v[156:159], 0\n"
    "ds_read_u16_d16_hi v202, v202\n"
    "v_cvt_pk_bf16_f32 v192, v234, v232\n"
    "v_mfma_f32_16x16x32_bf16 v[176:179], a[136:139], v[160:163], v[176:179]\n"
    "v_pk_fma_f16 v232, v196, v196, s48\n"
    "v_pk_mul_f16 v232, v196, v232\n"
    "v_mfma_f32_16x16x32_bf16 v[176:179], a[52:55], v[164:167], v[176:179]\n"
    "ds_read_u16 v187, v187\n"
    "v_exp_f16_e32 v234, v232\n"
    "v_mfma_f32_16x16x32_bf16 v[180:183], a[92:95], v[156:159], 0\n"
    "s_waitcnt lgkmcnt(6)\n"
    "ds_read_b128 v[44:47], v244 offset:22528\n"
    "v_exp_f16_sdwa v234, v232 dst_sel:WORD_1 dst_unused:UNUSED_PRESERVE src0_sel:WORD_1\n"
    "v_mfma_f32_16x16x32_bf16 v[180:183], a[140:143], v[160:163], v[180:183]\n"
    "ds_read_u16_d16_hi v203, v203\n"
    "v_pk_fma_f16 v234, v234, s49, s49\n"
    "v_mfma_f32_16x16x32_bf16 v[180:183], v[56:59], v[164:167], v[180:183]\n"
    "v_rcp_f16_e64 v232, v234\n"
    "v_mfma_f32_16x16x32_bf16 v[96:99], a[208:211], v[192:195], v[96:99]\n"
    "ds_read_u16 v188, v188\n"
    "v_rcp_f16_sdwa v232, v234 dst_sel:WORD_1 dst_unused:UNUSED_PRESERVE src0_sel:WORD_1\n"
    "v_mfma_f32_16x16x32_bf16 v[100:103], a[212:215], v[192:195], v[100:103]\n"
    "ds_read_u16_d16_hi v204, v204\n"
    "v_fma_mix_f32 v234, v196, v232, 0 op_sel_hi:[1,1,0]\n"
    "v_mfma_f32_16x16x32_bf16 v[112:115], a[216:219], v[192:195], v[112:115]\n"
    "v_fma_mix_f32 v232, v196, v232, 0 op_sel:[1,1,0] op_sel_hi:[1,1,0]\n"
    "v_or_b32_e32 v184, v184, v200\n"
    "v_mfma_f32_16x16x32_bf16 v[116:119], a[220:223], v[192:195], v[116:119]\n"
    "s_waitcnt lgkmcnt(8)\n"
    "ds_read_u16 v189, v189\n"
    "v_cvt_pk_bf16_f32 v196, v234, v232\n"
    "v_or_b32_e32 v185, v185, v201\n"
    "s_waitcnt lgkmcnt(7)\n"
    "v_mfma_f32_16x16x32_bf16 v[128:131], v[40:43], v[192:195], v[128:131]\n"
    "ds_read_u16_d16_hi v205, v205\n"
    "v_lshlrev_b32_sdwa v201, 1, v171 dst_sel:DWORD dst_unused:UNUSED_PAD src0_sel:DWORD src1_sel:WORD_1\n"
    "v_mfma_f32_16x16x32_bf16 v[104:107], a[208:211], v[196:199], v[104:107]\n"
    "v_cvt_pk_bf16_f32 v96, v96, v97\n"
    "v_cvt_pk_bf16_f32 v97, v98, v99\n"
    "v_mfma_f32_16x16x32_bf16 v[108:111], a[212:215], v[196:199], v[108:111]\n"
    "ds_read_u16 v190, v190\n"
    "v_cvt_pk_bf16_f32 v98, v100, v101\n"
    "v_mfma_f32_16x16x32_bf16 v[120:123], a[216:219], v[196:199], v[120:123]\n"
    "s_waitcnt lgkmcnt(8)\n"
    "v_or_b32_e64 v186, v186, v202\n"
    "v_lshlrev_b32_sdwa v202, 1, v173 dst_sel:DWORD dst_unused:UNUSED_PAD src0_sel:DWORD src1_sel:WORD_1\n"
    "v_mfma_f32_16x16x32_bf16 v[124:127], a[220:223], v[196:199], v[124:127]\n"
    "s_waitcnt lgkmcnt(6)\n"
    "ds_read_u16_d16_hi v206, v206\n"
    "v_cvt_pk_bf16_f32 v99, v102, v103\n"
    "v_mfma_f32_16x16x32_bf16 v[132:135], v[44:47], v[192:195], v[132:135]\n"
    "v_cvt_pk_f16_f32 v192, v168, v169\n"
    "v_lshlrev_b32_sdwa v193, 1, v170 dst_sel:DWORD dst_unused:UNUSED_PAD src0_sel:DWORD src1_sel:WORD_1\n"
    "v_mfma_f32_16x16x32_bf16 v[136:139], v[40:43], v[196:199], v[136:139]\n"
    "v_lshlrev_b32_sdwa v194, 1, v172 dst_sel:DWORD dst_unused:UNUSED_PAD src0_sel:DWORD src1_sel:WORD_1\n"
    "v_lshlrev_b32_sdwa v195, 1, v174 dst_sel:DWORD dst_unused:UNUSED_PAD src0_sel:DWORD src1_sel:WORD_1\n"
    "v_mfma_f32_16x16x32_bf16 v[140:143], v[44:47], v[196:199], v[140:143]\n"
    "s_waitcnt lgkmcnt(6)\n"
    "ds_write_b128 v242, v[96:99] offset:24640\n"
    "v_mfma_f32_16x16x32_bf16 v[168:171], a[96:99], v[144:147], 0\n"
    "s_nop 0\n"
    "v_cvt_pk_f16_f32 v196, v176, v177\n"
    "v_lshlrev_b32_sdwa v197, 1, v178 dst_sel:DWORD dst_unused:UNUSED_PAD src0_sel:DWORD src1_sel:WORD_1\n"
    "v_mfma_f32_16x16x32_bf16 v[168:171], a[144:147], v[148:151], v[168:171]\n"
    "v_lshlrev_b32_sdwa v198, 1, v180 dst_sel:DWORD dst_unused:UNUSED_PAD src0_sel:DWORD src1_sel:WORD_1\n"
    "v_lshlrev_b32_sdwa v199, 1, v182 dst_sel:DWORD dst_unused:UNUSED_PAD src0_sel:DWORD src1_sel:WORD_1\n"
    "v_mfma_f32_16x16x32_bf16 v[168:171], a[56:59], v[152:155], v[168:171]\n"
    "s_waitcnt lgkmcnt(5)\n"
    "ds_read_u16 v191, v191\n"
    "ds_read_u16_d16_hi v207, v207\n"
    "v_or_b32_e32 v187, v187, v203\n"
    "v_lshlrev_b32_sdwa v203, 1, v175 dst_sel:DWORD dst_unused:UNUSED_PAD src0_sel:DWORD src1_sel:WORD_1\n"
    "v_mfma_f32_16x16x32_bf16 v[172:175], a[100:103], v[144:147], 0\n"
    "ds_read_u16 v193, v193\n"
    "ds_read_u16_d16_hi v201, v201\n"
    "v_mfma_f32_16x16x32_bf16 v[172:175], a[148:151], v[148:151], v[172:175]\n"
    "v_or_b32_e64 v188, v188, v204\n"
    "v_cvt_pk_bf16_f32 v112, v112, v113\n"
    "v_mfma_f32_16x16x32_bf16 v[172:175], a[60:63], v[152:155], v[172:175]\n"
    "s_waitcnt lgkmcnt(7)\n"
    "ds_read_u16 v194, v194\n"
    "v_or_b32_e32 v189, v189, v205\n"
    "v_lshlrev_b32_sdwa v205, 1, v179 dst_sel:DWORD dst_unused:UNUSED_PAD src0_sel:DWORD src1_sel:WORD_1\n"
    "v_mfma_f32_16x16x32_bf16 v[176:179], a[96:99], v[156:159], 0\n"
    "v_cvt_pk_bf16_f32 v113, v114, v115\n"
    "v_cvt_pk_bf16_f32 v114, v116, v117\n"
    "v_mfma_f32_16x16x32_bf16 v[176:179], a[144:147], v[160:163], v[176:179]\n"
    "ds_read_u16_d16_hi v202, v202\n"
    "v_cvt_pk_bf16_f32 v115, v118, v119\n"
    "v_mfma_f32_16x16x32_bf16 v[176:179], a[56:59], v[164:167], v[176:179]\n"
    "s_waitcnt lgkmcnt(6)\n"
    "v_or_b32_e32 v190, v190, v206\n"
    "v_lshlrev_b32_sdwa v206, 1, v181 dst_sel:DWORD dst_unused:UNUSED_PAD src0_sel:DWORD src1_sel:WORD_1\n"
    "v_mfma_f32_16x16x32_bf16 v[96:99], a[0:3], v[184:187], 0\n"
    "v_cvt_pk_bf16_f32 v128, v128, v129\n"
    "v_cvt_pk_bf16_f32 v129, v130, v131\n"
    "v_mfma_f32_16x16x32_bf16 v[100:103], a[4:7], v[184:187], 0\n"
    "ds_write_b128 v242, v[112:115] offset:26688\n"
    "v_mfma_f32_16x16x32_bf16 v[112:115], a[8:11], v[184:187], 0\n"
    "v_cvt_pk_bf16_f32 v130, v132, v133\n"
    "v_cvt_pk_bf16_f32 v131, v134, v135\n"
    "v_cvt_pk_bf16_f32 v104, v104, v105\n"
    "v_cvt_pk_bf16_f32 v105, v106, v107\n"
    "v_cvt_pk_bf16_f32 v106, v108, v109\n"
    "v_cvt_pk_bf16_f32 v107, v110, v111\n"
    "ds_write_b128 v242, v[128:131] offset:28736\n"
    "s_waitcnt lgkmcnt(5)\n"
    "v_or_b32_e32 v191, v191, v207\n"
    "v_mfma_f32_16x16x32_bf16 v[116:119], a[12:15], v[184:187], 0\n"
    "v_lshlrev_b32_sdwa v207, 1, v183 dst_sel:DWORD dst_unused:UNUSED_PAD src0_sel:DWORD src1_sel:WORD_1\n"
    "v_cvt_pk_bf16_f32 v120, v120, v121\n"
    "v_mfma_f32_16x16x32_bf16 v[180:183], a[100:103], v[156:159], 0\n"
    "s_waitcnt lgkmcnt(4)\n"
    "ds_read_u16 v195, v195\n"
    "ds_write_b128 v242, v[104:107] offset:25664\n"
    "v_mfma_f32_16x16x32_bf16 v[180:183], a[148:151], v[160:163], v[180:183]\n"
    "v_or_b32_e32 v193, v193, v201\n"
    "v_cvt_pk_bf16_f32 v121, v122, v123\n"
    "v_mfma_f32_16x16x32_bf16 v[180:183], a[60:63], v[164:167], v[180:183]\n"
    "v_cvt_pk_bf16_f32 v122, v124, v125\n"
    "v_cvt_pk_bf16_f32 v123, v126, v127\n"
    "v_mfma_f32_16x16x32_bf16 v[128:131], v[0:3], v[184:187], 0\n"
    "s_waitcnt lgkmcnt(3)\n"
    "ds_read_u16_d16_hi v203, v203\n"
    "ds_write_b128 v242, v[120:123] offset:27712\n"
    "v_mfma_f32_16x16x32_bf16 v[132:135], v[4:7], v[184:187], 0\n"
    "v_or_b32_e32 v194, v194, v202\n"
    "v_cvt_pk_bf16_f32 v136, v136, v137\n"
    "v_mfma_f32_16x16x32_bf16 v[104:107], a[0:3], v[188:191], 0\n"
    "v_pk_fma_f16 v232, v192, v192, s48\n"
    "v_cvt_pk_bf16_f32 v137, v138, v139\n"
    "v_mfma_f32_16x16x32_bf16 v[108:111], a[4:7], v[188:191], 0\n"
    "ds_read_u16 v197, v197\n"
    "ds_read_u16_d16_hi v205, v205\n"
    "v_mfma_f32_16x16x32_bf16 v[120:123], a[8:11], v[188:191], 0\n"
    "ds_read_u16 v198, v198\n"
    "ds_read_u16_d16_hi v206, v206\n"
    "v_mfma_f32_16x16x32_bf16 v[124:127], a[12:15], v[188:191], 0\n"
    "v_cvt_pk_bf16_f32 v138, v140, v141\n"
    "v_cvt_pk_bf16_f32 v139, v142, v143\n"
    "v_pk_mul_f16 v232, v192, v232\n"
    "v_cvt_pk_f16_f32 v184, v168, v169\n"
    "v_exp_f16_e64 v234, v232\n"
    "v_mfma_f32_16x16x32_bf16 v[140:143], v[4:7], v[188:191], 0\n"
    "v_exp_f16_sdwa v234, v232 dst_sel:WORD_1 dst_unused:UNUSED_PRESERVE src0_sel:WORD_1\n"
    "ds_write_b128 v242, v[136:139] offset:29760\n"
    "v_mfma_f32_16x16x32_bf16 v[136:139], v[0:3], v[188:191], 0\n"
    "s_waitcnt lgkmcnt(5)\n"
    "v_or_b32_e32 v195, v195, v203\n"
    "v_pk_fma_f16 v234, v234, s49, s49\n"
    "v_lshlrev_b32_sdwa v185, 1, v170 dst_sel:DWORD dst_unused:UNUSED_PAD src0_sel:DWORD src1_sel:WORD_1\n"
    "v_lshlrev_b32_sdwa v201, 1, v171 dst_sel:DWORD dst_unused:UNUSED_PAD src0_sel:DWORD src1_sel:WORD_1\n"
    "v_mfma_f32_16x16x32_bf16 v[168:171], a[104:107], v[144:147], 0\n"
    "ds_read_u16 v199, v199\n"
    "ds_read_u16_d16_hi v207, v207\n"
    "v_mfma_f32_16x16x32_bf16 v[168:171], a[152:155], v[148:151], v[168:171]\n"
    "ds_read_u16 v185, v185\n"
    "v_lshlrev_b32_sdwa v186, 1, v172 dst_sel:DWORD dst_unused:UNUSED_PAD src0_sel:DWORD src1_sel:WORD_1\n"
    "v_lshlrev_b32_sdwa v202, 1, v173 dst_sel:DWORD dst_unused:UNUSED_PAD src0_sel:DWORD src1_sel:WORD_1\n"
    "v_lshlrev_b32_sdwa v187, 1, v174 dst_sel:DWORD dst_unused:UNUSED_PAD src0_sel:DWORD src1_sel:WORD_1\n"
    "v_lshlrev_b32_sdwa v203, 1, v175 dst_sel:DWORD dst_unused:UNUSED_PAD src0_sel:DWORD src1_sel:WORD_1\n"
    "v_rcp_f16_e64 v232, v234\n"
    "v_mfma_f32_16x16x32_bf16 v[168:171], a[64:67], v[152:155], v[168:171]\n"
    "ds_read_u16_d16_hi v201, v201\n"
    "s_waitcnt lgkmcnt(7)\n"
    "v_or_b32_e32 v197, v197, v205\n"
    "v_rcp_f16_sdwa v232, v234 dst_sel:WORD_1 dst_unused:UNUSED_PRESERVE src0_sel:WORD_1\n"
    "v_mfma_f32_16x16x32_bf16 v[172:175], a[108:111], v[144:147], 0\n"
    "ds_read_u16 v186, v186\n"
    "v_cvt_pk_f16_f32 v188, v176, v177\n"
    "v_mfma_f32_16x16x32_bf16 v[172:175], a[156:159], v[148:151], v[172:175]\n"
    "ds_read_u16_d16_hi v202, v202\n"
    "v_lshlrev_b32_sdwa v189, 1, v178 dst_sel:DWORD dst_unused:UNUSED_PAD src0_sel:DWORD src1_sel:WORD_1\n"
    "v_lshlrev_b32_sdwa v205, 1, v179 dst_sel:DWORD dst_unused:UNUSED_PAD src0_sel:DWORD src1_sel:WORD_1\n"
    "v_lshlrev_b32_sdwa v190, 1, v180 dst_sel:DWORD dst_unused:UNUSED_PAD src0_sel:DWORD src1_sel:WORD_1\n"
    "s_waitcnt lgkmcnt(6)\n"
    "v_or_b32_e32 v198, v198, v206\n"
    "v_lshlrev_b32_sdwa v206, 1, v181 dst_sel:DWORD dst_unused:UNUSED_PAD src0_sel:DWORD src1_sel:WORD_1\n"
    "v_lshlrev_b32_sdwa v191, 1, v182 dst_sel:DWORD dst_unused:UNUSED_PAD src0_sel:DWORD src1_sel:WORD_1\n"
    "v_fma_mix_f32 v234, v192, v232, 0 op_sel_hi:[1,1,0]\n"
    "v_mfma_f32_16x16x32_bf16 v[172:175], a[68:71], v[152:155], v[172:175]\n"
    "s_barrier\n"
    "v_fma_mix_f32 v232, v192, v232, 0 op_sel:[1,1,0] op_sel_hi:[1,1,0]\n"
    "v_mfma_f32_16x16x32_bf16 v[176:179], a[104:107], v[156:159], 0\n"
    "s_waitcnt lgkmcnt(4)\n"
    "ds_read_u16 v187, v187\n"
    "ds_read_u16_d16_hi v203, v203\n"
    "v_or_b32_e64 v199, v199, v207\n"
    "v_lshlrev_b32_sdwa v207, 1, v183 dst_sel:DWORD dst_unused:UNUSED_PAD src0_sel:DWORD src1_sel:WORD_1\n"
    "v_cvt_pk_bf16_f32 v192, v234, v232\n"
    "v_mfma_f32_16x16x32_bf16 v[176:179], a[152:155], v[160:163], v[176:179]\n"
    "ds_read_u16 v189, v189\n"
    "ds_read_u16_d16_hi v205, v205\n"
    "v_mfma_f32_16x16x32_bf16 v[176:179], a[64:67], v[164:167], v[176:179]\n"
    "s_nop 0\n"
    "v_pk_fma_f16 v232, v196, v196, s48\n"
    "v_lshlrev_b32_sdwa v200, 1, v169 dst_sel:DWORD dst_unused:UNUSED_PAD src0_sel:DWORD src1_sel:WORD_1\n"
    "v_mfma_f32_16x16x32_bf16 v[180:183], a[108:111], v[156:159], 0\n"
    "s_waitcnt lgkmcnt(6)\n"
    "ds_read_u16 v190, v190\n"
    "v_pk_mul_f16 v232, v196, v232\n"
    "v_mfma_f32_16x16x32_bf16 v[180:183], a[156:159], v[160:163], v[180:183]\n"
    "ds_read_u16_d16_hi v206, v206\n"
    "v_exp_f16_e32 v234, v232\n"
    "v_mfma_f32_16x16x32_bf16 v[180:183], a[68:71], v[164:167], v[180:183]\n"
    "v_exp_f16_sdwa v234, v232 dst_sel:WORD_1 dst_unused:UNUSED_PRESERVE src0_sel:WORD_1\n"
    "v_mfma_f32_16x16x32_bf16 v[96:99], a[16:19], v[192:195], v[96:99]\n"
    "s_waitcnt lgkmcnt(6)\n"
    "ds_read_u16 v191, v191\n"
    "v_pk_fma_f16 v234, v234, s49, s49\n"
    "v_mfma_f32_16x16x32_bf16 v[100:103], a[20:23], v[192:195], v[100:103]\n"
    "v_rcp_f16_e64 v232, v234\n"
    "v_mfma_f32_16x16x32_bf16 v[112:115], a[24:27], v[192:195], v[112:115]\n"
    "ds_read_u16_d16_hi v207, v207\n"
    "v_rcp_f16_sdwa v232, v234 dst_sel:WORD_1 dst_unused:UNUSED_PRESERVE src0_sel:WORD_1\n"
    "v_mfma_f32_16x16x32_bf16 v[116:119], a[28:31], v[192:195], v[116:119]\n"
    "v_or_b32_e64 v185, v185, v201\n"
    "v_fma_mix_f32 v234, v196, v232, 0 op_sel_hi:[1,1,0]\n"
    "v_mfma_f32_16x16x32_bf16 v[128:131], v[8:11], v[192:195], v[128:131]\n"
    "v_fma_mix_f32 v232, v196, v232, 0 op_sel:[1,1,0] op_sel_hi:[1,1,0]\n"
    "v_or_b32_e32 v186, v186, v202\n"
    "v_mfma_f32_16x16x32_bf16 v[132:135], v[12:15], v[192:195], v[132:135]\n"
    "s_waitcnt lgkmcnt(6)\n"
    "v_cvt_pk_bf16_f32 v196, v234, v232\n"
    "v_pk_fma_f16 v232, v184, v184, s48\n"
    "v_or_b32_e64 v187, v187, v203\n"
    "v_mfma_f32_16x16x32_bf16 v[104:107], a[16:19], v[196:199], v[104:107]\n"
    "ds_read_u16_d16_hi v200, v200\n"
    "v_pk_mul_f16 v232, v184, v232\n"
    "v_mfma_f32_16x16x32_bf16 v[108:111], a[20:23], v[196:199], v[108:111]\n"
    "v_exp_f16_e32 v234, v232\n"
    "v_mfma_f32_16x16x32_bf16 v[120:123], a[24:27], v[196:199], v[120:123]\n"
    "s_waitcnt lgkmcnt(5)\n"
    "v_or_b32_e32 v189, v189, v205\n"
    "v_exp_f16_sdwa v234, v232 dst_sel:WORD_1 dst_unused:UNUSED_PRESERVE src0_sel:WORD_1\n"
    "v_mfma_f32_16x16x32_bf16 v[124:127], a[28:31], v[196:199], v[124:127]\n"
    "s_waitcnt vmcnt(7)\n"
    "v_pk_fma_f16 v234, v234, s49, s49\n"
    "v_lshlrev_b32_sdwa v192, 1, v168 dst_sel:DWORD dst_unused:UNUSED_PAD src0_sel:DWORD src1_sel:WORD_1\n"
    "v_mfma_f32_16x16x32_bf16 v[136:139], v[8:11], v[196:199], v[136:139]\n"
    "s_nop 0\n"
    "ds_read_u16 v192, v192\n"
    "v_lshlrev_b32_sdwa v193, 1, v170 dst_sel:DWORD dst_unused:UNUSED_PAD src0_sel:DWORD src1_sel:WORD_1\n"
    "v_mfma_f32_16x16x32_bf16 v[140:143], v[12:15], v[196:199], v[140:143]\n"
    "s_waitcnt vmcnt(6) lgkmcnt(4)\n"
    "ds_read_u16 v193, v193\n"
    "v_lshlrev_b32_sdwa v201, 1, v171 dst_sel:DWORD dst_unused:UNUSED_PAD src0_sel:DWORD src1_sel:WORD_1\n"
    "v_smfmac_f32_32x32x32_bf16 v[112:127], a[248:251], a[232:239], v241\n"
    "ds_read_u16_d16_hi v201, v201\n"
    "ds_read_b128 a[232:235], v243 offset:24640\n"
    "v_lshlrev_b32_sdwa v194, 1, v172 dst_sel:DWORD dst_unused:UNUSED_PAD src0_sel:DWORD src1_sel:WORD_1\n"
    "v_lshlrev_b32_sdwa v202, 1, v173 dst_sel:DWORD dst_unused:UNUSED_PAD src0_sel:DWORD src1_sel:WORD_1\n"
    "v_lshlrev_b32_sdwa v195, 1, v174 dst_sel:DWORD dst_unused:UNUSED_PAD src0_sel:DWORD src1_sel:WORD_1\n"
    "v_smfmac_f32_32x32x32_bf16 v[96:111], a[248:251], a[224:231], v241\n"
    "ds_read_u16 v194, v194\n"
    "ds_read_u16_d16_hi v202, v202\n"
    "v_lshlrev_b32_sdwa v203, 1, v175 dst_sel:DWORD dst_unused:UNUSED_PAD src0_sel:DWORD src1_sel:WORD_1\n"
    "v_rcp_f16_e32 v232, v234\n"
    "v_smfmac_f32_32x32x32_bf16 v[128:143], a[248:251], v[208:215], v241\n"
    "s_waitcnt lgkmcnt(6)\n"
    "ds_read_b128 a[236:239], v243 offset:25664\n"
    "v_or_b32_e64 v190, v190, v206\n"
    "v_rcp_f16_sdwa v232, v234 dst_sel:WORD_1 dst_unused:UNUSED_PRESERVE src0_sel:WORD_1\n"
    "v_lshlrev_b32_sdwa v196, 1, v176 dst_sel:DWORD dst_unused:UNUSED_PAD src0_sel:DWORD src1_sel:WORD_1\n"
    "v_mfma_f32_16x16x32_bf16 v[168:171], a[112:115], v[144:147], 0\n"
    "ds_read_u16 v195, v195\n"
    "v_lshlrev_b32_sdwa v204, 1, v177 dst_sel:DWORD dst_unused:UNUSED_PAD src0_sel:DWORD src1_sel:WORD_1\n"
    "v_mfma_f32_16x16x32_bf16 v[168:171], a[160:163], v[148:151], v[168:171]\n"
    "ds_read_u16_d16_hi v203, v203\n"
    "v_lshlrev_b32_sdwa v197, 1, v178 dst_sel:DWORD dst_unused:UNUSED_PAD src0_sel:DWORD src1_sel:WORD_1\n"
    "v_mfma_f32_16x16x32_bf16 v[168:171], a[72:75], v[152:155], v[168:171]\n"
    "v_lshlrev_b32_sdwa v205, 1, v179 dst_sel:DWORD dst_unused:UNUSED_PAD src0_sel:DWORD src1_sel:WORD_1\n"
    "v_lshlrev_b32_sdwa v198, 1, v180 dst_sel:DWORD dst_unused:UNUSED_PAD src0_sel:DWORD src1_sel:WORD_1\n"
    "v_mfma_f32_16x16x32_bf16 v[172:175], a[116:119], v[144:147], 0\n"
    "v_lshlrev_b32_sdwa v206, 1, v181 dst_sel:DWORD dst_unused:UNUSED_PAD src0_sel:DWORD src1_sel:WORD_1\n"
    "v_lshlrev_b32_sdwa v199, 1, v182 dst_sel:DWORD dst_unused:UNUSED_PAD src0_sel:DWORD src1_sel:WORD_1\n"
    "v_fma_mix_f32 v234, v184, v232, 0 op_sel_hi:[1,1,0]\n"
    "v_mfma_f32_16x16x32_bf16 v[172:175], a[164:167], v[148:151], v[172:175]\n"
    "s_waitcnt lgkmcnt(7)\n"
    "ds_read_u16 v196, v196\n"
    "ds_read_u16_d16_hi v204, v204\n"
    "v_mfma_f32_16x16x32_bf16 v[172:175], v[52:55], v[152:155], v[172:175]\n"
    "v_or_b32_e32 v191, v191, v207\n"
    "v_lshlrev_b32_sdwa v207, 1, v183 dst_sel:DWORD dst_unused:UNUSED_PAD src0_sel:DWORD src1_sel:WORD_1\n"
    "v_fma_mix_f32 v232, v184, v232, 0 op_sel:[1,1,0] op_sel_hi:[1,1,0]\n"
    "v_mfma_f32_16x16x32_bf16 v[176:179], a[112:115], v[156:159], 0\n"
    "ds_read_u16 v197, v197\n"
    "v_cvt_pk_bf16_f32 v184, v234, v232\n"
    "v_mfma_f32_16x16x32_bf16 v[176:179], a[160:163], v[160:163], v[176:179]\n"
    "ds_read_u16_d16_hi v205, v205\n"
    "v_pk_fma_f16 v232, v188, v188, s48\n"
    "v_mfma_f32_16x16x32_bf16 v[176:179], a[72:75], v[164:167], v[176:179]\n"
    "s_waitcnt lgkmcnt(8)\n"
    "v_pk_mul_f16 v232, v188, v232\n"
    "v_or_b32_e32 v192, v192, v200\n"
    "v_mfma_f32_16x16x32_bf16 v[180:183], a[116:119], v[156:159], 0\n"
    "ds_read_u16 v198, v198\n"
    "v_exp_f16_e32 v234, v232\n"
    "v_mfma_f32_16x16x32_bf16 v[180:183], a[164:167], v[160:163], v[180:183]\n"
    "s_waitcnt lgkmcnt(7)\n"
    "v_exp_f16_sdwa v234, v232 dst_sel:WORD_1 dst_unused:UNUSED_PRESERVE src0_sel:WORD_1\n"
    "v_smfmac_f32_32x32x32_bf16 v[96:111], a[248:251], a[232:239], v241\n"
    "ds_read_u16_d16_hi v206, v206\n"
    "v_pk_fma_f16 v234, v234, s49, s49\n"
    "v_or_b32_e64 v193, v193, v201\n"
    "v_rcp_f16_e32 v232, v234\n"
    "v_mfma_f32_16x16x32_bf16 v[180:183], v[52:55], v[164:167], v[180:183]\n"
    "ds_read_u16 v199, v199\n"
    "v_rcp_f16_sdwa v232, v234 dst_sel:WORD_1 dst_unused:UNUSED_PRESERVE src0_sel:WORD_1\n"
    "v_mfma_f32_16x16x32_bf16 v[112:115], a[40:43], v[184:187], v[112:115]\n"
    "s_waitcnt lgkmcnt(7)\n"
    "ds_read_u16_d16_hi v207, v207\n"
    "v_fma_mix_f32 v234, v188, v232, 0 op_sel_hi:[1,1,0]\n"
    "v_mfma_f32_16x16x32_bf16 v[96:99], a[32:35], v[184:187], v[96:99]\n"
    "v_fma_mix_f32 v232, v188, v232, 0 op_sel:[1,1,0] op_sel_hi:[1,1,0]\n"
    "v_lshlrev_b32_sdwa v201, 1, v171 dst_sel:DWORD dst_unused:UNUSED_PAD src0_sel:DWORD src1_sel:WORD_1\n"
    "v_mfma_f32_16x16x32_bf16 v[100:103], a[36:39], v[184:187], v[100:103]\n"
    "ds_read_b128 a[232:235], v243 offset:26688\n"
    "ds_read_b128 a[236:239], v243 offset:27712\n"
    "v_mfma_f32_16x16x32_bf16 v[116:119], a[44:47], v[184:187], v[116:119]\n"
    "s_waitcnt lgkmcnt(8)\n"
    "v_cvt_pk_bf16_f32 v188, v234, v232\n"
    "v_or_b32_e32 v194, v194, v202\n"
    "v_mfma_f32_16x16x32_bf16 v[128:131], v[16:19], v[184:187], v[128:131]\n"
    "ds_read_u16_d16_hi v201, v201\n"
    "v_lshlrev_b32_sdwa v202, 1, v173 dst_sel:DWORD dst_unused:UNUSED_PAD src0_sel:DWORD src1_sel:WORD_1\n"
    "v_mfma_f32_16x16x32_bf16 v[132:135], v[20:23], v[184:187], v[132:135]\n"
    "ds_read_u16_d16_hi v202, v202\n"
    "v_cvt_pk_f16_f32 v184, v168, v169\n"
    "v_mfma_f32_16x16x32_bf16 v[104:107], a[32:35], v[188:191], v[104:107]\n"
    "v_lshlrev_b32_sdwa v185, 1, v170 dst_sel:DWORD dst_unused:UNUSED_PAD src0_sel:DWORD src1_sel:WORD_1\n"
    "v_lshlrev_b32_sdwa v186, 1, v172 dst_sel:DWORD dst_unused:UNUSED_PAD src0_sel:DWORD src1_sel:WORD_1\n"
    "v_mfma_f32_16x16x32_bf16 v[108:111], a[36:39], v[188:191], v[108:111]\n"
    "s_waitcnt lgkmcnt(8)\n"
    "ds_read_u16 v185, v185\n"
    "v_lshlrev_b32_sdwa v187, 1, v174 dst_sel:DWORD dst_unused:UNUSED_PAD src0_sel:DWORD src1_sel:WORD_1\n"
    "v_mfma_f32_16x16x32_bf16 v[120:123], a[40:43], v[188:191], v[120:123]\n"
    "v_or_b32_e32 v195, v195, v203\n"
    "v_lshlrev_b32_sdwa v203, 1, v175 dst_sel:DWORD dst_unused:UNUSED_PAD src0_sel:DWORD src1_sel:WORD_1\n"
    "v_mfma_f32_16x16x32_bf16 v[124:127], a[44:47], v[188:191], v[124:127]\n"
    "ds_read_u16 v186, v186\n"
    "v_or_b32_e64 v196, v196, v204\n"
    "v_mfma_f32_16x16x32_bf16 v[136:139], v[16:19], v[188:191], v[136:139]\n"
    "s_waitcnt lgkmcnt(8)\n"
    "v_or_b32_e32 v197, v197, v205\n"
    "v_lshlrev_b32_sdwa v205, 1, v179 dst_sel:DWORD dst_unused:UNUSED_PAD src0_sel:DWORD src1_sel:WORD_1\n"
    "v_mfma_f32_16x16x32_bf16 v[140:143], v[20:23], v[188:191], v[140:143]\n"
    "ds_read_u16 v187, v187\n"
    "v_cvt_pk_f16_f32 v188, v176, v177\n"
    "v_mfma_f32_16x16x32_bf16 v[168:171], a[120:123], v[144:147], 0\n"
    "ds_read_u16_d16_hi v203, v203\n"
    "v_lshlrev_b32_sdwa v189, 1, v178 dst_sel:DWORD dst_unused:UNUSED_PAD src0_sel:DWORD src1_sel:WORD_1\n"
    "v_mfma_f32_16x16x32_bf16 v[168:171], a[168:171], v[148:151], v[168:171]\n"
    "v_lshlrev_b32_sdwa v190, 1, v180 dst_sel:DWORD dst_unused:UNUSED_PAD src0_sel:DWORD src1_sel:WORD_1\n"
    "v_lshlrev_b32_sdwa v191, 1, v182 dst_sel:DWORD dst_unused:UNUSED_PAD src0_sel:DWORD src1_sel:WORD_1\n"
    "v_mfma_f32_16x16x32_bf16 v[168:171], a[76:79], v[152:155], v[168:171]\n"
    "s_waitcnt lgkmcnt(7)\n"
    "ds_read_u16 v189, v189\n"
    "v_or_b32_e32 v198, v198, v206\n"
    "v_mfma_f32_16x16x32_bf16 v[172:175], a[124:127], v[144:147], 0\n"
    "s_waitcnt lgkmcnt(7)\n"
    "v_lshlrev_b32_sdwa v206, 1, v181 dst_sel:DWORD dst_unused:UNUSED_PAD src0_sel:DWORD src1_sel:WORD_1\n"
    "v_or_b32_e32 v199, v199, v207\n"
    "v_smfmac_f32_32x32x32_bf16 v[112:127], a[248:251], a[232:239], v241\n"
    "ds_read_b128 a[232:235], v243 offset:28736\n"
    "ds_read_u16_d16_hi v205, v205\n"
    "v_lshlrev_b32_sdwa v207, 1, v183 dst_sel:DWORD dst_unused:UNUSED_PAD src0_sel:DWORD src1_sel:WORD_1\n"
    "v_pk_fma_f16 v232, v184, v184, s48\n"
    "v_pk_mul_f16 v232, v184, v232\n"
    "v_mfma_f32_16x16x32_bf16 v[172:175], a[172:175], v[148:151], v[172:175]\n"
    "v_exp_f16_e64 v234, v232\n"
    "v_mfma_f32_16x16x32_bf16 v[172:175], v[60:63], v[152:155], v[172:175]\n"
    "ds_read_b128 a[236:239], v243 offset:29760\n"
    "ds_read_u16 v190, v190\n"
    "v_mfma_f32_16x16x32_bf16 v[176:179], a[120:123], v[156:159], 0\n"
    "s_nop 0\n"
    "v_exp_f16_sdwa v234, v232 dst_sel:WORD_1 dst_unused:UNUSED_PRESERVE src0_sel:WORD_1\n"
    "v_mfma_f32_16x16x32_bf16 v[176:179], a[168:171], v[160:163], v[176:179]\n"
    "s_waitcnt lgkmcnt(8)\n"
    "ds_read_b128 v[144:147], v242 offset:0\n"
    "v_or_b32_e64 v185, v185, v201\n"
    "v_mfma_f32_16x16x32_bf16 v[176:179], a[76:79], v[164:167], v[176:179]\n"
    "ds_read_u16_d16_hi v206, v206\n"
    "v_pk_fma_f16 v234, v234, s49, s49\n"
    "v_mfma_f32_16x16x32_bf16 v[180:183], a[124:127], v[156:159], 0\n"
    "s_waitcnt lgkmcnt(9)\n"
    "v_or_b32_e32 v186, v186, v202\n"
    "v_lshlrev_b32_sdwa v201, 1, v171 dst_sel:DWORD dst_unused:UNUSED_PAD src0_sel:DWORD src1_sel:WORD_1\n"
    "v_mfma_f32_16x16x32_bf16 v[180:183], a[172:175], v[160:163], v[180:183]\n"
    "ds_read_b128 v[148:151], v242 offset:1024\n"
    "v_lshlrev_b32_sdwa v202, 1, v173 dst_sel:DWORD dst_unused:UNUSED_PAD src0_sel:DWORD src1_sel:WORD_1\n"
    "v_mfma_f32_16x16x32_bf16 v[180:183], v[60:63], v[164:167], v[180:183]\n"
    "ds_read_u16 v191, v191\n"
    "v_rcp_f16_e64 v232, v234\n"
    "v_mfma_f32_16x16x32_bf16 v[96:99], a[176:179], v[192:195], v[96:99]\n"
    "s_waitcnt lgkmcnt(9)\n"
    "v_or_b32_e32 v187, v187, v203\n"
    "v_lshlrev_b32_sdwa v203, 1, v175 dst_sel:DWORD dst_unused:UNUSED_PAD src0_sel:DWORD src1_sel:WORD_1\n"
    "v_mfma_f32_16x16x32_bf16 v[100:103], a[180:183], v[192:195], v[100:103]\n"
    "ds_read_b128 v[152:155], v242 offset:2048\n"
    "ds_read_u16_d16_hi v207, v207\n"
    "v_mfma_f32_16x16x32_bf16 v[112:115], a[184:187], v[192:195], v[112:115]\n"
    "v_rcp_f16_sdwa v232, v234 dst_sel:WORD_1 dst_unused:UNUSED_PRESERVE src0_sel:WORD_1\n"
    "v_mfma_f32_16x16x32_bf16 v[116:119], a[188:191], v[192:195], v[116:119]\n"
    "ds_read_b128 v[156:159], v242 offset:3072\n"
    "v_fma_mix_f32 v234, v184, v232, 0 op_sel_hi:[1,1,0]\n"
    "v_mfma_f32_16x16x32_bf16 v[128:131], v[24:27], v[192:195], v[128:131]\n"
    "ds_read_b128 v[160:163], v242 offset:4096\n"
    "v_fma_mix_f32 v232, v184, v232, 0 op_sel:[1,1,0] op_sel_hi:[1,1,0]\n"
    "v_mfma_f32_16x16x32_bf16 v[132:135], v[28:31], v[192:195], v[132:135]\n"
    "s_waitcnt lgkmcnt(9)\n"
    "ds_read_b128 v[164:167], v242 offset:5120\n"
    "v_or_b32_e32 v189, v189, v205\n"
    "v_mfma_f32_16x16x32_bf16 v[104:107], a[176:179], v[196:199], v[104:107]\n"
    "ds_read_u16_d16_hi v201, v201\n"
    "v_cvt_pk_f16_f32 v192, v168, v169\n"
    "v_mfma_f32_16x16x32_bf16 v[108:111], a[180:183], v[196:199], v[108:111]\n"
    "s_nop 0\n"
    "v_lshlrev_b32_sdwa v193, 1, v170 dst_sel:DWORD dst_unused:UNUSED_PAD src0_sel:DWORD src1_sel:WORD_1\n"
    "v_lshlrev_b32_sdwa v194, 1, v172 dst_sel:DWORD dst_unused:UNUSED_PAD src0_sel:DWORD src1_sel:WORD_1\n"
    "v_mfma_f32_16x16x32_bf16 v[120:123], a[184:187], v[196:199], v[120:123]\n"
    "s_waitcnt lgkmcnt(9)\n"
    "ds_read_u16 v193, v193\n"
    "v_lshlrev_b32_sdwa v195, 1, v174 dst_sel:DWORD dst_unused:UNUSED_PAD src0_sel:DWORD src1_sel:WORD_1\n"
    "v_mfma_f32_16x16x32_bf16 v[124:127], a[188:191], v[196:199], v[124:127]\n"
    "v_lshlrev_b32_sdwa v205, 1, v179 dst_sel:DWORD dst_unused:UNUSED_PAD src0_sel:DWORD src1_sel:WORD_1\n"
    "v_cvt_pk_bf16_f32 v184, v234, v232\n"
    "v_mfma_f32_16x16x32_bf16 v[136:139], v[24:27], v[196:199], v[136:139]\n"
    "s_waitcnt lgkmcnt(8)\n"
    "ds_read_u16 v194, v194\n"
    "v_or_b32_e32 v190, v190, v206\n"
    "v_mfma_f32_16x16x32_bf16 v[140:143], v[28:31], v[196:199], v[140:143]\n"
    "v_cvt_pk_f16_f32 v196, v176, v177\n"
    "v_lshlrev_b32_sdwa v197, 1, v178 dst_sel:DWORD dst_unused:UNUSED_PAD src0_sel:DWORD src1_sel:WORD_1\n"
    "v_mfma_f32_16x16x32_bf16 v[168:171], a[80:83], v[144:147], 0\n"
    "s_nop 0\n"
    "ds_read_u16_d16_hi v202, v202\n"
    "v_lshlrev_b32_sdwa v198, 1, v180 dst_sel:DWORD dst_unused:UNUSED_PAD src0_sel:DWORD src1_sel:WORD_1\n"
    "v_mfma_f32_16x16x32_bf16 v[168:171], a[128:131], v[148:151], v[168:171]\n"
    "s_waitcnt lgkmcnt(8)\n"
    "ds_read_u16 v195, v195\n"
    "v_lshlrev_b32_sdwa v206, 1, v181 dst_sel:DWORD dst_unused:UNUSED_PAD src0_sel:DWORD src1_sel:WORD_1\n"
    "v_smfmac_f32_32x32x32_bf16 v[128:143], a[248:251], a[232:239], v241\n"
    "v_lshlrev_b32_sdwa v199, 1, v182 dst_sel:DWORD dst_unused:UNUSED_PAD src0_sel:DWORD src1_sel:WORD_1\n"
    "v_pk_fma_f16 v232, v188, v188, s48\n"
    "v_pk_mul_f16 v232, v188, v232\n"
    "v_exp_f16_e32 v234, v232\n"
    "v_mfma_f32_16x16x32_bf16 v[168:171], a[48:51], v[152:155], v[168:171]\n"
    "s_waitcnt lgkmcnt(6)\n"
    "ds_read_u16_d16_hi v203, v203\n"
    "v_or_b32_e64 v191, v191, v207\n"
    "v_mfma_f32_16x16x32_bf16 v[172:175], a[84:87], v[144:147], 0\n"
    "ds_read_u16 v197, v197\n"
    "v_lshlrev_b32_sdwa v207, 1, v183 dst_sel:DWORD dst_unused:UNUSED_PAD src0_sel:DWORD src1_sel:WORD_1\n"
    "v_mfma_f32_16x16x32_bf16 v[172:175], a[132:135], v[148:151], v[172:175]\n"
    "v_exp_f16_sdwa v234, v232 dst_sel:WORD_1 dst_unused:UNUSED_PRESERVE src0_sel:WORD_1\n"
    "v_mfma_f32_16x16x32_bf16 v[172:175], v[48:51], v[152:155], v[172:175]\n"
    "ds_read_u16_d16_hi v205, v205\n"
    "v_pk_fma_f16 v234, v234, s49, s49\n"
    "v_mfma_f32_16x16x32_bf16 v[176:179], a[80:83], v[156:159], 0\n"
    "ds_read_u16 v198, v198\n"
    "v_rcp_f16_e64 v232, v234\n"
    "v_mfma_f32_16x16x32_bf16 v[176:179], a[128:131], v[160:163], v[176:179]\n"
    "s_waitcnt lgkmcnt(8)\n"
    "v_rcp_f16_sdwa v232, v234 dst_sel:WORD_1 dst_unused:UNUSED_PRESERVE src0_sel:WORD_1\n"
    "v_mfma_f32_16x16x32_bf16 v[176:179], a[48:51], v[164:167], v[176:179]\n"
    "s_waitcnt lgkmcnt(7)\n"
    "ds_read_u16_d16_hi v206, v206\n"
    "v_or_b32_e64 v193, v193, v201\n"
    "v_mfma_f32_16x16x32_bf16 v[180:183], a[84:87], v[156:159], 0\n"
    "v_fma_mix_f32 v234, v188, v232, 0 op_sel_hi:[1,1,0]\n"
    "v_fma_mix_f32 v232, v188, v232, 0 op_sel:[1,1,0] op_sel_hi:[1,1,0]\n"
    "v_mfma_f32_16x16x32_bf16 v[180:183], a[132:135], v[160:163], v[180:183]\n"
    "ds_read_u16 v199, v199\n"
    "v_cvt_pk_bf16_f32 v188, v234, v232\n"
    "v_mfma_f32_16x16x32_bf16 v[180:183], v[48:51], v[164:167], v[180:183]\n"
    "s_waitcnt lgkmcnt(7)\n"
    "ds_read_u16_d16_hi v207, v207\n"
    "v_or_b32_e32 v194, v194, v202\n"
    "v_mfma_f32_16x16x32_bf16 v[96:99], a[192:195], v[184:187], v[96:99]\n"
    "v_pk_fma_f16 v232, v192, v192, s48\n"
    "v_lshlrev_b32_sdwa v200, 1, v169 dst_sel:DWORD dst_unused:UNUSED_PAD src0_sel:DWORD src1_sel:WORD_1\n"
    "v_mfma_f32_16x16x32_bf16 v[100:103], a[196:199], v[184:187], v[100:103]\n"
    "ds_read_u16_d16_hi v200, v200\n"
    "v_pk_mul_f16 v232, v192, v232\n"
    "v_mfma_f32_16x16x32_bf16 v[112:115], a[200:203], v[184:187], v[112:115]\n"
    "v_exp_f16_e64 v234, v232\n"
    "v_mfma_f32_16x16x32_bf16 v[116:119], a[204:207], v[184:187], v[116:119]\n"
    "s_waitcnt lgkmcnt(7)\n"
    "v_or_b32_e32 v195, v195, v203\n"
    "v_exp_f16_sdwa v234, v232 dst_sel:WORD_1 dst_unused:UNUSED_PRESERVE src0_sel:WORD_1\n"
    "v_mfma_f32_16x16x32_bf16 v[128:131], v[32:35], v[184:187], v[128:131]\n"
    "v_pk_fma_f16 v234, v234, s49, s49\n"
    "v_lshlrev_b32_sdwa v201, 1, v171 dst_sel:DWORD dst_unused:UNUSED_PAD src0_sel:DWORD src1_sel:WORD_1\n"
    "v_mfma_f32_16x16x32_bf16 v[132:135], v[36:39], v[184:187], v[132:135]\n"
    "ds_read_u16_d16_hi v201, v201\n"
    "v_lshlrev_b32_sdwa v184, 1, v168 dst_sel:DWORD dst_unused:UNUSED_PAD src0_sel:DWORD src1_sel:WORD_1\n"
    "v_mfma_f32_16x16x32_bf16 v[104:107], a[192:195], v[188:191], v[104:107]\n"
    "s_waitcnt lgkmcnt(6)\n"
    "ds_read_u16 v184, v184\n"
    "v_or_b32_e32 v197, v197, v205\n"
    "v_mfma_f32_16x16x32_bf16 v[108:111], a[196:199], v[188:191], v[108:111]\n"
    "v_lshlrev_b32_sdwa v185, 1, v170 dst_sel:DWORD dst_unused:UNUSED_PAD src0_sel:DWORD src1_sel:WORD_1\n"
    "v_lshlrev_b32_sdwa v186, 1, v172 dst_sel:DWORD dst_unused:UNUSED_PAD src0_sel:DWORD src1_sel:WORD_1\n"
    "v_mfma_f32_16x16x32_bf16 v[120:123], a[200:203], v[188:191], v[120:123]\n"
    "ds_read_u16 v185, v185\n"
    "ds_read_u16 v186, v186\n"
    "v_mfma_f32_16x16x32_bf16 v[124:127], a[204:207], v[188:191], v[124:127]\n"
    "v_lshlrev_b32_sdwa v202, 1, v173 dst_sel:DWORD dst_unused:UNUSED_PAD src0_sel:DWORD src1_sel:WORD_1\n"
    "v_lshlrev_b32_sdwa v187, 1, v174 dst_sel:DWORD dst_unused:UNUSED_PAD src0_sel:DWORD src1_sel:WORD_1\n"
    "v_mfma_f32_16x16x32_bf16 v[136:139], v[32:35], v[188:191], v[136:139]\n"
    "s_waitcnt lgkmcnt(7)\n"
    "ds_read_u16_d16_hi v202, v202\n"
    "v_lshlrev_b32_sdwa v203, 1, v175 dst_sel:DWORD dst_unused:UNUSED_PAD src0_sel:DWORD src1_sel:WORD_1\n"
    "v_mfma_f32_16x16x32_bf16 v[140:143], v[36:39], v[188:191], v[140:143]\n"
    "v_rcp_f16_e32 v232, v234\n"
    "v_mfma_f32_16x16x32_bf16 v[168:171], a[88:91], v[144:147], 0\n"
    "ds_read_u16 v187, v187\n"
    "v_or_b32_e32 v198, v198, v206\n"
    "v_mfma_f32_16x16x32_bf16 v[168:171], a[136:139], v[148:151], v[168:171]\n"
    "s_waitcnt lgkmcnt(7)\n"
    "v_rcp_f16_sdwa v232, v234 dst_sel:WORD_1 dst_unused:UNUSED_PRESERVE src0_sel:WORD_1\n"
    "v_mfma_f32_16x16x32_bf16 v[168:171], a[52:55], v[152:155], v[168:171]\n"
    "ds_read_u16_d16_hi v203, v203\n"
    "v_lshlrev_b32_sdwa v188, 1, v176 dst_sel:DWORD dst_unused:UNUSED_PAD src0_sel:DWORD src1_sel:WORD_1\n"
    "v_mfma_f32_16x16x32_bf16 v[172:175], a[92:95], v[144:147], 0\n"
    "v_lshlrev_b32_sdwa v204, 1, v177 dst_sel:DWORD dst_unused:UNUSED_PAD src0_sel:DWORD src1_sel:WORD_1\n"
    "v_lshlrev_b32_sdwa v189, 1, v178 dst_sel:DWORD dst_unused:UNUSED_PAD src0_sel:DWORD src1_sel:WORD_1\n"
    "v_lshlrev_b32_sdwa v205, 1, v179 dst_sel:DWORD dst_unused:UNUSED_PAD src0_sel:DWORD src1_sel:WORD_1\n"
    "v_lshlrev_b32_sdwa v190, 1, v180 dst_sel:DWORD dst_unused:UNUSED_PAD src0_sel:DWORD src1_sel:WORD_1\n"
    "v_lshlrev_b32_sdwa v206, 1, v181 dst_sel:DWORD dst_unused:UNUSED_PAD src0_sel:DWORD src1_sel:WORD_1\n"
    "v_lshlrev_b32_sdwa v191, 1, v182 dst_sel:DWORD dst_unused:UNUSED_PAD src0_sel:DWORD src1_sel:WORD_1\n"
    "v_fma_mix_f32 v234, v192, v232, 0 op_sel_hi:[1,1,0]\n"
    "v_mfma_f32_16x16x32_bf16 v[172:175], a[140:143], v[148:151], v[172:175]\n"
    "ds_read_u16 v188, v188\n"
    "ds_read_u16_d16_hi v204, v204\n"
    "v_mfma_f32_16x16x32_bf16 v[172:175], v[56:59], v[152:155], v[172:175]\n"
    "s_waitcnt lgkmcnt(7)\n"
    "v_or_b32_e32 v199, v199, v207\n"
    "v_lshlrev_b32_sdwa v207, 1, v183 dst_sel:DWORD dst_unused:UNUSED_PAD src0_sel:DWORD src1_sel:WORD_1\n"
    "v_fma_mix_f32 v232, v192, v232, 0 op_sel:[1,1,0] op_sel_hi:[1,1,0]\n"
    "v_mfma_f32_16x16x32_bf16 v[176:179], a[88:91], v[156:159], 0\n"
    "ds_read_u16 v189, v189\n"
    "v_cvt_pk_bf16_f32 v192, v234, v232\n"
    "v_mfma_f32_16x16x32_bf16 v[176:179], a[136:139], v[160:163], v[176:179]\n"
    "ds_read_u16_d16_hi v205, v205\n"
    "v_pk_fma_f16 v232, v196, v196, s48\n"
    "v_mfma_f32_16x16x32_bf16 v[176:179], a[52:55], v[164:167], v[176:179]\n"
    "s_waitcnt lgkmcnt(7)\n"
    "ds_read_u16 v190, v190\n"
    "v_pk_mul_f16 v232, v196, v232\n"
    "v_mfma_f32_16x16x32_bf16 v[180:183], a[92:95], v[156:159], 0\n"
    "v_exp_f16_e64 v234, v232\n"
    "v_mfma_f32_16x16x32_bf16 v[180:183], a[140:143], v[160:163], v[180:183]\n"
    "s_waitcnt lgkmcnt(7)\n"
    "ds_read_u16_d16_hi v206, v206\n"
    "buffer_load_dwordx4 a[232:235], v245, s[40:43], s58 offen offset:64\n"
    "buffer_load_dwordx4 a[224:227], v245, s[40:43], s58 offen offset:0\n"
    "buffer_load_dwordx4 v[208:211], v245, s[40:43], s58 offen offset:128\n"
    "v_mfma_f32_16x16x32_bf16 v[180:183], v[56:59], v[164:167], v[180:183]\n"
    "ds_read_u16 v191, v191\n"
    "v_exp_f16_sdwa v234, v232 dst_sel:WORD_1 dst_unused:UNUSED_PRESERVE src0_sel:WORD_1\n"
    "v_or_b32_e64 v184, v184, v200\n"
    "v_pk_fma_f16 v234, v234, s49, s49\n"
    "v_mfma_f32_16x16x32_bf16 v[96:99], a[208:211], v[192:195], v[96:99]\n"
    "s_waitcnt lgkmcnt(7)\n"
    "ds_read_u16_d16_hi v207, v207\n"
    "v_rcp_f16_e32 v232, v234\n"
    "v_mfma_f32_16x16x32_bf16 v[100:103], a[212:215], v[192:195], v[100:103]\n"
    "v_rcp_f16_sdwa v232, v234 dst_sel:WORD_1 dst_unused:UNUSED_PRESERVE src0_sel:WORD_1\n"
    "v_mfma_f32_16x16x32_bf16 v[112:115], a[216:219], v[192:195], v[112:115]\n"
    "v_fma_mix_f32 v234, v196, v232, 0 op_sel_hi:[1,1,0]\n"
    "v_fma_mix_f32 v232, v196, v232, 0 op_sel:[1,1,0] op_sel_hi:[1,1,0]\n"
    "v_mfma_f32_16x16x32_bf16 v[116:119], a[220:223], v[192:195], v[116:119]\n"
    "v_cvt_pk_bf16_f32 v196, v234, v232\n"
    "v_or_b32_e32 v185, v185, v201\n"
    "v_mfma_f32_16x16x32_bf16 v[128:131], v[40:43], v[192:195], v[128:131]\n"
    "s_waitcnt lgkmcnt(6)\n"
    "v_lshlrev_b32_sdwa v201, 1, v171 dst_sel:DWORD dst_unused:UNUSED_PAD src0_sel:DWORD src1_sel:WORD_1\n"
    "v_or_b32_e64 v186, v186, v202\n"
    "v_mfma_f32_16x16x32_bf16 v[132:135], v[44:47], v[192:195], v[132:135]\n"
    "ds_read_u16_d16_hi v201, v201\n"
    "v_cvt_pk_f16_f32 v192, v168, v169\n"
    "v_mfma_f32_16x16x32_bf16 v[104:107], a[208:211], v[196:199], v[104:107]\n"
    "s_nop 0\n"
    "v_lshlrev_b32_sdwa v193, 1, v170 dst_sel:DWORD dst_unused:UNUSED_PAD src0_sel:DWORD src1_sel:WORD_1\n"
    "v_lshlrev_b32_sdwa v194, 1, v172 dst_sel:DWORD dst_unused:UNUSED_PAD src0_sel:DWORD src1_sel:WORD_1\n"
    "v_mfma_f32_16x16x32_bf16 v[108:111], a[212:215], v[196:199], v[108:111]\n"
    "s_waitcnt lgkmcnt(5)\n"
    "ds_read_u16 v193, v193\n"
    "buffer_load_dwordx4 a[236:239], v245, s[40:43], s89 offen offset:64\n"
    "buffer_load_dwordx4 a[228:231], v245, s[40:43], s89 offen offset:0\n"
    "buffer_load_dwordx4 v[212:215], v245, s[40:43], s89 offen offset:128\n"
    "v_mfma_f32_16x16x32_bf16 v[120:123], a[216:219], v[196:199], v[120:123]\n"
    "ds_read_u16 v194, v194\n"
    "v_lshlrev_b32_sdwa v202, 1, v173 dst_sel:DWORD dst_unused:UNUSED_PAD src0_sel:DWORD src1_sel:WORD_1\n"
    "v_mfma_f32_16x16x32_bf16 v[124:127], a[220:223], v[196:199], v[124:127]\n"
    "ds_read_u16_d16_hi v202, v202\n"
    "v_lshlrev_b32_sdwa v195, 1, v174 dst_sel:DWORD dst_unused:UNUSED_PAD src0_sel:DWORD src1_sel:WORD_1\n"
    "v_mfma_f32_16x16x32_bf16 v[136:139], v[40:43], v[196:199], v[136:139]\n"
    "s_waitcnt lgkmcnt(6)\n"
    "ds_read_u16 v195, v195\n"
    "v_or_b32_e32 v187, v187, v203\n"
    "v_mfma_f32_16x16x32_bf16 v[140:143], v[44:47], v[196:199], v[140:143]\n"
    "v_lshlrev_b32_sdwa v203, 1, v175 dst_sel:DWORD dst_unused:UNUSED_PAD src0_sel:DWORD src1_sel:WORD_1\n"
    "v_cvt_pk_f16_f32 v196, v176, v177\n"
    "v_mfma_f32_16x16x32_bf16 v[168:171], a[96:99], v[144:147], 0\n"
    "ds_read_u16_d16_hi v203, v203\n"
    "v_lshlrev_b32_sdwa v197, 1, v178 dst_sel:DWORD dst_unused:UNUSED_PAD src0_sel:DWORD src1_sel:WORD_1\n"
    "v_mfma_f32_16x16x32_bf16 v[168:171], a[144:147], v[148:151], v[168:171]\n"
    "ds_read_u16 v197, v197\n"
    "v_lshlrev_b32_sdwa v198, 1, v180 dst_sel:DWORD dst_unused:UNUSED_PAD src0_sel:DWORD src1_sel:WORD_1\n"
    "v_mfma_f32_16x16x32_bf16 v[168:171], a[56:59], v[152:155], v[168:171]\n"
    "s_waitcnt lgkmcnt(7)\n"
    "v_lshlrev_b32_sdwa v199, 1, v182 dst_sel:DWORD dst_unused:UNUSED_PAD src0_sel:DWORD src1_sel:WORD_1\n"
    "v_or_b32_e32 v188, v188, v204\n"
    "v_mfma_f32_16x16x32_bf16 v[172:175], a[100:103], v[144:147], 0\n"
    "ds_read_u16 v198, v198\n"
    "v_or_b32_e32 v189, v189, v205\n"
    "v_mfma_f32_16x16x32_bf16 v[172:175], a[148:151], v[148:151], v[172:175]\n"
    "ds_read_u16 v199, v199\n"
    "v_lshlrev_b32_sdwa v205, 1, v179 dst_sel:DWORD dst_unused:UNUSED_PAD src0_sel:DWORD src1_sel:WORD_1\n"
    "v_mfma_f32_16x16x32_bf16 v[172:175], a[60:63], v[152:155], v[172:175]\n"
    "s_waitcnt lgkmcnt(7)\n"
    "ds_read_u16_d16_hi v205, v205\n"
    "v_cvt_pk_bf16_f32 v96, v96, v97\n"
    "v_mfma_f32_16x16x32_bf16 v[176:179], a[96:99], v[156:159], 0\n"
    "v_cvt_pk_bf16_f32 v97, v98, v99\n"
    "v_cvt_pk_bf16_f32 v98, v100, v101\n"
    "v_mfma_f32_16x16x32_bf16 v[176:179], a[144:147], v[160:163], v[176:179]\n"
    "v_cvt_pk_bf16_f32 v99, v102, v103\n"
    "v_or_b32_e32 v190, v190, v206\n"
    "v_mfma_f32_16x16x32_bf16 v[176:179], a[56:59], v[164:167], v[176:179]\n"
    "ds_write_b128 v243, v[96:99] offset:24640\n"
    "v_mfma_f32_16x16x32_bf16 v[96:99], a[0:3], v[184:187], 0\n"
    "s_waitcnt lgkmcnt(7)\n"
    "v_lshlrev_b32_sdwa v206, 1, v181 dst_sel:DWORD dst_unused:UNUSED_PAD src0_sel:DWORD src1_sel:WORD_1\n"
    "v_cvt_pk_bf16_f32 v112, v112, v113\n"
    "v_mfma_f32_16x16x32_bf16 v[100:103], a[4:7], v[184:187], 0\n"
    "v_cvt_pk_bf16_f32 v113, v114, v115\n"
    "v_cvt_pk_bf16_f32 v114, v116, v117\n"
    "v_cvt_pk_bf16_f32 v115, v118, v119\n"
    "ds_write_b128 v243, v[112:115] offset:26688\n"
    "v_mfma_f32_16x16x32_bf16 v[112:115], a[8:11], v[184:187], 0\n"
    "s_waitcnt lgkmcnt(5)\n"
    "v_cvt_pk_bf16_f32 v128, v128, v129\n"
    "v_cvt_pk_bf16_f32 v129, v130, v131\n"
    "v_or_b32_e32 v191, v191, v207\n"
    "v_mfma_f32_16x16x32_bf16 v[116:119], a[12:15], v[184:187], 0\n"
    "ds_read_b128 a[252:255], v249 offset:0\n"
    "ds_read_u16_d16_hi v206, v206\n"
    "v_lshlrev_b32_sdwa v207, 1, v183 dst_sel:DWORD dst_unused:UNUSED_PAD src0_sel:DWORD src1_sel:WORD_1\n"
    "v_mfma_f32_16x16x32_bf16 v[180:183], a[100:103], v[156:159], 0\n"
    "ds_read_u16_d16_hi v207, v207\n"
    "v_cvt_pk_bf16_f32 v130, v132, v133\n"
    "v_mfma_f32_16x16x32_bf16 v[180:183], a[148:151], v[160:163], v[180:183]\n"
    "v_or_b32_e64 v193, v193, v201\n"
    "v_cvt_pk_bf16_f32 v131, v134, v135\n"
    "v_mfma_f32_16x16x32_bf16 v[180:183], a[60:63], v[164:167], v[180:183]\n"
    "s_nop 0\n"
    "ds_write_b128 v243, v[128:131] offset:28736\n"
    "v_mfma_f32_16x16x32_bf16 v[128:131], v[0:3], v[184:187], 0\n"
    "s_waitcnt lgkmcnt(5)\n"
    "v_cvt_pk_bf16_f32 v104, v104, v105\n"
    "v_cvt_pk_bf16_f32 v105, v106, v107\n"
    "v_mfma_f32_16x16x32_bf16 v[132:135], v[4:7], v[184:187], 0\n"
    "v_cvt_pk_bf16_f32 v106, v108, v109\n"
    "v_cvt_pk_bf16_f32 v107, v110, v111\n"
    "v_mfma_f32_16x16x32_bf16 v[108:111], a[4:7], v[188:191], 0\n"
    "ds_write_b128 v243, v[104:107] offset:25664\n"
    "v_mfma_f32_16x16x32_bf16 v[104:107], a[0:3], v[188:191], 0\n"
    "s_waitcnt lgkmcnt(4)\n"
    "buffer_store_dwordx4 a[252:255], v255, s[44:47], s57 offen offset:0 sc1\n"
    "v_or_b32_e32 v194, v194, v202\n"
    "v_cvt_pk_bf16_f32 v120, v120, v121\n"
    "v_cvt_pk_bf16_f32 v121, v122, v123\n"
    "v_cvt_pk_bf16_f32 v122, v124, v125\n"
    "v_cvt_pk_bf16_f32 v123, v126, v127\n"
    "ds_write_b128 v243, v[120:123] offset:27712\n"
    "v_mfma_f32_16x16x32_bf16 v[120:123], a[8:11], v[188:191], 0\n"
    "s_waitcnt lgkmcnt(2)\n"
    "v_pk_fma_f16 v232, v192, v192, s48\n"
    "v_cvt_pk_bf16_f32 v136, v136, v137\n"
    "v_mfma_f32_16x16x32_bf16 v[124:127], a[12:15], v[188:191], 0\n"
    "s_nop 0\n"
    "ds_read_b128 a[252:255], v249 offset:2048\n"
    "v_cvt_pk_bf16_f32 v137, v138, v139\n"
    "v_cvt_pk_bf16_f32 v138, v140, v141\n"
    "v_or_b32_e64 v195, v195, v203\n"
    "v_cvt_pk_bf16_f32 v139, v142, v143\n"
    "ds_write_b128 v243, v[136:139] offset:29760\n"
    "v_pk_mul_f16 v232, v192, v232\n"
    "v_mfma_f32_16x16x32_bf16 v[136:139], v[0:3], v[188:191], 0\n"
    "v_exp_f16_e64 v234, v232\n"
    "v_mfma_f32_16x16x32_bf16 v[140:143], v[4:7], v[188:191], 0\n"
    "v_or_b32_e64 v197, v197, v205\n"
    "v_exp_f16_sdwa v234, v232 dst_sel:WORD_1 dst_unused:UNUSED_PRESERVE src0_sel:WORD_1\n"
    "v_cvt_pk_f16_f32 v184, v168, v169\n"
    "v_pk_fma_f16 v234, v234, s49, s49\n"
    "v_lshlrev_b32_sdwa v185, 1, v170 dst_sel:DWORD dst_unused:UNUSED_PAD src0_sel:DWORD src1_sel:WORD_1\n"
    "v_lshlrev_b32_sdwa v201, 1, v171 dst_sel:DWORD dst_unused:UNUSED_PAD src0_sel:DWORD src1_sel:WORD_1\n"
    "v_mfma_f32_16x16x32_bf16 v[168:171], a[104:107], v[144:147], 0\n"
    "s_nop 0\n"
    "ds_read_u16 v185, v185\n"
    "ds_read_u16_d16_hi v201, v201\n"
    "v_mfma_f32_16x16x32_bf16 v[168:171], a[152:155], v[148:151], v[168:171]\n"
    "s_waitcnt lgkmcnt(3)\n"
    "buffer_store_dwordx4 a[252:255], v255, s[44:47], s57 offen offset:64 sc1\n"
    "v_lshlrev_b32_sdwa v186, 1, v172 dst_sel:DWORD dst_unused:UNUSED_PAD src0_sel:DWORD src1_sel:WORD_1\n"
    "v_lshlrev_b32_sdwa v202, 1, v173 dst_sel:DWORD dst_unused:UNUSED_PAD src0_sel:DWORD src1_sel:WORD_1\n"
    "v_lshlrev_b32_sdwa v187, 1, v174 dst_sel:DWORD dst_unused:UNUSED_PAD src0_sel:DWORD src1_sel:WORD_1\n"
    "v_lshlrev_b32_sdwa v203, 1, v175 dst_sel:DWORD dst_unused:UNUSED_PAD src0_sel:DWORD src1_sel:WORD_1\n"
    "v_rcp_f16_e64 v232, v234\n"
    "v_mfma_f32_16x16x32_bf16 v[168:171], a[64:67], v[152:155], v[168:171]\n"
    "ds_read_u16 v186, v186\n"
    "ds_read_u16_d16_hi v202, v202\n"
    "v_or_b32_e64 v198, v198, v206\n"
    "v_rcp_f16_sdwa v232, v234 dst_sel:WORD_1 dst_unused:UNUSED_PRESERVE src0_sel:WORD_1\n"
    "v_mfma_f32_16x16x32_bf16 v[172:175], a[108:111], v[144:147], 0\n"
    "ds_read_u16 v187, v187\n"
    "ds_read_u16_d16_hi v203, v203\n"
    "v_cvt_pk_f16_f32 v188, v176, v177\n"
    "v_lshlrev_b32_sdwa v189, 1, v178 dst_sel:DWORD dst_unused:UNUSED_PAD src0_sel:DWORD src1_sel:WORD_1\n"
    "v_lshlrev_b32_sdwa v205, 1, v179 dst_sel:DWORD dst_unused:UNUSED_PAD src0_sel:DWORD src1_sel:WORD_1\n"
    "v_lshlrev_b32_sdwa v190, 1, v180 dst_sel:DWORD dst_unused:UNUSED_PAD src0_sel:DWORD src1_sel:WORD_1\n"
    "v_lshlrev_b32_sdwa v206, 1, v181 dst_sel:DWORD dst_unused:UNUSED_PAD src0_sel:DWORD src1_sel:WORD_1\n"
    "v_lshlrev_b32_sdwa v191, 1, v182 dst_sel:DWORD dst_unused:UNUSED_PAD src0_sel:DWORD src1_sel:WORD_1\n"
    "v_fma_mix_f32 v234, v192, v232, 0 op_sel_hi:[1,1,0]\n"
    "v_mfma_f32_16x16x32_bf16 v[172:175], a[156:159], v[148:151], v[172:175]\n"
    "ds_read_b128 a[252:255], v249 offset:4096\n"
    "ds_read_u16 v189, v189\n"
    "v_mfma_f32_16x16x32_bf16 v[172:175], a[68:71], v[152:155], v[172:175]\n"
    "v_or_b32_e64 v199, v199, v207\n"
    "v_lshlrev_b32_sdwa v207, 1, v183 dst_sel:DWORD dst_unused:UNUSED_PAD src0_sel:DWORD src1_sel:WORD_1\n"
    "v_fma_mix_f32 v232, v192, v232, 0 op_sel:[1,1,0] op_sel_hi:[1,1,0]\n"
    "v_mfma_f32_16x16x32_bf16 v[176:179], a[104:107], v[156:159], 0\n"
    "s_nop 0\n"
    "ds_read_u16_d16_hi v205, v205\n"
    "v_cvt_pk_bf16_f32 v192, v234, v232\n"
    "v_mfma_f32_16x16x32_bf16 v[176:179], a[152:155], v[160:163], v[176:179]\n"
    "s_waitcnt lgkmcnt(7)\n"
    "ds_read_u16 v190, v190\n"
    "v_pk_fma_f16 v232, v196, v196, s48\n"
    "v_mfma_f32_16x16x32_bf16 v[176:179], a[64:67], v[164:167], v[176:179]\n"
    "v_pk_mul_f16 v232, v196, v232\n"
    "v_or_b32_e64 v185, v185, v201\n"
    "v_mfma_f32_16x16x32_bf16 v[180:183], a[108:111], v[156:159], 0\n"
    "ds_read_u16_d16_hi v206, v206\n"
    "v_exp_f16_e64 v234, v232\n"
    "v_mfma_f32_16x16x32_bf16 v[180:183], a[156:159], v[160:163], v[180:183]\n"
    "ds_read_u16 v191, v191\n"
    "v_exp_f16_sdwa v234, v232 dst_sel:WORD_1 dst_unused:UNUSED_PRESERVE src0_sel:WORD_1\n"
    "v_mfma_f32_16x16x32_bf16 v[180:183], a[68:71], v[164:167], v[180:183]\n"
    "s_waitcnt lgkmcnt(8)\n"
    "v_pk_fma_f16 v234, v234, s49, s49\n"
    "v_or_b32_e32 v186, v186, v202\n"
    "v_mfma_f32_16x16x32_bf16 v[96:99], a[16:19], v[192:195], v[96:99]\n"
    "ds_read_u16_d16_hi v207, v207\n"
    "v_rcp_f16_e32 v232, v234\n"
    "v_mfma_f32_16x16x32_bf16 v[100:103], a[20:23], v[192:195], v[100:103]\n"
    "v_rcp_f16_sdwa v232, v234 dst_sel:WORD_1 dst_unused:UNUSED_PRESERVE src0_sel:WORD_1\n"
    "v_mfma_f32_16x16x32_bf16 v[112:115], a[24:27], v[192:195], v[112:115]\n"
    "s_waitcnt lgkmcnt(6)\n"
    "buffer_store_dwordx4 a[252:255], v255, s[44:47], s57 offen offset:128 sc1\n"
    "v_mfma_f32_16x16x32_bf16 v[116:119], a[28:31], v[192:195], v[116:119]\n"
    "v_fma_mix_f32 v234, v196, v232, 0 op_sel_hi:[1,1,0]\n"
    "v_fma_mix_f32 v232, v196, v232, 0 op_sel:[1,1,0] op_sel_hi:[1,1,0]\n"
    "v_mfma_f32_16x16x32_bf16 v[128:131], v[8:11], v[192:195], v[128:131]\n"
    "ds_read_b128 a[252:255], v249 offset:1024\n"
    "v_cvt_pk_bf16_f32 v196, v234, v232\n"
    "v_mfma_f32_16x16x32_bf16 v[132:135], v[12:15], v[192:195], v[132:135]\n"
    "v_pk_fma_f16 v232, v184, v184, s48\n"
    "v_or_b32_e32 v187, v187, v203\n"
    "v_mfma_f32_16x16x32_bf16 v[104:107], a[16:19], v[196:199], v[104:107]\n"
    "s_waitcnt lgkmcnt(5)\n"
    "v_pk_mul_f16 v232, v184, v232\n"
    "v_or_b32_e64 v189, v189, v205\n"
    "v_mfma_f32_16x16x32_bf16 v[108:111], a[20:23], v[196:199], v[108:111]\n"
    "v_exp_f16_e32 v234, v232\n"
    "v_mfma_f32_16x16x32_bf16 v[120:123], a[24:27], v[196:199], v[120:123]\n"
    "v_exp_f16_sdwa v234, v232 dst_sel:WORD_1 dst_unused:UNUSED_PRESERVE src0_sel:WORD_1\n"
    "v_mfma_f32_16x16x32_bf16 v[124:127], a[28:31], v[196:199], v[124:127]\n"
    "s_waitcnt lgkmcnt(3)\n"
    "v_pk_fma_f16 v234, v234, s49, s49\n"
    "v_lshlrev_b32_sdwa v192, 1, v168 dst_sel:DWORD dst_unused:UNUSED_PAD src0_sel:DWORD src1_sel:WORD_1\n"
    "v_mfma_f32_16x16x32_bf16 v[136:139], v[8:11], v[196:199], v[136:139]\n"
    "ds_read_u16 v192, v192\n"
    "v_lshlrev_b32_sdwa v200, 1, v169 dst_sel:DWORD dst_unused:UNUSED_PAD src0_sel:DWORD src1_sel:WORD_1\n"
    "v_mfma_f32_16x16x32_bf16 v[140:143], v[12:15], v[196:199], v[140:143]\n"
    "ds_read_u16_d16_hi v200, v200\n"
    "v_lshlrev_b32_sdwa v193, 1, v170 dst_sel:DWORD dst_unused:UNUSED_PAD src0_sel:DWORD src1_sel:WORD_1\n"
    "v_lshlrev_b32_sdwa v201, 1, v171 dst_sel:DWORD dst_unused:UNUSED_PAD src0_sel:DWORD src1_sel:WORD_1\n"
    "v_mfma_f32_16x16x32_bf16 v[168:171], a[112:115], v[144:147], 0\n"
    "s_waitcnt lgkmcnt(2)\n"
    "ds_read_u16 v193, v193\n"
    "ds_read_u16_d16_hi v201, v201\n"
    "v_mfma_f32_16x16x32_bf16 v[168:171], a[160:163], v[148:151], v[168:171]\n"
    "s_nop 0\n"
    "buffer_store_dwordx4 a[252:255], v255, s[44:47], s88 offen offset:0 sc1\n"
    "v_lshlrev_b32_sdwa v194, 1, v172 dst_sel:DWORD dst_unused:UNUSED_PAD src0_sel:DWORD src1_sel:WORD_1\n"
    "v_lshlrev_b32_sdwa v202, 1, v173 dst_sel:DWORD dst_unused:UNUSED_PAD src0_sel:DWORD src1_sel:WORD_1\n"
    "v_lshlrev_b32_sdwa v195, 1, v174 dst_sel:DWORD dst_unused:UNUSED_PAD src0_sel:DWORD src1_sel:WORD_1\n"
    "v_lshlrev_b32_sdwa v203, 1, v175 dst_sel:DWORD dst_unused:UNUSED_PAD src0_sel:DWORD src1_sel:WORD_1\n"
    "v_rcp_f16_e64 v232, v234\n"
    "v_mfma_f32_16x16x32_bf16 v[168:171], a[72:75], v[152:155], v[168:171]\n"
    "ds_read_u16 v194, v194\n"
    "ds_read_u16_d16_hi v202, v202\n"
    "v_or_b32_e64 v190, v190, v206\n"
    "v_rcp_f16_sdwa v232, v234 dst_sel:WORD_1 dst_unused:UNUSED_PRESERVE src0_sel:WORD_1\n"
    "v_mfma_f32_16x16x32_bf16 v[172:175], a[116:119], v[144:147], 0\n"
    "ds_read_u16 v195, v195\n"
    "ds_read_u16_d16_hi v203, v203\n"
    "v_lshlrev_b32_sdwa v196, 1, v176 dst_sel:DWORD dst_unused:UNUSED_PAD src0_sel:DWORD src1_sel:WORD_1\n"
    "v_lshlrev_b32_sdwa v204, 1, v177 dst_sel:DWORD dst_unused:UNUSED_PAD src0_sel:DWORD src1_sel:WORD_1\n"
    "v_lshlrev_b32_sdwa v197, 1, v178 dst_sel:DWORD dst_unused:UNUSED_PAD src0_sel:DWORD src1_sel:WORD_1\n"
    "v_lshlrev_b32_sdwa v205, 1, v179 dst_sel:DWORD dst_unused:UNUSED_PAD src0_sel:DWORD src1_sel:WORD_1\n"
    "v_lshlrev_b32_sdwa v198, 1, v180 dst_sel:DWORD dst_unused:UNUSED_PAD src0_sel:DWORD src1_sel:WORD_1\n"
    "v_lshlrev_b32_sdwa v206, 1, v181 dst_sel:DWORD dst_unused:UNUSED_PAD src0_sel:DWORD src1_sel:WORD_1\n"
    "v_lshlrev_b32_sdwa v199, 1, v182 dst_sel:DWORD dst_unused:UNUSED_PAD src0_sel:DWORD src1_sel:WORD_1\n"
    "v_fma_mix_f32 v234, v184, v232, 0 op_sel_hi:[1,1,0]\n"
    "v_mfma_f32_16x16x32_bf16 v[172:175], a[164:167], v[148:151], v[172:175]\n"
    "ds_read_u16 v196, v196\n"
    "v_or_b32_e32 v191, v191, v207\n"
    "v_mfma_f32_16x16x32_bf16 v[172:175], v[52:55], v[152:155], v[172:175]\n"
    "s_waitcnt lgkmcnt(7)\n"
    "ds_read_u16_d16_hi v204, v204\n"
    "v_lshlrev_b32_sdwa v207, 1, v183 dst_sel:DWORD dst_unused:UNUSED_PAD src0_sel:DWORD src1_sel:WORD_1\n"
    "v_fma_mix_f32 v232, v184, v232, 0 op_sel:[1,1,0] op_sel_hi:[1,1,0]\n"
    "v_mfma_f32_16x16x32_bf16 v[176:179], a[112:115], v[156:159], 0\n"
    "ds_read_u16 v197, v197\n"
    "v_cvt_pk_bf16_f32 v184, v234, v232\n"
    "v_mfma_f32_16x16x32_bf16 v[176:179], a[160:163], v[160:163], v[176:179]\n"
    "v_pk_fma_f16 v232, v188, v188, s48\n"
    "v_or_b32_e32 v192, v192, v200\n"
    "v_mfma_f32_16x16x32_bf16 v[176:179], a[72:75], v[164:167], v[176:179]\n"
    "s_waitcnt lgkmcnt(7)\n"
    "ds_read_b128 a[252:255], v249 offset:3072\n"
    "v_pk_mul_f16 v232, v188, v232\n"
    "v_mfma_f32_16x16x32_bf16 v[180:183], a[116:119], v[156:159], 0\n"
    "ds_read_u16_d16_hi v205, v205\n"
    "v_exp_f16_e32 v234, v232\n"
    "v_mfma_f32_16x16x32_bf16 v[180:183], a[164:167], v[160:163], v[180:183]\n"
    "v_exp_f16_sdwa v234, v232 dst_sel:WORD_1 dst_unused:UNUSED_PRESERVE src0_sel:WORD_1\n"
    "v_mfma_f32_16x16x32_bf16 v[180:183], v[52:55], v[164:167], v[180:183]\n"
    "s_waitcnt lgkmcnt(7)\n"
    "ds_read_u16 v198, v198\n"
    "v_pk_fma_f16 v234, v234, s49, s49\n"
    "v_mfma_f32_16x16x32_bf16 v[96:99], a[32:35], v[184:187], v[96:99]\n"
    "ds_read_u16_d16_hi v206, v206\n"
    "v_rcp_f16_e64 v232, v234\n"
    "v_mfma_f32_16x16x32_bf16 v[100:103], a[36:39], v[184:187], v[100:103]\n"
    "v_rcp_f16_sdwa v232, v234 dst_sel:WORD_1 dst_unused:UNUSED_PRESERVE src0_sel:WORD_1\n"
    "v_mfma_f32_16x16x32_bf16 v[112:115], a[40:43], v[184:187], v[112:115]\n"
    "ds_read_u16 v199, v199\n"
    "v_fma_mix_f32 v234, v188, v232, 0 op_sel_hi:[1,1,0]\n"
    "v_mfma_f32_16x16x32_bf16 v[116:119], a[44:47], v[184:187], v[116:119]\n"
    "s_waitcnt lgkmcnt(8)\n"
    "v_fma_mix_f32 v232, v188, v232, 0 op_sel:[1,1,0] op_sel_hi:[1,1,0]\n"
    "v_or_b32_e32 v193, v193, v201\n"
    "v_mfma_f32_16x16x32_bf16 v[128:131], v[16:19], v[184:187], v[128:131]\n"
    "ds_read_u16_d16_hi v207, v207\n"
    "v_cvt_pk_bf16_f32 v188, v234, v232\n"
    "v_mfma_f32_16x16x32_bf16 v[132:135], v[20:23], v[184:187], v[132:135]\n"
    "v_cvt_pk_f16_f32 v184, v168, v169\n"
    "v_lshlrev_b32_sdwa v185, 1, v170 dst_sel:DWORD dst_unused:UNUSED_PAD src0_sel:DWORD src1_sel:WORD_1\n"
    "v_mfma_f32_16x16x32_bf16 v[104:107], a[32:35], v[188:191], v[104:107]\n"
    "s_waitcnt lgkmcnt(7)\n"
    "ds_read_u16 v185, v185\n"
    "v_lshlrev_b32_sdwa v201, 1, v171 dst_sel:DWORD dst_unused:UNUSED_PAD src0_sel:DWORD src1_sel:WORD_1\n"
    "v_mfma_f32_16x16x32_bf16 v[108:111], a[36:39], v[188:191], v[108:111]\n"
    "ds_read_u16_d16_hi v201, v201\n"
    "v_lshlrev_b32_sdwa v186, 1, v172 dst_sel:DWORD dst_unused:UNUSED_PAD src0_sel:DWORD src1_sel:WORD_1\n"
    "v_mfma_f32_16x16x32_bf16 v[120:123], a[40:43], v[188:191], v[120:123]\n"
    "s_waitcnt lgkmcnt(7)\n"
    "buffer_store_dwordx4 a[252:255], v255, s[44:47], s88 offen offset:64 sc1\n"
    "ds_read_u16 v186, v186\n"
    "v_mfma_f32_16x16x32_bf16 v[124:127], a[44:47], v[188:191], v[124:127]\n"
    "v_or_b32_e32 v194, v194, v202\n"
    "v_lshlrev_b32_sdwa v202, 1, v173 dst_sel:DWORD dst_unused:UNUSED_PAD src0_sel:DWORD src1_sel:WORD_1\n"
    "v_mfma_f32_16x16x32_bf16 v[136:139], v[16:19], v[188:191], v[136:139]\n"
    "s_waitcnt lgkmcnt(7)\n"
    "ds_read_b128 a[252:255], v249 offset:5120\n"
    "ds_read_u16_d16_hi v202, v202\n"
    "v_mfma_f32_16x16x32_bf16 v[140:143], v[20:23], v[188:191], v[140:143]\n"
    "v_lshlrev_b32_sdwa v187, 1, v174 dst_sel:DWORD dst_unused:UNUSED_PAD src0_sel:DWORD src1_sel:WORD_1\n"
    "v_cvt_pk_f16_f32 v188, v176, v177\n"
    "v_mfma_f32_16x16x32_bf16 v[168:171], a[120:123], v[144:147], 0\n"
    "ds_read_u16 v187, v187\n"
    "v_lshlrev_b32_sdwa v189, 1, v178 dst_sel:DWORD dst_unused:UNUSED_PAD src0_sel:DWORD src1_sel:WORD_1\n"
    "v_mfma_f32_16x16x32_bf16 v[168:171], a[168:171], v[148:151], v[168:171]\n"
    "s_waitcnt lgkmcnt(8)\n"
    "v_lshlrev_b32_sdwa v190, 1, v180 dst_sel:DWORD dst_unused:UNUSED_PAD src0_sel:DWORD src1_sel:WORD_1\n"
    "v_lshlrev_b32_sdwa v191, 1, v182 dst_sel:DWORD dst_unused:UNUSED_PAD src0_sel:DWORD src1_sel:WORD_1\n"
    "v_mfma_f32_16x16x32_bf16 v[168:171], a[76:79], v[152:155], v[168:171]\n"
    "v_or_b32_e32 v195, v195, v203\n"
    "v_lshlrev_b32_sdwa v203, 1, v175 dst_sel:DWORD dst_unused:UNUSED_PAD src0_sel:DWORD src1_sel:WORD_1\n"
    "v_mfma_f32_16x16x32_bf16 v[172:175], a[124:127], v[144:147], 0\n"
    "ds_read_u16_d16_hi v203, v203\n"
    "v_or_b32_e64 v196, v196, v204\n"
    "v_mfma_f32_16x16x32_bf16 v[172:175], a[172:175], v[148:151], v[172:175]\n"
    "ds_read_u16 v189, v189\n"
    "v_or_b32_e32 v197, v197, v205\n"
    "v_mfma_f32_16x16x32_bf16 v[172:175], v[60:63], v[152:155], v[172:175]\n"
    "s_waitcnt lgkmcnt(8)\n"
    "v_lshlrev_b32_sdwa v205, 1, v179 dst_sel:DWORD dst_unused:UNUSED_PAD src0_sel:DWORD src1_sel:WORD_1\n"
    "v_or_b32_e64 v198, v198, v206\n"
    "v_mfma_f32_16x16x32_bf16 v[176:179], a[120:123], v[156:159], 0\n"
    "ds_read_u16_d16_hi v205, v205\n"
    "v_lshlrev_b32_sdwa v206, 1, v181 dst_sel:DWORD dst_unused:UNUSED_PAD src0_sel:DWORD src1_sel:WORD_1\n"
    "v_mfma_f32_16x16x32_bf16 v[176:179], a[168:171], v[160:163], v[176:179]\n"
    "ds_read_u16 v190, v190\n"
    "v_or_b32_e32 v199, v199, v207\n"
    "v_mfma_f32_16x16x32_bf16 v[176:179], a[76:79], v[164:167], v[176:179]\n"
    "s_waitcnt lgkmcnt(8)\n"
    "v_lshlrev_b32_sdwa v207, 1, v183 dst_sel:DWORD dst_unused:UNUSED_PAD src0_sel:DWORD src1_sel:WORD_1\n"
    "v_or_b32_e64 v185, v185, v201\n"
    "v_mfma_f32_16x16x32_bf16 v[180:183], a[124:127], v[156:159], 0\n"
    "ds_read_b128 v[144:147], v243 offset:0\n"
    "v_pk_fma_f16 v232, v184, v184, s48\n"
    "v_mfma_f32_16x16x32_bf16 v[180:183], a[172:175], v[160:163], v[180:183]\n"
    "s_waitcnt lgkmcnt(7)\n"
    "ds_read_u16_d16_hi v206, v206\n"
    "buffer_store_dwordx4 a[252:255], v255, s[44:47], s88 offen offset:128 sc1\n"
    "v_mfma_f32_16x16x32_bf16 v[180:183], v[60:63], v[164:167], v[180:183]\n"
    "s_nop 0\n"
    "ds_read_b128 v[148:151], v243 offset:1024\n"
    "v_pk_mul_f16 v232, v184, v232\n"
    "v_mfma_f32_16x16x32_bf16 v[96:99], a[176:179], v[192:195], v[96:99]\n"
    "s_waitcnt lgkmcnt(8)\n"
    "ds_read_u16 v191, v191\n"
    "v_or_b32_e32 v186, v186, v202\n"
    "v_mfma_f32_16x16x32_bf16 v[100:103], a[180:183], v[192:195], v[100:103]\n"
    "v_exp_f16_e64 v234, v232\n"
    "v_mfma_f32_16x16x32_bf16 v[112:115], a[184:187], v[192:195], v[112:115]\n"
    "ds_read_b128 v[152:155], v243 offset:2048\n"
    "ds_read_u16_d16_hi v207, v207\n"
    "v_mfma_f32_16x16x32_bf16 v[116:119], a[188:191], v[192:195], v[116:119]\n"
    "s_waitcnt lgkmcnt(9)\n"
    "v_or_b32_e32 v187, v187, v203\n"
    "v_exp_f16_sdwa v234, v232 dst_sel:WORD_1 dst_unused:UNUSED_PRESERVE src0_sel:WORD_1\n"
    "v_mfma_f32_16x16x32_bf16 v[128:131], v[24:27], v[192:195], v[128:131]\n"
    "ds_read_b128 v[156:159], v243 offset:3072\n"
    "v_pk_fma_f16 v234, v234, s49, s49\n"
    "v_mfma_f32_16x16x32_bf16 v[132:135], v[28:31], v[192:195], v[132:135]\n"
    "ds_read_b128 v[160:163], v243 offset:4096\n"
    "v_cvt_pk_f16_f32 v192, v168, v169\n"
    "v_mfma_f32_16x16x32_bf16 v[104:107], a[176:179], v[196:199], v[104:107]\n"
    "s_waitcnt lgkmcnt(9)\n"
    "ds_read_b128 v[164:167], v243 offset:5120\n"
    "v_or_b32_e32 v189, v189, v205\n"
    "v_mfma_f32_16x16x32_bf16 v[108:111], a[180:183], v[196:199], v[108:111]\n"
    "s_nop 0\n"
    "v_lshlrev_b32_sdwa v193, 1, v170 dst_sel:DWORD dst_unused:UNUSED_PAD src0_sel:DWORD src1_sel:WORD_1\n"
    "v_lshlrev_b32_sdwa v201, 1, v171 dst_sel:DWORD dst_unused:UNUSED_PAD src0_sel:DWORD src1_sel:WORD_1\n"
    "v_mfma_f32_16x16x32_bf16 v[120:123], a[184:187], v[196:199], v[120:123]\n"
    "s_waitcnt lgkmcnt(8)\n"
    "ds_read_u16 v193, v193\n"
    "v_lshlrev_b32_sdwa v194, 1, v172 dst_sel:DWORD dst_unused:UNUSED_PAD src0_sel:DWORD src1_sel:WORD_1\n"
    "v_mfma_f32_16x16x32_bf16 v[124:127], a[188:191], v[196:199], v[124:127]\n"
    "ds_read_u16_d16_hi v201, v201\n"
    "v_lshlrev_b32_sdwa v202, 1, v173 dst_sel:DWORD dst_unused:UNUSED_PAD src0_sel:DWORD src1_sel:WORD_1\n"
    "v_mfma_f32_16x16x32_bf16 v[136:139], v[24:27], v[196:199], v[136:139]\n"
    "v_lshlrev_b32_sdwa v195, 1, v174 dst_sel:DWORD dst_unused:UNUSED_PAD src0_sel:DWORD src1_sel:WORD_1\n"
    "v_lshlrev_b32_sdwa v203, 1, v175 dst_sel:DWORD dst_unused:UNUSED_PAD src0_sel:DWORD src1_sel:WORD_1\n"
    "v_mfma_f32_16x16x32_bf16 v[140:143], v[28:31], v[196:199], v[140:143]\n"
    "s_waitcnt lgkmcnt(8)\n"
    "ds_read_u16 v194, v194\n"
    "v_rcp_f16_e32 v232, v234\n"
    "v_mfma_f32_16x16x32_bf16 v[168:171], a[80:83], v[144:147], 0\n"
    "ds_read_u16_d16_hi v202, v202\n"
    "v_or_b32_e32 v190, v190, v206\n"
    "v_mfma_f32_16x16x32_bf16 v[168:171], a[128:131], v[148:151], v[168:171]\n"
    "s_waitcnt lgkmcnt(8)\n"
    "v_rcp_f16_sdwa v232, v234 dst_sel:WORD_1 dst_unused:UNUSED_PRESERVE src0_sel:WORD_1\n"
    "v_mfma_f32_16x16x32_bf16 v[168:171], a[48:51], v[152:155], v[168:171]\n"
    "ds_read_u16 v195, v195\n"
    "v_cvt_pk_f16_f32 v196, v176, v177\n"
    "v_mfma_f32_16x16x32_bf16 v[172:175], a[84:87], v[144:147], 0\n"
    "s_nop 0\n"
    "v_lshlrev_b32_sdwa v197, 1, v178 dst_sel:DWORD dst_unused:UNUSED_PAD src0_sel:DWORD src1_sel:WORD_1\n"
    "v_lshlrev_b32_sdwa v205, 1, v179 dst_sel:DWORD dst_unused:UNUSED_PAD src0_sel:DWORD src1_sel:WORD_1\n"
    "v_mfma_f32_16x16x32_bf16 v[172:175], a[132:135], v[148:151], v[172:175]\n"
    "s_waitcnt lgkmcnt(7)\n"
    "ds_read_u16_d16_hi v203, v203\n"
    "v_lshlrev_b32_sdwa v198, 1, v180 dst_sel:DWORD dst_unused:UNUSED_PAD src0_sel:DWORD src1_sel:WORD_1\n"
    "v_lshlrev_b32_sdwa v206, 1, v181 dst_sel:DWORD dst_unused:UNUSED_PAD src0_sel:DWORD src1_sel:WORD_1\n"
    "v_lshlrev_b32_sdwa v199, 1, v182 dst_sel:DWORD dst_unused:UNUSED_PAD src0_sel:DWORD src1_sel:WORD_1\n"
    "v_fma_mix_f32 v234, v184, v232, 0 op_sel_hi:[1,1,0]\n"
    "v_mfma_f32_16x16x32_bf16 v[172:175], v[48:51], v[152:155], v[172:175]\n"
    "s_waitcnt lgkmcnt(6)\n"
    "ds_read_u16 v197, v197\n"
    "v_or_b32_e32 v191, v191, v207\n"
    "v_lshlrev_b32_sdwa v207, 1, v183 dst_sel:DWORD dst_unused:UNUSED_PAD src0_sel:DWORD src1_sel:WORD_1\n"
    "v_fma_mix_f32 v232, v184, v232, 0 op_sel:[1,1,0] op_sel_hi:[1,1,0]\n"
    "v_mfma_f32_16x16x32_bf16 v[176:179], a[80:83], v[156:159], 0\n"
    "s_barrier\n"
    "v_cvt_pk_bf16_f32 v184, v234, v232\n"
    "v_mfma_f32_16x16x32_bf16 v[176:179], a[128:131], v[160:163], v[176:179]\n"
    "s_waitcnt lgkmcnt(5)\n"
    "ds_read_u16_d16_hi v205, v205\n"
    "ds_read_u16 v198, v198\n"
    "v_mfma_f32_16x16x32_bf16 v[176:179], a[48:51], v[164:167], v[176:179]\n"
    "v_pk_fma_f16 v232, v188, v188, s48\n"
    "v_or_b32_e64 v193, v193, v201\n"
    "v_mfma_f32_16x16x32_bf16 v[180:183], a[84:87], v[156:159], 0\n"
    "ds_read_u16_d16_hi v206, v206\n"
    "v_pk_mul_f16 v232, v188, v232\n"
    "v_mfma_f32_16x16x32_bf16 v[180:183], a[132:135], v[160:163], v[180:183]\n"
    "s_waitcnt lgkmcnt(6)\n"
    "ds_read_u16 v199, v199\n"
    "v_exp_f16_e32 v234, v232\n"
    "v_mfma_f32_16x16x32_bf16 v[180:183], v[48:51], v[164:167], v[180:183]\n"
    "ds_read_u16_d16_hi v207, v207\n"
    "v_exp_f16_sdwa v234, v232 dst_sel:WORD_1 dst_unused:UNUSED_PRESERVE src0_sel:WORD_1\n"
    "v_mfma_f32_16x16x32_bf16 v[96:99], a[192:195], v[184:187], v[96:99]\n"
    "v_pk_fma_f16 v234, v234, s49, s49\n"
    "v_or_b32_e64 v194, v194, v202\n"
    "v_mfma_f32_16x16x32_bf16 v[100:103], a[196:199], v[184:187], v[100:103]\n"
    "s_waitcnt lgkmcnt(6)\n"
    "v_rcp_f16_e32 v232, v234\n"
    "v_mfma_f32_16x16x32_bf16 v[112:115], a[200:203], v[184:187], v[112:115]\n"
    "v_rcp_f16_sdwa v232, v234 dst_sel:WORD_1 dst_unused:UNUSED_PRESERVE src0_sel:WORD_1\n"
    "v_mfma_f32_16x16x32_bf16 v[116:119], a[204:207], v[184:187], v[116:119]\n"
    "v_fma_mix_f32 v234, v188, v232, 0 op_sel_hi:[1,1,0]\n"
    "v_fma_mix_f32 v232, v188, v232, 0 op_sel:[1,1,0] op_sel_hi:[1,1,0]\n"
    "v_mfma_f32_16x16x32_bf16 v[128:131], v[32:35], v[184:187], v[128:131]\n"
    "v_cvt_pk_bf16_f32 v188, v234, v232\n"
    "v_pk_fma_f16 v232, v192, v192, s48\n"
    "v_mfma_f32_16x16x32_bf16 v[132:135], v[36:39], v[184:187], v[132:135]\n"
    "s_waitcnt lgkmcnt(4)\n"
    "v_or_b32_e32 v195, v195, v203\n"
    "v_pk_mul_f16 v232, v192, v232\n"
    "v_mfma_f32_16x16x32_bf16 v[104:107], a[192:195], v[188:191], v[104:107]\n"
    "v_exp_f16_e64 v234, v232\n"
    "v_mfma_f32_16x16x32_bf16 v[108:111], a[196:199], v[188:191], v[108:111]\n"
    "v_exp_f16_sdwa v234, v232 dst_sel:WORD_1 dst_unused:UNUSED_PRESERVE src0_sel:WORD_1\n"
    "v_mfma_f32_16x16x32_bf16 v[120:123], a[200:203], v[188:191], v[120:123]\n"
    "s_waitcnt lgkmcnt(2)\n"
    "v_or_b32_e32 v197, v197, v205\n"
    "v_pk_fma_f16 v234, v234, s49, s49\n"
    "v_mfma_f32_16x16x32_bf16 v[124:127], a[204:207], v[188:191], v[124:127]\n"
    "v_lshlrev_b32_sdwa v184, 1, v168 dst_sel:DWORD dst_unused:UNUSED_PAD src0_sel:DWORD src1_sel:WORD_1\n"
    "v_lshlrev_b32_sdwa v200, 1, v169 dst_sel:DWORD dst_unused:UNUSED_PAD src0_sel:DWORD src1_sel:WORD_1\n"
    "v_mfma_f32_16x16x32_bf16 v[136:139], v[32:35], v[188:191], v[136:139]\n"
    "ds_read_u16 v184, v184\n"
    "ds_read_u16_d16_hi v200, v200\n"
    "v_mfma_f32_16x16x32_bf16 v[140:143], v[36:39], v[188:191], v[140:143]\n"
    "s_nop 0\n"
    "v_lshlrev_b32_sdwa v185, 1, v170 dst_sel:DWORD dst_unused:UNUSED_PAD src0_sel:DWORD src1_sel:WORD_1\n"
    "v_lshlrev_b32_sdwa v201, 1, v171 dst_sel:DWORD dst_unused:UNUSED_PAD src0_sel:DWORD src1_sel:WORD_1\n"
    "v_mfma_f32_16x16x32_bf16 v[168:171], a[88:91], v[144:147], 0\n"
    "s_waitcnt lgkmcnt(2)\n"
    "ds_read_u16 v185, v185\n"
    "ds_read_u16_d16_hi v201, v201\n"
    "v_mfma_f32_16x16x32_bf16 v[168:171], a[136:139], v[148:151], v[168:171]\n"
    "v_lshlrev_b32_sdwa v186, 1, v172 dst_sel:DWORD dst_unused:UNUSED_PAD src0_sel:DWORD src1_sel:WORD_1\n"
    "v_lshlrev_b32_sdwa v202, 1, v173 dst_sel:DWORD dst_unused:UNUSED_PAD src0_sel:DWORD src1_sel:WORD_1\n"
    "v_lshlrev_b32_sdwa v187, 1, v174 dst_sel:DWORD dst_unused:UNUSED_PAD src0_sel:DWORD src1_sel:WORD_1\n"
    "v_lshlrev_b32_sdwa v203, 1, v175 dst_sel:DWORD dst_unused:UNUSED_PAD src0_sel:DWORD src1_sel:WORD_1\n"
    "v_rcp_f16_e64 v232, v234\n"
    "v_mfma_f32_16x16x32_bf16 v[168:171], a[52:55], v[152:155], v[168:171]\n"
    "ds_read_u16 v186, v186\n"
    "ds_read_u16_d16_hi v202, v202\n"
    "v_or_b32_e64 v198, v198, v206\n"
    "v_rcp_f16_sdwa v232, v234 dst_sel:WORD_1 dst_unused:UNUSED_PRESERVE src0_sel:WORD_1\n"
    "v_mfma_f32_16x16x32_bf16 v[172:175], a[92:95], v[144:147], 0\n"
    "ds_read_u16 v187, v187\n"
    "v_lshlrev_b32_sdwa v188, 1, v176 dst_sel:DWORD dst_unused:UNUSED_PAD src0_sel:DWORD src1_sel:WORD_1\n"
    "v_mfma_f32_16x16x32_bf16 v[172:175], a[140:143], v[148:151], v[172:175]\n"
    "v_lshlrev_b32_sdwa v204, 1, v177 dst_sel:DWORD dst_unused:UNUSED_PAD src0_sel:DWORD src1_sel:WORD_1\n"
    "v_lshlrev_b32_sdwa v189, 1, v178 dst_sel:DWORD dst_unused:UNUSED_PAD src0_sel:DWORD src1_sel:WORD_1\n"
    "v_lshlrev_b32_sdwa v205, 1, v179 dst_sel:DWORD dst_unused:UNUSED_PAD src0_sel:DWORD src1_sel:WORD_1\n"
    "v_lshlrev_b32_sdwa v190, 1, v180 dst_sel:DWORD dst_unused:UNUSED_PAD src0_sel:DWORD src1_sel:WORD_1\n"
    "v_lshlrev_b32_sdwa v206, 1, v181 dst_sel:DWORD dst_unused:UNUSED_PAD src0_sel:DWORD src1_sel:WORD_1\n"
    "v_lshlrev_b32_sdwa v191, 1, v182 dst_sel:DWORD dst_unused:UNUSED_PAD src0_sel:DWORD src1_sel:WORD_1\n"
    "v_fma_mix_f32 v234, v192, v232, 0 op_sel_hi:[1,1,0]\n"
    "v_mfma_f32_16x16x32_bf16 v[172:175], v[56:59], v[152:155], v[172:175]\n"
    "ds_read_u16_d16_hi v203, v203\n"
    "ds_read_u16 v188, v188\n"
    "v_or_b32_e32 v199, v199, v207\n"
    "v_lshlrev_b32_sdwa v207, 1, v183 dst_sel:DWORD dst_unused:UNUSED_PAD src0_sel:DWORD src1_sel:WORD_1\n"
    "v_fma_mix_f32 v232, v192, v232, 0 op_sel:[1,1,0] op_sel_hi:[1,1,0]\n"
    "v_mfma_f32_16x16x32_bf16 v[176:179], a[88:91], v[156:159], 0\n"
    "s_waitcnt lgkmcnt(7)\n"
    "ds_read_u16_d16_hi v204, v204\n"
    "v_cvt_pk_bf16_f32 v192, v234, v232\n"
    "v_mfma_f32_16x16x32_bf16 v[176:179], a[136:139], v[160:163], v[176:179]\n"
    "v_pk_fma_f16 v232, v196, v196, s48\n"
    "v_or_b32_e64 v184, v184, v200\n"
    "v_mfma_f32_16x16x32_bf16 v[176:179], a[52:55], v[164:167], v[176:179]\n"
    "ds_read_u16 v189, v189\n"
    "v_pk_mul_f16 v232, v196, v232\n"
    "v_mfma_f32_16x16x32_bf16 v[180:183], a[92:95], v[156:159], 0\n"
    "v_exp_f16_e32 v234, v232\n"
    "v_mfma_f32_16x16x32_bf16 v[180:183], a[140:143], v[160:163], v[180:183]\n"
    "s_waitcnt lgkmcnt(7)\n"
    "ds_read_u16_d16_hi v205, v205\n"
    "v_exp_f16_sdwa v234, v232 dst_sel:WORD_1 dst_unused:UNUSED_PRESERVE src0_sel:WORD_1\n"
    "v_mfma_f32_16x16x32_bf16 v[180:183], v[56:59], v[164:167], v[180:183]\n"
    "ds_read_u16 v190, v190\n"
    "v_pk_fma_f16 v234, v234, s49, s49\n"
    "v_mfma_f32_16x16x32_bf16 v[96:99], a[208:211], v[192:195], v[96:99]\n"
    "v_rcp_f16_e32 v232, v234\n"
    "v_mfma_f32_16x16x32_bf16 v[100:103], a[212:215], v[192:195], v[100:103]\n"
    "s_waitcnt lgkmcnt(7)\n"
    "ds_read_u16_d16_hi v206, v206\n"
    "v_rcp_f16_sdwa v232, v234 dst_sel:WORD_1 dst_unused:UNUSED_PRESERVE src0_sel:WORD_1\n"
    "v_mfma_f32_16x16x32_bf16 v[112:115], a[216:219], v[192:195], v[112:115]\n"
    "v_fma_mix_f32 v234, v196, v232, 0 op_sel_hi:[1,1,0]\n"
    "v_fma_mix_f32 v232, v196, v232, 0 op_sel:[1,1,0] op_sel_hi:[1,1,0]\n"
    "v_mfma_f32_16x16x32_bf16 v[116:119], a[220:223], v[192:195], v[116:119]\n"
    "ds_read_u16 v191, v191\n"
    "v_cvt_pk_bf16_f32 v196, v234, v232\n"
    "v_mfma_f32_16x16x32_bf16 v[128:131], v[40:43], v[192:195], v[128:131]\n"
    "s_waitcnt lgkmcnt(7)\n"
    "ds_read_u16_d16_hi v207, v207\n"
    "v_or_b32_e32 v185, v185, v201\n"
    "v_mfma_f32_16x16x32_bf16 v[132:135], v[44:47], v[192:195], v[132:135]\n"
    "v_cvt_pk_f16_f32 v192, v168, v169\n"
    "v_lshlrev_b32_sdwa v193, 1, v170 dst_sel:DWORD dst_unused:UNUSED_PAD src0_sel:DWORD src1_sel:WORD_1\n"
    "v_mfma_f32_16x16x32_bf16 v[104:107], a[208:211], v[196:199], v[104:107]\n"
    "ds_read_u16 v193, v193\n"
    "v_lshlrev_b32_sdwa v201, 1, v171 dst_sel:DWORD dst_unused:UNUSED_PAD src0_sel:DWORD src1_sel:WORD_1\n"
    "v_mfma_f32_16x16x32_bf16 v[108:111], a[212:215], v[196:199], v[108:111]\n"
    "v_lshlrev_b32_sdwa v194, 1, v172 dst_sel:DWORD dst_unused:UNUSED_PAD src0_sel:DWORD src1_sel:WORD_1\n"
    "v_or_b32_e32 v186, v186, v202\n"
    "v_mfma_f32_16x16x32_bf16 v[120:123], a[216:219], v[196:199], v[120:123]\n"
    "s_waitcnt lgkmcnt(7)\n"
    "ds_read_u16_d16_hi v201, v201\n"
    "v_lshlrev_b32_sdwa v202, 1, v173 dst_sel:DWORD dst_unused:UNUSED_PAD src0_sel:DWORD src1_sel:WORD_1\n"
    "v_mfma_f32_16x16x32_bf16 v[124:127], a[220:223], v[196:199], v[124:127]\n"
    "v_lshlrev_b32_sdwa v195, 1, v174 dst_sel:DWORD dst_unused:UNUSED_PAD src0_sel:DWORD src1_sel:WORD_1\n"
    "v_or_b32_e32 v187, v187, v203\n"
    "v_mfma_f32_16x16x32_bf16 v[136:139], v[40:43], v[196:199], v[136:139]\n"
    "ds_read_u16 v194, v194\n"
    "v_lshlrev_b32_sdwa v203, 1, v175 dst_sel:DWORD dst_unused:UNUSED_PAD src0_sel:DWORD src1_sel:WORD_1\n"
    "v_mfma_f32_16x16x32_bf16 v[140:143], v[44:47], v[196:199], v[140:143]\n"
    "s_waitcnt lgkmcnt(7)\n"
    "ds_read_u16_d16_hi v202, v202\n"
    "v_cvt_pk_f16_f32 v196, v176, v177\n"
    "v_mfma_f32_16x16x32_bf16 v[168:171], a[96:99], v[144:147], 0\n"
    "v_lshlrev_b32_sdwa v197, 1, v178 dst_sel:DWORD dst_unused:UNUSED_PAD src0_sel:DWORD src1_sel:WORD_1\n"
    "v_lshlrev_b32_sdwa v198, 1, v180 dst_sel:DWORD dst_unused:UNUSED_PAD src0_sel:DWORD src1_sel:WORD_1\n"
    "v_mfma_f32_16x16x32_bf16 v[168:171], a[144:147], v[148:151], v[168:171]\n"
    "ds_read_u16 v195, v195\n"
    "v_lshlrev_b32_sdwa v199, 1, v182 dst_sel:DWORD dst_unused:UNUSED_PAD src0_sel:DWORD src1_sel:WORD_1\n"
    "v_mfma_f32_16x16x32_bf16 v[168:171], a[56:59], v[152:155], v[168:171]\n"
    "v_or_b32_e64 v188, v188, v204\n"
    "v_or_b32_e32 v189, v189, v205\n"
    "v_mfma_f32_16x16x32_bf16 v[172:175], a[100:103], v[144:147], 0\n"
    "s_waitcnt lgkmcnt(7)\n"
    "ds_read_u16_d16_hi v203, v203\n"
    "v_lshlrev_b32_sdwa v205, 1, v179 dst_sel:DWORD dst_unused:UNUSED_PAD src0_sel:DWORD src1_sel:WORD_1\n"
    "v_mfma_f32_16x16x32_bf16 v[172:175], a[148:151], v[148:151], v[172:175]\n"
    "v_cvt_pk_bf16_f32 v96, v96, v97\n"
    "v_cvt_pk_bf16_f32 v97, v98, v99\n"
    "v_mfma_f32_16x16x32_bf16 v[172:175], a[60:63], v[152:155], v[172:175]\n"
    "ds_read_u16 v197, v197\n"
    "v_cvt_pk_bf16_f32 v98, v100, v101\n"
    "v_mfma_f32_16x16x32_bf16 v[176:179], a[96:99], v[156:159], 0\n"
    "s_waitcnt lgkmcnt(7)\n"
    "ds_read_u16_d16_hi v205, v205\n"
    "v_cvt_pk_bf16_f32 v99, v102, v103\n"
    "v_mfma_f32_16x16x32_bf16 v[176:179], a[144:147], v[160:163], v[176:179]\n"
    "v_or_b32_e32 v190, v190, v206\n"
    "v_lshlrev_b32_sdwa v206, 1, v181 dst_sel:DWORD dst_unused:UNUSED_PAD src0_sel:DWORD src1_sel:WORD_1\n"
    "v_mfma_f32_16x16x32_bf16 v[176:179], a[56:59], v[164:167], v[176:179]\n"
    "v_cvt_pk_bf16_f32 v112, v112, v113\n"
    "v_cvt_pk_bf16_f32 v113, v114, v115\n"
    "v_mfma_f32_16x16x32_bf16 v[100:103], a[4:7], v[184:187], 0\n"
    "s_nop 0\n"
    "v_cvt_pk_bf16_f32 v114, v116, v117\n"
    "ds_write_b128 v242, v[96:99] offset:24640\n"
    "v_mfma_f32_16x16x32_bf16 v[96:99], a[0:3], v[184:187], 0\n"
    "s_waitcnt lgkmcnt(6)\n"
    "v_cvt_pk_bf16_f32 v115, v118, v119\n"
    "v_cvt_pk_bf16_f32 v128, v128, v129\n"
    "v_cvt_pk_bf16_f32 v129, v130, v131\n"
    "v_or_b32_e32 v191, v191, v207\n"
    "v_mfma_f32_16x16x32_bf16 v[116:119], a[12:15], v[184:187], 0\n"
    "s_waitcnt lgkmcnt(5)\n"
    "ds_read_u16 v198, v198\n"
    "ds_write_b128 v242, v[112:115] offset:26688\n"
    "v_mfma_f32_16x16x32_bf16 v[112:115], a[8:11], v[184:187], 0\n"
    "v_lshlrev_b32_sdwa v207, 1, v183 dst_sel:DWORD dst_unused:UNUSED_PAD src0_sel:DWORD src1_sel:WORD_1\n"
    "v_cvt_pk_bf16_f32 v130, v132, v133\n"
    "v_mfma_f32_16x16x32_bf16 v[180:183], a[100:103], v[156:159], 0\n"
    "v_or_b32_e64 v193, v193, v201\n"
    "v_cvt_pk_bf16_f32 v131, v134, v135\n"
    "v_mfma_f32_16x16x32_bf16 v[180:183], a[148:151], v[160:163], v[180:183]\n"
    "s_waitcnt lgkmcnt(5)\n"
    "ds_read_u16_d16_hi v206, v206\n"
    "ds_write_b128 v242, v[128:131] offset:28736\n"
    "v_mfma_f32_16x16x32_bf16 v[180:183], a[60:63], v[164:167], v[180:183]\n"
    "v_cvt_pk_bf16_f32 v104, v104, v105\n"
    "v_cvt_pk_bf16_f32 v105, v106, v107\n"
    "v_mfma_f32_16x16x32_bf16 v[128:131], v[0:3], v[184:187], 0\n"
    "s_waitcnt lgkmcnt(4)\n"
    "v_cvt_pk_bf16_f32 v106, v108, v109\n"
    "v_cvt_pk_bf16_f32 v107, v110, v111\n"
    "v_mfma_f32_16x16x32_bf16 v[132:135], v[4:7], v[184:187], 0\n"
    "ds_read_u16 v199, v199\n"
    "ds_write_b128 v242, v[104:107] offset:25664\n"
    "v_mfma_f32_16x16x32_bf16 v[104:107], a[0:3], v[188:191], 0\n"
    "v_or_b32_e64 v194, v194, v202\n"
    "v_cvt_pk_bf16_f32 v120, v120, v121\n"
    "v_mfma_f32_16x16x32_bf16 v[108:111], a[4:7], v[188:191], 0\n"
    "v_cvt_pk_bf16_f32 v121, v122, v123\n"
    "v_cvt_pk_bf16_f32 v122, v124, v125\n"
    "v_cvt_pk_bf16_f32 v123, v126, v127\n"
    "ds_write_b128 v242, v[120:123] offset:27712\n"
    "v_mfma_f32_16x16x32_bf16 v[120:123], a[8:11], v[188:191], 0\n"
    "s_nop 0\n"
    "v_cvt_pk_bf16_f32 v136, v136, v137\n"
    "v_pk_fma_f16 v232, v192, v192, s48\n"
    "v_mfma_f32_16x16x32_bf16 v[124:127], a[12:15], v[188:191], 0\n"
    "s_waitcnt lgkmcnt(3)\n"
    "ds_read_u16_d16_hi v207, v207\n"
    "v_cvt_pk_bf16_f32 v137, v138, v139\n"
    "v_cvt_pk_bf16_f32 v138, v140, v141\n"
    "v_or_b32_e64 v195, v195, v203\n"
    "v_cvt_pk_bf16_f32 v139, v142, v143\n"
    "ds_write_b128 v242, v[136:139] offset:29760\n"
    "v_pk_mul_f16 v232, v192, v232\n"
    "v_mfma_f32_16x16x32_bf16 v[136:139], v[0:3], v[188:191], 0\n"
    "v_exp_f16_e64 v234, v232\n"
    "v_mfma_f32_16x16x32_bf16 v[140:143], v[4:7], v[188:191], 0\n"
    "v_exp_f16_sdwa v234, v232 dst_sel:WORD_1 dst_unused:UNUSED_PRESERVE src0_sel:WORD_1\n"
    "v_or_b32_e64 v197, v197, v205\n"
    "v_pk_fma_f16 v234, v234, s49, s49\n"
    "v_cvt_pk_f16_f32 v184, v168, v169\n"
    "v_lshlrev_b32_sdwa v185, 1, v170 dst_sel:DWORD dst_unused:UNUSED_PAD src0_sel:DWORD src1_sel:WORD_1\n"
    "v_lshlrev_b32_sdwa v201, 1, v171 dst_sel:DWORD dst_unused:UNUSED_PAD src0_sel:DWORD src1_sel:WORD_1\n"
    "v_mfma_f32_16x16x32_bf16 v[168:171], a[104:107], v[144:147], 0\n"
    "ds_read_u16 v185, v185\n"
    "ds_read_u16_d16_hi v201, v201\n"
    "v_mfma_f32_16x16x32_bf16 v[168:171], a[152:155], v[148:151], v[168:171]\n"
    "v_lshlrev_b32_sdwa v186, 1, v172 dst_sel:DWORD dst_unused:UNUSED_PAD src0_sel:DWORD src1_sel:WORD_1\n"
    "v_lshlrev_b32_sdwa v202, 1, v173 dst_sel:DWORD dst_unused:UNUSED_PAD src0_sel:DWORD src1_sel:WORD_1\n"
    "v_lshlrev_b32_sdwa v187, 1, v174 dst_sel:DWORD dst_unused:UNUSED_PAD src0_sel:DWORD src1_sel:WORD_1\n"
    "v_lshlrev_b32_sdwa v203, 1, v175 dst_sel:DWORD dst_unused:UNUSED_PAD src0_sel:DWORD src1_sel:WORD_1\n"
    "v_rcp_f16_e32 v232, v234\n"
    "v_mfma_f32_16x16x32_bf16 v[168:171], a[64:67], v[152:155], v[168:171]\n"
    "s_waitcnt lgkmcnt(2)\n"
    "ds_read_u16 v186, v186\n"
    "ds_read_u16_d16_hi v202, v202\n"
    "v_or_b32_e32 v198, v198, v206\n"
    "v_rcp_f16_sdwa v232, v234 dst_sel:WORD_1 dst_unused:UNUSED_PRESERVE src0_sel:WORD_1\n"
    "v_mfma_f32_16x16x32_bf16 v[172:175], a[108:111], v[144:147], 0\n"
    "s_barrier\n"
    "v_mfma_f32_16x16x32_bf16 v[172:175], a[156:159], v[148:151], v[172:175]\n"
    "ds_read_u16 v187, v187\n"
    "ds_read_u16_d16_hi v203, v203\n"
    "v_cvt_pk_f16_f32 v188, v176, v177\n"
    "v_lshlrev_b32_sdwa v189, 1, v178 dst_sel:DWORD dst_unused:UNUSED_PAD src0_sel:DWORD src1_sel:WORD_1\n"
    "v_lshlrev_b32_sdwa v205, 1, v179 dst_sel:DWORD dst_unused:UNUSED_PAD src0_sel:DWORD src1_sel:WORD_1\n"
    "v_lshlrev_b32_sdwa v190, 1, v180 dst_sel:DWORD dst_unused:UNUSED_PAD src0_sel:DWORD src1_sel:WORD_1\n"
    "v_lshlrev_b32_sdwa v206, 1, v181 dst_sel:DWORD dst_unused:UNUSED_PAD src0_sel:DWORD src1_sel:WORD_1\n"
    "v_lshlrev_b32_sdwa v191, 1, v182 dst_sel:DWORD dst_unused:UNUSED_PAD src0_sel:DWORD src1_sel:WORD_1\n"
    "v_fma_mix_f32 v234, v192, v232, 0 op_sel_hi:[1,1,0]\n"
    "v_mfma_f32_16x16x32_bf16 v[172:175], a[68:71], v[152:155], v[172:175]\n"
    "ds_read_u16 v189, v189\n"
    "v_or_b32_e32 v199, v199, v207\n"
    "v_lshlrev_b32_sdwa v207, 1, v183 dst_sel:DWORD dst_unused:UNUSED_PAD src0_sel:DWORD src1_sel:WORD_1\n"
    "v_fma_mix_f32 v232, v192, v232, 0 op_sel:[1,1,0] op_sel_hi:[1,1,0]\n"
    "v_mfma_f32_16x16x32_bf16 v[176:179], a[104:107], v[156:159], 0\n"
    "s_waitcnt lgkmcnt(5)\n"
    "ds_read_u16_d16_hi v205, v205\n"
    "v_cvt_pk_bf16_f32 v192, v234, v232\n"
    "v_mfma_f32_16x16x32_bf16 v[176:179], a[152:155], v[160:163], v[176:179]\n"
    "ds_read_u16 v190, v190\n"
    "v_pk_fma_f16 v232, v196, v196, s48\n"
    "v_mfma_f32_16x16x32_bf16 v[176:179], a[64:67], v[164:167], v[176:179]\n"
    "ds_read_u16_d16_hi v206, v206\n"
    "v_pk_mul_f16 v232, v196, v232\n"
    "v_mfma_f32_16x16x32_bf16 v[180:183], a[108:111], v[156:159], 0\n"
    "s_waitcnt lgkmcnt(6)\n"
    "ds_read_u16 v191, v191\n"
    "v_exp_f16_e32 v234, v232\n"
    "v_mfma_f32_16x16x32_bf16 v[180:183], a[156:159], v[160:163], v[180:183]\n"
    "ds_read_u16_d16_hi v207, v207\n"
    "v_exp_f16_sdwa v234, v232 dst_sel:WORD_1 dst_unused:UNUSED_PRESERVE src0_sel:WORD_1\n"
    "v_mfma_f32_16x16x32_bf16 v[180:183], a[68:71], v[164:167], v[180:183]\n"
    "v_pk_fma_f16 v234, v234, s49, s49\n"
    "v_or_b32_e64 v185, v185, v201\n"
    "v_mfma_f32_16x16x32_bf16 v[96:99], a[16:19], v[192:195], v[96:99]\n"
    "v_rcp_f16_e32 v232, v234\n"
    "v_mfma_f32_16x16x32_bf16 v[100:103], a[20:23], v[192:195], v[100:103]\n"
    "s_waitcnt lgkmcnt(6)\n"
    "v_rcp_f16_sdwa v232, v234 dst_sel:WORD_1 dst_unused:UNUSED_PRESERVE src0_sel:WORD_1\n"
    "v_mfma_f32_16x16x32_bf16 v[112:115], a[24:27], v[192:195], v[112:115]\n"
    "v_fma_mix_f32 v234, v196, v232, 0 op_sel_hi:[1,1,0]\n"
    "v_fma_mix_f32 v232, v196, v232, 0 op_sel:[1,1,0] op_sel_hi:[1,1,0]\n"
    "v_mfma_f32_16x16x32_bf16 v[116:119], a[28:31], v[192:195], v[116:119]\n"
    "v_or_b32_e64 v186, v186, v202\n"
    "v_cvt_pk_bf16_f32 v196, v234, v232\n"
    "v_mfma_f32_16x16x32_bf16 v[128:131], v[8:11], v[192:195], v[128:131]\n"
    "v_pk_fma_f16 v232, v184, v184, s48\n"
    "v_or_b32_e32 v187, v187, v203\n"
    "v_mfma_f32_16x16x32_bf16 v[132:135], v[12:15], v[192:195], v[132:135]\n"
    "s_waitcnt lgkmcnt(4)\n"
    "v_pk_mul_f16 v232, v184, v232\n"
    "v_or_b32_e64 v189, v189, v205\n"
    "v_mfma_f32_16x16x32_bf16 v[104:107], a[16:19], v[196:199], v[104:107]\n"
    "v_exp_f16_e32 v234, v232\n"
    "v_mfma_f32_16x16x32_bf16 v[108:111], a[20:23], v[196:199], v[108:111]\n"
    "v_exp_f16_sdwa v234, v232 dst_sel:WORD_1 dst_unused:UNUSED_PRESERVE src0_sel:WORD_1\n"
    "v_mfma_f32_16x16x32_bf16 v[120:123], a[24:27], v[196:199], v[120:123]\n"
    "s_waitcnt vmcnt(7) lgkmcnt(2)\n"
    "v_pk_fma_f16 v234, v234, s49, s49\n"
    "v_lshlrev_b32_sdwa v192, 1, v168 dst_sel:DWORD dst_unused:UNUSED_PAD src0_sel:DWORD src1_sel:WORD_1\n"
    "v_mfma_f32_16x16x32_bf16 v[124:127], a[28:31], v[196:199], v[124:127]\n"
    "ds_read_u16 v192, v192\n"
    "v_lshlrev_b32_sdwa v200, 1, v169 dst_sel:DWORD dst_unused:UNUSED_PAD src0_sel:DWORD src1_sel:WORD_1\n"
    "v_mfma_f32_16x16x32_bf16 v[136:139], v[8:11], v[196:199], v[136:139]\n"
    "ds_read_u16_d16_hi v200, v200\n"
    "v_lshlrev_b32_sdwa v193, 1, v170 dst_sel:DWORD dst_unused:UNUSED_PAD src0_sel:DWORD src1_sel:WORD_1\n"
    "v_mfma_f32_16x16x32_bf16 v[140:143], v[12:15], v[196:199], v[140:143]\n"
    "s_waitcnt vmcnt(6)\n"
    "ds_read_u16 v193, v193\n"
    "v_lshlrev_b32_sdwa v201, 1, v171 dst_sel:DWORD dst_unused:UNUSED_PAD src0_sel:DWORD src1_sel:WORD_1\n"
    "v_smfmac_f32_32x32x32_bf16 v[112:127], a[248:251], a[232:239], v241\n"
    "s_waitcnt lgkmcnt(3)\n"
    "ds_read_u16_d16_hi v201, v201\n"
    "ds_read_b128 a[232:235], v243 offset:24640\n"
    "v_lshlrev_b32_sdwa v194, 1, v172 dst_sel:DWORD dst_unused:UNUSED_PAD src0_sel:DWORD src1_sel:WORD_1\n"
    "v_lshlrev_b32_sdwa v202, 1, v173 dst_sel:DWORD dst_unused:UNUSED_PAD src0_sel:DWORD src1_sel:WORD_1\n"
    "v_lshlrev_b32_sdwa v195, 1, v174 dst_sel:DWORD dst_unused:UNUSED_PAD src0_sel:DWORD src1_sel:WORD_1\n"
    "v_smfmac_f32_32x32x32_bf16 v[96:111], a[248:251], a[224:231], v241\n"
    "ds_read_u16 v194, v194\n"
    "v_lshlrev_b32_sdwa v203, 1, v175 dst_sel:DWORD dst_unused:UNUSED_PAD src0_sel:DWORD src1_sel:WORD_1\n"
    "v_rcp_f16_e64 v232, v234\n"
    "v_or_b32_e64 v190, v190, v206\n"
    "v_smfmac_f32_32x32x32_bf16 v[128:143], a[248:251], v[208:215], v241\n"
    "ds_read_u16_d16_hi v202, v202\n"
    "ds_read_b128 a[236:239], v243 offset:25664\n"
    "v_rcp_f16_sdwa v232, v234 dst_sel:WORD_1 dst_unused:UNUSED_PRESERVE src0_sel:WORD_1\n"
    "v_lshlrev_b32_sdwa v196, 1, v176 dst_sel:DWORD dst_unused:UNUSED_PAD src0_sel:DWORD src1_sel:WORD_1\n"
    "v_mfma_f32_16x16x32_bf16 v[168:171], a[112:115], v[144:147], 0\n"
    "ds_read_u16 v195, v195\n"
    "v_lshlrev_b32_sdwa v204, 1, v177 dst_sel:DWORD dst_unused:UNUSED_PAD src0_sel:DWORD src1_sel:WORD_1\n"
    "v_mfma_f32_16x16x32_bf16 v[168:171], a[160:163], v[148:151], v[168:171]\n"
    "v_lshlrev_b32_sdwa v197, 1, v178 dst_sel:DWORD dst_unused:UNUSED_PAD src0_sel:DWORD src1_sel:WORD_1\n"
    "v_lshlrev_b32_sdwa v205, 1, v179 dst_sel:DWORD dst_unused:UNUSED_PAD src0_sel:DWORD src1_sel:WORD_1\n"
    "v_mfma_f32_16x16x32_bf16 v[168:171], a[72:75], v[152:155], v[168:171]\n"
    "ds_read_u16_d16_hi v203, v203\n"
    "v_lshlrev_b32_sdwa v198, 1, v180 dst_sel:DWORD dst_unused:UNUSED_PAD src0_sel:DWORD src1_sel:WORD_1\n"
    "v_mfma_f32_16x16x32_bf16 v[172:175], a[116:119], v[144:147], 0\n"
    "ds_read_u16 v196, v196\n"
    "v_lshlrev_b32_sdwa v206, 1, v181 dst_sel:DWORD dst_unused:UNUSED_PAD src0_sel:DWORD src1_sel:WORD_1\n"
    "v_mfma_f32_16x16x32_bf16 v[172:175], a[164:167], v[148:151], v[172:175]\n"
    "v_lshlrev_b32_sdwa v199, 1, v182 dst_sel:DWORD dst_unused:UNUSED_PAD src0_sel:DWORD src1_sel:WORD_1\n"
    "v_fma_mix_f32 v234, v184, v232, 0 op_sel_hi:[1,1,0]\n"
    "v_mfma_f32_16x16x32_bf16 v[172:175], v[52:55], v[152:155], v[172:175]\n"
    "s_waitcnt lgkmcnt(8)\n"
    "ds_read_u16_d16_hi v204, v204\n"
    "v_or_b32_e32 v191, v191, v207\n"
    "v_lshlrev_b32_sdwa v207, 1, v183 dst_sel:DWORD dst_unused:UNUSED_PAD src0_sel:DWORD src1_sel:WORD_1\n"
    "v_fma_mix_f32 v232, v184, v232, 0 op_sel:[1,1,0] op_sel_hi:[1,1,0]\n"
    "v_mfma_f32_16x16x32_bf16 v[176:179], a[112:115], v[156:159], 0\n"
    "ds_read_u16 v197, v197\n"
    "v_cvt_pk_bf16_f32 v184, v234, v232\n"
    "v_mfma_f32_16x16x32_bf16 v[176:179], a[160:163], v[160:163], v[176:179]\n"
    "v_pk_fma_f16 v232, v188, v188, s48\n"
    "v_or_b32_e32 v192, v192, v200\n"
    "v_mfma_f32_16x16x32_bf16 v[176:179], a[72:75], v[164:167], v[176:179]\n"
    "s_waitcnt lgkmcnt(8)\n"
    "ds_read_u16_d16_hi v205, v205\n"
    "v_pk_mul_f16 v232, v188, v232\n"
    "v_mfma_f32_16x16x32_bf16 v[180:183], a[116:119], v[156:159], 0\n"
    "v_exp_f16_e32 v234, v232\n"
    "v_mfma_f32_16x16x32_bf16 v[180:183], a[164:167], v[160:163], v[180:183]\n"
    "s_waitcnt lgkmcnt(6)\n"
    "ds_read_u16 v198, v198\n"
    "v_exp_f16_sdwa v234, v232 dst_sel:WORD_1 dst_unused:UNUSED_PRESERVE src0_sel:WORD_1\n"
    "v_smfmac_f32_32x32x32_bf16 v[96:111], a[248:251], a[232:239], v241\n"
    "ds_read_u16_d16_hi v206, v206\n"
    "v_pk_fma_f16 v234, v234, s49, s49\n"
    "v_or_b32_e64 v193, v193, v201\n"
    "v_rcp_f16_e64 v232, v234\n"
    "v_mfma_f32_16x16x32_bf16 v[180:183], v[52:55], v[164:167], v[180:183]\n"
    "ds_read_u16 v199, v199\n"
    "v_rcp_f16_sdwa v232, v234 dst_sel:WORD_1 dst_unused:UNUSED_PRESERVE src0_sel:WORD_1\n"
    "v_mfma_f32_16x16x32_bf16 v[112:115], a[40:43], v[184:187], v[112:115]\n"
    "s_nop 0\n"
    "v_fma_mix_f32 v234, v188, v232, 0 op_sel_hi:[1,1,0]\n"
    "v_fma_mix_f32 v232, v188, v232, 0 op_sel:[1,1,0] op_sel_hi:[1,1,0]\n"
    "v_mfma_f32_16x16x32_bf16 v[96:99], a[32:35], v[184:187], v[96:99]\n"
    "s_waitcnt lgkmcnt(7)\n"
    "ds_read_u16_d16_hi v207, v207\n"
    "v_cvt_pk_bf16_f32 v188, v234, v232\n"
    "v_mfma_f32_16x16x32_bf16 v[100:103], a[36:39], v[184:187], v[100:103]\n"
    "ds_read_b128 a[232:235], v243 offset:26688\n"
    "v_lshlrev_b32_sdwa v201, 1, v171 dst_sel:DWORD dst_unused:UNUSED_PAD src0_sel:DWORD src1_sel:WORD_1\n"
    "v_mfma_f32_16x16x32_bf16 v[116:119], a[44:47], v[184:187], v[116:119]\n"
    "ds_read_b128 a[236:239], v243 offset:27712\n"
    "v_or_b32_e32 v194, v194, v202\n"
    "v_mfma_f32_16x16x32_bf16 v[128:131], v[16:19], v[184:187], v[128:131]\n"
    "s_waitcnt lgkmcnt(8)\n"
    "ds_read_u16_d16_hi v201, v201\n"
    "v_lshlrev_b32_sdwa v202, 1, v173 dst_sel:DWORD dst_unused:UNUSED_PAD src0_sel:DWORD src1_sel:WORD_1\n"
    "v_mfma_f32_16x16x32_bf16 v[132:135], v[20:23], v[184:187], v[132:135]\n"
    "v_cvt_pk_f16_f32 v184, v168, v169\n"
    "v_lshlrev_b32_sdwa v185, 1, v170 dst_sel:DWORD dst_unused:UNUSED_PAD src0_sel:DWORD src1_sel:WORD_1\n"
    "v_mfma_f32_16x16x32_bf16 v[104:107], a[32:35], v[188:191], v[104:107]\n"
    "ds_read_u16 v185, v185\n"
    "v_lshlrev_b32_sdwa v186, 1, v172 dst_sel:DWORD dst_unused:UNUSED_PAD src0_sel:DWORD src1_sel:WORD_1\n"
    "v_mfma_f32_16x16x32_bf16 v[108:111], a[36:39], v[188:191], v[108:111]\n"
    "v_lshlrev_b32_sdwa v187, 1, v174 dst_sel:DWORD dst_unused:UNUSED_PAD src0_sel:DWORD src1_sel:WORD_1\n"
    "v_or_b32_e32 v195, v195, v203\n"
    "v_mfma_f32_16x16x32_bf16 v[120:123], a[40:43], v[188:191], v[120:123]\n"
    "s_waitcnt lgkmcnt(8)\n"
    "ds_read_u16 v186, v186\n"
    "v_lshlrev_b32_sdwa v203, 1, v175 dst_sel:DWORD dst_unused:UNUSED_PAD src0_sel:DWORD src1_sel:WORD_1\n"
    "v_mfma_f32_16x16x32_bf16 v[124:127], a[44:47], v[188:191], v[124:127]\n"
    "v_or_b32_e64 v196, v196, v204\n"
    "v_or_b32_e32 v197, v197, v205\n"
    "v_mfma_f32_16x16x32_bf16 v[136:139], v[16:19], v[188:191], v[136:139]\n"
    "ds_read_u16_d16_hi v202, v202\n"
    "v_lshlrev_b32_sdwa v205, 1, v179 dst_sel:DWORD dst_unused:UNUSED_PAD src0_sel:DWORD src1_sel:WORD_1\n"
    "v_mfma_f32_16x16x32_bf16 v[140:143], v[20:23], v[188:191], v[140:143]\n"
    "s_waitcnt lgkmcnt(8)\n"
    "ds_read_u16 v187, v187\n"
    "v_cvt_pk_f16_f32 v188, v176, v177\n"
    "v_mfma_f32_16x16x32_bf16 v[168:171], a[120:123], v[144:147], 0\n"
    "v_lshlrev_b32_sdwa v189, 1, v178 dst_sel:DWORD dst_unused:UNUSED_PAD src0_sel:DWORD src1_sel:WORD_1\n"
    "v_lshlrev_b32_sdwa v190, 1, v180 dst_sel:DWORD dst_unused:UNUSED_PAD src0_sel:DWORD src1_sel:WORD_1\n"
    "v_mfma_f32_16x16x32_bf16 v[168:171], a[168:171], v[148:151], v[168:171]\n"
    "ds_read_u16_d16_hi v203, v203\n"
    "v_lshlrev_b32_sdwa v191, 1, v182 dst_sel:DWORD dst_unused:UNUSED_PAD src0_sel:DWORD src1_sel:WORD_1\n"
    "v_mfma_f32_16x16x32_bf16 v[168:171], a[76:79], v[152:155], v[168:171]\n"
    "v_or_b32_e64 v198, v198, v206\n"
    "v_lshlrev_b32_sdwa v206, 1, v181 dst_sel:DWORD dst_unused:UNUSED_PAD src0_sel:DWORD src1_sel:WORD_1\n"
    "v_mfma_f32_16x16x32_bf16 v[172:175], a[124:127], v[144:147], 0\n"
    "s_waitcnt lgkmcnt(6)\n"
    "ds_read_u16 v189, v189\n"
    "v_or_b32_e32 v199, v199, v207\n"
    "v_smfmac_f32_32x32x32_bf16 v[112:127], a[248:251], a[232:239], v241\n"
    "ds_read_b128 a[232:235], v243 offset:28736\n"
    "v_lshlrev_b32_sdwa v207, 1, v183 dst_sel:DWORD dst_unused:UNUSED_PAD src0_sel:DWORD src1_sel:WORD_1\n"
    "v_pk_fma_f16 v232, v184, v184, s48\n"
    "v_pk_mul_f16 v232, v184, v232\n"
    "v_exp_f16_e64 v234, v232\n"
    "v_mfma_f32_16x16x32_bf16 v[172:175], a[172:175], v[148:151], v[172:175]\n"
    "ds_read_u16_d16_hi v205, v205\n"
    "v_exp_f16_sdwa v234, v232 dst_sel:WORD_1 dst_unused:UNUSED_PRESERVE src0_sel:WORD_1\n"
    "v_mfma_f32_16x16x32_bf16 v[172:175], v[60:63], v[152:155], v[172:175]\n"
    "ds_read_b128 a[236:239], v243 offset:29760\n"
    "v_pk_fma_f16 v234, v234, s49, s49\n"
    "v_mfma_f32_16x16x32_bf16 v[176:179], a[120:123], v[156:159], 0\n"
    "s_waitcnt lgkmcnt(8)\n"
    "ds_read_u16 v190, v190\n"
    "v_or_b32_e32 v185, v185, v201\n"
    "v_mfma_f32_16x16x32_bf16 v[176:179], a[168:171], v[160:163], v[176:179]\n"
    "v_lshlrev_b32_sdwa v201, 1, v171 dst_sel:DWORD dst_unused:UNUSED_PAD src0_sel:DWORD src1_sel:WORD_1\n"
    "v_rcp_f16_e64 v232, v234\n"
    "v_mfma_f32_16x16x32_bf16 v[176:179], a[76:79], v[164:167], v[176:179]\n"
    "ds_read_u16_d16_hi v206, v206\n"
    "v_rcp_f16_sdwa v232, v234 dst_sel:WORD_1 dst_unused:UNUSED_PRESERVE src0_sel:WORD_1\n"
    "v_mfma_f32_16x16x32_bf16 v[180:183], a[124:127], v[156:159], 0\n"
    "s_waitcnt lgkmcnt(8)\n"
    "ds_read_u16 v191, v191\n"
    "v_or_b32_e32 v186, v186, v202\n"
    "v_mfma_f32_16x16x32_bf16 v[180:183], a[172:175], v[160:163], v[180:183]\n"
    "v_lshlrev_b32_sdwa v202, 1, v173 dst_sel:DWORD dst_unused:UNUSED_PAD src0_sel:DWORD src1_sel:WORD_1\n"
    "v_fma_mix_f32 v234, v184, v232, 0 op_sel_hi:[1,1,0]\n"
    "v_mfma_f32_16x16x32_bf16 v[180:183], v[60:63], v[164:167], v[180:183]\n"
    "ds_read_u16_d16_hi v207, v207\n"
    "v_fma_mix_f32 v232, v184, v232, 0 op_sel:[1,1,0] op_sel_hi:[1,1,0]\n"
    "v_mfma_f32_16x16x32_bf16 v[96:99], a[176:179], v[192:195], v[96:99]\n"
    "s_waitcnt lgkmcnt(8)\n"
    "ds_read_u16_d16_hi v201, v201\n"
    "v_or_b32_e32 v187, v187, v203\n"
    "v_mfma_f32_16x16x32_bf16 v[100:103], a[180:183], v[192:195], v[100:103]\n"
    "v_lshlrev_b32_sdwa v203, 1, v175 dst_sel:DWORD dst_unused:UNUSED_PAD src0_sel:DWORD src1_sel:WORD_1\n"
    "v_cvt_pk_bf16_f32 v184, v234, v232\n"
    "v_mfma_f32_16x16x32_bf16 v[112:115], a[184:187], v[192:195], v[112:115]\n"
    "ds_read_u16_d16_hi v202, v202\n"
    "v_pk_fma_f16 v232, v188, v188, s48\n"
    "v_mfma_f32_16x16x32_bf16 v[116:119], a[188:191], v[192:195], v[116:119]\n"
    "v_pk_mul_f16 v232, v188, v232\n"
    "v_exp_f16_e32 v234, v232\n"
    "v_mfma_f32_16x16x32_bf16 v[128:131], v[24:27], v[192:195], v[128:131]\n"
    "s_waitcnt lgkmcnt(6)\n"
    "ds_read_u16_d16_hi v203, v203\n"
    "v_or_b32_e64 v189, v189, v205\n"
    "v_mfma_f32_16x16x32_bf16 v[132:135], v[28:31], v[192:195], v[132:135]\n"
    "v_cvt_pk_f16_f32 v192, v168, v169\n"
    "v_lshlrev_b32_sdwa v193, 1, v170 dst_sel:DWORD dst_unused:UNUSED_PAD src0_sel:DWORD src1_sel:WORD_1\n"
    "v_mfma_f32_16x16x32_bf16 v[104:107], a[176:179], v[196:199], v[104:107]\n"
    "ds_read_u16 v193, v193\n"
    "v_lshlrev_b32_sdwa v194, 1, v172 dst_sel:DWORD dst_unused:UNUSED_PAD src0_sel:DWORD src1_sel:WORD_1\n"
    "v_mfma_f32_16x16x32_bf16 v[108:111], a[180:183], v[196:199], v[108:111]\n"
    "ds_read_u16 v194, v194\n"
    "v_lshlrev_b32_sdwa v195, 1, v174 dst_sel:DWORD dst_unused:UNUSED_PAD src0_sel:DWORD src1_sel:WORD_1\n"
    "v_mfma_f32_16x16x32_bf16 v[120:123], a[184:187], v[196:199], v[120:123]\n"
    "v_lshlrev_b32_sdwa v205, 1, v179 dst_sel:DWORD dst_unused:UNUSED_PAD src0_sel:DWORD src1_sel:WORD_1\n"
    "v_exp_f16_sdwa v234, v232 dst_sel:WORD_1 dst_unused:UNUSED_PRESERVE src0_sel:WORD_1\n"
    "v_mfma_f32_16x16x32_bf16 v[124:127], a[188:191], v[196:199], v[124:127]\n"
    "s_waitcnt lgkmcnt(7)\n"
    "ds_read_u16 v195, v195\n"
    "v_or_b32_e32 v190, v190, v206\n"
    "v_mfma_f32_16x16x32_bf16 v[136:139], v[24:27], v[196:199], v[136:139]\n"
    "ds_read_u16_d16_hi v205, v205\n"
    "v_lshlrev_b32_sdwa v206, 1, v181 dst_sel:DWORD dst_unused:UNUSED_PAD src0_sel:DWORD src1_sel:WORD_1\n"
    "v_mfma_f32_16x16x32_bf16 v[140:143], v[28:31], v[196:199], v[140:143]\n"
    "s_nop 0\n"
    "v_cvt_pk_f16_f32 v196, v176, v177\n"
    "v_lshlrev_b32_sdwa v197, 1, v178 dst_sel:DWORD dst_unused:UNUSED_PAD src0_sel:DWORD src1_sel:WORD_1\n"
    "v_mfma_f32_16x16x32_bf16 v[96:99], a[192:195], v[184:187], v[96:99]\n"
    "s_waitcnt lgkmcnt(7)\n"
    "ds_read_u16 v197, v197\n"
    "v_lshlrev_b32_sdwa v198, 1, v180 dst_sel:DWORD dst_unused:UNUSED_PAD src0_sel:DWORD src1_sel:WORD_1\n"
    "v_smfmac_f32_32x32x32_bf16 v[128:143], a[248:251], a[232:239], v241\n"
    "v_lshlrev_b32_sdwa v199, 1, v182 dst_sel:DWORD dst_unused:UNUSED_PAD src0_sel:DWORD src1_sel:WORD_1\n"
    "v_or_b32_e64 v191, v191, v207\n"
    "v_lshlrev_b32_sdwa v207, 1, v183 dst_sel:DWORD dst_unused:UNUSED_PAD src0_sel:DWORD src1_sel:WORD_1\n"
    "v_pk_fma_f16 v234, v234, s49, s49\n"
    "v_rcp_f16_e64 v232, v234\n"
    "v_mfma_f32_16x16x32_bf16 v[100:103], a[196:199], v[184:187], v[100:103]\n"
    "ds_read_u16 v198, v198\n"
    "ds_read_u16_d16_hi v206, v206\n"
    "v_mfma_f32_16x16x32_bf16 v[112:115], a[200:203], v[184:187], v[112:115]\n"
    "v_rcp_f16_sdwa v232, v234 dst_sel:WORD_1 dst_unused:UNUSED_PRESERVE src0_sel:WORD_1\n"
    "v_mfma_f32_16x16x32_bf16 v[116:119], a[204:207], v[184:187], v[116:119]\n"
    "ds_read_u16 v199, v199\n"
    "v_fma_mix_f32 v234, v188, v232, 0 op_sel_hi:[1,1,0]\n"
    "v_mfma_f32_16x16x32_bf16 v[128:131], v[32:35], v[184:187], v[128:131]\n"
    "v_fma_mix_f32 v232, v188, v232, 0 op_sel:[1,1,0] op_sel_hi:[1,1,0]\n"
    "v_cvt_pk_bf16_f32 v188, v234, v232\n"
    "v_mfma_f32_16x16x32_bf16 v[132:135], v[36:39], v[184:187], v[132:135]\n"
    "s_waitcnt lgkmcnt(7)\n"
    "ds_read_u16_d16_hi v207, v207\n"
    "v_or_b32_e32 v193, v193, v201\n"
    "v_mfma_f32_16x16x32_bf16 v[104:107], a[192:195], v[188:191], v[104:107]\n"
    "v_pk_fma_f16 v232, v192, v192, s48\n"
    "v_pk_mul_f16 v232, v192, v232\n"
    "v_mfma_f32_16x16x32_bf16 v[108:111], a[196:199], v[188:191], v[108:111]\n"
    "s_waitcnt lgkmcnt(7)\n"
    "v_or_b32_e32 v194, v194, v202\n"
    "v_exp_f16_e64 v234, v232\n"
    "v_mfma_f32_16x16x32_bf16 v[120:123], a[200:203], v[188:191], v[120:123]\n"
    "s_waitcnt lgkmcnt(6)\n"
    "v_or_b32_e32 v195, v195, v203\n"
    "v_exp_f16_sdwa v234, v232 dst_sel:WORD_1 dst_unused:UNUSED_PRESERVE src0_sel:WORD_1\n"
    "v_mfma_f32_16x16x32_bf16 v[124:127], a[204:207], v[188:191], v[124:127]\n"
    "v_pk_fma_f16 v234, v234, s49, s49\n"
    "v_rcp_f16_e64 v232, v234\n"
    "v_mfma_f32_16x16x32_bf16 v[136:139], v[32:35], v[188:191], v[136:139]\n"
    "v_rcp_f16_sdwa v232, v234 dst_sel:WORD_1 dst_unused:UNUSED_PRESERVE src0_sel:WORD_1\n"
    "v_mfma_f32_16x16x32_bf16 v[140:143], v[36:39], v[188:191], v[140:143]\n"
    "s_waitcnt lgkmcnt(4)\n"
    "v_or_b32_e32 v197, v197, v205\n"
    "v_fma_mix_f32 v234, v192, v232, 0 op_sel_hi:[1,1,0]\n"
    "v_fma_mix_f32 v232, v192, v232, 0 op_sel:[1,1,0] op_sel_hi:[1,1,0]\n"
    "v_cvt_pk_bf16_f32 v192, v234, v232\n"
    "v_pk_fma_f16 v232, v196, v196, s48\n"
    "v_pk_mul_f16 v232, v196, v232\n"
    "v_exp_f16_e64 v234, v232\n"
    "v_mfma_f32_16x16x32_bf16 v[96:99], a[208:211], v[192:195], v[96:99]\n"
    "v_exp_f16_sdwa v234, v232 dst_sel:WORD_1 dst_unused:UNUSED_PRESERVE src0_sel:WORD_1\n"
    "v_mfma_f32_16x16x32_bf16 v[100:103], a[212:215], v[192:195], v[100:103]\n"
    "s_waitcnt lgkmcnt(2)\n"
    "v_or_b32_e32 v198, v198, v206\n"
    "v_pk_fma_f16 v234, v234, s49, s49\n"
    "v_rcp_f16_e64 v232, v234\n"
    "v_mfma_f32_16x16x32_bf16 v[112:115], a[216:219], v[192:195], v[112:115]\n"
    "v_rcp_f16_sdwa v232, v234 dst_sel:WORD_1 dst_unused:UNUSED_PRESERVE src0_sel:WORD_1\n"
    "v_mfma_f32_16x16x32_bf16 v[116:119], a[220:223], v[192:195], v[116:119]\n"
    "v_fma_mix_f32 v234, v196, v232, 0 op_sel_hi:[1,1,0]\n"
    "v_fma_mix_f32 v232, v196, v232, 0 op_sel:[1,1,0] op_sel_hi:[1,1,0]\n"
    "v_mfma_f32_16x16x32_bf16 v[128:131], v[40:43], v[192:195], v[128:131]\n"
    "s_waitcnt lgkmcnt(0)\n"
    "v_or_b32_e32 v199, v199, v207\n"
    "v_cvt_pk_bf16_f32 v196, v234, v232\n"
    "v_mfma_f32_16x16x32_bf16 v[132:135], v[44:47], v[192:195], v[132:135]\n"
    "v_cvt_pk_bf16_f32 v96, v96, v97\n"
    "v_cvt_pk_bf16_f32 v97, v98, v99\n"
    "v_mfma_f32_16x16x32_bf16 v[104:107], a[208:211], v[196:199], v[104:107]\n"
    "v_cvt_pk_bf16_f32 v98, v100, v101\n"
    "v_cvt_pk_bf16_f32 v99, v102, v103\n"
    "v_mfma_f32_16x16x32_bf16 v[108:111], a[212:215], v[196:199], v[108:111]\n"
    "ds_write_b128 v243, v[96:99] offset:24640\n"
    "v_mfma_f32_16x16x32_bf16 v[120:123], a[216:219], v[196:199], v[120:123]\n"
    "v_cvt_pk_bf16_f32 v112, v112, v113\n"
    "v_cvt_pk_bf16_f32 v113, v114, v115\n"
    "v_mfma_f32_16x16x32_bf16 v[124:127], a[220:223], v[196:199], v[124:127]\n"
    "v_cvt_pk_bf16_f32 v114, v116, v117\n"
    "v_cvt_pk_bf16_f32 v115, v118, v119\n"
    "v_mfma_f32_16x16x32_bf16 v[136:139], v[40:43], v[196:199], v[136:139]\n"
    "ds_read_b128 a[224:227], v249 offset:0\n"
    "ds_write_b128 v243, v[112:115] offset:26688\n"
    "v_mfma_f32_16x16x32_bf16 v[140:143], v[44:47], v[196:199], v[140:143]\n"
    "v_cvt_pk_bf16_f32 v128, v128, v129\n"
    "v_cvt_pk_bf16_f32 v129, v130, v131\n"
    "v_cvt_pk_bf16_f32 v130, v132, v133\n"
    "v_cvt_pk_bf16_f32 v131, v134, v135\n"
    "v_cvt_pk_bf16_f32 v104, v104, v105\n"
    "v_cvt_pk_bf16_f32 v105, v106, v107\n"
    "v_cvt_pk_bf16_f32 v106, v108, v109\n"
    "ds_read_b128 a[228:231], v249 offset:2048\n"
    "ds_write_b128 v243, v[128:131] offset:28736\n"
    "v_cvt_pk_bf16_f32 v107, v110, v111\n"
    "v_cvt_pk_bf16_f32 v120, v120, v121\n"
    "v_cvt_pk_bf16_f32 v121, v122, v123\n"
    "v_cvt_pk_bf16_f32 v122, v124, v125\n"
    "v_cvt_pk_bf16_f32 v123, v126, v127\n"
    "v_cvt_pk_bf16_f32 v136, v136, v137\n"
    "v_cvt_pk_bf16_f32 v137, v138, v139\n"
    "v_cvt_pk_bf16_f32 v138, v140, v141\n"
    "ds_read_b128 a[232:235], v249 offset:4096\n"
    "s_waitcnt lgkmcnt(4)\n"
    "buffer_store_dwordx4 a[224:227], v255, s[44:47], s58 offen offset:0 sc1\n"
    "ds_write_b128 v243, v[104:107] offset:25664\n"
    "v_cvt_pk_bf16_f32 v139, v142, v143\n"
    "ds_read_b128 a[236:239], v249 offset:1024\n"
    "ds_write_b128 v243, v[120:123] offset:27712\n"
    "ds_read_b128 a[252:255], v249 offset:3072\n"
    "ds_write_b128 v243, v[136:139] offset:29760\n"
    "s_waitcnt lgkmcnt(4)\n"
    "buffer_store_dwordx4 a[228:231], v255, s[44:47], s58 offen offset:64 sc1\n"
    "ds_read_b128 a[240:243], v249 offset:5120\n"
    "buffer_store_dwordx4 a[232:235], v255, s[44:47], s58 offen offset:128 sc1\n"
    "s_waitcnt lgkmcnt(3)\n"
    "buffer_store_dwordx4 a[236:239], v255, s[44:47], s89 offen offset:0 sc1\n"
    "s_waitcnt lgkmcnt(2)\n"
    "buffer_store_dwordx4 a[252:255], v255, s[44:47], s89 offen offset:64 sc1\n"
    "s_waitcnt lgkmcnt(0)\n"
    "buffer_store_dwordx4 a[240:243], v255, s[44:47], s89 offen offset:128 sc1\n"
    "97:\n"
    "98:\n"
    "s_waitcnt vmcnt(0) lgkmcnt(0)\n"
    ".Lreplk_end:\n"
    : "+{s66}"(flg), "+{v208}"(tid), "+{s65}"(blk), "+{s[98:99]}"(ka)
    : "{s[96:97]}"(CTAB)
    : "memory", "scc", "vcc", "m0", "v0", "v1", "v2", "v3", "v4", "v5", "v6", "v7", "v8", "v9", "v10", "v11", "v12", "v13", "v14", "v15", "v16", "v17", "v18", "v19", "v20", "v21", "v22", "v23", "v24", "v25", "v26", "v27", "v28", "v29", "v30", "v31", "v32", "v33", "v34", "v35", "v36", "v37", "v38", "v39", "v40", "v41", "v42", "v43", "v44", "v45", "v46", "v47", "v48", "v49", "v50", "v51", "v52", "v53", "v54", "v55", "v56", "v57", "v58", "v59", "v60", "v61", "v62", "v63", "v64", "v65", "v66", "v67", "v68", "v69", "v70", "v71", "v72", "v73", "v74", "v75", "v76", "v77", "v78", "v79", "v80", "v81", "v82", "v83", "v84", "v85", "v86", "v87", "v88", "v89", "v90", "v91", "v92", "v93", "v94", "v95", "v96", "v97", "v98", "v99", "v100", "v101", "v102", "v103", "v104", "v105", "v106", "v107", "v108", "v109", "v110", "v111", "v112", "v113", "v114", "v115", "v116", "v117", "v118", "v119", "v120", "v121", "v122", "v123", "v124", "v125", "v126", "v127", "v128", "v129", "v130", "v131", "v132", "v133", "v134", "v135", "v136", "v137", "v138", "v139", "v140", "v141", "v142", "v143", "v144", "v145", "v146", "v147", "v148", "v149", "v150", "v151", "v152", "v153", "v154", "v155", "v156", "v157", "v158", "v159", "v160", "v161", "v162", "v163", "v164", "v165", "v166", "v167", "v168", "v169", "v170", "v171", "v172", "v173", "v174", "v175", "v176", "v177", "v178", "v179", "v180", "v181", "v182", "v183", "v184", "v185", "v186", "v187", "v188", "v189", "v190", "v191", "v192", "v193", "v194", "v195", "v196", "v197", "v198", "v199", "v200", "v201", "v202", "v203", "v204", "v205", "v206", "v207", "v209", "v210", "v211", "v212", "v213", "v214", "v215", "v216", "v217", "v218", "v219", "v220", "v221", "v222", "v223", "v224", "v225", "v226", "v227", "v228", "v229", "v230", "v231", "v232", "v233", "v234", "v235", "v236", "v237", "v238", "v239", "v240", "v241", "v242", "v243", "v244", "v245", "v246", "v247", "v248", "v249", "v250", "v251", "v252", "v253", "v254", "v255", "a0", "a1", "a2", "a3", "a4", "a5", "a6", "a7", "a8", "a9", "a10", "a11", "a12", "a13", "a14", "a15", "a16", "a17", "a18", "a19", "a20", "a21", "a22", "a23", "a24", "a25", "a26", "a27", "a28", "a29", "a30", "a31", "a32", "a33", "a34", "a35", "a36", "a37", "a38", "a39", "a40", "a41", "a42", "a43", "a44", "a45", "a46", "a47", "a48", "a49", "a50", "a51", "a52", "a53", "a54", "a55", "a56", "a57", "a58", "a59", "a60", "a61", "a62", "a63", "a64", "a65", "a66", "a67", "a68", "a69", "a70", "a71", "a72", "a73", "a74", "a75", "a76", "a77", "a78", "a79", "a80", "a81", "a82", "a83", "a84", "a85", "a86", "a87", "a88", "a89", "a90", "a91", "a92", "a93", "a94", "a95", "a96", "a97", "a98", "a99", "a100", "a101", "a102", "a103", "a104", "a105", "a106", "a107", "a108", "a109", "a110", "a111", "a112", "a113", "a114", "a115", "a116", "a117", "a118", "a119", "a120", "a121", "a122", "a123", "a124", "a125", "a126", "a127", "a128", "a129", "a130", "a131", "a132", "a133", "a134", "a135", "a136", "a137", "a138", "a139", "a140", "a141", "a142", "a143", "a144", "a145", "a146", "a147", "a148", "a149", "a150", "a151", "a152", "a153", "a154", "a155", "a156", "a157", "a158", "a159", "a160", "a161", "a162", "a163", "a164", "a165", "a166", "a167", "a168", "a169", "a170", "a171", "a172", "a173", "a174", "a175", "a176", "a177", "a178", "a179", "a180", "a181", "a182", "a183", "a184", "a185", "a186", "a187", "a188", "a189", "a190", "a191", "a192", "a193", "a194", "a195", "a196", "a197", "a198", "a199", "a200", "a201", "a202", "a203", "a204", "a205", "a206", "a207", "a208", "a209", "a210", "a211", "a212", "a213", "a214", "a215", "a216", "a217", "a218", "a219", "a220", "a221", "a222", "a223", "a224", "a225", "a226", "a227", "a228", "a229", "a230", "a231", "a232", "a233", "a234", "a235", "a236", "a237", "a238", "a239", "a240", "a241", "a242", "a243", "a244", "a245", "a246", "a247", "a248", "a249", "a250", "a251", "a252", "a253", "a254", "a255", "s36", "s37", "s38", "s39", "s40", "s41", "s42", "s43", "s44", "s45", "s46", "s47", "s48", "s49", "s50", "s51", "s52", "s53", "s54", "s55", "s56", "s57", "s58", "s59", "s60", "s61", "s62", "s63", "s64", "s67", "s68", "s69", "s70", "s71", "s72", "s73", "s74", "s75", "s76", "s77", "s78", "s79", "s80", "s81", "s82", "s83", "s84", "s85", "s86", "s87", "s88", "s89", "s90", "s91", "s92", "s93", "s94", "s95");
  if (flg != 2u) {
    const int l2 = __builtin_amdgcn_mbcnt_hi(~0u, __builtin_amdgcn_mbcnt_lo(~0u, 0u));
    slow_path(x, dwl, dws, w1, w2, out, (float*)smem, w * 64 + l2, blockIdx.x >> 3, (blockIdx.x & 7) * 4);
  }
}

extern "C" void kernel_launch(void* const* d_in, const int* in_sizes, int n_in, void* d_out, int out_size, void* d_ws,
                              size_t ws_size, hipStream_t stream) {
  replk_kernel<<<256, 256, 0, stream>>>((const u16*)d_in[0], (const u16*)d_in[1], (const u16*)d_in[2], (const u16*)d_in[3],
                                        (const u16*)d_in[4], (u16*)d_out
                                        );
}
```

```cpp
#include <hip/hip_runtime.h>
#include <hip/hip_bf16.h>

typedef unsigned u32;
typedef unsigned short u16;
typedef unsigned long long u64;
typedef __bf16 bf16x2 __attribute__((ext_vector_type(2)));
typedef float f32x2 __attribute__((ext_vector_type(2)));
#define DI __device__ __forceinline__

constexpr int IMG = 512;
constexpr int CH = 96;
constexpr int HID = 384;
constexpr int PXB = 192;
constexpr int ROWB = 98304;
constexpr int TOEP_CS = 336;
constexpr int XT_RS = 320;
constexpr int XT_PP = 1360;
constexpr int XT_CG = 6752;
constexpr int GS = 6160;
constexpr int L_LUT = 0;
constexpr int L_TOEP = 8208;
constexpr int L_W = 40960;
constexpr int L_LUTN = 65536;
constexpr int L_XT = 81920;
constexpr int L_H = 108928;
constexpr int L_EX = 133568;
constexpr int XT_Q = 27024;
constexpr int L_MISC = 162992;
constexpr int L_TOTAL = 163056;
constexpr int LUTK = 106;
constexpr float GC1 = -2.302208198144325f, GC3 = -0.1029432395800235f;
__device__ const u32 CTAB[3 * 1024] = {
671109120,671098880,134490112,673187840,671372952,671229956,145500164,673318996,671636784,671361032,156510216,673450152,671900616,671492108,167520268,673581308,672164452,684206128,178530352,686295376,672428284,684337204,189540404,686426532,672692116,684468280,200550456,686557688,672955948,684599356,211560508,686688844,673219784,697313376,222570592,699402912,673483616,697444452,233580644,699534068,673747448,697575528,244590696,699665224,674011280,697706604,255600748,699796380,674254849,710420624,266610832,712510448,674518681,710551700,277620884,712641604,674782513,710682776,288630936,712772760,675046345,710813852,299640988,712903916,
684223077,723527872,310651072,721423680,684486909,723658948,321661124,721554836,683702165,723790024,332671176,721685992,683965997,723921100,343681228,721817148,685278409,736635120,354691312,734531216,685542241,736766196,365701364,734662372,684757497,736897272,376711416,734793528,685021329,737028348,387721468,734924684,686313474,749742368,398731552,747638752,686577306,749873444,409741604,747769908,685792562,750004520,420751656,747901064,686056394,750135596,431761708,748032220,687368806,762849616,442771792,760746288,687632638,762980692,453781844,760877444,686847894,763111768,464791896,761008600,687111726,763242844,475801948,761139756,
697337034,771762560,486812032,773853824,697600866,771893636,497822084,773984980,697864698,772024712,508832136,774116136,698128530,772155788,519842188,774247292,696274947,784869808,530852272,786961360,696538779,785000884,541862324,787092516,696802611,785131960,552872376,787223672,697066443,785263036,563882428,787354828,699427431,797977056,574892512,800068896,699691263,798108132,585902564,800200052,699955095,798239208,596912616,800331208,700218927,798370284,607922668,800462364,698385611,811084304,618932752,813176432,698649443,811215380,629942804,813307588,698913275,811346456,640952856,813438744,699177107,811477532,651962908,813569900,
710430724,824191552,135539264,822089664,710694556,824322628,146549316,822220820,709909812,824453704,157559368,822351976,710173644,824584780,168569420,822483132,709388904,837298800,179579504,835197200,709652736,837429876,190589556,835328356,708867992,837560952,201599608,835459512,709131824,837692028,212609660,835590668,712541388,850406048,223619744,848304736,712805220,850537124,234629796,848435892,712020476,850668200,245639848,848567048,712284308,850799276,256649900,848698204,711479301,863513296,267659984,861412272,711743133,863644372,278670036,861543428,710958389,863775448,289680088,861674584,711222221,863906524,300690140,861805740,
723544681,677394944,311704064,679484672,723808513,677526020,322714116,679615828,724072345,677657096,333724168,679746984,724336177,677788172,344734220,679878140,724600013,690502192,355744304,692592208,724863845,690633268,366754356,692723364,725127677,690764344,377764408,692854520,725391509,690895420,388774460,692985676,721440774,703609440,399784544,705699744,721704606,703740516,410794596,705830900,721968438,703871592,421804648,705962056,722232270,704002668,432814700,706093212,722496106,716716688,443824784,718807280,722759938,716847764,454834836,718938436,723023770,716978840,465844888,719069592,723287602,717109916,476854940,719200748,
736658638,725629632,487865024,731914816,736922470,725760708,498875076,732045972,736137726,725891784,509885128,732177128,736401558,726022860,520895180,732308284,737693703,738736880,531905264,745022352,737957535,738867956,542915316,745153508,737172791,738999032,553925368,745284664,737436623,739130108,564935420,745415820,734554731,751844128,575945504,758129888,734818563,751975204,586955556,758261044,734033819,752106280,597965608,758392200,734297651,752237356,608975660,758523356,735610063,764951376,619985744,771237424,735873895,765082452,630995796,771368580,735089151,765213528,642005848,771499736,735352983,765344604,653015900,771630892,
749752328,778058624,136592256,780150656,750016160,778189700,147602308,780281812,750279992,778320776,158612360,780443436,750543824,778451852,169622412,780574508,748710508,791165872,180632496,793288492,748974340,791296948,191642548,793419564,749238172,791428024,202652600,793550636,749502004,791559100,213662652,793681708,747668688,804273120,224672736,806395692,747932520,804404196,235682788,806526764,748196352,804535272,246692840,806657836,748460184,804666348,257702892,806788908,746606601,817380368,268712976,819502892,746870433,817511444,279723028,819633964,747134265,817642520,290733080,819765036,747398097,817773596,301743132,819896108,
762866285,826293312,312753216,832610092,763130117,826424388,323763268,832741164,762345373,826555464,334773320,832872236,762609205,826686540,345783372,833003308,761824465,839400560,356793456,845717292,762088297,839531636,367803508,845848364,761303553,839662712,378813560,845979436,761567385,839793788,389823612,846110508,760762378,852507808,400833696,858824492,761026210,852638884,411843748,858955564,760241466,852769960,422853800,859086636,760505298,852901036,433863852,859217708,759720558,865615056,444873936,871931692,759984390,865746132,455883988,872062764,759199646,865877208,466894040,872193836,759463478,866008284,477904092,872324908,
771785938,671098882,488908800,673226540,772049770,671229958,499918852,673357612,772313602,671361034,510928904,673488684,772577434,671492110,521938956,673619756,772821003,684206130,532949040,686333740,773084835,684337206,543959092,686464812,773348667,684468282,554969144,686595884,773612499,684599358,565979196,686726956,773876335,697313378,576989280,699440940,774140167,697444454,587999332,699572012,774403999,697575530,599009384,699703084,774667831,697706606,610019436,699834156,774931667,710420626,621029520,712548140,775195499,710551702,632039572,712679212,775459331,710682778,643049624,712810284,775723163,710813854,654059676,712941356,
784879628,723527874,137636032,721461036,785143460,723658950,148646084,721592108,784358716,723790026,159656136,721723180,784622548,723921102,170666188,721854252,785934960,736635122,181676272,734568236,786198792,736766198,192686324,734699308,785414048,736897274,203696376,734830380,785677880,737028350,214706428,734961452,786990292,749742370,225716512,747675436,787254124,749873446,236726564,747806508,786469380,750004522,247736616,747937580,786733212,750135598,258746668,748068652,788025357,762849618,269756752,760782636,788289189,762980694,280766804,760913708,787504445,763111770,291776856,761044780,787768277,763242846,302786908,761175852,
797993585,771762562,313796992,773889836,798257417,771893638,324807044,774020908,798521249,772024714,335817096,774151980,798785081,772155790,346827148,774283052,796951765,784869810,357837232,786997036,797215597,785000886,368847284,787128108,797479429,785131962,379857336,787259180,797743261,785263038,390867388,787390252,800083982,797977058,401877472,800104236,800347814,798108134,412887524,800235308,800611646,798239210,423897576,800366380,800875478,798370286,434907628,800497452,799042162,811084306,445917712,813211436,799305994,811215382,456927764,813342508,799569826,811346458,467937816,813473580,799833658,811477534,478947868,813604652,
811107542,824191554,489957952,822124332,811371374,824322630,500968004,822255404,810586630,824453706,511978056,822386476,810850462,824584782,522988108,822517548,810045455,837298802,533998192,835231532,810309287,837429878,545008244,835362604,809524543,837560954,556018296,835493676,809788375,837692030,567028348,835624748,813197939,850406050,578038432,848338732,813461771,850537126,589048484,848469804,812677027,850668202,600058536,848600876,812940859,850799278,611068588,848731948,812156119,863513298,622078672,861445932,812419951,863644374,633088724,861577004,811635207,863775450,644098776,861708076,811899039,863906526,655108828,861839148,
824201232,677394946,138689024,679517996,824465064,677526022,149699076,679649068,824728896,677657098,160709128,679780140,824992728,677788174,171719180,679911212,825256564,690502194,182729264,692625196,825520396,690633270,193739316,692756268,825784228,690764346,204749368,692887340,826048060,690895422,215759420,693018412,822117592,703609442,226769504,705732396,822381424,703740518,237779556,705863468,822645256,703871594,248789608,705994540,822909088,704002670,259799660,706125612,823152657,716716690,270809744,718839596,823416489,716847766,281819796,718970668,823680321,716978842,292829848,719101740,823944153,717109918,303839900,719232812,
837315189,725629634,314849984,731946796,837579021,725760710,325860036,732077868,836794277,725891786,336870088,732208940,837058109,726022862,347880140,732340012,838370521,738736882,358890224,745053996,838634353,738867958,369900276,745185068,837849609,738999034,380910328,745316140,838113441,739130110,391920380,745447212,835211282,751844130,402930464,758161196,835475114,751975206,413940516,758292268,834690370,752106282,424950568,758423340,834954202,752237358,435960620,758554412,836266614,764951378,446970704,771268396,836530446,765082454,457980756,771399468,835745702,765213530,468990808,771530540,836009534,765344606,480000860,771661612,
850429146,778058626,491010944,780181292,850692978,778189702,502020996,780312364,850956810,778320778,513031048,780443436,851220642,778451854,524041100,780574508,849367059,791165874,535051184,793288492,849630891,791296950,546061236,793419564,849894723,791428026,557071288,793550636,850158555,791559102,568081340,793681708,848325239,804273122,579091424,806395692,848589071,804404198,590101476,806526764,848852903,804535274,601111528,806657836,849116735,804666350,612121580,806788908,847283419,817380370,623131664,819502892,847547251,817511446,634141716,819633964,847811083,817642522,645151768,819765036,848074915,817773598,656161820,819896108,
863541411,826293314,656161856,832610092,863803555,826424390,656161860,832741164,863017123,826555466,656161864,832872236,863279267,826686542,656161868,833003308,862492835,839400562,656161904,845717292,862754979,839531638,656161908,845848364,861968547,839662714,656161912,845979436,862230691,839793790,656161916,846110508,861444259,852507810,656161952,858824492,861706403,852638886,656161956,858955564,860919971,852769962,656161960,859086636,861182115,852901038,656161964,859217708,860395683,865615058,656162000,871931692,860657827,865746134,656162004,872062764,859871395,865877210,656162008,872193836,860133539,866008286,656162012,872324908,
2152998912u,1885629024,1342187520,1342212212,2152998916u,1885891172,1452812292,1364494564,2151688200u,1886153320,1563437064,1386776836,2218797068u,1886415468,1674061836,1409059156,2164598800u,1886677616,1342253072,1342474360,2164598804u,1886939764,1452877844,1364756712,2164271128u,1887201912,1563502616,1387038984,2231379996u,1887464060,1674127388,1409321304,1083254816,1887726208,1342318624,1342998652,1083254820,1887988356,1452943396,1365281004,1082140712,1888250504,1563568168,1387563276,1149249580,1888512652,1674192940,1409845596,1108617264,1888774800,1342384176,1342736512,1108617268,1889036948,1453008948,1365018864,1107306552,1889299096,1563633720,1387301136,1174415420,1889561244,1674258492,1409583456,
269756480,1892969168,1342449728,1342474372,269756484,1893231316,1453074500,1364756692,268445768,1893493464,1563699272,1387038964,335554636,1893755612,1674324044,1409321316,268773456,1891920576,1342515280,1342736520,268773460,1892182724,1453140052,1365018840,268445784,1892444872,1563764824,1387301112,335554652,1892707020,1674389596,1409583464,1343301728,1890871984,1342580832,1343260812,1343301732,1891134132,1453205604,1365543132,1342187624,1891396280,1563830376,1387825404,1409296492,1891658428,1674455148,1410107756,1343498352,1889823392,1342646384,1342998672,1343498356,1890085540,1453271156,1365280992,1342187640,1890347688,1563895928,1387563264,1409296508,1890609836,1674520700,1409845616,
538192000,1895066352,1342711936,1364494484,538192004,1895328500,1453336708,1386776772,536881288,1895590648,1563961480,1409059108,603990156,1895852796,1674586252,1431341364,537208976,1894017760,1342777488,1364756632,537208980,1894279908,1453402260,1387038920,536881304,1894542056,1564027032,1409321256,603990172,1894804204,1674651804,1431603512,1611737248,1897163536,1342843040,1365280924,1611737252,1897425684,1453467812,1387563212,1610623144,1897687832,1564092584,1409845548,1677732012,1897949980,1674717356,1432127804,1611933872,1896114944,1342908592,1365018784,1611933876,1896377092,1453533364,1387301072,1610623160,1896639240,1564158136,1409583408,1677732028,1896901388,1674782908,1431865664,
810821824,1900309312,1342974144,1364756644,810821828,1900571460,1453598916,1387038900,809511112,1900833608,1564223688,1409321236,876619980,1901095756,1674848460,1431603524,822421712,1901357904,1343039696,1365018792,822421716,1901620052,1453664468,1387301048,822094040,1901882200,1564289240,1409583384,889202908,1902144348,1674914012,1431865672,1888561376,1898212128,1343105248,1365543084,1888561380,1898474276,1453730020,1387825340,1887447272,1898736424,1564354792,1410107676,1954556140,1898998572,1674979564,1432389964,1913923824,1899260720,1343170800,1365280944,1913923828,1899522868,1453795572,1387563200,1912613112,1899785016,1564420344,1409845536,1979721980,1900047164,1675045116,1432127824,
2152999168u,1784705124,1347430656,1452835440,2152999172u,1784967272,1458055428,1475117792,2151688456u,1785229420,1568680200,1497400064,2218797324u,1785491568,1679304972,1519682384,2164599056u,1785753716,1347496208,1453097588,2164599060u,1786015864,1458120980,1475379940,2164271384u,1786278012,1568745752,1497662212,2231380252u,1786540160,1679370524,1519944532,1083255072,1786802308,1347561760,1453621880,1083255076,1787064456,1458186532,1475904232,1082140968,1787326604,1568811304,1498186504,1149249836,1787588752,1679436076,1520468824,1108617520,1787850900,1347627312,1453359740,1108617524,1788113048,1458252084,1475642092,1107306808,1788375196,1568876856,1497924364,1174415676,1788637344,1679501628,1520206684,
269756736,1792045268,1347692864,1453097600,269756740,1792307416,1458317636,1475379920,268446024,1792569564,1568942408,1497662192,335554892,1792831712,1679567180,1519944544,268773712,1790996676,1347758416,1453359748,268773716,1791258824,1458383188,1475642068,268446040,1791520972,1569007960,1497924340,335554908,1791783120,1679632732,1520206692,1343301984,1789948084,1347823968,1453884040,1343301988,1790210232,1458448740,1476166360,1342187880,1790472380,1569073512,1498448632,1409296748,1790734528,1679698284,1520730984,1343498608,1788899492,1347889520,1453621900,1343498612,1789161640,1458514292,1475904220,1342187896,1789423788,1569139064,1498186492,1409296764,1789685936,1679763836,1520468844,
538192256,1794142452,1347955072,1475117712,538192260,1794404600,1458579844,1497400000,536881544,1794666748,1569204616,1519682336,603990412,1794928896,1679829388,1541964592,537209232,1793093860,1348020624,1475379860,537209236,1793356008,1458645396,1497662148,536881560,1793618156,1569270168,1519944484,603990428,1793880304,1679894940,1542226740,1611737504,1796239636,1348086176,1475904152,1611737508,1796501784,1458710948,1498186440,1610623400,1796763932,1569335720,1520468776,1677732268,1797026080,1679960492,1542751032,1611934128,1795191044,1348151728,1475642012,1611934132,1795453192,1458776500,1497924300,1610623416,1795715340,1569401272,1520206636,1677732284,1795977488,1680026044,1542488892,
810822080,1799385412,1348217280,1475379872,810822084,1799647560,1458842052,1497662128,809511368,1799909708,1569466824,1519944464,876620236,1800171856,1680091596,1542226752,822421968,1800434004,1348282832,1475642020,822421972,1800696152,1458907604,1497924276,822094296,1800958300,1569532376,1520206612,889203164,1801220448,1680157148,1542488900,1888561632,1797288228,1348348384,1476166312,1888561636,1797550376,1458973156,1498448568,1887447528,1797812524,1569597928,1520730904,1954556396,1798074672,1680222700,1543013192,1913924080,1798336820,1348413936,1475904172,1913924084,1798598968,1459038708,1498186428,1912613368,1798861116,1569663480,1520468764,1979722236,1799123264,1680288252,1542751052,
2152999424u,2087482984,1352673280,1563464828,2152999428u,2087745132,1463298052,1585747180,2151688712u,2088007280,1573922824,1608029452,2218797580u,2088269428,1684547596,1630311772,2164599312u,2088531576,1352738832,1563726976,2164599316u,2088793724,1463363604,1586009328,2164271640u,2089055872,1573988376,1608291600,2231380508u,2089318020,1684613148,1630573920,1083255328,2089580168,1352804384,1564251268,1083255332,2089842316,1463429156,1586533620,1082141224,2090104464,1574053928,1608815892,1149250092,2090366612,1684678700,1631098212,1108617776,2090628760,1352869936,1563989128,1108617780,2090890908,1463494708,1586271480,1107307064,2091153056,1574119480,1608553752,1174415932,2091415204,1684744252,1630836072,
269756992,2094823128,1352935488,1563726988,269756996,2095085276,1463560260,1586009308,268446280,2095347424,1574185032,1608291580,335555148,2095609572,1684809804,1630573932,268773968,2093774536,1353001040,1563989136,268773972,2094036684,1463625812,1586271456,268446296,2094298832,1574250584,1608553728,335555164,2094560980,1684875356,1630836080,1343302240,2092725944,1353066592,1564513428,1343302244,2092988092,1463691364,1586795748,1342188136,2093250240,1574316136,1609078020,1409297004,2093512388,1684940908,1631360372,1343498864,2091677352,1353132144,1564251288,1343498868,2091939500,1463756916,1586533608,1342188152,2092201648,1574381688,1608815880,1409297020,2092463796,1685006460,1631098232,
538192512,2096920312,1353197696,1585747100,538192516,2097182460,1463822468,1608029388,536881800,2097444608,1574447240,1630311724,603990668,2097706756,1685072012,1652593980,537209488,2095871720,1353263248,1586009248,537209492,2096133868,1463888020,1608291536,536881816,2096396016,1574512792,1630573872,603990684,2096658164,1685137564,1652856128,1611737760,2099017496,1353328800,1586533540,1611737764,2099279644,1463953572,1608815828,1610623656,2099541792,1574578344,1631098164,1677732524,2099803940,1685203116,1653380420,1611934384,2097968904,1353394352,1586271400,1611934388,2098231052,1464019124,1608553688,1610623672,2098493200,1574643896,1630836024,1677732540,2098755348,1685268668,1653118280,
810822336,2102163272,1353459904,1586009260,810822340,2102425420,1464084676,1608291516,809511624,2102687568,1574709448,1630573852,876620492,2102949716,1685334220,1652856140,822422224,2103211864,1353525456,1586271408,822422228,2103474012,1464150228,1608553664,822094552,2103736160,1574775000,1630836000,889203420,2103998308,1685399772,1653118288,1888561888,2100066088,1353591008,1586795700,1888561892,2100328236,1464215780,1609077956,1887447784,2100590384,1574840552,1631360292,1954556652,2100852532,1685465324,1653642580,1913924336,2101114680,1353656560,1586533560,1913924340,2101376828,1464281332,1608815816,1912613624,2101638976,1574906104,1631098152,1979722492,2101901124,1685530876,1653380440,
2152999680u,1986559084,1357916416,1674088056,2152999684u,1986821232,1468541188,1696370408,2151688968u,1987083380,1579165960,1718652680,2218797836u,1987345528,1689790732,1740935000,2164599568u,1987607676,1357981968,1674350204,2164599572u,1987869824,1468606740,1696632556,2164271896u,1988131972,1579231512,1718914828,2231380764u,1988394120,1689856284,1741197148,1083255584,1988656268,1358047520,1674874496,1083255588,1988918416,1468672292,1697156848,1082141480,1989180564,1579297064,1719439120,1149250348,1989442712,1689921836,1741721440,1108618032,1989704860,1358113072,1674612356,1108618036,1989967008,1468737844,1696894708,1107307320,1990229156,1579362616,1719176980,1174416188,1990491304,1689987388,1741459300,
269757248,1993899228,1358178624,1674350216,269757252,1994161376,1468803396,1696632536,268446536,1994423524,1579428168,1718914808,335555404,1994685672,1690052940,1741197160,268774224,1992850636,1358244176,1674612364,268774228,1993112784,1468868948,1696894684,268446552,1993374932,1579493720,1719176956,335555420,1993637080,1690118492,1741459308,1343302496,1991802044,1358309728,1675136656,1343302500,1992064192,1468934500,1697418976,1342188392,1992326340,1579559272,1719701248,1409297260,1992588488,1690184044,1741983600,1343499120,1990753452,1358375280,1674874516,1343499124,1991015600,1469000052,1697156836,1342188408,1991277748,1579624824,1719439108,1409297276,1991539896,1690249596,1741721460,
538192768,1995996412,1358440832,1696370328,538192772,1996258560,1469065604,1718652616,536882056,1996520708,1579690376,1740934952,603990924,1996782856,1690315148,1763217208,537209744,1994947820,1358506384,1696632476,537209748,1995209968,1469131156,1718914764,536882072,1995472116,1579755928,1741197100,603990940,1995734264,1690380700,1763479356,1611738016,1998093596,1358571936,1697156768,1611738020,1998355744,1469196708,1719439056,1610623912,1998617892,1579821480,1741721392,1677732780,1998880040,1690446252,1764003648,1611934640,1997045004,1358637488,1696894628,1611934644,1997307152,1469262260,1719176916,1610623928,1997569300,1579887032,1741459252,1677732796,1997831448,1690511804,1763741508,
810822592,2001239372,1358703040,1696632488,810822596,2001501520,1469327812,1718914744,809511880,2001763668,1579952584,1741197080,876620748,2002025816,1690577356,1763479368,822422480,2002287964,1358768592,1696894636,822422484,2002550112,1469393364,1719176892,822094808,2002812260,1580018136,1741459228,889203676,2003074408,1690642908,1763741516,1888562144,1999142188,1358834144,1697418928,1888562148,1999404336,1469458916,1719701184,1887448040,1999666484,1580083688,1741983520,1954556908,1999928632,1690708460,1764265808,1913924592,2000190780,1358899696,1697156788,1913924596,2000452928,1469524468,1719439044,1912613880,2000715076,1580149240,1741721380,1979722748,2000977224,1690774012,1764003668,
136579076,1343253088,52428,12582912,135530496,1453879396,843229,12648449,134481920,1564505704,1634030,12713986,145229824,1675132012,2411724,12779523,136841224,1343318640,3198156,13369356,135792808,1453944948,3988957,13434893,134744232,1564571256,4779758,13500430,139724968,1675197564,5557452,13565967,137103372,1343384208,6343884,14155800,136054952,1454010516,7134685,14221337,135006376,1564636824,7925486,14286874,139724968,1675263132,8703180,14352411,137365520,1343449728,9489612,14942244,136316928,1454076036,10280413,15007781,135268352,1564702344,11071214,15073318,145229824,1675328652,11848908,15138855,
139724868,1365535332,117964,12582960,139724852,1476161640,908765,12648497,138676260,1586787948,1699566,12714034,137627668,1697414256,2477260,12779571,145229896,1365600884,3263692,13369404,145229880,1476227192,4054493,13434941,138938408,1586853500,4845294,13500478,137889816,1697479808,5622988,13566015,145229900,1365666452,6409420,14155848,145229884,1476292760,7200221,14221385,139200556,1586919068,7991022,14286922,138151964,1697545376,8768716,14352459,139724880,1365731972,9555148,14942292,139724864,1476358280,10345949,15007829,139462704,1586984588,11136750,15073366,138414112,1697610896,11914444,15138903,
137627668,1387817576,183500,12583008,136579076,1498443884,974301,12648545,135530496,1609070192,1765102,12714082,134481920,1719696500,2542796,12779619,137889816,1387883128,3329228,13369452,136841224,1498509436,4120029,13434989,135792808,1609135744,4910830,13500526,134744232,1719762052,5688524,13566063,138151964,1387948696,6474956,14155896,137103372,1498575004,7265757,14221433,136054952,1609201312,8056558,14286970,135006376,1719827620,8834252,14352507,138414112,1388014216,9620684,14942340,137365520,1498640524,10411485,15007877,136316928,1609266832,11202286,15073414,135268352,1719893140,11979980,15138951,
139724968,1410099820,249036,12583056,139724868,1520726128,1039837,12648593,139724852,1631352436,1830638,12714130,138676260,1741978744,2608332,12779667,145229824,1410165372,3394764,13369500,145229896,1520791680,4185565,13435037,145229880,1631417988,4976366,13500574,138938408,1742044296,5754060,13566111,145229824,1410230940,6540492,14155944,145229900,1520857248,7331293,14221481,145229884,1631483556,8122094,14287018,139200556,1742109864,8899788,14352555,139724968,1410296460,9686220,14942388,139724880,1520922768,10477021,15007925,139724864,1631549076,11267822,15073462,139462704,1742175384,12045516,15138999,
180619940,1348496080,52428,12582912,179571360,1459122388,843229,12648449,178522784,1569748696,1634030,12713986,189270688,1680375004,2411724,12779523,180882088,1348561600,3198156,13369356,179833672,1459187908,3988957,13434893,178785096,1569814216,4779758,13500430,183765832,1680440524,5557452,13565967,181144236,1348627104,6343884,14155800,180095816,1459253412,7134685,14221337,179047240,1569879720,7925486,14286874,183765832,1680506028,8703180,14352411,181406384,1348692656,9489612,14942244,180357792,1459318964,10280413,15007781,179309216,1569945272,11071214,15073318,189270688,1680571580,11848908,15138855,
183765732,1370778324,117964,12582960,183765716,1481404632,908765,12648497,182717124,1592030940,1699566,12714034,181668532,1702657248,2477260,12779571,189270760,1370843844,3263692,13369404,189270744,1481470152,4054493,13434941,182979272,1592096460,4845294,13500478,181930680,1702722768,5622988,13566015,189270764,1370909348,6409420,14155848,189270748,1481535656,7200221,14221385,183241420,1592161964,7991022,14286922,182192828,1702788272,8768716,14352459,183765744,1370974900,9555148,14942292,183765728,1481601208,10345949,15007829,183503568,1592227516,11136750,15073366,182454976,1702853824,11914444,15138903,
181668532,1393060568,183500,12583008,180619940,1503686876,974301,12648545,179571360,1614313184,1765102,12714082,178522784,1724939492,2542796,12779619,181930680,1393126088,3329228,13369452,180882088,1503752396,4120029,13434989,179833672,1614378704,4910830,13500526,178785096,1725005012,5688524,13566063,182192828,1393191592,6474956,14155896,181144236,1503817900,7265757,14221433,180095816,1614444208,8056558,14286970,179047240,1725070516,8834252,14352507,182454976,1393257144,9620684,14942340,181406384,1503883452,10411485,15007877,180357792,1614509760,11202286,15073414,179309216,1725136068,11979980,15138951,
183765832,1415342812,249036,12583056,183765732,1525969120,1039837,12648593,183765716,1636595428,1830638,12714130,182717124,1747221736,2608332,12779667,189270688,1415408332,3394764,13369500,189270760,1526034640,4185565,13435037,189270744,1636660948,4976366,13500574,182979272,1747287256,5754060,13566111,189270688,1415473836,6540492,14155944,189270764,1526100144,7331293,14221481,189270748,1636726452,8122094,14287018,183241420,1747352760,8899788,14352555,183765832,1415539388,9686220,14942388,183765744,1526165696,10477021,15007925,183765728,1636792004,11267822,15073462,183503568,1747418312,12045516,15138999,
224660804,1353738992,52428,12582912,223612224,1464365300,843229,12648449,222563648,1574991608,1634030,12713986,233311552,1685617916,2411724,12779523,224922952,1353804512,3198156,13369356,223874536,1464430820,3988957,13434893,222825960,1575057128,4779758,13500430,227806696,1685683436,5557452,13565967,225185100,1353870080,6343884,14155800,224136680,1464496388,7134685,14221337,223088104,1575122696,7925486,14286874,227806696,1685749004,8703180,14352411,225447248,1353935632,9489612,14942244,224398656,1464561940,10280413,15007781,223350080,1575188248,11071214,15073318,233311552,1685814556,11848908,15138855,
227806596,1376021236,117964,12582960,227806580,1486647544,908765,12648497,226757988,1597273852,1699566,12714034,225709396,1707900160,2477260,12779571,233311624,1376086756,3263692,13369404,233311608,1486713064,4054493,13434941,227020136,1597339372,4845294,13500478,225971544,1707965680,5622988,13566015,233311628,1376152324,6409420,14155848,233311612,1486778632,7200221,14221385,227282284,1597404940,7991022,14286922,226233692,1708031248,8768716,14352459,227806608,1376217876,9555148,14942292,227806592,1486844184,10345949,15007829,227544432,1597470492,11136750,15073366,226495840,1708096800,11914444,15138903,
225709396,1398303480,183500,12583008,224660804,1508929788,974301,12648545,223612224,1619556096,1765102,12714082,222563648,1730182404,2542796,12779619,225971544,1398369000,3329228,13369452,224922952,1508995308,4120029,13434989,223874536,1619621616,4910830,13500526,222825960,1730247924,5688524,13566063,226233692,1398434568,6474956,14155896,225185100,1509060876,7265757,14221433,224136680,1619687184,8056558,14286970,223088104,1730313492,8834252,14352507,226495840,1398500120,9620684,14942340,225447248,1509126428,10411485,15007877,224398656,1619752736,11202286,15073414,223350080,1730379044,11979980,15138951,
227806696,1420585724,249036,12583056,227806596,1531212032,1039837,12648593,227806580,1641838340,1830638,12714130,226757988,1752464648,2608332,12779667,233311552,1420651244,3394764,13369500,233311624,1531277552,4185565,13435037,233311608,1641903860,4976366,13500574,227020136,1752530168,5754060,13566111,233311552,1420716812,6540492,14155944,233311628,1531343120,7331293,14221481,233311612,1641969428,8122094,14287018,227282284,1752595736,8899788,14352555,227806696,1420782364,9686220,14942388,227806608,1531408672,10477021,15007925,227806592,1642034980,11267822,15073462,227544432,1752661288,12045516,15138999,
268701668,1358981952,52428,12582912,267653088,1469608260,843229,12648449,266604512,1580234568,1634030,12713986,277352416,1690860876,2411724,12779523,268963816,1359047504,3198156,13369356,267915400,1469673812,3988957,13434893,266866824,1580300120,4779758,13500430,271847560,1690926428,5557452,13565967,269225964,1359113008,6343884,14155800,268177544,1469739316,7134685,14221337,267128968,1580365624,7925486,14286874,271847560,1690991932,8703180,14352411,269488112,1359178528,9489612,14942244,268439520,1469804836,10280413,15007781,267390944,1580431144,11071214,15073318,277352416,1691057452,11848908,15138855,
271847460,1381264196,117964,12582960,271847444,1491890504,908765,12648497,270798852,1602516812,1699566,12714034,269750260,1713143120,2477260,12779571,277352488,1381329748,3263692,13369404,277352472,1491956056,4054493,13434941,271061000,1602582364,4845294,13500478,270012408,1713208672,5622988,13566015,277352492,1381395252,6409420,14155848,277352476,1492021560,7200221,14221385,271323148,1602647868,7991022,14286922,270274556,1713274176,8768716,14352459,271847472,1381460772,9555148,14942292,271847456,1492087080,10345949,15007829,271585296,1602713388,11136750,15073366,270536704,1713339696,11914444,15138903,
269750260,1403546440,183500,12583008,268701668,1514172748,974301,12648545,267653088,1624799056,1765102,12714082,266604512,1735425364,2542796,12779619,270012408,1403611992,3329228,13369452,268963816,1514238300,4120029,13434989,267915400,1624864608,4910830,13500526,266866824,1735490916,5688524,13566063,270274556,1403677496,6474956,14155896,269225964,1514303804,7265757,14221433,268177544,1624930112,8056558,14286970,267128968,1735556420,8834252,14352507,270536704,1403743016,9620684,14942340,269488112,1514369324,10411485,15007877,268439520,1624995632,11202286,15073414,267390944,1735621940,11979980,15138951,
271847560,1425828684,249036,12583056,271847460,1536454992,1039837,12648593,271847444,1647081300,1830638,12714130,270798852,1757707608,2608332,12779667,277352416,1425894236,3394764,13369500,277352488,1536520544,4185565,13435037,277352472,1647146852,4976366,13500574,271061000,1757773160,5754060,13566111,277352416,1425959740,6540492,14155944,277352492,1536586048,7331293,14221481,277352476,1647212356,8122094,14287018,271323148,1757838664,8899788,14352555,271847560,1426025260,9686220,14942388,271847472,1536651568,10477021,15007925,271847456,1647277876,11267822,15073462,271585296,1757904184,12045516,15138999};

DI float bf_lo(u32 v) { return __uint_as_float(v << 16); }
DI u32 pk_bf16(float a, float b) { f32x2 t = {a, b}; return __builtin_bit_cast(u32, __builtin_convertvector(t, bf16x2)); }

DI void slow_path(const u16* __restrict__ x, const u16* __restrict__ dwl, const u16* __restrict__ dws, const u16* __restrict__ w1,
                  const u16* __restrict__ w2, u16* __restrict__ out, float* sm, int t, int tx, int ty0) {
  float* o = sm;
  float* g = sm + 8 * CH;
  float* st = g + 8 * HID;
#pragma unroll 1
  for (int b = 0; b < 128; ++b) {
    const int y = ty0 * 16 + (b >> 1), x0 = tx * 16 + (b & 1) * 8;
    __syncthreads();
#pragma unroll 1
    for (int i = t; i < 8 * CH; i += 256) {
      const int p = i / CH, c = i - p * CH;
      float acc = 0.f;
#pragma unroll 1
      for (int a = 0; a < 5; ++a)
#pragma unroll 1
        for (int e = 0; e < 5; ++e) {
          const int yy = y + a - 2, xc = x0 + p + e - 2;
          if (yy < 0 || yy >= IMG || xc < 0 || xc >= IMG) continue;
          const float xv = bf_lo(x[(size_t)(yy * IMG + xc) * CH + c]);
          acc = __builtin_fmaf(bf_lo(dws[(a * 5 + e) * CH + c]), xv, acc);
          if (a >= 1 && a <= 3 && e >= 1 && e <= 3) acc = __builtin_fmaf(bf_lo(dwl[((a - 1) * 3 + (e - 1)) * CH + c]), xv, acc);
        }
      o[i] = acc;
    }
    __syncthreads();
    if (t < 8) {
      float m = 0.f, v = 0.f;
#pragma unroll 1
      for (int c = 0; c < CH; ++c) m += o[t * CH + c];
      m *= (1.0f / CH);
#pragma unroll 1
      for (int c = 0; c < CH; ++c) { const float d = o[t * CH + c] - m; v = __builtin_fmaf(d, d, v); }
      st[2 * t] = m; st[2 * t + 1] = __builtin_amdgcn_rsqf(v * (1.0f / CH) + 1e-5f);
    }
    __syncthreads();
#pragma unroll 1
    for (int i = t; i < 8 * HID; i += 256) {
      const int p = i / HID, e = i - p * HID;
      const float m = st[2 * p], r = st[2 * p + 1];
      float u = 0.f;
#pragma unroll 1
      for (int c = 0; c < CH; ++c) u = __builtin_fmaf((o[p * CH + c] - m) * r, bf_lo(w1[c * HID + e]), u);
      const float z = u * __builtin_fmaf(u * u, GC3, GC1);
      g[i] = u * __builtin_amdgcn_rcpf(1.0f + __builtin_amdgcn_exp2f(z));
    }
    __syncthreads();
#pragma unroll 1
    for (int i = t; i < 8 * CH; i += 256) {
      const int p = i / CH, c = i - p * CH;
      float acc = 0.f;
#pragma unroll 1
      for (int e = 0; e < HID; ++e) acc = __builtin_fmaf(g[p * HID + e], bf_lo(w2[e * CH + c]), acc);
      const size_t idx = (size_t)(y * IMG + x0 + p) * CH + c;
      out[idx] = (u16)pk_bf16(bf_lo(x[idx]) + acc, 0.f);
    }
  }
}

__global__ __launch_bounds__(256, 1) void replk_kernel(const u16* __restrict__ x, const u16* __restrict__ dwl,
    const u16* __restrict__ dws, const u16* __restrict__ w1, const u16* __restrict__ w2, u16* __restrict__ out
    ) {
  __shared__ __attribute__((aligned(16))) char smem[L_TOTAL];
  __attribute__((address_space(3))) char* smem3 = (__attribute__((address_space(3))) char*)smem;
  const int w = __builtin_amdgcn_readfirstlane(threadIdx.x >> 6);
  u32 tid = threadIdx.x, blk = blockIdx.x;
  u32 flg = (u32)(size_t)smem3 ? 1u : 0u;
  u64 ka = (u64)__builtin_amdgcn_kernarg_segment_ptr();
  asm volatile(
    ".p2align 3\n"
    "s_load_dwordx2 s[40:41], s[98:99], 0x0\n"
    "s_load_dwordx2 s[88:89], s[98:99], 0x10\n"
    "s_load_dwordx2 s[92:93], s[98:99], 0x8\n"
    "s_load_dwordx2 s[84:85], s[98:99], 0x20\n"
    "s_load_dwordx2 s[80:81], s[98:99], 0x18\n"
    "s_load_dwordx2 s[44:45], s[98:99], 0x28\n"
    "v_lshlrev_b32_e32 v213, 4, v208\n"
    "v_lshrrev_b32_e32 v0, 6, v208\n"
    "s_add_u32 vcc_lo, s96, 0\n"
    "s_addc_u32 vcc_hi, s97, 0\n"
    "global_load_dwordx4 v[168:171], v213, vcc\n"
    "s_add_u32 vcc_lo, s96, 4096\n"
    "s_addc_u32 vcc_hi, s97, 0\n"
    "global_load_dwordx4 v[172:175], v213, vcc\n"
    "s_add_u32 vcc_lo, s96, 8192\n"
    "s_addc_u32 vcc_hi, s97, 0\n"
    "global_load_dwordx4 v[176:179], v213, vcc\n"
    "s_getpc_b64 s[76:77]\n"
    ".Lreplk_pc0:\n"
    "s_bfe_u32 s63, s65, 0x30003\n"
    "v_lshrrev_b32_e32 v5, 2, v208\n"
    "s_lshl_b32 s63, s63, 6\n"
    "v_add_lshl_u32 v5, s63, v5, 7\n"
    "v_min_u32_e32 v215, .Lreplk_end-.Lreplk_pc0-4, v5\n"
    "global_load_dword v233, v215, s[76:77]\n"
    "v_readfirstlane_b32 s60, v0\n"
    "v_min_u32_e32 v1, 239, v208\n"
    "v_mul_u32_u24_e32 v2, 1366, v1\n"
    "s_lshr_b32 s61, s65, 3\n"
    "s_and_b32 s62, s65, 7\n"
    "v_lshrrev_b32_e32 v2, 16, v2\n"
    "v_lshlrev_b32_e32 v3, 2, v1\n"
    "s_lshl_b32 s62, s62, 6\n"
    "s_movk_i32 s63, 768\n"
    "v_mad_u32_u24 v211, v2, s63, v3\n"
    "v_mul_u32_u24_e32 v4, 384, v2\n"
    "v_add_u32_e32 v2, -1, v2\n"
    "s_movk_i32 s90, 4800\n"
    "s_mov_b32 s91, 0x20000\n"
    "v_add_u32_e32 v3, -576, v3\n"
    "v_cmp_gt_u32_e32 vcc, 3, v2\n"
    "v_add_u32_e32 v3, v3, v4\n"
    "v_bfrev_b32_e32 v4, 1\n"
    "s_movk_i32 s94, 1728\n"
    "s_mov_b32 s95, 0x20000\n"
    "v_cndmask_b32_e32 v212, v4, v3, vcc\n"
    "s_lshl_b32 s99, s62, 5\n"
    "s_add_u32 s99, s99, s61\n"
    "s_mulk_i32 s99, 3072\n"
    "s_lshr_b32 s51, s60, 1\n"
    "s_mul_i32 s63, s51, 18432\n"
    "s_lshl_b32 s52, s60, 13\n"
    "s_add_u32 s63, s63, s52\n"
    "v_add_u32_e32 v210, s63, v213\n"
    "s_mul_i32 s38, s60, 9216\n"
    "s_add_u32 s38, s38, 40960\n"
    "s_mov_b32 s86, 73728\n"
    "s_mov_b32 s87, 0x20000\n"
    "v_and_b32_e32 v5, 63, v208\n"
    "v_min_u32_e32 v5, 8, v5\n"
    "s_and_b32 s52, s60, 1\n"
    "s_lshl_b32 s53, s61, 1\n"
    "s_add_u32 s52, s52, s53\n"
    "s_mul_i32 s52, s52, 9\n"
    "v_add_lshl_u32 v214, s52, v5, 7\n"
    "s_cmp_eq_u32 s61, 0\n"
    "s_cselect_b32 s52, 24, 0\n"
    "s_cmp_eq_u32 s61, 31\n"
    "s_cselect_b32 s53, 40, s52\n"
    "s_sub_u32 s53, 256, s53\n"
    "v_subrev_u32_e32 v6, s52, v208\n"
    "s_sub_u32 s63, s99, 384\n"
    "v_cmp_gt_u32_e32 vcc, s53, v6\n"
    "v_lshl_add_u32 v7, v1, 4, s63\n"
    "v_bfe_u32 v8, v208, 4, 2\n"
    "v_lshl_or_b32 v8, v0, 3, v8\n"
    "v_and_b32_e32 v9, 0xf0, v213\n"
    "v_cndmask_b32_e32 v208, v4, v7, vcc\n"
    "v_mul_u32_u24_e32 v8, 768, v8\n"
    "s_mov_b32 s42, 50331648\n"
    "s_mov_b32 s43, 0x20000\n"
    "v_or_b32_e32 v209, v8, v9\n"
    "s_mov_b32 s82, 73728\n"
    "s_mov_b32 s83, 0x20000\n"
    "s_cmp_eq_u32 s62, 0\n"
    "s_cselect_b32 s78, 0, s42\n"
    "s_mov_b32 s79, 0x20000\n"
    "s_waitcnt lgkmcnt(0)\n"
    "s_cmp_lt_u32 s60, 2\n"
    "s_nop 0\n"
    "s_cselect_b64 s[70:71], s[80:81], s[84:85]\n"
    "s_sub_u32 s76, s40, 196608\n"
    "s_subb_u32 s77, s41, 0\n"
    "s_mov_b32 s65, 0\n"
    "buffer_load_dwordx4 a[224:227], v208, s[76:79], s65 offen\n"
    "s_mov_b32 s65, 98304\n"
    "buffer_load_dwordx4 a[228:231], v208, s[76:79], s65 offen\n"
    "s_mov_b32 s65, 196608\n"
    "buffer_load_dwordx4 a[232:235], v208, s[40:43], s65 offen\n"
    "s_mov_b32 s65, 294912\n"
    "buffer_load_dwordx4 a[236:239], v208, s[40:43], s65 offen\n"
    "buffer_load_dword v224, v211, s[88:91], 0 offen offset:0\n"
    "buffer_load_dword v225, v211, s[88:91], 0 offen offset:192\n"
    "buffer_load_dword v226, v211, s[88:91], 0 offen offset:384\n"
    "buffer_load_dword v227, v211, s[88:91], 0 offen offset:576\n"
    "buffer_load_dword v228, v211, s[88:91], 0 offen offset:768\n"
    "v_mov_b32_e64 v88, 0\n"
    "v_mov_b32_e32 v240, 0xdddd8888\n"
    "v_mov_b32_e64 v89, 0\n"
    "v_mov_b32_e64 v200, 0\n"
    "v_mov_b32_e64 v90, 0\n"
    "v_mov_b32_e64 v91, 0\n"
    "v_mov_b32_e64 v59, 0\n"
    "v_mov_b32_e64 v60, 0\n"
    "v_mov_b32_e64 v67, 0\n"
    "v_mov_b32_e64 v68, 0\n"
    "v_mov_b32_e64 v75, 0\n"
    "v_mov_b32_e64 v76, 0\n"
    "v_mov_b32_e64 v83, 0\n"
    "v_mov_b32_e64 v84, 0\n"
    "v_mov_b32_e32 v36, 0x75089ba4\n"
    "v_mov_b32_e32 v38, 0xbdd2d3e8\n"
    "v_mov_b32_e32 v32, 0xc0135761\n"
    "v_lshlrev_b32_e64 v34, 16, v213\n"
    "v_lshlrev_b32_e64 v234, 1, v213\n"
    "v_or_b32_e32 v0, 0x8000, v34\n"
    "v_or_b32_e32 v1, 0x18000, v34\n"
    "v_or_b32_e32 v2, 0x28000, v34\n"
    "v_or_b32_e32 v3, 0x38000, v34\n"
    "v_or_b32_e32 v4, 0x48000, v34\n"
    "v_or_b32_e32 v5, 0x58000, v34\n"
    "v_or_b32_e32 v6, 0x68000, v34\n"
    "v_or_b32_e32 v7, 0x78000, v34\n"
    "v_cmp_ne_u32_e64 s[72:73], 0, v34\n"
    "s_nop 0\n"
    "s_nop 1\n"
    "v_cndmask_b32_e64 v0, 0, v0, s[72:73]\n"
    "v_pk_mul_f32 v[0:1], v[0:1], v[36:37] op_sel_hi:[1,0]\n"
    "v_pk_mul_f32 v[2:3], v[2:3], v[36:37] op_sel_hi:[1,0]\n"
    "v_pk_mul_f32 v[4:5], v[4:5], v[36:37] op_sel_hi:[1,0]\n"
    "v_pk_mul_f32 v[6:7], v[6:7], v[36:37] op_sel_hi:[1,0]\n"
    "v_pk_mul_f32 v[8:9], v[0:1], v[0:1]\n"
    "v_pk_mul_f32 v[10:11], v[2:3], v[2:3]\n"
    "v_pk_mul_f32 v[12:13], v[4:5], v[4:5]\n"
    "v_pk_mul_f32 v[14:15], v[6:7], v[6:7]\n"
    "v_pk_fma_f32 v[8:9], v[8:9], v[38:39], v[32:33] op_sel_hi:[1,0,0]\n"
    "v_pk_fma_f32 v[10:11], v[10:11], v[38:39], v[32:33] op_sel_hi:[1,0,0]\n"
    "v_pk_fma_f32 v[12:13], v[12:13], v[38:39], v[32:33] op_sel_hi:[1,0,0]\n"
    "v_pk_fma_f32 v[14:15], v[14:15], v[38:39], v[32:33] op_sel_hi:[1,0,0]\n"
    "v_pk_mul_f32 v[8:9], v[8:9], v[0:1]\n"
    "v_pk_mul_f32 v[10:11], v[10:11], v[2:3]\n"
    "v_pk_mul_f32 v[12:13], v[12:13], v[4:5]\n"
    "v_pk_mul_f32 v[14:15], v[14:15], v[6:7]\n"
    "v_exp_f32_e64 v8, v8\n"
    "v_exp_f32_e64 v9, v9\n"
    "v_exp_f32_e64 v10, v10\n"
    "v_exp_f32_e64 v11, v11\n"
    "v_exp_f32_e64 v12, v12\n"
    "v_exp_f32_e64 v13, v13\n"
    "v_exp_f32_e64 v14, v14\n"
    "v_exp_f32_e64 v15, v15\n"
    "v_pk_add_f32 v[16:17], v[8:9], 1.0 op_sel_hi:[1,0]\n"
    "v_pk_add_f32 v[18:19], v[10:11], 1.0 op_sel_hi:[1,0]\n"
    "v_pk_add_f32 v[20:21], v[12:13], 1.0 op_sel_hi:[1,0]\n"
    "v_pk_add_f32 v[22:23], v[14:15], 1.0 op_sel_hi:[1,0]\n"
    "v_rcp_f32_e64 v16, v16\n"
    "v_rcp_f32_e64 v17, v17\n"
    "v_rcp_f32_e64 v18, v18\n"
    "v_rcp_f32_e64 v19, v19\n"
    "v_rcp_f32_e64 v20, v20\n"
    "v_rcp_f32_e64 v21, v21\n"
    "v_rcp_f32_e64 v22, v22\n"
    "v_rcp_f32_e64 v23, v23\n"
    "v_pk_mul_f32 v[16:17], v[16:17], v[0:1]\n"
    "v_pk_mul_f32 v[18:19], v[18:19], v[2:3]\n"
    "v_pk_mul_f32 v[20:21], v[20:21], v[4:5]\n"
    "v_pk_mul_f32 v[22:23], v[22:23], v[6:7]\n"
    "v_pk_mul_f32 v[8:9], v[8:9], v[16:17]\n"
    "v_pk_mul_f32 v[10:11], v[10:11], v[18:19]\n"
    "v_pk_mul_f32 v[12:13], v[12:13], v[20:21]\n"
    "v_pk_mul_f32 v[14:15], v[14:15], v[22:23]\n"
    "v_cvt_pk_bf16_f32 v188, v16, v17\n"
    "v_cvt_pk_bf16_f32 v189, v18, v19\n"
    "v_cvt_pk_bf16_f32 v190, v20, v21\n"
    "v_cvt_pk_bf16_f32 v191, v22, v23\n"
    "v_cvt_pk_bf16_f32 v24, -v8, -v9\n"
    "v_cvt_pk_bf16_f32 v25, -v10, -v11\n"
    "v_cvt_pk_bf16_f32 v26, -v12, -v13\n"
    "v_cvt_pk_bf16_f32 v27, -v14, -v15\n"
    "ds_write_b128 v234, v[24:27] offset:0\n"
    "v_or_b32_e32 v0, 0x88000, v34\n"
    "v_or_b32_e32 v1, 0x98000, v34\n"
    "v_or_b32_e32 v2, 0xa8000, v34\n"
    "v_or_b32_e32 v3, 0xb8000, v34\n"
    "v_or_b32_e32 v4, 0xc8000, v34\n"
    "v_or_b32_e32 v5, 0xd8000, v34\n"
    "v_or_b32_e32 v6, 0xe8000, v34\n"
    "v_or_b32_e32 v7, 0xf8000, v34\n"
    "v_pk_mul_f32 v[0:1], v[0:1], v[36:37] op_sel_hi:[1,0]\n"
    "v_pk_mul_f32 v[2:3], v[2:3], v[36:37] op_sel_hi:[1,0]\n"
    "v_pk_mul_f32 v[4:5], v[4:5], v[36:37] op_sel_hi:[1,0]\n"
    "v_pk_mul_f32 v[6:7], v[6:7], v[36:37] op_sel_hi:[1,0]\n"
    "v_pk_mul_f32 v[8:9], v[0:1], v[0:1]\n"
    "v_pk_mul_f32 v[10:11], v[2:3], v[2:3]\n"
    "v_pk_mul_f32 v[12:13], v[4:5], v[4:5]\n"
    "v_pk_mul_f32 v[14:15], v[6:7], v[6:7]\n"
    "v_pk_fma_f32 v[8:9], v[8:9], v[38:39], v[32:33] op_sel_hi:[1,0,0]\n"
    "v_pk_fma_f32 v[10:11], v[10:11], v[38:39], v[32:33] op_sel_hi:[1,0,0]\n"
    "v_pk_fma_f32 v[12:13], v[12:13], v[38:39], v[32:33] op_sel_hi:[1,0,0]\n"
    "v_pk_fma_f32 v[14:15], v[14:15], v[38:39], v[32:33] op_sel_hi:[1,0,0]\n"
    "v_pk_mul_f32 v[8:9], v[8:9], v[0:1]\n"
    "v_pk_mul_f32 v[10:11], v[10:11], v[2:3]\n"
    "v_pk_mul_f32 v[12:13], v[12:13], v[4:5]\n"
    "v_pk_mul_f32 v[14:15], v[14:15], v[6:7]\n"
    "v_exp_f32_e64 v8, v8\n"
    "v_exp_f32_e64 v9, v9\n"
    "v_exp_f32_e64 v10, v10\n"
    "v_exp_f32_e64 v11, v11\n"
    "v_exp_f32_e64 v12, v12\n"
    "v_exp_f32_e64 v13, v13\n"
    "v_exp_f32_e64 v14, v14\n"
    "v_exp_f32_e64 v15, v15\n"
    "v_pk_add_f32 v[16:17], v[8:9], 1.0 op_sel_hi:[1,0]\n"
    "v_pk_add_f32 v[18:19], v[10:11], 1.0 op_sel_hi:[1,0]\n"
    "v_pk_add_f32 v[20:21], v[12:13], 1.0 op_sel_hi:[1,0]\n"
    "v_pk_add_f32 v[22:23], v[14:15], 1.0 op_sel_hi:[1,0]\n"
    "v_rcp_f32_e64 v16, v16\n"
    "v_rcp_f32_e64 v17, v17\n"
    "v_rcp_f32_e64 v18, v18\n"
    "v_rcp_f32_e64 v19, v19\n"
    "v_rcp_f32_e64 v20, v20\n"
    "v_rcp_f32_e64 v21, v21\n"
    "v_rcp_f32_e64 v22, v22\n"
    "v_rcp_f32_e64 v23, v23\n"
    "v_pk_mul_f32 v[16:17], v[16:17], v[0:1]\n"
    "v_pk_mul_f32 v[18:19], v[18:19], v[2:3]\n"
    "v_pk_mul_f32 v[20:21], v[20:21], v[4:5]\n"
    "v_pk_mul_f32 v[22:23], v[22:23], v[6:7]\n"
    "v_pk_mul_f32 v[8:9], v[8:9], v[16:17]\n"
    "v_pk_mul_f32 v[10:11], v[10:11], v[18:19]\n"
    "v_pk_mul_f32 v[12:13], v[12:13], v[20:21]\n"
    "v_pk_mul_f32 v[14:15], v[14:15], v[22:23]\n"
    "v_cvt_pk_bf16_f32 v192, v16, v17\n"
    "v_cvt_pk_bf16_f32 v193, v18, v19\n"
    "v_cvt_pk_bf16_f32 v194, v20, v21\n"
    "v_cvt_pk_bf16_f32 v195, v22, v23\n"
    "v_cvt_pk_bf16_f32 v24, -v8, -v9\n"
    "v_cvt_pk_bf16_f32 v25, -v10, -v11\n"
    "v_cvt_pk_bf16_f32 v26, -v12, -v13\n"
    "v_cvt_pk_bf16_f32 v27, -v14, -v15\n"
    "ds_write_b128 v234, v[24:27] offset:16\n"
    "v_add_u32_e32 v234, 65536, v234\n"
    "buffer_load_dword v229, v212, s[92:95], 0 offen offset:0\n"
    "s_nop 0\n"
    "s_waitcnt vmcnt(13)\n"
    "v_lshlrev_b32_sdwa v216, 2, v168 dst_sel:DWORD dst_unused:UNUSED_PAD src0_sel:DWORD src1_sel:WORD_0\n"
    "v_lshlrev_b32_sdwa v217, 2, v168 dst_sel:DWORD dst_unused:UNUSED_PAD src0_sel:DWORD src1_sel:WORD_1\n"
    "v_lshlrev_b32_sdwa v218, 2, v169 dst_sel:DWORD dst_unused:UNUSED_PAD src0_sel:DWORD src1_sel:WORD_0\n"
    "v_lshlrev_b32_sdwa v219, 2, v169 dst_sel:DWORD dst_unused:UNUSED_PAD src0_sel:DWORD src1_sel:WORD_1\n"
    "v_lshlrev_b32_sdwa v220, 2, v170 dst_sel:DWORD dst_unused:UNUSED_PAD src0_sel:DWORD src1_sel:WORD_0\n"
    "v_lshlrev_b32_sdwa v221, 2, v170 dst_sel:DWORD dst_unused:UNUSED_PAD src0_sel:DWORD src1_sel:WORD_1\n"
    "v_lshlrev_b32_sdwa v222, 2, v171 dst_sel:DWORD dst_unused:UNUSED_PAD src0_sel:DWORD src1_sel:WORD_0\n"
    "v_lshlrev_b32_sdwa v223, 2, v171 dst_sel:DWORD dst_unused:UNUSED_PAD src0_sel:DWORD src1_sel:WORD_1\n"
    "ds_write_b128 v222, v[88:91]\n"
    "buffer_load_dword v230, v212, s[92:95], 0 offen offset:192\n"
    "s_nop 0\n"
    "s_waitcnt vmcnt(13)\n"
    "v_lshlrev_b32_sdwa v207, 2, v172 dst_sel:DWORD dst_unused:UNUSED_PAD src0_sel:DWORD src1_sel:WORD_0\n"
    "v_lshlrev_b32_sdwa v242, 2, v173 dst_sel:DWORD dst_unused:UNUSED_PAD src0_sel:DWORD src1_sel:WORD_0\n"
    "v_lshlrev_b32_sdwa v243, 2, v173 dst_sel:DWORD dst_unused:UNUSED_PAD src0_sel:DWORD src1_sel:WORD_1\n"
    "v_lshlrev_b32_sdwa v244, 2, v174 dst_sel:DWORD dst_unused:UNUSED_PAD src0_sel:DWORD src1_sel:WORD_0\n"
    "v_lshlrev_b32_sdwa v248, 2, v174 dst_sel:DWORD dst_unused:UNUSED_PAD src0_sel:DWORD src1_sel:WORD_1\n"
    "v_lshlrev_b32_sdwa v249, 2, v175 dst_sel:DWORD dst_unused:UNUSED_PAD src0_sel:DWORD src1_sel:WORD_0\n"
    "v_lshlrev_b32_sdwa v250, 2, v175 dst_sel:DWORD dst_unused:UNUSED_PAD src0_sel:DWORD src1_sel:WORD_1\n"
    "v_bfe_i32 v201, v172, 16, 1\n"
    "v_and_b32_e32 v201, 0x35003500, v201\n"
    "v_bfe_i32 v203, v172, 18, 1\n"
    "v_and_b32_e32 v203, 0x35003500, v203\n"
    "v_bfe_i32 v205, v172, 20, 1\n"
    "v_and_b32_e32 v205, 0x35003500, v205\n"
    "v_bfe_i32 v92, v172, 26, 1\n"
    "v_xor_b32_e32 v92, 0xffff, v92\n"
    "v_and_b32_e32 v92, 0x3f803f80, v92\n"
    "v_bfe_i32 v236, v172, 22, 1\n"
    "v_and_b32_e64 v236, v92, v236\n"
    "v_bfe_i32 v237, v172, 23, 1\n"
    "v_and_b32_e32 v237, v92, v237\n"
    "buffer_load_dword v231, v212, s[92:95], 0 offen offset:384\n"
    "s_waitcnt vmcnt(13)\n"
    "v_lshlrev_b32_sdwa v251, 2, v176 dst_sel:DWORD dst_unused:UNUSED_PAD src0_sel:DWORD src1_sel:WORD_0\n"
    "v_lshlrev_b32_sdwa v252, 2, v176 dst_sel:DWORD dst_unused:UNUSED_PAD src0_sel:DWORD src1_sel:WORD_1\n"
    "v_lshlrev_b32_sdwa v253, 2, v177 dst_sel:DWORD dst_unused:UNUSED_PAD src0_sel:DWORD src1_sel:WORD_0\n"
    "v_lshlrev_b32_sdwa v254, 2, v177 dst_sel:DWORD dst_unused:UNUSED_PAD src0_sel:DWORD src1_sel:WORD_1\n"
    "v_lshlrev_b32_sdwa v241, 0, v178 dst_sel:DWORD dst_unused:UNUSED_PAD src0_sel:DWORD src1_sel:WORD_0\n"
    "v_lshlrev_b32_sdwa v245, 4, v178 dst_sel:DWORD dst_unused:UNUSED_PAD src0_sel:DWORD src1_sel:WORD_1\n"
    "v_lshlrev_b32_sdwa v255, 4, v179 dst_sel:DWORD dst_unused:UNUSED_PAD src0_sel:DWORD src1_sel:WORD_0\n"
    "v_lshlrev_b32_sdwa v247, 4, v179 dst_sel:DWORD dst_unused:UNUSED_PAD src0_sel:DWORD src1_sel:WORD_1\n"
    "v_bfe_i32 v238, v172, 24, 1\n"
    "v_and_b32_e64 v238, v92, v238\n"
    "v_bfe_i32 v239, v172, 25, 1\n"
    "v_and_b32_e64 v239, v92, v239\n"
    "v_bfe_u32 v93, v172, 28, 2\n"
    "v_mul_u32_u24_e32 v93, 393216, v93\n"
    "v_mov_b32_e64 v222, v254\n"
    "v_mov_b32_e32 v254, 0\n"
    "s_nop 1\n"
    "v_mfma_f32_32x32x1_2b_f32 v[0:31], v254, v254, 0\n"
    "global_load_dword v233, v214, s[70:71]\n"
    "s_mov_b32 s48, 0x44ea44ea\n"
    "s_mov_b32 s49, 0xb77fb77f\n"
    "s_mov_b32 s50, 0x89efde6e\n"
    "s_mov_b32 s46, s42\n"
    "s_mov_b32 s47, s43\n"
    "s_mul_i32 s54, s60, 592\n"
    "s_add_u32 s54, s54, -27008\n"
    "s_mul_i32 s55, s60, -1284\n"
    "s_add_u32 s55, s55, 54688\n"
    "s_mul_i32 s56, s60, -6160\n"
    "s_add_u32 s56, s56, -27008\n"
    "s_add_u32 s98, s99, 1375872\n"
    "s_and_b32 s36, s60, 1\n"
    "s_lshl_b32 s36, s36, 13\n"
    "s_add_u32 s36, s36, 327680\n"
    "s_mul_i32 s53, s60, 98304\n"
    "s_add_u32 s98, s53, s98\n"
    "s_xor_b32 s53, s60, 1\n"
    "s_mul_i32 s53, s53, 393216\n"
    "s_add_u32 s99, s99, s53\n"
    "s_add_u32 s53, s98, 393216\n"
    "s_sub_u32 s53, s53, s36\n"
    "s_and_b32 s37, s61, 1\n"
    "s_lshl_b32 s37, s37, 2\n"
    "s_add_u32 s37, s37, s60\n"
    "s_lshl_b32 s37, s37, 12\n"
    "s_cmp_eq_u32 s61, 0\n"
    "s_cselect_b32 s67, 2, 0\n"
    "s_cmp_eq_u32 s61, 31\n"
    "s_cselect_b32 s67, 1, s67\n"
    "s_lshl_b32 s67, s67, 30\n"
    "s_mov_b64 s[68:69], s[44:45]\n"
    "s_mov_b32 s70, 0\n"
    "s_mov_b32 s71, s47\n"
    "s_mov_b64 s[72:73], s[40:41]\n"
    "s_mov_b64 s[74:75], s[42:43]\n"
    "s_mov_b32 s57, 0\n"
    "s_mov_b32 s58, 196608\n"
    "s_mov_b32 s60, 0\n"
    "s_mov_b32 s61, 393216\n"
    "s_mov_b32 s62, 786432\n"
    "s_mov_b32 s63, 1179648\n"
    "s_mov_b32 s52, 1572864\n"
    "s_mov_b32 s88, 98304\n"
    "s_mov_b32 s89, 294912\n"
    "s_mov_b32 s39, 0\n"
    "s_getpc_b64 s[76:77]\n"
    ".Lreplk_pc1:\n"
    "s_add_u32 s76, s76, .Lreplk_last-.Lreplk_pc1\n"
    "s_addc_u32 s77, s77, 0\n"
    "s_sub_u32 s76, s76, s36\n"
    "s_subb_u32 s77, s77, 0\n"
    "s_add_u32 s78, s36, .Lreplk_end-.Lreplk_last\n"
    "s_mov_b32 s79, s37\n"
    "s_add_u32 m0, s38, 0\n"
    "s_mov_b32 s64, 0\n"
    "buffer_load_dwordx4 v210, s[84:87], s64 offen offset:0 lds\n"
    "s_waitcnt vmcnt(13)\n"
    "ds_write2_b32 v216, a224, a225 offset0:0 offset1:20\n"
    "v_add3_u32 v247, v247, v93, s98\n"
    "v_add_u32_e32 v246, s98, v255\n"
    "v_add_u32_e64 v245, s99, v245\n"
    "v_add_u32_e64 v255, s99, v255\n"
    "ds_write2_b32 v216, a226, a227 offset0:40 offset1:60\n"
    "v_and_b32_e64 v93, s67, v172\n"
    "v_and_b32_e32 v94, 0x80000000, v93\n"
    "s_waitcnt vmcnt(12)\n"
    "ds_write2_b32 v216, a228, a229 offset0:80 offset1:100\n"
    "v_and_b32_e32 v93, 0x40000000, v93\n"
    "v_or_b32_e64 v246, v246, v94\n"
    "v_or_b32_e64 v247, v247, v93\n"
    "ds_write2_b32 v216, a230, a231 offset0:120 offset1:140\n"
    "v_add_u32_e32 v216, 1360, v216\n"
    "buffer_load_dwordx4 v210, s[84:87], s64 offen offset:1024 lds\n"
    "s_waitcnt vmcnt(12)\n"
    "ds_write2_b32 v216, a232, a233 offset0:0 offset1:20\n"
    "ds_write2_b32 v216, a234, a235 offset0:40 offset1:60\n"
    "buffer_load_dwordx4 v210, s[84:87], s64 offen offset:2048 lds\n"
    "s_waitcnt vmcnt(12)\n"
    "ds_write2_b32 v216, a236, a237 offset0:80 offset1:100\n"
    "s_add_u32 m0, s38, 3072\n"
    "s_mov_b32 s64, 3072\n"
    "buffer_load_dwordx4 v210, s[84:87], s64 offen offset:0 lds\n"
    "buffer_load_dwordx4 v210, s[84:87], s64 offen offset:1024 lds\n"
    "ds_write2_b32 v216, a238, a239 offset0:120 offset1:140\n"
    "buffer_load_dwordx4 v210, s[84:87], s64 offen offset:2048 lds\n"
    "s_add_u32 m0, s38, 6144\n"
    "s_mov_b32 s64, 6144\n"
    "buffer_load_dwordx4 v210, s[84:87], s64 offen offset:0 lds\n"
    "buffer_load_dwordx4 v210, s[84:87], s64 offen offset:1024 lds\n"
    "v_add_u32_e32 v216, 1360, v216\n"
    "s_waitcnt vmcnt(16)\n"
    "v_lshlrev_b32_e32 v40, 16, v224\n"
    "buffer_load_dwordx4 v210, s[84:87], s64 offen offset:2048 lds\n"
    "s_mov_b32 s65, 589824\n"
    "buffer_load_dwordx4 a[240:243], v208, s[40:43], s65 offen\n"
    "s_waitcnt vmcnt(17)\n"
    "v_lshlrev_b32_e32 v41, 16, v225\n"
    "s_mov_b32 s65, 688128\n"
    "buffer_load_dwordx4 a[244:247], v208, s[40:43], s65 offen\n"
    "s_mov_b32 s59, 49152\n"
    "buffer_load_dwordx4 v[96:99], v209, s[80:83], s59 offen offset:0\n"
    "s_waitcnt vmcnt(18)\n"
    "v_lshlrev_b32_e32 v42, 16, v226\n"
    "s_waitcnt vmcnt(17)\n"
    "v_lshlrev_b32_e32 v43, 16, v227\n"
    "s_waitcnt vmcnt(16)\n"
    "v_lshlrev_b32_e32 v44, 16, v228\n"
    "s_waitcnt vmcnt(15)\n"
    "v_lshlrev_b32_e32 v45, 16, v229\n"
    "s_waitcnt vmcnt(14)\n"
    "v_lshlrev_b32_e32 v46, 16, v230\n"
    "s_waitcnt vmcnt(13)\n"
    "v_lshlrev_b32_e32 v47, 16, v231\n"
    "v_add_f32_e64 v41, v41, v45\n"
    "v_add_f32_e64 v42, v42, v46\n"
    "v_add_f32_e64 v43, v43, v47\n"
    "v_cvt_pk_bf16_f32 v56, v40, v41\n"
    "v_cvt_pk_bf16_f32 v57, v42, v43\n"
    "v_cvt_pk_bf16_f32 v58, v44, 0\n"
    "v_cvt_pk_bf16_f32 v64, 0, v40\n"
    "buffer_load_dwordx4 v[100:103], v209, s[80:83], s59 offen offset:256\n"
    "ds_write_b128 v221, v[56:59] offset:0\n"
    "v_cvt_pk_bf16_f32 v65, v41, v42\n"
    "v_cvt_pk_bf16_f32 v66, v43, v44\n"
    "v_cvt_pk_bf16_f32 v61, v40, v41\n"
    "v_cvt_pk_bf16_f32 v62, v42, v43\n"
    "v_cvt_pk_bf16_f32 v63, v44, 0\n"
    "v_cvt_pk_bf16_f32 v69, 0, v40\n"
    "v_cvt_pk_bf16_f32 v70, v41, v42\n"
    "v_cvt_pk_bf16_f32 v71, v43, v44\n"
    "ds_write_b128 v221, v[64:67] offset:16\n"
    "buffer_load_dwordx4 v[104:107], v209, s[80:83], s59 offen offset:512\n"
    "v_and_b32_e32 v48, 0xffff0000, v224\n"
    "v_and_b32_e32 v49, 0xffff0000, v225\n"
    "v_and_b32_e32 v50, 0xffff0000, v226\n"
    "v_and_b32_e32 v51, 0xffff0000, v227\n"
    "v_and_b32_e32 v52, 0xffff0000, v228\n"
    "ds_write_b128 v221, v[60:63] offset:32\n"
    "v_and_b32_e32 v53, 0xffff0000, v229\n"
    "v_and_b32_e32 v54, 0xffff0000, v230\n"
    "v_and_b32_e32 v55, 0xffff0000, v231\n"
    "v_add_f32_e64 v49, v49, v53\n"
    "v_add_f32_e64 v50, v50, v54\n"
    "v_add_f32_e64 v51, v51, v55\n"
    "ds_write_b128 v221, v[68:71] offset:48\n"
    "s_mov_b32 s65, 983040\n"
    "buffer_load_dwordx4 a[248:251], v208, s[40:43], s65 offen\n"
    "v_cvt_pk_bf16_f32 v72, v48, v49\n"
    "v_cvt_pk_bf16_f32 v73, v50, v51\n"
    "v_cvt_pk_bf16_f32 v74, v52, 0\n"
    "v_cvt_pk_bf16_f32 v80, 0, v48\n"
    "v_cvt_pk_bf16_f32 v81, v49, v50\n"
    "v_cvt_pk_bf16_f32 v82, v51, v52\n"
    "v_cvt_pk_bf16_f32 v77, v48, v49\n"
    "v_cvt_pk_bf16_f32 v78, v50, v51\n"
    "v_cvt_pk_bf16_f32 v79, v52, 0\n"
    "v_cvt_pk_bf16_f32 v85, 0, v48\n"
    "v_cvt_pk_bf16_f32 v86, v49, v50\n"
    "v_cvt_pk_bf16_f32 v87, v51, v52\n"
    "ds_write_b128 v221, v[72:75] offset:336\n"
    "s_mov_b32 s65, 1081344\n"
    "buffer_load_dwordx4 a[252:255], v208, s[40:43], s65 offen\n"
    "v_xor_b32_e32 v224, 0, v219\n"
    "v_xor_b32_e32 v225, 32, v219\n"
    "v_xor_b32_e32 v226, 64, v219\n"
    "v_xor_b32_e32 v227, 96, v219\n"
    "ds_write_b128 v221, v[80:83] offset:352\n"
    "v_xor_b32_e32 v228, 0, v223\n"
    "v_xor_b32_e32 v229, 32, v223\n"
    "s_mov_b32 s59, 52224\n"
    "buffer_load_dwordx4 v[108:111], v209, s[80:83], s59 offen offset:0\n"
    "v_xor_b32_e32 v230, 64, v223\n"
    "v_xor_b32_e32 v231, 96, v223\n"
    "ds_write_b128 v221, v[76:79] offset:368\n"
    "v_lshl_add_u32 v196, s51, 8, v225\n"
    "v_lshl_add_u32 v197, s51, 8, v227\n"
    "v_mfma_f32_32x32x1_2b_f32 v[32:63], v254, v254, 0\n"
    "ds_write_b128 v221, v[84:87] offset:384\n"
    "buffer_load_dwordx4 v[112:115], v209, s[80:83], s59 offen offset:256\n"
    "v_mfma_f32_32x32x1_2b_f32 v[64:95], v254, v254, 0\n"
    "v_add_u32_e64 v254, s54, v242\n"
    "buffer_load_dwordx4 v[116:119], v209, s[80:83], s59 offen offset:512\n"
    "s_mov_b32 s65, 1376256\n"
    "buffer_load_dwordx4 a[176:179], v208, s[40:43], s65 offen\n"
    "s_mov_b32 s65, 1474560\n"
    "buffer_load_dwordx4 a[180:183], v208, s[40:43], s65 offen\n"
    "s_waitcnt vmcnt(12)\n"
    "s_barrier\n"
    "ds_read_b64_tr_b16 a[0:1], v220 offset:0\n"
    "ds_read_b64_tr_b16 a[2:3], v220 offset:3072\n"
    "ds_read_b64_tr_b16 a[4:5], v220 offset:8\n"
    "ds_read_b64_tr_b16 a[6:7], v220 offset:3080\n"
    "ds_read_b64_tr_b16 a[8:9], v220 offset:64\n"
    "ds_read_b64_tr_b16 a[10:11], v220 offset:3136\n"
    "ds_read_b64_tr_b16 a[12:13], v220 offset:72\n"
    "ds_read_b64_tr_b16 a[14:15], v220 offset:3144\n"
    "ds_read_b64_tr_b16 v[168:169], v218 offset:128\n"
    "ds_read_b64_tr_b16 v[170:171], v218 offset:3200\n"
    "ds_read_b64_tr_b16 a[16:17], v220 offset:6144\n"
    "ds_read_b64_tr_b16 a[18:19], v220 offset:9216\n"
    "ds_read_b64_tr_b16 a[20:21], v220 offset:6152\n"
    "ds_read_b64_tr_b16 a[22:23], v220 offset:9224\n"
    "ds_read_b64_tr_b16 a[24:25], v220 offset:6208\n"
    "ds_read_b64_tr_b16 a[26:27], v220 offset:9280\n"
    "ds_read_b64_tr_b16 a[28:29], v220 offset:6216\n"
    "ds_read_b64_tr_b16 a[30:31], v220 offset:9288\n"
    "ds_read_b64_tr_b16 v[172:173], v218 offset:6272\n"
    "s_nop 0\n"
    "s_mov_b32 s59, 0\n"
    "buffer_load_dwordx4 v[120:123], v209, s[80:83], s59 offen offset:0\n"
    "ds_read_b64_tr_b16 v[174:175], v218 offset:9344\n"
    "ds_read_b64_tr_b16 a[32:33], v220 offset:12288\n"
    "ds_read_b64_tr_b16 a[34:35], v220 offset:15360\n"
    "ds_read_b64_tr_b16 a[36:37], v220 offset:12296\n"
    "ds_read_b64_tr_b16 a[38:39], v220 offset:15368\n"
    "ds_read_b64_tr_b16 a[40:41], v220 offset:12352\n"
    "ds_read_b64_tr_b16 a[42:43], v220 offset:15424\n"
    "ds_read_b64_tr_b16 a[44:45], v220 offset:12360\n"
    "ds_read_b64_tr_b16 a[46:47], v220 offset:15432\n"
    "ds_read_b64_tr_b16 v[176:177], v218 offset:12416\n"
    "ds_read_b64_tr_b16 v[178:179], v218 offset:15488\n"
    "s_waitcnt vmcnt(12)\n"
    "ds_write2_b32 v216, a240, a241 offset0:0 offset1:20\n"
    "ds_write2_b32 v216, a242, a243 offset0:40 offset1:60\n"
    "buffer_load_dwordx4 v[124:127], v209, s[80:83], s59 offen offset:256\n"
    "s_waitcnt vmcnt(12)\n"
    "ds_write2_b32 v216, a244, a245 offset0:80 offset1:100\n"
    "s_waitcnt lgkmcnt(2)\n"
    "s_barrier\n"
    "ds_write2_b32 v216, a246, a247 offset0:120 offset1:140\n"
    "buffer_load_dwordx4 v[128:131], v209, s[80:83], s59 offen offset:512\n"
    "v_add_u32_e32 v216, 1360, v216\n"
    "s_waitcnt vmcnt(12)\n"
    "v_or3_b32 v200, v200, v96, v97\n"
    "v_or3_b32 v200, v200, v98, v99\n"
    "v_and_b32_e32 v232, 0x7fff7fff, v96\n"
    "v_add_u32_e32 v96, v201, v96\n"
    "v_add_u32_e64 v98, v201, v98\n"
    "ds_write_b128 v217, v[96:99] offset:24576\n"
    "s_mov_b32 s65, 0\n"
    "buffer_load_dwordx4 a[184:187], v208, s[40:43], s65 offen\n"
    "s_waitcnt vmcnt(12)\n"
    "v_or3_b32 v200, v200, v100, v101\n"
    "v_or3_b32 v200, v200, v102, v103\n"
    "v_add_u32_e64 v100, v203, v100\n"
    "v_add_u32_e64 v102, v203, v102\n"
    "ds_write_b128 v217, v[100:103] offset:24832\n"
    "s_mov_b32 s65, 98304\n"
    "buffer_load_dwordx4 a[188:191], v208, s[40:43], s65 offen\n"
    "s_waitcnt vmcnt(12)\n"
    "v_or3_b32 v200, v200, v104, v105\n"
    "v_or3_b32 v200, v200, v106, v107\n"
    "v_add_u32_e32 v104, v205, v104\n"
    "v_add_u32_e64 v106, v205, v106\n"
    "ds_write_b128 v217, v[104:107] offset:25088\n"
    "s_waitcnt lgkmcnt(0)\n"
    "s_barrier\n"
    "s_mov_b32 s59, 3072\n"
    "buffer_load_dwordx4 v[132:135], v209, s[80:83], s59 offen offset:0\n"
    "ds_read_b64_tr_b16 a[48:49], v224 offset:24576\n"
    "ds_read_b64_tr_b16 v[180:181], v196 offset:24576\n"
    "ds_read_b64_tr_b16 a[52:53], v226 offset:24576\n"
    "ds_read_b64_tr_b16 v[184:185], v197 offset:24576\n"
    "ds_read_b64_tr_b16 a[56:57], v228 offset:24576\n"
    "ds_read_b64_tr_b16 a[60:61], v229 offset:24576\n"
    "ds_read_b64_tr_b16 a[64:65], v230 offset:24576\n"
    "ds_read_b64_tr_b16 a[68:69], v231 offset:24576\n"
    "ds_read_b64_tr_b16 a[72:73], v224 offset:24832\n"
    "ds_read_b64_tr_b16 a[76:77], v226 offset:24832\n"
    "s_waitcnt vmcnt(12)\n"
    "ds_write2_b32 v216, a248, a249 offset0:0 offset1:20\n"
    "ds_write2_b32 v216, a250, a251 offset0:40 offset1:60\n"
    "buffer_load_dwordx4 v[136:139], v209, s[80:83], s59 offen offset:256\n"
    "s_waitcnt vmcnt(12)\n"
    "ds_write2_b32 v216, a252, a253 offset0:80 offset1:100\n"
    "ds_write2_b32 v216, a254, a255 offset0:120 offset1:140\n"
    "v_add_u32_e32 v216, 1360, v216\n"
    "buffer_load_dwordx4 v[140:143], v209, s[80:83], s59 offen offset:512\n"
    "s_waitcnt vmcnt(12)\n"
    "v_or3_b32 v200, v200, v108, v109\n"
    "v_or3_b32 v200, v200, v110, v111\n"
    "v_add_u32_e32 v108, v201, v108\n"
    "v_add_u32_e64 v110, v201, v110\n"
    "ds_write_b128 v217, v[108:111] offset:0\n"
    "s_mov_b32 s65, 393216\n"
    "buffer_load_dwordx4 a[192:195], v208, s[40:43], s65 offen\n"
    "s_waitcnt vmcnt(12)\n"
    "v_or3_b32 v200, v200, v112, v113\n"
    "v_or3_b32 v200, v200, v114, v115\n"
    "v_add_u32_e32 v112, v203, v112\n"
    "v_add_u32_e64 v114, v203, v114\n"
    "ds_write_b128 v217, v[112:115] offset:256\n"
    "v_mov_b32_e64 v112, v188\n"
    "v_mov_b32_e64 v113, v189\n"
    "v_mov_b32_e64 v114, v190\n"
    "v_mov_b32_e32 v115, v191\n"
    "s_mov_b32 s65, 491520\n"
    "buffer_load_dwordx4 a[196:199], v208, s[40:43], s65 offen\n"
    "s_waitcnt vmcnt(12)\n"
    "v_or3_b32 v200, v200, v116, v117\n"
    "v_or3_b32 v200, v200, v118, v119\n"
    "v_add_u32_e64 v116, v205, v116\n"
    "v_add_u32_e64 v118, v205, v118\n"
    "ds_write_b128 v217, v[116:119] offset:512\n"
    "v_mov_b32_e64 v116, v192\n"
    "v_mov_b32_e64 v117, v193\n"
    "v_mov_b32_e64 v118, v194\n"
    "v_mov_b32_e64 v119, v195\n"
    "s_waitcnt lgkmcnt(0)\n"
    "s_barrier\n"
    "s_mov_b32 s59, 24576\n"
    "buffer_load_dwordx4 v[144:147], v209, s[80:83], s59 offen offset:0\n"
    "ds_read_b64_tr_b16 a[50:51], v224 offset:0\n"
    "ds_read_b64_tr_b16 v[182:183], v196 offset:0\n"
    "ds_read_b64_tr_b16 a[54:55], v226 offset:0\n"
    "ds_read_b64_tr_b16 v[186:187], v197 offset:0\n"
    "ds_read_b64_tr_b16 a[58:59], v228 offset:0\n"
    "ds_read_b64_tr_b16 a[62:63], v229 offset:0\n"
    "ds_read_b64_tr_b16 a[66:67], v230 offset:0\n"
    "ds_read_b64_tr_b16 a[70:71], v231 offset:0\n"
    "ds_read_b64_tr_b16 a[74:75], v224 offset:256\n"
    "ds_read_b64_tr_b16 a[78:79], v226 offset:256\n"
    "s_waitcnt vmcnt(12)\n"
    "ds_write2_b32 v216, a176, a177 offset0:0 offset1:20\n"
    "ds_write2_b32 v216, a178, a179 offset0:40 offset1:60\n"
    "s_waitcnt lgkmcnt(8)\n"
    "ds_write_b128 v207, v[184:187] offset:36864\n"
    "buffer_load_dwordx4 v[148:151], v209, s[80:83], s59 offen offset:256\n"
    "s_waitcnt vmcnt(12)\n"
    "ds_write2_b32 v216, a180, a181 offset0:80 offset1:100\n"
    "ds_write2_b32 v216, a182, a183 offset0:120 offset1:140\n"
    "v_add_u32_e32 v216, -4800, v216\n"
    "buffer_load_dwordx4 v[152:155], v209, s[80:83], s59 offen offset:512\n"
    "s_waitcnt vmcnt(12)\n"
    "v_or3_b32 v200, v200, v120, v121\n"
    "v_or3_b32 v200, v200, v122, v123\n"
    "v_add_u32_e64 v120, v201, v120\n"
    "v_add_u32_e64 v122, v201, v122\n"
    "ds_write_b128 v217, v[120:123] offset:12288\n"
    "s_waitcnt lgkmcnt(1)\n"
    "s_barrier\n"
    "s_mov_b32 s65, 786432\n"
    "buffer_load_dwordx4 a[200:203], v208, s[40:43], s65 offen\n"
    "s_waitcnt vmcnt(12)\n"
    "v_or3_b32 v200, v200, v124, v125\n"
    "v_or3_b32 v200, v200, v126, v127\n"
    "v_add_u32_e32 v124, v203, v124\n"
    "v_add_u32_e64 v126, v203, v126\n"
    "ds_write_b128 v217, v[124:127] offset:12544\n"
    "s_mov_b32 s65, 884736\n"
    "buffer_load_dwordx4 a[204:207], v208, s[40:43], s65 offen\n"
    "s_waitcnt vmcnt(12)\n"
    "v_or3_b32 v200, v200, v128, v129\n"
    "v_or3_b32 v200, v200, v130, v131\n"
    "v_add_u32_e32 v128, v205, v128\n"
    "v_add_u32_e64 v130, v205, v130\n"
    "ds_write_b128 v217, v[128:131] offset:12800\n"
    "ds_read_b128 a[224:227], v250 offset:0\n"
    "ds_read_b128 a[228:231], v250 offset:320\n"
    "ds_read_b128 a[232:235], v251 offset:0\n"
    "ds_read_b128 a[236:239], v251 offset:336\n"
    "ds_read_b128 a[240:243], v250 offset:80\n"
    "ds_read_b128 a[244:247], v250 offset:400\n"
    "ds_read_b128 a[248:251], v251 offset:672\n"
    "ds_read_b128 a[252:255], v251 offset:1008\n"
    "ds_read_b128 v[104:107], v252 offset:1344\n"
    "ds_read_b128 v[108:111], v252 offset:1680\n"
    "ds_read_b128 v[128:131], v252 offset:2016\n"
    "s_waitcnt lgkmcnt(11)\n"
    "s_barrier\n"
    "s_mov_b32 s59, 27648\n"
    "buffer_load_dwordx4 v[156:159], v209, s[80:83], s59 offen offset:0\n"
    "ds_read_b64_tr_b16 a[80:81], v224 offset:12288\n"
    "ds_read_b64_tr_b16 a[84:85], v225 offset:12288\n"
    "ds_read_b64_tr_b16 a[88:89], v226 offset:12288\n"
    "ds_read_b64_tr_b16 a[92:93], v227 offset:12288\n"
    "s_waitcnt lgkmcnt(12)\n"
    "v_smfmac_f32_16x16x64_bf16 v[0:3], a[232:235], a[224:231], v240\n"
    "s_waitcnt lgkmcnt(11)\n"
    "ds_read_b64_tr_b16 a[96:97], v228 offset:12288\n"
    "ds_read_b64_tr_b16 a[100:101], v229 offset:12288\n"
    "v_smfmac_f32_16x16x64_bf16 v[4:7], a[236:239], a[224:231], v240 abid:1\n"
    "ds_read_b64_tr_b16 a[104:105], v230 offset:12288\n"
    "ds_read_b64_tr_b16 a[108:109], v231 offset:12288\n"
    "ds_read_b64_tr_b16 a[112:113], v224 offset:12544\n"
    "ds_read_b64_tr_b16 a[116:117], v225 offset:12544\n"
    "ds_read_b64_tr_b16 a[120:121], v226 offset:12544\n"
    "s_waitcnt lgkmcnt(15)\n"
    "v_smfmac_f32_16x16x64_bf16 v[8:11], a[248:251], a[240:247], v240\n"
    "s_waitcnt lgkmcnt(14)\n"
    "ds_read_b64_tr_b16 a[124:125], v227 offset:12544\n"
    "s_waitcnt vmcnt(12)\n"
    "ds_write2_b32 v216, a184, a185 offset0:0 offset1:20\n"
    "v_smfmac_f32_16x16x64_bf16 v[12:15], a[252:255], a[240:247], v240 abid:1\n"
    "s_nop 0\n"
    "ds_write2_b32 v216, a186, a187 offset0:40 offset1:60\n"
    "ds_read_b128 a[224:227], v250 offset:160\n"
    "ds_read_b128 a[228:231], v250 offset:480\n"
    "ds_read_b128 a[232:235], v251 offset:1344\n"
    "ds_read_b128 a[236:239], v251 offset:1680\n"
    "buffer_load_dwordx4 v[160:163], v209, s[80:83], s59 offen offset:256\n"
    "s_waitcnt vmcnt(12)\n"
    "ds_write2_b32 v216, a188, a189 offset0:80 offset1:100\n"
    "ds_write2_b32 v216, a190, a191 offset0:120 offset1:140\n"
    "v_add_u32_e32 v216, 1360, v216\n"
    "ds_read_b128 a[240:243], v250 offset:240\n"
    "ds_read_b128 a[244:247], v250 offset:560\n"
    "buffer_load_dwordx4 v[164:167], v209, s[80:83], s59 offen offset:512\n"
    "s_waitcnt vmcnt(12)\n"
    "v_or3_b32 v200, v200, v132, v133\n"
    "v_or3_b32 v200, v200, v134, v135\n"
    "v_add_u32_e64 v132, v201, v132\n"
    "v_add_u32_e64 v134, v201, v134\n"
    "ds_write_b128 v217, v[132:135] offset:24576\n"
    "s_waitcnt lgkmcnt(6)\n"
    "v_smfmac_f32_16x16x64_bf16 v[16:19], a[232:235], a[224:231], v240\n"
    "s_waitcnt lgkmcnt(5)\n"
    "v_smfmac_f32_16x16x64_bf16 v[20:23], a[236:239], a[224:231], v240 abid:1\n"
    "s_mov_b32 s65, 1179648\n"
    "buffer_load_dwordx4 a[208:211], v208, s[40:43], s65 offen\n"
    "s_waitcnt vmcnt(12)\n"
    "v_or3_b32 v200, v200, v136, v137\n"
    "v_or3_b32 v200, v200, v138, v139\n"
    "v_add_u32_e32 v136, v203, v136\n"
    "v_add_u32_e64 v138, v203, v138\n"
    "ds_write_b128 v217, v[136:139] offset:24832\n"
    "ds_read_b128 a[248:251], v251 offset:2016\n"
    "ds_read_b128 a[252:255], v251 offset:2352\n"
    "s_mov_b32 s65, 1277952\n"
    "buffer_load_dwordx4 a[212:215], v208, s[40:43], s65 offen\n"
    "s_waitcnt vmcnt(12)\n"
    "v_or3_b32 v200, v200, v140, v141\n"
    "v_or3_b32 v200, v200, v142, v143\n"
    "v_add_u32_e32 v140, v205, v140\n"
    "v_add_u32_e64 v142, v205, v142\n"
    "ds_write_b128 v217, v[140:143] offset:25088\n"
    "ds_read_b128 a[224:227], v250 offset:27024\n"
    "ds_read_b128 a[228:231], v250 offset:27344\n"
    "ds_read_b128 a[232:235], v251 offset:10752\n"
    "ds_read_b128 a[236:239], v251 offset:11088\n"
    "ds_read_b128 v[132:135], v252 offset:2352\n"
    "s_waitcnt lgkmcnt(7)\n"
    "v_smfmac_f32_16x16x64_bf16 v[24:27], a[248:251], a[240:247], v240\n"
    "ds_read_b128 a[248:251], v251 offset:11424\n"
    "s_waitcnt lgkmcnt(7)\n"
    "v_smfmac_f32_16x16x64_bf16 v[28:31], a[252:255], a[240:247], v240 abid:1\n"
    "ds_read_b128 a[240:243], v250 offset:27104\n"
    "ds_read_b128 a[244:247], v250 offset:27424\n"
    "ds_read_b128 a[252:255], v251 offset:11760\n"
    "s_waitcnt lgkmcnt(9)\n"
    "s_barrier\n"
    "s_mov_b32 s65, 1572864\n"
    "buffer_load_dwordx4 a[216:219], v208, s[40:43], s65 offen\n"
    "ds_read_b64_tr_b16 a[82:83], v224 offset:24576\n"
    "ds_read_b64_tr_b16 a[86:87], v225 offset:24576\n"
    "ds_read_b64_tr_b16 a[90:91], v226 offset:24576\n"
    "s_waitcnt lgkmcnt(9)\n"
    "v_smfmac_f32_16x16x64_bf16 v[32:35], a[232:235], a[224:231], v240\n"
    "s_waitcnt lgkmcnt(8)\n"
    "ds_read_b64_tr_b16 a[94:95], v227 offset:24576\n"
    "ds_read_b64_tr_b16 a[98:99], v228 offset:24576\n"
    "v_smfmac_f32_16x16x64_bf16 v[36:39], a[236:239], a[224:231], v240 abid:1\n"
    "ds_read_b64_tr_b16 a[102:103], v229 offset:24576\n"
    "ds_read_b64_tr_b16 a[106:107], v230 offset:24576\n"
    "ds_read_b64_tr_b16 a[110:111], v231 offset:24576\n"
    "ds_read_b64_tr_b16 a[114:115], v224 offset:24832\n"
    "ds_read_b64_tr_b16 a[118:119], v225 offset:24832\n"
    "ds_read_b64_tr_b16 a[122:123], v226 offset:24832\n"
    "ds_read_b64_tr_b16 a[126:127], v227 offset:24832\n"
    "s_waitcnt lgkmcnt(13)\n"
    "v_smfmac_f32_16x16x64_bf16 v[40:43], a[248:251], a[240:247], v240\n"
    "s_waitcnt lgkmcnt(12)\n"
    "s_waitcnt vmcnt(12)\n"
    "ds_write2_b32 v216, a192, a193 offset0:0 offset1:20\n"
    "v_smfmac_f32_16x16x64_bf16 v[44:47], a[252:255], a[240:247], v240 abid:1\n"
    "s_nop 0\n"
    "ds_write2_b32 v216, a194, a195 offset0:40 offset1:60\n"
    "ds_read_b128 a[224:227], v250 offset:27184\n"
    "ds_read_b128 a[228:231], v250 offset:27504\n"
    "ds_read_b128 a[232:235], v251 offset:12096\n"
    "s_mov_b32 s65, 1671168\n"
    "buffer_load_dwordx4 a[220:223], v208, s[40:43], s65 offen\n"
    "s_waitcnt vmcnt(12)\n"
    "ds_write2_b32 v216, a196, a197 offset0:80 offset1:100\n"
    "ds_write2_b32 v216, a198, a199 offset0:120 offset1:140\n"
    "v_add_u32_e32 v216, 1360, v216\n"
    "ds_read_b128 a[236:239], v251 offset:12432\n"
    "ds_read_b128 a[240:243], v250 offset:27264\n"
    "ds_read_b128 a[244:247], v250 offset:27584\n"
    "s_waitcnt vmcnt(11)\n"
    "v_or3_b32 v200, v200, v144, v145\n"
    "v_or3_b32 v200, v200, v146, v147\n"
    "v_add_u32_e64 v144, v201, v144\n"
    "v_add_u32_e32 v146, v201, v146\n"
    "ds_write_b128 v217, v[144:147] offset:0\n"
    "s_waitcnt lgkmcnt(6)\n"
    "v_smfmac_f32_16x16x64_bf16 v[48:51], a[232:235], a[224:231], v240\n"
    "ds_read_b128 a[248:251], v251 offset:12768\n"
    "ds_read_b128 a[252:255], v251 offset:13104\n"
    "ds_read_b128 a[232:235], v251 offset:21504\n"
    "s_waitcnt vmcnt(10)\n"
    "v_or3_b32 v200, v200, v148, v149\n"
    "v_or3_b32 v200, v200, v150, v151\n"
    "v_add_u32_e32 v148, v203, v148\n"
    "v_add_u32_e32 v150, v203, v150\n"
    "ds_write_b128 v217, v[148:151] offset:256\n"
    "s_waitcnt lgkmcnt(6)\n"
    "v_smfmac_f32_16x16x64_bf16 v[52:55], a[236:239], a[224:231], v240 abid:1\n"
    "ds_read_b128 a[224:227], v250 offset:54048\n"
    "ds_read_b128 a[228:231], v250 offset:54368\n"
    "ds_read_b128 a[236:239], v251 offset:21840\n"
    "s_waitcnt vmcnt(9)\n"
    "v_or3_b32 v200, v200, v152, v153\n"
    "v_or3_b32 v200, v200, v154, v155\n"
    "v_add_u32_e32 v152, v205, v152\n"
    "v_add_u32_e32 v154, v205, v154\n"
    "ds_write_b128 v217, v[152:155] offset:512\n"
    "s_waitcnt lgkmcnt(6)\n"
    "v_smfmac_f32_16x16x64_bf16 v[56:59], a[248:251], a[240:247], v240\n"
    "v_smfmac_f32_16x16x64_bf16 v[60:63], a[252:255], a[240:247], v240 abid:1\n"
    "ds_read_b128 a[240:243], v250 offset:54128\n"
    "ds_read_b128 a[244:247], v250 offset:54448\n"
    "ds_read_b128 a[248:251], v251 offset:22176\n"
    "ds_read_b128 a[252:255], v251 offset:22512\n"
    "ds_read_b128 v[144:147], v252 offset:10752\n"
    "ds_read_b128 v[148:151], v252 offset:11088\n"
    "s_waitcnt lgkmcnt(8)\n"
    "v_smfmac_f32_16x16x64_bf16 v[64:67], a[232:235], a[224:231], v240\n"
    "ds_read_b128 a[232:235], v251 offset:22848\n"
    "s_waitcnt lgkmcnt(8)\n"
    "v_smfmac_f32_16x16x64_bf16 v[68:71], a[236:239], a[224:231], v240 abid:1\n"
    "ds_read_b128 a[224:227], v250 offset:54208\n"
    "ds_read_b128 a[228:231], v250 offset:54528\n"
    "s_waitcnt lgkmcnt(9)\n"
    "s_barrier\n"
    "ds_read_b64_tr_b16 a[128:129], v224 offset:0\n"
    "ds_read_b64_tr_b16 a[132:133], v225 offset:0\n"
    "ds_read_b64_tr_b16 a[136:137], v226 offset:0\n"
    "s_waitcnt lgkmcnt(9)\n"
    "v_smfmac_f32_16x16x64_bf16 v[72:75], a[248:251], a[240:247], v240\n"
    "s_waitcnt lgkmcnt(8)\n"
    "ds_read_b64_tr_b16 a[140:141], v227 offset:0\n"
    "ds_read_b64_tr_b16 a[144:145], v228 offset:0\n"
    "v_smfmac_f32_16x16x64_bf16 v[76:79], a[252:255], a[240:247], v240 abid:1\n"
    "ds_read_b64_tr_b16 a[148:149], v229 offset:0\n"
    "ds_read_b64_tr_b16 a[152:153], v230 offset:0\n"
    "ds_read_b64_tr_b16 a[156:157], v231 offset:0\n"
    "ds_read_b64_tr_b16 a[160:161], v224 offset:256\n"
    "ds_read_b64_tr_b16 a[164:165], v225 offset:256\n"
    "ds_read_b64_tr_b16 a[168:169], v226 offset:256\n"
    "ds_read_b64_tr_b16 a[172:173], v227 offset:256\n"
    "s_waitcnt vmcnt(8)\n"
    "ds_write2_b32 v216, a200, a201 offset0:0 offset1:20\n"
    "s_waitcnt lgkmcnt(13)\n"
    "v_smfmac_f32_16x16x64_bf16 v[80:83], a[232:235], a[224:231], v240\n"
    "ds_write2_b32 v216, a202, a203 offset0:40 offset1:60\n"
    "ds_read_b128 a[236:239], v251 offset:23184\n"
    "ds_read_b128 a[240:243], v250 offset:54288\n"
    "ds_read_b128 a[244:247], v250 offset:54608\n"
    "ds_read_b128 a[248:251], v251 offset:23520\n"
    "s_waitcnt vmcnt(7)\n"
    "ds_write2_b32 v216, a204, a205 offset0:80 offset1:100\n"
    "ds_write2_b32 v216, a206, a207 offset0:120 offset1:140\n"
    "v_add_u32_e32 v216, 1360, v216\n"
    "ds_read_b128 a[252:255], v251 offset:23856\n"
    "ds_read_b128 a[232:235], v252 offset:0\n"
    "s_waitcnt vmcnt(6)\n"
    "v_or3_b32 v200, v200, v156, v157\n"
    "v_or3_b32 v200, v200, v158, v159\n"
    "v_add_u32_e64 v156, v201, v156\n"
    "v_add_u32_e64 v158, v201, v158\n"
    "ds_write_b128 v217, v[156:159] offset:12288\n"
    "s_waitcnt lgkmcnt(7)\n"
    "v_smfmac_f32_16x16x64_bf16 v[84:87], a[236:239], a[224:231], v240 abid:1\n"
    "s_waitcnt lgkmcnt(5)\n"
    "v_smfmac_f32_16x16x64_bf16 v[88:91], a[248:251], a[240:247], v240\n"
    "ds_read_b128 a[236:239], v252 offset:336\n"
    "ds_read_b128 a[248:251], v252 offset:672\n"
    "s_waitcnt vmcnt(5)\n"
    "v_or3_b32 v200, v200, v160, v161\n"
    "v_or3_b32 v200, v200, v162, v163\n"
    "v_add_u32_e32 v160, v203, v160\n"
    "v_add_u32_e64 v162, v203, v162\n"
    "ds_write_b128 v217, v[160:163] offset:12544\n"
    "s_waitcnt lgkmcnt(4)\n"
    "v_smfmac_f32_16x16x64_bf16 v[92:95], a[252:255], a[240:247], v240 abid:1\n"
    "ds_read_b128 a[252:255], v252 offset:1008\n"
    "ds_read_b128 v[160:163], v252 offset:11424\n"
    "s_waitcnt vmcnt(4)\n"
    "v_or3_b32 v200, v200, v164, v165\n"
    "v_or3_b32 v200, v200, v166, v167\n"
    "v_add_u32_e64 v164, v205, v164\n"
    "v_add_u32_e64 v166, v205, v166\n"
    "ds_write_b128 v217, v[164:167] offset:12800\n"
    "v_and_b32_e32 v200, 0x40004000, v200\n"
    "v_cmp_le_u32_e32 vcc, 1, v200\n"
    "s_nop 1\n"
    "s_cmp_lg_u64 vcc, 0\n"
    "s_cselect_b32 vcc_lo, 1, 0\n"
    "s_or_b32 s66, s66, vcc_lo\n"
    "v_cmp_le_u32_e32 vcc, 981467136, v232\n"
    "s_nop 1\n"
    "s_cmp_lg_u64 vcc, 0\n"
    "s_cselect_b32 vcc_lo, 2, 0\n"
    "s_or_b32 s66, s66, vcc_lo\n"
    "v_mov_b32_e32 v200, s66\n"
    "v_mov_b32_e32 v232, 162992\n"
    "s_mov_b64 exec, 1\n"
    "ds_or_b32 v232, v200\n"
    "s_mov_b64 exec, -1\n"
    "ds_read_b128 v[164:167], v252 offset:11760\n"
    "s_waitcnt lgkmcnt(2)\n"
    "s_barrier\n"
    "ds_read_b64_tr_b16 a[130:131], v224 offset:12288\n"
    "ds_read_b64_tr_b16 a[134:135], v225 offset:12288\n"
    "ds_read_b64_tr_b16 a[138:139], v226 offset:12288\n"
    "ds_read_b64_tr_b16 a[142:143], v227 offset:12288\n"
    "ds_read_b64_tr_b16 a[146:147], v228 offset:12288\n"
    "ds_read_b64_tr_b16 a[150:151], v229 offset:12288\n"
    "ds_read_b64_tr_b16 a[154:155], v230 offset:12288\n"
    "ds_read_b64_tr_b16 a[158:159], v231 offset:12288\n"
    "ds_read_b64_tr_b16 a[162:163], v224 offset:12544\n"
    "ds_read_b64_tr_b16 a[166:167], v225 offset:12544\n"
    "ds_read_b64_tr_b16 a[170:171], v226 offset:12544\n"
    "ds_read_b64_tr_b16 a[174:175], v227 offset:12544\n"
    "s_nop 0\n"
    "s_waitcnt vmcnt(3)\n"
    "ds_write2_b32 v216, a208, a209 offset0:0 offset1:20\n"
    "ds_write2_b32 v216, a210, a211 offset0:40 offset1:60\n"
    "s_waitcnt vmcnt(2)\n"
    "ds_write2_b32 v216, a212, a213 offset0:80 offset1:100\n"
    "s_waitcnt lgkmcnt(2)\n"
    "s_barrier\n"
    "s_add_u32 m0, s38, 0\n"
    "s_mov_b32 s64, 18432\n"
    "buffer_load_dwordx4 v210, s[84:87], s64 offen offset:0 lds\n"
    "buffer_load_dwordx4 v210, s[84:87], s64 offen offset:1024 lds\n"
    "buffer_load_dwordx4 v210, s[84:87], s64 offen offset:2048 lds\n"
    "s_add_u32 m0, s38, 3072\n"
    "s_mov_b32 s64, 21504\n"
    "buffer_load_dwordx4 v210, s[84:87], s64 offen offset:0 lds\n"
    "buffer_load_dwordx4 v210, s[84:87], s64 offen offset:1024 lds\n"
    "buffer_load_dwordx4 v210, s[84:87], s64 offen offset:2048 lds\n"
    "s_add_u32 m0, s38, 6144\n"
    "s_mov_b32 s64, 24576\n"
    "buffer_load_dwordx4 v210, s[84:87], s64 offen offset:0 lds\n"
    "buffer_load_dwordx4 v210, s[84:87], s64 offen offset:1024 lds\n"
    "buffer_load_dwordx4 v210, s[84:87], s64 offen offset:2048 lds\n"
    "v_lshlrev_b32_e64 v235, 3, v244\n"
    "s_mov_b32 exec_hi, 0\n"
    "buffer_load_dword v233, v235, s[40:43], s53 offen sc1\n"
    "s_mov_b32 exec_hi, -1\n"
    "s_add_u32 s53, s53, 393216\n"
    "ds_write2_b32 v216, a214, a215 offset0:120 offset1:140\n"
    "v_add_u32_e32 v216, 1360, v216\n"
    "s_mov_b32 s64, 0\n"
    "s_mov_b32 s59, 0\n"
    "ds_read_b32 v232, v232\n"
    "v_lshlrev_b32_e32 v235, 3, v244\n"
    "s_mov_b32 exec_hi, 0\n"
    "buffer_load_dword v233, v235, s[40:43], s53 offen sc1\n"
    "s_mov_b32 exec_hi, -1\n"
    "s_add_u32 s53, s53, 393216\n"
    "s_waitcnt vmcnt(12)\n"
    "ds_write2_b32 v216, a216, a217 offset0:0 offset1:20\n"
    "ds_write2_b32 v216, a218, a219 offset0:40 offset1:60\n"
    "v_lshlrev_b32_e32 v235, 3, v244\n"
    "s_mov_b32 exec_hi, 0\n"
    "buffer_load_dword v233, v235, s[40:43], s53 offen sc1\n"
    "s_mov_b32 exec_hi, -1\n"
    "s_add_u32 s53, s53, 393216\n"
    "s_waitcnt vmcnt(12)\n"
    "ds_write2_b32 v216, a220, a221 offset0:80 offset1:100\n"
    "ds_write2_b32 v216, a222, a223 offset0:120 offset1:140\n"
    "v_lshlrev_b32_e64 v235, 3, v244\n"
    "s_mov_b32 exec_hi, 0\n"
    "buffer_load_dword v233, v235, s[40:43], s53 offen sc1\n"
    "s_mov_b32 exec_hi, -1\n"
    "s_add_u32 s53, s53, 393216\n"
    "v_mov_b32_e64 v235, v222\n"
    "s_waitcnt lgkmcnt(0)\n"
    "s_barrier\n"
    "s_getpc_b64 vcc\n"
    ".Lreplk_pc2:\n"
    "s_sub_u32 vcc_lo, vcc_lo, .Lreplk_pc2-.Lreplk_pc0\n"
    "s_subb_u32 vcc_hi, vcc_hi, 0\n"
    "global_load_dword v233, v215, vcc\n"
    "ds_read_b128 a[224:227], v250 offset:640\n"
    "ds_read_b128 a[228:231], v250 offset:960\n"
    "ds_read_b128 a[240:243], v250 offset:720\n"
    "ds_read_b128 a[244:247], v250 offset:1040\n"
    "ds_read_b128 v[96:99], v250 offset:800\n"
    "ds_read_b128 v[100:103], v250 offset:1120\n"
    "ds_read_b128 v[120:123], v250 offset:880\n"
    "ds_read_b128 v[124:127], v250 offset:1200\n"
    "ds_read_b128 v[136:139], v250 offset:27664\n"
    "ds_read_b128 v[140:143], v250 offset:27984\n"
    "ds_read_b128 v[152:155], v250 offset:27744\n"
    "ds_read_b128 v[156:159], v250 offset:28064\n"
    "s_waitcnt lgkmcnt(10)\n"
    "v_smfmac_f32_16x16x64_bf16 v[0:3], a[232:235], a[224:231], v240\n"
    "s_nop 0\n"
    "ds_read_b128 a[232:235], v252 offset:12096\n"
    "v_smfmac_f32_16x16x64_bf16 v[4:7], a[236:239], a[224:231], v240 abid:1\n"
    "ds_read_b128 a[224:227], v250 offset:27824\n"
    "s_waitcnt lgkmcnt(10)\n"
    "v_smfmac_f32_16x16x64_bf16 v[8:11], a[248:251], a[240:247], v240\n"
    "ds_read_b128 a[228:231], v250 offset:28144\n"
    "v_smfmac_f32_16x16x64_bf16 v[12:15], a[252:255], a[240:247], v240 abid:1\n"
    "s_waitcnt lgkmcnt(9)\n"
    "ds_read_b128 a[236:239], v252 offset:12432\n"
    "v_smfmac_f32_16x16x64_bf16 v[16:19], v[104:107], v[96:103], v240\n"
    "ds_read_b128 a[240:243], v250 offset:27904\n"
    "v_smfmac_f32_16x16x64_bf16 v[20:23], v[108:111], v[96:103], v240 abid:1\n"
    "s_waitcnt lgkmcnt(9)\n"
    "ds_read_b128 a[244:247], v250 offset:28224\n"
    "v_smfmac_f32_16x16x64_bf16 v[24:27], v[128:131], v[120:127], v240\n"
    "ds_read_b128 a[248:251], v252 offset:12768\n"
    "v_smfmac_f32_16x16x64_bf16 v[28:31], v[132:135], v[120:127], v240 abid:1\n"
    "s_waitcnt lgkmcnt(9)\n"
    "ds_read_b128 a[252:255], v252 offset:13104\n"
    "v_smfmac_f32_16x16x64_bf16 v[32:35], v[144:147], v[136:143], v240\n"
    "s_nop 0\n"
    "ds_read_b128 v[96:99], v250 offset:54688\n"
    "v_smfmac_f32_16x16x64_bf16 v[36:39], v[148:151], v[136:143], v240 abid:1\n"
    "s_waitcnt lgkmcnt(9)\n"
    "ds_read_b128 v[100:103], v250 offset:55008\n"
    "v_smfmac_f32_16x16x64_bf16 v[40:43], v[160:163], v[152:159], v240\n"
    "ds_read_b128 v[104:107], v252 offset:21504\n"
    "v_smfmac_f32_16x16x64_bf16 v[44:47], v[164:167], v[152:159], v240 abid:1\n"
    "ds_read_b128 v[108:111], v252 offset:21840\n"
    "ds_read_b128 v[120:123], v250 offset:54768\n"
    "ds_read_b128 v[124:127], v250 offset:55088\n"
    "v_pk_mov_b32 v[224:225], v[0:1], v[0:1] op_sel:[0,1]\n"
    "v_pk_mov_b32 v[226:227], v[2:3], v[2:3] op_sel:[0,1]\n"
    "v_pk_mul_f32 v[228:229], v[0:1], v[0:1]\n"
    "v_pk_mul_f32 v[230:231], v[2:3], v[2:3]\n"
    "v_pk_add_f32 v[224:225], v[224:225], v[4:5]\n"
    "v_pk_add_f32 v[226:227], v[226:227], v[6:7]\n"
    "v_pk_fma_f32 v[228:229], v[4:5], v[4:5], v[228:229]\n"
    "v_pk_fma_f32 v[230:231], v[6:7], v[6:7], v[230:231]\n"
    "v_pk_add_f32 v[224:225], v[224:225], v[8:9]\n"
    "v_pk_add_f32 v[226:227], v[226:227], v[10:11]\n"
    "v_pk_fma_f32 v[228:229], v[8:9], v[8:9], v[228:229]\n"
    "v_pk_fma_f32 v[230:231], v[10:11], v[10:11], v[230:231]\n"
    "v_pk_add_f32 v[224:225], v[224:225], v[12:13]\n"
    "v_pk_add_f32 v[226:227], v[226:227], v[14:15]\n"
    "v_pk_fma_f32 v[228:229], v[12:13], v[12:13], v[228:229]\n"
    "v_pk_fma_f32 v[230:231], v[14:15], v[14:15], v[230:231]\n"
    "v_pk_add_f32 v[224:225], v[224:225], v[16:17]\n"
    "v_pk_add_f32 v[226:227], v[226:227], v[18:19]\n"
    "v_pk_fma_f32 v[228:229], v[16:17], v[16:17], v[228:229]\n"
    "v_pk_fma_f32 v[230:231], v[18:19], v[18:19], v[230:231]\n"
    "v_pk_add_f32 v[224:225], v[224:225], v[20:21]\n"
    "v_pk_add_f32 v[226:227], v[226:227], v[22:23]\n"
    "v_pk_fma_f32 v[228:229], v[20:21], v[20:21], v[228:229]\n"
    "v_pk_fma_f32 v[230:231], v[22:23], v[22:23], v[230:231]\n"
    "v_pk_add_f32 v[224:225], v[224:225], v[24:25]\n"
    "v_pk_add_f32 v[226:227], v[226:227], v[26:27]\n"
    "v_pk_fma_f32 v[228:229], v[24:25], v[24:25], v[228:229]\n"
    "v_pk_fma_f32 v[230:231], v[26:27], v[26:27], v[230:231]\n"
    "v_pk_add_f32 v[224:225], v[224:225], v[28:29]\n"
    "v_pk_add_f32 v[226:227], v[226:227], v[30:31]\n"
    "v_pk_fma_f32 v[228:229], v[28:29], v[28:29], v[228:229]\n"
    "v_pk_fma_f32 v[230:231], v[30:31], v[30:31], v[230:231]\n"
    "v_pk_add_f32 v[224:225], v[224:225], v[32:33]\n"
    "v_pk_add_f32 v[226:227], v[226:227], v[34:35]\n"
    "v_pk_fma_f32 v[228:229], v[32:33], v[32:33], v[228:229]\n"
    "v_pk_fma_f32 v[230:231], v[34:35], v[34:35], v[230:231]\n"
    "v_pk_add_f32 v[224:225], v[224:225], v[36:37]\n"
    "v_pk_add_f32 v[226:227], v[226:227], v[38:39]\n"
    "v_pk_fma_f32 v[228:229], v[36:37], v[36:37], v[228:229]\n"
    "v_pk_fma_f32 v[230:231], v[38:39], v[38:39], v[230:231]\n"
    "v_pk_add_f32 v[224:225], v[224:225], v[40:41]\n"
    "v_pk_add_f32 v[226:227], v[226:227], v[42:43]\n"
    "v_pk_fma_f32 v[228:229], v[40:41], v[40:41], v[228:229]\n"
    "v_pk_fma_f32 v[230:231], v[42:43], v[42:43], v[230:231]\n"
    "v_pk_add_f32 v[224:225], v[224:225], v[44:45]\n"
    "v_pk_add_f32 v[226:227], v[226:227], v[46:47]\n"
    "v_pk_fma_f32 v[228:229], v[44:45], v[44:45], v[228:229]\n"
    "v_pk_fma_f32 v[230:231], v[46:47], v[46:47], v[230:231]\n"
    "s_waitcnt lgkmcnt(0)\n"
    "v_smfmac_f32_16x16x64_bf16 v[48:51], a[232:235], a[224:231], v240\n"
    "s_nop 0\n"
    "ds_read_b128 v[128:131], v252 offset:22176\n"
    "v_smfmac_f32_16x16x64_bf16 v[52:55], a[236:239], a[224:231], v240 abid:1\n"
    "ds_read_b128 v[132:135], v252 offset:22512\n"
    "v_smfmac_f32_16x16x64_bf16 v[56:59], a[248:251], a[240:247], v240\n"
    "ds_read_b128 v[136:139], v250 offset:54848\n"
    "v_smfmac_f32_16x16x64_bf16 v[60:63], a[252:255], a[240:247], v240 abid:1\n"
    "ds_read_b128 v[140:143], v250 offset:55168\n"
    "v_smfmac_f32_16x16x64_bf16 v[64:67], v[104:107], v[96:103], v240\n"
    "ds_read_b128 v[144:147], v252 offset:22848\n"
    "v_smfmac_f32_16x16x64_bf16 v[68:71], v[108:111], v[96:103], v240 abid:1\n"
    "ds_read_b128 v[148:151], v252 offset:23184\n"
    "ds_read_b128 v[152:155], v250 offset:54928\n"
    "v_accvgpr_write_b32 a248, v236\n"
    "ds_read_b128 v[156:159], v250 offset:55248\n"
    "v_pk_add_f32 v[224:225], v[224:225], v[48:49]\n"
    "v_pk_add_f32 v[226:227], v[226:227], v[50:51]\n"
    "v_pk_fma_f32 v[228:229], v[48:49], v[48:49], v[228:229]\n"
    "v_pk_fma_f32 v[230:231], v[50:51], v[50:51], v[230:231]\n"
    "v_pk_add_f32 v[224:225], v[224:225], v[52:53]\n"
    "s_waitcnt vmcnt(5)\n"
    "s_barrier\n"
    "ds_read_b64_tr_b16 a[176:177], v220 offset:0\n"
    "ds_read_b64_tr_b16 a[178:179], v220 offset:3072\n"
    "v_pk_add_f32 v[226:227], v[226:227], v[54:55]\n"
    "ds_read_b64_tr_b16 a[180:181], v220 offset:8\n"
    "ds_read_b64_tr_b16 a[182:183], v220 offset:3080\n"
    "ds_read_b64_tr_b16 a[184:185], v220 offset:64\n"
    "v_pk_fma_f32 v[228:229], v[52:53], v[52:53], v[228:229]\n"
    "ds_read_b64_tr_b16 a[186:187], v220 offset:3136\n"
    "ds_read_b64_tr_b16 a[188:189], v220 offset:72\n"
    "ds_read_b64_tr_b16 a[190:191], v220 offset:3144\n"
    "v_pk_fma_f32 v[230:231], v[54:55], v[54:55], v[230:231]\n"
    "ds_read_b64_tr_b16 v[188:189], v218 offset:128\n"
    "ds_read_b64_tr_b16 v[190:191], v218 offset:3200\n"
    "ds_read_b64_tr_b16 a[192:193], v220 offset:6144\n"
    "v_pk_add_f32 v[224:225], v[224:225], v[56:57]\n"
    "ds_read_b64_tr_b16 a[194:195], v220 offset:9216\n"
    "ds_read_b64_tr_b16 a[196:197], v220 offset:6152\n"
    "ds_read_b64_tr_b16 a[198:199], v220 offset:9224\n"
    "v_pk_add_f32 v[226:227], v[226:227], v[58:59]\n"
    "ds_read_b64_tr_b16 a[200:201], v220 offset:6208\n"
    "ds_read_b64_tr_b16 a[202:203], v220 offset:9280\n"
    "ds_read_b64_tr_b16 a[204:205], v220 offset:6216\n"
    "v_pk_fma_f32 v[228:229], v[56:57], v[56:57], v[228:229]\n"
    "ds_read_b64_tr_b16 a[206:207], v220 offset:9288\n"
    "ds_read_b64_tr_b16 v[192:193], v218 offset:6272\n"
    "ds_read_b64_tr_b16 v[194:195], v218 offset:9344\n"
    "v_pk_fma_f32 v[230:231], v[58:59], v[58:59], v[230:231]\n"
    "ds_read_b64_tr_b16 a[208:209], v220 offset:12288\n"
    "ds_read_b64_tr_b16 a[210:211], v220 offset:15360\n"
    "ds_read_b64_tr_b16 a[212:213], v220 offset:12296\n"
    "v_pk_add_f32 v[224:225], v[224:225], v[60:61]\n"
    "ds_read_b64_tr_b16 a[214:215], v220 offset:15368\n"
    "ds_read_b64_tr_b16 a[216:217], v220 offset:12352\n"
    "ds_read_b64_tr_b16 a[218:219], v220 offset:15424\n"
    "v_pk_add_f32 v[226:227], v[226:227], v[62:63]\n"
    "ds_read_b64_tr_b16 a[220:221], v220 offset:12360\n"
    "ds_read_b64_tr_b16 a[222:223], v220 offset:15432\n"
    "ds_read_b64_tr_b16 v[196:197], v218 offset:12416\n"
    "v_pk_fma_f32 v[228:229], v[60:61], v[60:61], v[228:229]\n"
    "ds_read_b64_tr_b16 v[198:199], v218 offset:15488\n"
    "v_pk_fma_f32 v[230:231], v[62:63], v[62:63], v[230:231]\n"
    "v_pk_add_f32 v[224:225], v[224:225], v[64:65]\n"
    "v_pk_add_f32 v[226:227], v[226:227], v[66:67]\n"
    "v_pk_fma_f32 v[228:229], v[64:65], v[64:65], v[228:229]\n"
    "v_pk_fma_f32 v[230:231], v[66:67], v[66:67], v[230:231]\n"
    "v_pk_add_f32 v[224:225], v[224:225], v[68:69]\n"
    "v_pk_add_f32 v[226:227], v[226:227], v[70:71]\n"
    "v_pk_fma_f32 v[228:229], v[68:69], v[68:69], v[228:229]\n"
    "v_pk_fma_f32 v[230:231], v[70:71], v[70:71], v[230:231]\n"
    "s_waitcnt lgkmcnt(10)\n"
    "v_smfmac_f32_16x16x64_bf16 v[72:75], v[128:131], v[120:127], v240\n"
    "s_nop 0\n"
    "ds_read_b128 v[160:163], v252 offset:23520\n"
    "v_smfmac_f32_16x16x64_bf16 v[76:79], v[132:135], v[120:127], v240 abid:1\n"
    "ds_read_b128 v[164:167], v252 offset:23856\n"
    "v_smfmac_f32_16x16x64_bf16 v[80:83], v[144:147], v[136:143], v240\n"
    "v_accvgpr_write_b32 a249, v237\n"
    "v_smfmac_f32_16x16x64_bf16 v[84:87], v[148:151], v[136:143], v240 abid:1\n"
    "v_accvgpr_write_b32 a250, v238\n"
    "v_accvgpr_write_b32 a251, v239\n"
    "v_pk_add_f32 v[224:225], v[224:225], v[72:73]\n"
    "v_pk_add_f32 v[226:227], v[226:227], v[74:75]\n"
    "v_pk_fma_f32 v[228:229], v[72:73], v[72:73], v[228:229]\n"
    "v_pk_fma_f32 v[230:231], v[74:75], v[74:75], v[230:231]\n"
    "v_pk_add_f32 v[224:225], v[224:225], v[76:77]\n"
    "v_pk_add_f32 v[226:227], v[226:227], v[78:79]\n"
    "v_pk_fma_f32 v[228:229], v[76:77], v[76:77], v[228:229]\n"
    "v_pk_fma_f32 v[230:231], v[78:79], v[78:79], v[230:231]\n"
    "v_pk_add_f32 v[224:225], v[224:225], v[80:81]\n"
    "v_pk_add_f32 v[226:227], v[226:227], v[82:83]\n"
    "v_pk_fma_f32 v[228:229], v[80:81], v[80:81], v[228:229]\n"
    "v_pk_fma_f32 v[230:231], v[82:83], v[82:83], v[230:231]\n"
    "v_pk_add_f32 v[224:225], v[224:225], v[84:85]\n"
    "v_pk_add_f32 v[226:227], v[226:227], v[86:87]\n"
    "v_pk_fma_f32 v[228:229], v[84:85], v[84:85], v[228:229]\n"
    "v_pk_fma_f32 v[230:231], v[86:87], v[86:87], v[230:231]\n"
    "s_waitcnt lgkmcnt(1)\n"
    "v_smfmac_f32_16x16x64_bf16 v[88:91], v[160:163], v[152:159], v240\n"
    "s_waitcnt lgkmcnt(0)\n"
    "v_smfmac_f32_16x16x64_bf16 v[92:95], v[164:167], v[152:159], v240 abid:1\n"
    "s_nop 0\n"
    "s_nop 5\n"
    "v_pk_add_f32 v[224:225], v[224:225], v[88:89]\n"
    "v_pk_add_f32 v[226:227], v[226:227], v[90:91]\n"
    "v_pk_fma_f32 v[228:229], v[88:89], v[88:89], v[228:229]\n"
    "v_pk_fma_f32 v[230:231], v[90:91], v[90:91], v[230:231]\n"
    "v_pk_add_f32 v[224:225], v[224:225], v[92:93]\n"
    "v_pk_add_f32 v[226:227], v[226:227], v[94:95]\n"
    "v_pk_fma_f32 v[228:229], v[92:93], v[92:93], v[228:229]\n"
    "v_pk_fma_f32 v[230:231], v[94:95], v[94:95], v[230:231]\n"
    "ds_write_b128 v254, v[224:227]\n"
    "ds_write_b128 v254, v[228:231] offset:1024\n"
    "v_add_u32_e64 v254, s56, v242\n"
    "s_waitcnt lgkmcnt(0)\n"
    "s_barrier\n"
    "ds_read_b128 v[128:131], v254 offset:0\n"
    "ds_read_b128 v[132:135], v254 offset:6752\n"
    "ds_read_b128 v[136:139], v254 offset:13504\n"
    "ds_read_b128 v[140:143], v254 offset:20256\n"
    "ds_read_b128 v[144:147], v254 offset:1024\n"
    "ds_read_b128 v[148:151], v254 offset:7776\n"
    "ds_read_b128 v[152:155], v254 offset:14528\n"
    "ds_read_b128 v[156:159], v254 offset:21280\n"
    "ds_write_b128 v234, v[112:115] offset:0\n"
    "ds_write_b128 v234, v[116:119] offset:16\n"
    "s_waitcnt lgkmcnt(8)\n"
    "v_pk_add_f32 v[128:129], v[128:129], v[132:133]\n"
    "v_pk_add_f32 v[130:131], v[130:131], v[134:135]\n"
    "ds_write_b128 v207, v[168:171] offset:0\n"
    "s_waitcnt lgkmcnt(6)\n"
    "v_pk_add_f32 v[136:137], v[136:137], v[140:141]\n"
    "v_pk_add_f32 v[138:139], v[138:139], v[142:143]\n"
    "s_waitcnt lgkmcnt(5)\n"
    "v_pk_add_f32 v[144:145], v[144:145], v[148:149]\n"
    "v_pk_add_f32 v[146:147], v[146:147], v[150:151]\n"
    "v_pk_add_f32 v[128:129], v[128:129], v[136:137]\n"
    "v_pk_add_f32 v[130:131], v[130:131], v[138:139]\n"
    "v_mul_f32_e32 v128, 0x3c2aaaab, v128\n"
    "v_mul_f32_e32 v129, 0x3c2aaaab, v129\n"
    "v_mul_f32_e32 v130, 0x3c2aaaab, v130\n"
    "s_waitcnt lgkmcnt(3)\n"
    "v_pk_add_f32 v[152:153], v[152:153], v[156:157]\n"
    "v_pk_add_f32 v[154:155], v[154:155], v[158:159]\n"
    "v_pk_add_f32 v[144:145], v[144:145], v[152:153]\n"
    "v_pk_add_f32 v[146:147], v[146:147], v[154:155]\n"
    "v_mul_f32_e32 v131, 0x3c2aaaab, v131\n"
    "v_mul_f32_e32 v144, 0x3c2aaaab, v144\n"
    "v_mul_f32_e32 v145, 0x3c2aaaab, v145\n"
    "v_mul_f32_e32 v146, 0x3c2aaaab, v146\n"
    "v_mul_f32_e32 v147, 0x3c2aaaab, v147\n"
    "v_fma_f32 v144, -v128, v128, v144\n"
    "v_fma_f32 v145, -v129, v129, v145\n"
    "v_fma_f32 v146, -v130, v130, v146\n"
    "v_fma_f32 v147, -v131, v131, v147\n"
    "v_max_f32_e64 v144, 0, v144\n"
    "v_max_f32_e64 v145, 0, v145\n"
    "v_max_f32_e64 v146, 0, v146\n"
    "v_max_f32_e64 v147, 0, v147\n"
    "v_add_f32_e32 v144, 0x3727c5ac, v144\n"
    "v_add_f32_e32 v145, 0x3727c5ac, v145\n"
    "v_add_f32_e32 v146, 0x3727c5ac, v146\n"
    "v_add_f32_e32 v147, 0x3727c5ac, v147\n"
    "v_rsq_f32_e64 v144, v144\n"
    "v_rsq_f32_e64 v145, v145\n"
    "v_rsq_f32_e64 v146, v146\n"
    "v_rsq_f32_e64 v147, v147\n"
    "v_mul_f32_e64 v224, s50, v144\n"
    "v_mul_f32_e64 v225, s50, v145\n"
    "v_mul_f32_e64 v226, s50, v146\n"
    "v_mul_f32_e64 v227, s50, v147\n"
    "v_mul_f32_e64 v228, v224, -v128\n"
    "v_mul_f32_e64 v229, v225, -v129\n"
    "v_mul_f32_e64 v230, v226, -v130\n"
    "v_mul_f32_e64 v231, v227, -v131\n"
    "ds_write_b128 v207, v[172:175] offset:4096\n"
    "v_pk_fma_f32 v[64:65], v[64:65], v[224:225], v[228:229]\n"
    "v_pk_fma_f32 v[66:67], v[66:67], v[226:227], v[230:231]\n"
    "v_pk_fma_f32 v[68:69], v[68:69], v[224:225], v[228:229]\n"
    "v_pk_fma_f32 v[70:71], v[70:71], v[226:227], v[230:231]\n"
    "v_pk_fma_f32 v[72:73], v[72:73], v[224:225], v[228:229]\n"
    "v_pk_fma_f32 v[74:75], v[74:75], v[226:227], v[230:231]\n"
    "ds_write_b128 v207, v[176:179] offset:8192\n"
    "v_pk_fma_f32 v[76:77], v[76:77], v[224:225], v[228:229]\n"
    "v_pk_fma_f32 v[78:79], v[78:79], v[226:227], v[230:231]\n"
    "v_pk_fma_f32 v[80:81], v[80:81], v[224:225], v[228:229]\n"
    "v_pk_fma_f32 v[82:83], v[82:83], v[226:227], v[230:231]\n"
    "v_pk_fma_f32 v[84:85], v[84:85], v[224:225], v[228:229]\n"
    "v_pk_fma_f32 v[86:87], v[86:87], v[226:227], v[230:231]\n"
    "ds_write_b128 v207, v[188:191] offset:12288\n"
    "v_pk_fma_f32 v[88:89], v[88:89], v[224:225], v[228:229]\n"
    "v_pk_fma_f32 v[90:91], v[90:91], v[226:227], v[230:231]\n"
    "v_pk_fma_f32 v[92:93], v[92:93], v[224:225], v[228:229]\n"
    "v_pk_fma_f32 v[94:95], v[94:95], v[226:227], v[230:231]\n"
    "v_cvt_pk_bf16_f32 v96, v64, v68\n"
    "v_cvt_pk_bf16_f32 v100, v65, v69\n"
    "ds_write_b128 v207, v[192:195] offset:16384\n"
    "v_cvt_pk_bf16_f32 v104, v66, v70\n"
    "v_cvt_pk_bf16_f32 v108, v67, v71\n"
    "v_cvt_pk_bf16_f32 v97, v72, v76\n"
    "v_cvt_pk_bf16_f32 v101, v73, v77\n"
    "v_cvt_pk_bf16_f32 v105, v74, v78\n"
    "v_cvt_pk_bf16_f32 v109, v75, v79\n"
    "ds_write_b128 v207, v[196:199] offset:20480\n"
    "v_cvt_pk_bf16_f32 v98, v80, v84\n"
    "v_cvt_pk_bf16_f32 v102, v81, v85\n"
    "v_cvt_pk_bf16_f32 v106, v82, v86\n"
    "v_cvt_pk_bf16_f32 v110, v83, v87\n"
    "v_cvt_pk_bf16_f32 v99, v88, v92\n"
    "v_cvt_pk_bf16_f32 v103, v89, v93\n"
    "ds_write_b128 v207, v[180:183] offset:32768\n"
    "v_cvt_pk_bf16_f32 v107, v90, v94\n"
    "v_cvt_pk_bf16_f32 v111, v91, v95\n"
    "v_pk_fma_f32 v[0:1], v[0:1], v[224:225], v[228:229]\n"
    "v_pk_fma_f32 v[2:3], v[2:3], v[226:227], v[230:231]\n"
    "v_pk_fma_f32 v[4:5], v[4:5], v[224:225], v[228:229]\n"
    "v_pk_fma_f32 v[6:7], v[6:7], v[226:227], v[230:231]\n"
    "ds_write_b128 v253, v[96:99] offset:2048\n"
    "v_pk_fma_f32 v[8:9], v[8:9], v[224:225], v[228:229]\n"
    "v_pk_fma_f32 v[10:11], v[10:11], v[226:227], v[230:231]\n"
    "v_pk_fma_f32 v[12:13], v[12:13], v[224:225], v[228:229]\n"
    "v_pk_fma_f32 v[14:15], v[14:15], v[226:227], v[230:231]\n"
    "v_pk_fma_f32 v[16:17], v[16:17], v[224:225], v[228:229]\n"
    "v_pk_fma_f32 v[18:19], v[18:19], v[226:227], v[230:231]\n"
    "ds_write_b128 v253, v[100:103] offset:5120\n"
    "v_pk_fma_f32 v[20:21], v[20:21], v[224:225], v[228:229]\n"
    "v_pk_fma_f32 v[22:23], v[22:23], v[226:227], v[230:231]\n"
    "v_pk_fma_f32 v[24:25], v[24:25], v[224:225], v[228:229]\n"
    "v_pk_fma_f32 v[26:27], v[26:27], v[226:227], v[230:231]\n"
    "v_pk_fma_f32 v[28:29], v[28:29], v[224:225], v[228:229]\n"
    "v_pk_fma_f32 v[30:31], v[30:31], v[226:227], v[230:231]\n"
    "v_cvt_pk_bf16_f32 v80, v0, v4\n"
    "v_cvt_pk_bf16_f32 v84, v1, v5\n"
    "v_cvt_pk_bf16_f32 v88, v2, v6\n"
    "v_cvt_pk_bf16_f32 v92, v3, v7\n"
    "v_cvt_pk_bf16_f32 v81, v8, v12\n"
    "v_cvt_pk_bf16_f32 v85, v9, v13\n"
    "v_cvt_pk_bf16_f32 v89, v10, v14\n"
    "v_cvt_pk_bf16_f32 v93, v11, v15\n"
    "v_cvt_pk_bf16_f32 v82, v16, v20\n"
    "v_cvt_pk_bf16_f32 v86, v17, v21\n"
    "v_cvt_pk_bf16_f32 v90, v18, v22\n"
    "v_cvt_pk_bf16_f32 v94, v19, v23\n"
    "v_cvt_pk_bf16_f32 v83, v24, v28\n"
    "ds_write_b128 v253, v[80:83] offset:0\n"
    "v_cvt_pk_bf16_f32 v87, v25, v29\n"
    "v_cvt_pk_bf16_f32 v91, v26, v30\n"
    "v_cvt_pk_bf16_f32 v95, v27, v31\n"
    "v_pk_fma_f32 v[32:33], v[32:33], v[224:225], v[228:229]\n"
    "v_pk_fma_f32 v[34:35], v[34:35], v[226:227], v[230:231]\n"
    "v_pk_fma_f32 v[36:37], v[36:37], v[224:225], v[228:229]\n"
    "ds_write_b128 v253, v[84:87] offset:3072\n"
    "v_pk_fma_f32 v[38:39], v[38:39], v[226:227], v[230:231]\n"
    "v_pk_fma_f32 v[40:41], v[40:41], v[224:225], v[228:229]\n"
    "v_pk_fma_f32 v[42:43], v[42:43], v[226:227], v[230:231]\n"
    "v_pk_fma_f32 v[44:45], v[44:45], v[224:225], v[228:229]\n"
    "v_pk_fma_f32 v[46:47], v[46:47], v[226:227], v[230:231]\n"
    "v_pk_fma_f32 v[48:49], v[48:49], v[224:225], v[228:229]\n"
    "v_pk_fma_f32 v[50:51], v[50:51], v[226:227], v[230:231]\n"
    "v_pk_fma_f32 v[52:53], v[52:53], v[224:225], v[228:229]\n"
    "v_pk_fma_f32 v[54:55], v[54:55], v[226:227], v[230:231]\n"
    "v_pk_fma_f32 v[56:57], v[56:57], v[224:225], v[228:229]\n"
    "v_pk_fma_f32 v[58:59], v[58:59], v[226:227], v[230:231]\n"
    "v_pk_fma_f32 v[60:61], v[60:61], v[224:225], v[228:229]\n"
    "v_pk_fma_f32 v[62:63], v[62:63], v[226:227], v[230:231]\n"
    "v_cvt_pk_bf16_f32 v64, v32, v36\n"
    "v_cvt_pk_bf16_f32 v68, v33, v37\n"
    "v_cvt_pk_bf16_f32 v72, v34, v38\n"
    "v_cvt_pk_bf16_f32 v76, v35, v39\n"
    "v_cvt_pk_bf16_f32 v65, v40, v44\n"
    "v_cvt_pk_bf16_f32 v69, v41, v45\n"
    "v_cvt_pk_bf16_f32 v73, v42, v46\n"
    "v_cvt_pk_bf16_f32 v77, v43, v47\n"
    "v_cvt_pk_bf16_f32 v66, v48, v52\n"
    "v_cvt_pk_bf16_f32 v70, v49, v53\n"
    "v_cvt_pk_bf16_f32 v74, v50, v54\n"
    "v_cvt_pk_bf16_f32 v78, v51, v55\n"
    "v_cvt_pk_bf16_f32 v67, v56, v60\n"
    "ds_write_b128 v253, v[64:67] offset:1024\n"
    "v_cvt_pk_bf16_f32 v71, v57, v61\n"
    "v_cvt_pk_bf16_f32 v75, v58, v62\n"
    "v_cvt_pk_bf16_f32 v79, v59, v63\n"
    "v_mov_b32_e64 v64, v104\n"
    "v_mov_b32_e64 v65, v105\n"
    "v_mov_b32_e64 v66, v106\n"
    "ds_write_b128 v253, v[68:71] offset:4096\n"
    "v_mov_b32_e64 v67, v107\n"
    "v_mov_b32_e64 v68, v108\n"
    "v_mov_b32_e64 v69, v109\n"
    "v_mov_b32_e64 v70, v110\n"
    "v_mov_b32_e64 v71, v111\n"
    "s_waitcnt lgkmcnt(0)\n"
    "s_barrier\n"
    "ds_read_b128 v[144:147], v242 offset:0\n"
    "ds_read_b128 v[148:151], v242 offset:1024\n"
    "ds_read_b128 v[152:155], v242 offset:2048\n"
    "ds_read_b128 v[156:159], v242 offset:3072\n"
    "ds_read_b128 v[160:163], v242 offset:4096\n"
    "ds_read_b128 v[164:167], v242 offset:5120\n"
    "ds_read_b128 a[240:243], v244 offset:32768\n"
    "s_waitcnt lgkmcnt(6)\n"
    "v_mfma_f32_16x16x32_bf16 v[168:171], a[80:83], v[144:147], 0\n"
    "v_mfma_f32_16x16x32_bf16 v[172:175], a[84:87], v[144:147], 0\n"
    "s_waitcnt lgkmcnt(5)\n"
    "v_mfma_f32_16x16x32_bf16 v[168:171], a[128:131], v[148:151], v[168:171]\n"
    "s_waitcnt lgkmcnt(4)\n"
    "v_mfma_f32_16x16x32_bf16 v[168:171], a[48:51], v[152:155], v[168:171]\n"
    "s_waitcnt lgkmcnt(3)\n"
    "v_mfma_f32_16x16x32_bf16 v[172:175], a[132:135], v[148:151], v[172:175]\n"
    "v_mfma_f32_16x16x32_bf16 v[176:179], a[80:83], v[156:159], 0\n"
    "s_waitcnt lgkmcnt(1)\n"
    "v_mfma_f32_16x16x32_bf16 v[176:179], a[128:131], v[160:163], v[176:179]\n"
    "s_waitcnt lgkmcnt(0)\n"
    "v_mfma_f32_16x16x32_bf16 v[172:175], a[240:243], v[152:155], v[172:175]\n"
    "v_mfma_f32_16x16x32_bf16 v[176:179], a[48:51], v[164:167], v[176:179]\n"
    "v_mfma_f32_16x16x32_bf16 v[180:183], a[84:87], v[156:159], 0\n"
    "v_mfma_f32_16x16x32_bf16 v[180:183], a[132:135], v[160:163], v[180:183]\n"
    "s_nop 0\n"
    "v_mfma_f32_16x16x32_bf16 v[180:183], a[240:243], v[164:167], v[180:183]\n"
    "s_sub_u32 s80, s76, .Lreplk_last-.Lreplk_loop-8192\n"
    "s_subb_u32 s81, s77, 0\n"
    "s_add_u32 s82, s36, .Lreplk_last-.Lreplk_loop-8192\n"
    "s_and_b32 s84, s37, 0x3fff\n"
    "v_readfirstlane_b32 s66, v232\n"
    "s_nop 1\n"
    "s_cmp_lg_u32 s66, 2\n"
    "s_cbranch_scc1 98f\n"
    "v_lshlrev_b32_sdwa v184, 1, v168 dst_sel:DWORD dst_unused:UNUSED_PAD src0_sel:DWORD src1_sel:WORD_1\n"
    "v_lshlrev_b32_sdwa v200, 1, v169 dst_sel:DWORD dst_unused:UNUSED_PAD src0_sel:DWORD src1_sel:WORD_1\n"
    "v_lshlrev_b32_sdwa v185, 1, v170 dst_sel:DWORD dst_unused:UNUSED_PAD src0_sel:DWORD src1_sel:WORD_1\n"
    "v_lshlrev_b32_sdwa v201, 1, v171 dst_sel:DWORD dst_unused:UNUSED_PAD src0_sel:DWORD src1_sel:WORD_1\n"
    "ds_read_b128 a[240:243], v244 offset:36864\n"
    "ds_read_u16 v184, v184\n"
    "v_mfma_f32_16x16x32_bf16 v[168:171], a[88:91], v[144:147], 0\n"
    "ds_read_u16_d16_hi v200, v200\n"
    "ds_read_u16 v185, v185\n"
    "v_mfma_f32_16x16x32_bf16 v[168:171], a[136:139], v[148:151], v[168:171]\n"
    "buffer_load_dwordx4 v[0:3], v246, s[40:43], s60 offen offset:0\n"
    "buffer_load_dwordx4 v[32:35], v246, s[40:43], s60 offen offset:64\n"
    "ds_read_u16_d16_hi v201, v201\n"
    "v_mfma_f32_16x16x32_bf16 v[168:171], a[52:55], v[152:155], v[168:171]\n"
    "v_lshlrev_b32_sdwa v186, 1, v172 dst_sel:DWORD dst_unused:UNUSED_PAD src0_sel:DWORD src1_sel:WORD_1\n"
    "v_lshlrev_b32_sdwa v202, 1, v173 dst_sel:DWORD dst_unused:UNUSED_PAD src0_sel:DWORD src1_sel:WORD_1\n"
    "ds_read_u16 v186, v186\n"
    "v_lshlrev_b32_sdwa v187, 1, v174 dst_sel:DWORD dst_unused:UNUSED_PAD src0_sel:DWORD src1_sel:WORD_1\n"
    "v_lshlrev_b32_sdwa v203, 1, v175 dst_sel:DWORD dst_unused:UNUSED_PAD src0_sel:DWORD src1_sel:WORD_1\n"
    "v_lshlrev_b32_sdwa v188, 1, v176 dst_sel:DWORD dst_unused:UNUSED_PAD src0_sel:DWORD src1_sel:WORD_1\n"
    "v_lshlrev_b32_sdwa v204, 1, v177 dst_sel:DWORD dst_unused:UNUSED_PAD src0_sel:DWORD src1_sel:WORD_1\n"
    "v_mfma_f32_16x16x32_bf16 v[172:175], a[92:95], v[144:147], 0\n"
    "v_lshlrev_b32_sdwa v189, 1, v178 dst_sel:DWORD dst_unused:UNUSED_PAD src0_sel:DWORD src1_sel:WORD_1\n"
    "v_mfma_f32_16x16x32_bf16 v[172:175], a[140:143], v[148:151], v[172:175]\n"
    "ds_read_u16_d16_hi v202, v202\n"
    "v_lshlrev_b32_sdwa v205, 1, v179 dst_sel:DWORD dst_unused:UNUSED_PAD src0_sel:DWORD src1_sel:WORD_1\n"
    "v_mfma_f32_16x16x32_bf16 v[176:179], a[88:91], v[156:159], 0\n"
    "v_lshlrev_b32_sdwa v190, 1, v180 dst_sel:DWORD dst_unused:UNUSED_PAD src0_sel:DWORD src1_sel:WORD_1\n"
    "v_lshlrev_b32_sdwa v206, 1, v181 dst_sel:DWORD dst_unused:UNUSED_PAD src0_sel:DWORD src1_sel:WORD_1\n"
    "s_waitcnt lgkmcnt(6)\n"
    "v_mfma_f32_16x16x32_bf16 v[172:175], a[240:243], v[152:155], v[172:175]\n"
    "s_nop 0\n"
    "ds_read_u16 v187, v187\n"
    "v_lshlrev_b32_sdwa v191, 1, v182 dst_sel:DWORD dst_unused:UNUSED_PAD src0_sel:DWORD src1_sel:WORD_1\n"
    "v_mfma_f32_16x16x32_bf16 v[176:179], a[136:139], v[160:163], v[176:179]\n"
    "v_lshlrev_b32_sdwa v207, 1, v183 dst_sel:DWORD dst_unused:UNUSED_PAD src0_sel:DWORD src1_sel:WORD_1\n"
    "v_mfma_f32_16x16x32_bf16 v[176:179], a[52:55], v[164:167], v[176:179]\n"
    "ds_read_u16_d16_hi v203, v203\n"
    "v_mfma_f32_16x16x32_bf16 v[180:183], a[92:95], v[156:159], 0\n"
    "v_mfma_f32_16x16x32_bf16 v[180:183], a[140:143], v[160:163], v[180:183]\n"
    "ds_read_u16 v188, v188\n"
    "v_mfma_f32_16x16x32_bf16 v[180:183], a[240:243], v[164:167], v[180:183]\n"
    "ds_read_u16_d16_hi v204, v204\n"
    "ds_read_u16 v189, v189\n"
    "s_waitcnt lgkmcnt(9)\n"
    "v_or_b32_e32 v184, v184, v200\n"
    "s_waitcnt lgkmcnt(7)\n"
    "v_or_b32_e32 v185, v185, v201\n"
    "v_lshlrev_b32_sdwa v201, 1, v171 dst_sel:DWORD dst_unused:UNUSED_PAD src0_sel:DWORD src1_sel:WORD_1\n"
    "ds_read_u16_d16_hi v205, v205\n"
    "buffer_load_dwordx4 v[20:23], v247, s[40:43], s60 offen offset:0\n"
    "buffer_load_dwordx4 v[52:55], v247, s[40:43], s60 offen offset:64\n"
    "ds_read_u16 v190, v190\n"
    "s_waitcnt lgkmcnt(7)\n"
    "v_or_b32_e32 v186, v186, v202\n"
    "v_lshlrev_b32_sdwa v202, 1, v173 dst_sel:DWORD dst_unused:UNUSED_PAD src0_sel:DWORD src1_sel:WORD_1\n"
    "ds_read_u16_d16_hi v206, v206\n"
    "v_cvt_pk_f16_f32 v192, v168, v169\n"
    "v_lshlrev_b32_sdwa v193, 1, v170 dst_sel:DWORD dst_unused:UNUSED_PAD src0_sel:DWORD src1_sel:WORD_1\n"
    "v_lshlrev_b32_sdwa v194, 1, v172 dst_sel:DWORD dst_unused:UNUSED_PAD src0_sel:DWORD src1_sel:WORD_1\n"
    "v_lshlrev_b32_sdwa v195, 1, v174 dst_sel:DWORD dst_unused:UNUSED_PAD src0_sel:DWORD src1_sel:WORD_1\n"
    "v_cvt_pk_f16_f32 v196, v176, v177\n"
    "v_lshlrev_b32_sdwa v197, 1, v178 dst_sel:DWORD dst_unused:UNUSED_PAD src0_sel:DWORD src1_sel:WORD_1\n"
    "v_mfma_f32_16x16x32_bf16 v[168:171], a[96:99], v[144:147], 0\n"
    "v_lshlrev_b32_sdwa v198, 1, v180 dst_sel:DWORD dst_unused:UNUSED_PAD src0_sel:DWORD src1_sel:WORD_1\n"
    "v_lshlrev_b32_sdwa v199, 1, v182 dst_sel:DWORD dst_unused:UNUSED_PAD src0_sel:DWORD src1_sel:WORD_1\n"
    "v_mfma_f32_16x16x32_bf16 v[168:171], a[144:147], v[148:151], v[168:171]\n"
    "ds_read_u16 v191, v191\n"
    "ds_read_u16_d16_hi v207, v207\n"
    "v_mfma_f32_16x16x32_bf16 v[168:171], a[56:59], v[152:155], v[168:171]\n"
    "ds_read_u16 v193, v193\n"
    "ds_read_u16_d16_hi v201, v201\n"
    "s_waitcnt lgkmcnt(10)\n"
    "v_or_b32_e32 v187, v187, v203\n"
    "v_lshlrev_b32_sdwa v203, 1, v175 dst_sel:DWORD dst_unused:UNUSED_PAD src0_sel:DWORD src1_sel:WORD_1\n"
    "v_mfma_f32_16x16x32_bf16 v[172:175], a[100:103], v[144:147], 0\n"
    "s_waitcnt lgkmcnt(8)\n"
    "v_or_b32_e32 v188, v188, v204\n"
    "v_mfma_f32_16x16x32_bf16 v[172:175], a[148:151], v[148:151], v[172:175]\n"
    "ds_read_u16 v194, v194\n"
    "s_waitcnt lgkmcnt(7)\n"
    "v_or_b32_e32 v189, v189, v205\n"
    "v_mfma_f32_16x16x32_bf16 v[172:175], a[60:63], v[152:155], v[172:175]\n"
    "v_lshlrev_b32_sdwa v205, 1, v179 dst_sel:DWORD dst_unused:UNUSED_PAD src0_sel:DWORD src1_sel:WORD_1\n"
    "v_mfma_f32_16x16x32_bf16 v[176:179], a[96:99], v[156:159], 0\n"
    "ds_read_u16_d16_hi v202, v202\n"
    "v_mfma_f32_16x16x32_bf16 v[176:179], a[144:147], v[160:163], v[176:179]\n"
    "ds_read_b128 v[236:239], v244 offset:0\n"
    "s_waitcnt lgkmcnt(7)\n"
    "v_or_b32_e32 v190, v190, v206\n"
    "v_mfma_f32_16x16x32_bf16 v[176:179], a[56:59], v[164:167], v[176:179]\n"
    "v_lshlrev_b32_sdwa v206, 1, v181 dst_sel:DWORD dst_unused:UNUSED_PAD src0_sel:DWORD src1_sel:WORD_1\n"
    "v_mfma_f32_16x16x32_bf16 v[96:99], a[0:3], v[184:187], 0\n"
    "ds_read_u16 v195, v195\n"
    "v_mfma_f32_16x16x32_bf16 v[100:103], a[4:7], v[184:187], 0\n"
    "buffer_load_dwordx4 v[4:7], v246, s[40:43], s61 offen offset:0\n"
    "buffer_load_dwordx4 v[36:39], v246, s[40:43], s61 offen offset:64\n"
    "v_mfma_f32_16x16x32_bf16 v[112:115], a[8:11], v[184:187], 0\n"
    "s_waitcnt lgkmcnt(6)\n"
    "v_or_b32_e32 v191, v191, v207\n"
    "v_lshlrev_b32_sdwa v207, 1, v183 dst_sel:DWORD dst_unused:UNUSED_PAD src0_sel:DWORD src1_sel:WORD_1\n"
    "v_mfma_f32_16x16x32_bf16 v[180:183], a[100:103], v[156:159], 0\n"
    "ds_read_u16_d16_hi v203, v203\n"
    "v_mfma_f32_16x16x32_bf16 v[180:183], a[148:151], v[160:163], v[180:183]\n"
    "s_waitcnt lgkmcnt(5)\n"
    "v_or_b32_e32 v193, v193, v201\n"
    "v_mfma_f32_16x16x32_bf16 v[180:183], a[60:63], v[164:167], v[180:183]\n"
    "s_waitcnt lgkmcnt(3)\n"
    "v_or_b32_e32 v194, v194, v202\n"
    "v_mfma_f32_16x16x32_bf16 v[116:119], a[12:15], v[184:187], 0\n"
    "ds_read_b128 a[244:247], v244 offset:2048\n"
    "s_waitcnt lgkmcnt(3)\n"
    "v_mfma_f32_16x16x32_bf16 v[128:131], v[236:239], v[184:187], 0\n"
    "s_nop 0\n"
    "v_pk_fma_f16 v232, v192, v192, s48\n"
    "v_mfma_f32_16x16x32_bf16 v[104:107], a[0:3], v[188:191], 0\n"
    "v_mfma_f32_16x16x32_bf16 v[108:111], a[4:7], v[188:191], 0\n"
    "ds_read_u16 v197, v197\n"
    "v_mfma_f32_16x16x32_bf16 v[120:123], a[8:11], v[188:191], 0\n"
    "v_mfma_f32_16x16x32_bf16 v[124:127], a[12:15], v[188:191], 0\n"
    "ds_read_u16_d16_hi v205, v205\n"
    "ds_read_u16 v198, v198\n"
    "s_waitcnt lgkmcnt(3)\n"
    "v_mfma_f32_16x16x32_bf16 v[132:135], a[244:247], v[184:187], 0\n"
    "s_nop 0\n"
    "ds_read_u16_d16_hi v206, v206\n"
    "ds_read_u16 v199, v199\n"
    "v_or_b32_e64 v195, v195, v203\n"
    "v_pk_mul_f16 v232, v192, v232\n"
    "v_cvt_pk_f16_f32 v184, v168, v169\n"
    "v_exp_f16_e64 v234, v232\n"
    "v_mfma_f32_16x16x32_bf16 v[140:143], a[244:247], v[188:191], 0\n"
    "v_exp_f16_sdwa v234, v232 dst_sel:WORD_1 dst_unused:UNUSED_PRESERVE src0_sel:WORD_1\n"
    "s_branch 2f\n"
    ".p2align 6\n"
    "1:\n"
    ".Lreplk_loop:\n"
    "v_lshlrev_b32_sdwa v184, 1, v168 dst_sel:DWORD dst_unused:UNUSED_PAD src0_sel:DWORD src1_sel:WORD_1\n"
    "v_lshlrev_b32_sdwa v200, 1, v169 dst_sel:DWORD dst_unused:UNUSED_PAD src0_sel:DWORD src1_sel:WORD_1\n"
    "v_lshlrev_b32_sdwa v185, 1, v170 dst_sel:DWORD dst_unused:UNUSED_PAD src0_sel:DWORD src1_sel:WORD_1\n"
    "v_lshlrev_b32_sdwa v201, 1, v171 dst_sel:DWORD dst_unused:UNUSED_PAD src0_sel:DWORD src1_sel:WORD_1\n"
    "v_mfma_f32_16x16x32_bf16 v[140:143], a[244:247], v[188:191], v[140:143]\n"
    "s_waitcnt vmcnt(1)\n"
    "ds_read_b128 a[240:243], v244 offset:36864\n"
    "ds_read_u16 v184, v184\n"
    "v_mfma_f32_16x16x32_bf16 v[168:171], a[88:91], v[144:147], 0\n"
    "s_nop 0\n"
    "ds_read_u16_d16_hi v200, v200\n"
    "ds_read_u16 v185, v185\n"
    "v_mfma_f32_16x16x32_bf16 v[168:171], a[136:139], v[148:151], v[168:171]\n"
    "buffer_load_dwordx4 v[0:3], v246, s[40:43], s60 offen offset:0\n"
    "buffer_load_dwordx4 v[32:35], v246, s[40:43], s60 offen offset:64\n"
    "ds_read_u16_d16_hi v201, v201\n"
    "v_mfma_f32_16x16x32_bf16 v[168:171], a[52:55], v[152:155], v[168:171]\n"
    "v_lshlrev_b32_sdwa v186, 1, v172 dst_sel:DWORD dst_unused:UNUSED_PAD src0_sel:DWORD src1_sel:WORD_1\n"
    "v_lshlrev_b32_sdwa v202, 1, v173 dst_sel:DWORD dst_unused:UNUSED_PAD src0_sel:DWORD src1_sel:WORD_1\n"
    "v_smfmac_f32_32x32x32_bf16 v[128:143], a[248:251], a[224:231], v241\n"
    "ds_read_u16 v186, v186\n"
    "v_lshlrev_b32_sdwa v187, 1, v174 dst_sel:DWORD dst_unused:UNUSED_PAD src0_sel:DWORD src1_sel:WORD_1\n"
    "v_lshlrev_b32_sdwa v203, 1, v175 dst_sel:DWORD dst_unused:UNUSED_PAD src0_sel:DWORD src1_sel:WORD_1\n"
    "v_lshlrev_b32_sdwa v188, 1, v176 dst_sel:DWORD dst_unused:UNUSED_PAD src0_sel:DWORD src1_sel:WORD_1\n"
    "v_lshlrev_b32_sdwa v204, 1, v177 dst_sel:DWORD dst_unused:UNUSED_PAD src0_sel:DWORD src1_sel:WORD_1\n"
    "v_mfma_f32_16x16x32_bf16 v[172:175], a[92:95], v[144:147], 0\n"
    "ds_read_b128 v[236:239], v244 offset:20480\n"
    "v_lshlrev_b32_sdwa v189, 1, v178 dst_sel:DWORD dst_unused:UNUSED_PAD src0_sel:DWORD src1_sel:WORD_1\n"
    "v_mfma_f32_16x16x32_bf16 v[172:175], a[140:143], v[148:151], v[172:175]\n"
    "s_nop 0\n"
    "ds_read_u16_d16_hi v202, v202\n"
    "v_lshlrev_b32_sdwa v205, 1, v179 dst_sel:DWORD dst_unused:UNUSED_PAD src0_sel:DWORD src1_sel:WORD_1\n"
    "v_mfma_f32_16x16x32_bf16 v[176:179], a[88:91], v[156:159], 0\n"
    "s_waitcnt lgkmcnt(7)\n"
    "v_lshlrev_b32_sdwa v190, 1, v180 dst_sel:DWORD dst_unused:UNUSED_PAD src0_sel:DWORD src1_sel:WORD_1\n"
    "v_lshlrev_b32_sdwa v206, 1, v181 dst_sel:DWORD dst_unused:UNUSED_PAD src0_sel:DWORD src1_sel:WORD_1\n"
    "v_mfma_f32_16x16x32_bf16 v[172:175], a[240:243], v[152:155], v[172:175]\n"
    "ds_read_u16 v187, v187\n"
    "v_lshlrev_b32_sdwa v191, 1, v182 dst_sel:DWORD dst_unused:UNUSED_PAD src0_sel:DWORD src1_sel:WORD_1\n"
    "v_mfma_f32_16x16x32_bf16 v[176:179], a[136:139], v[160:163], v[176:179]\n"
    "ds_read_b128 a[244:247], v244 offset:22528\n"
    "v_lshlrev_b32_sdwa v207, 1, v183 dst_sel:DWORD dst_unused:UNUSED_PAD src0_sel:DWORD src1_sel:WORD_1\n"
    "v_mfma_f32_16x16x32_bf16 v[176:179], a[52:55], v[164:167], v[176:179]\n"
    "ds_read_u16_d16_hi v203, v203\n"
    "v_pk_fma_f16 v234, v234, s49, s49\n"
    "v_mfma_f32_16x16x32_bf16 v[180:183], a[92:95], v[156:159], 0\n"
    "v_rcp_f16_e32 v232, v234\n"
    "v_mfma_f32_16x16x32_bf16 v[180:183], a[140:143], v[160:163], v[180:183]\n"
    "s_waitcnt lgkmcnt(8)\n"
    "ds_read_u16 v188, v188\n"
    "v_rcp_f16_sdwa v232, v234 dst_sel:WORD_1 dst_unused:UNUSED_PRESERVE src0_sel:WORD_1\n"
    "v_mfma_f32_16x16x32_bf16 v[180:183], a[240:243], v[164:167], v[180:183]\n"
    "v_fma_mix_f32 v234, v196, v232, 0 op_sel_hi:[1,1,0]\n"
    "v_fma_mix_f32 v232, v196, v232, 0 op_sel:[1,1,0] op_sel_hi:[1,1,0]\n"
    "v_mfma_f32_16x16x32_bf16 v[96:99], a[208:211], v[192:195], v[96:99]\n"
    "ds_read_u16_d16_hi v204, v204\n"
    "v_cvt_pk_bf16_f32 v196, v234, v232\n"
    "v_mfma_f32_16x16x32_bf16 v[100:103], a[212:215], v[192:195], v[100:103]\n"
    "s_waitcnt lgkmcnt(8)\n"
    "ds_read_u16 v189, v189\n"
    "v_or_b32_e32 v184, v184, v200\n"
    "v_mfma_f32_16x16x32_bf16 v[112:115], a[216:219], v[192:195], v[112:115]\n"
    "v_or_b32_e32 v185, v185, v201\n"
    "v_lshlrev_b32_sdwa v201, 1, v171 dst_sel:DWORD dst_unused:UNUSED_PAD src0_sel:DWORD src1_sel:WORD_1\n"
    "v_mfma_f32_16x16x32_bf16 v[116:119], a[220:223], v[192:195], v[116:119]\n"
    "s_waitcnt lgkmcnt(7)\n"
    "ds_read_u16_d16_hi v205, v205\n"
    "v_cvt_pk_bf16_f32 v96, v96, v97\n"
    "v_mfma_f32_16x16x32_bf16 v[128:131], v[236:239], v[192:195], v[128:131]\n"
    "buffer_load_dwordx4 v[20:23], v247, s[40:43], s60 offen offset:0\n"
    "buffer_load_dwordx4 v[52:55], v247, s[40:43], s60 offen offset:64\n"
    "ds_read_u16 v190, v190\n"
    "v_mfma_f32_16x16x32_bf16 v[104:107], a[208:211], v[196:199], v[104:107]\n"
    "s_waitcnt lgkmcnt(8)\n"
    "v_or_b32_e32 v186, v186, v202\n"
    "v_lshlrev_b32_sdwa v202, 1, v173 dst_sel:DWORD dst_unused:UNUSED_PAD src0_sel:DWORD src1_sel:WORD_1\n"
    "v_mfma_f32_16x16x32_bf16 v[108:111], a[212:215], v[196:199], v[108:111]\n"
    "s_nop 0\n"
    "ds_read_u16_d16_hi v206, v206\n"
    "v_cvt_pk_bf16_f32 v97, v98, v99\n"
    "v_mfma_f32_16x16x32_bf16 v[120:123], a[216:219], v[196:199], v[120:123]\n"
    "s_waitcnt lgkmcnt(7)\n"
    "v_cvt_pk_bf16_f32 v98, v100, v101\n"
    "v_cvt_pk_bf16_f32 v99, v102, v103\n"
    "v_mfma_f32_16x16x32_bf16 v[132:135], a[244:247], v[192:195], v[132:135]\n"
    "v_cvt_pk_f16_f32 v192, v168, v169\n"
    "v_lshlrev_b32_sdwa v193, 1, v170 dst_sel:DWORD dst_unused:UNUSED_PAD src0_sel:DWORD src1_sel:WORD_1\n"
    "v_mfma_f32_16x16x32_bf16 v[124:127], a[220:223], v[196:199], v[124:127]\n"
    "v_lshlrev_b32_sdwa v194, 1, v172 dst_sel:DWORD dst_unused:UNUSED_PAD src0_sel:DWORD src1_sel:WORD_1\n"
    "v_lshlrev_b32_sdwa v195, 1, v174 dst_sel:DWORD dst_unused:UNUSED_PAD src0_sel:DWORD src1_sel:WORD_1\n"
    "v_mfma_f32_16x16x32_bf16 v[136:139], v[236:239], v[196:199], v[136:139]\n"
    "s_waitcnt lgkmcnt(6)\n"
    "ds_write_b128 v243, v[96:99] offset:24640\n"
    "v_mfma_f32_16x16x32_bf16 v[140:143], a[244:247], v[196:199], v[140:143]\n"
    "s_nop 0\n"
    "v_cvt_pk_f16_f32 v196, v176, v177\n"
    "v_lshlrev_b32_sdwa v197, 1, v178 dst_sel:DWORD dst_unused:UNUSED_PAD src0_sel:DWORD src1_sel:WORD_1\n"
    "v_mfma_f32_16x16x32_bf16 v[168:171], a[96:99], v[144:147], 0\n"
    "s_nop 0\n"
    "v_lshlrev_b32_sdwa v198, 1, v180 dst_sel:DWORD dst_unused:UNUSED_PAD src0_sel:DWORD src1_sel:WORD_1\n"
    "v_lshlrev_b32_sdwa v199, 1, v182 dst_sel:DWORD dst_unused:UNUSED_PAD src0_sel:DWORD src1_sel:WORD_1\n"
    "v_mfma_f32_16x16x32_bf16 v[168:171], a[144:147], v[148:151], v[168:171]\n"
    "s_waitcnt lgkmcnt(5)\n"
    "ds_read_u16 v191, v191\n"
    "ds_read_u16_d16_hi v207, v207\n"
    "v_mfma_f32_16x16x32_bf16 v[168:171], a[56:59], v[152:155], v[168:171]\n"
    "ds_read_u16 v193, v193\n"
    "ds_read_u16_d16_hi v201, v201\n"
    "v_or_b32_e64 v187, v187, v203\n"
    "v_lshlrev_b32_sdwa v203, 1, v175 dst_sel:DWORD dst_unused:UNUSED_PAD src0_sel:DWORD src1_sel:WORD_1\n"
    "v_mfma_f32_16x16x32_bf16 v[172:175], a[100:103], v[144:147], 0\n"
    "v_or_b32_e64 v188, v188, v204\n"
    "v_cvt_pk_bf16_f32 v112, v112, v113\n"
    "v_mfma_f32_16x16x32_bf16 v[172:175], a[148:151], v[148:151], v[172:175]\n"
    "s_waitcnt lgkmcnt(7)\n"
    "ds_read_u16 v194, v194\n"
    "v_or_b32_e32 v189, v189, v205\n"
    "v_mfma_f32_16x16x32_bf16 v[172:175], a[60:63], v[152:155], v[172:175]\n"
    "v_lshlrev_b32_sdwa v205, 1, v179 dst_sel:DWORD dst_unused:UNUSED_PAD src0_sel:DWORD src1_sel:WORD_1\n"
    "v_cvt_pk_bf16_f32 v113, v114, v115\n"
    "v_mfma_f32_16x16x32_bf16 v[176:179], a[96:99], v[156:159], 0\n"
    "ds_read_u16_d16_hi v202, v202\n"
    "v_cvt_pk_bf16_f32 v114, v116, v117\n"
    "v_mfma_f32_16x16x32_bf16 v[176:179], a[144:147], v[160:163], v[176:179]\n"
    "s_waitcnt lgkmcnt(6)\n"
    "ds_read_b128 v[236:239], v244 offset:0\n"
    "v_or_b32_e32 v190, v190, v206\n"
    "v_mfma_f32_16x16x32_bf16 v[176:179], a[56:59], v[164:167], v[176:179]\n"
    "ds_read_b128 a[252:255], v249 offset:0\n"
    "v_lshlrev_b32_sdwa v206, 1, v181 dst_sel:DWORD dst_unused:UNUSED_PAD src0_sel:DWORD src1_sel:WORD_1\n"
    "v_mfma_f32_16x16x32_bf16 v[96:99], a[0:3], v[184:187], 0\n"
    "ds_read_u16 v195, v195\n"
    "v_cvt_pk_bf16_f32 v115, v118, v119\n"
    "v_mfma_f32_16x16x32_bf16 v[100:103], a[4:7], v[184:187], 0\n"
    "v_cvt_pk_bf16_f32 v128, v128, v129\n"
    "v_cvt_pk_bf16_f32 v129, v130, v131\n"
    "v_cvt_pk_bf16_f32 v130, v132, v133\n"
    "v_cvt_pk_bf16_f32 v131, v134, v135\n"
    "v_cvt_pk_bf16_f32 v104, v104, v105\n"
    "v_cvt_pk_bf16_f32 v105, v106, v107\n"
    "v_cvt_pk_bf16_f32 v106, v108, v109\n"
    "v_cvt_pk_bf16_f32 v107, v110, v111\n"
    "v_cvt_pk_bf16_f32 v120, v120, v121\n"
    "ds_write_b128 v243, v[112:115] offset:26688\n"
    "buffer_load_dwordx4 v[4:7], v246, s[40:43], s61 offen offset:0\n"
    "buffer_load_dwordx4 v[36:39], v246, s[40:43], s61 offen offset:64\n"
    "v_mfma_f32_16x16x32_bf16 v[112:115], a[8:11], v[184:187], 0\n"
    "s_waitcnt lgkmcnt(7)\n"
    "v_or_b32_e32 v191, v191, v207\n"
    "v_lshlrev_b32_sdwa v207, 1, v183 dst_sel:DWORD dst_unused:UNUSED_PAD src0_sel:DWORD src1_sel:WORD_1\n"
    "v_mfma_f32_16x16x32_bf16 v[180:183], a[100:103], v[156:159], 0\n"
    "s_waitcnt lgkmcnt(6)\n"
    "ds_read_u16_d16_hi v203, v203\n"
    "ds_write_b128 v243, v[128:131] offset:28736\n"
    "v_mfma_f32_16x16x32_bf16 v[180:183], a[148:151], v[160:163], v[180:183]\n"
    "v_or_b32_e32 v193, v193, v201\n"
    "v_cvt_pk_bf16_f32 v121, v122, v123\n"
    "v_mfma_f32_16x16x32_bf16 v[180:183], a[60:63], v[164:167], v[180:183]\n"
    "s_waitcnt lgkmcnt(5)\n"
    "v_or_b32_e32 v194, v194, v202\n"
    "v_cvt_pk_bf16_f32 v122, v124, v125\n"
    "v_mfma_f32_16x16x32_bf16 v[116:119], a[12:15], v[184:187], 0\n"
    "s_waitcnt lgkmcnt(4)\n"
    "ds_read_b128 a[244:247], v244 offset:2048\n"
    "ds_write_b128 v243, v[104:107] offset:25664\n"
    "v_mfma_f32_16x16x32_bf16 v[128:131], v[236:239], v[184:187], 0\n"
    "s_nop 0\n"
    "v_cvt_pk_bf16_f32 v123, v126, v127\n"
    "v_pk_fma_f16 v232, v192, v192, s48\n"
    "v_mfma_f32_16x16x32_bf16 v[104:107], a[0:3], v[188:191], 0\n"
    "v_cvt_pk_bf16_f32 v136, v136, v137\n"
    "v_cvt_pk_bf16_f32 v137, v138, v139\n"
    "v_mfma_f32_16x16x32_bf16 v[108:111], a[4:7], v[188:191], 0\n"
    "ds_read_u16 v197, v197\n"
    "ds_write_b128 v243, v[120:123] offset:27712\n"
    "v_mfma_f32_16x16x32_bf16 v[120:123], a[8:11], v[188:191], 0\n"
    "s_nop 0\n"
    "buffer_store_dwordx4 a[252:255], v255, s[68:71], s59 offen offset:0 sc1\n"
    "v_mfma_f32_16x16x32_bf16 v[124:127], a[12:15], v[188:191], 0\n"
    "s_waitcnt lgkmcnt(4)\n"
    "ds_read_u16_d16_hi v205, v205\n"
    "ds_read_u16 v198, v198\n"
    "v_cvt_pk_bf16_f32 v138, v140, v141\n"
    "s_waitcnt lgkmcnt(5)\n"
    "v_mfma_f32_16x16x32_bf16 v[132:135], a[244:247], v[184:187], 0\n"
    "s_nop 0\n"
    "ds_read_u16_d16_hi v206, v206\n"
    "ds_read_u16 v199, v199\n"
    "v_or_b32_e64 v195, v195, v203\n"
    "v_cvt_pk_bf16_f32 v139, v142, v143\n"
    "v_pk_mul_f16 v232, v192, v232\n"
    "v_cvt_pk_f16_f32 v184, v168, v169\n"
    "v_exp_f16_e64 v234, v232\n"
    "v_mfma_f32_16x16x32_bf16 v[140:143], a[244:247], v[188:191], 0\n"
    "v_exp_f16_sdwa v234, v232 dst_sel:WORD_1 dst_unused:UNUSED_PRESERVE src0_sel:WORD_1\n"
    "ds_write_b128 v243, v[136:139] offset:29760\n"
    "2:\n"
    "v_mfma_f32_16x16x32_bf16 v[136:139], v[236:239], v[188:191], 0\n"
    "v_pk_fma_f16 v234, v234, s49, s49\n"
    "v_lshlrev_b32_sdwa v185, 1, v170 dst_sel:DWORD dst_unused:UNUSED_PAD src0_sel:DWORD src1_sel:WORD_1\n"
    "v_lshlrev_b32_sdwa v201, 1, v171 dst_sel:DWORD dst_unused:UNUSED_PAD src0_sel:DWORD src1_sel:WORD_1\n"
    "v_mfma_f32_16x16x32_bf16 v[168:171], a[104:107], v[144:147], 0\n"
    "ds_read_u16_d16_hi v207, v207\n"
    "ds_read_b128 a[252:255], v249 offset:2048\n"
    "v_mfma_f32_16x16x32_bf16 v[168:171], a[152:155], v[148:151], v[168:171]\n"
    "buffer_load_dwordx4 v[24:27], v247, s[40:43], s61 offen offset:0\n"
    "buffer_load_dwordx4 v[56:59], v247, s[40:43], s61 offen offset:64\n"
    "ds_read_u16 v185, v185\n"
    "v_lshlrev_b32_sdwa v186, 1, v172 dst_sel:DWORD dst_unused:UNUSED_PAD src0_sel:DWORD src1_sel:WORD_1\n"
    "s_waitcnt lgkmcnt(7)\n"
    "s_waitcnt lgkmcnt(6)\n"
    "v_or_b32_e64 v197, v197, v205\n"
    "v_lshlrev_b32_sdwa v202, 1, v173 dst_sel:DWORD dst_unused:UNUSED_PAD src0_sel:DWORD src1_sel:WORD_1\n"
    "v_lshlrev_b32_sdwa v187, 1, v174 dst_sel:DWORD dst_unused:UNUSED_PAD src0_sel:DWORD src1_sel:WORD_1\n"
    "v_lshlrev_b32_sdwa v203, 1, v175 dst_sel:DWORD dst_unused:UNUSED_PAD src0_sel:DWORD src1_sel:WORD_1\n"
    "v_rcp_f16_e64 v232, v234\n"
    "v_mfma_f32_16x16x32_bf16 v[168:171], a[64:67], v[152:155], v[168:171]\n"
    "ds_read_u16_d16_hi v201, v201\n"
    "ds_read_u16 v186, v186\n"
    "v_rcp_f16_sdwa v232, v234 dst_sel:WORD_1 dst_unused:UNUSED_PRESERVE src0_sel:WORD_1\n"
    "v_mfma_f32_16x16x32_bf16 v[172:175], a[108:111], v[144:147], 0\n"
    "s_waitcnt lgkmcnt(7)\n"
    "ds_read_u16_d16_hi v202, v202\n"
    "s_waitcnt lgkmcnt(7)\n"
    "v_or_b32_e64 v198, v198, v206\n"
    "v_mfma_f32_16x16x32_bf16 v[172:175], a[156:159], v[148:151], v[172:175]\n"
    "ds_read_b128 v[236:239], v244 offset:4096\n"
    "v_cvt_pk_f16_f32 v188, v176, v177\n"
    "v_lshlrev_b32_sdwa v189, 1, v178 dst_sel:DWORD dst_unused:UNUSED_PAD src0_sel:DWORD src1_sel:WORD_1\n"
    "v_lshlrev_b32_sdwa v205, 1, v179 dst_sel:DWORD dst_unused:UNUSED_PAD src0_sel:DWORD src1_sel:WORD_1\n"
    "v_lshlrev_b32_sdwa v190, 1, v180 dst_sel:DWORD dst_unused:UNUSED_PAD src0_sel:DWORD src1_sel:WORD_1\n"
    "v_lshlrev_b32_sdwa v206, 1, v181 dst_sel:DWORD dst_unused:UNUSED_PAD src0_sel:DWORD src1_sel:WORD_1\n"
    "v_lshlrev_b32_sdwa v191, 1, v182 dst_sel:DWORD dst_unused:UNUSED_PAD src0_sel:DWORD src1_sel:WORD_1\n"
    "v_fma_mix_f32 v234, v192, v232, 0 op_sel_hi:[1,1,0]\n"
    "v_mfma_f32_16x16x32_bf16 v[172:175], a[68:71], v[152:155], v[172:175]\n"
    "ds_read_u16 v187, v187\n"
    "ds_read_u16_d16_hi v203, v203\n"
    "v_fma_mix_f32 v232, v192, v232, 0 op_sel:[1,1,0] op_sel_hi:[1,1,0]\n"
    "v_mfma_f32_16x16x32_bf16 v[176:179], a[104:107], v[156:159], 0\n"
    "v_cvt_pk_bf16_f32 v192, v234, v232\n"
    "v_pk_fma_f16 v232, v196, v196, s48\n"
    "v_mfma_f32_16x16x32_bf16 v[176:179], a[152:155], v[160:163], v[176:179]\n"
    "s_waitcnt lgkmcnt(7)\n"
    "ds_read_b128 a[244:247], v244 offset:6144\n"
    "buffer_store_dwordx4 a[252:255], v255, s[68:71], s59 offen offset:64 sc1\n"
    "v_mfma_f32_16x16x32_bf16 v[176:179], a[64:67], v[164:167], v[176:179]\n"
    "s_nop 0\n"
    "ds_read_u16 v189, v189\n"
    "v_or_b32_e64 v199, v199, v207\n"
    "v_lshlrev_b32_sdwa v207, 1, v183 dst_sel:DWORD dst_unused:UNUSED_PAD src0_sel:DWORD src1_sel:WORD_1\n"
    "v_mfma_f32_16x16x32_bf16 v[180:183], a[108:111], v[156:159], 0\n"
    "ds_read_b128 a[252:255], v249 offset:4096\n"
    "ds_read_u16_d16_hi v205, v205\n"
    "v_pk_mul_f16 v232, v196, v232\n"
    "v_mfma_f32_16x16x32_bf16 v[180:183], a[156:159], v[160:163], v[180:183]\n"
    "s_waitcnt lgkmcnt(9)\n"
    "v_exp_f16_e32 v234, v232\n"
    "v_mfma_f32_16x16x32_bf16 v[180:183], a[68:71], v[164:167], v[180:183]\n"
    "ds_read_u16 v190, v190\n"
    "v_exp_f16_sdwa v234, v232 dst_sel:WORD_1 dst_unused:UNUSED_PRESERVE src0_sel:WORD_1\n"
    "v_mfma_f32_16x16x32_bf16 v[96:99], a[16:19], v[192:195], v[96:99]\n"
    "v_pk_fma_f16 v234, v234, s49, s49\n"
    "v_or_b32_e32 v185, v185, v201\n"
    "v_mfma_f32_16x16x32_bf16 v[100:103], a[20:23], v[192:195], v[100:103]\n"
    "s_waitcnt lgkmcnt(7)\n"
    "ds_read_u16_d16_hi v206, v206\n"
    "v_rcp_f16_e64 v232, v234\n"
    "v_mfma_f32_16x16x32_bf16 v[112:115], a[24:27], v[192:195], v[112:115]\n"
    "ds_read_u16 v191, v191\n"
    "v_rcp_f16_sdwa v232, v234 dst_sel:WORD_1 dst_unused:UNUSED_PRESERVE src0_sel:WORD_1\n"
    "v_mfma_f32_16x16x32_bf16 v[116:119], a[28:31], v[192:195], v[116:119]\n"
    "buffer_load_dwordx4 v[8:11], v246, s[40:43], s62 offen offset:0\n"
    "buffer_load_dwordx4 v[40:43], v246, s[40:43], s62 offen offset:64\n"
    "ds_read_u16_d16_hi v207, v207\n"
    "v_mfma_f32_16x16x32_bf16 v[128:131], v[236:239], v[192:195], v[128:131]\n"
    "v_fma_mix_f32 v234, v196, v232, 0 op_sel_hi:[1,1,0]\n"
    "v_fma_mix_f32 v232, v196, v232, 0 op_sel:[1,1,0] op_sel_hi:[1,1,0]\n"
    "s_waitcnt lgkmcnt(7)\n"
    "v_mfma_f32_16x16x32_bf16 v[132:135], a[244:247], v[192:195], v[132:135]\n"
    "s_nop 0\n"
    "ds_read_b128 a[240:243], v244 offset:34816\n"
    "v_or_b32_e64 v186, v186, v202\n"
    "v_cvt_pk_bf16_f32 v196, v234, v232\n"
    "v_pk_fma_f16 v232, v184, v184, s48\n"
    "v_or_b32_e32 v187, v187, v203\n"
    "v_mfma_f32_16x16x32_bf16 v[104:107], a[16:19], v[196:199], v[104:107]\n"
    "s_waitcnt lgkmcnt(6)\n"
    "v_pk_mul_f16 v232, v184, v232\n"
    "v_lshlrev_b32_sdwa v192, 1, v168 dst_sel:DWORD dst_unused:UNUSED_PAD src0_sel:DWORD src1_sel:WORD_1\n"
    "v_mfma_f32_16x16x32_bf16 v[108:111], a[20:23], v[196:199], v[108:111]\n"
    "ds_read_u16 v192, v192\n"
    "v_exp_f16_e64 v234, v232\n"
    "v_mfma_f32_16x16x32_bf16 v[120:123], a[24:27], v[196:199], v[120:123]\n"
    "v_exp_f16_sdwa v234, v232 dst_sel:WORD_1 dst_unused:UNUSED_PRESERVE src0_sel:WORD_1\n"
    "v_mfma_f32_16x16x32_bf16 v[124:127], a[28:31], v[196:199], v[124:127]\n"
    "s_waitcnt lgkmcnt(6)\n"
    "v_or_b32_e32 v189, v189, v205\n"
    "v_pk_fma_f16 v234, v234, s49, s49\n"
    "v_mfma_f32_16x16x32_bf16 v[136:139], v[236:239], v[196:199], v[136:139]\n"
    "ds_read_b128 v[236:239], v244 offset:8192\n"
    "v_lshlrev_b32_sdwa v200, 1, v169 dst_sel:DWORD dst_unused:UNUSED_PAD src0_sel:DWORD src1_sel:WORD_1\n"
    "v_mfma_f32_16x16x32_bf16 v[140:143], a[244:247], v[196:199], v[140:143]\n"
    "ds_read_u16_d16_hi v200, v200\n"
    "buffer_store_dwordx4 a[252:255], v255, s[68:71], s59 offen offset:128 sc1\n"
    "v_lshlrev_b32_sdwa v193, 1, v170 dst_sel:DWORD dst_unused:UNUSED_PAD src0_sel:DWORD src1_sel:WORD_1\n"
    "v_lshlrev_b32_sdwa v201, 1, v171 dst_sel:DWORD dst_unused:UNUSED_PAD src0_sel:DWORD src1_sel:WORD_1\n"
    "v_mfma_f32_16x16x32_bf16 v[168:171], a[112:115], v[144:147], 0\n"
    "s_waitcnt lgkmcnt(6)\n"
    "ds_read_b128 a[252:255], v249 offset:1024\n"
    "ds_read_u16 v193, v193\n"
    "v_mfma_f32_16x16x32_bf16 v[168:171], a[160:163], v[148:151], v[168:171]\n"
    "s_nop 0\n"
    "ds_read_u16_d16_hi v201, v201\n"
    "v_lshlrev_b32_sdwa v194, 1, v172 dst_sel:DWORD dst_unused:UNUSED_PAD src0_sel:DWORD src1_sel:WORD_1\n"
    "v_lshlrev_b32_sdwa v202, 1, v173 dst_sel:DWORD dst_unused:UNUSED_PAD src0_sel:DWORD src1_sel:WORD_1\n"
    "v_lshlrev_b32_sdwa v195, 1, v174 dst_sel:DWORD dst_unused:UNUSED_PAD src0_sel:DWORD src1_sel:WORD_1\n"
    "v_lshlrev_b32_sdwa v203, 1, v175 dst_sel:DWORD dst_unused:UNUSED_PAD src0_sel:DWORD src1_sel:WORD_1\n"
    "v_rcp_f16_e32 v232, v234\n"
    "v_mfma_f32_16x16x32_bf16 v[168:171], a[72:75], v[152:155], v[168:171]\n"
    "s_waitcnt lgkmcnt(6)\n"
    "ds_read_u16 v194, v194\n"
    "ds_read_u16_d16_hi v202, v202\n"
    "v_or_b32_e64 v190, v190, v206\n"
    "v_rcp_f16_sdwa v232, v234 dst_sel:WORD_1 dst_unused:UNUSED_PRESERVE src0_sel:WORD_1\n"
    "v_mfma_f32_16x16x32_bf16 v[172:175], a[116:119], v[144:147], 0\n"
    "ds_read_u16 v195, v195\n"
    "v_lshlrev_b32_sdwa v196, 1, v176 dst_sel:DWORD dst_unused:UNUSED_PAD src0_sel:DWORD src1_sel:WORD_1\n"
    "v_mfma_f32_16x16x32_bf16 v[172:175], a[164:167], v[148:151], v[172:175]\n"
    "ds_read_b128 a[244:247], v244 offset:10240\n"
    "v_lshlrev_b32_sdwa v204, 1, v177 dst_sel:DWORD dst_unused:UNUSED_PAD src0_sel:DWORD src1_sel:WORD_1\n"
    "v_lshlrev_b32_sdwa v197, 1, v178 dst_sel:DWORD dst_unused:UNUSED_PAD src0_sel:DWORD src1_sel:WORD_1\n"
    "v_lshlrev_b32_sdwa v205, 1, v179 dst_sel:DWORD dst_unused:UNUSED_PAD src0_sel:DWORD src1_sel:WORD_1\n"
    "v_lshlrev_b32_sdwa v198, 1, v180 dst_sel:DWORD dst_unused:UNUSED_PAD src0_sel:DWORD src1_sel:WORD_1\n"
    "v_lshlrev_b32_sdwa v206, 1, v181 dst_sel:DWORD dst_unused:UNUSED_PAD src0_sel:DWORD src1_sel:WORD_1\n"
    "v_lshlrev_b32_sdwa v199, 1, v182 dst_sel:DWORD dst_unused:UNUSED_PAD src0_sel:DWORD src1_sel:WORD_1\n"
    "v_fma_mix_f32 v234, v184, v232, 0 op_sel_hi:[1,1,0]\n"
    "v_mfma_f32_16x16x32_bf16 v[172:175], a[240:243], v[152:155], v[172:175]\n"
    "s_waitcnt lgkmcnt(6)\n"
    "buffer_load_dwordx4 v[12:15], v246, s[40:43], s63 offen offset:0\n"
    "buffer_load_dwordx4 v[44:47], v246, s[40:43], s63 offen offset:64\n"
    "ds_read_u16_d16_hi v203, v203\n"
    "v_or_b32_e32 v191, v191, v207\n"
    "v_lshlrev_b32_sdwa v207, 1, v183 dst_sel:DWORD dst_unused:UNUSED_PAD src0_sel:DWORD src1_sel:WORD_1\n"
    "v_fma_mix_f32 v232, v184, v232, 0 op_sel:[1,1,0] op_sel_hi:[1,1,0]\n"
    "v_mfma_f32_16x16x32_bf16 v[176:179], a[112:115], v[156:159], 0\n"
    "ds_read_u16 v196, v196\n"
    "ds_read_u16_d16_hi v204, v204\n"
    "v_cvt_pk_bf16_f32 v184, v234, v232\n"
    "v_mfma_f32_16x16x32_bf16 v[176:179], a[160:163], v[160:163], v[176:179]\n"
    "v_pk_fma_f16 v232, v188, v188, s48\n"
    "v_or_b32_e32 v192, v192, v200\n"
    "v_mfma_f32_16x16x32_bf16 v[176:179], a[72:75], v[164:167], v[176:179]\n"
    "s_waitcnt lgkmcnt(7)\n"
    "ds_read_u16 v197, v197\n"
    "v_pk_mul_f16 v232, v188, v232\n"
    "v_mfma_f32_16x16x32_bf16 v[180:183], a[116:119], v[156:159], 0\n"
    "ds_read_u16_d16_hi v205, v205\n"
    "v_exp_f16_e64 v234, v232\n"
    "v_mfma_f32_16x16x32_bf16 v[180:183], a[164:167], v[160:163], v[180:183]\n"
    "v_exp_f16_sdwa v234, v232 dst_sel:WORD_1 dst_unused:UNUSED_PRESERVE src0_sel:WORD_1\n"
    "v_mfma_f32_16x16x32_bf16 v[180:183], a[240:243], v[164:167], v[180:183]\n"
    "ds_read_u16 v198, v198\n"
    "v_pk_fma_f16 v234, v234, s49, s49\n"
    "v_mfma_f32_16x16x32_bf16 v[96:99], a[32:35], v[184:187], v[96:99]\n"
    "s_waitcnt lgkmcnt(8)\n"
    "v_rcp_f16_e32 v232, v234\n"
    "v_mfma_f32_16x16x32_bf16 v[100:103], a[36:39], v[184:187], v[100:103]\n"
    "ds_read_u16_d16_hi v206, v206\n"
    "buffer_store_dwordx4 a[252:255], v255, s[68:71], s39 offen offset:0 sc1\n"
    "v_mfma_f32_16x16x32_bf16 v[112:115], a[40:43], v[184:187], v[112:115]\n"
    "s_nop 0\n"
    "ds_read_u16 v199, v199\n"
    "v_rcp_f16_sdwa v232, v234 dst_sel:WORD_1 dst_unused:UNUSED_PRESERVE src0_sel:WORD_1\n"
    "v_mfma_f32_16x16x32_bf16 v[116:119], a[44:47], v[184:187], v[116:119]\n"
    "s_waitcnt lgkmcnt(7)\n"
    "ds_read_b128 a[252:255], v249 offset:3072\n"
    "v_fma_mix_f32 v234, v188, v232, 0 op_sel_hi:[1,1,0]\n"
    "v_mfma_f32_16x16x32_bf16 v[128:131], v[236:239], v[184:187], v[128:131]\n"
    "ds_read_u16_d16_hi v207, v207\n"
    "v_fma_mix_f32 v232, v188, v232, 0 op_sel:[1,1,0] op_sel_hi:[1,1,0]\n"
    "v_mfma_f32_16x16x32_bf16 v[132:135], a[244:247], v[184:187], v[132:135]\n"
    "v_cvt_pk_bf16_f32 v188, v234, v232\n"
    "v_or_b32_e32 v193, v193, v201\n"
    "v_cvt_pk_f16_f32 v184, v168, v169\n"
    "v_mfma_f32_16x16x32_bf16 v[104:107], a[32:35], v[188:191], v[104:107]\n"
    "s_waitcnt lgkmcnt(7)\n"
    "ds_read_b128 a[240:243], v244 offset:38912\n"
    "v_lshlrev_b32_sdwa v185, 1, v170 dst_sel:DWORD dst_unused:UNUSED_PAD src0_sel:DWORD src1_sel:WORD_1\n"
    "v_mfma_f32_16x16x32_bf16 v[108:111], a[36:39], v[188:191], v[108:111]\n"
    "ds_read_u16 v185, v185\n"
    "v_lshlrev_b32_sdwa v201, 1, v171 dst_sel:DWORD dst_unused:UNUSED_PAD src0_sel:DWORD src1_sel:WORD_1\n"
    "v_mfma_f32_16x16x32_bf16 v[120:123], a[40:43], v[188:191], v[120:123]\n"
    "ds_read_u16_d16_hi v201, v201\n"
    "v_lshlrev_b32_sdwa v186, 1, v172 dst_sel:DWORD dst_unused:UNUSED_PAD src0_sel:DWORD src1_sel:WORD_1\n"
    "v_mfma_f32_16x16x32_bf16 v[124:127], a[44:47], v[188:191], v[124:127]\n"
    "v_or_b32_e32 v194, v194, v202\n"
    "v_lshlrev_b32_sdwa v202, 1, v173 dst_sel:DWORD dst_unused:UNUSED_PAD src0_sel:DWORD src1_sel:WORD_1\n"
    "v_mfma_f32_16x16x32_bf16 v[136:139], v[236:239], v[188:191], v[136:139]\n"
    "s_waitcnt lgkmcnt(8)\n"
    "buffer_load_dwordx4 v[16:19], v246, s[40:43], s52 offen offset:0\n"
    "buffer_load_dwordx4 v[48:51], v246, s[40:43], s52 offen offset:64\n"
    "ds_read_u16 v186, v186\n"
    "v_mfma_f32_16x16x32_bf16 v[140:143], a[244:247], v[188:191], v[140:143]\n"
    "ds_read_u16_d16_hi v202, v202\n"
    "v_lshlrev_b32_sdwa v187, 1, v174 dst_sel:DWORD dst_unused:UNUSED_PAD src0_sel:DWORD src1_sel:WORD_1\n"
    "v_mfma_f32_16x16x32_bf16 v[168:171], a[120:123], v[144:147], 0\n"
    "v_cvt_pk_f16_f32 v188, v176, v177\n"
    "v_lshlrev_b32_sdwa v189, 1, v178 dst_sel:DWORD dst_unused:UNUSED_PAD src0_sel:DWORD src1_sel:WORD_1\n"
    "v_mfma_f32_16x16x32_bf16 v[168:171], a[168:171], v[148:151], v[168:171]\n"
    "s_waitcnt lgkmcnt(8)\n"
    "ds_read_b128 v[236:239], v244 offset:12288\n"
    "v_lshlrev_b32_sdwa v190, 1, v180 dst_sel:DWORD dst_unused:UNUSED_PAD src0_sel:DWORD src1_sel:WORD_1\n"
    "v_mfma_f32_16x16x32_bf16 v[168:171], a[76:79], v[152:155], v[168:171]\n"
    "s_waitcnt lgkmcnt(7)\n"
    "ds_read_u16 v187, v187\n"
    "v_lshlrev_b32_sdwa v191, 1, v182 dst_sel:DWORD dst_unused:UNUSED_PAD src0_sel:DWORD src1_sel:WORD_1\n"
    "v_or_b32_e64 v195, v195, v203\n"
    "v_lshlrev_b32_sdwa v203, 1, v175 dst_sel:DWORD dst_unused:UNUSED_PAD src0_sel:DWORD src1_sel:WORD_1\n"
    "v_mfma_f32_16x16x32_bf16 v[172:175], a[124:127], v[144:147], 0\n"
    "ds_read_u16_d16_hi v203, v203\n"
    "v_or_b32_e64 v196, v196, v204\n"
    "v_mfma_f32_16x16x32_bf16 v[172:175], a[172:175], v[148:151], v[172:175]\n"
    "v_or_b32_e64 v197, v197, v205\n"
    "v_lshlrev_b32_sdwa v205, 1, v179 dst_sel:DWORD dst_unused:UNUSED_PAD src0_sel:DWORD src1_sel:WORD_1\n"
    "v_mfma_f32_16x16x32_bf16 v[176:179], a[120:123], v[156:159], 0\n"
    "s_waitcnt lgkmcnt(7)\n"
    "ds_read_b128 a[244:247], v244 offset:14336\n"
    "v_or_b32_e32 v198, v198, v206\n"
    "v_mfma_f32_16x16x32_bf16 v[172:175], a[240:243], v[152:155], v[172:175]\n"
    "ds_read_u16 v189, v189\n"
    "v_lshlrev_b32_sdwa v206, 1, v181 dst_sel:DWORD dst_unused:UNUSED_PAD src0_sel:DWORD src1_sel:WORD_1\n"
    "v_mfma_f32_16x16x32_bf16 v[176:179], a[168:171], v[160:163], v[176:179]\n"
    "buffer_store_dwordx4 a[252:255], v255, s[68:71], s39 offen offset:64 sc1\n"
    "ds_read_u16_d16_hi v205, v205\n"
    "v_mfma_f32_16x16x32_bf16 v[176:179], a[76:79], v[164:167], v[176:179]\n"
    "s_waitcnt lgkmcnt(8)\n"
    "v_or_b32_e32 v199, v199, v207\n"
    "v_lshlrev_b32_sdwa v207, 1, v183 dst_sel:DWORD dst_unused:UNUSED_PAD src0_sel:DWORD src1_sel:WORD_1\n"
    "v_mfma_f32_16x16x32_bf16 v[180:183], a[124:127], v[156:159], 0\n"
    "ds_read_b128 a[252:255], v249 offset:5120\n"
    "ds_read_u16 v190, v190\n"
    "v_mfma_f32_16x16x32_bf16 v[180:183], a[172:175], v[160:163], v[180:183]\n"
    "v_or_b32_e64 v185, v185, v201\n"
    "v_pk_fma_f16 v232, v184, v184, s48\n"
    "v_mfma_f32_16x16x32_bf16 v[180:183], a[240:243], v[164:167], v[180:183]\n"
    "ds_read_b128 v[144:147], v243 offset:0\n"
    "v_pk_mul_f16 v232, v184, v232\n"
    "v_mfma_f32_16x16x32_bf16 v[96:99], a[176:179], v[192:195], v[96:99]\n"
    "s_waitcnt lgkmcnt(8)\n"
    "ds_read_u16_d16_hi v206, v206\n"
    "v_or_b32_e32 v186, v186, v202\n"
    "v_mfma_f32_16x16x32_bf16 v[100:103], a[180:183], v[192:195], v[100:103]\n"
    "v_exp_f16_e64 v234, v232\n"
    "v_mfma_f32_16x16x32_bf16 v[112:115], a[184:187], v[192:195], v[112:115]\n"
    "ds_read_b128 v[148:151], v243 offset:1024\n"
    "v_exp_f16_sdwa v234, v232 dst_sel:WORD_1 dst_unused:UNUSED_PRESERVE src0_sel:WORD_1\n"
    "v_mfma_f32_16x16x32_bf16 v[116:119], a[188:191], v[192:195], v[116:119]\n"
    "ds_read_u16 v191, v191\n"
    "v_pk_fma_f16 v234, v234, s49, s49\n"
    "v_mfma_f32_16x16x32_bf16 v[128:131], v[236:239], v[192:195], v[128:131]\n"
    "s_waitcnt lgkmcnt(8)\n"
    "v_or_b32_e32 v187, v187, v203\n"
    "v_lshlrev_b32_sdwa v201, 1, v171 dst_sel:DWORD dst_unused:UNUSED_PAD src0_sel:DWORD src1_sel:WORD_1\n"
    "v_mfma_f32_16x16x32_bf16 v[132:135], a[244:247], v[192:195], v[132:135]\n"
    "ds_read_b128 v[152:155], v243 offset:2048\n"
    "ds_read_u16_d16_hi v207, v207\n"
    "v_mfma_f32_16x16x32_bf16 v[104:107], a[176:179], v[196:199], v[104:107]\n"
    "v_cvt_pk_f16_f32 v192, v168, v169\n"
    "v_lshlrev_b32_sdwa v193, 1, v170 dst_sel:DWORD dst_unused:UNUSED_PAD src0_sel:DWORD src1_sel:WORD_1\n"
    "v_mfma_f32_16x16x32_bf16 v[108:111], a[180:183], v[196:199], v[108:111]\n"
    "ds_read_b128 v[156:159], v243 offset:3072\n"
    "v_lshlrev_b32_sdwa v194, 1, v172 dst_sel:DWORD dst_unused:UNUSED_PAD src0_sel:DWORD src1_sel:WORD_1\n"
    "v_mfma_f32_16x16x32_bf16 v[120:123], a[184:187], v[196:199], v[120:123]\n"
    "s_waitcnt lgkmcnt(8)\n"
    "ds_read_b128 v[160:163], v243 offset:4096\n"
    "buffer_store_dwordx4 a[252:255], v255, s[68:71], s39 offen offset:128 sc1\n"
    "v_mfma_f32_16x16x32_bf16 v[124:127], a[188:191], v[196:199], v[124:127]\n"
    "s_nop 0\n"
    "ds_read_b128 v[164:167], v243 offset:5120\n"
    "ds_read_b128 a[240:243], v244 offset:32768\n"
    "v_mfma_f32_16x16x32_bf16 v[136:139], v[236:239], v[196:199], v[136:139]\n"
    "s_waitcnt lgkmcnt(9)\n"
    "ds_read_u16 v193, v193\n"
    "v_or_b32_e32 v189, v189, v205\n"
    "v_mfma_f32_16x16x32_bf16 v[140:143], a[244:247], v[196:199], v[140:143]\n"
    "v_lshlrev_b32_sdwa v202, 1, v173 dst_sel:DWORD dst_unused:UNUSED_PAD src0_sel:DWORD src1_sel:WORD_1\n"
    "v_lshlrev_b32_sdwa v195, 1, v174 dst_sel:DWORD dst_unused:UNUSED_PAD src0_sel:DWORD src1_sel:WORD_1\n"
    "v_mfma_f32_16x16x32_bf16 v[168:171], a[80:83], v[144:147], 0\n"
    "ds_read_u16_d16_hi v201, v201\n"
    "v_lshlrev_b32_sdwa v203, 1, v175 dst_sel:DWORD dst_unused:UNUSED_PAD src0_sel:DWORD src1_sel:WORD_1\n"
    "s_waitcnt lgkmcnt(9)\n"
    "v_mfma_f32_16x16x32_bf16 v[168:171], a[128:131], v[148:151], v[168:171]\n"
    "v_rcp_f16_e32 v232, v234\n"
    "v_or_b32_e64 v190, v190, v206\n"
    "v_rcp_f16_sdwa v232, v234 dst_sel:WORD_1 dst_unused:UNUSED_PRESERVE src0_sel:WORD_1\n"
    "v_mfma_f32_16x16x32_bf16 v[172:175], a[84:87], v[144:147], 0\n"
    "ds_read_u16 v194, v194\n"
    "ds_read_u16_d16_hi v202, v202\n"
    "s_waitcnt lgkmcnt(9)\n"
    "v_mfma_f32_16x16x32_bf16 v[168:171], a[48:51], v[152:155], v[168:171]\n"
    "s_nop 0\n"
    "v_cvt_pk_f16_f32 v196, v176, v177\n"
    "v_lshlrev_b32_sdwa v197, 1, v178 dst_sel:DWORD dst_unused:UNUSED_PAD src0_sel:DWORD src1_sel:WORD_1\n"
    "v_lshlrev_b32_sdwa v205, 1, v179 dst_sel:DWORD dst_unused:UNUSED_PAD src0_sel:DWORD src1_sel:WORD_1\n"
    "v_lshlrev_b32_sdwa v198, 1, v180 dst_sel:DWORD dst_unused:UNUSED_PAD src0_sel:DWORD src1_sel:WORD_1\n"
    "v_lshlrev_b32_sdwa v206, 1, v181 dst_sel:DWORD dst_unused:UNUSED_PAD src0_sel:DWORD src1_sel:WORD_1\n"
    "v_lshlrev_b32_sdwa v199, 1, v182 dst_sel:DWORD dst_unused:UNUSED_PAD src0_sel:DWORD src1_sel:WORD_1\n"
    "v_fma_mix_f32 v234, v184, v232, 0 op_sel_hi:[1,1,0]\n"
    "v_mfma_f32_16x16x32_bf16 v[172:175], a[132:135], v[148:151], v[172:175]\n"
    "s_waitcnt lgkmcnt(7)\n"
    "ds_read_b128 v[236:239], v244 offset:16384\n"
    "ds_read_u16 v195, v195\n"
    "v_or_b32_e32 v191, v191, v207\n"
    "v_lshlrev_b32_sdwa v207, 1, v183 dst_sel:DWORD dst_unused:UNUSED_PAD src0_sel:DWORD src1_sel:WORD_1\n"
    "v_fma_mix_f32 v232, v184, v232, 0 op_sel:[1,1,0] op_sel_hi:[1,1,0]\n"
    "v_mfma_f32_16x16x32_bf16 v[176:179], a[80:83], v[156:159], 0\n"
    "s_waitcnt lgkmcnt(7)\n"
    "ds_read_u16_d16_hi v203, v203\n"
    "v_cvt_pk_bf16_f32 v184, v234, v232\n"
    "v_mfma_f32_16x16x32_bf16 v[176:179], a[128:131], v[160:163], v[176:179]\n"
    "s_waitcnt lgkmcnt(7)\n"
    "v_pk_fma_f16 v232, v188, v188, s48\n"
    "v_lshlrev_b32_sdwa v200, 1, v169 dst_sel:DWORD dst_unused:UNUSED_PAD src0_sel:DWORD src1_sel:WORD_1\n"
    "v_mfma_f32_16x16x32_bf16 v[172:175], a[240:243], v[152:155], v[172:175]\n"
    "s_barrier\n"
    "v_mfma_f32_16x16x32_bf16 v[176:179], a[48:51], v[164:167], v[176:179]\n"
    "s_waitcnt lgkmcnt(5)\n"
    "ds_read_b128 a[244:247], v244 offset:18432\n"
    "ds_write_b128 v253, v[64:67] offset:2048\n"
    "v_mfma_f32_16x16x32_bf16 v[180:183], a[84:87], v[156:159], 0\n"
    "v_pk_mul_f16 v232, v188, v232\n"
    "v_or_b32_e64 v193, v193, v201\n"
    "v_mfma_f32_16x16x32_bf16 v[180:183], a[132:135], v[160:163], v[180:183]\n"
    "v_exp_f16_e32 v234, v232\n"
    "v_mfma_f32_16x16x32_bf16 v[180:183], a[240:243], v[164:167], v[180:183]\n"
    "s_waitcnt lgkmcnt(4)\n"
    "ds_read_u16 v197, v197\n"
    "ds_write_b128 v253, v[68:71] offset:5120\n"
    "v_exp_f16_sdwa v234, v232 dst_sel:WORD_1 dst_unused:UNUSED_PRESERVE src0_sel:WORD_1\n"
    "v_mfma_f32_16x16x32_bf16 v[96:99], a[192:195], v[184:187], v[96:99]\n"
    "v_pk_fma_f16 v234, v234, s49, s49\n"
    "v_or_b32_e64 v194, v194, v202\n"
    "v_mfma_f32_16x16x32_bf16 v[100:103], a[196:199], v[184:187], v[100:103]\n"
    "v_rcp_f16_e32 v232, v234\n"
    "v_mfma_f32_16x16x32_bf16 v[112:115], a[200:203], v[184:187], v[112:115]\n"
    "s_waitcnt lgkmcnt(3)\n"
    "ds_read_u16_d16_hi v205, v205\n"
    "ds_write_b128 v253, v[88:91] offset:0\n"
    "v_rcp_f16_sdwa v232, v234 dst_sel:WORD_1 dst_unused:UNUSED_PRESERVE src0_sel:WORD_1\n"
    "v_mfma_f32_16x16x32_bf16 v[116:119], a[204:207], v[184:187], v[116:119]\n"
    "v_fma_mix_f32 v234, v188, v232, 0 op_sel_hi:[1,1,0]\n"
    "v_fma_mix_f32 v232, v188, v232, 0 op_sel:[1,1,0] op_sel_hi:[1,1,0]\n"
    "v_mfma_f32_16x16x32_bf16 v[128:131], v[236:239], v[184:187], v[128:131]\n"
    "v_cvt_pk_bf16_f32 v188, v234, v232\n"
    "v_pk_fma_f16 v232, v192, v192, s48\n"
    "v_mfma_f32_16x16x32_bf16 v[132:135], a[244:247], v[184:187], v[132:135]\n"
    "buffer_load_dwordx4 a[232:235], v245, s[40:43], s57 offen offset:64\n"
    "buffer_load_dwordx4 a[224:227], v245, s[40:43], s57 offen offset:0\n"
    "ds_read_u16 v198, v198\n"
    "v_mfma_f32_16x16x32_bf16 v[104:107], a[192:195], v[188:191], v[104:107]\n"
    "ds_read_u16_d16_hi v206, v206\n"
    "ds_write_b128 v253, v[92:95] offset:3072\n"
    "v_mfma_f32_16x16x32_bf16 v[108:111], a[196:199], v[188:191], v[108:111]\n"
    "v_or_b32_e64 v195, v195, v203\n"
    "v_pk_mul_f16 v232, v192, v232\n"
    "v_mfma_f32_16x16x32_bf16 v[120:123], a[200:203], v[188:191], v[120:123]\n"
    "v_exp_f16_e64 v234, v232\n"
    "v_mfma_f32_16x16x32_bf16 v[124:127], a[204:207], v[188:191], v[124:127]\n"
    "ds_read_u16 v199, v199\n"
    "ds_write_b128 v253, v[72:75] offset:1024\n"
    "v_mfma_f32_16x16x32_bf16 v[136:139], v[236:239], v[188:191], v[136:139]\n"
    "s_waitcnt lgkmcnt(5)\n"
    "v_or_b32_e32 v197, v197, v205\n"
    "v_exp_f16_sdwa v234, v232 dst_sel:WORD_1 dst_unused:UNUSED_PRESERVE src0_sel:WORD_1\n"
    "v_mfma_f32_16x16x32_bf16 v[140:143], a[244:247], v[188:191], v[140:143]\n"
    "v_pk_fma_f16 v234, v234, s49, s49\n"
    "v_lshlrev_b32_sdwa v184, 1, v168 dst_sel:DWORD dst_unused:UNUSED_PAD src0_sel:DWORD src1_sel:WORD_1\n"
    "v_lshlrev_b32_sdwa v185, 1, v170 dst_sel:DWORD dst_unused:UNUSED_PAD src0_sel:DWORD src1_sel:WORD_1\n"
    "v_lshlrev_b32_sdwa v201, 1, v171 dst_sel:DWORD dst_unused:UNUSED_PAD src0_sel:DWORD src1_sel:WORD_1\n"
    "v_mfma_f32_16x16x32_bf16 v[168:171], a[88:91], v[144:147], 0\n"
    "ds_read_u16_d16_hi v207, v207\n"
    "ds_write_b128 v253, v[76:79] offset:4096\n"
    "v_mfma_f32_16x16x32_bf16 v[168:171], a[136:139], v[148:151], v[168:171]\n"
    "v_lshlrev_b32_sdwa v186, 1, v172 dst_sel:DWORD dst_unused:UNUSED_PAD src0_sel:DWORD src1_sel:WORD_1\n"
    "v_lshlrev_b32_sdwa v202, 1, v173 dst_sel:DWORD dst_unused:UNUSED_PAD src0_sel:DWORD src1_sel:WORD_1\n"
    "v_lshlrev_b32_sdwa v187, 1, v174 dst_sel:DWORD dst_unused:UNUSED_PAD src0_sel:DWORD src1_sel:WORD_1\n"
    "v_lshlrev_b32_sdwa v203, 1, v175 dst_sel:DWORD dst_unused:UNUSED_PAD src0_sel:DWORD src1_sel:WORD_1\n"
    "v_rcp_f16_e32 v232, v234\n"
    "v_mfma_f32_16x16x32_bf16 v[168:171], a[52:55], v[152:155], v[168:171]\n"
    "s_waitcnt lgkmcnt(4)\n"
    "ds_read_b128 a[240:243], v244 offset:36864\n"
    "ds_read_u16 v184, v184\n"
    "v_or_b32_e64 v198, v198, v206\n"
    "v_rcp_f16_sdwa v232, v234 dst_sel:WORD_1 dst_unused:UNUSED_PRESERVE src0_sel:WORD_1\n"
    "v_mfma_f32_16x16x32_bf16 v[172:175], a[92:95], v[144:147], 0\n"
    "ds_read_u16_d16_hi v200, v200\n"
    "ds_read_u16 v185, v185\n"
    "v_lshlrev_b32_sdwa v188, 1, v176 dst_sel:DWORD dst_unused:UNUSED_PAD src0_sel:DWORD src1_sel:WORD_1\n"
    "v_lshlrev_b32_sdwa v204, 1, v177 dst_sel:DWORD dst_unused:UNUSED_PAD src0_sel:DWORD src1_sel:WORD_1\n"
    "v_lshlrev_b32_sdwa v189, 1, v178 dst_sel:DWORD dst_unused:UNUSED_PAD src0_sel:DWORD src1_sel:WORD_1\n"
    "v_lshlrev_b32_sdwa v205, 1, v179 dst_sel:DWORD dst_unused:UNUSED_PAD src0_sel:DWORD src1_sel:WORD_1\n"
    "v_lshlrev_b32_sdwa v190, 1, v180 dst_sel:DWORD dst_unused:UNUSED_PAD src0_sel:DWORD src1_sel:WORD_1\n"
    "v_lshlrev_b32_sdwa v206, 1, v181 dst_sel:DWORD dst_unused:UNUSED_PAD src0_sel:DWORD src1_sel:WORD_1\n"
    "v_lshlrev_b32_sdwa v191, 1, v182 dst_sel:DWORD dst_unused:UNUSED_PAD src0_sel:DWORD src1_sel:WORD_1\n"
    "v_fma_mix_f32 v234, v192, v232, 0 op_sel_hi:[1,1,0]\n"
    "v_mfma_f32_16x16x32_bf16 v[172:175], a[140:143], v[148:151], v[172:175]\n"
    "ds_read_u16_d16_hi v201, v201\n"
    "ds_read_u16 v186, v186\n"
    "buffer_load_dwordx4 a[236:239], v245, s[40:43], s88 offen offset:64\n"
    "buffer_load_dwordx4 a[228:231], v245, s[40:43], s88 offen offset:0\n"
    "v_fma_mix_f32 v232, v192, v232, 0 op_sel:[1,1,0] op_sel_hi:[1,1,0]\n"
    "v_mfma_f32_16x16x32_bf16 v[176:179], a[88:91], v[156:159], 0\n"
    "s_waitcnt lgkmcnt(6)\n"
    "ds_read_b128 v[236:239], v244 offset:20480\n"
    "ds_read_u16_d16_hi v202, v202\n"
    "v_or_b32_e32 v199, v199, v207\n"
    "v_lshlrev_b32_sdwa v207, 1, v183 dst_sel:DWORD dst_unused:UNUSED_PAD src0_sel:DWORD src1_sel:WORD_1\n"
    "v_cvt_pk_bf16_f32 v192, v234, v232\n"
    "v_mfma_f32_16x16x32_bf16 v[176:179], a[136:139], v[160:163], v[176:179]\n"
    "s_waitcnt lgkmcnt(7)\n"
    "v_pk_fma_f16 v232, v196, v196, s48\n"
    "v_mov_b32_e32 v254, 0\n"
    "v_mfma_f32_16x16x32_bf16 v[172:175], a[240:243], v[152:155], v[172:175]\n"
    "ds_read_u16 v187, v187\n"
    "v_pk_mul_f16 v232, v196, v232\n"
    "v_mfma_f32_16x16x32_bf16 v[176:179], a[52:55], v[164:167], v[176:179]\n"
    "ds_read_b128 a[244:247], v244 offset:22528\n"
    "v_exp_f16_e64 v234, v232\n"
    "v_mfma_f32_16x16x32_bf16 v[180:183], a[92:95], v[156:159], 0\n"
    "ds_read_u16_d16_hi v203, v203\n"
    "v_exp_f16_sdwa v234, v232 dst_sel:WORD_1 dst_unused:UNUSED_PRESERVE src0_sel:WORD_1\n"
    "v_mfma_f32_16x16x32_bf16 v[180:183], a[140:143], v[160:163], v[180:183]\n"
    "s_waitcnt lgkmcnt(8)\n"
    "v_pk_fma_f16 v234, v234, s49, s49\n"
    "v_or_b32_e32 v184, v184, v200\n"
    "v_mfma_f32_16x16x32_bf16 v[180:183], a[240:243], v[164:167], v[180:183]\n"
    "ds_read_u16 v188, v188\n"
    "v_rcp_f16_e32 v232, v234\n"
    "v_mfma_f32_16x16x32_bf16 v[96:99], a[208:211], v[192:195], v[96:99]\n"
    "ds_read_u16_d16_hi v204, v204\n"
    "v_rcp_f16_sdwa v232, v234 dst_sel:WORD_1 dst_unused:UNUSED_PRESERVE src0_sel:WORD_1\n"
    "v_mfma_f32_16x16x32_bf16 v[100:103], a[212:215], v[192:195], v[100:103]\n"
    "s_waitcnt lgkmcnt(8)\n"
    "v_fma_mix_f32 v234, v196, v232, 0 op_sel_hi:[1,1,0]\n"
    "v_fma_mix_f32 v232, v196, v232, 0 op_sel:[1,1,0] op_sel_hi:[1,1,0]\n"
    "v_mfma_f32_16x16x32_bf16 v[112:115], a[216:219], v[192:195], v[112:115]\n"
    "ds_read_u16 v189, v189\n"
    "v_cvt_pk_bf16_f32 v196, v234, v232\n"
    "v_mfma_f32_16x16x32_bf16 v[116:119], a[220:223], v[192:195], v[116:119]\n"
    "s_waitcnt lgkmcnt(7)\n"
    "ds_read_u16_d16_hi v205, v205\n"
    "v_or_b32_e64 v185, v185, v201\n"
    "v_mfma_f32_16x16x32_bf16 v[128:131], v[236:239], v[192:195], v[128:131]\n"
    "s_waitcnt vmcnt(21)\n"
    "v_lshlrev_b32_sdwa v201, 1, v171 dst_sel:DWORD dst_unused:UNUSED_PAD src0_sel:DWORD src1_sel:WORD_1\n"
    "v_cvt_pk_bf16_f32 v96, v96, v97\n"
    "v_mfma_f32_16x16x32_bf16 v[104:107], a[208:211], v[196:199], v[104:107]\n"
    "v_cvt_pk_bf16_f32 v97, v98, v99\n"
    "v_cvt_pk_bf16_f32 v98, v100, v101\n"
    "v_mfma_f32_16x16x32_bf16 v[108:111], a[212:215], v[196:199], v[108:111]\n"
    "s_waitcnt lgkmcnt(7)\n"
    "v_or_b32_e32 v186, v186, v202\n"
    "v_lshlrev_b32_sdwa v202, 1, v173 dst_sel:DWORD dst_unused:UNUSED_PAD src0_sel:DWORD src1_sel:WORD_1\n"
    "v_mfma_f32_16x16x32_bf16 v[120:123], a[216:219], v[196:199], v[120:123]\n"
    "s_waitcnt lgkmcnt(5)\n"
    "ds_write2_b32 v248, v0, v1 offset0:0 offset1:20\n"
    "v_mfma_f32_16x16x32_bf16 v[132:135], a[244:247], v[192:195], v[132:135]\n"
    "s_nop 0\n"
    "v_cvt_pk_f16_f32 v192, v168, v169\n"
    "v_lshlrev_b32_sdwa v193, 1, v170 dst_sel:DWORD dst_unused:UNUSED_PAD src0_sel:DWORD src1_sel:WORD_1\n"
    "v_mfma_f32_16x16x32_bf16 v[124:127], a[220:223], v[196:199], v[124:127]\n"
    "ds_read_u16 v190, v190\n"
    "ds_write2_b32 v248, v2, v3 offset0:40 offset1:60\n"
    "v_mfma_f32_16x16x32_bf16 v[136:139], v[236:239], v[196:199], v[136:139]\n"
    "s_waitcnt lgkmcnt(7)\n"
    "v_add_u32_e32 v248, 1360, v248\n"
    "v_lshlrev_b32_sdwa v194, 1, v172 dst_sel:DWORD dst_unused:UNUSED_PAD src0_sel:DWORD src1_sel:WORD_1\n"
    "v_mfma_f32_16x16x32_bf16 v[140:143], a[244:247], v[196:199], v[140:143]\n"
    "s_waitcnt vmcnt(19)\n"
    "ds_read_u16_d16_hi v206, v206\n"
    "ds_write2_b32 v235, v20, v21 offset0:0 offset1:20\n"
    "v_mfma_f32_16x16x32_bf16 v[168:171], a[96:99], v[144:147], 0\n"
    "v_lshlrev_b32_sdwa v195, 1, v174 dst_sel:DWORD dst_unused:UNUSED_PAD src0_sel:DWORD src1_sel:WORD_1\n"
    "v_cvt_pk_f16_f32 v196, v176, v177\n"
    "v_mfma_f32_16x16x32_bf16 v[168:171], a[144:147], v[148:151], v[168:171]\n"
    "s_waitcnt lgkmcnt(7)\n"
    "ds_write2_b32 v235, v22, v23 offset0:40 offset1:60\n"
    "v_mfma_f32_16x16x32_bf16 v[168:171], a[56:59], v[152:155], v[168:171]\n"
    "v_add_u32_e32 v235, 1360, v235\n"
    "v_lshlrev_b32_sdwa v197, 1, v178 dst_sel:DWORD dst_unused:UNUSED_PAD src0_sel:DWORD src1_sel:WORD_1\n"
    "s_waitcnt vmcnt(18)\n"
    "ds_write2_b32 v248, v4, v5 offset0:0 offset1:20\n"
    "v_lshlrev_b32_sdwa v198, 1, v180 dst_sel:DWORD dst_unused:UNUSED_PAD src0_sel:DWORD src1_sel:WORD_1\n"
    "v_lshlrev_b32_sdwa v199, 1, v182 dst_sel:DWORD dst_unused:UNUSED_PAD src0_sel:DWORD src1_sel:WORD_1\n"
    "v_or_b32_e32 v187, v187, v203\n"
    "v_lshlrev_b32_sdwa v203, 1, v175 dst_sel:DWORD dst_unused:UNUSED_PAD src0_sel:DWORD src1_sel:WORD_1\n"
    "v_mfma_f32_16x16x32_bf16 v[172:175], a[100:103], v[144:147], 0\n"
    "s_waitcnt vmcnt(16) lgkmcnt(6)\n"
    "ds_read_u16 v191, v191\n"
    "ds_write2_b32 v248, v6, v7 offset0:40 offset1:60\n"
    "v_mfma_f32_16x16x32_bf16 v[172:175], a[148:151], v[148:151], v[172:175]\n"
    "v_add_u32_e32 v248, 1360, v248\n"
    "v_or_b32_e32 v188, v188, v204\n"
    "v_mfma_f32_16x16x32_bf16 v[172:175], a[60:63], v[152:155], v[172:175]\n"
    "s_waitcnt vmcnt(13)\n"
    "ds_read_u16_d16_hi v207, v207\n"
    "ds_write2_b32 v235, v24, v25 offset0:0 offset1:20\n"
    "v_or_b32_e64 v189, v189, v205\n"
    "v_lshlrev_b32_sdwa v205, 1, v179 dst_sel:DWORD dst_unused:UNUSED_PAD src0_sel:DWORD src1_sel:WORD_1\n"
    "v_mfma_f32_16x16x32_bf16 v[176:179], a[96:99], v[156:159], 0\n"
    "s_waitcnt lgkmcnt(5)\n"
    "v_cvt_pk_bf16_f32 v99, v102, v103\n"
    "v_or_b32_e32 v190, v190, v206\n"
    "v_mfma_f32_16x16x32_bf16 v[176:179], a[144:147], v[160:163], v[176:179]\n"
    "ds_read_u16 v193, v193\n"
    "ds_write_b128 v242, v[96:99] offset:24640\n"
    "v_mfma_f32_16x16x32_bf16 v[176:179], a[56:59], v[164:167], v[176:179]\n"
    "buffer_load_dwordx4 v[64:67], v246, s[40:43], s60 offen offset:128\n"
    "v_lshlrev_b32_sdwa v206, 1, v181 dst_sel:DWORD dst_unused:UNUSED_PAD src0_sel:DWORD src1_sel:WORD_1\n"
    "v_mfma_f32_16x16x32_bf16 v[96:99], a[0:3], v[184:187], 0\n"
    "v_cvt_pk_bf16_f32 v112, v112, v113\n"
    "v_cvt_pk_bf16_f32 v113, v114, v115\n"
    "v_mfma_f32_16x16x32_bf16 v[100:103], a[4:7], v[184:187], 0\n"
    "ds_read_u16_d16_hi v201, v201\n"
    "ds_write2_b32 v235, v26, v27 offset0:40 offset1:60\n"
    "v_add_u32_e32 v235, -1360, v235\n"
    "v_cvt_pk_bf16_f32 v114, v116, v117\n"
    "v_cvt_pk_bf16_f32 v115, v118, v119\n"
    "v_cvt_pk_bf16_f32 v128, v128, v129\n"
    "v_cvt_pk_bf16_f32 v129, v130, v131\n"
    "ds_write_b128 v242, v[112:115] offset:26688\n"
    "v_mfma_f32_16x16x32_bf16 v[112:115], a[8:11], v[184:187], 0\n"
    "s_waitcnt lgkmcnt(5)\n"
    "v_or_b32_e32 v191, v191, v207\n"
    "v_lshlrev_b32_sdwa v207, 1, v183 dst_sel:DWORD dst_unused:UNUSED_PAD src0_sel:DWORD src1_sel:WORD_1\n"
    "v_mfma_f32_16x16x32_bf16 v[180:183], a[100:103], v[156:159], 0\n"
    "ds_read_u16 v194, v194\n"
    "ds_write2_b32 v248, v8, v9 offset0:0 offset1:20\n"
    "v_mfma_f32_16x16x32_bf16 v[180:183], a[148:151], v[160:163], v[180:183]\n"
    "v_cvt_pk_bf16_f32 v130, v132, v133\n"
    "v_cvt_pk_bf16_f32 v131, v134, v135\n"
    "v_mfma_f32_16x16x32_bf16 v[180:183], a[60:63], v[164:167], v[180:183]\n"
    "ds_read_u16_d16_hi v202, v202\n"
    "ds_write_b128 v242, v[128:131] offset:28736\n"
    "v_mfma_f32_16x16x32_bf16 v[116:119], a[12:15], v[184:187], 0\n"
    "s_waitcnt lgkmcnt(5)\n"
    "v_or_b32_e32 v193, v193, v201\n"
    "v_cvt_pk_bf16_f32 v104, v104, v105\n"
    "v_cvt_pk_bf16_f32 v105, v106, v107\n"
    "v_cvt_pk_bf16_f32 v106, v108, v109\n"
    "v_cvt_pk_bf16_f32 v107, v110, v111\n"
    "v_mfma_f32_16x16x32_bf16 v[108:111], a[4:7], v[188:191], 0\n"
    "ds_read_b128 v[236:239], v244 offset:0\n"
    "ds_write_b128 v242, v[104:107] offset:25664\n"
    "v_mfma_f32_16x16x32_bf16 v[104:107], a[0:3], v[188:191], 0\n"
    "v_cvt_pk_bf16_f32 v120, v120, v121\n"
    "v_cvt_pk_bf16_f32 v121, v122, v123\n"
    "v_cvt_pk_bf16_f32 v122, v124, v125\n"
    "v_cvt_pk_bf16_f32 v123, v126, v127\n"
    "v_cvt_pk_bf16_f32 v136, v136, v137\n"
    "v_pk_fma_f16 v232, v192, v192, s48\n"
    "v_mfma_f32_16x16x32_bf16 v[124:127], a[12:15], v[188:191], 0\n"
    "ds_read_u16 v195, v195\n"
    "ds_write_b128 v242, v[120:123] offset:27712\n"
    "v_mfma_f32_16x16x32_bf16 v[120:123], a[8:11], v[188:191], 0\n"
    "v_cvt_pk_bf16_f32 v137, v138, v139\n"
    "v_cvt_pk_bf16_f32 v138, v140, v141\n"
    "s_waitcnt lgkmcnt(4)\n"
    "v_or_b32_e32 v194, v194, v202\n"
    "v_cvt_pk_bf16_f32 v139, v142, v143\n"
    "v_pk_mul_f16 v232, v192, v232\n"
    "v_lshlrev_b32_sdwa v201, 1, v171 dst_sel:DWORD dst_unused:UNUSED_PAD src0_sel:DWORD src1_sel:WORD_1\n"
    "ds_write2_b32 v248, v10, v11 offset0:40 offset1:60\n"
    "buffer_load_dwordx4 v[68:71], v246, s[40:43], s61 offen offset:128\n"
    "s_waitcnt lgkmcnt(4)\n"
    "v_mfma_f32_16x16x32_bf16 v[128:131], v[236:239], v[184:187], 0\n"
    "s_nop 0\n"
    "ds_read_u16_d16_hi v203, v203\n"
    "ds_write_b128 v242, v[136:139] offset:29760\n"
    "v_mfma_f32_16x16x32_bf16 v[136:139], v[236:239], v[188:191], 0\n"
    "v_add_u32_e32 v248, 1360, v248\n"
    "v_exp_f16_e32 v234, v232\n"
    "v_lshlrev_b32_sdwa v202, 1, v173 dst_sel:DWORD dst_unused:UNUSED_PAD src0_sel:DWORD src1_sel:WORD_1\n"
    "v_exp_f16_sdwa v234, v232 dst_sel:WORD_1 dst_unused:UNUSED_PRESERVE src0_sel:WORD_1\n"
    "s_waitcnt vmcnt(12)\n"
    "ds_write2_b32 v248, v12, v13 offset0:0 offset1:20\n"
    "v_pk_fma_f16 v234, v234, s49, s49\n"
    "v_rcp_f16_e64 v232, v234\n"
    "ds_read_b128 a[244:247], v244 offset:2048\n"
    "ds_write2_b32 v248, v14, v15 offset0:40 offset1:60\n"
    "v_add_u32_e32 v248, 1360, v248\n"
    "v_rcp_f16_sdwa v232, v234 dst_sel:WORD_1 dst_unused:UNUSED_PRESERVE src0_sel:WORD_1\n"
    "ds_read_u16 v197, v197\n"
    "s_waitcnt vmcnt(9)\n"
    "ds_write2_b32 v248, v16, v17 offset0:0 offset1:20\n"
    "v_fma_mix_f32 v234, v192, v232, 0 op_sel_hi:[1,1,0]\n"
    "s_waitcnt lgkmcnt(5)\n"
    "v_or_b32_e64 v195, v195, v203\n"
    "v_lshlrev_b32_sdwa v203, 1, v175 dst_sel:DWORD dst_unused:UNUSED_PAD src0_sel:DWORD src1_sel:WORD_1\n"
    "ds_read_u16_d16_hi v205, v205\n"
    "ds_write2_b32 v248, v18, v19 offset0:40 offset1:60\n"
    "v_mfma_f32_32x32x1_2b_f32 v[0:31], v254, v254, 0\n"
    "v_add_u32_e32 v248, -5440, v248\n"
    "v_fma_mix_f32 v232, v192, v232, 0 op_sel:[1,1,0] op_sel_hi:[1,1,0]\n"
    "ds_read_u16 v198, v198\n"
    "ds_read_u16_d16_hi v206, v206\n"
    "ds_read_u16 v199, v199\n"
    "v_cvt_pk_bf16_f32 v192, v234, v232\n"
    "ds_read_u16_d16_hi v207, v207\n"
    "v_pk_fma_f16 v232, v196, v196, s48\n"
    "v_pk_mul_f16 v232, v196, v232\n"
    "v_exp_f16_e32 v234, v232\n"
    "s_waitcnt lgkmcnt(8)\n"
    "v_mfma_f32_16x16x32_bf16 v[132:135], a[244:247], v[184:187], 0\n"
    "ds_read_b128 v[216:219], v251 offset:0\n"
    "ds_read_b128 v[220:223], v251 offset:336\n"
    "v_mfma_f32_16x16x32_bf16 v[140:143], a[244:247], v[188:191], 0\n"
    "v_cvt_pk_f16_f32 v184, v168, v169\n"
    "v_lshlrev_b32_sdwa v185, 1, v170 dst_sel:DWORD dst_unused:UNUSED_PAD src0_sel:DWORD src1_sel:WORD_1\n"
    "v_mfma_f32_16x16x32_bf16 v[168:171], a[104:107], v[144:147], 0\n"
    "ds_read_u16 v185, v185\n"
    "v_lshlrev_b32_sdwa v186, 1, v172 dst_sel:DWORD dst_unused:UNUSED_PAD src0_sel:DWORD src1_sel:WORD_1\n"
    "v_mfma_f32_16x16x32_bf16 v[168:171], a[152:155], v[148:151], v[168:171]\n"
    "s_waitcnt lgkmcnt(7)\n"
    "ds_read_u16_d16_hi v201, v201\n"
    "v_or_b32_e32 v197, v197, v205\n"
    "v_mfma_f32_16x16x32_bf16 v[168:171], a[64:67], v[152:155], v[168:171]\n"
    "s_barrier\n"
    "buffer_load_dwordx4 v[72:75], v246, s[40:43], s62 offen offset:128\n"
    "ds_read_b128 v[208:211], v250 offset:0\n"
    "v_lshlrev_b32_sdwa v187, 1, v174 dst_sel:DWORD dst_unused:UNUSED_PAD src0_sel:DWORD src1_sel:WORD_1\n"
    "v_mfma_f32_16x16x32_bf16 v[172:175], a[108:111], v[144:147], 0\n"
    "s_waitcnt lgkmcnt(7)\n"
    "ds_read_b128 v[212:215], v250 offset:320\n"
    "ds_read_u16 v186, v186\n"
    "v_mfma_f32_16x16x32_bf16 v[172:175], a[156:159], v[148:151], v[172:175]\n"
    "ds_read_u16_d16_hi v202, v202\n"
    "v_or_b32_e64 v198, v198, v206\n"
    "v_mfma_f32_16x16x32_bf16 v[172:175], a[68:71], v[152:155], v[172:175]\n"
    "ds_read_b128 v[236:239], v244 offset:4096\n"
    "v_cvt_pk_f16_f32 v188, v176, v177\n"
    "v_lshlrev_b32_sdwa v189, 1, v178 dst_sel:DWORD dst_unused:UNUSED_PAD src0_sel:DWORD src1_sel:WORD_1\n"
    "v_lshlrev_b32_sdwa v205, 1, v179 dst_sel:DWORD dst_unused:UNUSED_PAD src0_sel:DWORD src1_sel:WORD_1\n"
    "v_mfma_f32_16x16x32_bf16 v[176:179], a[104:107], v[156:159], 0\n"
    "s_waitcnt lgkmcnt(8)\n"
    "ds_read_u16 v187, v187\n"
    "v_lshlrev_b32_sdwa v190, 1, v180 dst_sel:DWORD dst_unused:UNUSED_PAD src0_sel:DWORD src1_sel:WORD_1\n"
    "v_mfma_f32_16x16x32_bf16 v[176:179], a[152:155], v[160:163], v[176:179]\n"
    "s_nop 0\n"
    "v_lshlrev_b32_sdwa v206, 1, v181 dst_sel:DWORD dst_unused:UNUSED_PAD src0_sel:DWORD src1_sel:WORD_1\n"
    "v_lshlrev_b32_sdwa v191, 1, v182 dst_sel:DWORD dst_unused:UNUSED_PAD src0_sel:DWORD src1_sel:WORD_1\n"
    "v_mfma_f32_16x16x32_bf16 v[176:179], a[64:67], v[164:167], v[176:179]\n"
    "ds_read_u16_d16_hi v203, v203\n"
    "v_or_b32_e64 v199, v199, v207\n"
    "v_lshlrev_b32_sdwa v207, 1, v183 dst_sel:DWORD dst_unused:UNUSED_PAD src0_sel:DWORD src1_sel:WORD_1\n"
    "v_mfma_f32_16x16x32_bf16 v[180:183], a[108:111], v[156:159], 0\n"
    "ds_read_b128 a[244:247], v244 offset:6144\n"
    "v_exp_f16_sdwa v234, v232 dst_sel:WORD_1 dst_unused:UNUSED_PRESERVE src0_sel:WORD_1\n"
    "v_mfma_f32_16x16x32_bf16 v[180:183], a[156:159], v[160:163], v[180:183]\n"
    "ds_read_u16 v189, v189\n"
    "v_pk_fma_f16 v234, v234, s49, s49\n"
    "v_mfma_f32_16x16x32_bf16 v[180:183], a[68:71], v[164:167], v[180:183]\n"
    "s_waitcnt lgkmcnt(8)\n"
    "v_rcp_f16_e64 v232, v234\n"
    "v_mfma_f32_16x16x32_bf16 v[96:99], a[16:19], v[192:195], v[96:99]\n"
    "s_waitcnt lgkmcnt(7)\n"
    "ds_read_u16_d16_hi v205, v205\n"
    "v_lshlrev_b32_e64 v254, 3, v244\n"
    "v_smfmac_f32_16x16x64_bf16 v[0:3], v[216:219], v[208:215], v240\n"
    "ds_read_b128 v[216:219], v252 offset:0\n"
    "v_smfmac_f32_16x16x64_bf16 v[4:7], v[220:223], v[208:215], v240 abid:1\n"
    "ds_read_b128 v[208:211], v250 offset:640\n"
    "v_mfma_f32_16x16x32_bf16 v[100:103], a[20:23], v[192:195], v[100:103]\n"
    "ds_read_b128 v[212:215], v250 offset:960\n"
    "v_rcp_f16_sdwa v232, v234 dst_sel:WORD_1 dst_unused:UNUSED_PRESERVE src0_sel:WORD_1\n"
    "v_mfma_f32_16x16x32_bf16 v[112:115], a[24:27], v[192:195], v[112:115]\n"
    "s_waitcnt lgkmcnt(8)\n"
    "ds_read_b128 v[220:223], v252 offset:336\n"
    "v_or_b32_e32 v185, v185, v201\n"
    "v_mfma_f32_16x16x32_bf16 v[116:119], a[28:31], v[192:195], v[116:119]\n"
    "ds_read_u16 v190, v190\n"
    "v_fma_mix_f32 v234, v196, v232, 0 op_sel_hi:[1,1,0]\n"
    "v_mfma_f32_16x16x32_bf16 v[128:131], v[236:239], v[192:195], v[128:131]\n"
    "v_fma_mix_f32 v232, v196, v232, 0 op_sel:[1,1,0] op_sel_hi:[1,1,0]\n"
    "v_or_b32_e64 v186, v186, v202\n"
    "v_cvt_pk_bf16_f32 v196, v234, v232\n"
    "v_pk_fma_f16 v232, v184, v184, s48\n"
    "v_lshlrev_b32_sdwa v200, 1, v169 dst_sel:DWORD dst_unused:UNUSED_PAD src0_sel:DWORD src1_sel:WORD_1\n"
    "v_mfma_f32_16x16x32_bf16 v[104:107], a[16:19], v[196:199], v[104:107]\n"
    "ds_read_u16_d16_hi v206, v206\n"
    "v_pk_mul_f16 v232, v184, v232\n"
    "v_mfma_f32_16x16x32_bf16 v[108:111], a[20:23], v[196:199], v[108:111]\n"
    "s_waitcnt lgkmcnt(8)\n"
    "ds_read_u16 v191, v191\n"
    "v_or_b32_e32 v187, v187, v203\n"
    "v_mfma_f32_16x16x32_bf16 v[132:135], a[244:247], v[192:195], v[132:135]\n"
    "v_exp_f16_e32 v234, v232\n"
    "v_mfma_f32_16x16x32_bf16 v[120:123], a[24:27], v[196:199], v[120:123]\n"
    "s_mov_b32 exec_hi, 0\n"
    "buffer_load_dword v233, v254, s[80:83], s84 offen sc1\n"
    "s_mov_b32 exec_hi, -1\n"
    "v_mfma_f32_16x16x32_bf16 v[124:127], a[28:31], v[196:199], v[124:127]\n"
    "s_nop 0\n"
    "ds_read_u16_d16_hi v207, v207\n"
    "ds_read_b128 a[240:243], v244 offset:34816\n"
    "v_mfma_f32_16x16x32_bf16 v[136:139], v[236:239], v[196:199], v[136:139]\n"
    "ds_read_u16_d16_hi v200, v200\n"
    "v_exp_f16_sdwa v234, v232 dst_sel:WORD_1 dst_unused:UNUSED_PRESERVE src0_sel:WORD_1\n"
    "v_mfma_f32_16x16x32_bf16 v[140:143], a[244:247], v[196:199], v[140:143]\n"
    "s_waitcnt lgkmcnt(8)\n"
    "v_or_b32_e32 v189, v189, v205\n"
    "v_pk_fma_f16 v234, v234, s49, s49\n"
    "v_lshlrev_b32_sdwa v192, 1, v168 dst_sel:DWORD dst_unused:UNUSED_PAD src0_sel:DWORD src1_sel:WORD_1\n"
    "s_waitcnt lgkmcnt(7)\n"
    "v_smfmac_f32_16x16x64_bf16 v[0:3], v[216:219], v[208:215], v240\n"
    "s_waitcnt lgkmcnt(6)\n"
    "ds_read_b128 v[216:219], v251 offset:672\n"
    "ds_read_u16 v192, v192\n"
    "v_smfmac_f32_16x16x64_bf16 v[4:7], v[220:223], v[208:215], v240 abid:1\n"
    "v_lshlrev_b32_sdwa v193, 1, v170 dst_sel:DWORD dst_unused:UNUSED_PAD src0_sel:DWORD src1_sel:WORD_1\n"
    "v_lshlrev_b32_sdwa v201, 1, v171 dst_sel:DWORD dst_unused:UNUSED_PAD src0_sel:DWORD src1_sel:WORD_1\n"
    "v_mfma_f32_16x16x32_bf16 v[168:171], a[112:115], v[144:147], 0\n"
    "ds_read_b128 v[208:211], v250 offset:80\n"
    "ds_read_b128 v[212:215], v250 offset:400\n"
    "v_lshlrev_b32_sdwa v194, 1, v172 dst_sel:DWORD dst_unused:UNUSED_PAD src0_sel:DWORD src1_sel:WORD_1\n"
    "v_lshlrev_b32_sdwa v202, 1, v173 dst_sel:DWORD dst_unused:UNUSED_PAD src0_sel:DWORD src1_sel:WORD_1\n"
    "v_lshlrev_b32_sdwa v195, 1, v174 dst_sel:DWORD dst_unused:UNUSED_PAD src0_sel:DWORD src1_sel:WORD_1\n"
    "v_pk_mov_b32 v[224:225], v[0:1], v[0:1] op_sel:[0,1]\n"
    "v_pk_mov_b32 v[226:227], v[2:3], v[2:3] op_sel:[0,1]\n"
    "v_pk_mul_f32 v[228:229], v[0:1], v[0:1]\n"
    "v_pk_mul_f32 v[230:231], v[2:3], v[2:3]\n"
    "v_pk_add_f32 v[224:225], v[224:225], v[4:5]\n"
    "v_pk_add_f32 v[226:227], v[226:227], v[6:7]\n"
    "v_pk_fma_f32 v[228:229], v[4:5], v[4:5], v[228:229]\n"
    "v_pk_fma_f32 v[230:231], v[6:7], v[6:7], v[230:231]\n"
    "v_mfma_f32_16x16x32_bf16 v[168:171], a[160:163], v[148:151], v[168:171]\n"
    "s_waitcnt lgkmcnt(8)\n"
    "buffer_load_dwordx4 v[76:79], v246, s[40:43], s63 offen offset:128\n"
    "ds_read_b128 v[220:223], v251 offset:1008\n"
    "v_lshlrev_b32_sdwa v203, 1, v175 dst_sel:DWORD dst_unused:UNUSED_PAD src0_sel:DWORD src1_sel:WORD_1\n"
    "v_rcp_f16_e32 v232, v234\n"
    "v_mfma_f32_16x16x32_bf16 v[168:171], a[72:75], v[152:155], v[168:171]\n"
    "ds_read_u16 v193, v193\n"
    "ds_read_u16_d16_hi v201, v201\n"
    "v_or_b32_e32 v190, v190, v206\n"
    "v_rcp_f16_sdwa v232, v234 dst_sel:WORD_1 dst_unused:UNUSED_PRESERVE src0_sel:WORD_1\n"
    "v_mfma_f32_16x16x32_bf16 v[172:175], a[116:119], v[144:147], 0\n"
    "s_waitcnt lgkmcnt(8)\n"
    "ds_read_u16 v194, v194\n"
    "ds_read_b128 v[236:239], v244 offset:8192\n"
    "v_lshlrev_b32_sdwa v196, 1, v176 dst_sel:DWORD dst_unused:UNUSED_PAD src0_sel:DWORD src1_sel:WORD_1\n"
    "v_lshlrev_b32_sdwa v204, 1, v177 dst_sel:DWORD dst_unused:UNUSED_PAD src0_sel:DWORD src1_sel:WORD_1\n"
    "v_lshlrev_b32_sdwa v197, 1, v178 dst_sel:DWORD dst_unused:UNUSED_PAD src0_sel:DWORD src1_sel:WORD_1\n"
    "v_lshlrev_b32_sdwa v205, 1, v179 dst_sel:DWORD dst_unused:UNUSED_PAD src0_sel:DWORD src1_sel:WORD_1\n"
    "v_lshlrev_b32_sdwa v198, 1, v180 dst_sel:DWORD dst_unused:UNUSED_PAD src0_sel:DWORD src1_sel:WORD_1\n"
    "v_lshlrev_b32_sdwa v206, 1, v181 dst_sel:DWORD dst_unused:UNUSED_PAD src0_sel:DWORD src1_sel:WORD_1\n"
    "v_lshlrev_b32_sdwa v199, 1, v182 dst_sel:DWORD dst_unused:UNUSED_PAD src0_sel:DWORD src1_sel:WORD_1\n"
    "v_fma_mix_f32 v234, v184, v232, 0 op_sel_hi:[1,1,0]\n"
    "v_mfma_f32_16x16x32_bf16 v[172:175], a[164:167], v[148:151], v[172:175]\n"
    "ds_read_u16_d16_hi v202, v202\n"
    "ds_read_u16 v195, v195\n"
    "v_mfma_f32_16x16x32_bf16 v[172:175], a[240:243], v[152:155], v[172:175]\n"
    "s_waitcnt vmcnt(6)\n"
    "v_or_b32_e64 v191, v191, v207\n"
    "v_lshlrev_b32_sdwa v207, 1, v183 dst_sel:DWORD dst_unused:UNUSED_PAD src0_sel:DWORD src1_sel:WORD_1\n"
    "v_smfmac_f32_32x32x32_bf16 v[112:127], a[248:251], a[232:239], v241\n"
    "s_waitcnt vmcnt(5) lgkmcnt(7)\n"
    "ds_read_b128 a[232:235], v243 offset:24640\n"
    "v_fma_mix_f32 v232, v184, v232, 0 op_sel:[1,1,0] op_sel_hi:[1,1,0]\n"
    "v_or_b32_e32 v192, v192, v200\n"
    "v_cvt_pk_bf16_f32 v184, v234, v232\n"
    "v_pk_fma_f16 v232, v188, v188, s48\n"
    "v_smfmac_f32_16x16x64_bf16 v[8:11], v[216:219], v[208:215], v240\n"
    "s_waitcnt lgkmcnt(7)\n"
    "ds_read_b128 v[216:219], v252 offset:672\n"
    "ds_read_b128 a[236:239], v243 offset:25664\n"
    "v_smfmac_f32_16x16x64_bf16 v[12:15], v[220:223], v[208:215], v240 abid:1\n"
    "ds_read_b128 v[208:211], v250 offset:720\n"
    "ds_read_b128 v[212:215], v250 offset:1040\n"
    "v_smfmac_f32_32x32x32_bf16 v[96:111], a[248:251], a[224:231], v241\n"
    "ds_read_b128 v[220:223], v252 offset:1008\n"
    "buffer_load_dwordx4 v[80:83], v246, s[40:43], s52 offen offset:128\n"
    "v_pk_mul_f16 v232, v188, v232\n"
    "v_exp_f16_e32 v234, v232\n"
    "v_mfma_f32_16x16x32_bf16 v[176:179], a[112:115], v[156:159], 0\n"
    "s_waitcnt lgkmcnt(10)\n"
    "ds_read_b128 a[244:247], v244 offset:10240\n"
    "ds_read_u16_d16_hi v203, v203\n"
    "v_mfma_f32_16x16x32_bf16 v[176:179], a[160:163], v[160:163], v[176:179]\n"
    "v_exp_f16_sdwa v234, v232 dst_sel:WORD_1 dst_unused:UNUSED_PRESERVE src0_sel:WORD_1\n"
    "v_mfma_f32_16x16x32_bf16 v[176:179], a[72:75], v[164:167], v[176:179]\n"
    "ds_read_u16 v196, v196\n"
    "v_pk_fma_f16 v234, v234, s49, s49\n"
    "v_mfma_f32_16x16x32_bf16 v[180:183], a[116:119], v[156:159], 0\n"
    "v_rcp_f16_e32 v232, v234\n"
    "v_mfma_f32_16x16x32_bf16 v[180:183], a[164:167], v[160:163], v[180:183]\n"
    "s_waitcnt lgkmcnt(10)\n"
    "ds_read_u16_d16_hi v204, v204\n"
    "v_rcp_f16_sdwa v232, v234 dst_sel:WORD_1 dst_unused:UNUSED_PRESERVE src0_sel:WORD_1\n"
    "v_mfma_f32_16x16x32_bf16 v[180:183], a[240:243], v[164:167], v[180:183]\n"
    "v_fma_mix_f32 v234, v188, v232, 0 op_sel_hi:[1,1,0]\n"
    "v_fma_mix_f32 v232, v188, v232, 0 op_sel:[1,1,0] op_sel_hi:[1,1,0]\n"
    "v_mfma_f32_16x16x32_bf16 v[112:115], a[40:43], v[184:187], v[112:115]\n"
    "ds_read_u16 v197, v197\n"
    "v_cvt_pk_bf16_f32 v188, v234, v232\n"
    "v_mfma_f32_16x16x32_bf16 v[116:119], a[44:47], v[184:187], v[116:119]\n"
    "s_waitcnt lgkmcnt(8)\n"
    "ds_read_u16_d16_hi v205, v205\n"
    "v_or_b32_e32 v193, v193, v201\n"
    "v_smfmac_f32_32x32x32_bf16 v[96:111], a[248:251], a[232:239], v241\n"
    "buffer_load_dwordx4 a[224:227], v245, s[40:43], s57 offen offset:128\n"
    "v_lshlrev_b32_sdwa v201, 1, v171 dst_sel:DWORD dst_unused:UNUSED_PAD src0_sel:DWORD src1_sel:WORD_1\n"
    "v_or_b32_e64 v194, v194, v202\n"
    "v_lshlrev_b32_sdwa v202, 1, v173 dst_sel:DWORD dst_unused:UNUSED_PAD src0_sel:DWORD src1_sel:WORD_1\n"
    "ds_read_u16 v198, v198\n"
    "v_mfma_f32_16x16x32_bf16 v[128:131], v[236:239], v[184:187], v[128:131]\n"
    "s_waitcnt lgkmcnt(7)\n"
    "ds_read_u16_d16_hi v206, v206\n"
    "v_smfmac_f32_16x16x64_bf16 v[8:11], v[216:219], v[208:215], v240\n"
    "s_waitcnt lgkmcnt(7)\n"
    "v_smfmac_f32_16x16x64_bf16 v[12:15], v[220:223], v[208:215], v240 abid:1\n"
    "ds_read_b128 v[208:211], v250 offset:160\n"
    "v_mfma_f32_16x16x32_bf16 v[96:99], a[32:35], v[184:187], v[96:99]\n"
    "ds_read_b128 v[212:215], v250 offset:480\n"
    "v_mfma_f32_16x16x32_bf16 v[100:103], a[36:39], v[184:187], v[100:103]\n"
    "s_waitcnt lgkmcnt(8)\n"
    "ds_read_b128 v[216:219], v251 offset:1344\n"
    "v_or_b32_e32 v195, v195, v203\n"
    "v_lshlrev_b32_sdwa v203, 1, v175 dst_sel:DWORD dst_unused:UNUSED_PAD src0_sel:DWORD src1_sel:WORD_1\n"
    "v_mfma_f32_16x16x32_bf16 v[132:135], a[244:247], v[184:187], v[132:135]\n"
    "ds_read_b128 v[220:223], v251 offset:1680\n"
    "v_cvt_pk_f16_f32 v184, v168, v169\n"
    "v_lshlrev_b32_sdwa v185, 1, v170 dst_sel:DWORD dst_unused:UNUSED_PAD src0_sel:DWORD src1_sel:WORD_1\n"
    "v_lshlrev_b32_sdwa v186, 1, v172 dst_sel:DWORD dst_unused:UNUSED_PAD src0_sel:DWORD src1_sel:WORD_1\n"
    "v_lshlrev_b32_sdwa v187, 1, v174 dst_sel:DWORD dst_unused:UNUSED_PAD src0_sel:DWORD src1_sel:WORD_1\n"
    "v_pk_add_f32 v[224:225], v[224:225], v[8:9]\n"
    "v_pk_add_f32 v[226:227], v[226:227], v[10:11]\n"
    "v_pk_fma_f32 v[228:229], v[8:9], v[8:9], v[228:229]\n"
    "v_pk_fma_f32 v[230:231], v[10:11], v[10:11], v[230:231]\n"
    "v_pk_add_f32 v[224:225], v[224:225], v[12:13]\n"
    "v_pk_add_f32 v[226:227], v[226:227], v[14:15]\n"
    "v_pk_fma_f32 v[228:229], v[12:13], v[12:13], v[228:229]\n"
    "v_pk_fma_f32 v[230:231], v[14:15], v[14:15], v[230:231]\n"
    "v_mfma_f32_16x16x32_bf16 v[104:107], a[32:35], v[188:191], v[104:107]\n"
    "s_waitcnt lgkmcnt(7)\n"
    "buffer_load_dwordx4 a[228:231], v245, s[40:43], s88 offen offset:128\n"
    "ds_read_u16 v199, v199\n"
    "v_mfma_f32_16x16x32_bf16 v[108:111], a[36:39], v[188:191], v[108:111]\n"
    "ds_read_u16_d16_hi v207, v207\n"
    "ds_read_b128 a[232:235], v243 offset:26688\n"
    "v_mfma_f32_16x16x32_bf16 v[120:123], a[40:43], v[188:191], v[120:123]\n"
    "s_waitcnt lgkmcnt(9)\n"
    "ds_read_b128 a[236:239], v243 offset:27712\n"
    "ds_read_b128 a[240:243], v244 offset:38912\n"
    "v_mfma_f32_16x16x32_bf16 v[124:127], a[44:47], v[188:191], v[124:127]\n"
    "v_or_b32_e64 v196, v196, v204\n"
    "v_or_b32_e64 v197, v197, v205\n"
    "v_mfma_f32_16x16x32_bf16 v[136:139], v[236:239], v[188:191], v[136:139]\n"
    "ds_read_u16 v185, v185\n"
    "v_lshlrev_b32_sdwa v205, 1, v179 dst_sel:DWORD dst_unused:UNUSED_PAD src0_sel:DWORD src1_sel:WORD_1\n"
    "v_mfma_f32_16x16x32_bf16 v[140:143], a[244:247], v[188:191], v[140:143]\n"
    "s_waitcnt lgkmcnt(9)\n"
    "ds_read_u16_d16_hi v201, v201\n"
    "v_cvt_pk_f16_f32 v188, v176, v177\n"
    "v_mfma_f32_16x16x32_bf16 v[168:171], a[120:123], v[144:147], 0\n"
    "v_lshlrev_b32_sdwa v189, 1, v178 dst_sel:DWORD dst_unused:UNUSED_PAD src0_sel:DWORD src1_sel:WORD_1\n"
    "v_lshlrev_b32_sdwa v190, 1, v180 dst_sel:DWORD dst_unused:UNUSED_PAD src0_sel:DWORD src1_sel:WORD_1\n"
    "v_mfma_f32_16x16x32_bf16 v[168:171], a[168:171], v[148:151], v[168:171]\n"
    "s_waitcnt lgkmcnt(8)\n"
    "ds_read_u16 v186, v186\n"
    "v_lshlrev_b32_sdwa v191, 1, v182 dst_sel:DWORD dst_unused:UNUSED_PAD src0_sel:DWORD src1_sel:WORD_1\n"
    "v_smfmac_f32_16x16x64_bf16 v[16:19], v[216:219], v[208:215], v240\n"
    "s_waitcnt lgkmcnt(8)\n"
    "v_or_b32_e32 v198, v198, v206\n"
    "v_smfmac_f32_16x16x64_bf16 v[20:23], v[220:223], v[208:215], v240 abid:1\n"
    "ds_read_b128 v[208:211], v250 offset:800\n"
    "ds_read_b128 v[212:215], v250 offset:1120\n"
    "v_mfma_f32_16x16x32_bf16 v[168:171], a[76:79], v[152:155], v[168:171]\n"
    "buffer_load_dwordx4 v[84:87], v247, s[40:43], s60 offen offset:128\n"
    "ds_read_b128 v[216:219], v252 offset:1344\n"
    "v_mfma_f32_16x16x32_bf16 v[172:175], a[124:127], v[144:147], 0\n"
    "v_lshlrev_b32_sdwa v206, 1, v181 dst_sel:DWORD dst_unused:UNUSED_PAD src0_sel:DWORD src1_sel:WORD_1\n"
    "v_pk_fma_f16 v232, v184, v184, s48\n"
    "v_mfma_f32_16x16x32_bf16 v[172:175], a[172:175], v[148:151], v[172:175]\n"
    "ds_read_b128 v[220:223], v252 offset:1680\n"
    "ds_read_u16_d16_hi v202, v202\n"
    "v_mfma_f32_16x16x32_bf16 v[176:179], a[120:123], v[156:159], 0\n"
    "s_waitcnt lgkmcnt(9)\n"
    "v_or_b32_e32 v199, v199, v207\n"
    "v_lshlrev_b32_sdwa v207, 1, v183 dst_sel:DWORD dst_unused:UNUSED_PAD src0_sel:DWORD src1_sel:WORD_1\n"
    "v_smfmac_f32_32x32x32_bf16 v[112:127], a[248:251], a[232:239], v241\n"
    "s_waitcnt lgkmcnt(8)\n"
    "ds_read_b128 v[236:239], v244 offset:12288\n"
    "v_pk_mul_f16 v232, v184, v232\n"
    "v_exp_f16_e32 v234, v232\n"
    "ds_read_u16 v187, v187\n"
    "v_mfma_f32_16x16x32_bf16 v[172:175], a[240:243], v[152:155], v[172:175]\n"
    "v_exp_f16_sdwa v234, v232 dst_sel:WORD_1 dst_unused:UNUSED_PRESERVE src0_sel:WORD_1\n"
    "v_mfma_f32_16x16x32_bf16 v[176:179], a[168:171], v[160:163], v[176:179]\n"
    "ds_read_u16_d16_hi v203, v203\n"
    "v_pk_fma_f16 v234, v234, s49, s49\n"
    "v_mfma_f32_16x16x32_bf16 v[176:179], a[76:79], v[164:167], v[176:179]\n"
    "ds_read_b128 a[244:247], v244 offset:14336\n"
    "v_rcp_f16_e32 v232, v234\n"
    "v_mfma_f32_16x16x32_bf16 v[180:183], a[124:127], v[156:159], 0\n"
    "s_waitcnt lgkmcnt(10)\n"
    "ds_read_u16 v189, v189\n"
    "v_lshlrev_b32_e64 v254, 3, v244\n"
    "v_mfma_f32_16x16x32_bf16 v[180:183], a[172:175], v[160:163], v[180:183]\n"
    "v_or_b32_e64 v185, v185, v201\n"
    "v_lshlrev_b32_sdwa v201, 1, v171 dst_sel:DWORD dst_unused:UNUSED_PAD src0_sel:DWORD src1_sel:WORD_1\n"
    "v_mfma_f32_16x16x32_bf16 v[180:183], a[240:243], v[164:167], v[180:183]\n"
    "ds_read_b128 a[232:235], v243 offset:28736\n"
    "v_rcp_f16_sdwa v232, v234 dst_sel:WORD_1 dst_unused:UNUSED_PRESERVE src0_sel:WORD_1\n"
    "v_mfma_f32_16x16x32_bf16 v[96:99], a[176:179], v[192:195], v[96:99]\n"
    "s_waitcnt lgkmcnt(8)\n"
    "ds_read_u16_d16_hi v205, v205\n"
    "v_fma_mix_f32 v234, v184, v232, 0 op_sel_hi:[1,1,0]\n"
    "v_smfmac_f32_16x16x64_bf16 v[16:19], v[216:219], v[208:215], v240\n"
    "s_waitcnt lgkmcnt(8)\n"
    "ds_read_b128 v[216:219], v251 offset:2016\n"
    "v_smfmac_f32_16x16x64_bf16 v[20:23], v[220:223], v[208:215], v240 abid:1\n"
    "ds_read_b128 v[208:211], v250 offset:240\n"
    "buffer_load_dwordx4 v[88:91], v247, s[40:43], s61 offen offset:128\n"
    "v_mfma_f32_16x16x32_bf16 v[100:103], a[180:183], v[192:195], v[100:103]\n"
    "s_waitcnt lgkmcnt(8)\n"
    "ds_read_b128 v[212:215], v250 offset:560\n"
    "v_or_b32_e32 v186, v186, v202\n"
    "v_lshlrev_b32_sdwa v202, 1, v173 dst_sel:DWORD dst_unused:UNUSED_PAD src0_sel:DWORD src1_sel:WORD_1\n"
    "ds_read_b128 v[220:223], v251 offset:2352\n"
    "v_pk_add_f32 v[224:225], v[224:225], v[16:17]\n"
    "v_pk_add_f32 v[226:227], v[226:227], v[18:19]\n"
    "v_pk_fma_f32 v[228:229], v[16:17], v[16:17], v[228:229]\n"
    "v_pk_fma_f32 v[230:231], v[18:19], v[18:19], v[230:231]\n"
    "v_pk_add_f32 v[224:225], v[224:225], v[20:21]\n"
    "v_pk_add_f32 v[226:227], v[226:227], v[22:23]\n"
    "v_pk_fma_f32 v[228:229], v[20:21], v[20:21], v[228:229]\n"
    "v_pk_fma_f32 v[230:231], v[22:23], v[22:23], v[230:231]\n"
    "v_mfma_f32_16x16x32_bf16 v[112:115], a[184:187], v[192:195], v[112:115]\n"
    "ds_read_b128 a[236:239], v243 offset:29760\n"
    "ds_read_u16 v190, v190\n"
    "v_mfma_f32_16x16x32_bf16 v[116:119], a[188:191], v[192:195], v[116:119]\n"
    "s_waitcnt lgkmcnt(9)\n"
    "ds_read_b128 v[144:147], v242 offset:0\n"
    "v_or_b32_e32 v187, v187, v203\n"
    "v_mfma_f32_16x16x32_bf16 v[128:131], v[236:239], v[192:195], v[128:131]\n"
    "ds_read_u16_d16_hi v206, v206\n"
    "v_lshlrev_b32_sdwa v203, 1, v175 dst_sel:DWORD dst_unused:UNUSED_PAD src0_sel:DWORD src1_sel:WORD_1\n"
    "v_mfma_f32_16x16x32_bf16 v[132:135], a[244:247], v[192:195], v[132:135]\n"
    "v_cvt_pk_f16_f32 v192, v168, v169\n"
    "v_lshlrev_b32_sdwa v193, 1, v170 dst_sel:DWORD dst_unused:UNUSED_PAD src0_sel:DWORD src1_sel:WORD_1\n"
    "v_mfma_f32_16x16x32_bf16 v[104:107], a[176:179], v[196:199], v[104:107]\n"
    "ds_read_b128 v[148:151], v242 offset:1024\n"
    "ds_read_u16 v191, v191\n"
    "v_mfma_f32_16x16x32_bf16 v[108:111], a[180:183], v[196:199], v[108:111]\n"
    "s_mov_b32 exec_hi, 0\n"
    "buffer_load_dword v233, v254, s[72:75], s53 offen sc1\n"
    "s_mov_b32 exec_hi, -1\n"
    "v_mfma_f32_16x16x32_bf16 v[120:123], a[184:187], v[196:199], v[120:123]\n"
    "s_waitcnt lgkmcnt(9)\n"
    "ds_read_b128 v[152:155], v242 offset:2048\n"
    "v_or_b32_e32 v189, v189, v205\n"
    "v_mfma_f32_16x16x32_bf16 v[124:127], a[188:191], v[196:199], v[124:127]\n"
    "ds_read_u16_d16_hi v207, v207\n"
    "v_lshlrev_b32_sdwa v194, 1, v172 dst_sel:DWORD dst_unused:UNUSED_PAD src0_sel:DWORD src1_sel:WORD_1\n"
    "v_mfma_f32_16x16x32_bf16 v[136:139], v[236:239], v[196:199], v[136:139]\n"
    "s_waitcnt lgkmcnt(9)\n"
    "v_lshlrev_b32_sdwa v195, 1, v174 dst_sel:DWORD dst_unused:UNUSED_PAD src0_sel:DWORD src1_sel:WORD_1\n"
    "v_lshlrev_b32_sdwa v205, 1, v179 dst_sel:DWORD dst_unused:UNUSED_PAD src0_sel:DWORD src1_sel:WORD_1\n"
    "v_smfmac_f32_16x16x64_bf16 v[24:27], v[216:219], v[208:215], v240\n"
    "s_waitcnt lgkmcnt(8)\n"
    "ds_read_b128 v[216:219], v252 offset:2016\n"
    "v_smfmac_f32_16x16x64_bf16 v[28:31], v[220:223], v[208:215], v240 abid:1\n"
    "s_nop 0\n"
    "ds_read_b128 v[208:211], v250 offset:880\n"
    "v_mfma_f32_16x16x32_bf16 v[140:143], a[244:247], v[196:199], v[140:143]\n"
    "s_waitcnt lgkmcnt(9)\n"
    "ds_read_b128 v[212:215], v250 offset:1200\n"
    "v_cvt_pk_f16_f32 v196, v176, v177\n"
    "v_lshlrev_b32_sdwa v197, 1, v178 dst_sel:DWORD dst_unused:UNUSED_PAD src0_sel:DWORD src1_sel:WORD_1\n"
    "ds_read_b128 v[220:223], v252 offset:2352\n"
    "s_waitcnt lgkmcnt(9)\n"
    "v_mfma_f32_16x16x32_bf16 v[168:171], a[80:83], v[144:147], 0\n"
    "s_nop 0\n"
    "ds_read_b128 v[156:159], v242 offset:3072\n"
    "v_lshlrev_b32_sdwa v198, 1, v180 dst_sel:DWORD dst_unused:UNUSED_PAD src0_sel:DWORD src1_sel:WORD_1\n"
    "v_smfmac_f32_32x32x32_bf16 v[128:143], a[248:251], a[232:239], v241\n"
    "ds_read_b128 v[160:163], v242 offset:4096\n"
    "ds_read_b128 v[164:167], v242 offset:5120\n"
    "v_lshlrev_b32_sdwa v199, 1, v182 dst_sel:DWORD dst_unused:UNUSED_PAD src0_sel:DWORD src1_sel:WORD_1\n"
    "v_fma_mix_f32 v232, v184, v232, 0 op_sel:[1,1,0] op_sel_hi:[1,1,0]\n"
    "v_mov_b32_e32 v254, 0\n"
    "v_mfma_f32_16x16x32_bf16 v[172:175], a[84:87], v[144:147], 0\n"
    "s_waitcnt lgkmcnt(10)\n"
    "ds_read_b128 a[240:243], v244 offset:32768\n"
    "v_or_b32_e32 v190, v190, v206\n"
    "v_mfma_f32_16x16x32_bf16 v[168:171], a[128:131], v[148:151], v[168:171]\n"
    "ds_read_u16 v193, v193\n"
    "v_lshlrev_b32_sdwa v206, 1, v181 dst_sel:DWORD dst_unused:UNUSED_PAD src0_sel:DWORD src1_sel:WORD_1\n"
    "v_mfma_f32_16x16x32_bf16 v[172:175], a[132:135], v[148:151], v[172:175]\n"
    "s_waitcnt lgkmcnt(10)\n"
    "v_cvt_pk_bf16_f32 v184, v234, v232\n"
    "v_pk_fma_f16 v232, v188, v188, s48\n"
    "v_mfma_f32_16x16x32_bf16 v[168:171], a[48:51], v[152:155], v[168:171]\n"
    "ds_read_u16_d16_hi v201, v201\n"
    "v_pk_mul_f16 v232, v188, v232\n"
    "v_exp_f16_e64 v234, v232\n"
    "ds_read_u16 v194, v194\n"
    "s_waitcnt lgkmcnt(10)\n"
    "v_or_b32_e32 v191, v191, v207\n"
    "v_lshlrev_b32_sdwa v207, 1, v183 dst_sel:DWORD dst_unused:UNUSED_PAD src0_sel:DWORD src1_sel:WORD_1\n"
    "v_exp_f16_sdwa v234, v232 dst_sel:WORD_1 dst_unused:UNUSED_PRESERVE src0_sel:WORD_1\n"
    "v_lshlrev_b32_sdwa v200, 1, v169 dst_sel:DWORD dst_unused:UNUSED_PAD src0_sel:DWORD src1_sel:WORD_1\n"
    "v_pk_fma_f16 v234, v234, s49, s49\n"
    "v_mfma_f32_16x16x32_bf16 v[96:99], a[192:195], v[184:187], v[96:99]\n"
    "s_waitcnt lgkmcnt(8)\n"
    "s_barrier\n"
    "v_smfmac_f32_16x16x64_bf16 v[24:27], v[216:219], v[208:215], v240\n"
    "s_waitcnt lgkmcnt(4)\n"
    "ds_read_u16_d16_hi v202, v202\n"
    "ds_write2_b32 v248, v32, v33 offset0:0 offset1:20\n"
    "v_smfmac_f32_16x16x64_bf16 v[28:31], v[220:223], v[208:215], v240 abid:1\n"
    "s_waitcnt lgkmcnt(5)\n"
    "v_rcp_f16_e64 v232, v234\n"
    "v_mfma_f32_16x16x32_bf16 v[172:175], a[240:243], v[152:155], v[172:175]\n"
    "ds_read_b128 v[236:239], v244 offset:16384\n"
    "ds_write2_b32 v248, v34, v35 offset0:40 offset1:60\n"
    "v_add_u32_e32 v248, 1360, v248\n"
    "v_pk_add_f32 v[224:225], v[224:225], v[24:25]\n"
    "v_pk_add_f32 v[226:227], v[226:227], v[26:27]\n"
    "v_pk_fma_f32 v[228:229], v[24:25], v[24:25], v[228:229]\n"
    "v_pk_fma_f32 v[230:231], v[26:27], v[26:27], v[230:231]\n"
    "v_pk_add_f32 v[224:225], v[224:225], v[28:29]\n"
    "v_pk_add_f32 v[226:227], v[226:227], v[30:31]\n"
    "v_pk_fma_f32 v[228:229], v[28:29], v[28:29], v[228:229]\n"
    "v_pk_fma_f32 v[230:231], v[30:31], v[30:31], v[230:231]\n"
    "v_mfma_f32_16x16x32_bf16 v[176:179], a[80:83], v[156:159], 0\n"
    "s_waitcnt lgkmcnt(5)\n"
    "ds_read_u16 v195, v195\n"
    "ds_write2_b32 v235, v52, v53 offset0:0 offset1:20\n"
    "v_mfma_f32_16x16x32_bf16 v[176:179], a[128:131], v[160:163], v[176:179]\n"
    "v_rcp_f16_sdwa v232, v234 dst_sel:WORD_1 dst_unused:UNUSED_PRESERVE src0_sel:WORD_1\n"
    "v_mfma_f32_16x16x32_bf16 v[176:179], a[48:51], v[164:167], v[176:179]\n"
    "ds_read_u16_d16_hi v203, v203\n"
    "ds_write2_b32 v235, v54, v55 offset0:40 offset1:60\n"
    "v_mfma_f32_16x16x32_bf16 v[180:183], a[84:87], v[156:159], 0\n"
    "s_waitcnt lgkmcnt(6)\n"
    "v_add_u32_e32 v235, 1360, v235\n"
    "v_or_b32_e64 v193, v193, v201\n"
    "v_mfma_f32_16x16x32_bf16 v[180:183], a[132:135], v[160:163], v[180:183]\n"
    "ds_read_b128 a[244:247], v244 offset:18432\n"
    "ds_write2_b32 v248, v36, v37 offset0:0 offset1:20\n"
    "v_mfma_f32_16x16x32_bf16 v[180:183], a[240:243], v[164:167], v[180:183]\n"
    "v_fma_mix_f32 v234, v188, v232, 0 op_sel_hi:[1,1,0]\n"
    "v_fma_mix_f32 v232, v188, v232, 0 op_sel:[1,1,0] op_sel_hi:[1,1,0]\n"
    "v_mfma_f32_16x16x32_bf16 v[100:103], a[196:199], v[184:187], v[100:103]\n"
    "s_waitcnt lgkmcnt(6)\n"
    "ds_read_u16 v197, v197\n"
    "ds_write2_b32 v248, v38, v39 offset0:40 offset1:60\n"
    "v_mfma_f32_16x16x32_bf16 v[112:115], a[200:203], v[184:187], v[112:115]\n"
    "s_nop 0\n"
    "v_add_u32_e32 v248, 1360, v248\n"
    "v_or_b32_e64 v194, v194, v202\n"
    "v_mfma_f32_16x16x32_bf16 v[116:119], a[204:207], v[184:187], v[116:119]\n"
    "ds_read_u16_d16_hi v205, v205\n"
    "ds_write2_b32 v235, v56, v57 offset0:0 offset1:20\n"
    "v_mfma_f32_16x16x32_bf16 v[128:131], v[236:239], v[184:187], v[128:131]\n"
    "v_cvt_pk_bf16_f32 v188, v234, v232\n"
    "v_pk_fma_f16 v232, v192, v192, s48\n"
    "v_lshlrev_b32_sdwa v201, 1, v171 dst_sel:DWORD dst_unused:UNUSED_PAD src0_sel:DWORD src1_sel:WORD_1\n"
    "v_mfma_f32_16x16x32_bf16 v[104:107], a[192:195], v[188:191], v[104:107]\n"
    "s_waitcnt lgkmcnt(6)\n"
    "ds_read_u16 v198, v198\n"
    "ds_write2_b32 v235, v58, v59 offset0:40 offset1:60\n"
    "v_mfma_f32_16x16x32_bf16 v[108:111], a[196:199], v[188:191], v[108:111]\n"
    "s_waitcnt lgkmcnt(6)\n"
    "v_add_u32_e32 v235, -1360, v235\n"
    "v_or_b32_e64 v195, v195, v203\n"
    "v_mfma_f32_16x16x32_bf16 v[132:135], a[244:247], v[184:187], v[132:135]\n"
    "ds_read_u16_d16_hi v206, v206\n"
    "ds_write2_b32 v248, v40, v41 offset0:0 offset1:20\n"
    "v_mfma_f32_16x16x32_bf16 v[120:123], a[200:203], v[188:191], v[120:123]\n"
    "v_pk_mul_f16 v232, v192, v232\n"
    "v_lshlrev_b32_sdwa v184, 1, v168 dst_sel:DWORD dst_unused:UNUSED_PAD src0_sel:DWORD src1_sel:WORD_1\n"
    "v_mfma_f32_16x16x32_bf16 v[124:127], a[204:207], v[188:191], v[124:127]\n"
    "ds_read_u16 v199, v199\n"
    "ds_write2_b32 v248, v42, v43 offset0:40 offset1:60\n"
    "v_mfma_f32_16x16x32_bf16 v[136:139], v[236:239], v[188:191], v[136:139]\n"
    "v_add_u32_e32 v248, 1360, v248\n"
    "v_exp_f16_e32 v234, v232\n"
    "v_mfma_f32_16x16x32_bf16 v[140:143], a[244:247], v[188:191], v[140:143]\n"
    "s_waitcnt lgkmcnt(6)\n"
    "ds_read_u16_d16_hi v207, v207\n"
    "ds_write2_b32 v248, v44, v45 offset0:0 offset1:20\n"
    "v_or_b32_e64 v197, v197, v205\n"
    "v_exp_f16_sdwa v234, v232 dst_sel:WORD_1 dst_unused:UNUSED_PRESERVE src0_sel:WORD_1\n"
    "v_lshlrev_b32_sdwa v185, 1, v170 dst_sel:DWORD dst_unused:UNUSED_PAD src0_sel:DWORD src1_sel:WORD_1\n"
    "v_pk_fma_f16 v234, v234, s49, s49\n"
    "v_mfma_f32_16x16x32_bf16 v[168:171], a[88:91], v[144:147], 0\n"
    "s_add_u32 s60, s60, 1572864\n"
    "ds_read_b128 a[240:243], v244 offset:36864\n"
    "ds_write2_b32 v248, v46, v47 offset0:40 offset1:60\n"
    "v_mfma_f32_16x16x32_bf16 v[168:171], a[136:139], v[148:151], v[168:171]\n"
    "v_add_u32_e32 v248, 1360, v248\n"
    "v_lshlrev_b32_sdwa v186, 1, v172 dst_sel:DWORD dst_unused:UNUSED_PAD src0_sel:DWORD src1_sel:WORD_1\n"
    "v_lshlrev_b32_sdwa v202, 1, v173 dst_sel:DWORD dst_unused:UNUSED_PAD src0_sel:DWORD src1_sel:WORD_1\n"
    "v_lshlrev_b32_sdwa v187, 1, v174 dst_sel:DWORD dst_unused:UNUSED_PAD src0_sel:DWORD src1_sel:WORD_1\n"
    "v_lshlrev_b32_sdwa v203, 1, v175 dst_sel:DWORD dst_unused:UNUSED_PAD src0_sel:DWORD src1_sel:WORD_1\n"
    "v_rcp_f16_e32 v232, v234\n"
    "v_mfma_f32_16x16x32_bf16 v[168:171], a[52:55], v[152:155], v[168:171]\n"
    "s_waitcnt lgkmcnt(6)\n"
    "ds_read_u16 v184, v184\n"
    "ds_write2_b32 v248, v48, v49 offset0:0 offset1:20\n"
    "v_or_b32_e64 v198, v198, v206\n"
    "v_rcp_f16_sdwa v232, v234 dst_sel:WORD_1 dst_unused:UNUSED_PRESERVE src0_sel:WORD_1\n"
    "v_mfma_f32_16x16x32_bf16 v[172:175], a[92:95], v[144:147], 0\n"
    "s_add_u32 s61, s61, 1572864\n"
    "ds_read_u16_d16_hi v200, v200\n"
    "ds_write2_b32 v248, v50, v51 offset0:40 offset1:60\n"
    "v_mfma_f32_32x32x1_2b_f32 v[32:63], v254, v254, 0\n"
    "v_add_u32_e32 v248, -5440, v248\n"
    "v_lshlrev_b32_sdwa v188, 1, v176 dst_sel:DWORD dst_unused:UNUSED_PAD src0_sel:DWORD src1_sel:WORD_1\n"
    "v_lshlrev_b32_sdwa v204, 1, v177 dst_sel:DWORD dst_unused:UNUSED_PAD src0_sel:DWORD src1_sel:WORD_1\n"
    "v_lshlrev_b32_sdwa v189, 1, v178 dst_sel:DWORD dst_unused:UNUSED_PAD src0_sel:DWORD src1_sel:WORD_1\n"
    "v_lshlrev_b32_sdwa v205, 1, v179 dst_sel:DWORD dst_unused:UNUSED_PAD src0_sel:DWORD src1_sel:WORD_1\n"
    "v_lshlrev_b32_sdwa v190, 1, v180 dst_sel:DWORD dst_unused:UNUSED_PAD src0_sel:DWORD src1_sel:WORD_1\n"
    "v_lshlrev_b32_sdwa v206, 1, v181 dst_sel:DWORD dst_unused:UNUSED_PAD src0_sel:DWORD src1_sel:WORD_1\n"
    "v_lshlrev_b32_sdwa v191, 1, v182 dst_sel:DWORD dst_unused:UNUSED_PAD src0_sel:DWORD src1_sel:WORD_1\n"
    "v_fma_mix_f32 v234, v192, v232, 0 op_sel_hi:[1,1,0]\n"
    "v_fma_mix_f32 v232, v192, v232, 0 op_sel:[1,1,0] op_sel_hi:[1,1,0]\n"
    "v_mov_b32_e32 v254, 0\n"
    "v_cvt_pk_bf16_f32 v192, v234, v232\n"
    "v_pk_fma_f16 v232, v196, v196, s48\n"
    "v_pk_mul_f16 v232, v196, v232\n"
    "v_mfma_f32_16x16x32_bf16 v[172:175], a[140:143], v[148:151], v[172:175]\n"
    "s_waitcnt lgkmcnt(4)\n"
    "ds_read_u16 v185, v185\n"
    "ds_read_u16_d16_hi v201, v201\n"
    "v_mfma_f32_16x16x32_bf16 v[172:175], a[240:243], v[152:155], v[172:175]\n"
    "s_add_u32 s62, s62, 1572864\n"
    "ds_read_u16 v186, v186\n"
    "ds_read_b128 v[236:239], v244 offset:20480\n"
    "v_mfma_f32_16x16x32_bf16 v[176:179], a[88:91], v[156:159], 0\n"
    "s_add_u32 s63, s63, 1572864\n"
    "v_or_b32_e64 v199, v199, v207\n"
    "v_lshlrev_b32_sdwa v207, 1, v183 dst_sel:DWORD dst_unused:UNUSED_PAD src0_sel:DWORD src1_sel:WORD_1\n"
    "v_mfma_f32_16x16x32_bf16 v[176:179], a[136:139], v[160:163], v[176:179]\n"
    "ds_read_u16_d16_hi v202, v202\n"
    "v_exp_f16_e64 v234, v232\n"
    "v_mfma_f32_16x16x32_bf16 v[176:179], a[52:55], v[164:167], v[176:179]\n"
    "s_waitcnt lgkmcnt(5)\n"
    "ds_read_b128 v[216:219], v251 offset:10752\n"
    "v_exp_f16_sdwa v234, v232 dst_sel:WORD_1 dst_unused:UNUSED_PRESERVE src0_sel:WORD_1\n"
    "v_mfma_f32_16x16x32_bf16 v[180:183], a[92:95], v[156:159], 0\n"
    "s_barrier\n"
    "v_mfma_f32_16x16x32_bf16 v[180:183], a[140:143], v[160:163], v[180:183]\n"
    "s_add_u32 s52, s52, 1572864\n"
    "s_add_u32 s53, s53, 393216\n"
    "ds_read_b128 v[208:211], v250 offset:0\n"
    "ds_read_b128 v[212:215], v250 offset:320\n"
    "v_mfma_f32_16x16x32_bf16 v[180:183], a[240:243], v[164:167], v[180:183]\n"
    "s_waitcnt vmcnt(3)\n"
    "ds_read_b128 v[220:223], v251 offset:11088\n"
    "ds_read_u16 v187, v187\n"
    "v_smfmac_f32_32x32x32_bf16 v[128:143], a[248:251], a[224:231], v241\n"
    "s_nop 0\n"
    "ds_read_b128 a[244:247], v244 offset:22528\n"
    "ds_read_u16_d16_hi v203, v203\n"
    "v_pk_fma_f16 v234, v234, s49, s49\n"
    "v_or_b32_e64 v184, v184, v200\n"
    "v_rcp_f16_e32 v232, v234\n"
    "v_mfma_f32_16x16x32_bf16 v[96:99], a[208:211], v[192:195], v[96:99]\n"
    "s_waitcnt lgkmcnt(8)\n"
    "ds_read_u16 v188, v188\n"
    "v_rcp_f16_sdwa v232, v234 dst_sel:WORD_1 dst_unused:UNUSED_PRESERVE src0_sel:WORD_1\n"
    "v_mfma_f32_16x16x32_bf16 v[100:103], a[212:215], v[192:195], v[100:103]\n"
    "ds_read_u16_d16_hi v204, v204\n"
    "v_fma_mix_f32 v234, v196, v232, 0 op_sel_hi:[1,1,0]\n"
    "v_mfma_f32_16x16x32_bf16 v[112:115], a[216:219], v[192:195], v[112:115]\n"
    "v_fma_mix_f32 v232, v196, v232, 0 op_sel:[1,1,0] op_sel_hi:[1,1,0]\n"
    "v_or_b32_e32 v185, v185, v201\n"
    "v_mfma_f32_16x16x32_bf16 v[116:119], a[220:223], v[192:195], v[116:119]\n"
    "s_waitcnt lgkmcnt(8)\n"
    "ds_read_u16 v189, v189\n"
    "v_cvt_pk_bf16_f32 v196, v234, v232\n"
    "v_mfma_f32_16x16x32_bf16 v[128:131], v[236:239], v[192:195], v[128:131]\n"
    "v_lshlrev_b32_sdwa v201, 1, v171 dst_sel:DWORD dst_unused:UNUSED_PAD src0_sel:DWORD src1_sel:WORD_1\n"
    "v_or_b32_e32 v186, v186, v202\n"
    "v_mfma_f32_16x16x32_bf16 v[104:107], a[208:211], v[196:199], v[104:107]\n"
    "s_waitcnt lgkmcnt(7)\n"
    "ds_read_u16_d16_hi v205, v205\n"
    "v_lshlrev_b32_sdwa v202, 1, v173 dst_sel:DWORD dst_unused:UNUSED_PAD src0_sel:DWORD src1_sel:WORD_1\n"
    "v_smfmac_f32_16x16x64_bf16 v[32:35], v[216:219], v[208:215], v240\n"
    "s_waitcnt lgkmcnt(7)\n"
    "ds_read_u16 v190, v190\n"
    "v_smfmac_f32_16x16x64_bf16 v[36:39], v[220:223], v[208:215], v240 abid:1\n"
    "s_nop 0\n"
    "v_cvt_pk_bf16_f32 v96, v96, v97\n"
    "v_mfma_f32_16x16x32_bf16 v[108:111], a[212:215], v[196:199], v[108:111]\n"
    "s_nop 0\n"
    "ds_read_b128 v[208:211], v250 offset:640\n"
    "v_cvt_pk_bf16_f32 v97, v98, v99\n"
    "v_mfma_f32_16x16x32_bf16 v[120:123], a[216:219], v[196:199], v[120:123]\n"
    "s_waitcnt lgkmcnt(7)\n"
    "ds_read_b128 v[212:215], v250 offset:960\n"
    "v_cvt_pk_bf16_f32 v98, v100, v101\n"
    "v_mfma_f32_16x16x32_bf16 v[132:135], a[244:247], v[192:195], v[132:135]\n"
    "ds_read_b128 v[216:219], v252 offset:10752\n"
    "v_cvt_pk_f16_f32 v192, v168, v169\n"
    "v_mfma_f32_16x16x32_bf16 v[124:127], a[220:223], v[196:199], v[124:127]\n"
    "s_nop 0\n"
    "ds_read_b128 v[220:223], v252 offset:11088\n"
    "v_lshlrev_b32_sdwa v193, 1, v170 dst_sel:DWORD dst_unused:UNUSED_PAD src0_sel:DWORD src1_sel:WORD_1\n"
    "v_mfma_f32_16x16x32_bf16 v[136:139], v[236:239], v[196:199], v[136:139]\n"
    "s_waitcnt lgkmcnt(9)\n"
    "ds_read_u16_d16_hi v206, v206\n"
    "v_lshlrev_b32_sdwa v194, 1, v172 dst_sel:DWORD dst_unused:UNUSED_PAD src0_sel:DWORD src1_sel:WORD_1\n"
    "v_mfma_f32_16x16x32_bf16 v[140:143], a[244:247], v[196:199], v[140:143]\n"
    "v_lshlrev_b32_sdwa v195, 1, v174 dst_sel:DWORD dst_unused:UNUSED_PAD src0_sel:DWORD src1_sel:WORD_1\n"
    "v_cvt_pk_f16_f32 v196, v176, v177\n"
    "v_mfma_f32_16x16x32_bf16 v[168:171], a[96:99], v[144:147], 0\n"
    "s_nop 0\n"
    "ds_read_u16 v191, v191\n"
    "v_lshlrev_b32_sdwa v197, 1, v178 dst_sel:DWORD dst_unused:UNUSED_PAD src0_sel:DWORD src1_sel:WORD_1\n"
    "v_mfma_f32_16x16x32_bf16 v[168:171], a[144:147], v[148:151], v[168:171]\n"
    "s_waitcnt lgkmcnt(9)\n"
    "v_lshlrev_b32_sdwa v198, 1, v180 dst_sel:DWORD dst_unused:UNUSED_PAD src0_sel:DWORD src1_sel:WORD_1\n"
    "v_lshlrev_b32_sdwa v199, 1, v182 dst_sel:DWORD dst_unused:UNUSED_PAD src0_sel:DWORD src1_sel:WORD_1\n"
    "v_mfma_f32_16x16x32_bf16 v[168:171], a[56:59], v[152:155], v[168:171]\n"
    "ds_read_u16_d16_hi v207, v207\n"
    "v_or_b32_e64 v187, v187, v203\n"
    "v_lshlrev_b32_sdwa v203, 1, v175 dst_sel:DWORD dst_unused:UNUSED_PAD src0_sel:DWORD src1_sel:WORD_1\n"
    "v_mfma_f32_16x16x32_bf16 v[172:175], a[100:103], v[144:147], 0\n"
    "ds_read_u16 v193, v193\n"
    "v_or_b32_e64 v188, v188, v204\n"
    "v_mfma_f32_16x16x32_bf16 v[172:175], a[148:151], v[148:151], v[172:175]\n"
    "s_waitcnt lgkmcnt(9)\n"
    "v_or_b32_e32 v189, v189, v205\n"
    "v_lshlrev_b32_sdwa v205, 1, v179 dst_sel:DWORD dst_unused:UNUSED_PAD src0_sel:DWORD src1_sel:WORD_1\n"
    "v_mfma_f32_16x16x32_bf16 v[172:175], a[60:63], v[152:155], v[172:175]\n"
    "ds_read_u16_d16_hi v201, v201\n"
    "v_cvt_pk_bf16_f32 v99, v102, v103\n"
    "v_mfma_f32_16x16x32_bf16 v[176:179], a[96:99], v[156:159], 0\n"
    "v_cvt_pk_bf16_f32 v112, v112, v113\n"
    "v_cvt_pk_bf16_f32 v113, v114, v115\n"
    "v_mfma_f32_16x16x32_bf16 v[176:179], a[144:147], v[160:163], v[176:179]\n"
    "v_cvt_pk_bf16_f32 v114, v116, v117\n"
    "v_cvt_pk_bf16_f32 v115, v118, v119\n"
    "v_mfma_f32_16x16x32_bf16 v[176:179], a[56:59], v[164:167], v[176:179]\n"
    "s_waitcnt lgkmcnt(6)\n"
    "ds_write_b128 v243, v[96:99] offset:24640\n"
    "v_smfmac_f32_16x16x64_bf16 v[32:35], v[216:219], v[208:215], v240\n"
    "s_waitcnt lgkmcnt(6)\n"
    "v_cvt_pk_bf16_f32 v128, v128, v129\n"
    "v_smfmac_f32_16x16x64_bf16 v[36:39], v[220:223], v[208:215], v240 abid:1\n"
    "v_cvt_pk_bf16_f32 v129, v130, v131\n"
    "ds_write_b128 v243, v[112:115] offset:26688\n"
    "s_waitcnt lgkmcnt(6)\n"
    "v_or_b32_e32 v190, v190, v206\n"
    "v_mfma_f32_16x16x32_bf16 v[96:99], a[0:3], v[184:187], 0\n"
    "v_lshlrev_b32_sdwa v206, 1, v181 dst_sel:DWORD dst_unused:UNUSED_PAD src0_sel:DWORD src1_sel:WORD_1\n"
    "v_cvt_pk_bf16_f32 v130, v132, v133\n"
    "ds_read_b128 v[208:211], v250 offset:80\n"
    "ds_read_b128 v[212:215], v250 offset:400\n"
    "v_pk_add_f32 v[224:225], v[224:225], v[32:33]\n"
    "v_pk_add_f32 v[226:227], v[226:227], v[34:35]\n"
    "v_pk_fma_f32 v[228:229], v[32:33], v[32:33], v[228:229]\n"
    "v_pk_fma_f32 v[230:231], v[34:35], v[34:35], v[230:231]\n"
    "v_pk_add_f32 v[224:225], v[224:225], v[36:37]\n"
    "v_pk_add_f32 v[226:227], v[226:227], v[38:39]\n"
    "v_pk_fma_f32 v[228:229], v[36:37], v[36:37], v[228:229]\n"
    "v_pk_fma_f32 v[230:231], v[38:39], v[38:39], v[230:231]\n"
    "v_mfma_f32_16x16x32_bf16 v[100:103], a[4:7], v[184:187], 0\n"
    "s_waitcnt lgkmcnt(5)\n"
    "ds_read_b128 v[216:219], v251 offset:11424\n"
    "ds_read_b128 v[220:223], v251 offset:11760\n"
    "v_mfma_f32_16x16x32_bf16 v[112:115], a[8:11], v[184:187], 0\n"
    "s_waitcnt lgkmcnt(5)\n"
    "ds_read_u16 v194, v194\n"
    "ds_read_u16_d16_hi v202, v202\n"
    "v_or_b32_e64 v191, v191, v207\n"
    "v_mfma_f32_16x16x32_bf16 v[116:119], a[12:15], v[184:187], 0\n"
    "ds_read_b128 v[236:239], v244 offset:0\n"
    "ds_read_b128 a[252:255], v249 offset:0\n"
    "v_lshlrev_b32_sdwa v207, 1, v183 dst_sel:DWORD dst_unused:UNUSED_PAD src0_sel:DWORD src1_sel:WORD_1\n"
    "v_mfma_f32_16x16x32_bf16 v[180:183], a[100:103], v[156:159], 0\n"
    "ds_read_u16 v195, v195\n"
    "v_or_b32_e64 v193, v193, v201\n"
    "v_mfma_f32_16x16x32_bf16 v[180:183], a[148:151], v[160:163], v[180:183]\n"
    "v_cvt_pk_bf16_f32 v131, v134, v135\n"
    "v_cvt_pk_bf16_f32 v104, v104, v105\n"
    "v_mfma_f32_16x16x32_bf16 v[180:183], a[60:63], v[164:167], v[180:183]\n"
    "v_cvt_pk_bf16_f32 v105, v106, v107\n"
    "v_cvt_pk_bf16_f32 v106, v108, v109\n"
    "v_cvt_pk_bf16_f32 v107, v110, v111\n"
    "v_mfma_f32_16x16x32_bf16 v[108:111], a[4:7], v[188:191], 0\n"
    "ds_write_b128 v243, v[128:131] offset:28736\n"
    "s_waitcnt lgkmcnt(7)\n"
    "v_smfmac_f32_16x16x64_bf16 v[40:43], v[216:219], v[208:215], v240\n"
    "s_waitcnt lgkmcnt(6)\n"
    "v_cvt_pk_bf16_f32 v120, v120, v121\n"
    "v_smfmac_f32_16x16x64_bf16 v[44:47], v[220:223], v[208:215], v240 abid:1\n"
    "ds_write_b128 v243, v[104:107] offset:25664\n"
    "v_mfma_f32_16x16x32_bf16 v[104:107], a[0:3], v[188:191], 0\n"
    "v_cvt_pk_bf16_f32 v121, v122, v123\n"
    "v_cvt_pk_bf16_f32 v122, v124, v125\n"
    "v_cvt_pk_bf16_f32 v123, v126, v127\n"
    "v_pk_fma_f16 v232, v192, v192, s48\n"
    "v_mfma_f32_16x16x32_bf16 v[124:127], a[12:15], v[188:191], 0\n"
    "ds_read_b128 v[208:211], v250 offset:720\n"
    "ds_write_b128 v243, v[120:123] offset:27712\n"
    "v_mfma_f32_16x16x32_bf16 v[120:123], a[8:11], v[188:191], 0\n"
    "s_waitcnt lgkmcnt(5)\n"
    "buffer_store_dwordx4 a[252:255], v255, s[44:47], s57 offen offset:0 sc1\n"
    "v_mfma_f32_16x16x32_bf16 v[128:131], v[236:239], v[184:187], 0\n"
    "s_nop 0\n"
    "ds_read_b128 v[212:215], v250 offset:1040\n"
    "ds_read_b128 v[216:219], v252 offset:11424\n"
    "v_or_b32_e64 v194, v194, v202\n"
    "ds_read_b128 v[220:223], v252 offset:11760\n"
    "ds_read_u16_d16_hi v203, v203\n"
    "v_cvt_pk_bf16_f32 v136, v136, v137\n"
    "v_cvt_pk_bf16_f32 v137, v138, v139\n"
    "v_cvt_pk_bf16_f32 v138, v140, v141\n"
    "v_cvt_pk_bf16_f32 v139, v142, v143\n"
    "ds_write_b128 v243, v[136:139] offset:29760\n"
    "v_pk_mul_f16 v232, v192, v232\n"
    "v_mfma_f32_16x16x32_bf16 v[136:139], v[236:239], v[188:191], 0\n"
    "v_exp_f16_e64 v234, v232\n"
    "v_lshlrev_b32_sdwa v201, 1, v171 dst_sel:DWORD dst_unused:UNUSED_PAD src0_sel:DWORD src1_sel:WORD_1\n"
    "v_exp_f16_sdwa v234, v232 dst_sel:WORD_1 dst_unused:UNUSED_PRESERVE src0_sel:WORD_1\n"
    "ds_read_b128 a[244:247], v244 offset:2048\n"
    "ds_read_u16 v197, v197\n"
    "v_pk_fma_f16 v234, v234, s49, s49\n"
    "v_lshlrev_b32_sdwa v202, 1, v173 dst_sel:DWORD dst_unused:UNUSED_PAD src0_sel:DWORD src1_sel:WORD_1\n"
    "v_rcp_f16_e64 v232, v234\n"
    "ds_read_u16_d16_hi v205, v205\n"
    "ds_read_u16 v198, v198\n"
    "s_waitcnt lgkmcnt(7)\n"
    "v_smfmac_f32_16x16x64_bf16 v[40:43], v[216:219], v[208:215], v240\n"
    "s_waitcnt lgkmcnt(6)\n"
    "ds_read_b128 v[216:219], v251 offset:12096\n"
    "ds_read_u16_d16_hi v206, v206\n"
    "v_smfmac_f32_16x16x64_bf16 v[44:47], v[220:223], v[208:215], v240 abid:1\n"
    "ds_read_b128 v[208:211], v250 offset:160\n"
    "v_rcp_f16_sdwa v232, v234 dst_sel:WORD_1 dst_unused:UNUSED_PRESERVE src0_sel:WORD_1\n"
    "ds_read_b128 v[212:215], v250 offset:480\n"
    "s_waitcnt lgkmcnt(8)\n"
    "v_or_b32_e32 v195, v195, v203\n"
    "v_pk_add_f32 v[224:225], v[224:225], v[40:41]\n"
    "v_pk_add_f32 v[226:227], v[226:227], v[42:43]\n"
    "v_pk_fma_f32 v[228:229], v[40:41], v[40:41], v[228:229]\n"
    "v_pk_fma_f32 v[230:231], v[42:43], v[42:43], v[230:231]\n"
    "v_pk_add_f32 v[224:225], v[224:225], v[44:45]\n"
    "v_pk_add_f32 v[226:227], v[226:227], v[46:47]\n"
    "v_pk_fma_f32 v[228:229], v[44:45], v[44:45], v[228:229]\n"
    "v_pk_fma_f32 v[230:231], v[46:47], v[46:47], v[230:231]\n"
    "s_waitcnt lgkmcnt(7)\n"
    "v_mfma_f32_16x16x32_bf16 v[132:135], a[244:247], v[184:187], 0\n"
    "s_nop 0\n"
    "ds_read_b128 v[220:223], v251 offset:12432\n"
    "ds_read_u16 v199, v199\n"
    "v_mfma_f32_16x16x32_bf16 v[140:143], a[244:247], v[188:191], 0\n"
    "ds_read_u16_d16_hi v207, v207\n"
    "v_cvt_pk_f16_f32 v184, v168, v169\n"
    "v_lshlrev_b32_sdwa v185, 1, v170 dst_sel:DWORD dst_unused:UNUSED_PAD src0_sel:DWORD src1_sel:WORD_1\n"
    "v_mfma_f32_16x16x32_bf16 v[168:171], a[104:107], v[144:147], 0\n"
    "ds_read_b128 a[252:255], v249 offset:2048\n"
    "v_lshlrev_b32_sdwa v186, 1, v172 dst_sel:DWORD dst_unused:UNUSED_PAD src0_sel:DWORD src1_sel:WORD_1\n"
    "v_mfma_f32_16x16x32_bf16 v[168:171], a[152:155], v[148:151], v[168:171]\n"
    "s_waitcnt lgkmcnt(9)\n"
    "ds_read_u16 v185, v185\n"
    "v_or_b32_e32 v197, v197, v205\n"
    "v_mfma_f32_16x16x32_bf16 v[168:171], a[64:67], v[152:155], v[168:171]\n"
    "v_lshlrev_b32_sdwa v187, 1, v174 dst_sel:DWORD dst_unused:UNUSED_PAD src0_sel:DWORD src1_sel:WORD_1\n"
    "v_lshlrev_b32_sdwa v203, 1, v175 dst_sel:DWORD dst_unused:UNUSED_PAD src0_sel:DWORD src1_sel:WORD_1\n"
    "v_mfma_f32_16x16x32_bf16 v[172:175], a[108:111], v[144:147], 0\n"
    "ds_read_u16_d16_hi v201, v201\n"
    "v_cvt_pk_f16_f32 v188, v176, v177\n"
    "v_lshlrev_b32_sdwa v189, 1, v178 dst_sel:DWORD dst_unused:UNUSED_PAD src0_sel:DWORD src1_sel:WORD_1\n"
    "v_lshlrev_b32_sdwa v205, 1, v179 dst_sel:DWORD dst_unused:UNUSED_PAD src0_sel:DWORD src1_sel:WORD_1\n"
    "v_lshlrev_b32_sdwa v190, 1, v180 dst_sel:DWORD dst_unused:UNUSED_PAD src0_sel:DWORD src1_sel:WORD_1\n"
    "v_lshlrev_b32_sdwa v191, 1, v182 dst_sel:DWORD dst_unused:UNUSED_PAD src0_sel:DWORD src1_sel:WORD_1\n"
    "v_fma_mix_f32 v234, v192, v232, 0 op_sel_hi:[1,1,0]\n"
    "v_mfma_f32_16x16x32_bf16 v[172:175], a[156:159], v[148:151], v[172:175]\n"
    "s_waitcnt lgkmcnt(7)\n"
    "ds_read_u16 v186, v186\n"
    "v_or_b32_e32 v198, v198, v206\n"
    "v_mfma_f32_16x16x32_bf16 v[172:175], a[68:71], v[152:155], v[172:175]\n"
    "s_waitcnt lgkmcnt(7)\n"
    "ds_read_u16_d16_hi v202, v202\n"
    "v_lshlrev_b32_sdwa v206, 1, v181 dst_sel:DWORD dst_unused:UNUSED_PAD src0_sel:DWORD src1_sel:WORD_1\n"
    "v_smfmac_f32_16x16x64_bf16 v[48:51], v[216:219], v[208:215], v240\n"
    "s_waitcnt lgkmcnt(7)\n"
    "ds_read_b128 v[216:219], v252 offset:12096\n"
    "v_smfmac_f32_16x16x64_bf16 v[52:55], v[220:223], v[208:215], v240 abid:1\n"
    "ds_read_b128 v[208:211], v250 offset:800\n"
    "v_fma_mix_f32 v232, v192, v232, 0 op_sel:[1,1,0] op_sel_hi:[1,1,0]\n"
    "v_mfma_f32_16x16x32_bf16 v[176:179], a[104:107], v[156:159], 0\n"
    "ds_read_b128 v[212:215], v250 offset:1120\n"
    "v_cvt_pk_bf16_f32 v192, v234, v232\n"
    "v_mfma_f32_16x16x32_bf16 v[176:179], a[152:155], v[160:163], v[176:179]\n"
    "ds_read_b128 v[220:223], v252 offset:12432\n"
    "v_pk_fma_f16 v232, v196, v196, s48\n"
    "v_mfma_f32_16x16x32_bf16 v[176:179], a[64:67], v[164:167], v[176:179]\n"
    "s_waitcnt lgkmcnt(8)\n"
    "ds_read_b128 v[236:239], v244 offset:4096\n"
    "buffer_store_dwordx4 a[252:255], v255, s[44:47], s57 offen offset:64 sc1\n"
    "v_or_b32_e32 v199, v199, v207\n"
    "v_lshlrev_b32_sdwa v207, 1, v183 dst_sel:DWORD dst_unused:UNUSED_PAD src0_sel:DWORD src1_sel:WORD_1\n"
    "v_mfma_f32_16x16x32_bf16 v[180:183], a[108:111], v[156:159], 0\n"
    "ds_read_u16 v187, v187\n"
    "ds_read_u16_d16_hi v203, v203\n"
    "v_pk_mul_f16 v232, v196, v232\n"
    "v_mfma_f32_16x16x32_bf16 v[180:183], a[156:159], v[160:163], v[180:183]\n"
    "v_exp_f16_e32 v234, v232\n"
    "v_mfma_f32_16x16x32_bf16 v[180:183], a[68:71], v[164:167], v[180:183]\n"
    "s_waitcnt lgkmcnt(9)\n"
    "ds_read_b128 a[244:247], v244 offset:6144\n"
    "v_exp_f16_sdwa v234, v232 dst_sel:WORD_1 dst_unused:UNUSED_PRESERVE src0_sel:WORD_1\n"
    "v_or_b32_e64 v185, v185, v201\n"
    "v_pk_fma_f16 v234, v234, s49, s49\n"
    "v_mfma_f32_16x16x32_bf16 v[96:99], a[16:19], v[192:195], v[96:99]\n"
    "ds_read_b128 a[252:255], v249 offset:4096\n"
    "ds_read_u16 v189, v189\n"
    "v_mfma_f32_16x16x32_bf16 v[100:103], a[20:23], v[192:195], v[100:103]\n"
    "v_rcp_f16_e32 v232, v234\n"
    "v_mfma_f32_16x16x32_bf16 v[112:115], a[24:27], v[192:195], v[112:115]\n"
    "s_waitcnt lgkmcnt(9)\n"
    "ds_read_u16_d16_hi v205, v205\n"
    "v_rcp_f16_sdwa v232, v234 dst_sel:WORD_1 dst_unused:UNUSED_PRESERVE src0_sel:WORD_1\n"
    "v_mfma_f32_16x16x32_bf16 v[116:119], a[28:31], v[192:195], v[116:119]\n"
    "v_fma_mix_f32 v234, v196, v232, 0 op_sel_hi:[1,1,0]\n"
    "v_fma_mix_f32 v232, v196, v232, 0 op_sel:[1,1,0] op_sel_hi:[1,1,0]\n"
    "s_waitcnt lgkmcnt(8)\n"
    "v_smfmac_f32_16x16x64_bf16 v[48:51], v[216:219], v[208:215], v240\n"
    "s_waitcnt lgkmcnt(7)\n"
    "ds_read_b128 v[216:219], v251 offset:12768\n"
    "ds_read_u16 v190, v190\n"
    "v_smfmac_f32_16x16x64_bf16 v[52:55], v[220:223], v[208:215], v240 abid:1\n"
    "s_waitcnt lgkmcnt(8)\n"
    "v_or_b32_e32 v186, v186, v202\n"
    "v_mfma_f32_16x16x32_bf16 v[128:131], v[236:239], v[192:195], v[128:131]\n"
    "ds_read_b128 v[208:211], v250 offset:240\n"
    "v_cvt_pk_bf16_f32 v196, v234, v232\n"
    "ds_read_b128 v[212:215], v250 offset:560\n"
    "v_pk_add_f32 v[224:225], v[224:225], v[48:49]\n"
    "v_pk_add_f32 v[226:227], v[226:227], v[50:51]\n"
    "v_pk_fma_f32 v[228:229], v[48:49], v[48:49], v[228:229]\n"
    "v_pk_fma_f32 v[230:231], v[50:51], v[50:51], v[230:231]\n"
    "v_pk_add_f32 v[224:225], v[224:225], v[52:53]\n"
    "v_pk_add_f32 v[226:227], v[226:227], v[54:55]\n"
    "v_pk_fma_f32 v[228:229], v[52:53], v[52:53], v[228:229]\n"
    "v_pk_fma_f32 v[230:231], v[54:55], v[54:55], v[230:231]\n"
    "v_mfma_f32_16x16x32_bf16 v[104:107], a[16:19], v[196:199], v[104:107]\n"
    "s_waitcnt lgkmcnt(7)\n"
    "ds_read_b128 v[220:223], v251 offset:13104\n"
    "ds_read_u16_d16_hi v206, v206\n"
    "v_mfma_f32_16x16x32_bf16 v[132:135], a[244:247], v[192:195], v[132:135]\n"
    "s_waitcnt lgkmcnt(8)\n"
    "ds_read_u16 v191, v191\n"
    "buffer_store_dwordx4 a[252:255], v255, s[44:47], s57 offen offset:128 sc1\n"
    "v_mfma_f32_16x16x32_bf16 v[108:111], a[20:23], v[196:199], v[108:111]\n"
    "ds_read_u16_d16_hi v207, v207\n"
    "v_pk_fma_f16 v232, v184, v184, s48\n"
    "v_mfma_f32_16x16x32_bf16 v[120:123], a[24:27], v[196:199], v[120:123]\n"
    "ds_read_b128 a[252:255], v249 offset:1024\n"
    "v_or_b32_e64 v187, v187, v203\n"
    "v_mfma_f32_16x16x32_bf16 v[124:127], a[28:31], v[196:199], v[124:127]\n"
    "ds_read_b128 a[240:243], v244 offset:34816\n"
    "v_pk_mul_f16 v232, v184, v232\n"
    "v_mfma_f32_16x16x32_bf16 v[136:139], v[236:239], v[196:199], v[136:139]\n"
    "s_waitcnt lgkmcnt(9)\n"
    "v_exp_f16_e32 v234, v232\n"
    "v_mfma_f32_16x16x32_bf16 v[140:143], a[244:247], v[196:199], v[140:143]\n"
    "v_or_b32_e64 v189, v189, v205\n"
    "v_exp_f16_sdwa v234, v232 dst_sel:WORD_1 dst_unused:UNUSED_PRESERVE src0_sel:WORD_1\n"
    "v_lshlrev_b32_sdwa v192, 1, v168 dst_sel:DWORD dst_unused:UNUSED_PAD src0_sel:DWORD src1_sel:WORD_1\n"
    "v_pk_fma_f16 v234, v234, s49, s49\n"
    "v_lshlrev_b32_sdwa v200, 1, v169 dst_sel:DWORD dst_unused:UNUSED_PAD src0_sel:DWORD src1_sel:WORD_1\n"
    "v_lshlrev_b32_sdwa v193, 1, v170 dst_sel:DWORD dst_unused:UNUSED_PAD src0_sel:DWORD src1_sel:WORD_1\n"
    "s_waitcnt lgkmcnt(6)\n"
    "v_smfmac_f32_16x16x64_bf16 v[56:59], v[216:219], v[208:215], v240\n"
    "s_waitcnt lgkmcnt(5)\n"
    "ds_read_b128 v[216:219], v252 offset:12768\n"
    "ds_read_u16 v192, v192\n"
    "v_smfmac_f32_16x16x64_bf16 v[60:63], v[220:223], v[208:215], v240 abid:1\n"
    "ds_read_b128 v[208:211], v250 offset:880\n"
    "ds_read_b128 v[212:215], v250 offset:1200\n"
    "v_lshlrev_b32_sdwa v201, 1, v171 dst_sel:DWORD dst_unused:UNUSED_PAD src0_sel:DWORD src1_sel:WORD_1\n"
    "v_mfma_f32_16x16x32_bf16 v[168:171], a[112:115], v[144:147], 0\n"
    "s_nop 0\n"
    "ds_read_b128 v[220:223], v252 offset:13104\n"
    "ds_read_u16_d16_hi v200, v200\n"
    "v_mfma_f32_16x16x32_bf16 v[168:171], a[160:163], v[148:151], v[168:171]\n"
    "s_waitcnt lgkmcnt(10)\n"
    "v_lshlrev_b32_sdwa v194, 1, v172 dst_sel:DWORD dst_unused:UNUSED_PAD src0_sel:DWORD src1_sel:WORD_1\n"
    "v_lshlrev_b32_sdwa v202, 1, v173 dst_sel:DWORD dst_unused:UNUSED_PAD src0_sel:DWORD src1_sel:WORD_1\n"
    "v_lshlrev_b32_sdwa v195, 1, v174 dst_sel:DWORD dst_unused:UNUSED_PAD src0_sel:DWORD src1_sel:WORD_1\n"
    "v_lshlrev_b32_sdwa v203, 1, v175 dst_sel:DWORD dst_unused:UNUSED_PAD src0_sel:DWORD src1_sel:WORD_1\n"
    "v_rcp_f16_e64 v232, v234\n"
    "v_mfma_f32_16x16x32_bf16 v[168:171], a[72:75], v[152:155], v[168:171]\n"
    "ds_read_u16 v193, v193\n"
    "ds_read_u16_d16_hi v201, v201\n"
    "v_or_b32_e32 v190, v190, v206\n"
    "v_rcp_f16_sdwa v232, v234 dst_sel:WORD_1 dst_unused:UNUSED_PRESERVE src0_sel:WORD_1\n"
    "v_mfma_f32_16x16x32_bf16 v[172:175], a[116:119], v[144:147], 0\n"
    "s_waitcnt lgkmcnt(8)\n"
    "ds_read_u16 v194, v194\n"
    "buffer_store_dwordx4 a[252:255], v255, s[44:47], s88 offen offset:0 sc1\n"
    "v_lshlrev_b32_sdwa v196, 1, v176 dst_sel:DWORD dst_unused:UNUSED_PAD src0_sel:DWORD src1_sel:WORD_1\n"
    "v_lshlrev_b32_sdwa v204, 1, v177 dst_sel:DWORD dst_unused:UNUSED_PAD src0_sel:DWORD src1_sel:WORD_1\n"
    "v_lshlrev_b32_sdwa v197, 1, v178 dst_sel:DWORD dst_unused:UNUSED_PAD src0_sel:DWORD src1_sel:WORD_1\n"
    "v_lshlrev_b32_sdwa v205, 1, v179 dst_sel:DWORD dst_unused:UNUSED_PAD src0_sel:DWORD src1_sel:WORD_1\n"
    "v_lshlrev_b32_sdwa v198, 1, v180 dst_sel:DWORD dst_unused:UNUSED_PAD src0_sel:DWORD src1_sel:WORD_1\n"
    "v_lshlrev_b32_sdwa v206, 1, v181 dst_sel:DWORD dst_unused:UNUSED_PAD src0_sel:DWORD src1_sel:WORD_1\n"
    "v_lshlrev_b32_sdwa v199, 1, v182 dst_sel:DWORD dst_unused:UNUSED_PAD src0_sel:DWORD src1_sel:WORD_1\n"
    "v_fma_mix_f32 v234, v184, v232, 0 op_sel_hi:[1,1,0]\n"
    "v_mfma_f32_16x16x32_bf16 v[172:175], a[164:167], v[148:151], v[172:175]\n"
    "ds_read_b128 v[236:239], v244 offset:8192\n"
    "ds_read_u16_d16_hi v202, v202\n"
    "v_mfma_f32_16x16x32_bf16 v[172:175], a[240:243], v[152:155], v[172:175]\n"
    "s_waitcnt lgkmcnt(7)\n"
    "ds_read_u16 v195, v195\n"
    "v_or_b32_e64 v191, v191, v207\n"
    "v_smfmac_f32_16x16x64_bf16 v[56:59], v[216:219], v[208:215], v240\n"
    "s_waitcnt lgkmcnt(7)\n"
    "ds_read_b128 a[244:247], v244 offset:10240\n"
    "v_smfmac_f32_16x16x64_bf16 v[60:63], v[220:223], v[208:215], v240 abid:1\n"
    "s_barrier\n"
    "v_lshlrev_b32_sdwa v207, 1, v183 dst_sel:DWORD dst_unused:UNUSED_PAD src0_sel:DWORD src1_sel:WORD_1\n"
    "v_fma_mix_f32 v232, v184, v232, 0 op_sel:[1,1,0] op_sel_hi:[1,1,0]\n"
    "v_mfma_f32_16x16x32_bf16 v[176:179], a[112:115], v[156:159], 0\n"
    "s_waitcnt vmcnt(6) lgkmcnt(6)\n"
    "ds_read_u16_d16_hi v203, v203\n"
    "ds_write2_b32 v248, v64, v65 offset0:0 offset1:20\n"
    "v_cvt_pk_bf16_f32 v184, v234, v232\n"
    "v_pk_add_f32 v[224:225], v[224:225], v[56:57]\n"
    "v_pk_add_f32 v[226:227], v[226:227], v[58:59]\n"
    "v_pk_fma_f32 v[228:229], v[56:57], v[56:57], v[228:229]\n"
    "v_pk_fma_f32 v[230:231], v[58:59], v[58:59], v[230:231]\n"
    "v_pk_add_f32 v[224:225], v[224:225], v[60:61]\n"
    "v_pk_add_f32 v[226:227], v[226:227], v[62:63]\n"
    "v_pk_fma_f32 v[228:229], v[60:61], v[60:61], v[228:229]\n"
    "v_pk_fma_f32 v[230:231], v[62:63], v[62:63], v[230:231]\n"
    "v_mfma_f32_16x16x32_bf16 v[176:179], a[160:163], v[160:163], v[176:179]\n"
    "ds_read_u16 v196, v196\n"
    "ds_write2_b32 v248, v66, v67 offset0:40 offset1:60\n"
    "v_mfma_f32_16x16x32_bf16 v[176:179], a[72:75], v[164:167], v[176:179]\n"
    "v_add_u32_e32 v248, 1360, v248\n"
    "v_pk_fma_f16 v232, v188, v188, s48\n"
    "v_mfma_f32_16x16x32_bf16 v[180:183], a[116:119], v[156:159], 0\n"
    "s_waitcnt lgkmcnt(6)\n"
    "ds_read_u16_d16_hi v204, v204\n"
    "ds_write2_b32 v235, v84, v85 offset0:0 offset1:20\n"
    "v_pk_mul_f16 v232, v188, v232\n"
    "v_mfma_f32_16x16x32_bf16 v[180:183], a[164:167], v[160:163], v[180:183]\n"
    "v_exp_f16_e32 v234, v232\n"
    "v_mfma_f32_16x16x32_bf16 v[180:183], a[240:243], v[164:167], v[180:183]\n"
    "ds_read_u16 v197, v197\n"
    "ds_write2_b32 v235, v86, v87 offset0:40 offset1:60\n"
    "v_add_u32_e32 v235, 1360, v235\n"
    "v_exp_f16_sdwa v234, v232 dst_sel:WORD_1 dst_unused:UNUSED_PRESERVE src0_sel:WORD_1\n"
    "v_or_b32_e32 v192, v192, v200\n"
    "v_pk_fma_f16 v234, v234, s49, s49\n"
    "v_mfma_f32_16x16x32_bf16 v[96:99], a[32:35], v[184:187], v[96:99]\n"
    "s_waitcnt vmcnt(5) lgkmcnt(6)\n"
    "ds_read_b128 a[252:255], v249 offset:3072\n"
    "ds_write2_b32 v248, v68, v69 offset0:0 offset1:20\n"
    "v_mfma_f32_16x16x32_bf16 v[100:103], a[36:39], v[184:187], v[100:103]\n"
    "v_rcp_f16_e32 v232, v234\n"
    "v_mfma_f32_16x16x32_bf16 v[112:115], a[40:43], v[184:187], v[112:115]\n"
    "ds_read_u16_d16_hi v205, v205\n"
    "ds_write2_b32 v248, v70, v71 offset0:40 offset1:60\n"
    "v_add_u32_e32 v248, 1360, v248\n"
    "v_rcp_f16_sdwa v232, v234 dst_sel:WORD_1 dst_unused:UNUSED_PRESERVE src0_sel:WORD_1\n"
    "v_mfma_f32_16x16x32_bf16 v[116:119], a[44:47], v[184:187], v[116:119]\n"
    "s_waitcnt lgkmcnt(6)\n"
    "ds_read_u16 v198, v198\n"
    "ds_read_u16_d16_hi v206, v206\n"
    "v_fma_mix_f32 v234, v188, v232, 0 op_sel_hi:[1,1,0]\n"
    "v_mfma_f32_16x16x32_bf16 v[128:131], v[236:239], v[184:187], v[128:131]\n"
    "ds_read_u16 v199, v199\n"
    "v_fma_mix_f32 v232, v188, v232, 0 op_sel:[1,1,0] op_sel_hi:[1,1,0]\n"
    "v_mfma_f32_16x16x32_bf16 v[132:135], a[244:247], v[184:187], v[132:135]\n"
    "ds_read_u16_d16_hi v207, v207\n"
    "v_cvt_pk_bf16_f32 v188, v234, v232\n"
    "v_or_b32_e32 v193, v193, v201\n"
    "v_cvt_pk_f16_f32 v184, v168, v169\n"
    "v_mfma_f32_16x16x32_bf16 v[104:107], a[32:35], v[188:191], v[104:107]\n"
    "s_waitcnt lgkmcnt(6)\n"
    "ds_read_b128 a[240:243], v244 offset:38912\n"
    "buffer_store_dwordx4 a[252:255], v255, s[44:47], s88 offen offset:64 sc1\n"
    "v_mfma_f32_16x16x32_bf16 v[108:111], a[36:39], v[188:191], v[108:111]\n"
    "v_lshlrev_b32_sdwa v185, 1, v170 dst_sel:DWORD dst_unused:UNUSED_PAD src0_sel:DWORD src1_sel:WORD_1\n"
    "v_lshlrev_b32_sdwa v201, 1, v171 dst_sel:DWORD dst_unused:UNUSED_PAD src0_sel:DWORD src1_sel:WORD_1\n"
    "v_mfma_f32_16x16x32_bf16 v[120:123], a[40:43], v[188:191], v[120:123]\n"
    "ds_read_u16 v185, v185\n"
    "ds_read_b128 a[252:255], v249 offset:5120\n"
    "v_mfma_f32_16x16x32_bf16 v[124:127], a[44:47], v[188:191], v[124:127]\n"
    "s_waitcnt lgkmcnt(7)\n"
    "ds_read_u16_d16_hi v201, v201\n"
    "v_lshlrev_b32_sdwa v186, 1, v172 dst_sel:DWORD dst_unused:UNUSED_PAD src0_sel:DWORD src1_sel:WORD_1\n"
    "v_mfma_f32_16x16x32_bf16 v[136:139], v[236:239], v[188:191], v[136:139]\n"
    "v_or_b32_e32 v194, v194, v202\n"
    "v_lshlrev_b32_sdwa v202, 1, v173 dst_sel:DWORD dst_unused:UNUSED_PAD src0_sel:DWORD src1_sel:WORD_1\n"
    "v_mfma_f32_16x16x32_bf16 v[140:143], a[244:247], v[188:191], v[140:143]\n"
    "v_lshlrev_b32_sdwa v187, 1, v174 dst_sel:DWORD dst_unused:UNUSED_PAD src0_sel:DWORD src1_sel:WORD_1\n"
    "v_cvt_pk_f16_f32 v188, v176, v177\n"
    "v_mfma_f32_16x16x32_bf16 v[168:171], a[120:123], v[144:147], 0\n"
    "ds_write2_b32 v235, v88, v89 offset0:0 offset1:20\n"
    "v_mfma_f32_16x16x32_bf16 v[168:171], a[168:171], v[148:151], v[168:171]\n"
    "s_nop 0\n"
    "v_lshlrev_b32_sdwa v189, 1, v178 dst_sel:DWORD dst_unused:UNUSED_PAD src0_sel:DWORD src1_sel:WORD_1\n"
    "v_lshlrev_b32_sdwa v190, 1, v180 dst_sel:DWORD dst_unused:UNUSED_PAD src0_sel:DWORD src1_sel:WORD_1\n"
    "v_mfma_f32_16x16x32_bf16 v[168:171], a[76:79], v[152:155], v[168:171]\n"
    "s_waitcnt lgkmcnt(7)\n"
    "ds_read_u16 v186, v186\n"
    "ds_write2_b32 v235, v90, v91 offset0:40 offset1:60\n"
    "v_add_u32_e32 v235, -1360, v235\n"
    "v_lshlrev_b32_sdwa v191, 1, v182 dst_sel:DWORD dst_unused:UNUSED_PAD src0_sel:DWORD src1_sel:WORD_1\n"
    "v_or_b32_e64 v195, v195, v203\n"
    "v_lshlrev_b32_sdwa v203, 1, v175 dst_sel:DWORD dst_unused:UNUSED_PAD src0_sel:DWORD src1_sel:WORD_1\n"
    "v_mfma_f32_16x16x32_bf16 v[172:175], a[124:127], v[144:147], 0\n"
    "ds_read_u16_d16_hi v202, v202\n"
    "ds_read_b128 v[236:239], v244 offset:12288\n"
    "v_mfma_f32_16x16x32_bf16 v[172:175], a[172:175], v[148:151], v[172:175]\n"
    "s_waitcnt lgkmcnt(8)\n"
    "ds_read_u16 v187, v187\n"
    "v_or_b32_e32 v196, v196, v204\n"
    "v_mfma_f32_16x16x32_bf16 v[172:175], a[240:243], v[152:155], v[172:175]\n"
    "v_or_b32_e64 v197, v197, v205\n"
    "v_lshlrev_b32_sdwa v205, 1, v179 dst_sel:DWORD dst_unused:UNUSED_PAD src0_sel:DWORD src1_sel:WORD_1\n"
    "v_mfma_f32_16x16x32_bf16 v[176:179], a[120:123], v[156:159], 0\n"
    "s_waitcnt lgkmcnt(7)\n"
    "buffer_store_dwordx4 a[252:255], v255, s[44:47], s88 offen offset:128 sc1\n"
    "ds_write2_b32 v248, v72, v73 offset0:0 offset1:20\n"
    "v_mfma_f32_16x16x32_bf16 v[176:179], a[168:171], v[160:163], v[176:179]\n"
    "s_nop 0\n"
    "v_or_b32_e32 v198, v198, v206\n"
    "v_lshlrev_b32_sdwa v206, 1, v181 dst_sel:DWORD dst_unused:UNUSED_PAD src0_sel:DWORD src1_sel:WORD_1\n"
    "v_mfma_f32_16x16x32_bf16 v[176:179], a[76:79], v[164:167], v[176:179]\n"
    "s_waitcnt lgkmcnt(6)\n"
    "ds_read_u16_d16_hi v203, v203\n"
    "ds_write2_b32 v248, v74, v75 offset0:40 offset1:60\n"
    "v_mfma_f32_16x16x32_bf16 v[96:99], a[176:179], v[192:195], v[96:99]\n"
    "v_add_u32_e32 v248, 1360, v248\n"
    "v_or_b32_e64 v199, v199, v207\n"
    "v_mfma_f32_16x16x32_bf16 v[100:103], a[180:183], v[192:195], v[100:103]\n"
    "ds_read_b128 a[244:247], v244 offset:14336\n"
    "ds_write2_b32 v248, v76, v77 offset0:0 offset1:20\n"
    "v_mfma_f32_16x16x32_bf16 v[112:115], a[184:187], v[192:195], v[112:115]\n"
    "v_lshlrev_b32_sdwa v207, 1, v183 dst_sel:DWORD dst_unused:UNUSED_PAD src0_sel:DWORD src1_sel:WORD_1\n"
    "v_or_b32_e32 v185, v185, v201\n"
    "v_mfma_f32_16x16x32_bf16 v[180:183], a[124:127], v[156:159], 0\n"
    "s_waitcnt lgkmcnt(6)\n"
    "ds_read_u16 v189, v189\n"
    "ds_write2_b32 v248, v78, v79 offset0:40 offset1:60\n"
    "v_mfma_f32_16x16x32_bf16 v[180:183], a[172:175], v[160:163], v[180:183]\n"
    "v_add_u32_e32 v248, 1360, v248\n"
    "v_or_b32_e64 v186, v186, v202\n"
    "v_mfma_f32_16x16x32_bf16 v[180:183], a[240:243], v[164:167], v[180:183]\n"
    "ds_read_u16_d16_hi v205, v205\n"
    "ds_read_u16 v190, v190\n"
    "v_mfma_f32_16x16x32_bf16 v[116:119], a[188:191], v[192:195], v[116:119]\n"
    "ds_read_b128 v[144:147], v243 offset:0\n"
    "ds_read_u16_d16_hi v206, v206\n"
    "v_mfma_f32_16x16x32_bf16 v[128:131], v[236:239], v[192:195], v[128:131]\n"
    "v_pk_fma_f16 v232, v184, v184, s48\n"
    "v_lshlrev_b32_sdwa v201, 1, v171 dst_sel:DWORD dst_unused:UNUSED_PAD src0_sel:DWORD src1_sel:WORD_1\n"
    "v_mfma_f32_16x16x32_bf16 v[104:107], a[176:179], v[196:199], v[104:107]\n"
    "s_waitcnt lgkmcnt(8)\n"
    "ds_read_b128 v[148:151], v243 offset:1024\n"
    "v_or_b32_e64 v187, v187, v203\n"
    "v_mfma_f32_16x16x32_bf16 v[108:111], a[180:183], v[196:199], v[108:111]\n"
    "s_waitcnt lgkmcnt(7)\n"
    "ds_read_u16 v191, v191\n"
    "v_pk_mul_f16 v232, v184, v232\n"
    "v_mfma_f32_16x16x32_bf16 v[132:135], a[244:247], v[192:195], v[132:135]\n"
    "v_exp_f16_e64 v234, v232\n"
    "v_mfma_f32_16x16x32_bf16 v[120:123], a[184:187], v[196:199], v[120:123]\n"
    "v_exp_f16_sdwa v234, v232 dst_sel:WORD_1 dst_unused:UNUSED_PRESERVE src0_sel:WORD_1\n"
    "v_mfma_f32_16x16x32_bf16 v[124:127], a[188:191], v[196:199], v[124:127]\n"
    "ds_write2_b32 v248, v80, v81 offset0:0 offset1:20\n"
    "v_mfma_f32_16x16x32_bf16 v[136:139], v[236:239], v[196:199], v[136:139]\n"
    "v_pk_fma_f16 v234, v234, s49, s49\n"
    "v_cvt_pk_f16_f32 v192, v168, v169\n"
    "v_mfma_f32_16x16x32_bf16 v[140:143], a[244:247], v[196:199], v[140:143]\n"
    "ds_read_b128 v[152:155], v243 offset:2048\n"
    "ds_read_u16_d16_hi v207, v207\n"
    "v_lshlrev_b32_sdwa v193, 1, v170 dst_sel:DWORD dst_unused:UNUSED_PAD src0_sel:DWORD src1_sel:WORD_1\n"
    "v_lshlrev_b32_sdwa v194, 1, v172 dst_sel:DWORD dst_unused:UNUSED_PAD src0_sel:DWORD src1_sel:WORD_1\n"
    "s_waitcnt lgkmcnt(8)\n"
    "v_or_b32_e32 v189, v189, v205\n"
    "v_lshlrev_b32_sdwa v202, 1, v173 dst_sel:DWORD dst_unused:UNUSED_PAD src0_sel:DWORD src1_sel:WORD_1\n"
    "v_lshlrev_b32_sdwa v195, 1, v174 dst_sel:DWORD dst_unused:UNUSED_PAD src0_sel:DWORD src1_sel:WORD_1\n"
    "v_lshlrev_b32_sdwa v203, 1, v175 dst_sel:DWORD dst_unused:UNUSED_PAD src0_sel:DWORD src1_sel:WORD_1\n"
    "v_rcp_f16_e32 v232, v234\n"
    "s_waitcnt lgkmcnt(6)\n"
    "v_mfma_f32_16x16x32_bf16 v[168:171], a[80:83], v[144:147], 0\n"
    "ds_read_b128 v[156:159], v243 offset:3072\n"
    "ds_write2_b32 v248, v82, v83 offset0:40 offset1:60\n"
    "v_mfma_f32_32x32x1_2b_f32 v[64:95], v254, v254, 0\n"
    "buffer_load_dwordx4 a[232:235], v245, s[40:43], s58 offen offset:64\n"
    "buffer_load_dwordx4 a[224:227], v245, s[40:43], s58 offen offset:0\n"
    "v_add_u32_e32 v248, -5440, v248\n"
    "s_waitcnt lgkmcnt(6)\n"
    "v_or_b32_e32 v190, v190, v206\n"
    "v_rcp_f16_sdwa v232, v234 dst_sel:WORD_1 dst_unused:UNUSED_PRESERVE src0_sel:WORD_1\n"
    "v_cvt_pk_f16_f32 v196, v176, v177\n"
    "v_lshlrev_b32_sdwa v197, 1, v178 dst_sel:DWORD dst_unused:UNUSED_PAD src0_sel:DWORD src1_sel:WORD_1\n"
    "v_lshlrev_b32_sdwa v205, 1, v179 dst_sel:DWORD dst_unused:UNUSED_PAD src0_sel:DWORD src1_sel:WORD_1\n"
    "v_lshlrev_b32_sdwa v198, 1, v180 dst_sel:DWORD dst_unused:UNUSED_PAD src0_sel:DWORD src1_sel:WORD_1\n"
    "v_lshlrev_b32_sdwa v206, 1, v181 dst_sel:DWORD dst_unused:UNUSED_PAD src0_sel:DWORD src1_sel:WORD_1\n"
    "v_lshlrev_b32_sdwa v199, 1, v182 dst_sel:DWORD dst_unused:UNUSED_PAD src0_sel:DWORD src1_sel:WORD_1\n"
    "v_fma_mix_f32 v234, v184, v232, 0 op_sel_hi:[1,1,0]\n"
    "v_mfma_f32_16x16x32_bf16 v[168:171], a[128:131], v[148:151], v[168:171]\n"
    "s_waitcnt lgkmcnt(3)\n"
    "ds_read_b128 v[160:163], v243 offset:4096\n"
    "ds_read_b128 v[164:167], v243 offset:5120\n"
    "v_mfma_f32_16x16x32_bf16 v[168:171], a[48:51], v[152:155], v[168:171]\n"
    "s_nop 0\n"
    "ds_read_b128 a[240:243], v244 offset:32768\n"
    "ds_read_u16 v193, v193\n"
    "v_mfma_f32_16x16x32_bf16 v[172:175], a[84:87], v[144:147], 0\n"
    "ds_read_u16_d16_hi v201, v201\n"
    "v_fma_mix_f32 v232, v184, v232, 0 op_sel:[1,1,0] op_sel_hi:[1,1,0]\n"
    "v_mfma_f32_16x16x32_bf16 v[172:175], a[132:135], v[148:151], v[172:175]\n"
    "s_waitcnt lgkmcnt(5)\n"
    "ds_read_u16 v194, v194\n"
    "v_or_b32_e32 v191, v191, v207\n"
    "v_mfma_f32_16x16x32_bf16 v[176:179], a[80:83], v[156:159], 0\n"
    "v_lshlrev_b32_sdwa v207, 1, v183 dst_sel:DWORD dst_unused:UNUSED_PAD src0_sel:DWORD src1_sel:WORD_1\n"
    "v_cvt_pk_bf16_f32 v184, v234, v232\n"
    "v_pk_fma_f16 v232, v188, v188, s48\n"
    "v_mfma_f32_16x16x32_bf16 v[180:183], a[84:87], v[156:159], 0\n"
    "ds_read_u16_d16_hi v202, v202\n"
    "v_pk_mul_f16 v232, v188, v232\n"
    "v_lshlrev_b32_e64 v254, 3, v244\n"
    "v_exp_f16_e64 v234, v232\n"
    "v_lshlrev_b32_sdwa v200, 1, v169 dst_sel:DWORD dst_unused:UNUSED_PAD src0_sel:DWORD src1_sel:WORD_1\n"
    "v_exp_f16_sdwa v234, v232 dst_sel:WORD_1 dst_unused:UNUSED_PRESERVE src0_sel:WORD_1\n"
    "v_mfma_f32_16x16x32_bf16 v[96:99], a[192:195], v[184:187], v[96:99]\n"
    "s_nop 0\n"
    "ds_read_b128 v[236:239], v244 offset:16384\n"
    "ds_read_u16 v195, v195\n"
    "s_waitcnt lgkmcnt(8)\n"
    "v_mfma_f32_16x16x32_bf16 v[176:179], a[128:131], v[160:163], v[176:179]\n"
    "s_waitcnt lgkmcnt(7)\n"
    "ds_read_b128 v[216:219], v251 offset:21504\n"
    "s_mov_b32 exec_hi, 0\n"
    "buffer_load_dword v233, v254, s[72:75], s53 offen sc1\n"
    "s_mov_b32 exec_hi, -1\n"
    "v_mfma_f32_16x16x32_bf16 v[176:179], a[48:51], v[164:167], v[176:179]\n"
    "s_waitcnt lgkmcnt(7)\n"
    "ds_read_b128 v[220:223], v251 offset:21840\n"
    "ds_read_u16_d16_hi v203, v203\n"
    "v_mfma_f32_16x16x32_bf16 v[172:175], a[240:243], v[152:155], v[172:175]\n"
    "s_barrier\n"
    "v_mfma_f32_16x16x32_bf16 v[180:183], a[132:135], v[160:163], v[180:183]\n"
    "s_waitcnt lgkmcnt(7)\n"
    "ds_read_b128 v[208:211], v250 offset:0\n"
    "ds_read_b128 v[212:215], v250 offset:320\n"
    "v_mfma_f32_16x16x32_bf16 v[180:183], a[240:243], v[164:167], v[180:183]\n"
    "s_add_u32 s53, s53, 393216\n"
    "ds_read_b128 a[244:247], v244 offset:18432\n"
    "ds_read_u16 v197, v197\n"
    "v_pk_fma_f16 v234, v234, s49, s49\n"
    "v_mfma_f32_16x16x32_bf16 v[100:103], a[196:199], v[184:187], v[100:103]\n"
    "ds_read_u16_d16_hi v205, v205\n"
    "v_rcp_f16_e32 v232, v234\n"
    "v_mfma_f32_16x16x32_bf16 v[112:115], a[200:203], v[184:187], v[112:115]\n"
    "s_waitcnt lgkmcnt(9)\n"
    "v_rcp_f16_sdwa v232, v234 dst_sel:WORD_1 dst_unused:UNUSED_PRESERVE src0_sel:WORD_1\n"
    "v_mfma_f32_16x16x32_bf16 v[116:119], a[204:207], v[184:187], v[116:119]\n"
    "ds_read_u16 v198, v198\n"
    "buffer_load_dwordx4 a[236:239], v245, s[40:43], s89 offen offset:64\n"
    "buffer_load_dwordx4 a[228:231], v245, s[40:43], s89 offen offset:0\n"
    "v_mfma_f32_16x16x32_bf16 v[128:131], v[236:239], v[184:187], v[128:131]\n"
    "ds_read_u16_d16_hi v206, v206\n"
    "v_or_b32_e64 v193, v193, v201\n"
    "v_fma_mix_f32 v234, v188, v232, 0 op_sel_hi:[1,1,0]\n"
    "v_fma_mix_f32 v232, v188, v232, 0 op_sel:[1,1,0] op_sel_hi:[1,1,0]\n"
    "v_or_b32_e64 v194, v194, v202\n"
    "v_cvt_pk_bf16_f32 v188, v234, v232\n"
    "v_pk_fma_f16 v232, v192, v192, s48\n"
    "v_lshlrev_b32_sdwa v201, 1, v171 dst_sel:DWORD dst_unused:UNUSED_PAD src0_sel:DWORD src1_sel:WORD_1\n"
    "v_mfma_f32_16x16x32_bf16 v[104:107], a[192:195], v[188:191], v[104:107]\n"
    "ds_read_u16 v199, v199\n"
    "v_pk_mul_f16 v232, v192, v232\n"
    "v_mfma_f32_16x16x32_bf16 v[108:111], a[196:199], v[188:191], v[108:111]\n"
    "ds_read_u16_d16_hi v207, v207\n"
    "v_exp_f16_e32 v234, v232\n"
    "v_mfma_f32_16x16x32_bf16 v[120:123], a[200:203], v[188:191], v[120:123]\n"
    "s_waitcnt lgkmcnt(7)\n"
    "v_or_b32_e32 v195, v195, v203\n"
    "v_exp_f16_sdwa v234, v232 dst_sel:WORD_1 dst_unused:UNUSED_PRESERVE src0_sel:WORD_1\n"
    "v_smfmac_f32_16x16x64_bf16 v[64:67], v[216:219], v[208:215], v240\n"
    "s_waitcnt lgkmcnt(6)\n"
    "ds_read_b128 v[216:219], v252 offset:21504\n"
    "ds_read_b128 a[240:243], v244 offset:36864\n"
    "v_smfmac_f32_16x16x64_bf16 v[68:71], v[220:223], v[208:215], v240 abid:1\n"
    "ds_read_b128 v[208:211], v250 offset:640\n"
    "v_mfma_f32_16x16x32_bf16 v[132:135], a[244:247], v[184:187], v[132:135]\n"
    "ds_read_b128 v[212:215], v250 offset:960\n"
    "v_pk_fma_f16 v234, v234, s49, s49\n"
    "v_mfma_f32_16x16x32_bf16 v[124:127], a[204:207], v[188:191], v[124:127]\n"
    "ds_read_b128 v[220:223], v252 offset:21840\n"
    "v_lshlrev_b32_sdwa v184, 1, v168 dst_sel:DWORD dst_unused:UNUSED_PAD src0_sel:DWORD src1_sel:WORD_1\n"
    "v_mfma_f32_16x16x32_bf16 v[136:139], v[236:239], v[188:191], v[136:139]\n"
    "ds_read_u16 v184, v184\n"
    "v_lshlrev_b32_sdwa v185, 1, v170 dst_sel:DWORD dst_unused:UNUSED_PAD src0_sel:DWORD src1_sel:WORD_1\n"
    "v_mfma_f32_16x16x32_bf16 v[140:143], a[244:247], v[188:191], v[140:143]\n"
    "s_waitcnt lgkmcnt(10)\n"
    "v_or_b32_e32 v197, v197, v205\n"
    "v_lshlrev_b32_sdwa v186, 1, v172 dst_sel:DWORD dst_unused:UNUSED_PAD src0_sel:DWORD src1_sel:WORD_1\n"
    "v_mfma_f32_16x16x32_bf16 v[168:171], a[88:91], v[144:147], 0\n"
    "ds_read_u16_d16_hi v200, v200\n"
    "v_lshlrev_b32_sdwa v202, 1, v173 dst_sel:DWORD dst_unused:UNUSED_PAD src0_sel:DWORD src1_sel:WORD_1\n"
    "v_mfma_f32_16x16x32_bf16 v[168:171], a[136:139], v[148:151], v[168:171]\n"
    "v_lshlrev_b32_sdwa v187, 1, v174 dst_sel:DWORD dst_unused:UNUSED_PAD src0_sel:DWORD src1_sel:WORD_1\n"
    "v_lshlrev_b32_sdwa v203, 1, v175 dst_sel:DWORD dst_unused:UNUSED_PAD src0_sel:DWORD src1_sel:WORD_1\n"
    "v_rcp_f16_e32 v232, v234\n"
    "v_mfma_f32_16x16x32_bf16 v[168:171], a[52:55], v[152:155], v[168:171]\n"
    "s_waitcnt lgkmcnt(9)\n"
    "ds_read_u16 v185, v185\n"
    "v_or_b32_e64 v198, v198, v206\n"
    "v_rcp_f16_sdwa v232, v234 dst_sel:WORD_1 dst_unused:UNUSED_PRESERVE src0_sel:WORD_1\n"
    "v_mfma_f32_16x16x32_bf16 v[172:175], a[92:95], v[144:147], 0\n"
    "ds_read_u16_d16_hi v201, v201\n"
    "v_lshlrev_b32_sdwa v188, 1, v176 dst_sel:DWORD dst_unused:UNUSED_PAD src0_sel:DWORD src1_sel:WORD_1\n"
    "v_mfma_f32_16x16x32_bf16 v[172:175], a[140:143], v[148:151], v[172:175]\n"
    "ds_read_u16 v186, v186\n"
    "v_lshlrev_b32_sdwa v204, 1, v177 dst_sel:DWORD dst_unused:UNUSED_PAD src0_sel:DWORD src1_sel:WORD_1\n"
    "v_lshlrev_b32_sdwa v189, 1, v178 dst_sel:DWORD dst_unused:UNUSED_PAD src0_sel:DWORD src1_sel:WORD_1\n"
    "v_lshlrev_b32_sdwa v205, 1, v179 dst_sel:DWORD dst_unused:UNUSED_PAD src0_sel:DWORD src1_sel:WORD_1\n"
    "v_lshlrev_b32_sdwa v190, 1, v180 dst_sel:DWORD dst_unused:UNUSED_PAD src0_sel:DWORD src1_sel:WORD_1\n"
    "v_lshlrev_b32_sdwa v206, 1, v181 dst_sel:DWORD dst_unused:UNUSED_PAD src0_sel:DWORD src1_sel:WORD_1\n"
    "v_lshlrev_b32_sdwa v191, 1, v182 dst_sel:DWORD dst_unused:UNUSED_PAD src0_sel:DWORD src1_sel:WORD_1\n"
    "v_fma_mix_f32 v234, v192, v232, 0 op_sel_hi:[1,1,0]\n"
    "s_waitcnt lgkmcnt(7)\n"
    "v_mfma_f32_16x16x32_bf16 v[172:175], a[240:243], v[152:155], v[172:175]\n"
    "s_nop 0\n"
    "ds_read_b128 v[236:239], v244 offset:20480\n"
    "ds_read_u16_d16_hi v202, v202\n"
    "s_waitcnt lgkmcnt(8)\n"
    "v_smfmac_f32_16x16x64_bf16 v[64:67], v[216:219], v[208:215], v240\n"
    "s_waitcnt lgkmcnt(7)\n"
    "v_or_b32_e64 v199, v199, v207\n"
    "v_smfmac_f32_16x16x64_bf16 v[68:71], v[220:223], v[208:215], v240 abid:1\n"
    "ds_read_b128 v[208:211], v250 offset:80\n"
    "ds_read_b128 v[212:215], v250 offset:400\n"
    "v_lshlrev_b32_sdwa v207, 1, v183 dst_sel:DWORD dst_unused:UNUSED_PAD src0_sel:DWORD src1_sel:WORD_1\n"
    "v_fma_mix_f32 v232, v192, v232, 0 op_sel:[1,1,0] op_sel_hi:[1,1,0]\n"
    "v_mfma_f32_16x16x32_bf16 v[176:179], a[88:91], v[156:159], 0\n"
    "ds_read_b128 v[216:219], v251 offset:22176\n"
    "v_cvt_pk_bf16_f32 v192, v234, v232\n"
    "v_pk_fma_f16 v232, v196, v196, s48\n"
    "v_lshlrev_b32_e64 v254, 3, v244\n"
    "v_pk_mul_f16 v232, v196, v232\n"
    "v_pk_add_f32 v[224:225], v[224:225], v[64:65]\n"
    "v_pk_add_f32 v[226:227], v[226:227], v[66:67]\n"
    "v_pk_fma_f32 v[228:229], v[64:65], v[64:65], v[228:229]\n"
    "v_pk_fma_f32 v[230:231], v[66:67], v[66:67], v[230:231]\n"
    "v_pk_add_f32 v[224:225], v[224:225], v[68:69]\n"
    "v_pk_add_f32 v[226:227], v[226:227], v[70:71]\n"
    "v_pk_fma_f32 v[228:229], v[68:69], v[68:69], v[228:229]\n"
    "v_pk_fma_f32 v[230:231], v[70:71], v[70:71], v[230:231]\n"
    "v_mfma_f32_16x16x32_bf16 v[176:179], a[136:139], v[160:163], v[176:179]\n"
    "s_waitcnt lgkmcnt(7)\n"
    "ds_read_b128 v[220:223], v251 offset:22512\n"
    "ds_read_u16 v187, v187\n"
    "v_mfma_f32_16x16x32_bf16 v[176:179], a[52:55], v[164:167], v[176:179]\n"
    "s_nop 0\n"
    "ds_read_b128 a[244:247], v244 offset:22528\n"
    "ds_read_u16_d16_hi v203, v203\n"
    "v_mfma_f32_16x16x32_bf16 v[180:183], a[92:95], v[156:159], 0\n"
    "s_waitcnt lgkmcnt(10)\n"
    "v_exp_f16_e32 v234, v232\n"
    "v_mfma_f32_16x16x32_bf16 v[180:183], a[140:143], v[160:163], v[180:183]\n"
    "ds_read_u16 v188, v188\n"
    "v_exp_f16_sdwa v234, v232 dst_sel:WORD_1 dst_unused:UNUSED_PRESERVE src0_sel:WORD_1\n"
    "v_mfma_f32_16x16x32_bf16 v[180:183], a[240:243], v[164:167], v[180:183]\n"
    "ds_read_u16_d16_hi v204, v204\n"
    "v_pk_fma_f16 v234, v234, s49, s49\n"
    "v_mfma_f32_16x16x32_bf16 v[96:99], a[208:211], v[192:195], v[96:99]\n"
    "v_rcp_f16_e32 v232, v234\n"
    "v_mfma_f32_16x16x32_bf16 v[100:103], a[212:215], v[192:195], v[100:103]\n"
    "s_waitcnt lgkmcnt(8)\n"
    "ds_read_u16 v189, v189\n"
    "v_rcp_f16_sdwa v232, v234 dst_sel:WORD_1 dst_unused:UNUSED_PRESERVE src0_sel:WORD_1\n"
    "v_mfma_f32_16x16x32_bf16 v[112:115], a[216:219], v[192:195], v[112:115]\n"
    "s_mov_b32 exec_hi, 0\n"
    "buffer_load_dword v233, v254, s[72:75], s53 offen sc1\n"
    "s_mov_b32 exec_hi, -1\n"
    "v_mfma_f32_16x16x32_bf16 v[116:119], a[220:223], v[192:195], v[116:119]\n"
    "s_waitcnt lgkmcnt(7)\n"
    "ds_read_u16_d16_hi v205, v205\n"
    "v_fma_mix_f32 v234, v196, v232, 0 op_sel_hi:[1,1,0]\n"
    "v_smfmac_f32_16x16x64_bf16 v[72:75], v[216:219], v[208:215], v240\n"
    "s_waitcnt lgkmcnt(7)\n"
    "ds_read_b128 v[216:219], v252 offset:22176\n"
    "v_smfmac_f32_16x16x64_bf16 v[76:79], v[220:223], v[208:215], v240 abid:1\n"
    "s_add_u32 s53, s53, 393216\n"
    "ds_read_b128 v[208:211], v250 offset:720\n"
    "v_mfma_f32_16x16x32_bf16 v[128:131], v[236:239], v[192:195], v[128:131]\n"
    "ds_read_b128 v[212:215], v250 offset:1040\n"
    "v_fma_mix_f32 v232, v196, v232, 0 op_sel:[1,1,0] op_sel_hi:[1,1,0]\n"
    "s_waitcnt lgkmcnt(8)\n"
    "v_mfma_f32_16x16x32_bf16 v[132:135], a[244:247], v[192:195], v[132:135]\n"
    "s_nop 0\n"
    "ds_read_b128 v[220:223], v252 offset:22512\n"
    "v_cvt_pk_bf16_f32 v196, v234, v232\n"
    "v_or_b32_e64 v184, v184, v200\n"
    "v_or_b32_e32 v185, v185, v201\n"
    "v_mfma_f32_16x16x32_bf16 v[104:107], a[208:211], v[196:199], v[104:107]\n"
    "ds_read_u16 v190, v190\n"
    "v_cvt_pk_f16_f32 v192, v168, v169\n"
    "v_mfma_f32_16x16x32_bf16 v[108:111], a[212:215], v[196:199], v[108:111]\n"
    "s_waitcnt lgkmcnt(9)\n"
    "ds_read_u16_d16_hi v206, v206\n"
    "v_lshlrev_b32_sdwa v193, 1, v170 dst_sel:DWORD dst_unused:UNUSED_PAD src0_sel:DWORD src1_sel:WORD_1\n"
    "v_mfma_f32_16x16x32_bf16 v[120:123], a[216:219], v[196:199], v[120:123]\n"
    "v_lshlrev_b32_sdwa v201, 1, v171 dst_sel:DWORD dst_unused:UNUSED_PAD src0_sel:DWORD src1_sel:WORD_1\n"
    "v_lshlrev_b32_sdwa v194, 1, v172 dst_sel:DWORD dst_unused:UNUSED_PAD src0_sel:DWORD src1_sel:WORD_1\n"
    "v_mfma_f32_16x16x32_bf16 v[124:127], a[220:223], v[196:199], v[124:127]\n"
    "ds_read_u16 v191, v191\n"
    "v_or_b32_e32 v186, v186, v202\n"
    "v_mfma_f32_16x16x32_bf16 v[136:139], v[236:239], v[196:199], v[136:139]\n"
    "s_waitcnt lgkmcnt(9)\n"
    "v_lshlrev_b32_sdwa v202, 1, v173 dst_sel:DWORD dst_unused:UNUSED_PAD src0_sel:DWORD src1_sel:WORD_1\n"
    "v_lshlrev_b32_sdwa v195, 1, v174 dst_sel:DWORD dst_unused:UNUSED_PAD src0_sel:DWORD src1_sel:WORD_1\n"
    "v_mfma_f32_16x16x32_bf16 v[140:143], a[244:247], v[196:199], v[140:143]\n"
    "ds_read_u16_d16_hi v207, v207\n"
    "v_cvt_pk_f16_f32 v196, v176, v177\n"
    "v_mfma_f32_16x16x32_bf16 v[168:171], a[96:99], v[144:147], 0\n"
    "v_lshlrev_b32_sdwa v197, 1, v178 dst_sel:DWORD dst_unused:UNUSED_PAD src0_sel:DWORD src1_sel:WORD_1\n"
    "v_lshlrev_b32_sdwa v198, 1, v180 dst_sel:DWORD dst_unused:UNUSED_PAD src0_sel:DWORD src1_sel:WORD_1\n"
    "v_mfma_f32_16x16x32_bf16 v[168:171], a[144:147], v[148:151], v[168:171]\n"
    "ds_read_u16 v193, v193\n"
    "v_lshlrev_b32_sdwa v199, 1, v182 dst_sel:DWORD dst_unused:UNUSED_PAD src0_sel:DWORD src1_sel:WORD_1\n"
    "v_mfma_f32_16x16x32_bf16 v[168:171], a[56:59], v[152:155], v[168:171]\n"
    "s_waitcnt lgkmcnt(8)\n"
    "ds_read_u16_d16_hi v201, v201\n"
    "v_or_b32_e32 v187, v187, v203\n"
    "v_lshlrev_b32_sdwa v203, 1, v175 dst_sel:DWORD dst_unused:UNUSED_PAD src0_sel:DWORD src1_sel:WORD_1\n"
    "v_mfma_f32_16x16x32_bf16 v[172:175], a[100:103], v[144:147], 0\n"
    "s_waitcnt lgkmcnt(7)\n"
    "v_or_b32_e32 v188, v188, v204\n"
    "v_or_b32_e32 v189, v189, v205\n"
    "v_smfmac_f32_16x16x64_bf16 v[72:75], v[216:219], v[208:215], v240\n"
    "s_waitcnt lgkmcnt(6)\n"
    "ds_read_b128 v[216:219], v251 offset:22848\n"
    "ds_read_u16 v194, v194\n"
    "v_smfmac_f32_16x16x64_bf16 v[76:79], v[220:223], v[208:215], v240 abid:1\n"
    "v_lshlrev_b32_sdwa v205, 1, v179 dst_sel:DWORD dst_unused:UNUSED_PAD src0_sel:DWORD src1_sel:WORD_1\n"
    "v_mfma_f32_16x16x32_bf16 v[172:175], a[148:151], v[148:151], v[172:175]\n"
    "ds_read_b128 v[208:211], v250 offset:160\n"
    "v_cvt_pk_bf16_f32 v96, v96, v97\n"
    "v_cvt_pk_bf16_f32 v97, v98, v99\n"
    "v_cvt_pk_bf16_f32 v98, v100, v101\n"
    "ds_read_b128 v[212:215], v250 offset:480\n"
    "v_pk_add_f32 v[224:225], v[224:225], v[72:73]\n"
    "v_pk_add_f32 v[226:227], v[226:227], v[74:75]\n"
    "v_pk_fma_f32 v[228:229], v[72:73], v[72:73], v[228:229]\n"
    "v_pk_fma_f32 v[230:231], v[74:75], v[74:75], v[230:231]\n"
    "v_pk_add_f32 v[224:225], v[224:225], v[76:77]\n"
    "v_pk_add_f32 v[226:227], v[226:227], v[78:79]\n"
    "v_pk_fma_f32 v[228:229], v[76:77], v[76:77], v[228:229]\n"
    "v_pk_fma_f32 v[230:231], v[78:79], v[78:79], v[230:231]\n"
    "v_mfma_f32_16x16x32_bf16 v[172:175], a[60:63], v[152:155], v[172:175]\n"
    "s_waitcnt lgkmcnt(7)\n"
    "ds_read_b128 v[220:223], v251 offset:23184\n"
    "ds_read_u16_d16_hi v202, v202\n"
    "v_mfma_f32_16x16x32_bf16 v[176:179], a[96:99], v[156:159], 0\n"
    "s_nop 0\n"
    "ds_read_b128 v[236:239], v244 offset:0\n"
    "v_cvt_pk_bf16_f32 v99, v102, v103\n"
    "v_mfma_f32_16x16x32_bf16 v[176:179], a[144:147], v[160:163], v[176:179]\n"
    "s_waitcnt lgkmcnt(9)\n"
    "v_or_b32_e32 v190, v190, v206\n"
    "v_lshlrev_b32_sdwa v206, 1, v181 dst_sel:DWORD dst_unused:UNUSED_PAD src0_sel:DWORD src1_sel:WORD_1\n"
    "v_mfma_f32_16x16x32_bf16 v[176:179], a[56:59], v[164:167], v[176:179]\n"
    "ds_write_b128 v242, v[96:99] offset:24640\n"
    "v_mfma_f32_16x16x32_bf16 v[96:99], a[0:3], v[184:187], 0\n"
    "s_nop 0\n"
    "v_cvt_pk_bf16_f32 v112, v112, v113\n"
    "v_cvt_pk_bf16_f32 v113, v114, v115\n"
    "v_mfma_f32_16x16x32_bf16 v[100:103], a[4:7], v[184:187], 0\n"
    "s_waitcnt lgkmcnt(7)\n"
    "v_cvt_pk_bf16_f32 v114, v116, v117\n"
    "v_cvt_pk_bf16_f32 v115, v118, v119\n"
    "ds_write_b128 v242, v[112:115] offset:26688\n"
    "v_mfma_f32_16x16x32_bf16 v[112:115], a[8:11], v[184:187], 0\n"
    "v_cvt_pk_bf16_f32 v128, v128, v129\n"
    "v_cvt_pk_bf16_f32 v129, v130, v131\n"
    "s_waitcnt lgkmcnt(5)\n"
    "v_smfmac_f32_16x16x64_bf16 v[80:83], v[216:219], v[208:215], v240\n"
    "s_waitcnt lgkmcnt(4)\n"
    "ds_read_b128 v[216:219], v252 offset:22848\n"
    "ds_read_u16 v195, v195\n"
    "v_smfmac_f32_16x16x64_bf16 v[84:87], v[220:223], v[208:215], v240 abid:1\n"
    "ds_read_b128 v[208:211], v250 offset:800\n"
    "ds_read_b128 v[212:215], v250 offset:1120\n"
    "v_or_b32_e64 v191, v191, v207\n"
    "v_mfma_f32_16x16x32_bf16 v[116:119], a[12:15], v[184:187], 0\n"
    "ds_read_b128 v[220:223], v252 offset:23184\n"
    "ds_read_u16_d16_hi v203, v203\n"
    "v_lshlrev_b32_sdwa v207, 1, v183 dst_sel:DWORD dst_unused:UNUSED_PAD src0_sel:DWORD src1_sel:WORD_1\n"
    "v_mfma_f32_16x16x32_bf16 v[180:183], a[100:103], v[156:159], 0\n"
    "s_waitcnt lgkmcnt(7)\n"
    "ds_read_b128 a[244:247], v244 offset:2048\n"
    "v_cvt_pk_bf16_f32 v130, v132, v133\n"
    "v_mfma_f32_16x16x32_bf16 v[180:183], a[148:151], v[160:163], v[180:183]\n"
    "s_nop 0\n"
    "ds_read_u16 v197, v197\n"
    "v_or_b32_e64 v193, v193, v201\n"
    "v_mfma_f32_16x16x32_bf16 v[180:183], a[60:63], v[164:167], v[180:183]\n"
    "v_cvt_pk_bf16_f32 v131, v134, v135\n"
    "v_cvt_pk_bf16_f32 v104, v104, v105\n"
    "v_cvt_pk_bf16_f32 v105, v106, v107\n"
    "v_cvt_pk_bf16_f32 v106, v108, v109\n"
    "v_cvt_pk_bf16_f32 v107, v110, v111\n"
    "v_mfma_f32_16x16x32_bf16 v[108:111], a[4:7], v[188:191], 0\n"
    "v_or_b32_e64 v194, v194, v202\n"
    "ds_write_b128 v242, v[128:131] offset:28736\n"
    "v_mfma_f32_16x16x32_bf16 v[128:131], v[236:239], v[184:187], 0\n"
    "v_cvt_pk_bf16_f32 v120, v120, v121\n"
    "v_cvt_pk_bf16_f32 v121, v122, v123\n"
    "v_cvt_pk_bf16_f32 v122, v124, v125\n"
    "v_cvt_pk_bf16_f32 v123, v126, v127\n"
    "ds_write_b128 v242, v[104:107] offset:25664\n"
    "v_mfma_f32_16x16x32_bf16 v[104:107], a[0:3], v[188:191], 0\n"
    "s_waitcnt lgkmcnt(5)\n"
    "v_add_u32_e32 v254, s54, v242\n"
    "v_cvt_pk_bf16_f32 v136, v136, v137\n"
    "v_smfmac_f32_16x16x64_bf16 v[80:83], v[216:219], v[208:215], v240\n"
    "ds_read_b128 v[216:219], v251 offset:23520\n"
    "ds_write_b128 v242, v[120:123] offset:27712\n"
    "v_smfmac_f32_16x16x64_bf16 v[84:87], v[220:223], v[208:215], v240 abid:1\n"
    "s_waitcnt lgkmcnt(5)\n"
    "v_pk_fma_f16 v232, v192, v192, s48\n"
    "v_mfma_f32_16x16x32_bf16 v[132:135], a[244:247], v[184:187], 0\n"
    "s_nop 0\n"
    "v_cvt_pk_bf16_f32 v137, v138, v139\n"
    "v_cvt_pk_bf16_f32 v138, v140, v141\n"
    "v_or_b32_e64 v195, v195, v203\n"
    "ds_read_b128 v[208:211], v250 offset:240\n"
    "ds_read_b128 v[212:215], v250 offset:560\n"
    "v_pk_add_f32 v[224:225], v[224:225], v[80:81]\n"
    "v_pk_add_f32 v[226:227], v[226:227], v[82:83]\n"
    "v_pk_fma_f32 v[228:229], v[80:81], v[80:81], v[228:229]\n"
    "v_pk_fma_f32 v[230:231], v[82:83], v[82:83], v[230:231]\n"
    "v_pk_add_f32 v[224:225], v[224:225], v[84:85]\n"
    "v_pk_add_f32 v[226:227], v[226:227], v[86:87]\n"
    "v_pk_fma_f32 v[228:229], v[84:85], v[84:85], v[228:229]\n"
    "v_pk_fma_f32 v[230:231], v[86:87], v[86:87], v[230:231]\n"
    "v_mfma_f32_16x16x32_bf16 v[120:123], a[8:11], v[188:191], 0\n"
    "ds_read_b128 v[220:223], v251 offset:23856\n"
    "ds_read_u16_d16_hi v205, v205\n"
    "v_mfma_f32_16x16x32_bf16 v[124:127], a[12:15], v[188:191], 0\n"
    "ds_read_u16 v198, v198\n"
    "ds_read_u16_d16_hi v206, v206\n"
    "v_cvt_pk_bf16_f32 v139, v142, v143\n"
    "v_pk_mul_f16 v232, v192, v232\n"
    "v_cvt_pk_f16_f32 v184, v168, v169\n"
    "v_exp_f16_e32 v234, v232\n"
    "v_mfma_f32_16x16x32_bf16 v[140:143], a[244:247], v[188:191], 0\n"
    "v_exp_f16_sdwa v234, v232 dst_sel:WORD_1 dst_unused:UNUSED_PRESERVE src0_sel:WORD_1\n"
    "ds_write_b128 v242, v[136:139] offset:29760\n"
    "v_mfma_f32_16x16x32_bf16 v[136:139], v[236:239], v[188:191], 0\n"
    "s_waitcnt lgkmcnt(5)\n"
    "v_pk_fma_f16 v234, v234, s49, s49\n"
    "v_lshlrev_b32_sdwa v185, 1, v170 dst_sel:DWORD dst_unused:UNUSED_PAD src0_sel:DWORD src1_sel:WORD_1\n"
    "v_smfmac_f32_16x16x64_bf16 v[88:91], v[216:219], v[208:215], v240\n"
    "ds_read_b128 v[216:219], v252 offset:23520\n"
    "ds_read_u16 v199, v199\n"
    "v_lshlrev_b32_sdwa v201, 1, v171 dst_sel:DWORD dst_unused:UNUSED_PAD src0_sel:DWORD src1_sel:WORD_1\n"
    "v_mfma_f32_16x16x32_bf16 v[168:171], a[104:107], v[144:147], 0\n"
    "s_waitcnt lgkmcnt(6)\n"
    "ds_read_u16_d16_hi v207, v207\n"
    "ds_read_u16 v185, v185\n"
    "v_smfmac_f32_16x16x64_bf16 v[92:95], v[220:223], v[208:215], v240 abid:1\n"
    "s_nop 0\n"
    "v_lshlrev_b32_sdwa v186, 1, v172 dst_sel:DWORD dst_unused:UNUSED_PAD src0_sel:DWORD src1_sel:WORD_1\n"
    "v_mfma_f32_16x16x32_bf16 v[168:171], a[152:155], v[148:151], v[168:171]\n"
    "ds_read_b128 v[208:211], v250 offset:880\n"
    "v_lshlrev_b32_sdwa v202, 1, v173 dst_sel:DWORD dst_unused:UNUSED_PAD src0_sel:DWORD src1_sel:WORD_1\n"
    "s_waitcnt lgkmcnt(8)\n"
    "v_or_b32_e32 v197, v197, v205\n"
    "ds_read_b128 v[212:215], v250 offset:1200\n"
    "v_lshlrev_b32_sdwa v187, 1, v174 dst_sel:DWORD dst_unused:UNUSED_PAD src0_sel:DWORD src1_sel:WORD_1\n"
    "ds_read_b128 v[220:223], v252 offset:23856\n"
    "v_lshlrev_b32_sdwa v203, 1, v175 dst_sel:DWORD dst_unused:UNUSED_PAD src0_sel:DWORD src1_sel:WORD_1\n"
    "v_rcp_f16_e32 v232, v234\n"
    "v_mfma_f32_16x16x32_bf16 v[168:171], a[64:67], v[152:155], v[168:171]\n"
    "s_waitcnt lgkmcnt(7)\n"
    "ds_read_u16_d16_hi v201, v201\n"
    "v_or_b32_e64 v198, v198, v206\n"
    "v_rcp_f16_sdwa v232, v234 dst_sel:WORD_1 dst_unused:UNUSED_PRESERVE src0_sel:WORD_1\n"
    "v_mfma_f32_16x16x32_bf16 v[172:175], a[108:111], v[144:147], 0\n"
    "ds_read_u16 v186, v186\n"
    "v_cvt_pk_f16_f32 v188, v176, v177\n"
    "v_mfma_f32_16x16x32_bf16 v[172:175], a[156:159], v[148:151], v[172:175]\n"
    "ds_read_u16_d16_hi v202, v202\n"
    "v_lshlrev_b32_sdwa v189, 1, v178 dst_sel:DWORD dst_unused:UNUSED_PAD src0_sel:DWORD src1_sel:WORD_1\n"
    "v_lshlrev_b32_sdwa v205, 1, v179 dst_sel:DWORD dst_unused:UNUSED_PAD src0_sel:DWORD src1_sel:WORD_1\n"
    "v_lshlrev_b32_sdwa v190, 1, v180 dst_sel:DWORD dst_unused:UNUSED_PAD src0_sel:DWORD src1_sel:WORD_1\n"
    "v_lshlrev_b32_sdwa v206, 1, v181 dst_sel:DWORD dst_unused:UNUSED_PAD src0_sel:DWORD src1_sel:WORD_1\n"
    "v_lshlrev_b32_sdwa v191, 1, v182 dst_sel:DWORD dst_unused:UNUSED_PAD src0_sel:DWORD src1_sel:WORD_1\n"
    "v_fma_mix_f32 v234, v192, v232, 0 op_sel_hi:[1,1,0]\n"
    "v_mfma_f32_16x16x32_bf16 v[172:175], a[68:71], v[152:155], v[172:175]\n"
    "ds_read_b128 v[236:239], v244 offset:4096\n"
    "v_fma_mix_f32 v232, v192, v232, 0 op_sel:[1,1,0] op_sel_hi:[1,1,0]\n"
    "v_mfma_f32_16x16x32_bf16 v[176:179], a[104:107], v[156:159], 0\n"
    "ds_read_u16 v187, v187\n"
    "v_cvt_pk_bf16_f32 v192, v234, v232\n"
    "v_mfma_f32_16x16x32_bf16 v[176:179], a[152:155], v[160:163], v[176:179]\n"
    "s_waitcnt lgkmcnt(9)\n"
    "v_or_b32_e32 v199, v199, v207\n"
    "v_lshlrev_b32_sdwa v207, 1, v183 dst_sel:DWORD dst_unused:UNUSED_PAD src0_sel:DWORD src1_sel:WORD_1\n"
    "v_mfma_f32_16x16x32_bf16 v[176:179], a[64:67], v[164:167], v[176:179]\n"
    "ds_read_u16_d16_hi v203, v203\n"
    "v_pk_fma_f16 v232, v196, v196, s48\n"
    "v_mfma_f32_16x16x32_bf16 v[180:183], a[108:111], v[156:159], 0\n"
    "ds_read_b128 a[244:247], v244 offset:6144\n"
    "v_pk_mul_f16 v232, v196, v232\n"
    "v_mfma_f32_16x16x32_bf16 v[180:183], a[156:159], v[160:163], v[180:183]\n"
    "s_waitcnt lgkmcnt(8)\n"
    "ds_read_u16 v189, v189\n"
    "v_exp_f16_e32 v234, v232\n"
    "v_smfmac_f32_16x16x64_bf16 v[88:91], v[216:219], v[208:215], v240\n"
    "s_waitcnt lgkmcnt(8)\n"
    "v_exp_f16_sdwa v234, v232 dst_sel:WORD_1 dst_unused:UNUSED_PRESERVE src0_sel:WORD_1\n"
    "v_smfmac_f32_16x16x64_bf16 v[92:95], v[220:223], v[208:215], v240 abid:1\n"
    "ds_read_u16_d16_hi v205, v205\n"
    "v_mfma_f32_16x16x32_bf16 v[180:183], a[68:71], v[164:167], v[180:183]\n"
    "s_waitcnt lgkmcnt(8)\n"
    "ds_read_u16 v190, v190\n"
    "v_pk_fma_f16 v234, v234, s49, s49\n"
    "v_or_b32_e64 v185, v185, v201\n"
    "v_rcp_f16_e64 v232, v234\n"
    "v_pk_add_f32 v[224:225], v[224:225], v[88:89]\n"
    "v_pk_add_f32 v[226:227], v[226:227], v[90:91]\n"
    "v_pk_fma_f32 v[228:229], v[88:89], v[88:89], v[228:229]\n"
    "v_pk_fma_f32 v[230:231], v[90:91], v[90:91], v[230:231]\n"
    "v_pk_add_f32 v[224:225], v[224:225], v[92:93]\n"
    "v_pk_add_f32 v[226:227], v[226:227], v[94:95]\n"
    "v_pk_fma_f32 v[228:229], v[92:93], v[92:93], v[228:229]\n"
    "v_pk_fma_f32 v[230:231], v[94:95], v[94:95], v[230:231]\n"
    "v_mfma_f32_16x16x32_bf16 v[96:99], a[16:19], v[192:195], v[96:99]\n"
    "s_waitcnt lgkmcnt(6)\n"
    "ds_write_b128 v254, v[224:227]\n"
    "v_mfma_f32_16x16x32_bf16 v[100:103], a[20:23], v[192:195], v[100:103]\n"
    "s_nop 0\n"
    "v_rcp_f16_sdwa v232, v234 dst_sel:WORD_1 dst_unused:UNUSED_PRESERVE src0_sel:WORD_1\n"
    "v_mfma_f32_16x16x32_bf16 v[112:115], a[24:27], v[192:195], v[112:115]\n"
    "v_fma_mix_f32 v234, v196, v232, 0 op_sel_hi:[1,1,0]\n"
    "v_fma_mix_f32 v232, v196, v232, 0 op_sel:[1,1,0] op_sel_hi:[1,1,0]\n"
    "v_mfma_f32_16x16x32_bf16 v[116:119], a[28:31], v[192:195], v[116:119]\n"
    "s_waitcnt lgkmcnt(4)\n"
    "ds_read_u16_d16_hi v206, v206\n"
    "ds_write_b128 v254, v[228:231] offset:1024\n"
    "v_mfma_f32_16x16x32_bf16 v[128:131], v[236:239], v[192:195], v[128:131]\n"
    "v_lshrrev_b32_e32 v254, 2, v242\n"
    "v_or_b32_e64 v186, v186, v202\n"
    "v_mfma_f32_16x16x32_bf16 v[132:135], a[244:247], v[192:195], v[132:135]\n"
    "v_add_u32_e64 v254, s55, v254\n"
    "v_cvt_pk_bf16_f32 v196, v234, v232\n"
    "v_pk_fma_f16 v232, v184, v184, s48\n"
    "v_or_b32_e64 v187, v187, v203\n"
    "v_mfma_f32_16x16x32_bf16 v[104:107], a[16:19], v[196:199], v[104:107]\n"
    "ds_read_u16 v191, v191\n"
    "ds_read_u16_d16_hi v207, v207\n"
    "v_mfma_f32_16x16x32_bf16 v[108:111], a[20:23], v[196:199], v[108:111]\n"
    "s_waitcnt lgkmcnt(6)\n"
    "ds_read_b128 a[240:243], v244 offset:34816\n"
    "v_pk_mul_f16 v232, v184, v232\n"
    "v_mfma_f32_16x16x32_bf16 v[120:123], a[24:27], v[196:199], v[120:123]\n"
    "v_exp_f16_e32 v234, v232\n"
    "v_mfma_f32_16x16x32_bf16 v[124:127], a[28:31], v[196:199], v[124:127]\n"
    "s_waitcnt vmcnt(2)\n"
    "v_exp_f16_sdwa v234, v232 dst_sel:WORD_1 dst_unused:UNUSED_PRESERVE src0_sel:WORD_1\n"
    "v_mfma_f32_16x16x32_bf16 v[136:139], v[236:239], v[196:199], v[136:139]\n"
    "s_waitcnt vmcnt(1)\n"
    "ds_read_b128 v[236:239], v244 offset:8192\n"
    "v_or_b32_e64 v189, v189, v205\n"
    "v_mfma_f32_16x16x32_bf16 v[140:143], a[244:247], v[196:199], v[140:143]\n"
    "v_pk_fma_f16 v234, v234, s49, s49\n"
    "v_lshlrev_b32_sdwa v192, 1, v168 dst_sel:DWORD dst_unused:UNUSED_PAD src0_sel:DWORD src1_sel:WORD_1\n"
    "v_smfmac_f32_32x32x32_bf16 v[112:127], a[248:251], a[232:239], v241\n"
    "s_waitcnt lgkmcnt(4)\n"
    "s_barrier\n"
    "v_smfmac_f32_32x32x32_bf16 v[96:111], a[248:251], a[224:231], v241\n"
    "ds_read_b32 v224, v254 offset:0\n"
    "ds_read_b32 v225, v254 offset:6752\n"
    "v_lshlrev_b32_sdwa v200, 1, v169 dst_sel:DWORD dst_unused:UNUSED_PAD src0_sel:DWORD src1_sel:WORD_1\n"
    "ds_read_b32 v226, v254 offset:13504\n"
    "ds_read_b32 v227, v254 offset:20256\n"
    "ds_read_b32 v228, v254 offset:1024\n"
    "v_lshlrev_b32_sdwa v193, 1, v170 dst_sel:DWORD dst_unused:UNUSED_PAD src0_sel:DWORD src1_sel:WORD_1\n"
    "ds_read_b32 v229, v254 offset:7776\n"
    "ds_read_b32 v230, v254 offset:14528\n"
    "ds_read_b32 v231, v254 offset:21280\n"
    "v_lshlrev_b32_sdwa v201, 1, v171 dst_sel:DWORD dst_unused:UNUSED_PAD src0_sel:DWORD src1_sel:WORD_1\n"
    "v_mfma_f32_16x16x32_bf16 v[168:171], a[112:115], v[144:147], 0\n"
    "s_waitcnt lgkmcnt(8)\n"
    "ds_read_u16 v192, v192\n"
    "ds_read_u16_d16_hi v200, v200\n"
    "v_mfma_f32_16x16x32_bf16 v[168:171], a[160:163], v[148:151], v[168:171]\n"
    "s_nop 0\n"
    "ds_read_u16 v193, v193\n"
    "ds_read_u16_d16_hi v201, v201\n"
    "v_lshlrev_b32_sdwa v194, 1, v172 dst_sel:DWORD dst_unused:UNUSED_PAD src0_sel:DWORD src1_sel:WORD_1\n"
    "v_lshlrev_b32_sdwa v202, 1, v173 dst_sel:DWORD dst_unused:UNUSED_PAD src0_sel:DWORD src1_sel:WORD_1\n"
    "v_lshlrev_b32_sdwa v195, 1, v174 dst_sel:DWORD dst_unused:UNUSED_PAD src0_sel:DWORD src1_sel:WORD_1\n"
    "v_lshlrev_b32_sdwa v203, 1, v175 dst_sel:DWORD dst_unused:UNUSED_PAD src0_sel:DWORD src1_sel:WORD_1\n"
    "v_rcp_f16_e64 v232, v234\n"
    "v_mfma_f32_16x16x32_bf16 v[168:171], a[72:75], v[152:155], v[168:171]\n"
    "ds_read_u16 v194, v194\n"
    "buffer_load_dwordx4 a[224:227], v245, s[40:43], s58 offen offset:128\n"
    "s_waitcnt lgkmcnt(11)\n"
    "v_add_f32_e32 v224, v224, v225\n"
    "v_or_b32_e32 v190, v190, v206\n"
    "s_waitcnt lgkmcnt(9)\n"
    "v_add_f32_e32 v226, v226, v227\n"
    "v_rcp_f16_sdwa v232, v234 dst_sel:WORD_1 dst_unused:UNUSED_PRESERVE src0_sel:WORD_1\n"
    "v_mfma_f32_16x16x32_bf16 v[172:175], a[116:119], v[144:147], 0\n"
    "s_waitcnt lgkmcnt(6)\n"
    "ds_read_b128 a[232:235], v243 offset:24640\n"
    "ds_read_u16_d16_hi v202, v202\n"
    "v_mfma_f32_16x16x32_bf16 v[172:175], a[164:167], v[148:151], v[172:175]\n"
    "s_waitcnt lgkmcnt(7)\n"
    "v_add_f32_e32 v228, v228, v229\n"
    "v_add_f32_e64 v230, v230, v231\n"
    "v_add_f32_e64 v224, v224, v226\n"
    "v_add_f32_e64 v228, v228, v230\n"
    "v_mul_f32_e32 v224, 0x3c2aaaab, v224\n"
    "v_mul_f32_e32 v228, 0x3c2aaaab, v228\n"
    "v_lshlrev_b32_sdwa v196, 1, v176 dst_sel:DWORD dst_unused:UNUSED_PAD src0_sel:DWORD src1_sel:WORD_1\n"
    "v_fma_f32 v228, -v224, v224, v228\n"
    "v_lshlrev_b32_sdwa v204, 1, v177 dst_sel:DWORD dst_unused:UNUSED_PAD src0_sel:DWORD src1_sel:WORD_1\n"
    "v_max_f32_e64 v228, 0, v228\n"
    "v_lshlrev_b32_sdwa v197, 1, v178 dst_sel:DWORD dst_unused:UNUSED_PAD src0_sel:DWORD src1_sel:WORD_1\n"
    "v_add_f32_e32 v228, 0x3727c5ac, v228\n"
    "v_lshlrev_b32_sdwa v205, 1, v179 dst_sel:DWORD dst_unused:UNUSED_PAD src0_sel:DWORD src1_sel:WORD_1\n"
    "v_rsq_f32_e64 v228, v228\n"
    "v_lshlrev_b32_sdwa v198, 1, v180 dst_sel:DWORD dst_unused:UNUSED_PAD src0_sel:DWORD src1_sel:WORD_1\n"
    "v_mul_f32_e64 v228, s50, v228\n"
    "ds_write_b32 v254, v228\n"
    "ds_read_b128 a[236:239], v243 offset:25664\n"
    "v_mul_f32_e64 v224, v228, -v224\n"
    "ds_write_b32 v254, v224 offset:1024\n"
    "ds_read_u16 v195, v195\n"
    "v_add_u32_e64 v254, s56, v242\n"
    "v_lshlrev_b32_sdwa v206, 1, v181 dst_sel:DWORD dst_unused:UNUSED_PAD src0_sel:DWORD src1_sel:WORD_1\n"
    "v_lshlrev_b32_sdwa v199, 1, v182 dst_sel:DWORD dst_unused:UNUSED_PAD src0_sel:DWORD src1_sel:WORD_1\n"
    "v_fma_mix_f32 v234, v184, v232, 0 op_sel_hi:[1,1,0]\n"
    "v_mfma_f32_16x16x32_bf16 v[172:175], a[240:243], v[152:155], v[172:175]\n"
    "s_waitcnt lgkmcnt(7)\n"
    "ds_read_b128 a[244:247], v244 offset:10240\n"
    "buffer_load_dwordx4 a[228:231], v245, s[40:43], s89 offen offset:128\n"
    "v_or_b32_e32 v191, v191, v207\n"
    "v_lshlrev_b32_sdwa v207, 1, v183 dst_sel:DWORD dst_unused:UNUSED_PAD src0_sel:DWORD src1_sel:WORD_1\n"
    "v_fma_mix_f32 v232, v184, v232, 0 op_sel:[1,1,0] op_sel_hi:[1,1,0]\n"
    "v_mfma_f32_16x16x32_bf16 v[176:179], a[112:115], v[156:159], 0\n"
    "ds_read_u16_d16_hi v203, v203\n"
    "ds_read_u16 v196, v196\n"
    "v_cvt_pk_bf16_f32 v184, v234, v232\n"
    "v_mfma_f32_16x16x32_bf16 v[176:179], a[160:163], v[160:163], v[176:179]\n"
    "ds_read_u16_d16_hi v204, v204\n"
    "ds_read_u16 v197, v197\n"
    "v_mfma_f32_16x16x32_bf16 v[176:179], a[72:75], v[164:167], v[176:179]\n"
    "v_pk_fma_f16 v232, v188, v188, s48\n"
    "v_or_b32_e64 v192, v192, v200\n"
    "v_mfma_f32_16x16x32_bf16 v[180:183], a[116:119], v[156:159], 0\n"
    "s_waitcnt lgkmcnt(6)\n"
    "s_barrier\n"
    "v_smfmac_f32_32x32x32_bf16 v[96:111], a[248:251], a[232:239], v241\n"
    "ds_read_b128 v[224:227], v254\n"
    "ds_read_b128 v[228:231], v254 offset:1024\n"
    "v_lshlrev_b32_e64 v254, 3, v244\n"
    "v_pk_mul_f16 v232, v188, v232\n"
    "v_or_b32_e32 v193, v193, v201\n"
    "v_mfma_f32_16x16x32_bf16 v[180:183], a[164:167], v[160:163], v[180:183]\n"
    "s_mov_b32 exec_hi, 0\n"
    "buffer_load_dword v233, v254, s[72:75], s53 offen sc1\n"
    "s_mov_b32 exec_hi, -1\n"
    "v_exp_f16_e32 v234, v232\n"
    "v_mfma_f32_16x16x32_bf16 v[180:183], a[240:243], v[164:167], v[180:183]\n"
    "s_add_u32 s53, s53, 393216\n"
    "ds_read_u16_d16_hi v205, v205\n"
    "ds_read_u16 v198, v198\n"
    "v_exp_f16_sdwa v234, v232 dst_sel:WORD_1 dst_unused:UNUSED_PRESERVE src0_sel:WORD_1\n"
    "v_mfma_f32_16x16x32_bf16 v[96:99], a[32:35], v[184:187], v[96:99]\n"
    "ds_read_u16_d16_hi v206, v206\n"
    "ds_read_u16 v199, v199\n"
    "v_pk_fma_f16 v234, v234, s49, s49\n"
    "v_mfma_f32_16x16x32_bf16 v[100:103], a[36:39], v[184:187], v[100:103]\n"
    "s_waitcnt lgkmcnt(8)\n"
    "v_rcp_f16_e32 v232, v234\n"
    "v_mfma_f32_16x16x32_bf16 v[112:115], a[40:43], v[184:187], v[112:115]\n"
    "ds_read_u16_d16_hi v207, v207\n"
    "v_rcp_f16_sdwa v232, v234 dst_sel:WORD_1 dst_unused:UNUSED_PRESERVE src0_sel:WORD_1\n"
    "v_mfma_f32_16x16x32_bf16 v[116:119], a[44:47], v[184:187], v[116:119]\n"
    "s_waitcnt lgkmcnt(6)\n"
    "ds_read_b128 a[232:235], v243 offset:26688\n"
    "v_fma_mix_f32 v234, v188, v232, 0 op_sel_hi:[1,1,0]\n"
    "v_mfma_f32_16x16x32_bf16 v[128:131], v[236:239], v[184:187], v[128:131]\n"
    "s_waitcnt lgkmcnt(6)\n"
    "ds_read_b128 a[236:239], v243 offset:27712\n"
    "v_fma_mix_f32 v232, v188, v232, 0 op_sel:[1,1,0] op_sel_hi:[1,1,0]\n"
    "v_lshlrev_b32_sdwa v201, 1, v171 dst_sel:DWORD dst_unused:UNUSED_PAD src0_sel:DWORD src1_sel:WORD_1\n"
    "v_cvt_pk_bf16_f32 v188, v234, v232\n"
    "v_or_b32_e64 v194, v194, v202\n"
    "v_pk_fma_f32 v[64:65], v[64:65], v[224:225], v[228:229]\n"
    "v_pk_fma_f32 v[66:67], v[66:67], v[226:227], v[230:231]\n"
    "v_pk_fma_f32 v[68:69], v[68:69], v[224:225], v[228:229]\n"
    "v_pk_fma_f32 v[70:71], v[70:71], v[226:227], v[230:231]\n"
    "v_pk_fma_f32 v[72:73], v[72:73], v[224:225], v[228:229]\n"
    "v_pk_fma_f32 v[74:75], v[74:75], v[226:227], v[230:231]\n"
    "v_pk_fma_f32 v[76:77], v[76:77], v[224:225], v[228:229]\n"
    "v_pk_fma_f32 v[78:79], v[78:79], v[226:227], v[230:231]\n"
    "v_pk_fma_f32 v[80:81], v[80:81], v[224:225], v[228:229]\n"
    "v_pk_fma_f32 v[82:83], v[82:83], v[226:227], v[230:231]\n"
    "v_pk_fma_f32 v[84:85], v[84:85], v[224:225], v[228:229]\n"
    "v_pk_fma_f32 v[86:87], v[86:87], v[226:227], v[230:231]\n"
    "v_pk_fma_f32 v[88:89], v[88:89], v[224:225], v[228:229]\n"
    "v_pk_fma_f32 v[90:91], v[90:91], v[226:227], v[230:231]\n"
    "v_pk_fma_f32 v[92:93], v[92:93], v[224:225], v[228:229]\n"
    "v_pk_fma_f32 v[94:95], v[94:95], v[226:227], v[230:231]\n"
    "v_pk_fma_f32 v[0:1], v[0:1], v[224:225], v[228:229]\n"
    "v_pk_fma_f32 v[2:3], v[2:3], v[226:227], v[230:231]\n"
    "v_pk_fma_f32 v[4:5], v[4:5], v[224:225], v[228:229]\n"
    "v_pk_fma_f32 v[6:7], v[6:7], v[226:227], v[230:231]\n"
    "v_pk_fma_f32 v[8:9], v[8:9], v[224:225], v[228:229]\n"
    "v_pk_fma_f32 v[10:11], v[10:11], v[226:227], v[230:231]\n"
    "v_pk_fma_f32 v[12:13], v[12:13], v[224:225], v[228:229]\n"
    "v_pk_fma_f32 v[14:15], v[14:15], v[226:227], v[230:231]\n"
    "v_pk_fma_f32 v[16:17], v[16:17], v[224:225], v[228:229]\n"
    "v_pk_fma_f32 v[18:19], v[18:19], v[226:227], v[230:231]\n"
    "v_pk_fma_f32 v[20:21], v[20:21], v[224:225], v[228:229]\n"
    "v_pk_fma_f32 v[22:23], v[22:23], v[226:227], v[230:231]\n"
    "v_pk_fma_f32 v[24:25], v[24:25], v[224:225], v[228:229]\n"
    "v_pk_fma_f32 v[26:27], v[26:27], v[226:227], v[230:231]\n"
    "v_pk_fma_f32 v[28:29], v[28:29], v[224:225], v[228:229]\n"
    "v_pk_fma_f32 v[30:31], v[30:31], v[226:227], v[230:231]\n"
    "v_pk_fma_f32 v[32:33], v[32:33], v[224:225], v[228:229]\n"
    "v_pk_fma_f32 v[34:35], v[34:35], v[226:227], v[230:231]\n"
    "v_pk_fma_f32 v[36:37], v[36:37], v[224:225], v[228:229]\n"
    "v_pk_fma_f32 v[38:39], v[38:39], v[226:227], v[230:231]\n"
    "v_pk_fma_f32 v[40:41], v[40:41], v[224:225], v[228:229]\n"
    "v_pk_fma_f32 v[42:43], v[42:43], v[226:227], v[230:231]\n"
    "v_pk_fma_f32 v[44:45], v[44:45], v[224:225], v[228:229]\n"
    "v_pk_fma_f32 v[46:47], v[46:47], v[226:227], v[230:231]\n"
    "v_pk_fma_f32 v[48:49], v[48:49], v[224:225], v[228:229]\n"
    "v_pk_fma_f32 v[50:51], v[50:51], v[226:227], v[230:231]\n"
    "v_pk_fma_f32 v[52:53], v[52:53], v[224:225], v[228:229]\n"
    "v_pk_fma_f32 v[54:55], v[54:55], v[226:227], v[230:231]\n"
    "v_pk_fma_f32 v[56:57], v[56:57], v[224:225], v[228:229]\n"
    "v_pk_fma_f32 v[58:59], v[58:59], v[226:227], v[230:231]\n"
    "v_pk_fma_f32 v[60:61], v[60:61], v[224:225], v[228:229]\n"
    "v_pk_fma_f32 v[62:63], v[62:63], v[226:227], v[230:231]\n"
    "v_mfma_f32_16x16x32_bf16 v[132:135], a[244:247], v[184:187], v[132:135]\n"
    "s_waitcnt lgkmcnt(0)\n"
    "ds_read_b128 a[240:243], v244 offset:38912\n"
    "ds_read_u16_d16_hi v201, v201\n"
    "v_mfma_f32_16x16x32_bf16 v[104:107], a[32:35], v[188:191], v[104:107]\n"
    "s_nop 0\n"
    "v_cvt_pk_bf16_f32 v208, v64, v68\n"
    "v_cvt_pk_bf16_f32 v212, v65, v69\n"
    "v_mfma_f32_16x16x32_bf16 v[108:111], a[36:39], v[188:191], v[108:111]\n"
    "v_cvt_pk_bf16_f32 v216, v66, v70\n"
    "v_cvt_pk_bf16_f32 v220, v67, v71\n"
    "v_mfma_f32_16x16x32_bf16 v[120:123], a[40:43], v[188:191], v[120:123]\n"
    "v_cvt_pk_bf16_f32 v209, v72, v76\n"
    "v_cvt_pk_bf16_f32 v213, v73, v77\n"
    "v_mfma_f32_16x16x32_bf16 v[124:127], a[44:47], v[188:191], v[124:127]\n"
    "v_cvt_pk_bf16_f32 v217, v74, v78\n"
    "v_cvt_pk_bf16_f32 v221, v75, v79\n"
    "v_mfma_f32_16x16x32_bf16 v[136:139], v[236:239], v[188:191], v[136:139]\n"
    "ds_read_b128 v[236:239], v244 offset:12288\n"
    "v_cvt_pk_bf16_f32 v210, v80, v84\n"
    "v_mfma_f32_16x16x32_bf16 v[140:143], a[244:247], v[188:191], v[140:143]\n"
    "ds_read_b128 a[244:247], v244 offset:14336\n"
    "v_cvt_pk_bf16_f32 v214, v81, v85\n"
    "v_smfmac_f32_32x32x32_bf16 v[112:127], a[248:251], a[232:239], v241\n"
    "v_cvt_pk_bf16_f32 v218, v82, v86\n"
    "v_cvt_pk_bf16_f32 v222, v83, v87\n"
    "v_cvt_pk_bf16_f32 v211, v88, v92\n"
    "ds_write_b128 v253, v[208:211] offset:2048\n"
    "v_cvt_pk_bf16_f32 v215, v89, v93\n"
    "v_cvt_pk_bf16_f32 v219, v90, v94\n"
    "v_cvt_pk_bf16_f32 v223, v91, v95\n"
    "v_cvt_pk_bf16_f32 v80, v0, v4\n"
    "v_cvt_pk_bf16_f32 v84, v1, v5\n"
    "v_cvt_pk_bf16_f32 v88, v2, v6\n"
    "ds_write_b128 v253, v[212:215] offset:5120\n"
    "v_cvt_pk_bf16_f32 v92, v3, v7\n"
    "v_cvt_pk_bf16_f32 v81, v8, v12\n"
    "v_cvt_pk_bf16_f32 v85, v9, v13\n"
    "v_cvt_pk_bf16_f32 v89, v10, v14\n"
    "v_cvt_pk_bf16_f32 v93, v11, v15\n"
    "v_cvt_pk_bf16_f32 v82, v16, v20\n"
    "v_cvt_pk_bf16_f32 v86, v17, v21\n"
    "v_cvt_pk_bf16_f32 v90, v18, v22\n"
    "v_cvt_pk_bf16_f32 v94, v19, v23\n"
    "v_cvt_pk_bf16_f32 v83, v24, v28\n"
    "v_cvt_pk_f16_f32 v184, v168, v169\n"
    "v_cvt_pk_bf16_f32 v87, v25, v29\n"
    "v_lshlrev_b32_sdwa v185, 1, v170 dst_sel:DWORD dst_unused:UNUSED_PAD src0_sel:DWORD src1_sel:WORD_1\n"
    "v_mfma_f32_16x16x32_bf16 v[168:171], a[120:123], v[144:147], 0\n"
    "ds_read_u16 v185, v185\n"
    "ds_write_b128 v253, v[80:83] offset:0\n"
    "v_mfma_f32_16x16x32_bf16 v[168:171], a[168:171], v[148:151], v[168:171]\n"
    "v_lshlrev_b32_sdwa v186, 1, v172 dst_sel:DWORD dst_unused:UNUSED_PAD src0_sel:DWORD src1_sel:WORD_1\n"
    "v_cvt_pk_bf16_f32 v91, v26, v30\n"
    "v_mfma_f32_16x16x32_bf16 v[168:171], a[76:79], v[152:155], v[168:171]\n"
    "v_lshlrev_b32_sdwa v202, 1, v173 dst_sel:DWORD dst_unused:UNUSED_PAD src0_sel:DWORD src1_sel:WORD_1\n"
    "v_lshlrev_b32_sdwa v187, 1, v174 dst_sel:DWORD dst_unused:UNUSED_PAD src0_sel:DWORD src1_sel:WORD_1\n"
    "v_cvt_pk_bf16_f32 v95, v27, v31\n"
    "v_cvt_pk_f16_f32 v188, v176, v177\n"
    "v_lshlrev_b32_sdwa v189, 1, v178 dst_sel:DWORD dst_unused:UNUSED_PAD src0_sel:DWORD src1_sel:WORD_1\n"
    "v_lshlrev_b32_sdwa v190, 1, v180 dst_sel:DWORD dst_unused:UNUSED_PAD src0_sel:DWORD src1_sel:WORD_1\n"
    "v_lshlrev_b32_sdwa v191, 1, v182 dst_sel:DWORD dst_unused:UNUSED_PAD src0_sel:DWORD src1_sel:WORD_1\n"
    "v_or_b32_e64 v195, v195, v203\n"
    "v_lshlrev_b32_sdwa v203, 1, v175 dst_sel:DWORD dst_unused:UNUSED_PAD src0_sel:DWORD src1_sel:WORD_1\n"
    "v_mfma_f32_16x16x32_bf16 v[172:175], a[124:127], v[144:147], 0\n"
    "s_nop 0\n"
    "ds_read_u16 v186, v186\n"
    "ds_write_b128 v253, v[84:87] offset:3072\n"
    "v_mfma_f32_16x16x32_bf16 v[172:175], a[172:175], v[148:151], v[172:175]\n"
    "s_waitcnt lgkmcnt(4)\n"
    "v_cvt_pk_bf16_f32 v64, v32, v36\n"
    "v_cvt_pk_bf16_f32 v68, v33, v37\n"
    "v_mfma_f32_16x16x32_bf16 v[172:175], a[240:243], v[152:155], v[172:175]\n"
    "v_cvt_pk_bf16_f32 v72, v34, v38\n"
    "v_cvt_pk_bf16_f32 v76, v35, v39\n"
    "v_cvt_pk_bf16_f32 v65, v40, v44\n"
    "v_or_b32_e64 v196, v196, v204\n"
    "v_cvt_pk_bf16_f32 v69, v41, v45\n"
    "v_cvt_pk_bf16_f32 v73, v42, v46\n"
    "v_cvt_pk_bf16_f32 v77, v43, v47\n"
    "v_cvt_pk_bf16_f32 v66, v48, v52\n"
    "v_cvt_pk_bf16_f32 v70, v49, v53\n"
    "v_or_b32_e32 v197, v197, v205\n"
    "v_lshlrev_b32_sdwa v205, 1, v179 dst_sel:DWORD dst_unused:UNUSED_PAD src0_sel:DWORD src1_sel:WORD_1\n"
    "v_mfma_f32_16x16x32_bf16 v[176:179], a[120:123], v[156:159], 0\n"
    "s_waitcnt lgkmcnt(2)\n"
    "ds_read_u16_d16_hi v202, v202\n"
    "ds_read_u16 v187, v187\n"
    "v_mfma_f32_16x16x32_bf16 v[176:179], a[168:171], v[160:163], v[176:179]\n"
    "ds_read_u16_d16_hi v203, v203\n"
    "ds_read_u16 v189, v189\n"
    "v_mfma_f32_16x16x32_bf16 v[176:179], a[76:79], v[164:167], v[176:179]\n"
    "v_cvt_pk_bf16_f32 v74, v50, v54\n"
    "v_cvt_pk_bf16_f32 v78, v51, v55\n"
    "v_cvt_pk_bf16_f32 v67, v56, v60\n"
    "v_cvt_pk_bf16_f32 v71, v57, v61\n"
    "v_cvt_pk_bf16_f32 v75, v58, v62\n"
    "v_or_b32_e64 v198, v198, v206\n"
    "v_mfma_f32_16x16x32_bf16 v[96:99], a[176:179], v[192:195], v[96:99]\n"
    "v_lshlrev_b32_sdwa v206, 1, v181 dst_sel:DWORD dst_unused:UNUSED_PAD src0_sel:DWORD src1_sel:WORD_1\n"
    "v_cvt_pk_bf16_f32 v79, v59, v63\n"
    "v_mfma_f32_16x16x32_bf16 v[100:103], a[180:183], v[192:195], v[100:103]\n"
    "ds_write_b128 v253, v[64:67] offset:1024\n"
    "v_mfma_f32_16x16x32_bf16 v[112:115], a[184:187], v[192:195], v[112:115]\n"
    "v_or_b32_e64 v199, v199, v207\n"
    "v_lshlrev_b32_sdwa v207, 1, v183 dst_sel:DWORD dst_unused:UNUSED_PAD src0_sel:DWORD src1_sel:WORD_1\n"
    "v_mfma_f32_16x16x32_bf16 v[180:183], a[124:127], v[156:159], 0\n"
    "v_or_b32_e64 v185, v185, v201\n"
    "v_pk_fma_f16 v232, v184, v184, s48\n"
    "v_mfma_f32_16x16x32_bf16 v[180:183], a[172:175], v[160:163], v[180:183]\n"
    "ds_read_b128 a[232:235], v243 offset:28736\n"
    "ds_write_b128 v253, v[68:71] offset:4096\n"
    "v_mfma_f32_16x16x32_bf16 v[180:183], a[240:243], v[164:167], v[180:183]\n"
    "s_waitcnt lgkmcnt(6)\n"
    "v_or_b32_e32 v186, v186, v202\n"
    "v_pk_mul_f16 v232, v184, v232\n"
    "v_mfma_f32_16x16x32_bf16 v[116:119], a[188:191], v[192:195], v[116:119]\n"
    "v_exp_f16_e64 v234, v232\n"
    "v_mfma_f32_16x16x32_bf16 v[128:131], v[236:239], v[192:195], v[128:131]\n"
    "ds_read_u16_d16_hi v205, v205\n"
    "ds_read_b128 a[236:239], v243 offset:29760\n"
    "v_mfma_f32_16x16x32_bf16 v[132:135], a[244:247], v[192:195], v[132:135]\n"
    "s_waitcnt lgkmcnt(6)\n"
    "ds_read_u16 v190, v190\n"
    "ds_read_u16_d16_hi v206, v206\n"
    "v_mfma_f32_16x16x32_bf16 v[104:107], a[176:179], v[196:199], v[104:107]\n"
    "v_or_b32_e32 v187, v187, v203\n"
    "v_exp_f16_sdwa v234, v232 dst_sel:WORD_1 dst_unused:UNUSED_PRESERVE src0_sel:WORD_1\n"
    "v_mfma_f32_16x16x32_bf16 v[108:111], a[180:183], v[196:199], v[108:111]\n"
    "ds_read_u16 v191, v191\n"
    "v_pk_fma_f16 v234, v234, s49, s49\n"
    "v_mfma_f32_16x16x32_bf16 v[120:123], a[184:187], v[196:199], v[120:123]\n"
    "ds_read_u16_d16_hi v207, v207\n"
    "v_cvt_pk_f16_f32 v192, v168, v169\n"
    "v_mfma_f32_16x16x32_bf16 v[124:127], a[188:191], v[196:199], v[124:127]\n"
    "v_lshlrev_b32_sdwa v193, 1, v170 dst_sel:DWORD dst_unused:UNUSED_PAD src0_sel:DWORD src1_sel:WORD_1\n"
    "v_lshlrev_b32_sdwa v201, 1, v171 dst_sel:DWORD dst_unused:UNUSED_PAD src0_sel:DWORD src1_sel:WORD_1\n"
    "v_mfma_f32_16x16x32_bf16 v[136:139], v[236:239], v[196:199], v[136:139]\n"
    "s_waitcnt lgkmcnt(6)\n"
    "ds_read_b128 a[240:243], v244 offset:32768\n"
    "v_lshlrev_b32_sdwa v194, 1, v172 dst_sel:DWORD dst_unused:UNUSED_PAD src0_sel:DWORD src1_sel:WORD_1\n"
    "v_mfma_f32_16x16x32_bf16 v[140:143], a[244:247], v[196:199], v[140:143]\n"
    "s_barrier\n"
    "s_waitcnt lgkmcnt(5)\n"
    "v_or_b32_e32 v189, v189, v205\n"
    "v_lshlrev_b32_sdwa v202, 1, v173 dst_sel:DWORD dst_unused:UNUSED_PAD src0_sel:DWORD src1_sel:WORD_1\n"
    "v_lshlrev_b32_sdwa v195, 1, v174 dst_sel:DWORD dst_unused:UNUSED_PAD src0_sel:DWORD src1_sel:WORD_1\n"
    "v_lshlrev_b32_sdwa v203, 1, v175 dst_sel:DWORD dst_unused:UNUSED_PAD src0_sel:DWORD src1_sel:WORD_1\n"
    "v_smfmac_f32_32x32x32_bf16 v[128:143], a[248:251], a[232:239], v241\n"
    "ds_read_b128 v[144:147], v242 offset:0\n"
    "ds_read_b128 v[148:151], v242 offset:1024\n"
    "v_rcp_f16_e64 v232, v234\n"
    "v_mov_b32_e64 v64, v216\n"
    "v_rcp_f16_sdwa v232, v234 dst_sel:WORD_1 dst_unused:UNUSED_PRESERVE src0_sel:WORD_1\n"
    "v_cvt_pk_f16_f32 v196, v176, v177\n"
    "v_mov_b32_e32 v65, v217\n"
    "s_waitcnt lgkmcnt(5)\n"
    "v_or_b32_e64 v190, v190, v206\n"
    "v_lshlrev_b32_sdwa v197, 1, v178 dst_sel:DWORD dst_unused:UNUSED_PAD src0_sel:DWORD src1_sel:WORD_1\n"
    "v_mov_b32_e64 v66, v218\n"
    "v_lshlrev_b32_sdwa v205, 1, v179 dst_sel:DWORD dst_unused:UNUSED_PAD src0_sel:DWORD src1_sel:WORD_1\n"
    "v_mov_b32_e64 v67, v219\n"
    "v_lshlrev_b32_sdwa v198, 1, v180 dst_sel:DWORD dst_unused:UNUSED_PAD src0_sel:DWORD src1_sel:WORD_1\n"
    "v_mov_b32_e64 v68, v220\n"
    "v_lshlrev_b32_sdwa v206, 1, v181 dst_sel:DWORD dst_unused:UNUSED_PAD src0_sel:DWORD src1_sel:WORD_1\n"
    "v_mov_b32_e64 v69, v221\n"
    "v_lshlrev_b32_sdwa v199, 1, v182 dst_sel:DWORD dst_unused:UNUSED_PAD src0_sel:DWORD src1_sel:WORD_1\n"
    "v_mov_b32_e64 v70, v222\n"
    "v_mov_b32_e64 v71, v223\n"
    "v_fma_mix_f32 v234, v184, v232, 0 op_sel_hi:[1,1,0]\n"
    "s_waitcnt lgkmcnt(2)\n"
    "v_or_b32_e32 v191, v191, v207\n"
    "v_lshlrev_b32_sdwa v207, 1, v183 dst_sel:DWORD dst_unused:UNUSED_PAD src0_sel:DWORD src1_sel:WORD_1\n"
    "v_fma_mix_f32 v232, v184, v232, 0 op_sel:[1,1,0] op_sel_hi:[1,1,0]\n"
    "v_cvt_pk_bf16_f32 v184, v234, v232\n"
    "ds_read_b128 v[152:155], v242 offset:2048\n"
    "ds_read_b128 v[156:159], v242 offset:3072\n"
    "s_waitcnt lgkmcnt(3)\n"
    "v_mfma_f32_16x16x32_bf16 v[168:171], a[80:83], v[144:147], 0\n"
    "ds_read_b128 v[160:163], v242 offset:4096\n"
    "ds_read_b128 v[164:167], v242 offset:5120\n"
    "v_mfma_f32_16x16x32_bf16 v[172:175], a[84:87], v[144:147], 0\n"
    "s_waitcnt lgkmcnt(4)\n"
    "ds_read_u16 v193, v193\n"
    "ds_read_u16_d16_hi v201, v201\n"
    "v_mfma_f32_16x16x32_bf16 v[168:171], a[128:131], v[148:151], v[168:171]\n"
    "ds_read_u16 v194, v194\n"
    "v_pk_fma_f16 v232, v188, v188, s48\n"
    "v_mfma_f32_16x16x32_bf16 v[172:175], a[132:135], v[148:151], v[172:175]\n"
    "v_pk_mul_f16 v232, v188, v232\n"
    "v_exp_f16_e64 v234, v232\n"
    "ds_read_u16_d16_hi v202, v202\n"
    "v_exp_f16_sdwa v234, v232 dst_sel:WORD_1 dst_unused:UNUSED_PRESERVE src0_sel:WORD_1\n"
    "v_mfma_f32_16x16x32_bf16 v[96:99], a[192:195], v[184:187], v[96:99]\n"
    "ds_read_b128 v[236:239], v244 offset:16384\n"
    "v_pk_fma_f16 v234, v234, s49, s49\n"
    "v_mfma_f32_16x16x32_bf16 v[100:103], a[196:199], v[184:187], v[100:103]\n"
    "s_waitcnt lgkmcnt(8)\n"
    "ds_read_u16 v195, v195\n"
    "v_rcp_f16_e32 v232, v234\n"
    "v_mfma_f32_16x16x32_bf16 v[168:171], a[48:51], v[152:155], v[168:171]\n"
    "s_waitcnt lgkmcnt(8)\n"
    "v_rcp_f16_sdwa v232, v234 dst_sel:WORD_1 dst_unused:UNUSED_PRESERVE src0_sel:WORD_1\n"
    "v_mfma_f32_16x16x32_bf16 v[172:175], a[240:243], v[152:155], v[172:175]\n"
    "ds_read_u16_d16_hi v203, v203\n"
    "v_fma_mix_f32 v234, v188, v232, 0 op_sel_hi:[1,1,0]\n"
    "v_mfma_f32_16x16x32_bf16 v[176:179], a[80:83], v[156:159], 0\n"
    "s_waitcnt lgkmcnt(7)\n"
    "ds_read_b128 a[244:247], v244 offset:18432\n"
    "v_fma_mix_f32 v232, v188, v232, 0 op_sel:[1,1,0] op_sel_hi:[1,1,0]\n"
    "v_mfma_f32_16x16x32_bf16 v[176:179], a[128:131], v[160:163], v[176:179]\n"
    "ds_read_u16 v197, v197\n"
    "v_cvt_pk_bf16_f32 v188, v234, v232\n"
    "v_mfma_f32_16x16x32_bf16 v[176:179], a[48:51], v[164:167], v[176:179]\n"
    "v_pk_fma_f16 v232, v192, v192, s48\n"
    "v_pk_mul_f16 v232, v192, v232\n"
    "v_mfma_f32_16x16x32_bf16 v[180:183], a[84:87], v[156:159], 0\n"
    "ds_read_u16_d16_hi v205, v205\n"
    "v_exp_f16_e64 v234, v232\n"
    "v_mfma_f32_16x16x32_bf16 v[180:183], a[132:135], v[160:163], v[180:183]\n"
    "s_waitcnt lgkmcnt(8)\n"
    "v_or_b32_e32 v193, v193, v201\n"
    "v_exp_f16_sdwa v234, v232 dst_sel:WORD_1 dst_unused:UNUSED_PRESERVE src0_sel:WORD_1\n"
    "v_mfma_f32_16x16x32_bf16 v[180:183], a[240:243], v[164:167], v[180:183]\n"
    "ds_read_u16 v198, v198\n"
    "v_pk_fma_f16 v234, v234, s49, s49\n"
    "v_mfma_f32_16x16x32_bf16 v[112:115], a[200:203], v[184:187], v[112:115]\n"
    "ds_read_u16_d16_hi v206, v206\n"
    "v_rcp_f16_e64 v232, v234\n"
    "v_mfma_f32_16x16x32_bf16 v[116:119], a[204:207], v[184:187], v[116:119]\n"
    "s_waitcnt lgkmcnt(7)\n"
    "v_or_b32_e32 v194, v194, v202\n"
    "v_rcp_f16_sdwa v232, v234 dst_sel:WORD_1 dst_unused:UNUSED_PRESERVE src0_sel:WORD_1\n"
    "v_mfma_f32_16x16x32_bf16 v[128:131], v[236:239], v[184:187], v[128:131]\n"
    "ds_read_u16 v199, v199\n"
    "v_fma_mix_f32 v234, v192, v232, 0 op_sel_hi:[1,1,0]\n"
    "v_mfma_f32_16x16x32_bf16 v[104:107], a[192:195], v[188:191], v[104:107]\n"
    "ds_read_u16_d16_hi v207, v207\n"
    "v_fma_mix_f32 v232, v192, v232, 0 op_sel:[1,1,0] op_sel_hi:[1,1,0]\n"
    "v_mfma_f32_16x16x32_bf16 v[108:111], a[196:199], v[188:191], v[108:111]\n"
    "v_cvt_pk_bf16_f32 v192, v234, v232\n"
    "v_pk_fma_f16 v232, v196, v196, s48\n"
    "v_mfma_f32_16x16x32_bf16 v[120:123], a[200:203], v[188:191], v[120:123]\n"
    "s_waitcnt lgkmcnt(6)\n"
    "v_or_b32_e32 v195, v195, v203\n"
    "v_pk_mul_f16 v232, v196, v232\n"
    "v_mfma_f32_16x16x32_bf16 v[132:135], a[244:247], v[184:187], v[132:135]\n"
    "v_exp_f16_e64 v234, v232\n"
    "v_mfma_f32_16x16x32_bf16 v[124:127], a[204:207], v[188:191], v[124:127]\n"
    "v_exp_f16_sdwa v234, v232 dst_sel:WORD_1 dst_unused:UNUSED_PRESERVE src0_sel:WORD_1\n"
    "v_mfma_f32_16x16x32_bf16 v[136:139], v[236:239], v[188:191], v[136:139]\n"
    "s_waitcnt lgkmcnt(4)\n"
    "v_or_b32_e32 v197, v197, v205\n"
    "s_waitcnt lgkmcnt(2)\n"
    "v_or_b32_e32 v198, v198, v206\n"
    "s_waitcnt lgkmcnt(0)\n"
    "v_or_b32_e32 v199, v199, v207\n"
    "s_mov_b32 s59, s58\n"
    "s_add_u32 s57, s57, 1572864\n"
    "s_add_u32 s58, s58, 1572864\n"
    "s_mov_b32 s70, s46\n"
    "s_add_u32 s88, s57, 98304\n"
    "s_add_u32 s89, s58, 98304\n"
    "s_add_u32 s39, s59, 98304\n"
    "s_mov_b32 s82, 0\n"
    "s_add_u32 s64, s64, 1\n"
    "s_cmp_lt_u32 s64, 2\n"
    "s_cselect_b64 s[72:73], s[72:73], s[76:77]\n"
    "s_cselect_b32 s74, s74, s78\n"
    "s_cselect_b32 s53, s53, s79\n"
    "s_cmp_lg_u32 s64, 3\n"
    "s_cbranch_scc1 1b\n"
    ".Lreplk_last:\n"
    "v_lshlrev_b32_sdwa v184, 1, v168 dst_sel:DWORD dst_unused:UNUSED_PAD src0_sel:DWORD src1_sel:WORD_1\n"
    "v_lshlrev_b32_sdwa v200, 1, v169 dst_sel:DWORD dst_unused:UNUSED_PAD src0_sel:DWORD src1_sel:WORD_1\n"
    "v_lshlrev_b32_sdwa v185, 1, v170 dst_sel:DWORD dst_unused:UNUSED_PAD src0_sel:DWORD src1_sel:WORD_1\n"
    "v_lshlrev_b32_sdwa v201, 1, v171 dst_sel:DWORD dst_unused:UNUSED_PAD src0_sel:DWORD src1_sel:WORD_1\n"
    "v_mfma_f32_16x16x32_bf16 v[140:143], a[244:247], v[188:191], v[140:143]\n"
    "s_waitcnt vmcnt(1)\n"
    "ds_read_b128 v[56:59], v244 offset:36864\n"
    "ds_read_u16 v184, v184\n"
    "v_mfma_f32_16x16x32_bf16 v[168:171], a[88:91], v[144:147], 0\n"
    "s_nop 0\n"
    "ds_read_u16_d16_hi v200, v200\n"
    "ds_read_u16 v185, v185\n"
    "v_mfma_f32_16x16x32_bf16 v[168:171], a[136:139], v[148:151], v[168:171]\n"
    "v_lshlrev_b32_sdwa v186, 1, v172 dst_sel:DWORD dst_unused:UNUSED_PAD src0_sel:DWORD src1_sel:WORD_1\n"
    "v_lshlrev_b32_sdwa v202, 1, v173 dst_sel:DWORD dst_unused:UNUSED_PAD src0_sel:DWORD src1_sel:WORD_1\n"
    "v_mfma_f32_16x16x32_bf16 v[168:171], a[52:55], v[152:155], v[168:171]\n"
    "ds_read_u16_d16_hi v201, v201\n"
    "v_lshlrev_b32_sdwa v187, 1, v174 dst_sel:DWORD dst_unused:UNUSED_PAD src0_sel:DWORD src1_sel:WORD_1\n"
    "v_smfmac_f32_32x32x32_bf16 v[128:143], a[248:251], a[224:231], v241\n"
    "ds_read_u16 v186, v186\n"
    "v_lshlrev_b32_sdwa v203, 1, v175 dst_sel:DWORD dst_unused:UNUSED_PAD src0_sel:DWORD src1_sel:WORD_1\n"
    "v_lshlrev_b32_sdwa v188, 1, v176 dst_sel:DWORD dst_unused:UNUSED_PAD src0_sel:DWORD src1_sel:WORD_1\n"
    "v_lshlrev_b32_sdwa v204, 1, v177 dst_sel:DWORD dst_unused:UNUSED_PAD src0_sel:DWORD src1_sel:WORD_1\n"
    "v_lshlrev_b32_sdwa v189, 1, v178 dst_sel:DWORD dst_unused:UNUSED_PAD src0_sel:DWORD src1_sel:WORD_1\n"
    "v_mfma_f32_16x16x32_bf16 v[172:175], a[92:95], v[144:147], 0\n"
    "ds_read_b128 v[236:239], v244 offset:20480\n"
    "v_lshlrev_b32_sdwa v205, 1, v179 dst_sel:DWORD dst_unused:UNUSED_PAD src0_sel:DWORD src1_sel:WORD_1\n"
    "v_mfma_f32_16x16x32_bf16 v[172:175], a[140:143], v[148:151], v[172:175]\n"
    "s_nop 0\n"
    "ds_read_u16_d16_hi v202, v202\n"
    "v_lshlrev_b32_sdwa v190, 1, v180 dst_sel:DWORD dst_unused:UNUSED_PAD src0_sel:DWORD src1_sel:WORD_1\n"
    "v_mfma_f32_16x16x32_bf16 v[176:179], a[88:91], v[156:159], 0\n"
    "s_waitcnt lgkmcnt(7)\n"
    "v_lshlrev_b32_sdwa v206, 1, v181 dst_sel:DWORD dst_unused:UNUSED_PAD src0_sel:DWORD src1_sel:WORD_1\n"
    "v_lshlrev_b32_sdwa v191, 1, v182 dst_sel:DWORD dst_unused:UNUSED_PAD src0_sel:DWORD src1_sel:WORD_1\n"
    "v_mfma_f32_16x16x32_bf16 v[172:175], v[56:59], v[152:155], v[172:175]\n"
    "ds_read_u16 v187, v187\n"
    "buffer_load_dwordx4 a[232:235], v245, s[40:43], s57 offen offset:64\n"
    "buffer_load_dwordx4 a[224:227], v245, s[40:43], s57 offen offset:0\n"
    "buffer_load_dwordx4 v[208:211], v245, s[40:43], s57 offen offset:128\n"
    "v_mfma_f32_16x16x32_bf16 v[176:179], a[136:139], v[160:163], v[176:179]\n"
    "ds_read_b128 a[244:247], v244 offset:22528\n"
    "v_lshlrev_b32_sdwa v207, 1, v183 dst_sel:DWORD dst_unused:UNUSED_PAD src0_sel:DWORD src1_sel:WORD_1\n"
    "v_mfma_f32_16x16x32_bf16 v[176:179], a[52:55], v[164:167], v[176:179]\n"
    "ds_read_u16_d16_hi v203, v203\n"
    "v_pk_fma_f16 v234, v234, s49, s49\n"
    "v_mfma_f32_16x16x32_bf16 v[180:183], a[92:95], v[156:159], 0\n"
    "v_rcp_f16_e32 v232, v234\n"
    "v_mfma_f32_16x16x32_bf16 v[180:183], a[140:143], v[160:163], v[180:183]\n"
    "s_waitcnt lgkmcnt(8)\n"
    "ds_read_u16 v188, v188\n"
    "v_rcp_f16_sdwa v232, v234 dst_sel:WORD_1 dst_unused:UNUSED_PRESERVE src0_sel:WORD_1\n"
    "v_mfma_f32_16x16x32_bf16 v[180:183], v[56:59], v[164:167], v[180:183]\n"
    "ds_read_u16_d16_hi v204, v204\n"
    "v_fma_mix_f32 v234, v196, v232, 0 op_sel_hi:[1,1,0]\n"
    "v_mfma_f32_16x16x32_bf16 v[96:99], a[208:211], v[192:195], v[96:99]\n"
    "v_fma_mix_f32 v232, v196, v232, 0 op_sel:[1,1,0] op_sel_hi:[1,1,0]\n"
    "v_or_b32_e32 v184, v184, v200\n"
    "v_mfma_f32_16x16x32_bf16 v[100:103], a[212:215], v[192:195], v[100:103]\n"
    "s_waitcnt lgkmcnt(8)\n"
    "ds_read_u16 v189, v189\n"
    "v_cvt_pk_bf16_f32 v196, v234, v232\n"
    "v_mfma_f32_16x16x32_bf16 v[112:115], a[216:219], v[192:195], v[112:115]\n"
    "s_waitcnt lgkmcnt(7)\n"
    "v_or_b32_e32 v185, v185, v201\n"
    "v_lshlrev_b32_sdwa v201, 1, v171 dst_sel:DWORD dst_unused:UNUSED_PAD src0_sel:DWORD src1_sel:WORD_1\n"
    "v_mfma_f32_16x16x32_bf16 v[116:119], a[220:223], v[192:195], v[116:119]\n"
    "ds_read_u16_d16_hi v205, v205\n"
    "v_cvt_pk_bf16_f32 v96, v96, v97\n"
    "v_mfma_f32_16x16x32_bf16 v[128:131], v[236:239], v[192:195], v[128:131]\n"
    "v_cvt_pk_bf16_f32 v97, v98, v99\n"
    "v_cvt_pk_bf16_f32 v98, v100, v101\n"
    "v_mfma_f32_16x16x32_bf16 v[104:107], a[208:211], v[196:199], v[104:107]\n"
    "s_waitcnt lgkmcnt(7)\n"
    "ds_read_u16 v190, v190\n"
    "v_or_b32_e32 v186, v186, v202\n"
    "v_mfma_f32_16x16x32_bf16 v[108:111], a[212:215], v[196:199], v[108:111]\n"
    "s_nop 0\n"
    "ds_read_u16_d16_hi v206, v206\n"
    "v_lshlrev_b32_sdwa v202, 1, v173 dst_sel:DWORD dst_unused:UNUSED_PAD src0_sel:DWORD src1_sel:WORD_1\n"
    "v_mfma_f32_16x16x32_bf16 v[120:123], a[216:219], v[196:199], v[120:123]\n"
    "s_waitcnt lgkmcnt(7)\n"
    "v_cvt_pk_bf16_f32 v99, v102, v103\n"
    "v_cvt_pk_bf16_f32 v112, v112, v113\n"
    "v_mfma_f32_16x16x32_bf16 v[132:135], a[244:247], v[192:195], v[132:135]\n"
    "v_cvt_pk_f16_f32 v192, v168, v169\n"
    "v_lshlrev_b32_sdwa v193, 1, v170 dst_sel:DWORD dst_unused:UNUSED_PAD src0_sel:DWORD src1_sel:WORD_1\n"
    "v_mfma_f32_16x16x32_bf16 v[124:127], a[220:223], v[196:199], v[124:127]\n"
    "buffer_load_dwordx4 a[236:239], v245, s[40:43], s88 offen offset:64\n"
    "buffer_load_dwordx4 a[228:231], v245, s[40:43], s88 offen offset:0\n"
    "buffer_load_dwordx4 v[212:215], v245, s[40:43], s88 offen offset:128\n"
    "ds_write_b128 v243, v[96:99] offset:24640\n"
    "v_mfma_f32_16x16x32_bf16 v[136:139], v[236:239], v[196:199], v[136:139]\n"
    "s_waitcnt lgkmcnt(6)\n"
    "v_lshlrev_b32_sdwa v194, 1, v172 dst_sel:DWORD dst_unused:UNUSED_PAD src0_sel:DWORD src1_sel:WORD_1\n"
    "v_lshlrev_b32_sdwa v195, 1, v174 dst_sel:DWORD dst_unused:UNUSED_PAD src0_sel:DWORD src1_sel:WORD_1\n"
    "v_mfma_f32_16x16x32_bf16 v[140:143], a[244:247], v[196:199], v[140:143]\n"
    "v_cvt_pk_f16_f32 v196, v176, v177\n"
    "v_lshlrev_b32_sdwa v197, 1, v178 dst_sel:DWORD dst_unused:UNUSED_PAD src0_sel:DWORD src1_sel:WORD_1\n"
    "v_mfma_f32_16x16x32_bf16 v[168:171], a[96:99], v[144:147], 0\n"
    "s_waitcnt lgkmcnt(5)\n"
    "ds_read_u16 v191, v191\n"
    "ds_read_u16_d16_hi v207, v207\n"
    "v_mfma_f32_16x16x32_bf16 v[168:171], a[144:147], v[148:151], v[168:171]\n"
    "ds_read_u16 v193, v193\n"
    "v_lshlrev_b32_sdwa v198, 1, v180 dst_sel:DWORD dst_unused:UNUSED_PAD src0_sel:DWORD src1_sel:WORD_1\n"
    "v_mfma_f32_16x16x32_bf16 v[168:171], a[56:59], v[152:155], v[168:171]\n"
    "ds_read_u16_d16_hi v201, v201\n"
    "v_lshlrev_b32_sdwa v199, 1, v182 dst_sel:DWORD dst_unused:UNUSED_PAD src0_sel:DWORD src1_sel:WORD_1\n"
    "v_or_b32_e64 v187, v187, v203\n"
    "v_lshlrev_b32_sdwa v203, 1, v175 dst_sel:DWORD dst_unused:UNUSED_PAD src0_sel:DWORD src1_sel:WORD_1\n"
    "v_mfma_f32_16x16x32_bf16 v[172:175], a[100:103], v[144:147], 0\n"
    "s_waitcnt lgkmcnt(7)\n"
    "ds_read_u16 v194, v194\n"
    "v_or_b32_e32 v188, v188, v204\n"
    "v_mfma_f32_16x16x32_bf16 v[172:175], a[148:151], v[148:151], v[172:175]\n"
    "v_or_b32_e64 v189, v189, v205\n"
    "v_lshlrev_b32_sdwa v205, 1, v179 dst_sel:DWORD dst_unused:UNUSED_PAD src0_sel:DWORD src1_sel:WORD_1\n"
    "v_mfma_f32_16x16x32_bf16 v[172:175], a[60:63], v[152:155], v[172:175]\n"
    "ds_read_u16_d16_hi v202, v202\n"
    "v_cvt_pk_bf16_f32 v113, v114, v115\n"
    "v_mfma_f32_16x16x32_bf16 v[176:179], a[96:99], v[156:159], 0\n"
    "s_waitcnt lgkmcnt(6)\n"
    "v_or_b32_e32 v190, v190, v206\n"
    "v_lshlrev_b32_sdwa v206, 1, v181 dst_sel:DWORD dst_unused:UNUSED_PAD src0_sel:DWORD src1_sel:WORD_1\n"
    "v_mfma_f32_16x16x32_bf16 v[176:179], a[144:147], v[160:163], v[176:179]\n"
    "ds_read_b128 v[0:3], v244 offset:0\n"
    "ds_read_b128 a[252:255], v249 offset:0\n"
    "v_mfma_f32_16x16x32_bf16 v[176:179], a[56:59], v[164:167], v[176:179]\n"
    "v_cvt_pk_bf16_f32 v114, v116, v117\n"
    "v_cvt_pk_bf16_f32 v115, v118, v119\n"
    "v_mfma_f32_16x16x32_bf16 v[96:99], a[0:3], v[184:187], 0\n"
    "v_cvt_pk_bf16_f32 v128, v128, v129\n"
    "v_cvt_pk_bf16_f32 v129, v130, v131\n"
    "v_mfma_f32_16x16x32_bf16 v[100:103], a[4:7], v[184:187], 0\n"
    "ds_write_b128 v243, v[112:115] offset:26688\n"
    "v_mfma_f32_16x16x32_bf16 v[112:115], a[8:11], v[184:187], 0\n"
    "s_waitcnt lgkmcnt(7)\n"
    "v_or_b32_e32 v191, v191, v207\n"
    "v_lshlrev_b32_sdwa v207, 1, v183 dst_sel:DWORD dst_unused:UNUSED_PAD src0_sel:DWORD src1_sel:WORD_1\n"
    "v_mfma_f32_16x16x32_bf16 v[180:183], a[100:103], v[156:159], 0\n"
    "v_cvt_pk_bf16_f32 v130, v132, v133\n"
    "v_cvt_pk_bf16_f32 v131, v134, v135\n"
    "v_mfma_f32_16x16x32_bf16 v[180:183], a[148:151], v[160:163], v[180:183]\n"
    "ds_read_u16 v195, v195\n"
    "ds_write_b128 v243, v[128:131] offset:28736\n"
    "v_mfma_f32_16x16x32_bf16 v[180:183], a[60:63], v[164:167], v[180:183]\n"
    "s_waitcnt lgkmcnt(7)\n"
    "v_or_b32_e32 v193, v193, v201\n"
    "v_cvt_pk_bf16_f32 v104, v104, v105\n"
    "v_mfma_f32_16x16x32_bf16 v[116:119], a[12:15], v[184:187], 0\n"
    "v_cvt_pk_bf16_f32 v105, v106, v107\n"
    "v_cvt_pk_bf16_f32 v106, v108, v109\n"
    "v_cvt_pk_bf16_f32 v107, v110, v111\n"
    "v_mfma_f32_16x16x32_bf16 v[108:111], a[4:7], v[188:191], 0\n"
    "ds_read_u16_d16_hi v203, v203\n"
    "ds_write_b128 v243, v[104:107] offset:25664\n"
    "v_mfma_f32_16x16x32_bf16 v[104:107], a[0:3], v[188:191], 0\n"
    "s_waitcnt lgkmcnt(5)\n"
    "buffer_store_dwordx4 a[252:255], v255, s[68:71], s59 offen offset:0 sc1\n"
    "v_mfma_f32_16x16x32_bf16 v[128:131], v[0:3], v[184:187], 0\n"
    "s_nop 0\n"
    "ds_read_b128 v[4:7], v244 offset:2048\n"
    "ds_read_u16 v197, v197\n"
    "v_or_b32_e64 v194, v194, v202\n"
    "v_cvt_pk_bf16_f32 v120, v120, v121\n"
    "v_cvt_pk_bf16_f32 v121, v122, v123\n"
    "v_cvt_pk_bf16_f32 v122, v124, v125\n"
    "v_cvt_pk_bf16_f32 v123, v126, v127\n"
    "ds_write_b128 v243, v[120:123] offset:27712\n"
    "v_pk_fma_f16 v232, v192, v192, s48\n"
    "v_mfma_f32_16x16x32_bf16 v[120:123], a[8:11], v[188:191], 0\n"
    "v_cvt_pk_bf16_f32 v136, v136, v137\n"
    "v_cvt_pk_bf16_f32 v137, v138, v139\n"
    "v_mfma_f32_16x16x32_bf16 v[124:127], a[12:15], v[188:191], 0\n"
    "ds_read_u16_d16_hi v205, v205\n"
    "ds_read_u16 v198, v198\n"
    "v_cvt_pk_bf16_f32 v138, v140, v141\n"
    "v_cvt_pk_bf16_f32 v139, v142, v143\n"
    "ds_write_b128 v243, v[136:139] offset:29760\n"
    "s_waitcnt lgkmcnt(5)\n"
    "v_mfma_f32_16x16x32_bf16 v[132:135], v[4:7], v[184:187], 0\n"
    "v_or_b32_e32 v195, v195, v203\n"
    "v_pk_mul_f16 v232, v192, v232\n"
    "v_mfma_f32_16x16x32_bf16 v[136:139], v[0:3], v[188:191], 0\n"
    "ds_read_u16_d16_hi v206, v206\n"
    "ds_read_u16 v199, v199\n"
    "v_exp_f16_e64 v234, v232\n"
    "v_mfma_f32_16x16x32_bf16 v[140:143], v[4:7], v[188:191], 0\n"
    "ds_read_u16_d16_hi v207, v207\n"
    "v_exp_f16_sdwa v234, v232 dst_sel:WORD_1 dst_unused:UNUSED_PRESERVE src0_sel:WORD_1\n"
    "v_cvt_pk_f16_f32 v184, v168, v169\n"
    "v_pk_fma_f16 v234, v234, s49, s49\n"
    "v_lshlrev_b32_sdwa v185, 1, v170 dst_sel:DWORD dst_unused:UNUSED_PAD src0_sel:DWORD src1_sel:WORD_1\n"
    "v_lshlrev_b32_sdwa v201, 1, v171 dst_sel:DWORD dst_unused:UNUSED_PAD src0_sel:DWORD src1_sel:WORD_1\n"
    "v_mfma_f32_16x16x32_bf16 v[168:171], a[104:107], v[144:147], 0\n"
    "ds_read_b128 a[252:255], v249 offset:2048\n"
    "ds_read_u16 v185, v185\n"
    "v_mfma_f32_16x16x32_bf16 v[168:171], a[152:155], v[148:151], v[168:171]\n"
    "v_lshlrev_b32_sdwa v186, 1, v172 dst_sel:DWORD dst_unused:UNUSED_PAD src0_sel:DWORD src1_sel:WORD_1\n"
    "v_lshlrev_b32_sdwa v202, 1, v173 dst_sel:DWORD dst_unused:UNUSED_PAD src0_sel:DWORD src1_sel:WORD_1\n"
    "v_lshlrev_b32_sdwa v187, 1, v174 dst_sel:DWORD dst_unused:UNUSED_PAD src0_sel:DWORD src1_sel:WORD_1\n"
    "s_waitcnt lgkmcnt(7)\n"
    "v_or_b32_e32 v197, v197, v205\n"
    "v_lshlrev_b32_sdwa v203, 1, v175 dst_sel:DWORD dst_unused:UNUSED_PAD src0_sel:DWORD src1_sel:WORD_1\n"
    "v_rcp_f16_e64 v232, v234\n"
    "v_mfma_f32_16x16x32_bf16 v[168:171], a[64:67], v[152:155], v[168:171]\n"
    "ds_read_u16_d16_hi v201, v201\n"
    "ds_read_u16 v186, v186\n"
    "v_rcp_f16_sdwa v232, v234 dst_sel:WORD_1 dst_unused:UNUSED_PRESERVE src0_sel:WORD_1\n"
    "v_mfma_f32_16x16x32_bf16 v[172:175], a[108:111], v[144:147], 0\n"
    "ds_read_u16_d16_hi v202, v202\n"
    "v_cvt_pk_f16_f32 v188, v176, v177\n"
    "v_mfma_f32_16x16x32_bf16 v[172:175], a[156:159], v[148:151], v[172:175]\n"
    "v_lshlrev_b32_sdwa v189, 1, v178 dst_sel:DWORD dst_unused:UNUSED_PAD src0_sel:DWORD src1_sel:WORD_1\n"
    "v_lshlrev_b32_sdwa v205, 1, v179 dst_sel:DWORD dst_unused:UNUSED_PAD src0_sel:DWORD src1_sel:WORD_1\n"
    "v_lshlrev_b32_sdwa v190, 1, v180 dst_sel:DWORD dst_unused:UNUSED_PAD src0_sel:DWORD src1_sel:WORD_1\n"
    "v_lshlrev_b32_sdwa v191, 1, v182 dst_sel:DWORD dst_unused:UNUSED_PAD src0_sel:DWORD src1_sel:WORD_1\n"
    "v_fma_mix_f32 v234, v192, v232, 0 op_sel_hi:[1,1,0]\n"
    "v_mfma_f32_16x16x32_bf16 v[172:175], a[68:71], v[152:155], v[172:175]\n"
    "s_waitcnt lgkmcnt(7)\n"
    "ds_read_b128 v[8:11], v244 offset:4096\n"
    "ds_read_u16 v187, v187\n"
    "v_or_b32_e32 v198, v198, v206\n"
    "v_lshlrev_b32_sdwa v206, 1, v181 dst_sel:DWORD dst_unused:UNUSED_PAD src0_sel:DWORD src1_sel:WORD_1\n"
    "v_fma_mix_f32 v232, v192, v232, 0 op_sel:[1,1,0] op_sel_hi:[1,1,0]\n"
    "v_mfma_f32_16x16x32_bf16 v[176:179], a[104:107], v[156:159], 0\n"
    "ds_read_u16_d16_hi v203, v203\n"
    "v_cvt_pk_bf16_f32 v192, v234, v232\n"
    "v_mfma_f32_16x16x32_bf16 v[176:179], a[152:155], v[160:163], v[176:179]\n"
    "s_waitcnt lgkmcnt(7)\n"
    "buffer_store_dwordx4 a[252:255], v255, s[68:71], s59 offen offset:64 sc1\n"
    "ds_read_b128 v[12:15], v244 offset:6144\n"
    "v_mfma_f32_16x16x32_bf16 v[176:179], a[64:67], v[164:167], v[176:179]\n"
    "s_nop 0\n"
    "ds_read_b128 a[252:255], v249 offset:4096\n"
    "v_or_b32_e64 v199, v199, v207\n"
    "v_lshlrev_b32_sdwa v207, 1, v183 dst_sel:DWORD dst_unused:UNUSED_PAD src0_sel:DWORD src1_sel:WORD_1\n"
    "v_pk_fma_f16 v232, v196, v196, s48\n"
    "v_mfma_f32_16x16x32_bf16 v[180:183], a[108:111], v[156:159], 0\n"
    "ds_read_u16 v189, v189\n"
    "v_pk_mul_f16 v232, v196, v232\n"
    "v_mfma_f32_16x16x32_bf16 v[180:183], a[156:159], v[160:163], v[180:183]\n"
    "ds_read_u16_d16_hi v205, v205\n"
    "v_exp_f16_e32 v234, v232\n"
    "v_mfma_f32_16x16x32_bf16 v[180:183], a[68:71], v[164:167], v[180:183]\n"
    "s_waitcnt lgkmcnt(9)\n"
    "v_exp_f16_sdwa v234, v232 dst_sel:WORD_1 dst_unused:UNUSED_PRESERVE src0_sel:WORD_1\n"
    "v_mfma_f32_16x16x32_bf16 v[96:99], a[16:19], v[192:195], v[96:99]\n"
    "ds_read_u16 v190, v190\n"
    "v_pk_fma_f16 v234, v234, s49, s49\n"
    "v_mfma_f32_16x16x32_bf16 v[100:103], a[20:23], v[192:195], v[100:103]\n"
    "ds_read_u16_d16_hi v206, v206\n"
    "v_rcp_f16_e32 v232, v234\n"
    "v_mfma_f32_16x16x32_bf16 v[112:115], a[24:27], v[192:195], v[112:115]\n"
    "s_waitcnt lgkmcnt(8)\n"
    "v_rcp_f16_sdwa v232, v234 dst_sel:WORD_1 dst_unused:UNUSED_PRESERVE src0_sel:WORD_1\n"
    "v_mfma_f32_16x16x32_bf16 v[116:119], a[28:31], v[192:195], v[116:119]\n"
    "ds_read_u16 v191, v191\n"
    "v_or_b32_e64 v185, v185, v201\n"
    "v_mfma_f32_16x16x32_bf16 v[128:131], v[8:11], v[192:195], v[128:131]\n"
    "v_fma_mix_f32 v234, v196, v232, 0 op_sel_hi:[1,1,0]\n"
    "v_fma_mix_f32 v232, v196, v232, 0 op_sel:[1,1,0] op_sel_hi:[1,1,0]\n"
    "v_or_b32_e64 v186, v186, v202\n"
    "v_cvt_pk_bf16_f32 v196, v234, v232\n"
    "v_pk_fma_f16 v232, v184, v184, s48\n"
    "v_lshlrev_b32_sdwa v200, 1, v169 dst_sel:DWORD dst_unused:UNUSED_PAD src0_sel:DWORD src1_sel:WORD_1\n"
    "v_mfma_f32_16x16x32_bf16 v[104:107], a[16:19], v[196:199], v[104:107]\n"
    "s_waitcnt lgkmcnt(6)\n"
    "ds_read_u16_d16_hi v207, v207\n"
    "ds_read_b128 v[52:55], v244 offset:34816\n"
    "v_mfma_f32_16x16x32_bf16 v[132:135], v[12:15], v[192:195], v[132:135]\n"
    "s_waitcnt lgkmcnt(7)\n"
    "ds_read_u16_d16_hi v200, v200\n"
    "buffer_store_dwordx4 a[252:255], v255, s[68:71], s59 offen offset:128 sc1\n"
    "v_mfma_f32_16x16x32_bf16 v[108:111], a[20:23], v[196:199], v[108:111]\n"
    "v_or_b32_e64 v187, v187, v203\n"
    "v_pk_mul_f16 v232, v184, v232\n"
    "v_mfma_f32_16x16x32_bf16 v[120:123], a[24:27], v[196:199], v[120:123]\n"
    "ds_read_b128 a[252:255], v249 offset:1024\n"
    "v_exp_f16_e64 v234, v232\n"
    "v_mfma_f32_16x16x32_bf16 v[124:127], a[28:31], v[196:199], v[124:127]\n"
    "s_waitcnt lgkmcnt(7)\n"
    "v_or_b32_e32 v189, v189, v205\n"
    "v_exp_f16_sdwa v234, v232 dst_sel:WORD_1 dst_unused:UNUSED_PRESERVE src0_sel:WORD_1\n"
    "v_mfma_f32_16x16x32_bf16 v[136:139], v[8:11], v[196:199], v[136:139]\n"
    "ds_read_b128 v[16:19], v244 offset:8192\n"
    "v_lshlrev_b32_sdwa v192, 1, v168 dst_sel:DWORD dst_unused:UNUSED_PAD src0_sel:DWORD src1_sel:WORD_1\n"
    "v_mfma_f32_16x16x32_bf16 v[140:143], v[12:15], v[196:199], v[140:143]\n"
    "ds_read_u16 v192, v192\n"
    "v_pk_fma_f16 v234, v234, s49, s49\n"
    "v_lshlrev_b32_sdwa v193, 1, v170 dst_sel:DWORD dst_unused:UNUSED_PAD src0_sel:DWORD src1_sel:WORD_1\n"
    "v_lshlrev_b32_sdwa v201, 1, v171 dst_sel:DWORD dst_unused:UNUSED_PAD src0_sel:DWORD src1_sel:WORD_1\n"
    "v_mfma_f32_16x16x32_bf16 v[168:171], a[112:115], v[144:147], 0\n"
    "s_waitcnt lgkmcnt(7)\n"
    "ds_read_u16 v193, v193\n"
    "ds_read_u16_d16_hi v201, v201\n"
    "v_mfma_f32_16x16x32_bf16 v[168:171], a[160:163], v[148:151], v[168:171]\n"
    "s_nop 0\n"
    "v_lshlrev_b32_sdwa v194, 1, v172 dst_sel:DWORD dst_unused:UNUSED_PAD src0_sel:DWORD src1_sel:WORD_1\n"
    "v_lshlrev_b32_sdwa v202, 1, v173 dst_sel:DWORD dst_unused:UNUSED_PAD src0_sel:DWORD src1_sel:WORD_1\n"
    "v_lshlrev_b32_sdwa v195, 1, v174 dst_sel:DWORD dst_unused:UNUSED_PAD src0_sel:DWORD src1_sel:WORD_1\n"
    "v_lshlrev_b32_sdwa v203, 1, v175 dst_sel:DWORD dst_unused:UNUSED_PAD src0_sel:DWORD src1_sel:WORD_1\n"
    "v_rcp_f16_e64 v232, v234\n"
    "v_mfma_f32_16x16x32_bf16 v[168:171], a[72:75], v[152:155], v[168:171]\n"
    "ds_read_u16 v194, v194\n"
    "ds_read_u16_d16_hi v202, v202\n"
    "v_or_b32_e64 v190, v190, v206\n"
    "v_rcp_f16_sdwa v232, v234 dst_sel:WORD_1 dst_unused:UNUSED_PRESERVE src0_sel:WORD_1\n"
    "v_mfma_f32_16x16x32_bf16 v[172:175], a[116:119], v[144:147], 0\n"
    "s_waitcnt lgkmcnt(8)\n"
    "ds_read_u16 v195, v195\n"
    "v_lshlrev_b32_sdwa v196, 1, v176 dst_sel:DWORD dst_unused:UNUSED_PAD src0_sel:DWORD src1_sel:WORD_1\n"
    "v_mfma_f32_16x16x32_bf16 v[172:175], a[164:167], v[148:151], v[172:175]\n"
    "s_waitcnt lgkmcnt(7)\n"
    "buffer_store_dwordx4 a[252:255], v255, s[68:71], s39 offen offset:0 sc1\n"
    "ds_read_b128 v[20:23], v244 offset:10240\n"
    "v_lshlrev_b32_sdwa v204, 1, v177 dst_sel:DWORD dst_unused:UNUSED_PAD src0_sel:DWORD src1_sel:WORD_1\n"
    "v_lshlrev_b32_sdwa v197, 1, v178 dst_sel:DWORD dst_unused:UNUSED_PAD src0_sel:DWORD src1_sel:WORD_1\n"
    "v_lshlrev_b32_sdwa v205, 1, v179 dst_sel:DWORD dst_unused:UNUSED_PAD src0_sel:DWORD src1_sel:WORD_1\n"
    "v_lshlrev_b32_sdwa v198, 1, v180 dst_sel:DWORD dst_unused:UNUSED_PAD src0_sel:DWORD src1_sel:WORD_1\n"
    "v_lshlrev_b32_sdwa v206, 1, v181 dst_sel:DWORD dst_unused:UNUSED_PAD src0_sel:DWORD src1_sel:WORD_1\n"
    "v_lshlrev_b32_sdwa v199, 1, v182 dst_sel:DWORD dst_unused:UNUSED_PAD src0_sel:DWORD src1_sel:WORD_1\n"
    "v_fma_mix_f32 v234, v184, v232, 0 op_sel_hi:[1,1,0]\n"
    "v_mfma_f32_16x16x32_bf16 v[172:175], v[52:55], v[152:155], v[172:175]\n"
    "ds_read_u16_d16_hi v203, v203\n"
    "ds_read_u16 v196, v196\n"
    "v_or_b32_e32 v191, v191, v207\n"
    "v_lshlrev_b32_sdwa v207, 1, v183 dst_sel:DWORD dst_unused:UNUSED_PAD src0_sel:DWORD src1_sel:WORD_1\n"
    "v_fma_mix_f32 v232, v184, v232, 0 op_sel:[1,1,0] op_sel_hi:[1,1,0]\n"
    "v_mfma_f32_16x16x32_bf16 v[176:179], a[112:115], v[156:159], 0\n"
    "s_waitcnt lgkmcnt(8)\n"
    "ds_read_u16_d16_hi v204, v204\n"
    "v_cvt_pk_bf16_f32 v184, v234, v232\n"
    "v_mfma_f32_16x16x32_bf16 v[176:179], a[160:163], v[160:163], v[176:179]\n"
    "v_pk_fma_f16 v232, v188, v188, s48\n"
    "v_or_b32_e32 v192, v192, v200\n"
    "v_mfma_f32_16x16x32_bf16 v[176:179], a[72:75], v[164:167], v[176:179]\n"
    "s_waitcnt lgkmcnt(7)\n"
    "ds_read_u16 v197, v197\n"
    "v_pk_mul_f16 v232, v188, v232\n"
    "v_mfma_f32_16x16x32_bf16 v[180:183], a[116:119], v[156:159], 0\n"
    "ds_read_b128 a[252:255], v249 offset:3072\n"
    "v_exp_f16_e64 v234, v232\n"
    "v_mfma_f32_16x16x32_bf16 v[180:183], a[164:167], v[160:163], v[180:183]\n"
    "ds_read_u16_d16_hi v205, v205\n"
    "v_exp_f16_sdwa v234, v232 dst_sel:WORD_1 dst_unused:UNUSED_PRESERVE src0_sel:WORD_1\n"
    "v_mfma_f32_16x16x32_bf16 v[180:183], v[52:55], v[164:167], v[180:183]\n"
    "s_waitcnt lgkmcnt(8)\n"
    "v_pk_fma_f16 v234, v234, s49, s49\n"
    "v_or_b32_e32 v193, v193, v201\n"
    "v_mfma_f32_16x16x32_bf16 v[96:99], a[32:35], v[184:187], v[96:99]\n"
    "ds_read_u16 v198, v198\n"
    "v_rcp_f16_e64 v232, v234\n"
    "v_mfma_f32_16x16x32_bf16 v[100:103], a[36:39], v[184:187], v[100:103]\n"
    "ds_read_u16_d16_hi v206, v206\n"
    "v_rcp_f16_sdwa v232, v234 dst_sel:WORD_1 dst_unused:UNUSED_PRESERVE src0_sel:WORD_1\n"
    "v_mfma_f32_16x16x32_bf16 v[112:115], a[40:43], v[184:187], v[112:115]\n"
    "s_nop 0\n"
    "v_fma_mix_f32 v234, v188, v232, 0 op_sel_hi:[1,1,0]\n"
    "v_fma_mix_f32 v232, v188, v232, 0 op_sel:[1,1,0] op_sel_hi:[1,1,0]\n"
    "v_mfma_f32_16x16x32_bf16 v[116:119], a[44:47], v[184:187], v[116:119]\n"
    "s_waitcnt lgkmcnt(7)\n"
    "ds_read_u16 v199, v199\n"
    "v_cvt_pk_bf16_f32 v188, v234, v232\n"
    "v_mfma_f32_16x16x32_bf16 v[128:131], v[16:19], v[184:187], v[128:131]\n"
    "ds_read_u16_d16_hi v207, v207\n"
    "v_lshlrev_b32_sdwa v201, 1, v171 dst_sel:DWORD dst_unused:UNUSED_PAD src0_sel:DWORD src1_sel:WORD_1\n"
    "v_mfma_f32_16x16x32_bf16 v[132:135], v[20:23], v[184:187], v[132:135]\n"
    "v_cvt_pk_f16_f32 v184, v168, v169\n"
    "v_lshlrev_b32_sdwa v185, 1, v170 dst_sel:DWORD dst_unused:UNUSED_PAD src0_sel:DWORD src1_sel:WORD_1\n"
    "v_mfma_f32_16x16x32_bf16 v[104:107], a[32:35], v[188:191], v[104:107]\n"
    "ds_read_b128 v[60:63], v244 offset:38912\n"
    "v_lshlrev_b32_sdwa v186, 1, v172 dst_sel:DWORD dst_unused:UNUSED_PAD src0_sel:DWORD src1_sel:WORD_1\n"
    "v_mfma_f32_16x16x32_bf16 v[108:111], a[36:39], v[188:191], v[108:111]\n"
    "s_waitcnt lgkmcnt(8)\n"
    "ds_read_u16 v185, v185\n"
    "v_or_b32_e32 v194, v194, v202\n"
    "v_mfma_f32_16x16x32_bf16 v[120:123], a[40:43], v[188:191], v[120:123]\n"
    "s_waitcnt lgkmcnt(7)\n"
    "buffer_store_dwordx4 a[252:255], v255, s[68:71], s39 offen offset:64 sc1\n"
    "ds_read_u16_d16_hi v201, v201\n"
    "v_mfma_f32_16x16x32_bf16 v[124:127], a[44:47], v[188:191], v[124:127]\n"
    "v_lshlrev_b32_sdwa v202, 1, v173 dst_sel:DWORD dst_unused:UNUSED_PAD src0_sel:DWORD src1_sel:WORD_1\n"
    "v_lshlrev_b32_sdwa v187, 1, v174 dst_sel:DWORD dst_unused:UNUSED_PAD src0_sel:DWORD src1_sel:WORD_1\n"
    "v_mfma_f32_16x16x32_bf16 v[136:139], v[16:19], v[188:191], v[136:139]\n"
    "s_waitcnt lgkmcnt(7)\n"
    "ds_read_b128 a[252:255], v249 offset:5120\n"
    "ds_read_u16 v186, v186\n"
    "v_mfma_f32_16x16x32_bf16 v[140:143], v[20:23], v[188:191], v[140:143]\n"
    "v_cvt_pk_f16_f32 v188, v176, v177\n"
    "v_lshlrev_b32_sdwa v189, 1, v178 dst_sel:DWORD dst_unused:UNUSED_PAD src0_sel:DWORD src1_sel:WORD_1\n"
    "v_mfma_f32_16x16x32_bf16 v[168:171], a[120:123], v[144:147], 0\n"
    "ds_read_u16_d16_hi v202, v202\n"
    "v_lshlrev_b32_sdwa v190, 1, v180 dst_sel:DWORD dst_unused:UNUSED_PAD src0_sel:DWORD src1_sel:WORD_1\n"
    "v_mfma_f32_16x16x32_bf16 v[168:171], a[168:171], v[148:151], v[168:171]\n"
    "v_lshlrev_b32_sdwa v191, 1, v182 dst_sel:DWORD dst_unused:UNUSED_PAD src0_sel:DWORD src1_sel:WORD_1\n"
    "v_or_b32_e32 v195, v195, v203\n"
    "v_mfma_f32_16x16x32_bf16 v[168:171], a[76:79], v[152:155], v[168:171]\n"
    "s_waitcnt lgkmcnt(8)\n"
    "ds_read_b128 v[24:27], v244 offset:12288\n"
    "v_lshlrev_b32_sdwa v203, 1, v175 dst_sel:DWORD dst_unused:UNUSED_PAD src0_sel:DWORD src1_sel:WORD_1\n"
    "v_mfma_f32_16x16x32_bf16 v[172:175], a[124:127], v[144:147], 0\n"
    "ds_read_u16 v187, v187\n"
    "v_or_b32_e64 v196, v196, v204\n"
    "v_mfma_f32_16x16x32_bf16 v[172:175], a[172:175], v[148:151], v[172:175]\n"
    "v_or_b32_e64 v197, v197, v205\n"
    "v_lshlrev_b32_sdwa v205, 1, v179 dst_sel:DWORD dst_unused:UNUSED_PAD src0_sel:DWORD src1_sel:WORD_1\n"
    "v_mfma_f32_16x16x32_bf16 v[176:179], a[120:123], v[156:159], 0\n"
    "s_waitcnt lgkmcnt(7)\n"
    "ds_read_u16_d16_hi v203, v203\n"
    "v_or_b32_e32 v198, v198, v206\n"
    "v_mfma_f32_16x16x32_bf16 v[172:175], v[60:63], v[152:155], v[172:175]\n"
    "v_lshlrev_b32_sdwa v206, 1, v181 dst_sel:DWORD dst_unused:UNUSED_PAD src0_sel:DWORD src1_sel:WORD_1\n"
    "v_or_b32_e64 v199, v199, v207\n"
    "v_mfma_f32_16x16x32_bf16 v[176:179], a[168:171], v[160:163], v[176:179]\n"
    "ds_read_b128 v[28:31], v244 offset:14336\n"
    "ds_read_u16 v189, v189\n"
    "v_mfma_f32_16x16x32_bf16 v[176:179], a[76:79], v[164:167], v[176:179]\n"
    "v_lshlrev_b32_sdwa v207, 1, v183 dst_sel:DWORD dst_unused:UNUSED_PAD src0_sel:DWORD src1_sel:WORD_1\n"
    "v_pk_fma_f16 v232, v184, v184, s48\n"
    "v_mfma_f32_16x16x32_bf16 v[180:183], a[124:127], v[156:159], 0\n"
    "ds_read_u16_d16_hi v205, v205\n"
    "v_pk_mul_f16 v232, v184, v232\n"
    "v_mfma_f32_16x16x32_bf16 v[180:183], a[172:175], v[160:163], v[180:183]\n"
    "s_waitcnt lgkmcnt(8)\n"
    "buffer_store_dwordx4 a[252:255], v255, s[68:71], s39 offen offset:128 sc1\n"
    "ds_read_u16 v190, v190\n"
    "v_mfma_f32_16x16x32_bf16 v[180:183], v[60:63], v[164:167], v[180:183]\n"
    "v_or_b32_e32 v185, v185, v201\n"
    "v_exp_f16_e64 v234, v232\n"
    "v_mfma_f32_16x16x32_bf16 v[96:99], a[176:179], v[192:195], v[96:99]\n"
    "ds_read_b128 v[144:147], v243 offset:0\n"
    "ds_read_u16_d16_hi v206, v206\n"
    "v_mfma_f32_16x16x32_bf16 v[100:103], a[180:183], v[192:195], v[100:103]\n"
    "s_waitcnt lgkmcnt(8)\n"
    "v_or_b32_e32 v186, v186, v202\n"
    "v_exp_f16_sdwa v234, v232 dst_sel:WORD_1 dst_unused:UNUSED_PRESERVE src0_sel:WORD_1\n"
    "v_mfma_f32_16x16x32_bf16 v[112:115], a[184:187], v[192:195], v[112:115]\n"
    "ds_read_b128 v[148:151], v243 offset:1024\n"
    "v_pk_fma_f16 v234, v234, s49, s49\n"
    "v_mfma_f32_16x16x32_bf16 v[116:119], a[188:191], v[192:195], v[116:119]\n"
    "ds_read_u16 v191, v191\n"
    "v_lshlrev_b32_sdwa v201, 1, v171 dst_sel:DWORD dst_unused:UNUSED_PAD src0_sel:DWORD src1_sel:WORD_1\n"
    "v_mfma_f32_16x16x32_bf16 v[128:131], v[24:27], v[192:195], v[128:131]\n"
    "v_lshlrev_b32_sdwa v202, 1, v173 dst_sel:DWORD dst_unused:UNUSED_PAD src0_sel:DWORD src1_sel:WORD_1\n"
    "v_rcp_f16_e32 v232, v234\n"
    "v_mfma_f32_16x16x32_bf16 v[104:107], a[176:179], v[196:199], v[104:107]\n"
    "s_waitcnt lgkmcnt(7)\n"
    "ds_read_b128 v[152:155], v243 offset:2048\n"
    "ds_read_u16_d16_hi v207, v207\n"
    "v_mfma_f32_16x16x32_bf16 v[132:135], v[28:31], v[192:195], v[132:135]\n"
    "v_or_b32_e64 v187, v187, v203\n"
    "v_cvt_pk_f16_f32 v192, v168, v169\n"
    "v_mfma_f32_16x16x32_bf16 v[108:111], a[180:183], v[196:199], v[108:111]\n"
    "ds_read_b128 v[156:159], v243 offset:3072\n"
    "v_lshlrev_b32_sdwa v193, 1, v170 dst_sel:DWORD dst_unused:UNUSED_PAD src0_sel:DWORD src1_sel:WORD_1\n"
    "v_mfma_f32_16x16x32_bf16 v[120:123], a[184:187], v[196:199], v[120:123]\n"
    "ds_read_b128 v[160:163], v243 offset:4096\n"
    "v_lshlrev_b32_sdwa v194, 1, v172 dst_sel:DWORD dst_unused:UNUSED_PAD src0_sel:DWORD src1_sel:WORD_1\n"
    "v_mfma_f32_16x16x32_bf16 v[124:127], a[188:191], v[196:199], v[124:127]\n"
    "ds_read_b128 v[164:167], v243 offset:5120\n"
    "v_lshlrev_b32_sdwa v195, 1, v174 dst_sel:DWORD dst_unused:UNUSED_PAD src0_sel:DWORD src1_sel:WORD_1\n"
    "v_mfma_f32_16x16x32_bf16 v[136:139], v[24:27], v[196:199], v[136:139]\n"
    "s_waitcnt lgkmcnt(10)\n"
    "ds_read_b128 v[48:51], v244 offset:32768\n"
    "v_or_b32_e64 v189, v189, v205\n"
    "v_mfma_f32_16x16x32_bf16 v[140:143], v[28:31], v[196:199], v[140:143]\n"
    "s_waitcnt lgkmcnt(9)\n"
    "ds_read_u16 v193, v193\n"
    "v_lshlrev_b32_sdwa v203, 1, v175 dst_sel:DWORD dst_unused:UNUSED_PAD src0_sel:DWORD src1_sel:WORD_1\n"
    "v_mfma_f32_16x16x32_bf16 v[168:171], a[80:83], v[144:147], 0\n"
    "v_rcp_f16_sdwa v232, v234 dst_sel:WORD_1 dst_unused:UNUSED_PRESERVE src0_sel:WORD_1\n"
    "v_mfma_f32_16x16x32_bf16 v[172:175], a[84:87], v[144:147], 0\n"
    "ds_read_u16_d16_hi v201, v201\n"
    "v_cvt_pk_f16_f32 v196, v176, v177\n"
    "s_waitcnt lgkmcnt(9)\n"
    "v_mfma_f32_16x16x32_bf16 v[168:171], a[128:131], v[148:151], v[168:171]\n"
    "v_or_b32_e32 v190, v190, v206\n"
    "v_lshlrev_b32_sdwa v197, 1, v178 dst_sel:DWORD dst_unused:UNUSED_PAD src0_sel:DWORD src1_sel:WORD_1\n"
    "v_lshlrev_b32_sdwa v205, 1, v179 dst_sel:DWORD dst_unused:UNUSED_PAD src0_sel:DWORD src1_sel:WORD_1\n"
    "v_lshlrev_b32_sdwa v198, 1, v180 dst_sel:DWORD dst_unused:UNUSED_PAD src0_sel:DWORD src1_sel:WORD_1\n"
    "v_lshlrev_b32_sdwa v206, 1, v181 dst_sel:DWORD dst_unused:UNUSED_PAD src0_sel:DWORD src1_sel:WORD_1\n"
    "v_lshlrev_b32_sdwa v199, 1, v182 dst_sel:DWORD dst_unused:UNUSED_PAD src0_sel:DWORD src1_sel:WORD_1\n"
    "v_fma_mix_f32 v234, v184, v232, 0 op_sel_hi:[1,1,0]\n"
    "v_mfma_f32_16x16x32_bf16 v[172:175], a[132:135], v[148:151], v[172:175]\n"
    "s_waitcnt lgkmcnt(7)\n"
    "ds_read_u16 v194, v194\n"
    "ds_read_u16_d16_hi v202, v202\n"
    "v_mfma_f32_16x16x32_bf16 v[168:171], a[48:51], v[152:155], v[168:171]\n"
    "s_nop 0\n"
    "v_fma_mix_f32 v232, v184, v232, 0 op_sel:[1,1,0] op_sel_hi:[1,1,0]\n"
    "v_cvt_pk_bf16_f32 v184, v234, v232\n"
    "v_pk_fma_f16 v232, v188, v188, s48\n"
    "v_pk_mul_f16 v232, v188, v232\n"
    "v_exp_f16_e32 v234, v232\n"
    "s_waitcnt lgkmcnt(7)\n"
    "v_mfma_f32_16x16x32_bf16 v[176:179], a[80:83], v[156:159], 0\n"
    "ds_read_b128 v[32:35], v244 offset:16384\n"
    "ds_read_u16 v195, v195\n"
    "s_waitcnt lgkmcnt(8)\n"
    "v_mfma_f32_16x16x32_bf16 v[176:179], a[128:131], v[160:163], v[176:179]\n"
    "s_waitcnt lgkmcnt(7)\n"
    "ds_read_u16_d16_hi v203, v203\n"
    "v_or_b32_e32 v191, v191, v207\n"
    "v_mfma_f32_16x16x32_bf16 v[176:179], a[48:51], v[164:167], v[176:179]\n"
    "s_waitcnt lgkmcnt(7)\n"
    "v_lshlrev_b32_sdwa v207, 1, v183 dst_sel:DWORD dst_unused:UNUSED_PAD src0_sel:DWORD src1_sel:WORD_1\n"
    "v_exp_f16_sdwa v234, v232 dst_sel:WORD_1 dst_unused:UNUSED_PRESERVE src0_sel:WORD_1\n"
    "v_mfma_f32_16x16x32_bf16 v[172:175], v[48:51], v[152:155], v[172:175]\n"
    "s_barrier\n"
    "v_mfma_f32_16x16x32_bf16 v[180:183], a[84:87], v[156:159], 0\n"
    "s_waitcnt lgkmcnt(5)\n"
    "ds_read_b128 v[36:39], v244 offset:18432\n"
    "ds_write_b128 v253, v[64:67] offset:2048\n"
    "v_mfma_f32_16x16x32_bf16 v[180:183], a[132:135], v[160:163], v[180:183]\n"
    "v_pk_fma_f16 v234, v234, s49, s49\n"
    "v_or_b32_e64 v193, v193, v201\n"
    "v_mfma_f32_16x16x32_bf16 v[180:183], v[48:51], v[164:167], v[180:183]\n"
    "v_rcp_f16_e32 v232, v234\n"
    "v_mfma_f32_16x16x32_bf16 v[96:99], a[192:195], v[184:187], v[96:99]\n"
    "s_waitcnt lgkmcnt(4)\n"
    "ds_read_u16 v197, v197\n"
    "ds_write_b128 v253, v[68:71] offset:5120\n"
    "v_mfma_f32_16x16x32_bf16 v[100:103], a[196:199], v[184:187], v[100:103]\n"
    "v_rcp_f16_sdwa v232, v234 dst_sel:WORD_1 dst_unused:UNUSED_PRESERVE src0_sel:WORD_1\n"
    "v_mfma_f32_16x16x32_bf16 v[112:115], a[200:203], v[184:187], v[112:115]\n"
    "v_fma_mix_f32 v234, v188, v232, 0 op_sel_hi:[1,1,0]\n"
    "v_fma_mix_f32 v232, v188, v232, 0 op_sel:[1,1,0] op_sel_hi:[1,1,0]\n"
    "v_mfma_f32_16x16x32_bf16 v[116:119], a[204:207], v[184:187], v[116:119]\n"
    "s_waitcnt lgkmcnt(3)\n"
    "ds_read_u16_d16_hi v205, v205\n"
    "ds_write_b128 v253, v[88:91] offset:0\n"
    "v_mfma_f32_16x16x32_bf16 v[128:131], v[32:35], v[184:187], v[128:131]\n"
    "v_or_b32_e32 v194, v194, v202\n"
    "v_cvt_pk_bf16_f32 v188, v234, v232\n"
    "v_mfma_f32_16x16x32_bf16 v[132:135], v[36:39], v[184:187], v[132:135]\n"
    "v_pk_fma_f16 v232, v192, v192, s48\n"
    "v_or_b32_e64 v195, v195, v203\n"
    "v_mfma_f32_16x16x32_bf16 v[104:107], a[192:195], v[188:191], v[104:107]\n"
    "ds_read_u16 v198, v198\n"
    "ds_write_b128 v253, v[92:95] offset:3072\n"
    "v_mfma_f32_16x16x32_bf16 v[108:111], a[196:199], v[188:191], v[108:111]\n"
    "v_pk_mul_f16 v232, v192, v232\n"
    "v_lshlrev_b32_sdwa v184, 1, v168 dst_sel:DWORD dst_unused:UNUSED_PAD src0_sel:DWORD src1_sel:WORD_1\n"
    "v_mfma_f32_16x16x32_bf16 v[120:123], a[200:203], v[188:191], v[120:123]\n"
    "v_exp_f16_e64 v234, v232\n"
    "v_mfma_f32_16x16x32_bf16 v[124:127], a[204:207], v[188:191], v[124:127]\n"
    "ds_read_u16_d16_hi v206, v206\n"
    "ds_read_u16 v199, v199\n"
    "v_mfma_f32_16x16x32_bf16 v[136:139], v[32:35], v[188:191], v[136:139]\n"
    "ds_read_u16_d16_hi v207, v207\n"
    "ds_read_u16 v184, v184\n"
    "v_mfma_f32_16x16x32_bf16 v[140:143], v[36:39], v[188:191], v[140:143]\n"
    "s_waitcnt lgkmcnt(6)\n"
    "v_or_b32_e32 v197, v197, v205\n"
    "v_exp_f16_sdwa v234, v232 dst_sel:WORD_1 dst_unused:UNUSED_PRESERVE src0_sel:WORD_1\n"
    "v_lshlrev_b32_sdwa v200, 1, v169 dst_sel:DWORD dst_unused:UNUSED_PAD src0_sel:DWORD src1_sel:WORD_1\n"
    "v_pk_fma_f16 v234, v234, s49, s49\n"
    "v_lshlrev_b32_sdwa v185, 1, v170 dst_sel:DWORD dst_unused:UNUSED_PAD src0_sel:DWORD src1_sel:WORD_1\n"
    "v_lshlrev_b32_sdwa v201, 1, v171 dst_sel:DWORD dst_unused:UNUSED_PAD src0_sel:DWORD src1_sel:WORD_1\n"
    "v_mfma_f32_16x16x32_bf16 v[168:171], a[88:91], v[144:147], 0\n"
    "v_lshlrev_b32_sdwa v186, 1, v172 dst_sel:DWORD dst_unused:UNUSED_PAD src0_sel:DWORD src1_sel:WORD_1\n"
    "v_lshlrev_b32_sdwa v202, 1, v173 dst_sel:DWORD dst_unused:UNUSED_PAD src0_sel:DWORD src1_sel:WORD_1\n"
    "v_mfma_f32_16x16x32_bf16 v[168:171], a[136:139], v[148:151], v[168:171]\n"
    "ds_write_b128 v253, v[72:75] offset:1024\n"
    "v_lshlrev_b32_sdwa v187, 1, v174 dst_sel:DWORD dst_unused:UNUSED_PAD src0_sel:DWORD src1_sel:WORD_1\n"
    "v_lshlrev_b32_sdwa v203, 1, v175 dst_sel:DWORD dst_unused:UNUSED_PAD src0_sel:DWORD src1_sel:WORD_1\n"
    "v_rcp_f16_e64 v232, v234\n"
    "v_mfma_f32_16x16x32_bf16 v[168:171], a[52:55], v[152:155], v[168:171]\n"
    "v_rcp_f16_sdwa v232, v234 dst_sel:WORD_1 dst_unused:UNUSED_PRESERVE src0_sel:WORD_1\n"
    "v_mfma_f32_16x16x32_bf16 v[172:175], a[92:95], v[144:147], 0\n"
    "ds_read_u16_d16_hi v200, v200\n"
    "ds_write_b128 v253, v[76:79] offset:4096\n"
    "s_waitcnt lgkmcnt(6)\n"
    "v_or_b32_e32 v198, v198, v206\n"
    "v_lshlrev_b32_sdwa v188, 1, v176 dst_sel:DWORD dst_unused:UNUSED_PAD src0_sel:DWORD src1_sel:WORD_1\n"
    "v_lshlrev_b32_sdwa v204, 1, v177 dst_sel:DWORD dst_unused:UNUSED_PAD src0_sel:DWORD src1_sel:WORD_1\n"
    "v_lshlrev_b32_sdwa v189, 1, v178 dst_sel:DWORD dst_unused:UNUSED_PAD src0_sel:DWORD src1_sel:WORD_1\n"
    "v_lshlrev_b32_sdwa v205, 1, v179 dst_sel:DWORD dst_unused:UNUSED_PAD src0_sel:DWORD src1_sel:WORD_1\n"
    "v_lshlrev_b32_sdwa v190, 1, v180 dst_sel:DWORD dst_unused:UNUSED_PAD src0_sel:DWORD src1_sel:WORD_1\n"
    "v_lshlrev_b32_sdwa v206, 1, v181 dst_sel:DWORD dst_unused:UNUSED_PAD src0_sel:DWORD src1_sel:WORD_1\n"
    "v_lshlrev_b32_sdwa v191, 1, v182 dst_sel:DWORD dst_unused:UNUSED_PAD src0_sel:DWORD src1_sel:WORD_1\n"
    "v_fma_mix_f32 v234, v192, v232, 0 op_sel_hi:[1,1,0]\n"
    "v_mfma_f32_16x16x32_bf16 v[172:175], a[140:143], v[148:151], v[172:175]\n"
    "s_waitcnt lgkmcnt(4)\n"
    "ds_read_u16 v185, v185\n"
    "ds_read_u16_d16_hi v201, v201\n"
    "v_mfma_f32_16x16x32_bf16 v[172:175], v[56:59], v[152:155], v[172:175]\n"
    "s_nop 0\n"
    "ds_read_u16 v186, v186\n"
    "ds_read_b128 v[40:43], v244 offset:20480\n"
    "v_or_b32_e64 v199, v199, v207\n"
    "v_lshlrev_b32_sdwa v207, 1, v183 dst_sel:DWORD dst_unused:UNUSED_PAD src0_sel:DWORD src1_sel:WORD_1\n"
    "v_fma_mix_f32 v232, v192, v232, 0 op_sel:[1,1,0] op_sel_hi:[1,1,0]\n"
    "v_mfma_f32_16x16x32_bf16 v[176:179], a[88:91], v[156:159], 0\n"
    "ds_read_u16_d16_hi v202, v202\n"
    "v_cvt_pk_bf16_f32 v192, v234, v232\n"
    "v_mfma_f32_16x16x32_bf16 v[176:179], a[136:139], v[160:163], v[176:179]\n"
    "v_pk_fma_f16 v232, v196, v196, s48\n"
    "v_pk_mul_f16 v232, v196, v232\n"
    "v_mfma_f32_16x16x32_bf16 v[176:179], a[52:55], v[164:167], v[176:179]\n"
    "ds_read_u16 v187, v187\n"
    "v_exp_f16_e32 v234, v232\n"
    "v_mfma_f32_16x16x32_bf16 v[180:183], a[92:95], v[156:159], 0\n"
    "s_waitcnt lgkmcnt(6)\n"
    "ds_read_b128 v[44:47], v244 offset:22528\n"
    "v_exp_f16_sdwa v234, v232 dst_sel:WORD_1 dst_unused:UNUSED_PRESERVE src0_sel:WORD_1\n"
    "v_mfma_f32_16x16x32_bf16 v[180:183], a[140:143], v[160:163], v[180:183]\n"
    "ds_read_u16_d16_hi v203, v203\n"
    "v_pk_fma_f16 v234, v234, s49, s49\n"
    "v_mfma_f32_16x16x32_bf16 v[180:183], v[56:59], v[164:167], v[180:183]\n"
    "v_rcp_f16_e64 v232, v234\n"
    "v_mfma_f32_16x16x32_bf16 v[96:99], a[208:211], v[192:195], v[96:99]\n"
    "ds_read_u16 v188, v188\n"
    "v_rcp_f16_sdwa v232, v234 dst_sel:WORD_1 dst_unused:UNUSED_PRESERVE src0_sel:WORD_1\n"
    "v_mfma_f32_16x16x32_bf16 v[100:103], a[212:215], v[192:195], v[100:103]\n"
    "ds_read_u16_d16_hi v204, v204\n"
    "v_fma_mix_f32 v234, v196, v232, 0 op_sel_hi:[1,1,0]\n"
    "v_mfma_f32_16x16x32_bf16 v[112:115], a[216:219], v[192:195], v[112:115]\n"
    "v_fma_mix_f32 v232, v196, v232, 0 op_sel:[1,1,0] op_sel_hi:[1,1,0]\n"
    "v_or_b32_e32 v184, v184, v200\n"
    "v_mfma_f32_16x16x32_bf16 v[116:119], a[220:223], v[192:195], v[116:119]\n"
    "s_waitcnt lgkmcnt(8)\n"
    "ds_read_u16 v189, v189\n"
    "v_cvt_pk_bf16_f32 v196, v234, v232\n"
    "v_or_b32_e32 v185, v185, v201\n"
    "s_waitcnt lgkmcnt(7)\n"
    "v_mfma_f32_16x16x32_bf16 v[128:131], v[40:43], v[192:195], v[128:131]\n"
    "ds_read_u16_d16_hi v205, v205\n"
    "v_lshlrev_b32_sdwa v201, 1, v171 dst_sel:DWORD dst_unused:UNUSED_PAD src0_sel:DWORD src1_sel:WORD_1\n"
    "v_mfma_f32_16x16x32_bf16 v[104:107], a[208:211], v[196:199], v[104:107]\n"
    "v_cvt_pk_bf16_f32 v96, v96, v97\n"
    "v_cvt_pk_bf16_f32 v97, v98, v99\n"
    "v_mfma_f32_16x16x32_bf16 v[108:111], a[212:215], v[196:199], v[108:111]\n"
    "ds_read_u16 v190, v190\n"
    "v_cvt_pk_bf16_f32 v98, v100, v101\n"
    "v_mfma_f32_16x16x32_bf16 v[120:123], a[216:219], v[196:199], v[120:123]\n"
    "s_waitcnt lgkmcnt(8)\n"
    "v_or_b32_e64 v186, v186, v202\n"
    "v_lshlrev_b32_sdwa v202, 1, v173 dst_sel:DWORD dst_unused:UNUSED_PAD src0_sel:DWORD src1_sel:WORD_1\n"
    "v_mfma_f32_16x16x32_bf16 v[124:127], a[220:223], v[196:199], v[124:127]\n"
    "s_waitcnt lgkmcnt(6)\n"
    "ds_read_u16_d16_hi v206, v206\n"
    "v_cvt_pk_bf16_f32 v99, v102, v103\n"
    "v_mfma_f32_16x16x32_bf16 v[132:135], v[44:47], v[192:195], v[132:135]\n"
    "v_cvt_pk_f16_f32 v192, v168, v169\n"
    "v_lshlrev_b32_sdwa v193, 1, v170 dst_sel:DWORD dst_unused:UNUSED_PAD src0_sel:DWORD src1_sel:WORD_1\n"
    "v_mfma_f32_16x16x32_bf16 v[136:139], v[40:43], v[196:199], v[136:139]\n"
    "v_lshlrev_b32_sdwa v194, 1, v172 dst_sel:DWORD dst_unused:UNUSED_PAD src0_sel:DWORD src1_sel:WORD_1\n"
    "v_lshlrev_b32_sdwa v195, 1, v174 dst_sel:DWORD dst_unused:UNUSED_PAD src0_sel:DWORD src1_sel:WORD_1\n"
    "v_mfma_f32_16x16x32_bf16 v[140:143], v[44:47], v[196:199], v[140:143]\n"
    "s_waitcnt lgkmcnt(6)\n"
    "ds_write_b128 v242, v[96:99] offset:24640\n"
    "v_mfma_f32_16x16x32_bf16 v[168:171], a[96:99], v[144:147], 0\n"
    "s_nop 0\n"
    "v_cvt_pk_f16_f32 v196, v176, v177\n"
    "v_lshlrev_b32_sdwa v197, 1, v178 dst_sel:DWORD dst_unused:UNUSED_PAD src0_sel:DWORD src1_sel:WORD_1\n"
    "v_mfma_f32_16x16x32_bf16 v[168:171], a[144:147], v[148:151], v[168:171]\n"
    "v_lshlrev_b32_sdwa v198, 1, v180 dst_sel:DWORD dst_unused:UNUSED_PAD src0_sel:DWORD src1_sel:WORD_1\n"
    "v_lshlrev_b32_sdwa v199, 1, v182 dst_sel:DWORD dst_unused:UNUSED_PAD src0_sel:DWORD src1_sel:WORD_1\n"
    "v_mfma_f32_16x16x32_bf16 v[168:171], a[56:59], v[152:155], v[168:171]\n"
    "s_waitcnt lgkmcnt(5)\n"
    "ds_read_u16 v191, v191\n"
    "ds_read_u16_d16_hi v207, v207\n"
    "v_or_b32_e32 v187, v187, v203\n"
    "v_lshlrev_b32_sdwa v203, 1, v175 dst_sel:DWORD dst_unused:UNUSED_PAD src0_sel:DWORD src1_sel:WORD_1\n"
    "v_mfma_f32_16x16x32_bf16 v[172:175], a[100:103], v[144:147], 0\n"
    "ds_read_u16 v193, v193\n"
    "ds_read_u16_d16_hi v201, v201\n"
    "v_mfma_f32_16x16x32_bf16 v[172:175], a[148:151], v[148:151], v[172:175]\n"
    "v_or_b32_e64 v188, v188, v204\n"
    "v_cvt_pk_bf16_f32 v112, v112, v113\n"
    "v_mfma_f32_16x16x32_bf16 v[172:175], a[60:63], v[152:155], v[172:175]\n"
    "s_waitcnt lgkmcnt(7)\n"
    "ds_read_u16 v194, v194\n"
    "v_or_b32_e32 v189, v189, v205\n"
    "v_lshlrev_b32_sdwa v205, 1, v179 dst_sel:DWORD dst_unused:UNUSED_PAD src0_sel:DWORD src1_sel:WORD_1\n"
    "v_mfma_f32_16x16x32_bf16 v[176:179], a[96:99], v[156:159], 0\n"
    "v_cvt_pk_bf16_f32 v113, v114, v115\n"
    "v_cvt_pk_bf16_f32 v114, v116, v117\n"
    "v_mfma_f32_16x16x32_bf16 v[176:179], a[144:147], v[160:163], v[176:179]\n"
    "ds_read_u16_d16_hi v202, v202\n"
    "v_cvt_pk_bf16_f32 v115, v118, v119\n"
    "v_mfma_f32_16x16x32_bf16 v[176:179], a[56:59], v[164:167], v[176:179]\n"
    "s_waitcnt lgkmcnt(6)\n"
    "v_or_b32_e32 v190, v190, v206\n"
    "v_lshlrev_b32_sdwa v206, 1, v181 dst_sel:DWORD dst_unused:UNUSED_PAD src0_sel:DWORD src1_sel:WORD_1\n"
    "v_mfma_f32_16x16x32_bf16 v[96:99], a[0:3], v[184:187], 0\n"
    "v_cvt_pk_bf16_f32 v128, v128, v129\n"
    "v_cvt_pk_bf16_f32 v129, v130, v131\n"
    "v_mfma_f32_16x16x32_bf16 v[100:103], a[4:7], v[184:187], 0\n"
    "ds_write_b128 v242, v[112:115] offset:26688\n"
    "v_mfma_f32_16x16x32_bf16 v[112:115], a[8:11], v[184:187], 0\n"
    "v_cvt_pk_bf16_f32 v130, v132, v133\n"
    "v_cvt_pk_bf16_f32 v131, v134, v135\n"
    "v_cvt_pk_bf16_f32 v104, v104, v105\n"
    "v_cvt_pk_bf16_f32 v105, v106, v107\n"
    "v_cvt_pk_bf16_f32 v106, v108, v109\n"
    "v_cvt_pk_bf16_f32 v107, v110, v111\n"
    "ds_write_b128 v242, v[128:131] offset:28736\n"
    "s_waitcnt lgkmcnt(5)\n"
    "v_or_b32_e32 v191, v191, v207\n"
    "v_mfma_f32_16x16x32_bf16 v[116:119], a[12:15], v[184:187], 0\n"
    "v_lshlrev_b32_sdwa v207, 1, v183 dst_sel:DWORD dst_unused:UNUSED_PAD src0_sel:DWORD src1_sel:WORD_1\n"
    "v_cvt_pk_bf16_f32 v120, v120, v121\n"
    "v_mfma_f32_16x16x32_bf16 v[180:183], a[100:103], v[156:159], 0\n"
    "s_waitcnt lgkmcnt(4)\n"
    "ds_read_u16 v195, v195\n"
    "ds_write_b128 v242, v[104:107] offset:25664\n"
    "v_mfma_f32_16x16x32_bf16 v[180:183], a[148:151], v[160:163], v[180:183]\n"
    "v_or_b32_e32 v193, v193, v201\n"
    "v_cvt_pk_bf16_f32 v121, v122, v123\n"
    "v_mfma_f32_16x16x32_bf16 v[180:183], a[60:63], v[164:167], v[180:183]\n"
    "v_cvt_pk_bf16_f32 v122, v124, v125\n"
    "v_cvt_pk_bf16_f32 v123, v126, v127\n"
    "v_mfma_f32_16x16x32_bf16 v[128:131], v[0:3], v[184:187], 0\n"
    "s_waitcnt lgkmcnt(3)\n"
    "ds_read_u16_d16_hi v203, v203\n"
    "ds_write_b128 v242, v[120:123] offset:27712\n"
    "v_mfma_f32_16x16x32_bf16 v[132:135], v[4:7], v[184:187], 0\n"
    "v_or_b32_e32 v194, v194, v202\n"
    "v_cvt_pk_bf16_f32 v136, v136, v137\n"
    "v_mfma_f32_16x16x32_bf16 v[104:107], a[0:3], v[188:191], 0\n"
    "v_pk_fma_f16 v232, v192, v192, s48\n"
    "v_cvt_pk_bf16_f32 v137, v138, v139\n"
    "v_mfma_f32_16x16x32_bf16 v[108:111], a[4:7], v[188:191], 0\n"
    "ds_read_u16 v197, v197\n"
    "ds_read_u16_d16_hi v205, v205\n"
    "v_mfma_f32_16x16x32_bf16 v[120:123], a[8:11], v[188:191], 0\n"
    "ds_read_u16 v198, v198\n"
    "ds_read_u16_d16_hi v206, v206\n"
    "v_mfma_f32_16x16x32_bf16 v[124:127], a[12:15], v[188:191], 0\n"
    "v_cvt_pk_bf16_f32 v138, v140, v141\n"
    "v_cvt_pk_bf16_f32 v139, v142, v143\n"
    "v_pk_mul_f16 v232, v192, v232\n"
    "v_cvt_pk_f16_f32 v184, v168, v169\n"
    "v_exp_f16_e64 v234, v232\n"
    "v_mfma_f32_16x16x32_bf16 v[140:143], v[4:7], v[188:191], 0\n"
    "v_exp_f16_sdwa v234, v232 dst_sel:WORD_1 dst_unused:UNUSED_PRESERVE src0_sel:WORD_1\n"
    "ds_write_b128 v242, v[136:139] offset:29760\n"
    "v_mfma_f32_16x16x32_bf16 v[136:139], v[0:3], v[188:191], 0\n"
    "s_waitcnt lgkmcnt(5)\n"
    "v_or_b32_e32 v195, v195, v203\n"
    "v_pk_fma_f16 v234, v234, s49, s49\n"
    "v_lshlrev_b32_sdwa v185, 1, v170 dst_sel:DWORD dst_unused:UNUSED_PAD src0_sel:DWORD src1_sel:WORD_1\n"
    "v_lshlrev_b32_sdwa v201, 1, v171 dst_sel:DWORD dst_unused:UNUSED_PAD src0_sel:DWORD src1_sel:WORD_1\n"
    "v_mfma_f32_16x16x32_bf16 v[168:171], a[104:107], v[144:147], 0\n"
    "ds_read_u16 v199, v199\n"
    "ds_read_u16_d16_hi v207, v207\n"
    "v_mfma_f32_16x16x32_bf16 v[168:171], a[152:155], v[148:151], v[168:171]\n"
    "ds_read_u16 v185, v185\n"
    "v_lshlrev_b32_sdwa v186, 1, v172 dst_sel:DWORD dst_unused:UNUSED_PAD src0_sel:DWORD src1_sel:WORD_1\n"
    "v_lshlrev_b32_sdwa v202, 1, v173 dst_sel:DWORD dst_unused:UNUSED_PAD src0_sel:DWORD src1_sel:WORD_1\n"
    "v_lshlrev_b32_sdwa v187, 1, v174 dst_sel:DWORD dst_unused:UNUSED_PAD src0_sel:DWORD src1_sel:WORD_1\n"
    "v_lshlrev_b32_sdwa v203, 1, v175 dst_sel:DWORD dst_unused:UNUSED_PAD src0_sel:DWORD src1_sel:WORD_1\n"
    "v_rcp_f16_e64 v232, v234\n"
    "v_mfma_f32_16x16x32_bf16 v[168:171], a[64:67], v[152:155], v[168:171]\n"
    "ds_read_u16_d16_hi v201, v201\n"
    "s_waitcnt lgkmcnt(7)\n"
    "v_or_b32_e32 v197, v197, v205\n"
    "v_rcp_f16_sdwa v232, v234 dst_sel:WORD_1 dst_unused:UNUSED_PRESERVE src0_sel:WORD_1\n"
    "v_mfma_f32_16x16x32_bf16 v[172:175], a[108:111], v[144:147], 0\n"
    "ds_read_u16 v186, v186\n"
    "v_cvt_pk_f16_f32 v188, v176, v177\n"
    "v_mfma_f32_16x16x32_bf16 v[172:175], a[156:159], v[148:151], v[172:175]\n"
    "ds_read_u16_d16_hi v202, v202\n"
    "v_lshlrev_b32_sdwa v189, 1, v178 dst_sel:DWORD dst_unused:UNUSED_PAD src0_sel:DWORD src1_sel:WORD_1\n"
    "v_lshlrev_b32_sdwa v205, 1, v179 dst_sel:DWORD dst_unused:UNUSED_PAD src0_sel:DWORD src1_sel:WORD_1\n"
    "v_lshlrev_b32_sdwa v190, 1, v180 dst_sel:DWORD dst_unused:UNUSED_PAD src0_sel:DWORD src1_sel:WORD_1\n"
    "s_waitcnt lgkmcnt(6)\n"
    "v_or_b32_e32 v198, v198, v206\n"
    "v_lshlrev_b32_sdwa v206, 1, v181 dst_sel:DWORD dst_unused:UNUSED_PAD src0_sel:DWORD src1_sel:WORD_1\n"
    "v_lshlrev_b32_sdwa v191, 1, v182 dst_sel:DWORD dst_unused:UNUSED_PAD src0_sel:DWORD src1_sel:WORD_1\n"
    "v_fma_mix_f32 v234, v192, v232, 0 op_sel_hi:[1,1,0]\n"
    "v_mfma_f32_16x16x32_bf16 v[172:175], a[68:71], v[152:155], v[172:175]\n"
    "s_barrier\n"
    "v_fma_mix_f32 v232, v192, v232, 0 op_sel:[1,1,0] op_sel_hi:[1,1,0]\n"
    "v_mfma_f32_16x16x32_bf16 v[176:179], a[104:107], v[156:159], 0\n"
    "s_waitcnt lgkmcnt(4)\n"
    "ds_read_u16 v187, v187\n"
    "ds_read_u16_d16_hi v203, v203\n"
    "v_or_b32_e64 v199, v199, v207\n"
    "v_lshlrev_b32_sdwa v207, 1, v183 dst_sel:DWORD dst_unused:UNUSED_PAD src0_sel:DWORD src1_sel:WORD_1\n"
    "v_cvt_pk_bf16_f32 v192, v234, v232\n"
    "v_mfma_f32_16x16x32_bf16 v[176:179], a[152:155], v[160:163], v[176:179]\n"
    "ds_read_u16 v189, v189\n"
    "ds_read_u16_d16_hi v205, v205\n"
    "v_mfma_f32_16x16x32_bf16 v[176:179], a[64:67], v[164:167], v[176:179]\n"
    "s_nop 0\n"
    "v_pk_fma_f16 v232, v196, v196, s48\n"
    "v_lshlrev_b32_sdwa v200, 1, v169 dst_sel:DWORD dst_unused:UNUSED_PAD src0_sel:DWORD src1_sel:WORD_1\n"
    "v_mfma_f32_16x16x32_bf16 v[180:183], a[108:111], v[156:159], 0\n"
    "s_waitcnt lgkmcnt(6)\n"
    "ds_read_u16 v190, v190\n"
    "v_pk_mul_f16 v232, v196, v232\n"
    "v_mfma_f32_16x16x32_bf16 v[180:183], a[156:159], v[160:163], v[180:183]\n"
    "ds_read_u16_d16_hi v206, v206\n"
    "v_exp_f16_e32 v234, v232\n"
    "v_mfma_f32_16x16x32_bf16 v[180:183], a[68:71], v[164:167], v[180:183]\n"
    "v_exp_f16_sdwa v234, v232 dst_sel:WORD_1 dst_unused:UNUSED_PRESERVE src0_sel:WORD_1\n"
    "v_mfma_f32_16x16x32_bf16 v[96:99], a[16:19], v[192:195], v[96:99]\n"
    "s_waitcnt lgkmcnt(6)\n"
    "ds_read_u16 v191, v191\n"
    "v_pk_fma_f16 v234, v234, s49, s49\n"
    "v_mfma_f32_16x16x32_bf16 v[100:103], a[20:23], v[192:195], v[100:103]\n"
    "v_rcp_f16_e64 v232, v234\n"
    "v_mfma_f32_16x16x32_bf16 v[112:115], a[24:27], v[192:195], v[112:115]\n"
    "ds_read_u16_d16_hi v207, v207\n"
    "v_rcp_f16_sdwa v232, v234 dst_sel:WORD_1 dst_unused:UNUSED_PRESERVE src0_sel:WORD_1\n"
    "v_mfma_f32_16x16x32_bf16 v[116:119], a[28:31], v[192:195], v[116:119]\n"
    "v_or_b32_e64 v185, v185, v201\n"
    "v_fma_mix_f32 v234, v196, v232, 0 op_sel_hi:[1,1,0]\n"
    "v_mfma_f32_16x16x32_bf16 v[128:131], v[8:11], v[192:195], v[128:131]\n"
    "v_fma_mix_f32 v232, v196, v232, 0 op_sel:[1,1,0] op_sel_hi:[1,1,0]\n"
    "v_or_b32_e32 v186, v186, v202\n"
    "v_mfma_f32_16x16x32_bf16 v[132:135], v[12:15], v[192:195], v[132:135]\n"
    "s_waitcnt lgkmcnt(6)\n"
    "v_cvt_pk_bf16_f32 v196, v234, v232\n"
    "v_pk_fma_f16 v232, v184, v184, s48\n"
    "v_or_b32_e64 v187, v187, v203\n"
    "v_mfma_f32_16x16x32_bf16 v[104:107], a[16:19], v[196:199], v[104:107]\n"
    "ds_read_u16_d16_hi v200, v200\n"
    "v_pk_mul_f16 v232, v184, v232\n"
    "v_mfma_f32_16x16x32_bf16 v[108:111], a[20:23], v[196:199], v[108:111]\n"
    "v_exp_f16_e32 v234, v232\n"
    "v_mfma_f32_16x16x32_bf16 v[120:123], a[24:27], v[196:199], v[120:123]\n"
    "s_waitcnt lgkmcnt(5)\n"
    "v_or_b32_e32 v189, v189, v205\n"
    "v_exp_f16_sdwa v234, v232 dst_sel:WORD_1 dst_unused:UNUSED_PRESERVE src0_sel:WORD_1\n"
    "v_mfma_f32_16x16x32_bf16 v[124:127], a[28:31], v[196:199], v[124:127]\n"
    "s_waitcnt vmcnt(7)\n"
    "v_pk_fma_f16 v234, v234, s49, s49\n"
    "v_lshlrev_b32_sdwa v192, 1, v168 dst_sel:DWORD dst_unused:UNUSED_PAD src0_sel:DWORD src1_sel:WORD_1\n"
    "v_mfma_f32_16x16x32_bf16 v[136:139], v[8:11], v[196:199], v[136:139]\n"
    "s_nop 0\n"
    "ds_read_u16 v192, v192\n"
    "v_lshlrev_b32_sdwa v193, 1, v170 dst_sel:DWORD dst_unused:UNUSED_PAD src0_sel:DWORD src1_sel:WORD_1\n"
    "v_mfma_f32_16x16x32_bf16 v[140:143], v[12:15], v[196:199], v[140:143]\n"
    "s_waitcnt vmcnt(6) lgkmcnt(4)\n"
    "ds_read_u16 v193, v193\n"
    "v_lshlrev_b32_sdwa v201, 1, v171 dst_sel:DWORD dst_unused:UNUSED_PAD src0_sel:DWORD src1_sel:WORD_1\n"
    "v_smfmac_f32_32x32x32_bf16 v[112:127], a[248:251], a[232:239], v241\n"
    "ds_read_u16_d16_hi v201, v201\n"
    "ds_read_b128 a[232:235], v243 offset:24640\n"
    "v_lshlrev_b32_sdwa v194, 1, v172 dst_sel:DWORD dst_unused:UNUSED_PAD src0_sel:DWORD src1_sel:WORD_1\n"
    "v_lshlrev_b32_sdwa v202, 1, v173 dst_sel:DWORD dst_unused:UNUSED_PAD src0_sel:DWORD src1_sel:WORD_1\n"
    "v_lshlrev_b32_sdwa v195, 1, v174 dst_sel:DWORD dst_unused:UNUSED_PAD src0_sel:DWORD src1_sel:WORD_1\n"
    "v_smfmac_f32_32x32x32_bf16 v[96:111], a[248:251], a[224:231], v241\n"
    "ds_read_u16 v194, v194\n"
    "ds_read_u16_d16_hi v202, v202\n"
    "v_lshlrev_b32_sdwa v203, 1, v175 dst_sel:DWORD dst_unused:UNUSED_PAD src0_sel:DWORD src1_sel:WORD_1\n"
    "v_rcp_f16_e32 v232, v234\n"
    "v_smfmac_f32_32x32x32_bf16 v[128:143], a[248:251], v[208:215], v241\n"
    "s_waitcnt lgkmcnt(6)\n"
    "ds_read_b128 a[236:239], v243 offset:25664\n"
    "v_or_b32_e64 v190, v190, v206\n"
    "v_rcp_f16_sdwa v232, v234 dst_sel:WORD_1 dst_unused:UNUSED_PRESERVE src0_sel:WORD_1\n"
    "v_lshlrev_b32_sdwa v196, 1, v176 dst_sel:DWORD dst_unused:UNUSED_PAD src0_sel:DWORD src1_sel:WORD_1\n"
    "v_mfma_f32_16x16x32_bf16 v[168:171], a[112:115], v[144:147], 0\n"
    "ds_read_u16 v195, v195\n"
    "v_lshlrev_b32_sdwa v204, 1, v177 dst_sel:DWORD dst_unused:UNUSED_PAD src0_sel:DWORD src1_sel:WORD_1\n"
    "v_mfma_f32_16x16x32_bf16 v[168:171], a[160:163], v[148:151], v[168:171]\n"
    "ds_read_u16_d16_hi v203, v203\n"
    "v_lshlrev_b32_sdwa v197, 1, v178 dst_sel:DWORD dst_unused:UNUSED_PAD src0_sel:DWORD src1_sel:WORD_1\n"
    "v_mfma_f32_16x16x32_bf16 v[168:171], a[72:75], v[152:155], v[168:171]\n"
    "v_lshlrev_b32_sdwa v205, 1, v179 dst_sel:DWORD dst_unused:UNUSED_PAD src0_sel:DWORD src1_sel:WORD_1\n"
    "v_lshlrev_b32_sdwa v198, 1, v180 dst_sel:DWORD dst_unused:UNUSED_PAD src0_sel:DWORD src1_sel:WORD_1\n"
    "v_mfma_f32_16x16x32_bf16 v[172:175], a[116:119], v[144:147], 0\n"
    "v_lshlrev_b32_sdwa v206, 1, v181 dst_sel:DWORD dst_unused:UNUSED_PAD src0_sel:DWORD src1_sel:WORD_1\n"
    "v_lshlrev_b32_sdwa v199, 1, v182 dst_sel:DWORD dst_unused:UNUSED_PAD src0_sel:DWORD src1_sel:WORD_1\n"
    "v_fma_mix_f32 v234, v184, v232, 0 op_sel_hi:[1,1,0]\n"
    "v_mfma_f32_16x16x32_bf16 v[172:175], a[164:167], v[148:151], v[172:175]\n"
    "s_waitcnt lgkmcnt(7)\n"
    "ds_read_u16 v196, v196\n"
    "ds_read_u16_d16_hi v204, v204\n"
    "v_mfma_f32_16x16x32_bf16 v[172:175], v[52:55], v[152:155], v[172:175]\n"
    "v_or_b32_e32 v191, v191, v207\n"
    "v_lshlrev_b32_sdwa v207, 1, v183 dst_sel:DWORD dst_unused:UNUSED_PAD src0_sel:DWORD src1_sel:WORD_1\n"
    "v_fma_mix_f32 v232, v184, v232, 0 op_sel:[1,1,0] op_sel_hi:[1,1,0]\n"
    "v_mfma_f32_16x16x32_bf16 v[176:179], a[112:115], v[156:159], 0\n"
    "ds_read_u16 v197, v197\n"
    "v_cvt_pk_bf16_f32 v184, v234, v232\n"
    "v_mfma_f32_16x16x32_bf16 v[176:179], a[160:163], v[160:163], v[176:179]\n"
    "ds_read_u16_d16_hi v205, v205\n"
    "v_pk_fma_f16 v232, v188, v188, s48\n"
    "v_mfma_f32_16x16x32_bf16 v[176:179], a[72:75], v[164:167], v[176:179]\n"
    "s_waitcnt lgkmcnt(8)\n"
    "v_pk_mul_f16 v232, v188, v232\n"
    "v_or_b32_e32 v192, v192, v200\n"
    "v_mfma_f32_16x16x32_bf16 v[180:183], a[116:119], v[156:159], 0\n"
    "ds_read_u16 v198, v198\n"
    "v_exp_f16_e32 v234, v232\n"
    "v_mfma_f32_16x16x32_bf16 v[180:183], a[164:167], v[160:163], v[180:183]\n"
    "s_waitcnt lgkmcnt(7)\n"
    "v_exp_f16_sdwa v234, v232 dst_sel:WORD_1 dst_unused:UNUSED_PRESERVE src0_sel:WORD_1\n"
    "v_smfmac_f32_32x32x32_bf16 v[96:111], a[248:251], a[232:239], v241\n"
    "ds_read_u16_d16_hi v206, v206\n"
    "v_pk_fma_f16 v234, v234, s49, s49\n"
    "v_or_b32_e64 v193, v193, v201\n"
    "v_rcp_f16_e32 v232, v234\n"
    "v_mfma_f32_16x16x32_bf16 v[180:183], v[52:55], v[164:167], v[180:183]\n"
    "ds_read_u16 v199, v199\n"
    "v_rcp_f16_sdwa v232, v234 dst_sel:WORD_1 dst_unused:UNUSED_PRESERVE src0_sel:WORD_1\n"
    "v_mfma_f32_16x16x32_bf16 v[112:115], a[40:43], v[184:187], v[112:115]\n"
    "s_waitcnt lgkmcnt(7)\n"
    "ds_read_u16_d16_hi v207, v207\n"
    "v_fma_mix_f32 v234, v188, v232, 0 op_sel_hi:[1,1,0]\n"
    "v_mfma_f32_16x16x32_bf16 v[96:99], a[32:35], v[184:187], v[96:99]\n"
    "v_fma_mix_f32 v232, v188, v232, 0 op_sel:[1,1,0] op_sel_hi:[1,1,0]\n"
    "v_lshlrev_b32_sdwa v201, 1, v171 dst_sel:DWORD dst_unused:UNUSED_PAD src0_sel:DWORD src1_sel:WORD_1\n"
    "v_mfma_f32_16x16x32_bf16 v[100:103], a[36:39], v[184:187], v[100:103]\n"
    "ds_read_b128 a[232:235], v243 offset:26688\n"
    "ds_read_b128 a[236:239], v243 offset:27712\n"
    "v_mfma_f32_16x16x32_bf16 v[116:119], a[44:47], v[184:187], v[116:119]\n"
    "s_waitcnt lgkmcnt(8)\n"
    "v_cvt_pk_bf16_f32 v188, v234, v232\n"
    "v_or_b32_e32 v194, v194, v202\n"
    "v_mfma_f32_16x16x32_bf16 v[128:131], v[16:19], v[184:187], v[128:131]\n"
    "ds_read_u16_d16_hi v201, v201\n"
    "v_lshlrev_b32_sdwa v202, 1, v173 dst_sel:DWORD dst_unused:UNUSED_PAD src0_sel:DWORD src1_sel:WORD_1\n"
    "v_mfma_f32_16x16x32_bf16 v[132:135], v[20:23], v[184:187], v[132:135]\n"
    "ds_read_u16_d16_hi v202, v202\n"
    "v_cvt_pk_f16_f32 v184, v168, v169\n"
    "v_mfma_f32_16x16x32_bf16 v[104:107], a[32:35], v[188:191], v[104:107]\n"
    "v_lshlrev_b32_sdwa v185, 1, v170 dst_sel:DWORD dst_unused:UNUSED_PAD src0_sel:DWORD src1_sel:WORD_1\n"
    "v_lshlrev_b32_sdwa v186, 1, v172 dst_sel:DWORD dst_unused:UNUSED_PAD src0_sel:DWORD src1_sel:WORD_1\n"
    "v_mfma_f32_16x16x32_bf16 v[108:111], a[36:39], v[188:191], v[108:111]\n"
    "s_waitcnt lgkmcnt(8)\n"
    "ds_read_u16 v185, v185\n"
    "v_lshlrev_b32_sdwa v187, 1, v174 dst_sel:DWORD dst_unused:UNUSED_PAD src0_sel:DWORD src1_sel:WORD_1\n"
    "v_mfma_f32_16x16x32_bf16 v[120:123], a[40:43], v[188:191], v[120:123]\n"
    "v_or_b32_e32 v195, v195, v203\n"
    "v_lshlrev_b32_sdwa v203, 1, v175 dst_sel:DWORD dst_unused:UNUSED_PAD src0_sel:DWORD src1_sel:WORD_1\n"
    "v_mfma_f32_16x16x32_bf16 v[124:127], a[44:47], v[188:191], v[124:127]\n"
    "ds_read_u16 v186, v186\n"
    "v_or_b32_e64 v196, v196, v204\n"
    "v_mfma_f32_16x16x32_bf16 v[136:139], v[16:19], v[188:191], v[136:139]\n"
    "s_waitcnt lgkmcnt(8)\n"
    "v_or_b32_e32 v197, v197, v205\n"
    "v_lshlrev_b32_sdwa v205, 1, v179 dst_sel:DWORD dst_unused:UNUSED_PAD src0_sel:DWORD src1_sel:WORD_1\n"
    "v_mfma_f32_16x16x32_bf16 v[140:143], v[20:23], v[188:191], v[140:143]\n"
    "ds_read_u16 v187, v187\n"
    "v_cvt_pk_f16_f32 v188, v176, v177\n"
    "v_mfma_f32_16x16x32_bf16 v[168:171], a[120:123], v[144:147], 0\n"
    "ds_read_u16_d16_hi v203, v203\n"
    "v_lshlrev_b32_sdwa v189, 1, v178 dst_sel:DWORD dst_unused:UNUSED_PAD src0_sel:DWORD src1_sel:WORD_1\n"
    "v_mfma_f32_16x16x32_bf16 v[168:171], a[168:171], v[148:151], v[168:171]\n"
    "v_lshlrev_b32_sdwa v190, 1, v180 dst_sel:DWORD dst_unused:UNUSED_PAD src0_sel:DWORD src1_sel:WORD_1\n"
    "v_lshlrev_b32_sdwa v191, 1, v182 dst_sel:DWORD dst_unused:UNUSED_PAD src0_sel:DWORD src1_sel:WORD_1\n"
    "v_mfma_f32_16x16x32_bf16 v[168:171], a[76:79], v[152:155], v[168:171]\n"
    "s_waitcnt lgkmcnt(7)\n"
    "ds_read_u16 v189, v189\n"
    "v_or_b32_e32 v198, v198, v206\n"
    "v_mfma_f32_16x16x32_bf16 v[172:175], a[124:127], v[144:147], 0\n"
    "s_waitcnt lgkmcnt(7)\n"
    "v_lshlrev_b32_sdwa v206, 1, v181 dst_sel:DWORD dst_unused:UNUSED_PAD src0_sel:DWORD src1_sel:WORD_1\n"
    "v_or_b32_e32 v199, v199, v207\n"
    "v_smfmac_f32_32x32x32_bf16 v[112:127], a[248:251], a[232:239], v241\n"
    "ds_read_b128 a[232:235], v243 offset:28736\n"
    "ds_read_u16_d16_hi v205, v205\n"
    "v_lshlrev_b32_sdwa v207, 1, v183 dst_sel:DWORD dst_unused:UNUSED_PAD src0_sel:DWORD src1_sel:WORD_1\n"
    "v_pk_fma_f16 v232, v184, v184, s48\n"
    "v_pk_mul_f16 v232, v184, v232\n"
    "v_mfma_f32_16x16x32_bf16 v[172:175], a[172:175], v[148:151], v[172:175]\n"
    "v_exp_f16_e64 v234, v232\n"
    "v_mfma_f32_16x16x32_bf16 v[172:175], v[60:63], v[152:155], v[172:175]\n"
    "ds_read_b128 a[236:239], v243 offset:29760\n"
    "ds_read_u16 v190, v190\n"
    "v_mfma_f32_16x16x32_bf16 v[176:179], a[120:123], v[156:159], 0\n"
    "s_nop 0\n"
    "v_exp_f16_sdwa v234, v232 dst_sel:WORD_1 dst_unused:UNUSED_PRESERVE src0_sel:WORD_1\n"
    "v_mfma_f32_16x16x32_bf16 v[176:179], a[168:171], v[160:163], v[176:179]\n"
    "s_waitcnt lgkmcnt(8)\n"
    "ds_read_b128 v[144:147], v242 offset:0\n"
    "v_or_b32_e64 v185, v185, v201\n"
    "v_mfma_f32_16x16x32_bf16 v[176:179], a[76:79], v[164:167], v[176:179]\n"
    "ds_read_u16_d16_hi v206, v206\n"
    "v_pk_fma_f16 v234, v234, s49, s49\n"
    "v_mfma_f32_16x16x32_bf16 v[180:183], a[124:127], v[156:159], 0\n"
    "s_waitcnt lgkmcnt(9)\n"
    "v_or_b32_e32 v186, v186, v202\n"
    "v_lshlrev_b32_sdwa v201, 1, v171 dst_sel:DWORD dst_unused:UNUSED_PAD src0_sel:DWORD src1_sel:WORD_1\n"
    "v_mfma_f32_16x16x32_bf16 v[180:183], a[172:175], v[160:163], v[180:183]\n"
    "ds_read_b128 v[148:151], v242 offset:1024\n"
    "v_lshlrev_b32_sdwa v202, 1, v173 dst_sel:DWORD dst_unused:UNUSED_PAD src0_sel:DWORD src1_sel:WORD_1\n"
    "v_mfma_f32_16x16x32_bf16 v[180:183], v[60:63], v[164:167], v[180:183]\n"
    "ds_read_u16 v191, v191\n"
    "v_rcp_f16_e64 v232, v234\n"
    "v_mfma_f32_16x16x32_bf16 v[96:99], a[176:179], v[192:195], v[96:99]\n"
    "s_waitcnt lgkmcnt(9)\n"
    "v_or_b32_e32 v187, v187, v203\n"
    "v_lshlrev_b32_sdwa v203, 1, v175 dst_sel:DWORD dst_unused:UNUSED_PAD src0_sel:DWORD src1_sel:WORD_1\n"
    "v_mfma_f32_16x16x32_bf16 v[100:103], a[180:183], v[192:195], v[100:103]\n"
    "ds_read_b128 v[152:155], v242 offset:2048\n"
    "ds_read_u16_d16_hi v207, v207\n"
    "v_mfma_f32_16x16x32_bf16 v[112:115], a[184:187], v[192:195], v[112:115]\n"
    "v_rcp_f16_sdwa v232, v234 dst_sel:WORD_1 dst_unused:UNUSED_PRESERVE src0_sel:WORD_1\n"
    "v_mfma_f32_16x16x32_bf16 v[116:119], a[188:191], v[192:195], v[116:119]\n"
    "ds_read_b128 v[156:159], v242 offset:3072\n"
    "v_fma_mix_f32 v234, v184, v232, 0 op_sel_hi:[1,1,0]\n"
    "v_mfma_f32_16x16x32_bf16 v[128:131], v[24:27], v[192:195], v[128:131]\n"
    "ds_read_b128 v[160:163], v242 offset:4096\n"
    "v_fma_mix_f32 v232, v184, v232, 0 op_sel:[1,1,0] op_sel_hi:[1,1,0]\n"
    "v_mfma_f32_16x16x32_bf16 v[132:135], v[28:31], v[192:195], v[132:135]\n"
    "s_waitcnt lgkmcnt(9)\n"
    "ds_read_b128 v[164:167], v242 offset:5120\n"
    "v_or_b32_e32 v189, v189, v205\n"
    "v_mfma_f32_16x16x32_bf16 v[104:107], a[176:179], v[196:199], v[104:107]\n"
    "ds_read_u16_d16_hi v201, v201\n"
    "v_cvt_pk_f16_f32 v192, v168, v169\n"
    "v_mfma_f32_16x16x32_bf16 v[108:111], a[180:183], v[196:199], v[108:111]\n"
    "s_nop 0\n"
    "v_lshlrev_b32_sdwa v193, 1, v170 dst_sel:DWORD dst_unused:UNUSED_PAD src0_sel:DWORD src1_sel:WORD_1\n"
    "v_lshlrev_b32_sdwa v194, 1, v172 dst_sel:DWORD dst_unused:UNUSED_PAD src0_sel:DWORD src1_sel:WORD_1\n"
    "v_mfma_f32_16x16x32_bf16 v[120:123], a[184:187], v[196:199], v[120:123]\n"
    "s_waitcnt lgkmcnt(9)\n"
    "ds_read_u16 v193, v193\n"
    "v_lshlrev_b32_sdwa v195, 1, v174 dst_sel:DWORD dst_unused:UNUSED_PAD src0_sel:DWORD src1_sel:WORD_1\n"
    "v_mfma_f32_16x16x32_bf16 v[124:127], a[188:191], v[196:199], v[124:127]\n"
    "v_lshlrev_b32_sdwa v205, 1, v179 dst_sel:DWORD dst_unused:UNUSED_PAD src0_sel:DWORD src1_sel:WORD_1\n"
    "v_cvt_pk_bf16_f32 v184, v234, v232\n"
    "v_mfma_f32_16x16x32_bf16 v[136:139], v[24:27], v[196:199], v[136:139]\n"
    "s_waitcnt lgkmcnt(8)\n"
    "ds_read_u16 v194, v194\n"
    "v_or_b32_e32 v190, v190, v206\n"
    "v_mfma_f32_16x16x32_bf16 v[140:143], v[28:31], v[196:199], v[140:143]\n"
    "v_cvt_pk_f16_f32 v196, v176, v177\n"
    "v_lshlrev_b32_sdwa v197, 1, v178 dst_sel:DWORD dst_unused:UNUSED_PAD src0_sel:DWORD src1_sel:WORD_1\n"
    "v_mfma_f32_16x16x32_bf16 v[168:171], a[80:83], v[144:147], 0\n"
    "s_nop 0\n"
    "ds_read_u16_d16_hi v202, v202\n"
    "v_lshlrev_b32_sdwa v198, 1, v180 dst_sel:DWORD dst_unused:UNUSED_PAD src0_sel:DWORD src1_sel:WORD_1\n"
    "v_mfma_f32_16x16x32_bf16 v[168:171], a[128:131], v[148:151], v[168:171]\n"
    "s_waitcnt lgkmcnt(8)\n"
    "ds_read_u16 v195, v195\n"
    "v_lshlrev_b32_sdwa v206, 1, v181 dst_sel:DWORD dst_unused:UNUSED_PAD src0_sel:DWORD src1_sel:WORD_1\n"
    "v_smfmac_f32_32x32x32_bf16 v[128:143], a[248:251], a[232:239], v241\n"
    "v_lshlrev_b32_sdwa v199, 1, v182 dst_sel:DWORD dst_unused:UNUSED_PAD src0_sel:DWORD src1_sel:WORD_1\n"
    "v_pk_fma_f16 v232, v188, v188, s48\n"
    "v_pk_mul_f16 v232, v188, v232\n"
    "v_exp_f16_e32 v234, v232\n"
    "v_mfma_f32_16x16x32_bf16 v[168:171], a[48:51], v[152:155], v[168:171]\n"
    "s_waitcnt lgkmcnt(6)\n"
    "ds_read_u16_d16_hi v203, v203\n"
    "v_or_b32_e64 v191, v191, v207\n"
    "v_mfma_f32_16x16x32_bf16 v[172:175], a[84:87], v[144:147], 0\n"
    "ds_read_u16 v197, v197\n"
    "v_lshlrev_b32_sdwa v207, 1, v183 dst_sel:DWORD dst_unused:UNUSED_PAD src0_sel:DWORD src1_sel:WORD_1\n"
    "v_mfma_f32_16x16x32_bf16 v[172:175], a[132:135], v[148:151], v[172:175]\n"
    "v_exp_f16_sdwa v234, v232 dst_sel:WORD_1 dst_unused:UNUSED_PRESERVE src0_sel:WORD_1\n"
    "v_mfma_f32_16x16x32_bf16 v[172:175], v[48:51], v[152:155], v[172:175]\n"
    "ds_read_u16_d16_hi v205, v205\n"
    "v_pk_fma_f16 v234, v234, s49, s49\n"
    "v_mfma_f32_16x16x32_bf16 v[176:179], a[80:83], v[156:159], 0\n"
    "ds_read_u16 v198, v198\n"
    "v_rcp_f16_e64 v232, v234\n"
    "v_mfma_f32_16x16x32_bf16 v[176:179], a[128:131], v[160:163], v[176:179]\n"
    "s_waitcnt lgkmcnt(8)\n"
    "v_rcp_f16_sdwa v232, v234 dst_sel:WORD_1 dst_unused:UNUSED_PRESERVE src0_sel:WORD_1\n"
    "v_mfma_f32_16x16x32_bf16 v[176:179], a[48:51], v[164:167], v[176:179]\n"
    "s_waitcnt lgkmcnt(7)\n"
    "ds_read_u16_d16_hi v206, v206\n"
    "v_or_b32_e64 v193, v193, v201\n"
    "v_mfma_f32_16x16x32_bf16 v[180:183], a[84:87], v[156:159], 0\n"
    "v_fma_mix_f32 v234, v188, v232, 0 op_sel_hi:[1,1,0]\n"
    "v_fma_mix_f32 v232, v188, v232, 0 op_sel:[1,1,0] op_sel_hi:[1,1,0]\n"
    "v_mfma_f32_16x16x32_bf16 v[180:183], a[132:135], v[160:163], v[180:183]\n"
    "ds_read_u16 v199, v199\n"
    "v_cvt_pk_bf16_f32 v188, v234, v232\n"
    "v_mfma_f32_16x16x32_bf16 v[180:183], v[48:51], v[164:167], v[180:183]\n"
    "s_waitcnt lgkmcnt(7)\n"
    "ds_read_u16_d16_hi v207, v207\n"
    "v_or_b32_e32 v194, v194, v202\n"
    "v_mfma_f32_16x16x32_bf16 v[96:99], a[192:195], v[184:187], v[96:99]\n"
    "v_pk_fma_f16 v232, v192, v192, s48\n"
    "v_lshlrev_b32_sdwa v200, 1, v169 dst_sel:DWORD dst_unused:UNUSED_PAD src0_sel:DWORD src1_sel:WORD_1\n"
    "v_mfma_f32_16x16x32_bf16 v[100:103], a[196:199], v[184:187], v[100:103]\n"
    "ds_read_u16_d16_hi v200, v200\n"
    "v_pk_mul_f16 v232, v192, v232\n"
    "v_mfma_f32_16x16x32_bf16 v[112:115], a[200:203], v[184:187], v[112:115]\n"
    "v_exp_f16_e64 v234, v232\n"
    "v_mfma_f32_16x16x32_bf16 v[116:119], a[204:207], v[184:187], v[116:119]\n"
    "s_waitcnt lgkmcnt(7)\n"
    "v_or_b32_e32 v195, v195, v203\n"
    "v_exp_f16_sdwa v234, v232 dst_sel:WORD_1 dst_unused:UNUSED_PRESERVE src0_sel:WORD_1\n"
    "v_mfma_f32_16x16x32_bf16 v[128:131], v[32:35], v[184:187], v[128:131]\n"
    "v_pk_fma_f16 v234, v234, s49, s49\n"
    "v_lshlrev_b32_sdwa v201, 1, v171 dst_sel:DWORD dst_unused:UNUSED_PAD src0_sel:DWORD src1_sel:WORD_1\n"
    "v_mfma_f32_16x16x32_bf16 v[132:135], v[36:39], v[184:187], v[132:135]\n"
    "ds_read_u16_d16_hi v201, v201\n"
    "v_lshlrev_b32_sdwa v184, 1, v168 dst_sel:DWORD dst_unused:UNUSED_PAD src0_sel:DWORD src1_sel:WORD_1\n"
    "v_mfma_f32_16x16x32_bf16 v[104:107], a[192:195], v[188:191], v[104:107]\n"
    "s_waitcnt lgkmcnt(6)\n"
    "ds_read_u16 v184, v184\n"
    "v_or_b32_e32 v197, v197, v205\n"
    "v_mfma_f32_16x16x32_bf16 v[108:111], a[196:199], v[188:191], v[108:111]\n"
    "v_lshlrev_b32_sdwa v185, 1, v170 dst_sel:DWORD dst_unused:UNUSED_PAD src0_sel:DWORD src1_sel:WORD_1\n"
    "v_lshlrev_b32_sdwa v186, 1, v172 dst_sel:DWORD dst_unused:UNUSED_PAD src0_sel:DWORD src1_sel:WORD_1\n"
    "v_mfma_f32_16x16x32_bf16 v[120:123], a[200:203], v[188:191], v[120:123]\n"
    "ds_read_u16 v185, v185\n"
    "ds_read_u16 v186, v186\n"
    "v_mfma_f32_16x16x32_bf16 v[124:127], a[204:207], v[188:191], v[124:127]\n"
    "v_lshlrev_b32_sdwa v202, 1, v173 dst_sel:DWORD dst_unused:UNUSED_PAD src0_sel:DWORD src1_sel:WORD_1\n"
    "v_lshlrev_b32_sdwa v187, 1, v174 dst_sel:DWORD dst_unused:UNUSED_PAD src0_sel:DWORD src1_sel:WORD_1\n"
    "v_mfma_f32_16x16x32_bf16 v[136:139], v[32:35], v[188:191], v[136:139]\n"
    "s_waitcnt lgkmcnt(7)\n"
    "ds_read_u16_d16_hi v202, v202\n"
    "v_lshlrev_b32_sdwa v203, 1, v175 dst_sel:DWORD dst_unused:UNUSED_PAD src0_sel:DWORD src1_sel:WORD_1\n"
    "v_mfma_f32_16x16x32_bf16 v[140:143], v[36:39], v[188:191], v[140:143]\n"
    "v_rcp_f16_e32 v232, v234\n"
    "v_mfma_f32_16x16x32_bf16 v[168:171], a[88:91], v[144:147], 0\n"
    "ds_read_u16 v187, v187\n"
    "v_or_b32_e32 v198, v198, v206\n"
    "v_mfma_f32_16x16x32_bf16 v[168:171], a[136:139], v[148:151], v[168:171]\n"
    "s_waitcnt lgkmcnt(7)\n"
    "v_rcp_f16_sdwa v232, v234 dst_sel:WORD_1 dst_unused:UNUSED_PRESERVE src0_sel:WORD_1\n"
    "v_mfma_f32_16x16x32_bf16 v[168:171], a[52:55], v[152:155], v[168:171]\n"
    "ds_read_u16_d16_hi v203, v203\n"
    "v_lshlrev_b32_sdwa v188, 1, v176 dst_sel:DWORD dst_unused:UNUSED_PAD src0_sel:DWORD src1_sel:WORD_1\n"
    "v_mfma_f32_16x16x32_bf16 v[172:175], a[92:95], v[144:147], 0\n"
    "v_lshlrev_b32_sdwa v204, 1, v177 dst_sel:DWORD dst_unused:UNUSED_PAD src0_sel:DWORD src1_sel:WORD_1\n"
    "v_lshlrev_b32_sdwa v189, 1, v178 dst_sel:DWORD dst_unused:UNUSED_PAD src0_sel:DWORD src1_sel:WORD_1\n"
    "v_lshlrev_b32_sdwa v205, 1, v179 dst_sel:DWORD dst_unused:UNUSED_PAD src0_sel:DWORD src1_sel:WORD_1\n"
    "v_lshlrev_b32_sdwa v190, 1, v180 dst_sel:DWORD dst_unused:UNUSED_PAD src0_sel:DWORD src1_sel:WORD_1\n"
    "v_lshlrev_b32_sdwa v206, 1, v181 dst_sel:DWORD dst_unused:UNUSED_PAD src0_sel:DWORD src1_sel:WORD_1\n"
    "v_lshlrev_b32_sdwa v191, 1, v182 dst_sel:DWORD dst_unused:UNUSED_PAD src0_sel:DWORD src1_sel:WORD_1\n"
    "v_fma_mix_f32 v234, v192, v232, 0 op_sel_hi:[1,1,0]\n"
    "v_mfma_f32_16x16x32_bf16 v[172:175], a[140:143], v[148:151], v[172:175]\n"
    "ds_read_u16 v188, v188\n"
    "ds_read_u16_d16_hi v204, v204\n"
    "v_mfma_f32_16x16x32_bf16 v[172:175], v[56:59], v[152:155], v[172:175]\n"
    "s_waitcnt lgkmcnt(7)\n"
    "v_or_b32_e32 v199, v199, v207\n"
    "v_lshlrev_b32_sdwa v207, 1, v183 dst_sel:DWORD dst_unused:UNUSED_PAD src0_sel:DWORD src1_sel:WORD_1\n"
    "v_fma_mix_f32 v232, v192, v232, 0 op_sel:[1,1,0] op_sel_hi:[1,1,0]\n"
    "v_mfma_f32_16x16x32_bf16 v[176:179], a[88:91], v[156:159], 0\n"
    "ds_read_u16 v189, v189\n"
    "v_cvt_pk_bf16_f32 v192, v234, v232\n"
    "v_mfma_f32_16x16x32_bf16 v[176:179], a[136:139], v[160:163], v[176:179]\n"
    "ds_read_u16_d16_hi v205, v205\n"
    "v_pk_fma_f16 v232, v196, v196, s48\n"
    "v_mfma_f32_16x16x32_bf16 v[176:179], a[52:55], v[164:167], v[176:179]\n"
    "s_waitcnt lgkmcnt(7)\n"
    "ds_read_u16 v190, v190\n"
    "v_pk_mul_f16 v232, v196, v232\n"
    "v_mfma_f32_16x16x32_bf16 v[180:183], a[92:95], v[156:159], 0\n"
    "v_exp_f16_e64 v234, v232\n"
    "v_mfma_f32_16x16x32_bf16 v[180:183], a[140:143], v[160:163], v[180:183]\n"
    "s_waitcnt lgkmcnt(7)\n"
    "ds_read_u16_d16_hi v206, v206\n"
    "buffer_load_dwordx4 a[232:235], v245, s[40:43], s58 offen offset:64\n"
    "buffer_load_dwordx4 a[224:227], v245, s[40:43], s58 offen offset:0\n"
    "buffer_load_dwordx4 v[208:211], v245, s[40:43], s58 offen offset:128\n"
    "v_mfma_f32_16x16x32_bf16 v[180:183], v[56:59], v[164:167], v[180:183]\n"
    "ds_read_u16 v191, v191\n"
    "v_exp_f16_sdwa v234, v232 dst_sel:WORD_1 dst_unused:UNUSED_PRESERVE src0_sel:WORD_1\n"
    "v_or_b32_e64 v184, v184, v200\n"
    "v_pk_fma_f16 v234, v234, s49, s49\n"
    "v_mfma_f32_16x16x32_bf16 v[96:99], a[208:211], v[192:195], v[96:99]\n"
    "s_waitcnt lgkmcnt(7)\n"
    "ds_read_u16_d16_hi v207, v207\n"
    "v_rcp_f16_e32 v232, v234\n"
    "v_mfma_f32_16x16x32_bf16 v[100:103], a[212:215], v[192:195], v[100:103]\n"
    "v_rcp_f16_sdwa v232, v234 dst_sel:WORD_1 dst_unused:UNUSED_PRESERVE src0_sel:WORD_1\n"
    "v_mfma_f32_16x16x32_bf16 v[112:115], a[216:219], v[192:195], v[112:115]\n"
    "v_fma_mix_f32 v234, v196, v232, 0 op_sel_hi:[1,1,0]\n"
    "v_fma_mix_f32 v232, v196, v232, 0 op_sel:[1,1,0] op_sel_hi:[1,1,0]\n"
    "v_mfma_f32_16x16x32_bf16 v[116:119], a[220:223], v[192:195], v[116:119]\n"
    "v_cvt_pk_bf16_f32 v196, v234, v232\n"
    "v_or_b32_e32 v185, v185, v201\n"
    "v_mfma_f32_16x16x32_bf16 v[128:131], v[40:43], v[192:195], v[128:131]\n"
    "s_waitcnt lgkmcnt(6)\n"
    "v_lshlrev_b32_sdwa v201, 1, v171 dst_sel:DWORD dst_unused:UNUSED_PAD src0_sel:DWORD src1_sel:WORD_1\n"
    "v_or_b32_e64 v186, v186, v202\n"
    "v_mfma_f32_16x16x32_bf16 v[132:135], v[44:47], v[192:195], v[132:135]\n"
    "ds_read_u16_d16_hi v201, v201\n"
    "v_cvt_pk_f16_f32 v192, v168, v169\n"
    "v_mfma_f32_16x16x32_bf16 v[104:107], a[208:211], v[196:199], v[104:107]\n"
    "s_nop 0\n"
    "v_lshlrev_b32_sdwa v193, 1, v170 dst_sel:DWORD dst_unused:UNUSED_PAD src0_sel:DWORD src1_sel:WORD_1\n"
    "v_lshlrev_b32_sdwa v194, 1, v172 dst_sel:DWORD dst_unused:UNUSED_PAD src0_sel:DWORD src1_sel:WORD_1\n"
    "v_mfma_f32_16x16x32_bf16 v[108:111], a[212:215], v[196:199], v[108:111]\n"
    "s_waitcnt lgkmcnt(5)\n"
    "ds_read_u16 v193, v193\n"
    "buffer_load_dwordx4 a[236:239], v245, s[40:43], s89 offen offset:64\n"
    "buffer_load_dwordx4 a[228:231], v245, s[40:43], s89 offen offset:0\n"
    "buffer_load_dwordx4 v[212:215], v245, s[40:43], s89 offen offset:128\n"
    "v_mfma_f32_16x16x32_bf16 v[120:123], a[216:219], v[196:199], v[120:123]\n"
    "ds_read_u16 v194, v194\n"
    "v_lshlrev_b32_sdwa v202, 1, v173 dst_sel:DWORD dst_unused:UNUSED_PAD src0_sel:DWORD src1_sel:WORD_1\n"
    "v_mfma_f32_16x16x32_bf16 v[124:127], a[220:223], v[196:199], v[124:127]\n"
    "ds_read_u16_d16_hi v202, v202\n"
    "v_lshlrev_b32_sdwa v195, 1, v174 dst_sel:DWORD dst_unused:UNUSED_PAD src0_sel:DWORD src1_sel:WORD_1\n"
    "v_mfma_f32_16x16x32_bf16 v[136:139], v[40:43], v[196:199], v[136:139]\n"
    "s_waitcnt lgkmcnt(6)\n"
    "ds_read_u16 v195, v195\n"
    "v_or_b32_e32 v187, v187, v203\n"
    "v_mfma_f32_16x16x32_bf16 v[140:143], v[44:47], v[196:199], v[140:143]\n"
    "v_lshlrev_b32_sdwa v203, 1, v175 dst_sel:DWORD dst_unused:UNUSED_PAD src0_sel:DWORD src1_sel:WORD_1\n"
    "v_cvt_pk_f16_f32 v196, v176, v177\n"
    "v_mfma_f32_16x16x32_bf16 v[168:171], a[96:99], v[144:147], 0\n"
    "ds_read_u16_d16_hi v203, v203\n"
    "v_lshlrev_b32_sdwa v197, 1, v178 dst_sel:DWORD dst_unused:UNUSED_PAD src0_sel:DWORD src1_sel:WORD_1\n"
    "v_mfma_f32_16x16x32_bf16 v[168:171], a[144:147], v[148:151], v[168:171]\n"
    "ds_read_u16 v197, v197\n"
    "v_lshlrev_b32_sdwa v198, 1, v180 dst_sel:DWORD dst_unused:UNUSED_PAD src0_sel:DWORD src1_sel:WORD_1\n"
    "v_mfma_f32_16x16x32_bf16 v[168:171], a[56:59], v[152:155], v[168:171]\n"
    "s_waitcnt lgkmcnt(7)\n"
    "v_lshlrev_b32_sdwa v199, 1, v182 dst_sel:DWORD dst_unused:UNUSED_PAD src0_sel:DWORD src1_sel:WORD_1\n"
    "v_or_b32_e32 v188, v188, v204\n"
    "v_mfma_f32_16x16x32_bf16 v[172:175], a[100:103], v[144:147], 0\n"
    "ds_read_u16 v198, v198\n"
    "v_or_b32_e32 v189, v189, v205\n"
    "v_mfma_f32_16x16x32_bf16 v[172:175], a[148:151], v[148:151], v[172:175]\n"
    "ds_read_u16 v199, v199\n"
    "v_lshlrev_b32_sdwa v205, 1, v179 dst_sel:DWORD dst_unused:UNUSED_PAD src0_sel:DWORD src1_sel:WORD_1\n"
    "v_mfma_f32_16x16x32_bf16 v[172:175], a[60:63], v[152:155], v[172:175]\n"
    "s_waitcnt lgkmcnt(7)\n"
    "ds_read_u16_d16_hi v205, v205\n"
    "v_cvt_pk_bf16_f32 v96, v96, v97\n"
    "v_mfma_f32_16x16x32_bf16 v[176:179], a[96:99], v[156:159], 0\n"
    "v_cvt_pk_bf16_f32 v97, v98, v99\n"
    "v_cvt_pk_bf16_f32 v98, v100, v101\n"
    "v_mfma_f32_16x16x32_bf16 v[176:179], a[144:147], v[160:163], v[176:179]\n"
    "v_cvt_pk_bf16_f32 v99, v102, v103\n"
    "v_or_b32_e32 v190, v190, v206\n"
    "v_mfma_f32_16x16x32_bf16 v[176:179], a[56:59], v[164:167], v[176:179]\n"
    "ds_write_b128 v243, v[96:99] offset:24640\n"
    "v_mfma_f32_16x16x32_bf16 v[96:99], a[0:3], v[184:187], 0\n"
    "s_waitcnt lgkmcnt(7)\n"
    "v_lshlrev_b32_sdwa v206, 1, v181 dst_sel:DWORD dst_unused:UNUSED_PAD src0_sel:DWORD src1_sel:WORD_1\n"
    "v_cvt_pk_bf16_f32 v112, v112, v113\n"
    "v_mfma_f32_16x16x32_bf16 v[100:103], a[4:7], v[184:187], 0\n"
    "v_cvt_pk_bf16_f32 v113, v114, v115\n"
    "v_cvt_pk_bf16_f32 v114, v116, v117\n"
    "v_cvt_pk_bf16_f32 v115, v118, v119\n"
    "ds_write_b128 v243, v[112:115] offset:26688\n"
    "v_mfma_f32_16x16x32_bf16 v[112:115], a[8:11], v[184:187], 0\n"
    "s_waitcnt lgkmcnt(5)\n"
    "v_cvt_pk_bf16_f32 v128, v128, v129\n"
    "v_cvt_pk_bf16_f32 v129, v130, v131\n"
    "v_or_b32_e32 v191, v191, v207\n"
    "v_mfma_f32_16x16x32_bf16 v[116:119], a[12:15], v[184:187], 0\n"
    "ds_read_b128 a[252:255], v249 offset:0\n"
    "ds_read_u16_d16_hi v206, v206\n"
    "v_lshlrev_b32_sdwa v207, 1, v183 dst_sel:DWORD dst_unused:UNUSED_PAD src0_sel:DWORD src1_sel:WORD_1\n"
    "v_mfma_f32_16x16x32_bf16 v[180:183], a[100:103], v[156:159], 0\n"
    "ds_read_u16_d16_hi v207, v207\n"
    "v_cvt_pk_bf16_f32 v130, v132, v133\n"
    "v_mfma_f32_16x16x32_bf16 v[180:183], a[148:151], v[160:163], v[180:183]\n"
    "v_or_b32_e64 v193, v193, v201\n"
    "v_cvt_pk_bf16_f32 v131, v134, v135\n"
    "v_mfma_f32_16x16x32_bf16 v[180:183], a[60:63], v[164:167], v[180:183]\n"
    "s_nop 0\n"
    "ds_write_b128 v243, v[128:131] offset:28736\n"
    "v_mfma_f32_16x16x32_bf16 v[128:131], v[0:3], v[184:187], 0\n"
    "s_waitcnt lgkmcnt(5)\n"
    "v_cvt_pk_bf16_f32 v104, v104, v105\n"
    "v_cvt_pk_bf16_f32 v105, v106, v107\n"
    "v_mfma_f32_16x16x32_bf16 v[132:135], v[4:7], v[184:187], 0\n"
    "v_cvt_pk_bf16_f32 v106, v108, v109\n"
    "v_cvt_pk_bf16_f32 v107, v110, v111\n"
    "v_mfma_f32_16x16x32_bf16 v[108:111], a[4:7], v[188:191], 0\n"
    "ds_write_b128 v243, v[104:107] offset:25664\n"
    "v_mfma_f32_16x16x32_bf16 v[104:107], a[0:3], v[188:191], 0\n"
    "s_waitcnt lgkmcnt(4)\n"
    "buffer_store_dwordx4 a[252:255], v255, s[44:47], s57 offen offset:0 sc1\n"
    "v_or_b32_e32 v194, v194, v202\n"
    "v_cvt_pk_bf16_f32 v120, v120, v121\n"
    "v_cvt_pk_bf16_f32 v121, v122, v123\n"
    "v_cvt_pk_bf16_f32 v122, v124, v125\n"
    "v_cvt_pk_bf16_f32 v123, v126, v127\n"
    "ds_write_b128 v243, v[120:123] offset:27712\n"
    "v_mfma_f32_16x16x32_bf16 v[120:123], a[8:11], v[188:191], 0\n"
    "s_waitcnt lgkmcnt(2)\n"
    "v_pk_fma_f16 v232, v192, v192, s48\n"
    "v_cvt_pk_bf16_f32 v136, v136, v137\n"
    "v_mfma_f32_16x16x32_bf16 v[124:127], a[12:15], v[188:191], 0\n"
    "s_nop 0\n"
    "ds_read_b128 a[252:255], v249 offset:2048\n"
    "v_cvt_pk_bf16_f32 v137, v138, v139\n"
    "v_cvt_pk_bf16_f32 v138, v140, v141\n"
    "v_or_b32_e64 v195, v195, v203\n"
    "v_cvt_pk_bf16_f32 v139, v142, v143\n"
    "ds_write_b128 v243, v[136:139] offset:29760\n"
    "v_pk_mul_f16 v232, v192, v232\n"
    "v_mfma_f32_16x16x32_bf16 v[136:139], v[0:3], v[188:191], 0\n"
    "v_exp_f16_e64 v234, v232\n"
    "v_mfma_f32_16x16x32_bf16 v[140:143], v[4:7], v[188:191], 0\n"
    "v_or_b32_e64 v197, v197, v205\n"
    "v_exp_f16_sdwa v234, v232 dst_sel:WORD_1 dst_unused:UNUSED_PRESERVE src0_sel:WORD_1\n"
    "v_cvt_pk_f16_f32 v184, v168, v169\n"
    "v_pk_fma_f16 v234, v234, s49, s49\n"
    "v_lshlrev_b32_sdwa v185, 1, v170 dst_sel:DWORD dst_unused:UNUSED_PAD src0_sel:DWORD src1_sel:WORD_1\n"
    "v_lshlrev_b32_sdwa v201, 1, v171 dst_sel:DWORD dst_unused:UNUSED_PAD src0_sel:DWORD src1_sel:WORD_1\n"
    "v_mfma_f32_16x16x32_bf16 v[168:171], a[104:107], v[144:147], 0\n"
    "s_nop 0\n"
    "ds_read_u16 v185, v185\n"
    "ds_read_u16_d16_hi v201, v201\n"
    "v_mfma_f32_16x16x32_bf16 v[168:171], a[152:155], v[148:151], v[168:171]\n"
    "s_waitcnt lgkmcnt(3)\n"
    "buffer_store_dwordx4 a[252:255], v255, s[44:47], s57 offen offset:64 sc1\n"
    "v_lshlrev_b32_sdwa v186, 1, v172 dst_sel:DWORD dst_unused:UNUSED_PAD src0_sel:DWORD src1_sel:WORD_1\n"
    "v_lshlrev_b32_sdwa v202, 1, v173 dst_sel:DWORD dst_unused:UNUSED_PAD src0_sel:DWORD src1_sel:WORD_1\n"
    "v_lshlrev_b32_sdwa v187, 1, v174 dst_sel:DWORD dst_unused:UNUSED_PAD src0_sel:DWORD src1_sel:WORD_1\n"
    "v_lshlrev_b32_sdwa v203, 1, v175 dst_sel:DWORD dst_unused:UNUSED_PAD src0_sel:DWORD src1_sel:WORD_1\n"
    "v_rcp_f16_e64 v232, v234\n"
    "v_mfma_f32_16x16x32_bf16 v[168:171], a[64:67], v[152:155], v[168:171]\n"
    "ds_read_u16 v186, v186\n"
    "ds_read_u16_d16_hi v202, v202\n"
    "v_or_b32_e64 v198, v198, v206\n"
    "v_rcp_f16_sdwa v232, v234 dst_sel:WORD_1 dst_unused:UNUSED_PRESERVE src0_sel:WORD_1\n"
    "v_mfma_f32_16x16x32_bf16 v[172:175], a[108:111], v[144:147], 0\n"
    "ds_read_u16 v187, v187\n"
    "ds_read_u16_d16_hi v203, v203\n"
    "v_cvt_pk_f16_f32 v188, v176, v177\n"
    "v_lshlrev_b32_sdwa v189, 1, v178 dst_sel:DWORD dst_unused:UNUSED_PAD src0_sel:DWORD src1_sel:WORD_1\n"
    "v_lshlrev_b32_sdwa v205, 1, v179 dst_sel:DWORD dst_unused:UNUSED_PAD src0_sel:DWORD src1_sel:WORD_1\n"
    "v_lshlrev_b32_sdwa v190, 1, v180 dst_sel:DWORD dst_unused:UNUSED_PAD src0_sel:DWORD src1_sel:WORD_1\n"
    "v_lshlrev_b32_sdwa v206, 1, v181 dst_sel:DWORD dst_unused:UNUSED_PAD src0_sel:DWORD src1_sel:WORD_1\n"
    "v_lshlrev_b32_sdwa v191, 1, v182 dst_sel:DWORD dst_unused:UNUSED_PAD src0_sel:DWORD src1_sel:WORD_1\n"
    "v_fma_mix_f32 v234, v192, v232, 0 op_sel_hi:[1,1,0]\n"
    "v_mfma_f32_16x16x32_bf16 v[172:175], a[156:159], v[148:151], v[172:175]\n"
    "ds_read_b128 a[252:255], v249 offset:4096\n"
    "ds_read_u16 v189, v189\n"
    "v_mfma_f32_16x16x32_bf16 v[172:175], a[68:71], v[152:155], v[172:175]\n"
    "v_or_b32_e64 v199, v199, v207\n"
    "v_lshlrev_b32_sdwa v207, 1, v183 dst_sel:DWORD dst_unused:UNUSED_PAD src0_sel:DWORD src1_sel:WORD_1\n"
    "v_fma_mix_f32 v232, v192, v232, 0 op_sel:[1,1,0] op_sel_hi:[1,1,0]\n"
    "v_mfma_f32_16x16x32_bf16 v[176:179], a[104:107], v[156:159], 0\n"
    "s_nop 0\n"
    "ds_read_u16_d16_hi v205, v205\n"
    "v_cvt_pk_bf16_f32 v192, v234, v232\n"
    "v_mfma_f32_16x16x32_bf16 v[176:179], a[152:155], v[160:163], v[176:179]\n"
    "s_waitcnt lgkmcnt(7)\n"
    "ds_read_u16 v190, v190\n"
    "v_pk_fma_f16 v232, v196, v196, s48\n"
    "v_mfma_f32_16x16x32_bf16 v[176:179], a[64:67], v[164:167], v[176:179]\n"
    "v_pk_mul_f16 v232, v196, v232\n"
    "v_or_b32_e64 v185, v185, v201\n"
    "v_mfma_f32_16x16x32_bf16 v[180:183], a[108:111], v[156:159], 0\n"
    "ds_read_u16_d16_hi v206, v206\n"
    "v_exp_f16_e64 v234, v232\n"
    "v_mfma_f32_16x16x32_bf16 v[180:183], a[156:159], v[160:163], v[180:183]\n"
    "ds_read_u16 v191, v191\n"
    "v_exp_f16_sdwa v234, v232 dst_sel:WORD_1 dst_unused:UNUSED_PRESERVE src0_sel:WORD_1\n"
    "v_mfma_f32_16x16x32_bf16 v[180:183], a[68:71], v[164:167], v[180:183]\n"
    "s_waitcnt lgkmcnt(8)\n"
    "v_pk_fma_f16 v234, v234, s49, s49\n"
    "v_or_b32_e32 v186, v186, v202\n"
    "v_mfma_f32_16x16x32_bf16 v[96:99], a[16:19], v[192:195], v[96:99]\n"
    "ds_read_u16_d16_hi v207, v207\n"
    "v_rcp_f16_e32 v232, v234\n"
    "v_mfma_f32_16x16x32_bf16 v[100:103], a[20:23], v[192:195], v[100:103]\n"
    "v_rcp_f16_sdwa v232, v234 dst_sel:WORD_1 dst_unused:UNUSED_PRESERVE src0_sel:WORD_1\n"
    "v_mfma_f32_16x16x32_bf16 v[112:115], a[24:27], v[192:195], v[112:115]\n"
    "s_waitcnt lgkmcnt(6)\n"
    "buffer_store_dwordx4 a[252:255], v255, s[44:47], s57 offen offset:128 sc1\n"
    "v_mfma_f32_16x16x32_bf16 v[116:119], a[28:31], v[192:195], v[116:119]\n"
    "v_fma_mix_f32 v234, v196, v232, 0 op_sel_hi:[1,1,0]\n"
    "v_fma_mix_f32 v232, v196, v232, 0 op_sel:[1,1,0] op_sel_hi:[1,1,0]\n"
    "v_mfma_f32_16x16x32_bf16 v[128:131], v[8:11], v[192:195], v[128:131]\n"
    "ds_read_b128 a[252:255], v249 offset:1024\n"
    "v_cvt_pk_bf16_f32 v196, v234, v232\n"
    "v_mfma_f32_16x16x32_bf16 v[132:135], v[12:15], v[192:195], v[132:135]\n"
    "v_pk_fma_f16 v232, v184, v184, s48\n"
    "v_or_b32_e32 v187, v187, v203\n"
    "v_mfma_f32_16x16x32_bf16 v[104:107], a[16:19], v[196:199], v[104:107]\n"
    "s_waitcnt lgkmcnt(5)\n"
    "v_pk_mul_f16 v232, v184, v232\n"
    "v_or_b32_e64 v189, v189, v205\n"
    "v_mfma_f32_16x16x32_bf16 v[108:111], a[20:23], v[196:199], v[108:111]\n"
    "v_exp_f16_e32 v234, v232\n"
    "v_mfma_f32_16x16x32_bf16 v[120:123], a[24:27], v[196:199], v[120:123]\n"
    "v_exp_f16_sdwa v234, v232 dst_sel:WORD_1 dst_unused:UNUSED_PRESERVE src0_sel:WORD_1\n"
    "v_mfma_f32_16x16x32_bf16 v[124:127], a[28:31], v[196:199], v[124:127]\n"
    "s_waitcnt lgkmcnt(3)\n"
    "v_pk_fma_f16 v234, v234, s49, s49\n"
    "v_lshlrev_b32_sdwa v192, 1, v168 dst_sel:DWORD dst_unused:UNUSED_PAD src0_sel:DWORD src1_sel:WORD_1\n"
    "v_mfma_f32_16x16x32_bf16 v[136:139], v[8:11], v[196:199], v[136:139]\n"
    "ds_read_u16 v192, v192\n"
    "v_lshlrev_b32_sdwa v200, 1, v169 dst_sel:DWORD dst_unused:UNUSED_PAD src0_sel:DWORD src1_sel:WORD_1\n"
    "v_mfma_f32_16x16x32_bf16 v[140:143], v[12:15], v[196:199], v[140:143]\n"
    "ds_read_u16_d16_hi v200, v200\n"
    "v_lshlrev_b32_sdwa v193, 1, v170 dst_sel:DWORD dst_unused:UNUSED_PAD src0_sel:DWORD src1_sel:WORD_1\n"
    "v_lshlrev_b32_sdwa v201, 1, v171 dst_sel:DWORD dst_unused:UNUSED_PAD src0_sel:DWORD src1_sel:WORD_1\n"
    "v_mfma_f32_16x16x32_bf16 v[168:171], a[112:115], v[144:147], 0\n"
    "s_waitcnt lgkmcnt(2)\n"
    "ds_read_u16 v193, v193\n"
    "ds_read_u16_d16_hi v201, v201\n"
    "v_mfma_f32_16x16x32_bf16 v[168:171], a[160:163], v[148:151], v[168:171]\n"
    "s_nop 0\n"
    "buffer_store_dwordx4 a[252:255], v255, s[44:47], s88 offen offset:0 sc1\n"
    "v_lshlrev_b32_sdwa v194, 1, v172 dst_sel:DWORD dst_unused:UNUSED_PAD src0_sel:DWORD src1_sel:WORD_1\n"
    "v_lshlrev_b32_sdwa v202, 1, v173 dst_sel:DWORD dst_unused:UNUSED_PAD src0_sel:DWORD src1_sel:WORD_1\n"
    "v_lshlrev_b32_sdwa v195, 1, v174 dst_sel:DWORD dst_unused:UNUSED_PAD src0_sel:DWORD src1_sel:WORD_1\n"
    "v_lshlrev_b32_sdwa v203, 1, v175 dst_sel:DWORD dst_unused:UNUSED_PAD src0_sel:DWORD src1_sel:WORD_1\n"
    "v_rcp_f16_e64 v232, v234\n"
    "v_mfma_f32_16x16x32_bf16 v[168:171], a[72:75], v[152:155], v[168:171]\n"
    "ds_read_u16 v194, v194\n"
    "ds_read_u16_d16_hi v202, v202\n"
    "v_or_b32_e64 v190, v190, v206\n"
    "v_rcp_f16_sdwa v232, v234 dst_sel:WORD_1 dst_unused:UNUSED_PRESERVE src0_sel:WORD_1\n"
    "v_mfma_f32_16x16x32_bf16 v[172:175], a[116:119], v[144:147], 0\n"
    "ds_read_u16 v195, v195\n"
    "ds_read_u16_d16_hi v203, v203\n"
    "v_lshlrev_b32_sdwa v196, 1, v176 dst_sel:DWORD dst_unused:UNUSED_PAD src0_sel:DWORD src1_sel:WORD_1\n"
    "v_lshlrev_b32_sdwa v204, 1, v177 dst_sel:DWORD dst_unused:UNUSED_PAD src0_sel:DWORD src1_sel:WORD_1\n"
    "v_lshlrev_b32_sdwa v197, 1, v178 dst_sel:DWORD dst_unused:UNUSED_PAD src0_sel:DWORD src1_sel:WORD_1\n"
    "v_lshlrev_b32_sdwa v205, 1, v179 dst_sel:DWORD dst_unused:UNUSED_PAD src0_sel:DWORD src1_sel:WORD_1\n"
    "v_lshlrev_b32_sdwa v198, 1, v180 dst_sel:DWORD dst_unused:UNUSED_PAD src0_sel:DWORD src1_sel:WORD_1\n"
    "v_lshlrev_b32_sdwa v206, 1, v181 dst_sel:DWORD dst_unused:UNUSED_PAD src0_sel:DWORD src1_sel:WORD_1\n"
    "v_lshlrev_b32_sdwa v199, 1, v182 dst_sel:DWORD dst_unused:UNUSED_PAD src0_sel:DWORD src1_sel:WORD_1\n"
    "v_fma_mix_f32 v234, v184, v232, 0 op_sel_hi:[1,1,0]\n"
    "v_mfma_f32_16x16x32_bf16 v[172:175], a[164:167], v[148:151], v[172:175]\n"
    "ds_read_u16 v196, v196\n"
    "v_or_b32_e32 v191, v191, v207\n"
    "v_mfma_f32_16x16x32_bf16 v[172:175], v[52:55], v[152:155], v[172:175]\n"
    "s_waitcnt lgkmcnt(7)\n"
    "ds_read_u16_d16_hi v204, v204\n"
    "v_lshlrev_b32_sdwa v207, 1, v183 dst_sel:DWORD dst_unused:UNUSED_PAD src0_sel:DWORD src1_sel:WORD_1\n"
    "v_fma_mix_f32 v232, v184, v232, 0 op_sel:[1,1,0] op_sel_hi:[1,1,0]\n"
    "v_mfma_f32_16x16x32_bf16 v[176:179], a[112:115], v[156:159], 0\n"
    "ds_read_u16 v197, v197\n"
    "v_cvt_pk_bf16_f32 v184, v234, v232\n"
    "v_mfma_f32_16x16x32_bf16 v[176:179], a[160:163], v[160:163], v[176:179]\n"
    "v_pk_fma_f16 v232, v188, v188, s48\n"
    "v_or_b32_e32 v192, v192, v200\n"
    "v_mfma_f32_16x16x32_bf16 v[176:179], a[72:75], v[164:167], v[176:179]\n"
    "s_waitcnt lgkmcnt(7)\n"
    "ds_read_b128 a[252:255], v249 offset:3072\n"
    "v_pk_mul_f16 v232, v188, v232\n"
    "v_mfma_f32_16x16x32_bf16 v[180:183], a[116:119], v[156:159], 0\n"
    "ds_read_u16_d16_hi v205, v205\n"
    "v_exp_f16_e32 v234, v232\n"
    "v_mfma_f32_16x16x32_bf16 v[180:183], a[164:167], v[160:163], v[180:183]\n"
    "v_exp_f16_sdwa v234, v232 dst_sel:WORD_1 dst_unused:UNUSED_PRESERVE src0_sel:WORD_1\n"
    "v_mfma_f32_16x16x32_bf16 v[180:183], v[52:55], v[164:167], v[180:183]\n"
    "s_waitcnt lgkmcnt(7)\n"
    "ds_read_u16 v198, v198\n"
    "v_pk_fma_f16 v234, v234, s49, s49\n"
    "v_mfma_f32_16x16x32_bf16 v[96:99], a[32:35], v[184:187], v[96:99]\n"
    "ds_read_u16_d16_hi v206, v206\n"
    "v_rcp_f16_e64 v232, v234\n"
    "v_mfma_f32_16x16x32_bf16 v[100:103], a[36:39], v[184:187], v[100:103]\n"
    "v_rcp_f16_sdwa v232, v234 dst_sel:WORD_1 dst_unused:UNUSED_PRESERVE src0_sel:WORD_1\n"
    "v_mfma_f32_16x16x32_bf16 v[112:115], a[40:43], v[184:187], v[112:115]\n"
    "ds_read_u16 v199, v199\n"
    "v_fma_mix_f32 v234, v188, v232, 0 op_sel_hi:[1,1,0]\n"
    "v_mfma_f32_16x16x32_bf16 v[116:119], a[44:47], v[184:187], v[116:119]\n"
    "s_waitcnt lgkmcnt(8)\n"
    "v_fma_mix_f32 v232, v188, v232, 0 op_sel:[1,1,0] op_sel_hi:[1,1,0]\n"
    "v_or_b32_e32 v193, v193, v201\n"
    "v_mfma_f32_16x16x32_bf16 v[128:131], v[16:19], v[184:187], v[128:131]\n"
    "ds_read_u16_d16_hi v207, v207\n"
    "v_cvt_pk_bf16_f32 v188, v234, v232\n"
    "v_mfma_f32_16x16x32_bf16 v[132:135], v[20:23], v[184:187], v[132:135]\n"
    "v_cvt_pk_f16_f32 v184, v168, v169\n"
    "v_lshlrev_b32_sdwa v185, 1, v170 dst_sel:DWORD dst_unused:UNUSED_PAD src0_sel:DWORD src1_sel:WORD_1\n"
    "v_mfma_f32_16x16x32_bf16 v[104:107], a[32:35], v[188:191], v[104:107]\n"
    "s_waitcnt lgkmcnt(7)\n"
    "ds_read_u16 v185, v185\n"
    "v_lshlrev_b32_sdwa v201, 1, v171 dst_sel:DWORD dst_unused:UNUSED_PAD src0_sel:DWORD src1_sel:WORD_1\n"
    "v_mfma_f32_16x16x32_bf16 v[108:111], a[36:39], v[188:191], v[108:111]\n"
    "ds_read_u16_d16_hi v201, v201\n"
    "v_lshlrev_b32_sdwa v186, 1, v172 dst_sel:DWORD dst_unused:UNUSED_PAD src0_sel:DWORD src1_sel:WORD_1\n"
    "v_mfma_f32_16x16x32_bf16 v[120:123], a[40:43], v[188:191], v[120:123]\n"
    "s_waitcnt lgkmcnt(7)\n"
    "buffer_store_dwordx4 a[252:255], v255, s[44:47], s88 offen offset:64 sc1\n"
    "ds_read_u16 v186, v186\n"
    "v_mfma_f32_16x16x32_bf16 v[124:127], a[44:47], v[188:191], v[124:127]\n"
    "v_or_b32_e32 v194, v194, v202\n"
    "v_lshlrev_b32_sdwa v202, 1, v173 dst_sel:DWORD dst_unused:UNUSED_PAD src0_sel:DWORD src1_sel:WORD_1\n"
    "v_mfma_f32_16x16x32_bf16 v[136:139], v[16:19], v[188:191], v[136:139]\n"
    "s_waitcnt lgkmcnt(7)\n"
    "ds_read_b128 a[252:255], v249 offset:5120\n"
    "ds_read_u16_d16_hi v202, v202\n"
    "v_mfma_f32_16x16x32_bf16 v[140:143], v[20:23], v[188:191], v[140:143]\n"
    "v_lshlrev_b32_sdwa v187, 1, v174 dst_sel:DWORD dst_unused:UNUSED_PAD src0_sel:DWORD src1_sel:WORD_1\n"
    "v_cvt_pk_f16_f32 v188, v176, v177\n"
    "v_mfma_f32_16x16x32_bf16 v[168:171], a[120:123], v[144:147], 0\n"
    "ds_read_u16 v187, v187\n"
    "v_lshlrev_b32_sdwa v189, 1, v178 dst_sel:DWORD dst_unused:UNUSED_PAD src0_sel:DWORD src1_sel:WORD_1\n"
    "v_mfma_f32_16x16x32_bf16 v[168:171], a[168:171], v[148:151], v[168:171]\n"
    "s_waitcnt lgkmcnt(8)\n"
    "v_lshlrev_b32_sdwa v190, 1, v180 dst_sel:DWORD dst_unused:UNUSED_PAD src0_sel:DWORD src1_sel:WORD_1\n"
    "v_lshlrev_b32_sdwa v191, 1, v182 dst_sel:DWORD dst_unused:UNUSED_PAD src0_sel:DWORD src1_sel:WORD_1\n"
    "v_mfma_f32_16x16x32_bf16 v[168:171], a[76:79], v[152:155], v[168:171]\n"
    "v_or_b32_e32 v195, v195, v203\n"
    "v_lshlrev_b32_sdwa v203, 1, v175 dst_sel:DWORD dst_unused:UNUSED_PAD src0_sel:DWORD src1_sel:WORD_1\n"
    "v_mfma_f32_16x16x32_bf16 v[172:175], a[124:127], v[144:147], 0\n"
    "ds_read_u16_d16_hi v203, v203\n"
    "v_or_b32_e64 v196, v196, v204\n"
    "v_mfma_f32_16x16x32_bf16 v[172:175], a[172:175], v[148:151], v[172:175]\n"
    "ds_read_u16 v189, v189\n"
    "v_or_b32_e32 v197, v197, v205\n"
    "v_mfma_f32_16x16x32_bf16 v[172:175], v[60:63], v[152:155], v[172:175]\n"
    "s_waitcnt lgkmcnt(8)\n"
    "v_lshlrev_b32_sdwa v205, 1, v179 dst_sel:DWORD dst_unused:UNUSED_PAD src0_sel:DWORD src1_sel:WORD_1\n"
    "v_or_b32_e64 v198, v198, v206\n"
    "v_mfma_f32_16x16x32_bf16 v[176:179], a[120:123], v[156:159], 0\n"
    "ds_read_u16_d16_hi v205, v205\n"
    "v_lshlrev_b32_sdwa v206, 1, v181 dst_sel:DWORD dst_unused:UNUSED_PAD src0_sel:DWORD src1_sel:WORD_1\n"
    "v_mfma_f32_16x16x32_bf16 v[176:179], a[168:171], v[160:163], v[176:179]\n"
    "ds_read_u16 v190, v190\n"
    "v_or_b32_e32 v199, v199, v207\n"
    "v_mfma_f32_16x16x32_bf16 v[176:179], a[76:79], v[164:167], v[176:179]\n"
    "s_waitcnt lgkmcnt(8)\n"
    "v_lshlrev_b32_sdwa v207, 1, v183 dst_sel:DWORD dst_unused:UNUSED_PAD src0_sel:DWORD src1_sel:WORD_1\n"
    "v_or_b32_e64 v185, v185, v201\n"
    "v_mfma_f32_16x16x32_bf16 v[180:183], a[124:127], v[156:159], 0\n"
    "ds_read_b128 v[144:147], v243 offset:0\n"
    "v_pk_fma_f16 v232, v184, v184, s48\n"
    "v_mfma_f32_16x16x32_bf16 v[180:183], a[172:175], v[160:163], v[180:183]\n"
    "s_waitcnt lgkmcnt(7)\n"
    "ds_read_u16_d16_hi v206, v206\n"
    "buffer_store_dwordx4 a[252:255], v255, s[44:47], s88 offen offset:128 sc1\n"
    "v_mfma_f32_16x16x32_bf16 v[180:183], v[60:63], v[164:167], v[180:183]\n"
    "s_nop 0\n"
    "ds_read_b128 v[148:151], v243 offset:1024\n"
    "v_pk_mul_f16 v232, v184, v232\n"
    "v_mfma_f32_16x16x32_bf16 v[96:99], a[176:179], v[192:195], v[96:99]\n"
    "s_waitcnt lgkmcnt(8)\n"
    "ds_read_u16 v191, v191\n"
    "v_or_b32_e32 v186, v186, v202\n"
    "v_mfma_f32_16x16x32_bf16 v[100:103], a[180:183], v[192:195], v[100:103]\n"
    "v_exp_f16_e64 v234, v232\n"
    "v_mfma_f32_16x16x32_bf16 v[112:115], a[184:187], v[192:195], v[112:115]\n"
    "ds_read_b128 v[152:155], v243 offset:2048\n"
    "ds_read_u16_d16_hi v207, v207\n"
    "v_mfma_f32_16x16x32_bf16 v[116:119], a[188:191], v[192:195], v[116:119]\n"
    "s_waitcnt lgkmcnt(9)\n"
    "v_or_b32_e32 v187, v187, v203\n"
    "v_exp_f16_sdwa v234, v232 dst_sel:WORD_1 dst_unused:UNUSED_PRESERVE src0_sel:WORD_1\n"
    "v_mfma_f32_16x16x32_bf16 v[128:131], v[24:27], v[192:195], v[128:131]\n"
    "ds_read_b128 v[156:159], v243 offset:3072\n"
    "v_pk_fma_f16 v234, v234, s49, s49\n"
    "v_mfma_f32_16x16x32_bf16 v[132:135], v[28:31], v[192:195], v[132:135]\n"
    "ds_read_b128 v[160:163], v243 offset:4096\n"
    "v_cvt_pk_f16_f32 v192, v168, v169\n"
    "v_mfma_f32_16x16x32_bf16 v[104:107], a[176:179], v[196:199], v[104:107]\n"
    "s_waitcnt lgkmcnt(9)\n"
    "ds_read_b128 v[164:167], v243 offset:5120\n"
    "v_or_b32_e32 v189, v189, v205\n"
    "v_mfma_f32_16x16x32_bf16 v[108:111], a[180:183], v[196:199], v[108:111]\n"
    "s_nop 0\n"
    "v_lshlrev_b32_sdwa v193, 1, v170 dst_sel:DWORD dst_unused:UNUSED_PAD src0_sel:DWORD src1_sel:WORD_1\n"
    "v_lshlrev_b32_sdwa v201, 1, v171 dst_sel:DWORD dst_unused:UNUSED_PAD src0_sel:DWORD src1_sel:WORD_1\n"
    "v_mfma_f32_16x16x32_bf16 v[120:123], a[184:187], v[196:199], v[120:123]\n"
    "s_waitcnt lgkmcnt(8)\n"
    "ds_read_u16 v193, v193\n"
    "v_lshlrev_b32_sdwa v194, 1, v172 dst_sel:DWORD dst_unused:UNUSED_PAD src0_sel:DWORD src1_sel:WORD_1\n"
    "v_mfma_f32_16x16x32_bf16 v[124:127], a[188:191], v[196:199], v[124:127]\n"
    "ds_read_u16_d16_hi v201, v201\n"
    "v_lshlrev_b32_sdwa v202, 1, v173 dst_sel:DWORD dst_unused:UNUSED_PAD src0_sel:DWORD src1_sel:WORD_1\n"
    "v_mfma_f32_16x16x32_bf16 v[136:139], v[24:27], v[196:199], v[136:139]\n"
    "v_lshlrev_b32_sdwa v195, 1, v174 dst_sel:DWORD dst_unused:UNUSED_PAD src0_sel:DWORD src1_sel:WORD_1\n"
    "v_lshlrev_b32_sdwa v203, 1, v175 dst_sel:DWORD dst_unused:UNUSED_PAD src0_sel:DWORD src1_sel:WORD_1\n"
    "v_mfma_f32_16x16x32_bf16 v[140:143], v[28:31], v[196:199], v[140:143]\n"
    "s_waitcnt lgkmcnt(8)\n"
    "ds_read_u16 v194, v194\n"
    "v_rcp_f16_e32 v232, v234\n"
    "v_mfma_f32_16x16x32_bf16 v[168:171], a[80:83], v[144:147], 0\n"
    "ds_read_u16_d16_hi v202, v202\n"
    "v_or_b32_e32 v190, v190, v206\n"
    "v_mfma_f32_16x16x32_bf16 v[168:171], a[128:131], v[148:151], v[168:171]\n"
    "s_waitcnt lgkmcnt(8)\n"
    "v_rcp_f16_sdwa v232, v234 dst_sel:WORD_1 dst_unused:UNUSED_PRESERVE src0_sel:WORD_1\n"
    "v_mfma_f32_16x16x32_bf16 v[168:171], a[48:51], v[152:155], v[168:171]\n"
    "ds_read_u16 v195, v195\n"
    "v_cvt_pk_f16_f32 v196, v176, v177\n"
    "v_mfma_f32_16x16x32_bf16 v[172:175], a[84:87], v[144:147], 0\n"
    "s_nop 0\n"
    "v_lshlrev_b32_sdwa v197, 1, v178 dst_sel:DWORD dst_unused:UNUSED_PAD src0_sel:DWORD src1_sel:WORD_1\n"
    "v_lshlrev_b32_sdwa v205, 1, v179 dst_sel:DWORD dst_unused:UNUSED_PAD src0_sel:DWORD src1_sel:WORD_1\n"
    "v_mfma_f32_16x16x32_bf16 v[172:175], a[132:135], v[148:151], v[172:175]\n"
    "s_waitcnt lgkmcnt(7)\n"
    "ds_read_u16_d16_hi v203, v203\n"
    "v_lshlrev_b32_sdwa v198, 1, v180 dst_sel:DWORD dst_unused:UNUSED_PAD src0_sel:DWORD src1_sel:WORD_1\n"
    "v_lshlrev_b32_sdwa v206, 1, v181 dst_sel:DWORD dst_unused:UNUSED_PAD src0_sel:DWORD src1_sel:WORD_1\n"
    "v_lshlrev_b32_sdwa v199, 1, v182 dst_sel:DWORD dst_unused:UNUSED_PAD src0_sel:DWORD src1_sel:WORD_1\n"
    "v_fma_mix_f32 v234, v184, v232, 0 op_sel_hi:[1,1,0]\n"
    "v_mfma_f32_16x16x32_bf16 v[172:175], v[48:51], v[152:155], v[172:175]\n"
    "s_waitcnt lgkmcnt(6)\n"
    "ds_read_u16 v197, v197\n"
    "v_or_b32_e32 v191, v191, v207\n"
    "v_lshlrev_b32_sdwa v207, 1, v183 dst_sel:DWORD dst_unused:UNUSED_PAD src0_sel:DWORD src1_sel:WORD_1\n"
    "v_fma_mix_f32 v232, v184, v232, 0 op_sel:[1,1,0] op_sel_hi:[1,1,0]\n"
    "v_mfma_f32_16x16x32_bf16 v[176:179], a[80:83], v[156:159], 0\n"
    "s_barrier\n"
    "v_cvt_pk_bf16_f32 v184, v234, v232\n"
    "v_mfma_f32_16x16x32_bf16 v[176:179], a[128:131], v[160:163], v[176:179]\n"
    "s_waitcnt lgkmcnt(5)\n"
    "ds_read_u16_d16_hi v205, v205\n"
    "ds_read_u16 v198, v198\n"
    "v_mfma_f32_16x16x32_bf16 v[176:179], a[48:51], v[164:167], v[176:179]\n"
    "v_pk_fma_f16 v232, v188, v188, s48\n"
    "v_or_b32_e64 v193, v193, v201\n"
    "v_mfma_f32_16x16x32_bf16 v[180:183], a[84:87], v[156:159], 0\n"
    "ds_read_u16_d16_hi v206, v206\n"
    "v_pk_mul_f16 v232, v188, v232\n"
    "v_mfma_f32_16x16x32_bf16 v[180:183], a[132:135], v[160:163], v[180:183]\n"
    "s_waitcnt lgkmcnt(6)\n"
    "ds_read_u16 v199, v199\n"
    "v_exp_f16_e32 v234, v232\n"
    "v_mfma_f32_16x16x32_bf16 v[180:183], v[48:51], v[164:167], v[180:183]\n"
    "ds_read_u16_d16_hi v207, v207\n"
    "v_exp_f16_sdwa v234, v232 dst_sel:WORD_1 dst_unused:UNUSED_PRESERVE src0_sel:WORD_1\n"
    "v_mfma_f32_16x16x32_bf16 v[96:99], a[192:195], v[184:187], v[96:99]\n"
    "v_pk_fma_f16 v234, v234, s49, s49\n"
    "v_or_b32_e64 v194, v194, v202\n"
    "v_mfma_f32_16x16x32_bf16 v[100:103], a[196:199], v[184:187], v[100:103]\n"
    "s_waitcnt lgkmcnt(6)\n"
    "v_rcp_f16_e32 v232, v234\n"
    "v_mfma_f32_16x16x32_bf16 v[112:115], a[200:203], v[184:187], v[112:115]\n"
    "v_rcp_f16_sdwa v232, v234 dst_sel:WORD_1 dst_unused:UNUSED_PRESERVE src0_sel:WORD_1\n"
    "v_mfma_f32_16x16x32_bf16 v[116:119], a[204:207], v[184:187], v[116:119]\n"
    "v_fma_mix_f32 v234, v188, v232, 0 op_sel_hi:[1,1,0]\n"
    "v_fma_mix_f32 v232, v188, v232, 0 op_sel:[1,1,0] op_sel_hi:[1,1,0]\n"
    "v_mfma_f32_16x16x32_bf16 v[128:131], v[32:35], v[184:187], v[128:131]\n"
    "v_cvt_pk_bf16_f32 v188, v234, v232\n"
    "v_pk_fma_f16 v232, v192, v192, s48\n"
    "v_mfma_f32_16x16x32_bf16 v[132:135], v[36:39], v[184:187], v[132:135]\n"
    "s_waitcnt lgkmcnt(4)\n"
    "v_or_b32_e32 v195, v195, v203\n"
    "v_pk_mul_f16 v232, v192, v232\n"
    "v_mfma_f32_16x16x32_bf16 v[104:107], a[192:195], v[188:191], v[104:107]\n"
    "v_exp_f16_e64 v234, v232\n"
    "v_mfma_f32_16x16x32_bf16 v[108:111], a[196:199], v[188:191], v[108:111]\n"
    "v_exp_f16_sdwa v234, v232 dst_sel:WORD_1 dst_unused:UNUSED_PRESERVE src0_sel:WORD_1\n"
    "v_mfma_f32_16x16x32_bf16 v[120:123], a[200:203], v[188:191], v[120:123]\n"
    "s_waitcnt lgkmcnt(2)\n"
    "v_or_b32_e32 v197, v197, v205\n"
    "v_pk_fma_f16 v234, v234, s49, s49\n"
    "v_mfma_f32_16x16x32_bf16 v[124:127], a[204:207], v[188:191], v[124:127]\n"
    "v_lshlrev_b32_sdwa v184, 1, v168 dst_sel:DWORD dst_unused:UNUSED_PAD src0_sel:DWORD src1_sel:WORD_1\n"
    "v_lshlrev_b32_sdwa v200, 1, v169 dst_sel:DWORD dst_unused:UNUSED_PAD src0_sel:DWORD src1_sel:WORD_1\n"
    "v_mfma_f32_16x16x32_bf16 v[136:139], v[32:35], v[188:191], v[136:139]\n"
    "ds_read_u16 v184, v184\n"
    "ds_read_u16_d16_hi v200, v200\n"
    "v_mfma_f32_16x16x32_bf16 v[140:143], v[36:39], v[188:191], v[140:143]\n"
    "s_nop 0\n"
    "v_lshlrev_b32_sdwa v185, 1, v170 dst_sel:DWORD dst_unused:UNUSED_PAD src0_sel:DWORD src1_sel:WORD_1\n"
    "v_lshlrev_b32_sdwa v201, 1, v171 dst_sel:DWORD dst_unused:UNUSED_PAD src0_sel:DWORD src1_sel:WORD_1\n"
    "v_mfma_f32_16x16x32_bf16 v[168:171], a[88:91], v[144:147], 0\n"
    "s_waitcnt lgkmcnt(2)\n"
    "ds_read_u16 v185, v185\n"
    "ds_read_u16_d16_hi v201, v201\n"
    "v_mfma_f32_16x16x32_bf16 v[168:171], a[136:139], v[148:151], v[168:171]\n"
    "v_lshlrev_b32_sdwa v186, 1, v172 dst_sel:DWORD dst_unused:UNUSED_PAD src0_sel:DWORD src1_sel:WORD_1\n"
    "v_lshlrev_b32_sdwa v202, 1, v173 dst_sel:DWORD dst_unused:UNUSED_PAD src0_sel:DWORD src1_sel:WORD_1\n"
    "v_lshlrev_b32_sdwa v187, 1, v174 dst_sel:DWORD dst_unused:UNUSED_PAD src0_sel:DWORD src1_sel:WORD_1\n"
    "v_lshlrev_b32_sdwa v203, 1, v175 dst_sel:DWORD dst_unused:UNUSED_PAD src0_sel:DWORD src1_sel:WORD_1\n"
    "v_rcp_f16_e64 v232, v234\n"
    "v_mfma_f32_16x16x32_bf16 v[168:171], a[52:55], v[152:155], v[168:171]\n"
    "ds_read_u16 v186, v186\n"
    "ds_read_u16_d16_hi v202, v202\n"
    "v_or_b32_e64 v198, v198, v206\n"
    "v_rcp_f16_sdwa v232, v234 dst_sel:WORD_1 dst_unused:UNUSED_PRESERVE src0_sel:WORD_1\n"
    "v_mfma_f32_16x16x32_bf16 v[172:175], a[92:95], v[144:147], 0\n"
    "ds_read_u16 v187, v187\n"
    "v_lshlrev_b32_sdwa v188, 1, v176 dst_sel:DWORD dst_unused:UNUSED_PAD src0_sel:DWORD src1_sel:WORD_1\n"
    "v_mfma_f32_16x16x32_bf16 v[172:175], a[140:143], v[148:151], v[172:175]\n"
    "v_lshlrev_b32_sdwa v204, 1, v177 dst_sel:DWORD dst_unused:UNUSED_PAD src0_sel:DWORD src1_sel:WORD_1\n"
    "v_lshlrev_b32_sdwa v189, 1, v178 dst_sel:DWORD dst_unused:UNUSED_PAD src0_sel:DWORD src1_sel:WORD_1\n"
    "v_lshlrev_b32_sdwa v205, 1, v179 dst_sel:DWORD dst_unused:UNUSED_PAD src0_sel:DWORD src1_sel:WORD_1\n"
    "v_lshlrev_b32_sdwa v190, 1, v180 dst_sel:DWORD dst_unused:UNUSED_PAD src0_sel:DWORD src1_sel:WORD_1\n"
    "v_lshlrev_b32_sdwa v206, 1, v181 dst_sel:DWORD dst_unused:UNUSED_PAD src0_sel:DWORD src1_sel:WORD_1\n"
    "v_lshlrev_b32_sdwa v191, 1, v182 dst_sel:DWORD dst_unused:UNUSED_PAD src0_sel:DWORD src1_sel:WORD_1\n"
    "v_fma_mix_f32 v234, v192, v232, 0 op_sel_hi:[1,1,0]\n"
    "v_mfma_f32_16x16x32_bf16 v[172:175], v[56:59], v[152:155], v[172:175]\n"
    "ds_read_u16_d16_hi v203, v203\n"
    "ds_read_u16 v188, v188\n"
    "v_or_b32_e32 v199, v199, v207\n"
    "v_lshlrev_b32_sdwa v207, 1, v183 dst_sel:DWORD dst_unused:UNUSED_PAD src0_sel:DWORD src1_sel:WORD_1\n"
    "v_fma_mix_f32 v232, v192, v232, 0 op_sel:[1,1,0] op_sel_hi:[1,1,0]\n"
    "v_mfma_f32_16x16x32_bf16 v[176:179], a[88:91], v[156:159], 0\n"
    "s_waitcnt lgkmcnt(7)\n"
    "ds_read_u16_d16_hi v204, v204\n"
    "v_cvt_pk_bf16_f32 v192, v234, v232\n"
    "v_mfma_f32_16x16x32_bf16 v[176:179], a[136:139], v[160:163], v[176:179]\n"
    "v_pk_fma_f16 v232, v196, v196, s48\n"
    "v_or_b32_e64 v184, v184, v200\n"
    "v_mfma_f32_16x16x32_bf16 v[176:179], a[52:55], v[164:167], v[176:179]\n"
    "ds_read_u16 v189, v189\n"
    "v_pk_mul_f16 v232, v196, v232\n"
    "v_mfma_f32_16x16x32_bf16 v[180:183], a[92:95], v[156:159], 0\n"
    "v_exp_f16_e32 v234, v232\n"
    "v_mfma_f32_16x16x32_bf16 v[180:183], a[140:143], v[160:163], v[180:183]\n"
    "s_waitcnt lgkmcnt(7)\n"
    "ds_read_u16_d16_hi v205, v205\n"
    "v_exp_f16_sdwa v234, v232 dst_sel:WORD_1 dst_unused:UNUSED_PRESERVE src0_sel:WORD_1\n"
    "v_mfma_f32_16x16x32_bf16 v[180:183], v[56:59], v[164:167], v[180:183]\n"
    "ds_read_u16 v190, v190\n"
    "v_pk_fma_f16 v234, v234, s49, s49\n"
    "v_mfma_f32_16x16x32_bf16 v[96:99], a[208:211], v[192:195], v[96:99]\n"
    "v_rcp_f16_e32 v232, v234\n"
    "v_mfma_f32_16x16x32_bf16 v[100:103], a[212:215], v[192:195], v[100:103]\n"
    "s_waitcnt lgkmcnt(7)\n"
    "ds_read_u16_d16_hi v206, v206\n"
    "v_rcp_f16_sdwa v232, v234 dst_sel:WORD_1 dst_unused:UNUSED_PRESERVE src0_sel:WORD_1\n"
    "v_mfma_f32_16x16x32_bf16 v[112:115], a[216:219], v[192:195], v[112:115]\n"
    "v_fma_mix_f32 v234, v196, v232, 0 op_sel_hi:[1,1,0]\n"
    "v_fma_mix_f32 v232, v196, v232, 0 op_sel:[1,1,0] op_sel_hi:[1,1,0]\n"
    "v_mfma_f32_16x16x32_bf16 v[116:119], a[220:223], v[192:195], v[116:119]\n"
    "ds_read_u16 v191, v191\n"
    "v_cvt_pk_bf16_f32 v196, v234, v232\n"
    "v_mfma_f32_16x16x32_bf16 v[128:131], v[40:43], v[192:195], v[128:131]\n"
    "s_waitcnt lgkmcnt(7)\n"
    "ds_read_u16_d16_hi v207, v207\n"
    "v_or_b32_e32 v185, v185, v201\n"
    "v_mfma_f32_16x16x32_bf16 v[132:135], v[44:47], v[192:195], v[132:135]\n"
    "v_cvt_pk_f16_f32 v192, v168, v169\n"
    "v_lshlrev_b32_sdwa v193, 1, v170 dst_sel:DWORD dst_unused:UNUSED_PAD src0_sel:DWORD src1_sel:WORD_1\n"
    "v_mfma_f32_16x16x32_bf16 v[104:107], a[208:211], v[196:199], v[104:107]\n"
    "ds_read_u16 v193, v193\n"
    "v_lshlrev_b32_sdwa v201, 1, v171 dst_sel:DWORD dst_unused:UNUSED_PAD src0_sel:DWORD src1_sel:WORD_1\n"
    "v_mfma_f32_16x16x32_bf16 v[108:111], a[212:215], v[196:199], v[108:111]\n"
    "v_lshlrev_b32_sdwa v194, 1, v172 dst_sel:DWORD dst_unused:UNUSED_PAD src0_sel:DWORD src1_sel:WORD_1\n"
    "v_or_b32_e32 v186, v186, v202\n"
    "v_mfma_f32_16x16x32_bf16 v[120:123], a[216:219], v[196:199], v[120:123]\n"
    "s_waitcnt lgkmcnt(7)\n"
    "ds_read_u16_d16_hi v201, v201\n"
    "v_lshlrev_b32_sdwa v202, 1, v173 dst_sel:DWORD dst_unused:UNUSED_PAD src0_sel:DWORD src1_sel:WORD_1\n"
    "v_mfma_f32_16x16x32_bf16 v[124:127], a[220:223], v[196:199], v[124:127]\n"
    "v_lshlrev_b32_sdwa v195, 1, v174 dst_sel:DWORD dst_unused:UNUSED_PAD src0_sel:DWORD src1_sel:WORD_1\n"
    "v_or_b32_e32 v187, v187, v203\n"
    "v_mfma_f32_16x16x32_bf16 v[136:139], v[40:43], v[196:199], v[136:139]\n"
    "ds_read_u16 v194, v194\n"
    "v_lshlrev_b32_sdwa v203, 1, v175 dst_sel:DWORD dst_unused:UNUSED_PAD src0_sel:DWORD src1_sel:WORD_1\n"
    "v_mfma_f32_16x16x32_bf16 v[140:143], v[44:47], v[196:199], v[140:143]\n"
    "s_waitcnt lgkmcnt(7)\n"
    "ds_read_u16_d16_hi v202, v202\n"
    "v_cvt_pk_f16_f32 v196, v176, v177\n"
    "v_mfma_f32_16x16x32_bf16 v[168:171], a[96:99], v[144:147], 0\n"
    "v_lshlrev_b32_sdwa v197, 1, v178 dst_sel:DWORD dst_unused:UNUSED_PAD src0_sel:DWORD src1_sel:WORD_1\n"
    "v_lshlrev_b32_sdwa v198, 1, v180 dst_sel:DWORD dst_unused:UNUSED_PAD src0_sel:DWORD src1_sel:WORD_1\n"
    "v_mfma_f32_16x16x32_bf16 v[168:171], a[144:147], v[148:151], v[168:171]\n"
    "ds_read_u16 v195, v195\n"
    "v_lshlrev_b32_sdwa v199, 1, v182 dst_sel:DWORD dst_unused:UNUSED_PAD src0_sel:DWORD src1_sel:WORD_1\n"
    "v_mfma_f32_16x16x32_bf16 v[168:171], a[56:59], v[152:155], v[168:171]\n"
    "v_or_b32_e64 v188, v188, v204\n"
    "v_or_b32_e32 v189, v189, v205\n"
    "v_mfma_f32_16x16x32_bf16 v[172:175], a[100:103], v[144:147], 0\n"
    "s_waitcnt lgkmcnt(7)\n"
    "ds_read_u16_d16_hi v203, v203\n"
    "v_lshlrev_b32_sdwa v205, 1, v179 dst_sel:DWORD dst_unused:UNUSED_PAD src0_sel:DWORD src1_sel:WORD_1\n"
    "v_mfma_f32_16x16x32_bf16 v[172:175], a[148:151], v[148:151], v[172:175]\n"
    "v_cvt_pk_bf16_f32 v96, v96, v97\n"
    "v_cvt_pk_bf16_f32 v97, v98, v99\n"
    "v_mfma_f32_16x16x32_bf16 v[172:175], a[60:63], v[152:155], v[172:175]\n"
    "ds_read_u16 v197, v197\n"
    "v_cvt_pk_bf16_f32 v98, v100, v101\n"
    "v_mfma_f32_16x16x32_bf16 v[176:179], a[96:99], v[156:159], 0\n"
    "s_waitcnt lgkmcnt(7)\n"
    "ds_read_u16_d16_hi v205, v205\n"
    "v_cvt_pk_bf16_f32 v99, v102, v103\n"
    "v_mfma_f32_16x16x32_bf16 v[176:179], a[144:147], v[160:163], v[176:179]\n"
    "v_or_b32_e32 v190, v190, v206\n"
    "v_lshlrev_b32_sdwa v206, 1, v181 dst_sel:DWORD dst_unused:UNUSED_PAD src0_sel:DWORD src1_sel:WORD_1\n"
    "v_mfma_f32_16x16x32_bf16 v[176:179], a[56:59], v[164:167], v[176:179]\n"
    "v_cvt_pk_bf16_f32 v112, v112, v113\n"
    "v_cvt_pk_bf16_f32 v113, v114, v115\n"
    "v_mfma_f32_16x16x32_bf16 v[100:103], a[4:7], v[184:187], 0\n"
    "s_nop 0\n"
    "v_cvt_pk_bf16_f32 v114, v116, v117\n"
    "ds_write_b128 v242, v[96:99] offset:24640\n"
    "v_mfma_f32_16x16x32_bf16 v[96:99], a[0:3], v[184:187], 0\n"
    "s_waitcnt lgkmcnt(6)\n"
    "v_cvt_pk_bf16_f32 v115, v118, v119\n"
    "v_cvt_pk_bf16_f32 v128, v128, v129\n"
    "v_cvt_pk_bf16_f32 v129, v130, v131\n"
    "v_or_b32_e32 v191, v191, v207\n"
    "v_mfma_f32_16x16x32_bf16 v[116:119], a[12:15], v[184:187], 0\n"
    "s_waitcnt lgkmcnt(5)\n"
    "ds_read_u16 v198, v198\n"
    "ds_write_b128 v242, v[112:115] offset:26688\n"
    "v_mfma_f32_16x16x32_bf16 v[112:115], a[8:11], v[184:187], 0\n"
    "v_lshlrev_b32_sdwa v207, 1, v183 dst_sel:DWORD dst_unused:UNUSED_PAD src0_sel:DWORD src1_sel:WORD_1\n"
    "v_cvt_pk_bf16_f32 v130, v132, v133\n"
    "v_mfma_f32_16x16x32_bf16 v[180:183], a[100:103], v[156:159], 0\n"
    "v_or_b32_e64 v193, v193, v201\n"
    "v_cvt_pk_bf16_f32 v131, v134, v135\n"
    "v_mfma_f32_16x16x32_bf16 v[180:183], a[148:151], v[160:163], v[180:183]\n"
    "s_waitcnt lgkmcnt(5)\n"
    "ds_read_u16_d16_hi v206, v206\n"
    "ds_write_b128 v242, v[128:131] offset:28736\n"
    "v_mfma_f32_16x16x32_bf16 v[180:183], a[60:63], v[164:167], v[180:183]\n"
    "v_cvt_pk_bf16_f32 v104, v104, v105\n"
    "v_cvt_pk_bf16_f32 v105, v106, v107\n"
    "v_mfma_f32_16x16x32_bf16 v[128:131], v[0:3], v[184:187], 0\n"
    "s_waitcnt lgkmcnt(4)\n"
    "v_cvt_pk_bf16_f32 v106, v108, v109\n"
    "v_cvt_pk_bf16_f32 v107, v110, v111\n"
    "v_mfma_f32_16x16x32_bf16 v[132:135], v[4:7], v[184:187], 0\n"
    "ds_read_u16 v199, v199\n"
    "ds_write_b128 v242, v[104:107] offset:25664\n"
    "v_mfma_f32_16x16x32_bf16 v[104:107], a[0:3], v[188:191], 0\n"
    "v_or_b32_e64 v194, v194, v202\n"
    "v_cvt_pk_bf16_f32 v120, v120, v121\n"
    "v_mfma_f32_16x16x32_bf16 v[108:111], a[4:7], v[188:191], 0\n"
    "v_cvt_pk_bf16_f32 v121, v122, v123\n"
    "v_cvt_pk_bf16_f32 v122, v124, v125\n"
    "v_cvt_pk_bf16_f32 v123, v126, v127\n"
    "ds_write_b128 v242, v[120:123] offset:27712\n"
    "v_mfma_f32_16x16x32_bf16 v[120:123], a[8:11], v[188:191], 0\n"
    "s_nop 0\n"
    "v_cvt_pk_bf16_f32 v136, v136, v137\n"
    "v_pk_fma_f16 v232, v192, v192, s48\n"
    "v_mfma_f32_16x16x32_bf16 v[124:127], a[12:15], v[188:191], 0\n"
    "s_waitcnt lgkmcnt(3)\n"
    "ds_read_u16_d16_hi v207, v207\n"
    "v_cvt_pk_bf16_f32 v137, v138, v139\n"
    "v_cvt_pk_bf16_f32 v138, v140, v141\n"
    "v_or_b32_e64 v195, v195, v203\n"
    "v_cvt_pk_bf16_f32 v139, v142, v143\n"
    "ds_write_b128 v242, v[136:139] offset:29760\n"
    "v_pk_mul_f16 v232, v192, v232\n"
    "v_mfma_f32_16x16x32_bf16 v[136:139], v[0:3], v[188:191], 0\n"
    "v_exp_f16_e64 v234, v232\n"
    "v_mfma_f32_16x16x32_bf16 v[140:143], v[4:7], v[188:191], 0\n"
    "v_exp_f16_sdwa v234, v232 dst_sel:WORD_1 dst_unused:UNUSED_PRESERVE src0_sel:WORD_1\n"
    "v_or_b32_e64 v197, v197, v205\n"
    "v_pk_fma_f16 v234, v234, s49, s49\n"
    "v_cvt_pk_f16_f32 v184, v168, v169\n"
    "v_lshlrev_b32_sdwa v185, 1, v170 dst_sel:DWORD dst_unused:UNUSED_PAD src0_sel:DWORD src1_sel:WORD_1\n"
    "v_lshlrev_b32_sdwa v201, 1, v171 dst_sel:DWORD dst_unused:UNUSED_PAD src0_sel:DWORD src1_sel:WORD_1\n"
    "v_mfma_f32_16x16x32_bf16 v[168:171], a[104:107], v[144:147], 0\n"
    "ds_read_u16 v185, v185\n"
    "ds_read_u16_d16_hi v201, v201\n"
    "v_mfma_f32_16x16x32_bf16 v[168:171], a[152:155], v[148:151], v[168:171]\n"
    "v_lshlrev_b32_sdwa v186, 1, v172 dst_sel:DWORD dst_unused:UNUSED_PAD src0_sel:DWORD src1_sel:WORD_1\n"
    "v_lshlrev_b32_sdwa v202, 1, v173 dst_sel:DWORD dst_unused:UNUSED_PAD src0_sel:DWORD src1_sel:WORD_1\n"
    "v_lshlrev_b32_sdwa v187, 1, v174 dst_sel:DWORD dst_unused:UNUSED_PAD src0_sel:DWORD src1_sel:WORD_1\n"
    "v_lshlrev_b32_sdwa v203, 1, v175 dst_sel:DWORD dst_unused:UNUSED_PAD src0_sel:DWORD src1_sel:WORD_1\n"
    "v_rcp_f16_e32 v232, v234\n"
    "v_mfma_f32_16x16x32_bf16 v[168:171], a[64:67], v[152:155], v[168:171]\n"
    "s_waitcnt lgkmcnt(2)\n"
    "ds_read_u16 v186, v186\n"
    "ds_read_u16_d16_hi v202, v202\n"
    "v_or_b32_e32 v198, v198, v206\n"
    "v_rcp_f16_sdwa v232, v234 dst_sel:WORD_1 dst_unused:UNUSED_PRESERVE src0_sel:WORD_1\n"
    "v_mfma_f32_16x16x32_bf16 v[172:175], a[108:111], v[144:147], 0\n"
    "s_barrier\n"
    "v_mfma_f32_16x16x32_bf16 v[172:175], a[156:159], v[148:151], v[172:175]\n"
    "ds_read_u16 v187, v187\n"
    "ds_read_u16_d16_hi v203, v203\n"
    "v_cvt_pk_f16_f32 v188, v176, v177\n"
    "v_lshlrev_b32_sdwa v189, 1, v178 dst_sel:DWORD dst_unused:UNUSED_PAD src0_sel:DWORD src1_sel:WORD_1\n"
    "v_lshlrev_b32_sdwa v205, 1, v179 dst_sel:DWORD dst_unused:UNUSED_PAD src0_sel:DWORD src1_sel:WORD_1\n"
    "v_lshlrev_b32_sdwa v190, 1, v180 dst_sel:DWORD dst_unused:UNUSED_PAD src0_sel:DWORD src1_sel:WORD_1\n"
    "v_lshlrev_b32_sdwa v206, 1, v181 dst_sel:DWORD dst_unused:UNUSED_PAD src0_sel:DWORD src1_sel:WORD_1\n"
    "v_lshlrev_b32_sdwa v191, 1, v182 dst_sel:DWORD dst_unused:UNUSED_PAD src0_sel:DWORD src1_sel:WORD_1\n"
    "v_fma_mix_f32 v234, v192, v232, 0 op_sel_hi:[1,1,0]\n"
    "v_mfma_f32_16x16x32_bf16 v[172:175], a[68:71], v[152:155], v[172:175]\n"
    "ds_read_u16 v189, v189\n"
    "v_or_b32_e32 v199, v199, v207\n"
    "v_lshlrev_b32_sdwa v207, 1, v183 dst_sel:DWORD dst_unused:UNUSED_PAD src0_sel:DWORD src1_sel:WORD_1\n"
    "v_fma_mix_f32 v232, v192, v232, 0 op_sel:[1,1,0] op_sel_hi:[1,1,0]\n"
    "v_mfma_f32_16x16x32_bf16 v[176:179], a[104:107], v[156:159], 0\n"
    "s_waitcnt lgkmcnt(5)\n"
    "ds_read_u16_d16_hi v205, v205\n"
    "v_cvt_pk_bf16_f32 v192, v234, v232\n"
    "v_mfma_f32_16x16x32_bf16 v[176:179], a[152:155], v[160:163], v[176:179]\n"
    "ds_read_u16 v190, v190\n"
    "v_pk_fma_f16 v232, v196, v196, s48\n"
    "v_mfma_f32_16x16x32_bf16 v[176:179], a[64:67], v[164:167], v[176:179]\n"
    "ds_read_u16_d16_hi v206, v206\n"
    "v_pk_mul_f16 v232, v196, v232\n"
    "v_mfma_f32_16x16x32_bf16 v[180:183], a[108:111], v[156:159], 0\n"
    "s_waitcnt lgkmcnt(6)\n"
    "ds_read_u16 v191, v191\n"
    "v_exp_f16_e32 v234, v232\n"
    "v_mfma_f32_16x16x32_bf16 v[180:183], a[156:159], v[160:163], v[180:183]\n"
    "ds_read_u16_d16_hi v207, v207\n"
    "v_exp_f16_sdwa v234, v232 dst_sel:WORD_1 dst_unused:UNUSED_PRESERVE src0_sel:WORD_1\n"
    "v_mfma_f32_16x16x32_bf16 v[180:183], a[68:71], v[164:167], v[180:183]\n"
    "v_pk_fma_f16 v234, v234, s49, s49\n"
    "v_or_b32_e64 v185, v185, v201\n"
    "v_mfma_f32_16x16x32_bf16 v[96:99], a[16:19], v[192:195], v[96:99]\n"
    "v_rcp_f16_e32 v232, v234\n"
    "v_mfma_f32_16x16x32_bf16 v[100:103], a[20:23], v[192:195], v[100:103]\n"
    "s_waitcnt lgkmcnt(6)\n"
    "v_rcp_f16_sdwa v232, v234 dst_sel:WORD_1 dst_unused:UNUSED_PRESERVE src0_sel:WORD_1\n"
    "v_mfma_f32_16x16x32_bf16 v[112:115], a[24:27], v[192:195], v[112:115]\n"
    "v_fma_mix_f32 v234, v196, v232, 0 op_sel_hi:[1,1,0]\n"
    "v_fma_mix_f32 v232, v196, v232, 0 op_sel:[1,1,0] op_sel_hi:[1,1,0]\n"
    "v_mfma_f32_16x16x32_bf16 v[116:119], a[28:31], v[192:195], v[116:119]\n"
    "v_or_b32_e64 v186, v186, v202\n"
    "v_cvt_pk_bf16_f32 v196, v234, v232\n"
    "v_mfma_f32_16x16x32_bf16 v[128:131], v[8:11], v[192:195], v[128:131]\n"
    "v_pk_fma_f16 v232, v184, v184, s48\n"
    "v_or_b32_e32 v187, v187, v203\n"
    "v_mfma_f32_16x16x32_bf16 v[132:135], v[12:15], v[192:195], v[132:135]\n"
    "s_waitcnt lgkmcnt(4)\n"
    "v_pk_mul_f16 v232, v184, v232\n"
    "v_or_b32_e64 v189, v189, v205\n"
    "v_mfma_f32_16x16x32_bf16 v[104:107], a[16:19], v[196:199], v[104:107]\n"
    "v_exp_f16_e32 v234, v232\n"
    "v_mfma_f32_16x16x32_bf16 v[108:111], a[20:23], v[196:199], v[108:111]\n"
    "v_exp_f16_sdwa v234, v232 dst_sel:WORD_1 dst_unused:UNUSED_PRESERVE src0_sel:WORD_1\n"
    "v_mfma_f32_16x16x32_bf16 v[120:123], a[24:27], v[196:199], v[120:123]\n"
    "s_waitcnt vmcnt(7) lgkmcnt(2)\n"
    "v_pk_fma_f16 v234, v234, s49, s49\n"
    "v_lshlrev_b32_sdwa v192, 1, v168 dst_sel:DWORD dst_unused:UNUSED_PAD src0_sel:DWORD src1_sel:WORD_1\n"
    "v_mfma_f32_16x16x32_bf16 v[124:127], a[28:31], v[196:199], v[124:127]\n"
    "ds_read_u16 v192, v192\n"
    "v_lshlrev_b32_sdwa v200, 1, v169 dst_sel:DWORD dst_unused:UNUSED_PAD src0_sel:DWORD src1_sel:WORD_1\n"
    "v_mfma_f32_16x16x32_bf16 v[136:139], v[8:11], v[196:199], v[136:139]\n"
    "ds_read_u16_d16_hi v200, v200\n"
    "v_lshlrev_b32_sdwa v193, 1, v170 dst_sel:DWORD dst_unused:UNUSED_PAD src0_sel:DWORD src1_sel:WORD_1\n"
    "v_mfma_f32_16x16x32_bf16 v[140:143], v[12:15], v[196:199], v[140:143]\n"
    "s_waitcnt vmcnt(6)\n"
    "ds_read_u16 v193, v193\n"
    "v_lshlrev_b32_sdwa v201, 1, v171 dst_sel:DWORD dst_unused:UNUSED_PAD src0_sel:DWORD src1_sel:WORD_1\n"
    "v_smfmac_f32_32x32x32_bf16 v[112:127], a[248:251], a[232:239], v241\n"
    "s_waitcnt lgkmcnt(3)\n"
    "ds_read_u16_d16_hi v201, v201\n"
    "ds_read_b128 a[232:235], v243 offset:24640\n"
    "v_lshlrev_b32_sdwa v194, 1, v172 dst_sel:DWORD dst_unused:UNUSED_PAD src0_sel:DWORD src1_sel:WORD_1\n"
    "v_lshlrev_b32_sdwa v202, 1, v173 dst_sel:DWORD dst_unused:UNUSED_PAD src0_sel:DWORD src1_sel:WORD_1\n"
    "v_lshlrev_b32_sdwa v195, 1, v174 dst_sel:DWORD dst_unused:UNUSED_PAD src0_sel:DWORD src1_sel:WORD_1\n"
    "v_smfmac_f32_32x32x32_bf16 v[96:111], a[248:251], a[224:231], v241\n"
    "ds_read_u16 v194, v194\n"
    "v_lshlrev_b32_sdwa v203, 1, v175 dst_sel:DWORD dst_unused:UNUSED_PAD src0_sel:DWORD src1_sel:WORD_1\n"
    "v_rcp_f16_e64 v232, v234\n"
    "v_or_b32_e64 v190, v190, v206\n"
    "v_smfmac_f32_32x32x32_bf16 v[128:143], a[248:251], v[208:215], v241\n"
    "ds_read_u16_d16_hi v202, v202\n"
    "ds_read_b128 a[236:239], v243 offset:25664\n"
    "v_rcp_f16_sdwa v232, v234 dst_sel:WORD_1 dst_unused:UNUSED_PRESERVE src0_sel:WORD_1\n"
    "v_lshlrev_b32_sdwa v196, 1, v176 dst_sel:DWORD dst_unused:UNUSED_PAD src0_sel:DWORD src1_sel:WORD_1\n"
    "v_mfma_f32_16x16x32_bf16 v[168:171], a[112:115], v[144:147], 0\n"
    "ds_read_u16 v195, v195\n"
    "v_lshlrev_b32_sdwa v204, 1, v177 dst_sel:DWORD dst_unused:UNUSED_PAD src0_sel:DWORD src1_sel:WORD_1\n"
    "v_mfma_f32_16x16x32_bf16 v[168:171], a[160:163], v[148:151], v[168:171]\n"
    "v_lshlrev_b32_sdwa v197, 1, v178 dst_sel:DWORD dst_unused:UNUSED_PAD src0_sel:DWORD src1_sel:WORD_1\n"
    "v_lshlrev_b32_sdwa v205, 1, v179 dst_sel:DWORD dst_unused:UNUSED_PAD src0_sel:DWORD src1_sel:WORD_1\n"
    "v_mfma_f32_16x16x32_bf16 v[168:171], a[72:75], v[152:155], v[168:171]\n"
    "ds_read_u16_d16_hi v203, v203\n"
    "v_lshlrev_b32_sdwa v198, 1, v180 dst_sel:DWORD dst_unused:UNUSED_PAD src0_sel:DWORD src1_sel:WORD_1\n"
    "v_mfma_f32_16x16x32_bf16 v[172:175], a[116:119], v[144:147], 0\n"
    "ds_read_u16 v196, v196\n"
    "v_lshlrev_b32_sdwa v206, 1, v181 dst_sel:DWORD dst_unused:UNUSED_PAD src0_sel:DWORD src1_sel:WORD_1\n"
    "v_mfma_f32_16x16x32_bf16 v[172:175], a[164:167], v[148:151], v[172:175]\n"
    "v_lshlrev_b32_sdwa v199, 1, v182 dst_sel:DWORD dst_unused:UNUSED_PAD src0_sel:DWORD src1_sel:WORD_1\n"
    "v_fma_mix_f32 v234, v184, v232, 0 op_sel_hi:[1,1,0]\n"
    "v_mfma_f32_16x16x32_bf16 v[172:175], v[52:55], v[152:155], v[172:175]\n"
    "s_waitcnt lgkmcnt(8)\n"
    "ds_read_u16_d16_hi v204, v204\n"
    "v_or_b32_e32 v191, v191, v207\n"
    "v_lshlrev_b32_sdwa v207, 1, v183 dst_sel:DWORD dst_unused:UNUSED_PAD src0_sel:DWORD src1_sel:WORD_1\n"
    "v_fma_mix_f32 v232, v184, v232, 0 op_sel:[1,1,0] op_sel_hi:[1,1,0]\n"
    "v_mfma_f32_16x16x32_bf16 v[176:179], a[112:115], v[156:159], 0\n"
    "ds_read_u16 v197, v197\n"
    "v_cvt_pk_bf16_f32 v184, v234, v232\n"
    "v_mfma_f32_16x16x32_bf16 v[176:179], a[160:163], v[160:163], v[176:179]\n"
    "v_pk_fma_f16 v232, v188, v188, s48\n"
    "v_or_b32_e32 v192, v192, v200\n"
    "v_mfma_f32_16x16x32_bf16 v[176:179], a[72:75], v[164:167], v[176:179]\n"
    "s_waitcnt lgkmcnt(8)\n"
    "ds_read_u16_d16_hi v205, v205\n"
    "v_pk_mul_f16 v232, v188, v232\n"
    "v_mfma_f32_16x16x32_bf16 v[180:183], a[116:119], v[156:159], 0\n"
    "v_exp_f16_e32 v234, v232\n"
    "v_mfma_f32_16x16x32_bf16 v[180:183], a[164:167], v[160:163], v[180:183]\n"
    "s_waitcnt lgkmcnt(6)\n"
    "ds_read_u16 v198, v198\n"
    "v_exp_f16_sdwa v234, v232 dst_sel:WORD_1 dst_unused:UNUSED_PRESERVE src0_sel:WORD_1\n"
    "v_smfmac_f32_32x32x32_bf16 v[96:111], a[248:251], a[232:239], v241\n"
    "ds_read_u16_d16_hi v206, v206\n"
    "v_pk_fma_f16 v234, v234, s49, s49\n"
    "v_or_b32_e64 v193, v193, v201\n"
    "v_rcp_f16_e64 v232, v234\n"
    "v_mfma_f32_16x16x32_bf16 v[180:183], v[52:55], v[164:167], v[180:183]\n"
    "ds_read_u16 v199, v199\n"
    "v_rcp_f16_sdwa v232, v234 dst_sel:WORD_1 dst_unused:UNUSED_PRESERVE src0_sel:WORD_1\n"
    "v_mfma_f32_16x16x32_bf16 v[112:115], a[40:43], v[184:187], v[112:115]\n"
    "s_nop 0\n"
    "v_fma_mix_f32 v234, v188, v232, 0 op_sel_hi:[1,1,0]\n"
    "v_fma_mix_f32 v232, v188, v232, 0 op_sel:[1,1,0] op_sel_hi:[1,1,0]\n"
    "v_mfma_f32_16x16x32_bf16 v[96:99], a[32:35], v[184:187], v[96:99]\n"
    "s_waitcnt lgkmcnt(7)\n"
    "ds_read_u16_d16_hi v207, v207\n"
    "v_cvt_pk_bf16_f32 v188, v234, v232\n"
    "v_mfma_f32_16x16x32_bf16 v[100:103], a[36:39], v[184:187], v[100:103]\n"
    "ds_read_b128 a[232:235], v243 offset:26688\n"
    "v_lshlrev_b32_sdwa v201, 1, v171 dst_sel:DWORD dst_unused:UNUSED_PAD src0_sel:DWORD src1_sel:WORD_1\n"
    "v_mfma_f32_16x16x32_bf16 v[116:119], a[44:47], v[184:187], v[116:119]\n"
    "ds_read_b128 a[236:239], v243 offset:27712\n"
    "v_or_b32_e32 v194, v194, v202\n"
    "v_mfma_f32_16x16x32_bf16 v[128:131], v[16:19], v[184:187], v[128:131]\n"
    "s_waitcnt lgkmcnt(8)\n"
    "ds_read_u16_d16_hi v201, v201\n"
    "v_lshlrev_b32_sdwa v202, 1, v173 dst_sel:DWORD dst_unused:UNUSED_PAD src0_sel:DWORD src1_sel:WORD_1\n"
    "v_mfma_f32_16x16x32_bf16 v[132:135], v[20:23], v[184:187], v[132:135]\n"
    "v_cvt_pk_f16_f32 v184, v168, v169\n"
    "v_lshlrev_b32_sdwa v185, 1, v170 dst_sel:DWORD dst_unused:UNUSED_PAD src0_sel:DWORD src1_sel:WORD_1\n"
    "v_mfma_f32_16x16x32_bf16 v[104:107], a[32:35], v[188:191], v[104:107]\n"
    "ds_read_u16 v185, v185\n"
    "v_lshlrev_b32_sdwa v186, 1, v172 dst_sel:DWORD dst_unused:UNUSED_PAD src0_sel:DWORD src1_sel:WORD_1\n"
    "v_mfma_f32_16x16x32_bf16 v[108:111], a[36:39], v[188:191], v[108:111]\n"
    "v_lshlrev_b32_sdwa v187, 1, v174 dst_sel:DWORD dst_unused:UNUSED_PAD src0_sel:DWORD src1_sel:WORD_1\n"
    "v_or_b32_e32 v195, v195, v203\n"
    "v_mfma_f32_16x16x32_bf16 v[120:123], a[40:43], v[188:191], v[120:123]\n"
    "s_waitcnt lgkmcnt(8)\n"
    "ds_read_u16 v186, v186\n"
    "v_lshlrev_b32_sdwa v203, 1, v175 dst_sel:DWORD dst_unused:UNUSED_PAD src0_sel:DWORD src1_sel:WORD_1\n"
    "v_mfma_f32_16x16x32_bf16 v[124:127], a[44:47], v[188:191], v[124:127]\n"
    "v_or_b32_e64 v196, v196, v204\n"
    "v_or_b32_e32 v197, v197, v205\n"
    "v_mfma_f32_16x16x32_bf16 v[136:139], v[16:19], v[188:191], v[136:139]\n"
    "ds_read_u16_d16_hi v202, v202\n"
    "v_lshlrev_b32_sdwa v205, 1, v179 dst_sel:DWORD dst_unused:UNUSED_PAD src0_sel:DWORD src1_sel:WORD_1\n"
    "v_mfma_f32_16x16x32_bf16 v[140:143], v[20:23], v[188:191], v[140:143]\n"
    "s_waitcnt lgkmcnt(8)\n"
    "ds_read_u16 v187, v187\n"
    "v_cvt_pk_f16_f32 v188, v176, v177\n"
    "v_mfma_f32_16x16x32_bf16 v[168:171], a[120:123], v[144:147], 0\n"
    "v_lshlrev_b32_sdwa v189, 1, v178 dst_sel:DWORD dst_unused:UNUSED_PAD src0_sel:DWORD src1_sel:WORD_1\n"
    "v_lshlrev_b32_sdwa v190, 1, v180 dst_sel:DWORD dst_unused:UNUSED_PAD src0_sel:DWORD src1_sel:WORD_1\n"
    "v_mfma_f32_16x16x32_bf16 v[168:171], a[168:171], v[148:151], v[168:171]\n"
    "ds_read_u16_d16_hi v203, v203\n"
    "v_lshlrev_b32_sdwa v191, 1, v182 dst_sel:DWORD dst_unused:UNUSED_PAD src0_sel:DWORD src1_sel:WORD_1\n"
    "v_mfma_f32_16x16x32_bf16 v[168:171], a[76:79], v[152:155], v[168:171]\n"
    "v_or_b32_e64 v198, v198, v206\n"
    "v_lshlrev_b32_sdwa v206, 1, v181 dst_sel:DWORD dst_unused:UNUSED_PAD src0_sel:DWORD src1_sel:WORD_1\n"
    "v_mfma_f32_16x16x32_bf16 v[172:175], a[124:127], v[144:147], 0\n"
    "s_waitcnt lgkmcnt(6)\n"
    "ds_read_u16 v189, v189\n"
    "v_or_b32_e32 v199, v199, v207\n"
    "v_smfmac_f32_32x32x32_bf16 v[112:127], a[248:251], a[232:239], v241\n"
    "ds_read_b128 a[232:235], v243 offset:28736\n"
    "v_lshlrev_b32_sdwa v207, 1, v183 dst_sel:DWORD dst_unused:UNUSED_PAD src0_sel:DWORD src1_sel:WORD_1\n"
    "v_pk_fma_f16 v232, v184, v184, s48\n"
    "v_pk_mul_f16 v232, v184, v232\n"
    "v_exp_f16_e64 v234, v232\n"
    "v_mfma_f32_16x16x32_bf16 v[172:175], a[172:175], v[148:151], v[172:175]\n"
    "ds_read_u16_d16_hi v205, v205\n"
    "v_exp_f16_sdwa v234, v232 dst_sel:WORD_1 dst_unused:UNUSED_PRESERVE src0_sel:WORD_1\n"
    "v_mfma_f32_16x16x32_bf16 v[172:175], v[60:63], v[152:155], v[172:175]\n"
    "ds_read_b128 a[236:239], v243 offset:29760\n"
    "v_pk_fma_f16 v234, v234, s49, s49\n"
    "v_mfma_f32_16x16x32_bf16 v[176:179], a[120:123], v[156:159], 0\n"
    "s_waitcnt lgkmcnt(8)\n"
    "ds_read_u16 v190, v190\n"
    "v_or_b32_e32 v185, v185, v201\n"
    "v_mfma_f32_16x16x32_bf16 v[176:179], a[168:171], v[160:163], v[176:179]\n"
    "v_lshlrev_b32_sdwa v201, 1, v171 dst_sel:DWORD dst_unused:UNUSED_PAD src0_sel:DWORD src1_sel:WORD_1\n"
    "v_rcp_f16_e64 v232, v234\n"
    "v_mfma_f32_16x16x32_bf16 v[176:179], a[76:79], v[164:167], v[176:179]\n"
    "ds_read_u16_d16_hi v206, v206\n"
    "v_rcp_f16_sdwa v232, v234 dst_sel:WORD_1 dst_unused:UNUSED_PRESERVE src0_sel:WORD_1\n"
    "v_mfma_f32_16x16x32_bf16 v[180:183], a[124:127], v[156:159], 0\n"
    "s_waitcnt lgkmcnt(8)\n"
    "ds_read_u16 v191, v191\n"
    "v_or_b32_e32 v186, v186, v202\n"
    "v_mfma_f32_16x16x32_bf16 v[180:183], a[172:175], v[160:163], v[180:183]\n"
    "v_lshlrev_b32_sdwa v202, 1, v173 dst_sel:DWORD dst_unused:UNUSED_PAD src0_sel:DWORD src1_sel:WORD_1\n"
    "v_fma_mix_f32 v234, v184, v232, 0 op_sel_hi:[1,1,0]\n"
    "v_mfma_f32_16x16x32_bf16 v[180:183], v[60:63], v[164:167], v[180:183]\n"
    "ds_read_u16_d16_hi v207, v207\n"
    "v_fma_mix_f32 v232, v184, v232, 0 op_sel:[1,1,0] op_sel_hi:[1,1,0]\n"
    "v_mfma_f32_16x16x32_bf16 v[96:99], a[176:179], v[192:195], v[96:99]\n"
    "s_waitcnt lgkmcnt(8)\n"
    "ds_read_u16_d16_hi v201, v201\n"
    "v_or_b32_e32 v187, v187, v203\n"
    "v_mfma_f32_16x16x32_bf16 v[100:103], a[180:183], v[192:195], v[100:103]\n"
    "v_lshlrev_b32_sdwa v203, 1, v175 dst_sel:DWORD dst_unused:UNUSED_PAD src0_sel:DWORD src1_sel:WORD_1\n"
    "v_cvt_pk_bf16_f32 v184, v234, v232\n"
    "v_mfma_f32_16x16x32_bf16 v[112:115], a[184:187], v[192:195], v[112:115]\n"
    "ds_read_u16_d16_hi v202, v202\n"
    "v_pk_fma_f16 v232, v188, v188, s48\n"
    "v_mfma_f32_16x16x32_bf16 v[116:119], a[188:191], v[192:195], v[116:119]\n"
    "v_pk_mul_f16 v232, v188, v232\n"
    "v_exp_f16_e32 v234, v232\n"
    "v_mfma_f32_16x16x32_bf16 v[128:131], v[24:27], v[192:195], v[128:131]\n"
    "s_waitcnt lgkmcnt(6)\n"
    "ds_read_u16_d16_hi v203, v203\n"
    "v_or_b32_e64 v189, v189, v205\n"
    "v_mfma_f32_16x16x32_bf16 v[132:135], v[28:31], v[192:195], v[132:135]\n"
    "v_cvt_pk_f16_f32 v192, v168, v169\n"
    "v_lshlrev_b32_sdwa v193, 1, v170 dst_sel:DWORD dst_unused:UNUSED_PAD src0_sel:DWORD src1_sel:WORD_1\n"
    "v_mfma_f32_16x16x32_bf16 v[104:107], a[176:179], v[196:199], v[104:107]\n"
    "ds_read_u16 v193, v193\n"
    "v_lshlrev_b32_sdwa v194, 1, v172 dst_sel:DWORD dst_unused:UNUSED_PAD src0_sel:DWORD src1_sel:WORD_1\n"
    "v_mfma_f32_16x16x32_bf16 v[108:111], a[180:183], v[196:199], v[108:111]\n"
    "ds_read_u16 v194, v194\n"
    "v_lshlrev_b32_sdwa v195, 1, v174 dst_sel:DWORD dst_unused:UNUSED_PAD src0_sel:DWORD src1_sel:WORD_1\n"
    "v_mfma_f32_16x16x32_bf16 v[120:123], a[184:187], v[196:199], v[120:123]\n"
    "v_lshlrev_b32_sdwa v205, 1, v179 dst_sel:DWORD dst_unused:UNUSED_PAD src0_sel:DWORD src1_sel:WORD_1\n"
    "v_exp_f16_sdwa v234, v232 dst_sel:WORD_1 dst_unused:UNUSED_PRESERVE src0_sel:WORD_1\n"
    "v_mfma_f32_16x16x32_bf16 v[124:127], a[188:191], v[196:199], v[124:127]\n"
    "s_waitcnt lgkmcnt(7)\n"
    "ds_read_u16 v195, v195\n"
    "v_or_b32_e32 v190, v190, v206\n"
    "v_mfma_f32_16x16x32_bf16 v[136:139], v[24:27], v[196:199], v[136:139]\n"
    "ds_read_u16_d16_hi v205, v205\n"
    "v_lshlrev_b32_sdwa v206, 1, v181 dst_sel:DWORD dst_unused:UNUSED_PAD src0_sel:DWORD src1_sel:WORD_1\n"
    "v_mfma_f32_16x16x32_bf16 v[140:143], v[28:31], v[196:199], v[140:143]\n"
    "s_nop 0\n"
    "v_cvt_pk_f16_f32 v196, v176, v177\n"
    "v_lshlrev_b32_sdwa v197, 1, v178 dst_sel:DWORD dst_unused:UNUSED_PAD src0_sel:DWORD src1_sel:WORD_1\n"
    "v_mfma_f32_16x16x32_bf16 v[96:99], a[192:195], v[184:187], v[96:99]\n"
    "s_waitcnt lgkmcnt(7)\n"
    "ds_read_u16 v197, v197\n"
    "v_lshlrev_b32_sdwa v198, 1, v180 dst_sel:DWORD dst_unused:UNUSED_PAD src0_sel:DWORD src1_sel:WORD_1\n"
    "v_smfmac_f32_32x32x32_bf16 v[128:143], a[248:251], a[232:239], v241\n"
    "v_lshlrev_b32_sdwa v199, 1, v182 dst_sel:DWORD dst_unused:UNUSED_PAD src0_sel:DWORD src1_sel:WORD_1\n"
    "v_or_b32_e64 v191, v191, v207\n"
    "v_lshlrev_b32_sdwa v207, 1, v183 dst_sel:DWORD dst_unused:UNUSED_PAD src0_sel:DWORD src1_sel:WORD_1\n"
    "v_pk_fma_f16 v234, v234, s49, s49\n"
    "v_rcp_f16_e64 v232, v234\n"
    "v_mfma_f32_16x16x32_bf16 v[100:103], a[196:199], v[184:187], v[100:103]\n"
    "ds_read_u16 v198, v198\n"
    "ds_read_u16_d16_hi v206, v206\n"
    "v_mfma_f32_16x16x32_bf16 v[112:115], a[200:203], v[184:187], v[112:115]\n"
    "v_rcp_f16_sdwa v232, v234 dst_sel:WORD_1 dst_unused:UNUSED_PRESERVE src0_sel:WORD_1\n"
    "v_mfma_f32_16x16x32_bf16 v[116:119], a[204:207], v[184:187], v[116:119]\n"
    "ds_read_u16 v199, v199\n"
    "v_fma_mix_f32 v234, v188, v232, 0 op_sel_hi:[1,1,0]\n"
    "v_mfma_f32_16x16x32_bf16 v[128:131], v[32:35], v[184:187], v[128:131]\n"
    "v_fma_mix_f32 v232, v188, v232, 0 op_sel:[1,1,0] op_sel_hi:[1,1,0]\n"
    "v_cvt_pk_bf16_f32 v188, v234, v232\n"
    "v_mfma_f32_16x16x32_bf16 v[132:135], v[36:39], v[184:187], v[132:135]\n"
    "s_waitcnt lgkmcnt(7)\n"
    "ds_read_u16_d16_hi v207, v207\n"
    "v_or_b32_e32 v193, v193, v201\n"
    "v_mfma_f32_16x16x32_bf16 v[104:107], a[192:195], v[188:191], v[104:107]\n"
    "v_pk_fma_f16 v232, v192, v192, s48\n"
    "v_pk_mul_f16 v232, v192, v232\n"
    "v_mfma_f32_16x16x32_bf16 v[108:111], a[196:199], v[188:191], v[108:111]\n"
    "s_waitcnt lgkmcnt(7)\n"
    "v_or_b32_e32 v194, v194, v202\n"
    "v_exp_f16_e64 v234, v232\n"
    "v_mfma_f32_16x16x32_bf16 v[120:123], a[200:203], v[188:191], v[120:123]\n"
    "s_waitcnt lgkmcnt(6)\n"
    "v_or_b32_e32 v195, v195, v203\n"
    "v_exp_f16_sdwa v234, v232 dst_sel:WORD_1 dst_unused:UNUSED_PRESERVE src0_sel:WORD_1\n"
    "v_mfma_f32_16x16x32_bf16 v[124:127], a[204:207], v[188:191], v[124:127]\n"
    "v_pk_fma_f16 v234, v234, s49, s49\n"
    "v_rcp_f16_e64 v232, v234\n"
    "v_mfma_f32_16x16x32_bf16 v[136:139], v[32:35], v[188:191], v[136:139]\n"
    "v_rcp_f16_sdwa v232, v234 dst_sel:WORD_1 dst_unused:UNUSED_PRESERVE src0_sel:WORD_1\n"
    "v_mfma_f32_16x16x32_bf16 v[140:143], v[36:39], v[188:191], v[140:143]\n"
    "s_waitcnt lgkmcnt(4)\n"
    "v_or_b32_e32 v197, v197, v205\n"
    "v_fma_mix_f32 v234, v192, v232, 0 op_sel_hi:[1,1,0]\n"
    "v_fma_mix_f32 v232, v192, v232, 0 op_sel:[1,1,0] op_sel_hi:[1,1,0]\n"
    "v_cvt_pk_bf16_f32 v192, v234, v232\n"
    "v_pk_fma_f16 v232, v196, v196, s48\n"
    "v_pk_mul_f16 v232, v196, v232\n"
    "v_exp_f16_e64 v234, v232\n"
    "v_mfma_f32_16x16x32_bf16 v[96:99], a[208:211], v[192:195], v[96:99]\n"
    "v_exp_f16_sdwa v234, v232 dst_sel:WORD_1 dst_unused:UNUSED_PRESERVE src0_sel:WORD_1\n"
    "v_mfma_f32_16x16x32_bf16 v[100:103], a[212:215], v[192:195], v[100:103]\n"
    "s_waitcnt lgkmcnt(2)\n"
    "v_or_b32_e32 v198, v198, v206\n"
    "v_pk_fma_f16 v234, v234, s49, s49\n"
    "v_rcp_f16_e64 v232, v234\n"
    "v_mfma_f32_16x16x32_bf16 v[112:115], a[216:219], v[192:195], v[112:115]\n"
    "v_rcp_f16_sdwa v232, v234 dst_sel:WORD_1 dst_unused:UNUSED_PRESERVE src0_sel:WORD_1\n"
    "v_mfma_f32_16x16x32_bf16 v[116:119], a[220:223], v[192:195], v[116:119]\n"
    "v_fma_mix_f32 v234, v196, v232, 0 op_sel_hi:[1,1,0]\n"
    "v_fma_mix_f32 v232, v196, v232, 0 op_sel:[1,1,0] op_sel_hi:[1,1,0]\n"
    "v_mfma_f32_16x16x32_bf16 v[128:131], v[40:43], v[192:195], v[128:131]\n"
    "s_waitcnt lgkmcnt(0)\n"
    "v_or_b32_e32 v199, v199, v207\n"
    "v_cvt_pk_bf16_f32 v196, v234, v232\n"
    "v_mfma_f32_16x16x32_bf16 v[132:135], v[44:47], v[192:195], v[132:135]\n"
    "v_cvt_pk_bf16_f32 v96, v96, v97\n"
    "v_cvt_pk_bf16_f32 v97, v98, v99\n"
    "v_mfma_f32_16x16x32_bf16 v[104:107], a[208:211], v[196:199], v[104:107]\n"
    "v_cvt_pk_bf16_f32 v98, v100, v101\n"
    "v_cvt_pk_bf16_f32 v99, v102, v103\n"
    "v_mfma_f32_16x16x32_bf16 v[108:111], a[212:215], v[196:199], v[108:111]\n"
    "ds_write_b128 v243, v[96:99] offset:24640\n"
    "v_mfma_f32_16x16x32_bf16 v[120:123], a[216:219], v[196:199], v[120:123]\n"
    "v_cvt_pk_bf16_f32 v112, v112, v113\n"
    "v_cvt_pk_bf16_f32 v113, v114, v115\n"
    "v_mfma_f32_16x16x32_bf16 v[124:127], a[220:223], v[196:199], v[124:127]\n"
    "v_cvt_pk_bf16_f32 v114, v116, v117\n"
    "v_cvt_pk_bf16_f32 v115, v118, v119\n"
    "v_mfma_f32_16x16x32_bf16 v[136:139], v[40:43], v[196:199], v[136:139]\n"
    "ds_read_b128 a[224:227], v249 offset:0\n"
    "ds_write_b128 v243, v[112:115] offset:26688\n"
    "v_mfma_f32_16x16x32_bf16 v[140:143], v[44:47], v[196:199], v[140:143]\n"
    "v_cvt_pk_bf16_f32 v128, v128, v129\n"
    "v_cvt_pk_bf16_f32 v129, v130, v131\n"
    "v_cvt_pk_bf16_f32 v130, v132, v133\n"
    "v_cvt_pk_bf16_f32 v131, v134, v135\n"
    "v_cvt_pk_bf16_f32 v104, v104, v105\n"
    "v_cvt_pk_bf16_f32 v105, v106, v107\n"
    "v_cvt_pk_bf16_f32 v106, v108, v109\n"
    "ds_read_b128 a[228:231], v249 offset:2048\n"
    "ds_write_b128 v243, v[128:131] offset:28736\n"
    "v_cvt_pk_bf16_f32 v107, v110, v111\n"
    "v_cvt_pk_bf16_f32 v120, v120, v121\n"
    "v_cvt_pk_bf16_f32 v121, v122, v123\n"
    "v_cvt_pk_bf16_f32 v122, v124, v125\n"
    "v_cvt_pk_bf16_f32 v123, v126, v127\n"
    "v_cvt_pk_bf16_f32 v136, v136, v137\n"
    "v_cvt_pk_bf16_f32 v137, v138, v139\n"
    "v_cvt_pk_bf16_f32 v138, v140, v141\n"
    "ds_read_b128 a[232:235], v249 offset:4096\n"
    "s_waitcnt lgkmcnt(4)\n"
    "buffer_store_dwordx4 a[224:227], v255, s[44:47], s58 offen offset:0 sc1\n"
    "ds_write_b128 v243, v[104:107] offset:25664\n"
    "v_cvt_pk_bf16_f32 v139, v142, v143\n"
    "ds_read_b128 a[236:239], v249 offset:1024\n"
    "ds_write_b128 v243, v[120:123] offset:27712\n"
    "ds_read_b128 a[252:255], v249 offset:3072\n"
    "ds_write_b128 v243, v[136:139] offset:29760\n"
    "s_waitcnt lgkmcnt(4)\n"
    "buffer_store_dwordx4 a[228:231], v255, s[44:47], s58 offen offset:64 sc1\n"
    "ds_read_b128 a[240:243], v249 offset:5120\n"
    "buffer_store_dwordx4 a[232:235], v255, s[44:47], s58 offen offset:128 sc1\n"
    "s_waitcnt lgkmcnt(3)\n"
    "buffer_store_dwordx4 a[236:239], v255, s[44:47], s89 offen offset:0 sc1\n"
    "s_waitcnt lgkmcnt(2)\n"
    "buffer_store_dwordx4 a[252:255], v255, s[44:47], s89 offen offset:64 sc1\n"
    "s_waitcnt lgkmcnt(0)\n"
    "buffer_store_dwordx4 a[240:243], v255, s[44:47], s89 offen offset:128 sc1\n"
    "97:\n"
    "98:\n"
    "s_waitcnt vmcnt(0) lgkmcnt(0)\n"
    ".Lreplk_end:\n"
    : "+{s66}"(flg), "+{v208}"(tid), "+{s65}"(blk), "+{s[98:99]}"(ka)
    : "{s[96:97]}"(CTAB)
    : "memory", "scc", "vcc", "m0", "v0", "v1", "v2", "v3", "v4", "v5", "v6", "v7", "v8", "v9", "v10", "v11", "v12", "v13", "v14", "v15", "v16", "v17", "v18", "v19", "v20", "v21", "v22", "v23", "v24", "v25", "v26", "v27", "v28", "v29", "v30", "v31", "v32", "v33", "v34", "v35", "v36", "v37", "v38", "v39", "v40", "v41", "v42", "v43", "v44", "v45", "v46", "v47", "v48", "v49", "v50", "v51", "v52", "v53", "v54", "v55", "v56", "v57", "v58", "v59", "v60", "v61", "v62", "v63", "v64", "v65", "v66", "v67", "v68", "v69", "v70", "v71", "v72", "v73", "v74", "v75", "v76", "v77", "v78", "v79", "v80", "v81", "v82", "v83", "v84", "v85", "v86", "v87", "v88", "v89", "v90", "v91", "v92", "v93", "v94", "v95", "v96", "v97", "v98", "v99", "v100", "v101", "v102", "v103", "v104", "v105", "v106", "v107", "v108", "v109", "v110", "v111", "v112", "v113", "v114", "v115", "v116", "v117", "v118", "v119", "v120", "v121", "v122", "v123", "v124", "v125", "v126", "v127", "v128", "v129", "v130", "v131", "v132", "v133", "v134", "v135", "v136", "v137", "v138", "v139", "v140", "v141", "v142", "v143", "v144", "v145", "v146", "v147", "v148", "v149", "v150", "v151", "v152", "v153", "v154", "v155", "v156", "v157", "v158", "v159", "v160", "v161", "v162", "v163", "v164", "v165", "v166", "v167", "v168", "v169", "v170", "v171", "v172", "v173", "v174", "v175", "v176", "v177", "v178", "v179", "v180", "v181", "v182", "v183", "v184", "v185", "v186", "v187", "v188", "v189", "v190", "v191", "v192", "v193", "v194", "v195", "v196", "v197", "v198", "v199", "v200", "v201", "v202", "v203", "v204", "v205", "v206", "v207", "v209", "v210", "v211", "v212", "v213", "v214", "v215", "v216", "v217", "v218", "v219", "v220", "v221", "v222", "v223", "v224", "v225", "v226", "v227", "v228", "v229", "v230", "v231", "v232", "v233", "v234", "v235", "v236", "v237", "v238", "v239", "v240", "v241", "v242", "v243", "v244", "v245", "v246", "v247", "v248", "v249", "v250", "v251", "v252", "v253", "v254", "v255", "a0", "a1", "a2", "a3", "a4", "a5", "a6", "a7", "a8", "a9", "a10", "a11", "a12", "a13", "a14", "a15", "a16", "a17", "a18", "a19", "a20", "a21", "a22", "a23", "a24", "a25", "a26", "a27", "a28", "a29", "a30", "a31", "a32", "a33", "a34", "a35", "a36", "a37", "a38", "a39", "a40", "a41", "a42", "a43", "a44", "a45", "a46", "a47", "a48", "a49", "a50", "a51", "a52", "a53", "a54", "a55", "a56", "a57", "a58", "a59", "a60", "a61", "a62", "a63", "a64", "a65", "a66", "a67", "a68", "a69", "a70", "a71", "a72", "a73", "a74", "a75", "a76", "a77", "a78", "a79", "a80", "a81", "a82", "a83", "a84", "a85", "a86", "a87", "a88", "a89", "a90", "a91", "a92", "a93", "a94", "a95", "a96", "a97", "a98", "a99", "a100", "a101", "a102", "a103", "a104", "a105", "a106", "a107", "a108", "a109", "a110", "a111", "a112", "a113", "a114", "a115", "a116", "a117", "a118", "a119", "a120", "a121", "a122", "a123", "a124", "a125", "a126", "a127", "a128", "a129", "a130", "a131", "a132", "a133", "a134", "a135", "a136", "a137", "a138", "a139", "a140", "a141", "a142", "a143", "a144", "a145", "a146", "a147", "a148", "a149", "a150", "a151", "a152", "a153", "a154", "a155", "a156", "a157", "a158", "a159", "a160", "a161", "a162", "a163", "a164", "a165", "a166", "a167", "a168", "a169", "a170", "a171", "a172", "a173", "a174", "a175", "a176", "a177", "a178", "a179", "a180", "a181", "a182", "a183", "a184", "a185", "a186", "a187", "a188", "a189", "a190", "a191", "a192", "a193", "a194", "a195", "a196", "a197", "a198", "a199", "a200", "a201", "a202", "a203", "a204", "a205", "a206", "a207", "a208", "a209", "a210", "a211", "a212", "a213", "a214", "a215", "a216", "a217", "a218", "a219", "a220", "a221", "a222", "a223", "a224", "a225", "a226", "a227", "a228", "a229", "a230", "a231", "a232", "a233", "a234", "a235", "a236", "a237", "a238", "a239", "a240", "a241", "a242", "a243", "a244", "a245", "a246", "a247", "a248", "a249", "a250", "a251", "a252", "a253", "a254", "a255", "s36", "s37", "s38", "s39", "s40", "s41", "s42", "s43", "s44", "s45", "s46", "s47", "s48", "s49", "s50", "s51", "s52", "s53", "s54", "s55", "s56", "s57", "s58", "s59", "s60", "s61", "s62", "s63", "s64", "s67", "s68", "s69", "s70", "s71", "s72", "s73", "s74", "s75", "s76", "s77", "s78", "s79", "s80", "s81", "s82", "s83", "s84", "s85", "s86", "s87", "s88", "s89", "s90", "s91", "s92", "s93", "s94", "s95");
  if (flg != 2u) {
    const int l2 = __builtin_amdgcn_mbcnt_hi(~0u, __builtin_amdgcn_mbcnt_lo(~0u, 0u));
    slow_path(x, dwl, dws, w1, w2, out, (float*)smem, w * 64 + l2, blockIdx.x >> 3, (blockIdx.x & 7) * 4);
  }
}

extern "C" void kernel_launch(void* const* d_in, const int* in_sizes, int n_in, void* d_out, int out_size, void* d_ws,
                              size_t ws_size, hipStream_t stream) {
  replk_kernel<<<256, 256, 0, stream>>>((const u16*)d_in[0], (const u16*)d_in[1], (const u16*)d_in[2], (const u16*)d_in[3],
                                        (const u16*)d_in[4], (u16*)d_out
                                        );
}
```

```cpp
#include <hip/hip_runtime.h>
#include <hip/hip_bf16.h>

typedef unsigned u32;
typedef unsigned short u16;
typedef unsigned long long u64;
typedef __bf16 bf16x2 __attribute__((ext_vector_type(2)));
typedef float f32x2 __attribute__((ext_vector_type(2)));
#define DI __device__ __forceinline__

constexpr int IMG = 512;
constexpr int CH = 96;
constexpr int HID = 384;
constexpr int PXB = 192;
constexpr int ROWB = 98304;
constexpr int TOEP_CS = 336;
constexpr int XT_RS = 320;
constexpr int XT_PP = 1360;
constexpr int XT_CG = 6752;
constexpr int GS = 6160;
constexpr int L_LUT = 0;
constexpr int L_TOEP = 8208;
constexpr int L_W = 40960;
constexpr int L_LUTN = 65536;
constexpr int L_XT = 81920;
constexpr int L_H = 108928;
constexpr int L_EX = 133568;
constexpr int XT_Q = 27024;
constexpr int L_MISC = 162992;
constexpr int L_TOTAL = 163056;
constexpr int LUTK = 106;
constexpr float GC1 = -2.302208198144325f, GC3 = -0.1029432395800235f;
__device__ const u32 CTAB[3 * 1024] = {
671109120,671098880,134490112,673187840,671372952,671229956,145500164,673318996,671636784,671361032,156510216,673450152,671900616,671492108,167520268,673581308,672164452,684206128,178530352,686295376,672428284,684337204,189540404,686426532,672692116,684468280,200550456,686557688,672955948,684599356,211560508,686688844,673219784,697313376,222570592,699402912,673483616,697444452,233580644,699534068,673747448,697575528,244590696,699665224,674011280,697706604,255600748,699796380,674254849,710420624,266610832,712510448,674518681,710551700,277620884,712641604,674782513,710682776,288630936,712772760,675046345,710813852,299640988,712903916,
684223077,723527872,310651072,721423680,684486909,723658948,321661124,721554836,683702165,723790024,332671176,721685992,683965997,723921100,343681228,721817148,685278409,736635120,354691312,734531216,685542241,736766196,365701364,734662372,684757497,736897272,376711416,734793528,685021329,737028348,387721468,734924684,686313474,749742368,398731552,747638752,686577306,749873444,409741604,747769908,685792562,750004520,420751656,747901064,686056394,750135596,431761708,748032220,687368806,762849616,442771792,760746288,687632638,762980692,453781844,760877444,686847894,763111768,464791896,761008600,687111726,763242844,475801948,761139756,
697337034,771762560,486812032,773853824,697600866,771893636,497822084,773984980,697864698,772024712,508832136,774116136,698128530,772155788,519842188,774247292,696274947,784869808,530852272,786961360,696538779,785000884,541862324,787092516,696802611,785131960,552872376,787223672,697066443,785263036,563882428,787354828,699427431,797977056,574892512,800068896,699691263,798108132,585902564,800200052,699955095,798239208,596912616,800331208,700218927,798370284,607922668,800462364,698385611,811084304,618932752,813176432,698649443,811215380,629942804,813307588,698913275,811346456,640952856,813438744,699177107,811477532,651962908,813569900,
710430724,824191552,135539264,822089664,710694556,824322628,146549316,822220820,709909812,824453704,157559368,822351976,710173644,824584780,168569420,822483132,709388904,837298800,179579504,835197200,709652736,837429876,190589556,835328356,708867992,837560952,201599608,835459512,709131824,837692028,212609660,835590668,712541388,850406048,223619744,848304736,712805220,850537124,234629796,848435892,712020476,850668200,245639848,848567048,712284308,850799276,256649900,848698204,711479301,863513296,267659984,861412272,711743133,863644372,278670036,861543428,710958389,863775448,289680088,861674584,711222221,863906524,300690140,861805740,
723544681,677394944,311704064,679484672,723808513,677526020,322714116,679615828,724072345,677657096,333724168,679746984,724336177,677788172,344734220,679878140,724600013,690502192,355744304,692592208,724863845,690633268,366754356,692723364,725127677,690764344,377764408,692854520,725391509,690895420,388774460,692985676,721440774,703609440,399784544,705699744,721704606,703740516,410794596,705830900,721968438,703871592,421804648,705962056,722232270,704002668,432814700,706093212,722496106,716716688,443824784,718807280,722759938,716847764,454834836,718938436,723023770,716978840,465844888,719069592,723287602,717109916,476854940,719200748,
736658638,725629632,487865024,731914816,736922470,725760708,498875076,732045972,736137726,725891784,509885128,732177128,736401558,726022860,520895180,732308284,737693703,738736880,531905264,745022352,737957535,738867956,542915316,745153508,737172791,738999032,553925368,745284664,737436623,739130108,564935420,745415820,734554731,751844128,575945504,758129888,734818563,751975204,586955556,758261044,734033819,752106280,597965608,758392200,734297651,752237356,608975660,758523356,735610063,764951376,619985744,771237424,735873895,765082452,630995796,771368580,735089151,765213528,642005848,771499736,735352983,765344604,653015900,771630892,
749752328,778058624,136592256,780150656,750016160,778189700,147602308,780281812,750279992,778320776,158612360,780443436,750543824,778451852,169622412,780574508,748710508,791165872,180632496,793288492,748974340,791296948,191642548,793419564,749238172,791428024,202652600,793550636,749502004,791559100,213662652,793681708,747668688,804273120,224672736,806395692,747932520,804404196,235682788,806526764,748196352,804535272,246692840,806657836,748460184,804666348,257702892,806788908,746606601,817380368,268712976,819502892,746870433,817511444,279723028,819633964,747134265,817642520,290733080,819765036,747398097,817773596,301743132,819896108,
762866285,826293312,312753216,832610092,763130117,826424388,323763268,832741164,762345373,826555464,334773320,832872236,762609205,826686540,345783372,833003308,761824465,839400560,356793456,845717292,762088297,839531636,367803508,845848364,761303553,839662712,378813560,845979436,761567385,839793788,389823612,846110508,760762378,852507808,400833696,858824492,761026210,852638884,411843748,858955564,760241466,852769960,422853800,859086636,760505298,852901036,433863852,859217708,759720558,865615056,444873936,871931692,759984390,865746132,455883988,872062764,759199646,865877208,466894040,872193836,759463478,866008284,477904092,872324908,
771785938,671098882,488908800,673226540,772049770,671229958,499918852,673357612,772313602,671361034,510928904,673488684,772577434,671492110,521938956,673619756,772821003,684206130,532949040,686333740,773084835,684337206,543959092,686464812,773348667,684468282,554969144,686595884,773612499,684599358,565979196,686726956,773876335,697313378,576989280,699440940,774140167,697444454,587999332,699572012,774403999,697575530,599009384,699703084,774667831,697706606,610019436,699834156,774931667,710420626,621029520,712548140,775195499,710551702,632039572,712679212,775459331,710682778,643049624,712810284,775723163,710813854,654059676,712941356,
784879628,723527874,137636032,721461036,785143460,723658950,148646084,721592108,784358716,723790026,159656136,721723180,784622548,723921102,170666188,721854252,785934960,736635122,181676272,734568236,786198792,736766198,192686324,734699308,785414048,736897274,203696376,734830380,785677880,737028350,214706428,734961452,786990292,749742370,225716512,747675436,787254124,749873446,236726564,747806508,786469380,750004522,247736616,747937580,786733212,750135598,258746668,748068652,788025357,762849618,269756752,760782636,788289189,762980694,280766804,760913708,787504445,763111770,291776856,761044780,787768277,763242846,302786908,761175852,
797993585,771762562,313796992,773889836,798257417,771893638,324807044,774020908,798521249,772024714,335817096,774151980,798785081,772155790,346827148,774283052,796951765,784869810,357837232,786997036,797215597,785000886,368847284,787128108,797479429,785131962,379857336,787259180,797743261,785263038,390867388,787390252,800083982,797977058,401877472,800104236,800347814,798108134,412887524,800235308,800611646,798239210,423897576,800366380,800875478,798370286,434907628,800497452,799042162,811084306,445917712,813211436,799305994,811215382,456927764,813342508,799569826,811346458,467937816,813473580,799833658,811477534,478947868,813604652,
811107542,824191554,489957952,822124332,811371374,824322630,500968004,822255404,810586630,824453706,511978056,822386476,810850462,824584782,522988108,822517548,810045455,837298802,533998192,835231532,810309287,837429878,545008244,835362604,809524543,837560954,556018296,835493676,809788375,837692030,567028348,835624748,813197939,850406050,578038432,848338732,813461771,850537126,589048484,848469804,812677027,850668202,600058536,848600876,812940859,850799278,611068588,848731948,812156119,863513298,622078672,861445932,812419951,863644374,633088724,861577004,811635207,863775450,644098776,861708076,811899039,863906526,655108828,861839148,
824201232,677394946,138689024,679517996,824465064,677526022,149699076,679649068,824728896,677657098,160709128,679780140,824992728,677788174,171719180,679911212,825256564,690502194,182729264,692625196,825520396,690633270,193739316,692756268,825784228,690764346,204749368,692887340,826048060,690895422,215759420,693018412,822117592,703609442,226769504,705732396,822381424,703740518,237779556,705863468,822645256,703871594,248789608,705994540,822909088,704002670,259799660,706125612,823152657,716716690,270809744,718839596,823416489,716847766,281819796,718970668,823680321,716978842,292829848,719101740,823944153,717109918,303839900,719232812,
837315189,725629634,314849984,731946796,837579021,725760710,325860036,732077868,836794277,725891786,336870088,732208940,837058109,726022862,347880140,732340012,838370521,738736882,358890224,745053996,838634353,738867958,369900276,745185068,837849609,738999034,380910328,745316140,838113441,739130110,391920380,745447212,835211282,751844130,402930464,758161196,835475114,751975206,413940516,758292268,834690370,752106282,424950568,758423340,834954202,752237358,435960620,758554412,836266614,764951378,446970704,771268396,836530446,765082454,457980756,771399468,835745702,765213530,468990808,771530540,836009534,765344606,480000860,771661612,
850429146,778058626,491010944,780181292,850692978,778189702,502020996,780312364,850956810,778320778,513031048,780443436,851220642,778451854,524041100,780574508,849367059,791165874,535051184,793288492,849630891,791296950,546061236,793419564,849894723,791428026,557071288,793550636,850158555,791559102,568081340,793681708,848325239,804273122,579091424,806395692,848589071,804404198,590101476,806526764,848852903,804535274,601111528,806657836,849116735,804666350,612121580,806788908,847283419,817380370,623131664,819502892,847547251,817511446,634141716,819633964,847811083,817642522,645151768,819765036,848074915,817773598,656161820,819896108,
863541411,826293314,656161856,832610092,863803555,826424390,656161860,832741164,863017123,826555466,656161864,832872236,863279267,826686542,656161868,833003308,862492835,839400562,656161904,845717292,862754979,839531638,656161908,845848364,861968547,839662714,656161912,845979436,862230691,839793790,656161916,846110508,861444259,852507810,656161952,858824492,861706403,852638886,656161956,858955564,860919971,852769962,656161960,859086636,861182115,852901038,656161964,859217708,860395683,865615058,656162000,871931692,860657827,865746134,656162004,872062764,859871395,865877210,656162008,872193836,860133539,866008286,656162012,872324908,
2152998912u,1885629024,1342187520,1342212212,2152998916u,1885891172,1452812292,1364494564,2151688200u,1886153320,1563437064,1386776836,2218797068u,1886415468,1674061836,1409059156,2164598800u,1886677616,1342253072,1342474360,2164598804u,1886939764,1452877844,1364756712,2164271128u,1887201912,1563502616,1387038984,2231379996u,1887464060,1674127388,1409321304,1083254816,1887726208,1342318624,1342998652,1083254820,1887988356,1452943396,1365281004,1082140712,1888250504,1563568168,1387563276,1149249580,1888512652,1674192940,1409845596,1108617264,1888774800,1342384176,1342736512,1108617268,1889036948,1453008948,1365018864,1107306552,1889299096,1563633720,1387301136,1174415420,1889561244,1674258492,1409583456,
269756480,1892969168,1342449728,1342474372,269756484,1893231316,1453074500,1364756692,268445768,1893493464,1563699272,1387038964,335554636,1893755612,1674324044,1409321316,268773456,1891920576,1342515280,1342736520,268773460,1892182724,1453140052,1365018840,268445784,1892444872,1563764824,1387301112,335554652,1892707020,1674389596,1409583464,1343301728,1890871984,1342580832,1343260812,1343301732,1891134132,1453205604,1365543132,1342187624,1891396280,1563830376,1387825404,1409296492,1891658428,1674455148,1410107756,1343498352,1889823392,1342646384,1342998672,1343498356,1890085540,1453271156,1365280992,1342187640,1890347688,1563895928,1387563264,1409296508,1890609836,1674520700,1409845616,
538192000,1895066352,1342711936,1364494484,538192004,1895328500,1453336708,1386776772,536881288,1895590648,1563961480,1409059108,603990156,1895852796,1674586252,1431341364,537208976,1894017760,1342777488,1364756632,537208980,1894279908,1453402260,1387038920,536881304,1894542056,1564027032,1409321256,603990172,1894804204,1674651804,1431603512,1611737248,1897163536,1342843040,1365280924,1611737252,1897425684,1453467812,1387563212,1610623144,1897687832,1564092584,1409845548,1677732012,1897949980,1674717356,1432127804,1611933872,1896114944,1342908592,1365018784,1611933876,1896377092,1453533364,1387301072,1610623160,1896639240,1564158136,1409583408,1677732028,1896901388,1674782908,1431865664,
810821824,1900309312,1342974144,1364756644,810821828,1900571460,1453598916,1387038900,809511112,1900833608,1564223688,1409321236,876619980,1901095756,1674848460,1431603524,822421712,1901357904,1343039696,1365018792,822421716,1901620052,1453664468,1387301048,822094040,1901882200,1564289240,1409583384,889202908,1902144348,1674914012,1431865672,1888561376,1898212128,1343105248,1365543084,1888561380,1898474276,1453730020,1387825340,1887447272,1898736424,1564354792,1410107676,1954556140,1898998572,1674979564,1432389964,1913923824,1899260720,1343170800,1365280944,1913923828,1899522868,1453795572,1387563200,1912613112,1899785016,1564420344,1409845536,1979721980,1900047164,1675045116,1432127824,
2152999168u,1784705124,1347430656,1452835440,2152999172u,1784967272,1458055428,1475117792,2151688456u,1785229420,1568680200,1497400064,2218797324u,1785491568,1679304972,1519682384,2164599056u,1785753716,1347496208,1453097588,2164599060u,1786015864,1458120980,1475379940,2164271384u,1786278012,1568745752,1497662212,2231380252u,1786540160,1679370524,1519944532,1083255072,1786802308,1347561760,1453621880,1083255076,1787064456,1458186532,1475904232,1082140968,1787326604,1568811304,1498186504,1149249836,1787588752,1679436076,1520468824,1108617520,1787850900,1347627312,1453359740,1108617524,1788113048,1458252084,1475642092,1107306808,1788375196,1568876856,1497924364,1174415676,1788637344,1679501628,1520206684,
269756736,1792045268,1347692864,1453097600,269756740,1792307416,1458317636,1475379920,268446024,1792569564,1568942408,1497662192,335554892,1792831712,1679567180,1519944544,268773712,1790996676,1347758416,1453359748,268773716,1791258824,1458383188,1475642068,268446040,1791520972,1569007960,1497924340,335554908,1791783120,1679632732,1520206692,1343301984,1789948084,1347823968,1453884040,1343301988,1790210232,1458448740,1476166360,1342187880,1790472380,1569073512,1498448632,1409296748,1790734528,1679698284,1520730984,1343498608,1788899492,1347889520,1453621900,1343498612,1789161640,1458514292,1475904220,1342187896,1789423788,1569139064,1498186492,1409296764,1789685936,1679763836,1520468844,
538192256,1794142452,1347955072,1475117712,538192260,1794404600,1458579844,1497400000,536881544,1794666748,1569204616,1519682336,603990412,1794928896,1679829388,1541964592,537209232,1793093860,1348020624,1475379860,537209236,1793356008,1458645396,1497662148,536881560,1793618156,1569270168,1519944484,603990428,1793880304,1679894940,1542226740,1611737504,1796239636,1348086176,1475904152,1611737508,1796501784,1458710948,1498186440,1610623400,1796763932,1569335720,1520468776,1677732268,1797026080,1679960492,1542751032,1611934128,1795191044,1348151728,1475642012,1611934132,1795453192,1458776500,1497924300,1610623416,1795715340,1569401272,1520206636,1677732284,1795977488,1680026044,1542488892,
810822080,1799385412,1348217280,1475379872,810822084,1799647560,1458842052,1497662128,809511368,1799909708,1569466824,1519944464,876620236,1800171856,1680091596,1542226752,822421968,1800434004,1348282832,1475642020,822421972,1800696152,1458907604,1497924276,822094296,1800958300,1569532376,1520206612,889203164,1801220448,1680157148,1542488900,1888561632,1797288228,1348348384,1476166312,1888561636,1797550376,1458973156,1498448568,1887447528,1797812524,1569597928,1520730904,1954556396,1798074672,1680222700,1543013192,1913924080,1798336820,1348413936,1475904172,1913924084,1798598968,1459038708,1498186428,1912613368,1798861116,1569663480,1520468764,1979722236,1799123264,1680288252,1542751052,
2152999424u,2087482984,1352673280,1563464828,2152999428u,2087745132,1463298052,1585747180,2151688712u,2088007280,1573922824,1608029452,2218797580u,2088269428,1684547596,1630311772,2164599312u,2088531576,1352738832,1563726976,2164599316u,2088793724,1463363604,1586009328,2164271640u,2089055872,1573988376,1608291600,2231380508u,2089318020,1684613148,1630573920,1083255328,2089580168,1352804384,1564251268,1083255332,2089842316,1463429156,1586533620,1082141224,2090104464,1574053928,1608815892,1149250092,2090366612,1684678700,1631098212,1108617776,2090628760,1352869936,1563989128,1108617780,2090890908,1463494708,1586271480,1107307064,2091153056,1574119480,1608553752,1174415932,2091415204,1684744252,1630836072,
269756992,2094823128,1352935488,1563726988,269756996,2095085276,1463560260,1586009308,268446280,2095347424,1574185032,1608291580,335555148,2095609572,1684809804,1630573932,268773968,2093774536,1353001040,1563989136,268773972,2094036684,1463625812,1586271456,268446296,2094298832,1574250584,1608553728,335555164,2094560980,1684875356,1630836080,1343302240,2092725944,1353066592,1564513428,1343302244,2092988092,1463691364,1586795748,1342188136,2093250240,1574316136,1609078020,1409297004,2093512388,1684940908,1631360372,1343498864,2091677352,1353132144,1564251288,1343498868,2091939500,1463756916,1586533608,1342188152,2092201648,1574381688,1608815880,1409297020,2092463796,1685006460,1631098232,
538192512,2096920312,1353197696,1585747100,538192516,2097182460,1463822468,1608029388,536881800,2097444608,1574447240,1630311724,603990668,2097706756,1685072012,1652593980,537209488,2095871720,1353263248,1586009248,537209492,2096133868,1463888020,1608291536,536881816,2096396016,1574512792,1630573872,603990684,2096658164,1685137564,1652856128,1611737760,2099017496,1353328800,1586533540,1611737764,2099279644,1463953572,1608815828,1610623656,2099541792,1574578344,1631098164,1677732524,2099803940,1685203116,1653380420,1611934384,2097968904,1353394352,1586271400,1611934388,2098231052,1464019124,1608553688,1610623672,2098493200,1574643896,1630836024,1677732540,2098755348,1685268668,1653118280,
810822336,2102163272,1353459904,1586009260,810822340,2102425420,1464084676,1608291516,809511624,2102687568,1574709448,1630573852,876620492,2102949716,1685334220,1652856140,822422224,2103211864,1353525456,1586271408,822422228,2103474012,1464150228,1608553664,822094552,2103736160,1574775000,1630836000,889203420,2103998308,1685399772,1653118288,1888561888,2100066088,1353591008,1586795700,1888561892,2100328236,1464215780,1609077956,1887447784,2100590384,1574840552,1631360292,1954556652,2100852532,1685465324,1653642580,1913924336,2101114680,1353656560,1586533560,1913924340,2101376828,1464281332,1608815816,1912613624,2101638976,1574906104,1631098152,1979722492,2101901124,1685530876,1653380440,
2152999680u,1986559084,1357916416,1674088056,2152999684u,1986821232,1468541188,1696370408,2151688968u,1987083380,1579165960,1718652680,2218797836u,1987345528,1689790732,1740935000,2164599568u,1987607676,1357981968,1674350204,2164599572u,1987869824,1468606740,1696632556,2164271896u,1988131972,1579231512,1718914828,2231380764u,1988394120,1689856284,1741197148,1083255584,1988656268,1358047520,1674874496,1083255588,1988918416,1468672292,1697156848,1082141480,1989180564,1579297064,1719439120,1149250348,1989442712,1689921836,1741721440,1108618032,1989704860,1358113072,1674612356,1108618036,1989967008,1468737844,1696894708,1107307320,1990229156,1579362616,1719176980,1174416188,1990491304,1689987388,1741459300,
269757248,1993899228,1358178624,1674350216,269757252,1994161376,1468803396,1696632536,268446536,1994423524,1579428168,1718914808,335555404,1994685672,1690052940,1741197160,268774224,1992850636,1358244176,1674612364,268774228,1993112784,1468868948,1696894684,268446552,1993374932,1579493720,1719176956,335555420,1993637080,1690118492,1741459308,1343302496,1991802044,1358309728,1675136656,1343302500,1992064192,1468934500,1697418976,1342188392,1992326340,1579559272,1719701248,1409297260,1992588488,1690184044,1741983600,1343499120,1990753452,1358375280,1674874516,1343499124,1991015600,1469000052,1697156836,1342188408,1991277748,1579624824,1719439108,1409297276,1991539896,1690249596,1741721460,
538192768,1995996412,1358440832,1696370328,538192772,1996258560,1469065604,1718652616,536882056,1996520708,1579690376,1740934952,603990924,1996782856,1690315148,1763217208,537209744,1994947820,1358506384,1696632476,537209748,1995209968,1469131156,1718914764,536882072,1995472116,1579755928,1741197100,603990940,1995734264,1690380700,1763479356,1611738016,1998093596,1358571936,1697156768,1611738020,1998355744,1469196708,1719439056,1610623912,1998617892,1579821480,1741721392,1677732780,1998880040,1690446252,1764003648,1611934640,1997045004,1358637488,1696894628,1611934644,1997307152,1469262260,1719176916,1610623928,1997569300,1579887032,1741459252,1677732796,1997831448,1690511804,1763741508,
810822592,2001239372,1358703040,1696632488,810822596,2001501520,1469327812,1718914744,809511880,2001763668,1579952584,1741197080,876620748,2002025816,1690577356,1763479368,822422480,2002287964,1358768592,1696894636,822422484,2002550112,1469393364,1719176892,822094808,2002812260,1580018136,1741459228,889203676,2003074408,1690642908,1763741516,1888562144,1999142188,1358834144,1697418928,1888562148,1999404336,1469458916,1719701184,1887448040,1999666484,1580083688,1741983520,1954556908,1999928632,1690708460,1764265808,1913924592,2000190780,1358899696,1697156788,1913924596,2000452928,1469524468,1719439044,1912613880,2000715076,1580149240,1741721380,1979722748,2000977224,1690774012,1764003668,
136579076,1343253088,52428,12582912,135530496,1453879396,843229,12648449,134481920,1564505704,1634030,12713986,145229824,1675132012,2411724,12779523,136841224,1343318640,3198156,13369356,135792808,1453944948,3988957,13434893,134744232,1564571256,4779758,13500430,139724968,1675197564,5557452,13565967,137103372,1343384208,6343884,14155800,136054952,1454010516,7134685,14221337,135006376,1564636824,7925486,14286874,139724968,1675263132,8703180,14352411,137365520,1343449728,9489612,14942244,136316928,1454076036,10280413,15007781,135268352,1564702344,11071214,15073318,145229824,1675328652,11848908,15138855,
139724868,1365535332,117964,12582960,139724852,1476161640,908765,12648497,138676260,1586787948,1699566,12714034,137627668,1697414256,2477260,12779571,145229896,1365600884,3263692,13369404,145229880,1476227192,4054493,13434941,138938408,1586853500,4845294,13500478,137889816,1697479808,5622988,13566015,145229900,1365666452,6409420,14155848,145229884,1476292760,7200221,14221385,139200556,1586919068,7991022,14286922,138151964,1697545376,8768716,14352459,139724880,1365731972,9555148,14942292,139724864,1476358280,10345949,15007829,139462704,1586984588,11136750,15073366,138414112,1697610896,11914444,15138903,
137627668,1387817576,183500,12583008,136579076,1498443884,974301,12648545,135530496,1609070192,1765102,12714082,134481920,1719696500,2542796,12779619,137889816,1387883128,3329228,13369452,136841224,1498509436,4120029,13434989,135792808,1609135744,4910830,13500526,134744232,1719762052,5688524,13566063,138151964,1387948696,6474956,14155896,137103372,1498575004,7265757,14221433,136054952,1609201312,8056558,14286970,135006376,1719827620,8834252,14352507,138414112,1388014216,9620684,14942340,137365520,1498640524,10411485,15007877,136316928,1609266832,11202286,15073414,135268352,1719893140,11979980,15138951,
139724968,1410099820,249036,12583056,139724868,1520726128,1039837,12648593,139724852,1631352436,1830638,12714130,138676260,1741978744,2608332,12779667,145229824,1410165372,3394764,13369500,145229896,1520791680,4185565,13435037,145229880,1631417988,4976366,13500574,138938408,1742044296,5754060,13566111,145229824,1410230940,6540492,14155944,145229900,1520857248,7331293,14221481,145229884,1631483556,8122094,14287018,139200556,1742109864,8899788,14352555,139724968,1410296460,9686220,14942388,139724880,1520922768,10477021,15007925,139724864,1631549076,11267822,15073462,139462704,1742175384,12045516,15138999,
180619940,1348496080,52428,12582912,179571360,1459122388,843229,12648449,178522784,1569748696,1634030,12713986,189270688,1680375004,2411724,12779523,180882088,1348561600,3198156,13369356,179833672,1459187908,3988957,13434893,178785096,1569814216,4779758,13500430,183765832,1680440524,5557452,13565967,181144236,1348627104,6343884,14155800,180095816,1459253412,7134685,14221337,179047240,1569879720,7925486,14286874,183765832,1680506028,8703180,14352411,181406384,1348692656,9489612,14942244,180357792,1459318964,10280413,15007781,179309216,1569945272,11071214,15073318,189270688,1680571580,11848908,15138855,
183765732,1370778324,117964,12582960,183765716,1481404632,908765,12648497,182717124,1592030940,1699566,12714034,181668532,1702657248,2477260,12779571,189270760,1370843844,3263692,13369404,189270744,1481470152,4054493,13434941,182979272,1592096460,4845294,13500478,181930680,1702722768,5622988,13566015,189270764,1370909348,6409420,14155848,189270748,1481535656,7200221,14221385,183241420,1592161964,7991022,14286922,182192828,1702788272,8768716,14352459,183765744,1370974900,9555148,14942292,183765728,1481601208,10345949,15007829,183503568,1592227516,11136750,15073366,182454976,1702853824,11914444,15138903,
181668532,1393060568,183500,12583008,180619940,1503686876,974301,12648545,179571360,1614313184,1765102,12714082,178522784,1724939492,2542796,12779619,181930680,1393126088,3329228,13369452,180882088,1503752396,4120029,13434989,179833672,1614378704,4910830,13500526,178785096,1725005012,5688524,13566063,182192828,1393191592,6474956,14155896,181144236,1503817900,7265757,14221433,180095816,1614444208,8056558,14286970,179047240,1725070516,8834252,14352507,182454976,1393257144,9620684,14942340,181406384,1503883452,10411485,15007877,180357792,1614509760,11202286,15073414,179309216,1725136068,11979980,15138951,
183765832,1415342812,249036,12583056,183765732,1525969120,1039837,12648593,183765716,1636595428,1830638,12714130,182717124,1747221736,2608332,12779667,189270688,1415408332,3394764,13369500,189270760,1526034640,4185565,13435037,189270744,1636660948,4976366,13500574,182979272,1747287256,5754060,13566111,189270688,1415473836,6540492,14155944,189270764,1526100144,7331293,14221481,189270748,1636726452,8122094,14287018,183241420,1747352760,8899788,14352555,183765832,1415539388,9686220,14942388,183765744,1526165696,10477021,15007925,183765728,1636792004,11267822,15073462,183503568,1747418312,12045516,15138999,
224660804,1353738992,52428,12582912,223612224,1464365300,843229,12648449,222563648,1574991608,1634030,12713986,233311552,1685617916,2411724,12779523,224922952,1353804512,3198156,13369356,223874536,1464430820,3988957,13434893,222825960,1575057128,4779758,13500430,227806696,1685683436,5557452,13565967,225185100,1353870080,6343884,14155800,224136680,1464496388,7134685,14221337,223088104,1575122696,7925486,14286874,227806696,1685749004,8703180,14352411,225447248,1353935632,9489612,14942244,224398656,1464561940,10280413,15007781,223350080,1575188248,11071214,15073318,233311552,1685814556,11848908,15138855,
227806596,1376021236,117964,12582960,227806580,1486647544,908765,12648497,226757988,1597273852,1699566,12714034,225709396,1707900160,2477260,12779571,233311624,1376086756,3263692,13369404,233311608,1486713064,4054493,13434941,227020136,1597339372,4845294,13500478,225971544,1707965680,5622988,13566015,233311628,1376152324,6409420,14155848,233311612,1486778632,7200221,14221385,227282284,1597404940,7991022,14286922,226233692,1708031248,8768716,14352459,227806608,1376217876,9555148,14942292,227806592,1486844184,10345949,15007829,227544432,1597470492,11136750,15073366,226495840,1708096800,11914444,15138903,
225709396,1398303480,183500,12583008,224660804,1508929788,974301,12648545,223612224,1619556096,1765102,12714082,222563648,1730182404,2542796,12779619,225971544,1398369000,3329228,13369452,224922952,1508995308,4120029,13434989,223874536,1619621616,4910830,13500526,222825960,1730247924,5688524,13566063,226233692,1398434568,6474956,14155896,225185100,1509060876,7265757,14221433,224136680,1619687184,8056558,14286970,223088104,1730313492,8834252,14352507,226495840,1398500120,9620684,14942340,225447248,1509126428,10411485,15007877,224398656,1619752736,11202286,15073414,223350080,1730379044,11979980,15138951,
227806696,1420585724,249036,12583056,227806596,1531212032,1039837,12648593,227806580,1641838340,1830638,12714130,226757988,1752464648,2608332,12779667,233311552,1420651244,3394764,13369500,233311624,1531277552,4185565,13435037,233311608,1641903860,4976366,13500574,227020136,1752530168,5754060,13566111,233311552,1420716812,6540492,14155944,233311628,1531343120,7331293,14221481,233311612,1641969428,8122094,14287018,227282284,1752595736,8899788,14352555,227806696,1420782364,9686220,14942388,227806608,1531408672,10477021,15007925,227806592,1642034980,11267822,15073462,227544432,1752661288,12045516,15138999,
268701668,1358981952,52428,12582912,267653088,1469608260,843229,12648449,266604512,1580234568,1634030,12713986,277352416,1690860876,2411724,12779523,268963816,1359047504,3198156,13369356,267915400,1469673812,3988957,13434893,266866824,1580300120,4779758,13500430,271847560,1690926428,5557452,13565967,269225964,1359113008,6343884,14155800,268177544,1469739316,7134685,14221337,267128968,1580365624,7925486,14286874,271847560,1690991932,8703180,14352411,269488112,1359178528,9489612,14942244,268439520,1469804836,10280413,15007781,267390944,1580431144,11071214,15073318,277352416,1691057452,11848908,15138855,
271847460,1381264196,117964,12582960,271847444,1491890504,908765,12648497,270798852,1602516812,1699566,12714034,269750260,1713143120,2477260,12779571,277352488,1381329748,3263692,13369404,277352472,1491956056,4054493,13434941,271061000,1602582364,4845294,13500478,270012408,1713208672,5622988,13566015,277352492,1381395252,6409420,14155848,277352476,1492021560,7200221,14221385,271323148,1602647868,7991022,14286922,270274556,1713274176,8768716,14352459,271847472,1381460772,9555148,14942292,271847456,1492087080,10345949,15007829,271585296,1602713388,11136750,15073366,270536704,1713339696,11914444,15138903,
269750260,1403546440,183500,12583008,268701668,1514172748,974301,12648545,267653088,1624799056,1765102,12714082,266604512,1735425364,2542796,12779619,270012408,1403611992,3329228,13369452,268963816,1514238300,4120029,13434989,267915400,1624864608,4910830,13500526,266866824,1735490916,5688524,13566063,270274556,1403677496,6474956,14155896,269225964,1514303804,7265757,14221433,268177544,1624930112,8056558,14286970,267128968,1735556420,8834252,14352507,270536704,1403743016,9620684,14942340,269488112,1514369324,10411485,15007877,268439520,1624995632,11202286,15073414,267390944,1735621940,11979980,15138951,
271847560,1425828684,249036,12583056,271847460,1536454992,1039837,12648593,271847444,1647081300,1830638,12714130,270798852,1757707608,2608332,12779667,277352416,1425894236,3394764,13369500,277352488,1536520544,4185565,13435037,277352472,1647146852,4976366,13500574,271061000,1757773160,5754060,13566111,277352416,1425959740,6540492,14155944,277352492,1536586048,7331293,14221481,277352476,1647212356,8122094,14287018,271323148,1757838664,8899788,14352555,271847560,1426025260,9686220,14942388,271847472,1536651568,10477021,15007925,271847456,1647277876,11267822,15073462,271585296,1757904184,12045516,15138999};

DI float bf_lo(u32 v) { return __uint_as_float(v << 16); }
DI u32 pk_bf16(float a, float b) { f32x2 t = {a, b}; return __builtin_bit_cast(u32, __builtin_convertvector(t, bf16x2)); }

DI void slow_path(const u16* __restrict__ x, const u16* __restrict__ dwl, const u16* __restrict__ dws, const u16* __restrict__ w1,
                  const u16* __restrict__ w2, u16* __restrict__ out, float* sm, int t, int tx, int ty0) {
  float* o = sm;
  float* g = sm + 8 * CH;
  float* st = g + 8 * HID;
#pragma unroll 1
  for (int b = 0; b < 128; ++b) {
    const int y = ty0 * 16 + (b >> 1), x0 = tx * 16 + (b & 1) * 8;
    __syncthreads();
#pragma unroll 1
    for (int i = t; i < 8 * CH; i += 256) {
      const int p = i / CH, c = i - p * CH;
      float acc = 0.f;
#pragma unroll 1
      for (int a = 0; a < 5; ++a)
#pragma unroll 1
        for (int e = 0; e < 5; ++e) {
          const int yy = y + a - 2, xc = x0 + p + e - 2;
          if (yy < 0 || yy >= IMG || xc < 0 || xc >= IMG) continue;
          const float xv = bf_lo(x[(size_t)(yy * IMG + xc) * CH + c]);
          acc = __builtin_fmaf(bf_lo(dws[(a * 5 + e) * CH + c]), xv, acc);
          if (a >= 1 && a <= 3 && e >= 1 && e <= 3) acc = __builtin_fmaf(bf_lo(dwl[((a - 1) * 3 + (e - 1)) * CH + c]), xv, acc);
        }
      o[i] = acc;
    }
    __syncthreads();
    if (t < 8) {
      float m = 0.f, v = 0.f;
#pragma unroll 1
      for (int c = 0; c < CH; ++c) m += o[t * CH + c];
      m *= (1.0f / CH);
#pragma unroll 1
      for (int c = 0; c < CH; ++c) { const float d = o[t * CH + c] - m; v = __builtin_fmaf(d, d, v); }
      st[2 * t] = m; st[2 * t + 1] = __builtin_amdgcn_rsqf(v * (1.0f / CH) + 1e-5f);
    }
    __syncthreads();
#pragma unroll 1
    for (int i = t; i < 8 * HID; i += 256) {
      const int p = i / HID, e = i - p * HID;
      const float m = st[2 * p], r = st[2 * p + 1];
      float u = 0.f;
#pragma unroll 1
      for (int c = 0; c < CH; ++c) u = __builtin_fmaf((o[p * CH + c] - m) * r, bf_lo(w1[c * HID + e]), u);
      const float z = u * __builtin_fmaf(u * u, GC3, GC1);
      g[i] = u * __builtin_amdgcn_rcpf(1.0f + __builtin_amdgcn_exp2f(z));
    }
    __syncthreads();
#pragma unroll 1
    for (int i = t; i < 8 * CH; i += 256) {
      const int p = i / CH, c = i - p * CH;
      float acc = 0.f;
#pragma unroll 1
      for (int e = 0; e < HID; ++e) acc = __builtin_fmaf(g[p * HID + e], bf_lo(w2[e * CH + c]), acc);
      const size_t idx = (size_t)(y * IMG + x0 + p) * CH + c;
      out[idx] = (u16)pk_bf16(bf_lo(x[idx]) + acc, 0.f);
    }
  }
}

__global__ __launch_bounds__(256, 1) void replk_kernel(const u16* __restrict__ x, const u16* __restrict__ dwl,
    const u16* __restrict__ dws, const u16* __restrict__ w1, const u16* __restrict__ w2, u16* __restrict__ out
    ) {
  __shared__ __attribute__((aligned(16))) char smem[L_TOTAL];
  __attribute__((address_space(3))) char* smem3 = (__attribute__((address_space(3))) char*)smem;
  const int w = __builtin_amdgcn_readfirstlane(threadIdx.x >> 6);
  u32 tid = threadIdx.x, blk = blockIdx.x;
  u32 flg = (u32)(size_t)smem3 ? 1u : 0u;
  u64 ka = (u64)__builtin_amdgcn_kernarg_segment_ptr();
  asm volatile(
    ".p2align 3\n"
    "s_load_dwordx2 s[40:41], s[98:99], 0x0\n"
    "s_load_dwordx2 s[88:89], s[98:99], 0x10\n"
    "s_load_dwordx2 s[92:93], s[98:99], 0x8\n"
    "s_load_dwordx2 s[84:85], s[98:99], 0x20\n"
    "s_load_dwordx2 s[80:81], s[98:99], 0x18\n"
    "s_load_dwordx2 s[44:45], s[98:99], 0x28\n"
    "v_lshlrev_b32_e32 v213, 4, v208\n"
    "v_lshrrev_b32_e32 v0, 6, v208\n"
    "s_add_u32 vcc_lo, s96, 0\n"
    "s_addc_u32 vcc_hi, s97, 0\n"
    "global_load_dwordx4 v[168:171], v213, vcc\n"
    "s_add_u32 vcc_lo, s96, 4096\n"
    "s_addc_u32 vcc_hi, s97, 0\n"
    "global_load_dwordx4 v[172:175], v213, vcc\n"
    "s_add_u32 vcc_lo, s96, 8192\n"
    "s_addc_u32 vcc_hi, s97, 0\n"
    "global_load_dwordx4 v[176:179], v213, vcc\n"
    "s_getpc_b64 s[76:77]\n"
    ".Lreplk_pc0:\n"
    "s_bfe_u32 s63, s65, 0x30003\n"
    "v_lshrrev_b32_e32 v5, 2, v208\n"
    "s_lshl_b32 s63, s63, 6\n"
    "v_add_lshl_u32 v5, s63, v5, 7\n"
    "v_min_u32_e32 v215, .Lreplk_end-.Lreplk_pc0-4, v5\n"
    "global_load_dword v233, v215, s[76:77]\n"
    "v_readfirstlane_b32 s60, v0\n"
    "v_min_u32_e32 v1, 239, v208\n"
    "v_mul_u32_u24_e32 v2, 1366, v1\n"
    "s_lshr_b32 s61, s65, 3\n"
    "s_and_b32 s62, s65, 7\n"
    "v_lshrrev_b32_e32 v2, 16, v2\n"
    "v_lshlrev_b32_e32 v3, 2, v1\n"
    "s_lshl_b32 s62, s62, 6\n"
    "s_movk_i32 s63, 768\n"
    "v_mad_u32_u24 v211, v2, s63, v3\n"
    "v_mul_u32_u24_e32 v4, 384, v2\n"
    "v_add_u32_e32 v2, -1, v2\n"
    "s_movk_i32 s90, 4800\n"
    "s_mov_b32 s91, 0x20000\n"
    "v_add_u32_e32 v3, -576, v3\n"
    "v_cmp_gt_u32_e32 vcc, 3, v2\n"
    "v_add_u32_e32 v3, v3, v4\n"
    "v_bfrev_b32_e32 v4, 1\n"
    "s_movk_i32 s94, 1728\n"
    "s_mov_b32 s95, 0x20000\n"
    "v_cndmask_b32_e32 v212, v4, v3, vcc\n"
    "s_lshl_b32 s99, s62, 5\n"
    "s_add_u32 s99, s99, s61\n"
    "s_mulk_i32 s99, 3072\n"
    "s_lshr_b32 s51, s60, 1\n"
    "s_mul_i32 s63, s51, 18432\n"
    "s_lshl_b32 s52, s60, 13\n"
    "s_add_u32 s63, s63, s52\n"
    "v_add_u32_e32 v210, s63, v213\n"
    "s_mul_i32 s38, s60, 9216\n"
    "s_add_u32 s38, s38, 40960\n"
    "s_mov_b32 s86, 73728\n"
    "s_mov_b32 s87, 0x20000\n"
    "v_and_b32_e32 v5, 63, v208\n"
    "v_min_u32_e32 v5, 8, v5\n"
    "s_and_b32 s52, s60, 1\n"
    "s_lshl_b32 s53, s61, 1\n"
    "s_add_u32 s52, s52, s53\n"
    "s_mul_i32 s52, s52, 9\n"
    "v_add_lshl_u32 v214, s52, v5, 7\n"
    "s_cmp_eq_u32 s61, 0\n"
    "s_cselect_b32 s52, 24, 0\n"
    "s_cmp_eq_u32 s61, 31\n"
    "s_cselect_b32 s53, 40, s52\n"
    "s_sub_u32 s53, 256, s53\n"
    "v_subrev_u32_e32 v6, s52, v208\n"
    "s_sub_u32 s63, s99, 384\n"
    "v_cmp_gt_u32_e32 vcc, s53, v6\n"
    "v_lshl_add_u32 v7, v1, 4, s63\n"
    "v_bfe_u32 v8, v208, 4, 2\n"
    "v_lshl_or_b32 v8, v0, 3, v8\n"
    "v_and_b32_e32 v9, 0xf0, v213\n"
    "v_cndmask_b32_e32 v208, v4, v7, vcc\n"
    "v_mul_u32_u24_e32 v8, 768, v8\n"
    "s_mov_b32 s42, 50331648\n"
    "s_mov_b32 s43, 0x20000\n"
    "v_or_b32_e32 v209, v8, v9\n"
    "s_mov_b32 s82, 73728\n"
    "s_mov_b32 s83, 0x20000\n"
    "s_cmp_eq_u32 s62, 0\n"
    "s_cselect_b32 s78, 0, s42\n"
    "s_mov_b32 s79, 0x20000\n"
    "s_waitcnt lgkmcnt(0)\n"
    "s_cmp_lt_u32 s60, 2\n"
    "s_nop 0\n"
    "s_cselect_b64 s[70:71], s[80:81], s[84:85]\n"
    "s_sub_u32 s76, s40, 196608\n"
    "s_subb_u32 s77, s41, 0\n"
    "s_mov_b32 s65, 0\n"
    "buffer_load_dwordx4 a[224:227], v208, s[76:79], s65 offen\n"
    "s_mov_b32 s65, 98304\n"
    "buffer_load_dwordx4 a[228:231], v208, s[76:79], s65 offen\n"
    "s_mov_b32 s65, 196608\n"
    "buffer_load_dwordx4 a[232:235], v208, s[40:43], s65 offen\n"
    "s_mov_b32 s65, 294912\n"
    "buffer_load_dwordx4 a[236:239], v208, s[40:43], s65 offen\n"
    "buffer_load_dword v224, v211, s[88:91], 0 offen offset:0\n"
    "buffer_load_dword v225, v211, s[88:91], 0 offen offset:192\n"
    "buffer_load_dword v226, v211, s[88:91], 0 offen offset:384\n"
    "buffer_load_dword v227, v211, s[88:91], 0 offen offset:576\n"
    "buffer_load_dword v228, v211, s[88:91], 0 offen offset:768\n"
    "v_mov_b32_e64 v88, 0\n"
    "v_mov_b32_e32 v240, 0xdddd8888\n"
    "v_mov_b32_e64 v89, 0\n"
    "v_mov_b32_e64 v200, 0\n"
    "v_mov_b32_e64 v90, 0\n"
    "v_mov_b32_e64 v91, 0\n"
    "v_mov_b32_e64 v59, 0\n"
    "v_mov_b32_e64 v60, 0\n"
    "v_mov_b32_e64 v67, 0\n"
    "v_mov_b32_e64 v68, 0\n"
    "v_mov_b32_e64 v75, 0\n"
    "v_mov_b32_e64 v76, 0\n"
    "v_mov_b32_e64 v83, 0\n"
    "v_mov_b32_e64 v84, 0\n"
    "v_mov_b32_e32 v36, 0x75089ba4\n"
    "v_mov_b32_e32 v38, 0xbdd2d3e8\n"
    "v_mov_b32_e32 v32, 0xc0135761\n"
    "v_lshlrev_b32_e64 v34, 16, v213\n"
    "v_lshlrev_b32_e64 v234, 1, v213\n"
    "v_or_b32_e32 v0, 0x8000, v34\n"
    "v_or_b32_e32 v1, 0x18000, v34\n"
    "v_or_b32_e32 v2, 0x28000, v34\n"
    "v_or_b32_e32 v3, 0x38000, v34\n"
    "v_or_b32_e32 v4, 0x48000, v34\n"
    "v_or_b32_e32 v5, 0x58000, v34\n"
    "v_or_b32_e32 v6, 0x68000, v34\n"
    "v_or_b32_e32 v7, 0x78000, v34\n"
    "v_cmp_ne_u32_e64 s[72:73], 0, v34\n"
    "s_nop 0\n"
    "s_nop 1\n"
    "v_cndmask_b32_e64 v0, 0, v0, s[72:73]\n"
    "v_pk_mul_f32 v[0:1], v[0:1], v[36:37] op_sel_hi:[1,0]\n"
    "v_pk_mul_f32 v[2:3], v[2:3], v[36:37] op_sel_hi:[1,0]\n"
    "v_pk_mul_f32 v[4:5], v[4:5], v[36:37] op_sel_hi:[1,0]\n"
    "v_pk_mul_f32 v[6:7], v[6:7], v[36:37] op_sel_hi:[1,0]\n"
    "v_pk_mul_f32 v[8:9], v[0:1], v[0:1]\n"
    "v_pk_mul_f32 v[10:11], v[2:3], v[2:3]\n"
    "v_pk_mul_f32 v[12:13], v[4:5], v[4:5]\n"
    "v_pk_mul_f32 v[14:15], v[6:7], v[6:7]\n"
    "v_pk_fma_f32 v[8:9], v[8:9], v[38:39], v[32:33] op_sel_hi:[1,0,0]\n"
    "v_pk_fma_f32 v[10:11], v[10:11], v[38:39], v[32:33] op_sel_hi:[1,0,0]\n"
    "v_pk_fma_f32 v[12:13], v[12:13], v[38:39], v[32:33] op_sel_hi:[1,0,0]\n"
    "v_pk_fma_f32 v[14:15], v[14:15], v[38:39], v[32:33] op_sel_hi:[1,0,0]\n"
    "v_pk_mul_f32 v[8:9], v[8:9], v[0:1]\n"
    "v_pk_mul_f32 v[10:11], v[10:11], v[2:3]\n"
    "v_pk_mul_f32 v[12:13], v[12:13], v[4:5]\n"
    "v_pk_mul_f32 v[14:15], v[14:15], v[6:7]\n"
    "v_exp_f32_e64 v8, v8\n"
    "v_exp_f32_e64 v9, v9\n"
    "v_exp_f32_e64 v10, v10\n"
    "v_exp_f32_e64 v11, v11\n"
    "v_exp_f32_e64 v12, v12\n"
    "v_exp_f32_e64 v13, v13\n"
    "v_exp_f32_e64 v14, v14\n"
    "v_exp_f32_e64 v15, v15\n"
    "v_pk_add_f32 v[16:17], v[8:9], 1.0 op_sel_hi:[1,0]\n"
    "v_pk_add_f32 v[18:19], v[10:11], 1.0 op_sel_hi:[1,0]\n"
    "v_pk_add_f32 v[20:21], v[12:13], 1.0 op_sel_hi:[1,0]\n"
    "v_pk_add_f32 v[22:23], v[14:15], 1.0 op_sel_hi:[1,0]\n"
    "v_rcp_f32_e64 v16, v16\n"
    "v_rcp_f32_e64 v17, v17\n"
    "v_rcp_f32_e64 v18, v18\n"
    "v_rcp_f32_e64 v19, v19\n"
    "v_rcp_f32_e64 v20, v20\n"
    "v_rcp_f32_e64 v21, v21\n"
    "v_rcp_f32_e64 v22, v22\n"
    "v_rcp_f32_e64 v23, v23\n"
    "v_pk_mul_f32 v[16:17], v[16:17], v[0:1]\n"
    "v_pk_mul_f32 v[18:19], v[18:19], v[2:3]\n"
    "v_pk_mul_f32 v[20:21], v[20:21], v[4:5]\n"
    "v_pk_mul_f32 v[22:23], v[22:23], v[6:7]\n"
    "v_pk_mul_f32 v[8:9], v[8:9], v[16:17]\n"
    "v_pk_mul_f32 v[10:11], v[10:11], v[18:19]\n"
    "v_pk_mul_f32 v[12:13], v[12:13], v[20:21]\n"
    "v_pk_mul_f32 v[14:15], v[14:15], v[22:23]\n"
    "v_cvt_pk_bf16_f32 v188, v16, v17\n"
    "v_cvt_pk_bf16_f32 v189, v18, v19\n"
    "v_cvt_pk_bf16_f32 v190, v20, v21\n"
    "v_cvt_pk_bf16_f32 v191, v22, v23\n"
    "v_cvt_pk_bf16_f32 v24, -v8, -v9\n"
    "v_cvt_pk_bf16_f32 v25, -v10, -v11\n"
    "v_cvt_pk_bf16_f32 v26, -v12, -v13\n"
    "v_cvt_pk_bf16_f32 v27, -v14, -v15\n"
    "ds_write_b128 v234, v[24:27] offset:0\n"
    "v_or_b32_e32 v0, 0x88000, v34\n"
    "v_or_b32_e32 v1, 0x98000, v34\n"
    "v_or_b32_e32 v2, 0xa8000, v34\n"
    "v_or_b32_e32 v3, 0xb8000, v34\n"
    "v_or_b32_e32 v4, 0xc8000, v34\n"
    "v_or_b32_e32 v5, 0xd8000, v34\n"
    "v_or_b32_e32 v6, 0xe8000, v34\n"
    "v_or_b32_e32 v7, 0xf8000, v34\n"
    "v_pk_mul_f32 v[0:1], v[0:1], v[36:37] op_sel_hi:[1,0]\n"
    "v_pk_mul_f32 v[2:3], v[2:3], v[36:37] op_sel_hi:[1,0]\n"
    "v_pk_mul_f32 v[4:5], v[4:5], v[36:37] op_sel_hi:[1,0]\n"
    "v_pk_mul_f32 v[6:7], v[6:7], v[36:37] op_sel_hi:[1,0]\n"
    "v_pk_mul_f32 v[8:9], v[0:1], v[0:1]\n"
    "v_pk_mul_f32 v[10:11], v[2:3], v[2:3]\n"
    "v_pk_mul_f32 v[12:13], v[4:5], v[4:5]\n"
    "v_pk_mul_f32 v[14:15], v[6:7], v[6:7]\n"
    "v_pk_fma_f32 v[8:9], v[8:9], v[38:39], v[32:33] op_sel_hi:[1,0,0]\n"
    "v_pk_fma_f32 v[10:11], v[10:11], v[38:39], v[32:33] op_sel_hi:[1,0,0]\n"
    "v_pk_fma_f32 v[12:13], v[12:13], v[38:39], v[32:33] op_sel_hi:[1,0,0]\n"
    "v_pk_fma_f32 v[14:15], v[14:15], v[38:39], v[32:33] op_sel_hi:[1,0,0]\n"
    "v_pk_mul_f32 v[8:9], v[8:9], v[0:1]\n"
    "v_pk_mul_f32 v[10:11], v[10:11], v[2:3]\n"
    "v_pk_mul_f32 v[12:13], v[12:13], v[4:5]\n"
    "v_pk_mul_f32 v[14:15], v[14:15], v[6:7]\n"
    "v_exp_f32_e64 v8, v8\n"
    "v_exp_f32_e64 v9, v9\n"
    "v_exp_f32_e64 v10, v10\n"
    "v_exp_f32_e64 v11, v11\n"
    "v_exp_f32_e64 v12, v12\n"
    "v_exp_f32_e64 v13, v13\n"
    "v_exp_f32_e64 v14, v14\n"
    "v_exp_f32_e64 v15, v15\n"
    "v_pk_add_f32 v[16:17], v[8:9], 1.0 op_sel_hi:[1,0]\n"
    "v_pk_add_f32 v[18:19], v[10:11], 1.0 op_sel_hi:[1,0]\n"
    "v_pk_add_f32 v[20:21], v[12:13], 1.0 op_sel_hi:[1,0]\n"
    "v_pk_add_f32 v[22:23], v[14:15], 1.0 op_sel_hi:[1,0]\n"
    "v_rcp_f32_e64 v16, v16\n"
    "v_rcp_f32_e64 v17, v17\n"
    "v_rcp_f32_e64 v18, v18\n"
    "v_rcp_f32_e64 v19, v19\n"
    "v_rcp_f32_e64 v20, v20\n"
    "v_rcp_f32_e64 v21, v21\n"
    "v_rcp_f32_e64 v22, v22\n"
    "v_rcp_f32_e64 v23, v23\n"
    "v_pk_mul_f32 v[16:17], v[16:17], v[0:1]\n"
    "v_pk_mul_f32 v[18:19], v[18:19], v[2:3]\n"
    "v_pk_mul_f32 v[20:21], v[20:21], v[4:5]\n"
    "v_pk_mul_f32 v[22:23], v[22:23], v[6:7]\n"
    "v_pk_mul_f32 v[8:9], v[8:9], v[16:17]\n"
    "v_pk_mul_f32 v[10:11], v[10:11], v[18:19]\n"
    "v_pk_mul_f32 v[12:13], v[12:13], v[20:21]\n"
    "v_pk_mul_f32 v[14:15], v[14:15], v[22:23]\n"
    "v_cvt_pk_bf16_f32 v192, v16, v17\n"
    "v_cvt_pk_bf16_f32 v193, v18, v19\n"
    "v_cvt_pk_bf16_f32 v194, v20, v21\n"
    "v_cvt_pk_bf16_f32 v195, v22, v23\n"
    "v_cvt_pk_bf16_f32 v24, -v8, -v9\n"
    "v_cvt_pk_bf16_f32 v25, -v10, -v11\n"
    "v_cvt_pk_bf16_f32 v26, -v12, -v13\n"
    "v_cvt_pk_bf16_f32 v27, -v14, -v15\n"
    "ds_write_b128 v234, v[24:27] offset:16\n"
    "v_add_u32_e32 v234, 65536, v234\n"
    "buffer_load_dword v229, v212, s[92:95], 0 offen offset:0\n"
    "s_nop 0\n"
    "s_waitcnt vmcnt(13)\n"
    "v_lshlrev_b32_sdwa v216, 2, v168 dst_sel:DWORD dst_unused:UNUSED_PAD src0_sel:DWORD src1_sel:WORD_0\n"
    "v_lshlrev_b32_sdwa v217, 2, v168 dst_sel:DWORD dst_unused:UNUSED_PAD src0_sel:DWORD src1_sel:WORD_1\n"
    "v_lshlrev_b32_sdwa v218, 2, v169 dst_sel:DWORD dst_unused:UNUSED_PAD src0_sel:DWORD src1_sel:WORD_0\n"
    "v_lshlrev_b32_sdwa v219, 2, v169 dst_sel:DWORD dst_unused:UNUSED_PAD src0_sel:DWORD src1_sel:WORD_1\n"
    "v_lshlrev_b32_sdwa v220, 2, v170 dst_sel:DWORD dst_unused:UNUSED_PAD src0_sel:DWORD src1_sel:WORD_0\n"
    "v_lshlrev_b32_sdwa v221, 2, v170 dst_sel:DWORD dst_unused:UNUSED_PAD src0_sel:DWORD src1_sel:WORD_1\n"
    "v_lshlrev_b32_sdwa v222, 2, v171 dst_sel:DWORD dst_unused:UNUSED_PAD src0_sel:DWORD src1_sel:WORD_0\n"
    "v_lshlrev_b32_sdwa v223, 2, v171 dst_sel:DWORD dst_unused:UNUSED_PAD src0_sel:DWORD src1_sel:WORD_1\n"
    "ds_write_b128 v222, v[88:91]\n"
    "buffer_load_dword v230, v212, s[92:95], 0 offen offset:192\n"
    "s_nop 0\n"
    "s_waitcnt vmcnt(13)\n"
    "v_lshlrev_b32_sdwa v207, 2, v172 dst_sel:DWORD dst_unused:UNUSED_PAD src0_sel:DWORD src1_sel:WORD_0\n"
    "v_lshlrev_b32_sdwa v242, 2, v173 dst_sel:DWORD dst_unused:UNUSED_PAD src0_sel:DWORD src1_sel:WORD_0\n"
    "v_lshlrev_b32_sdwa v243, 2, v173 dst_sel:DWORD dst_unused:UNUSED_PAD src0_sel:DWORD src1_sel:WORD_1\n"
    "v_lshlrev_b32_sdwa v244, 2, v174 dst_sel:DWORD dst_unused:UNUSED_PAD src0_sel:DWORD src1_sel:WORD_0\n"
    "v_lshlrev_b32_sdwa v248, 2, v174 dst_sel:DWORD dst_unused:UNUSED_PAD src0_sel:DWORD src1_sel:WORD_1\n"
    "v_lshlrev_b32_sdwa v249, 2, v175 dst_sel:DWORD dst_unused:UNUSED_PAD src0_sel:DWORD src1_sel:WORD_0\n"
    "v_lshlrev_b32_sdwa v250, 2, v175 dst_sel:DWORD dst_unused:UNUSED_PAD src0_sel:DWORD src1_sel:WORD_1\n"
    "v_bfe_i32 v201, v172, 16, 1\n"
    "v_and_b32_e32 v201, 0x35003500, v201\n"
    "v_bfe_i32 v203, v172, 18, 1\n"
    "v_and_b32_e32 v203, 0x35003500, v203\n"
    "v_bfe_i32 v205, v172, 20, 1\n"
    "v_and_b32_e32 v205, 0x35003500, v205\n"
    "v_bfe_i32 v92, v172, 26, 1\n"
    "v_xor_b32_e32 v92, 0xffff, v92\n"
    "v_and_b32_e32 v92, 0x3f803f80, v92\n"
    "v_bfe_i32 v236, v172, 22, 1\n"
    "v_and_b32_e64 v236, v92, v236\n"
    "v_bfe_i32 v237, v172, 23, 1\n"
    "v_and_b32_e32 v237, v92, v237\n"
    "buffer_load_dword v231, v212, s[92:95], 0 offen offset:384\n"
    "s_waitcnt vmcnt(13)\n"
    "v_lshlrev_b32_sdwa v251, 2, v176 dst_sel:DWORD dst_unused:UNUSED_PAD src0_sel:DWORD src1_sel:WORD_0\n"
    "v_lshlrev_b32_sdwa v252, 2, v176 dst_sel:DWORD dst_unused:UNUSED_PAD src0_sel:DWORD src1_sel:WORD_1\n"
    "v_lshlrev_b32_sdwa v253, 2, v177 dst_sel:DWORD dst_unused:UNUSED_PAD src0_sel:DWORD src1_sel:WORD_0\n"
    "v_lshlrev_b32_sdwa v254, 2, v177 dst_sel:DWORD dst_unused:UNUSED_PAD src0_sel:DWORD src1_sel:WORD_1\n"
    "v_lshlrev_b32_sdwa v241, 0, v178 dst_sel:DWORD dst_unused:UNUSED_PAD src0_sel:DWORD src1_sel:WORD_0\n"
    "v_lshlrev_b32_sdwa v245, 4, v178 dst_sel:DWORD dst_unused:UNUSED_PAD src0_sel:DWORD src1_sel:WORD_1\n"
    "v_lshlrev_b32_sdwa v255, 4, v179 dst_sel:DWORD dst_unused:UNUSED_PAD src0_sel:DWORD src1_sel:WORD_0\n"
    "v_lshlrev_b32_sdwa v247, 4, v179 dst_sel:DWORD dst_unused:UNUSED_PAD src0_sel:DWORD src1_sel:WORD_1\n"
    "v_bfe_i32 v238, v172, 24, 1\n"
    "v_and_b32_e64 v238, v92, v238\n"
    "v_bfe_i32 v239, v172, 25, 1\n"
    "v_and_b32_e64 v239, v92, v239\n"
    "v_bfe_u32 v93, v172, 28, 2\n"
    "v_mul_u32_u24_e32 v93, 393216, v93\n"
    "v_mov_b32_e64 v222, v254\n"
    "v_mov_b32_e32 v254, 0\n"
    "s_nop 1\n"
    "v_mfma_f32_32x32x1_2b_f32 v[0:31], v254, v254, 0\n"
    "global_load_dword v233, v214, s[70:71]\n"
    "s_mov_b32 s48, 0x44ea44ea\n"
    "s_mov_b32 s49, 0xb77fb77f\n"
    "s_mov_b32 s50, 0x89efde6e\n"
    "s_mov_b32 s46, s42\n"
    "s_mov_b32 s47, s43\n"
    "s_mul_i32 s54, s60, 592\n"
    "s_add_u32 s54, s54, -27008\n"
    "s_mul_i32 s55, s60, -1284\n"
    "s_add_u32 s55, s55, 54688\n"
    "s_mul_i32 s56, s60, -6160\n"
    "s_add_u32 s56, s56, -27008\n"
    "s_add_u32 s98, s99, 1375872\n"
    "s_and_b32 s36, s60, 1\n"
    "s_lshl_b32 s36, s36, 13\n"
    "s_add_u32 s36, s36, 327680\n"
    "s_mul_i32 s53, s60, 98304\n"
    "s_add_u32 s98, s53, s98\n"
    "s_xor_b32 s53, s60, 1\n"
    "s_mul_i32 s53, s53, 393216\n"
    "s_add_u32 s99, s99, s53\n"
    "s_add_u32 s53, s98, 393216\n"
    "s_sub_u32 s53, s53, s36\n"
    "s_and_b32 s37, s61, 1\n"
    "s_lshl_b32 s37, s37, 2\n"
    "s_add_u32 s37, s37, s60\n"
    "s_lshl_b32 s37, s37, 12\n"
    "s_cmp_eq_u32 s61, 0\n"
    "s_cselect_b32 s67, 2, 0\n"
    "s_cmp_eq_u32 s61, 31\n"
    "s_cselect_b32 s67, 1, s67\n"
    "s_lshl_b32 s67, s67, 30\n"
    "s_mov_b64 s[68:69], s[44:45]\n"
    "s_mov_b32 s70, 0\n"
    "s_mov_b32 s71, s47\n"
    "s_mov_b64 s[72:73], s[40:41]\n"
    "s_mov_b64 s[74:75], s[42:43]\n"
    "s_mov_b32 s57, 0\n"
    "s_mov_b32 s58, 196608\n"
    "s_mov_b32 s60, 0\n"
    "s_mov_b32 s61, 393216\n"
    "s_mov_b32 s62, 786432\n"
    "s_mov_b32 s63, 1179648\n"
    "s_mov_b32 s52, 1572864\n"
    "s_mov_b32 s88, 98304\n"
    "s_mov_b32 s89, 294912\n"
    "s_mov_b32 s39, 0\n"
    "s_getpc_b64 s[76:77]\n"
    ".Lreplk_pc1:\n"
    "s_add_u32 s76, s76, .Lreplk_last-.Lreplk_pc1\n"
    "s_addc_u32 s77, s77, 0\n"
    "s_sub_u32 s76, s76, s36\n"
    "s_subb_u32 s77, s77, 0\n"
    "s_add_u32 s78, s36, .Lreplk_end-.Lreplk_last\n"
    "s_mov_b32 s79, s37\n"
    "s_add_u32 m0, s38, 0\n"
    "s_mov_b32 s64, 0\n"
    "buffer_load_dwordx4 v210, s[84:87], s64 offen offset:0 lds\n"
    "s_waitcnt vmcnt(13)\n"
    "ds_write2_b32 v216, a224, a225 offset0:0 offset1:20\n"
    "v_add3_u32 v247, v247, v93, s98\n"
    "v_add_u32_e32 v246, s98, v255\n"
    "v_add_u32_e64 v245, s99, v245\n"
    "v_add_u32_e64 v255, s99, v255\n"
    "ds_write2_b32 v216, a226, a227 offset0:40 offset1:60\n"
    "v_and_b32_e64 v93, s67, v172\n"
    "v_and_b32_e32 v94, 0x80000000, v93\n"
    "s_waitcnt vmcnt(12)\n"
    "ds_write2_b32 v216, a228, a229 offset0:80 offset1:100\n"
    "v_and_b32_e32 v93, 0x40000000, v93\n"
    "v_or_b32_e64 v246, v246, v94\n"
    "v_or_b32_e64 v247, v247, v93\n"
    "ds_write2_b32 v216, a230, a231 offset0:120 offset1:140\n"
    "v_add_u32_e32 v216, 1360, v216\n"
    "buffer_load_dwordx4 v210, s[84:87], s64 offen offset:1024 lds\n"
    "s_waitcnt vmcnt(12)\n"
    "ds_write2_b32 v216, a232, a233 offset0:0 offset1:20\n"
    "ds_write2_b32 v216, a234, a235 offset0:40 offset1:60\n"
    "buffer_load_dwordx4 v210, s[84:87], s64 offen offset:2048 lds\n"
    "s_waitcnt vmcnt(12)\n"
    "ds_write2_b32 v216, a236, a237 offset0:80 offset1:100\n"
    "s_add_u32 m0, s38, 3072\n"
    "s_mov_b32 s64, 3072\n"
    "buffer_load_dwordx4 v210, s[84:87], s64 offen offset:0 lds\n"
    "buffer_load_dwordx4 v210, s[84:87], s64 offen offset:1024 lds\n"
    "ds_write2_b32 v216, a238, a239 offset0:120 offset1:140\n"
    "buffer_load_dwordx4 v210, s[84:87], s64 offen offset:2048 lds\n"
    "s_add_u32 m0, s38, 6144\n"
    "s_mov_b32 s64, 6144\n"
    "buffer_load_dwordx4 v210, s[84:87], s64 offen offset:0 lds\n"
    "buffer_load_dwordx4 v210, s[84:87], s64 offen offset:1024 lds\n"
    "v_add_u32_e32 v216, 1360, v216\n"
    "s_waitcnt vmcnt(16)\n"
    "v_lshlrev_b32_e32 v40, 16, v224\n"
    "buffer_load_dwordx4 v210, s[84:87], s64 offen offset:2048 lds\n"
    "s_mov_b32 s65, 589824\n"
    "buffer_load_dwordx4 a[240:243], v208, s[40:43], s65 offen\n"
    "s_waitcnt vmcnt(17)\n"
    "v_lshlrev_b32_e32 v41, 16, v225\n"
    "s_mov_b32 s65, 688128\n"
    "buffer_load_dwordx4 a[244:247], v208, s[40:43], s65 offen\n"
    "s_mov_b32 s59, 49152\n"
    "buffer_load_dwordx4 v[96:99], v209, s[80:83], s59 offen offset:0\n"
    "s_waitcnt vmcnt(18)\n"
    "v_lshlrev_b32_e32 v42, 16, v226\n"
    "s_waitcnt vmcnt(17)\n"
    "v_lshlrev_b32_e32 v43, 16, v227\n"
    "s_waitcnt vmcnt(16)\n"
    "v_lshlrev_b32_e32 v44, 16, v228\n"
    "s_waitcnt vmcnt(15)\n"
    "v_lshlrev_b32_e32 v45, 16, v229\n"
    "s_waitcnt vmcnt(14)\n"
    "v_lshlrev_b32_e32 v46, 16, v230\n"
    "s_waitcnt vmcnt(13)\n"
    "v_lshlrev_b32_e32 v47, 16, v231\n"
    "v_add_f32_e64 v41, v41, v45\n"
    "v_add_f32_e64 v42, v42, v46\n"
    "v_add_f32_e64 v43, v43, v47\n"
    "v_cvt_pk_bf16_f32 v56, v40, v41\n"
    "v_cvt_pk_bf16_f32 v57, v42, v43\n"
    "v_cvt_pk_bf16_f32 v58, v44, 0\n"
    "v_cvt_pk_bf16_f32 v64, 0, v40\n"
    "buffer_load_dwordx4 v[100:103], v209, s[80:83], s59 offen offset:256\n"
    "ds_write_b128 v221, v[56:59] offset:0\n"
    "v_cvt_pk_bf16_f32 v65, v41, v42\n"
    "v_cvt_pk_bf16_f32 v66, v43, v44\n"
    "v_cvt_pk_bf16_f32 v61, v40, v41\n"
    "v_cvt_pk_bf16_f32 v62, v42, v43\n"
    "v_cvt_pk_bf16_f32 v63, v44, 0\n"
    "v_cvt_pk_bf16_f32 v69, 0, v40\n"
    "v_cvt_pk_bf16_f32 v70, v41, v42\n"
    "v_cvt_pk_bf16_f32 v71, v43, v44\n"
    "ds_write_b128 v221, v[64:67] offset:16\n"
    "buffer_load_dwordx4 v[104:107], v209, s[80:83], s59 offen offset:512\n"
    "v_and_b32_e32 v48, 0xffff0000, v224\n"
    "v_and_b32_e32 v49, 0xffff0000, v225\n"
    "v_and_b32_e32 v50, 0xffff0000, v226\n"
    "v_and_b32_e32 v51, 0xffff0000, v227\n"
    "v_and_b32_e32 v52, 0xffff0000, v228\n"
    "ds_write_b128 v221, v[60:63] offset:32\n"
    "v_and_b32_e32 v53, 0xffff0000, v229\n"
    "v_and_b32_e32 v54, 0xffff0000, v230\n"
    "v_and_b32_e32 v55, 0xffff0000, v231\n"
    "v_add_f32_e64 v49, v49, v53\n"
    "v_add_f32_e64 v50, v50, v54\n"
    "v_add_f32_e64 v51, v51, v55\n"
    "ds_write_b128 v221, v[68:71] offset:48\n"
    "s_mov_b32 s65, 983040\n"
    "buffer_load_dwordx4 a[248:251], v208, s[40:43], s65 offen\n"
    "v_cvt_pk_bf16_f32 v72, v48, v49\n"
    "v_cvt_pk_bf16_f32 v73, v50, v51\n"
    "v_cvt_pk_bf16_f32 v74, v52, 0\n"
    "v_cvt_pk_bf16_f32 v80, 0, v48\n"
    "v_cvt_pk_bf16_f32 v81, v49, v50\n"
    "v_cvt_pk_bf16_f32 v82, v51, v52\n"
    "v_cvt_pk_bf16_f32 v77, v48, v49\n"
    "v_cvt_pk_bf16_f32 v78, v50, v51\n"
    "v_cvt_pk_bf16_f32 v79, v52, 0\n"
    "v_cvt_pk_bf16_f32 v85, 0, v48\n"
    "v_cvt_pk_bf16_f32 v86, v49, v50\n"
    "v_cvt_pk_bf16_f32 v87, v51, v52\n"
    "ds_write_b128 v221, v[72:75] offset:336\n"
    "s_mov_b32 s65, 1081344\n"
    "buffer_load_dwordx4 a[252:255], v208, s[40:43], s65 offen\n"
    "v_xor_b32_e32 v224, 0, v219\n"
    "v_xor_b32_e32 v225, 32, v219\n"
    "v_xor_b32_e32 v226, 64, v219\n"
    "v_xor_b32_e32 v227, 96, v219\n"
    "ds_write_b128 v221, v[80:83] offset:352\n"
    "v_xor_b32_e32 v228, 0, v223\n"
    "v_xor_b32_e32 v229, 32, v223\n"
    "s_mov_b32 s59, 52224\n"
    "buffer_load_dwordx4 v[108:111], v209, s[80:83], s59 offen offset:0\n"
    "v_xor_b32_e32 v230, 64, v223\n"
    "v_xor_b32_e32 v231, 96, v223\n"
    "ds_write_b128 v221, v[76:79] offset:368\n"
    "v_lshl_add_u32 v196, s51, 8, v225\n"
    "v_lshl_add_u32 v197, s51, 8, v227\n"
    "v_mfma_f32_32x32x1_2b_f32 v[32:63], v254, v254, 0\n"
    "ds_write_b128 v221, v[84:87] offset:384\n"
    "buffer_load_dwordx4 v[112:115], v209, s[80:83], s59 offen offset:256\n"
    "v_mfma_f32_32x32x1_2b_f32 v[64:95], v254, v254, 0\n"
    "v_add_u32_e64 v254, s54, v242\n"
    "buffer_load_dwordx4 v[116:119], v209, s[80:83], s59 offen offset:512\n"
    "s_mov_b32 s65, 1376256\n"
    "buffer_load_dwordx4 a[176:179], v208, s[40:43], s65 offen\n"
    "s_mov_b32 s65, 1474560\n"
    "buffer_load_dwordx4 a[180:183], v208, s[40:43], s65 offen\n"
    "s_waitcnt vmcnt(12)\n"
    "s_barrier\n"
    "ds_read_b64_tr_b16 a[0:1], v220 offset:0\n"
    "ds_read_b64_tr_b16 a[2:3], v220 offset:3072\n"
    "ds_read_b64_tr_b16 a[4:5], v220 offset:8\n"
    "ds_read_b64_tr_b16 a[6:7], v220 offset:3080\n"
    "ds_read_b64_tr_b16 a[8:9], v220 offset:64\n"
    "ds_read_b64_tr_b16 a[10:11], v220 offset:3136\n"
    "ds_read_b64_tr_b16 a[12:13], v220 offset:72\n"
    "ds_read_b64_tr_b16 a[14:15], v220 offset:3144\n"
    "ds_read_b64_tr_b16 v[168:169], v218 offset:128\n"
    "ds_read_b64_tr_b16 v[170:171], v218 offset:3200\n"
    "ds_read_b64_tr_b16 a[16:17], v220 offset:6144\n"
    "ds_read_b64_tr_b16 a[18:19], v220 offset:9216\n"
    "ds_read_b64_tr_b16 a[20:21], v220 offset:6152\n"
    "ds_read_b64_tr_b16 a[22:23], v220 offset:9224\n"
    "ds_read_b64_tr_b16 a[24:25], v220 offset:6208\n"
    "ds_read_b64_tr_b16 a[26:27], v220 offset:9280\n"
    "ds_read_b64_tr_b16 a[28:29], v220 offset:6216\n"
    "ds_read_b64_tr_b16 a[30:31], v220 offset:9288\n"
    "ds_read_b64_tr_b16 v[172:173], v218 offset:6272\n"
    "s_nop 0\n"
    "s_mov_b32 s59, 0\n"
    "buffer_load_dwordx4 v[120:123], v209, s[80:83], s59 offen offset:0\n"
    "ds_read_b64_tr_b16 v[174:175], v218 offset:9344\n"
    "ds_read_b64_tr_b16 a[32:33], v220 offset:12288\n"
    "ds_read_b64_tr_b16 a[34:35], v220 offset:15360\n"
    "ds_read_b64_tr_b16 a[36:37], v220 offset:12296\n"
    "ds_read_b64_tr_b16 a[38:39], v220 offset:15368\n"
    "ds_read_b64_tr_b16 a[40:41], v220 offset:12352\n"
    "ds_read_b64_tr_b16 a[42:43], v220 offset:15424\n"
    "ds_read_b64_tr_b16 a[44:45], v220 offset:12360\n"
    "ds_read_b64_tr_b16 a[46:47], v220 offset:15432\n"
    "ds_read_b64_tr_b16 v[176:177], v218 offset:12416\n"
    "ds_read_b64_tr_b16 v[178:179], v218 offset:15488\n"
    "s_waitcnt vmcnt(12)\n"
    "ds_write2_b32 v216, a240, a241 offset0:0 offset1:20\n"
    "ds_write2_b32 v216, a242, a243 offset0:40 offset1:60\n"
    "buffer_load_dwordx4 v[124:127], v209, s[80:83], s59 offen offset:256\n"
    "s_waitcnt vmcnt(12)\n"
    "ds_write2_b32 v216, a244, a245 offset0:80 offset1:100\n"
    "s_waitcnt lgkmcnt(2)\n"
    "s_barrier\n"
    "ds_write2_b32 v216, a246, a247 offset0:120 offset1:140\n"
    "buffer_load_dwordx4 v[128:131], v209, s[80:83], s59 offen offset:512\n"
    "v_add_u32_e32 v216, 1360, v216\n"
    "s_waitcnt vmcnt(12)\n"
    "v_or3_b32 v200, v200, v96, v97\n"
    "v_or3_b32 v200, v200, v98, v99\n"
    "v_and_b32_e32 v232, 0x7fff7fff, v96\n"
    "v_add_u32_e32 v96, v201, v96\n"
    "v_add_u32_e64 v98, v201, v98\n"
    "ds_write_b128 v217, v[96:99] offset:24576\n"
    "s_mov_b32 s65, 0\n"
    "buffer_load_dwordx4 a[184:187], v208, s[40:43], s65 offen\n"
    "s_waitcnt vmcnt(12)\n"
    "v_or3_b32 v200, v200, v100, v101\n"
    "v_or3_b32 v200, v200, v102, v103\n"
    "v_add_u32_e64 v100, v203, v100\n"
    "v_add_u32_e64 v102, v203, v102\n"
    "ds_write_b128 v217, v[100:103] offset:24832\n"
    "s_mov_b32 s65, 98304\n"
    "buffer_load_dwordx4 a[188:191], v208, s[40:43], s65 offen\n"
    "s_waitcnt vmcnt(12)\n"
    "v_or3_b32 v200, v200, v104, v105\n"
    "v_or3_b32 v200, v200, v106, v107\n"
    "v_add_u32_e32 v104, v205, v104\n"
    "v_add_u32_e64 v106, v205, v106\n"
    "ds_write_b128 v217, v[104:107] offset:25088\n"
    "s_waitcnt lgkmcnt(0)\n"
    "s_barrier\n"
    "s_mov_b32 s59, 3072\n"
    "buffer_load_dwordx4 v[132:135], v209, s[80:83], s59 offen offset:0\n"
    "ds_read_b64_tr_b16 a[48:49], v224 offset:24576\n"
    "ds_read_b64_tr_b16 v[180:181], v196 offset:24576\n"
    "ds_read_b64_tr_b16 a[52:53], v226 offset:24576\n"
    "ds_read_b64_tr_b16 v[184:185], v197 offset:24576\n"
    "ds_read_b64_tr_b16 a[56:57], v228 offset:24576\n"
    "ds_read_b64_tr_b16 a[60:61], v229 offset:24576\n"
    "ds_read_b64_tr_b16 a[64:65], v230 offset:24576\n"
    "ds_read_b64_tr_b16 a[68:69], v231 offset:24576\n"
    "ds_read_b64_tr_b16 a[72:73], v224 offset:24832\n"
    "ds_read_b64_tr_b16 a[76:77], v226 offset:24832\n"
    "s_waitcnt vmcnt(12)\n"
    "ds_write2_b32 v216, a248, a249 offset0:0 offset1:20\n"
    "ds_write2_b32 v216, a250, a251 offset0:40 offset1:60\n"
    "buffer_load_dwordx4 v[136:139], v209, s[80:83], s59 offen offset:256\n"
    "s_waitcnt vmcnt(12)\n"
    "ds_write2_b32 v216, a252, a253 offset0:80 offset1:100\n"
    "ds_write2_b32 v216, a254, a255 offset0:120 offset1:140\n"
    "v_add_u32_e32 v216, 1360, v216\n"
    "buffer_load_dwordx4 v[140:143], v209, s[80:83], s59 offen offset:512\n"
    "s_waitcnt vmcnt(12)\n"
    "v_or3_b32 v200, v200, v108, v109\n"
    "v_or3_b32 v200, v200, v110, v111\n"
    "v_add_u32_e32 v108, v201, v108\n"
    "v_add_u32_e64 v110, v201, v110\n"
    "ds_write_b128 v217, v[108:111] offset:0\n"
    "s_mov_b32 s65, 393216\n"
    "buffer_load_dwordx4 a[192:195], v208, s[40:43], s65 offen\n"
    "s_waitcnt vmcnt(12)\n"
    "v_or3_b32 v200, v200, v112, v113\n"
    "v_or3_b32 v200, v200, v114, v115\n"
    "v_add_u32_e32 v112, v203, v112\n"
    "v_add_u32_e64 v114, v203, v114\n"
    "ds_write_b128 v217, v[112:115] offset:256\n"
    "v_mov_b32_e64 v112, v188\n"
    "v_mov_b32_e64 v113, v189\n"
    "v_mov_b32_e64 v114, v190\n"
    "v_mov_b32_e32 v115, v191\n"
    "s_mov_b32 s65, 491520\n"
    "buffer_load_dwordx4 a[196:199], v208, s[40:43], s65 offen\n"
    "s_waitcnt vmcnt(12)\n"
    "v_or3_b32 v200, v200, v116, v117\n"
    "v_or3_b32 v200, v200, v118, v119\n"
    "v_add_u32_e64 v116, v205, v116\n"
    "v_add_u32_e64 v118, v205, v118\n"
    "ds_write_b128 v217, v[116:119] offset:512\n"
    "v_mov_b32_e64 v116, v192\n"
    "v_mov_b32_e64 v117, v193\n"
    "v_mov_b32_e64 v118, v194\n"
    "v_mov_b32_e64 v119, v195\n"
    "s_waitcnt lgkmcnt(0)\n"
    "s_barrier\n"
    "s_mov_b32 s59, 24576\n"
    "buffer_load_dwordx4 v[144:147], v209, s[80:83], s59 offen offset:0\n"
    "ds_read_b64_tr_b16 a[50:51], v224 offset:0\n"
    "ds_read_b64_tr_b16 v[182:183], v196 offset:0\n"
    "ds_read_b64_tr_b16 a[54:55], v226 offset:0\n"
    "ds_read_b64_tr_b16 v[186:187], v197 offset:0\n"
    "ds_read_b64_tr_b16 a[58:59], v228 offset:0\n"
    "ds_read_b64_tr_b16 a[62:63], v229 offset:0\n"
    "ds_read_b64_tr_b16 a[66:67], v230 offset:0\n"
    "ds_read_b64_tr_b16 a[70:71], v231 offset:0\n"
    "ds_read_b64_tr_b16 a[74:75], v224 offset:256\n"
    "ds_read_b64_tr_b16 a[78:79], v226 offset:256\n"
    "s_waitcnt vmcnt(12)\n"
    "ds_write2_b32 v216, a176, a177 offset0:0 offset1:20\n"
    "ds_write2_b32 v216, a178, a179 offset0:40 offset1:60\n"
    "s_waitcnt lgkmcnt(8)\n"
    "ds_write_b128 v207, v[184:187] offset:36864\n"
    "buffer_load_dwordx4 v[148:151], v209, s[80:83], s59 offen offset:256\n"
    "s_waitcnt vmcnt(12)\n"
    "ds_write2_b32 v216, a180, a181 offset0:80 offset1:100\n"
    "ds_write2_b32 v216, a182, a183 offset0:120 offset1:140\n"
    "v_add_u32_e32 v216, -4800, v216\n"
    "buffer_load_dwordx4 v[152:155], v209, s[80:83], s59 offen offset:512\n"
    "s_waitcnt vmcnt(12)\n"
    "v_or3_b32 v200, v200, v120, v121\n"
    "v_or3_b32 v200, v200, v122, v123\n"
    "v_add_u32_e64 v120, v201, v120\n"
    "v_add_u32_e64 v122, v201, v122\n"
    "ds_write_b128 v217, v[120:123] offset:12288\n"
    "s_waitcnt lgkmcnt(1)\n"
    "s_barrier\n"
    "s_mov_b32 s65, 786432\n"
    "buffer_load_dwordx4 a[200:203], v208, s[40:43], s65 offen\n"
    "s_waitcnt vmcnt(12)\n"
    "v_or3_b32 v200, v200, v124, v125\n"
    "v_or3_b32 v200, v200, v126, v127\n"
    "v_add_u32_e32 v124, v203, v124\n"
    "v_add_u32_e64 v126, v203, v126\n"
    "ds_write_b128 v217, v[124:127] offset:12544\n"
    "s_mov_b32 s65, 884736\n"
    "buffer_load_dwordx4 a[204:207], v208, s[40:43], s65 offen\n"
    "s_waitcnt vmcnt(12)\n"
    "v_or3_b32 v200, v200, v128, v129\n"
    "v_or3_b32 v200, v200, v130, v131\n"
    "v_add_u32_e32 v128, v205, v128\n"
    "v_add_u32_e64 v130, v205, v130\n"
    "ds_write_b128 v217, v[128:131] offset:12800\n"
    "ds_read_b128 a[224:227], v250 offset:0\n"
    "ds_read_b128 a[228:231], v250 offset:320\n"
    "ds_read_b128 a[232:235], v251 offset:0\n"
    "ds_read_b128 a[236:239], v251 offset:336\n"
    "ds_read_b128 a[240:243], v250 offset:80\n"
    "ds_read_b128 a[244:247], v250 offset:400\n"
    "ds_read_b128 a[248:251], v251 offset:672\n"
    "ds_read_b128 a[252:255], v251 offset:1008\n"
    "ds_read_b128 v[104:107], v252 offset:1344\n"
    "ds_read_b128 v[108:111], v252 offset:1680\n"
    "ds_read_b128 v[128:131], v252 offset:2016\n"
    "s_waitcnt lgkmcnt(11)\n"
    "s_barrier\n"
    "s_mov_b32 s59, 27648\n"
    "buffer_load_dwordx4 v[156:159], v209, s[80:83], s59 offen offset:0\n"
    "ds_read_b64_tr_b16 a[80:81], v224 offset:12288\n"
    "ds_read_b64_tr_b16 a[84:85], v225 offset:12288\n"
    "ds_read_b64_tr_b16 a[88:89], v226 offset:12288\n"
    "ds_read_b64_tr_b16 a[92:93], v227 offset:12288\n"
    "s_waitcnt lgkmcnt(12)\n"
    "v_smfmac_f32_16x16x64_bf16 v[0:3], a[232:235], a[224:231], v240\n"
    "s_waitcnt lgkmcnt(11)\n"
    "ds_read_b64_tr_b16 a[96:97], v228 offset:12288\n"
    "ds_read_b64_tr_b16 a[100:101], v229 offset:12288\n"
    "v_smfmac_f32_16x16x64_bf16 v[4:7], a[236:239], a[224:231], v240 abid:1\n"
    "ds_read_b64_tr_b16 a[104:105], v230 offset:12288\n"
    "ds_read_b64_tr_b16 a[108:109], v231 offset:12288\n"
    "ds_read_b64_tr_b16 a[112:113], v224 offset:12544\n"
    "ds_read_b64_tr_b16 a[116:117], v225 offset:12544\n"
    "ds_read_b64_tr_b16 a[120:121], v226 offset:12544\n"
    "s_waitcnt lgkmcnt(15)\n"
    "v_smfmac_f32_16x16x64_bf16 v[8:11], a[248:251], a[240:247], v240\n"
    "s_waitcnt lgkmcnt(14)\n"
    "ds_read_b64_tr_b16 a[124:125], v227 offset:12544\n"
    "s_waitcnt vmcnt(12)\n"
    "ds_write2_b32 v216, a184, a185 offset0:0 offset1:20\n"
    "v_smfmac_f32_16x16x64_bf16 v[12:15], a[252:255], a[240:247], v240 abid:1\n"
    "s_nop 0\n"
    "ds_write2_b32 v216, a186, a187 offset0:40 offset1:60\n"
    "ds_read_b128 a[224:227], v250 offset:160\n"
    "ds_read_b128 a[228:231], v250 offset:480\n"
    "ds_read_b128 a[232:235], v251 offset:1344\n"
    "ds_read_b128 a[236:239], v251 offset:1680\n"
    "buffer_load_dwordx4 v[160:163], v209, s[80:83], s59 offen offset:256\n"
    "s_waitcnt vmcnt(12)\n"
    "ds_write2_b32 v216, a188, a189 offset0:80 offset1:100\n"
    "ds_write2_b32 v216, a190, a191 offset0:120 offset1:140\n"
    "v_add_u32_e32 v216, 1360, v216\n"
    "ds_read_b128 a[240:243], v250 offset:240\n"
    "ds_read_b128 a[244:247], v250 offset:560\n"
    "buffer_load_dwordx4 v[164:167], v209, s[80:83], s59 offen offset:512\n"
    "s_waitcnt vmcnt(12)\n"
    "v_or3_b32 v200, v200, v132, v133\n"
    "v_or3_b32 v200, v200, v134, v135\n"
    "v_add_u32_e64 v132, v201, v132\n"
    "v_add_u32_e64 v134, v201, v134\n"
    "ds_write_b128 v217, v[132:135] offset:24576\n"
    "s_waitcnt lgkmcnt(6)\n"
    "v_smfmac_f32_16x16x64_bf16 v[16:19], a[232:235], a[224:231], v240\n"
    "s_waitcnt lgkmcnt(5)\n"
    "v_smfmac_f32_16x16x64_bf16 v[20:23], a[236:239], a[224:231], v240 abid:1\n"
    "s_mov_b32 s65, 1179648\n"
    "buffer_load_dwordx4 a[208:211], v208, s[40:43], s65 offen\n"
    "s_waitcnt vmcnt(12)\n"
    "v_or3_b32 v200, v200, v136, v137\n"
    "v_or3_b32 v200, v200, v138, v139\n"
    "v_add_u32_e32 v136, v203, v136\n"
    "v_add_u32_e64 v138, v203, v138\n"
    "ds_write_b128 v217, v[136:139] offset:24832\n"
    "ds_read_b128 a[248:251], v251 offset:2016\n"
    "ds_read_b128 a[252:255], v251 offset:2352\n"
    "s_mov_b32 s65, 1277952\n"
    "buffer_load_dwordx4 a[212:215], v208, s[40:43], s65 offen\n"
    "s_waitcnt vmcnt(12)\n"
    "v_or3_b32 v200, v200, v140, v141\n"
    "v_or3_b32 v200, v200, v142, v143\n"
    "v_add_u32_e32 v140, v205, v140\n"
    "v_add_u32_e64 v142, v205, v142\n"
    "ds_write_b128 v217, v[140:143] offset:25088\n"
    "ds_read_b128 a[224:227], v250 offset:27024\n"
    "ds_read_b128 a[228:231], v250 offset:27344\n"
    "ds_read_b128 a[232:235], v251 offset:10752\n"
    "ds_read_b128 a[236:239], v251 offset:11088\n"
    "ds_read_b128 v[132:135], v252 offset:2352\n"
    "s_waitcnt lgkmcnt(7)\n"
    "v_smfmac_f32_16x16x64_bf16 v[24:27], a[248:251], a[240:247], v240\n"
    "ds_read_b128 a[248:251], v251 offset:11424\n"
    "s_waitcnt lgkmcnt(7)\n"
    "v_smfmac_f32_16x16x64_bf16 v[28:31], a[252:255], a[240:247], v240 abid:1\n"
    "ds_read_b128 a[240:243], v250 offset:27104\n"
    "ds_read_b128 a[244:247], v250 offset:27424\n"
    "ds_read_b128 a[252:255], v251 offset:11760\n"
    "s_waitcnt lgkmcnt(9)\n"
    "s_barrier\n"
    "s_mov_b32 s65, 1572864\n"
    "buffer_load_dwordx4 a[216:219], v208, s[40:43], s65 offen\n"
    "ds_read_b64_tr_b16 a[82:83], v224 offset:24576\n"
    "ds_read_b64_tr_b16 a[86:87], v225 offset:24576\n"
    "ds_read_b64_tr_b16 a[90:91], v226 offset:24576\n"
    "s_waitcnt lgkmcnt(9)\n"
    "v_smfmac_f32_16x16x64_bf16 v[32:35], a[232:235], a[224:231], v240\n"
    "s_waitcnt lgkmcnt(8)\n"
    "ds_read_b64_tr_b16 a[94:95], v227 offset:24576\n"
    "ds_read_b64_tr_b16 a[98:99], v228 offset:24576\n"
    "v_smfmac_f32_16x16x64_bf16 v[36:39], a[236:239], a[224:231], v240 abid:1\n"
    "ds_read_b64_tr_b16 a[102:103], v229 offset:24576\n"
    "ds_read_b64_tr_b16 a[106:107], v230 offset:24576\n"
    "ds_read_b64_tr_b16 a[110:111], v231 offset:24576\n"
    "ds_read_b64_tr_b16 a[114:115], v224 offset:24832\n"
    "ds_read_b64_tr_b16 a[118:119], v225 offset:24832\n"
    "ds_read_b64_tr_b16 a[122:123], v226 offset:24832\n"
    "ds_read_b64_tr_b16 a[126:127], v227 offset:24832\n"
    "s_waitcnt lgkmcnt(13)\n"
    "v_smfmac_f32_16x16x64_bf16 v[40:43], a[248:251], a[240:247], v240\n"
    "s_waitcnt lgkmcnt(12)\n"
    "s_waitcnt vmcnt(12)\n"
    "ds_write2_b32 v216, a192, a193 offset0:0 offset1:20\n"
    "v_smfmac_f32_16x16x64_bf16 v[44:47], a[252:255], a[240:247], v240 abid:1\n"
    "s_nop 0\n"
    "ds_write2_b32 v216, a194, a195 offset0:40 offset1:60\n"
    "ds_read_b128 a[224:227], v250 offset:27184\n"
    "ds_read_b128 a[228:231], v250 offset:27504\n"
    "ds_read_b128 a[232:235], v251 offset:12096\n"
    "s_mov_b32 s65, 1671168\n"
    "buffer_load_dwordx4 a[220:223], v208, s[40:43], s65 offen\n"
    "s_waitcnt vmcnt(12)\n"
    "ds_write2_b32 v216, a196, a197 offset0:80 offset1:100\n"
    "ds_write2_b32 v216, a198, a199 offset0:120 offset1:140\n"
    "v_add_u32_e32 v216, 1360, v216\n"
    "ds_read_b128 a[236:239], v251 offset:12432\n"
    "ds_read_b128 a[240:243], v250 offset:27264\n"
    "ds_read_b128 a[244:247], v250 offset:27584\n"
    "s_waitcnt vmcnt(11)\n"
    "v_or3_b32 v200, v200, v144, v145\n"
    "v_or3_b32 v200, v200, v146, v147\n"
    "v_add_u32_e64 v144, v201, v144\n"
    "v_add_u32_e32 v146, v201, v146\n"
    "ds_write_b128 v217, v[144:147] offset:0\n"
    "s_waitcnt lgkmcnt(6)\n"
    "v_smfmac_f32_16x16x64_bf16 v[48:51], a[232:235], a[224:231], v240\n"
    "ds_read_b128 a[248:251], v251 offset:12768\n"
    "ds_read_b128 a[252:255], v251 offset:13104\n"
    "ds_read_b128 a[232:235], v251 offset:21504\n"
    "s_waitcnt vmcnt(10)\n"
    "v_or3_b32 v200, v200, v148, v149\n"
    "v_or3_b32 v200, v200, v150, v151\n"
    "v_add_u32_e32 v148, v203, v148\n"
    "v_add_u32_e32 v150, v203, v150\n"
    "ds_write_b128 v217, v[148:151] offset:256\n"
    "s_waitcnt lgkmcnt(6)\n"
    "v_smfmac_f32_16x16x64_bf16 v[52:55], a[236:239], a[224:231], v240 abid:1\n"
    "ds_read_b128 a[224:227], v250 offset:54048\n"
    "ds_read_b128 a[228:231], v250 offset:54368\n"
    "ds_read_b128 a[236:239], v251 offset:21840\n"
    "s_waitcnt vmcnt(9)\n"
    "v_or3_b32 v200, v200, v152, v153\n"
    "v_or3_b32 v200, v200, v154, v155\n"
    "v_add_u32_e32 v152, v205, v152\n"
    "v_add_u32_e32 v154, v205, v154\n"
    "ds_write_b128 v217, v[152:155] offset:512\n"
    "s_waitcnt lgkmcnt(6)\n"
    "v_smfmac_f32_16x16x64_bf16 v[56:59], a[248:251], a[240:247], v240\n"
    "v_smfmac_f32_16x16x64_bf16 v[60:63], a[252:255], a[240:247], v240 abid:1\n"
    "ds_read_b128 a[240:243], v250 offset:54128\n"
    "ds_read_b128 a[244:247], v250 offset:54448\n"
    "ds_read_b128 a[248:251], v251 offset:22176\n"
    "ds_read_b128 a[252:255], v251 offset:22512\n"
    "ds_read_b128 v[144:147], v252 offset:10752\n"
    "ds_read_b128 v[148:151], v252 offset:11088\n"
    "s_waitcnt lgkmcnt(8)\n"
    "v_smfmac_f32_16x16x64_bf16 v[64:67], a[232:235], a[224:231], v240\n"
    "ds_read_b128 a[232:235], v251 offset:22848\n"
    "s_waitcnt lgkmcnt(8)\n"
    "v_smfmac_f32_16x16x64_bf16 v[68:71], a[236:239], a[224:231], v240 abid:1\n"
    "ds_read_b128 a[224:227], v250 offset:54208\n"
    "ds_read_b128 a[228:231], v250 offset:54528\n"
    "s_waitcnt lgkmcnt(9)\n"
    "s_barrier\n"
    "ds_read_b64_tr_b16 a[128:129], v224 offset:0\n"
    "ds_read_b64_tr_b16 a[132:133], v225 offset:0\n"
    "ds_read_b64_tr_b16 a[136:137], v226 offset:0\n"
    "s_waitcnt lgkmcnt(9)\n"
    "v_smfmac_f32_16x16x64_bf16 v[72:75], a[248:251], a[240:247], v240\n"
    "s_waitcnt lgkmcnt(8)\n"
    "ds_read_b64_tr_b16 a[140:141], v227 offset:0\n"
    "ds_read_b64_tr_b16 a[144:145], v228 offset:0\n"
    "v_smfmac_f32_16x16x64_bf16 v[76:79], a[252:255], a[240:247], v240 abid:1\n"
    "ds_read_b64_tr_b16 a[148:149], v229 offset:0\n"
    "ds_read_b64_tr_b16 a[152:153], v230 offset:0\n"
    "ds_read_b64_tr_b16 a[156:157], v231 offset:0\n"
    "ds_read_b64_tr_b16 a[160:161], v224 offset:256\n"
    "ds_read_b64_tr_b16 a[164:165], v225 offset:256\n"
    "ds_read_b64_tr_b16 a[168:169], v226 offset:256\n"
    "ds_read_b64_tr_b16 a[172:173], v227 offset:256\n"
    "s_waitcnt vmcnt(8)\n"
    "ds_write2_b32 v216, a200, a201 offset0:0 offset1:20\n"
    "s_waitcnt lgkmcnt(13)\n"
    "v_smfmac_f32_16x16x64_bf16 v[80:83], a[232:235], a[224:231], v240\n"
    "ds_write2_b32 v216, a202, a203 offset0:40 offset1:60\n"
    "ds_read_b128 a[236:239], v251 offset:23184\n"
    "ds_read_b128 a[240:243], v250 offset:54288\n"
    "ds_read_b128 a[244:247], v250 offset:54608\n"
    "ds_read_b128 a[248:251], v251 offset:23520\n"
    "s_waitcnt vmcnt(7)\n"
    "ds_write2_b32 v216, a204, a205 offset0:80 offset1:100\n"
    "ds_write2_b32 v216, a206, a207 offset0:120 offset1:140\n"
    "v_add_u32_e32 v216, 1360, v216\n"
    "ds_read_b128 a[252:255], v251 offset:23856\n"
    "ds_read_b128 a[232:235], v252 offset:0\n"
    "s_waitcnt vmcnt(6)\n"
    "v_or3_b32 v200, v200, v156, v157\n"
    "v_or3_b32 v200, v200, v158, v159\n"
    "v_add_u32_e64 v156, v201, v156\n"
    "v_add_u32_e64 v158, v201, v158\n"
    "ds_write_b128 v217, v[156:159] offset:12288\n"
    "s_waitcnt lgkmcnt(7)\n"
    "v_smfmac_f32_16x16x64_bf16 v[84:87], a[236:239], a[224:231], v240 abid:1\n"
    "s_waitcnt lgkmcnt(5)\n"
    "v_smfmac_f32_16x16x64_bf16 v[88:91], a[248:251], a[240:247], v240\n"
    "ds_read_b128 a[236:239], v252 offset:336\n"
    "ds_read_b128 a[248:251], v252 offset:672\n"
    "s_waitcnt vmcnt(5)\n"
    "v_or3_b32 v200, v200, v160, v161\n"
    "v_or3_b32 v200, v200, v162, v163\n"
    "v_add_u32_e32 v160, v203, v160\n"
    "v_add_u32_e64 v162, v203, v162\n"
    "ds_write_b128 v217, v[160:163] offset:12544\n"
    "s_waitcnt lgkmcnt(4)\n"
    "v_smfmac_f32_16x16x64_bf16 v[92:95], a[252:255], a[240:247], v240 abid:1\n"
    "ds_read_b128 a[252:255], v252 offset:1008\n"
    "ds_read_b128 v[160:163], v252 offset:11424\n"
    "s_waitcnt vmcnt(4)\n"
    "v_or3_b32 v200, v200, v164, v165\n"
    "v_or3_b32 v200, v200, v166, v167\n"
    "v_add_u32_e64 v164, v205, v164\n"
    "v_add_u32_e64 v166, v205, v166\n"
    "ds_write_b128 v217, v[164:167] offset:12800\n"
    "v_and_b32_e32 v200, 0x40004000, v200\n"
    "v_cmp_le_u32_e32 vcc, 1, v200\n"
    "s_nop 1\n"
    "s_cmp_lg_u64 vcc, 0\n"
    "s_cselect_b32 vcc_lo, 1, 0\n"
    "s_or_b32 s66, s66, vcc_lo\n"
    "v_cmp_le_u32_e32 vcc, 981467136, v232\n"
    "s_nop 1\n"
    "s_cmp_lg_u64 vcc, 0\n"
    "s_cselect_b32 vcc_lo, 2, 0\n"
    "s_or_b32 s66, s66, vcc_lo\n"
    "v_mov_b32_e32 v200, s66\n"
    "v_mov_b32_e32 v232, 162992\n"
    "s_mov_b64 exec, 1\n"
    "ds_or_b32 v232, v200\n"
    "s_mov_b64 exec, -1\n"
    "ds_read_b128 v[164:167], v252 offset:11760\n"
    "s_waitcnt lgkmcnt(2)\n"
    "s_barrier\n"
    "ds_read_b64_tr_b16 a[130:131], v224 offset:12288\n"
    "ds_read_b64_tr_b16 a[134:135], v225 offset:12288\n"
    "ds_read_b64_tr_b16 a[138:139], v226 offset:12288\n"
    "ds_read_b64_tr_b16 a[142:143], v227 offset:12288\n"
    "ds_read_b64_tr_b16 a[146:147], v228 offset:12288\n"
    "ds_read_b64_tr_b16 a[150:151], v229 offset:12288\n"
    "ds_read_b64_tr_b16 a[154:155], v230 offset:12288\n"
    "ds_read_b64_tr_b16 a[158:159], v231 offset:12288\n"
    "ds_read_b64_tr_b16 a[162:163], v224 offset:12544\n"
    "ds_read_b64_tr_b16 a[166:167], v225 offset:12544\n"
    "ds_read_b64_tr_b16 a[170:171], v226 offset:12544\n"
    "ds_read_b64_tr_b16 a[174:175], v227 offset:12544\n"
    "s_nop 0\n"
    "s_waitcnt vmcnt(3)\n"
    "ds_write2_b32 v216, a208, a209 offset0:0 offset1:20\n"
    "ds_write2_b32 v216, a210, a211 offset0:40 offset1:60\n"
    "s_waitcnt vmcnt(2)\n"
    "ds_write2_b32 v216, a212, a213 offset0:80 offset1:100\n"
    "s_waitcnt lgkmcnt(2)\n"
    "s_barrier\n"
    "s_add_u32 m0, s38, 0\n"
    "s_mov_b32 s64, 18432\n"
    "buffer_load_dwordx4 v210, s[84:87], s64 offen offset:0 lds\n"
    "buffer_load_dwordx4 v210, s[84:87], s64 offen offset:1024 lds\n"
    "buffer_load_dwordx4 v210, s[84:87], s64 offen offset:2048 lds\n"
    "s_add_u32 m0, s38, 3072\n"
    "s_mov_b32 s64, 21504\n"
    "buffer_load_dwordx4 v210, s[84:87], s64 offen offset:0 lds\n"
    "buffer_load_dwordx4 v210, s[84:87], s64 offen offset:1024 lds\n"
    "buffer_load_dwordx4 v210, s[84:87], s64 offen offset:2048 lds\n"
    "s_add_u32 m0, s38, 6144\n"
    "s_mov_b32 s64, 24576\n"
    "buffer_load_dwordx4 v210, s[84:87], s64 offen offset:0 lds\n"
    "buffer_load_dwordx4 v210, s[84:87], s64 offen offset:1024 lds\n"
    "buffer_load_dwordx4 v210, s[84:87], s64 offen offset:2048 lds\n"
    "s_sub_u32 s80, s76, .Lreplk_last-.Lreplk_loop\n"
    "s_subb_u32 s81, s77, 0\n"
    "s_add_u32 s82, s36, .Lreplk_last-.Lreplk_loop\n"
    "s_and_b32 s85, s37, 0x3fff\n"
    "s_add_u32 s84, s85, 8192\n"
    "s_lshr_b32 s85, s85, 2\n"
    "v_lshlrev_b32_e64 v235, 3, v244\n"
    "s_mov_b32 exec_hi, 0\n"
    "buffer_load_dword v233, v235, s[40:43], s53 offen sc1\n"
    "s_mov_b32 exec_hi, -1\n"
    "s_add_u32 s53, s53, 393216\n"
    "ds_write2_b32 v216, a214, a215 offset0:120 offset1:140\n"
    "v_add_u32_e32 v216, 1360, v216\n"
    "s_mov_b32 s64, 0\n"
    "s_mov_b32 s59, 0\n"
    "ds_read_b32 v232, v232\n"
    "v_lshlrev_b32_e32 v235, 3, v244\n"
    "s_mov_b32 exec_hi, 0\n"
    "buffer_load_dword v233, v235, s[40:43], s53 offen sc1\n"
    "s_mov_b32 exec_hi, -1\n"
    "s_add_u32 s53, s53, 393216\n"
    "s_waitcnt vmcnt(12)\n"
    "ds_write2_b32 v216, a216, a217 offset0:0 offset1:20\n"
    "ds_write2_b32 v216, a218, a219 offset0:40 offset1:60\n"
    "v_lshlrev_b32_e32 v235, 3, v244\n"
    "s_mov_b32 exec_hi, 0\n"
    "buffer_load_dword v233, v235, s[40:43], s53 offen sc1\n"
    "s_mov_b32 exec_hi, -1\n"
    "s_add_u32 s53, s53, 393216\n"
    "s_waitcnt vmcnt(12)\n"
    "ds_write2_b32 v216, a220, a221 offset0:80 offset1:100\n"
    "ds_write2_b32 v216, a222, a223 offset0:120 offset1:140\n"
    "v_lshlrev_b32_e64 v235, 3, v244\n"
    "s_mov_b32 exec_hi, 0\n"
    "buffer_load_dword v233, v235, s[40:43], s53 offen sc1\n"
    "s_mov_b32 exec_hi, -1\n"
    "s_add_u32 s53, s53, 393216\n"
    "v_mov_b32_e64 v235, v222\n"
    "s_waitcnt lgkmcnt(0)\n"
    "s_barrier\n"
    "s_getpc_b64 vcc\n"
    ".Lreplk_pc2:\n"
    "s_sub_u32 vcc_lo, vcc_lo, .Lreplk_pc2-.Lreplk_pc0\n"
    "s_subb_u32 vcc_hi, vcc_hi, 0\n"
    "global_load_dword v233, v215, vcc\n"
    "ds_read_b128 a[224:227], v250 offset:640\n"
    "ds_read_b128 a[228:231], v250 offset:960\n"
    "ds_read_b128 a[240:243], v250 offset:720\n"
    "ds_read_b128 a[244:247], v250 offset:1040\n"
    "ds_read_b128 v[96:99], v250 offset:800\n"
    "ds_read_b128 v[100:103], v250 offset:1120\n"
    "ds_read_b128 v[120:123], v250 offset:880\n"
    "ds_read_b128 v[124:127], v250 offset:1200\n"
    "ds_read_b128 v[136:139], v250 offset:27664\n"
    "ds_read_b128 v[140:143], v250 offset:27984\n"
    "ds_read_b128 v[152:155], v250 offset:27744\n"
    "ds_read_b128 v[156:159], v250 offset:28064\n"
    "s_waitcnt lgkmcnt(10)\n"
    "v_smfmac_f32_16x16x64_bf16 v[0:3], a[232:235], a[224:231], v240\n"
    "s_nop 0\n"
    "ds_read_b128 a[232:235], v252 offset:12096\n"
    "v_smfmac_f32_16x16x64_bf16 v[4:7], a[236:239], a[224:231], v240 abid:1\n"
    "ds_read_b128 a[224:227], v250 offset:27824\n"
    "s_waitcnt lgkmcnt(10)\n"
    "v_smfmac_f32_16x16x64_bf16 v[8:11], a[248:251], a[240:247], v240\n"
    "ds_read_b128 a[228:231], v250 offset:28144\n"
    "v_smfmac_f32_16x16x64_bf16 v[12:15], a[252:255], a[240:247], v240 abid:1\n"
    "s_waitcnt lgkmcnt(9)\n"
    "ds_read_b128 a[236:239], v252 offset:12432\n"
    "v_smfmac_f32_16x16x64_bf16 v[16:19], v[104:107], v[96:103], v240\n"
    "ds_read_b128 a[240:243], v250 offset:27904\n"
    "v_smfmac_f32_16x16x64_bf16 v[20:23], v[108:111], v[96:103], v240 abid:1\n"
    "s_waitcnt lgkmcnt(9)\n"
    "ds_read_b128 a[244:247], v250 offset:28224\n"
    "v_smfmac_f32_16x16x64_bf16 v[24:27], v[128:131], v[120:127], v240\n"
    "ds_read_b128 a[248:251], v252 offset:12768\n"
    "v_smfmac_f32_16x16x64_bf16 v[28:31], v[132:135], v[120:127], v240 abid:1\n"
    "s_waitcnt lgkmcnt(9)\n"
    "ds_read_b128 a[252:255], v252 offset:13104\n"
    "v_smfmac_f32_16x16x64_bf16 v[32:35], v[144:147], v[136:143], v240\n"
    "s_nop 0\n"
    "ds_read_b128 v[96:99], v250 offset:54688\n"
    "v_smfmac_f32_16x16x64_bf16 v[36:39], v[148:151], v[136:143], v240 abid:1\n"
    "s_waitcnt lgkmcnt(9)\n"
    "ds_read_b128 v[100:103], v250 offset:55008\n"
    "v_smfmac_f32_16x16x64_bf16 v[40:43], v[160:163], v[152:159], v240\n"
    "ds_read_b128 v[104:107], v252 offset:21504\n"
    "v_smfmac_f32_16x16x64_bf16 v[44:47], v[164:167], v[152:159], v240 abid:1\n"
    "ds_read_b128 v[108:111], v252 offset:21840\n"
    "ds_read_b128 v[120:123], v250 offset:54768\n"
    "ds_read_b128 v[124:127], v250 offset:55088\n"
    "v_pk_mov_b32 v[224:225], v[0:1], v[0:1] op_sel:[0,1]\n"
    "v_pk_mov_b32 v[226:227], v[2:3], v[2:3] op_sel:[0,1]\n"
    "v_pk_mul_f32 v[228:229], v[0:1], v[0:1]\n"
    "v_pk_mul_f32 v[230:231], v[2:3], v[2:3]\n"
    "v_pk_add_f32 v[224:225], v[224:225], v[4:5]\n"
    "v_pk_add_f32 v[226:227], v[226:227], v[6:7]\n"
    "v_pk_fma_f32 v[228:229], v[4:5], v[4:5], v[228:229]\n"
    "v_pk_fma_f32 v[230:231], v[6:7], v[6:7], v[230:231]\n"
    "v_pk_add_f32 v[224:225], v[224:225], v[8:9]\n"
    "v_pk_add_f32 v[226:227], v[226:227], v[10:11]\n"
    "v_pk_fma_f32 v[228:229], v[8:9], v[8:9], v[228:229]\n"
    "v_pk_fma_f32 v[230:231], v[10:11], v[10:11], v[230:231]\n"
    "v_pk_add_f32 v[224:225], v[224:225], v[12:13]\n"
    "v_pk_add_f32 v[226:227], v[226:227], v[14:15]\n"
    "v_pk_fma_f32 v[228:229], v[12:13], v[12:13], v[228:229]\n"
    "v_pk_fma_f32 v[230:231], v[14:15], v[14:15], v[230:231]\n"
    "v_pk_add_f32 v[224:225], v[224:225], v[16:17]\n"
    "v_pk_add_f32 v[226:227], v[226:227], v[18:19]\n"
    "v_pk_fma_f32 v[228:229], v[16:17], v[16:17], v[228:229]\n"
    "v_pk_fma_f32 v[230:231], v[18:19], v[18:19], v[230:231]\n"
    "v_pk_add_f32 v[224:225], v[224:225], v[20:21]\n"
    "v_pk_add_f32 v[226:227], v[226:227], v[22:23]\n"
    "v_pk_fma_f32 v[228:229], v[20:21], v[20:21], v[228:229]\n"
    "v_pk_fma_f32 v[230:231], v[22:23], v[22:23], v[230:231]\n"
    "v_pk_add_f32 v[224:225], v[224:225], v[24:25]\n"
    "v_pk_add_f32 v[226:227], v[226:227], v[26:27]\n"
    "v_pk_fma_f32 v[228:229], v[24:25], v[24:25], v[228:229]\n"
    "v_pk_fma_f32 v[230:231], v[26:27], v[26:27], v[230:231]\n"
    "v_pk_add_f32 v[224:225], v[224:225], v[28:29]\n"
    "v_pk_add_f32 v[226:227], v[226:227], v[30:31]\n"
    "v_pk_fma_f32 v[228:229], v[28:29], v[28:29], v[228:229]\n"
    "v_pk_fma_f32 v[230:231], v[30:31], v[30:31], v[230:231]\n"
    "v_pk_add_f32 v[224:225], v[224:225], v[32:33]\n"
    "v_pk_add_f32 v[226:227], v[226:227], v[34:35]\n"
    "v_pk_fma_f32 v[228:229], v[32:33], v[32:33], v[228:229]\n"
    "v_pk_fma_f32 v[230:231], v[34:35], v[34:35], v[230:231]\n"
    "v_pk_add_f32 v[224:225], v[224:225], v[36:37]\n"
    "v_pk_add_f32 v[226:227], v[226:227], v[38:39]\n"
    "v_pk_fma_f32 v[228:229], v[36:37], v[36:37], v[228:229]\n"
    "v_pk_fma_f32 v[230:231], v[38:39], v[38:39], v[230:231]\n"
    "v_pk_add_f32 v[224:225], v[224:225], v[40:41]\n"
    "v_pk_add_f32 v[226:227], v[226:227], v[42:43]\n"
    "v_pk_fma_f32 v[228:229], v[40:41], v[40:41], v[228:229]\n"
    "v_pk_fma_f32 v[230:231], v[42:43], v[42:43], v[230:231]\n"
    "v_pk_add_f32 v[224:225], v[224:225], v[44:45]\n"
    "v_pk_add_f32 v[226:227], v[226:227], v[46:47]\n"
    "v_pk_fma_f32 v[228:229], v[44:45], v[44:45], v[228:229]\n"
    "v_pk_fma_f32 v[230:231], v[46:47], v[46:47], v[230:231]\n"
    "s_waitcnt lgkmcnt(0)\n"
    "v_smfmac_f32_16x16x64_bf16 v[48:51], a[232:235], a[224:231], v240\n"
    "s_nop 0\n"
    "ds_read_b128 v[128:131], v252 offset:22176\n"
    "v_smfmac_f32_16x16x64_bf16 v[52:55], a[236:239], a[224:231], v240 abid:1\n"
    "ds_read_b128 v[132:135], v252 offset:22512\n"
    "v_smfmac_f32_16x16x64_bf16 v[56:59], a[248:251], a[240:247], v240\n"
    "ds_read_b128 v[136:139], v250 offset:54848\n"
    "v_smfmac_f32_16x16x64_bf16 v[60:63], a[252:255], a[240:247], v240 abid:1\n"
    "ds_read_b128 v[140:143], v250 offset:55168\n"
    "v_smfmac_f32_16x16x64_bf16 v[64:67], v[104:107], v[96:103], v240\n"
    "ds_read_b128 v[144:147], v252 offset:22848\n"
    "v_smfmac_f32_16x16x64_bf16 v[68:71], v[108:111], v[96:103], v240 abid:1\n"
    "ds_read_b128 v[148:151], v252 offset:23184\n"
    "ds_read_b128 v[152:155], v250 offset:54928\n"
    "v_accvgpr_write_b32 a248, v236\n"
    "ds_read_b128 v[156:159], v250 offset:55248\n"
    "v_pk_add_f32 v[224:225], v[224:225], v[48:49]\n"
    "v_pk_add_f32 v[226:227], v[226:227], v[50:51]\n"
    "v_pk_fma_f32 v[228:229], v[48:49], v[48:49], v[228:229]\n"
    "v_pk_fma_f32 v[230:231], v[50:51], v[50:51], v[230:231]\n"
    "v_pk_add_f32 v[224:225], v[224:225], v[52:53]\n"
    "s_waitcnt vmcnt(5)\n"
    "s_barrier\n"
    "ds_read_b64_tr_b16 a[176:177], v220 offset:0\n"
    "ds_read_b64_tr_b16 a[178:179], v220 offset:3072\n"
    "v_pk_add_f32 v[226:227], v[226:227], v[54:55]\n"
    "ds_read_b64_tr_b16 a[180:181], v220 offset:8\n"
    "ds_read_b64_tr_b16 a[182:183], v220 offset:3080\n"
    "ds_read_b64_tr_b16 a[184:185], v220 offset:64\n"
    "v_pk_fma_f32 v[228:229], v[52:53], v[52:53], v[228:229]\n"
    "ds_read_b64_tr_b16 a[186:187], v220 offset:3136\n"
    "ds_read_b64_tr_b16 a[188:189], v220 offset:72\n"
    "ds_read_b64_tr_b16 a[190:191], v220 offset:3144\n"
    "v_pk_fma_f32 v[230:231], v[54:55], v[54:55], v[230:231]\n"
    "ds_read_b64_tr_b16 v[188:189], v218 offset:128\n"
    "ds_read_b64_tr_b16 v[190:191], v218 offset:3200\n"
    "ds_read_b64_tr_b16 a[192:193], v220 offset:6144\n"
    "v_pk_add_f32 v[224:225], v[224:225], v[56:57]\n"
    "ds_read_b64_tr_b16 a[194:195], v220 offset:9216\n"
    "ds_read_b64_tr_b16 a[196:197], v220 offset:6152\n"
    "ds_read_b64_tr_b16 a[198:199], v220 offset:9224\n"
    "v_pk_add_f32 v[226:227], v[226:227], v[58:59]\n"
    "ds_read_b64_tr_b16 a[200:201], v220 offset:6208\n"
    "ds_read_b64_tr_b16 a[202:203], v220 offset:9280\n"
    "ds_read_b64_tr_b16 a[204:205], v220 offset:6216\n"
    "v_pk_fma_f32 v[228:229], v[56:57], v[56:57], v[228:229]\n"
    "ds_read_b64_tr_b16 a[206:207], v220 offset:9288\n"
    "ds_read_b64_tr_b16 v[192:193], v218 offset:6272\n"
    "ds_read_b64_tr_b16 v[194:195], v218 offset:9344\n"
    "v_pk_fma_f32 v[230:231], v[58:59], v[58:59], v[230:231]\n"
    "ds_read_b64_tr_b16 a[208:209], v220 offset:12288\n"
    "ds_read_b64_tr_b16 a[210:211], v220 offset:15360\n"
    "ds_read_b64_tr_b16 a[212:213], v220 offset:12296\n"
    "v_pk_add_f32 v[224:225], v[224:225], v[60:61]\n"
    "ds_read_b64_tr_b16 a[214:215], v220 offset:15368\n"
    "ds_read_b64_tr_b16 a[216:217], v220 offset:12352\n"
    "ds_read_b64_tr_b16 a[218:219], v220 offset:15424\n"
    "v_pk_add_f32 v[226:227], v[226:227], v[62:63]\n"
    "ds_read_b64_tr_b16 a[220:221], v220 offset:12360\n"
    "ds_read_b64_tr_b16 a[222:223], v220 offset:15432\n"
    "ds_read_b64_tr_b16 v[196:197], v218 offset:12416\n"
    "v_pk_fma_f32 v[228:229], v[60:61], v[60:61], v[228:229]\n"
    "ds_read_b64_tr_b16 v[198:199], v218 offset:15488\n"
    "v_pk_fma_f32 v[230:231], v[62:63], v[62:63], v[230:231]\n"
    "v_pk_add_f32 v[224:225], v[224:225], v[64:65]\n"
    "v_pk_add_f32 v[226:227], v[226:227], v[66:67]\n"
    "v_pk_fma_f32 v[228:229], v[64:65], v[64:65], v[228:229]\n"
    "v_pk_fma_f32 v[230:231], v[66:67], v[66:67], v[230:231]\n"
    "v_pk_add_f32 v[224:225], v[224:225], v[68:69]\n"
    "v_pk_add_f32 v[226:227], v[226:227], v[70:71]\n"
    "v_pk_fma_f32 v[228:229], v[68:69], v[68:69], v[228:229]\n"
    "v_pk_fma_f32 v[230:231], v[70:71], v[70:71], v[230:231]\n"
    "s_waitcnt lgkmcnt(10)\n"
    "v_smfmac_f32_16x16x64_bf16 v[72:75], v[128:131], v[120:127], v240\n"
    "s_nop 0\n"
    "ds_read_b128 v[160:163], v252 offset:23520\n"
    "v_smfmac_f32_16x16x64_bf16 v[76:79], v[132:135], v[120:127], v240 abid:1\n"
    "ds_read_b128 v[164:167], v252 offset:23856\n"
    "v_smfmac_f32_16x16x64_bf16 v[80:83], v[144:147], v[136:143], v240\n"
    "v_accvgpr_write_b32 a249, v237\n"
    "v_smfmac_f32_16x16x64_bf16 v[84:87], v[148:151], v[136:143], v240 abid:1\n"
    "v_accvgpr_write_b32 a250, v238\n"
    "v_accvgpr_write_b32 a251, v239\n"
    "v_pk_add_f32 v[224:225], v[224:225], v[72:73]\n"
    "v_pk_add_f32 v[226:227], v[226:227], v[74:75]\n"
    "v_pk_fma_f32 v[228:229], v[72:73], v[72:73], v[228:229]\n"
    "v_pk_fma_f32 v[230:231], v[74:75], v[74:75], v[230:231]\n"
    "v_pk_add_f32 v[224:225], v[224:225], v[76:77]\n"
    "v_pk_add_f32 v[226:227], v[226:227], v[78:79]\n"
    "v_pk_fma_f32 v[228:229], v[76:77], v[76:77], v[228:229]\n"
    "v_pk_fma_f32 v[230:231], v[78:79], v[78:79], v[230:231]\n"
    "v_pk_add_f32 v[224:225], v[224:225], v[80:81]\n"
    "v_pk_add_f32 v[226:227], v[226:227], v[82:83]\n"
    "v_pk_fma_f32 v[228:229], v[80:81], v[80:81], v[228:229]\n"
    "v_pk_fma_f32 v[230:231], v[82:83], v[82:83], v[230:231]\n"
    "v_pk_add_f32 v[224:225], v[224:225], v[84:85]\n"
    "v_pk_add_f32 v[226:227], v[226:227], v[86:87]\n"
    "v_pk_fma_f32 v[228:229], v[84:85], v[84:85], v[228:229]\n"
    "v_pk_fma_f32 v[230:231], v[86:87], v[86:87], v[230:231]\n"
    "s_waitcnt lgkmcnt(1)\n"
    "v_smfmac_f32_16x16x64_bf16 v[88:91], v[160:163], v[152:159], v240\n"
    "s_waitcnt lgkmcnt(0)\n"
    "v_smfmac_f32_16x16x64_bf16 v[92:95], v[164:167], v[152:159], v240 abid:1\n"
    "s_nop 0\n"
    "s_nop 5\n"
    "v_pk_add_f32 v[224:225], v[224:225], v[88:89]\n"
    "v_pk_add_f32 v[226:227], v[226:227], v[90:91]\n"
    "v_pk_fma_f32 v[228:229], v[88:89], v[88:89], v[228:229]\n"
    "v_pk_fma_f32 v[230:231], v[90:91], v[90:91], v[230:231]\n"
    "v_pk_add_f32 v[224:225], v[224:225], v[92:93]\n"
    "v_pk_add_f32 v[226:227], v[226:227], v[94:95]\n"
    "v_pk_fma_f32 v[228:229], v[92:93], v[92:93], v[228:229]\n"
    "v_pk_fma_f32 v[230:231], v[94:95], v[94:95], v[230:231]\n"
    "ds_write_b128 v254, v[224:227]\n"
    "ds_write_b128 v254, v[228:231] offset:1024\n"
    "v_add_u32_e64 v254, s56, v242\n"
    "s_waitcnt lgkmcnt(0)\n"
    "s_barrier\n"
    "ds_read_b128 v[128:131], v254 offset:0\n"
    "ds_read_b128 v[132:135], v254 offset:6752\n"
    "ds_read_b128 v[136:139], v254 offset:13504\n"
    "ds_read_b128 v[140:143], v254 offset:20256\n"
    "ds_read_b128 v[144:147], v254 offset:1024\n"
    "ds_read_b128 v[148:151], v254 offset:7776\n"
    "ds_read_b128 v[152:155], v254 offset:14528\n"
    "ds_read_b128 v[156:159], v254 offset:21280\n"
    "ds_write_b128 v234, v[112:115] offset:0\n"
    "ds_write_b128 v234, v[116:119] offset:16\n"
    "s_waitcnt lgkmcnt(8)\n"
    "v_pk_add_f32 v[128:129], v[128:129], v[132:133]\n"
    "v_pk_add_f32 v[130:131], v[130:131], v[134:135]\n"
    "ds_write_b128 v207, v[168:171] offset:0\n"
    "s_waitcnt lgkmcnt(6)\n"
    "v_pk_add_f32 v[136:137], v[136:137], v[140:141]\n"
    "v_pk_add_f32 v[138:139], v[138:139], v[142:143]\n"
    "s_waitcnt lgkmcnt(5)\n"
    "v_pk_add_f32 v[144:145], v[144:145], v[148:149]\n"
    "v_pk_add_f32 v[146:147], v[146:147], v[150:151]\n"
    "v_pk_add_f32 v[128:129], v[128:129], v[136:137]\n"
    "v_pk_add_f32 v[130:131], v[130:131], v[138:139]\n"
    "v_mul_f32_e32 v128, 0x3c2aaaab, v128\n"
    "v_mul_f32_e32 v129, 0x3c2aaaab, v129\n"
    "v_mul_f32_e32 v130, 0x3c2aaaab, v130\n"
    "s_waitcnt lgkmcnt(3)\n"
    "v_pk_add_f32 v[152:153], v[152:153], v[156:157]\n"
    "v_pk_add_f32 v[154:155], v[154:155], v[158:159]\n"
    "v_pk_add_f32 v[144:145], v[144:145], v[152:153]\n"
    "v_pk_add_f32 v[146:147], v[146:147], v[154:155]\n"
    "v_mul_f32_e32 v131, 0x3c2aaaab, v131\n"
    "v_mul_f32_e32 v144, 0x3c2aaaab, v144\n"
    "v_mul_f32_e32 v145, 0x3c2aaaab, v145\n"
    "v_mul_f32_e32 v146, 0x3c2aaaab, v146\n"
    "v_mul_f32_e32 v147, 0x3c2aaaab, v147\n"
    "v_fma_f32 v144, -v128, v128, v144\n"
    "v_fma_f32 v145, -v129, v129, v145\n"
    "v_fma_f32 v146, -v130, v130, v146\n"
    "v_fma_f32 v147, -v131, v131, v147\n"
    "v_max_f32_e64 v144, 0, v144\n"
    "v_max_f32_e64 v145, 0, v145\n"
    "v_max_f32_e64 v146, 0, v146\n"
    "v_max_f32_e64 v147, 0, v147\n"
    "v_add_f32_e32 v144, 0x3727c5ac, v144\n"
    "v_add_f32_e32 v145, 0x3727c5ac, v145\n"
    "v_add_f32_e32 v146, 0x3727c5ac, v146\n"
    "v_add_f32_e32 v147, 0x3727c5ac, v147\n"
    "v_rsq_f32_e64 v144, v144\n"
    "v_rsq_f32_e64 v145, v145\n"
    "v_rsq_f32_e64 v146, v146\n"
    "v_rsq_f32_e64 v147, v147\n"
    "v_mul_f32_e64 v224, s50, v144\n"
    "v_mul_f32_e64 v225, s50, v145\n"
    "v_mul_f32_e64 v226, s50, v146\n"
    "v_mul_f32_e64 v227, s50, v147\n"
    "v_mul_f32_e64 v228, v224, -v128\n"
    "v_mul_f32_e64 v229, v225, -v129\n"
    "v_mul_f32_e64 v230, v226, -v130\n"
    "v_mul_f32_e64 v231, v227, -v131\n"
    "ds_write_b128 v207, v[172:175] offset:4096\n"
    "v_pk_fma_f32 v[64:65], v[64:65], v[224:225], v[228:229]\n"
    "v_pk_fma_f32 v[66:67], v[66:67], v[226:227], v[230:231]\n"
    "v_pk_fma_f32 v[68:69], v[68:69], v[224:225], v[228:229]\n"
    "v_pk_fma_f32 v[70:71], v[70:71], v[226:227], v[230:231]\n"
    "v_pk_fma_f32 v[72:73], v[72:73], v[224:225], v[228:229]\n"
    "v_pk_fma_f32 v[74:75], v[74:75], v[226:227], v[230:231]\n"
    "ds_write_b128 v207, v[176:179] offset:8192\n"
    "v_pk_fma_f32 v[76:77], v[76:77], v[224:225], v[228:229]\n"
    "v_pk_fma_f32 v[78:79], v[78:79], v[226:227], v[230:231]\n"
    "v_pk_fma_f32 v[80:81], v[80:81], v[224:225], v[228:229]\n"
    "v_pk_fma_f32 v[82:83], v[82:83], v[226:227], v[230:231]\n"
    "v_pk_fma_f32 v[84:85], v[84:85], v[224:225], v[228:229]\n"
    "v_pk_fma_f32 v[86:87], v[86:87], v[226:227], v[230:231]\n"
    "ds_write_b128 v207, v[188:191] offset:12288\n"
    "v_pk_fma_f32 v[88:89], v[88:89], v[224:225], v[228:229]\n"
    "v_pk_fma_f32 v[90:91], v[90:91], v[226:227], v[230:231]\n"
    "v_pk_fma_f32 v[92:93], v[92:93], v[224:225], v[228:229]\n"
    "v_pk_fma_f32 v[94:95], v[94:95], v[226:227], v[230:231]\n"
    "v_cvt_pk_bf16_f32 v96, v64, v68\n"
    "v_cvt_pk_bf16_f32 v100, v65, v69\n"
    "ds_write_b128 v207, v[192:195] offset:16384\n"
    "v_cvt_pk_bf16_f32 v104, v66, v70\n"
    "v_cvt_pk_bf16_f32 v108, v67, v71\n"
    "v_cvt_pk_bf16_f32 v97, v72, v76\n"
    "v_cvt_pk_bf16_f32 v101, v73, v77\n"
    "v_cvt_pk_bf16_f32 v105, v74, v78\n"
    "v_cvt_pk_bf16_f32 v109, v75, v79\n"
    "ds_write_b128 v207, v[196:199] offset:20480\n"
    "v_cvt_pk_bf16_f32 v98, v80, v84\n"
    "v_cvt_pk_bf16_f32 v102, v81, v85\n"
    "v_cvt_pk_bf16_f32 v106, v82, v86\n"
    "v_cvt_pk_bf16_f32 v110, v83, v87\n"
    "v_cvt_pk_bf16_f32 v99, v88, v92\n"
    "v_cvt_pk_bf16_f32 v103, v89, v93\n"
    "ds_write_b128 v207, v[180:183] offset:32768\n"
    "v_cvt_pk_bf16_f32 v107, v90, v94\n"
    "v_cvt_pk_bf16_f32 v111, v91, v95\n"
    "v_pk_fma_f32 v[0:1], v[0:1], v[224:225], v[228:229]\n"
    "v_pk_fma_f32 v[2:3], v[2:3], v[226:227], v[230:231]\n"
    "v_pk_fma_f32 v[4:5], v[4:5], v[224:225], v[228:229]\n"
    "v_pk_fma_f32 v[6:7], v[6:7], v[226:227], v[230:231]\n"
    "ds_write_b128 v253, v[96:99] offset:2048\n"
    "v_pk_fma_f32 v[8:9], v[8:9], v[224:225], v[228:229]\n"
    "v_pk_fma_f32 v[10:11], v[10:11], v[226:227], v[230:231]\n"
    "v_pk_fma_f32 v[12:13], v[12:13], v[224:225], v[228:229]\n"
    "v_pk_fma_f32 v[14:15], v[14:15], v[226:227], v[230:231]\n"
    "v_pk_fma_f32 v[16:17], v[16:17], v[224:225], v[228:229]\n"
    "v_pk_fma_f32 v[18:19], v[18:19], v[226:227], v[230:231]\n"
    "ds_write_b128 v253, v[100:103] offset:5120\n"
    "v_pk_fma_f32 v[20:21], v[20:21], v[224:225], v[228:229]\n"
    "v_pk_fma_f32 v[22:23], v[22:23], v[226:227], v[230:231]\n"
    "v_pk_fma_f32 v[24:25], v[24:25], v[224:225], v[228:229]\n"
    "v_pk_fma_f32 v[26:27], v[26:27], v[226:227], v[230:231]\n"
    "v_pk_fma_f32 v[28:29], v[28:29], v[224:225], v[228:229]\n"
    "v_pk_fma_f32 v[30:31], v[30:31], v[226:227], v[230:231]\n"
    "v_cvt_pk_bf16_f32 v80, v0, v4\n"
    "v_cvt_pk_bf16_f32 v84, v1, v5\n"
    "v_cvt_pk_bf16_f32 v88, v2, v6\n"
    "v_cvt_pk_bf16_f32 v92, v3, v7\n"
    "v_cvt_pk_bf16_f32 v81, v8, v12\n"
    "v_cvt_pk_bf16_f32 v85, v9, v13\n"
    "v_cvt_pk_bf16_f32 v89, v10, v14\n"
    "v_cvt_pk_bf16_f32 v93, v11, v15\n"
    "v_cvt_pk_bf16_f32 v82, v16, v20\n"
    "v_cvt_pk_bf16_f32 v86, v17, v21\n"
    "v_cvt_pk_bf16_f32 v90, v18, v22\n"
    "v_cvt_pk_bf16_f32 v94, v19, v23\n"
    "v_cvt_pk_bf16_f32 v83, v24, v28\n"
    "ds_write_b128 v253, v[80:83] offset:0\n"
    "v_cvt_pk_bf16_f32 v87, v25, v29\n"
    "v_cvt_pk_bf16_f32 v91, v26, v30\n"
    "v_cvt_pk_bf16_f32 v95, v27, v31\n"
    "v_pk_fma_f32 v[32:33], v[32:33], v[224:225], v[228:229]\n"
    "v_pk_fma_f32 v[34:35], v[34:35], v[226:227], v[230:231]\n"
    "v_pk_fma_f32 v[36:37], v[36:37], v[224:225], v[228:229]\n"
    "ds_write_b128 v253, v[84:87] offset:3072\n"
    "v_pk_fma_f32 v[38:39], v[38:39], v[226:227], v[230:231]\n"
    "v_pk_fma_f32 v[40:41], v[40:41], v[224:225], v[228:229]\n"
    "v_pk_fma_f32 v[42:43], v[42:43], v[226:227], v[230:231]\n"
    "v_pk_fma_f32 v[44:45], v[44:45], v[224:225], v[228:229]\n"
    "v_pk_fma_f32 v[46:47], v[46:47], v[226:227], v[230:231]\n"
    "v_pk_fma_f32 v[48:49], v[48:49], v[224:225], v[228:229]\n"
    "v_pk_fma_f32 v[50:51], v[50:51], v[226:227], v[230:231]\n"
    "v_pk_fma_f32 v[52:53], v[52:53], v[224:225], v[228:229]\n"
    "v_pk_fma_f32 v[54:55], v[54:55], v[226:227], v[230:231]\n"
    "v_pk_fma_f32 v[56:57], v[56:57], v[224:225], v[228:229]\n"
    "v_pk_fma_f32 v[58:59], v[58:59], v[226:227], v[230:231]\n"
    "v_pk_fma_f32 v[60:61], v[60:61], v[224:225], v[228:229]\n"
    "v_pk_fma_f32 v[62:63], v[62:63], v[226:227], v[230:231]\n"
    "v_cvt_pk_bf16_f32 v64, v32, v36\n"
    "v_cvt_pk_bf16_f32 v68, v33, v37\n"
    "v_cvt_pk_bf16_f32 v72, v34, v38\n"
    "v_cvt_pk_bf16_f32 v76, v35, v39\n"
    "v_cvt_pk_bf16_f32 v65, v40, v44\n"
    "v_cvt_pk_bf16_f32 v69, v41, v45\n"
    "v_cvt_pk_bf16_f32 v73, v42, v46\n"
    "v_cvt_pk_bf16_f32 v77, v43, v47\n"
    "v_cvt_pk_bf16_f32 v66, v48, v52\n"
    "v_cvt_pk_bf16_f32 v70, v49, v53\n"
    "v_cvt_pk_bf16_f32 v74, v50, v54\n"
    "v_cvt_pk_bf16_f32 v78, v51, v55\n"
    "v_cvt_pk_bf16_f32 v67, v56, v60\n"
    "ds_write_b128 v253, v[64:67] offset:1024\n"
    "v_cvt_pk_bf16_f32 v71, v57, v61\n"
    "v_cvt_pk_bf16_f32 v75, v58, v62\n"
    "v_cvt_pk_bf16_f32 v79, v59, v63\n"
    "v_mov_b32_e64 v64, v104\n"
    "v_mov_b32_e64 v65, v105\n"
    "v_mov_b32_e64 v66, v106\n"
    "ds_write_b128 v253, v[68:71] offset:4096\n"
    "v_mov_b32_e64 v67, v107\n"
    "v_mov_b32_e64 v68, v108\n"
    "v_mov_b32_e64 v69, v109\n"
    "v_mov_b32_e64 v70, v110\n"
    "v_mov_b32_e64 v71, v111\n"
    "s_waitcnt lgkmcnt(0)\n"
    "s_barrier\n"
    "ds_read_b128 v[144:147], v242 offset:0\n"
    "ds_read_b128 v[148:151], v242 offset:1024\n"
    "ds_read_b128 v[152:155], v242 offset:2048\n"
    "ds_read_b128 v[156:159], v242 offset:3072\n"
    "ds_read_b128 v[160:163], v242 offset:4096\n"
    "ds_read_b128 v[164:167], v242 offset:5120\n"
    "ds_read_b128 a[240:243], v244 offset:32768\n"
    "s_waitcnt lgkmcnt(6)\n"
    "v_mfma_f32_16x16x32_bf16 v[168:171], a[80:83], v[144:147], 0\n"
    "v_mfma_f32_16x16x32_bf16 v[172:175], a[84:87], v[144:147], 0\n"
    "s_waitcnt lgkmcnt(5)\n"
    "v_mfma_f32_16x16x32_bf16 v[168:171], a[128:131], v[148:151], v[168:171]\n"
    "s_waitcnt lgkmcnt(4)\n"
    "v_mfma_f32_16x16x32_bf16 v[168:171], a[48:51], v[152:155], v[168:171]\n"
    "s_waitcnt lgkmcnt(3)\n"
    "v_mfma_f32_16x16x32_bf16 v[172:175], a[132:135], v[148:151], v[172:175]\n"
    "v_mfma_f32_16x16x32_bf16 v[176:179], a[80:83], v[156:159], 0\n"
    "s_waitcnt lgkmcnt(1)\n"
    "v_mfma_f32_16x16x32_bf16 v[176:179], a[128:131], v[160:163], v[176:179]\n"
    "s_waitcnt lgkmcnt(0)\n"
    "v_mfma_f32_16x16x32_bf16 v[172:175], a[240:243], v[152:155], v[172:175]\n"
    "v_mfma_f32_16x16x32_bf16 v[176:179], a[48:51], v[164:167], v[176:179]\n"
    "v_mfma_f32_16x16x32_bf16 v[180:183], a[84:87], v[156:159], 0\n"
    "v_mfma_f32_16x16x32_bf16 v[180:183], a[132:135], v[160:163], v[180:183]\n"
    "v_mfma_f32_16x16x32_bf16 v[180:183], a[240:243], v[164:167], v[180:183]\n"
    "v_readfirstlane_b32 s66, v232\n"
    "s_nop 1\n"
    "s_cmp_lg_u32 s66, 2\n"
    "s_cbranch_scc1 98f\n"
    "ds_read_b128 a[240:243], v244 offset:36864\n"
    "v_lshlrev_b32_sdwa v184, 1, v168 dst_sel:DWORD dst_unused:UNUSED_PAD src0_sel:DWORD src1_sel:WORD_1\n"
    "ds_read_u16 v184, v184\n"
    "v_lshlrev_b32_sdwa v200, 1, v169 dst_sel:DWORD dst_unused:UNUSED_PAD src0_sel:DWORD src1_sel:WORD_1\n"
    "ds_read_u16_d16_hi v200, v200\n"
    "v_lshlrev_b32_sdwa v185, 1, v170 dst_sel:DWORD dst_unused:UNUSED_PAD src0_sel:DWORD src1_sel:WORD_1\n"
    "ds_read_u16 v185, v185\n"
    "v_lshlrev_b32_sdwa v201, 1, v171 dst_sel:DWORD dst_unused:UNUSED_PAD src0_sel:DWORD src1_sel:WORD_1\n"
    "v_mfma_f32_16x16x32_bf16 v[168:171], a[88:91], v[144:147], 0\n"
    "ds_read_u16_d16_hi v201, v201\n"
    "v_lshlrev_b32_sdwa v186, 1, v172 dst_sel:DWORD dst_unused:UNUSED_PAD src0_sel:DWORD src1_sel:WORD_1\n"
    "v_mfma_f32_16x16x32_bf16 v[168:171], a[136:139], v[148:151], v[168:171]\n"
    "ds_read_u16 v186, v186\n"
    "v_lshlrev_b32_sdwa v202, 1, v173 dst_sel:DWORD dst_unused:UNUSED_PAD src0_sel:DWORD src1_sel:WORD_1\n"
    "v_mfma_f32_16x16x32_bf16 v[168:171], a[52:55], v[152:155], v[168:171]\n"
    "ds_read_u16_d16_hi v202, v202\n"
    "v_lshlrev_b32_sdwa v187, 1, v174 dst_sel:DWORD dst_unused:UNUSED_PAD src0_sel:DWORD src1_sel:WORD_1\n"
    "v_lshlrev_b32_sdwa v203, 1, v175 dst_sel:DWORD dst_unused:UNUSED_PAD src0_sel:DWORD src1_sel:WORD_1\n"
    "v_mfma_f32_16x16x32_bf16 v[172:175], a[92:95], v[144:147], 0\n"
    "s_nop 0\n"
    "v_lshlrev_b32_sdwa v188, 1, v176 dst_sel:DWORD dst_unused:UNUSED_PAD src0_sel:DWORD src1_sel:WORD_1\n"
    "v_lshlrev_b32_sdwa v204, 1, v177 dst_sel:DWORD dst_unused:UNUSED_PAD src0_sel:DWORD src1_sel:WORD_1\n"
    "v_mfma_f32_16x16x32_bf16 v[172:175], a[140:143], v[148:151], v[172:175]\n"
    "s_waitcnt lgkmcnt(6)\n"
    "ds_read_u16 v187, v187\n"
    "v_lshlrev_b32_sdwa v189, 1, v178 dst_sel:DWORD dst_unused:UNUSED_PAD src0_sel:DWORD src1_sel:WORD_1\n"
    "v_mfma_f32_16x16x32_bf16 v[172:175], a[240:243], v[152:155], v[172:175]\n"
    "ds_read_u16_d16_hi v203, v203\n"
    "v_lshlrev_b32_sdwa v205, 1, v179 dst_sel:DWORD dst_unused:UNUSED_PAD src0_sel:DWORD src1_sel:WORD_1\n"
    "v_mfma_f32_16x16x32_bf16 v[176:179], a[88:91], v[156:159], 0\n"
    "v_lshlrev_b32_sdwa v190, 1, v180 dst_sel:DWORD dst_unused:UNUSED_PAD src0_sel:DWORD src1_sel:WORD_1\n"
    "v_lshlrev_b32_sdwa v206, 1, v181 dst_sel:DWORD dst_unused:UNUSED_PAD src0_sel:DWORD src1_sel:WORD_1\n"
    "v_mfma_f32_16x16x32_bf16 v[176:179], a[136:139], v[160:163], v[176:179]\n"
    "ds_read_u16 v188, v188\n"
    "v_lshlrev_b32_sdwa v191, 1, v182 dst_sel:DWORD dst_unused:UNUSED_PAD src0_sel:DWORD src1_sel:WORD_1\n"
    "v_mfma_f32_16x16x32_bf16 v[176:179], a[52:55], v[164:167], v[176:179]\n"
    "v_lshlrev_b32_sdwa v207, 1, v183 dst_sel:DWORD dst_unused:UNUSED_PAD src0_sel:DWORD src1_sel:WORD_1\n"
    "v_cvt_pk_f16_f32 v192, v168, v169\n"
    "v_mfma_f32_16x16x32_bf16 v[180:183], a[92:95], v[156:159], 0\n"
    "s_waitcnt lgkmcnt(7)\n"
    "ds_read_u16_d16_hi v204, v204\n"
    "v_or_b32_e32 v184, v184, v200\n"
    "v_mfma_f32_16x16x32_bf16 v[180:183], a[140:143], v[160:163], v[180:183]\n"
    "v_lshlrev_b32_sdwa v193, 1, v170 dst_sel:DWORD dst_unused:UNUSED_PAD src0_sel:DWORD src1_sel:WORD_1\n"
    "v_lshlrev_b32_sdwa v194, 1, v172 dst_sel:DWORD dst_unused:UNUSED_PAD src0_sel:DWORD src1_sel:WORD_1\n"
    "v_mfma_f32_16x16x32_bf16 v[180:183], a[240:243], v[164:167], v[180:183]\n"
    "ds_read_u16 v189, v189\n"
    "v_lshlrev_b32_sdwa v195, 1, v174 dst_sel:DWORD dst_unused:UNUSED_PAD src0_sel:DWORD src1_sel:WORD_1\n"
    "v_cvt_pk_f16_f32 v196, v176, v177\n"
    "v_lshlrev_b32_sdwa v197, 1, v178 dst_sel:DWORD dst_unused:UNUSED_PAD src0_sel:DWORD src1_sel:WORD_1\n"
    "s_waitcnt lgkmcnt(7)\n"
    "v_or_b32_e32 v185, v185, v201\n"
    "v_lshlrev_b32_sdwa v201, 1, v171 dst_sel:DWORD dst_unused:UNUSED_PAD src0_sel:DWORD src1_sel:WORD_1\n"
    "v_mfma_f32_16x16x32_bf16 v[168:171], a[96:99], v[144:147], 0\n"
    "ds_read_u16_d16_hi v205, v205\n"
    "v_lshlrev_b32_sdwa v198, 1, v180 dst_sel:DWORD dst_unused:UNUSED_PAD src0_sel:DWORD src1_sel:WORD_1\n"
    "v_lshlrev_b32_sdwa v199, 1, v182 dst_sel:DWORD dst_unused:UNUSED_PAD src0_sel:DWORD src1_sel:WORD_1\n"
    "v_pk_fma_f16 v232, v192, v192, s48\n"
    "v_pk_mul_f16 v232, v192, v232\n"
    "v_exp_f16_e64 v234, v232\n"
    "ds_read_u16 v190, v190\n"
    "ds_read_u16_d16_hi v206, v206\n"
    "s_waitcnt lgkmcnt(8)\n"
    "v_or_b32_e32 v186, v186, v202\n"
    "v_mfma_f32_16x16x32_bf16 v[168:171], a[144:147], v[148:151], v[168:171]\n"
    "v_lshlrev_b32_sdwa v202, 1, v173 dst_sel:DWORD dst_unused:UNUSED_PAD src0_sel:DWORD src1_sel:WORD_1\n"
    "v_exp_f16_sdwa v234, v232 dst_sel:WORD_1 dst_unused:UNUSED_PRESERVE src0_sel:WORD_1\n"
    "v_mfma_f32_16x16x32_bf16 v[168:171], a[56:59], v[152:155], v[168:171]\n"
    "ds_read_u16 v191, v191\n"
    "v_pk_fma_f16 v234, v234, s49, s49\n"
    "v_rcp_f16_e32 v232, v234\n"
    "s_waitcnt lgkmcnt(7)\n"
    "v_or_b32_e64 v187, v187, v203\n"
    "v_lshlrev_b32_sdwa v203, 1, v175 dst_sel:DWORD dst_unused:UNUSED_PAD src0_sel:DWORD src1_sel:WORD_1\n"
    "v_mfma_f32_16x16x32_bf16 v[172:175], a[100:103], v[144:147], 0\n"
    "ds_read_u16_d16_hi v207, v207\n"
    "ds_read_u16 v193, v193\n"
    "v_mfma_f32_16x16x32_bf16 v[172:175], a[148:151], v[148:151], v[172:175]\n"
    "s_nop 0\n"
    "v_rcp_f16_sdwa v232, v234 dst_sel:WORD_1 dst_unused:UNUSED_PRESERVE src0_sel:WORD_1\n"
    "v_mfma_f32_16x16x32_bf16 v[172:175], a[60:63], v[152:155], v[172:175]\n"
    "s_waitcnt lgkmcnt(7)\n"
    "ds_read_u16_d16_hi v201, v201\n"
    "v_or_b32_e64 v188, v188, v204\n"
    "v_fma_mix_f32 v234, v192, v232, 0 op_sel_hi:[1,1,0]\n"
    "v_fma_mix_f32 v232, v192, v232, 0 op_sel:[1,1,0] op_sel_hi:[1,1,0]\n"
    "v_cvt_pk_bf16_f32 v192, v234, v232\n"
    "v_pk_fma_f16 v232, v196, v196, s48\n"
    "v_pk_mul_f16 v232, v196, v232\n"
    "v_exp_f16_e64 v234, v232\n"
    "ds_read_u16 v194, v194\n"
    "v_exp_f16_sdwa v234, v232 dst_sel:WORD_1 dst_unused:UNUSED_PRESERVE src0_sel:WORD_1\n"
    "s_waitcnt lgkmcnt(7)\n"
    "v_or_b32_e32 v189, v189, v205\n"
    "v_lshlrev_b32_sdwa v205, 1, v179 dst_sel:DWORD dst_unused:UNUSED_PAD src0_sel:DWORD src1_sel:WORD_1\n"
    "v_mfma_f32_16x16x32_bf16 v[176:179], a[96:99], v[156:159], 0\n"
    "ds_read_u16_d16_hi v202, v202\n"
    "v_pk_fma_f16 v234, v234, s49, s49\n"
    "v_mfma_f32_16x16x32_bf16 v[176:179], a[144:147], v[160:163], v[176:179]\n"
    "ds_read_b128 v[236:239], v244 offset:0\n"
    "v_rcp_f16_e32 v232, v234\n"
    "v_mfma_f32_16x16x32_bf16 v[176:179], a[56:59], v[164:167], v[176:179]\n"
    "s_waitcnt lgkmcnt(7)\n"
    "ds_read_u16 v195, v195\n"
    "v_or_b32_e64 v190, v190, v206\n"
    "v_mfma_f32_16x16x32_bf16 v[96:99], a[0:3], v[184:187], 0\n"
    "ds_read_u16_d16_hi v203, v203\n"
    "v_lshlrev_b32_sdwa v206, 1, v181 dst_sel:DWORD dst_unused:UNUSED_PAD src0_sel:DWORD src1_sel:WORD_1\n"
    "v_mfma_f32_16x16x32_bf16 v[100:103], a[4:7], v[184:187], 0\n"
    "v_rcp_f16_sdwa v232, v234 dst_sel:WORD_1 dst_unused:UNUSED_PRESERVE src0_sel:WORD_1\n"
    "v_mfma_f32_16x16x32_bf16 v[112:115], a[8:11], v[184:187], 0\n"
    "ds_read_b128 a[244:247], v244 offset:2048\n"
    "v_fma_mix_f32 v234, v196, v232, 0 op_sel_hi:[1,1,0]\n"
    "s_waitcnt lgkmcnt(8)\n"
    "v_or_b32_e32 v191, v191, v207\n"
    "v_mfma_f32_16x16x32_bf16 v[116:119], a[12:15], v[184:187], 0\n"
    "ds_read_u16 v197, v197\n"
    "v_lshlrev_b32_sdwa v207, 1, v183 dst_sel:DWORD dst_unused:UNUSED_PAD src0_sel:DWORD src1_sel:WORD_1\n"
    "v_mfma_f32_16x16x32_bf16 v[180:183], a[100:103], v[156:159], 0\n"
    "ds_read_u16_d16_hi v205, v205\n"
    "v_fma_mix_f32 v232, v196, v232, 0 op_sel:[1,1,0] op_sel_hi:[1,1,0]\n"
    "v_mfma_f32_16x16x32_bf16 v[180:183], a[148:151], v[160:163], v[180:183]\n"
    "v_cvt_pk_bf16_f32 v196, v234, v232\n"
    "v_mfma_f32_16x16x32_bf16 v[180:183], a[60:63], v[164:167], v[180:183]\n"
    "s_waitcnt lgkmcnt(8)\n"
    "ds_read_u16 v198, v198\n"
    "v_or_b32_e32 v193, v193, v201\n"
    "v_mfma_f32_16x16x32_bf16 v[104:107], a[0:3], v[188:191], 0\n"
    "v_lshlrev_b32_sdwa v201, 1, v171 dst_sel:DWORD dst_unused:UNUSED_PAD src0_sel:DWORD src1_sel:WORD_1\n"
    "v_mfma_f32_16x16x32_bf16 v[108:111], a[4:7], v[188:191], 0\n"
    "ds_read_u16_d16_hi v206, v206\n"
    "v_mfma_f32_16x16x32_bf16 v[120:123], a[8:11], v[188:191], 0\n"
    "s_waitcnt lgkmcnt(7)\n"
    "v_or_b32_e32 v194, v194, v202\n"
    "v_lshlrev_b32_sdwa v202, 1, v173 dst_sel:DWORD dst_unused:UNUSED_PAD src0_sel:DWORD src1_sel:WORD_1\n"
    "v_mfma_f32_16x16x32_bf16 v[128:131], v[236:239], v[184:187], 0\n"
    "ds_read_u16 v199, v199\n"
    "v_mfma_f32_16x16x32_bf16 v[124:127], a[12:15], v[188:191], 0\n"
    "ds_read_u16_d16_hi v207, v207\n"
    "v_mfma_f32_16x16x32_bf16 v[136:139], v[236:239], v[188:191], 0\n"
    "ds_read_u16_d16_hi v201, v201\n"
    "ds_read_u16_d16_hi v202, v202\n"
    "ds_read_b128 v[236:239], v244 offset:4096\n"
    "ds_read_b128 a[240:243], v244 offset:34816\n"
    "s_waitcnt lgkmcnt(10)\n"
    "v_mfma_f32_16x16x32_bf16 v[132:135], a[244:247], v[184:187], 0\n"
    "v_or_b32_e32 v195, v195, v203\n"
    "v_cvt_pk_f16_f32 v184, v168, v169\n"
    "v_mfma_f32_16x16x32_bf16 v[140:143], a[244:247], v[188:191], 0\n"
    "v_lshlrev_b32_sdwa v185, 1, v170 dst_sel:DWORD dst_unused:UNUSED_PAD src0_sel:DWORD src1_sel:WORD_1\n"
    "v_lshlrev_b32_sdwa v186, 1, v172 dst_sel:DWORD dst_unused:UNUSED_PAD src0_sel:DWORD src1_sel:WORD_1\n"
    "v_mfma_f32_16x16x32_bf16 v[168:171], a[104:107], v[144:147], 0\n"
    "v_lshlrev_b32_sdwa v187, 1, v174 dst_sel:DWORD dst_unused:UNUSED_PAD src0_sel:DWORD src1_sel:WORD_1\n"
    "v_lshlrev_b32_sdwa v203, 1, v175 dst_sel:DWORD dst_unused:UNUSED_PAD src0_sel:DWORD src1_sel:WORD_1\n"
    "v_mfma_f32_16x16x32_bf16 v[168:171], a[152:155], v[148:151], v[168:171]\n"
    "v_cvt_pk_f16_f32 v188, v176, v177\n"
    "v_lshlrev_b32_sdwa v189, 1, v178 dst_sel:DWORD dst_unused:UNUSED_PAD src0_sel:DWORD src1_sel:WORD_1\n"
    "v_mfma_f32_16x16x32_bf16 v[168:171], a[64:67], v[152:155], v[168:171]\n"
    "v_lshlrev_b32_sdwa v190, 1, v180 dst_sel:DWORD dst_unused:UNUSED_PAD src0_sel:DWORD src1_sel:WORD_1\n"
    "v_lshlrev_b32_sdwa v191, 1, v182 dst_sel:DWORD dst_unused:UNUSED_PAD src0_sel:DWORD src1_sel:WORD_1\n"
    "v_mfma_f32_16x16x32_bf16 v[172:175], a[108:111], v[144:147], 0\n"
    "s_waitcnt lgkmcnt(8)\n"
    "ds_read_u16 v185, v185\n"
    "v_or_b32_e32 v197, v197, v205\n"
    "v_mfma_f32_16x16x32_bf16 v[172:175], a[156:159], v[148:151], v[172:175]\n"
    "v_lshlrev_b32_sdwa v205, 1, v179 dst_sel:DWORD dst_unused:UNUSED_PAD src0_sel:DWORD src1_sel:WORD_1\n"
    "v_pk_fma_f16 v232, v184, v184, s48\n"
    "v_mfma_f32_16x16x32_bf16 v[172:175], a[68:71], v[152:155], v[172:175]\n"
    "ds_read_u16 v186, v186\n"
    "v_pk_mul_f16 v232, v184, v232\n"
    "v_mfma_f32_16x16x32_bf16 v[176:179], a[104:107], v[156:159], 0\n"
    "s_waitcnt lgkmcnt(8)\n"
    "ds_read_u16 v187, v187\n"
    "v_or_b32_e32 v198, v198, v206\n"
    "v_mfma_f32_16x16x32_bf16 v[176:179], a[152:155], v[160:163], v[176:179]\n"
    "v_lshlrev_b32_sdwa v206, 1, v181 dst_sel:DWORD dst_unused:UNUSED_PAD src0_sel:DWORD src1_sel:WORD_1\n"
    "v_exp_f16_e64 v234, v232\n"
    "v_mfma_f32_16x16x32_bf16 v[176:179], a[64:67], v[164:167], v[176:179]\n"
    "ds_read_u16_d16_hi v203, v203\n"
    "v_exp_f16_sdwa v234, v232 dst_sel:WORD_1 dst_unused:UNUSED_PRESERVE src0_sel:WORD_1\n"
    "v_mfma_f32_16x16x32_bf16 v[96:99], a[16:19], v[192:195], v[96:99]\n"
    "s_waitcnt lgkmcnt(8)\n"
    "ds_read_b128 a[244:247], v244 offset:6144\n"
    "v_or_b32_e32 v199, v199, v207\n"
    "v_mfma_f32_16x16x32_bf16 v[100:103], a[20:23], v[192:195], v[100:103]\n"
    "ds_read_u16 v189, v189\n"
    "v_lshlrev_b32_sdwa v207, 1, v183 dst_sel:DWORD dst_unused:UNUSED_PAD src0_sel:DWORD src1_sel:WORD_1\n"
    "v_mfma_f32_16x16x32_bf16 v[180:183], a[108:111], v[156:159], 0\n"
    "v_pk_fma_f16 v234, v234, s49, s49\n"
    "v_lshlrev_b32_sdwa v200, 1, v169 dst_sel:DWORD dst_unused:UNUSED_PAD src0_sel:DWORD src1_sel:WORD_1\n"
    "v_mfma_f32_16x16x32_bf16 v[180:183], a[156:159], v[160:163], v[180:183]\n"
    "s_waitcnt lgkmcnt(7)\n"
    "ds_read_u16_d16_hi v205, v205\n"
    "v_rcp_f16_e32 v232, v234\n"
    "v_mfma_f32_16x16x32_bf16 v[180:183], a[68:71], v[164:167], v[180:183]\n"
    "v_rcp_f16_sdwa v232, v234 dst_sel:WORD_1 dst_unused:UNUSED_PRESERVE src0_sel:WORD_1\n"
    "v_mfma_f32_16x16x32_bf16 v[112:115], a[24:27], v[192:195], v[112:115]\n"
    "ds_read_u16 v190, v190\n"
    "v_lshlrev_b32_sdwa v204, 1, v177 dst_sel:DWORD dst_unused:UNUSED_PAD src0_sel:DWORD src1_sel:WORD_1\n"
    "v_mfma_f32_16x16x32_bf16 v[116:119], a[28:31], v[192:195], v[116:119]\n"
    "ds_read_u16_d16_hi v206, v206\n"
    "v_fma_mix_f32 v234, v184, v232, 0 op_sel_hi:[1,1,0]\n"
    "v_mfma_f32_16x16x32_bf16 v[128:131], v[236:239], v[192:195], v[128:131]\n"
    "s_waitcnt lgkmcnt(8)\n"
    "v_or_b32_e32 v185, v185, v201\n"
    "v_lshlrev_b32_sdwa v201, 1, v171 dst_sel:DWORD dst_unused:UNUSED_PAD src0_sel:DWORD src1_sel:WORD_1\n"
    "v_mfma_f32_16x16x32_bf16 v[104:107], a[16:19], v[196:199], v[104:107]\n"
    "ds_read_u16 v191, v191\n"
    "v_fma_mix_f32 v232, v184, v232, 0 op_sel:[1,1,0] op_sel_hi:[1,1,0]\n"
    "v_mfma_f32_16x16x32_bf16 v[108:111], a[20:23], v[196:199], v[108:111]\n"
    "s_waitcnt lgkmcnt(8)\n"
    "v_or_b32_e32 v186, v186, v202\n"
    "v_lshlrev_b32_sdwa v202, 1, v173 dst_sel:DWORD dst_unused:UNUSED_PAD src0_sel:DWORD src1_sel:WORD_1\n"
    "v_mfma_f32_16x16x32_bf16 v[120:123], a[24:27], v[196:199], v[120:123]\n"
    "buffer_load_dwordx4 v[0:3], v246, s[40:43], s60 offen offset:0\n"
    "buffer_load_dwordx4 v[32:35], v246, s[40:43], s60 offen offset:64\n"
    "ds_read_u16_d16_hi v207, v207\n"
    "v_mfma_f32_16x16x32_bf16 v[124:127], a[28:31], v[196:199], v[124:127]\n"
    "ds_read_u16_d16_hi v200, v200\n"
    "v_cvt_pk_bf16_f32 v184, v234, v232\n"
    "v_mfma_f32_16x16x32_bf16 v[136:139], v[236:239], v[196:199], v[136:139]\n"
    "s_waitcnt lgkmcnt(7)\n"
    "v_or_b32_e32 v187, v187, v203\n"
    "v_lshlrev_b32_sdwa v203, 1, v175 dst_sel:DWORD dst_unused:UNUSED_PAD src0_sel:DWORD src1_sel:WORD_1\n"
    "v_mfma_f32_16x16x32_bf16 v[132:135], a[244:247], v[192:195], v[132:135]\n"
    "ds_read_u16_d16_hi v201, v201\n"
    "v_lshlrev_b32_sdwa v192, 1, v168 dst_sel:DWORD dst_unused:UNUSED_PAD src0_sel:DWORD src1_sel:WORD_1\n"
    "v_mfma_f32_16x16x32_bf16 v[140:143], a[244:247], v[196:199], v[140:143]\n"
    "v_lshlrev_b32_sdwa v193, 1, v170 dst_sel:DWORD dst_unused:UNUSED_PAD src0_sel:DWORD src1_sel:WORD_1\n"
    "v_lshlrev_b32_sdwa v194, 1, v172 dst_sel:DWORD dst_unused:UNUSED_PAD src0_sel:DWORD src1_sel:WORD_1\n"
    "v_mfma_f32_16x16x32_bf16 v[168:171], a[112:115], v[144:147], 0\n"
    "ds_read_u16 v192, v192\n"
    "v_lshlrev_b32_sdwa v195, 1, v174 dst_sel:DWORD dst_unused:UNUSED_PAD src0_sel:DWORD src1_sel:WORD_1\n"
    "v_mfma_f32_16x16x32_bf16 v[168:171], a[160:163], v[148:151], v[168:171]\n"
    "v_lshlrev_b32_sdwa v196, 1, v176 dst_sel:DWORD dst_unused:UNUSED_PAD src0_sel:DWORD src1_sel:WORD_1\n"
    "v_lshlrev_b32_sdwa v197, 1, v178 dst_sel:DWORD dst_unused:UNUSED_PAD src0_sel:DWORD src1_sel:WORD_1\n"
    "v_mfma_f32_16x16x32_bf16 v[168:171], a[72:75], v[152:155], v[168:171]\n"
    "s_waitcnt lgkmcnt(7)\n"
    "ds_read_u16 v193, v193\n"
    "v_or_b32_e32 v189, v189, v205\n"
    "v_mfma_f32_16x16x32_bf16 v[172:175], a[116:119], v[144:147], 0\n"
    "ds_read_u16 v194, v194\n"
    "v_lshlrev_b32_sdwa v205, 1, v179 dst_sel:DWORD dst_unused:UNUSED_PAD src0_sel:DWORD src1_sel:WORD_1\n"
    "v_mfma_f32_16x16x32_bf16 v[172:175], a[164:167], v[148:151], v[172:175]\n"
    "s_nop 0\n"
    "v_lshlrev_b32_sdwa v198, 1, v180 dst_sel:DWORD dst_unused:UNUSED_PAD src0_sel:DWORD src1_sel:WORD_1\n"
    "v_lshlrev_b32_sdwa v199, 1, v182 dst_sel:DWORD dst_unused:UNUSED_PAD src0_sel:DWORD src1_sel:WORD_1\n"
    "v_mfma_f32_16x16x32_bf16 v[172:175], a[240:243], v[152:155], v[172:175]\n"
    "s_waitcnt lgkmcnt(7)\n"
    "buffer_load_dwordx4 v[20:23], v247, s[40:43], s60 offen offset:0\n"
    "buffer_load_dwordx4 v[52:55], v247, s[40:43], s60 offen offset:64\n"
    "ds_read_b128 v[236:239], v244 offset:8192\n"
    "v_mfma_f32_16x16x32_bf16 v[176:179], a[112:115], v[156:159], 0\n"
    "ds_read_u16_d16_hi v202, v202\n"
    "v_or_b32_e64 v190, v190, v206\n"
    "v_mfma_f32_16x16x32_bf16 v[176:179], a[160:163], v[160:163], v[176:179]\n"
    "v_lshlrev_b32_sdwa v206, 1, v181 dst_sel:DWORD dst_unused:UNUSED_PAD src0_sel:DWORD src1_sel:WORD_1\n"
    "v_pk_fma_f16 v232, v188, v188, s48\n"
    "v_mfma_f32_16x16x32_bf16 v[176:179], a[72:75], v[164:167], v[176:179]\n"
    "s_waitcnt lgkmcnt(7)\n"
    "ds_read_u16 v195, v195\n"
    "v_or_b32_e32 v191, v191, v207\n"
    "v_lshlrev_b32_sdwa v207, 1, v183 dst_sel:DWORD dst_unused:UNUSED_PAD src0_sel:DWORD src1_sel:WORD_1\n"
    "v_mfma_f32_16x16x32_bf16 v[180:183], a[116:119], v[156:159], 0\n"
    "ds_read_b128 a[244:247], v244 offset:10240\n"
    "v_pk_mul_f16 v232, v188, v232\n"
    "v_mfma_f32_16x16x32_bf16 v[180:183], a[164:167], v[160:163], v[180:183]\n"
    "ds_read_u16_d16_hi v203, v203\n"
    "v_exp_f16_e64 v234, v232\n"
    "v_mfma_f32_16x16x32_bf16 v[180:183], a[240:243], v[164:167], v[180:183]\n"
    "v_exp_f16_sdwa v234, v232 dst_sel:WORD_1 dst_unused:UNUSED_PRESERVE src0_sel:WORD_1\n"
    "v_mfma_f32_16x16x32_bf16 v[96:99], a[32:35], v[184:187], v[96:99]\n"
    "ds_read_u16 v196, v196\n"
    "v_pk_fma_f16 v234, v234, s49, s49\n"
    "v_mfma_f32_16x16x32_bf16 v[100:103], a[36:39], v[184:187], v[100:103]\n"
    "s_waitcnt lgkmcnt(8)\n"
    "ds_read_u16_d16_hi v204, v204\n"
    "v_rcp_f16_e32 v232, v234\n"
    "v_mfma_f32_16x16x32_bf16 v[112:115], a[40:43], v[184:187], v[112:115]\n"
    "buffer_load_dwordx4 v[4:7], v246, s[40:43], s61 offen offset:0\n"
    "buffer_load_dwordx4 v[36:39], v246, s[40:43], s61 offen offset:64\n"
    "ds_read_u16 v197, v197\n"
    "v_rcp_f16_sdwa v232, v234 dst_sel:WORD_1 dst_unused:UNUSED_PRESERVE src0_sel:WORD_1\n"
    "v_mfma_f32_16x16x32_bf16 v[116:119], a[44:47], v[184:187], v[116:119]\n"
    "s_waitcnt lgkmcnt(7)\n"
    "v_fma_mix_f32 v234, v188, v232, 0 op_sel_hi:[1,1,0]\n"
    "v_fma_mix_f32 v232, v188, v232, 0 op_sel:[1,1,0] op_sel_hi:[1,1,0]\n"
    "v_mfma_f32_16x16x32_bf16 v[128:131], v[236:239], v[184:187], v[128:131]\n"
    "s_nop 0\n"
    "ds_read_u16_d16_hi v205, v205\n"
    "v_cvt_pk_bf16_f32 v188, v234, v232\n"
    "v_or_b32_e64 v192, v192, v200\n"
    "v_or_b32_e64 v193, v193, v201\n"
    "v_mfma_f32_16x16x32_bf16 v[104:107], a[32:35], v[188:191], v[104:107]\n"
    "ds_read_u16 v198, v198\n"
    "v_lshlrev_b32_sdwa v201, 1, v171 dst_sel:DWORD dst_unused:UNUSED_PAD src0_sel:DWORD src1_sel:WORD_1\n"
    "v_mfma_f32_16x16x32_bf16 v[108:111], a[36:39], v[188:191], v[108:111]\n"
    "s_waitcnt lgkmcnt(8)\n"
    "v_or_b32_e32 v194, v194, v202\n"
    "v_lshlrev_b32_sdwa v202, 1, v173 dst_sel:DWORD dst_unused:UNUSED_PAD src0_sel:DWORD src1_sel:WORD_1\n"
    "v_mfma_f32_16x16x32_bf16 v[120:123], a[40:43], v[188:191], v[120:123]\n"
    "s_waitcnt lgkmcnt(6)\n"
    "ds_read_u16_d16_hi v206, v206\n"
    "v_mfma_f32_16x16x32_bf16 v[132:135], a[244:247], v[184:187], v[132:135]\n"
    "s_nop 0\n"
    "v_cvt_pk_f16_f32 v184, v168, v169\n"
    "v_lshlrev_b32_sdwa v185, 1, v170 dst_sel:DWORD dst_unused:UNUSED_PAD src0_sel:DWORD src1_sel:WORD_1\n"
    "v_mfma_f32_16x16x32_bf16 v[124:127], a[44:47], v[188:191], v[124:127]\n"
    "s_nop 0\n"
    "ds_read_u16 v199, v199\n"
    "v_lshlrev_b32_sdwa v186, 1, v172 dst_sel:DWORD dst_unused:UNUSED_PAD src0_sel:DWORD src1_sel:WORD_1\n"
    "v_mfma_f32_16x16x32_bf16 v[136:139], v[236:239], v[188:191], v[136:139]\n"
    "s_waitcnt lgkmcnt(7)\n"
    "ds_read_u16_d16_hi v207, v207\n"
    "v_lshlrev_b32_sdwa v187, 1, v174 dst_sel:DWORD dst_unused:UNUSED_PAD src0_sel:DWORD src1_sel:WORD_1\n"
    "v_mfma_f32_16x16x32_bf16 v[140:143], a[244:247], v[188:191], v[140:143]\n"
    "buffer_load_dwordx4 v[24:27], v247, s[40:43], s61 offen offset:0\n"
    "buffer_load_dwordx4 v[56:59], v247, s[40:43], s61 offen offset:64\n"
    "v_mfma_f32_16x16x32_bf16 v[168:171], a[120:123], v[144:147], 0\n"
    "ds_read_b128 a[240:243], v244 offset:38912\n"
    "v_cvt_pk_f16_f32 v188, v176, v177\n"
    "v_mfma_f32_16x16x32_bf16 v[168:171], a[168:171], v[148:151], v[168:171]\n"
    "ds_read_u16 v185, v185\n"
    "v_lshlrev_b32_sdwa v189, 1, v178 dst_sel:DWORD dst_unused:UNUSED_PAD src0_sel:DWORD src1_sel:WORD_1\n"
    "v_mfma_f32_16x16x32_bf16 v[168:171], a[76:79], v[152:155], v[168:171]\n"
    "s_waitcnt lgkmcnt(8)\n"
    "v_lshlrev_b32_sdwa v190, 1, v180 dst_sel:DWORD dst_unused:UNUSED_PAD src0_sel:DWORD src1_sel:WORD_1\n"
    "v_lshlrev_b32_sdwa v191, 1, v182 dst_sel:DWORD dst_unused:UNUSED_PAD src0_sel:DWORD src1_sel:WORD_1\n"
    "v_or_b32_e32 v195, v195, v203\n"
    "v_lshlrev_b32_sdwa v203, 1, v175 dst_sel:DWORD dst_unused:UNUSED_PAD src0_sel:DWORD src1_sel:WORD_1\n"
    "v_mfma_f32_16x16x32_bf16 v[172:175], a[124:127], v[144:147], 0\n"
    "ds_read_u16_d16_hi v201, v201\n"
    "v_or_b32_e64 v196, v196, v204\n"
    "v_mfma_f32_16x16x32_bf16 v[172:175], a[172:175], v[148:151], v[172:175]\n"
    "ds_read_u16 v186, v186\n"
    "v_pk_fma_f16 v232, v184, v184, s48\n"
    "s_waitcnt lgkmcnt(8)\n"
    "v_or_b32_e32 v197, v197, v205\n"
    "v_lshlrev_b32_sdwa v205, 1, v179 dst_sel:DWORD dst_unused:UNUSED_PAD src0_sel:DWORD src1_sel:WORD_1\n"
    "v_mfma_f32_16x16x32_bf16 v[176:179], a[120:123], v[156:159], 0\n"
    "ds_read_u16_d16_hi v202, v202\n"
    "v_pk_mul_f16 v232, v184, v232\n"
    "v_mfma_f32_16x16x32_bf16 v[176:179], a[168:171], v[160:163], v[176:179]\n"
    "v_exp_f16_e64 v234, v232\n"
    "v_mfma_f32_16x16x32_bf16 v[176:179], a[76:79], v[164:167], v[176:179]\n"
    "ds_read_b128 v[236:239], v244 offset:12288\n"
    "s_waitcnt lgkmcnt(8)\n"
    "v_or_b32_e32 v198, v198, v206\n"
    "v_mfma_f32_16x16x32_bf16 v[96:99], a[176:179], v[192:195], v[96:99]\n"
    "buffer_load_dwordx4 v[8:11], v246, s[40:43], s62 offen offset:0\n"
    "buffer_load_dwordx4 v[40:43], v246, s[40:43], s62 offen offset:64\n"
    "ds_read_u16 v187, v187\n"
    "v_mfma_f32_16x16x32_bf16 v[100:103], a[180:183], v[192:195], v[100:103]\n"
    "ds_read_u16_d16_hi v203, v203\n"
    "v_lshlrev_b32_sdwa v206, 1, v181 dst_sel:DWORD dst_unused:UNUSED_PAD src0_sel:DWORD src1_sel:WORD_1\n"
    "v_mfma_f32_16x16x32_bf16 v[112:115], a[184:187], v[192:195], v[112:115]\n"
    "s_waitcnt lgkmcnt(7)\n"
    "v_or_b32_e32 v199, v199, v207\n"
    "v_lshlrev_b32_sdwa v207, 1, v183 dst_sel:DWORD dst_unused:UNUSED_PAD src0_sel:DWORD src1_sel:WORD_1\n"
    "v_mfma_f32_16x16x32_bf16 v[172:175], a[240:243], v[152:155], v[172:175]\n"
    "ds_read_b128 a[244:247], v244 offset:14336\n"
    "v_exp_f16_sdwa v234, v232 dst_sel:WORD_1 dst_unused:UNUSED_PRESERVE src0_sel:WORD_1\n"
    "v_mfma_f32_16x16x32_bf16 v[180:183], a[124:127], v[156:159], 0\n"
    "ds_read_u16 v189, v189\n"
    "v_pk_fma_f16 v234, v234, s49, s49\n"
    "v_mfma_f32_16x16x32_bf16 v[180:183], a[172:175], v[160:163], v[180:183]\n"
    "ds_read_u16_d16_hi v205, v205\n"
    "v_rcp_f16_e32 v232, v234\n"
    "v_mfma_f32_16x16x32_bf16 v[180:183], a[240:243], v[164:167], v[180:183]\n"
    "v_rcp_f16_sdwa v232, v234 dst_sel:WORD_1 dst_unused:UNUSED_PRESERVE src0_sel:WORD_1\n"
    "v_mfma_f32_16x16x32_bf16 v[116:119], a[188:191], v[192:195], v[116:119]\n"
    "s_waitcnt lgkmcnt(8)\n"
    "ds_read_u16 v190, v190\n"
    "v_or_b32_e64 v185, v185, v201\n"
    "v_mfma_f32_16x16x32_bf16 v[104:107], a[176:179], v[196:199], v[104:107]\n"
    "v_lshlrev_b32_sdwa v201, 1, v171 dst_sel:DWORD dst_unused:UNUSED_PAD src0_sel:DWORD src1_sel:WORD_1\n"
    "v_fma_mix_f32 v234, v184, v232, 0 op_sel_hi:[1,1,0]\n"
    "v_mfma_f32_16x16x32_bf16 v[108:111], a[180:183], v[196:199], v[108:111]\n"
    "buffer_load_dwordx4 v[12:15], v246, s[40:43], s63 offen offset:0\n"
    "buffer_load_dwordx4 v[44:47], v246, s[40:43], s63 offen offset:64\n"
    "ds_read_b128 v[144:147], v243 offset:0\n"
    "v_mfma_f32_16x16x32_bf16 v[120:123], a[184:187], v[196:199], v[120:123]\n"
    "s_waitcnt lgkmcnt(7)\n"
    "ds_read_u16_d16_hi v206, v206\n"
    "v_or_b32_e32 v186, v186, v202\n"
    "v_mfma_f32_16x16x32_bf16 v[128:131], v[236:239], v[192:195], v[128:131]\n"
    "ds_read_b128 v[148:151], v243 offset:1024\n"
    "v_lshlrev_b32_sdwa v202, 1, v173 dst_sel:DWORD dst_unused:UNUSED_PAD src0_sel:DWORD src1_sel:WORD_1\n"
    "v_mfma_f32_16x16x32_bf16 v[124:127], a[188:191], v[196:199], v[124:127]\n"
    "ds_read_u16 v191, v191\n"
    "v_fma_mix_f32 v232, v184, v232, 0 op_sel:[1,1,0] op_sel_hi:[1,1,0]\n"
    "v_mfma_f32_16x16x32_bf16 v[136:139], v[236:239], v[196:199], v[136:139]\n"
    "v_cvt_pk_bf16_f32 v184, v234, v232\n"
    "v_pk_fma_f16 v232, v188, v188, s48\n"
    "ds_read_b128 v[152:155], v243 offset:2048\n"
    "s_waitcnt lgkmcnt(8)\n"
    "v_mfma_f32_16x16x32_bf16 v[132:135], a[244:247], v[192:195], v[132:135]\n"
    "s_nop 0\n"
    "ds_read_u16_d16_hi v207, v207\n"
    "v_or_b32_e64 v187, v187, v203\n"
    "v_mfma_f32_16x16x32_bf16 v[140:143], a[244:247], v[196:199], v[140:143]\n"
    "v_cvt_pk_f16_f32 v192, v168, v169\n"
    "ds_read_b128 v[156:159], v243 offset:3072\n"
    "v_lshlrev_b32_sdwa v193, 1, v170 dst_sel:DWORD dst_unused:UNUSED_PAD src0_sel:DWORD src1_sel:WORD_1\n"
    "ds_read_b128 v[160:163], v243 offset:4096\n"
    "v_lshlrev_b32_sdwa v194, 1, v172 dst_sel:DWORD dst_unused:UNUSED_PAD src0_sel:DWORD src1_sel:WORD_1\n"
    "ds_read_b128 v[164:167], v243 offset:5120\n"
    "buffer_load_dwordx4 v[16:19], v246, s[40:43], s52 offen offset:0\n"
    "buffer_load_dwordx4 v[48:51], v246, s[40:43], s52 offen offset:64\n"
    "ds_read_b128 a[240:243], v244 offset:32768\n"
    "s_waitcnt lgkmcnt(11)\n"
    "v_or_b32_e32 v189, v189, v205\n"
    "s_waitcnt lgkmcnt(9)\n"
    "v_mfma_f32_16x16x32_bf16 v[168:171], a[80:83], v[144:147], 0\n"
    "s_nop 0\n"
    "ds_read_u16 v193, v193\n"
    "v_lshlrev_b32_sdwa v195, 1, v174 dst_sel:DWORD dst_unused:UNUSED_PAD src0_sel:DWORD src1_sel:WORD_1\n"
    "v_lshlrev_b32_sdwa v203, 1, v175 dst_sel:DWORD dst_unused:UNUSED_PAD src0_sel:DWORD src1_sel:WORD_1\n"
    "v_mfma_f32_16x16x32_bf16 v[172:175], a[84:87], v[144:147], 0\n"
    "ds_read_u16_d16_hi v201, v201\n"
    "v_cvt_pk_f16_f32 v196, v176, v177\n"
    "s_waitcnt lgkmcnt(9)\n"
    "v_mfma_f32_16x16x32_bf16 v[168:171], a[128:131], v[148:151], v[168:171]\n"
    "v_or_b32_e32 v190, v190, v206\n"
    "v_lshlrev_b32_sdwa v197, 1, v178 dst_sel:DWORD dst_unused:UNUSED_PAD src0_sel:DWORD src1_sel:WORD_1\n"
    "v_mfma_f32_16x16x32_bf16 v[172:175], a[132:135], v[148:151], v[172:175]\n"
    "ds_read_u16 v194, v194\n"
    "v_lshlrev_b32_sdwa v205, 1, v179 dst_sel:DWORD dst_unused:UNUSED_PAD src0_sel:DWORD src1_sel:WORD_1\n"
    "v_lshlrev_b32_sdwa v198, 1, v180 dst_sel:DWORD dst_unused:UNUSED_PAD src0_sel:DWORD src1_sel:WORD_1\n"
    "v_lshlrev_b32_sdwa v206, 1, v181 dst_sel:DWORD dst_unused:UNUSED_PAD src0_sel:DWORD src1_sel:WORD_1\n"
    "s_waitcnt lgkmcnt(8)\n"
    "v_mfma_f32_16x16x32_bf16 v[168:171], a[48:51], v[152:155], v[168:171]\n"
    "s_nop 0\n"
    "ds_read_u16_d16_hi v202, v202\n"
    "v_lshlrev_b32_sdwa v199, 1, v182 dst_sel:DWORD dst_unused:UNUSED_PAD src0_sel:DWORD src1_sel:WORD_1\n"
    "ds_read_b128 v[236:239], v244 offset:16384\n"
    "ds_read_u16 v195, v195\n"
    "ds_read_u16_d16_hi v203, v203\n"
    "s_waitcnt lgkmcnt(10)\n"
    "v_mfma_f32_16x16x32_bf16 v[176:179], a[80:83], v[156:159], 0\n"
    "v_or_b32_e32 v191, v191, v207\n"
    "v_lshlrev_b32_sdwa v207, 1, v183 dst_sel:DWORD dst_unused:UNUSED_PAD src0_sel:DWORD src1_sel:WORD_1\n"
    "s_waitcnt lgkmcnt(9)\n"
    "v_mfma_f32_16x16x32_bf16 v[176:179], a[128:131], v[160:163], v[176:179]\n"
    "s_nop 0\n"
    "v_lshlrev_b32_sdwa v200, 1, v169 dst_sel:DWORD dst_unused:UNUSED_PAD src0_sel:DWORD src1_sel:WORD_1\n"
    "s_waitcnt lgkmcnt(7)\n"
    "v_mfma_f32_16x16x32_bf16 v[172:175], a[240:243], v[152:155], v[172:175]\n"
    "s_branch 2f\n"
    ".p2align 6\n"
    "1:\n"
    ".Lreplk_loop:\n"
    "v_lshlrev_b32_sdwa v184, 1, v168 dst_sel:DWORD dst_unused:UNUSED_PAD src0_sel:DWORD src1_sel:WORD_1\n"
    "v_lshlrev_b32_sdwa v200, 1, v169 dst_sel:DWORD dst_unused:UNUSED_PAD src0_sel:DWORD src1_sel:WORD_1\n"
    "v_lshlrev_b32_sdwa v185, 1, v170 dst_sel:DWORD dst_unused:UNUSED_PAD src0_sel:DWORD src1_sel:WORD_1\n"
    "v_lshlrev_b32_sdwa v201, 1, v171 dst_sel:DWORD dst_unused:UNUSED_PAD src0_sel:DWORD src1_sel:WORD_1\n"
    "v_mfma_f32_16x16x32_bf16 v[140:143], a[244:247], v[188:191], v[140:143]\n"
    "s_waitcnt vmcnt(2)\n"
    "ds_read_b128 a[240:243], v244 offset:36864\n"
    "ds_read_u16 v184, v184\n"
    "v_mfma_f32_16x16x32_bf16 v[168:171], a[88:91], v[144:147], 0\n"
    "s_nop 0\n"
    "ds_read_u16_d16_hi v200, v200\n"
    "ds_read_u16 v185, v185\n"
    "v_mfma_f32_16x16x32_bf16 v[168:171], a[136:139], v[148:151], v[168:171]\n"
    "buffer_load_dwordx4 v[0:3], v246, s[40:43], s60 offen offset:0\n"
    "buffer_load_dwordx4 v[32:35], v246, s[40:43], s60 offen offset:64\n"
    "ds_read_u16_d16_hi v201, v201\n"
    "v_mfma_f32_16x16x32_bf16 v[168:171], a[52:55], v[152:155], v[168:171]\n"
    "v_lshlrev_b32_sdwa v186, 1, v172 dst_sel:DWORD dst_unused:UNUSED_PAD src0_sel:DWORD src1_sel:WORD_1\n"
    "v_lshlrev_b32_sdwa v202, 1, v173 dst_sel:DWORD dst_unused:UNUSED_PAD src0_sel:DWORD src1_sel:WORD_1\n"
    "v_smfmac_f32_32x32x32_bf16 v[128:143], a[248:251], a[224:231], v241\n"
    "ds_read_u16 v186, v186\n"
    "v_lshlrev_b32_sdwa v187, 1, v174 dst_sel:DWORD dst_unused:UNUSED_PAD src0_sel:DWORD src1_sel:WORD_1\n"
    "v_lshlrev_b32_sdwa v203, 1, v175 dst_sel:DWORD dst_unused:UNUSED_PAD src0_sel:DWORD src1_sel:WORD_1\n"
    "v_lshlrev_b32_sdwa v188, 1, v176 dst_sel:DWORD dst_unused:UNUSED_PAD src0_sel:DWORD src1_sel:WORD_1\n"
    "v_lshlrev_b32_sdwa v204, 1, v177 dst_sel:DWORD dst_unused:UNUSED_PAD src0_sel:DWORD src1_sel:WORD_1\n"
    "v_mfma_f32_16x16x32_bf16 v[172:175], a[92:95], v[144:147], 0\n"
    "ds_read_b128 v[236:239], v244 offset:20480\n"
    "v_lshlrev_b32_sdwa v189, 1, v178 dst_sel:DWORD dst_unused:UNUSED_PAD src0_sel:DWORD src1_sel:WORD_1\n"
    "v_mfma_f32_16x16x32_bf16 v[172:175], a[140:143], v[148:151], v[172:175]\n"
    "s_nop 0\n"
    "ds_read_u16_d16_hi v202, v202\n"
    "v_lshlrev_b32_sdwa v205, 1, v179 dst_sel:DWORD dst_unused:UNUSED_PAD src0_sel:DWORD src1_sel:WORD_1\n"
    "v_mfma_f32_16x16x32_bf16 v[176:179], a[88:91], v[156:159], 0\n"
    "s_waitcnt lgkmcnt(7)\n"
    "v_lshlrev_b32_sdwa v190, 1, v180 dst_sel:DWORD dst_unused:UNUSED_PAD src0_sel:DWORD src1_sel:WORD_1\n"
    "v_lshlrev_b32_sdwa v206, 1, v181 dst_sel:DWORD dst_unused:UNUSED_PAD src0_sel:DWORD src1_sel:WORD_1\n"
    "v_mfma_f32_16x16x32_bf16 v[172:175], a[240:243], v[152:155], v[172:175]\n"
    "ds_read_u16 v187, v187\n"
    "v_lshlrev_b32_sdwa v191, 1, v182 dst_sel:DWORD dst_unused:UNUSED_PAD src0_sel:DWORD src1_sel:WORD_1\n"
    "v_mfma_f32_16x16x32_bf16 v[176:179], a[136:139], v[160:163], v[176:179]\n"
    "ds_read_b128 a[244:247], v244 offset:22528\n"
    "v_lshlrev_b32_sdwa v207, 1, v183 dst_sel:DWORD dst_unused:UNUSED_PAD src0_sel:DWORD src1_sel:WORD_1\n"
    "v_mfma_f32_16x16x32_bf16 v[176:179], a[52:55], v[164:167], v[176:179]\n"
    "ds_read_u16_d16_hi v203, v203\n"
    "v_pk_fma_f16 v234, v234, s49, s49\n"
    "v_mfma_f32_16x16x32_bf16 v[180:183], a[92:95], v[156:159], 0\n"
    "v_rcp_f16_e32 v232, v234\n"
    "v_mfma_f32_16x16x32_bf16 v[180:183], a[140:143], v[160:163], v[180:183]\n"
    "s_waitcnt lgkmcnt(8)\n"
    "ds_read_u16 v188, v188\n"
    "v_rcp_f16_sdwa v232, v234 dst_sel:WORD_1 dst_unused:UNUSED_PRESERVE src0_sel:WORD_1\n"
    "v_mfma_f32_16x16x32_bf16 v[180:183], a[240:243], v[164:167], v[180:183]\n"
    "v_fma_mix_f32 v234, v196, v232, 0 op_sel_hi:[1,1,0]\n"
    "v_fma_mix_f32 v232, v196, v232, 0 op_sel:[1,1,0] op_sel_hi:[1,1,0]\n"
    "v_mfma_f32_16x16x32_bf16 v[96:99], a[208:211], v[192:195], v[96:99]\n"
    "ds_read_u16_d16_hi v204, v204\n"
    "v_cvt_pk_bf16_f32 v196, v234, v232\n"
    "v_mfma_f32_16x16x32_bf16 v[100:103], a[212:215], v[192:195], v[100:103]\n"
    "s_waitcnt lgkmcnt(8)\n"
    "ds_read_u16 v189, v189\n"
    "v_or_b32_e32 v184, v184, v200\n"
    "v_mfma_f32_16x16x32_bf16 v[112:115], a[216:219], v[192:195], v[112:115]\n"
    "v_or_b32_e32 v185, v185, v201\n"
    "v_lshlrev_b32_sdwa v201, 1, v171 dst_sel:DWORD dst_unused:UNUSED_PAD src0_sel:DWORD src1_sel:WORD_1\n"
    "v_mfma_f32_16x16x32_bf16 v[116:119], a[220:223], v[192:195], v[116:119]\n"
    "s_waitcnt lgkmcnt(7)\n"
    "ds_read_u16_d16_hi v205, v205\n"
    "v_cvt_pk_bf16_f32 v96, v96, v97\n"
    "v_mfma_f32_16x16x32_bf16 v[128:131], v[236:239], v[192:195], v[128:131]\n"
    "buffer_load_dwordx4 v[20:23], v247, s[40:43], s60 offen offset:0\n"
    "buffer_load_dwordx4 v[52:55], v247, s[40:43], s60 offen offset:64\n"
    "ds_read_u16 v190, v190\n"
    "v_mfma_f32_16x16x32_bf16 v[104:107], a[208:211], v[196:199], v[104:107]\n"
    "s_waitcnt lgkmcnt(8)\n"
    "v_or_b32_e32 v186, v186, v202\n"
    "v_lshlrev_b32_sdwa v202, 1, v173 dst_sel:DWORD dst_unused:UNUSED_PAD src0_sel:DWORD src1_sel:WORD_1\n"
    "v_mfma_f32_16x16x32_bf16 v[108:111], a[212:215], v[196:199], v[108:111]\n"
    "s_nop 0\n"
    "ds_read_u16_d16_hi v206, v206\n"
    "v_cvt_pk_bf16_f32 v97, v98, v99\n"
    "v_mfma_f32_16x16x32_bf16 v[120:123], a[216:219], v[196:199], v[120:123]\n"
    "s_waitcnt lgkmcnt(7)\n"
    "v_cvt_pk_bf16_f32 v98, v100, v101\n"
    "v_cvt_pk_bf16_f32 v99, v102, v103\n"
    "v_mfma_f32_16x16x32_bf16 v[132:135], a[244:247], v[192:195], v[132:135]\n"
    "v_cvt_pk_f16_f32 v192, v168, v169\n"
    "v_lshlrev_b32_sdwa v193, 1, v170 dst_sel:DWORD dst_unused:UNUSED_PAD src0_sel:DWORD src1_sel:WORD_1\n"
    "v_mfma_f32_16x16x32_bf16 v[124:127], a[220:223], v[196:199], v[124:127]\n"
    "v_lshlrev_b32_sdwa v194, 1, v172 dst_sel:DWORD dst_unused:UNUSED_PAD src0_sel:DWORD src1_sel:WORD_1\n"
    "v_lshlrev_b32_sdwa v195, 1, v174 dst_sel:DWORD dst_unused:UNUSED_PAD src0_sel:DWORD src1_sel:WORD_1\n"
    "v_mfma_f32_16x16x32_bf16 v[136:139], v[236:239], v[196:199], v[136:139]\n"
    "s_waitcnt lgkmcnt(6)\n"
    "ds_write_b128 v243, v[96:99] offset:24640\n"
    "v_mfma_f32_16x16x32_bf16 v[140:143], a[244:247], v[196:199], v[140:143]\n"
    "s_nop 0\n"
    "v_cvt_pk_f16_f32 v196, v176, v177\n"
    "v_lshlrev_b32_sdwa v197, 1, v178 dst_sel:DWORD dst_unused:UNUSED_PAD src0_sel:DWORD src1_sel:WORD_1\n"
    "v_mfma_f32_16x16x32_bf16 v[168:171], a[96:99], v[144:147], 0\n"
    "s_nop 0\n"
    "v_lshlrev_b32_sdwa v198, 1, v180 dst_sel:DWORD dst_unused:UNUSED_PAD src0_sel:DWORD src1_sel:WORD_1\n"
    "v_lshlrev_b32_sdwa v199, 1, v182 dst_sel:DWORD dst_unused:UNUSED_PAD src0_sel:DWORD src1_sel:WORD_1\n"
    "v_mfma_f32_16x16x32_bf16 v[168:171], a[144:147], v[148:151], v[168:171]\n"
    "s_waitcnt lgkmcnt(5)\n"
    "ds_read_u16 v191, v191\n"
    "ds_read_u16_d16_hi v207, v207\n"
    "v_mfma_f32_16x16x32_bf16 v[168:171], a[56:59], v[152:155], v[168:171]\n"
    "ds_read_u16 v193, v193\n"
    "ds_read_u16_d16_hi v201, v201\n"
    "v_or_b32_e64 v187, v187, v203\n"
    "v_lshlrev_b32_sdwa v203, 1, v175 dst_sel:DWORD dst_unused:UNUSED_PAD src0_sel:DWORD src1_sel:WORD_1\n"
    "v_mfma_f32_16x16x32_bf16 v[172:175], a[100:103], v[144:147], 0\n"
    "v_or_b32_e64 v188, v188, v204\n"
    "v_cvt_pk_bf16_f32 v112, v112, v113\n"
    "v_mfma_f32_16x16x32_bf16 v[172:175], a[148:151], v[148:151], v[172:175]\n"
    "s_waitcnt lgkmcnt(7)\n"
    "ds_read_u16 v194, v194\n"
    "v_or_b32_e32 v189, v189, v205\n"
    "v_mfma_f32_16x16x32_bf16 v[172:175], a[60:63], v[152:155], v[172:175]\n"
    "v_lshlrev_b32_sdwa v205, 1, v179 dst_sel:DWORD dst_unused:UNUSED_PAD src0_sel:DWORD src1_sel:WORD_1\n"
    "v_cvt_pk_bf16_f32 v113, v114, v115\n"
    "v_mfma_f32_16x16x32_bf16 v[176:179], a[96:99], v[156:159], 0\n"
    "ds_read_u16_d16_hi v202, v202\n"
    "v_cvt_pk_bf16_f32 v114, v116, v117\n"
    "v_mfma_f32_16x16x32_bf16 v[176:179], a[144:147], v[160:163], v[176:179]\n"
    "s_waitcnt lgkmcnt(6)\n"
    "ds_read_b128 v[236:239], v244 offset:0\n"
    "v_or_b32_e32 v190, v190, v206\n"
    "v_mfma_f32_16x16x32_bf16 v[176:179], a[56:59], v[164:167], v[176:179]\n"
    "ds_read_b128 a[252:255], v249 offset:0\n"
    "v_lshlrev_b32_sdwa v206, 1, v181 dst_sel:DWORD dst_unused:UNUSED_PAD src0_sel:DWORD src1_sel:WORD_1\n"
    "v_mfma_f32_16x16x32_bf16 v[96:99], a[0:3], v[184:187], 0\n"
    "ds_read_u16 v195, v195\n"
    "v_cvt_pk_bf16_f32 v115, v118, v119\n"
    "v_mfma_f32_16x16x32_bf16 v[100:103], a[4:7], v[184:187], 0\n"
    "v_cvt_pk_bf16_f32 v128, v128, v129\n"
    "v_cvt_pk_bf16_f32 v129, v130, v131\n"
    "v_cvt_pk_bf16_f32 v130, v132, v133\n"
    "v_cvt_pk_bf16_f32 v131, v134, v135\n"
    "v_cvt_pk_bf16_f32 v104, v104, v105\n"
    "v_cvt_pk_bf16_f32 v105, v106, v107\n"
    "v_cvt_pk_bf16_f32 v106, v108, v109\n"
    "v_cvt_pk_bf16_f32 v107, v110, v111\n"
    "v_cvt_pk_bf16_f32 v120, v120, v121\n"
    "ds_write_b128 v243, v[112:115] offset:26688\n"
    "buffer_load_dwordx4 v[4:7], v246, s[40:43], s61 offen offset:0\n"
    "buffer_load_dwordx4 v[36:39], v246, s[40:43], s61 offen offset:64\n"
    "v_mfma_f32_16x16x32_bf16 v[112:115], a[8:11], v[184:187], 0\n"
    "s_waitcnt lgkmcnt(7)\n"
    "v_or_b32_e32 v191, v191, v207\n"
    "v_lshlrev_b32_sdwa v207, 1, v183 dst_sel:DWORD dst_unused:UNUSED_PAD src0_sel:DWORD src1_sel:WORD_1\n"
    "v_mfma_f32_16x16x32_bf16 v[180:183], a[100:103], v[156:159], 0\n"
    "s_waitcnt lgkmcnt(6)\n"
    "ds_read_u16_d16_hi v203, v203\n"
    "ds_write_b128 v243, v[128:131] offset:28736\n"
    "v_mfma_f32_16x16x32_bf16 v[180:183], a[148:151], v[160:163], v[180:183]\n"
    "v_or_b32_e32 v193, v193, v201\n"
    "v_cvt_pk_bf16_f32 v121, v122, v123\n"
    "v_mfma_f32_16x16x32_bf16 v[180:183], a[60:63], v[164:167], v[180:183]\n"
    "s_waitcnt lgkmcnt(5)\n"
    "v_or_b32_e32 v194, v194, v202\n"
    "v_cvt_pk_bf16_f32 v122, v124, v125\n"
    "v_mfma_f32_16x16x32_bf16 v[116:119], a[12:15], v[184:187], 0\n"
    "s_waitcnt lgkmcnt(4)\n"
    "ds_read_b128 a[244:247], v244 offset:2048\n"
    "ds_write_b128 v243, v[104:107] offset:25664\n"
    "v_mfma_f32_16x16x32_bf16 v[128:131], v[236:239], v[184:187], 0\n"
    "s_nop 0\n"
    "v_cvt_pk_bf16_f32 v123, v126, v127\n"
    "v_pk_fma_f16 v232, v192, v192, s48\n"
    "v_mfma_f32_16x16x32_bf16 v[104:107], a[0:3], v[188:191], 0\n"
    "v_cvt_pk_bf16_f32 v136, v136, v137\n"
    "v_cvt_pk_bf16_f32 v137, v138, v139\n"
    "v_mfma_f32_16x16x32_bf16 v[108:111], a[4:7], v[188:191], 0\n"
    "ds_read_u16 v197, v197\n"
    "ds_write_b128 v243, v[120:123] offset:27712\n"
    "v_mfma_f32_16x16x32_bf16 v[120:123], a[8:11], v[188:191], 0\n"
    "s_nop 0\n"
    "buffer_store_dwordx4 a[252:255], v255, s[68:71], s59 offen offset:0 sc1\n"
    "v_mfma_f32_16x16x32_bf16 v[124:127], a[12:15], v[188:191], 0\n"
    "s_waitcnt lgkmcnt(4)\n"
    "ds_read_u16_d16_hi v205, v205\n"
    "ds_read_u16 v198, v198\n"
    "v_cvt_pk_bf16_f32 v138, v140, v141\n"
    "s_waitcnt lgkmcnt(5)\n"
    "v_mfma_f32_16x16x32_bf16 v[132:135], a[244:247], v[184:187], 0\n"
    "s_nop 0\n"
    "ds_read_u16_d16_hi v206, v206\n"
    "ds_read_u16 v199, v199\n"
    "v_or_b32_e64 v195, v195, v203\n"
    "v_cvt_pk_bf16_f32 v139, v142, v143\n"
    "v_pk_mul_f16 v232, v192, v232\n"
    "v_cvt_pk_f16_f32 v184, v168, v169\n"
    "v_exp_f16_e64 v234, v232\n"
    "v_mfma_f32_16x16x32_bf16 v[140:143], a[244:247], v[188:191], 0\n"
    "v_exp_f16_sdwa v234, v232 dst_sel:WORD_1 dst_unused:UNUSED_PRESERVE src0_sel:WORD_1\n"
    "ds_write_b128 v243, v[136:139] offset:29760\n"
    "v_mfma_f32_16x16x32_bf16 v[136:139], v[236:239], v[188:191], 0\n"
    "v_pk_fma_f16 v234, v234, s49, s49\n"
    "v_lshlrev_b32_sdwa v185, 1, v170 dst_sel:DWORD dst_unused:UNUSED_PAD src0_sel:DWORD src1_sel:WORD_1\n"
    "v_lshlrev_b32_sdwa v201, 1, v171 dst_sel:DWORD dst_unused:UNUSED_PAD src0_sel:DWORD src1_sel:WORD_1\n"
    "v_mfma_f32_16x16x32_bf16 v[168:171], a[104:107], v[144:147], 0\n"
    "ds_read_u16_d16_hi v207, v207\n"
    "ds_read_b128 a[252:255], v249 offset:2048\n"
    "v_mfma_f32_16x16x32_bf16 v[168:171], a[152:155], v[148:151], v[168:171]\n"
    "buffer_load_dwordx4 v[24:27], v247, s[40:43], s61 offen offset:0\n"
    "buffer_load_dwordx4 v[56:59], v247, s[40:43], s61 offen offset:64\n"
    "ds_read_u16 v185, v185\n"
    "v_lshlrev_b32_sdwa v186, 1, v172 dst_sel:DWORD dst_unused:UNUSED_PAD src0_sel:DWORD src1_sel:WORD_1\n"
    "s_waitcnt lgkmcnt(7)\n"
    "v_or_b32_e32 v197, v197, v205\n"
    "v_lshlrev_b32_sdwa v202, 1, v173 dst_sel:DWORD dst_unused:UNUSED_PAD src0_sel:DWORD src1_sel:WORD_1\n"
    "v_lshlrev_b32_sdwa v187, 1, v174 dst_sel:DWORD dst_unused:UNUSED_PAD src0_sel:DWORD src1_sel:WORD_1\n"
    "v_lshlrev_b32_sdwa v203, 1, v175 dst_sel:DWORD dst_unused:UNUSED_PAD src0_sel:DWORD src1_sel:WORD_1\n"
    "v_rcp_f16_e64 v232, v234\n"
    "v_mfma_f32_16x16x32_bf16 v[168:171], a[64:67], v[152:155], v[168:171]\n"
    "ds_read_u16_d16_hi v201, v201\n"
    "ds_read_u16 v186, v186\n"
    "v_rcp_f16_sdwa v232, v234 dst_sel:WORD_1 dst_unused:UNUSED_PRESERVE src0_sel:WORD_1\n"
    "v_mfma_f32_16x16x32_bf16 v[172:175], a[108:111], v[144:147], 0\n"
    "s_waitcnt lgkmcnt(7)\n"
    "ds_read_u16_d16_hi v202, v202\n"
    "v_or_b32_e32 v198, v198, v206\n"
    "v_mfma_f32_16x16x32_bf16 v[172:175], a[156:159], v[148:151], v[172:175]\n"
    "ds_read_b128 v[236:239], v244 offset:4096\n"
    "v_cvt_pk_f16_f32 v188, v176, v177\n"
    "v_lshlrev_b32_sdwa v189, 1, v178 dst_sel:DWORD dst_unused:UNUSED_PAD src0_sel:DWORD src1_sel:WORD_1\n"
    "v_lshlrev_b32_sdwa v205, 1, v179 dst_sel:DWORD dst_unused:UNUSED_PAD src0_sel:DWORD src1_sel:WORD_1\n"
    "v_lshlrev_b32_sdwa v190, 1, v180 dst_sel:DWORD dst_unused:UNUSED_PAD src0_sel:DWORD src1_sel:WORD_1\n"
    "v_lshlrev_b32_sdwa v206, 1, v181 dst_sel:DWORD dst_unused:UNUSED_PAD src0_sel:DWORD src1_sel:WORD_1\n"
    "v_lshlrev_b32_sdwa v191, 1, v182 dst_sel:DWORD dst_unused:UNUSED_PAD src0_sel:DWORD src1_sel:WORD_1\n"
    "v_fma_mix_f32 v234, v192, v232, 0 op_sel_hi:[1,1,0]\n"
    "v_mfma_f32_16x16x32_bf16 v[172:175], a[68:71], v[152:155], v[172:175]\n"
    "ds_read_u16 v187, v187\n"
    "ds_read_u16_d16_hi v203, v203\n"
    "v_fma_mix_f32 v232, v192, v232, 0 op_sel:[1,1,0] op_sel_hi:[1,1,0]\n"
    "v_mfma_f32_16x16x32_bf16 v[176:179], a[104:107], v[156:159], 0\n"
    "v_cvt_pk_bf16_f32 v192, v234, v232\n"
    "v_pk_fma_f16 v232, v196, v196, s48\n"
    "v_mfma_f32_16x16x32_bf16 v[176:179], a[152:155], v[160:163], v[176:179]\n"
    "s_waitcnt lgkmcnt(7)\n"
    "ds_read_b128 a[244:247], v244 offset:6144\n"
    "buffer_store_dwordx4 a[252:255], v255, s[68:71], s59 offen offset:64 sc1\n"
    "v_mfma_f32_16x16x32_bf16 v[176:179], a[64:67], v[164:167], v[176:179]\n"
    "s_nop 0\n"
    "ds_read_u16 v189, v189\n"
    "v_or_b32_e64 v199, v199, v207\n"
    "v_lshlrev_b32_sdwa v207, 1, v183 dst_sel:DWORD dst_unused:UNUSED_PAD src0_sel:DWORD src1_sel:WORD_1\n"
    "v_mfma_f32_16x16x32_bf16 v[180:183], a[108:111], v[156:159], 0\n"
    "ds_read_b128 a[252:255], v249 offset:4096\n"
    "ds_read_u16_d16_hi v205, v205\n"
    "v_pk_mul_f16 v232, v196, v232\n"
    "v_mfma_f32_16x16x32_bf16 v[180:183], a[156:159], v[160:163], v[180:183]\n"
    "s_waitcnt lgkmcnt(9)\n"
    "v_exp_f16_e32 v234, v232\n"
    "v_mfma_f32_16x16x32_bf16 v[180:183], a[68:71], v[164:167], v[180:183]\n"
    "ds_read_u16 v190, v190\n"
    "v_exp_f16_sdwa v234, v232 dst_sel:WORD_1 dst_unused:UNUSED_PRESERVE src0_sel:WORD_1\n"
    "v_mfma_f32_16x16x32_bf16 v[96:99], a[16:19], v[192:195], v[96:99]\n"
    "v_pk_fma_f16 v234, v234, s49, s49\n"
    "v_or_b32_e32 v185, v185, v201\n"
    "v_mfma_f32_16x16x32_bf16 v[100:103], a[20:23], v[192:195], v[100:103]\n"
    "s_waitcnt lgkmcnt(7)\n"
    "ds_read_u16_d16_hi v206, v206\n"
    "v_rcp_f16_e64 v232, v234\n"
    "v_mfma_f32_16x16x32_bf16 v[112:115], a[24:27], v[192:195], v[112:115]\n"
    "ds_read_u16 v191, v191\n"
    "v_rcp_f16_sdwa v232, v234 dst_sel:WORD_1 dst_unused:UNUSED_PRESERVE src0_sel:WORD_1\n"
    "v_mfma_f32_16x16x32_bf16 v[116:119], a[28:31], v[192:195], v[116:119]\n"
    "buffer_load_dwordx4 v[8:11], v246, s[40:43], s62 offen offset:0\n"
    "buffer_load_dwordx4 v[40:43], v246, s[40:43], s62 offen offset:64\n"
    "ds_read_u16_d16_hi v207, v207\n"
    "v_mfma_f32_16x16x32_bf16 v[128:131], v[236:239], v[192:195], v[128:131]\n"
    "v_fma_mix_f32 v234, v196, v232, 0 op_sel_hi:[1,1,0]\n"
    "v_fma_mix_f32 v232, v196, v232, 0 op_sel:[1,1,0] op_sel_hi:[1,1,0]\n"
    "s_waitcnt lgkmcnt(7)\n"
    "v_mfma_f32_16x16x32_bf16 v[132:135], a[244:247], v[192:195], v[132:135]\n"
    "s_nop 0\n"
    "ds_read_b128 a[240:243], v244 offset:34816\n"
    "v_or_b32_e64 v186, v186, v202\n"
    "v_cvt_pk_bf16_f32 v196, v234, v232\n"
    "v_pk_fma_f16 v232, v184, v184, s48\n"
    "v_or_b32_e32 v187, v187, v203\n"
    "v_mfma_f32_16x16x32_bf16 v[104:107], a[16:19], v[196:199], v[104:107]\n"
    "s_waitcnt lgkmcnt(6)\n"
    "v_pk_mul_f16 v232, v184, v232\n"
    "v_lshlrev_b32_sdwa v192, 1, v168 dst_sel:DWORD dst_unused:UNUSED_PAD src0_sel:DWORD src1_sel:WORD_1\n"
    "v_mfma_f32_16x16x32_bf16 v[108:111], a[20:23], v[196:199], v[108:111]\n"
    "ds_read_u16 v192, v192\n"
    "v_exp_f16_e64 v234, v232\n"
    "v_mfma_f32_16x16x32_bf16 v[120:123], a[24:27], v[196:199], v[120:123]\n"
    "v_exp_f16_sdwa v234, v232 dst_sel:WORD_1 dst_unused:UNUSED_PRESERVE src0_sel:WORD_1\n"
    "v_mfma_f32_16x16x32_bf16 v[124:127], a[28:31], v[196:199], v[124:127]\n"
    "s_waitcnt lgkmcnt(6)\n"
    "v_or_b32_e32 v189, v189, v205\n"
    "v_pk_fma_f16 v234, v234, s49, s49\n"
    "v_mfma_f32_16x16x32_bf16 v[136:139], v[236:239], v[196:199], v[136:139]\n"
    "ds_read_b128 v[236:239], v244 offset:8192\n"
    "v_lshlrev_b32_sdwa v200, 1, v169 dst_sel:DWORD dst_unused:UNUSED_PAD src0_sel:DWORD src1_sel:WORD_1\n"
    "v_mfma_f32_16x16x32_bf16 v[140:143], a[244:247], v[196:199], v[140:143]\n"
    "ds_read_u16_d16_hi v200, v200\n"
    "buffer_store_dwordx4 a[252:255], v255, s[68:71], s59 offen offset:128 sc1\n"
    "v_lshlrev_b32_sdwa v193, 1, v170 dst_sel:DWORD dst_unused:UNUSED_PAD src0_sel:DWORD src1_sel:WORD_1\n"
    "v_lshlrev_b32_sdwa v201, 1, v171 dst_sel:DWORD dst_unused:UNUSED_PAD src0_sel:DWORD src1_sel:WORD_1\n"
    "v_mfma_f32_16x16x32_bf16 v[168:171], a[112:115], v[144:147], 0\n"
    "s_waitcnt lgkmcnt(6)\n"
    "ds_read_b128 a[252:255], v249 offset:1024\n"
    "ds_read_u16 v193, v193\n"
    "v_mfma_f32_16x16x32_bf16 v[168:171], a[160:163], v[148:151], v[168:171]\n"
    "s_nop 0\n"
    "ds_read_u16_d16_hi v201, v201\n"
    "v_lshlrev_b32_sdwa v194, 1, v172 dst_sel:DWORD dst_unused:UNUSED_PAD src0_sel:DWORD src1_sel:WORD_1\n"
    "v_lshlrev_b32_sdwa v202, 1, v173 dst_sel:DWORD dst_unused:UNUSED_PAD src0_sel:DWORD src1_sel:WORD_1\n"
    "v_lshlrev_b32_sdwa v195, 1, v174 dst_sel:DWORD dst_unused:UNUSED_PAD src0_sel:DWORD src1_sel:WORD_1\n"
    "v_lshlrev_b32_sdwa v203, 1, v175 dst_sel:DWORD dst_unused:UNUSED_PAD src0_sel:DWORD src1_sel:WORD_1\n"
    "v_rcp_f16_e32 v232, v234\n"
    "v_mfma_f32_16x16x32_bf16 v[168:171], a[72:75], v[152:155], v[168:171]\n"
    "s_waitcnt lgkmcnt(6)\n"
    "ds_read_u16 v194, v194\n"
    "ds_read_u16_d16_hi v202, v202\n"
    "v_or_b32_e64 v190, v190, v206\n"
    "v_rcp_f16_sdwa v232, v234 dst_sel:WORD_1 dst_unused:UNUSED_PRESERVE src0_sel:WORD_1\n"
    "v_mfma_f32_16x16x32_bf16 v[172:175], a[116:119], v[144:147], 0\n"
    "ds_read_u16 v195, v195\n"
    "v_lshlrev_b32_sdwa v196, 1, v176 dst_sel:DWORD dst_unused:UNUSED_PAD src0_sel:DWORD src1_sel:WORD_1\n"
    "v_mfma_f32_16x16x32_bf16 v[172:175], a[164:167], v[148:151], v[172:175]\n"
    "ds_read_b128 a[244:247], v244 offset:10240\n"
    "v_lshlrev_b32_sdwa v204, 1, v177 dst_sel:DWORD dst_unused:UNUSED_PAD src0_sel:DWORD src1_sel:WORD_1\n"
    "v_lshlrev_b32_sdwa v197, 1, v178 dst_sel:DWORD dst_unused:UNUSED_PAD src0_sel:DWORD src1_sel:WORD_1\n"
    "v_lshlrev_b32_sdwa v205, 1, v179 dst_sel:DWORD dst_unused:UNUSED_PAD src0_sel:DWORD src1_sel:WORD_1\n"
    "v_lshlrev_b32_sdwa v198, 1, v180 dst_sel:DWORD dst_unused:UNUSED_PAD src0_sel:DWORD src1_sel:WORD_1\n"
    "v_lshlrev_b32_sdwa v206, 1, v181 dst_sel:DWORD dst_unused:UNUSED_PAD src0_sel:DWORD src1_sel:WORD_1\n"
    "v_lshlrev_b32_sdwa v199, 1, v182 dst_sel:DWORD dst_unused:UNUSED_PAD src0_sel:DWORD src1_sel:WORD_1\n"
    "v_fma_mix_f32 v234, v184, v232, 0 op_sel_hi:[1,1,0]\n"
    "v_mfma_f32_16x16x32_bf16 v[172:175], a[240:243], v[152:155], v[172:175]\n"
    "s_waitcnt lgkmcnt(6)\n"
    "buffer_load_dwordx4 v[12:15], v246, s[40:43], s63 offen offset:0\n"
    "buffer_load_dwordx4 v[44:47], v246, s[40:43], s63 offen offset:64\n"
    "ds_read_u16_d16_hi v203, v203\n"
    "v_or_b32_e32 v191, v191, v207\n"
    "v_lshlrev_b32_sdwa v207, 1, v183 dst_sel:DWORD dst_unused:UNUSED_PAD src0_sel:DWORD src1_sel:WORD_1\n"
    "v_fma_mix_f32 v232, v184, v232, 0 op_sel:[1,1,0] op_sel_hi:[1,1,0]\n"
    "v_mfma_f32_16x16x32_bf16 v[176:179], a[112:115], v[156:159], 0\n"
    "ds_read_u16 v196, v196\n"
    "ds_read_u16_d16_hi v204, v204\n"
    "v_cvt_pk_bf16_f32 v184, v234, v232\n"
    "v_mfma_f32_16x16x32_bf16 v[176:179], a[160:163], v[160:163], v[176:179]\n"
    "v_pk_fma_f16 v232, v188, v188, s48\n"
    "v_or_b32_e32 v192, v192, v200\n"
    "v_mfma_f32_16x16x32_bf16 v[176:179], a[72:75], v[164:167], v[176:179]\n"
    "s_waitcnt lgkmcnt(7)\n"
    "ds_read_u16 v197, v197\n"
    "v_pk_mul_f16 v232, v188, v232\n"
    "v_mfma_f32_16x16x32_bf16 v[180:183], a[116:119], v[156:159], 0\n"
    "ds_read_u16_d16_hi v205, v205\n"
    "v_exp_f16_e64 v234, v232\n"
    "v_mfma_f32_16x16x32_bf16 v[180:183], a[164:167], v[160:163], v[180:183]\n"
    "v_exp_f16_sdwa v234, v232 dst_sel:WORD_1 dst_unused:UNUSED_PRESERVE src0_sel:WORD_1\n"
    "v_mfma_f32_16x16x32_bf16 v[180:183], a[240:243], v[164:167], v[180:183]\n"
    "ds_read_u16 v198, v198\n"
    "v_pk_fma_f16 v234, v234, s49, s49\n"
    "v_mfma_f32_16x16x32_bf16 v[96:99], a[32:35], v[184:187], v[96:99]\n"
    "s_waitcnt lgkmcnt(8)\n"
    "v_rcp_f16_e32 v232, v234\n"
    "v_mfma_f32_16x16x32_bf16 v[100:103], a[36:39], v[184:187], v[100:103]\n"
    "ds_read_u16_d16_hi v206, v206\n"
    "buffer_store_dwordx4 a[252:255], v255, s[68:71], s39 offen offset:0 sc1\n"
    "v_mfma_f32_16x16x32_bf16 v[112:115], a[40:43], v[184:187], v[112:115]\n"
    "s_nop 0\n"
    "ds_read_u16 v199, v199\n"
    "v_rcp_f16_sdwa v232, v234 dst_sel:WORD_1 dst_unused:UNUSED_PRESERVE src0_sel:WORD_1\n"
    "v_mfma_f32_16x16x32_bf16 v[116:119], a[44:47], v[184:187], v[116:119]\n"
    "s_waitcnt lgkmcnt(7)\n"
    "ds_read_b128 a[252:255], v249 offset:3072\n"
    "v_fma_mix_f32 v234, v188, v232, 0 op_sel_hi:[1,1,0]\n"
    "v_mfma_f32_16x16x32_bf16 v[128:131], v[236:239], v[184:187], v[128:131]\n"
    "ds_read_u16_d16_hi v207, v207\n"
    "v_fma_mix_f32 v232, v188, v232, 0 op_sel:[1,1,0] op_sel_hi:[1,1,0]\n"
    "v_mfma_f32_16x16x32_bf16 v[132:135], a[244:247], v[184:187], v[132:135]\n"
    "v_cvt_pk_bf16_f32 v188, v234, v232\n"
    "v_or_b32_e32 v193, v193, v201\n"
    "v_cvt_pk_f16_f32 v184, v168, v169\n"
    "v_mfma_f32_16x16x32_bf16 v[104:107], a[32:35], v[188:191], v[104:107]\n"
    "s_waitcnt lgkmcnt(7)\n"
    "ds_read_b128 a[240:243], v244 offset:38912\n"
    "v_lshlrev_b32_sdwa v185, 1, v170 dst_sel:DWORD dst_unused:UNUSED_PAD src0_sel:DWORD src1_sel:WORD_1\n"
    "v_mfma_f32_16x16x32_bf16 v[108:111], a[36:39], v[188:191], v[108:111]\n"
    "ds_read_u16 v185, v185\n"
    "v_lshlrev_b32_sdwa v201, 1, v171 dst_sel:DWORD dst_unused:UNUSED_PAD src0_sel:DWORD src1_sel:WORD_1\n"
    "v_mfma_f32_16x16x32_bf16 v[120:123], a[40:43], v[188:191], v[120:123]\n"
    "ds_read_u16_d16_hi v201, v201\n"
    "v_lshlrev_b32_sdwa v186, 1, v172 dst_sel:DWORD dst_unused:UNUSED_PAD src0_sel:DWORD src1_sel:WORD_1\n"
    "v_mfma_f32_16x16x32_bf16 v[124:127], a[44:47], v[188:191], v[124:127]\n"
    "v_or_b32_e32 v194, v194, v202\n"
    "v_lshlrev_b32_sdwa v202, 1, v173 dst_sel:DWORD dst_unused:UNUSED_PAD src0_sel:DWORD src1_sel:WORD_1\n"
    "v_mfma_f32_16x16x32_bf16 v[136:139], v[236:239], v[188:191], v[136:139]\n"
    "s_waitcnt lgkmcnt(8)\n"
    "buffer_load_dwordx4 v[16:19], v246, s[40:43], s52 offen offset:0\n"
    "buffer_load_dwordx4 v[48:51], v246, s[40:43], s52 offen offset:64\n"
    "ds_read_u16 v186, v186\n"
    "v_mfma_f32_16x16x32_bf16 v[140:143], a[244:247], v[188:191], v[140:143]\n"
    "ds_read_u16_d16_hi v202, v202\n"
    "v_lshlrev_b32_sdwa v187, 1, v174 dst_sel:DWORD dst_unused:UNUSED_PAD src0_sel:DWORD src1_sel:WORD_1\n"
    "v_mfma_f32_16x16x32_bf16 v[168:171], a[120:123], v[144:147], 0\n"
    "v_cvt_pk_f16_f32 v188, v176, v177\n"
    "v_lshlrev_b32_sdwa v189, 1, v178 dst_sel:DWORD dst_unused:UNUSED_PAD src0_sel:DWORD src1_sel:WORD_1\n"
    "v_mfma_f32_16x16x32_bf16 v[168:171], a[168:171], v[148:151], v[168:171]\n"
    "s_waitcnt lgkmcnt(8)\n"
    "ds_read_b128 v[236:239], v244 offset:12288\n"
    "v_lshlrev_b32_sdwa v190, 1, v180 dst_sel:DWORD dst_unused:UNUSED_PAD src0_sel:DWORD src1_sel:WORD_1\n"
    "v_mfma_f32_16x16x32_bf16 v[168:171], a[76:79], v[152:155], v[168:171]\n"
    "s_waitcnt lgkmcnt(7)\n"
    "ds_read_u16 v187, v187\n"
    "v_lshlrev_b32_sdwa v191, 1, v182 dst_sel:DWORD dst_unused:UNUSED_PAD src0_sel:DWORD src1_sel:WORD_1\n"
    "v_or_b32_e64 v195, v195, v203\n"
    "v_lshlrev_b32_sdwa v203, 1, v175 dst_sel:DWORD dst_unused:UNUSED_PAD src0_sel:DWORD src1_sel:WORD_1\n"
    "v_mfma_f32_16x16x32_bf16 v[172:175], a[124:127], v[144:147], 0\n"
    "ds_read_u16_d16_hi v203, v203\n"
    "v_or_b32_e64 v196, v196, v204\n"
    "v_mfma_f32_16x16x32_bf16 v[172:175], a[172:175], v[148:151], v[172:175]\n"
    "v_or_b32_e64 v197, v197, v205\n"
    "v_lshlrev_b32_sdwa v205, 1, v179 dst_sel:DWORD dst_unused:UNUSED_PAD src0_sel:DWORD src1_sel:WORD_1\n"
    "v_mfma_f32_16x16x32_bf16 v[176:179], a[120:123], v[156:159], 0\n"
    "s_waitcnt lgkmcnt(7)\n"
    "ds_read_b128 a[244:247], v244 offset:14336\n"
    "v_or_b32_e32 v198, v198, v206\n"
    "v_mfma_f32_16x16x32_bf16 v[172:175], a[240:243], v[152:155], v[172:175]\n"
    "ds_read_u16 v189, v189\n"
    "v_lshlrev_b32_sdwa v206, 1, v181 dst_sel:DWORD dst_unused:UNUSED_PAD src0_sel:DWORD src1_sel:WORD_1\n"
    "v_mfma_f32_16x16x32_bf16 v[176:179], a[168:171], v[160:163], v[176:179]\n"
    "buffer_store_dwordx4 a[252:255], v255, s[68:71], s39 offen offset:64 sc1\n"
    "ds_read_u16_d16_hi v205, v205\n"
    "v_mfma_f32_16x16x32_bf16 v[176:179], a[76:79], v[164:167], v[176:179]\n"
    "s_waitcnt lgkmcnt(8)\n"
    "v_or_b32_e32 v199, v199, v207\n"
    "v_lshlrev_b32_sdwa v207, 1, v183 dst_sel:DWORD dst_unused:UNUSED_PAD src0_sel:DWORD src1_sel:WORD_1\n"
    "v_mfma_f32_16x16x32_bf16 v[180:183], a[124:127], v[156:159], 0\n"
    "ds_read_b128 a[252:255], v249 offset:5120\n"
    "ds_read_u16 v190, v190\n"
    "v_mfma_f32_16x16x32_bf16 v[180:183], a[172:175], v[160:163], v[180:183]\n"
    "v_or_b32_e64 v185, v185, v201\n"
    "v_pk_fma_f16 v232, v184, v184, s48\n"
    "v_mfma_f32_16x16x32_bf16 v[180:183], a[240:243], v[164:167], v[180:183]\n"
    "ds_read_b128 v[144:147], v243 offset:0\n"
    "v_pk_mul_f16 v232, v184, v232\n"
    "v_mfma_f32_16x16x32_bf16 v[96:99], a[176:179], v[192:195], v[96:99]\n"
    "s_waitcnt lgkmcnt(8)\n"
    "ds_read_u16_d16_hi v206, v206\n"
    "v_or_b32_e32 v186, v186, v202\n"
    "v_mfma_f32_16x16x32_bf16 v[100:103], a[180:183], v[192:195], v[100:103]\n"
    "v_exp_f16_e64 v234, v232\n"
    "v_mfma_f32_16x16x32_bf16 v[112:115], a[184:187], v[192:195], v[112:115]\n"
    "ds_read_b128 v[148:151], v243 offset:1024\n"
    "v_exp_f16_sdwa v234, v232 dst_sel:WORD_1 dst_unused:UNUSED_PRESERVE src0_sel:WORD_1\n"
    "v_mfma_f32_16x16x32_bf16 v[116:119], a[188:191], v[192:195], v[116:119]\n"
    "ds_read_u16 v191, v191\n"
    "v_pk_fma_f16 v234, v234, s49, s49\n"
    "v_mfma_f32_16x16x32_bf16 v[128:131], v[236:239], v[192:195], v[128:131]\n"
    "s_waitcnt lgkmcnt(8)\n"
    "v_or_b32_e32 v187, v187, v203\n"
    "v_lshlrev_b32_sdwa v201, 1, v171 dst_sel:DWORD dst_unused:UNUSED_PAD src0_sel:DWORD src1_sel:WORD_1\n"
    "v_mfma_f32_16x16x32_bf16 v[132:135], a[244:247], v[192:195], v[132:135]\n"
    "ds_read_b128 v[152:155], v243 offset:2048\n"
    "ds_read_u16_d16_hi v207, v207\n"
    "v_mfma_f32_16x16x32_bf16 v[104:107], a[176:179], v[196:199], v[104:107]\n"
    "v_cvt_pk_f16_f32 v192, v168, v169\n"
    "v_lshlrev_b32_sdwa v193, 1, v170 dst_sel:DWORD dst_unused:UNUSED_PAD src0_sel:DWORD src1_sel:WORD_1\n"
    "v_mfma_f32_16x16x32_bf16 v[108:111], a[180:183], v[196:199], v[108:111]\n"
    "ds_read_b128 v[156:159], v243 offset:3072\n"
    "v_lshlrev_b32_sdwa v194, 1, v172 dst_sel:DWORD dst_unused:UNUSED_PAD src0_sel:DWORD src1_sel:WORD_1\n"
    "v_mfma_f32_16x16x32_bf16 v[120:123], a[184:187], v[196:199], v[120:123]\n"
    "s_waitcnt lgkmcnt(8)\n"
    "ds_read_b128 v[160:163], v243 offset:4096\n"
    "buffer_store_dwordx4 a[252:255], v255, s[68:71], s39 offen offset:128 sc1\n"
    "v_mfma_f32_16x16x32_bf16 v[124:127], a[188:191], v[196:199], v[124:127]\n"
    "s_nop 0\n"
    "ds_read_b128 v[164:167], v243 offset:5120\n"
    "ds_read_b128 a[240:243], v244 offset:32768\n"
    "v_mfma_f32_16x16x32_bf16 v[136:139], v[236:239], v[196:199], v[136:139]\n"
    "s_waitcnt lgkmcnt(9)\n"
    "ds_read_u16 v193, v193\n"
    "v_or_b32_e32 v189, v189, v205\n"
    "v_mfma_f32_16x16x32_bf16 v[140:143], a[244:247], v[196:199], v[140:143]\n"
    "v_lshlrev_b32_sdwa v202, 1, v173 dst_sel:DWORD dst_unused:UNUSED_PAD src0_sel:DWORD src1_sel:WORD_1\n"
    "v_lshlrev_b32_sdwa v195, 1, v174 dst_sel:DWORD dst_unused:UNUSED_PAD src0_sel:DWORD src1_sel:WORD_1\n"
    "v_mfma_f32_16x16x32_bf16 v[168:171], a[80:83], v[144:147], 0\n"
    "ds_read_u16_d16_hi v201, v201\n"
    "v_lshlrev_b32_sdwa v203, 1, v175 dst_sel:DWORD dst_unused:UNUSED_PAD src0_sel:DWORD src1_sel:WORD_1\n"
    "s_waitcnt lgkmcnt(9)\n"
    "v_mfma_f32_16x16x32_bf16 v[168:171], a[128:131], v[148:151], v[168:171]\n"
    "v_rcp_f16_e32 v232, v234\n"
    "v_or_b32_e64 v190, v190, v206\n"
    "v_rcp_f16_sdwa v232, v234 dst_sel:WORD_1 dst_unused:UNUSED_PRESERVE src0_sel:WORD_1\n"
    "v_mfma_f32_16x16x32_bf16 v[172:175], a[84:87], v[144:147], 0\n"
    "ds_read_u16 v194, v194\n"
    "ds_read_u16_d16_hi v202, v202\n"
    "s_waitcnt lgkmcnt(9)\n"
    "v_mfma_f32_16x16x32_bf16 v[168:171], a[48:51], v[152:155], v[168:171]\n"
    "s_nop 0\n"
    "v_cvt_pk_f16_f32 v196, v176, v177\n"
    "v_lshlrev_b32_sdwa v197, 1, v178 dst_sel:DWORD dst_unused:UNUSED_PAD src0_sel:DWORD src1_sel:WORD_1\n"
    "v_lshlrev_b32_sdwa v205, 1, v179 dst_sel:DWORD dst_unused:UNUSED_PAD src0_sel:DWORD src1_sel:WORD_1\n"
    "v_lshlrev_b32_sdwa v198, 1, v180 dst_sel:DWORD dst_unused:UNUSED_PAD src0_sel:DWORD src1_sel:WORD_1\n"
    "v_lshlrev_b32_sdwa v206, 1, v181 dst_sel:DWORD dst_unused:UNUSED_PAD src0_sel:DWORD src1_sel:WORD_1\n"
    "v_lshlrev_b32_sdwa v199, 1, v182 dst_sel:DWORD dst_unused:UNUSED_PAD src0_sel:DWORD src1_sel:WORD_1\n"
    "v_fma_mix_f32 v234, v184, v232, 0 op_sel_hi:[1,1,0]\n"
    "v_mfma_f32_16x16x32_bf16 v[172:175], a[132:135], v[148:151], v[172:175]\n"
    "s_waitcnt lgkmcnt(7)\n"
    "ds_read_b128 v[236:239], v244 offset:16384\n"
    "ds_read_u16 v195, v195\n"
    "v_or_b32_e32 v191, v191, v207\n"
    "v_lshlrev_b32_sdwa v207, 1, v183 dst_sel:DWORD dst_unused:UNUSED_PAD src0_sel:DWORD src1_sel:WORD_1\n"
    "v_fma_mix_f32 v232, v184, v232, 0 op_sel:[1,1,0] op_sel_hi:[1,1,0]\n"
    "v_mfma_f32_16x16x32_bf16 v[176:179], a[80:83], v[156:159], 0\n"
    "s_waitcnt lgkmcnt(7)\n"
    "ds_read_u16_d16_hi v203, v203\n"
    "v_cvt_pk_bf16_f32 v184, v234, v232\n"
    "v_mfma_f32_16x16x32_bf16 v[176:179], a[128:131], v[160:163], v[176:179]\n"
    "s_waitcnt lgkmcnt(7)\n"
    "v_pk_fma_f16 v232, v188, v188, s48\n"
    "v_lshlrev_b32_sdwa v200, 1, v169 dst_sel:DWORD dst_unused:UNUSED_PAD src0_sel:DWORD src1_sel:WORD_1\n"
    "v_mfma_f32_16x16x32_bf16 v[172:175], a[240:243], v[152:155], v[172:175]\n"
    "2:\n"
    "s_barrier\n"
    "v_mfma_f32_16x16x32_bf16 v[176:179], a[48:51], v[164:167], v[176:179]\n"
    "s_waitcnt lgkmcnt(5)\n"
    "ds_read_b128 a[244:247], v244 offset:18432\n"
    "ds_write_b128 v253, v[64:67] offset:2048\n"
    "v_mfma_f32_16x16x32_bf16 v[180:183], a[84:87], v[156:159], 0\n"
    "v_pk_mul_f16 v232, v188, v232\n"
    "v_or_b32_e64 v193, v193, v201\n"
    "v_mfma_f32_16x16x32_bf16 v[180:183], a[132:135], v[160:163], v[180:183]\n"
    "v_exp_f16_e32 v234, v232\n"
    "v_mfma_f32_16x16x32_bf16 v[180:183], a[240:243], v[164:167], v[180:183]\n"
    "s_waitcnt lgkmcnt(4)\n"
    "ds_read_u16 v197, v197\n"
    "ds_write_b128 v253, v[68:71] offset:5120\n"
    "v_exp_f16_sdwa v234, v232 dst_sel:WORD_1 dst_unused:UNUSED_PRESERVE src0_sel:WORD_1\n"
    "v_mfma_f32_16x16x32_bf16 v[96:99], a[192:195], v[184:187], v[96:99]\n"
    "v_pk_fma_f16 v234, v234, s49, s49\n"
    "v_or_b32_e64 v194, v194, v202\n"
    "v_mfma_f32_16x16x32_bf16 v[100:103], a[196:199], v[184:187], v[100:103]\n"
    "v_rcp_f16_e32 v232, v234\n"
    "v_mfma_f32_16x16x32_bf16 v[112:115], a[200:203], v[184:187], v[112:115]\n"
    "s_waitcnt lgkmcnt(3)\n"
    "ds_read_u16_d16_hi v205, v205\n"
    "ds_write_b128 v253, v[88:91] offset:0\n"
    "v_rcp_f16_sdwa v232, v234 dst_sel:WORD_1 dst_unused:UNUSED_PRESERVE src0_sel:WORD_1\n"
    "v_mfma_f32_16x16x32_bf16 v[116:119], a[204:207], v[184:187], v[116:119]\n"
    "v_fma_mix_f32 v234, v188, v232, 0 op_sel_hi:[1,1,0]\n"
    "v_fma_mix_f32 v232, v188, v232, 0 op_sel:[1,1,0] op_sel_hi:[1,1,0]\n"
    "v_mfma_f32_16x16x32_bf16 v[128:131], v[236:239], v[184:187], v[128:131]\n"
    "v_cvt_pk_bf16_f32 v188, v234, v232\n"
    "v_pk_fma_f16 v232, v192, v192, s48\n"
    "v_mfma_f32_16x16x32_bf16 v[132:135], a[244:247], v[184:187], v[132:135]\n"
    "buffer_load_dwordx4 a[232:235], v245, s[40:43], s57 offen offset:64\n"
    "buffer_load_dwordx4 a[224:227], v245, s[40:43], s57 offen offset:0\n"
    "ds_read_u16 v198, v198\n"
    "v_mfma_f32_16x16x32_bf16 v[104:107], a[192:195], v[188:191], v[104:107]\n"
    "ds_read_u16_d16_hi v206, v206\n"
    "ds_write_b128 v253, v[92:95] offset:3072\n"
    "v_mfma_f32_16x16x32_bf16 v[108:111], a[196:199], v[188:191], v[108:111]\n"
    "v_or_b32_e64 v195, v195, v203\n"
    "v_pk_mul_f16 v232, v192, v232\n"
    "v_mfma_f32_16x16x32_bf16 v[120:123], a[200:203], v[188:191], v[120:123]\n"
    "v_exp_f16_e64 v234, v232\n"
    "v_mfma_f32_16x16x32_bf16 v[124:127], a[204:207], v[188:191], v[124:127]\n"
    "ds_read_u16 v199, v199\n"
    "ds_write_b128 v253, v[72:75] offset:1024\n"
    "v_mfma_f32_16x16x32_bf16 v[136:139], v[236:239], v[188:191], v[136:139]\n"
    "s_waitcnt lgkmcnt(5)\n"
    "v_or_b32_e32 v197, v197, v205\n"
    "v_exp_f16_sdwa v234, v232 dst_sel:WORD_1 dst_unused:UNUSED_PRESERVE src0_sel:WORD_1\n"
    "v_mfma_f32_16x16x32_bf16 v[140:143], a[244:247], v[188:191], v[140:143]\n"
    "v_pk_fma_f16 v234, v234, s49, s49\n"
    "v_lshlrev_b32_sdwa v184, 1, v168 dst_sel:DWORD dst_unused:UNUSED_PAD src0_sel:DWORD src1_sel:WORD_1\n"
    "v_lshlrev_b32_sdwa v185, 1, v170 dst_sel:DWORD dst_unused:UNUSED_PAD src0_sel:DWORD src1_sel:WORD_1\n"
    "v_lshlrev_b32_sdwa v201, 1, v171 dst_sel:DWORD dst_unused:UNUSED_PAD src0_sel:DWORD src1_sel:WORD_1\n"
    "v_mfma_f32_16x16x32_bf16 v[168:171], a[88:91], v[144:147], 0\n"
    "ds_read_u16_d16_hi v207, v207\n"
    "ds_write_b128 v253, v[76:79] offset:4096\n"
    "v_mfma_f32_16x16x32_bf16 v[168:171], a[136:139], v[148:151], v[168:171]\n"
    "v_lshlrev_b32_sdwa v186, 1, v172 dst_sel:DWORD dst_unused:UNUSED_PAD src0_sel:DWORD src1_sel:WORD_1\n"
    "v_lshlrev_b32_sdwa v202, 1, v173 dst_sel:DWORD dst_unused:UNUSED_PAD src0_sel:DWORD src1_sel:WORD_1\n"
    "v_lshlrev_b32_sdwa v187, 1, v174 dst_sel:DWORD dst_unused:UNUSED_PAD src0_sel:DWORD src1_sel:WORD_1\n"
    "v_lshlrev_b32_sdwa v203, 1, v175 dst_sel:DWORD dst_unused:UNUSED_PAD src0_sel:DWORD src1_sel:WORD_1\n"
    "v_rcp_f16_e32 v232, v234\n"
    "v_mfma_f32_16x16x32_bf16 v[168:171], a[52:55], v[152:155], v[168:171]\n"
    "s_waitcnt lgkmcnt(4)\n"
    "ds_read_b128 a[240:243], v244 offset:36864\n"
    "ds_read_u16 v184, v184\n"
    "v_or_b32_e64 v198, v198, v206\n"
    "v_rcp_f16_sdwa v232, v234 dst_sel:WORD_1 dst_unused:UNUSED_PRESERVE src0_sel:WORD_1\n"
    "v_mfma_f32_16x16x32_bf16 v[172:175], a[92:95], v[144:147], 0\n"
    "ds_read_u16_d16_hi v200, v200\n"
    "ds_read_u16 v185, v185\n"
    "v_lshlrev_b32_sdwa v188, 1, v176 dst_sel:DWORD dst_unused:UNUSED_PAD src0_sel:DWORD src1_sel:WORD_1\n"
    "v_lshlrev_b32_sdwa v204, 1, v177 dst_sel:DWORD dst_unused:UNUSED_PAD src0_sel:DWORD src1_sel:WORD_1\n"
    "v_lshlrev_b32_sdwa v189, 1, v178 dst_sel:DWORD dst_unused:UNUSED_PAD src0_sel:DWORD src1_sel:WORD_1\n"
    "v_lshlrev_b32_sdwa v205, 1, v179 dst_sel:DWORD dst_unused:UNUSED_PAD src0_sel:DWORD src1_sel:WORD_1\n"
    "v_lshlrev_b32_sdwa v190, 1, v180 dst_sel:DWORD dst_unused:UNUSED_PAD src0_sel:DWORD src1_sel:WORD_1\n"
    "v_lshlrev_b32_sdwa v206, 1, v181 dst_sel:DWORD dst_unused:UNUSED_PAD src0_sel:DWORD src1_sel:WORD_1\n"
    "v_lshlrev_b32_sdwa v191, 1, v182 dst_sel:DWORD dst_unused:UNUSED_PAD src0_sel:DWORD src1_sel:WORD_1\n"
    "v_fma_mix_f32 v234, v192, v232, 0 op_sel_hi:[1,1,0]\n"
    "v_mfma_f32_16x16x32_bf16 v[172:175], a[140:143], v[148:151], v[172:175]\n"
    "ds_read_u16_d16_hi v201, v201\n"
    "ds_read_u16 v186, v186\n"
    "buffer_load_dwordx4 a[236:239], v245, s[40:43], s88 offen offset:64\n"
    "buffer_load_dwordx4 a[228:231], v245, s[40:43], s88 offen offset:0\n"
    "v_fma_mix_f32 v232, v192, v232, 0 op_sel:[1,1,0] op_sel_hi:[1,1,0]\n"
    "v_mfma_f32_16x16x32_bf16 v[176:179], a[88:91], v[156:159], 0\n"
    "s_waitcnt lgkmcnt(6)\n"
    "ds_read_b128 v[236:239], v244 offset:20480\n"
    "ds_read_u16_d16_hi v202, v202\n"
    "v_or_b32_e32 v199, v199, v207\n"
    "v_lshlrev_b32_sdwa v207, 1, v183 dst_sel:DWORD dst_unused:UNUSED_PAD src0_sel:DWORD src1_sel:WORD_1\n"
    "v_cvt_pk_bf16_f32 v192, v234, v232\n"
    "v_mfma_f32_16x16x32_bf16 v[176:179], a[136:139], v[160:163], v[176:179]\n"
    "s_waitcnt lgkmcnt(7)\n"
    "v_pk_fma_f16 v232, v196, v196, s48\n"
    "v_mov_b32_e32 v254, 0\n"
    "v_mfma_f32_16x16x32_bf16 v[172:175], a[240:243], v[152:155], v[172:175]\n"
    "ds_read_u16 v187, v187\n"
    "v_pk_mul_f16 v232, v196, v232\n"
    "v_mfma_f32_16x16x32_bf16 v[176:179], a[52:55], v[164:167], v[176:179]\n"
    "ds_read_b128 a[244:247], v244 offset:22528\n"
    "v_exp_f16_e64 v234, v232\n"
    "v_mfma_f32_16x16x32_bf16 v[180:183], a[92:95], v[156:159], 0\n"
    "ds_read_u16_d16_hi v203, v203\n"
    "v_exp_f16_sdwa v234, v232 dst_sel:WORD_1 dst_unused:UNUSED_PRESERVE src0_sel:WORD_1\n"
    "v_mfma_f32_16x16x32_bf16 v[180:183], a[140:143], v[160:163], v[180:183]\n"
    "s_waitcnt lgkmcnt(8)\n"
    "v_pk_fma_f16 v234, v234, s49, s49\n"
    "v_or_b32_e32 v184, v184, v200\n"
    "v_mfma_f32_16x16x32_bf16 v[180:183], a[240:243], v[164:167], v[180:183]\n"
    "ds_read_u16 v188, v188\n"
    "v_rcp_f16_e32 v232, v234\n"
    "v_mfma_f32_16x16x32_bf16 v[96:99], a[208:211], v[192:195], v[96:99]\n"
    "ds_read_u16_d16_hi v204, v204\n"
    "v_rcp_f16_sdwa v232, v234 dst_sel:WORD_1 dst_unused:UNUSED_PRESERVE src0_sel:WORD_1\n"
    "v_mfma_f32_16x16x32_bf16 v[100:103], a[212:215], v[192:195], v[100:103]\n"
    "s_waitcnt lgkmcnt(8)\n"
    "v_fma_mix_f32 v234, v196, v232, 0 op_sel_hi:[1,1,0]\n"
    "v_fma_mix_f32 v232, v196, v232, 0 op_sel:[1,1,0] op_sel_hi:[1,1,0]\n"
    "v_mfma_f32_16x16x32_bf16 v[112:115], a[216:219], v[192:195], v[112:115]\n"
    "ds_read_u16 v189, v189\n"
    "v_cvt_pk_bf16_f32 v196, v234, v232\n"
    "v_mfma_f32_16x16x32_bf16 v[116:119], a[220:223], v[192:195], v[116:119]\n"
    "s_waitcnt lgkmcnt(7)\n"
    "ds_read_u16_d16_hi v205, v205\n"
    "v_or_b32_e64 v185, v185, v201\n"
    "v_mfma_f32_16x16x32_bf16 v[128:131], v[236:239], v[192:195], v[128:131]\n"
    "s_waitcnt vmcnt(21)\n"
    "v_lshlrev_b32_sdwa v201, 1, v171 dst_sel:DWORD dst_unused:UNUSED_PAD src0_sel:DWORD src1_sel:WORD_1\n"
    "v_cvt_pk_bf16_f32 v96, v96, v97\n"
    "v_mfma_f32_16x16x32_bf16 v[104:107], a[208:211], v[196:199], v[104:107]\n"
    "v_cvt_pk_bf16_f32 v97, v98, v99\n"
    "v_cvt_pk_bf16_f32 v98, v100, v101\n"
    "v_mfma_f32_16x16x32_bf16 v[108:111], a[212:215], v[196:199], v[108:111]\n"
    "s_waitcnt lgkmcnt(7)\n"
    "v_or_b32_e32 v186, v186, v202\n"
    "v_lshlrev_b32_sdwa v202, 1, v173 dst_sel:DWORD dst_unused:UNUSED_PAD src0_sel:DWORD src1_sel:WORD_1\n"
    "v_mfma_f32_16x16x32_bf16 v[120:123], a[216:219], v[196:199], v[120:123]\n"
    "s_waitcnt lgkmcnt(5)\n"
    "s_waitcnt vmcnt(17)\n"
    "ds_write2_b32 v248, v0, v1 offset0:0 offset1:20\n"
    "v_mfma_f32_16x16x32_bf16 v[132:135], a[244:247], v[192:195], v[132:135]\n"
    "v_cvt_pk_f16_f32 v192, v168, v169\n"
    "v_lshlrev_b32_sdwa v193, 1, v170 dst_sel:DWORD dst_unused:UNUSED_PAD src0_sel:DWORD src1_sel:WORD_1\n"
    "v_mfma_f32_16x16x32_bf16 v[124:127], a[220:223], v[196:199], v[124:127]\n"
    "s_nop 0\n"
    "ds_read_u16 v190, v190\n"
    "ds_write2_b32 v248, v2, v3 offset0:40 offset1:60\n"
    "v_mfma_f32_16x16x32_bf16 v[136:139], v[236:239], v[196:199], v[136:139]\n"
    "s_waitcnt lgkmcnt(7)\n"
    "v_add_u32_e32 v248, 1360, v248\n"
    "v_lshlrev_b32_sdwa v194, 1, v172 dst_sel:DWORD dst_unused:UNUSED_PAD src0_sel:DWORD src1_sel:WORD_1\n"
    "v_mfma_f32_16x16x32_bf16 v[140:143], a[244:247], v[196:199], v[140:143]\n"
    "s_waitcnt vmcnt(19)\n"
    "ds_read_u16_d16_hi v206, v206\n"
    "s_waitcnt vmcnt(15)\n"
    "ds_write2_b32 v235, v20, v21 offset0:0 offset1:20\n"
    "v_mfma_f32_16x16x32_bf16 v[168:171], a[96:99], v[144:147], 0\n"
    "v_lshlrev_b32_sdwa v195, 1, v174 dst_sel:DWORD dst_unused:UNUSED_PAD src0_sel:DWORD src1_sel:WORD_1\n"
    "v_cvt_pk_f16_f32 v196, v176, v177\n"
    "v_mfma_f32_16x16x32_bf16 v[168:171], a[144:147], v[148:151], v[168:171]\n"
    "s_waitcnt lgkmcnt(7)\n"
    "ds_write2_b32 v235, v22, v23 offset0:40 offset1:60\n"
    "v_mfma_f32_16x16x32_bf16 v[168:171], a[56:59], v[152:155], v[168:171]\n"
    "v_add_u32_e32 v235, 1360, v235\n"
    "v_lshlrev_b32_sdwa v197, 1, v178 dst_sel:DWORD dst_unused:UNUSED_PAD src0_sel:DWORD src1_sel:WORD_1\n"
    "s_waitcnt vmcnt(13)\n"
    "ds_write2_b32 v248, v4, v5 offset0:0 offset1:20\n"
    "v_lshlrev_b32_sdwa v198, 1, v180 dst_sel:DWORD dst_unused:UNUSED_PAD src0_sel:DWORD src1_sel:WORD_1\n"
    "v_lshlrev_b32_sdwa v199, 1, v182 dst_sel:DWORD dst_unused:UNUSED_PAD src0_sel:DWORD src1_sel:WORD_1\n"
    "v_or_b32_e32 v187, v187, v203\n"
    "v_lshlrev_b32_sdwa v203, 1, v175 dst_sel:DWORD dst_unused:UNUSED_PAD src0_sel:DWORD src1_sel:WORD_1\n"
    "v_mfma_f32_16x16x32_bf16 v[172:175], a[100:103], v[144:147], 0\n"
    "s_waitcnt vmcnt(16) lgkmcnt(6)\n"
    "ds_read_u16 v191, v191\n"
    "ds_write2_b32 v248, v6, v7 offset0:40 offset1:60\n"
    "v_mfma_f32_16x16x32_bf16 v[172:175], a[148:151], v[148:151], v[172:175]\n"
    "v_add_u32_e32 v248, 1360, v248\n"
    "v_or_b32_e32 v188, v188, v204\n"
    "v_mfma_f32_16x16x32_bf16 v[172:175], a[60:63], v[152:155], v[172:175]\n"
    "s_waitcnt vmcnt(13)\n"
    "ds_read_u16_d16_hi v207, v207\n"
    "s_waitcnt vmcnt(11)\n"
    "ds_write2_b32 v235, v24, v25 offset0:0 offset1:20\n"
    "v_or_b32_e32 v189, v189, v205\n"
    "v_lshlrev_b32_sdwa v205, 1, v179 dst_sel:DWORD dst_unused:UNUSED_PAD src0_sel:DWORD src1_sel:WORD_1\n"
    "v_mfma_f32_16x16x32_bf16 v[176:179], a[96:99], v[156:159], 0\n"
    "s_waitcnt lgkmcnt(5)\n"
    "v_cvt_pk_bf16_f32 v99, v102, v103\n"
    "v_or_b32_e32 v190, v190, v206\n"
    "v_mfma_f32_16x16x32_bf16 v[176:179], a[144:147], v[160:163], v[176:179]\n"
    "ds_read_u16 v193, v193\n"
    "ds_write_b128 v242, v[96:99] offset:24640\n"
    "v_mfma_f32_16x16x32_bf16 v[176:179], a[56:59], v[164:167], v[176:179]\n"
    "buffer_load_dwordx4 v[64:67], v246, s[40:43], s60 offen offset:128\n"
    "v_lshlrev_b32_sdwa v206, 1, v181 dst_sel:DWORD dst_unused:UNUSED_PAD src0_sel:DWORD src1_sel:WORD_1\n"
    "v_mfma_f32_16x16x32_bf16 v[96:99], a[0:3], v[184:187], 0\n"
    "v_cvt_pk_bf16_f32 v112, v112, v113\n"
    "v_cvt_pk_bf16_f32 v113, v114, v115\n"
    "v_mfma_f32_16x16x32_bf16 v[100:103], a[4:7], v[184:187], 0\n"
    "ds_read_u16_d16_hi v201, v201\n"
    "ds_write2_b32 v235, v26, v27 offset0:40 offset1:60\n"
    "v_add_u32_e32 v235, -1360, v235\n"
    "v_cvt_pk_bf16_f32 v114, v116, v117\n"
    "v_cvt_pk_bf16_f32 v115, v118, v119\n"
    "v_cvt_pk_bf16_f32 v128, v128, v129\n"
    "v_cvt_pk_bf16_f32 v129, v130, v131\n"
    "ds_write_b128 v242, v[112:115] offset:26688\n"
    "v_mfma_f32_16x16x32_bf16 v[112:115], a[8:11], v[184:187], 0\n"
    "s_waitcnt lgkmcnt(5)\n"
    "v_or_b32_e32 v191, v191, v207\n"
    "v_lshlrev_b32_sdwa v207, 1, v183 dst_sel:DWORD dst_unused:UNUSED_PAD src0_sel:DWORD src1_sel:WORD_1\n"
    "v_mfma_f32_16x16x32_bf16 v[180:183], a[100:103], v[156:159], 0\n"
    "ds_read_u16 v194, v194\n"
    "s_waitcnt vmcnt(10)\n"
    "ds_write2_b32 v248, v8, v9 offset0:0 offset1:20\n"
    "v_mfma_f32_16x16x32_bf16 v[180:183], a[148:151], v[160:163], v[180:183]\n"
    "s_nop 0\n"
    "v_cvt_pk_bf16_f32 v130, v132, v133\n"
    "v_cvt_pk_bf16_f32 v131, v134, v135\n"
    "v_mfma_f32_16x16x32_bf16 v[180:183], a[60:63], v[164:167], v[180:183]\n"
    "ds_read_u16_d16_hi v202, v202\n"
    "ds_write_b128 v242, v[128:131] offset:28736\n"
    "v_mfma_f32_16x16x32_bf16 v[116:119], a[12:15], v[184:187], 0\n"
    "s_waitcnt lgkmcnt(5)\n"
    "v_or_b32_e32 v193, v193, v201\n"
    "v_cvt_pk_bf16_f32 v104, v104, v105\n"
    "v_cvt_pk_bf16_f32 v105, v106, v107\n"
    "v_cvt_pk_bf16_f32 v106, v108, v109\n"
    "v_cvt_pk_bf16_f32 v107, v110, v111\n"
    "v_mfma_f32_16x16x32_bf16 v[108:111], a[4:7], v[188:191], 0\n"
    "ds_read_b128 v[236:239], v244 offset:0\n"
    "ds_write_b128 v242, v[104:107] offset:25664\n"
    "v_mfma_f32_16x16x32_bf16 v[104:107], a[0:3], v[188:191], 0\n"
    "v_cvt_pk_bf16_f32 v120, v120, v121\n"
    "v_cvt_pk_bf16_f32 v121, v122, v123\n"
    "v_cvt_pk_bf16_f32 v122, v124, v125\n"
    "v_cvt_pk_bf16_f32 v123, v126, v127\n"
    "v_cvt_pk_bf16_f32 v136, v136, v137\n"
    "v_pk_fma_f16 v232, v192, v192, s48\n"
    "v_mfma_f32_16x16x32_bf16 v[124:127], a[12:15], v[188:191], 0\n"
    "ds_read_u16 v195, v195\n"
    "ds_write_b128 v242, v[120:123] offset:27712\n"
    "v_mfma_f32_16x16x32_bf16 v[120:123], a[8:11], v[188:191], 0\n"
    "v_cvt_pk_bf16_f32 v137, v138, v139\n"
    "v_cvt_pk_bf16_f32 v138, v140, v141\n"
    "s_waitcnt lgkmcnt(4)\n"
    "v_or_b32_e32 v194, v194, v202\n"
    "v_cvt_pk_bf16_f32 v139, v142, v143\n"
    "v_pk_mul_f16 v232, v192, v232\n"
    "v_lshlrev_b32_sdwa v201, 1, v171 dst_sel:DWORD dst_unused:UNUSED_PAD src0_sel:DWORD src1_sel:WORD_1\n"
    "ds_write2_b32 v248, v10, v11 offset0:40 offset1:60\n"
    "buffer_load_dwordx4 v[68:71], v246, s[40:43], s61 offen offset:128\n"
    "s_waitcnt lgkmcnt(4)\n"
    "v_mfma_f32_16x16x32_bf16 v[128:131], v[236:239], v[184:187], 0\n"
    "s_nop 0\n"
    "ds_read_u16_d16_hi v203, v203\n"
    "ds_write_b128 v242, v[136:139] offset:29760\n"
    "v_mfma_f32_16x16x32_bf16 v[136:139], v[236:239], v[188:191], 0\n"
    "v_add_u32_e32 v248, 1360, v248\n"
    "v_exp_f16_e64 v234, v232\n"
    "v_lshlrev_b32_sdwa v202, 1, v173 dst_sel:DWORD dst_unused:UNUSED_PAD src0_sel:DWORD src1_sel:WORD_1\n"
    "v_exp_f16_sdwa v234, v232 dst_sel:WORD_1 dst_unused:UNUSED_PRESERVE src0_sel:WORD_1\n"
    "s_waitcnt vmcnt(12)\n"
    "s_waitcnt vmcnt(9)\n"
    "ds_write2_b32 v248, v12, v13 offset0:0 offset1:20\n"
    "v_pk_fma_f16 v234, v234, s49, s49\n"
    "v_rcp_f16_e64 v232, v234\n"
    "ds_read_b128 a[244:247], v244 offset:2048\n"
    "ds_write2_b32 v248, v14, v15 offset0:40 offset1:60\n"
    "v_add_u32_e32 v248, 1360, v248\n"
    "v_rcp_f16_sdwa v232, v234 dst_sel:WORD_1 dst_unused:UNUSED_PRESERVE src0_sel:WORD_1\n"
    "ds_read_u16 v197, v197\n"
    "s_waitcnt vmcnt(9)\n"
    "s_waitcnt vmcnt(7)\n"
    "ds_write2_b32 v248, v16, v17 offset0:0 offset1:20\n"
    "v_fma_mix_f32 v234, v192, v232, 0 op_sel_hi:[1,1,0]\n"
    "s_waitcnt lgkmcnt(5)\n"
    "v_or_b32_e32 v195, v195, v203\n"
    "v_lshlrev_b32_sdwa v203, 1, v175 dst_sel:DWORD dst_unused:UNUSED_PAD src0_sel:DWORD src1_sel:WORD_1\n"
    "ds_read_u16_d16_hi v205, v205\n"
    "ds_write2_b32 v248, v18, v19 offset0:40 offset1:60\n"
    "v_mfma_f32_32x32x1_2b_f32 v[0:31], v254, v254, 0\n"
    "v_add_u32_e32 v248, -5440, v248\n"
    "v_fma_mix_f32 v232, v192, v232, 0 op_sel:[1,1,0] op_sel_hi:[1,1,0]\n"
    "ds_read_u16 v198, v198\n"
    "ds_read_u16_d16_hi v206, v206\n"
    "ds_read_u16 v199, v199\n"
    "v_cvt_pk_bf16_f32 v192, v234, v232\n"
    "ds_read_u16_d16_hi v207, v207\n"
    "v_pk_fma_f16 v232, v196, v196, s48\n"
    "v_pk_mul_f16 v232, v196, v232\n"
    "v_exp_f16_e32 v234, v232\n"
    "s_waitcnt lgkmcnt(8)\n"
    "v_mfma_f32_16x16x32_bf16 v[132:135], a[244:247], v[184:187], 0\n"
    "ds_read_b128 v[216:219], v251 offset:0\n"
    "ds_read_b128 v[220:223], v251 offset:336\n"
    "v_mfma_f32_16x16x32_bf16 v[140:143], a[244:247], v[188:191], 0\n"
    "v_cvt_pk_f16_f32 v184, v168, v169\n"
    "v_lshlrev_b32_sdwa v185, 1, v170 dst_sel:DWORD dst_unused:UNUSED_PAD src0_sel:DWORD src1_sel:WORD_1\n"
    "v_mfma_f32_16x16x32_bf16 v[168:171], a[104:107], v[144:147], 0\n"
    "ds_read_u16 v185, v185\n"
    "v_lshlrev_b32_sdwa v186, 1, v172 dst_sel:DWORD dst_unused:UNUSED_PAD src0_sel:DWORD src1_sel:WORD_1\n"
    "v_mfma_f32_16x16x32_bf16 v[168:171], a[152:155], v[148:151], v[168:171]\n"
    "s_waitcnt lgkmcnt(7)\n"
    "ds_read_u16_d16_hi v201, v201\n"
    "v_or_b32_e32 v197, v197, v205\n"
    "v_mfma_f32_16x16x32_bf16 v[168:171], a[64:67], v[152:155], v[168:171]\n"
    "s_barrier\n"
    "buffer_load_dwordx4 v[72:75], v246, s[40:43], s62 offen offset:128\n"
    "ds_read_b128 v[208:211], v250 offset:0\n"
    "v_lshlrev_b32_sdwa v187, 1, v174 dst_sel:DWORD dst_unused:UNUSED_PAD src0_sel:DWORD src1_sel:WORD_1\n"
    "v_mfma_f32_16x16x32_bf16 v[172:175], a[108:111], v[144:147], 0\n"
    "s_waitcnt lgkmcnt(7)\n"
    "ds_read_b128 v[212:215], v250 offset:320\n"
    "ds_read_u16 v186, v186\n"
    "v_mfma_f32_16x16x32_bf16 v[172:175], a[156:159], v[148:151], v[172:175]\n"
    "ds_read_u16_d16_hi v202, v202\n"
    "v_or_b32_e64 v198, v198, v206\n"
    "v_mfma_f32_16x16x32_bf16 v[172:175], a[68:71], v[152:155], v[172:175]\n"
    "ds_read_b128 v[236:239], v244 offset:4096\n"
    "v_cvt_pk_f16_f32 v188, v176, v177\n"
    "v_lshlrev_b32_sdwa v189, 1, v178 dst_sel:DWORD dst_unused:UNUSED_PAD src0_sel:DWORD src1_sel:WORD_1\n"
    "v_lshlrev_b32_sdwa v205, 1, v179 dst_sel:DWORD dst_unused:UNUSED_PAD src0_sel:DWORD src1_sel:WORD_1\n"
    "v_mfma_f32_16x16x32_bf16 v[176:179], a[104:107], v[156:159], 0\n"
    "s_waitcnt lgkmcnt(8)\n"
    "ds_read_u16 v187, v187\n"
    "v_lshlrev_b32_sdwa v190, 1, v180 dst_sel:DWORD dst_unused:UNUSED_PAD src0_sel:DWORD src1_sel:WORD_1\n"
    "v_mfma_f32_16x16x32_bf16 v[176:179], a[152:155], v[160:163], v[176:179]\n"
    "s_nop 0\n"
    "v_lshlrev_b32_sdwa v206, 1, v181 dst_sel:DWORD dst_unused:UNUSED_PAD src0_sel:DWORD src1_sel:WORD_1\n"
    "v_lshlrev_b32_sdwa v191, 1, v182 dst_sel:DWORD dst_unused:UNUSED_PAD src0_sel:DWORD src1_sel:WORD_1\n"
    "v_mfma_f32_16x16x32_bf16 v[176:179], a[64:67], v[164:167], v[176:179]\n"
    "ds_read_u16_d16_hi v203, v203\n"
    "v_or_b32_e64 v199, v199, v207\n"
    "v_lshlrev_b32_sdwa v207, 1, v183 dst_sel:DWORD dst_unused:UNUSED_PAD src0_sel:DWORD src1_sel:WORD_1\n"
    "v_mfma_f32_16x16x32_bf16 v[180:183], a[108:111], v[156:159], 0\n"
    "ds_read_b128 a[244:247], v244 offset:6144\n"
    "v_exp_f16_sdwa v234, v232 dst_sel:WORD_1 dst_unused:UNUSED_PRESERVE src0_sel:WORD_1\n"
    "v_mfma_f32_16x16x32_bf16 v[180:183], a[156:159], v[160:163], v[180:183]\n"
    "ds_read_u16 v189, v189\n"
    "v_pk_fma_f16 v234, v234, s49, s49\n"
    "v_mfma_f32_16x16x32_bf16 v[180:183], a[68:71], v[164:167], v[180:183]\n"
    "s_waitcnt lgkmcnt(8)\n"
    "v_rcp_f16_e64 v232, v234\n"
    "v_mfma_f32_16x16x32_bf16 v[96:99], a[16:19], v[192:195], v[96:99]\n"
    "s_waitcnt lgkmcnt(7)\n"
    "ds_read_u16_d16_hi v205, v205\n"
    "v_rcp_f16_sdwa v232, v234 dst_sel:WORD_1 dst_unused:UNUSED_PRESERVE src0_sel:WORD_1\n"
    "v_smfmac_f32_16x16x64_bf16 v[0:3], v[216:219], v[208:215], v240\n"
    "ds_read_b128 v[216:219], v252 offset:0\n"
    "v_smfmac_f32_16x16x64_bf16 v[4:7], v[220:223], v[208:215], v240 abid:1\n"
    "ds_read_b128 v[208:211], v250 offset:640\n"
    "v_mfma_f32_16x16x32_bf16 v[100:103], a[20:23], v[192:195], v[100:103]\n"
    "ds_read_b128 v[212:215], v250 offset:960\n"
    "v_or_b32_e32 v185, v185, v201\n"
    "v_mfma_f32_16x16x32_bf16 v[112:115], a[24:27], v[192:195], v[112:115]\n"
    "s_waitcnt lgkmcnt(8)\n"
    "ds_read_b128 v[220:223], v252 offset:336\n"
    "v_fma_mix_f32 v234, v196, v232, 0 op_sel_hi:[1,1,0]\n"
    "v_mfma_f32_16x16x32_bf16 v[116:119], a[28:31], v[192:195], v[116:119]\n"
    "ds_read_u16 v190, v190\n"
    "v_fma_mix_f32 v232, v196, v232, 0 op_sel:[1,1,0] op_sel_hi:[1,1,0]\n"
    "v_mfma_f32_16x16x32_bf16 v[128:131], v[236:239], v[192:195], v[128:131]\n"
    "v_or_b32_e64 v186, v186, v202\n"
    "v_cvt_pk_bf16_f32 v196, v234, v232\n"
    "v_pk_fma_f16 v232, v184, v184, s48\n"
    "v_lshlrev_b32_sdwa v200, 1, v169 dst_sel:DWORD dst_unused:UNUSED_PAD src0_sel:DWORD src1_sel:WORD_1\n"
    "v_mfma_f32_16x16x32_bf16 v[104:107], a[16:19], v[196:199], v[104:107]\n"
    "ds_read_u16_d16_hi v206, v206\n"
    "v_pk_mul_f16 v232, v184, v232\n"
    "v_mfma_f32_16x16x32_bf16 v[108:111], a[20:23], v[196:199], v[108:111]\n"
    "s_waitcnt lgkmcnt(8)\n"
    "ds_read_u16 v191, v191\n"
    "v_or_b32_e32 v187, v187, v203\n"
    "v_mfma_f32_16x16x32_bf16 v[132:135], a[244:247], v[192:195], v[132:135]\n"
    "v_exp_f16_e64 v234, v232\n"
    "v_mfma_f32_16x16x32_bf16 v[120:123], a[24:27], v[196:199], v[120:123]\n"
    "buffer_load_dwordx4 v[76:79], v246, s[40:43], s63 offen offset:128\n"
    "ds_read_u16_d16_hi v207, v207\n"
    "v_mfma_f32_16x16x32_bf16 v[124:127], a[28:31], v[196:199], v[124:127]\n"
    "v_exp_f16_sdwa v234, v232 dst_sel:WORD_1 dst_unused:UNUSED_PRESERVE src0_sel:WORD_1\n"
    "v_mfma_f32_16x16x32_bf16 v[136:139], v[236:239], v[196:199], v[136:139]\n"
    "ds_read_b128 a[240:243], v244 offset:34816\n"
    "ds_read_u16_d16_hi v200, v200\n"
    "v_mfma_f32_16x16x32_bf16 v[140:143], a[244:247], v[196:199], v[140:143]\n"
    "s_waitcnt lgkmcnt(9)\n"
    "v_or_b32_e32 v189, v189, v205\n"
    "v_pk_fma_f16 v234, v234, s49, s49\n"
    "v_lshlrev_b32_sdwa v192, 1, v168 dst_sel:DWORD dst_unused:UNUSED_PAD src0_sel:DWORD src1_sel:WORD_1\n"
    "v_lshlrev_b32_sdwa v193, 1, v170 dst_sel:DWORD dst_unused:UNUSED_PAD src0_sel:DWORD src1_sel:WORD_1\n"
    "v_lshlrev_b32_sdwa v201, 1, v171 dst_sel:DWORD dst_unused:UNUSED_PAD src0_sel:DWORD src1_sel:WORD_1\n"
    "v_mfma_f32_16x16x32_bf16 v[168:171], a[112:115], v[144:147], 0\n"
    "s_waitcnt lgkmcnt(7)\n"
    "ds_read_u16 v192, v192\n"
    "v_lshlrev_b32_sdwa v194, 1, v172 dst_sel:DWORD dst_unused:UNUSED_PAD src0_sel:DWORD src1_sel:WORD_1\n"
    "v_smfmac_f32_16x16x64_bf16 v[0:3], v[216:219], v[208:215], v240\n"
    "s_waitcnt lgkmcnt(7)\n"
    "ds_read_b128 v[216:219], v251 offset:672\n"
    "v_smfmac_f32_16x16x64_bf16 v[4:7], v[220:223], v[208:215], v240 abid:1\n"
    "ds_read_b128 v[208:211], v250 offset:80\n"
    "v_mfma_f32_16x16x32_bf16 v[168:171], a[160:163], v[148:151], v[168:171]\n"
    "ds_read_b128 v[212:215], v250 offset:400\n"
    "v_lshlrev_b32_sdwa v202, 1, v173 dst_sel:DWORD dst_unused:UNUSED_PAD src0_sel:DWORD src1_sel:WORD_1\n"
    "v_lshlrev_b32_sdwa v195, 1, v174 dst_sel:DWORD dst_unused:UNUSED_PAD src0_sel:DWORD src1_sel:WORD_1\n"
    "v_lshlrev_b32_sdwa v203, 1, v175 dst_sel:DWORD dst_unused:UNUSED_PAD src0_sel:DWORD src1_sel:WORD_1\n"
    "v_rcp_f16_e64 v232, v234\n"
    "v_pk_mov_b32 v[224:225], v[0:1], v[0:1] op_sel:[0,1]\n"
    "v_pk_mov_b32 v[226:227], v[2:3], v[2:3] op_sel:[0,1]\n"
    "v_pk_mul_f32 v[228:229], v[0:1], v[0:1]\n"
    "v_pk_mul_f32 v[230:231], v[2:3], v[2:3]\n"
    "v_pk_add_f32 v[224:225], v[224:225], v[4:5]\n"
    "v_pk_add_f32 v[226:227], v[226:227], v[6:7]\n"
    "v_pk_fma_f32 v[228:229], v[4:5], v[4:5], v[228:229]\n"
    "v_pk_fma_f32 v[230:231], v[6:7], v[6:7], v[230:231]\n"
    "v_mfma_f32_16x16x32_bf16 v[168:171], a[72:75], v[152:155], v[168:171]\n"
    "s_waitcnt lgkmcnt(7)\n"
    "ds_read_b128 v[220:223], v251 offset:1008\n"
    "ds_read_u16 v193, v193\n"
    "v_or_b32_e32 v190, v190, v206\n"
    "v_lshlrev_b32_e32 v254, 3, v244\n"
    "v_rcp_f16_sdwa v232, v234 dst_sel:WORD_1 dst_unused:UNUSED_PRESERVE src0_sel:WORD_1\n"
    "v_mfma_f32_16x16x32_bf16 v[172:175], a[116:119], v[144:147], 0\n"
    "s_waitcnt lgkmcnt(7)\n"
    "ds_read_u16_d16_hi v201, v201\n"
    "ds_read_u16 v194, v194\n"
    "v_lshlrev_b32_sdwa v196, 1, v176 dst_sel:DWORD dst_unused:UNUSED_PAD src0_sel:DWORD src1_sel:WORD_1\n"
    "v_lshlrev_b32_sdwa v204, 1, v177 dst_sel:DWORD dst_unused:UNUSED_PAD src0_sel:DWORD src1_sel:WORD_1\n"
    "v_lshlrev_b32_sdwa v197, 1, v178 dst_sel:DWORD dst_unused:UNUSED_PAD src0_sel:DWORD src1_sel:WORD_1\n"
    "v_lshlrev_b32_sdwa v205, 1, v179 dst_sel:DWORD dst_unused:UNUSED_PAD src0_sel:DWORD src1_sel:WORD_1\n"
    "v_lshlrev_b32_sdwa v198, 1, v180 dst_sel:DWORD dst_unused:UNUSED_PAD src0_sel:DWORD src1_sel:WORD_1\n"
    "v_lshlrev_b32_sdwa v206, 1, v181 dst_sel:DWORD dst_unused:UNUSED_PAD src0_sel:DWORD src1_sel:WORD_1\n"
    "v_lshlrev_b32_sdwa v199, 1, v182 dst_sel:DWORD dst_unused:UNUSED_PAD src0_sel:DWORD src1_sel:WORD_1\n"
    "v_fma_mix_f32 v234, v184, v232, 0 op_sel_hi:[1,1,0]\n"
    "v_mfma_f32_16x16x32_bf16 v[172:175], a[164:167], v[148:151], v[172:175]\n"
    "ds_read_b128 v[236:239], v244 offset:8192\n"
    "ds_read_u16_d16_hi v202, v202\n"
    "v_mfma_f32_16x16x32_bf16 v[172:175], a[240:243], v[152:155], v[172:175]\n"
    "ds_read_u16 v195, v195\n"
    "v_or_b32_e64 v191, v191, v207\n"
    "v_lshlrev_b32_sdwa v207, 1, v183 dst_sel:DWORD dst_unused:UNUSED_PAD src0_sel:DWORD src1_sel:WORD_1\n"
    "v_fma_mix_f32 v232, v184, v232, 0 op_sel:[1,1,0] op_sel_hi:[1,1,0]\n"
    "v_mfma_f32_16x16x32_bf16 v[176:179], a[112:115], v[156:159], 0\n"
    "s_waitcnt vmcnt(4) lgkmcnt(8)\n"
    "v_cvt_pk_bf16_f32 v184, v234, v232\n"
    "v_pk_fma_f16 v232, v188, v188, s48\n"
    "v_smfmac_f32_32x32x32_bf16 v[112:127], a[248:251], a[232:239], v241\n"
    "s_waitcnt lgkmcnt(7)\n"
    "ds_read_b128 a[232:235], v243 offset:24640\n"
    "ds_read_b128 a[236:239], v243 offset:25664\n"
    "v_pk_mul_f16 v232, v188, v232\n"
    "v_or_b32_e64 v192, v192, v200\n"
    "v_exp_f16_e32 v234, v232\n"
    "v_smfmac_f32_16x16x64_bf16 v[8:11], v[216:219], v[208:215], v240\n"
    "s_waitcnt lgkmcnt(8)\n"
    "ds_read_b128 v[216:219], v252 offset:672\n"
    "ds_read_b128 a[244:247], v244 offset:10240\n"
    "v_smfmac_f32_16x16x64_bf16 v[12:15], v[220:223], v[208:215], v240 abid:1\n"
    "s_nop 0\n"
    "ds_read_b128 v[208:211], v250 offset:720\n"
    "s_mov_b32 exec_hi, 0\n"
    "buffer_load_dword v233, v254, s[80:83], s84 offen sc1\n"
    "s_mov_b32 exec_hi, -1\n"
    "v_smfmac_f32_32x32x32_bf16 v[96:111], a[248:251], a[224:231], v241\n"
    "s_waitcnt lgkmcnt(9)\n"
    "ds_read_b128 v[212:215], v250 offset:1040\n"
    "ds_read_b128 v[220:223], v252 offset:1008\n"
    "v_exp_f16_sdwa v234, v232 dst_sel:WORD_1 dst_unused:UNUSED_PRESERVE src0_sel:WORD_1\n"
    "v_or_b32_e64 v193, v193, v201\n"
    "v_mfma_f32_16x16x32_bf16 v[176:179], a[160:163], v[160:163], v[176:179]\n"
    "ds_read_u16_d16_hi v203, v203\n"
    "v_pk_fma_f16 v234, v234, s49, s49\n"
    "v_mfma_f32_16x16x32_bf16 v[176:179], a[72:75], v[164:167], v[176:179]\n"
    "ds_read_u16 v196, v196\n"
    "v_rcp_f16_e32 v232, v234\n"
    "v_mfma_f32_16x16x32_bf16 v[180:183], a[116:119], v[156:159], 0\n"
    "v_rcp_f16_sdwa v232, v234 dst_sel:WORD_1 dst_unused:UNUSED_PRESERVE src0_sel:WORD_1\n"
    "v_mfma_f32_16x16x32_bf16 v[180:183], a[164:167], v[160:163], v[180:183]\n"
    "s_waitcnt lgkmcnt(10)\n"
    "ds_read_u16_d16_hi v204, v204\n"
    "v_fma_mix_f32 v234, v188, v232, 0 op_sel_hi:[1,1,0]\n"
    "v_mfma_f32_16x16x32_bf16 v[180:183], a[240:243], v[164:167], v[180:183]\n"
    "s_nop 0\n"
    "ds_read_u16 v197, v197\n"
    "v_fma_mix_f32 v232, v188, v232, 0 op_sel:[1,1,0] op_sel_hi:[1,1,0]\n"
    "v_mfma_f32_16x16x32_bf16 v[112:115], a[40:43], v[184:187], v[112:115]\n"
    "s_waitcnt lgkmcnt(9)\n"
    "v_cvt_pk_bf16_f32 v188, v234, v232\n"
    "v_lshlrev_b32_sdwa v201, 1, v171 dst_sel:DWORD dst_unused:UNUSED_PAD src0_sel:DWORD src1_sel:WORD_1\n"
    "v_smfmac_f32_32x32x32_bf16 v[96:111], a[248:251], a[232:239], v241\n"
    "buffer_load_dwordx4 v[80:83], v246, s[40:43], s52 offen offset:128\n"
    "ds_read_u16_d16_hi v205, v205\n"
    "v_or_b32_e32 v194, v194, v202\n"
    "v_lshlrev_b32_sdwa v202, 1, v173 dst_sel:DWORD dst_unused:UNUSED_PAD src0_sel:DWORD src1_sel:WORD_1\n"
    "v_mfma_f32_16x16x32_bf16 v[116:119], a[44:47], v[184:187], v[116:119]\n"
    "s_waitcnt lgkmcnt(7)\n"
    "ds_read_u16 v198, v198\n"
    "v_mfma_f32_16x16x32_bf16 v[128:131], v[236:239], v[184:187], v[128:131]\n"
    "s_waitcnt lgkmcnt(7)\n"
    "v_smfmac_f32_16x16x64_bf16 v[8:11], v[216:219], v[208:215], v240\n"
    "s_waitcnt lgkmcnt(6)\n"
    "ds_read_b128 v[216:219], v251 offset:1344\n"
    "ds_read_u16_d16_hi v206, v206\n"
    "v_smfmac_f32_16x16x64_bf16 v[12:15], v[220:223], v[208:215], v240 abid:1\n"
    "v_mfma_f32_16x16x32_bf16 v[96:99], a[32:35], v[184:187], v[96:99]\n"
    "ds_read_b128 v[208:211], v250 offset:160\n"
    "v_mfma_f32_16x16x32_bf16 v[100:103], a[36:39], v[184:187], v[100:103]\n"
    "s_waitcnt lgkmcnt(8)\n"
    "ds_read_b128 v[212:215], v250 offset:480\n"
    "v_or_b32_e32 v195, v195, v203\n"
    "v_lshlrev_b32_sdwa v203, 1, v175 dst_sel:DWORD dst_unused:UNUSED_PAD src0_sel:DWORD src1_sel:WORD_1\n"
    "v_mfma_f32_16x16x32_bf16 v[132:135], a[244:247], v[184:187], v[132:135]\n"
    "ds_read_b128 v[220:223], v251 offset:1680\n"
    "v_cvt_pk_f16_f32 v184, v168, v169\n"
    "v_lshlrev_b32_sdwa v185, 1, v170 dst_sel:DWORD dst_unused:UNUSED_PAD src0_sel:DWORD src1_sel:WORD_1\n"
    "v_lshlrev_b32_sdwa v186, 1, v172 dst_sel:DWORD dst_unused:UNUSED_PAD src0_sel:DWORD src1_sel:WORD_1\n"
    "v_lshlrev_b32_sdwa v187, 1, v174 dst_sel:DWORD dst_unused:UNUSED_PAD src0_sel:DWORD src1_sel:WORD_1\n"
    "v_pk_add_f32 v[224:225], v[224:225], v[8:9]\n"
    "v_pk_add_f32 v[226:227], v[226:227], v[10:11]\n"
    "v_pk_fma_f32 v[228:229], v[8:9], v[8:9], v[228:229]\n"
    "v_pk_fma_f32 v[230:231], v[10:11], v[10:11], v[230:231]\n"
    "v_pk_add_f32 v[224:225], v[224:225], v[12:13]\n"
    "v_pk_add_f32 v[226:227], v[226:227], v[14:15]\n"
    "v_pk_fma_f32 v[228:229], v[12:13], v[12:13], v[228:229]\n"
    "v_pk_fma_f32 v[230:231], v[14:15], v[14:15], v[230:231]\n"
    "v_mfma_f32_16x16x32_bf16 v[104:107], a[32:35], v[188:191], v[104:107]\n"
    "s_waitcnt lgkmcnt(7)\n"
    "buffer_load_dwordx4 a[224:227], v245, s[40:43], s57 offen offset:128\n"
    "ds_read_u16 v199, v199\n"
    "v_mfma_f32_16x16x32_bf16 v[108:111], a[36:39], v[188:191], v[108:111]\n"
    "ds_read_u16_d16_hi v207, v207\n"
    "ds_read_b128 a[232:235], v243 offset:26688\n"
    "v_mfma_f32_16x16x32_bf16 v[120:123], a[40:43], v[188:191], v[120:123]\n"
    "s_waitcnt lgkmcnt(9)\n"
    "ds_read_b128 a[236:239], v243 offset:27712\n"
    "ds_read_b128 a[240:243], v244 offset:38912\n"
    "v_mfma_f32_16x16x32_bf16 v[124:127], a[44:47], v[188:191], v[124:127]\n"
    "v_or_b32_e64 v196, v196, v204\n"
    "v_or_b32_e64 v197, v197, v205\n"
    "v_mfma_f32_16x16x32_bf16 v[136:139], v[236:239], v[188:191], v[136:139]\n"
    "ds_read_u16 v185, v185\n"
    "v_lshlrev_b32_sdwa v205, 1, v179 dst_sel:DWORD dst_unused:UNUSED_PAD src0_sel:DWORD src1_sel:WORD_1\n"
    "v_mfma_f32_16x16x32_bf16 v[140:143], a[244:247], v[188:191], v[140:143]\n"
    "ds_read_u16_d16_hi v201, v201\n"
    "v_cvt_pk_f16_f32 v188, v176, v177\n"
    "v_mfma_f32_16x16x32_bf16 v[168:171], a[120:123], v[144:147], 0\n"
    "s_waitcnt lgkmcnt(9)\n"
    "v_lshlrev_b32_sdwa v189, 1, v178 dst_sel:DWORD dst_unused:UNUSED_PAD src0_sel:DWORD src1_sel:WORD_1\n"
    "v_lshlrev_b32_sdwa v190, 1, v180 dst_sel:DWORD dst_unused:UNUSED_PAD src0_sel:DWORD src1_sel:WORD_1\n"
    "v_mfma_f32_16x16x32_bf16 v[168:171], a[168:171], v[148:151], v[168:171]\n"
    "s_waitcnt lgkmcnt(8)\n"
    "ds_read_u16 v186, v186\n"
    "v_lshlrev_b32_sdwa v191, 1, v182 dst_sel:DWORD dst_unused:UNUSED_PAD src0_sel:DWORD src1_sel:WORD_1\n"
    "v_smfmac_f32_16x16x64_bf16 v[16:19], v[216:219], v[208:215], v240\n"
    "s_waitcnt lgkmcnt(8)\n"
    "v_or_b32_e32 v198, v198, v206\n"
    "v_smfmac_f32_16x16x64_bf16 v[20:23], v[220:223], v[208:215], v240 abid:1\n"
    "ds_read_b128 v[208:211], v250 offset:800\n"
    "ds_read_b128 v[212:215], v250 offset:1120\n"
    "v_mfma_f32_16x16x32_bf16 v[168:171], a[76:79], v[152:155], v[168:171]\n"
    "buffer_load_dwordx4 a[228:231], v245, s[40:43], s88 offen offset:128\n"
    "ds_read_b128 v[216:219], v252 offset:1344\n"
    "v_mfma_f32_16x16x32_bf16 v[172:175], a[124:127], v[144:147], 0\n"
    "v_lshlrev_b32_sdwa v206, 1, v181 dst_sel:DWORD dst_unused:UNUSED_PAD src0_sel:DWORD src1_sel:WORD_1\n"
    "v_pk_fma_f16 v232, v184, v184, s48\n"
    "v_mfma_f32_16x16x32_bf16 v[172:175], a[172:175], v[148:151], v[172:175]\n"
    "ds_read_b128 v[220:223], v252 offset:1680\n"
    "ds_read_u16_d16_hi v202, v202\n"
    "v_mfma_f32_16x16x32_bf16 v[176:179], a[120:123], v[156:159], 0\n"
    "s_waitcnt lgkmcnt(9)\n"
    "v_or_b32_e32 v199, v199, v207\n"
    "v_lshlrev_b32_sdwa v207, 1, v183 dst_sel:DWORD dst_unused:UNUSED_PAD src0_sel:DWORD src1_sel:WORD_1\n"
    "v_smfmac_f32_32x32x32_bf16 v[112:127], a[248:251], a[232:239], v241\n"
    "s_waitcnt lgkmcnt(8)\n"
    "ds_read_b128 v[236:239], v244 offset:12288\n"
    "v_pk_mul_f16 v232, v184, v232\n"
    "v_exp_f16_e32 v234, v232\n"
    "ds_read_u16 v187, v187\n"
    "v_mfma_f32_16x16x32_bf16 v[172:175], a[240:243], v[152:155], v[172:175]\n"
    "v_exp_f16_sdwa v234, v232 dst_sel:WORD_1 dst_unused:UNUSED_PRESERVE src0_sel:WORD_1\n"
    "v_mfma_f32_16x16x32_bf16 v[176:179], a[168:171], v[160:163], v[176:179]\n"
    "ds_read_u16_d16_hi v203, v203\n"
    "v_pk_fma_f16 v234, v234, s49, s49\n"
    "v_mfma_f32_16x16x32_bf16 v[176:179], a[76:79], v[164:167], v[176:179]\n"
    "ds_read_b128 a[244:247], v244 offset:14336\n"
    "v_rcp_f16_e32 v232, v234\n"
    "v_mfma_f32_16x16x32_bf16 v[180:183], a[124:127], v[156:159], 0\n"
    "s_waitcnt lgkmcnt(10)\n"
    "buffer_load_dwordx4 v[84:87], v247, s[40:43], s60 offen offset:128\n"
    "ds_read_u16 v189, v189\n"
    "v_mfma_f32_16x16x32_bf16 v[180:183], a[172:175], v[160:163], v[180:183]\n"
    "v_lshlrev_b32_e64 v254, 3, v244\n"
    "v_or_b32_e64 v185, v185, v201\n"
    "v_mfma_f32_16x16x32_bf16 v[180:183], a[240:243], v[164:167], v[180:183]\n"
    "ds_read_b128 a[232:235], v243 offset:28736\n"
    "v_lshlrev_b32_sdwa v201, 1, v171 dst_sel:DWORD dst_unused:UNUSED_PAD src0_sel:DWORD src1_sel:WORD_1\n"
    "v_mfma_f32_16x16x32_bf16 v[96:99], a[176:179], v[192:195], v[96:99]\n"
    "s_waitcnt lgkmcnt(8)\n"
    "ds_read_u16_d16_hi v205, v205\n"
    "v_rcp_f16_sdwa v232, v234 dst_sel:WORD_1 dst_unused:UNUSED_PRESERVE src0_sel:WORD_1\n"
    "v_smfmac_f32_16x16x64_bf16 v[16:19], v[216:219], v[208:215], v240\n"
    "s_waitcnt lgkmcnt(8)\n"
    "ds_read_b128 v[216:219], v251 offset:2016\n"
    "v_smfmac_f32_16x16x64_bf16 v[20:23], v[220:223], v[208:215], v240 abid:1\n"
    "ds_read_b128 v[208:211], v250 offset:240\n"
    "v_mfma_f32_16x16x32_bf16 v[100:103], a[180:183], v[192:195], v[100:103]\n"
    "s_waitcnt lgkmcnt(8)\n"
    "ds_read_b128 v[212:215], v250 offset:560\n"
    "v_or_b32_e32 v186, v186, v202\n"
    "v_lshlrev_b32_sdwa v202, 1, v173 dst_sel:DWORD dst_unused:UNUSED_PAD src0_sel:DWORD src1_sel:WORD_1\n"
    "v_fma_mix_f32 v234, v184, v232, 0 op_sel_hi:[1,1,0]\n"
    "ds_read_b128 v[220:223], v251 offset:2352\n"
    "v_pk_add_f32 v[224:225], v[224:225], v[16:17]\n"
    "v_pk_add_f32 v[226:227], v[226:227], v[18:19]\n"
    "v_pk_fma_f32 v[228:229], v[16:17], v[16:17], v[228:229]\n"
    "v_pk_fma_f32 v[230:231], v[18:19], v[18:19], v[230:231]\n"
    "v_pk_add_f32 v[224:225], v[224:225], v[20:21]\n"
    "v_pk_add_f32 v[226:227], v[226:227], v[22:23]\n"
    "v_pk_fma_f32 v[228:229], v[20:21], v[20:21], v[228:229]\n"
    "v_pk_fma_f32 v[230:231], v[22:23], v[22:23], v[230:231]\n"
    "v_mfma_f32_16x16x32_bf16 v[112:115], a[184:187], v[192:195], v[112:115]\n"
    "s_nop 0\n"
    "buffer_load_dwordx4 v[88:91], v247, s[40:43], s61 offen offset:128\n"
    "ds_read_b128 a[236:239], v243 offset:29760\n"
    "v_mfma_f32_16x16x32_bf16 v[116:119], a[188:191], v[192:195], v[116:119]\n"
    "s_waitcnt lgkmcnt(8)\n"
    "ds_read_u16 v190, v190\n"
    "ds_read_b128 v[144:147], v242 offset:0\n"
    "v_mfma_f32_16x16x32_bf16 v[128:131], v[236:239], v[192:195], v[128:131]\n"
    "ds_read_u16_d16_hi v206, v206\n"
    "v_or_b32_e64 v187, v187, v203\n"
    "v_mfma_f32_16x16x32_bf16 v[132:135], a[244:247], v[192:195], v[132:135]\n"
    "v_cvt_pk_f16_f32 v192, v168, v169\n"
    "v_lshlrev_b32_sdwa v193, 1, v170 dst_sel:DWORD dst_unused:UNUSED_PAD src0_sel:DWORD src1_sel:WORD_1\n"
    "v_mfma_f32_16x16x32_bf16 v[104:107], a[176:179], v[196:199], v[104:107]\n"
    "ds_read_b128 v[148:151], v242 offset:1024\n"
    "ds_read_u16 v191, v191\n"
    "v_mfma_f32_16x16x32_bf16 v[108:111], a[180:183], v[196:199], v[108:111]\n"
    "v_lshlrev_b32_sdwa v194, 1, v172 dst_sel:DWORD dst_unused:UNUSED_PAD src0_sel:DWORD src1_sel:WORD_1\n"
    "v_lshlrev_b32_sdwa v195, 1, v174 dst_sel:DWORD dst_unused:UNUSED_PAD src0_sel:DWORD src1_sel:WORD_1\n"
    "v_mfma_f32_16x16x32_bf16 v[120:123], a[184:187], v[196:199], v[120:123]\n"
    "s_waitcnt lgkmcnt(9)\n"
    "ds_read_b128 v[152:155], v242 offset:2048\n"
    "v_or_b32_e32 v189, v189, v205\n"
    "v_mfma_f32_16x16x32_bf16 v[124:127], a[188:191], v[196:199], v[124:127]\n"
    "ds_read_u16_d16_hi v207, v207\n"
    "v_lshlrev_b32_sdwa v203, 1, v175 dst_sel:DWORD dst_unused:UNUSED_PAD src0_sel:DWORD src1_sel:WORD_1\n"
    "v_mfma_f32_16x16x32_bf16 v[136:139], v[236:239], v[196:199], v[136:139]\n"
    "s_waitcnt lgkmcnt(9)\n"
    "v_lshlrev_b32_sdwa v205, 1, v179 dst_sel:DWORD dst_unused:UNUSED_PAD src0_sel:DWORD src1_sel:WORD_1\n"
    "v_fma_mix_f32 v232, v184, v232, 0 op_sel:[1,1,0] op_sel_hi:[1,1,0]\n"
    "v_smfmac_f32_16x16x64_bf16 v[24:27], v[216:219], v[208:215], v240\n"
    "s_waitcnt lgkmcnt(8)\n"
    "ds_read_b128 v[216:219], v252 offset:2016\n"
    "s_mov_b32 exec_hi, 0\n"
    "buffer_load_dword v233, v254, s[72:75], s53 offen sc1\n"
    "s_mov_b32 exec_hi, -1\n"
    "v_smfmac_f32_16x16x64_bf16 v[28:31], v[220:223], v[208:215], v240 abid:1\n"
    "ds_read_b128 v[208:211], v250 offset:880\n"
    "ds_read_b128 v[212:215], v250 offset:1200\n"
    "v_mfma_f32_16x16x32_bf16 v[140:143], a[244:247], v[196:199], v[140:143]\n"
    "s_waitcnt lgkmcnt(10)\n"
    "ds_read_b128 v[220:223], v252 offset:2352\n"
    "v_cvt_pk_f16_f32 v196, v176, v177\n"
    "s_waitcnt lgkmcnt(9)\n"
    "v_mfma_f32_16x16x32_bf16 v[168:171], a[80:83], v[144:147], 0\n"
    "ds_read_b128 v[156:159], v242 offset:3072\n"
    "v_lshlrev_b32_sdwa v197, 1, v178 dst_sel:DWORD dst_unused:UNUSED_PAD src0_sel:DWORD src1_sel:WORD_1\n"
    "v_smfmac_f32_32x32x32_bf16 v[128:143], a[248:251], a[232:239], v241\n"
    "ds_read_b128 v[160:163], v242 offset:4096\n"
    "ds_read_b128 v[164:167], v242 offset:5120\n"
    "v_lshlrev_b32_sdwa v198, 1, v180 dst_sel:DWORD dst_unused:UNUSED_PAD src0_sel:DWORD src1_sel:WORD_1\n"
    "v_lshlrev_b32_sdwa v199, 1, v182 dst_sel:DWORD dst_unused:UNUSED_PAD src0_sel:DWORD src1_sel:WORD_1\n"
    "v_cvt_pk_bf16_f32 v184, v234, v232\n"
    "v_mfma_f32_16x16x32_bf16 v[172:175], a[84:87], v[144:147], 0\n"
    "s_waitcnt lgkmcnt(10)\n"
    "ds_read_b128 a[240:243], v244 offset:32768\n"
    "v_or_b32_e32 v190, v190, v206\n"
    "v_mfma_f32_16x16x32_bf16 v[168:171], a[128:131], v[148:151], v[168:171]\n"
    "ds_read_u16 v193, v193\n"
    "v_lshlrev_b32_sdwa v206, 1, v181 dst_sel:DWORD dst_unused:UNUSED_PAD src0_sel:DWORD src1_sel:WORD_1\n"
    "v_mfma_f32_16x16x32_bf16 v[172:175], a[132:135], v[148:151], v[172:175]\n"
    "s_waitcnt lgkmcnt(10)\n"
    "v_pk_fma_f16 v232, v188, v188, s48\n"
    "v_mov_b32_e32 v254, 0\n"
    "v_mfma_f32_16x16x32_bf16 v[168:171], a[48:51], v[152:155], v[168:171]\n"
    "ds_read_u16_d16_hi v201, v201\n"
    "v_pk_mul_f16 v232, v188, v232\n"
    "v_exp_f16_e64 v234, v232\n"
    "ds_read_u16 v194, v194\n"
    "s_waitcnt lgkmcnt(10)\n"
    "v_or_b32_e32 v191, v191, v207\n"
    "v_lshlrev_b32_sdwa v207, 1, v183 dst_sel:DWORD dst_unused:UNUSED_PAD src0_sel:DWORD src1_sel:WORD_1\n"
    "v_exp_f16_sdwa v234, v232 dst_sel:WORD_1 dst_unused:UNUSED_PRESERVE src0_sel:WORD_1\n"
    "v_lshlrev_b32_sdwa v200, 1, v169 dst_sel:DWORD dst_unused:UNUSED_PAD src0_sel:DWORD src1_sel:WORD_1\n"
    "v_pk_fma_f16 v234, v234, s49, s49\n"
    "v_mfma_f32_16x16x32_bf16 v[96:99], a[192:195], v[184:187], v[96:99]\n"
    "s_waitcnt lgkmcnt(8)\n"
    "s_barrier\n"
    "v_smfmac_f32_16x16x64_bf16 v[24:27], v[216:219], v[208:215], v240\n"
    "s_waitcnt lgkmcnt(4)\n"
    "ds_read_u16_d16_hi v202, v202\n"
    "ds_write2_b32 v248, v32, v33 offset0:0 offset1:20\n"
    "v_smfmac_f32_16x16x64_bf16 v[28:31], v[220:223], v[208:215], v240 abid:1\n"
    "s_waitcnt lgkmcnt(5)\n"
    "v_rcp_f16_e64 v232, v234\n"
    "v_mfma_f32_16x16x32_bf16 v[172:175], a[240:243], v[152:155], v[172:175]\n"
    "ds_read_b128 v[236:239], v244 offset:16384\n"
    "ds_write2_b32 v248, v34, v35 offset0:40 offset1:60\n"
    "v_add_u32_e32 v248, 1360, v248\n"
    "v_pk_add_f32 v[224:225], v[224:225], v[24:25]\n"
    "v_pk_add_f32 v[226:227], v[226:227], v[26:27]\n"
    "v_pk_fma_f32 v[228:229], v[24:25], v[24:25], v[228:229]\n"
    "v_pk_fma_f32 v[230:231], v[26:27], v[26:27], v[230:231]\n"
    "v_pk_add_f32 v[224:225], v[224:225], v[28:29]\n"
    "v_pk_add_f32 v[226:227], v[226:227], v[30:31]\n"
    "v_pk_fma_f32 v[228:229], v[28:29], v[28:29], v[228:229]\n"
    "v_pk_fma_f32 v[230:231], v[30:31], v[30:31], v[230:231]\n"
    "v_mfma_f32_16x16x32_bf16 v[176:179], a[80:83], v[156:159], 0\n"
    "s_waitcnt lgkmcnt(5)\n"
    "ds_read_u16 v195, v195\n"
    "ds_write2_b32 v235, v52, v53 offset0:0 offset1:20\n"
    "v_mfma_f32_16x16x32_bf16 v[176:179], a[128:131], v[160:163], v[176:179]\n"
    "v_rcp_f16_sdwa v232, v234 dst_sel:WORD_1 dst_unused:UNUSED_PRESERVE src0_sel:WORD_1\n"
    "v_mfma_f32_16x16x32_bf16 v[176:179], a[48:51], v[164:167], v[176:179]\n"
    "ds_read_u16_d16_hi v203, v203\n"
    "ds_write2_b32 v235, v54, v55 offset0:40 offset1:60\n"
    "v_mfma_f32_16x16x32_bf16 v[180:183], a[84:87], v[156:159], 0\n"
    "s_waitcnt lgkmcnt(6)\n"
    "v_add_u32_e32 v235, 1360, v235\n"
    "v_or_b32_e64 v193, v193, v201\n"
    "v_mfma_f32_16x16x32_bf16 v[180:183], a[132:135], v[160:163], v[180:183]\n"
    "ds_read_b128 a[244:247], v244 offset:18432\n"
    "ds_write2_b32 v248, v36, v37 offset0:0 offset1:20\n"
    "v_mfma_f32_16x16x32_bf16 v[180:183], a[240:243], v[164:167], v[180:183]\n"
    "v_fma_mix_f32 v234, v188, v232, 0 op_sel_hi:[1,1,0]\n"
    "v_fma_mix_f32 v232, v188, v232, 0 op_sel:[1,1,0] op_sel_hi:[1,1,0]\n"
    "v_mfma_f32_16x16x32_bf16 v[100:103], a[196:199], v[184:187], v[100:103]\n"
    "s_waitcnt lgkmcnt(6)\n"
    "ds_read_u16 v197, v197\n"
    "ds_write2_b32 v248, v38, v39 offset0:40 offset1:60\n"
    "v_mfma_f32_16x16x32_bf16 v[112:115], a[200:203], v[184:187], v[112:115]\n"
    "s_nop 0\n"
    "v_add_u32_e32 v248, 1360, v248\n"
    "v_or_b32_e64 v194, v194, v202\n"
    "v_mfma_f32_16x16x32_bf16 v[116:119], a[204:207], v[184:187], v[116:119]\n"
    "ds_read_u16_d16_hi v205, v205\n"
    "ds_write2_b32 v235, v56, v57 offset0:0 offset1:20\n"
    "v_mfma_f32_16x16x32_bf16 v[128:131], v[236:239], v[184:187], v[128:131]\n"
    "v_cvt_pk_bf16_f32 v188, v234, v232\n"
    "v_pk_fma_f16 v232, v192, v192, s48\n"
    "v_lshlrev_b32_sdwa v201, 1, v171 dst_sel:DWORD dst_unused:UNUSED_PAD src0_sel:DWORD src1_sel:WORD_1\n"
    "v_mfma_f32_16x16x32_bf16 v[104:107], a[192:195], v[188:191], v[104:107]\n"
    "s_waitcnt lgkmcnt(6)\n"
    "ds_read_u16 v198, v198\n"
    "ds_write2_b32 v235, v58, v59 offset0:40 offset1:60\n"
    "v_mfma_f32_16x16x32_bf16 v[108:111], a[196:199], v[188:191], v[108:111]\n"
    "s_waitcnt lgkmcnt(6)\n"
    "v_add_u32_e32 v235, -1360, v235\n"
    "v_or_b32_e64 v195, v195, v203\n"
    "v_mfma_f32_16x16x32_bf16 v[132:135], a[244:247], v[184:187], v[132:135]\n"
    "ds_read_u16_d16_hi v206, v206\n"
    "ds_write2_b32 v248, v40, v41 offset0:0 offset1:20\n"
    "v_mfma_f32_16x16x32_bf16 v[120:123], a[200:203], v[188:191], v[120:123]\n"
    "v_pk_mul_f16 v232, v192, v232\n"
    "v_lshlrev_b32_sdwa v184, 1, v168 dst_sel:DWORD dst_unused:UNUSED_PAD src0_sel:DWORD src1_sel:WORD_1\n"
    "v_mfma_f32_16x16x32_bf16 v[124:127], a[204:207], v[188:191], v[124:127]\n"
    "ds_read_u16 v199, v199\n"
    "ds_write2_b32 v248, v42, v43 offset0:40 offset1:60\n"
    "v_mfma_f32_16x16x32_bf16 v[136:139], v[236:239], v[188:191], v[136:139]\n"
    "v_add_u32_e32 v248, 1360, v248\n"
    "v_exp_f16_e32 v234, v232\n"
    "v_mfma_f32_16x16x32_bf16 v[140:143], a[244:247], v[188:191], v[140:143]\n"
    "s_waitcnt lgkmcnt(6)\n"
    "ds_read_u16_d16_hi v207, v207\n"
    "ds_write2_b32 v248, v44, v45 offset0:0 offset1:20\n"
    "v_or_b32_e64 v197, v197, v205\n"
    "v_exp_f16_sdwa v234, v232 dst_sel:WORD_1 dst_unused:UNUSED_PRESERVE src0_sel:WORD_1\n"
    "v_lshlrev_b32_sdwa v185, 1, v170 dst_sel:DWORD dst_unused:UNUSED_PAD src0_sel:DWORD src1_sel:WORD_1\n"
    "v_pk_fma_f16 v234, v234, s49, s49\n"
    "v_mfma_f32_16x16x32_bf16 v[168:171], a[88:91], v[144:147], 0\n"
    "s_add_u32 s60, s60, 1572864\n"
    "ds_read_b128 a[240:243], v244 offset:36864\n"
    "ds_write2_b32 v248, v46, v47 offset0:40 offset1:60\n"
    "v_mfma_f32_16x16x32_bf16 v[168:171], a[136:139], v[148:151], v[168:171]\n"
    "v_add_u32_e32 v248, 1360, v248\n"
    "v_lshlrev_b32_sdwa v186, 1, v172 dst_sel:DWORD dst_unused:UNUSED_PAD src0_sel:DWORD src1_sel:WORD_1\n"
    "v_lshlrev_b32_sdwa v202, 1, v173 dst_sel:DWORD dst_unused:UNUSED_PAD src0_sel:DWORD src1_sel:WORD_1\n"
    "v_lshlrev_b32_sdwa v187, 1, v174 dst_sel:DWORD dst_unused:UNUSED_PAD src0_sel:DWORD src1_sel:WORD_1\n"
    "v_lshlrev_b32_sdwa v203, 1, v175 dst_sel:DWORD dst_unused:UNUSED_PAD src0_sel:DWORD src1_sel:WORD_1\n"
    "v_rcp_f16_e32 v232, v234\n"
    "v_mfma_f32_16x16x32_bf16 v[168:171], a[52:55], v[152:155], v[168:171]\n"
    "s_waitcnt lgkmcnt(6)\n"
    "ds_read_u16 v184, v184\n"
    "ds_write2_b32 v248, v48, v49 offset0:0 offset1:20\n"
    "v_or_b32_e64 v198, v198, v206\n"
    "v_rcp_f16_sdwa v232, v234 dst_sel:WORD_1 dst_unused:UNUSED_PRESERVE src0_sel:WORD_1\n"
    "v_mfma_f32_16x16x32_bf16 v[172:175], a[92:95], v[144:147], 0\n"
    "s_add_u32 s61, s61, 1572864\n"
    "ds_read_u16_d16_hi v200, v200\n"
    "ds_write2_b32 v248, v50, v51 offset0:40 offset1:60\n"
    "v_mfma_f32_32x32x1_2b_f32 v[32:63], v254, v254, 0\n"
    "v_add_u32_e32 v248, -5440, v248\n"
    "v_lshlrev_b32_sdwa v188, 1, v176 dst_sel:DWORD dst_unused:UNUSED_PAD src0_sel:DWORD src1_sel:WORD_1\n"
    "v_lshlrev_b32_sdwa v204, 1, v177 dst_sel:DWORD dst_unused:UNUSED_PAD src0_sel:DWORD src1_sel:WORD_1\n"
    "v_lshlrev_b32_sdwa v189, 1, v178 dst_sel:DWORD dst_unused:UNUSED_PAD src0_sel:DWORD src1_sel:WORD_1\n"
    "v_lshlrev_b32_sdwa v205, 1, v179 dst_sel:DWORD dst_unused:UNUSED_PAD src0_sel:DWORD src1_sel:WORD_1\n"
    "v_lshlrev_b32_sdwa v190, 1, v180 dst_sel:DWORD dst_unused:UNUSED_PAD src0_sel:DWORD src1_sel:WORD_1\n"
    "v_lshlrev_b32_sdwa v206, 1, v181 dst_sel:DWORD dst_unused:UNUSED_PAD src0_sel:DWORD src1_sel:WORD_1\n"
    "v_lshlrev_b32_sdwa v191, 1, v182 dst_sel:DWORD dst_unused:UNUSED_PAD src0_sel:DWORD src1_sel:WORD_1\n"
    "v_fma_mix_f32 v234, v192, v232, 0 op_sel_hi:[1,1,0]\n"
    "v_fma_mix_f32 v232, v192, v232, 0 op_sel:[1,1,0] op_sel_hi:[1,1,0]\n"
    "v_mov_b32_e32 v254, 0\n"
    "v_cvt_pk_bf16_f32 v192, v234, v232\n"
    "v_pk_fma_f16 v232, v196, v196, s48\n"
    "v_pk_mul_f16 v232, v196, v232\n"
    "v_mfma_f32_16x16x32_bf16 v[172:175], a[140:143], v[148:151], v[172:175]\n"
    "s_waitcnt lgkmcnt(4)\n"
    "ds_read_u16 v185, v185\n"
    "ds_read_u16_d16_hi v201, v201\n"
    "v_mfma_f32_16x16x32_bf16 v[172:175], a[240:243], v[152:155], v[172:175]\n"
    "s_add_u32 s62, s62, 1572864\n"
    "ds_read_u16 v186, v186\n"
    "ds_read_b128 v[236:239], v244 offset:20480\n"
    "v_mfma_f32_16x16x32_bf16 v[176:179], a[88:91], v[156:159], 0\n"
    "s_add_u32 s63, s63, 1572864\n"
    "v_or_b32_e64 v199, v199, v207\n"
    "v_lshlrev_b32_sdwa v207, 1, v183 dst_sel:DWORD dst_unused:UNUSED_PAD src0_sel:DWORD src1_sel:WORD_1\n"
    "v_mfma_f32_16x16x32_bf16 v[176:179], a[136:139], v[160:163], v[176:179]\n"
    "ds_read_u16_d16_hi v202, v202\n"
    "v_exp_f16_e64 v234, v232\n"
    "v_mfma_f32_16x16x32_bf16 v[176:179], a[52:55], v[164:167], v[176:179]\n"
    "s_waitcnt lgkmcnt(5)\n"
    "ds_read_b128 v[216:219], v251 offset:10752\n"
    "v_exp_f16_sdwa v234, v232 dst_sel:WORD_1 dst_unused:UNUSED_PRESERVE src0_sel:WORD_1\n"
    "v_mfma_f32_16x16x32_bf16 v[180:183], a[92:95], v[156:159], 0\n"
    "s_barrier\n"
    "v_mfma_f32_16x16x32_bf16 v[180:183], a[140:143], v[160:163], v[180:183]\n"
    "s_add_u32 s52, s52, 1572864\n"
    "s_add_u32 s53, s53, 393216\n"
    "ds_read_b128 v[208:211], v250 offset:0\n"
    "ds_read_b128 v[212:215], v250 offset:320\n"
    "v_mfma_f32_16x16x32_bf16 v[180:183], a[240:243], v[164:167], v[180:183]\n"
    "s_waitcnt vmcnt(3)\n"
    "ds_read_b128 v[220:223], v251 offset:11088\n"
    "ds_read_u16 v187, v187\n"
    "v_smfmac_f32_32x32x32_bf16 v[128:143], a[248:251], a[224:231], v241\n"
    "s_nop 0\n"
    "ds_read_b128 a[244:247], v244 offset:22528\n"
    "ds_read_u16_d16_hi v203, v203\n"
    "v_pk_fma_f16 v234, v234, s49, s49\n"
    "v_or_b32_e64 v184, v184, v200\n"
    "v_rcp_f16_e32 v232, v234\n"
    "v_mfma_f32_16x16x32_bf16 v[96:99], a[208:211], v[192:195], v[96:99]\n"
    "s_waitcnt lgkmcnt(8)\n"
    "ds_read_u16 v188, v188\n"
    "v_rcp_f16_sdwa v232, v234 dst_sel:WORD_1 dst_unused:UNUSED_PRESERVE src0_sel:WORD_1\n"
    "v_mfma_f32_16x16x32_bf16 v[100:103], a[212:215], v[192:195], v[100:103]\n"
    "ds_read_u16_d16_hi v204, v204\n"
    "v_fma_mix_f32 v234, v196, v232, 0 op_sel_hi:[1,1,0]\n"
    "v_mfma_f32_16x16x32_bf16 v[112:115], a[216:219], v[192:195], v[112:115]\n"
    "v_fma_mix_f32 v232, v196, v232, 0 op_sel:[1,1,0] op_sel_hi:[1,1,0]\n"
    "v_or_b32_e32 v185, v185, v201\n"
    "v_mfma_f32_16x16x32_bf16 v[116:119], a[220:223], v[192:195], v[116:119]\n"
    "s_waitcnt lgkmcnt(8)\n"
    "ds_read_u16 v189, v189\n"
    "v_cvt_pk_bf16_f32 v196, v234, v232\n"
    "v_mfma_f32_16x16x32_bf16 v[128:131], v[236:239], v[192:195], v[128:131]\n"
    "v_lshlrev_b32_sdwa v201, 1, v171 dst_sel:DWORD dst_unused:UNUSED_PAD src0_sel:DWORD src1_sel:WORD_1\n"
    "v_or_b32_e32 v186, v186, v202\n"
    "v_mfma_f32_16x16x32_bf16 v[104:107], a[208:211], v[196:199], v[104:107]\n"
    "s_waitcnt lgkmcnt(7)\n"
    "ds_read_u16_d16_hi v205, v205\n"
    "v_lshlrev_b32_sdwa v202, 1, v173 dst_sel:DWORD dst_unused:UNUSED_PAD src0_sel:DWORD src1_sel:WORD_1\n"
    "v_smfmac_f32_16x16x64_bf16 v[32:35], v[216:219], v[208:215], v240\n"
    "s_waitcnt lgkmcnt(7)\n"
    "ds_read_u16 v190, v190\n"
    "v_smfmac_f32_16x16x64_bf16 v[36:39], v[220:223], v[208:215], v240 abid:1\n"
    "s_nop 0\n"
    "v_cvt_pk_bf16_f32 v96, v96, v97\n"
    "v_mfma_f32_16x16x32_bf16 v[108:111], a[212:215], v[196:199], v[108:111]\n"
    "s_nop 0\n"
    "ds_read_b128 v[208:211], v250 offset:640\n"
    "v_cvt_pk_bf16_f32 v97, v98, v99\n"
    "v_mfma_f32_16x16x32_bf16 v[120:123], a[216:219], v[196:199], v[120:123]\n"
    "s_waitcnt lgkmcnt(7)\n"
    "ds_read_b128 v[212:215], v250 offset:960\n"
    "v_cvt_pk_bf16_f32 v98, v100, v101\n"
    "v_mfma_f32_16x16x32_bf16 v[132:135], a[244:247], v[192:195], v[132:135]\n"
    "ds_read_b128 v[216:219], v252 offset:10752\n"
    "v_cvt_pk_f16_f32 v192, v168, v169\n"
    "v_mfma_f32_16x16x32_bf16 v[124:127], a[220:223], v[196:199], v[124:127]\n"
    "s_nop 0\n"
    "ds_read_b128 v[220:223], v252 offset:11088\n"
    "v_lshlrev_b32_sdwa v193, 1, v170 dst_sel:DWORD dst_unused:UNUSED_PAD src0_sel:DWORD src1_sel:WORD_1\n"
    "v_mfma_f32_16x16x32_bf16 v[136:139], v[236:239], v[196:199], v[136:139]\n"
    "s_waitcnt lgkmcnt(9)\n"
    "ds_read_u16_d16_hi v206, v206\n"
    "v_lshlrev_b32_sdwa v194, 1, v172 dst_sel:DWORD dst_unused:UNUSED_PAD src0_sel:DWORD src1_sel:WORD_1\n"
    "v_mfma_f32_16x16x32_bf16 v[140:143], a[244:247], v[196:199], v[140:143]\n"
    "v_lshlrev_b32_sdwa v195, 1, v174 dst_sel:DWORD dst_unused:UNUSED_PAD src0_sel:DWORD src1_sel:WORD_1\n"
    "v_cvt_pk_f16_f32 v196, v176, v177\n"
    "v_mfma_f32_16x16x32_bf16 v[168:171], a[96:99], v[144:147], 0\n"
    "s_nop 0\n"
    "ds_read_u16 v191, v191\n"
    "v_lshlrev_b32_sdwa v197, 1, v178 dst_sel:DWORD dst_unused:UNUSED_PAD src0_sel:DWORD src1_sel:WORD_1\n"
    "v_mfma_f32_16x16x32_bf16 v[168:171], a[144:147], v[148:151], v[168:171]\n"
    "s_waitcnt lgkmcnt(9)\n"
    "v_lshlrev_b32_sdwa v198, 1, v180 dst_sel:DWORD dst_unused:UNUSED_PAD src0_sel:DWORD src1_sel:WORD_1\n"
    "v_lshlrev_b32_sdwa v199, 1, v182 dst_sel:DWORD dst_unused:UNUSED_PAD src0_sel:DWORD src1_sel:WORD_1\n"
    "v_mfma_f32_16x16x32_bf16 v[168:171], a[56:59], v[152:155], v[168:171]\n"
    "ds_read_u16_d16_hi v207, v207\n"
    "v_or_b32_e64 v187, v187, v203\n"
    "v_lshlrev_b32_sdwa v203, 1, v175 dst_sel:DWORD dst_unused:UNUSED_PAD src0_sel:DWORD src1_sel:WORD_1\n"
    "v_mfma_f32_16x16x32_bf16 v[172:175], a[100:103], v[144:147], 0\n"
    "ds_read_u16 v193, v193\n"
    "v_or_b32_e64 v188, v188, v204\n"
    "v_mfma_f32_16x16x32_bf16 v[172:175], a[148:151], v[148:151], v[172:175]\n"
    "s_waitcnt lgkmcnt(9)\n"
    "v_or_b32_e32 v189, v189, v205\n"
    "v_lshlrev_b32_sdwa v205, 1, v179 dst_sel:DWORD dst_unused:UNUSED_PAD src0_sel:DWORD src1_sel:WORD_1\n"
    "v_mfma_f32_16x16x32_bf16 v[172:175], a[60:63], v[152:155], v[172:175]\n"
    "ds_read_u16_d16_hi v201, v201\n"
    "v_cvt_pk_bf16_f32 v99, v102, v103\n"
    "v_mfma_f32_16x16x32_bf16 v[176:179], a[96:99], v[156:159], 0\n"
    "v_cvt_pk_bf16_f32 v112, v112, v113\n"
    "v_cvt_pk_bf16_f32 v113, v114, v115\n"
    "v_mfma_f32_16x16x32_bf16 v[176:179], a[144:147], v[160:163], v[176:179]\n"
    "v_cvt_pk_bf16_f32 v114, v116, v117\n"
    "v_cvt_pk_bf16_f32 v115, v118, v119\n"
    "v_mfma_f32_16x16x32_bf16 v[176:179], a[56:59], v[164:167], v[176:179]\n"
    "s_waitcnt lgkmcnt(6)\n"
    "ds_write_b128 v243, v[96:99] offset:24640\n"
    "v_smfmac_f32_16x16x64_bf16 v[32:35], v[216:219], v[208:215], v240\n"
    "s_waitcnt lgkmcnt(6)\n"
    "v_cvt_pk_bf16_f32 v128, v128, v129\n"
    "v_smfmac_f32_16x16x64_bf16 v[36:39], v[220:223], v[208:215], v240 abid:1\n"
    "v_cvt_pk_bf16_f32 v129, v130, v131\n"
    "ds_write_b128 v243, v[112:115] offset:26688\n"
    "s_waitcnt lgkmcnt(6)\n"
    "v_or_b32_e32 v190, v190, v206\n"
    "v_mfma_f32_16x16x32_bf16 v[96:99], a[0:3], v[184:187], 0\n"
    "v_lshlrev_b32_sdwa v206, 1, v181 dst_sel:DWORD dst_unused:UNUSED_PAD src0_sel:DWORD src1_sel:WORD_1\n"
    "v_cvt_pk_bf16_f32 v130, v132, v133\n"
    "ds_read_b128 v[208:211], v250 offset:80\n"
    "ds_read_b128 v[212:215], v250 offset:400\n"
    "v_pk_add_f32 v[224:225], v[224:225], v[32:33]\n"
    "v_pk_add_f32 v[226:227], v[226:227], v[34:35]\n"
    "v_pk_fma_f32 v[228:229], v[32:33], v[32:33], v[228:229]\n"
    "v_pk_fma_f32 v[230:231], v[34:35], v[34:35], v[230:231]\n"
    "v_pk_add_f32 v[224:225], v[224:225], v[36:37]\n"
    "v_pk_add_f32 v[226:227], v[226:227], v[38:39]\n"
    "v_pk_fma_f32 v[228:229], v[36:37], v[36:37], v[228:229]\n"
    "v_pk_fma_f32 v[230:231], v[38:39], v[38:39], v[230:231]\n"
    "v_mfma_f32_16x16x32_bf16 v[100:103], a[4:7], v[184:187], 0\n"
    "s_waitcnt lgkmcnt(5)\n"
    "ds_read_b128 v[216:219], v251 offset:11424\n"
    "ds_read_b128 v[220:223], v251 offset:11760\n"
    "v_mfma_f32_16x16x32_bf16 v[112:115], a[8:11], v[184:187], 0\n"
    "s_waitcnt lgkmcnt(5)\n"
    "ds_read_u16 v194, v194\n"
    "ds_read_u16_d16_hi v202, v202\n"
    "v_or_b32_e64 v191, v191, v207\n"
    "v_mfma_f32_16x16x32_bf16 v[116:119], a[12:15], v[184:187], 0\n"
    "ds_read_b128 v[236:239], v244 offset:0\n"
    "ds_read_b128 a[252:255], v249 offset:0\n"
    "v_lshlrev_b32_sdwa v207, 1, v183 dst_sel:DWORD dst_unused:UNUSED_PAD src0_sel:DWORD src1_sel:WORD_1\n"
    "v_mfma_f32_16x16x32_bf16 v[180:183], a[100:103], v[156:159], 0\n"
    "ds_read_u16 v195, v195\n"
    "v_or_b32_e64 v193, v193, v201\n"
    "v_mfma_f32_16x16x32_bf16 v[180:183], a[148:151], v[160:163], v[180:183]\n"
    "v_cvt_pk_bf16_f32 v131, v134, v135\n"
    "v_cvt_pk_bf16_f32 v104, v104, v105\n"
    "v_mfma_f32_16x16x32_bf16 v[180:183], a[60:63], v[164:167], v[180:183]\n"
    "v_cvt_pk_bf16_f32 v105, v106, v107\n"
    "v_cvt_pk_bf16_f32 v106, v108, v109\n"
    "v_cvt_pk_bf16_f32 v107, v110, v111\n"
    "v_mfma_f32_16x16x32_bf16 v[108:111], a[4:7], v[188:191], 0\n"
    "ds_write_b128 v243, v[128:131] offset:28736\n"
    "s_waitcnt lgkmcnt(7)\n"
    "v_smfmac_f32_16x16x64_bf16 v[40:43], v[216:219], v[208:215], v240\n"
    "s_waitcnt lgkmcnt(6)\n"
    "v_cvt_pk_bf16_f32 v120, v120, v121\n"
    "v_smfmac_f32_16x16x64_bf16 v[44:47], v[220:223], v[208:215], v240 abid:1\n"
    "ds_write_b128 v243, v[104:107] offset:25664\n"
    "v_mfma_f32_16x16x32_bf16 v[104:107], a[0:3], v[188:191], 0\n"
    "v_cvt_pk_bf16_f32 v121, v122, v123\n"
    "v_cvt_pk_bf16_f32 v122, v124, v125\n"
    "v_cvt_pk_bf16_f32 v123, v126, v127\n"
    "v_pk_fma_f16 v232, v192, v192, s48\n"
    "v_mfma_f32_16x16x32_bf16 v[124:127], a[12:15], v[188:191], 0\n"
    "ds_read_b128 v[208:211], v250 offset:720\n"
    "ds_write_b128 v243, v[120:123] offset:27712\n"
    "v_mfma_f32_16x16x32_bf16 v[120:123], a[8:11], v[188:191], 0\n"
    "s_waitcnt lgkmcnt(5)\n"
    "buffer_store_dwordx4 a[252:255], v255, s[44:47], s57 offen offset:0 sc1\n"
    "v_mfma_f32_16x16x32_bf16 v[128:131], v[236:239], v[184:187], 0\n"
    "s_nop 0\n"
    "ds_read_b128 v[212:215], v250 offset:1040\n"
    "ds_read_b128 v[216:219], v252 offset:11424\n"
    "v_or_b32_e64 v194, v194, v202\n"
    "ds_read_b128 v[220:223], v252 offset:11760\n"
    "ds_read_u16_d16_hi v203, v203\n"
    "v_cvt_pk_bf16_f32 v136, v136, v137\n"
    "v_cvt_pk_bf16_f32 v137, v138, v139\n"
    "v_cvt_pk_bf16_f32 v138, v140, v141\n"
    "v_cvt_pk_bf16_f32 v139, v142, v143\n"
    "ds_write_b128 v243, v[136:139] offset:29760\n"
    "v_pk_mul_f16 v232, v192, v232\n"
    "v_mfma_f32_16x16x32_bf16 v[136:139], v[236:239], v[188:191], 0\n"
    "v_exp_f16_e64 v234, v232\n"
    "v_lshlrev_b32_sdwa v201, 1, v171 dst_sel:DWORD dst_unused:UNUSED_PAD src0_sel:DWORD src1_sel:WORD_1\n"
    "v_exp_f16_sdwa v234, v232 dst_sel:WORD_1 dst_unused:UNUSED_PRESERVE src0_sel:WORD_1\n"
    "ds_read_b128 a[244:247], v244 offset:2048\n"
    "ds_read_u16 v197, v197\n"
    "v_pk_fma_f16 v234, v234, s49, s49\n"
    "v_lshlrev_b32_sdwa v202, 1, v173 dst_sel:DWORD dst_unused:UNUSED_PAD src0_sel:DWORD src1_sel:WORD_1\n"
    "v_rcp_f16_e64 v232, v234\n"
    "ds_read_u16_d16_hi v205, v205\n"
    "ds_read_u16 v198, v198\n"
    "s_waitcnt lgkmcnt(7)\n"
    "v_smfmac_f32_16x16x64_bf16 v[40:43], v[216:219], v[208:215], v240\n"
    "s_waitcnt lgkmcnt(6)\n"
    "ds_read_b128 v[216:219], v251 offset:12096\n"
    "ds_read_u16_d16_hi v206, v206\n"
    "v_smfmac_f32_16x16x64_bf16 v[44:47], v[220:223], v[208:215], v240 abid:1\n"
    "ds_read_b128 v[208:211], v250 offset:160\n"
    "v_rcp_f16_sdwa v232, v234 dst_sel:WORD_1 dst_unused:UNUSED_PRESERVE src0_sel:WORD_1\n"
    "ds_read_b128 v[212:215], v250 offset:480\n"
    "s_waitcnt lgkmcnt(8)\n"
    "v_or_b32_e32 v195, v195, v203\n"
    "v_pk_add_f32 v[224:225], v[224:225], v[40:41]\n"
    "v_pk_add_f32 v[226:227], v[226:227], v[42:43]\n"
    "v_pk_fma_f32 v[228:229], v[40:41], v[40:41], v[228:229]\n"
    "v_pk_fma_f32 v[230:231], v[42:43], v[42:43], v[230:231]\n"
    "v_pk_add_f32 v[224:225], v[224:225], v[44:45]\n"
    "v_pk_add_f32 v[226:227], v[226:227], v[46:47]\n"
    "v_pk_fma_f32 v[228:229], v[44:45], v[44:45], v[228:229]\n"
    "v_pk_fma_f32 v[230:231], v[46:47], v[46:47], v[230:231]\n"
    "s_waitcnt lgkmcnt(7)\n"
    "v_mfma_f32_16x16x32_bf16 v[132:135], a[244:247], v[184:187], 0\n"
    "s_nop 0\n"
    "ds_read_b128 v[220:223], v251 offset:12432\n"
    "ds_read_u16 v199, v199\n"
    "v_mfma_f32_16x16x32_bf16 v[140:143], a[244:247], v[188:191], 0\n"
    "ds_read_u16_d16_hi v207, v207\n"
    "v_cvt_pk_f16_f32 v184, v168, v169\n"
    "v_lshlrev_b32_sdwa v185, 1, v170 dst_sel:DWORD dst_unused:UNUSED_PAD src0_sel:DWORD src1_sel:WORD_1\n"
    "v_mfma_f32_16x16x32_bf16 v[168:171], a[104:107], v[144:147], 0\n"
    "ds_read_b128 a[252:255], v249 offset:2048\n"
    "v_lshlrev_b32_sdwa v186, 1, v172 dst_sel:DWORD dst_unused:UNUSED_PAD src0_sel:DWORD src1_sel:WORD_1\n"
    "v_mfma_f32_16x16x32_bf16 v[168:171], a[152:155], v[148:151], v[168:171]\n"
    "s_waitcnt lgkmcnt(9)\n"
    "ds_read_u16 v185, v185\n"
    "v_or_b32_e32 v197, v197, v205\n"
    "v_mfma_f32_16x16x32_bf16 v[168:171], a[64:67], v[152:155], v[168:171]\n"
    "v_lshlrev_b32_sdwa v187, 1, v174 dst_sel:DWORD dst_unused:UNUSED_PAD src0_sel:DWORD src1_sel:WORD_1\n"
    "v_lshlrev_b32_sdwa v203, 1, v175 dst_sel:DWORD dst_unused:UNUSED_PAD src0_sel:DWORD src1_sel:WORD_1\n"
    "v_mfma_f32_16x16x32_bf16 v[172:175], a[108:111], v[144:147], 0\n"
    "ds_read_u16_d16_hi v201, v201\n"
    "v_cvt_pk_f16_f32 v188, v176, v177\n"
    "v_lshlrev_b32_sdwa v189, 1, v178 dst_sel:DWORD dst_unused:UNUSED_PAD src0_sel:DWORD src1_sel:WORD_1\n"
    "v_lshlrev_b32_sdwa v205, 1, v179 dst_sel:DWORD dst_unused:UNUSED_PAD src0_sel:DWORD src1_sel:WORD_1\n"
    "v_lshlrev_b32_sdwa v190, 1, v180 dst_sel:DWORD dst_unused:UNUSED_PAD src0_sel:DWORD src1_sel:WORD_1\n"
    "v_lshlrev_b32_sdwa v191, 1, v182 dst_sel:DWORD dst_unused:UNUSED_PAD src0_sel:DWORD src1_sel:WORD_1\n"
    "v_fma_mix_f32 v234, v192, v232, 0 op_sel_hi:[1,1,0]\n"
    "v_mfma_f32_16x16x32_bf16 v[172:175], a[156:159], v[148:151], v[172:175]\n"
    "s_waitcnt lgkmcnt(7)\n"
    "ds_read_u16 v186, v186\n"
    "v_or_b32_e32 v198, v198, v206\n"
    "v_mfma_f32_16x16x32_bf16 v[172:175], a[68:71], v[152:155], v[172:175]\n"
    "s_waitcnt lgkmcnt(7)\n"
    "ds_read_u16_d16_hi v202, v202\n"
    "v_lshlrev_b32_sdwa v206, 1, v181 dst_sel:DWORD dst_unused:UNUSED_PAD src0_sel:DWORD src1_sel:WORD_1\n"
    "v_smfmac_f32_16x16x64_bf16 v[48:51], v[216:219], v[208:215], v240\n"
    "s_waitcnt lgkmcnt(7)\n"
    "ds_read_b128 v[216:219], v252 offset:12096\n"
    "v_smfmac_f32_16x16x64_bf16 v[52:55], v[220:223], v[208:215], v240 abid:1\n"
    "ds_read_b128 v[208:211], v250 offset:800\n"
    "v_fma_mix_f32 v232, v192, v232, 0 op_sel:[1,1,0] op_sel_hi:[1,1,0]\n"
    "v_mfma_f32_16x16x32_bf16 v[176:179], a[104:107], v[156:159], 0\n"
    "ds_read_b128 v[212:215], v250 offset:1120\n"
    "v_cvt_pk_bf16_f32 v192, v234, v232\n"
    "v_mfma_f32_16x16x32_bf16 v[176:179], a[152:155], v[160:163], v[176:179]\n"
    "ds_read_b128 v[220:223], v252 offset:12432\n"
    "v_pk_fma_f16 v232, v196, v196, s48\n"
    "v_mfma_f32_16x16x32_bf16 v[176:179], a[64:67], v[164:167], v[176:179]\n"
    "s_waitcnt lgkmcnt(8)\n"
    "ds_read_b128 v[236:239], v244 offset:4096\n"
    "buffer_store_dwordx4 a[252:255], v255, s[44:47], s57 offen offset:64 sc1\n"
    "v_or_b32_e32 v199, v199, v207\n"
    "v_lshlrev_b32_sdwa v207, 1, v183 dst_sel:DWORD dst_unused:UNUSED_PAD src0_sel:DWORD src1_sel:WORD_1\n"
    "v_mfma_f32_16x16x32_bf16 v[180:183], a[108:111], v[156:159], 0\n"
    "ds_read_u16 v187, v187\n"
    "ds_read_u16_d16_hi v203, v203\n"
    "v_pk_mul_f16 v232, v196, v232\n"
    "v_mfma_f32_16x16x32_bf16 v[180:183], a[156:159], v[160:163], v[180:183]\n"
    "v_exp_f16_e32 v234, v232\n"
    "v_mfma_f32_16x16x32_bf16 v[180:183], a[68:71], v[164:167], v[180:183]\n"
    "s_waitcnt lgkmcnt(9)\n"
    "ds_read_b128 a[244:247], v244 offset:6144\n"
    "v_exp_f16_sdwa v234, v232 dst_sel:WORD_1 dst_unused:UNUSED_PRESERVE src0_sel:WORD_1\n"
    "v_or_b32_e64 v185, v185, v201\n"
    "v_pk_fma_f16 v234, v234, s49, s49\n"
    "v_mfma_f32_16x16x32_bf16 v[96:99], a[16:19], v[192:195], v[96:99]\n"
    "ds_read_b128 a[252:255], v249 offset:4096\n"
    "ds_read_u16 v189, v189\n"
    "v_mfma_f32_16x16x32_bf16 v[100:103], a[20:23], v[192:195], v[100:103]\n"
    "v_rcp_f16_e32 v232, v234\n"
    "v_mfma_f32_16x16x32_bf16 v[112:115], a[24:27], v[192:195], v[112:115]\n"
    "s_waitcnt lgkmcnt(9)\n"
    "ds_read_u16_d16_hi v205, v205\n"
    "v_rcp_f16_sdwa v232, v234 dst_sel:WORD_1 dst_unused:UNUSED_PRESERVE src0_sel:WORD_1\n"
    "v_mfma_f32_16x16x32_bf16 v[116:119], a[28:31], v[192:195], v[116:119]\n"
    "v_fma_mix_f32 v234, v196, v232, 0 op_sel_hi:[1,1,0]\n"
    "v_fma_mix_f32 v232, v196, v232, 0 op_sel:[1,1,0] op_sel_hi:[1,1,0]\n"
    "s_waitcnt lgkmcnt(8)\n"
    "v_smfmac_f32_16x16x64_bf16 v[48:51], v[216:219], v[208:215], v240\n"
    "s_waitcnt lgkmcnt(7)\n"
    "ds_read_b128 v[216:219], v251 offset:12768\n"
    "ds_read_u16 v190, v190\n"
    "v_smfmac_f32_16x16x64_bf16 v[52:55], v[220:223], v[208:215], v240 abid:1\n"
    "s_waitcnt lgkmcnt(8)\n"
    "v_or_b32_e32 v186, v186, v202\n"
    "v_mfma_f32_16x16x32_bf16 v[128:131], v[236:239], v[192:195], v[128:131]\n"
    "ds_read_b128 v[208:211], v250 offset:240\n"
    "v_cvt_pk_bf16_f32 v196, v234, v232\n"
    "ds_read_b128 v[212:215], v250 offset:560\n"
    "v_pk_add_f32 v[224:225], v[224:225], v[48:49]\n"
    "v_pk_add_f32 v[226:227], v[226:227], v[50:51]\n"
    "v_pk_fma_f32 v[228:229], v[48:49], v[48:49], v[228:229]\n"
    "v_pk_fma_f32 v[230:231], v[50:51], v[50:51], v[230:231]\n"
    "v_pk_add_f32 v[224:225], v[224:225], v[52:53]\n"
    "v_pk_add_f32 v[226:227], v[226:227], v[54:55]\n"
    "v_pk_fma_f32 v[228:229], v[52:53], v[52:53], v[228:229]\n"
    "v_pk_fma_f32 v[230:231], v[54:55], v[54:55], v[230:231]\n"
    "v_mfma_f32_16x16x32_bf16 v[104:107], a[16:19], v[196:199], v[104:107]\n"
    "s_waitcnt lgkmcnt(7)\n"
    "ds_read_b128 v[220:223], v251 offset:13104\n"
    "ds_read_u16_d16_hi v206, v206\n"
    "v_mfma_f32_16x16x32_bf16 v[132:135], a[244:247], v[192:195], v[132:135]\n"
    "s_waitcnt lgkmcnt(8)\n"
    "ds_read_u16 v191, v191\n"
    "buffer_store_dwordx4 a[252:255], v255, s[44:47], s57 offen offset:128 sc1\n"
    "v_mfma_f32_16x16x32_bf16 v[108:111], a[20:23], v[196:199], v[108:111]\n"
    "ds_read_u16_d16_hi v207, v207\n"
    "v_pk_fma_f16 v232, v184, v184, s48\n"
    "v_mfma_f32_16x16x32_bf16 v[120:123], a[24:27], v[196:199], v[120:123]\n"
    "ds_read_b128 a[252:255], v249 offset:1024\n"
    "v_or_b32_e64 v187, v187, v203\n"
    "v_mfma_f32_16x16x32_bf16 v[124:127], a[28:31], v[196:199], v[124:127]\n"
    "ds_read_b128 a[240:243], v244 offset:34816\n"
    "v_pk_mul_f16 v232, v184, v232\n"
    "v_mfma_f32_16x16x32_bf16 v[136:139], v[236:239], v[196:199], v[136:139]\n"
    "s_waitcnt lgkmcnt(9)\n"
    "v_exp_f16_e32 v234, v232\n"
    "v_mfma_f32_16x16x32_bf16 v[140:143], a[244:247], v[196:199], v[140:143]\n"
    "v_or_b32_e64 v189, v189, v205\n"
    "v_exp_f16_sdwa v234, v232 dst_sel:WORD_1 dst_unused:UNUSED_PRESERVE src0_sel:WORD_1\n"
    "v_lshlrev_b32_sdwa v192, 1, v168 dst_sel:DWORD dst_unused:UNUSED_PAD src0_sel:DWORD src1_sel:WORD_1\n"
    "v_pk_fma_f16 v234, v234, s49, s49\n"
    "v_lshlrev_b32_sdwa v200, 1, v169 dst_sel:DWORD dst_unused:UNUSED_PAD src0_sel:DWORD src1_sel:WORD_1\n"
    "v_lshlrev_b32_sdwa v193, 1, v170 dst_sel:DWORD dst_unused:UNUSED_PAD src0_sel:DWORD src1_sel:WORD_1\n"
    "s_waitcnt lgkmcnt(6)\n"
    "v_smfmac_f32_16x16x64_bf16 v[56:59], v[216:219], v[208:215], v240\n"
    "s_waitcnt lgkmcnt(5)\n"
    "ds_read_b128 v[216:219], v252 offset:12768\n"
    "ds_read_u16 v192, v192\n"
    "v_smfmac_f32_16x16x64_bf16 v[60:63], v[220:223], v[208:215], v240 abid:1\n"
    "ds_read_b128 v[208:211], v250 offset:880\n"
    "ds_read_b128 v[212:215], v250 offset:1200\n"
    "v_lshlrev_b32_sdwa v201, 1, v171 dst_sel:DWORD dst_unused:UNUSED_PAD src0_sel:DWORD src1_sel:WORD_1\n"
    "v_mfma_f32_16x16x32_bf16 v[168:171], a[112:115], v[144:147], 0\n"
    "s_nop 0\n"
    "ds_read_b128 v[220:223], v252 offset:13104\n"
    "ds_read_u16_d16_hi v200, v200\n"
    "v_mfma_f32_16x16x32_bf16 v[168:171], a[160:163], v[148:151], v[168:171]\n"
    "s_waitcnt lgkmcnt(10)\n"
    "v_lshlrev_b32_sdwa v194, 1, v172 dst_sel:DWORD dst_unused:UNUSED_PAD src0_sel:DWORD src1_sel:WORD_1\n"
    "v_lshlrev_b32_sdwa v202, 1, v173 dst_sel:DWORD dst_unused:UNUSED_PAD src0_sel:DWORD src1_sel:WORD_1\n"
    "v_lshlrev_b32_sdwa v195, 1, v174 dst_sel:DWORD dst_unused:UNUSED_PAD src0_sel:DWORD src1_sel:WORD_1\n"
    "v_lshlrev_b32_sdwa v203, 1, v175 dst_sel:DWORD dst_unused:UNUSED_PAD src0_sel:DWORD src1_sel:WORD_1\n"
    "v_rcp_f16_e64 v232, v234\n"
    "v_mfma_f32_16x16x32_bf16 v[168:171], a[72:75], v[152:155], v[168:171]\n"
    "ds_read_u16 v193, v193\n"
    "ds_read_u16_d16_hi v201, v201\n"
    "v_or_b32_e32 v190, v190, v206\n"
    "v_rcp_f16_sdwa v232, v234 dst_sel:WORD_1 dst_unused:UNUSED_PRESERVE src0_sel:WORD_1\n"
    "v_mfma_f32_16x16x32_bf16 v[172:175], a[116:119], v[144:147], 0\n"
    "s_waitcnt lgkmcnt(8)\n"
    "ds_read_u16 v194, v194\n"
    "buffer_store_dwordx4 a[252:255], v255, s[44:47], s88 offen offset:0 sc1\n"
    "v_lshlrev_b32_sdwa v196, 1, v176 dst_sel:DWORD dst_unused:UNUSED_PAD src0_sel:DWORD src1_sel:WORD_1\n"
    "v_lshlrev_b32_sdwa v204, 1, v177 dst_sel:DWORD dst_unused:UNUSED_PAD src0_sel:DWORD src1_sel:WORD_1\n"
    "v_lshlrev_b32_sdwa v197, 1, v178 dst_sel:DWORD dst_unused:UNUSED_PAD src0_sel:DWORD src1_sel:WORD_1\n"
    "v_lshlrev_b32_sdwa v205, 1, v179 dst_sel:DWORD dst_unused:UNUSED_PAD src0_sel:DWORD src1_sel:WORD_1\n"
    "v_lshlrev_b32_sdwa v198, 1, v180 dst_sel:DWORD dst_unused:UNUSED_PAD src0_sel:DWORD src1_sel:WORD_1\n"
    "v_lshlrev_b32_sdwa v206, 1, v181 dst_sel:DWORD dst_unused:UNUSED_PAD src0_sel:DWORD src1_sel:WORD_1\n"
    "v_lshlrev_b32_sdwa v199, 1, v182 dst_sel:DWORD dst_unused:UNUSED_PAD src0_sel:DWORD src1_sel:WORD_1\n"
    "v_fma_mix_f32 v234, v184, v232, 0 op_sel_hi:[1,1,0]\n"
    "v_mfma_f32_16x16x32_bf16 v[172:175], a[164:167], v[148:151], v[172:175]\n"
    "ds_read_b128 v[236:239], v244 offset:8192\n"
    "ds_read_u16_d16_hi v202, v202\n"
    "v_mfma_f32_16x16x32_bf16 v[172:175], a[240:243], v[152:155], v[172:175]\n"
    "s_waitcnt lgkmcnt(7)\n"
    "ds_read_u16 v195, v195\n"
    "v_or_b32_e64 v191, v191, v207\n"
    "v_smfmac_f32_16x16x64_bf16 v[56:59], v[216:219], v[208:215], v240\n"
    "s_waitcnt lgkmcnt(7)\n"
    "ds_read_b128 a[244:247], v244 offset:10240\n"
    "v_smfmac_f32_16x16x64_bf16 v[60:63], v[220:223], v[208:215], v240 abid:1\n"
    "s_barrier\n"
    "v_lshlrev_b32_sdwa v207, 1, v183 dst_sel:DWORD dst_unused:UNUSED_PAD src0_sel:DWORD src1_sel:WORD_1\n"
    "v_fma_mix_f32 v232, v184, v232, 0 op_sel:[1,1,0] op_sel_hi:[1,1,0]\n"
    "v_mfma_f32_16x16x32_bf16 v[176:179], a[112:115], v[156:159], 0\n"
    "s_waitcnt vmcnt(6) lgkmcnt(6)\n"
    "ds_read_u16_d16_hi v203, v203\n"
    "ds_write2_b32 v248, v64, v65 offset0:0 offset1:20\n"
    "v_cvt_pk_bf16_f32 v184, v234, v232\n"
    "v_pk_add_f32 v[224:225], v[224:225], v[56:57]\n"
    "v_pk_add_f32 v[226:227], v[226:227], v[58:59]\n"
    "v_pk_fma_f32 v[228:229], v[56:57], v[56:57], v[228:229]\n"
    "v_pk_fma_f32 v[230:231], v[58:59], v[58:59], v[230:231]\n"
    "v_pk_add_f32 v[224:225], v[224:225], v[60:61]\n"
    "v_pk_add_f32 v[226:227], v[226:227], v[62:63]\n"
    "v_pk_fma_f32 v[228:229], v[60:61], v[60:61], v[228:229]\n"
    "v_pk_fma_f32 v[230:231], v[62:63], v[62:63], v[230:231]\n"
    "v_mfma_f32_16x16x32_bf16 v[176:179], a[160:163], v[160:163], v[176:179]\n"
    "ds_read_u16 v196, v196\n"
    "ds_write2_b32 v248, v66, v67 offset0:40 offset1:60\n"
    "v_mfma_f32_16x16x32_bf16 v[176:179], a[72:75], v[164:167], v[176:179]\n"
    "v_add_u32_e32 v248, 1360, v248\n"
    "v_pk_fma_f16 v232, v188, v188, s48\n"
    "v_mfma_f32_16x16x32_bf16 v[180:183], a[116:119], v[156:159], 0\n"
    "s_waitcnt lgkmcnt(6)\n"
    "ds_read_u16_d16_hi v204, v204\n"
    "ds_write2_b32 v235, v84, v85 offset0:0 offset1:20\n"
    "v_pk_mul_f16 v232, v188, v232\n"
    "v_mfma_f32_16x16x32_bf16 v[180:183], a[164:167], v[160:163], v[180:183]\n"
    "v_exp_f16_e32 v234, v232\n"
    "v_mfma_f32_16x16x32_bf16 v[180:183], a[240:243], v[164:167], v[180:183]\n"
    "ds_read_u16 v197, v197\n"
    "ds_write2_b32 v235, v86, v87 offset0:40 offset1:60\n"
    "v_add_u32_e32 v235, 1360, v235\n"
    "v_exp_f16_sdwa v234, v232 dst_sel:WORD_1 dst_unused:UNUSED_PRESERVE src0_sel:WORD_1\n"
    "v_or_b32_e32 v192, v192, v200\n"
    "v_pk_fma_f16 v234, v234, s49, s49\n"
    "v_mfma_f32_16x16x32_bf16 v[96:99], a[32:35], v[184:187], v[96:99]\n"
    "s_waitcnt vmcnt(5) lgkmcnt(6)\n"
    "ds_read_b128 a[252:255], v249 offset:3072\n"
    "ds_write2_b32 v248, v68, v69 offset0:0 offset1:20\n"
    "v_mfma_f32_16x16x32_bf16 v[100:103], a[36:39], v[184:187], v[100:103]\n"
    "v_rcp_f16_e32 v232, v234\n"
    "v_mfma_f32_16x16x32_bf16 v[112:115], a[40:43], v[184:187], v[112:115]\n"
    "ds_read_u16_d16_hi v205, v205\n"
    "ds_write2_b32 v248, v70, v71 offset0:40 offset1:60\n"
    "v_add_u32_e32 v248, 1360, v248\n"
    "v_rcp_f16_sdwa v232, v234 dst_sel:WORD_1 dst_unused:UNUSED_PRESERVE src0_sel:WORD_1\n"
    "v_mfma_f32_16x16x32_bf16 v[116:119], a[44:47], v[184:187], v[116:119]\n"
    "s_waitcnt lgkmcnt(6)\n"
    "ds_read_u16 v198, v198\n"
    "ds_read_u16_d16_hi v206, v206\n"
    "v_fma_mix_f32 v234, v188, v232, 0 op_sel_hi:[1,1,0]\n"
    "v_mfma_f32_16x16x32_bf16 v[128:131], v[236:239], v[184:187], v[128:131]\n"
    "ds_read_u16 v199, v199\n"
    "v_fma_mix_f32 v232, v188, v232, 0 op_sel:[1,1,0] op_sel_hi:[1,1,0]\n"
    "v_mfma_f32_16x16x32_bf16 v[132:135], a[244:247], v[184:187], v[132:135]\n"
    "ds_read_u16_d16_hi v207, v207\n"
    "v_cvt_pk_bf16_f32 v188, v234, v232\n"
    "v_or_b32_e32 v193, v193, v201\n"
    "v_cvt_pk_f16_f32 v184, v168, v169\n"
    "v_mfma_f32_16x16x32_bf16 v[104:107], a[32:35], v[188:191], v[104:107]\n"
    "s_waitcnt lgkmcnt(6)\n"
    "ds_read_b128 a[240:243], v244 offset:38912\n"
    "buffer_store_dwordx4 a[252:255], v255, s[44:47], s88 offen offset:64 sc1\n"
    "v_mfma_f32_16x16x32_bf16 v[108:111], a[36:39], v[188:191], v[108:111]\n"
    "v_lshlrev_b32_sdwa v185, 1, v170 dst_sel:DWORD dst_unused:UNUSED_PAD src0_sel:DWORD src1_sel:WORD_1\n"
    "v_lshlrev_b32_sdwa v201, 1, v171 dst_sel:DWORD dst_unused:UNUSED_PAD src0_sel:DWORD src1_sel:WORD_1\n"
    "v_mfma_f32_16x16x32_bf16 v[120:123], a[40:43], v[188:191], v[120:123]\n"
    "ds_read_u16 v185, v185\n"
    "ds_read_b128 a[252:255], v249 offset:5120\n"
    "v_mfma_f32_16x16x32_bf16 v[124:127], a[44:47], v[188:191], v[124:127]\n"
    "s_waitcnt lgkmcnt(7)\n"
    "ds_read_u16_d16_hi v201, v201\n"
    "v_lshlrev_b32_sdwa v186, 1, v172 dst_sel:DWORD dst_unused:UNUSED_PAD src0_sel:DWORD src1_sel:WORD_1\n"
    "v_mfma_f32_16x16x32_bf16 v[136:139], v[236:239], v[188:191], v[136:139]\n"
    "v_or_b32_e32 v194, v194, v202\n"
    "v_lshlrev_b32_sdwa v202, 1, v173 dst_sel:DWORD dst_unused:UNUSED_PAD src0_sel:DWORD src1_sel:WORD_1\n"
    "v_mfma_f32_16x16x32_bf16 v[140:143], a[244:247], v[188:191], v[140:143]\n"
    "v_lshlrev_b32_sdwa v187, 1, v174 dst_sel:DWORD dst_unused:UNUSED_PAD src0_sel:DWORD src1_sel:WORD_1\n"
    "v_cvt_pk_f16_f32 v188, v176, v177\n"
    "v_mfma_f32_16x16x32_bf16 v[168:171], a[120:123], v[144:147], 0\n"
    "ds_write2_b32 v235, v88, v89 offset0:0 offset1:20\n"
    "v_mfma_f32_16x16x32_bf16 v[168:171], a[168:171], v[148:151], v[168:171]\n"
    "s_nop 0\n"
    "v_lshlrev_b32_sdwa v189, 1, v178 dst_sel:DWORD dst_unused:UNUSED_PAD src0_sel:DWORD src1_sel:WORD_1\n"
    "v_lshlrev_b32_sdwa v190, 1, v180 dst_sel:DWORD dst_unused:UNUSED_PAD src0_sel:DWORD src1_sel:WORD_1\n"
    "v_mfma_f32_16x16x32_bf16 v[168:171], a[76:79], v[152:155], v[168:171]\n"
    "s_waitcnt lgkmcnt(7)\n"
    "ds_read_u16 v186, v186\n"
    "ds_write2_b32 v235, v90, v91 offset0:40 offset1:60\n"
    "v_add_u32_e32 v235, -1360, v235\n"
    "v_lshlrev_b32_sdwa v191, 1, v182 dst_sel:DWORD dst_unused:UNUSED_PAD src0_sel:DWORD src1_sel:WORD_1\n"
    "v_or_b32_e64 v195, v195, v203\n"
    "v_lshlrev_b32_sdwa v203, 1, v175 dst_sel:DWORD dst_unused:UNUSED_PAD src0_sel:DWORD src1_sel:WORD_1\n"
    "v_mfma_f32_16x16x32_bf16 v[172:175], a[124:127], v[144:147], 0\n"
    "ds_read_u16_d16_hi v202, v202\n"
    "ds_read_b128 v[236:239], v244 offset:12288\n"
    "v_mfma_f32_16x16x32_bf16 v[172:175], a[172:175], v[148:151], v[172:175]\n"
    "s_waitcnt lgkmcnt(8)\n"
    "ds_read_u16 v187, v187\n"
    "v_or_b32_e32 v196, v196, v204\n"
    "v_mfma_f32_16x16x32_bf16 v[172:175], a[240:243], v[152:155], v[172:175]\n"
    "v_or_b32_e64 v197, v197, v205\n"
    "v_lshlrev_b32_sdwa v205, 1, v179 dst_sel:DWORD dst_unused:UNUSED_PAD src0_sel:DWORD src1_sel:WORD_1\n"
    "v_mfma_f32_16x16x32_bf16 v[176:179], a[120:123], v[156:159], 0\n"
    "s_waitcnt lgkmcnt(7)\n"
    "buffer_store_dwordx4 a[252:255], v255, s[44:47], s88 offen offset:128 sc1\n"
    "ds_write2_b32 v248, v72, v73 offset0:0 offset1:20\n"
    "v_mfma_f32_16x16x32_bf16 v[176:179], a[168:171], v[160:163], v[176:179]\n"
    "s_nop 0\n"
    "v_or_b32_e32 v198, v198, v206\n"
    "v_lshlrev_b32_sdwa v206, 1, v181 dst_sel:DWORD dst_unused:UNUSED_PAD src0_sel:DWORD src1_sel:WORD_1\n"
    "v_mfma_f32_16x16x32_bf16 v[176:179], a[76:79], v[164:167], v[176:179]\n"
    "s_waitcnt lgkmcnt(6)\n"
    "ds_read_u16_d16_hi v203, v203\n"
    "ds_write2_b32 v248, v74, v75 offset0:40 offset1:60\n"
    "v_mfma_f32_16x16x32_bf16 v[96:99], a[176:179], v[192:195], v[96:99]\n"
    "v_add_u32_e32 v248, 1360, v248\n"
    "v_or_b32_e64 v199, v199, v207\n"
    "v_mfma_f32_16x16x32_bf16 v[100:103], a[180:183], v[192:195], v[100:103]\n"
    "ds_read_b128 a[244:247], v244 offset:14336\n"
    "ds_write2_b32 v248, v76, v77 offset0:0 offset1:20\n"
    "v_mfma_f32_16x16x32_bf16 v[112:115], a[184:187], v[192:195], v[112:115]\n"
    "v_lshlrev_b32_sdwa v207, 1, v183 dst_sel:DWORD dst_unused:UNUSED_PAD src0_sel:DWORD src1_sel:WORD_1\n"
    "v_or_b32_e32 v185, v185, v201\n"
    "v_mfma_f32_16x16x32_bf16 v[180:183], a[124:127], v[156:159], 0\n"
    "s_waitcnt lgkmcnt(6)\n"
    "ds_read_u16 v189, v189\n"
    "ds_write2_b32 v248, v78, v79 offset0:40 offset1:60\n"
    "v_mfma_f32_16x16x32_bf16 v[180:183], a[172:175], v[160:163], v[180:183]\n"
    "v_add_u32_e32 v248, 1360, v248\n"
    "v_or_b32_e64 v186, v186, v202\n"
    "v_mfma_f32_16x16x32_bf16 v[180:183], a[240:243], v[164:167], v[180:183]\n"
    "ds_read_u16_d16_hi v205, v205\n"
    "ds_read_u16 v190, v190\n"
    "v_mfma_f32_16x16x32_bf16 v[116:119], a[188:191], v[192:195], v[116:119]\n"
    "ds_read_b128 v[144:147], v243 offset:0\n"
    "ds_read_u16_d16_hi v206, v206\n"
    "v_mfma_f32_16x16x32_bf16 v[128:131], v[236:239], v[192:195], v[128:131]\n"
    "v_pk_fma_f16 v232, v184, v184, s48\n"
    "v_lshlrev_b32_sdwa v201, 1, v171 dst_sel:DWORD dst_unused:UNUSED_PAD src0_sel:DWORD src1_sel:WORD_1\n"
    "v_mfma_f32_16x16x32_bf16 v[104:107], a[176:179], v[196:199], v[104:107]\n"
    "s_waitcnt lgkmcnt(8)\n"
    "ds_read_b128 v[148:151], v243 offset:1024\n"
    "v_or_b32_e64 v187, v187, v203\n"
    "v_mfma_f32_16x16x32_bf16 v[108:111], a[180:183], v[196:199], v[108:111]\n"
    "s_waitcnt lgkmcnt(7)\n"
    "ds_read_u16 v191, v191\n"
    "v_pk_mul_f16 v232, v184, v232\n"
    "v_mfma_f32_16x16x32_bf16 v[132:135], a[244:247], v[192:195], v[132:135]\n"
    "v_exp_f16_e64 v234, v232\n"
    "v_mfma_f32_16x16x32_bf16 v[120:123], a[184:187], v[196:199], v[120:123]\n"
    "v_exp_f16_sdwa v234, v232 dst_sel:WORD_1 dst_unused:UNUSED_PRESERVE src0_sel:WORD_1\n"
    "v_mfma_f32_16x16x32_bf16 v[124:127], a[188:191], v[196:199], v[124:127]\n"
    "ds_write2_b32 v248, v80, v81 offset0:0 offset1:20\n"
    "v_mfma_f32_16x16x32_bf16 v[136:139], v[236:239], v[196:199], v[136:139]\n"
    "v_pk_fma_f16 v234, v234, s49, s49\n"
    "v_cvt_pk_f16_f32 v192, v168, v169\n"
    "v_mfma_f32_16x16x32_bf16 v[140:143], a[244:247], v[196:199], v[140:143]\n"
    "ds_read_b128 v[152:155], v243 offset:2048\n"
    "ds_read_u16_d16_hi v207, v207\n"
    "v_lshlrev_b32_sdwa v193, 1, v170 dst_sel:DWORD dst_unused:UNUSED_PAD src0_sel:DWORD src1_sel:WORD_1\n"
    "v_lshlrev_b32_sdwa v194, 1, v172 dst_sel:DWORD dst_unused:UNUSED_PAD src0_sel:DWORD src1_sel:WORD_1\n"
    "s_waitcnt lgkmcnt(8)\n"
    "v_or_b32_e32 v189, v189, v205\n"
    "v_lshlrev_b32_sdwa v202, 1, v173 dst_sel:DWORD dst_unused:UNUSED_PAD src0_sel:DWORD src1_sel:WORD_1\n"
    "v_lshlrev_b32_sdwa v195, 1, v174 dst_sel:DWORD dst_unused:UNUSED_PAD src0_sel:DWORD src1_sel:WORD_1\n"
    "v_lshlrev_b32_sdwa v203, 1, v175 dst_sel:DWORD dst_unused:UNUSED_PAD src0_sel:DWORD src1_sel:WORD_1\n"
    "v_rcp_f16_e32 v232, v234\n"
    "s_waitcnt lgkmcnt(6)\n"
    "v_mfma_f32_16x16x32_bf16 v[168:171], a[80:83], v[144:147], 0\n"
    "ds_read_b128 v[156:159], v243 offset:3072\n"
    "ds_write2_b32 v248, v82, v83 offset0:40 offset1:60\n"
    "v_mfma_f32_32x32x1_2b_f32 v[64:95], v254, v254, 0\n"
    "buffer_load_dwordx4 a[232:235], v245, s[40:43], s58 offen offset:64\n"
    "buffer_load_dwordx4 a[224:227], v245, s[40:43], s58 offen offset:0\n"
    "v_add_u32_e32 v248, -5440, v248\n"
    "s_waitcnt lgkmcnt(6)\n"
    "v_or_b32_e32 v190, v190, v206\n"
    "v_rcp_f16_sdwa v232, v234 dst_sel:WORD_1 dst_unused:UNUSED_PRESERVE src0_sel:WORD_1\n"
    "v_cvt_pk_f16_f32 v196, v176, v177\n"
    "v_lshlrev_b32_sdwa v197, 1, v178 dst_sel:DWORD dst_unused:UNUSED_PAD src0_sel:DWORD src1_sel:WORD_1\n"
    "v_lshlrev_b32_sdwa v205, 1, v179 dst_sel:DWORD dst_unused:UNUSED_PAD src0_sel:DWORD src1_sel:WORD_1\n"
    "v_lshlrev_b32_sdwa v198, 1, v180 dst_sel:DWORD dst_unused:UNUSED_PAD src0_sel:DWORD src1_sel:WORD_1\n"
    "v_lshlrev_b32_sdwa v206, 1, v181 dst_sel:DWORD dst_unused:UNUSED_PAD src0_sel:DWORD src1_sel:WORD_1\n"
    "v_lshlrev_b32_sdwa v199, 1, v182 dst_sel:DWORD dst_unused:UNUSED_PAD src0_sel:DWORD src1_sel:WORD_1\n"
    "v_fma_mix_f32 v234, v184, v232, 0 op_sel_hi:[1,1,0]\n"
    "v_mfma_f32_16x16x32_bf16 v[168:171], a[128:131], v[148:151], v[168:171]\n"
    "s_waitcnt lgkmcnt(3)\n"
    "ds_read_b128 v[160:163], v243 offset:4096\n"
    "ds_read_b128 v[164:167], v243 offset:5120\n"
    "v_mfma_f32_16x16x32_bf16 v[168:171], a[48:51], v[152:155], v[168:171]\n"
    "s_nop 0\n"
    "ds_read_b128 a[240:243], v244 offset:32768\n"
    "ds_read_u16 v193, v193\n"
    "v_mfma_f32_16x16x32_bf16 v[172:175], a[84:87], v[144:147], 0\n"
    "ds_read_u16_d16_hi v201, v201\n"
    "v_fma_mix_f32 v232, v184, v232, 0 op_sel:[1,1,0] op_sel_hi:[1,1,0]\n"
    "v_mfma_f32_16x16x32_bf16 v[172:175], a[132:135], v[148:151], v[172:175]\n"
    "s_waitcnt lgkmcnt(5)\n"
    "ds_read_u16 v194, v194\n"
    "v_or_b32_e32 v191, v191, v207\n"
    "v_mfma_f32_16x16x32_bf16 v[176:179], a[80:83], v[156:159], 0\n"
    "v_lshlrev_b32_sdwa v207, 1, v183 dst_sel:DWORD dst_unused:UNUSED_PAD src0_sel:DWORD src1_sel:WORD_1\n"
    "v_cvt_pk_bf16_f32 v184, v234, v232\n"
    "v_pk_fma_f16 v232, v188, v188, s48\n"
    "v_mfma_f32_16x16x32_bf16 v[180:183], a[84:87], v[156:159], 0\n"
    "ds_read_u16_d16_hi v202, v202\n"
    "v_pk_mul_f16 v232, v188, v232\n"
    "v_lshlrev_b32_e64 v254, 3, v244\n"
    "v_exp_f16_e64 v234, v232\n"
    "v_lshlrev_b32_sdwa v200, 1, v169 dst_sel:DWORD dst_unused:UNUSED_PAD src0_sel:DWORD src1_sel:WORD_1\n"
    "v_exp_f16_sdwa v234, v232 dst_sel:WORD_1 dst_unused:UNUSED_PRESERVE src0_sel:WORD_1\n"
    "v_mfma_f32_16x16x32_bf16 v[96:99], a[192:195], v[184:187], v[96:99]\n"
    "s_nop 0\n"
    "ds_read_b128 v[236:239], v244 offset:16384\n"
    "ds_read_u16 v195, v195\n"
    "s_waitcnt lgkmcnt(8)\n"
    "v_mfma_f32_16x16x32_bf16 v[176:179], a[128:131], v[160:163], v[176:179]\n"
    "s_waitcnt lgkmcnt(7)\n"
    "ds_read_b128 v[216:219], v251 offset:21504\n"
    "s_mov_b32 exec_hi, 0\n"
    "buffer_load_dword v233, v254, s[72:75], s53 offen sc1\n"
    "s_mov_b32 exec_hi, -1\n"
    "v_mfma_f32_16x16x32_bf16 v[176:179], a[48:51], v[164:167], v[176:179]\n"
    "s_waitcnt lgkmcnt(7)\n"
    "ds_read_b128 v[220:223], v251 offset:21840\n"
    "ds_read_u16_d16_hi v203, v203\n"
    "v_mfma_f32_16x16x32_bf16 v[172:175], a[240:243], v[152:155], v[172:175]\n"
    "s_barrier\n"
    "v_mfma_f32_16x16x32_bf16 v[180:183], a[132:135], v[160:163], v[180:183]\n"
    "s_waitcnt lgkmcnt(7)\n"
    "ds_read_b128 v[208:211], v250 offset:0\n"
    "ds_read_b128 v[212:215], v250 offset:320\n"
    "v_mfma_f32_16x16x32_bf16 v[180:183], a[240:243], v[164:167], v[180:183]\n"
    "s_add_u32 s53, s53, 393216\n"
    "ds_read_b128 a[244:247], v244 offset:18432\n"
    "ds_read_u16 v197, v197\n"
    "v_pk_fma_f16 v234, v234, s49, s49\n"
    "v_mfma_f32_16x16x32_bf16 v[100:103], a[196:199], v[184:187], v[100:103]\n"
    "ds_read_u16_d16_hi v205, v205\n"
    "v_rcp_f16_e32 v232, v234\n"
    "v_mfma_f32_16x16x32_bf16 v[112:115], a[200:203], v[184:187], v[112:115]\n"
    "s_waitcnt lgkmcnt(9)\n"
    "v_rcp_f16_sdwa v232, v234 dst_sel:WORD_1 dst_unused:UNUSED_PRESERVE src0_sel:WORD_1\n"
    "v_mfma_f32_16x16x32_bf16 v[116:119], a[204:207], v[184:187], v[116:119]\n"
    "ds_read_u16 v198, v198\n"
    "buffer_load_dwordx4 a[236:239], v245, s[40:43], s89 offen offset:64\n"
    "buffer_load_dwordx4 a[228:231], v245, s[40:43], s89 offen offset:0\n"
    "v_mfma_f32_16x16x32_bf16 v[128:131], v[236:239], v[184:187], v[128:131]\n"
    "ds_read_u16_d16_hi v206, v206\n"
    "v_or_b32_e64 v193, v193, v201\n"
    "v_fma_mix_f32 v234, v188, v232, 0 op_sel_hi:[1,1,0]\n"
    "v_fma_mix_f32 v232, v188, v232, 0 op_sel:[1,1,0] op_sel_hi:[1,1,0]\n"
    "v_or_b32_e64 v194, v194, v202\n"
    "v_cvt_pk_bf16_f32 v188, v234, v232\n"
    "v_pk_fma_f16 v232, v192, v192, s48\n"
    "v_lshlrev_b32_sdwa v201, 1, v171 dst_sel:DWORD dst_unused:UNUSED_PAD src0_sel:DWORD src1_sel:WORD_1\n"
    "v_mfma_f32_16x16x32_bf16 v[104:107], a[192:195], v[188:191], v[104:107]\n"
    "ds_read_u16 v199, v199\n"
    "v_pk_mul_f16 v232, v192, v232\n"
    "v_mfma_f32_16x16x32_bf16 v[108:111], a[196:199], v[188:191], v[108:111]\n"
    "ds_read_u16_d16_hi v207, v207\n"
    "v_exp_f16_e32 v234, v232\n"
    "v_mfma_f32_16x16x32_bf16 v[120:123], a[200:203], v[188:191], v[120:123]\n"
    "s_waitcnt lgkmcnt(7)\n"
    "v_or_b32_e32 v195, v195, v203\n"
    "v_exp_f16_sdwa v234, v232 dst_sel:WORD_1 dst_unused:UNUSED_PRESERVE src0_sel:WORD_1\n"
    "v_smfmac_f32_16x16x64_bf16 v[64:67], v[216:219], v[208:215], v240\n"
    "s_waitcnt lgkmcnt(6)\n"
    "ds_read_b128 v[216:219], v252 offset:21504\n"
    "ds_read_b128 a[240:243], v244 offset:36864\n"
    "v_smfmac_f32_16x16x64_bf16 v[68:71], v[220:223], v[208:215], v240 abid:1\n"
    "ds_read_b128 v[208:211], v250 offset:640\n"
    "v_mfma_f32_16x16x32_bf16 v[132:135], a[244:247], v[184:187], v[132:135]\n"
    "ds_read_b128 v[212:215], v250 offset:960\n"
    "v_pk_fma_f16 v234, v234, s49, s49\n"
    "v_mfma_f32_16x16x32_bf16 v[124:127], a[204:207], v[188:191], v[124:127]\n"
    "ds_read_b128 v[220:223], v252 offset:21840\n"
    "v_lshlrev_b32_sdwa v184, 1, v168 dst_sel:DWORD dst_unused:UNUSED_PAD src0_sel:DWORD src1_sel:WORD_1\n"
    "v_mfma_f32_16x16x32_bf16 v[136:139], v[236:239], v[188:191], v[136:139]\n"
    "ds_read_u16 v184, v184\n"
    "v_lshlrev_b32_sdwa v185, 1, v170 dst_sel:DWORD dst_unused:UNUSED_PAD src0_sel:DWORD src1_sel:WORD_1\n"
    "v_mfma_f32_16x16x32_bf16 v[140:143], a[244:247], v[188:191], v[140:143]\n"
    "s_waitcnt lgkmcnt(10)\n"
    "v_or_b32_e32 v197, v197, v205\n"
    "v_lshlrev_b32_sdwa v186, 1, v172 dst_sel:DWORD dst_unused:UNUSED_PAD src0_sel:DWORD src1_sel:WORD_1\n"
    "v_mfma_f32_16x16x32_bf16 v[168:171], a[88:91], v[144:147], 0\n"
    "ds_read_u16_d16_hi v200, v200\n"
    "v_lshlrev_b32_sdwa v202, 1, v173 dst_sel:DWORD dst_unused:UNUSED_PAD src0_sel:DWORD src1_sel:WORD_1\n"
    "v_mfma_f32_16x16x32_bf16 v[168:171], a[136:139], v[148:151], v[168:171]\n"
    "v_lshlrev_b32_sdwa v187, 1, v174 dst_sel:DWORD dst_unused:UNUSED_PAD src0_sel:DWORD src1_sel:WORD_1\n"
    "v_lshlrev_b32_sdwa v203, 1, v175 dst_sel:DWORD dst_unused:UNUSED_PAD src0_sel:DWORD src1_sel:WORD_1\n"
    "v_rcp_f16_e32 v232, v234\n"
    "v_mfma_f32_16x16x32_bf16 v[168:171], a[52:55], v[152:155], v[168:171]\n"
    "s_waitcnt lgkmcnt(9)\n"
    "ds_read_u16 v185, v185\n"
    "v_or_b32_e64 v198, v198, v206\n"
    "v_rcp_f16_sdwa v232, v234 dst_sel:WORD_1 dst_unused:UNUSED_PRESERVE src0_sel:WORD_1\n"
    "v_mfma_f32_16x16x32_bf16 v[172:175], a[92:95], v[144:147], 0\n"
    "ds_read_u16_d16_hi v201, v201\n"
    "v_lshlrev_b32_sdwa v188, 1, v176 dst_sel:DWORD dst_unused:UNUSED_PAD src0_sel:DWORD src1_sel:WORD_1\n"
    "v_mfma_f32_16x16x32_bf16 v[172:175], a[140:143], v[148:151], v[172:175]\n"
    "ds_read_u16 v186, v186\n"
    "v_lshlrev_b32_sdwa v204, 1, v177 dst_sel:DWORD dst_unused:UNUSED_PAD src0_sel:DWORD src1_sel:WORD_1\n"
    "v_lshlrev_b32_sdwa v189, 1, v178 dst_sel:DWORD dst_unused:UNUSED_PAD src0_sel:DWORD src1_sel:WORD_1\n"
    "v_lshlrev_b32_sdwa v205, 1, v179 dst_sel:DWORD dst_unused:UNUSED_PAD src0_sel:DWORD src1_sel:WORD_1\n"
    "v_lshlrev_b32_sdwa v190, 1, v180 dst_sel:DWORD dst_unused:UNUSED_PAD src0_sel:DWORD src1_sel:WORD_1\n"
    "v_lshlrev_b32_sdwa v206, 1, v181 dst_sel:DWORD dst_unused:UNUSED_PAD src0_sel:DWORD src1_sel:WORD_1\n"
    "v_lshlrev_b32_sdwa v191, 1, v182 dst_sel:DWORD dst_unused:UNUSED_PAD src0_sel:DWORD src1_sel:WORD_1\n"
    "v_fma_mix_f32 v234, v192, v232, 0 op_sel_hi:[1,1,0]\n"
    "s_waitcnt lgkmcnt(7)\n"
    "v_mfma_f32_16x16x32_bf16 v[172:175], a[240:243], v[152:155], v[172:175]\n"
    "s_nop 0\n"
    "ds_read_b128 v[236:239], v244 offset:20480\n"
    "ds_read_u16_d16_hi v202, v202\n"
    "s_waitcnt lgkmcnt(8)\n"
    "v_smfmac_f32_16x16x64_bf16 v[64:67], v[216:219], v[208:215], v240\n"
    "s_waitcnt lgkmcnt(7)\n"
    "v_or_b32_e64 v199, v199, v207\n"
    "v_smfmac_f32_16x16x64_bf16 v[68:71], v[220:223], v[208:215], v240 abid:1\n"
    "ds_read_b128 v[208:211], v250 offset:80\n"
    "ds_read_b128 v[212:215], v250 offset:400\n"
    "v_lshlrev_b32_sdwa v207, 1, v183 dst_sel:DWORD dst_unused:UNUSED_PAD src0_sel:DWORD src1_sel:WORD_1\n"
    "v_fma_mix_f32 v232, v192, v232, 0 op_sel:[1,1,0] op_sel_hi:[1,1,0]\n"
    "v_mfma_f32_16x16x32_bf16 v[176:179], a[88:91], v[156:159], 0\n"
    "ds_read_b128 v[216:219], v251 offset:22176\n"
    "v_cvt_pk_bf16_f32 v192, v234, v232\n"
    "v_pk_fma_f16 v232, v196, v196, s48\n"
    "v_lshlrev_b32_e64 v254, 3, v244\n"
    "v_pk_mul_f16 v232, v196, v232\n"
    "v_pk_add_f32 v[224:225], v[224:225], v[64:65]\n"
    "v_pk_add_f32 v[226:227], v[226:227], v[66:67]\n"
    "v_pk_fma_f32 v[228:229], v[64:65], v[64:65], v[228:229]\n"
    "v_pk_fma_f32 v[230:231], v[66:67], v[66:67], v[230:231]\n"
    "v_pk_add_f32 v[224:225], v[224:225], v[68:69]\n"
    "v_pk_add_f32 v[226:227], v[226:227], v[70:71]\n"
    "v_pk_fma_f32 v[228:229], v[68:69], v[68:69], v[228:229]\n"
    "v_pk_fma_f32 v[230:231], v[70:71], v[70:71], v[230:231]\n"
    "v_mfma_f32_16x16x32_bf16 v[176:179], a[136:139], v[160:163], v[176:179]\n"
    "s_waitcnt lgkmcnt(7)\n"
    "ds_read_b128 v[220:223], v251 offset:22512\n"
    "ds_read_u16 v187, v187\n"
    "v_mfma_f32_16x16x32_bf16 v[176:179], a[52:55], v[164:167], v[176:179]\n"
    "s_nop 0\n"
    "ds_read_b128 a[244:247], v244 offset:22528\n"
    "ds_read_u16_d16_hi v203, v203\n"
    "v_mfma_f32_16x16x32_bf16 v[180:183], a[92:95], v[156:159], 0\n"
    "s_waitcnt lgkmcnt(10)\n"
    "v_exp_f16_e32 v234, v232\n"
    "v_mfma_f32_16x16x32_bf16 v[180:183], a[140:143], v[160:163], v[180:183]\n"
    "ds_read_u16 v188, v188\n"
    "v_exp_f16_sdwa v234, v232 dst_sel:WORD_1 dst_unused:UNUSED_PRESERVE src0_sel:WORD_1\n"
    "v_mfma_f32_16x16x32_bf16 v[180:183], a[240:243], v[164:167], v[180:183]\n"
    "ds_read_u16_d16_hi v204, v204\n"
    "v_pk_fma_f16 v234, v234, s49, s49\n"
    "v_mfma_f32_16x16x32_bf16 v[96:99], a[208:211], v[192:195], v[96:99]\n"
    "v_rcp_f16_e32 v232, v234\n"
    "v_mfma_f32_16x16x32_bf16 v[100:103], a[212:215], v[192:195], v[100:103]\n"
    "s_waitcnt lgkmcnt(8)\n"
    "ds_read_u16 v189, v189\n"
    "v_rcp_f16_sdwa v232, v234 dst_sel:WORD_1 dst_unused:UNUSED_PRESERVE src0_sel:WORD_1\n"
    "v_mfma_f32_16x16x32_bf16 v[112:115], a[216:219], v[192:195], v[112:115]\n"
    "s_mov_b32 exec_hi, 0\n"
    "buffer_load_dword v233, v254, s[72:75], s53 offen sc1\n"
    "s_mov_b32 exec_hi, -1\n"
    "v_mfma_f32_16x16x32_bf16 v[116:119], a[220:223], v[192:195], v[116:119]\n"
    "s_waitcnt lgkmcnt(7)\n"
    "ds_read_u16_d16_hi v205, v205\n"
    "v_fma_mix_f32 v234, v196, v232, 0 op_sel_hi:[1,1,0]\n"
    "v_smfmac_f32_16x16x64_bf16 v[72:75], v[216:219], v[208:215], v240\n"
    "s_waitcnt lgkmcnt(7)\n"
    "ds_read_b128 v[216:219], v252 offset:22176\n"
    "v_smfmac_f32_16x16x64_bf16 v[76:79], v[220:223], v[208:215], v240 abid:1\n"
    "s_add_u32 s53, s53, 393216\n"
    "ds_read_b128 v[208:211], v250 offset:720\n"
    "v_mfma_f32_16x16x32_bf16 v[128:131], v[236:239], v[192:195], v[128:131]\n"
    "ds_read_b128 v[212:215], v250 offset:1040\n"
    "v_fma_mix_f32 v232, v196, v232, 0 op_sel:[1,1,0] op_sel_hi:[1,1,0]\n"
    "s_waitcnt lgkmcnt(8)\n"
    "v_mfma_f32_16x16x32_bf16 v[132:135], a[244:247], v[192:195], v[132:135]\n"
    "s_nop 0\n"
    "ds_read_b128 v[220:223], v252 offset:22512\n"
    "v_cvt_pk_bf16_f32 v196, v234, v232\n"
    "v_or_b32_e64 v184, v184, v200\n"
    "v_or_b32_e32 v185, v185, v201\n"
    "v_mfma_f32_16x16x32_bf16 v[104:107], a[208:211], v[196:199], v[104:107]\n"
    "ds_read_u16 v190, v190\n"
    "v_cvt_pk_f16_f32 v192, v168, v169\n"
    "v_mfma_f32_16x16x32_bf16 v[108:111], a[212:215], v[196:199], v[108:111]\n"
    "s_waitcnt lgkmcnt(9)\n"
    "ds_read_u16_d16_hi v206, v206\n"
    "v_lshlrev_b32_sdwa v193, 1, v170 dst_sel:DWORD dst_unused:UNUSED_PAD src0_sel:DWORD src1_sel:WORD_1\n"
    "v_mfma_f32_16x16x32_bf16 v[120:123], a[216:219], v[196:199], v[120:123]\n"
    "v_lshlrev_b32_sdwa v201, 1, v171 dst_sel:DWORD dst_unused:UNUSED_PAD src0_sel:DWORD src1_sel:WORD_1\n"
    "v_lshlrev_b32_sdwa v194, 1, v172 dst_sel:DWORD dst_unused:UNUSED_PAD src0_sel:DWORD src1_sel:WORD_1\n"
    "v_mfma_f32_16x16x32_bf16 v[124:127], a[220:223], v[196:199], v[124:127]\n"
    "ds_read_u16 v191, v191\n"
    "v_or_b32_e32 v186, v186, v202\n"
    "v_mfma_f32_16x16x32_bf16 v[136:139], v[236:239], v[196:199], v[136:139]\n"
    "s_waitcnt lgkmcnt(9)\n"
    "v_lshlrev_b32_sdwa v202, 1, v173 dst_sel:DWORD dst_unused:UNUSED_PAD src0_sel:DWORD src1_sel:WORD_1\n"
    "v_lshlrev_b32_sdwa v195, 1, v174 dst_sel:DWORD dst_unused:UNUSED_PAD src0_sel:DWORD src1_sel:WORD_1\n"
    "v_mfma_f32_16x16x32_bf16 v[140:143], a[244:247], v[196:199], v[140:143]\n"
    "ds_read_u16_d16_hi v207, v207\n"
    "v_cvt_pk_f16_f32 v196, v176, v177\n"
    "v_mfma_f32_16x16x32_bf16 v[168:171], a[96:99], v[144:147], 0\n"
    "v_lshlrev_b32_sdwa v197, 1, v178 dst_sel:DWORD dst_unused:UNUSED_PAD src0_sel:DWORD src1_sel:WORD_1\n"
    "v_lshlrev_b32_sdwa v198, 1, v180 dst_sel:DWORD dst_unused:UNUSED_PAD src0_sel:DWORD src1_sel:WORD_1\n"
    "v_mfma_f32_16x16x32_bf16 v[168:171], a[144:147], v[148:151], v[168:171]\n"
    "ds_read_u16 v193, v193\n"
    "v_lshlrev_b32_sdwa v199, 1, v182 dst_sel:DWORD dst_unused:UNUSED_PAD src0_sel:DWORD src1_sel:WORD_1\n"
    "v_mfma_f32_16x16x32_bf16 v[168:171], a[56:59], v[152:155], v[168:171]\n"
    "s_waitcnt lgkmcnt(8)\n"
    "ds_read_u16_d16_hi v201, v201\n"
    "v_or_b32_e32 v187, v187, v203\n"
    "v_lshlrev_b32_sdwa v203, 1, v175 dst_sel:DWORD dst_unused:UNUSED_PAD src0_sel:DWORD src1_sel:WORD_1\n"
    "v_mfma_f32_16x16x32_bf16 v[172:175], a[100:103], v[144:147], 0\n"
    "s_waitcnt lgkmcnt(7)\n"
    "v_or_b32_e32 v188, v188, v204\n"
    "v_or_b32_e32 v189, v189, v205\n"
    "v_smfmac_f32_16x16x64_bf16 v[72:75], v[216:219], v[208:215], v240\n"
    "s_waitcnt lgkmcnt(6)\n"
    "ds_read_b128 v[216:219], v251 offset:22848\n"
    "ds_read_u16 v194, v194\n"
    "v_smfmac_f32_16x16x64_bf16 v[76:79], v[220:223], v[208:215], v240 abid:1\n"
    "v_lshlrev_b32_sdwa v205, 1, v179 dst_sel:DWORD dst_unused:UNUSED_PAD src0_sel:DWORD src1_sel:WORD_1\n"
    "v_mfma_f32_16x16x32_bf16 v[172:175], a[148:151], v[148:151], v[172:175]\n"
    "ds_read_b128 v[208:211], v250 offset:160\n"
    "v_cvt_pk_bf16_f32 v96, v96, v97\n"
    "v_cvt_pk_bf16_f32 v97, v98, v99\n"
    "v_cvt_pk_bf16_f32 v98, v100, v101\n"
    "ds_read_b128 v[212:215], v250 offset:480\n"
    "v_pk_add_f32 v[224:225], v[224:225], v[72:73]\n"
    "v_pk_add_f32 v[226:227], v[226:227], v[74:75]\n"
    "v_pk_fma_f32 v[228:229], v[72:73], v[72:73], v[228:229]\n"
    "v_pk_fma_f32 v[230:231], v[74:75], v[74:75], v[230:231]\n"
    "v_pk_add_f32 v[224:225], v[224:225], v[76:77]\n"
    "v_pk_add_f32 v[226:227], v[226:227], v[78:79]\n"
    "v_pk_fma_f32 v[228:229], v[76:77], v[76:77], v[228:229]\n"
    "v_pk_fma_f32 v[230:231], v[78:79], v[78:79], v[230:231]\n"
    "v_mfma_f32_16x16x32_bf16 v[172:175], a[60:63], v[152:155], v[172:175]\n"
    "s_waitcnt lgkmcnt(7)\n"
    "ds_read_b128 v[220:223], v251 offset:23184\n"
    "ds_read_u16_d16_hi v202, v202\n"
    "v_mfma_f32_16x16x32_bf16 v[176:179], a[96:99], v[156:159], 0\n"
    "s_nop 0\n"
    "ds_read_b128 v[236:239], v244 offset:0\n"
    "v_cvt_pk_bf16_f32 v99, v102, v103\n"
    "v_mfma_f32_16x16x32_bf16 v[176:179], a[144:147], v[160:163], v[176:179]\n"
    "s_waitcnt lgkmcnt(9)\n"
    "v_or_b32_e32 v190, v190, v206\n"
    "v_lshlrev_b32_sdwa v206, 1, v181 dst_sel:DWORD dst_unused:UNUSED_PAD src0_sel:DWORD src1_sel:WORD_1\n"
    "v_mfma_f32_16x16x32_bf16 v[176:179], a[56:59], v[164:167], v[176:179]\n"
    "ds_write_b128 v242, v[96:99] offset:24640\n"
    "v_mfma_f32_16x16x32_bf16 v[96:99], a[0:3], v[184:187], 0\n"
    "s_nop 0\n"
    "v_cvt_pk_bf16_f32 v112, v112, v113\n"
    "v_cvt_pk_bf16_f32 v113, v114, v115\n"
    "v_mfma_f32_16x16x32_bf16 v[100:103], a[4:7], v[184:187], 0\n"
    "s_waitcnt lgkmcnt(7)\n"
    "v_cvt_pk_bf16_f32 v114, v116, v117\n"
    "v_cvt_pk_bf16_f32 v115, v118, v119\n"
    "ds_write_b128 v242, v[112:115] offset:26688\n"
    "v_mfma_f32_16x16x32_bf16 v[112:115], a[8:11], v[184:187], 0\n"
    "v_cvt_pk_bf16_f32 v128, v128, v129\n"
    "v_cvt_pk_bf16_f32 v129, v130, v131\n"
    "s_waitcnt lgkmcnt(5)\n"
    "v_smfmac_f32_16x16x64_bf16 v[80:83], v[216:219], v[208:215], v240\n"
    "s_waitcnt lgkmcnt(4)\n"
    "ds_read_b128 v[216:219], v252 offset:22848\n"
    "ds_read_u16 v195, v195\n"
    "v_smfmac_f32_16x16x64_bf16 v[84:87], v[220:223], v[208:215], v240 abid:1\n"
    "ds_read_b128 v[208:211], v250 offset:800\n"
    "ds_read_b128 v[212:215], v250 offset:1120\n"
    "v_or_b32_e64 v191, v191, v207\n"
    "v_mfma_f32_16x16x32_bf16 v[116:119], a[12:15], v[184:187], 0\n"
    "ds_read_b128 v[220:223], v252 offset:23184\n"
    "ds_read_u16_d16_hi v203, v203\n"
    "v_lshlrev_b32_sdwa v207, 1, v183 dst_sel:DWORD dst_unused:UNUSED_PAD src0_sel:DWORD src1_sel:WORD_1\n"
    "v_mfma_f32_16x16x32_bf16 v[180:183], a[100:103], v[156:159], 0\n"
    "s_waitcnt lgkmcnt(7)\n"
    "ds_read_b128 a[244:247], v244 offset:2048\n"
    "v_cvt_pk_bf16_f32 v130, v132, v133\n"
    "v_mfma_f32_16x16x32_bf16 v[180:183], a[148:151], v[160:163], v[180:183]\n"
    "s_nop 0\n"
    "ds_read_u16 v197, v197\n"
    "v_or_b32_e64 v193, v193, v201\n"
    "v_mfma_f32_16x16x32_bf16 v[180:183], a[60:63], v[164:167], v[180:183]\n"
    "v_cvt_pk_bf16_f32 v131, v134, v135\n"
    "v_cvt_pk_bf16_f32 v104, v104, v105\n"
    "v_cvt_pk_bf16_f32 v105, v106, v107\n"
    "v_cvt_pk_bf16_f32 v106, v108, v109\n"
    "v_cvt_pk_bf16_f32 v107, v110, v111\n"
    "v_mfma_f32_16x16x32_bf16 v[108:111], a[4:7], v[188:191], 0\n"
    "v_or_b32_e64 v194, v194, v202\n"
    "ds_write_b128 v242, v[128:131] offset:28736\n"
    "v_mfma_f32_16x16x32_bf16 v[128:131], v[236:239], v[184:187], 0\n"
    "v_cvt_pk_bf16_f32 v120, v120, v121\n"
    "v_cvt_pk_bf16_f32 v121, v122, v123\n"
    "v_cvt_pk_bf16_f32 v122, v124, v125\n"
    "v_cvt_pk_bf16_f32 v123, v126, v127\n"
    "ds_write_b128 v242, v[104:107] offset:25664\n"
    "v_mfma_f32_16x16x32_bf16 v[104:107], a[0:3], v[188:191], 0\n"
    "s_waitcnt lgkmcnt(5)\n"
    "v_add_u32_e32 v254, s54, v242\n"
    "v_cvt_pk_bf16_f32 v136, v136, v137\n"
    "v_smfmac_f32_16x16x64_bf16 v[80:83], v[216:219], v[208:215], v240\n"
    "ds_read_b128 v[216:219], v251 offset:23520\n"
    "ds_write_b128 v242, v[120:123] offset:27712\n"
    "v_smfmac_f32_16x16x64_bf16 v[84:87], v[220:223], v[208:215], v240 abid:1\n"
    "s_waitcnt lgkmcnt(5)\n"
    "v_pk_fma_f16 v232, v192, v192, s48\n"
    "v_mfma_f32_16x16x32_bf16 v[132:135], a[244:247], v[184:187], 0\n"
    "s_nop 0\n"
    "v_cvt_pk_bf16_f32 v137, v138, v139\n"
    "v_cvt_pk_bf16_f32 v138, v140, v141\n"
    "v_or_b32_e64 v195, v195, v203\n"
    "ds_read_b128 v[208:211], v250 offset:240\n"
    "ds_read_b128 v[212:215], v250 offset:560\n"
    "v_pk_add_f32 v[224:225], v[224:225], v[80:81]\n"
    "v_pk_add_f32 v[226:227], v[226:227], v[82:83]\n"
    "v_pk_fma_f32 v[228:229], v[80:81], v[80:81], v[228:229]\n"
    "v_pk_fma_f32 v[230:231], v[82:83], v[82:83], v[230:231]\n"
    "v_pk_add_f32 v[224:225], v[224:225], v[84:85]\n"
    "v_pk_add_f32 v[226:227], v[226:227], v[86:87]\n"
    "v_pk_fma_f32 v[228:229], v[84:85], v[84:85], v[228:229]\n"
    "v_pk_fma_f32 v[230:231], v[86:87], v[86:87], v[230:231]\n"
    "v_mfma_f32_16x16x32_bf16 v[120:123], a[8:11], v[188:191], 0\n"
    "ds_read_b128 v[220:223], v251 offset:23856\n"
    "ds_read_u16_d16_hi v205, v205\n"
    "v_mfma_f32_16x16x32_bf16 v[124:127], a[12:15], v[188:191], 0\n"
    "ds_read_u16 v198, v198\n"
    "ds_read_u16_d16_hi v206, v206\n"
    "v_cvt_pk_bf16_f32 v139, v142, v143\n"
    "v_pk_mul_f16 v232, v192, v232\n"
    "v_cvt_pk_f16_f32 v184, v168, v169\n"
    "v_exp_f16_e32 v234, v232\n"
    "v_mfma_f32_16x16x32_bf16 v[140:143], a[244:247], v[188:191], 0\n"
    "v_exp_f16_sdwa v234, v232 dst_sel:WORD_1 dst_unused:UNUSED_PRESERVE src0_sel:WORD_1\n"
    "ds_write_b128 v242, v[136:139] offset:29760\n"
    "v_mfma_f32_16x16x32_bf16 v[136:139], v[236:239], v[188:191], 0\n"
    "s_waitcnt lgkmcnt(5)\n"
    "v_pk_fma_f16 v234, v234, s49, s49\n"
    "v_lshlrev_b32_sdwa v185, 1, v170 dst_sel:DWORD dst_unused:UNUSED_PAD src0_sel:DWORD src1_sel:WORD_1\n"
    "v_smfmac_f32_16x16x64_bf16 v[88:91], v[216:219], v[208:215], v240\n"
    "ds_read_b128 v[216:219], v252 offset:23520\n"
    "ds_read_u16 v199, v199\n"
    "v_lshlrev_b32_sdwa v201, 1, v171 dst_sel:DWORD dst_unused:UNUSED_PAD src0_sel:DWORD src1_sel:WORD_1\n"
    "v_mfma_f32_16x16x32_bf16 v[168:171], a[104:107], v[144:147], 0\n"
    "s_waitcnt lgkmcnt(6)\n"
    "ds_read_u16_d16_hi v207, v207\n"
    "ds_read_u16 v185, v185\n"
    "v_smfmac_f32_16x16x64_bf16 v[92:95], v[220:223], v[208:215], v240 abid:1\n"
    "s_nop 0\n"
    "v_lshlrev_b32_sdwa v186, 1, v172 dst_sel:DWORD dst_unused:UNUSED_PAD src0_sel:DWORD src1_sel:WORD_1\n"
    "v_mfma_f32_16x16x32_bf16 v[168:171], a[152:155], v[148:151], v[168:171]\n"
    "ds_read_b128 v[208:211], v250 offset:880\n"
    "v_lshlrev_b32_sdwa v202, 1, v173 dst_sel:DWORD dst_unused:UNUSED_PAD src0_sel:DWORD src1_sel:WORD_1\n"
    "s_waitcnt lgkmcnt(8)\n"
    "v_or_b32_e32 v197, v197, v205\n"
    "ds_read_b128 v[212:215], v250 offset:1200\n"
    "v_lshlrev_b32_sdwa v187, 1, v174 dst_sel:DWORD dst_unused:UNUSED_PAD src0_sel:DWORD src1_sel:WORD_1\n"
    "ds_read_b128 v[220:223], v252 offset:23856\n"
    "v_lshlrev_b32_sdwa v203, 1, v175 dst_sel:DWORD dst_unused:UNUSED_PAD src0_sel:DWORD src1_sel:WORD_1\n"
    "v_rcp_f16_e32 v232, v234\n"
    "v_mfma_f32_16x16x32_bf16 v[168:171], a[64:67], v[152:155], v[168:171]\n"
    "s_waitcnt lgkmcnt(7)\n"
    "ds_read_u16_d16_hi v201, v201\n"
    "v_or_b32_e64 v198, v198, v206\n"
    "v_rcp_f16_sdwa v232, v234 dst_sel:WORD_1 dst_unused:UNUSED_PRESERVE src0_sel:WORD_1\n"
    "v_mfma_f32_16x16x32_bf16 v[172:175], a[108:111], v[144:147], 0\n"
    "ds_read_u16 v186, v186\n"
    "v_cvt_pk_f16_f32 v188, v176, v177\n"
    "v_mfma_f32_16x16x32_bf16 v[172:175], a[156:159], v[148:151], v[172:175]\n"
    "ds_read_u16_d16_hi v202, v202\n"
    "v_lshlrev_b32_sdwa v189, 1, v178 dst_sel:DWORD dst_unused:UNUSED_PAD src0_sel:DWORD src1_sel:WORD_1\n"
    "v_lshlrev_b32_sdwa v205, 1, v179 dst_sel:DWORD dst_unused:UNUSED_PAD src0_sel:DWORD src1_sel:WORD_1\n"
    "v_lshlrev_b32_sdwa v190, 1, v180 dst_sel:DWORD dst_unused:UNUSED_PAD src0_sel:DWORD src1_sel:WORD_1\n"
    "v_lshlrev_b32_sdwa v206, 1, v181 dst_sel:DWORD dst_unused:UNUSED_PAD src0_sel:DWORD src1_sel:WORD_1\n"
    "v_lshlrev_b32_sdwa v191, 1, v182 dst_sel:DWORD dst_unused:UNUSED_PAD src0_sel:DWORD src1_sel:WORD_1\n"
    "v_fma_mix_f32 v234, v192, v232, 0 op_sel_hi:[1,1,0]\n"
    "v_mfma_f32_16x16x32_bf16 v[172:175], a[68:71], v[152:155], v[172:175]\n"
    "ds_read_b128 v[236:239], v244 offset:4096\n"
    "v_fma_mix_f32 v232, v192, v232, 0 op_sel:[1,1,0] op_sel_hi:[1,1,0]\n"
    "v_mfma_f32_16x16x32_bf16 v[176:179], a[104:107], v[156:159], 0\n"
    "ds_read_u16 v187, v187\n"
    "v_cvt_pk_bf16_f32 v192, v234, v232\n"
    "v_mfma_f32_16x16x32_bf16 v[176:179], a[152:155], v[160:163], v[176:179]\n"
    "s_waitcnt lgkmcnt(9)\n"
    "v_or_b32_e32 v199, v199, v207\n"
    "v_lshlrev_b32_sdwa v207, 1, v183 dst_sel:DWORD dst_unused:UNUSED_PAD src0_sel:DWORD src1_sel:WORD_1\n"
    "v_mfma_f32_16x16x32_bf16 v[176:179], a[64:67], v[164:167], v[176:179]\n"
    "ds_read_u16_d16_hi v203, v203\n"
    "v_pk_fma_f16 v232, v196, v196, s48\n"
    "v_mfma_f32_16x16x32_bf16 v[180:183], a[108:111], v[156:159], 0\n"
    "ds_read_b128 a[244:247], v244 offset:6144\n"
    "v_pk_mul_f16 v232, v196, v232\n"
    "v_mfma_f32_16x16x32_bf16 v[180:183], a[156:159], v[160:163], v[180:183]\n"
    "s_waitcnt lgkmcnt(8)\n"
    "ds_read_u16 v189, v189\n"
    "v_exp_f16_e32 v234, v232\n"
    "v_smfmac_f32_16x16x64_bf16 v[88:91], v[216:219], v[208:215], v240\n"
    "s_waitcnt lgkmcnt(8)\n"
    "v_exp_f16_sdwa v234, v232 dst_sel:WORD_1 dst_unused:UNUSED_PRESERVE src0_sel:WORD_1\n"
    "v_smfmac_f32_16x16x64_bf16 v[92:95], v[220:223], v[208:215], v240 abid:1\n"
    "ds_read_u16_d16_hi v205, v205\n"
    "v_mfma_f32_16x16x32_bf16 v[180:183], a[68:71], v[164:167], v[180:183]\n"
    "s_waitcnt lgkmcnt(8)\n"
    "ds_read_u16 v190, v190\n"
    "v_pk_fma_f16 v234, v234, s49, s49\n"
    "v_or_b32_e64 v185, v185, v201\n"
    "v_rcp_f16_e64 v232, v234\n"
    "v_pk_add_f32 v[224:225], v[224:225], v[88:89]\n"
    "v_pk_add_f32 v[226:227], v[226:227], v[90:91]\n"
    "v_pk_fma_f32 v[228:229], v[88:89], v[88:89], v[228:229]\n"
    "v_pk_fma_f32 v[230:231], v[90:91], v[90:91], v[230:231]\n"
    "v_pk_add_f32 v[224:225], v[224:225], v[92:93]\n"
    "v_pk_add_f32 v[226:227], v[226:227], v[94:95]\n"
    "v_pk_fma_f32 v[228:229], v[92:93], v[92:93], v[228:229]\n"
    "v_pk_fma_f32 v[230:231], v[94:95], v[94:95], v[230:231]\n"
    "v_mfma_f32_16x16x32_bf16 v[96:99], a[16:19], v[192:195], v[96:99]\n"
    "s_waitcnt lgkmcnt(6)\n"
    "ds_write_b128 v254, v[224:227]\n"
    "v_mfma_f32_16x16x32_bf16 v[100:103], a[20:23], v[192:195], v[100:103]\n"
    "s_nop 0\n"
    "v_rcp_f16_sdwa v232, v234 dst_sel:WORD_1 dst_unused:UNUSED_PRESERVE src0_sel:WORD_1\n"
    "v_mfma_f32_16x16x32_bf16 v[112:115], a[24:27], v[192:195], v[112:115]\n"
    "v_fma_mix_f32 v234, v196, v232, 0 op_sel_hi:[1,1,0]\n"
    "v_fma_mix_f32 v232, v196, v232, 0 op_sel:[1,1,0] op_sel_hi:[1,1,0]\n"
    "v_mfma_f32_16x16x32_bf16 v[116:119], a[28:31], v[192:195], v[116:119]\n"
    "s_waitcnt lgkmcnt(4)\n"
    "ds_read_u16_d16_hi v206, v206\n"
    "ds_write_b128 v254, v[228:231] offset:1024\n"
    "v_mfma_f32_16x16x32_bf16 v[128:131], v[236:239], v[192:195], v[128:131]\n"
    "v_lshrrev_b32_e32 v254, 2, v242\n"
    "v_or_b32_e64 v186, v186, v202\n"
    "v_mfma_f32_16x16x32_bf16 v[132:135], a[244:247], v[192:195], v[132:135]\n"
    "v_add_u32_e64 v254, s55, v254\n"
    "v_cvt_pk_bf16_f32 v196, v234, v232\n"
    "v_pk_fma_f16 v232, v184, v184, s48\n"
    "v_or_b32_e64 v187, v187, v203\n"
    "v_mfma_f32_16x16x32_bf16 v[104:107], a[16:19], v[196:199], v[104:107]\n"
    "ds_read_u16 v191, v191\n"
    "ds_read_u16_d16_hi v207, v207\n"
    "v_mfma_f32_16x16x32_bf16 v[108:111], a[20:23], v[196:199], v[108:111]\n"
    "s_waitcnt lgkmcnt(6)\n"
    "ds_read_b128 a[240:243], v244 offset:34816\n"
    "v_pk_mul_f16 v232, v184, v232\n"
    "v_mfma_f32_16x16x32_bf16 v[120:123], a[24:27], v[196:199], v[120:123]\n"
    "v_exp_f16_e32 v234, v232\n"
    "v_mfma_f32_16x16x32_bf16 v[124:127], a[28:31], v[196:199], v[124:127]\n"
    "s_waitcnt vmcnt(2)\n"
    "v_exp_f16_sdwa v234, v232 dst_sel:WORD_1 dst_unused:UNUSED_PRESERVE src0_sel:WORD_1\n"
    "v_mfma_f32_16x16x32_bf16 v[136:139], v[236:239], v[196:199], v[136:139]\n"
    "s_waitcnt vmcnt(1)\n"
    "ds_read_b128 v[236:239], v244 offset:8192\n"
    "v_or_b32_e64 v189, v189, v205\n"
    "v_mfma_f32_16x16x32_bf16 v[140:143], a[244:247], v[196:199], v[140:143]\n"
    "v_pk_fma_f16 v234, v234, s49, s49\n"
    "v_lshlrev_b32_sdwa v192, 1, v168 dst_sel:DWORD dst_unused:UNUSED_PAD src0_sel:DWORD src1_sel:WORD_1\n"
    "v_smfmac_f32_32x32x32_bf16 v[112:127], a[248:251], a[232:239], v241\n"
    "s_waitcnt lgkmcnt(4)\n"
    "s_barrier\n"
    "v_smfmac_f32_32x32x32_bf16 v[96:111], a[248:251], a[224:231], v241\n"
    "ds_read_b32 v224, v254 offset:0\n"
    "ds_read_b32 v225, v254 offset:6752\n"
    "v_lshlrev_b32_sdwa v200, 1, v169 dst_sel:DWORD dst_unused:UNUSED_PAD src0_sel:DWORD src1_sel:WORD_1\n"
    "ds_read_b32 v226, v254 offset:13504\n"
    "ds_read_b32 v227, v254 offset:20256\n"
    "ds_read_b32 v228, v254 offset:1024\n"
    "v_lshlrev_b32_sdwa v193, 1, v170 dst_sel:DWORD dst_unused:UNUSED_PAD src0_sel:DWORD src1_sel:WORD_1\n"
    "ds_read_b32 v229, v254 offset:7776\n"
    "ds_read_b32 v230, v254 offset:14528\n"
    "ds_read_b32 v231, v254 offset:21280\n"
    "v_lshlrev_b32_sdwa v201, 1, v171 dst_sel:DWORD dst_unused:UNUSED_PAD src0_sel:DWORD src1_sel:WORD_1\n"
    "v_mfma_f32_16x16x32_bf16 v[168:171], a[112:115], v[144:147], 0\n"
    "s_waitcnt lgkmcnt(8)\n"
    "ds_read_u16 v192, v192\n"
    "ds_read_u16_d16_hi v200, v200\n"
    "v_mfma_f32_16x16x32_bf16 v[168:171], a[160:163], v[148:151], v[168:171]\n"
    "s_nop 0\n"
    "ds_read_u16 v193, v193\n"
    "ds_read_u16_d16_hi v201, v201\n"
    "v_lshlrev_b32_sdwa v194, 1, v172 dst_sel:DWORD dst_unused:UNUSED_PAD src0_sel:DWORD src1_sel:WORD_1\n"
    "v_lshlrev_b32_sdwa v202, 1, v173 dst_sel:DWORD dst_unused:UNUSED_PAD src0_sel:DWORD src1_sel:WORD_1\n"
    "v_lshlrev_b32_sdwa v195, 1, v174 dst_sel:DWORD dst_unused:UNUSED_PAD src0_sel:DWORD src1_sel:WORD_1\n"
    "v_lshlrev_b32_sdwa v203, 1, v175 dst_sel:DWORD dst_unused:UNUSED_PAD src0_sel:DWORD src1_sel:WORD_1\n"
    "v_rcp_f16_e64 v232, v234\n"
    "v_mfma_f32_16x16x32_bf16 v[168:171], a[72:75], v[152:155], v[168:171]\n"
    "ds_read_u16 v194, v194\n"
    "buffer_load_dwordx4 a[224:227], v245, s[40:43], s58 offen offset:128\n"
    "s_waitcnt lgkmcnt(11)\n"
    "v_add_f32_e32 v224, v224, v225\n"
    "v_or_b32_e32 v190, v190, v206\n"
    "s_waitcnt lgkmcnt(9)\n"
    "v_add_f32_e32 v226, v226, v227\n"
    "v_rcp_f16_sdwa v232, v234 dst_sel:WORD_1 dst_unused:UNUSED_PRESERVE src0_sel:WORD_1\n"
    "v_mfma_f32_16x16x32_bf16 v[172:175], a[116:119], v[144:147], 0\n"
    "s_waitcnt lgkmcnt(6)\n"
    "ds_read_b128 a[232:235], v243 offset:24640\n"
    "ds_read_u16_d16_hi v202, v202\n"
    "v_mfma_f32_16x16x32_bf16 v[172:175], a[164:167], v[148:151], v[172:175]\n"
    "s_waitcnt lgkmcnt(7)\n"
    "v_add_f32_e32 v228, v228, v229\n"
    "v_add_f32_e64 v230, v230, v231\n"
    "v_add_f32_e64 v224, v224, v226\n"
    "v_add_f32_e64 v228, v228, v230\n"
    "v_mul_f32_e32 v224, 0x3c2aaaab, v224\n"
    "v_mul_f32_e32 v228, 0x3c2aaaab, v228\n"
    "v_lshlrev_b32_sdwa v196, 1, v176 dst_sel:DWORD dst_unused:UNUSED_PAD src0_sel:DWORD src1_sel:WORD_1\n"
    "v_fma_f32 v228, -v224, v224, v228\n"
    "v_lshlrev_b32_sdwa v204, 1, v177 dst_sel:DWORD dst_unused:UNUSED_PAD src0_sel:DWORD src1_sel:WORD_1\n"
    "v_max_f32_e64 v228, 0, v228\n"
    "v_lshlrev_b32_sdwa v197, 1, v178 dst_sel:DWORD dst_unused:UNUSED_PAD src0_sel:DWORD src1_sel:WORD_1\n"
    "v_add_f32_e32 v228, 0x3727c5ac, v228\n"
    "v_lshlrev_b32_sdwa v205, 1, v179 dst_sel:DWORD dst_unused:UNUSED_PAD src0_sel:DWORD src1_sel:WORD_1\n"
    "v_rsq_f32_e64 v228, v228\n"
    "v_lshlrev_b32_sdwa v198, 1, v180 dst_sel:DWORD dst_unused:UNUSED_PAD src0_sel:DWORD src1_sel:WORD_1\n"
    "v_mul_f32_e64 v228, s50, v228\n"
    "ds_write_b32 v254, v228\n"
    "ds_read_b128 a[236:239], v243 offset:25664\n"
    "v_mul_f32_e64 v224, v228, -v224\n"
    "ds_write_b32 v254, v224 offset:1024\n"
    "ds_read_u16 v195, v195\n"
    "v_add_u32_e64 v254, s56, v242\n"
    "v_lshlrev_b32_sdwa v206, 1, v181 dst_sel:DWORD dst_unused:UNUSED_PAD src0_sel:DWORD src1_sel:WORD_1\n"
    "v_lshlrev_b32_sdwa v199, 1, v182 dst_sel:DWORD dst_unused:UNUSED_PAD src0_sel:DWORD src1_sel:WORD_1\n"
    "v_fma_mix_f32 v234, v184, v232, 0 op_sel_hi:[1,1,0]\n"
    "v_mfma_f32_16x16x32_bf16 v[172:175], a[240:243], v[152:155], v[172:175]\n"
    "s_waitcnt lgkmcnt(7)\n"
    "ds_read_b128 a[244:247], v244 offset:10240\n"
    "buffer_load_dwordx4 a[228:231], v245, s[40:43], s89 offen offset:128\n"
    "v_or_b32_e32 v191, v191, v207\n"
    "v_lshlrev_b32_sdwa v207, 1, v183 dst_sel:DWORD dst_unused:UNUSED_PAD src0_sel:DWORD src1_sel:WORD_1\n"
    "v_fma_mix_f32 v232, v184, v232, 0 op_sel:[1,1,0] op_sel_hi:[1,1,0]\n"
    "v_mfma_f32_16x16x32_bf16 v[176:179], a[112:115], v[156:159], 0\n"
    "ds_read_u16_d16_hi v203, v203\n"
    "ds_read_u16 v196, v196\n"
    "v_cvt_pk_bf16_f32 v184, v234, v232\n"
    "v_mfma_f32_16x16x32_bf16 v[176:179], a[160:163], v[160:163], v[176:179]\n"
    "ds_read_u16_d16_hi v204, v204\n"
    "ds_read_u16 v197, v197\n"
    "v_mfma_f32_16x16x32_bf16 v[176:179], a[72:75], v[164:167], v[176:179]\n"
    "v_pk_fma_f16 v232, v188, v188, s48\n"
    "v_or_b32_e64 v192, v192, v200\n"
    "v_mfma_f32_16x16x32_bf16 v[180:183], a[116:119], v[156:159], 0\n"
    "s_waitcnt lgkmcnt(6)\n"
    "s_barrier\n"
    "v_smfmac_f32_32x32x32_bf16 v[96:111], a[248:251], a[232:239], v241\n"
    "ds_read_b128 v[224:227], v254\n"
    "ds_read_b128 v[228:231], v254 offset:1024\n"
    "v_lshlrev_b32_e64 v254, 3, v244\n"
    "v_pk_mul_f16 v232, v188, v232\n"
    "v_or_b32_e32 v193, v193, v201\n"
    "v_mfma_f32_16x16x32_bf16 v[180:183], a[164:167], v[160:163], v[180:183]\n"
    "s_mov_b32 exec_hi, 0\n"
    "buffer_load_dword v233, v254, s[72:75], s53 offen sc1\n"
    "s_mov_b32 exec_hi, -1\n"
    "v_exp_f16_e32 v234, v232\n"
    "v_mfma_f32_16x16x32_bf16 v[180:183], a[240:243], v[164:167], v[180:183]\n"
    "s_add_u32 s53, s53, 393216\n"
    "ds_read_u16_d16_hi v205, v205\n"
    "ds_read_u16 v198, v198\n"
    "v_exp_f16_sdwa v234, v232 dst_sel:WORD_1 dst_unused:UNUSED_PRESERVE src0_sel:WORD_1\n"
    "v_mfma_f32_16x16x32_bf16 v[96:99], a[32:35], v[184:187], v[96:99]\n"
    "ds_read_u16_d16_hi v206, v206\n"
    "ds_read_u16 v199, v199\n"
    "v_pk_fma_f16 v234, v234, s49, s49\n"
    "v_mfma_f32_16x16x32_bf16 v[100:103], a[36:39], v[184:187], v[100:103]\n"
    "s_waitcnt lgkmcnt(8)\n"
    "v_rcp_f16_e32 v232, v234\n"
    "v_mfma_f32_16x16x32_bf16 v[112:115], a[40:43], v[184:187], v[112:115]\n"
    "ds_read_u16_d16_hi v207, v207\n"
    "v_rcp_f16_sdwa v232, v234 dst_sel:WORD_1 dst_unused:UNUSED_PRESERVE src0_sel:WORD_1\n"
    "v_mfma_f32_16x16x32_bf16 v[116:119], a[44:47], v[184:187], v[116:119]\n"
    "s_waitcnt lgkmcnt(6)\n"
    "ds_read_b128 a[232:235], v243 offset:26688\n"
    "v_fma_mix_f32 v234, v188, v232, 0 op_sel_hi:[1,1,0]\n"
    "v_mfma_f32_16x16x32_bf16 v[128:131], v[236:239], v[184:187], v[128:131]\n"
    "s_waitcnt lgkmcnt(6)\n"
    "ds_read_b128 a[236:239], v243 offset:27712\n"
    "v_fma_mix_f32 v232, v188, v232, 0 op_sel:[1,1,0] op_sel_hi:[1,1,0]\n"
    "v_lshlrev_b32_e64 v254, 3, v244\n"
    "v_cvt_pk_bf16_f32 v188, v234, v232\n"
    "v_lshlrev_b32_sdwa v201, 1, v171 dst_sel:DWORD dst_unused:UNUSED_PAD src0_sel:DWORD src1_sel:WORD_1\n"
    "v_pk_fma_f32 v[64:65], v[64:65], v[224:225], v[228:229]\n"
    "v_pk_fma_f32 v[66:67], v[66:67], v[226:227], v[230:231]\n"
    "v_pk_fma_f32 v[68:69], v[68:69], v[224:225], v[228:229]\n"
    "v_pk_fma_f32 v[70:71], v[70:71], v[226:227], v[230:231]\n"
    "v_pk_fma_f32 v[72:73], v[72:73], v[224:225], v[228:229]\n"
    "v_pk_fma_f32 v[74:75], v[74:75], v[226:227], v[230:231]\n"
    "v_pk_fma_f32 v[76:77], v[76:77], v[224:225], v[228:229]\n"
    "v_pk_fma_f32 v[78:79], v[78:79], v[226:227], v[230:231]\n"
    "v_pk_fma_f32 v[80:81], v[80:81], v[224:225], v[228:229]\n"
    "v_pk_fma_f32 v[82:83], v[82:83], v[226:227], v[230:231]\n"
    "v_pk_fma_f32 v[84:85], v[84:85], v[224:225], v[228:229]\n"
    "v_pk_fma_f32 v[86:87], v[86:87], v[226:227], v[230:231]\n"
    "v_pk_fma_f32 v[88:89], v[88:89], v[224:225], v[228:229]\n"
    "v_pk_fma_f32 v[90:91], v[90:91], v[226:227], v[230:231]\n"
    "v_pk_fma_f32 v[92:93], v[92:93], v[224:225], v[228:229]\n"
    "v_pk_fma_f32 v[94:95], v[94:95], v[226:227], v[230:231]\n"
    "v_pk_fma_f32 v[0:1], v[0:1], v[224:225], v[228:229]\n"
    "v_pk_fma_f32 v[2:3], v[2:3], v[226:227], v[230:231]\n"
    "v_pk_fma_f32 v[4:5], v[4:5], v[224:225], v[228:229]\n"
    "v_pk_fma_f32 v[6:7], v[6:7], v[226:227], v[230:231]\n"
    "v_pk_fma_f32 v[8:9], v[8:9], v[224:225], v[228:229]\n"
    "v_pk_fma_f32 v[10:11], v[10:11], v[226:227], v[230:231]\n"
    "v_pk_fma_f32 v[12:13], v[12:13], v[224:225], v[228:229]\n"
    "v_pk_fma_f32 v[14:15], v[14:15], v[226:227], v[230:231]\n"
    "v_pk_fma_f32 v[16:17], v[16:17], v[224:225], v[228:229]\n"
    "v_pk_fma_f32 v[18:19], v[18:19], v[226:227], v[230:231]\n"
    "v_pk_fma_f32 v[20:21], v[20:21], v[224:225], v[228:229]\n"
    "v_pk_fma_f32 v[22:23], v[22:23], v[226:227], v[230:231]\n"
    "v_pk_fma_f32 v[24:25], v[24:25], v[224:225], v[228:229]\n"
    "v_pk_fma_f32 v[26:27], v[26:27], v[226:227], v[230:231]\n"
    "v_pk_fma_f32 v[28:29], v[28:29], v[224:225], v[228:229]\n"
    "v_pk_fma_f32 v[30:31], v[30:31], v[226:227], v[230:231]\n"
    "v_pk_fma_f32 v[32:33], v[32:33], v[224:225], v[228:229]\n"
    "v_pk_fma_f32 v[34:35], v[34:35], v[226:227], v[230:231]\n"
    "v_pk_fma_f32 v[36:37], v[36:37], v[224:225], v[228:229]\n"
    "v_pk_fma_f32 v[38:39], v[38:39], v[226:227], v[230:231]\n"
    "v_pk_fma_f32 v[40:41], v[40:41], v[224:225], v[228:229]\n"
    "v_pk_fma_f32 v[42:43], v[42:43], v[226:227], v[230:231]\n"
    "v_pk_fma_f32 v[44:45], v[44:45], v[224:225], v[228:229]\n"
    "v_pk_fma_f32 v[46:47], v[46:47], v[226:227], v[230:231]\n"
    "v_pk_fma_f32 v[48:49], v[48:49], v[224:225], v[228:229]\n"
    "v_pk_fma_f32 v[50:51], v[50:51], v[226:227], v[230:231]\n"
    "v_pk_fma_f32 v[52:53], v[52:53], v[224:225], v[228:229]\n"
    "v_pk_fma_f32 v[54:55], v[54:55], v[226:227], v[230:231]\n"
    "v_pk_fma_f32 v[56:57], v[56:57], v[224:225], v[228:229]\n"
    "v_pk_fma_f32 v[58:59], v[58:59], v[226:227], v[230:231]\n"
    "v_pk_fma_f32 v[60:61], v[60:61], v[224:225], v[228:229]\n"
    "v_pk_fma_f32 v[62:63], v[62:63], v[226:227], v[230:231]\n"
    "v_mfma_f32_16x16x32_bf16 v[132:135], a[244:247], v[184:187], v[132:135]\n"
    "s_waitcnt lgkmcnt(0)\n"
    "s_mov_b32 exec_hi, 0\n"
    "buffer_load_dword v233, v254, s[80:83], s85 offen sc1\n"
    "s_mov_b32 exec_hi, -1\n"
    "v_mfma_f32_16x16x32_bf16 v[104:107], a[32:35], v[188:191], v[104:107]\n"
    "s_nop 0\n"
    "ds_read_b128 a[240:243], v244 offset:38912\n"
    "ds_read_u16_d16_hi v201, v201\n"
    "v_mfma_f32_16x16x32_bf16 v[108:111], a[36:39], v[188:191], v[108:111]\n"
    "v_cvt_pk_bf16_f32 v208, v64, v68\n"
    "v_cvt_pk_bf16_f32 v212, v65, v69\n"
    "v_mfma_f32_16x16x32_bf16 v[120:123], a[40:43], v[188:191], v[120:123]\n"
    "v_cvt_pk_bf16_f32 v216, v66, v70\n"
    "v_cvt_pk_bf16_f32 v220, v67, v71\n"
    "v_mfma_f32_16x16x32_bf16 v[124:127], a[44:47], v[188:191], v[124:127]\n"
    "v_cvt_pk_bf16_f32 v209, v72, v76\n"
    "v_cvt_pk_bf16_f32 v213, v73, v77\n"
    "v_mfma_f32_16x16x32_bf16 v[136:139], v[236:239], v[188:191], v[136:139]\n"
    "ds_read_b128 v[236:239], v244 offset:12288\n"
    "v_cvt_pk_bf16_f32 v217, v74, v78\n"
    "v_mfma_f32_16x16x32_bf16 v[140:143], a[244:247], v[188:191], v[140:143]\n"
    "v_cvt_pk_bf16_f32 v221, v75, v79\n"
    "v_cvt_pk_bf16_f32 v210, v80, v84\n"
    "v_smfmac_f32_32x32x32_bf16 v[112:127], a[248:251], a[232:239], v241\n"
    "v_cvt_pk_bf16_f32 v214, v81, v85\n"
    "v_cvt_pk_bf16_f32 v218, v82, v86\n"
    "v_cvt_pk_bf16_f32 v222, v83, v87\n"
    "v_cvt_pk_bf16_f32 v211, v88, v92\n"
    "ds_write_b128 v253, v[208:211] offset:2048\n"
    "v_cvt_pk_bf16_f32 v215, v89, v93\n"
    "v_cvt_pk_bf16_f32 v219, v90, v94\n"
    "v_cvt_pk_bf16_f32 v223, v91, v95\n"
    "v_cvt_pk_bf16_f32 v80, v0, v4\n"
    "v_cvt_pk_bf16_f32 v84, v1, v5\n"
    "v_cvt_pk_bf16_f32 v88, v2, v6\n"
    "v_cvt_pk_bf16_f32 v92, v3, v7\n"
    "v_cvt_pk_bf16_f32 v81, v8, v12\n"
    "v_cvt_pk_bf16_f32 v85, v9, v13\n"
    "v_cvt_pk_bf16_f32 v89, v10, v14\n"
    "v_cvt_pk_bf16_f32 v93, v11, v15\n"
    "v_cvt_pk_bf16_f32 v82, v16, v20\n"
    "v_cvt_pk_bf16_f32 v86, v17, v21\n"
    "v_cvt_pk_bf16_f32 v90, v18, v22\n"
    "v_cvt_pk_bf16_f32 v94, v19, v23\n"
    "v_cvt_pk_bf16_f32 v83, v24, v28\n"
    "v_cvt_pk_f16_f32 v184, v168, v169\n"
    "v_cvt_pk_bf16_f32 v87, v25, v29\n"
    "v_lshlrev_b32_sdwa v185, 1, v170 dst_sel:DWORD dst_unused:UNUSED_PAD src0_sel:DWORD src1_sel:WORD_1\n"
    "v_mfma_f32_16x16x32_bf16 v[168:171], a[120:123], v[144:147], 0\n"
    "ds_read_u16 v185, v185\n"
    "ds_write_b128 v253, v[212:215] offset:5120\n"
    "v_lshlrev_b32_sdwa v186, 1, v172 dst_sel:DWORD dst_unused:UNUSED_PAD src0_sel:DWORD src1_sel:WORD_1\n"
    "v_cvt_pk_bf16_f32 v91, v26, v30\n"
    "v_or_b32_e64 v194, v194, v202\n"
    "v_mfma_f32_16x16x32_bf16 v[168:171], a[168:171], v[148:151], v[168:171]\n"
    "v_lshlrev_b32_sdwa v202, 1, v173 dst_sel:DWORD dst_unused:UNUSED_PAD src0_sel:DWORD src1_sel:WORD_1\n"
    "v_lshlrev_b32_sdwa v187, 1, v174 dst_sel:DWORD dst_unused:UNUSED_PAD src0_sel:DWORD src1_sel:WORD_1\n"
    "v_mfma_f32_16x16x32_bf16 v[168:171], a[76:79], v[152:155], v[168:171]\n"
    "ds_read_u16 v186, v186\n"
    "ds_write_b128 v253, v[80:83] offset:0\n"
    "v_cvt_pk_bf16_f32 v95, v27, v31\n"
    "v_cvt_pk_f16_f32 v188, v176, v177\n"
    "v_lshlrev_b32_sdwa v189, 1, v178 dst_sel:DWORD dst_unused:UNUSED_PAD src0_sel:DWORD src1_sel:WORD_1\n"
    "v_lshlrev_b32_sdwa v190, 1, v180 dst_sel:DWORD dst_unused:UNUSED_PAD src0_sel:DWORD src1_sel:WORD_1\n"
    "v_lshlrev_b32_sdwa v191, 1, v182 dst_sel:DWORD dst_unused:UNUSED_PAD src0_sel:DWORD src1_sel:WORD_1\n"
    "v_or_b32_e64 v195, v195, v203\n"
    "v_lshlrev_b32_sdwa v203, 1, v175 dst_sel:DWORD dst_unused:UNUSED_PAD src0_sel:DWORD src1_sel:WORD_1\n"
    "v_mfma_f32_16x16x32_bf16 v[172:175], a[124:127], v[144:147], 0\n"
    "ds_read_u16_d16_hi v202, v202\n"
    "ds_write_b128 v253, v[84:87] offset:3072\n"
    "v_mfma_f32_16x16x32_bf16 v[172:175], a[172:175], v[148:151], v[172:175]\n"
    "s_waitcnt lgkmcnt(6)\n"
    "v_cvt_pk_bf16_f32 v64, v32, v36\n"
    "v_cvt_pk_bf16_f32 v68, v33, v37\n"
    "v_mfma_f32_16x16x32_bf16 v[172:175], a[240:243], v[152:155], v[172:175]\n"
    "s_waitcnt lgkmcnt(4)\n"
    "v_cvt_pk_bf16_f32 v72, v34, v38\n"
    "v_cvt_pk_bf16_f32 v76, v35, v39\n"
    "v_cvt_pk_bf16_f32 v65, v40, v44\n"
    "v_or_b32_e64 v196, v196, v204\n"
    "v_cvt_pk_bf16_f32 v69, v41, v45\n"
    "v_cvt_pk_bf16_f32 v73, v42, v46\n"
    "v_cvt_pk_bf16_f32 v77, v43, v47\n"
    "v_cvt_pk_bf16_f32 v66, v48, v52\n"
    "v_cvt_pk_bf16_f32 v70, v49, v53\n"
    "v_or_b32_e64 v197, v197, v205\n"
    "v_lshlrev_b32_sdwa v205, 1, v179 dst_sel:DWORD dst_unused:UNUSED_PAD src0_sel:DWORD src1_sel:WORD_1\n"
    "v_mfma_f32_16x16x32_bf16 v[176:179], a[120:123], v[156:159], 0\n"
    "ds_read_u16 v187, v187\n"
    "ds_read_u16_d16_hi v203, v203\n"
    "v_mfma_f32_16x16x32_bf16 v[176:179], a[168:171], v[160:163], v[176:179]\n"
    "ds_read_b128 a[244:247], v244 offset:14336\n"
    "ds_read_u16 v189, v189\n"
    "v_mfma_f32_16x16x32_bf16 v[176:179], a[76:79], v[164:167], v[176:179]\n"
    "v_cvt_pk_bf16_f32 v74, v50, v54\n"
    "v_cvt_pk_bf16_f32 v78, v51, v55\n"
    "v_cvt_pk_bf16_f32 v67, v56, v60\n"
    "v_cvt_pk_bf16_f32 v71, v57, v61\n"
    "v_cvt_pk_bf16_f32 v75, v58, v62\n"
    "v_or_b32_e32 v198, v198, v206\n"
    "v_mfma_f32_16x16x32_bf16 v[96:99], a[176:179], v[192:195], v[96:99]\n"
    "s_waitcnt lgkmcnt(4)\n"
    "ds_write_b128 v253, v[64:67] offset:1024\n"
    "v_mfma_f32_16x16x32_bf16 v[100:103], a[180:183], v[192:195], v[100:103]\n"
    "v_lshlrev_b32_sdwa v206, 1, v181 dst_sel:DWORD dst_unused:UNUSED_PAD src0_sel:DWORD src1_sel:WORD_1\n"
    "v_cvt_pk_bf16_f32 v79, v59, v63\n"
    "v_mfma_f32_16x16x32_bf16 v[112:115], a[184:187], v[192:195], v[112:115]\n"
    "v_or_b32_e64 v199, v199, v207\n"
    "v_lshlrev_b32_sdwa v207, 1, v183 dst_sel:DWORD dst_unused:UNUSED_PAD src0_sel:DWORD src1_sel:WORD_1\n"
    "v_mfma_f32_16x16x32_bf16 v[180:183], a[124:127], v[156:159], 0\n"
    "ds_read_b128 a[232:235], v243 offset:28736\n"
    "ds_write_b128 v253, v[68:71] offset:4096\n"
    "v_mfma_f32_16x16x32_bf16 v[180:183], a[172:175], v[160:163], v[180:183]\n"
    "v_or_b32_e64 v185, v185, v201\n"
    "v_or_b32_e32 v186, v186, v202\n"
    "v_mfma_f32_16x16x32_bf16 v[180:183], a[240:243], v[164:167], v[180:183]\n"
    "v_pk_fma_f16 v232, v184, v184, s48\n"
    "v_lshlrev_b32_sdwa v201, 1, v171 dst_sel:DWORD dst_unused:UNUSED_PAD src0_sel:DWORD src1_sel:WORD_1\n"
    "v_mfma_f32_16x16x32_bf16 v[116:119], a[188:191], v[192:195], v[116:119]\n"
    "s_waitcnt lgkmcnt(4)\n"
    "ds_read_u16_d16_hi v205, v205\n"
    "ds_read_b128 a[236:239], v243 offset:29760\n"
    "v_mfma_f32_16x16x32_bf16 v[128:131], v[236:239], v[192:195], v[128:131]\n"
    "ds_read_u16 v190, v190\n"
    "ds_read_u16_d16_hi v206, v206\n"
    "v_mfma_f32_16x16x32_bf16 v[132:135], a[244:247], v[192:195], v[132:135]\n"
    "v_or_b32_e64 v187, v187, v203\n"
    "v_pk_mul_f16 v232, v184, v232\n"
    "v_mfma_f32_16x16x32_bf16 v[104:107], a[176:179], v[196:199], v[104:107]\n"
    "ds_read_u16 v191, v191\n"
    "v_exp_f16_e64 v234, v232\n"
    "v_mfma_f32_16x16x32_bf16 v[108:111], a[180:183], v[196:199], v[108:111]\n"
    "ds_read_u16_d16_hi v207, v207\n"
    "v_exp_f16_sdwa v234, v232 dst_sel:WORD_1 dst_unused:UNUSED_PRESERVE src0_sel:WORD_1\n"
    "v_mfma_f32_16x16x32_bf16 v[120:123], a[184:187], v[196:199], v[120:123]\n"
    "s_nop 0\n"
    "v_pk_fma_f16 v234, v234, s49, s49\n"
    "v_cvt_pk_f16_f32 v192, v168, v169\n"
    "v_mfma_f32_16x16x32_bf16 v[124:127], a[188:191], v[196:199], v[124:127]\n"
    "s_waitcnt lgkmcnt(6)\n"
    "ds_read_b128 a[240:243], v244 offset:32768\n"
    "ds_read_u16_d16_hi v201, v201\n"
    "v_mfma_f32_16x16x32_bf16 v[136:139], v[236:239], v[196:199], v[136:139]\n"
    "s_barrier\n"
    "v_mfma_f32_16x16x32_bf16 v[140:143], a[244:247], v[196:199], v[140:143]\n"
    "s_waitcnt lgkmcnt(6)\n"
    "ds_read_b128 v[144:147], v242 offset:0\n"
    "ds_read_b128 v[148:151], v242 offset:1024\n"
    "v_or_b32_e64 v189, v189, v205\n"
    "v_lshlrev_b32_sdwa v193, 1, v170 dst_sel:DWORD dst_unused:UNUSED_PAD src0_sel:DWORD src1_sel:WORD_1\n"
    "v_lshlrev_b32_sdwa v194, 1, v172 dst_sel:DWORD dst_unused:UNUSED_PAD src0_sel:DWORD src1_sel:WORD_1\n"
    "v_smfmac_f32_32x32x32_bf16 v[128:143], a[248:251], a[232:239], v241\n"
    "ds_read_b128 v[152:155], v242 offset:2048\n"
    "ds_read_b128 v[156:159], v242 offset:3072\n"
    "v_lshlrev_b32_sdwa v202, 1, v173 dst_sel:DWORD dst_unused:UNUSED_PAD src0_sel:DWORD src1_sel:WORD_1\n"
    "v_lshlrev_b32_sdwa v195, 1, v174 dst_sel:DWORD dst_unused:UNUSED_PAD src0_sel:DWORD src1_sel:WORD_1\n"
    "v_lshlrev_b32_sdwa v203, 1, v175 dst_sel:DWORD dst_unused:UNUSED_PAD src0_sel:DWORD src1_sel:WORD_1\n"
    "v_rcp_f16_e32 v232, v234\n"
    "s_waitcnt lgkmcnt(8)\n"
    "v_or_b32_e64 v190, v190, v206\n"
    "v_mov_b32_e64 v64, v216\n"
    "v_rcp_f16_sdwa v232, v234 dst_sel:WORD_1 dst_unused:UNUSED_PRESERVE src0_sel:WORD_1\n"
    "v_cvt_pk_f16_f32 v196, v176, v177\n"
    "v_mov_b32_e64 v65, v217\n"
    "v_lshlrev_b32_sdwa v197, 1, v178 dst_sel:DWORD dst_unused:UNUSED_PAD src0_sel:DWORD src1_sel:WORD_1\n"
    "v_mov_b32_e64 v66, v218\n"
    "v_lshlrev_b32_sdwa v205, 1, v179 dst_sel:DWORD dst_unused:UNUSED_PAD src0_sel:DWORD src1_sel:WORD_1\n"
    "v_mov_b32_e64 v67, v219\n"
    "v_lshlrev_b32_sdwa v198, 1, v180 dst_sel:DWORD dst_unused:UNUSED_PAD src0_sel:DWORD src1_sel:WORD_1\n"
    "v_mov_b32_e64 v68, v220\n"
    "v_lshlrev_b32_sdwa v206, 1, v181 dst_sel:DWORD dst_unused:UNUSED_PAD src0_sel:DWORD src1_sel:WORD_1\n"
    "v_mov_b32_e64 v69, v221\n"
    "v_lshlrev_b32_sdwa v199, 1, v182 dst_sel:DWORD dst_unused:UNUSED_PAD src0_sel:DWORD src1_sel:WORD_1\n"
    "v_mov_b32_e64 v70, v222\n"
    "v_mov_b32_e64 v71, v223\n"
    "v_fma_mix_f32 v234, v184, v232, 0 op_sel_hi:[1,1,0]\n"
    "s_waitcnt lgkmcnt(5)\n"
    "v_or_b32_e32 v191, v191, v207\n"
    "v_lshlrev_b32_sdwa v207, 1, v183 dst_sel:DWORD dst_unused:UNUSED_PAD src0_sel:DWORD src1_sel:WORD_1\n"
    "v_fma_mix_f32 v232, v184, v232, 0 op_sel:[1,1,0] op_sel_hi:[1,1,0]\n"
    "s_waitcnt lgkmcnt(3)\n"
    "v_mfma_f32_16x16x32_bf16 v[168:171], a[80:83], v[144:147], 0\n"
    "ds_read_b128 v[160:163], v242 offset:4096\n"
    "ds_read_b128 v[164:167], v242 offset:5120\n"
    "v_mfma_f32_16x16x32_bf16 v[172:175], a[84:87], v[144:147], 0\n"
    "s_waitcnt lgkmcnt(4)\n"
    "ds_read_u16 v193, v193\n"
    "ds_read_u16 v194, v194\n"
    "v_mfma_f32_16x16x32_bf16 v[168:171], a[128:131], v[148:151], v[168:171]\n"
    "s_waitcnt lgkmcnt(5)\n"
    "ds_read_u16_d16_hi v202, v202\n"
    "v_cvt_pk_bf16_f32 v184, v234, v232\n"
    "v_mfma_f32_16x16x32_bf16 v[168:171], a[48:51], v[152:155], v[168:171]\n"
    "s_waitcnt lgkmcnt(5)\n"
    "v_pk_fma_f16 v232, v188, v188, s48\n"
    "v_pk_mul_f16 v232, v188, v232\n"
    "v_mfma_f32_16x16x32_bf16 v[172:175], a[132:135], v[148:151], v[172:175]\n"
    "ds_read_b128 v[236:239], v244 offset:16384\n"
    "ds_read_u16 v195, v195\n"
    "v_mfma_f32_16x16x32_bf16 v[172:175], a[240:243], v[152:155], v[172:175]\n"
    "v_exp_f16_e64 v234, v232\n"
    "v_mfma_f32_16x16x32_bf16 v[176:179], a[80:83], v[156:159], 0\n"
    "ds_read_u16_d16_hi v203, v203\n"
    "v_exp_f16_sdwa v234, v232 dst_sel:WORD_1 dst_unused:UNUSED_PRESERVE src0_sel:WORD_1\n"
    "v_mfma_f32_16x16x32_bf16 v[180:183], a[84:87], v[156:159], 0\n"
    "v_pk_fma_f16 v234, v234, s49, s49\n"
    "v_rcp_f16_e32 v232, v234\n"
    "v_mfma_f32_16x16x32_bf16 v[96:99], a[192:195], v[184:187], v[96:99]\n"
    "s_waitcnt lgkmcnt(7)\n"
    "ds_read_b128 a[244:247], v244 offset:18432\n"
    "ds_read_u16 v197, v197\n"
    "v_mfma_f32_16x16x32_bf16 v[176:179], a[128:131], v[160:163], v[176:179]\n"
    "s_waitcnt lgkmcnt(8)\n"
    "v_rcp_f16_sdwa v232, v234 dst_sel:WORD_1 dst_unused:UNUSED_PRESERVE src0_sel:WORD_1\n"
    "v_mfma_f32_16x16x32_bf16 v[176:179], a[48:51], v[164:167], v[176:179]\n"
    "s_nop 0\n"
    "ds_read_u16_d16_hi v205, v205\n"
    "v_fma_mix_f32 v234, v188, v232, 0 op_sel_hi:[1,1,0]\n"
    "v_mfma_f32_16x16x32_bf16 v[180:183], a[132:135], v[160:163], v[180:183]\n"
    "v_fma_mix_f32 v232, v188, v232, 0 op_sel:[1,1,0] op_sel_hi:[1,1,0]\n"
    "v_cvt_pk_bf16_f32 v188, v234, v232\n"
    "v_mfma_f32_16x16x32_bf16 v[180:183], a[240:243], v[164:167], v[180:183]\n"
    "s_waitcnt lgkmcnt(8)\n"
    "ds_read_u16 v198, v198\n"
    "v_or_b32_e32 v193, v193, v201\n"
    "v_mfma_f32_16x16x32_bf16 v[100:103], a[196:199], v[184:187], v[100:103]\n"
    "v_pk_fma_f16 v232, v192, v192, s48\n"
    "v_pk_mul_f16 v232, v192, v232\n"
    "v_mfma_f32_16x16x32_bf16 v[112:115], a[200:203], v[184:187], v[112:115]\n"
    "ds_read_u16_d16_hi v206, v206\n"
    "v_exp_f16_e32 v234, v232\n"
    "v_mfma_f32_16x16x32_bf16 v[116:119], a[204:207], v[184:187], v[116:119]\n"
    "s_waitcnt lgkmcnt(7)\n"
    "ds_read_u16 v199, v199\n"
    "v_or_b32_e64 v194, v194, v202\n"
    "v_mfma_f32_16x16x32_bf16 v[128:131], v[236:239], v[184:187], v[128:131]\n"
    "v_exp_f16_sdwa v234, v232 dst_sel:WORD_1 dst_unused:UNUSED_PRESERVE src0_sel:WORD_1\n"
    "v_mfma_f32_16x16x32_bf16 v[104:107], a[192:195], v[188:191], v[104:107]\n"
    "ds_read_u16_d16_hi v207, v207\n"
    "v_pk_fma_f16 v234, v234, s49, s49\n"
    "v_mfma_f32_16x16x32_bf16 v[108:111], a[196:199], v[188:191], v[108:111]\n"
    "v_rcp_f16_e64 v232, v234\n"
    "v_mfma_f32_16x16x32_bf16 v[120:123], a[200:203], v[188:191], v[120:123]\n"
    "s_waitcnt lgkmcnt(6)\n"
    "v_or_b32_e32 v195, v195, v203\n"
    "v_rcp_f16_sdwa v232, v234 dst_sel:WORD_1 dst_unused:UNUSED_PRESERVE src0_sel:WORD_1\n"
    "v_mfma_f32_16x16x32_bf16 v[132:135], a[244:247], v[184:187], v[132:135]\n"
    "v_fma_mix_f32 v234, v192, v232, 0 op_sel_hi:[1,1,0]\n"
    "v_fma_mix_f32 v232, v192, v232, 0 op_sel:[1,1,0] op_sel_hi:[1,1,0]\n"
    "v_mfma_f32_16x16x32_bf16 v[124:127], a[204:207], v[188:191], v[124:127]\n"
    "v_cvt_pk_bf16_f32 v192, v234, v232\n"
    "v_pk_fma_f16 v232, v196, v196, s48\n"
    "v_mfma_f32_16x16x32_bf16 v[136:139], v[236:239], v[188:191], v[136:139]\n"
    "v_pk_mul_f16 v232, v196, v232\n"
    "v_exp_f16_e32 v234, v232\n"
    "s_nop 0\n"
    "v_exp_f16_sdwa v234, v232 dst_sel:WORD_1 dst_unused:UNUSED_PRESERVE src0_sel:WORD_1\n"
    "s_waitcnt lgkmcnt(4)\n"
    "v_or_b32_e32 v197, v197, v205\n"
    "s_waitcnt lgkmcnt(2)\n"
    "v_or_b32_e32 v198, v198, v206\n"
    "s_waitcnt lgkmcnt(0)\n"
    "v_or_b32_e32 v199, v199, v207\n"
    "s_mov_b32 s59, s58\n"
    "s_add_u32 s57, s57, 1572864\n"
    "s_add_u32 s58, s58, 1572864\n"
    "s_mov_b32 s70, s46\n"
    "s_add_u32 s88, s57, 98304\n"
    "s_add_u32 s89, s58, 98304\n"
    "s_add_u32 s39, s59, 98304\n"
    "s_mov_b32 s82, 0\n"
    "s_add_u32 s64, s64, 1\n"
    "s_cmp_lt_u32 s64, 2\n"
    "s_cselect_b64 s[72:73], s[72:73], s[76:77]\n"
    "s_cselect_b32 s74, s74, s78\n"
    "s_cselect_b32 s53, s53, s79\n"
    "s_cmp_lg_u32 s64, 3\n"
    "s_cbranch_scc1 1b\n"
    ".Lreplk_last:\n"
    "v_lshlrev_b32_sdwa v184, 1, v168 dst_sel:DWORD dst_unused:UNUSED_PAD src0_sel:DWORD src1_sel:WORD_1\n"
    "v_lshlrev_b32_sdwa v200, 1, v169 dst_sel:DWORD dst_unused:UNUSED_PAD src0_sel:DWORD src1_sel:WORD_1\n"
    "v_lshlrev_b32_sdwa v185, 1, v170 dst_sel:DWORD dst_unused:UNUSED_PAD src0_sel:DWORD src1_sel:WORD_1\n"
    "v_lshlrev_b32_sdwa v201, 1, v171 dst_sel:DWORD dst_unused:UNUSED_PAD src0_sel:DWORD src1_sel:WORD_1\n"
    "v_mfma_f32_16x16x32_bf16 v[140:143], a[244:247], v[188:191], v[140:143]\n"
    "s_waitcnt vmcnt(2)\n"
    "ds_read_b128 v[56:59], v244 offset:36864\n"
    "ds_read_u16 v184, v184\n"
    "v_mfma_f32_16x16x32_bf16 v[168:171], a[88:91], v[144:147], 0\n"
    "s_nop 0\n"
    "ds_read_u16_d16_hi v200, v200\n"
    "ds_read_u16 v185, v185\n"
    "v_mfma_f32_16x16x32_bf16 v[168:171], a[136:139], v[148:151], v[168:171]\n"
    "v_lshlrev_b32_sdwa v186, 1, v172 dst_sel:DWORD dst_unused:UNUSED_PAD src0_sel:DWORD src1_sel:WORD_1\n"
    "v_lshlrev_b32_sdwa v202, 1, v173 dst_sel:DWORD dst_unused:UNUSED_PAD src0_sel:DWORD src1_sel:WORD_1\n"
    "v_mfma_f32_16x16x32_bf16 v[168:171], a[52:55], v[152:155], v[168:171]\n"
    "ds_read_u16_d16_hi v201, v201\n"
    "v_lshlrev_b32_sdwa v187, 1, v174 dst_sel:DWORD dst_unused:UNUSED_PAD src0_sel:DWORD src1_sel:WORD_1\n"
    "v_smfmac_f32_32x32x32_bf16 v[128:143], a[248:251], a[224:231], v241\n"
    "ds_read_u16 v186, v186\n"
    "v_lshlrev_b32_sdwa v203, 1, v175 dst_sel:DWORD dst_unused:UNUSED_PAD src0_sel:DWORD src1_sel:WORD_1\n"
    "v_lshlrev_b32_sdwa v188, 1, v176 dst_sel:DWORD dst_unused:UNUSED_PAD src0_sel:DWORD src1_sel:WORD_1\n"
    "v_lshlrev_b32_sdwa v204, 1, v177 dst_sel:DWORD dst_unused:UNUSED_PAD src0_sel:DWORD src1_sel:WORD_1\n"
    "v_lshlrev_b32_sdwa v189, 1, v178 dst_sel:DWORD dst_unused:UNUSED_PAD src0_sel:DWORD src1_sel:WORD_1\n"
    "v_mfma_f32_16x16x32_bf16 v[172:175], a[92:95], v[144:147], 0\n"
    "ds_read_b128 v[236:239], v244 offset:20480\n"
    "v_lshlrev_b32_sdwa v205, 1, v179 dst_sel:DWORD dst_unused:UNUSED_PAD src0_sel:DWORD src1_sel:WORD_1\n"
    "v_mfma_f32_16x16x32_bf16 v[172:175], a[140:143], v[148:151], v[172:175]\n"
    "s_nop 0\n"
    "ds_read_u16_d16_hi v202, v202\n"
    "v_lshlrev_b32_sdwa v190, 1, v180 dst_sel:DWORD dst_unused:UNUSED_PAD src0_sel:DWORD src1_sel:WORD_1\n"
    "v_mfma_f32_16x16x32_bf16 v[176:179], a[88:91], v[156:159], 0\n"
    "s_waitcnt lgkmcnt(7)\n"
    "v_lshlrev_b32_sdwa v206, 1, v181 dst_sel:DWORD dst_unused:UNUSED_PAD src0_sel:DWORD src1_sel:WORD_1\n"
    "v_lshlrev_b32_sdwa v191, 1, v182 dst_sel:DWORD dst_unused:UNUSED_PAD src0_sel:DWORD src1_sel:WORD_1\n"
    "v_mfma_f32_16x16x32_bf16 v[172:175], v[56:59], v[152:155], v[172:175]\n"
    "ds_read_u16 v187, v187\n"
    "buffer_load_dwordx4 a[232:235], v245, s[40:43], s57 offen offset:64\n"
    "buffer_load_dwordx4 a[224:227], v245, s[40:43], s57 offen offset:0\n"
    "buffer_load_dwordx4 v[208:211], v245, s[40:43], s57 offen offset:128\n"
    "v_mfma_f32_16x16x32_bf16 v[176:179], a[136:139], v[160:163], v[176:179]\n"
    "ds_read_b128 a[244:247], v244 offset:22528\n"
    "v_lshlrev_b32_sdwa v207, 1, v183 dst_sel:DWORD dst_unused:UNUSED_PAD src0_sel:DWORD src1_sel:WORD_1\n"
    "v_mfma_f32_16x16x32_bf16 v[176:179], a[52:55], v[164:167], v[176:179]\n"
    "ds_read_u16_d16_hi v203, v203\n"
    "v_pk_fma_f16 v234, v234, s49, s49\n"
    "v_mfma_f32_16x16x32_bf16 v[180:183], a[92:95], v[156:159], 0\n"
    "v_rcp_f16_e32 v232, v234\n"
    "v_mfma_f32_16x16x32_bf16 v[180:183], a[140:143], v[160:163], v[180:183]\n"
    "s_waitcnt lgkmcnt(8)\n"
    "ds_read_u16 v188, v188\n"
    "v_rcp_f16_sdwa v232, v234 dst_sel:WORD_1 dst_unused:UNUSED_PRESERVE src0_sel:WORD_1\n"
    "v_mfma_f32_16x16x32_bf16 v[180:183], v[56:59], v[164:167], v[180:183]\n"
    "ds_read_u16_d16_hi v204, v204\n"
    "v_fma_mix_f32 v234, v196, v232, 0 op_sel_hi:[1,1,0]\n"
    "v_mfma_f32_16x16x32_bf16 v[96:99], a[208:211], v[192:195], v[96:99]\n"
    "v_fma_mix_f32 v232, v196, v232, 0 op_sel:[1,1,0] op_sel_hi:[1,1,0]\n"
    "v_or_b32_e32 v184, v184, v200\n"
    "v_mfma_f32_16x16x32_bf16 v[100:103], a[212:215], v[192:195], v[100:103]\n"
    "s_waitcnt lgkmcnt(8)\n"
    "ds_read_u16 v189, v189\n"
    "v_cvt_pk_bf16_f32 v196, v234, v232\n"
    "v_mfma_f32_16x16x32_bf16 v[112:115], a[216:219], v[192:195], v[112:115]\n"
    "s_waitcnt lgkmcnt(7)\n"
    "v_or_b32_e32 v185, v185, v201\n"
    "v_lshlrev_b32_sdwa v201, 1, v171 dst_sel:DWORD dst_unused:UNUSED_PAD src0_sel:DWORD src1_sel:WORD_1\n"
    "v_mfma_f32_16x16x32_bf16 v[116:119], a[220:223], v[192:195], v[116:119]\n"
    "ds_read_u16_d16_hi v205, v205\n"
    "v_cvt_pk_bf16_f32 v96, v96, v97\n"
    "v_mfma_f32_16x16x32_bf16 v[128:131], v[236:239], v[192:195], v[128:131]\n"
    "v_cvt_pk_bf16_f32 v97, v98, v99\n"
    "v_cvt_pk_bf16_f32 v98, v100, v101\n"
    "v_mfma_f32_16x16x32_bf16 v[104:107], a[208:211], v[196:199], v[104:107]\n"
    "s_waitcnt lgkmcnt(7)\n"
    "ds_read_u16 v190, v190\n"
    "v_or_b32_e32 v186, v186, v202\n"
    "v_mfma_f32_16x16x32_bf16 v[108:111], a[212:215], v[196:199], v[108:111]\n"
    "s_nop 0\n"
    "ds_read_u16_d16_hi v206, v206\n"
    "v_lshlrev_b32_sdwa v202, 1, v173 dst_sel:DWORD dst_unused:UNUSED_PAD src0_sel:DWORD src1_sel:WORD_1\n"
    "v_mfma_f32_16x16x32_bf16 v[120:123], a[216:219], v[196:199], v[120:123]\n"
    "s_waitcnt lgkmcnt(7)\n"
    "v_cvt_pk_bf16_f32 v99, v102, v103\n"
    "v_cvt_pk_bf16_f32 v112, v112, v113\n"
    "v_mfma_f32_16x16x32_bf16 v[132:135], a[244:247], v[192:195], v[132:135]\n"
    "v_cvt_pk_f16_f32 v192, v168, v169\n"
    "v_lshlrev_b32_sdwa v193, 1, v170 dst_sel:DWORD dst_unused:UNUSED_PAD src0_sel:DWORD src1_sel:WORD_1\n"
    "v_mfma_f32_16x16x32_bf16 v[124:127], a[220:223], v[196:199], v[124:127]\n"
    "buffer_load_dwordx4 a[236:239], v245, s[40:43], s88 offen offset:64\n"
    "buffer_load_dwordx4 a[228:231], v245, s[40:43], s88 offen offset:0\n"
    "buffer_load_dwordx4 v[212:215], v245, s[40:43], s88 offen offset:128\n"
    "ds_write_b128 v243, v[96:99] offset:24640\n"
    "v_mfma_f32_16x16x32_bf16 v[136:139], v[236:239], v[196:199], v[136:139]\n"
    "s_waitcnt lgkmcnt(6)\n"
    "v_lshlrev_b32_sdwa v194, 1, v172 dst_sel:DWORD dst_unused:UNUSED_PAD src0_sel:DWORD src1_sel:WORD_1\n"
    "v_lshlrev_b32_sdwa v195, 1, v174 dst_sel:DWORD dst_unused:UNUSED_PAD src0_sel:DWORD src1_sel:WORD_1\n"
    "v_mfma_f32_16x16x32_bf16 v[140:143], a[244:247], v[196:199], v[140:143]\n"
    "v_cvt_pk_f16_f32 v196, v176, v177\n"
    "v_lshlrev_b32_sdwa v197, 1, v178 dst_sel:DWORD dst_unused:UNUSED_PAD src0_sel:DWORD src1_sel:WORD_1\n"
    "v_mfma_f32_16x16x32_bf16 v[168:171], a[96:99], v[144:147], 0\n"
    "s_waitcnt lgkmcnt(5)\n"
    "ds_read_u16 v191, v191\n"
    "ds_read_u16_d16_hi v207, v207\n"
    "v_mfma_f32_16x16x32_bf16 v[168:171], a[144:147], v[148:151], v[168:171]\n"
    "ds_read_u16 v193, v193\n"
    "v_lshlrev_b32_sdwa v198, 1, v180 dst_sel:DWORD dst_unused:UNUSED_PAD src0_sel:DWORD src1_sel:WORD_1\n"
    "v_mfma_f32_16x16x32_bf16 v[168:171], a[56:59], v[152:155], v[168:171]\n"
    "ds_read_u16_d16_hi v201, v201\n"
    "v_lshlrev_b32_sdwa v199, 1, v182 dst_sel:DWORD dst_unused:UNUSED_PAD src0_sel:DWORD src1_sel:WORD_1\n"
    "v_or_b32_e64 v187, v187, v203\n"
    "v_lshlrev_b32_sdwa v203, 1, v175 dst_sel:DWORD dst_unused:UNUSED_PAD src0_sel:DWORD src1_sel:WORD_1\n"
    "v_mfma_f32_16x16x32_bf16 v[172:175], a[100:103], v[144:147], 0\n"
    "s_waitcnt lgkmcnt(7)\n"
    "ds_read_u16 v194, v194\n"
    "v_or_b32_e32 v188, v188, v204\n"
    "v_mfma_f32_16x16x32_bf16 v[172:175], a[148:151], v[148:151], v[172:175]\n"
    "v_or_b32_e64 v189, v189, v205\n"
    "v_lshlrev_b32_sdwa v205, 1, v179 dst_sel:DWORD dst_unused:UNUSED_PAD src0_sel:DWORD src1_sel:WORD_1\n"
    "v_mfma_f32_16x16x32_bf16 v[172:175], a[60:63], v[152:155], v[172:175]\n"
    "ds_read_u16_d16_hi v202, v202\n"
    "v_cvt_pk_bf16_f32 v113, v114, v115\n"
    "v_mfma_f32_16x16x32_bf16 v[176:179], a[96:99], v[156:159], 0\n"
    "s_waitcnt lgkmcnt(6)\n"
    "v_or_b32_e32 v190, v190, v206\n"
    "v_lshlrev_b32_sdwa v206, 1, v181 dst_sel:DWORD dst_unused:UNUSED_PAD src0_sel:DWORD src1_sel:WORD_1\n"
    "v_mfma_f32_16x16x32_bf16 v[176:179], a[144:147], v[160:163], v[176:179]\n"
    "ds_read_b128 v[0:3], v244 offset:0\n"
    "ds_read_b128 a[252:255], v249 offset:0\n"
    "v_mfma_f32_16x16x32_bf16 v[176:179], a[56:59], v[164:167], v[176:179]\n"
    "v_cvt_pk_bf16_f32 v114, v116, v117\n"
    "v_cvt_pk_bf16_f32 v115, v118, v119\n"
    "v_mfma_f32_16x16x32_bf16 v[96:99], a[0:3], v[184:187], 0\n"
    "v_cvt_pk_bf16_f32 v128, v128, v129\n"
    "v_cvt_pk_bf16_f32 v129, v130, v131\n"
    "v_mfma_f32_16x16x32_bf16 v[100:103], a[4:7], v[184:187], 0\n"
    "ds_write_b128 v243, v[112:115] offset:26688\n"
    "v_mfma_f32_16x16x32_bf16 v[112:115], a[8:11], v[184:187], 0\n"
    "s_waitcnt lgkmcnt(7)\n"
    "v_or_b32_e32 v191, v191, v207\n"
    "v_lshlrev_b32_sdwa v207, 1, v183 dst_sel:DWORD dst_unused:UNUSED_PAD src0_sel:DWORD src1_sel:WORD_1\n"
    "v_mfma_f32_16x16x32_bf16 v[180:183], a[100:103], v[156:159], 0\n"
    "v_cvt_pk_bf16_f32 v130, v132, v133\n"
    "v_cvt_pk_bf16_f32 v131, v134, v135\n"
    "v_mfma_f32_16x16x32_bf16 v[180:183], a[148:151], v[160:163], v[180:183]\n"
    "ds_read_u16 v195, v195\n"
    "ds_write_b128 v243, v[128:131] offset:28736\n"
    "v_mfma_f32_16x16x32_bf16 v[180:183], a[60:63], v[164:167], v[180:183]\n"
    "s_waitcnt lgkmcnt(7)\n"
    "v_or_b32_e32 v193, v193, v201\n"
    "v_cvt_pk_bf16_f32 v104, v104, v105\n"
    "v_mfma_f32_16x16x32_bf16 v[116:119], a[12:15], v[184:187], 0\n"
    "v_cvt_pk_bf16_f32 v105, v106, v107\n"
    "v_cvt_pk_bf16_f32 v106, v108, v109\n"
    "v_cvt_pk_bf16_f32 v107, v110, v111\n"
    "v_mfma_f32_16x16x32_bf16 v[108:111], a[4:7], v[188:191], 0\n"
    "ds_read_u16_d16_hi v203, v203\n"
    "ds_write_b128 v243, v[104:107] offset:25664\n"
    "v_mfma_f32_16x16x32_bf16 v[104:107], a[0:3], v[188:191], 0\n"
    "s_waitcnt lgkmcnt(5)\n"
    "buffer_store_dwordx4 a[252:255], v255, s[68:71], s59 offen offset:0 sc1\n"
    "v_mfma_f32_16x16x32_bf16 v[128:131], v[0:3], v[184:187], 0\n"
    "s_nop 0\n"
    "ds_read_b128 v[4:7], v244 offset:2048\n"
    "ds_read_u16 v197, v197\n"
    "v_or_b32_e64 v194, v194, v202\n"
    "v_cvt_pk_bf16_f32 v120, v120, v121\n"
    "v_cvt_pk_bf16_f32 v121, v122, v123\n"
    "v_cvt_pk_bf16_f32 v122, v124, v125\n"
    "v_cvt_pk_bf16_f32 v123, v126, v127\n"
    "ds_write_b128 v243, v[120:123] offset:27712\n"
    "v_pk_fma_f16 v232, v192, v192, s48\n"
    "v_mfma_f32_16x16x32_bf16 v[120:123], a[8:11], v[188:191], 0\n"
    "v_cvt_pk_bf16_f32 v136, v136, v137\n"
    "v_cvt_pk_bf16_f32 v137, v138, v139\n"
    "v_mfma_f32_16x16x32_bf16 v[124:127], a[12:15], v[188:191], 0\n"
    "ds_read_u16_d16_hi v205, v205\n"
    "ds_read_u16 v198, v198\n"
    "v_cvt_pk_bf16_f32 v138, v140, v141\n"
    "v_cvt_pk_bf16_f32 v139, v142, v143\n"
    "ds_write_b128 v243, v[136:139] offset:29760\n"
    "s_waitcnt lgkmcnt(5)\n"
    "v_mfma_f32_16x16x32_bf16 v[132:135], v[4:7], v[184:187], 0\n"
    "v_or_b32_e32 v195, v195, v203\n"
    "v_pk_mul_f16 v232, v192, v232\n"
    "v_mfma_f32_16x16x32_bf16 v[136:139], v[0:3], v[188:191], 0\n"
    "ds_read_u16_d16_hi v206, v206\n"
    "ds_read_u16 v199, v199\n"
    "v_exp_f16_e64 v234, v232\n"
    "v_mfma_f32_16x16x32_bf16 v[140:143], v[4:7], v[188:191], 0\n"
    "ds_read_u16_d16_hi v207, v207\n"
    "v_exp_f16_sdwa v234, v232 dst_sel:WORD_1 dst_unused:UNUSED_PRESERVE src0_sel:WORD_1\n"
    "v_cvt_pk_f16_f32 v184, v168, v169\n"
    "v_pk_fma_f16 v234, v234, s49, s49\n"
    "v_lshlrev_b32_sdwa v185, 1, v170 dst_sel:DWORD dst_unused:UNUSED_PAD src0_sel:DWORD src1_sel:WORD_1\n"
    "v_lshlrev_b32_sdwa v201, 1, v171 dst_sel:DWORD dst_unused:UNUSED_PAD src0_sel:DWORD src1_sel:WORD_1\n"
    "v_mfma_f32_16x16x32_bf16 v[168:171], a[104:107], v[144:147], 0\n"
    "ds_read_b128 a[252:255], v249 offset:2048\n"
    "ds_read_u16 v185, v185\n"
    "v_mfma_f32_16x16x32_bf16 v[168:171], a[152:155], v[148:151], v[168:171]\n"
    "v_lshlrev_b32_sdwa v186, 1, v172 dst_sel:DWORD dst_unused:UNUSED_PAD src0_sel:DWORD src1_sel:WORD_1\n"
    "v_lshlrev_b32_sdwa v202, 1, v173 dst_sel:DWORD dst_unused:UNUSED_PAD src0_sel:DWORD src1_sel:WORD_1\n"
    "v_lshlrev_b32_sdwa v187, 1, v174 dst_sel:DWORD dst_unused:UNUSED_PAD src0_sel:DWORD src1_sel:WORD_1\n"
    "s_waitcnt lgkmcnt(7)\n"
    "v_or_b32_e32 v197, v197, v205\n"
    "v_lshlrev_b32_sdwa v203, 1, v175 dst_sel:DWORD dst_unused:UNUSED_PAD src0_sel:DWORD src1_sel:WORD_1\n"
    "v_rcp_f16_e64 v232, v234\n"
    "v_mfma_f32_16x16x32_bf16 v[168:171], a[64:67], v[152:155], v[168:171]\n"
    "ds_read_u16_d16_hi v201, v201\n"
    "ds_read_u16 v186, v186\n"
    "v_rcp_f16_sdwa v232, v234 dst_sel:WORD_1 dst_unused:UNUSED_PRESERVE src0_sel:WORD_1\n"
    "v_mfma_f32_16x16x32_bf16 v[172:175], a[108:111], v[144:147], 0\n"
    "ds_read_u16_d16_hi v202, v202\n"
    "v_cvt_pk_f16_f32 v188, v176, v177\n"
    "v_mfma_f32_16x16x32_bf16 v[172:175], a[156:159], v[148:151], v[172:175]\n"
    "v_lshlrev_b32_sdwa v189, 1, v178 dst_sel:DWORD dst_unused:UNUSED_PAD src0_sel:DWORD src1_sel:WORD_1\n"
    "v_lshlrev_b32_sdwa v205, 1, v179 dst_sel:DWORD dst_unused:UNUSED_PAD src0_sel:DWORD src1_sel:WORD_1\n"
    "v_lshlrev_b32_sdwa v190, 1, v180 dst_sel:DWORD dst_unused:UNUSED_PAD src0_sel:DWORD src1_sel:WORD_1\n"
    "v_lshlrev_b32_sdwa v191, 1, v182 dst_sel:DWORD dst_unused:UNUSED_PAD src0_sel:DWORD src1_sel:WORD_1\n"
    "v_fma_mix_f32 v234, v192, v232, 0 op_sel_hi:[1,1,0]\n"
    "v_mfma_f32_16x16x32_bf16 v[172:175], a[68:71], v[152:155], v[172:175]\n"
    "s_waitcnt lgkmcnt(7)\n"
    "ds_read_b128 v[8:11], v244 offset:4096\n"
    "ds_read_u16 v187, v187\n"
    "v_or_b32_e32 v198, v198, v206\n"
    "v_lshlrev_b32_sdwa v206, 1, v181 dst_sel:DWORD dst_unused:UNUSED_PAD src0_sel:DWORD src1_sel:WORD_1\n"
    "v_fma_mix_f32 v232, v192, v232, 0 op_sel:[1,1,0] op_sel_hi:[1,1,0]\n"
    "v_mfma_f32_16x16x32_bf16 v[176:179], a[104:107], v[156:159], 0\n"
    "ds_read_u16_d16_hi v203, v203\n"
    "v_cvt_pk_bf16_f32 v192, v234, v232\n"
    "v_mfma_f32_16x16x32_bf16 v[176:179], a[152:155], v[160:163], v[176:179]\n"
    "s_waitcnt lgkmcnt(7)\n"
    "buffer_store_dwordx4 a[252:255], v255, s[68:71], s59 offen offset:64 sc1\n"
    "ds_read_b128 v[12:15], v244 offset:6144\n"
    "v_mfma_f32_16x16x32_bf16 v[176:179], a[64:67], v[164:167], v[176:179]\n"
    "s_nop 0\n"
    "ds_read_b128 a[252:255], v249 offset:4096\n"
    "v_or_b32_e64 v199, v199, v207\n"
    "v_lshlrev_b32_sdwa v207, 1, v183 dst_sel:DWORD dst_unused:UNUSED_PAD src0_sel:DWORD src1_sel:WORD_1\n"
    "v_pk_fma_f16 v232, v196, v196, s48\n"
    "v_mfma_f32_16x16x32_bf16 v[180:183], a[108:111], v[156:159], 0\n"
    "ds_read_u16 v189, v189\n"
    "v_pk_mul_f16 v232, v196, v232\n"
    "v_mfma_f32_16x16x32_bf16 v[180:183], a[156:159], v[160:163], v[180:183]\n"
    "ds_read_u16_d16_hi v205, v205\n"
    "v_exp_f16_e32 v234, v232\n"
    "v_mfma_f32_16x16x32_bf16 v[180:183], a[68:71], v[164:167], v[180:183]\n"
    "s_waitcnt lgkmcnt(9)\n"
    "v_exp_f16_sdwa v234, v232 dst_sel:WORD_1 dst_unused:UNUSED_PRESERVE src0_sel:WORD_1\n"
    "v_mfma_f32_16x16x32_bf16 v[96:99], a[16:19], v[192:195], v[96:99]\n"
    "ds_read_u16 v190, v190\n"
    "v_pk_fma_f16 v234, v234, s49, s49\n"
    "v_mfma_f32_16x16x32_bf16 v[100:103], a[20:23], v[192:195], v[100:103]\n"
    "ds_read_u16_d16_hi v206, v206\n"
    "v_rcp_f16_e32 v232, v234\n"
    "v_mfma_f32_16x16x32_bf16 v[112:115], a[24:27], v[192:195], v[112:115]\n"
    "s_waitcnt lgkmcnt(8)\n"
    "v_rcp_f16_sdwa v232, v234 dst_sel:WORD_1 dst_unused:UNUSED_PRESERVE src0_sel:WORD_1\n"
    "v_mfma_f32_16x16x32_bf16 v[116:119], a[28:31], v[192:195], v[116:119]\n"
    "ds_read_u16 v191, v191\n"
    "v_or_b32_e64 v185, v185, v201\n"
    "v_mfma_f32_16x16x32_bf16 v[128:131], v[8:11], v[192:195], v[128:131]\n"
    "v_fma_mix_f32 v234, v196, v232, 0 op_sel_hi:[1,1,0]\n"
    "v_fma_mix_f32 v232, v196, v232, 0 op_sel:[1,1,0] op_sel_hi:[1,1,0]\n"
    "v_or_b32_e64 v186, v186, v202\n"
    "v_cvt_pk_bf16_f32 v196, v234, v232\n"
    "v_pk_fma_f16 v232, v184, v184, s48\n"
    "v_lshlrev_b32_sdwa v200, 1, v169 dst_sel:DWORD dst_unused:UNUSED_PAD src0_sel:DWORD src1_sel:WORD_1\n"
    "v_mfma_f32_16x16x32_bf16 v[104:107], a[16:19], v[196:199], v[104:107]\n"
    "s_waitcnt lgkmcnt(6)\n"
    "ds_read_u16_d16_hi v207, v207\n"
    "ds_read_b128 v[52:55], v244 offset:34816\n"
    "v_mfma_f32_16x16x32_bf16 v[132:135], v[12:15], v[192:195], v[132:135]\n"
    "s_waitcnt lgkmcnt(7)\n"
    "ds_read_u16_d16_hi v200, v200\n"
    "buffer_store_dwordx4 a[252:255], v255, s[68:71], s59 offen offset:128 sc1\n"
    "v_mfma_f32_16x16x32_bf16 v[108:111], a[20:23], v[196:199], v[108:111]\n"
    "v_or_b32_e64 v187, v187, v203\n"
    "v_pk_mul_f16 v232, v184, v232\n"
    "v_mfma_f32_16x16x32_bf16 v[120:123], a[24:27], v[196:199], v[120:123]\n"
    "ds_read_b128 a[252:255], v249 offset:1024\n"
    "v_exp_f16_e64 v234, v232\n"
    "v_mfma_f32_16x16x32_bf16 v[124:127], a[28:31], v[196:199], v[124:127]\n"
    "s_waitcnt lgkmcnt(7)\n"
    "v_or_b32_e32 v189, v189, v205\n"
    "v_exp_f16_sdwa v234, v232 dst_sel:WORD_1 dst_unused:UNUSED_PRESERVE src0_sel:WORD_1\n"
    "v_mfma_f32_16x16x32_bf16 v[136:139], v[8:11], v[196:199], v[136:139]\n"
    "ds_read_b128 v[16:19], v244 offset:8192\n"
    "v_lshlrev_b32_sdwa v192, 1, v168 dst_sel:DWORD dst_unused:UNUSED_PAD src0_sel:DWORD src1_sel:WORD_1\n"
    "v_mfma_f32_16x16x32_bf16 v[140:143], v[12:15], v[196:199], v[140:143]\n"
    "ds_read_u16 v192, v192\n"
    "v_pk_fma_f16 v234, v234, s49, s49\n"
    "v_lshlrev_b32_sdwa v193, 1, v170 dst_sel:DWORD dst_unused:UNUSED_PAD src0_sel:DWORD src1_sel:WORD_1\n"
    "v_lshlrev_b32_sdwa v201, 1, v171 dst_sel:DWORD dst_unused:UNUSED_PAD src0_sel:DWORD src1_sel:WORD_1\n"
    "v_mfma_f32_16x16x32_bf16 v[168:171], a[112:115], v[144:147], 0\n"
    "s_waitcnt lgkmcnt(7)\n"
    "ds_read_u16 v193, v193\n"
    "ds_read_u16_d16_hi v201, v201\n"
    "v_mfma_f32_16x16x32_bf16 v[168:171], a[160:163], v[148:151], v[168:171]\n"
    "s_nop 0\n"
    "v_lshlrev_b32_sdwa v194, 1, v172 dst_sel:DWORD dst_unused:UNUSED_PAD src0_sel:DWORD src1_sel:WORD_1\n"
    "v_lshlrev_b32_sdwa v202, 1, v173 dst_sel:DWORD dst_unused:UNUSED_PAD src0_sel:DWORD src1_sel:WORD_1\n"
    "v_lshlrev_b32_sdwa v195, 1, v174 dst_sel:DWORD dst_unused:UNUSED_PAD src0_sel:DWORD src1_sel:WORD_1\n"
    "v_lshlrev_b32_sdwa v203, 1, v175 dst_sel:DWORD dst_unused:UNUSED_PAD src0_sel:DWORD src1_sel:WORD_1\n"
    "v_rcp_f16_e64 v232, v234\n"
    "v_mfma_f32_16x16x32_bf16 v[168:171], a[72:75], v[152:155], v[168:171]\n"
    "ds_read_u16 v194, v194\n"
    "ds_read_u16_d16_hi v202, v202\n"
    "v_or_b32_e64 v190, v190, v206\n"
    "v_rcp_f16_sdwa v232, v234 dst_sel:WORD_1 dst_unused:UNUSED_PRESERVE src0_sel:WORD_1\n"
    "v_mfma_f32_16x16x32_bf16 v[172:175], a[116:119], v[144:147], 0\n"
    "s_waitcnt lgkmcnt(8)\n"
    "ds_read_u16 v195, v195\n"
    "v_lshlrev_b32_sdwa v196, 1, v176 dst_sel:DWORD dst_unused:UNUSED_PAD src0_sel:DWORD src1_sel:WORD_1\n"
    "v_mfma_f32_16x16x32_bf16 v[172:175], a[164:167], v[148:151], v[172:175]\n"
    "s_waitcnt lgkmcnt(7)\n"
    "buffer_store_dwordx4 a[252:255], v255, s[68:71], s39 offen offset:0 sc1\n"
    "ds_read_b128 v[20:23], v244 offset:10240\n"
    "v_lshlrev_b32_sdwa v204, 1, v177 dst_sel:DWORD dst_unused:UNUSED_PAD src0_sel:DWORD src1_sel:WORD_1\n"
    "v_lshlrev_b32_sdwa v197, 1, v178 dst_sel:DWORD dst_unused:UNUSED_PAD src0_sel:DWORD src1_sel:WORD_1\n"
    "v_lshlrev_b32_sdwa v205, 1, v179 dst_sel:DWORD dst_unused:UNUSED_PAD src0_sel:DWORD src1_sel:WORD_1\n"
    "v_lshlrev_b32_sdwa v198, 1, v180 dst_sel:DWORD dst_unused:UNUSED_PAD src0_sel:DWORD src1_sel:WORD_1\n"
    "v_lshlrev_b32_sdwa v206, 1, v181 dst_sel:DWORD dst_unused:UNUSED_PAD src0_sel:DWORD src1_sel:WORD_1\n"
    "v_lshlrev_b32_sdwa v199, 1, v182 dst_sel:DWORD dst_unused:UNUSED_PAD src0_sel:DWORD src1_sel:WORD_1\n"
    "v_fma_mix_f32 v234, v184, v232, 0 op_sel_hi:[1,1,0]\n"
    "v_mfma_f32_16x16x32_bf16 v[172:175], v[52:55], v[152:155], v[172:175]\n"
    "ds_read_u16_d16_hi v203, v203\n"
    "ds_read_u16 v196, v196\n"
    "v_or_b32_e32 v191, v191, v207\n"
    "v_lshlrev_b32_sdwa v207, 1, v183 dst_sel:DWORD dst_unused:UNUSED_PAD src0_sel:DWORD src1_sel:WORD_1\n"
    "v_fma_mix_f32 v232, v184, v232, 0 op_sel:[1,1,0] op_sel_hi:[1,1,0]\n"
    "v_mfma_f32_16x16x32_bf16 v[176:179], a[112:115], v[156:159], 0\n"
    "s_waitcnt lgkmcnt(8)\n"
    "ds_read_u16_d16_hi v204, v204\n"
    "v_cvt_pk_bf16_f32 v184, v234, v232\n"
    "v_mfma_f32_16x16x32_bf16 v[176:179], a[160:163], v[160:163], v[176:179]\n"
    "v_pk_fma_f16 v232, v188, v188, s48\n"
    "v_or_b32_e32 v192, v192, v200\n"
    "v_mfma_f32_16x16x32_bf16 v[176:179], a[72:75], v[164:167], v[176:179]\n"
    "s_waitcnt lgkmcnt(7)\n"
    "ds_read_u16 v197, v197\n"
    "v_pk_mul_f16 v232, v188, v232\n"
    "v_mfma_f32_16x16x32_bf16 v[180:183], a[116:119], v[156:159], 0\n"
    "ds_read_b128 a[252:255], v249 offset:3072\n"
    "v_exp_f16_e64 v234, v232\n"
    "v_mfma_f32_16x16x32_bf16 v[180:183], a[164:167], v[160:163], v[180:183]\n"
    "ds_read_u16_d16_hi v205, v205\n"
    "v_exp_f16_sdwa v234, v232 dst_sel:WORD_1 dst_unused:UNUSED_PRESERVE src0_sel:WORD_1\n"
    "v_mfma_f32_16x16x32_bf16 v[180:183], v[52:55], v[164:167], v[180:183]\n"
    "s_waitcnt lgkmcnt(8)\n"
    "v_pk_fma_f16 v234, v234, s49, s49\n"
    "v_or_b32_e32 v193, v193, v201\n"
    "v_mfma_f32_16x16x32_bf16 v[96:99], a[32:35], v[184:187], v[96:99]\n"
    "ds_read_u16 v198, v198\n"
    "v_rcp_f16_e64 v232, v234\n"
    "v_mfma_f32_16x16x32_bf16 v[100:103], a[36:39], v[184:187], v[100:103]\n"
    "ds_read_u16_d16_hi v206, v206\n"
    "v_rcp_f16_sdwa v232, v234 dst_sel:WORD_1 dst_unused:UNUSED_PRESERVE src0_sel:WORD_1\n"
    "v_mfma_f32_16x16x32_bf16 v[112:115], a[40:43], v[184:187], v[112:115]\n"
    "s_nop 0\n"
    "v_fma_mix_f32 v234, v188, v232, 0 op_sel_hi:[1,1,0]\n"
    "v_fma_mix_f32 v232, v188, v232, 0 op_sel:[1,1,0] op_sel_hi:[1,1,0]\n"
    "v_mfma_f32_16x16x32_bf16 v[116:119], a[44:47], v[184:187], v[116:119]\n"
    "s_waitcnt lgkmcnt(7)\n"
    "ds_read_u16 v199, v199\n"
    "v_cvt_pk_bf16_f32 v188, v234, v232\n"
    "v_mfma_f32_16x16x32_bf16 v[128:131], v[16:19], v[184:187], v[128:131]\n"
    "ds_read_u16_d16_hi v207, v207\n"
    "v_lshlrev_b32_sdwa v201, 1, v171 dst_sel:DWORD dst_unused:UNUSED_PAD src0_sel:DWORD src1_sel:WORD_1\n"
    "v_mfma_f32_16x16x32_bf16 v[132:135], v[20:23], v[184:187], v[132:135]\n"
    "v_cvt_pk_f16_f32 v184, v168, v169\n"
    "v_lshlrev_b32_sdwa v185, 1, v170 dst_sel:DWORD dst_unused:UNUSED_PAD src0_sel:DWORD src1_sel:WORD_1\n"
    "v_mfma_f32_16x16x32_bf16 v[104:107], a[32:35], v[188:191], v[104:107]\n"
    "ds_read_b128 v[60:63], v244 offset:38912\n"
    "v_lshlrev_b32_sdwa v186, 1, v172 dst_sel:DWORD dst_unused:UNUSED_PAD src0_sel:DWORD src1_sel:WORD_1\n"
    "v_mfma_f32_16x16x32_bf16 v[108:111], a[36:39], v[188:191], v[108:111]\n"
    "s_waitcnt lgkmcnt(8)\n"
    "ds_read_u16 v185, v185\n"
    "v_or_b32_e32 v194, v194, v202\n"
    "v_mfma_f32_16x16x32_bf16 v[120:123], a[40:43], v[188:191], v[120:123]\n"
    "s_waitcnt lgkmcnt(7)\n"
    "buffer_store_dwordx4 a[252:255], v255, s[68:71], s39 offen offset:64 sc1\n"
    "ds_read_u16_d16_hi v201, v201\n"
    "v_mfma_f32_16x16x32_bf16 v[124:127], a[44:47], v[188:191], v[124:127]\n"
    "v_lshlrev_b32_sdwa v202, 1, v173 dst_sel:DWORD dst_unused:UNUSED_PAD src0_sel:DWORD src1_sel:WORD_1\n"
    "v_lshlrev_b32_sdwa v187, 1, v174 dst_sel:DWORD dst_unused:UNUSED_PAD src0_sel:DWORD src1_sel:WORD_1\n"
    "v_mfma_f32_16x16x32_bf16 v[136:139], v[16:19], v[188:191], v[136:139]\n"
    "s_waitcnt lgkmcnt(7)\n"
    "ds_read_b128 a[252:255], v249 offset:5120\n"
    "ds_read_u16 v186, v186\n"
    "v_mfma_f32_16x16x32_bf16 v[140:143], v[20:23], v[188:191], v[140:143]\n"
    "v_cvt_pk_f16_f32 v188, v176, v177\n"
    "v_lshlrev_b32_sdwa v189, 1, v178 dst_sel:DWORD dst_unused:UNUSED_PAD src0_sel:DWORD src1_sel:WORD_1\n"
    "v_mfma_f32_16x16x32_bf16 v[168:171], a[120:123], v[144:147], 0\n"
    "ds_read_u16_d16_hi v202, v202\n"
    "v_lshlrev_b32_sdwa v190, 1, v180 dst_sel:DWORD dst_unused:UNUSED_PAD src0_sel:DWORD src1_sel:WORD_1\n"
    "v_mfma_f32_16x16x32_bf16 v[168:171], a[168:171], v[148:151], v[168:171]\n"
    "v_lshlrev_b32_sdwa v191, 1, v182 dst_sel:DWORD dst_unused:UNUSED_PAD src0_sel:DWORD src1_sel:WORD_1\n"
    "v_or_b32_e32 v195, v195, v203\n"
    "v_mfma_f32_16x16x32_bf16 v[168:171], a[76:79], v[152:155], v[168:171]\n"
    "s_waitcnt lgkmcnt(8)\n"
    "ds_read_b128 v[24:27], v244 offset:12288\n"
    "v_lshlrev_b32_sdwa v203, 1, v175 dst_sel:DWORD dst_unused:UNUSED_PAD src0_sel:DWORD src1_sel:WORD_1\n"
    "v_mfma_f32_16x16x32_bf16 v[172:175], a[124:127], v[144:147], 0\n"
    "ds_read_u16 v187, v187\n"
    "v_or_b32_e64 v196, v196, v204\n"
    "v_mfma_f32_16x16x32_bf16 v[172:175], a[172:175], v[148:151], v[172:175]\n"
    "v_or_b32_e64 v197, v197, v205\n"
    "v_lshlrev_b32_sdwa v205, 1, v179 dst_sel:DWORD dst_unused:UNUSED_PAD src0_sel:DWORD src1_sel:WORD_1\n"
    "v_mfma_f32_16x16x32_bf16 v[176:179], a[120:123], v[156:159], 0\n"
    "s_waitcnt lgkmcnt(7)\n"
    "ds_read_u16_d16_hi v203, v203\n"
    "v_or_b32_e32 v198, v198, v206\n"
    "v_mfma_f32_16x16x32_bf16 v[172:175], v[60:63], v[152:155], v[172:175]\n"
    "v_lshlrev_b32_sdwa v206, 1, v181 dst_sel:DWORD dst_unused:UNUSED_PAD src0_sel:DWORD src1_sel:WORD_1\n"
    "v_or_b32_e64 v199, v199, v207\n"
    "v_mfma_f32_16x16x32_bf16 v[176:179], a[168:171], v[160:163], v[176:179]\n"
    "ds_read_b128 v[28:31], v244 offset:14336\n"
    "ds_read_u16 v189, v189\n"
    "v_mfma_f32_16x16x32_bf16 v[176:179], a[76:79], v[164:167], v[176:179]\n"
    "v_lshlrev_b32_sdwa v207, 1, v183 dst_sel:DWORD dst_unused:UNUSED_PAD src0_sel:DWORD src1_sel:WORD_1\n"
    "v_pk_fma_f16 v232, v184, v184, s48\n"
    "v_mfma_f32_16x16x32_bf16 v[180:183], a[124:127], v[156:159], 0\n"
    "ds_read_u16_d16_hi v205, v205\n"
    "v_pk_mul_f16 v232, v184, v232\n"
    "v_mfma_f32_16x16x32_bf16 v[180:183], a[172:175], v[160:163], v[180:183]\n"
    "s_waitcnt lgkmcnt(8)\n"
    "buffer_store_dwordx4 a[252:255], v255, s[68:71], s39 offen offset:128 sc1\n"
    "ds_read_u16 v190, v190\n"
    "v_mfma_f32_16x16x32_bf16 v[180:183], v[60:63], v[164:167], v[180:183]\n"
    "v_or_b32_e32 v185, v185, v201\n"
    "v_exp_f16_e64 v234, v232\n"
    "v_mfma_f32_16x16x32_bf16 v[96:99], a[176:179], v[192:195], v[96:99]\n"
    "ds_read_b128 v[144:147], v243 offset:0\n"
    "ds_read_u16_d16_hi v206, v206\n"
    "v_mfma_f32_16x16x32_bf16 v[100:103], a[180:183], v[192:195], v[100:103]\n"
    "s_waitcnt lgkmcnt(8)\n"
    "v_or_b32_e32 v186, v186, v202\n"
    "v_exp_f16_sdwa v234, v232 dst_sel:WORD_1 dst_unused:UNUSED_PRESERVE src0_sel:WORD_1\n"
    "v_mfma_f32_16x16x32_bf16 v[112:115], a[184:187], v[192:195], v[112:115]\n"
    "ds_read_b128 v[148:151], v243 offset:1024\n"
    "v_pk_fma_f16 v234, v234, s49, s49\n"
    "v_mfma_f32_16x16x32_bf16 v[116:119], a[188:191], v[192:195], v[116:119]\n"
    "ds_read_u16 v191, v191\n"
    "v_lshlrev_b32_sdwa v201, 1, v171 dst_sel:DWORD dst_unused:UNUSED_PAD src0_sel:DWORD src1_sel:WORD_1\n"
    "v_mfma_f32_16x16x32_bf16 v[128:131], v[24:27], v[192:195], v[128:131]\n"
    "v_lshlrev_b32_sdwa v202, 1, v173 dst_sel:DWORD dst_unused:UNUSED_PAD src0_sel:DWORD src1_sel:WORD_1\n"
    "v_rcp_f16_e32 v232, v234\n"
    "v_mfma_f32_16x16x32_bf16 v[104:107], a[176:179], v[196:199], v[104:107]\n"
    "s_waitcnt lgkmcnt(7)\n"
    "ds_read_b128 v[152:155], v243 offset:2048\n"
    "ds_read_u16_d16_hi v207, v207\n"
    "v_mfma_f32_16x16x32_bf16 v[132:135], v[28:31], v[192:195], v[132:135]\n"
    "v_or_b32_e64 v187, v187, v203\n"
    "v_cvt_pk_f16_f32 v192, v168, v169\n"
    "v_mfma_f32_16x16x32_bf16 v[108:111], a[180:183], v[196:199], v[108:111]\n"
    "ds_read_b128 v[156:159], v243 offset:3072\n"
    "v_lshlrev_b32_sdwa v193, 1, v170 dst_sel:DWORD dst_unused:UNUSED_PAD src0_sel:DWORD src1_sel:WORD_1\n"
    "v_mfma_f32_16x16x32_bf16 v[120:123], a[184:187], v[196:199], v[120:123]\n"
    "ds_read_b128 v[160:163], v243 offset:4096\n"
    "v_lshlrev_b32_sdwa v194, 1, v172 dst_sel:DWORD dst_unused:UNUSED_PAD src0_sel:DWORD src1_sel:WORD_1\n"
    "v_mfma_f32_16x16x32_bf16 v[124:127], a[188:191], v[196:199], v[124:127]\n"
    "ds_read_b128 v[164:167], v243 offset:5120\n"
    "v_lshlrev_b32_sdwa v195, 1, v174 dst_sel:DWORD dst_unused:UNUSED_PAD src0_sel:DWORD src1_sel:WORD_1\n"
    "v_mfma_f32_16x16x32_bf16 v[136:139], v[24:27], v[196:199], v[136:139]\n"
    "s_waitcnt lgkmcnt(10)\n"
    "ds_read_b128 v[48:51], v244 offset:32768\n"
    "v_or_b32_e64 v189, v189, v205\n"
    "v_mfma_f32_16x16x32_bf16 v[140:143], v[28:31], v[196:199], v[140:143]\n"
    "s_waitcnt lgkmcnt(9)\n"
    "ds_read_u16 v193, v193\n"
    "v_lshlrev_b32_sdwa v203, 1, v175 dst_sel:DWORD dst_unused:UNUSED_PAD src0_sel:DWORD src1_sel:WORD_1\n"
    "v_mfma_f32_16x16x32_bf16 v[168:171], a[80:83], v[144:147], 0\n"
    "v_rcp_f16_sdwa v232, v234 dst_sel:WORD_1 dst_unused:UNUSED_PRESERVE src0_sel:WORD_1\n"
    "v_mfma_f32_16x16x32_bf16 v[172:175], a[84:87], v[144:147], 0\n"
    "ds_read_u16_d16_hi v201, v201\n"
    "v_cvt_pk_f16_f32 v196, v176, v177\n"
    "s_waitcnt lgkmcnt(9)\n"
    "v_mfma_f32_16x16x32_bf16 v[168:171], a[128:131], v[148:151], v[168:171]\n"
    "v_or_b32_e32 v190, v190, v206\n"
    "v_lshlrev_b32_sdwa v197, 1, v178 dst_sel:DWORD dst_unused:UNUSED_PAD src0_sel:DWORD src1_sel:WORD_1\n"
    "v_lshlrev_b32_sdwa v205, 1, v179 dst_sel:DWORD dst_unused:UNUSED_PAD src0_sel:DWORD src1_sel:WORD_1\n"
    "v_lshlrev_b32_sdwa v198, 1, v180 dst_sel:DWORD dst_unused:UNUSED_PAD src0_sel:DWORD src1_sel:WORD_1\n"
    "v_lshlrev_b32_sdwa v206, 1, v181 dst_sel:DWORD dst_unused:UNUSED_PAD src0_sel:DWORD src1_sel:WORD_1\n"
    "v_lshlrev_b32_sdwa v199, 1, v182 dst_sel:DWORD dst_unused:UNUSED_PAD src0_sel:DWORD src1_sel:WORD_1\n"
    "v_fma_mix_f32 v234, v184, v232, 0 op_sel_hi:[1,1,0]\n"
    "v_mfma_f32_16x16x32_bf16 v[172:175], a[132:135], v[148:151], v[172:175]\n"
    "s_waitcnt lgkmcnt(7)\n"
    "ds_read_u16 v194, v194\n"
    "ds_read_u16_d16_hi v202, v202\n"
    "v_mfma_f32_16x16x32_bf16 v[168:171], a[48:51], v[152:155], v[168:171]\n"
    "s_nop 0\n"
    "v_fma_mix_f32 v232, v184, v232, 0 op_sel:[1,1,0] op_sel_hi:[1,1,0]\n"
    "v_cvt_pk_bf16_f32 v184, v234, v232\n"
    "v_pk_fma_f16 v232, v188, v188, s48\n"
    "v_pk_mul_f16 v232, v188, v232\n"
    "v_exp_f16_e32 v234, v232\n"
    "s_waitcnt lgkmcnt(7)\n"
    "v_mfma_f32_16x16x32_bf16 v[176:179], a[80:83], v[156:159], 0\n"
    "ds_read_b128 v[32:35], v244 offset:16384\n"
    "ds_read_u16 v195, v195\n"
    "s_waitcnt lgkmcnt(8)\n"
    "v_mfma_f32_16x16x32_bf16 v[176:179], a[128:131], v[160:163], v[176:179]\n"
    "s_waitcnt lgkmcnt(7)\n"
    "ds_read_u16_d16_hi v203, v203\n"
    "v_or_b32_e32 v191, v191, v207\n"
    "v_mfma_f32_16x16x32_bf16 v[176:179], a[48:51], v[164:167], v[176:179]\n"
    "s_waitcnt lgkmcnt(7)\n"
    "v_lshlrev_b32_sdwa v207, 1, v183 dst_sel:DWORD dst_unused:UNUSED_PAD src0_sel:DWORD src1_sel:WORD_1\n"
    "v_exp_f16_sdwa v234, v232 dst_sel:WORD_1 dst_unused:UNUSED_PRESERVE src0_sel:WORD_1\n"
    "v_mfma_f32_16x16x32_bf16 v[172:175], v[48:51], v[152:155], v[172:175]\n"
    "s_barrier\n"
    "v_mfma_f32_16x16x32_bf16 v[180:183], a[84:87], v[156:159], 0\n"
    "s_waitcnt lgkmcnt(5)\n"
    "ds_read_b128 v[36:39], v244 offset:18432\n"
    "ds_write_b128 v253, v[64:67] offset:2048\n"
    "v_mfma_f32_16x16x32_bf16 v[180:183], a[132:135], v[160:163], v[180:183]\n"
    "v_pk_fma_f16 v234, v234, s49, s49\n"
    "v_or_b32_e64 v193, v193, v201\n"
    "v_mfma_f32_16x16x32_bf16 v[180:183], v[48:51], v[164:167], v[180:183]\n"
    "v_rcp_f16_e32 v232, v234\n"
    "v_mfma_f32_16x16x32_bf16 v[96:99], a[192:195], v[184:187], v[96:99]\n"
    "s_waitcnt lgkmcnt(4)\n"
    "ds_read_u16 v197, v197\n"
    "ds_write_b128 v253, v[68:71] offset:5120\n"
    "v_mfma_f32_16x16x32_bf16 v[100:103], a[196:199], v[184:187], v[100:103]\n"
    "v_rcp_f16_sdwa v232, v234 dst_sel:WORD_1 dst_unused:UNUSED_PRESERVE src0_sel:WORD_1\n"
    "v_mfma_f32_16x16x32_bf16 v[112:115], a[200:203], v[184:187], v[112:115]\n"
    "v_fma_mix_f32 v234, v188, v232, 0 op_sel_hi:[1,1,0]\n"
    "v_fma_mix_f32 v232, v188, v232, 0 op_sel:[1,1,0] op_sel_hi:[1,1,0]\n"
    "v_mfma_f32_16x16x32_bf16 v[116:119], a[204:207], v[184:187], v[116:119]\n"
    "s_waitcnt lgkmcnt(3)\n"
    "ds_read_u16_d16_hi v205, v205\n"
    "ds_write_b128 v253, v[88:91] offset:0\n"
    "v_mfma_f32_16x16x32_bf16 v[128:131], v[32:35], v[184:187], v[128:131]\n"
    "v_or_b32_e32 v194, v194, v202\n"
    "v_cvt_pk_bf16_f32 v188, v234, v232\n"
    "v_mfma_f32_16x16x32_bf16 v[132:135], v[36:39], v[184:187], v[132:135]\n"
    "v_pk_fma_f16 v232, v192, v192, s48\n"
    "v_or_b32_e64 v195, v195, v203\n"
    "v_mfma_f32_16x16x32_bf16 v[104:107], a[192:195], v[188:191], v[104:107]\n"
    "ds_read_u16 v198, v198\n"
    "ds_write_b128 v253, v[92:95] offset:3072\n"
    "v_mfma_f32_16x16x32_bf16 v[108:111], a[196:199], v[188:191], v[108:111]\n"
    "v_pk_mul_f16 v232, v192, v232\n"
    "v_lshlrev_b32_sdwa v184, 1, v168 dst_sel:DWORD dst_unused:UNUSED_PAD src0_sel:DWORD src1_sel:WORD_1\n"
    "v_mfma_f32_16x16x32_bf16 v[120:123], a[200:203], v[188:191], v[120:123]\n"
    "v_exp_f16_e64 v234, v232\n"
    "v_mfma_f32_16x16x32_bf16 v[124:127], a[204:207], v[188:191], v[124:127]\n"
    "ds_read_u16_d16_hi v206, v206\n"
    "ds_read_u16 v199, v199\n"
    "v_mfma_f32_16x16x32_bf16 v[136:139], v[32:35], v[188:191], v[136:139]\n"
    "ds_read_u16_d16_hi v207, v207\n"
    "ds_read_u16 v184, v184\n"
    "v_mfma_f32_16x16x32_bf16 v[140:143], v[36:39], v[188:191], v[140:143]\n"
    "s_waitcnt lgkmcnt(6)\n"
    "v_or_b32_e32 v197, v197, v205\n"
    "v_exp_f16_sdwa v234, v232 dst_sel:WORD_1 dst_unused:UNUSED_PRESERVE src0_sel:WORD_1\n"
    "v_lshlrev_b32_sdwa v200, 1, v169 dst_sel:DWORD dst_unused:UNUSED_PAD src0_sel:DWORD src1_sel:WORD_1\n"
    "v_pk_fma_f16 v234, v234, s49, s49\n"
    "v_lshlrev_b32_sdwa v185, 1, v170 dst_sel:DWORD dst_unused:UNUSED_PAD src0_sel:DWORD src1_sel:WORD_1\n"
    "v_lshlrev_b32_sdwa v201, 1, v171 dst_sel:DWORD dst_unused:UNUSED_PAD src0_sel:DWORD src1_sel:WORD_1\n"
    "v_mfma_f32_16x16x32_bf16 v[168:171], a[88:91], v[144:147], 0\n"
    "v_lshlrev_b32_sdwa v186, 1, v172 dst_sel:DWORD dst_unused:UNUSED_PAD src0_sel:DWORD src1_sel:WORD_1\n"
    "v_lshlrev_b32_sdwa v202, 1, v173 dst_sel:DWORD dst_unused:UNUSED_PAD src0_sel:DWORD src1_sel:WORD_1\n"
    "v_mfma_f32_16x16x32_bf16 v[168:171], a[136:139], v[148:151], v[168:171]\n"
    "ds_write_b128 v253, v[72:75] offset:1024\n"
    "v_lshlrev_b32_sdwa v187, 1, v174 dst_sel:DWORD dst_unused:UNUSED_PAD src0_sel:DWORD src1_sel:WORD_1\n"
    "v_lshlrev_b32_sdwa v203, 1, v175 dst_sel:DWORD dst_unused:UNUSED_PAD src0_sel:DWORD src1_sel:WORD_1\n"
    "v_rcp_f16_e64 v232, v234\n"
    "v_mfma_f32_16x16x32_bf16 v[168:171], a[52:55], v[152:155], v[168:171]\n"
    "v_rcp_f16_sdwa v232, v234 dst_sel:WORD_1 dst_unused:UNUSED_PRESERVE src0_sel:WORD_1\n"
    "v_mfma_f32_16x16x32_bf16 v[172:175], a[92:95], v[144:147], 0\n"
    "ds_read_u16_d16_hi v200, v200\n"
    "ds_write_b128 v253, v[76:79] offset:4096\n"
    "s_waitcnt lgkmcnt(6)\n"
    "v_or_b32_e32 v198, v198, v206\n"
    "v_lshlrev_b32_sdwa v188, 1, v176 dst_sel:DWORD dst_unused:UNUSED_PAD src0_sel:DWORD src1_sel:WORD_1\n"
    "v_lshlrev_b32_sdwa v204, 1, v177 dst_sel:DWORD dst_unused:UNUSED_PAD src0_sel:DWORD src1_sel:WORD_1\n"
    "v_lshlrev_b32_sdwa v189, 1, v178 dst_sel:DWORD dst_unused:UNUSED_PAD src0_sel:DWORD src1_sel:WORD_1\n"
    "v_lshlrev_b32_sdwa v205, 1, v179 dst_sel:DWORD dst_unused:UNUSED_PAD src0_sel:DWORD src1_sel:WORD_1\n"
    "v_lshlrev_b32_sdwa v190, 1, v180 dst_sel:DWORD dst_unused:UNUSED_PAD src0_sel:DWORD src1_sel:WORD_1\n"
    "v_lshlrev_b32_sdwa v206, 1, v181 dst_sel:DWORD dst_unused:UNUSED_PAD src0_sel:DWORD src1_sel:WORD_1\n"
    "v_lshlrev_b32_sdwa v191, 1, v182 dst_sel:DWORD dst_unused:UNUSED_PAD src0_sel:DWORD src1_sel:WORD_1\n"
    "v_fma_mix_f32 v234, v192, v232, 0 op_sel_hi:[1,1,0]\n"
    "v_mfma_f32_16x16x32_bf16 v[172:175], a[140:143], v[148:151], v[172:175]\n"
    "s_waitcnt lgkmcnt(4)\n"
    "ds_read_u16 v185, v185\n"
    "ds_read_u16_d16_hi v201, v201\n"
    "v_mfma_f32_16x16x32_bf16 v[172:175], v[56:59], v[152:155], v[172:175]\n"
    "s_nop 0\n"
    "ds_read_u16 v186, v186\n"
    "ds_read_b128 v[40:43], v244 offset:20480\n"
    "v_or_b32_e64 v199, v199, v207\n"
    "v_lshlrev_b32_sdwa v207, 1, v183 dst_sel:DWORD dst_unused:UNUSED_PAD src0_sel:DWORD src1_sel:WORD_1\n"
    "v_fma_mix_f32 v232, v192, v232, 0 op_sel:[1,1,0] op_sel_hi:[1,1,0]\n"
    "v_mfma_f32_16x16x32_bf16 v[176:179], a[88:91], v[156:159], 0\n"
    "ds_read_u16_d16_hi v202, v202\n"
    "v_cvt_pk_bf16_f32 v192, v234, v232\n"
    "v_mfma_f32_16x16x32_bf16 v[176:179], a[136:139], v[160:163], v[176:179]\n"
    "v_pk_fma_f16 v232, v196, v196, s48\n"
    "v_pk_mul_f16 v232, v196, v232\n"
    "v_mfma_f32_16x16x32_bf16 v[176:179], a[52:55], v[164:167], v[176:179]\n"
    "ds_read_u16 v187, v187\n"
    "v_exp_f16_e32 v234, v232\n"
    "v_mfma_f32_16x16x32_bf16 v[180:183], a[92:95], v[156:159], 0\n"
    "s_waitcnt lgkmcnt(6)\n"
    "ds_read_b128 v[44:47], v244 offset:22528\n"
    "v_exp_f16_sdwa v234, v232 dst_sel:WORD_1 dst_unused:UNUSED_PRESERVE src0_sel:WORD_1\n"
    "v_mfma_f32_16x16x32_bf16 v[180:183], a[140:143], v[160:163], v[180:183]\n"
    "ds_read_u16_d16_hi v203, v203\n"
    "v_pk_fma_f16 v234, v234, s49, s49\n"
    "v_mfma_f32_16x16x32_bf16 v[180:183], v[56:59], v[164:167], v[180:183]\n"
    "v_rcp_f16_e64 v232, v234\n"
    "v_mfma_f32_16x16x32_bf16 v[96:99], a[208:211], v[192:195], v[96:99]\n"
    "ds_read_u16 v188, v188\n"
    "v_rcp_f16_sdwa v232, v234 dst_sel:WORD_1 dst_unused:UNUSED_PRESERVE src0_sel:WORD_1\n"
    "v_mfma_f32_16x16x32_bf16 v[100:103], a[212:215], v[192:195], v[100:103]\n"
    "ds_read_u16_d16_hi v204, v204\n"
    "v_fma_mix_f32 v234, v196, v232, 0 op_sel_hi:[1,1,0]\n"
    "v_mfma_f32_16x16x32_bf16 v[112:115], a[216:219], v[192:195], v[112:115]\n"
    "v_fma_mix_f32 v232, v196, v232, 0 op_sel:[1,1,0] op_sel_hi:[1,1,0]\n"
    "v_or_b32_e32 v184, v184, v200\n"
    "v_mfma_f32_16x16x32_bf16 v[116:119], a[220:223], v[192:195], v[116:119]\n"
    "s_waitcnt lgkmcnt(8)\n"
    "ds_read_u16 v189, v189\n"
    "v_cvt_pk_bf16_f32 v196, v234, v232\n"
    "v_or_b32_e32 v185, v185, v201\n"
    "s_waitcnt lgkmcnt(7)\n"
    "v_mfma_f32_16x16x32_bf16 v[128:131], v[40:43], v[192:195], v[128:131]\n"
    "ds_read_u16_d16_hi v205, v205\n"
    "v_lshlrev_b32_sdwa v201, 1, v171 dst_sel:DWORD dst_unused:UNUSED_PAD src0_sel:DWORD src1_sel:WORD_1\n"
    "v_mfma_f32_16x16x32_bf16 v[104:107], a[208:211], v[196:199], v[104:107]\n"
    "v_cvt_pk_bf16_f32 v96, v96, v97\n"
    "v_cvt_pk_bf16_f32 v97, v98, v99\n"
    "v_mfma_f32_16x16x32_bf16 v[108:111], a[212:215], v[196:199], v[108:111]\n"
    "ds_read_u16 v190, v190\n"
    "v_cvt_pk_bf16_f32 v98, v100, v101\n"
    "v_mfma_f32_16x16x32_bf16 v[120:123], a[216:219], v[196:199], v[120:123]\n"
    "s_waitcnt lgkmcnt(8)\n"
    "v_or_b32_e64 v186, v186, v202\n"
    "v_lshlrev_b32_sdwa v202, 1, v173 dst_sel:DWORD dst_unused:UNUSED_PAD src0_sel:DWORD src1_sel:WORD_1\n"
    "v_mfma_f32_16x16x32_bf16 v[124:127], a[220:223], v[196:199], v[124:127]\n"
    "s_waitcnt lgkmcnt(6)\n"
    "ds_read_u16_d16_hi v206, v206\n"
    "v_cvt_pk_bf16_f32 v99, v102, v103\n"
    "v_mfma_f32_16x16x32_bf16 v[132:135], v[44:47], v[192:195], v[132:135]\n"
    "v_cvt_pk_f16_f32 v192, v168, v169\n"
    "v_lshlrev_b32_sdwa v193, 1, v170 dst_sel:DWORD dst_unused:UNUSED_PAD src0_sel:DWORD src1_sel:WORD_1\n"
    "v_mfma_f32_16x16x32_bf16 v[136:139], v[40:43], v[196:199], v[136:139]\n"
    "v_lshlrev_b32_sdwa v194, 1, v172 dst_sel:DWORD dst_unused:UNUSED_PAD src0_sel:DWORD src1_sel:WORD_1\n"
    "v_lshlrev_b32_sdwa v195, 1, v174 dst_sel:DWORD dst_unused:UNUSED_PAD src0_sel:DWORD src1_sel:WORD_1\n"
    "v_mfma_f32_16x16x32_bf16 v[140:143], v[44:47], v[196:199], v[140:143]\n"
    "s_waitcnt lgkmcnt(6)\n"
    "ds_write_b128 v242, v[96:99] offset:24640\n"
    "v_mfma_f32_16x16x32_bf16 v[168:171], a[96:99], v[144:147], 0\n"
    "s_nop 0\n"
    "v_cvt_pk_f16_f32 v196, v176, v177\n"
    "v_lshlrev_b32_sdwa v197, 1, v178 dst_sel:DWORD dst_unused:UNUSED_PAD src0_sel:DWORD src1_sel:WORD_1\n"
    "v_mfma_f32_16x16x32_bf16 v[168:171], a[144:147], v[148:151], v[168:171]\n"
    "v_lshlrev_b32_sdwa v198, 1, v180 dst_sel:DWORD dst_unused:UNUSED_PAD src0_sel:DWORD src1_sel:WORD_1\n"
    "v_lshlrev_b32_sdwa v199, 1, v182 dst_sel:DWORD dst_unused:UNUSED_PAD src0_sel:DWORD src1_sel:WORD_1\n"
    "v_mfma_f32_16x16x32_bf16 v[168:171], a[56:59], v[152:155], v[168:171]\n"
    "s_waitcnt lgkmcnt(5)\n"
    "ds_read_u16 v191, v191\n"
    "ds_read_u16_d16_hi v207, v207\n"
    "v_or_b32_e32 v187, v187, v203\n"
    "v_lshlrev_b32_sdwa v203, 1, v175 dst_sel:DWORD dst_unused:UNUSED_PAD src0_sel:DWORD src1_sel:WORD_1\n"
    "v_mfma_f32_16x16x32_bf16 v[172:175], a[100:103], v[144:147], 0\n"
    "ds_read_u16 v193, v193\n"
    "ds_read_u16_d16_hi v201, v201\n"
    "v_mfma_f32_16x16x32_bf16 v[172:175], a[148:151], v[148:151], v[172:175]\n"
    "v_or_b32_e64 v188, v188, v204\n"
    "v_cvt_pk_bf16_f32 v112, v112, v113\n"
    "v_mfma_f32_16x16x32_bf16 v[172:175], a[60:63], v[152:155], v[172:175]\n"
    "s_waitcnt lgkmcnt(7)\n"
    "ds_read_u16 v194, v194\n"
    "v_or_b32_e32 v189, v189, v205\n"
    "v_lshlrev_b32_sdwa v205, 1, v179 dst_sel:DWORD dst_unused:UNUSED_PAD src0_sel:DWORD src1_sel:WORD_1\n"
    "v_mfma_f32_16x16x32_bf16 v[176:179], a[96:99], v[156:159], 0\n"
    "v_cvt_pk_bf16_f32 v113, v114, v115\n"
    "v_cvt_pk_bf16_f32 v114, v116, v117\n"
    "v_mfma_f32_16x16x32_bf16 v[176:179], a[144:147], v[160:163], v[176:179]\n"
    "ds_read_u16_d16_hi v202, v202\n"
    "v_cvt_pk_bf16_f32 v115, v118, v119\n"
    "v_mfma_f32_16x16x32_bf16 v[176:179], a[56:59], v[164:167], v[176:179]\n"
    "s_waitcnt lgkmcnt(6)\n"
    "v_or_b32_e32 v190, v190, v206\n"
    "v_lshlrev_b32_sdwa v206, 1, v181 dst_sel:DWORD dst_unused:UNUSED_PAD src0_sel:DWORD src1_sel:WORD_1\n"
    "v_mfma_f32_16x16x32_bf16 v[96:99], a[0:3], v[184:187], 0\n"
    "v_cvt_pk_bf16_f32 v128, v128, v129\n"
    "v_cvt_pk_bf16_f32 v129, v130, v131\n"
    "v_mfma_f32_16x16x32_bf16 v[100:103], a[4:7], v[184:187], 0\n"
    "ds_write_b128 v242, v[112:115] offset:26688\n"
    "v_mfma_f32_16x16x32_bf16 v[112:115], a[8:11], v[184:187], 0\n"
    "v_cvt_pk_bf16_f32 v130, v132, v133\n"
    "v_cvt_pk_bf16_f32 v131, v134, v135\n"
    "v_cvt_pk_bf16_f32 v104, v104, v105\n"
    "v_cvt_pk_bf16_f32 v105, v106, v107\n"
    "v_cvt_pk_bf16_f32 v106, v108, v109\n"
    "v_cvt_pk_bf16_f32 v107, v110, v111\n"
    "ds_write_b128 v242, v[128:131] offset:28736\n"
    "s_waitcnt lgkmcnt(5)\n"
    "v_or_b32_e32 v191, v191, v207\n"
    "v_mfma_f32_16x16x32_bf16 v[116:119], a[12:15], v[184:187], 0\n"
    "v_lshlrev_b32_sdwa v207, 1, v183 dst_sel:DWORD dst_unused:UNUSED_PAD src0_sel:DWORD src1_sel:WORD_1\n"
    "v_cvt_pk_bf16_f32 v120, v120, v121\n"
    "v_mfma_f32_16x16x32_bf16 v[180:183], a[100:103], v[156:159], 0\n"
    "s_waitcnt lgkmcnt(4)\n"
    "ds_read_u16 v195, v195\n"
    "ds_write_b128 v242, v[104:107] offset:25664\n"
    "v_mfma_f32_16x16x32_bf16 v[180:183], a[148:151], v[160:163], v[180:183]\n"
    "v_or_b32_e32 v193, v193, v201\n"
    "v_cvt_pk_bf16_f32 v121, v122, v123\n"
    "v_mfma_f32_16x16x32_bf16 v[180:183], a[60:63], v[164:167], v[180:183]\n"
    "v_cvt_pk_bf16_f32 v122, v124, v125\n"
    "v_cvt_pk_bf16_f32 v123, v126, v127\n"
    "v_mfma_f32_16x16x32_bf16 v[128:131], v[0:3], v[184:187], 0\n"
    "s_waitcnt lgkmcnt(3)\n"
    "ds_read_u16_d16_hi v203, v203\n"
    "ds_write_b128 v242, v[120:123] offset:27712\n"
    "v_mfma_f32_16x16x32_bf16 v[132:135], v[4:7], v[184:187], 0\n"
    "v_or_b32_e32 v194, v194, v202\n"
    "v_cvt_pk_bf16_f32 v136, v136, v137\n"
    "v_mfma_f32_16x16x32_bf16 v[104:107], a[0:3], v[188:191], 0\n"
    "v_pk_fma_f16 v232, v192, v192, s48\n"
    "v_cvt_pk_bf16_f32 v137, v138, v139\n"
    "v_mfma_f32_16x16x32_bf16 v[108:111], a[4:7], v[188:191], 0\n"
    "ds_read_u16 v197, v197\n"
    "ds_read_u16_d16_hi v205, v205\n"
    "v_mfma_f32_16x16x32_bf16 v[120:123], a[8:11], v[188:191], 0\n"
    "ds_read_u16 v198, v198\n"
    "ds_read_u16_d16_hi v206, v206\n"
    "v_mfma_f32_16x16x32_bf16 v[124:127], a[12:15], v[188:191], 0\n"
    "v_cvt_pk_bf16_f32 v138, v140, v141\n"
    "v_cvt_pk_bf16_f32 v139, v142, v143\n"
    "v_pk_mul_f16 v232, v192, v232\n"
    "v_cvt_pk_f16_f32 v184, v168, v169\n"
    "v_exp_f16_e64 v234, v232\n"
    "v_mfma_f32_16x16x32_bf16 v[140:143], v[4:7], v[188:191], 0\n"
    "v_exp_f16_sdwa v234, v232 dst_sel:WORD_1 dst_unused:UNUSED_PRESERVE src0_sel:WORD_1\n"
    "ds_write_b128 v242, v[136:139] offset:29760\n"
    "v_mfma_f32_16x16x32_bf16 v[136:139], v[0:3], v[188:191], 0\n"
    "s_waitcnt lgkmcnt(5)\n"
    "v_or_b32_e32 v195, v195, v203\n"
    "v_pk_fma_f16 v234, v234, s49, s49\n"
    "v_lshlrev_b32_sdwa v185, 1, v170 dst_sel:DWORD dst_unused:UNUSED_PAD src0_sel:DWORD src1_sel:WORD_1\n"
    "v_lshlrev_b32_sdwa v201, 1, v171 dst_sel:DWORD dst_unused:UNUSED_PAD src0_sel:DWORD src1_sel:WORD_1\n"
    "v_mfma_f32_16x16x32_bf16 v[168:171], a[104:107], v[144:147], 0\n"
    "ds_read_u16 v199, v199\n"
    "ds_read_u16_d16_hi v207, v207\n"
    "v_mfma_f32_16x16x32_bf16 v[168:171], a[152:155], v[148:151], v[168:171]\n"
    "ds_read_u16 v185, v185\n"
    "v_lshlrev_b32_sdwa v186, 1, v172 dst_sel:DWORD dst_unused:UNUSED_PAD src0_sel:DWORD src1_sel:WORD_1\n"
    "v_lshlrev_b32_sdwa v202, 1, v173 dst_sel:DWORD dst_unused:UNUSED_PAD src0_sel:DWORD src1_sel:WORD_1\n"
    "v_lshlrev_b32_sdwa v187, 1, v174 dst_sel:DWORD dst_unused:UNUSED_PAD src0_sel:DWORD src1_sel:WORD_1\n"
    "v_lshlrev_b32_sdwa v203, 1, v175 dst_sel:DWORD dst_unused:UNUSED_PAD src0_sel:DWORD src1_sel:WORD_1\n"
    "v_rcp_f16_e64 v232, v234\n"
    "v_mfma_f32_16x16x32_bf16 v[168:171], a[64:67], v[152:155], v[168:171]\n"
    "ds_read_u16_d16_hi v201, v201\n"
    "s_waitcnt lgkmcnt(7)\n"
    "v_or_b32_e32 v197, v197, v205\n"
    "v_rcp_f16_sdwa v232, v234 dst_sel:WORD_1 dst_unused:UNUSED_PRESERVE src0_sel:WORD_1\n"
    "v_mfma_f32_16x16x32_bf16 v[172:175], a[108:111], v[144:147], 0\n"
    "ds_read_u16 v186, v186\n"
    "v_cvt_pk_f16_f32 v188, v176, v177\n"
    "v_mfma_f32_16x16x32_bf16 v[172:175], a[156:159], v[148:151], v[172:175]\n"
    "ds_read_u16_d16_hi v202, v202\n"
    "v_lshlrev_b32_sdwa v189, 1, v178 dst_sel:DWORD dst_unused:UNUSED_PAD src0_sel:DWORD src1_sel:WORD_1\n"
    "v_lshlrev_b32_sdwa v205, 1, v179 dst_sel:DWORD dst_unused:UNUSED_PAD src0_sel:DWORD src1_sel:WORD_1\n"
    "v_lshlrev_b32_sdwa v190, 1, v180 dst_sel:DWORD dst_unused:UNUSED_PAD src0_sel:DWORD src1_sel:WORD_1\n"
    "s_waitcnt lgkmcnt(6)\n"
    "v_or_b32_e32 v198, v198, v206\n"
    "v_lshlrev_b32_sdwa v206, 1, v181 dst_sel:DWORD dst_unused:UNUSED_PAD src0_sel:DWORD src1_sel:WORD_1\n"
    "v_lshlrev_b32_sdwa v191, 1, v182 dst_sel:DWORD dst_unused:UNUSED_PAD src0_sel:DWORD src1_sel:WORD_1\n"
    "v_fma_mix_f32 v234, v192, v232, 0 op_sel_hi:[1,1,0]\n"
    "v_mfma_f32_16x16x32_bf16 v[172:175], a[68:71], v[152:155], v[172:175]\n"
    "s_barrier\n"
    "v_fma_mix_f32 v232, v192, v232, 0 op_sel:[1,1,0] op_sel_hi:[1,1,0]\n"
    "v_mfma_f32_16x16x32_bf16 v[176:179], a[104:107], v[156:159], 0\n"
    "s_waitcnt lgkmcnt(4)\n"
    "ds_read_u16 v187, v187\n"
    "ds_read_u16_d16_hi v203, v203\n"
    "v_or_b32_e64 v199, v199, v207\n"
    "v_lshlrev_b32_sdwa v207, 1, v183 dst_sel:DWORD dst_unused:UNUSED_PAD src0_sel:DWORD src1_sel:WORD_1\n"
    "v_cvt_pk_bf16_f32 v192, v234, v232\n"
    "v_mfma_f32_16x16x32_bf16 v[176:179], a[152:155], v[160:163], v[176:179]\n"
    "ds_read_u16 v189, v189\n"
    "ds_read_u16_d16_hi v205, v205\n"
    "v_mfma_f32_16x16x32_bf16 v[176:179], a[64:67], v[164:167], v[176:179]\n"
    "s_nop 0\n"
    "v_pk_fma_f16 v232, v196, v196, s48\n"
    "v_lshlrev_b32_sdwa v200, 1, v169 dst_sel:DWORD dst_unused:UNUSED_PAD src0_sel:DWORD src1_sel:WORD_1\n"
    "v_mfma_f32_16x16x32_bf16 v[180:183], a[108:111], v[156:159], 0\n"
    "s_waitcnt lgkmcnt(6)\n"
    "ds_read_u16 v190, v190\n"
    "v_pk_mul_f16 v232, v196, v232\n"
    "v_mfma_f32_16x16x32_bf16 v[180:183], a[156:159], v[160:163], v[180:183]\n"
    "ds_read_u16_d16_hi v206, v206\n"
    "v_exp_f16_e32 v234, v232\n"
    "v_mfma_f32_16x16x32_bf16 v[180:183], a[68:71], v[164:167], v[180:183]\n"
    "v_exp_f16_sdwa v234, v232 dst_sel:WORD_1 dst_unused:UNUSED_PRESERVE src0_sel:WORD_1\n"
    "v_mfma_f32_16x16x32_bf16 v[96:99], a[16:19], v[192:195], v[96:99]\n"
    "s_waitcnt lgkmcnt(6)\n"
    "ds_read_u16 v191, v191\n"
    "v_pk_fma_f16 v234, v234, s49, s49\n"
    "v_mfma_f32_16x16x32_bf16 v[100:103], a[20:23], v[192:195], v[100:103]\n"
    "v_rcp_f16_e64 v232, v234\n"
    "v_mfma_f32_16x16x32_bf16 v[112:115], a[24:27], v[192:195], v[112:115]\n"
    "ds_read_u16_d16_hi v207, v207\n"
    "v_rcp_f16_sdwa v232, v234 dst_sel:WORD_1 dst_unused:UNUSED_PRESERVE src0_sel:WORD_1\n"
    "v_mfma_f32_16x16x32_bf16 v[116:119], a[28:31], v[192:195], v[116:119]\n"
    "v_or_b32_e64 v185, v185, v201\n"
    "v_fma_mix_f32 v234, v196, v232, 0 op_sel_hi:[1,1,0]\n"
    "v_mfma_f32_16x16x32_bf16 v[128:131], v[8:11], v[192:195], v[128:131]\n"
    "v_fma_mix_f32 v232, v196, v232, 0 op_sel:[1,1,0] op_sel_hi:[1,1,0]\n"
    "v_or_b32_e32 v186, v186, v202\n"
    "v_mfma_f32_16x16x32_bf16 v[132:135], v[12:15], v[192:195], v[132:135]\n"
    "s_waitcnt lgkmcnt(6)\n"
    "v_cvt_pk_bf16_f32 v196, v234, v232\n"
    "v_pk_fma_f16 v232, v184, v184, s48\n"
    "v_or_b32_e64 v187, v187, v203\n"
    "v_mfma_f32_16x16x32_bf16 v[104:107], a[16:19], v[196:199], v[104:107]\n"
    "ds_read_u16_d16_hi v200, v200\n"
    "v_pk_mul_f16 v232, v184, v232\n"
    "v_mfma_f32_16x16x32_bf16 v[108:111], a[20:23], v[196:199], v[108:111]\n"
    "v_exp_f16_e32 v234, v232\n"
    "v_mfma_f32_16x16x32_bf16 v[120:123], a[24:27], v[196:199], v[120:123]\n"
    "s_waitcnt lgkmcnt(5)\n"
    "v_or_b32_e32 v189, v189, v205\n"
    "v_exp_f16_sdwa v234, v232 dst_sel:WORD_1 dst_unused:UNUSED_PRESERVE src0_sel:WORD_1\n"
    "v_mfma_f32_16x16x32_bf16 v[124:127], a[28:31], v[196:199], v[124:127]\n"
    "s_waitcnt vmcnt(7)\n"
    "v_pk_fma_f16 v234, v234, s49, s49\n"
    "v_lshlrev_b32_sdwa v192, 1, v168 dst_sel:DWORD dst_unused:UNUSED_PAD src0_sel:DWORD src1_sel:WORD_1\n"
    "v_mfma_f32_16x16x32_bf16 v[136:139], v[8:11], v[196:199], v[136:139]\n"
    "s_nop 0\n"
    "ds_read_u16 v192, v192\n"
    "v_lshlrev_b32_sdwa v193, 1, v170 dst_sel:DWORD dst_unused:UNUSED_PAD src0_sel:DWORD src1_sel:WORD_1\n"
    "v_mfma_f32_16x16x32_bf16 v[140:143], v[12:15], v[196:199], v[140:143]\n"
    "s_waitcnt vmcnt(6) lgkmcnt(4)\n"
    "ds_read_u16 v193, v193\n"
    "v_lshlrev_b32_sdwa v201, 1, v171 dst_sel:DWORD dst_unused:UNUSED_PAD src0_sel:DWORD src1_sel:WORD_1\n"
    "v_smfmac_f32_32x32x32_bf16 v[112:127], a[248:251], a[232:239], v241\n"
    "ds_read_u16_d16_hi v201, v201\n"
    "ds_read_b128 a[232:235], v243 offset:24640\n"
    "v_lshlrev_b32_sdwa v194, 1, v172 dst_sel:DWORD dst_unused:UNUSED_PAD src0_sel:DWORD src1_sel:WORD_1\n"
    "v_lshlrev_b32_sdwa v202, 1, v173 dst_sel:DWORD dst_unused:UNUSED_PAD src0_sel:DWORD src1_sel:WORD_1\n"
    "v_lshlrev_b32_sdwa v195, 1, v174 dst_sel:DWORD dst_unused:UNUSED_PAD src0_sel:DWORD src1_sel:WORD_1\n"
    "v_smfmac_f32_32x32x32_bf16 v[96:111], a[248:251], a[224:231], v241\n"
    "ds_read_u16 v194, v194\n"
    "ds_read_u16_d16_hi v202, v202\n"
    "v_lshlrev_b32_sdwa v203, 1, v175 dst_sel:DWORD dst_unused:UNUSED_PAD src0_sel:DWORD src1_sel:WORD_1\n"
    "v_rcp_f16_e32 v232, v234\n"
    "v_smfmac_f32_32x32x32_bf16 v[128:143], a[248:251], v[208:215], v241\n"
    "s_waitcnt lgkmcnt(6)\n"
    "ds_read_b128 a[236:239], v243 offset:25664\n"
    "v_or_b32_e64 v190, v190, v206\n"
    "v_rcp_f16_sdwa v232, v234 dst_sel:WORD_1 dst_unused:UNUSED_PRESERVE src0_sel:WORD_1\n"
    "v_lshlrev_b32_sdwa v196, 1, v176 dst_sel:DWORD dst_unused:UNUSED_PAD src0_sel:DWORD src1_sel:WORD_1\n"
    "v_mfma_f32_16x16x32_bf16 v[168:171], a[112:115], v[144:147], 0\n"
    "ds_read_u16 v195, v195\n"
    "v_lshlrev_b32_sdwa v204, 1, v177 dst_sel:DWORD dst_unused:UNUSED_PAD src0_sel:DWORD src1_sel:WORD_1\n"
    "v_mfma_f32_16x16x32_bf16 v[168:171], a[160:163], v[148:151], v[168:171]\n"
    "ds_read_u16_d16_hi v203, v203\n"
    "v_lshlrev_b32_sdwa v197, 1, v178 dst_sel:DWORD dst_unused:UNUSED_PAD src0_sel:DWORD src1_sel:WORD_1\n"
    "v_mfma_f32_16x16x32_bf16 v[168:171], a[72:75], v[152:155], v[168:171]\n"
    "v_lshlrev_b32_sdwa v205, 1, v179 dst_sel:DWORD dst_unused:UNUSED_PAD src0_sel:DWORD src1_sel:WORD_1\n"
    "v_lshlrev_b32_sdwa v198, 1, v180 dst_sel:DWORD dst_unused:UNUSED_PAD src0_sel:DWORD src1_sel:WORD_1\n"
    "v_mfma_f32_16x16x32_bf16 v[172:175], a[116:119], v[144:147], 0\n"
    "v_lshlrev_b32_sdwa v206, 1, v181 dst_sel:DWORD dst_unused:UNUSED_PAD src0_sel:DWORD src1_sel:WORD_1\n"
    "v_lshlrev_b32_sdwa v199, 1, v182 dst_sel:DWORD dst_unused:UNUSED_PAD src0_sel:DWORD src1_sel:WORD_1\n"
    "v_fma_mix_f32 v234, v184, v232, 0 op_sel_hi:[1,1,0]\n"
    "v_mfma_f32_16x16x32_bf16 v[172:175], a[164:167], v[148:151], v[172:175]\n"
    "s_waitcnt lgkmcnt(7)\n"
    "ds_read_u16 v196, v196\n"
    "ds_read_u16_d16_hi v204, v204\n"
    "v_mfma_f32_16x16x32_bf16 v[172:175], v[52:55], v[152:155], v[172:175]\n"
    "v_or_b32_e32 v191, v191, v207\n"
    "v_lshlrev_b32_sdwa v207, 1, v183 dst_sel:DWORD dst_unused:UNUSED_PAD src0_sel:DWORD src1_sel:WORD_1\n"
    "v_fma_mix_f32 v232, v184, v232, 0 op_sel:[1,1,0] op_sel_hi:[1,1,0]\n"
    "v_mfma_f32_16x16x32_bf16 v[176:179], a[112:115], v[156:159], 0\n"
    "ds_read_u16 v197, v197\n"
    "v_cvt_pk_bf16_f32 v184, v234, v232\n"
    "v_mfma_f32_16x16x32_bf16 v[176:179], a[160:163], v[160:163], v[176:179]\n"
    "ds_read_u16_d16_hi v205, v205\n"
    "v_pk_fma_f16 v232, v188, v188, s48\n"
    "v_mfma_f32_16x16x32_bf16 v[176:179], a[72:75], v[164:167], v[176:179]\n"
    "s_waitcnt lgkmcnt(8)\n"
    "v_pk_mul_f16 v232, v188, v232\n"
    "v_or_b32_e32 v192, v192, v200\n"
    "v_mfma_f32_16x16x32_bf16 v[180:183], a[116:119], v[156:159], 0\n"
    "ds_read_u16 v198, v198\n"
    "v_exp_f16_e32 v234, v232\n"
    "v_mfma_f32_16x16x32_bf16 v[180:183], a[164:167], v[160:163], v[180:183]\n"
    "s_waitcnt lgkmcnt(7)\n"
    "v_exp_f16_sdwa v234, v232 dst_sel:WORD_1 dst_unused:UNUSED_PRESERVE src0_sel:WORD_1\n"
    "v_smfmac_f32_32x32x32_bf16 v[96:111], a[248:251], a[232:239], v241\n"
    "ds_read_u16_d16_hi v206, v206\n"
    "v_pk_fma_f16 v234, v234, s49, s49\n"
    "v_or_b32_e64 v193, v193, v201\n"
    "v_rcp_f16_e32 v232, v234\n"
    "v_mfma_f32_16x16x32_bf16 v[180:183], v[52:55], v[164:167], v[180:183]\n"
    "ds_read_u16 v199, v199\n"
    "v_rcp_f16_sdwa v232, v234 dst_sel:WORD_1 dst_unused:UNUSED_PRESERVE src0_sel:WORD_1\n"
    "v_mfma_f32_16x16x32_bf16 v[112:115], a[40:43], v[184:187], v[112:115]\n"
    "s_waitcnt lgkmcnt(7)\n"
    "ds_read_u16_d16_hi v207, v207\n"
    "v_fma_mix_f32 v234, v188, v232, 0 op_sel_hi:[1,1,0]\n"
    "v_mfma_f32_16x16x32_bf16 v[96:99], a[32:35], v[184:187], v[96:99]\n"
    "v_fma_mix_f32 v232, v188, v232, 0 op_sel:[1,1,0] op_sel_hi:[1,1,0]\n"
    "v_lshlrev_b32_sdwa v201, 1, v171 dst_sel:DWORD dst_unused:UNUSED_PAD src0_sel:DWORD src1_sel:WORD_1\n"
    "v_mfma_f32_16x16x32_bf16 v[100:103], a[36:39], v[184:187], v[100:103]\n"
    "ds_read_b128 a[232:235], v243 offset:26688\n"
    "ds_read_b128 a[236:239], v243 offset:27712\n"
    "v_mfma_f32_16x16x32_bf16 v[116:119], a[44:47], v[184:187], v[116:119]\n"
    "s_waitcnt lgkmcnt(8)\n"
    "v_cvt_pk_bf16_f32 v188, v234, v232\n"
    "v_or_b32_e32 v194, v194, v202\n"
    "v_mfma_f32_16x16x32_bf16 v[128:131], v[16:19], v[184:187], v[128:131]\n"
    "ds_read_u16_d16_hi v201, v201\n"
    "v_lshlrev_b32_sdwa v202, 1, v173 dst_sel:DWORD dst_unused:UNUSED_PAD src0_sel:DWORD src1_sel:WORD_1\n"
    "v_mfma_f32_16x16x32_bf16 v[132:135], v[20:23], v[184:187], v[132:135]\n"
    "ds_read_u16_d16_hi v202, v202\n"
    "v_cvt_pk_f16_f32 v184, v168, v169\n"
    "v_mfma_f32_16x16x32_bf16 v[104:107], a[32:35], v[188:191], v[104:107]\n"
    "v_lshlrev_b32_sdwa v185, 1, v170 dst_sel:DWORD dst_unused:UNUSED_PAD src0_sel:DWORD src1_sel:WORD_1\n"
    "v_lshlrev_b32_sdwa v186, 1, v172 dst_sel:DWORD dst_unused:UNUSED_PAD src0_sel:DWORD src1_sel:WORD_1\n"
    "v_mfma_f32_16x16x32_bf16 v[108:111], a[36:39], v[188:191], v[108:111]\n"
    "s_waitcnt lgkmcnt(8)\n"
    "ds_read_u16 v185, v185\n"
    "v_lshlrev_b32_sdwa v187, 1, v174 dst_sel:DWORD dst_unused:UNUSED_PAD src0_sel:DWORD src1_sel:WORD_1\n"
    "v_mfma_f32_16x16x32_bf16 v[120:123], a[40:43], v[188:191], v[120:123]\n"
    "v_or_b32_e32 v195, v195, v203\n"
    "v_lshlrev_b32_sdwa v203, 1, v175 dst_sel:DWORD dst_unused:UNUSED_PAD src0_sel:DWORD src1_sel:WORD_1\n"
    "v_mfma_f32_16x16x32_bf16 v[124:127], a[44:47], v[188:191], v[124:127]\n"
    "ds_read_u16 v186, v186\n"
    "v_or_b32_e64 v196, v196, v204\n"
    "v_mfma_f32_16x16x32_bf16 v[136:139], v[16:19], v[188:191], v[136:139]\n"
    "s_waitcnt lgkmcnt(8)\n"
    "v_or_b32_e32 v197, v197, v205\n"
    "v_lshlrev_b32_sdwa v205, 1, v179 dst_sel:DWORD dst_unused:UNUSED_PAD src0_sel:DWORD src1_sel:WORD_1\n"
    "v_mfma_f32_16x16x32_bf16 v[140:143], v[20:23], v[188:191], v[140:143]\n"
    "ds_read_u16 v187, v187\n"
    "v_cvt_pk_f16_f32 v188, v176, v177\n"
    "v_mfma_f32_16x16x32_bf16 v[168:171], a[120:123], v[144:147], 0\n"
    "ds_read_u16_d16_hi v203, v203\n"
    "v_lshlrev_b32_sdwa v189, 1, v178 dst_sel:DWORD dst_unused:UNUSED_PAD src0_sel:DWORD src1_sel:WORD_1\n"
    "v_mfma_f32_16x16x32_bf16 v[168:171], a[168:171], v[148:151], v[168:171]\n"
    "v_lshlrev_b32_sdwa v190, 1, v180 dst_sel:DWORD dst_unused:UNUSED_PAD src0_sel:DWORD src1_sel:WORD_1\n"
    "v_lshlrev_b32_sdwa v191, 1, v182 dst_sel:DWORD dst_unused:UNUSED_PAD src0_sel:DWORD src1_sel:WORD_1\n"
    "v_mfma_f32_16x16x32_bf16 v[168:171], a[76:79], v[152:155], v[168:171]\n"
    "s_waitcnt lgkmcnt(7)\n"
    "ds_read_u16 v189, v189\n"
    "v_or_b32_e32 v198, v198, v206\n"
    "v_mfma_f32_16x16x32_bf16 v[172:175], a[124:127], v[144:147], 0\n"
    "s_waitcnt lgkmcnt(7)\n"
    "v_lshlrev_b32_sdwa v206, 1, v181 dst_sel:DWORD dst_unused:UNUSED_PAD src0_sel:DWORD src1_sel:WORD_1\n"
    "v_or_b32_e32 v199, v199, v207\n"
    "v_smfmac_f32_32x32x32_bf16 v[112:127], a[248:251], a[232:239], v241\n"
    "ds_read_b128 a[232:235], v243 offset:28736\n"
    "ds_read_u16_d16_hi v205, v205\n"
    "v_lshlrev_b32_sdwa v207, 1, v183 dst_sel:DWORD dst_unused:UNUSED_PAD src0_sel:DWORD src1_sel:WORD_1\n"
    "v_pk_fma_f16 v232, v184, v184, s48\n"
    "v_pk_mul_f16 v232, v184, v232\n"
    "v_mfma_f32_16x16x32_bf16 v[172:175], a[172:175], v[148:151], v[172:175]\n"
    "v_exp_f16_e64 v234, v232\n"
    "v_mfma_f32_16x16x32_bf16 v[172:175], v[60:63], v[152:155], v[172:175]\n"
    "ds_read_b128 a[236:239], v243 offset:29760\n"
    "ds_read_u16 v190, v190\n"
    "v_mfma_f32_16x16x32_bf16 v[176:179], a[120:123], v[156:159], 0\n"
    "s_nop 0\n"
    "v_exp_f16_sdwa v234, v232 dst_sel:WORD_1 dst_unused:UNUSED_PRESERVE src0_sel:WORD_1\n"
    "v_mfma_f32_16x16x32_bf16 v[176:179], a[168:171], v[160:163], v[176:179]\n"
    "s_waitcnt lgkmcnt(8)\n"
    "ds_read_b128 v[144:147], v242 offset:0\n"
    "v_or_b32_e64 v185, v185, v201\n"
    "v_mfma_f32_16x16x32_bf16 v[176:179], a[76:79], v[164:167], v[176:179]\n"
    "ds_read_u16_d16_hi v206, v206\n"
    "v_pk_fma_f16 v234, v234, s49, s49\n"
    "v_mfma_f32_16x16x32_bf16 v[180:183], a[124:127], v[156:159], 0\n"
    "s_waitcnt lgkmcnt(9)\n"
    "v_or_b32_e32 v186, v186, v202\n"
    "v_lshlrev_b32_sdwa v201, 1, v171 dst_sel:DWORD dst_unused:UNUSED_PAD src0_sel:DWORD src1_sel:WORD_1\n"
    "v_mfma_f32_16x16x32_bf16 v[180:183], a[172:175], v[160:163], v[180:183]\n"
    "ds_read_b128 v[148:151], v242 offset:1024\n"
    "v_lshlrev_b32_sdwa v202, 1, v173 dst_sel:DWORD dst_unused:UNUSED_PAD src0_sel:DWORD src1_sel:WORD_1\n"
    "v_mfma_f32_16x16x32_bf16 v[180:183], v[60:63], v[164:167], v[180:183]\n"
    "ds_read_u16 v191, v191\n"
    "v_rcp_f16_e64 v232, v234\n"
    "v_mfma_f32_16x16x32_bf16 v[96:99], a[176:179], v[192:195], v[96:99]\n"
    "s_waitcnt lgkmcnt(9)\n"
    "v_or_b32_e32 v187, v187, v203\n"
    "v_lshlrev_b32_sdwa v203, 1, v175 dst_sel:DWORD dst_unused:UNUSED_PAD src0_sel:DWORD src1_sel:WORD_1\n"
    "v_mfma_f32_16x16x32_bf16 v[100:103], a[180:183], v[192:195], v[100:103]\n"
    "ds_read_b128 v[152:155], v242 offset:2048\n"
    "ds_read_u16_d16_hi v207, v207\n"
    "v_mfma_f32_16x16x32_bf16 v[112:115], a[184:187], v[192:195], v[112:115]\n"
    "v_rcp_f16_sdwa v232, v234 dst_sel:WORD_1 dst_unused:UNUSED_PRESERVE src0_sel:WORD_1\n"
    "v_mfma_f32_16x16x32_bf16 v[116:119], a[188:191], v[192:195], v[116:119]\n"
    "ds_read_b128 v[156:159], v242 offset:3072\n"
    "v_fma_mix_f32 v234, v184, v232, 0 op_sel_hi:[1,1,0]\n"
    "v_mfma_f32_16x16x32_bf16 v[128:131], v[24:27], v[192:195], v[128:131]\n"
    "ds_read_b128 v[160:163], v242 offset:4096\n"
    "v_fma_mix_f32 v232, v184, v232, 0 op_sel:[1,1,0] op_sel_hi:[1,1,0]\n"
    "v_mfma_f32_16x16x32_bf16 v[132:135], v[28:31], v[192:195], v[132:135]\n"
    "s_waitcnt lgkmcnt(9)\n"
    "ds_read_b128 v[164:167], v242 offset:5120\n"
    "v_or_b32_e32 v189, v189, v205\n"
    "v_mfma_f32_16x16x32_bf16 v[104:107], a[176:179], v[196:199], v[104:107]\n"
    "ds_read_u16_d16_hi v201, v201\n"
    "v_cvt_pk_f16_f32 v192, v168, v169\n"
    "v_mfma_f32_16x16x32_bf16 v[108:111], a[180:183], v[196:199], v[108:111]\n"
    "s_nop 0\n"
    "v_lshlrev_b32_sdwa v193, 1, v170 dst_sel:DWORD dst_unused:UNUSED_PAD src0_sel:DWORD src1_sel:WORD_1\n"
    "v_lshlrev_b32_sdwa v194, 1, v172 dst_sel:DWORD dst_unused:UNUSED_PAD src0_sel:DWORD src1_sel:WORD_1\n"
    "v_mfma_f32_16x16x32_bf16 v[120:123], a[184:187], v[196:199], v[120:123]\n"
    "s_waitcnt lgkmcnt(9)\n"
    "ds_read_u16 v193, v193\n"
    "v_lshlrev_b32_sdwa v195, 1, v174 dst_sel:DWORD dst_unused:UNUSED_PAD src0_sel:DWORD src1_sel:WORD_1\n"
    "v_mfma_f32_16x16x32_bf16 v[124:127], a[188:191], v[196:199], v[124:127]\n"
    "v_lshlrev_b32_sdwa v205, 1, v179 dst_sel:DWORD dst_unused:UNUSED_PAD src0_sel:DWORD src1_sel:WORD_1\n"
    "v_cvt_pk_bf16_f32 v184, v234, v232\n"
    "v_mfma_f32_16x16x32_bf16 v[136:139], v[24:27], v[196:199], v[136:139]\n"
    "s_waitcnt lgkmcnt(8)\n"
    "ds_read_u16 v194, v194\n"
    "v_or_b32_e32 v190, v190, v206\n"
    "v_mfma_f32_16x16x32_bf16 v[140:143], v[28:31], v[196:199], v[140:143]\n"
    "v_cvt_pk_f16_f32 v196, v176, v177\n"
    "v_lshlrev_b32_sdwa v197, 1, v178 dst_sel:DWORD dst_unused:UNUSED_PAD src0_sel:DWORD src1_sel:WORD_1\n"
    "v_mfma_f32_16x16x32_bf16 v[168:171], a[80:83], v[144:147], 0\n"
    "s_nop 0\n"
    "ds_read_u16_d16_hi v202, v202\n"
    "v_lshlrev_b32_sdwa v198, 1, v180 dst_sel:DWORD dst_unused:UNUSED_PAD src0_sel:DWORD src1_sel:WORD_1\n"
    "v_mfma_f32_16x16x32_bf16 v[168:171], a[128:131], v[148:151], v[168:171]\n"
    "s_waitcnt lgkmcnt(8)\n"
    "ds_read_u16 v195, v195\n"
    "v_lshlrev_b32_sdwa v206, 1, v181 dst_sel:DWORD dst_unused:UNUSED_PAD src0_sel:DWORD src1_sel:WORD_1\n"
    "v_smfmac_f32_32x32x32_bf16 v[128:143], a[248:251], a[232:239], v241\n"
    "v_lshlrev_b32_sdwa v199, 1, v182 dst_sel:DWORD dst_unused:UNUSED_PAD src0_sel:DWORD src1_sel:WORD_1\n"
    "v_pk_fma_f16 v232, v188, v188, s48\n"
    "v_pk_mul_f16 v232, v188, v232\n"
    "v_exp_f16_e32 v234, v232\n"
    "v_mfma_f32_16x16x32_bf16 v[168:171], a[48:51], v[152:155], v[168:171]\n"
    "s_waitcnt lgkmcnt(6)\n"
    "ds_read_u16_d16_hi v203, v203\n"
    "v_or_b32_e64 v191, v191, v207\n"
    "v_mfma_f32_16x16x32_bf16 v[172:175], a[84:87], v[144:147], 0\n"
    "ds_read_u16 v197, v197\n"
    "v_lshlrev_b32_sdwa v207, 1, v183 dst_sel:DWORD dst_unused:UNUSED_PAD src0_sel:DWORD src1_sel:WORD_1\n"
    "v_mfma_f32_16x16x32_bf16 v[172:175], a[132:135], v[148:151], v[172:175]\n"
    "v_exp_f16_sdwa v234, v232 dst_sel:WORD_1 dst_unused:UNUSED_PRESERVE src0_sel:WORD_1\n"
    "v_mfma_f32_16x16x32_bf16 v[172:175], v[48:51], v[152:155], v[172:175]\n"
    "ds_read_u16_d16_hi v205, v205\n"
    "v_pk_fma_f16 v234, v234, s49, s49\n"
    "v_mfma_f32_16x16x32_bf16 v[176:179], a[80:83], v[156:159], 0\n"
    "ds_read_u16 v198, v198\n"
    "v_rcp_f16_e64 v232, v234\n"
    "v_mfma_f32_16x16x32_bf16 v[176:179], a[128:131], v[160:163], v[176:179]\n"
    "s_waitcnt lgkmcnt(8)\n"
    "v_rcp_f16_sdwa v232, v234 dst_sel:WORD_1 dst_unused:UNUSED_PRESERVE src0_sel:WORD_1\n"
    "v_mfma_f32_16x16x32_bf16 v[176:179], a[48:51], v[164:167], v[176:179]\n"
    "s_waitcnt lgkmcnt(7)\n"
    "ds_read_u16_d16_hi v206, v206\n"
    "v_or_b32_e64 v193, v193, v201\n"
    "v_mfma_f32_16x16x32_bf16 v[180:183], a[84:87], v[156:159], 0\n"
    "v_fma_mix_f32 v234, v188, v232, 0 op_sel_hi:[1,1,0]\n"
    "v_fma_mix_f32 v232, v188, v232, 0 op_sel:[1,1,0] op_sel_hi:[1,1,0]\n"
    "v_mfma_f32_16x16x32_bf16 v[180:183], a[132:135], v[160:163], v[180:183]\n"
    "ds_read_u16 v199, v199\n"
    "v_cvt_pk_bf16_f32 v188, v234, v232\n"
    "v_mfma_f32_16x16x32_bf16 v[180:183], v[48:51], v[164:167], v[180:183]\n"
    "s_waitcnt lgkmcnt(7)\n"
    "ds_read_u16_d16_hi v207, v207\n"
    "v_or_b32_e32 v194, v194, v202\n"
    "v_mfma_f32_16x16x32_bf16 v[96:99], a[192:195], v[184:187], v[96:99]\n"
    "v_pk_fma_f16 v232, v192, v192, s48\n"
    "v_lshlrev_b32_sdwa v200, 1, v169 dst_sel:DWORD dst_unused:UNUSED_PAD src0_sel:DWORD src1_sel:WORD_1\n"
    "v_mfma_f32_16x16x32_bf16 v[100:103], a[196:199], v[184:187], v[100:103]\n"
    "ds_read_u16_d16_hi v200, v200\n"
    "v_pk_mul_f16 v232, v192, v232\n"
    "v_mfma_f32_16x16x32_bf16 v[112:115], a[200:203], v[184:187], v[112:115]\n"
    "v_exp_f16_e64 v234, v232\n"
    "v_mfma_f32_16x16x32_bf16 v[116:119], a[204:207], v[184:187], v[116:119]\n"
    "s_waitcnt lgkmcnt(7)\n"
    "v_or_b32_e32 v195, v195, v203\n"
    "v_exp_f16_sdwa v234, v232 dst_sel:WORD_1 dst_unused:UNUSED_PRESERVE src0_sel:WORD_1\n"
    "v_mfma_f32_16x16x32_bf16 v[128:131], v[32:35], v[184:187], v[128:131]\n"
    "v_pk_fma_f16 v234, v234, s49, s49\n"
    "v_lshlrev_b32_sdwa v201, 1, v171 dst_sel:DWORD dst_unused:UNUSED_PAD src0_sel:DWORD src1_sel:WORD_1\n"
    "v_mfma_f32_16x16x32_bf16 v[132:135], v[36:39], v[184:187], v[132:135]\n"
    "ds_read_u16_d16_hi v201, v201\n"
    "v_lshlrev_b32_sdwa v184, 1, v168 dst_sel:DWORD dst_unused:UNUSED_PAD src0_sel:DWORD src1_sel:WORD_1\n"
    "v_mfma_f32_16x16x32_bf16 v[104:107], a[192:195], v[188:191], v[104:107]\n"
    "s_waitcnt lgkmcnt(6)\n"
    "ds_read_u16 v184, v184\n"
    "v_or_b32_e32 v197, v197, v205\n"
    "v_mfma_f32_16x16x32_bf16 v[108:111], a[196:199], v[188:191], v[108:111]\n"
    "v_lshlrev_b32_sdwa v185, 1, v170 dst_sel:DWORD dst_unused:UNUSED_PAD src0_sel:DWORD src1_sel:WORD_1\n"
    "v_lshlrev_b32_sdwa v186, 1, v172 dst_sel:DWORD dst_unused:UNUSED_PAD src0_sel:DWORD src1_sel:WORD_1\n"
    "v_mfma_f32_16x16x32_bf16 v[120:123], a[200:203], v[188:191], v[120:123]\n"
    "ds_read_u16 v185, v185\n"
    "ds_read_u16 v186, v186\n"
    "v_mfma_f32_16x16x32_bf16 v[124:127], a[204:207], v[188:191], v[124:127]\n"
    "v_lshlrev_b32_sdwa v202, 1, v173 dst_sel:DWORD dst_unused:UNUSED_PAD src0_sel:DWORD src1_sel:WORD_1\n"
    "v_lshlrev_b32_sdwa v187, 1, v174 dst_sel:DWORD dst_unused:UNUSED_PAD src0_sel:DWORD src1_sel:WORD_1\n"
    "v_mfma_f32_16x16x32_bf16 v[136:139], v[32:35], v[188:191], v[136:139]\n"
    "s_waitcnt lgkmcnt(7)\n"
    "ds_read_u16_d16_hi v202, v202\n"
    "v_lshlrev_b32_sdwa v203, 1, v175 dst_sel:DWORD dst_unused:UNUSED_PAD src0_sel:DWORD src1_sel:WORD_1\n"
    "v_mfma_f32_16x16x32_bf16 v[140:143], v[36:39], v[188:191], v[140:143]\n"
    "v_rcp_f16_e32 v232, v234\n"
    "v_mfma_f32_16x16x32_bf16 v[168:171], a[88:91], v[144:147], 0\n"
    "ds_read_u16 v187, v187\n"
    "v_or_b32_e32 v198, v198, v206\n"
    "v_mfma_f32_16x16x32_bf16 v[168:171], a[136:139], v[148:151], v[168:171]\n"
    "s_waitcnt lgkmcnt(7)\n"
    "v_rcp_f16_sdwa v232, v234 dst_sel:WORD_1 dst_unused:UNUSED_PRESERVE src0_sel:WORD_1\n"
    "v_mfma_f32_16x16x32_bf16 v[168:171], a[52:55], v[152:155], v[168:171]\n"
    "ds_read_u16_d16_hi v203, v203\n"
    "v_lshlrev_b32_sdwa v188, 1, v176 dst_sel:DWORD dst_unused:UNUSED_PAD src0_sel:DWORD src1_sel:WORD_1\n"
    "v_mfma_f32_16x16x32_bf16 v[172:175], a[92:95], v[144:147], 0\n"
    "v_lshlrev_b32_sdwa v204, 1, v177 dst_sel:DWORD dst_unused:UNUSED_PAD src0_sel:DWORD src1_sel:WORD_1\n"
    "v_lshlrev_b32_sdwa v189, 1, v178 dst_sel:DWORD dst_unused:UNUSED_PAD src0_sel:DWORD src1_sel:WORD_1\n"
    "v_lshlrev_b32_sdwa v205, 1, v179 dst_sel:DWORD dst_unused:UNUSED_PAD src0_sel:DWORD src1_sel:WORD_1\n"
    "v_lshlrev_b32_sdwa v190, 1, v180 dst_sel:DWORD dst_unused:UNUSED_PAD src0_sel:DWORD src1_sel:WORD_1\n"
    "v_lshlrev_b32_sdwa v206, 1, v181 dst_sel:DWORD dst_unused:UNUSED_PAD src0_sel:DWORD src1_sel:WORD_1\n"
    "v_lshlrev_b32_sdwa v191, 1, v182 dst_sel:DWORD dst_unused:UNUSED_PAD src0_sel:DWORD src1_sel:WORD_1\n"
    "v_fma_mix_f32 v234, v192, v232, 0 op_sel_hi:[1,1,0]\n"
    "v_mfma_f32_16x16x32_bf16 v[172:175], a[140:143], v[148:151], v[172:175]\n"
    "ds_read_u16 v188, v188\n"
    "ds_read_u16_d16_hi v204, v204\n"
    "v_mfma_f32_16x16x32_bf16 v[172:175], v[56:59], v[152:155], v[172:175]\n"
    "s_waitcnt lgkmcnt(7)\n"
    "v_or_b32_e32 v199, v199, v207\n"
    "v_lshlrev_b32_sdwa v207, 1, v183 dst_sel:DWORD dst_unused:UNUSED_PAD src0_sel:DWORD src1_sel:WORD_1\n"
    "v_fma_mix_f32 v232, v192, v232, 0 op_sel:[1,1,0] op_sel_hi:[1,1,0]\n"
    "v_mfma_f32_16x16x32_bf16 v[176:179], a[88:91], v[156:159], 0\n"
    "ds_read_u16 v189, v189\n"
    "v_cvt_pk_bf16_f32 v192, v234, v232\n"
    "v_mfma_f32_16x16x32_bf16 v[176:179], a[136:139], v[160:163], v[176:179]\n"
    "ds_read_u16_d16_hi v205, v205\n"
    "v_pk_fma_f16 v232, v196, v196, s48\n"
    "v_mfma_f32_16x16x32_bf16 v[176:179], a[52:55], v[164:167], v[176:179]\n"
    "s_waitcnt lgkmcnt(7)\n"
    "ds_read_u16 v190, v190\n"
    "v_pk_mul_f16 v232, v196, v232\n"
    "v_mfma_f32_16x16x32_bf16 v[180:183], a[92:95], v[156:159], 0\n"
    "v_exp_f16_e64 v234, v232\n"
    "v_mfma_f32_16x16x32_bf16 v[180:183], a[140:143], v[160:163], v[180:183]\n"
    "s_waitcnt lgkmcnt(7)\n"
    "ds_read_u16_d16_hi v206, v206\n"
    "buffer_load_dwordx4 a[232:235], v245, s[40:43], s58 offen offset:64\n"
    "buffer_load_dwordx4 a[224:227], v245, s[40:43], s58 offen offset:0\n"
    "buffer_load_dwordx4 v[208:211], v245, s[40:43], s58 offen offset:128\n"
    "v_mfma_f32_16x16x32_bf16 v[180:183], v[56:59], v[164:167], v[180:183]\n"
    "ds_read_u16 v191, v191\n"
    "v_exp_f16_sdwa v234, v232 dst_sel:WORD_1 dst_unused:UNUSED_PRESERVE src0_sel:WORD_1\n"
    "v_or_b32_e64 v184, v184, v200\n"
    "v_pk_fma_f16 v234, v234, s49, s49\n"
    "v_mfma_f32_16x16x32_bf16 v[96:99], a[208:211], v[192:195], v[96:99]\n"
    "s_waitcnt lgkmcnt(7)\n"
    "ds_read_u16_d16_hi v207, v207\n"
    "v_rcp_f16_e32 v232, v234\n"
    "v_mfma_f32_16x16x32_bf16 v[100:103], a[212:215], v[192:195], v[100:103]\n"
    "v_rcp_f16_sdwa v232, v234 dst_sel:WORD_1 dst_unused:UNUSED_PRESERVE src0_sel:WORD_1\n"
    "v_mfma_f32_16x16x32_bf16 v[112:115], a[216:219], v[192:195], v[112:115]\n"
    "v_fma_mix_f32 v234, v196, v232, 0 op_sel_hi:[1,1,0]\n"
    "v_fma_mix_f32 v232, v196, v232, 0 op_sel:[1,1,0] op_sel_hi:[1,1,0]\n"
    "v_mfma_f32_16x16x32_bf16 v[116:119], a[220:223], v[192:195], v[116:119]\n"
    "v_cvt_pk_bf16_f32 v196, v234, v232\n"
    "v_or_b32_e32 v185, v185, v201\n"
    "v_mfma_f32_16x16x32_bf16 v[128:131], v[40:43], v[192:195], v[128:131]\n"
    "s_waitcnt lgkmcnt(6)\n"
    "v_lshlrev_b32_sdwa v201, 1, v171 dst_sel:DWORD dst_unused:UNUSED_PAD src0_sel:DWORD src1_sel:WORD_1\n"
    "v_or_b32_e64 v186, v186, v202\n"
    "v_mfma_f32_16x16x32_bf16 v[132:135], v[44:47], v[192:195], v[132:135]\n"
    "ds_read_u16_d16_hi v201, v201\n"
    "v_cvt_pk_f16_f32 v192, v168, v169\n"
    "v_mfma_f32_16x16x32_bf16 v[104:107], a[208:211], v[196:199], v[104:107]\n"
    "s_nop 0\n"
    "v_lshlrev_b32_sdwa v193, 1, v170 dst_sel:DWORD dst_unused:UNUSED_PAD src0_sel:DWORD src1_sel:WORD_1\n"
    "v_lshlrev_b32_sdwa v194, 1, v172 dst_sel:DWORD dst_unused:UNUSED_PAD src0_sel:DWORD src1_sel:WORD_1\n"
    "v_mfma_f32_16x16x32_bf16 v[108:111], a[212:215], v[196:199], v[108:111]\n"
    "s_waitcnt lgkmcnt(5)\n"
    "ds_read_u16 v193, v193\n"
    "buffer_load_dwordx4 a[236:239], v245, s[40:43], s89 offen offset:64\n"
    "buffer_load_dwordx4 a[228:231], v245, s[40:43], s89 offen offset:0\n"
    "buffer_load_dwordx4 v[212:215], v245, s[40:43], s89 offen offset:128\n"
    "v_mfma_f32_16x16x32_bf16 v[120:123], a[216:219], v[196:199], v[120:123]\n"
    "ds_read_u16 v194, v194\n"
    "v_lshlrev_b32_sdwa v202, 1, v173 dst_sel:DWORD dst_unused:UNUSED_PAD src0_sel:DWORD src1_sel:WORD_1\n"
    "v_mfma_f32_16x16x32_bf16 v[124:127], a[220:223], v[196:199], v[124:127]\n"
    "ds_read_u16_d16_hi v202, v202\n"
    "v_lshlrev_b32_sdwa v195, 1, v174 dst_sel:DWORD dst_unused:UNUSED_PAD src0_sel:DWORD src1_sel:WORD_1\n"
    "v_mfma_f32_16x16x32_bf16 v[136:139], v[40:43], v[196:199], v[136:139]\n"
    "s_waitcnt lgkmcnt(6)\n"
    "ds_read_u16 v195, v195\n"
    "v_or_b32_e32 v187, v187, v203\n"
    "v_mfma_f32_16x16x32_bf16 v[140:143], v[44:47], v[196:199], v[140:143]\n"
    "v_lshlrev_b32_sdwa v203, 1, v175 dst_sel:DWORD dst_unused:UNUSED_PAD src0_sel:DWORD src1_sel:WORD_1\n"
    "v_cvt_pk_f16_f32 v196, v176, v177\n"
    "v_mfma_f32_16x16x32_bf16 v[168:171], a[96:99], v[144:147], 0\n"
    "ds_read_u16_d16_hi v203, v203\n"
    "v_lshlrev_b32_sdwa v197, 1, v178 dst_sel:DWORD dst_unused:UNUSED_PAD src0_sel:DWORD src1_sel:WORD_1\n"
    "v_mfma_f32_16x16x32_bf16 v[168:171], a[144:147], v[148:151], v[168:171]\n"
    "ds_read_u16 v197, v197\n"
    "v_lshlrev_b32_sdwa v198, 1, v180 dst_sel:DWORD dst_unused:UNUSED_PAD src0_sel:DWORD src1_sel:WORD_1\n"
    "v_mfma_f32_16x16x32_bf16 v[168:171], a[56:59], v[152:155], v[168:171]\n"
    "s_waitcnt lgkmcnt(7)\n"
    "v_lshlrev_b32_sdwa v199, 1, v182 dst_sel:DWORD dst_unused:UNUSED_PAD src0_sel:DWORD src1_sel:WORD_1\n"
    "v_or_b32_e32 v188, v188, v204\n"
    "v_mfma_f32_16x16x32_bf16 v[172:175], a[100:103], v[144:147], 0\n"
    "ds_read_u16 v198, v198\n"
    "v_or_b32_e32 v189, v189, v205\n"
    "v_mfma_f32_16x16x32_bf16 v[172:175], a[148:151], v[148:151], v[172:175]\n"
    "ds_read_u16 v199, v199\n"
    "v_lshlrev_b32_sdwa v205, 1, v179 dst_sel:DWORD dst_unused:UNUSED_PAD src0_sel:DWORD src1_sel:WORD_1\n"
    "v_mfma_f32_16x16x32_bf16 v[172:175], a[60:63], v[152:155], v[172:175]\n"
    "s_waitcnt lgkmcnt(7)\n"
    "ds_read_u16_d16_hi v205, v205\n"
    "v_cvt_pk_bf16_f32 v96, v96, v97\n"
    "v_mfma_f32_16x16x32_bf16 v[176:179], a[96:99], v[156:159], 0\n"
    "v_cvt_pk_bf16_f32 v97, v98, v99\n"
    "v_cvt_pk_bf16_f32 v98, v100, v101\n"
    "v_mfma_f32_16x16x32_bf16 v[176:179], a[144:147], v[160:163], v[176:179]\n"
    "v_cvt_pk_bf16_f32 v99, v102, v103\n"
    "v_or_b32_e32 v190, v190, v206\n"
    "v_mfma_f32_16x16x32_bf16 v[176:179], a[56:59], v[164:167], v[176:179]\n"
    "ds_write_b128 v243, v[96:99] offset:24640\n"
    "v_mfma_f32_16x16x32_bf16 v[96:99], a[0:3], v[184:187], 0\n"
    "s_waitcnt lgkmcnt(7)\n"
    "v_lshlrev_b32_sdwa v206, 1, v181 dst_sel:DWORD dst_unused:UNUSED_PAD src0_sel:DWORD src1_sel:WORD_1\n"
    "v_cvt_pk_bf16_f32 v112, v112, v113\n"
    "v_mfma_f32_16x16x32_bf16 v[100:103], a[4:7], v[184:187], 0\n"
    "v_cvt_pk_bf16_f32 v113, v114, v115\n"
    "v_cvt_pk_bf16_f32 v114, v116, v117\n"
    "v_cvt_pk_bf16_f32 v115, v118, v119\n"
    "ds_write_b128 v243, v[112:115] offset:26688\n"
    "v_mfma_f32_16x16x32_bf16 v[112:115], a[8:11], v[184:187], 0\n"
    "s_waitcnt lgkmcnt(5)\n"
    "v_cvt_pk_bf16_f32 v128, v128, v129\n"
    "v_cvt_pk_bf16_f32 v129, v130, v131\n"
    "v_or_b32_e32 v191, v191, v207\n"
    "v_mfma_f32_16x16x32_bf16 v[116:119], a[12:15], v[184:187], 0\n"
    "ds_read_b128 a[252:255], v249 offset:0\n"
    "ds_read_u16_d16_hi v206, v206\n"
    "v_lshlrev_b32_sdwa v207, 1, v183 dst_sel:DWORD dst_unused:UNUSED_PAD src0_sel:DWORD src1_sel:WORD_1\n"
    "v_mfma_f32_16x16x32_bf16 v[180:183], a[100:103], v[156:159], 0\n"
    "ds_read_u16_d16_hi v207, v207\n"
    "v_cvt_pk_bf16_f32 v130, v132, v133\n"
    "v_mfma_f32_16x16x32_bf16 v[180:183], a[148:151], v[160:163], v[180:183]\n"
    "v_or_b32_e64 v193, v193, v201\n"
    "v_cvt_pk_bf16_f32 v131, v134, v135\n"
    "v_mfma_f32_16x16x32_bf16 v[180:183], a[60:63], v[164:167], v[180:183]\n"
    "s_nop 0\n"
    "ds_write_b128 v243, v[128:131] offset:28736\n"
    "v_mfma_f32_16x16x32_bf16 v[128:131], v[0:3], v[184:187], 0\n"
    "s_waitcnt lgkmcnt(5)\n"
    "v_cvt_pk_bf16_f32 v104, v104, v105\n"
    "v_cvt_pk_bf16_f32 v105, v106, v107\n"
    "v_mfma_f32_16x16x32_bf16 v[132:135], v[4:7], v[184:187], 0\n"
    "v_cvt_pk_bf16_f32 v106, v108, v109\n"
    "v_cvt_pk_bf16_f32 v107, v110, v111\n"
    "v_mfma_f32_16x16x32_bf16 v[108:111], a[4:7], v[188:191], 0\n"
    "ds_write_b128 v243, v[104:107] offset:25664\n"
    "v_mfma_f32_16x16x32_bf16 v[104:107], a[0:3], v[188:191], 0\n"
    "s_waitcnt lgkmcnt(4)\n"
    "buffer_store_dwordx4 a[252:255], v255, s[44:47], s57 offen offset:0 sc1\n"
    "v_or_b32_e32 v194, v194, v202\n"
    "v_cvt_pk_bf16_f32 v120, v120, v121\n"
    "v_cvt_pk_bf16_f32 v121, v122, v123\n"
    "v_cvt_pk_bf16_f32 v122, v124, v125\n"
    "v_cvt_pk_bf16_f32 v123, v126, v127\n"
    "ds_write_b128 v243, v[120:123] offset:27712\n"
    "v_mfma_f32_16x16x32_bf16 v[120:123], a[8:11], v[188:191], 0\n"
    "s_waitcnt lgkmcnt(2)\n"
    "v_pk_fma_f16 v232, v192, v192, s48\n"
    "v_cvt_pk_bf16_f32 v136, v136, v137\n"
    "v_mfma_f32_16x16x32_bf16 v[124:127], a[12:15], v[188:191], 0\n"
    "s_nop 0\n"
    "ds_read_b128 a[252:255], v249 offset:2048\n"
    "v_cvt_pk_bf16_f32 v137, v138, v139\n"
    "v_cvt_pk_bf16_f32 v138, v140, v141\n"
    "v_or_b32_e64 v195, v195, v203\n"
    "v_cvt_pk_bf16_f32 v139, v142, v143\n"
    "ds_write_b128 v243, v[136:139] offset:29760\n"
    "v_pk_mul_f16 v232, v192, v232\n"
    "v_mfma_f32_16x16x32_bf16 v[136:139], v[0:3], v[188:191], 0\n"
    "v_exp_f16_e64 v234, v232\n"
    "v_mfma_f32_16x16x32_bf16 v[140:143], v[4:7], v[188:191], 0\n"
    "v_or_b32_e64 v197, v197, v205\n"
    "v_exp_f16_sdwa v234, v232 dst_sel:WORD_1 dst_unused:UNUSED_PRESERVE src0_sel:WORD_1\n"
    "v_cvt_pk_f16_f32 v184, v168, v169\n"
    "v_pk_fma_f16 v234, v234, s49, s49\n"
    "v_lshlrev_b32_sdwa v185, 1, v170 dst_sel:DWORD dst_unused:UNUSED_PAD src0_sel:DWORD src1_sel:WORD_1\n"
    "v_lshlrev_b32_sdwa v201, 1, v171 dst_sel:DWORD dst_unused:UNUSED_PAD src0_sel:DWORD src1_sel:WORD_1\n"
    "v_mfma_f32_16x16x32_bf16 v[168:171], a[104:107], v[144:147], 0\n"
    "s_nop 0\n"
    "ds_read_u16 v185, v185\n"
    "ds_read_u16_d16_hi v201, v201\n"
    "v_mfma_f32_16x16x32_bf16 v[168:171], a[152:155], v[148:151], v[168:171]\n"
    "s_waitcnt lgkmcnt(3)\n"
    "buffer_store_dwordx4 a[252:255], v255, s[44:47], s57 offen offset:64 sc1\n"
    "v_lshlrev_b32_sdwa v186, 1, v172 dst_sel:DWORD dst_unused:UNUSED_PAD src0_sel:DWORD src1_sel:WORD_1\n"
    "v_lshlrev_b32_sdwa v202, 1, v173 dst_sel:DWORD dst_unused:UNUSED_PAD src0_sel:DWORD src1_sel:WORD_1\n"
    "v_lshlrev_b32_sdwa v187, 1, v174 dst_sel:DWORD dst_unused:UNUSED_PAD src0_sel:DWORD src1_sel:WORD_1\n"
    "v_lshlrev_b32_sdwa v203, 1, v175 dst_sel:DWORD dst_unused:UNUSED_PAD src0_sel:DWORD src1_sel:WORD_1\n"
    "v_rcp_f16_e64 v232, v234\n"
    "v_mfma_f32_16x16x32_bf16 v[168:171], a[64:67], v[152:155], v[168:171]\n"
    "ds_read_u16 v186, v186\n"
    "ds_read_u16_d16_hi v202, v202\n"
    "v_or_b32_e64 v198, v198, v206\n"
    "v_rcp_f16_sdwa v232, v234 dst_sel:WORD_1 dst_unused:UNUSED_PRESERVE src0_sel:WORD_1\n"
    "v_mfma_f32_16x16x32_bf16 v[172:175], a[108:111], v[144:147], 0\n"
    "ds_read_u16 v187, v187\n"
    "ds_read_u16_d16_hi v203, v203\n"
    "v_cvt_pk_f16_f32 v188, v176, v177\n"
    "v_lshlrev_b32_sdwa v189, 1, v178 dst_sel:DWORD dst_unused:UNUSED_PAD src0_sel:DWORD src1_sel:WORD_1\n"
    "v_lshlrev_b32_sdwa v205, 1, v179 dst_sel:DWORD dst_unused:UNUSED_PAD src0_sel:DWORD src1_sel:WORD_1\n"
    "v_lshlrev_b32_sdwa v190, 1, v180 dst_sel:DWORD dst_unused:UNUSED_PAD src0_sel:DWORD src1_sel:WORD_1\n"
    "v_lshlrev_b32_sdwa v206, 1, v181 dst_sel:DWORD dst_unused:UNUSED_PAD src0_sel:DWORD src1_sel:WORD_1\n"
    "v_lshlrev_b32_sdwa v191, 1, v182 dst_sel:DWORD dst_unused:UNUSED_PAD src0_sel:DWORD src1_sel:WORD_1\n"
    "v_fma_mix_f32 v234, v192, v232, 0 op_sel_hi:[1,1,0]\n"
    "v_mfma_f32_16x16x32_bf16 v[172:175], a[156:159], v[148:151], v[172:175]\n"
    "ds_read_b128 a[252:255], v249 offset:4096\n"
    "ds_read_u16 v189, v189\n"
    "v_mfma_f32_16x16x32_bf16 v[172:175], a[68:71], v[152:155], v[172:175]\n"
    "v_or_b32_e64 v199, v199, v207\n"
    "v_lshlrev_b32_sdwa v207, 1, v183 dst_sel:DWORD dst_unused:UNUSED_PAD src0_sel:DWORD src1_sel:WORD_1\n"
    "v_fma_mix_f32 v232, v192, v232, 0 op_sel:[1,1,0] op_sel_hi:[1,1,0]\n"
    "v_mfma_f32_16x16x32_bf16 v[176:179], a[104:107], v[156:159], 0\n"
    "s_nop 0\n"
    "ds_read_u16_d16_hi v205, v205\n"
    "v_cvt_pk_bf16_f32 v192, v234, v232\n"
    "v_mfma_f32_16x16x32_bf16 v[176:179], a[152:155], v[160:163], v[176:179]\n"
    "s_waitcnt lgkmcnt(7)\n"
    "ds_read_u16 v190, v190\n"
    "v_pk_fma_f16 v232, v196, v196, s48\n"
    "v_mfma_f32_16x16x32_bf16 v[176:179], a[64:67], v[164:167], v[176:179]\n"
    "v_pk_mul_f16 v232, v196, v232\n"
    "v_or_b32_e64 v185, v185, v201\n"
    "v_mfma_f32_16x16x32_bf16 v[180:183], a[108:111], v[156:159], 0\n"
    "ds_read_u16_d16_hi v206, v206\n"
    "v_exp_f16_e64 v234, v232\n"
    "v_mfma_f32_16x16x32_bf16 v[180:183], a[156:159], v[160:163], v[180:183]\n"
    "ds_read_u16 v191, v191\n"
    "v_exp_f16_sdwa v234, v232 dst_sel:WORD_1 dst_unused:UNUSED_PRESERVE src0_sel:WORD_1\n"
    "v_mfma_f32_16x16x32_bf16 v[180:183], a[68:71], v[164:167], v[180:183]\n"
    "s_waitcnt lgkmcnt(8)\n"
    "v_pk_fma_f16 v234, v234, s49, s49\n"
    "v_or_b32_e32 v186, v186, v202\n"
    "v_mfma_f32_16x16x32_bf16 v[96:99], a[16:19], v[192:195], v[96:99]\n"
    "ds_read_u16_d16_hi v207, v207\n"
    "v_rcp_f16_e32 v232, v234\n"
    "v_mfma_f32_16x16x32_bf16 v[100:103], a[20:23], v[192:195], v[100:103]\n"
    "v_rcp_f16_sdwa v232, v234 dst_sel:WORD_1 dst_unused:UNUSED_PRESERVE src0_sel:WORD_1\n"
    "v_mfma_f32_16x16x32_bf16 v[112:115], a[24:27], v[192:195], v[112:115]\n"
    "s_waitcnt lgkmcnt(6)\n"
    "buffer_store_dwordx4 a[252:255], v255, s[44:47], s57 offen offset:128 sc1\n"
    "v_mfma_f32_16x16x32_bf16 v[116:119], a[28:31], v[192:195], v[116:119]\n"
    "v_fma_mix_f32 v234, v196, v232, 0 op_sel_hi:[1,1,0]\n"
    "v_fma_mix_f32 v232, v196, v232, 0 op_sel:[1,1,0] op_sel_hi:[1,1,0]\n"
    "v_mfma_f32_16x16x32_bf16 v[128:131], v[8:11], v[192:195], v[128:131]\n"
    "ds_read_b128 a[252:255], v249 offset:1024\n"
    "v_cvt_pk_bf16_f32 v196, v234, v232\n"
    "v_mfma_f32_16x16x32_bf16 v[132:135], v[12:15], v[192:195], v[132:135]\n"
    "v_pk_fma_f16 v232, v184, v184, s48\n"
    "v_or_b32_e32 v187, v187, v203\n"
    "v_mfma_f32_16x16x32_bf16 v[104:107], a[16:19], v[196:199], v[104:107]\n"
    "s_waitcnt lgkmcnt(5)\n"
    "v_pk_mul_f16 v232, v184, v232\n"
    "v_or_b32_e64 v189, v189, v205\n"
    "v_mfma_f32_16x16x32_bf16 v[108:111], a[20:23], v[196:199], v[108:111]\n"
    "v_exp_f16_e32 v234, v232\n"
    "v_mfma_f32_16x16x32_bf16 v[120:123], a[24:27], v[196:199], v[120:123]\n"
    "v_exp_f16_sdwa v234, v232 dst_sel:WORD_1 dst_unused:UNUSED_PRESERVE src0_sel:WORD_1\n"
    "v_mfma_f32_16x16x32_bf16 v[124:127], a[28:31], v[196:199], v[124:127]\n"
    "s_waitcnt lgkmcnt(3)\n"
    "v_pk_fma_f16 v234, v234, s49, s49\n"
    "v_lshlrev_b32_sdwa v192, 1, v168 dst_sel:DWORD dst_unused:UNUSED_PAD src0_sel:DWORD src1_sel:WORD_1\n"
    "v_mfma_f32_16x16x32_bf16 v[136:139], v[8:11], v[196:199], v[136:139]\n"
    "ds_read_u16 v192, v192\n"
    "v_lshlrev_b32_sdwa v200, 1, v169 dst_sel:DWORD dst_unused:UNUSED_PAD src0_sel:DWORD src1_sel:WORD_1\n"
    "v_mfma_f32_16x16x32_bf16 v[140:143], v[12:15], v[196:199], v[140:143]\n"
    "ds_read_u16_d16_hi v200, v200\n"
    "v_lshlrev_b32_sdwa v193, 1, v170 dst_sel:DWORD dst_unused:UNUSED_PAD src0_sel:DWORD src1_sel:WORD_1\n"
    "v_lshlrev_b32_sdwa v201, 1, v171 dst_sel:DWORD dst_unused:UNUSED_PAD src0_sel:DWORD src1_sel:WORD_1\n"
    "v_mfma_f32_16x16x32_bf16 v[168:171], a[112:115], v[144:147], 0\n"
    "s_waitcnt lgkmcnt(2)\n"
    "ds_read_u16 v193, v193\n"
    "ds_read_u16_d16_hi v201, v201\n"
    "v_mfma_f32_16x16x32_bf16 v[168:171], a[160:163], v[148:151], v[168:171]\n"
    "s_nop 0\n"
    "buffer_store_dwordx4 a[252:255], v255, s[44:47], s88 offen offset:0 sc1\n"
    "v_lshlrev_b32_sdwa v194, 1, v172 dst_sel:DWORD dst_unused:UNUSED_PAD src0_sel:DWORD src1_sel:WORD_1\n"
    "v_lshlrev_b32_sdwa v202, 1, v173 dst_sel:DWORD dst_unused:UNUSED_PAD src0_sel:DWORD src1_sel:WORD_1\n"
    "v_lshlrev_b32_sdwa v195, 1, v174 dst_sel:DWORD dst_unused:UNUSED_PAD src0_sel:DWORD src1_sel:WORD_1\n"
    "v_lshlrev_b32_sdwa v203, 1, v175 dst_sel:DWORD dst_unused:UNUSED_PAD src0_sel:DWORD src1_sel:WORD_1\n"
    "v_rcp_f16_e64 v232, v234\n"
    "v_mfma_f32_16x16x32_bf16 v[168:171], a[72:75], v[152:155], v[168:171]\n"
    "ds_read_u16 v194, v194\n"
    "ds_read_u16_d16_hi v202, v202\n"
    "v_or_b32_e64 v190, v190, v206\n"
    "v_rcp_f16_sdwa v232, v234 dst_sel:WORD_1 dst_unused:UNUSED_PRESERVE src0_sel:WORD_1\n"
    "v_mfma_f32_16x16x32_bf16 v[172:175], a[116:119], v[144:147], 0\n"
    "ds_read_u16 v195, v195\n"
    "ds_read_u16_d16_hi v203, v203\n"
    "v_lshlrev_b32_sdwa v196, 1, v176 dst_sel:DWORD dst_unused:UNUSED_PAD src0_sel:DWORD src1_sel:WORD_1\n"
    "v_lshlrev_b32_sdwa v204, 1, v177 dst_sel:DWORD dst_unused:UNUSED_PAD src0_sel:DWORD src1_sel:WORD_1\n"
    "v_lshlrev_b32_sdwa v197, 1, v178 dst_sel:DWORD dst_unused:UNUSED_PAD src0_sel:DWORD src1_sel:WORD_1\n"
    "v_lshlrev_b32_sdwa v205, 1, v179 dst_sel:DWORD dst_unused:UNUSED_PAD src0_sel:DWORD src1_sel:WORD_1\n"
    "v_lshlrev_b32_sdwa v198, 1, v180 dst_sel:DWORD dst_unused:UNUSED_PAD src0_sel:DWORD src1_sel:WORD_1\n"
    "v_lshlrev_b32_sdwa v206, 1, v181 dst_sel:DWORD dst_unused:UNUSED_PAD src0_sel:DWORD src1_sel:WORD_1\n"
    "v_lshlrev_b32_sdwa v199, 1, v182 dst_sel:DWORD dst_unused:UNUSED_PAD src0_sel:DWORD src1_sel:WORD_1\n"
    "v_fma_mix_f32 v234, v184, v232, 0 op_sel_hi:[1,1,0]\n"
    "v_mfma_f32_16x16x32_bf16 v[172:175], a[164:167], v[148:151], v[172:175]\n"
    "ds_read_u16 v196, v196\n"
    "v_or_b32_e32 v191, v191, v207\n"
    "v_mfma_f32_16x16x32_bf16 v[172:175], v[52:55], v[152:155], v[172:175]\n"
    "s_waitcnt lgkmcnt(7)\n"
    "ds_read_u16_d16_hi v204, v204\n"
    "v_lshlrev_b32_sdwa v207, 1, v183 dst_sel:DWORD dst_unused:UNUSED_PAD src0_sel:DWORD src1_sel:WORD_1\n"
    "v_fma_mix_f32 v232, v184, v232, 0 op_sel:[1,1,0] op_sel_hi:[1,1,0]\n"
    "v_mfma_f32_16x16x32_bf16 v[176:179], a[112:115], v[156:159], 0\n"
    "ds_read_u16 v197, v197\n"
    "v_cvt_pk_bf16_f32 v184, v234, v232\n"
    "v_mfma_f32_16x16x32_bf16 v[176:179], a[160:163], v[160:163], v[176:179]\n"
    "v_pk_fma_f16 v232, v188, v188, s48\n"
    "v_or_b32_e32 v192, v192, v200\n"
    "v_mfma_f32_16x16x32_bf16 v[176:179], a[72:75], v[164:167], v[176:179]\n"
    "s_waitcnt lgkmcnt(7)\n"
    "ds_read_b128 a[252:255], v249 offset:3072\n"
    "v_pk_mul_f16 v232, v188, v232\n"
    "v_mfma_f32_16x16x32_bf16 v[180:183], a[116:119], v[156:159], 0\n"
    "ds_read_u16_d16_hi v205, v205\n"
    "v_exp_f16_e32 v234, v232\n"
    "v_mfma_f32_16x16x32_bf16 v[180:183], a[164:167], v[160:163], v[180:183]\n"
    "v_exp_f16_sdwa v234, v232 dst_sel:WORD_1 dst_unused:UNUSED_PRESERVE src0_sel:WORD_1\n"
    "v_mfma_f32_16x16x32_bf16 v[180:183], v[52:55], v[164:167], v[180:183]\n"
    "s_waitcnt lgkmcnt(7)\n"
    "ds_read_u16 v198, v198\n"
    "v_pk_fma_f16 v234, v234, s49, s49\n"
    "v_mfma_f32_16x16x32_bf16 v[96:99], a[32:35], v[184:187], v[96:99]\n"
    "ds_read_u16_d16_hi v206, v206\n"
    "v_rcp_f16_e64 v232, v234\n"
    "v_mfma_f32_16x16x32_bf16 v[100:103], a[36:39], v[184:187], v[100:103]\n"
    "v_rcp_f16_sdwa v232, v234 dst_sel:WORD_1 dst_unused:UNUSED_PRESERVE src0_sel:WORD_1\n"
    "v_mfma_f32_16x16x32_bf16 v[112:115], a[40:43], v[184:187], v[112:115]\n"
    "ds_read_u16 v199, v199\n"
    "v_fma_mix_f32 v234, v188, v232, 0 op_sel_hi:[1,1,0]\n"
    "v_mfma_f32_16x16x32_bf16 v[116:119], a[44:47], v[184:187], v[116:119]\n"
    "s_waitcnt lgkmcnt(8)\n"
    "v_fma_mix_f32 v232, v188, v232, 0 op_sel:[1,1,0] op_sel_hi:[1,1,0]\n"
    "v_or_b32_e32 v193, v193, v201\n"
    "v_mfma_f32_16x16x32_bf16 v[128:131], v[16:19], v[184:187], v[128:131]\n"
    "ds_read_u16_d16_hi v207, v207\n"
    "v_cvt_pk_bf16_f32 v188, v234, v232\n"
    "v_mfma_f32_16x16x32_bf16 v[132:135], v[20:23], v[184:187], v[132:135]\n"
    "v_cvt_pk_f16_f32 v184, v168, v169\n"
    "v_lshlrev_b32_sdwa v185, 1, v170 dst_sel:DWORD dst_unused:UNUSED_PAD src0_sel:DWORD src1_sel:WORD_1\n"
    "v_mfma_f32_16x16x32_bf16 v[104:107], a[32:35], v[188:191], v[104:107]\n"
    "s_waitcnt lgkmcnt(7)\n"
    "ds_read_u16 v185, v185\n"
    "v_lshlrev_b32_sdwa v201, 1, v171 dst_sel:DWORD dst_unused:UNUSED_PAD src0_sel:DWORD src1_sel:WORD_1\n"
    "v_mfma_f32_16x16x32_bf16 v[108:111], a[36:39], v[188:191], v[108:111]\n"
    "ds_read_u16_d16_hi v201, v201\n"
    "v_lshlrev_b32_sdwa v186, 1, v172 dst_sel:DWORD dst_unused:UNUSED_PAD src0_sel:DWORD src1_sel:WORD_1\n"
    "v_mfma_f32_16x16x32_bf16 v[120:123], a[40:43], v[188:191], v[120:123]\n"
    "s_waitcnt lgkmcnt(7)\n"
    "buffer_store_dwordx4 a[252:255], v255, s[44:47], s88 offen offset:64 sc1\n"
    "ds_read_u16 v186, v186\n"
    "v_mfma_f32_16x16x32_bf16 v[124:127], a[44:47], v[188:191], v[124:127]\n"
    "v_or_b32_e32 v194, v194, v202\n"
    "v_lshlrev_b32_sdwa v202, 1, v173 dst_sel:DWORD dst_unused:UNUSED_PAD src0_sel:DWORD src1_sel:WORD_1\n"
    "v_mfma_f32_16x16x32_bf16 v[136:139], v[16:19], v[188:191], v[136:139]\n"
    "s_waitcnt lgkmcnt(7)\n"
    "ds_read_b128 a[252:255], v249 offset:5120\n"
    "ds_read_u16_d16_hi v202, v202\n"
    "v_mfma_f32_16x16x32_bf16 v[140:143], v[20:23], v[188:191], v[140:143]\n"
    "v_lshlrev_b32_sdwa v187, 1, v174 dst_sel:DWORD dst_unused:UNUSED_PAD src0_sel:DWORD src1_sel:WORD_1\n"
    "v_cvt_pk_f16_f32 v188, v176, v177\n"
    "v_mfma_f32_16x16x32_bf16 v[168:171], a[120:123], v[144:147], 0\n"
    "ds_read_u16 v187, v187\n"
    "v_lshlrev_b32_sdwa v189, 1, v178 dst_sel:DWORD dst_unused:UNUSED_PAD src0_sel:DWORD src1_sel:WORD_1\n"
    "v_mfma_f32_16x16x32_bf16 v[168:171], a[168:171], v[148:151], v[168:171]\n"
    "s_waitcnt lgkmcnt(8)\n"
    "v_lshlrev_b32_sdwa v190, 1, v180 dst_sel:DWORD dst_unused:UNUSED_PAD src0_sel:DWORD src1_sel:WORD_1\n"
    "v_lshlrev_b32_sdwa v191, 1, v182 dst_sel:DWORD dst_unused:UNUSED_PAD src0_sel:DWORD src1_sel:WORD_1\n"
    "v_mfma_f32_16x16x32_bf16 v[168:171], a[76:79], v[152:155], v[168:171]\n"
    "v_or_b32_e32 v195, v195, v203\n"
    "v_lshlrev_b32_sdwa v203, 1, v175 dst_sel:DWORD dst_unused:UNUSED_PAD src0_sel:DWORD src1_sel:WORD_1\n"
    "v_mfma_f32_16x16x32_bf16 v[172:175], a[124:127], v[144:147], 0\n"
    "ds_read_u16_d16_hi v203, v203\n"
    "v_or_b32_e64 v196, v196, v204\n"
    "v_mfma_f32_16x16x32_bf16 v[172:175], a[172:175], v[148:151], v[172:175]\n"
    "ds_read_u16 v189, v189\n"
    "v_or_b32_e32 v197, v197, v205\n"
    "v_mfma_f32_16x16x32_bf16 v[172:175], v[60:63], v[152:155], v[172:175]\n"
    "s_waitcnt lgkmcnt(8)\n"
    "v_lshlrev_b32_sdwa v205, 1, v179 dst_sel:DWORD dst_unused:UNUSED_PAD src0_sel:DWORD src1_sel:WORD_1\n"
    "v_or_b32_e64 v198, v198, v206\n"
    "v_mfma_f32_16x16x32_bf16 v[176:179], a[120:123], v[156:159], 0\n"
    "ds_read_u16_d16_hi v205, v205\n"
    "v_lshlrev_b32_sdwa v206, 1, v181 dst_sel:DWORD dst_unused:UNUSED_PAD src0_sel:DWORD src1_sel:WORD_1\n"
    "v_mfma_f32_16x16x32_bf16 v[176:179], a[168:171], v[160:163], v[176:179]\n"
    "ds_read_u16 v190, v190\n"
    "v_or_b32_e32 v199, v199, v207\n"
    "v_mfma_f32_16x16x32_bf16 v[176:179], a[76:79], v[164:167], v[176:179]\n"
    "s_waitcnt lgkmcnt(8)\n"
    "v_lshlrev_b32_sdwa v207, 1, v183 dst_sel:DWORD dst_unused:UNUSED_PAD src0_sel:DWORD src1_sel:WORD_1\n"
    "v_or_b32_e64 v185, v185, v201\n"
    "v_mfma_f32_16x16x32_bf16 v[180:183], a[124:127], v[156:159], 0\n"
    "ds_read_b128 v[144:147], v243 offset:0\n"
    "v_pk_fma_f16 v232, v184, v184, s48\n"
    "v_mfma_f32_16x16x32_bf16 v[180:183], a[172:175], v[160:163], v[180:183]\n"
    "s_waitcnt lgkmcnt(7)\n"
    "ds_read_u16_d16_hi v206, v206\n"
    "buffer_store_dwordx4 a[252:255], v255, s[44:47], s88 offen offset:128 sc1\n"
    "v_mfma_f32_16x16x32_bf16 v[180:183], v[60:63], v[164:167], v[180:183]\n"
    "s_nop 0\n"
    "ds_read_b128 v[148:151], v243 offset:1024\n"
    "v_pk_mul_f16 v232, v184, v232\n"
    "v_mfma_f32_16x16x32_bf16 v[96:99], a[176:179], v[192:195], v[96:99]\n"
    "s_waitcnt lgkmcnt(8)\n"
    "ds_read_u16 v191, v191\n"
    "v_or_b32_e32 v186, v186, v202\n"
    "v_mfma_f32_16x16x32_bf16 v[100:103], a[180:183], v[192:195], v[100:103]\n"
    "v_exp_f16_e64 v234, v232\n"
    "v_mfma_f32_16x16x32_bf16 v[112:115], a[184:187], v[192:195], v[112:115]\n"
    "ds_read_b128 v[152:155], v243 offset:2048\n"
    "ds_read_u16_d16_hi v207, v207\n"
    "v_mfma_f32_16x16x32_bf16 v[116:119], a[188:191], v[192:195], v[116:119]\n"
    "s_waitcnt lgkmcnt(9)\n"
    "v_or_b32_e32 v187, v187, v203\n"
    "v_exp_f16_sdwa v234, v232 dst_sel:WORD_1 dst_unused:UNUSED_PRESERVE src0_sel:WORD_1\n"
    "v_mfma_f32_16x16x32_bf16 v[128:131], v[24:27], v[192:195], v[128:131]\n"
    "ds_read_b128 v[156:159], v243 offset:3072\n"
    "v_pk_fma_f16 v234, v234, s49, s49\n"
    "v_mfma_f32_16x16x32_bf16 v[132:135], v[28:31], v[192:195], v[132:135]\n"
    "ds_read_b128 v[160:163], v243 offset:4096\n"
    "v_cvt_pk_f16_f32 v192, v168, v169\n"
    "v_mfma_f32_16x16x32_bf16 v[104:107], a[176:179], v[196:199], v[104:107]\n"
    "s_waitcnt lgkmcnt(9)\n"
    "ds_read_b128 v[164:167], v243 offset:5120\n"
    "v_or_b32_e32 v189, v189, v205\n"
    "v_mfma_f32_16x16x32_bf16 v[108:111], a[180:183], v[196:199], v[108:111]\n"
    "s_nop 0\n"
    "v_lshlrev_b32_sdwa v193, 1, v170 dst_sel:DWORD dst_unused:UNUSED_PAD src0_sel:DWORD src1_sel:WORD_1\n"
    "v_lshlrev_b32_sdwa v201, 1, v171 dst_sel:DWORD dst_unused:UNUSED_PAD src0_sel:DWORD src1_sel:WORD_1\n"
    "v_mfma_f32_16x16x32_bf16 v[120:123], a[184:187], v[196:199], v[120:123]\n"
    "s_waitcnt lgkmcnt(8)\n"
    "ds_read_u16 v193, v193\n"
    "v_lshlrev_b32_sdwa v194, 1, v172 dst_sel:DWORD dst_unused:UNUSED_PAD src0_sel:DWORD src1_sel:WORD_1\n"
    "v_mfma_f32_16x16x32_bf16 v[124:127], a[188:191], v[196:199], v[124:127]\n"
    "ds_read_u16_d16_hi v201, v201\n"
    "v_lshlrev_b32_sdwa v202, 1, v173 dst_sel:DWORD dst_unused:UNUSED_PAD src0_sel:DWORD src1_sel:WORD_1\n"
    "v_mfma_f32_16x16x32_bf16 v[136:139], v[24:27], v[196:199], v[136:139]\n"
    "v_lshlrev_b32_sdwa v195, 1, v174 dst_sel:DWORD dst_unused:UNUSED_PAD src0_sel:DWORD src1_sel:WORD_1\n"
    "v_lshlrev_b32_sdwa v203, 1, v175 dst_sel:DWORD dst_unused:UNUSED_PAD src0_sel:DWORD src1_sel:WORD_1\n"
    "v_mfma_f32_16x16x32_bf16 v[140:143], v[28:31], v[196:199], v[140:143]\n"
    "s_waitcnt lgkmcnt(8)\n"
    "ds_read_u16 v194, v194\n"
    "v_rcp_f16_e32 v232, v234\n"
    "v_mfma_f32_16x16x32_bf16 v[168:171], a[80:83], v[144:147], 0\n"
    "ds_read_u16_d16_hi v202, v202\n"
    "v_or_b32_e32 v190, v190, v206\n"
    "v_mfma_f32_16x16x32_bf16 v[168:171], a[128:131], v[148:151], v[168:171]\n"
    "s_waitcnt lgkmcnt(8)\n"
    "v_rcp_f16_sdwa v232, v234 dst_sel:WORD_1 dst_unused:UNUSED_PRESERVE src0_sel:WORD_1\n"
    "v_mfma_f32_16x16x32_bf16 v[168:171], a[48:51], v[152:155], v[168:171]\n"
    "ds_read_u16 v195, v195\n"
    "v_cvt_pk_f16_f32 v196, v176, v177\n"
    "v_mfma_f32_16x16x32_bf16 v[172:175], a[84:87], v[144:147], 0\n"
    "s_nop 0\n"
    "v_lshlrev_b32_sdwa v197, 1, v178 dst_sel:DWORD dst_unused:UNUSED_PAD src0_sel:DWORD src1_sel:WORD_1\n"
    "v_lshlrev_b32_sdwa v205, 1, v179 dst_sel:DWORD dst_unused:UNUSED_PAD src0_sel:DWORD src1_sel:WORD_1\n"
    "v_mfma_f32_16x16x32_bf16 v[172:175], a[132:135], v[148:151], v[172:175]\n"
    "s_waitcnt lgkmcnt(7)\n"
    "ds_read_u16_d16_hi v203, v203\n"
    "v_lshlrev_b32_sdwa v198, 1, v180 dst_sel:DWORD dst_unused:UNUSED_PAD src0_sel:DWORD src1_sel:WORD_1\n"
    "v_lshlrev_b32_sdwa v206, 1, v181 dst_sel:DWORD dst_unused:UNUSED_PAD src0_sel:DWORD src1_sel:WORD_1\n"
    "v_lshlrev_b32_sdwa v199, 1, v182 dst_sel:DWORD dst_unused:UNUSED_PAD src0_sel:DWORD src1_sel:WORD_1\n"
    "v_fma_mix_f32 v234, v184, v232, 0 op_sel_hi:[1,1,0]\n"
    "v_mfma_f32_16x16x32_bf16 v[172:175], v[48:51], v[152:155], v[172:175]\n"
    "s_waitcnt lgkmcnt(6)\n"
    "ds_read_u16 v197, v197\n"
    "v_or_b32_e32 v191, v191, v207\n"
    "v_lshlrev_b32_sdwa v207, 1, v183 dst_sel:DWORD dst_unused:UNUSED_PAD src0_sel:DWORD src1_sel:WORD_1\n"
    "v_fma_mix_f32 v232, v184, v232, 0 op_sel:[1,1,0] op_sel_hi:[1,1,0]\n"
    "v_mfma_f32_16x16x32_bf16 v[176:179], a[80:83], v[156:159], 0\n"
    "s_barrier\n"
    "v_cvt_pk_bf16_f32 v184, v234, v232\n"
    "v_mfma_f32_16x16x32_bf16 v[176:179], a[128:131], v[160:163], v[176:179]\n"
    "s_waitcnt lgkmcnt(5)\n"
    "ds_read_u16_d16_hi v205, v205\n"
    "ds_read_u16 v198, v198\n"
    "v_mfma_f32_16x16x32_bf16 v[176:179], a[48:51], v[164:167], v[176:179]\n"
    "v_pk_fma_f16 v232, v188, v188, s48\n"
    "v_or_b32_e64 v193, v193, v201\n"
    "v_mfma_f32_16x16x32_bf16 v[180:183], a[84:87], v[156:159], 0\n"
    "ds_read_u16_d16_hi v206, v206\n"
    "v_pk_mul_f16 v232, v188, v232\n"
    "v_mfma_f32_16x16x32_bf16 v[180:183], a[132:135], v[160:163], v[180:183]\n"
    "s_waitcnt lgkmcnt(6)\n"
    "ds_read_u16 v199, v199\n"
    "v_exp_f16_e32 v234, v232\n"
    "v_mfma_f32_16x16x32_bf16 v[180:183], v[48:51], v[164:167], v[180:183]\n"
    "ds_read_u16_d16_hi v207, v207\n"
    "v_exp_f16_sdwa v234, v232 dst_sel:WORD_1 dst_unused:UNUSED_PRESERVE src0_sel:WORD_1\n"
    "v_mfma_f32_16x16x32_bf16 v[96:99], a[192:195], v[184:187], v[96:99]\n"
    "v_pk_fma_f16 v234, v234, s49, s49\n"
    "v_or_b32_e64 v194, v194, v202\n"
    "v_mfma_f32_16x16x32_bf16 v[100:103], a[196:199], v[184:187], v[100:103]\n"
    "s_waitcnt lgkmcnt(6)\n"
    "v_rcp_f16_e32 v232, v234\n"
    "v_mfma_f32_16x16x32_bf16 v[112:115], a[200:203], v[184:187], v[112:115]\n"
    "v_rcp_f16_sdwa v232, v234 dst_sel:WORD_1 dst_unused:UNUSED_PRESERVE src0_sel:WORD_1\n"
    "v_mfma_f32_16x16x32_bf16 v[116:119], a[204:207], v[184:187], v[116:119]\n"
    "v_fma_mix_f32 v234, v188, v232, 0 op_sel_hi:[1,1,0]\n"
    "v_fma_mix_f32 v232, v188, v232, 0 op_sel:[1,1,0] op_sel_hi:[1,1,0]\n"
    "v_mfma_f32_16x16x32_bf16 v[128:131], v[32:35], v[184:187], v[128:131]\n"
    "v_cvt_pk_bf16_f32 v188, v234, v232\n"
    "v_pk_fma_f16 v232, v192, v192, s48\n"
    "v_mfma_f32_16x16x32_bf16 v[132:135], v[36:39], v[184:187], v[132:135]\n"
    "s_waitcnt lgkmcnt(4)\n"
    "v_or_b32_e32 v195, v195, v203\n"
    "v_pk_mul_f16 v232, v192, v232\n"
    "v_mfma_f32_16x16x32_bf16 v[104:107], a[192:195], v[188:191], v[104:107]\n"
    "v_exp_f16_e64 v234, v232\n"
    "v_mfma_f32_16x16x32_bf16 v[108:111], a[196:199], v[188:191], v[108:111]\n"
    "v_exp_f16_sdwa v234, v232 dst_sel:WORD_1 dst_unused:UNUSED_PRESERVE src0_sel:WORD_1\n"
    "v_mfma_f32_16x16x32_bf16 v[120:123], a[200:203], v[188:191], v[120:123]\n"
    "s_waitcnt lgkmcnt(2)\n"
    "v_or_b32_e32 v197, v197, v205\n"
    "v_pk_fma_f16 v234, v234, s49, s49\n"
    "v_mfma_f32_16x16x32_bf16 v[124:127], a[204:207], v[188:191], v[124:127]\n"
    "v_lshlrev_b32_sdwa v184, 1, v168 dst_sel:DWORD dst_unused:UNUSED_PAD src0_sel:DWORD src1_sel:WORD_1\n"
    "v_lshlrev_b32_sdwa v200, 1, v169 dst_sel:DWORD dst_unused:UNUSED_PAD src0_sel:DWORD src1_sel:WORD_1\n"
    "v_mfma_f32_16x16x32_bf16 v[136:139], v[32:35], v[188:191], v[136:139]\n"
    "ds_read_u16 v184, v184\n"
    "ds_read_u16_d16_hi v200, v200\n"
    "v_mfma_f32_16x16x32_bf16 v[140:143], v[36:39], v[188:191], v[140:143]\n"
    "s_nop 0\n"
    "v_lshlrev_b32_sdwa v185, 1, v170 dst_sel:DWORD dst_unused:UNUSED_PAD src0_sel:DWORD src1_sel:WORD_1\n"
    "v_lshlrev_b32_sdwa v201, 1, v171 dst_sel:DWORD dst_unused:UNUSED_PAD src0_sel:DWORD src1_sel:WORD_1\n"
    "v_mfma_f32_16x16x32_bf16 v[168:171], a[88:91], v[144:147], 0\n"
    "s_waitcnt lgkmcnt(2)\n"
    "ds_read_u16 v185, v185\n"
    "ds_read_u16_d16_hi v201, v201\n"
    "v_mfma_f32_16x16x32_bf16 v[168:171], a[136:139], v[148:151], v[168:171]\n"
    "v_lshlrev_b32_sdwa v186, 1, v172 dst_sel:DWORD dst_unused:UNUSED_PAD src0_sel:DWORD src1_sel:WORD_1\n"
    "v_lshlrev_b32_sdwa v202, 1, v173 dst_sel:DWORD dst_unused:UNUSED_PAD src0_sel:DWORD src1_sel:WORD_1\n"
    "v_lshlrev_b32_sdwa v187, 1, v174 dst_sel:DWORD dst_unused:UNUSED_PAD src0_sel:DWORD src1_sel:WORD_1\n"
    "v_lshlrev_b32_sdwa v203, 1, v175 dst_sel:DWORD dst_unused:UNUSED_PAD src0_sel:DWORD src1_sel:WORD_1\n"
    "v_rcp_f16_e64 v232, v234\n"
    "v_mfma_f32_16x16x32_bf16 v[168:171], a[52:55], v[152:155], v[168:171]\n"
    "ds_read_u16 v186, v186\n"
    "ds_read_u16_d16_hi v202, v202\n"
    "v_or_b32_e64 v198, v198, v206\n"
    "v_rcp_f16_sdwa v232, v234 dst_sel:WORD_1 dst_unused:UNUSED_PRESERVE src0_sel:WORD_1\n"
    "v_mfma_f32_16x16x32_bf16 v[172:175], a[92:95], v[144:147], 0\n"
    "ds_read_u16 v187, v187\n"
    "v_lshlrev_b32_sdwa v188, 1, v176 dst_sel:DWORD dst_unused:UNUSED_PAD src0_sel:DWORD src1_sel:WORD_1\n"
    "v_mfma_f32_16x16x32_bf16 v[172:175], a[140:143], v[148:151], v[172:175]\n"
    "v_lshlrev_b32_sdwa v204, 1, v177 dst_sel:DWORD dst_unused:UNUSED_PAD src0_sel:DWORD src1_sel:WORD_1\n"
    "v_lshlrev_b32_sdwa v189, 1, v178 dst_sel:DWORD dst_unused:UNUSED_PAD src0_sel:DWORD src1_sel:WORD_1\n"
    "v_lshlrev_b32_sdwa v205, 1, v179 dst_sel:DWORD dst_unused:UNUSED_PAD src0_sel:DWORD src1_sel:WORD_1\n"
    "v_lshlrev_b32_sdwa v190, 1, v180 dst_sel:DWORD dst_unused:UNUSED_PAD src0_sel:DWORD src1_sel:WORD_1\n"
    "v_lshlrev_b32_sdwa v206, 1, v181 dst_sel:DWORD dst_unused:UNUSED_PAD src0_sel:DWORD src1_sel:WORD_1\n"
    "v_lshlrev_b32_sdwa v191, 1, v182 dst_sel:DWORD dst_unused:UNUSED_PAD src0_sel:DWORD src1_sel:WORD_1\n"
    "v_fma_mix_f32 v234, v192, v232, 0 op_sel_hi:[1,1,0]\n"
    "v_mfma_f32_16x16x32_bf16 v[172:175], v[56:59], v[152:155], v[172:175]\n"
    "ds_read_u16_d16_hi v203, v203\n"
    "ds_read_u16 v188, v188\n"
    "v_or_b32_e32 v199, v199, v207\n"
    "v_lshlrev_b32_sdwa v207, 1, v183 dst_sel:DWORD dst_unused:UNUSED_PAD src0_sel:DWORD src1_sel:WORD_1\n"
    "v_fma_mix_f32 v232, v192, v232, 0 op_sel:[1,1,0] op_sel_hi:[1,1,0]\n"
    "v_mfma_f32_16x16x32_bf16 v[176:179], a[88:91], v[156:159], 0\n"
    "s_waitcnt lgkmcnt(7)\n"
    "ds_read_u16_d16_hi v204, v204\n"
    "v_cvt_pk_bf16_f32 v192, v234, v232\n"
    "v_mfma_f32_16x16x32_bf16 v[176:179], a[136:139], v[160:163], v[176:179]\n"
    "v_pk_fma_f16 v232, v196, v196, s48\n"
    "v_or_b32_e64 v184, v184, v200\n"
    "v_mfma_f32_16x16x32_bf16 v[176:179], a[52:55], v[164:167], v[176:179]\n"
    "ds_read_u16 v189, v189\n"
    "v_pk_mul_f16 v232, v196, v232\n"
    "v_mfma_f32_16x16x32_bf16 v[180:183], a[92:95], v[156:159], 0\n"
    "v_exp_f16_e32 v234, v232\n"
    "v_mfma_f32_16x16x32_bf16 v[180:183], a[140:143], v[160:163], v[180:183]\n"
    "s_waitcnt lgkmcnt(7)\n"
    "ds_read_u16_d16_hi v205, v205\n"
    "v_exp_f16_sdwa v234, v232 dst_sel:WORD_1 dst_unused:UNUSED_PRESERVE src0_sel:WORD_1\n"
    "v_mfma_f32_16x16x32_bf16 v[180:183], v[56:59], v[164:167], v[180:183]\n"
    "ds_read_u16 v190, v190\n"
    "v_pk_fma_f16 v234, v234, s49, s49\n"
    "v_mfma_f32_16x16x32_bf16 v[96:99], a[208:211], v[192:195], v[96:99]\n"
    "v_rcp_f16_e32 v232, v234\n"
    "v_mfma_f32_16x16x32_bf16 v[100:103], a[212:215], v[192:195], v[100:103]\n"
    "s_waitcnt lgkmcnt(7)\n"
    "ds_read_u16_d16_hi v206, v206\n"
    "v_rcp_f16_sdwa v232, v234 dst_sel:WORD_1 dst_unused:UNUSED_PRESERVE src0_sel:WORD_1\n"
    "v_mfma_f32_16x16x32_bf16 v[112:115], a[216:219], v[192:195], v[112:115]\n"
    "v_fma_mix_f32 v234, v196, v232, 0 op_sel_hi:[1,1,0]\n"
    "v_fma_mix_f32 v232, v196, v232, 0 op_sel:[1,1,0] op_sel_hi:[1,1,0]\n"
    "v_mfma_f32_16x16x32_bf16 v[116:119], a[220:223], v[192:195], v[116:119]\n"
    "ds_read_u16 v191, v191\n"
    "v_cvt_pk_bf16_f32 v196, v234, v232\n"
    "v_mfma_f32_16x16x32_bf16 v[128:131], v[40:43], v[192:195], v[128:131]\n"
    "s_waitcnt lgkmcnt(7)\n"
    "ds_read_u16_d16_hi v207, v207\n"
    "v_or_b32_e32 v185, v185, v201\n"
    "v_mfma_f32_16x16x32_bf16 v[132:135], v[44:47], v[192:195], v[132:135]\n"
    "v_cvt_pk_f16_f32 v192, v168, v169\n"
    "v_lshlrev_b32_sdwa v193, 1, v170 dst_sel:DWORD dst_unused:UNUSED_PAD src0_sel:DWORD src1_sel:WORD_1\n"
    "v_mfma_f32_16x16x32_bf16 v[104:107], a[208:211], v[196:199], v[104:107]\n"
    "ds_read_u16 v193, v193\n"
    "v_lshlrev_b32_sdwa v201, 1, v171 dst_sel:DWORD dst_unused:UNUSED_PAD src0_sel:DWORD src1_sel:WORD_1\n"
    "v_mfma_f32_16x16x32_bf16 v[108:111], a[212:215], v[196:199], v[108:111]\n"
    "v_lshlrev_b32_sdwa v194, 1, v172 dst_sel:DWORD dst_unused:UNUSED_PAD src0_sel:DWORD src1_sel:WORD_1\n"
    "v_or_b32_e32 v186, v186, v202\n"
    "v_mfma_f32_16x16x32_bf16 v[120:123], a[216:219], v[196:199], v[120:123]\n"
    "s_waitcnt lgkmcnt(7)\n"
    "ds_read_u16_d16_hi v201, v201\n"
    "v_lshlrev_b32_sdwa v202, 1, v173 dst_sel:DWORD dst_unused:UNUSED_PAD src0_sel:DWORD src1_sel:WORD_1\n"
    "v_mfma_f32_16x16x32_bf16 v[124:127], a[220:223], v[196:199], v[124:127]\n"
    "v_lshlrev_b32_sdwa v195, 1, v174 dst_sel:DWORD dst_unused:UNUSED_PAD src0_sel:DWORD src1_sel:WORD_1\n"
    "v_or_b32_e32 v187, v187, v203\n"
    "v_mfma_f32_16x16x32_bf16 v[136:139], v[40:43], v[196:199], v[136:139]\n"
    "ds_read_u16 v194, v194\n"
    "v_lshlrev_b32_sdwa v203, 1, v175 dst_sel:DWORD dst_unused:UNUSED_PAD src0_sel:DWORD src1_sel:WORD_1\n"
    "v_mfma_f32_16x16x32_bf16 v[140:143], v[44:47], v[196:199], v[140:143]\n"
    "s_waitcnt lgkmcnt(7)\n"
    "ds_read_u16_d16_hi v202, v202\n"
    "v_cvt_pk_f16_f32 v196, v176, v177\n"
    "v_mfma_f32_16x16x32_bf16 v[168:171], a[96:99], v[144:147], 0\n"
    "v_lshlrev_b32_sdwa v197, 1, v178 dst_sel:DWORD dst_unused:UNUSED_PAD src0_sel:DWORD src1_sel:WORD_1\n"
    "v_lshlrev_b32_sdwa v198, 1, v180 dst_sel:DWORD dst_unused:UNUSED_PAD src0_sel:DWORD src1_sel:WORD_1\n"
    "v_mfma_f32_16x16x32_bf16 v[168:171], a[144:147], v[148:151], v[168:171]\n"
    "ds_read_u16 v195, v195\n"
    "v_lshlrev_b32_sdwa v199, 1, v182 dst_sel:DWORD dst_unused:UNUSED_PAD src0_sel:DWORD src1_sel:WORD_1\n"
    "v_mfma_f32_16x16x32_bf16 v[168:171], a[56:59], v[152:155], v[168:171]\n"
    "v_or_b32_e64 v188, v188, v204\n"
    "v_or_b32_e32 v189, v189, v205\n"
    "v_mfma_f32_16x16x32_bf16 v[172:175], a[100:103], v[144:147], 0\n"
    "s_waitcnt lgkmcnt(7)\n"
    "ds_read_u16_d16_hi v203, v203\n"
    "v_lshlrev_b32_sdwa v205, 1, v179 dst_sel:DWORD dst_unused:UNUSED_PAD src0_sel:DWORD src1_sel:WORD_1\n"
    "v_mfma_f32_16x16x32_bf16 v[172:175], a[148:151], v[148:151], v[172:175]\n"
    "v_cvt_pk_bf16_f32 v96, v96, v97\n"
    "v_cvt_pk_bf16_f32 v97, v98, v99\n"
    "v_mfma_f32_16x16x32_bf16 v[172:175], a[60:63], v[152:155], v[172:175]\n"
    "ds_read_u16 v197, v197\n"
    "v_cvt_pk_bf16_f32 v98, v100, v101\n"
    "v_mfma_f32_16x16x32_bf16 v[176:179], a[96:99], v[156:159], 0\n"
    "s_waitcnt lgkmcnt(7)\n"
    "ds_read_u16_d16_hi v205, v205\n"
    "v_cvt_pk_bf16_f32 v99, v102, v103\n"
    "v_mfma_f32_16x16x32_bf16 v[176:179], a[144:147], v[160:163], v[176:179]\n"
    "v_or_b32_e32 v190, v190, v206\n"
    "v_lshlrev_b32_sdwa v206, 1, v181 dst_sel:DWORD dst_unused:UNUSED_PAD src0_sel:DWORD src1_sel:WORD_1\n"
    "v_mfma_f32_16x16x32_bf16 v[176:179], a[56:59], v[164:167], v[176:179]\n"
    "v_cvt_pk_bf16_f32 v112, v112, v113\n"
    "v_cvt_pk_bf16_f32 v113, v114, v115\n"
    "v_mfma_f32_16x16x32_bf16 v[100:103], a[4:7], v[184:187], 0\n"
    "s_nop 0\n"
    "v_cvt_pk_bf16_f32 v114, v116, v117\n"
    "ds_write_b128 v242, v[96:99] offset:24640\n"
    "v_mfma_f32_16x16x32_bf16 v[96:99], a[0:3], v[184:187], 0\n"
    "s_waitcnt lgkmcnt(6)\n"
    "v_cvt_pk_bf16_f32 v115, v118, v119\n"
    "v_cvt_pk_bf16_f32 v128, v128, v129\n"
    "v_cvt_pk_bf16_f32 v129, v130, v131\n"
    "v_or_b32_e32 v191, v191, v207\n"
    "v_mfma_f32_16x16x32_bf16 v[116:119], a[12:15], v[184:187], 0\n"
    "s_waitcnt lgkmcnt(5)\n"
    "ds_read_u16 v198, v198\n"
    "ds_write_b128 v242, v[112:115] offset:26688\n"
    "v_mfma_f32_16x16x32_bf16 v[112:115], a[8:11], v[184:187], 0\n"
    "v_lshlrev_b32_sdwa v207, 1, v183 dst_sel:DWORD dst_unused:UNUSED_PAD src0_sel:DWORD src1_sel:WORD_1\n"
    "v_cvt_pk_bf16_f32 v130, v132, v133\n"
    "v_mfma_f32_16x16x32_bf16 v[180:183], a[100:103], v[156:159], 0\n"
    "v_or_b32_e64 v193, v193, v201\n"
    "v_cvt_pk_bf16_f32 v131, v134, v135\n"
    "v_mfma_f32_16x16x32_bf16 v[180:183], a[148:151], v[160:163], v[180:183]\n"
    "s_waitcnt lgkmcnt(5)\n"
    "ds_read_u16_d16_hi v206, v206\n"
    "ds_write_b128 v242, v[128:131] offset:28736\n"
    "v_mfma_f32_16x16x32_bf16 v[180:183], a[60:63], v[164:167], v[180:183]\n"
    "v_cvt_pk_bf16_f32 v104, v104, v105\n"
    "v_cvt_pk_bf16_f32 v105, v106, v107\n"
    "v_mfma_f32_16x16x32_bf16 v[128:131], v[0:3], v[184:187], 0\n"
    "s_waitcnt lgkmcnt(4)\n"
    "v_cvt_pk_bf16_f32 v106, v108, v109\n"
    "v_cvt_pk_bf16_f32 v107, v110, v111\n"
    "v_mfma_f32_16x16x32_bf16 v[132:135], v[4:7], v[184:187], 0\n"
    "ds_read_u16 v199, v199\n"
    "ds_write_b128 v242, v[104:107] offset:25664\n"
    "v_mfma_f32_16x16x32_bf16 v[104:107], a[0:3], v[188:191], 0\n"
    "v_or_b32_e64 v194, v194, v202\n"
    "v_cvt_pk_bf16_f32 v120, v120, v121\n"
    "v_mfma_f32_16x16x32_bf16 v[108:111], a[4:7], v[188:191], 0\n"
    "v_cvt_pk_bf16_f32 v121, v122, v123\n"
    "v_cvt_pk_bf16_f32 v122, v124, v125\n"
    "v_cvt_pk_bf16_f32 v123, v126, v127\n"
    "ds_write_b128 v242, v[120:123] offset:27712\n"
    "v_mfma_f32_16x16x32_bf16 v[120:123], a[8:11], v[188:191], 0\n"
    "s_nop 0\n"
    "v_cvt_pk_bf16_f32 v136, v136, v137\n"
    "v_pk_fma_f16 v232, v192, v192, s48\n"
    "v_mfma_f32_16x16x32_bf16 v[124:127], a[12:15], v[188:191], 0\n"
    "s_waitcnt lgkmcnt(3)\n"
    "ds_read_u16_d16_hi v207, v207\n"
    "v_cvt_pk_bf16_f32 v137, v138, v139\n"
    "v_cvt_pk_bf16_f32 v138, v140, v141\n"
    "v_or_b32_e64 v195, v195, v203\n"
    "v_cvt_pk_bf16_f32 v139, v142, v143\n"
    "ds_write_b128 v242, v[136:139] offset:29760\n"
    "v_pk_mul_f16 v232, v192, v232\n"
    "v_mfma_f32_16x16x32_bf16 v[136:139], v[0:3], v[188:191], 0\n"
    "v_exp_f16_e64 v234, v232\n"
    "v_mfma_f32_16x16x32_bf16 v[140:143], v[4:7], v[188:191], 0\n"
    "v_exp_f16_sdwa v234, v232 dst_sel:WORD_1 dst_unused:UNUSED_PRESERVE src0_sel:WORD_1\n"
    "v_or_b32_e64 v197, v197, v205\n"
    "v_pk_fma_f16 v234, v234, s49, s49\n"
    "v_cvt_pk_f16_f32 v184, v168, v169\n"
    "v_lshlrev_b32_sdwa v185, 1, v170 dst_sel:DWORD dst_unused:UNUSED_PAD src0_sel:DWORD src1_sel:WORD_1\n"
    "v_lshlrev_b32_sdwa v201, 1, v171 dst_sel:DWORD dst_unused:UNUSED_PAD src0_sel:DWORD src1_sel:WORD_1\n"
    "v_mfma_f32_16x16x32_bf16 v[168:171], a[104:107], v[144:147], 0\n"
    "ds_read_u16 v185, v185\n"
    "ds_read_u16_d16_hi v201, v201\n"
    "v_mfma_f32_16x16x32_bf16 v[168:171], a[152:155], v[148:151], v[168:171]\n"
    "v_lshlrev_b32_sdwa v186, 1, v172 dst_sel:DWORD dst_unused:UNUSED_PAD src0_sel:DWORD src1_sel:WORD_1\n"
    "v_lshlrev_b32_sdwa v202, 1, v173 dst_sel:DWORD dst_unused:UNUSED_PAD src0_sel:DWORD src1_sel:WORD_1\n"
    "v_lshlrev_b32_sdwa v187, 1, v174 dst_sel:DWORD dst_unused:UNUSED_PAD src0_sel:DWORD src1_sel:WORD_1\n"
    "v_lshlrev_b32_sdwa v203, 1, v175 dst_sel:DWORD dst_unused:UNUSED_PAD src0_sel:DWORD src1_sel:WORD_1\n"
    "v_rcp_f16_e32 v232, v234\n"
    "v_mfma_f32_16x16x32_bf16 v[168:171], a[64:67], v[152:155], v[168:171]\n"
    "s_waitcnt lgkmcnt(2)\n"
    "ds_read_u16 v186, v186\n"
    "ds_read_u16_d16_hi v202, v202\n"
    "v_or_b32_e32 v198, v198, v206\n"
    "v_rcp_f16_sdwa v232, v234 dst_sel:WORD_1 dst_unused:UNUSED_PRESERVE src0_sel:WORD_1\n"
    "v_mfma_f32_16x16x32_bf16 v[172:175], a[108:111], v[144:147], 0\n"
    "s_barrier\n"
    "v_mfma_f32_16x16x32_bf16 v[172:175], a[156:159], v[148:151], v[172:175]\n"
    "ds_read_u16 v187, v187\n"
    "ds_read_u16_d16_hi v203, v203\n"
    "v_cvt_pk_f16_f32 v188, v176, v177\n"
    "v_lshlrev_b32_sdwa v189, 1, v178 dst_sel:DWORD dst_unused:UNUSED_PAD src0_sel:DWORD src1_sel:WORD_1\n"
    "v_lshlrev_b32_sdwa v205, 1, v179 dst_sel:DWORD dst_unused:UNUSED_PAD src0_sel:DWORD src1_sel:WORD_1\n"
    "v_lshlrev_b32_sdwa v190, 1, v180 dst_sel:DWORD dst_unused:UNUSED_PAD src0_sel:DWORD src1_sel:WORD_1\n"
    "v_lshlrev_b32_sdwa v206, 1, v181 dst_sel:DWORD dst_unused:UNUSED_PAD src0_sel:DWORD src1_sel:WORD_1\n"
    "v_lshlrev_b32_sdwa v191, 1, v182 dst_sel:DWORD dst_unused:UNUSED_PAD src0_sel:DWORD src1_sel:WORD_1\n"
    "v_fma_mix_f32 v234, v192, v232, 0 op_sel_hi:[1,1,0]\n"
    "v_mfma_f32_16x16x32_bf16 v[172:175], a[68:71], v[152:155], v[172:175]\n"
    "ds_read_u16 v189, v189\n"
    "v_or_b32_e32 v199, v199, v207\n"
    "v_lshlrev_b32_sdwa v207, 1, v183 dst_sel:DWORD dst_unused:UNUSED_PAD src0_sel:DWORD src1_sel:WORD_1\n"
    "v_fma_mix_f32 v232, v192, v232, 0 op_sel:[1,1,0] op_sel_hi:[1,1,0]\n"
    "v_mfma_f32_16x16x32_bf16 v[176:179], a[104:107], v[156:159], 0\n"
    "s_waitcnt lgkmcnt(5)\n"
    "ds_read_u16_d16_hi v205, v205\n"
    "v_cvt_pk_bf16_f32 v192, v234, v232\n"
    "v_mfma_f32_16x16x32_bf16 v[176:179], a[152:155], v[160:163], v[176:179]\n"
    "ds_read_u16 v190, v190\n"
    "v_pk_fma_f16 v232, v196, v196, s48\n"
    "v_mfma_f32_16x16x32_bf16 v[176:179], a[64:67], v[164:167], v[176:179]\n"
    "ds_read_u16_d16_hi v206, v206\n"
    "v_pk_mul_f16 v232, v196, v232\n"
    "v_mfma_f32_16x16x32_bf16 v[180:183], a[108:111], v[156:159], 0\n"
    "s_waitcnt lgkmcnt(6)\n"
    "ds_read_u16 v191, v191\n"
    "v_exp_f16_e32 v234, v232\n"
    "v_mfma_f32_16x16x32_bf16 v[180:183], a[156:159], v[160:163], v[180:183]\n"
    "ds_read_u16_d16_hi v207, v207\n"
    "v_exp_f16_sdwa v234, v232 dst_sel:WORD_1 dst_unused:UNUSED_PRESERVE src0_sel:WORD_1\n"
    "v_mfma_f32_16x16x32_bf16 v[180:183], a[68:71], v[164:167], v[180:183]\n"
    "v_pk_fma_f16 v234, v234, s49, s49\n"
    "v_or_b32_e64 v185, v185, v201\n"
    "v_mfma_f32_16x16x32_bf16 v[96:99], a[16:19], v[192:195], v[96:99]\n"
    "v_rcp_f16_e32 v232, v234\n"
    "v_mfma_f32_16x16x32_bf16 v[100:103], a[20:23], v[192:195], v[100:103]\n"
    "s_waitcnt lgkmcnt(6)\n"
    "v_rcp_f16_sdwa v232, v234 dst_sel:WORD_1 dst_unused:UNUSED_PRESERVE src0_sel:WORD_1\n"
    "v_mfma_f32_16x16x32_bf16 v[112:115], a[24:27], v[192:195], v[112:115]\n"
    "v_fma_mix_f32 v234, v196, v232, 0 op_sel_hi:[1,1,0]\n"
    "v_fma_mix_f32 v232, v196, v232, 0 op_sel:[1,1,0] op_sel_hi:[1,1,0]\n"
    "v_mfma_f32_16x16x32_bf16 v[116:119], a[28:31], v[192:195], v[116:119]\n"
    "v_or_b32_e64 v186, v186, v202\n"
    "v_cvt_pk_bf16_f32 v196, v234, v232\n"
    "v_mfma_f32_16x16x32_bf16 v[128:131], v[8:11], v[192:195], v[128:131]\n"
    "v_pk_fma_f16 v232, v184, v184, s48\n"
    "v_or_b32_e32 v187, v187, v203\n"
    "v_mfma_f32_16x16x32_bf16 v[132:135], v[12:15], v[192:195], v[132:135]\n"
    "s_waitcnt lgkmcnt(4)\n"
    "v_pk_mul_f16 v232, v184, v232\n"
    "v_or_b32_e64 v189, v189, v205\n"
    "v_mfma_f32_16x16x32_bf16 v[104:107], a[16:19], v[196:199], v[104:107]\n"
    "v_exp_f16_e32 v234, v232\n"
    "v_mfma_f32_16x16x32_bf16 v[108:111], a[20:23], v[196:199], v[108:111]\n"
    "v_exp_f16_sdwa v234, v232 dst_sel:WORD_1 dst_unused:UNUSED_PRESERVE src0_sel:WORD_1\n"
    "v_mfma_f32_16x16x32_bf16 v[120:123], a[24:27], v[196:199], v[120:123]\n"
    "s_waitcnt vmcnt(7) lgkmcnt(2)\n"
    "v_pk_fma_f16 v234, v234, s49, s49\n"
    "v_lshlrev_b32_sdwa v192, 1, v168 dst_sel:DWORD dst_unused:UNUSED_PAD src0_sel:DWORD src1_sel:WORD_1\n"
    "v_mfma_f32_16x16x32_bf16 v[124:127], a[28:31], v[196:199], v[124:127]\n"
    "ds_read_u16 v192, v192\n"
    "v_lshlrev_b32_sdwa v200, 1, v169 dst_sel:DWORD dst_unused:UNUSED_PAD src0_sel:DWORD src1_sel:WORD_1\n"
    "v_mfma_f32_16x16x32_bf16 v[136:139], v[8:11], v[196:199], v[136:139]\n"
    "ds_read_u16_d16_hi v200, v200\n"
    "v_lshlrev_b32_sdwa v193, 1, v170 dst_sel:DWORD dst_unused:UNUSED_PAD src0_sel:DWORD src1_sel:WORD_1\n"
    "v_mfma_f32_16x16x32_bf16 v[140:143], v[12:15], v[196:199], v[140:143]\n"
    "s_waitcnt vmcnt(6)\n"
    "ds_read_u16 v193, v193\n"
    "v_lshlrev_b32_sdwa v201, 1, v171 dst_sel:DWORD dst_unused:UNUSED_PAD src0_sel:DWORD src1_sel:WORD_1\n"
    "v_smfmac_f32_32x32x32_bf16 v[112:127], a[248:251], a[232:239], v241\n"
    "s_waitcnt lgkmcnt(3)\n"
    "ds_read_u16_d16_hi v201, v201\n"
    "ds_read_b128 a[232:235], v243 offset:24640\n"
    "v_lshlrev_b32_sdwa v194, 1, v172 dst_sel:DWORD dst_unused:UNUSED_PAD src0_sel:DWORD src1_sel:WORD_1\n"
    "v_lshlrev_b32_sdwa v202, 1, v173 dst_sel:DWORD dst_unused:UNUSED_PAD src0_sel:DWORD src1_sel:WORD_1\n"
    "v_lshlrev_b32_sdwa v195, 1, v174 dst_sel:DWORD dst_unused:UNUSED_PAD src0_sel:DWORD src1_sel:WORD_1\n"
    "v_smfmac_f32_32x32x32_bf16 v[96:111], a[248:251], a[224:231], v241\n"
    "ds_read_u16 v194, v194\n"
    "v_lshlrev_b32_sdwa v203, 1, v175 dst_sel:DWORD dst_unused:UNUSED_PAD src0_sel:DWORD src1_sel:WORD_1\n"
    "v_rcp_f16_e64 v232, v234\n"
    "v_or_b32_e64 v190, v190, v206\n"
    "v_smfmac_f32_32x32x32_bf16 v[128:143], a[248:251], v[208:215], v241\n"
    "ds_read_u16_d16_hi v202, v202\n"
    "ds_read_b128 a[236:239], v243 offset:25664\n"
    "v_rcp_f16_sdwa v232, v234 dst_sel:WORD_1 dst_unused:UNUSED_PRESERVE src0_sel:WORD_1\n"
    "v_lshlrev_b32_sdwa v196, 1, v176 dst_sel:DWORD dst_unused:UNUSED_PAD src0_sel:DWORD src1_sel:WORD_1\n"
    "v_mfma_f32_16x16x32_bf16 v[168:171], a[112:115], v[144:147], 0\n"
    "ds_read_u16 v195, v195\n"
    "v_lshlrev_b32_sdwa v204, 1, v177 dst_sel:DWORD dst_unused:UNUSED_PAD src0_sel:DWORD src1_sel:WORD_1\n"
    "v_mfma_f32_16x16x32_bf16 v[168:171], a[160:163], v[148:151], v[168:171]\n"
    "v_lshlrev_b32_sdwa v197, 1, v178 dst_sel:DWORD dst_unused:UNUSED_PAD src0_sel:DWORD src1_sel:WORD_1\n"
    "v_lshlrev_b32_sdwa v205, 1, v179 dst_sel:DWORD dst_unused:UNUSED_PAD src0_sel:DWORD src1_sel:WORD_1\n"
    "v_mfma_f32_16x16x32_bf16 v[168:171], a[72:75], v[152:155], v[168:171]\n"
    "ds_read_u16_d16_hi v203, v203\n"
    "v_lshlrev_b32_sdwa v198, 1, v180 dst_sel:DWORD dst_unused:UNUSED_PAD src0_sel:DWORD src1_sel:WORD_1\n"
    "v_mfma_f32_16x16x32_bf16 v[172:175], a[116:119], v[144:147], 0\n"
    "ds_read_u16 v196, v196\n"
    "v_lshlrev_b32_sdwa v206, 1, v181 dst_sel:DWORD dst_unused:UNUSED_PAD src0_sel:DWORD src1_sel:WORD_1\n"
    "v_mfma_f32_16x16x32_bf16 v[172:175], a[164:167], v[148:151], v[172:175]\n"
    "v_lshlrev_b32_sdwa v199, 1, v182 dst_sel:DWORD dst_unused:UNUSED_PAD src0_sel:DWORD src1_sel:WORD_1\n"
    "v_fma_mix_f32 v234, v184, v232, 0 op_sel_hi:[1,1,0]\n"
    "v_mfma_f32_16x16x32_bf16 v[172:175], v[52:55], v[152:155], v[172:175]\n"
    "s_waitcnt lgkmcnt(8)\n"
    "ds_read_u16_d16_hi v204, v204\n"
    "v_or_b32_e32 v191, v191, v207\n"
    "v_lshlrev_b32_sdwa v207, 1, v183 dst_sel:DWORD dst_unused:UNUSED_PAD src0_sel:DWORD src1_sel:WORD_1\n"
    "v_fma_mix_f32 v232, v184, v232, 0 op_sel:[1,1,0] op_sel_hi:[1,1,0]\n"
    "v_mfma_f32_16x16x32_bf16 v[176:179], a[112:115], v[156:159], 0\n"
    "ds_read_u16 v197, v197\n"
    "v_cvt_pk_bf16_f32 v184, v234, v232\n"
    "v_mfma_f32_16x16x32_bf16 v[176:179], a[160:163], v[160:163], v[176:179]\n"
    "v_pk_fma_f16 v232, v188, v188, s48\n"
    "v_or_b32_e32 v192, v192, v200\n"
    "v_mfma_f32_16x16x32_bf16 v[176:179], a[72:75], v[164:167], v[176:179]\n"
    "s_waitcnt lgkmcnt(8)\n"
    "ds_read_u16_d16_hi v205, v205\n"
    "v_pk_mul_f16 v232, v188, v232\n"
    "v_mfma_f32_16x16x32_bf16 v[180:183], a[116:119], v[156:159], 0\n"
    "v_exp_f16_e32 v234, v232\n"
    "v_mfma_f32_16x16x32_bf16 v[180:183], a[164:167], v[160:163], v[180:183]\n"
    "s_waitcnt lgkmcnt(6)\n"
    "ds_read_u16 v198, v198\n"
    "v_exp_f16_sdwa v234, v232 dst_sel:WORD_1 dst_unused:UNUSED_PRESERVE src0_sel:WORD_1\n"
    "v_smfmac_f32_32x32x32_bf16 v[96:111], a[248:251], a[232:239], v241\n"
    "ds_read_u16_d16_hi v206, v206\n"
    "v_pk_fma_f16 v234, v234, s49, s49\n"
    "v_or_b32_e64 v193, v193, v201\n"
    "v_rcp_f16_e64 v232, v234\n"
    "v_mfma_f32_16x16x32_bf16 v[180:183], v[52:55], v[164:167], v[180:183]\n"
    "ds_read_u16 v199, v199\n"
    "v_rcp_f16_sdwa v232, v234 dst_sel:WORD_1 dst_unused:UNUSED_PRESERVE src0_sel:WORD_1\n"
    "v_mfma_f32_16x16x32_bf16 v[112:115], a[40:43], v[184:187], v[112:115]\n"
    "s_nop 0\n"
    "v_fma_mix_f32 v234, v188, v232, 0 op_sel_hi:[1,1,0]\n"
    "v_fma_mix_f32 v232, v188, v232, 0 op_sel:[1,1,0] op_sel_hi:[1,1,0]\n"
    "v_mfma_f32_16x16x32_bf16 v[96:99], a[32:35], v[184:187], v[96:99]\n"
    "s_waitcnt lgkmcnt(7)\n"
    "ds_read_u16_d16_hi v207, v207\n"
    "v_cvt_pk_bf16_f32 v188, v234, v232\n"
    "v_mfma_f32_16x16x32_bf16 v[100:103], a[36:39], v[184:187], v[100:103]\n"
    "ds_read_b128 a[232:235], v243 offset:26688\n"
    "v_lshlrev_b32_sdwa v201, 1, v171 dst_sel:DWORD dst_unused:UNUSED_PAD src0_sel:DWORD src1_sel:WORD_1\n"
    "v_mfma_f32_16x16x32_bf16 v[116:119], a[44:47], v[184:187], v[116:119]\n"
    "ds_read_b128 a[236:239], v243 offset:27712\n"
    "v_or_b32_e32 v194, v194, v202\n"
    "v_mfma_f32_16x16x32_bf16 v[128:131], v[16:19], v[184:187], v[128:131]\n"
    "s_waitcnt lgkmcnt(8)\n"
    "ds_read_u16_d16_hi v201, v201\n"
    "v_lshlrev_b32_sdwa v202, 1, v173 dst_sel:DWORD dst_unused:UNUSED_PAD src0_sel:DWORD src1_sel:WORD_1\n"
    "v_mfma_f32_16x16x32_bf16 v[132:135], v[20:23], v[184:187], v[132:135]\n"
    "v_cvt_pk_f16_f32 v184, v168, v169\n"
    "v_lshlrev_b32_sdwa v185, 1, v170 dst_sel:DWORD dst_unused:UNUSED_PAD src0_sel:DWORD src1_sel:WORD_1\n"
    "v_mfma_f32_16x16x32_bf16 v[104:107], a[32:35], v[188:191], v[104:107]\n"
    "ds_read_u16 v185, v185\n"
    "v_lshlrev_b32_sdwa v186, 1, v172 dst_sel:DWORD dst_unused:UNUSED_PAD src0_sel:DWORD src1_sel:WORD_1\n"
    "v_mfma_f32_16x16x32_bf16 v[108:111], a[36:39], v[188:191], v[108:111]\n"
    "v_lshlrev_b32_sdwa v187, 1, v174 dst_sel:DWORD dst_unused:UNUSED_PAD src0_sel:DWORD src1_sel:WORD_1\n"
    "v_or_b32_e32 v195, v195, v203\n"
    "v_mfma_f32_16x16x32_bf16 v[120:123], a[40:43], v[188:191], v[120:123]\n"
    "s_waitcnt lgkmcnt(8)\n"
    "ds_read_u16 v186, v186\n"
    "v_lshlrev_b32_sdwa v203, 1, v175 dst_sel:DWORD dst_unused:UNUSED_PAD src0_sel:DWORD src1_sel:WORD_1\n"
    "v_mfma_f32_16x16x32_bf16 v[124:127], a[44:47], v[188:191], v[124:127]\n"
    "v_or_b32_e64 v196, v196, v204\n"
    "v_or_b32_e32 v197, v197, v205\n"
    "v_mfma_f32_16x16x32_bf16 v[136:139], v[16:19], v[188:191], v[136:139]\n"
    "ds_read_u16_d16_hi v202, v202\n"
    "v_lshlrev_b32_sdwa v205, 1, v179 dst_sel:DWORD dst_unused:UNUSED_PAD src0_sel:DWORD src1_sel:WORD_1\n"
    "v_mfma_f32_16x16x32_bf16 v[140:143], v[20:23], v[188:191], v[140:143]\n"
    "s_waitcnt lgkmcnt(8)\n"
    "ds_read_u16 v187, v187\n"
    "v_cvt_pk_f16_f32 v188, v176, v177\n"
    "v_mfma_f32_16x16x32_bf16 v[168:171], a[120:123], v[144:147], 0\n"
    "v_lshlrev_b32_sdwa v189, 1, v178 dst_sel:DWORD dst_unused:UNUSED_PAD src0_sel:DWORD src1_sel:WORD_1\n"
    "v_lshlrev_b32_sdwa v190, 1, v180 dst_sel:DWORD dst_unused:UNUSED_PAD src0_sel:DWORD src1_sel:WORD_1\n"
    "v_mfma_f32_16x16x32_bf16 v[168:171], a[168:171], v[148:151], v[168:171]\n"
    "ds_read_u16_d16_hi v203, v203\n"
    "v_lshlrev_b32_sdwa v191, 1, v182 dst_sel:DWORD dst_unused:UNUSED_PAD src0_sel:DWORD src1_sel:WORD_1\n"
    "v_mfma_f32_16x16x32_bf16 v[168:171], a[76:79], v[152:155], v[168:171]\n"
    "v_or_b32_e64 v198, v198, v206\n"
    "v_lshlrev_b32_sdwa v206, 1, v181 dst_sel:DWORD dst_unused:UNUSED_PAD src0_sel:DWORD src1_sel:WORD_1\n"
    "v_mfma_f32_16x16x32_bf16 v[172:175], a[124:127], v[144:147], 0\n"
    "s_waitcnt lgkmcnt(6)\n"
    "ds_read_u16 v189, v189\n"
    "v_or_b32_e32 v199, v199, v207\n"
    "v_smfmac_f32_32x32x32_bf16 v[112:127], a[248:251], a[232:239], v241\n"
    "ds_read_b128 a[232:235], v243 offset:28736\n"
    "v_lshlrev_b32_sdwa v207, 1, v183 dst_sel:DWORD dst_unused:UNUSED_PAD src0_sel:DWORD src1_sel:WORD_1\n"
    "v_pk_fma_f16 v232, v184, v184, s48\n"
    "v_pk_mul_f16 v232, v184, v232\n"
    "v_exp_f16_e64 v234, v232\n"
    "v_mfma_f32_16x16x32_bf16 v[172:175], a[172:175], v[148:151], v[172:175]\n"
    "ds_read_u16_d16_hi v205, v205\n"
    "v_exp_f16_sdwa v234, v232 dst_sel:WORD_1 dst_unused:UNUSED_PRESERVE src0_sel:WORD_1\n"
    "v_mfma_f32_16x16x32_bf16 v[172:175], v[60:63], v[152:155], v[172:175]\n"
    "ds_read_b128 a[236:239], v243 offset:29760\n"
    "v_pk_fma_f16 v234, v234, s49, s49\n"
    "v_mfma_f32_16x16x32_bf16 v[176:179], a[120:123], v[156:159], 0\n"
    "s_waitcnt lgkmcnt(8)\n"
    "ds_read_u16 v190, v190\n"
    "v_or_b32_e32 v185, v185, v201\n"
    "v_mfma_f32_16x16x32_bf16 v[176:179], a[168:171], v[160:163], v[176:179]\n"
    "v_lshlrev_b32_sdwa v201, 1, v171 dst_sel:DWORD dst_unused:UNUSED_PAD src0_sel:DWORD src1_sel:WORD_1\n"
    "v_rcp_f16_e64 v232, v234\n"
    "v_mfma_f32_16x16x32_bf16 v[176:179], a[76:79], v[164:167], v[176:179]\n"
    "ds_read_u16_d16_hi v206, v206\n"
    "v_rcp_f16_sdwa v232, v234 dst_sel:WORD_1 dst_unused:UNUSED_PRESERVE src0_sel:WORD_1\n"
    "v_mfma_f32_16x16x32_bf16 v[180:183], a[124:127], v[156:159], 0\n"
    "s_waitcnt lgkmcnt(8)\n"
    "ds_read_u16 v191, v191\n"
    "v_or_b32_e32 v186, v186, v202\n"
    "v_mfma_f32_16x16x32_bf16 v[180:183], a[172:175], v[160:163], v[180:183]\n"
    "v_lshlrev_b32_sdwa v202, 1, v173 dst_sel:DWORD dst_unused:UNUSED_PAD src0_sel:DWORD src1_sel:WORD_1\n"
    "v_fma_mix_f32 v234, v184, v232, 0 op_sel_hi:[1,1,0]\n"
    "v_mfma_f32_16x16x32_bf16 v[180:183], v[60:63], v[164:167], v[180:183]\n"
    "ds_read_u16_d16_hi v207, v207\n"
    "v_fma_mix_f32 v232, v184, v232, 0 op_sel:[1,1,0] op_sel_hi:[1,1,0]\n"
    "v_mfma_f32_16x16x32_bf16 v[96:99], a[176:179], v[192:195], v[96:99]\n"
    "s_waitcnt lgkmcnt(8)\n"
    "ds_read_u16_d16_hi v201, v201\n"
    "v_or_b32_e32 v187, v187, v203\n"
    "v_mfma_f32_16x16x32_bf16 v[100:103], a[180:183], v[192:195], v[100:103]\n"
    "v_lshlrev_b32_sdwa v203, 1, v175 dst_sel:DWORD dst_unused:UNUSED_PAD src0_sel:DWORD src1_sel:WORD_1\n"
    "v_cvt_pk_bf16_f32 v184, v234, v232\n"
    "v_mfma_f32_16x16x32_bf16 v[112:115], a[184:187], v[192:195], v[112:115]\n"
    "ds_read_u16_d16_hi v202, v202\n"
    "v_pk_fma_f16 v232, v188, v188, s48\n"
    "v_mfma_f32_16x16x32_bf16 v[116:119], a[188:191], v[192:195], v[116:119]\n"
    "v_pk_mul_f16 v232, v188, v232\n"
    "v_exp_f16_e32 v234, v232\n"
    "v_mfma_f32_16x16x32_bf16 v[128:131], v[24:27], v[192:195], v[128:131]\n"
    "s_waitcnt lgkmcnt(6)\n"
    "ds_read_u16_d16_hi v203, v203\n"
    "v_or_b32_e64 v189, v189, v205\n"
    "v_mfma_f32_16x16x32_bf16 v[132:135], v[28:31], v[192:195], v[132:135]\n"
    "v_cvt_pk_f16_f32 v192, v168, v169\n"
    "v_lshlrev_b32_sdwa v193, 1, v170 dst_sel:DWORD dst_unused:UNUSED_PAD src0_sel:DWORD src1_sel:WORD_1\n"
    "v_mfma_f32_16x16x32_bf16 v[104:107], a[176:179], v[196:199], v[104:107]\n"
    "ds_read_u16 v193, v193\n"
    "v_lshlrev_b32_sdwa v194, 1, v172 dst_sel:DWORD dst_unused:UNUSED_PAD src0_sel:DWORD src1_sel:WORD_1\n"
    "v_mfma_f32_16x16x32_bf16 v[108:111], a[180:183], v[196:199], v[108:111]\n"
    "ds_read_u16 v194, v194\n"
    "v_lshlrev_b32_sdwa v195, 1, v174 dst_sel:DWORD dst_unused:UNUSED_PAD src0_sel:DWORD src1_sel:WORD_1\n"
    "v_mfma_f32_16x16x32_bf16 v[120:123], a[184:187], v[196:199], v[120:123]\n"
    "v_lshlrev_b32_sdwa v205, 1, v179 dst_sel:DWORD dst_unused:UNUSED_PAD src0_sel:DWORD src1_sel:WORD_1\n"
    "v_exp_f16_sdwa v234, v232 dst_sel:WORD_1 dst_unused:UNUSED_PRESERVE src0_sel:WORD_1\n"
    "v_mfma_f32_16x16x32_bf16 v[124:127], a[188:191], v[196:199], v[124:127]\n"
    "s_waitcnt lgkmcnt(7)\n"
    "ds_read_u16 v195, v195\n"
    "v_or_b32_e32 v190, v190, v206\n"
    "v_mfma_f32_16x16x32_bf16 v[136:139], v[24:27], v[196:199], v[136:139]\n"
    "ds_read_u16_d16_hi v205, v205\n"
    "v_lshlrev_b32_sdwa v206, 1, v181 dst_sel:DWORD dst_unused:UNUSED_PAD src0_sel:DWORD src1_sel:WORD_1\n"
    "v_mfma_f32_16x16x32_bf16 v[140:143], v[28:31], v[196:199], v[140:143]\n"
    "s_nop 0\n"
    "v_cvt_pk_f16_f32 v196, v176, v177\n"
    "v_lshlrev_b32_sdwa v197, 1, v178 dst_sel:DWORD dst_unused:UNUSED_PAD src0_sel:DWORD src1_sel:WORD_1\n"
    "v_mfma_f32_16x16x32_bf16 v[96:99], a[192:195], v[184:187], v[96:99]\n"
    "s_waitcnt lgkmcnt(7)\n"
    "ds_read_u16 v197, v197\n"
    "v_lshlrev_b32_sdwa v198, 1, v180 dst_sel:DWORD dst_unused:UNUSED_PAD src0_sel:DWORD src1_sel:WORD_1\n"
    "v_smfmac_f32_32x32x32_bf16 v[128:143], a[248:251], a[232:239], v241\n"
    "v_lshlrev_b32_sdwa v199, 1, v182 dst_sel:DWORD dst_unused:UNUSED_PAD src0_sel:DWORD src1_sel:WORD_1\n"
    "v_or_b32_e64 v191, v191, v207\n"
    "v_lshlrev_b32_sdwa v207, 1, v183 dst_sel:DWORD dst_unused:UNUSED_PAD src0_sel:DWORD src1_sel:WORD_1\n"
    "v_pk_fma_f16 v234, v234, s49, s49\n"
    "v_rcp_f16_e64 v232, v234\n"
    "v_mfma_f32_16x16x32_bf16 v[100:103], a[196:199], v[184:187], v[100:103]\n"
    "ds_read_u16 v198, v198\n"
    "ds_read_u16_d16_hi v206, v206\n"
    "v_mfma_f32_16x16x32_bf16 v[112:115], a[200:203], v[184:187], v[112:115]\n"
    "v_rcp_f16_sdwa v232, v234 dst_sel:WORD_1 dst_unused:UNUSED_PRESERVE src0_sel:WORD_1\n"
    "v_mfma_f32_16x16x32_bf16 v[116:119], a[204:207], v[184:187], v[116:119]\n"
    "ds_read_u16 v199, v199\n"
    "v_fma_mix_f32 v234, v188, v232, 0 op_sel_hi:[1,1,0]\n"
    "v_mfma_f32_16x16x32_bf16 v[128:131], v[32:35], v[184:187], v[128:131]\n"
    "v_fma_mix_f32 v232, v188, v232, 0 op_sel:[1,1,0] op_sel_hi:[1,1,0]\n"
    "v_cvt_pk_bf16_f32 v188, v234, v232\n"
    "v_mfma_f32_16x16x32_bf16 v[132:135], v[36:39], v[184:187], v[132:135]\n"
    "s_waitcnt lgkmcnt(7)\n"
    "ds_read_u16_d16_hi v207, v207\n"
    "v_or_b32_e32 v193, v193, v201\n"
    "v_mfma_f32_16x16x32_bf16 v[104:107], a[192:195], v[188:191], v[104:107]\n"
    "v_pk_fma_f16 v232, v192, v192, s48\n"
    "v_pk_mul_f16 v232, v192, v232\n"
    "v_mfma_f32_16x16x32_bf16 v[108:111], a[196:199], v[188:191], v[108:111]\n"
    "s_waitcnt lgkmcnt(7)\n"
    "v_or_b32_e32 v194, v194, v202\n"
    "v_exp_f16_e64 v234, v232\n"
    "v_mfma_f32_16x16x32_bf16 v[120:123], a[200:203], v[188:191], v[120:123]\n"
    "s_waitcnt lgkmcnt(6)\n"
    "v_or_b32_e32 v195, v195, v203\n"
    "v_exp_f16_sdwa v234, v232 dst_sel:WORD_1 dst_unused:UNUSED_PRESERVE src0_sel:WORD_1\n"
    "v_mfma_f32_16x16x32_bf16 v[124:127], a[204:207], v[188:191], v[124:127]\n"
    "v_pk_fma_f16 v234, v234, s49, s49\n"
    "v_rcp_f16_e64 v232, v234\n"
    "v_mfma_f32_16x16x32_bf16 v[136:139], v[32:35], v[188:191], v[136:139]\n"
    "v_rcp_f16_sdwa v232, v234 dst_sel:WORD_1 dst_unused:UNUSED_PRESERVE src0_sel:WORD_1\n"
    "v_mfma_f32_16x16x32_bf16 v[140:143], v[36:39], v[188:191], v[140:143]\n"
    "s_waitcnt lgkmcnt(4)\n"
    "v_or_b32_e32 v197, v197, v205\n"
    "v_fma_mix_f32 v234, v192, v232, 0 op_sel_hi:[1,1,0]\n"
    "v_fma_mix_f32 v232, v192, v232, 0 op_sel:[1,1,0] op_sel_hi:[1,1,0]\n"
    "v_cvt_pk_bf16_f32 v192, v234, v232\n"
    "v_pk_fma_f16 v232, v196, v196, s48\n"
    "v_pk_mul_f16 v232, v196, v232\n"
    "v_exp_f16_e64 v234, v232\n"
    "v_mfma_f32_16x16x32_bf16 v[96:99], a[208:211], v[192:195], v[96:99]\n"
    "v_exp_f16_sdwa v234, v232 dst_sel:WORD_1 dst_unused:UNUSED_PRESERVE src0_sel:WORD_1\n"
    "v_mfma_f32_16x16x32_bf16 v[100:103], a[212:215], v[192:195], v[100:103]\n"
    "s_waitcnt lgkmcnt(2)\n"
    "v_or_b32_e32 v198, v198, v206\n"
    "v_pk_fma_f16 v234, v234, s49, s49\n"
    "v_rcp_f16_e64 v232, v234\n"
    "v_mfma_f32_16x16x32_bf16 v[112:115], a[216:219], v[192:195], v[112:115]\n"
    "v_rcp_f16_sdwa v232, v234 dst_sel:WORD_1 dst_unused:UNUSED_PRESERVE src0_sel:WORD_1\n"
    "v_mfma_f32_16x16x32_bf16 v[116:119], a[220:223], v[192:195], v[116:119]\n"
    "v_fma_mix_f32 v234, v196, v232, 0 op_sel_hi:[1,1,0]\n"
    "v_fma_mix_f32 v232, v196, v232, 0 op_sel:[1,1,0] op_sel_hi:[1,1,0]\n"
    "v_mfma_f32_16x16x32_bf16 v[128:131], v[40:43], v[192:195], v[128:131]\n"
    "s_waitcnt lgkmcnt(0)\n"
    "v_or_b32_e32 v199, v199, v207\n"
    "v_cvt_pk_bf16_f32 v196, v234, v232\n"
    "v_mfma_f32_16x16x32_bf16 v[132:135], v[44:47], v[192:195], v[132:135]\n"
    "v_cvt_pk_bf16_f32 v96, v96, v97\n"
    "v_cvt_pk_bf16_f32 v97, v98, v99\n"
    "v_mfma_f32_16x16x32_bf16 v[104:107], a[208:211], v[196:199], v[104:107]\n"
    "v_cvt_pk_bf16_f32 v98, v100, v101\n"
    "v_cvt_pk_bf16_f32 v99, v102, v103\n"
    "v_mfma_f32_16x16x32_bf16 v[108:111], a[212:215], v[196:199], v[108:111]\n"
    "ds_write_b128 v243, v[96:99] offset:24640\n"
    "v_mfma_f32_16x16x32_bf16 v[120:123], a[216:219], v[196:199], v[120:123]\n"
    "v_cvt_pk_bf16_f32 v112, v112, v113\n"
    "v_cvt_pk_bf16_f32 v113, v114, v115\n"
    "v_mfma_f32_16x16x32_bf16 v[124:127], a[220:223], v[196:199], v[124:127]\n"
    "v_cvt_pk_bf16_f32 v114, v116, v117\n"
    "v_cvt_pk_bf16_f32 v115, v118, v119\n"
    "v_mfma_f32_16x16x32_bf16 v[136:139], v[40:43], v[196:199], v[136:139]\n"
    "ds_read_b128 a[224:227], v249 offset:0\n"
    "ds_write_b128 v243, v[112:115] offset:26688\n"
    "v_mfma_f32_16x16x32_bf16 v[140:143], v[44:47], v[196:199], v[140:143]\n"
    "v_cvt_pk_bf16_f32 v128, v128, v129\n"
    "v_cvt_pk_bf16_f32 v129, v130, v131\n"
    "v_cvt_pk_bf16_f32 v130, v132, v133\n"
    "v_cvt_pk_bf16_f32 v131, v134, v135\n"
    "v_cvt_pk_bf16_f32 v104, v104, v105\n"
    "v_cvt_pk_bf16_f32 v105, v106, v107\n"
    "v_cvt_pk_bf16_f32 v106, v108, v109\n"
    "ds_read_b128 a[228:231], v249 offset:2048\n"
    "ds_write_b128 v243, v[128:131] offset:28736\n"
    "v_cvt_pk_bf16_f32 v107, v110, v111\n"
    "v_cvt_pk_bf16_f32 v120, v120, v121\n"
    "v_cvt_pk_bf16_f32 v121, v122, v123\n"
    "v_cvt_pk_bf16_f32 v122, v124, v125\n"
    "v_cvt_pk_bf16_f32 v123, v126, v127\n"
    "v_cvt_pk_bf16_f32 v136, v136, v137\n"
    "v_cvt_pk_bf16_f32 v137, v138, v139\n"
    "v_cvt_pk_bf16_f32 v138, v140, v141\n"
    "ds_read_b128 a[232:235], v249 offset:4096\n"
    "s_waitcnt lgkmcnt(4)\n"
    "buffer_store_dwordx4 a[224:227], v255, s[44:47], s58 offen offset:0 sc1\n"
    "ds_write_b128 v243, v[104:107] offset:25664\n"
    "v_cvt_pk_bf16_f32 v139, v142, v143\n"
    "ds_read_b128 a[236:239], v249 offset:1024\n"
    "ds_write_b128 v243, v[120:123] offset:27712\n"
    "ds_read_b128 a[252:255], v249 offset:3072\n"
    "ds_write_b128 v243, v[136:139] offset:29760\n"
    "s_waitcnt lgkmcnt(4)\n"
    "buffer_store_dwordx4 a[228:231], v255, s[44:47], s58 offen offset:64 sc1\n"
    "ds_read_b128 a[240:243], v249 offset:5120\n"
    "buffer_store_dwordx4 a[232:235], v255, s[44:47], s58 offen offset:128 sc1\n"
    "s_waitcnt lgkmcnt(3)\n"
    "buffer_store_dwordx4 a[236:239], v255, s[44:47], s89 offen offset:0 sc1\n"
    "s_waitcnt lgkmcnt(2)\n"
    "buffer_store_dwordx4 a[252:255], v255, s[44:47], s89 offen offset:64 sc1\n"
    "s_waitcnt lgkmcnt(0)\n"
    "buffer_store_dwordx4 a[240:243], v255, s[44:47], s89 offen offset:128 sc1\n"
    "97:\n"
    "98:\n"
    "s_waitcnt vmcnt(0) lgkmcnt(0)\n"
    ".Lreplk_end:\n"
    : "+{s66}"(flg), "+{v208}"(tid), "+{s65}"(blk), "+{s[98:99]}"(ka)
    : "{s[96:97]}"(CTAB)
    : "memory", "scc", "vcc", "m0", "v0", "v1", "v2", "v3", "v4", "v5", "v6", "v7", "v8", "v9", "v10", "v11", "v12", "v13", "v14", "v15", "v16", "v17", "v18", "v19", "v20", "v21", "v22", "v23", "v24", "v25", "v26", "v27", "v28", "v29", "v30", "v31", "v32", "v33", "v34", "v35", "v36", "v37", "v38", "v39", "v40", "v41", "v42", "v43", "v44", "v45", "v46", "v47", "v48", "v49", "v50", "v51", "v52", "v53", "v54", "v55", "v56", "v57", "v58", "v59", "v60", "v61", "v62", "v63", "v64", "v65", "v66", "v67", "v68", "v69", "v70", "v71", "v72", "v73", "v74", "v75", "v76", "v77", "v78", "v79", "v80", "v81", "v82", "v83", "v84", "v85", "v86", "v87", "v88", "v89", "v90", "v91", "v92", "v93", "v94", "v95", "v96", "v97", "v98", "v99", "v100", "v101", "v102", "v103", "v104", "v105", "v106", "v107", "v108", "v109", "v110", "v111", "v112", "v113", "v114", "v115", "v116", "v117", "v118", "v119", "v120", "v121", "v122", "v123", "v124", "v125", "v126", "v127", "v128", "v129", "v130", "v131", "v132", "v133", "v134", "v135", "v136", "v137", "v138", "v139", "v140", "v141", "v142", "v143", "v144", "v145", "v146", "v147", "v148", "v149", "v150", "v151", "v152", "v153", "v154", "v155", "v156", "v157", "v158", "v159", "v160", "v161", "v162", "v163", "v164", "v165", "v166", "v167", "v168", "v169", "v170", "v171", "v172", "v173", "v174", "v175", "v176", "v177", "v178", "v179", "v180", "v181", "v182", "v183", "v184", "v185", "v186", "v187", "v188", "v189", "v190", "v191", "v192", "v193", "v194", "v195", "v196", "v197", "v198", "v199", "v200", "v201", "v202", "v203", "v204", "v205", "v206", "v207", "v209", "v210", "v211", "v212", "v213", "v214", "v215", "v216", "v217", "v218", "v219", "v220", "v221", "v222", "v223", "v224", "v225", "v226", "v227", "v228", "v229", "v230", "v231", "v232", "v233", "v234", "v235", "v236", "v237", "v238", "v239", "v240", "v241", "v242", "v243", "v244", "v245", "v246", "v247", "v248", "v249", "v250", "v251", "v252", "v253", "v254", "v255", "a0", "a1", "a2", "a3", "a4", "a5", "a6", "a7", "a8", "a9", "a10", "a11", "a12", "a13", "a14", "a15", "a16", "a17", "a18", "a19", "a20", "a21", "a22", "a23", "a24", "a25", "a26", "a27", "a28", "a29", "a30", "a31", "a32", "a33", "a34", "a35", "a36", "a37", "a38", "a39", "a40", "a41", "a42", "a43", "a44", "a45", "a46", "a47", "a48", "a49", "a50", "a51", "a52", "a53", "a54", "a55", "a56", "a57", "a58", "a59", "a60", "a61", "a62", "a63", "a64", "a65", "a66", "a67", "a68", "a69", "a70", "a71", "a72", "a73", "a74", "a75", "a76", "a77", "a78", "a79", "a80", "a81", "a82", "a83", "a84", "a85", "a86", "a87", "a88", "a89", "a90", "a91", "a92", "a93", "a94", "a95", "a96", "a97", "a98", "a99", "a100", "a101", "a102", "a103", "a104", "a105", "a106", "a107", "a108", "a109", "a110", "a111", "a112", "a113", "a114", "a115", "a116", "a117", "a118", "a119", "a120", "a121", "a122", "a123", "a124", "a125", "a126", "a127", "a128", "a129", "a130", "a131", "a132", "a133", "a134", "a135", "a136", "a137", "a138", "a139", "a140", "a141", "a142", "a143", "a144", "a145", "a146", "a147", "a148", "a149", "a150", "a151", "a152", "a153", "a154", "a155", "a156", "a157", "a158", "a159", "a160", "a161", "a162", "a163", "a164", "a165", "a166", "a167", "a168", "a169", "a170", "a171", "a172", "a173", "a174", "a175", "a176", "a177", "a178", "a179", "a180", "a181", "a182", "a183", "a184", "a185", "a186", "a187", "a188", "a189", "a190", "a191", "a192", "a193", "a194", "a195", "a196", "a197", "a198", "a199", "a200", "a201", "a202", "a203", "a204", "a205", "a206", "a207", "a208", "a209", "a210", "a211", "a212", "a213", "a214", "a215", "a216", "a217", "a218", "a219", "a220", "a221", "a222", "a223", "a224", "a225", "a226", "a227", "a228", "a229", "a230", "a231", "a232", "a233", "a234", "a235", "a236", "a237", "a238", "a239", "a240", "a241", "a242", "a243", "a244", "a245", "a246", "a247", "a248", "a249", "a250", "a251", "a252", "a253", "a254", "a255", "s36", "s37", "s38", "s39", "s40", "s41", "s42", "s43", "s44", "s45", "s46", "s47", "s48", "s49", "s50", "s51", "s52", "s53", "s54", "s55", "s56", "s57", "s58", "s59", "s60", "s61", "s62", "s63", "s64", "s67", "s68", "s69", "s70", "s71", "s72", "s73", "s74", "s75", "s76", "s77", "s78", "s79", "s80", "s81", "s82", "s83", "s84", "s85", "s86", "s87", "s88", "s89", "s90", "s91", "s92", "s93", "s94", "s95");
  if (flg != 2u) {
    const int l2 = __builtin_amdgcn_mbcnt_hi(~0u, __builtin_amdgcn_mbcnt_lo(~0u, 0u));
    slow_path(x, dwl, dws, w1, w2, out, (float*)smem, w * 64 + l2, blockIdx.x >> 3, (blockIdx.x & 7) * 4);
  }
}

extern "C" void kernel_launch(void* const* d_in, const int* in_sizes, int n_in, void* d_out, int out_size, void* d_ws,
                              size_t ws_size, hipStream_t stream) {
  replk_kernel<<<256, 256, 0, stream>>>((const u16*)d_in[0], (const u16*)d_in[1], (const u16*)d_in[2], (const u16*)d_in[3],
                                        (const u16*)d_in[4], (u16*)d_out
                                        );
}
```

```cpp
#include <hip/hip_runtime.h>
#include <hip/hip_bf16.h>

typedef unsigned u32;
typedef unsigned short u16;
typedef unsigned long long u64;
typedef __bf16 bf16x2 __attribute__((ext_vector_type(2)));
typedef float f32x2 __attribute__((ext_vector_type(2)));
#define DI __device__ __forceinline__

constexpr int IMG = 512;
constexpr int CH = 96;
constexpr int HID = 384;
constexpr int PXB = 192;
constexpr int ROWB = 98304;
constexpr int TOEP_CS = 336;
constexpr int XT_RS = 320;
constexpr int XT_PP = 1360;
constexpr int XT_CG = 6752;
constexpr int GS = 6160;
constexpr int L_LUT = 0;
constexpr int L_TOEP = 8208;
constexpr int L_W = 40960;
constexpr int L_LUTN = 65536;
constexpr int L_XT = 81920;
constexpr int L_H = 108928;
constexpr int L_EX = 133568;
constexpr int XT_Q = 27024;
constexpr int L_MISC = 162992;
constexpr int L_TOTAL = 163056;
constexpr int LUTK = 106;
constexpr float GC1 = -2.302208198144325f, GC3 = -0.1029432395800235f;
__device__ const u32 CTAB[3 * 1024] = {
671109120,671098880,134490112,673187840,671372952,671229956,145500164,673318996,671636784,671361032,156510216,673450152,671900616,671492108,167520268,673581308,672164452,684206128,178530352,686295376,672428284,684337204,189540404,686426532,672692116,684468280,200550456,686557688,672955948,684599356,211560508,686688844,673219784,697313376,222570592,699402912,673483616,697444452,233580644,699534068,673747448,697575528,244590696,699665224,674011280,697706604,255600748,699796380,674254849,710420624,266610832,712510448,674518681,710551700,277620884,712641604,674782513,710682776,288630936,712772760,675046345,710813852,299640988,712903916,
684223077,723527872,310651072,721423680,684486909,723658948,321661124,721554836,683702165,723790024,332671176,721685992,683965997,723921100,343681228,721817148,685278409,736635120,354691312,734531216,685542241,736766196,365701364,734662372,684757497,736897272,376711416,734793528,685021329,737028348,387721468,734924684,686313474,749742368,398731552,747638752,686577306,749873444,409741604,747769908,685792562,750004520,420751656,747901064,686056394,750135596,431761708,748032220,687368806,762849616,442771792,760746288,687632638,762980692,453781844,760877444,686847894,763111768,464791896,761008600,687111726,763242844,475801948,761139756,
697337034,771762560,486812032,773853824,697600866,771893636,497822084,773984980,697864698,772024712,508832136,774116136,698128530,772155788,519842188,774247292,696274947,784869808,530852272,786961360,696538779,785000884,541862324,787092516,696802611,785131960,552872376,787223672,697066443,785263036,563882428,787354828,699427431,797977056,574892512,800068896,699691263,798108132,585902564,800200052,699955095,798239208,596912616,800331208,700218927,798370284,607922668,800462364,698385611,811084304,618932752,813176432,698649443,811215380,629942804,813307588,698913275,811346456,640952856,813438744,699177107,811477532,651962908,813569900,
710430724,824191552,135539264,822089664,710694556,824322628,146549316,822220820,709909812,824453704,157559368,822351976,710173644,824584780,168569420,822483132,709388904,837298800,179579504,835197200,709652736,837429876,190589556,835328356,708867992,837560952,201599608,835459512,709131824,837692028,212609660,835590668,712541388,850406048,223619744,848304736,712805220,850537124,234629796,848435892,712020476,850668200,245639848,848567048,712284308,850799276,256649900,848698204,711479301,863513296,267659984,861412272,711743133,863644372,278670036,861543428,710958389,863775448,289680088,861674584,711222221,863906524,300690140,861805740,
723544681,677394944,311704064,679484672,723808513,677526020,322714116,679615828,724072345,677657096,333724168,679746984,724336177,677788172,344734220,679878140,724600013,690502192,355744304,692592208,724863845,690633268,366754356,692723364,725127677,690764344,377764408,692854520,725391509,690895420,388774460,692985676,721440774,703609440,399784544,705699744,721704606,703740516,410794596,705830900,721968438,703871592,421804648,705962056,722232270,704002668,432814700,706093212,722496106,716716688,443824784,718807280,722759938,716847764,454834836,718938436,723023770,716978840,465844888,719069592,723287602,717109916,476854940,719200748,
736658638,725629632,487865024,731914816,736922470,725760708,498875076,732045972,736137726,725891784,509885128,732177128,736401558,726022860,520895180,732308284,737693703,738736880,531905264,745022352,737957535,738867956,542915316,745153508,737172791,738999032,553925368,745284664,737436623,739130108,564935420,745415820,734554731,751844128,575945504,758129888,734818563,751975204,586955556,758261044,734033819,752106280,597965608,758392200,734297651,752237356,608975660,758523356,735610063,764951376,619985744,771237424,735873895,765082452,630995796,771368580,735089151,765213528,642005848,771499736,735352983,765344604,653015900,771630892,
749752328,778058624,136592256,780150656,750016160,778189700,147602308,780281812,750279992,778320776,158612360,780443436,750543824,778451852,169622412,780574508,748710508,791165872,180632496,793288492,748974340,791296948,191642548,793419564,749238172,791428024,202652600,793550636,749502004,791559100,213662652,793681708,747668688,804273120,224672736,806395692,747932520,804404196,235682788,806526764,748196352,804535272,246692840,806657836,748460184,804666348,257702892,806788908,746606601,817380368,268712976,819502892,746870433,817511444,279723028,819633964,747134265,817642520,290733080,819765036,747398097,817773596,301743132,819896108,
762866285,826293312,312753216,832610092,763130117,826424388,323763268,832741164,762345373,826555464,334773320,832872236,762609205,826686540,345783372,833003308,761824465,839400560,356793456,845717292,762088297,839531636,367803508,845848364,761303553,839662712,378813560,845979436,761567385,839793788,389823612,846110508,760762378,852507808,400833696,858824492,761026210,852638884,411843748,858955564,760241466,852769960,422853800,859086636,760505298,852901036,433863852,859217708,759720558,865615056,444873936,871931692,759984390,865746132,455883988,872062764,759199646,865877208,466894040,872193836,759463478,866008284,477904092,872324908,
771785938,671098882,488908800,673226540,772049770,671229958,499918852,673357612,772313602,671361034,510928904,673488684,772577434,671492110,521938956,673619756,772821003,684206130,532949040,686333740,773084835,684337206,543959092,686464812,773348667,684468282,554969144,686595884,773612499,684599358,565979196,686726956,773876335,697313378,576989280,699440940,774140167,697444454,587999332,699572012,774403999,697575530,599009384,699703084,774667831,697706606,610019436,699834156,774931667,710420626,621029520,712548140,775195499,710551702,632039572,712679212,775459331,710682778,643049624,712810284,775723163,710813854,654059676,712941356,
784879628,723527874,137636032,721461036,785143460,723658950,148646084,721592108,784358716,723790026,159656136,721723180,784622548,723921102,170666188,721854252,785934960,736635122,181676272,734568236,786198792,736766198,192686324,734699308,785414048,736897274,203696376,734830380,785677880,737028350,214706428,734961452,786990292,749742370,225716512,747675436,787254124,749873446,236726564,747806508,786469380,750004522,247736616,747937580,786733212,750135598,258746668,748068652,788025357,762849618,269756752,760782636,788289189,762980694,280766804,760913708,787504445,763111770,291776856,761044780,787768277,763242846,302786908,761175852,
797993585,771762562,313796992,773889836,798257417,771893638,324807044,774020908,798521249,772024714,335817096,774151980,798785081,772155790,346827148,774283052,796951765,784869810,357837232,786997036,797215597,785000886,368847284,787128108,797479429,785131962,379857336,787259180,797743261,785263038,390867388,787390252,800083982,797977058,401877472,800104236,800347814,798108134,412887524,800235308,800611646,798239210,423897576,800366380,800875478,798370286,434907628,800497452,799042162,811084306,445917712,813211436,799305994,811215382,456927764,813342508,799569826,811346458,467937816,813473580,799833658,811477534,478947868,813604652,
811107542,824191554,489957952,822124332,811371374,824322630,500968004,822255404,810586630,824453706,511978056,822386476,810850462,824584782,522988108,822517548,810045455,837298802,533998192,835231532,810309287,837429878,545008244,835362604,809524543,837560954,556018296,835493676,809788375,837692030,567028348,835624748,813197939,850406050,578038432,848338732,813461771,850537126,589048484,848469804,812677027,850668202,600058536,848600876,812940859,850799278,611068588,848731948,812156119,863513298,622078672,861445932,812419951,863644374,633088724,861577004,811635207,863775450,644098776,861708076,811899039,863906526,655108828,861839148,
824201232,677394946,138689024,679517996,824465064,677526022,149699076,679649068,824728896,677657098,160709128,679780140,824992728,677788174,171719180,679911212,825256564,690502194,182729264,692625196,825520396,690633270,193739316,692756268,825784228,690764346,204749368,692887340,826048060,690895422,215759420,693018412,822117592,703609442,226769504,705732396,822381424,703740518,237779556,705863468,822645256,703871594,248789608,705994540,822909088,704002670,259799660,706125612,823152657,716716690,270809744,718839596,823416489,716847766,281819796,718970668,823680321,716978842,292829848,719101740,823944153,717109918,303839900,719232812,
837315189,725629634,314849984,731946796,837579021,725760710,325860036,732077868,836794277,725891786,336870088,732208940,837058109,726022862,347880140,732340012,838370521,738736882,358890224,745053996,838634353,738867958,369900276,745185068,837849609,738999034,380910328,745316140,838113441,739130110,391920380,745447212,835211282,751844130,402930464,758161196,835475114,751975206,413940516,758292268,834690370,752106282,424950568,758423340,834954202,752237358,435960620,758554412,836266614,764951378,446970704,771268396,836530446,765082454,457980756,771399468,835745702,765213530,468990808,771530540,836009534,765344606,480000860,771661612,
850429146,778058626,491010944,780181292,850692978,778189702,502020996,780312364,850956810,778320778,513031048,780443436,851220642,778451854,524041100,780574508,849367059,791165874,535051184,793288492,849630891,791296950,546061236,793419564,849894723,791428026,557071288,793550636,850158555,791559102,568081340,793681708,848325239,804273122,579091424,806395692,848589071,804404198,590101476,806526764,848852903,804535274,601111528,806657836,849116735,804666350,612121580,806788908,847283419,817380370,623131664,819502892,847547251,817511446,634141716,819633964,847811083,817642522,645151768,819765036,848074915,817773598,656161820,819896108,
863541411,826293314,656161856,832610092,863803555,826424390,656161860,832741164,863017123,826555466,656161864,832872236,863279267,826686542,656161868,833003308,862492835,839400562,656161904,845717292,862754979,839531638,656161908,845848364,861968547,839662714,656161912,845979436,862230691,839793790,656161916,846110508,861444259,852507810,656161952,858824492,861706403,852638886,656161956,858955564,860919971,852769962,656161960,859086636,861182115,852901038,656161964,859217708,860395683,865615058,656162000,871931692,860657827,865746134,656162004,872062764,859871395,865877210,656162008,872193836,860133539,866008286,656162012,872324908,
2151688192u,1885629024,1342187520,1342212212,2151688196u,1885891172,1452812292,1364494564,2151688200u,1886153320,1563437064,1386776836,2218797068u,1886415468,1674061836,1409059156,2165647376u,1886677616,1342253072,1342474360,2165647380u,1886939764,1452877844,1364756712,2164271128u,1887201912,1563502616,1387038984,2231379996u,1887464060,1674127388,1409321304,1082140704,1887726208,1342318624,1342998652,1082140708,1887988356,1452943396,1365281004,1082140712,1888250504,1563568168,1387563276,1149249580,1888512652,1674192940,1409845596,1108682800,1888774800,1342384176,1342736512,1108682804,1889036948,1453008948,1365018864,1107306552,1889299096,1563633720,1387301136,1174415420,1889561244,1674258492,1409583456,
268445760,1892969168,1342449728,1342474372,268445764,1893231316,1453074500,1364756692,268445768,1893493464,1563699272,1387038964,335554636,1893755612,1674324044,1409321316,269822032,1891920576,1342515280,1342736520,269822036,1892182724,1453140052,1365018840,268445784,1892444872,1563764824,1387301112,335554652,1892707020,1674389596,1409583464,1342187616,1890871984,1342580832,1343260812,1342187620,1891134132,1453205604,1365543132,1342187624,1891396280,1563830376,1387825404,1409296492,1891658428,1674455148,1410107756,1343563888,1889823392,1342646384,1342998672,1343563892,1890085540,1453271156,1365280992,1342187640,1890347688,1563895928,1387563264,1409296508,1890609836,1674520700,1409845616,
536881280,1895066352,1342711936,1364494484,536881284,1895328500,1453336708,1386776772,536881288,1895590648,1563961480,1409059108,603990156,1895852796,1674586252,1431341364,538257552,1894017760,1342777488,1364756632,538257556,1894279908,1453402260,1387038920,536881304,1894542056,1564027032,1409321256,603990172,1894804204,1674651804,1431603512,1610623136,1897163536,1342843040,1365280924,1610623140,1897425684,1453467812,1387563212,1610623144,1897687832,1564092584,1409845548,1677732012,1897949980,1674717356,1432127804,1611999408,1896114944,1342908592,1365018784,1611999412,1896377092,1453533364,1387301072,1610623160,1896639240,1564158136,1409583408,1677732028,1896901388,1674782908,1431865664,
809511104,1900309312,1342974144,1364756644,809511108,1900571460,1453598916,1387038900,809511112,1900833608,1564223688,1409321236,876619980,1901095756,1674848460,1431603524,823470288,1901357904,1343039696,1365018792,823470292,1901620052,1453664468,1387301048,822094040,1901882200,1564289240,1409583384,889202908,1902144348,1674914012,1431865672,1887447264,1898212128,1343105248,1365543084,1887447268,1898474276,1453730020,1387825340,1887447272,1898736424,1564354792,1410107676,1954556140,1898998572,1674979564,1432389964,1913989360,1899260720,1343170800,1365280944,1913989364,1899522868,1453795572,1387563200,1912613112,1899785016,1564420344,1409845536,1979721980,1900047164,1675045116,1432127824,
2151688448u,1784705124,1347430656,1452835440,2151688452u,1784967272,1458055428,1475117792,2151688456u,1785229420,1568680200,1497400064,2218797324u,1785491568,1679304972,1519682384,2165647632u,1785753716,1347496208,1453097588,2165647636u,1786015864,1458120980,1475379940,2164271384u,1786278012,1568745752,1497662212,2231380252u,1786540160,1679370524,1519944532,1082140960,1786802308,1347561760,1453621880,1082140964,1787064456,1458186532,1475904232,1082140968,1787326604,1568811304,1498186504,1149249836,1787588752,1679436076,1520468824,1108683056,1787850900,1347627312,1453359740,1108683060,1788113048,1458252084,1475642092,1107306808,1788375196,1568876856,1497924364,1174415676,1788637344,1679501628,1520206684,
268446016,1792045268,1347692864,1453097600,268446020,1792307416,1458317636,1475379920,268446024,1792569564,1568942408,1497662192,335554892,1792831712,1679567180,1519944544,269822288,1790996676,1347758416,1453359748,269822292,1791258824,1458383188,1475642068,268446040,1791520972,1569007960,1497924340,335554908,1791783120,1679632732,1520206692,1342187872,1789948084,1347823968,1453884040,1342187876,1790210232,1458448740,1476166360,1342187880,1790472380,1569073512,1498448632,1409296748,1790734528,1679698284,1520730984,1343564144,1788899492,1347889520,1453621900,1343564148,1789161640,1458514292,1475904220,1342187896,1789423788,1569139064,1498186492,1409296764,1789685936,1679763836,1520468844,
536881536,1794142452,1347955072,1475117712,536881540,1794404600,1458579844,1497400000,536881544,1794666748,1569204616,1519682336,603990412,1794928896,1679829388,1541964592,538257808,1793093860,1348020624,1475379860,538257812,1793356008,1458645396,1497662148,536881560,1793618156,1569270168,1519944484,603990428,1793880304,1679894940,1542226740,1610623392,1796239636,1348086176,1475904152,1610623396,1796501784,1458710948,1498186440,1610623400,1796763932,1569335720,1520468776,1677732268,1797026080,1679960492,1542751032,1611999664,1795191044,1348151728,1475642012,1611999668,1795453192,1458776500,1497924300,1610623416,1795715340,1569401272,1520206636,1677732284,1795977488,1680026044,1542488892,
809511360,1799385412,1348217280,1475379872,809511364,1799647560,1458842052,1497662128,809511368,1799909708,1569466824,1519944464,876620236,1800171856,1680091596,1542226752,823470544,1800434004,1348282832,1475642020,823470548,1800696152,1458907604,1497924276,822094296,1800958300,1569532376,1520206612,889203164,1801220448,1680157148,1542488900,1887447520,1797288228,1348348384,1476166312,1887447524,1797550376,1458973156,1498448568,1887447528,1797812524,1569597928,1520730904,1954556396,1798074672,1680222700,1543013192,1913989616,1798336820,1348413936,1475904172,1913989620,1798598968,1459038708,1498186428,1912613368,1798861116,1569663480,1520468764,1979722236,1799123264,1680288252,1542751052,
2151688704u,2087482984,1352673280,1563464828,2151688708u,2087745132,1463298052,1585747180,2151688712u,2088007280,1573922824,1608029452,2218797580u,2088269428,1684547596,1630311772,2165647888u,2088531576,1352738832,1563726976,2165647892u,2088793724,1463363604,1586009328,2164271640u,2089055872,1573988376,1608291600,2231380508u,2089318020,1684613148,1630573920,1082141216,2089580168,1352804384,1564251268,1082141220,2089842316,1463429156,1586533620,1082141224,2090104464,1574053928,1608815892,1149250092,2090366612,1684678700,1631098212,1108683312,2090628760,1352869936,1563989128,1108683316,2090890908,1463494708,1586271480,1107307064,2091153056,1574119480,1608553752,1174415932,2091415204,1684744252,1630836072,
268446272,2094823128,1352935488,1563726988,268446276,2095085276,1463560260,1586009308,268446280,2095347424,1574185032,1608291580,335555148,2095609572,1684809804,1630573932,269822544,2093774536,1353001040,1563989136,269822548,2094036684,1463625812,1586271456,268446296,2094298832,1574250584,1608553728,335555164,2094560980,1684875356,1630836080,1342188128,2092725944,1353066592,1564513428,1342188132,2092988092,1463691364,1586795748,1342188136,2093250240,1574316136,1609078020,1409297004,2093512388,1684940908,1631360372,1343564400,2091677352,1353132144,1564251288,1343564404,2091939500,1463756916,1586533608,1342188152,2092201648,1574381688,1608815880,1409297020,2092463796,1685006460,1631098232,
536881792,2096920312,1353197696,1585747100,536881796,2097182460,1463822468,1608029388,536881800,2097444608,1574447240,1630311724,603990668,2097706756,1685072012,1652593980,538258064,2095871720,1353263248,1586009248,538258068,2096133868,1463888020,1608291536,536881816,2096396016,1574512792,1630573872,603990684,2096658164,1685137564,1652856128,1610623648,2099017496,1353328800,1586533540,1610623652,2099279644,1463953572,1608815828,1610623656,2099541792,1574578344,1631098164,1677732524,2099803940,1685203116,1653380420,1611999920,2097968904,1353394352,1586271400,1611999924,2098231052,1464019124,1608553688,1610623672,2098493200,1574643896,1630836024,1677732540,2098755348,1685268668,1653118280,
809511616,2102163272,1353459904,1586009260,809511620,2102425420,1464084676,1608291516,809511624,2102687568,1574709448,1630573852,876620492,2102949716,1685334220,1652856140,823470800,2103211864,1353525456,1586271408,823470804,2103474012,1464150228,1608553664,822094552,2103736160,1574775000,1630836000,889203420,2103998308,1685399772,1653118288,1887447776,2100066088,1353591008,1586795700,1887447780,2100328236,1464215780,1609077956,1887447784,2100590384,1574840552,1631360292,1954556652,2100852532,1685465324,1653642580,1913989872,2101114680,1353656560,1586533560,1913989876,2101376828,1464281332,1608815816,1912613624,2101638976,1574906104,1631098152,1979722492,2101901124,1685530876,1653380440,
2151688960u,1986559084,1357916416,1674088056,2151688964u,1986821232,1468541188,1696370408,2151688968u,1987083380,1579165960,1718652680,2218797836u,1987345528,1689790732,1740935000,2165648144u,1987607676,1357981968,1674350204,2165648148u,1987869824,1468606740,1696632556,2164271896u,1988131972,1579231512,1718914828,2231380764u,1988394120,1689856284,1741197148,1082141472,1988656268,1358047520,1674874496,1082141476,1988918416,1468672292,1697156848,1082141480,1989180564,1579297064,1719439120,1149250348,1989442712,1689921836,1741721440,1108683568,1989704860,1358113072,1674612356,1108683572,1989967008,1468737844,1696894708,1107307320,1990229156,1579362616,1719176980,1174416188,1990491304,1689987388,1741459300,
268446528,1993899228,1358178624,1674350216,268446532,1994161376,1468803396,1696632536,268446536,1994423524,1579428168,1718914808,335555404,1994685672,1690052940,1741197160,269822800,1992850636,1358244176,1674612364,269822804,1993112784,1468868948,1696894684,268446552,1993374932,1579493720,1719176956,335555420,1993637080,1690118492,1741459308,1342188384,1991802044,1358309728,1675136656,1342188388,1992064192,1468934500,1697418976,1342188392,1992326340,1579559272,1719701248,1409297260,1992588488,1690184044,1741983600,1343564656,1990753452,1358375280,1674874516,1343564660,1991015600,1469000052,1697156836,1342188408,1991277748,1579624824,1719439108,1409297276,1991539896,1690249596,1741721460,
536882048,1995996412,1358440832,1696370328,536882052,1996258560,1469065604,1718652616,536882056,1996520708,1579690376,1740934952,603990924,1996782856,1690315148,1763217208,538258320,1994947820,1358506384,1696632476,538258324,1995209968,1469131156,1718914764,536882072,1995472116,1579755928,1741197100,603990940,1995734264,1690380700,1763479356,1610623904,1998093596,1358571936,1697156768,1610623908,1998355744,1469196708,1719439056,1610623912,1998617892,1579821480,1741721392,1677732780,1998880040,1690446252,1764003648,1612000176,1997045004,1358637488,1696894628,1612000180,1997307152,1469262260,1719176916,1610623928,1997569300,1579887032,1741459252,1677732796,1997831448,1690511804,1763741508,
809511872,2001239372,1358703040,1696632488,809511876,2001501520,1469327812,1718914744,809511880,2001763668,1579952584,1741197080,876620748,2002025816,1690577356,1763479368,823471056,2002287964,1358768592,1696894636,823471060,2002550112,1469393364,1719176892,822094808,2002812260,1580018136,1741459228,889203676,2003074408,1690642908,1763741516,1887448032,1999142188,1358834144,1697418928,1887448036,1999404336,1469458916,1719701184,1887448040,1999666484,1580083688,1741983520,1954556908,1999928632,1690708460,1764265808,1913990128,2000190780,1358899696,1697156788,1913990132,2000452928,1469524468,1719439044,1912613880,2000715076,1580149240,1741721380,1979722748,2000977224,1690774012,1764003668,
136579076,1343253088,52428,12582912,135530496,1453879396,843229,12648449,134481920,1564505704,1634030,12713986,145229824,1675132012,2411724,12779523,136841224,1343318640,3198156,13369356,135792808,1453944948,3988957,13434893,134744232,1564571256,4779758,13500430,139724968,1675197564,5557452,13565967,137103372,1343384208,6343884,14155800,136054952,1454010516,7134685,14221337,135006376,1564636824,7925486,14286874,139724968,1675263132,8703180,14352411,137365520,1343449728,9489612,14942244,136316928,1454076036,10280413,15007781,135268352,1564702344,11071214,15073318,145229824,1675328652,11848908,15138855,
139724868,1365535332,117964,12582960,139724852,1476161640,908765,12648497,138676260,1586787948,1699566,12714034,137627668,1697414256,2477260,12779571,145229896,1365600884,3263692,13369404,145229880,1476227192,4054493,13434941,138938408,1586853500,4845294,13500478,137889816,1697479808,5622988,13566015,145229900,1365666452,6409420,14155848,145229884,1476292760,7200221,14221385,139200556,1586919068,7991022,14286922,138151964,1697545376,8768716,14352459,139724880,1365731972,9555148,14942292,139724864,1476358280,10345949,15007829,139462704,1586984588,11136750,15073366,138414112,1697610896,11914444,15138903,
137627668,1387817576,183500,12583008,136579076,1498443884,974301,12648545,135530496,1609070192,1765102,12714082,134481920,1719696500,2542796,12779619,137889816,1387883128,3329228,13369452,136841224,1498509436,4120029,13434989,135792808,1609135744,4910830,13500526,134744232,1719762052,5688524,13566063,138151964,1387948696,6474956,14155896,137103372,1498575004,7265757,14221433,136054952,1609201312,8056558,14286970,135006376,1719827620,8834252,14352507,138414112,1388014216,9620684,14942340,137365520,1498640524,10411485,15007877,136316928,1609266832,11202286,15073414,135268352,1719893140,11979980,15138951,
139724968,1410099820,249036,12583056,139724868,1520726128,1039837,12648593,139724852,1631352436,1830638,12714130,138676260,1741978744,2608332,12779667,145229824,1410165372,3394764,13369500,145229896,1520791680,4185565,13435037,145229880,1631417988,4976366,13500574,138938408,1742044296,5754060,13566111,145229824,1410230940,6540492,14155944,145229900,1520857248,7331293,14221481,145229884,1631483556,8122094,14287018,139200556,1742109864,8899788,14352555,139724968,1410296460,9686220,14942388,139724880,1520922768,10477021,15007925,139724864,1631549076,11267822,15073462,139462704,1742175384,12045516,15138999,
180619940,1348496080,52428,12582912,179571360,1459122388,843229,12648449,178522784,1569748696,1634030,12713986,189270688,1680375004,2411724,12779523,180882088,1348561600,3198156,13369356,179833672,1459187908,3988957,13434893,178785096,1569814216,4779758,13500430,183765832,1680440524,5557452,13565967,181144236,1348627104,6343884,14155800,180095816,1459253412,7134685,14221337,179047240,1569879720,7925486,14286874,183765832,1680506028,8703180,14352411,181406384,1348692656,9489612,14942244,180357792,1459318964,10280413,15007781,179309216,1569945272,11071214,15073318,189270688,1680571580,11848908,15138855,
183765732,1370778324,117964,12582960,183765716,1481404632,908765,12648497,182717124,1592030940,1699566,12714034,181668532,1702657248,2477260,12779571,189270760,1370843844,3263692,13369404,189270744,1481470152,4054493,13434941,182979272,1592096460,4845294,13500478,181930680,1702722768,5622988,13566015,189270764,1370909348,6409420,14155848,189270748,1481535656,7200221,14221385,183241420,1592161964,7991022,14286922,182192828,1702788272,8768716,14352459,183765744,1370974900,9555148,14942292,183765728,1481601208,10345949,15007829,183503568,1592227516,11136750,15073366,182454976,1702853824,11914444,15138903,
181668532,1393060568,183500,12583008,180619940,1503686876,974301,12648545,179571360,1614313184,1765102,12714082,178522784,1724939492,2542796,12779619,181930680,1393126088,3329228,13369452,180882088,1503752396,4120029,13434989,179833672,1614378704,4910830,13500526,178785096,1725005012,5688524,13566063,182192828,1393191592,6474956,14155896,181144236,1503817900,7265757,14221433,180095816,1614444208,8056558,14286970,179047240,1725070516,8834252,14352507,182454976,1393257144,9620684,14942340,181406384,1503883452,10411485,15007877,180357792,1614509760,11202286,15073414,179309216,1725136068,11979980,15138951,
183765832,1415342812,249036,12583056,183765732,1525969120,1039837,12648593,183765716,1636595428,1830638,12714130,182717124,1747221736,2608332,12779667,189270688,1415408332,3394764,13369500,189270760,1526034640,4185565,13435037,189270744,1636660948,4976366,13500574,182979272,1747287256,5754060,13566111,189270688,1415473836,6540492,14155944,189270764,1526100144,7331293,14221481,189270748,1636726452,8122094,14287018,183241420,1747352760,8899788,14352555,183765832,1415539388,9686220,14942388,183765744,1526165696,10477021,15007925,183765728,1636792004,11267822,15073462,183503568,1747418312,12045516,15138999,
224660804,1353738992,52428,12582912,223612224,1464365300,843229,12648449,222563648,1574991608,1634030,12713986,233311552,1685617916,2411724,12779523,224922952,1353804512,3198156,13369356,223874536,1464430820,3988957,13434893,222825960,1575057128,4779758,13500430,227806696,1685683436,5557452,13565967,225185100,1353870080,6343884,14155800,224136680,1464496388,7134685,14221337,223088104,1575122696,7925486,14286874,227806696,1685749004,8703180,14352411,225447248,1353935632,9489612,14942244,224398656,1464561940,10280413,15007781,223350080,1575188248,11071214,15073318,233311552,1685814556,11848908,15138855,
227806596,1376021236,117964,12582960,227806580,1486647544,908765,12648497,226757988,1597273852,1699566,12714034,225709396,1707900160,2477260,12779571,233311624,1376086756,3263692,13369404,233311608,1486713064,4054493,13434941,227020136,1597339372,4845294,13500478,225971544,1707965680,5622988,13566015,233311628,1376152324,6409420,14155848,233311612,1486778632,7200221,14221385,227282284,1597404940,7991022,14286922,226233692,1708031248,8768716,14352459,227806608,1376217876,9555148,14942292,227806592,1486844184,10345949,15007829,227544432,1597470492,11136750,15073366,226495840,1708096800,11914444,15138903,
225709396,1398303480,183500,12583008,224660804,1508929788,974301,12648545,223612224,1619556096,1765102,12714082,222563648,1730182404,2542796,12779619,225971544,1398369000,3329228,13369452,224922952,1508995308,4120029,13434989,223874536,1619621616,4910830,13500526,222825960,1730247924,5688524,13566063,226233692,1398434568,6474956,14155896,225185100,1509060876,7265757,14221433,224136680,1619687184,8056558,14286970,223088104,1730313492,8834252,14352507,226495840,1398500120,9620684,14942340,225447248,1509126428,10411485,15007877,224398656,1619752736,11202286,15073414,223350080,1730379044,11979980,15138951,
227806696,1420585724,249036,12583056,227806596,1531212032,1039837,12648593,227806580,1641838340,1830638,12714130,226757988,1752464648,2608332,12779667,233311552,1420651244,3394764,13369500,233311624,1531277552,4185565,13435037,233311608,1641903860,4976366,13500574,227020136,1752530168,5754060,13566111,233311552,1420716812,6540492,14155944,233311628,1531343120,7331293,14221481,233311612,1641969428,8122094,14287018,227282284,1752595736,8899788,14352555,227806696,1420782364,9686220,14942388,227806608,1531408672,10477021,15007925,227806592,1642034980,11267822,15073462,227544432,1752661288,12045516,15138999,
268701668,1358981952,52428,12582912,267653088,1469608260,843229,12648449,266604512,1580234568,1634030,12713986,277352416,1690860876,2411724,12779523,268963816,1359047504,3198156,13369356,267915400,1469673812,3988957,13434893,266866824,1580300120,4779758,13500430,271847560,1690926428,5557452,13565967,269225964,1359113008,6343884,14155800,268177544,1469739316,7134685,14221337,267128968,1580365624,7925486,14286874,271847560,1690991932,8703180,14352411,269488112,1359178528,9489612,14942244,268439520,1469804836,10280413,15007781,267390944,1580431144,11071214,15073318,277352416,1691057452,11848908,15138855,
271847460,1381264196,117964,12582960,271847444,1491890504,908765,12648497,270798852,1602516812,1699566,12714034,269750260,1713143120,2477260,12779571,277352488,1381329748,3263692,13369404,277352472,1491956056,4054493,13434941,271061000,1602582364,4845294,13500478,270012408,1713208672,5622988,13566015,277352492,1381395252,6409420,14155848,277352476,1492021560,7200221,14221385,271323148,1602647868,7991022,14286922,270274556,1713274176,8768716,14352459,271847472,1381460772,9555148,14942292,271847456,1492087080,10345949,15007829,271585296,1602713388,11136750,15073366,270536704,1713339696,11914444,15138903,
269750260,1403546440,183500,12583008,268701668,1514172748,974301,12648545,267653088,1624799056,1765102,12714082,266604512,1735425364,2542796,12779619,270012408,1403611992,3329228,13369452,268963816,1514238300,4120029,13434989,267915400,1624864608,4910830,13500526,266866824,1735490916,5688524,13566063,270274556,1403677496,6474956,14155896,269225964,1514303804,7265757,14221433,268177544,1624930112,8056558,14286970,267128968,1735556420,8834252,14352507,270536704,1403743016,9620684,14942340,269488112,1514369324,10411485,15007877,268439520,1624995632,11202286,15073414,267390944,1735621940,11979980,15138951,
271847560,1425828684,249036,12583056,271847460,1536454992,1039837,12648593,271847444,1647081300,1830638,12714130,270798852,1757707608,2608332,12779667,277352416,1425894236,3394764,13369500,277352488,1536520544,4185565,13435037,277352472,1647146852,4976366,13500574,271061000,1757773160,5754060,13566111,277352416,1425959740,6540492,14155944,277352492,1536586048,7331293,14221481,277352476,1647212356,8122094,14287018,271323148,1757838664,8899788,14352555,271847560,1426025260,9686220,14942388,271847472,1536651568,10477021,15007925,271847456,1647277876,11267822,15073462,271585296,1757904184,12045516,15138999};

DI float bf_lo(u32 v) { return __uint_as_float(v << 16); }
DI u32 pk_bf16(float a, float b) { f32x2 t = {a, b}; return __builtin_bit_cast(u32, __builtin_convertvector(t, bf16x2)); }

DI void slow_path(const u16* __restrict__ x, const u16* __restrict__ dwl, const u16* __restrict__ dws, const u16* __restrict__ w1,
                  const u16* __restrict__ w2, u16* __restrict__ out, float* sm, int t, int tx, int ty0) {
  float* o = sm;
  float* g = sm + 8 * CH;
  float* st = g + 8 * HID;
#pragma unroll 1
  for (int b = 0; b < 128; ++b) {
    const int y = ty0 * 16 + (b >> 1), x0 = tx * 16 + (b & 1) * 8;
    __syncthreads();
#pragma unroll 1
    for (int i = t; i < 8 * CH; i += 256) {
      const int p = i / CH, c = i - p * CH;
      float acc = 0.f;
#pragma unroll 1
      for (int a = 0; a < 5; ++a)
#pragma unroll 1
        for (int e = 0; e < 5; ++e) {
          const int yy = y + a - 2, xc = x0 + p + e - 2;
          if (yy < 0 || yy >= IMG || xc < 0 || xc >= IMG) continue;
          const float xv = bf_lo(x[(size_t)(yy * IMG + xc) * CH + c]);
          acc = __builtin_fmaf(bf_lo(dws[(a * 5 + e) * CH + c]), xv, acc);
          if (a >= 1 && a <= 3 && e >= 1 && e <= 3) acc = __builtin_fmaf(bf_lo(dwl[((a - 1) * 3 + (e - 1)) * CH + c]), xv, acc);
        }
      o[i] = acc;
    }
    __syncthreads();
    if (t < 8) {
      float m = 0.f, v = 0.f;
#pragma unroll 1
      for (int c = 0; c < CH; ++c) m += o[t * CH + c];
      m *= (1.0f / CH);
#pragma unroll 1
      for (int c = 0; c < CH; ++c) { const float d = o[t * CH + c] - m; v = __builtin_fmaf(d, d, v); }
      st[2 * t] = m; st[2 * t + 1] = __builtin_amdgcn_rsqf(v * (1.0f / CH) + 1e-5f);
    }
    __syncthreads();
#pragma unroll 1
    for (int i = t; i < 8 * HID; i += 256) {
      const int p = i / HID, e = i - p * HID;
      const float m = st[2 * p], r = st[2 * p + 1];
      float u = 0.f;
#pragma unroll 1
      for (int c = 0; c < CH; ++c) u = __builtin_fmaf((o[p * CH + c] - m) * r, bf_lo(w1[c * HID + e]), u);
      const float z = u * __builtin_fmaf(u * u, GC3, GC1);
      g[i] = u * __builtin_amdgcn_rcpf(1.0f + __builtin_amdgcn_exp2f(z));
    }
    __syncthreads();
#pragma unroll 1
    for (int i = t; i < 8 * CH; i += 256) {
      const int p = i / CH, c = i - p * CH;
      float acc = 0.f;
#pragma unroll 1
      for (int e = 0; e < HID; ++e) acc = __builtin_fmaf(g[p * HID + e], bf_lo(w2[e * CH + c]), acc);
      const size_t idx = (size_t)(y * IMG + x0 + p) * CH + c;
      out[idx] = (u16)pk_bf16(bf_lo(x[idx]) + acc, 0.f);
    }
  }
}

__global__ __launch_bounds__(256, 1) void replk_kernel(const u16* __restrict__ x, const u16* __restrict__ dwl,
    const u16* __restrict__ dws, const u16* __restrict__ w1, const u16* __restrict__ w2, u16* __restrict__ out
    ) {
  __shared__ __attribute__((aligned(16))) char smem[L_TOTAL];
  __attribute__((address_space(3))) char* smem3 = (__attribute__((address_space(3))) char*)smem;
  const int w = __builtin_amdgcn_readfirstlane(threadIdx.x >> 6);
  u32 tid = threadIdx.x, blk = blockIdx.x;
  u32 flg = (u32)(size_t)smem3 ? 1u : 0u;
  u64 ka = (u64)__builtin_amdgcn_kernarg_segment_ptr();
  asm volatile(
    ".p2align 3\n"
    "s_load_dwordx2 s[40:41], s[98:99], 0x0\n"
    "s_load_dwordx2 s[88:89], s[98:99], 0x10\n"
    "s_load_dwordx2 s[92:93], s[98:99], 0x8\n"
    "s_load_dwordx2 s[84:85], s[98:99], 0x20\n"
    "s_load_dwordx2 s[80:81], s[98:99], 0x18\n"
    "s_load_dwordx2 s[44:45], s[98:99], 0x28\n"
    "v_lshlrev_b32_e32 v213, 4, v208\n"
    "v_lshrrev_b32_e32 v0, 6, v208\n"
    "s_add_u32 vcc_lo, s96, 0\n"
    "s_addc_u32 vcc_hi, s97, 0\n"
    "global_load_dwordx4 v[168:171], v213, vcc\n"
    "s_add_u32 vcc_lo, s96, 4096\n"
    "s_addc_u32 vcc_hi, s97, 0\n"
    "global_load_dwordx4 v[172:175], v213, vcc\n"
    "s_add_u32 vcc_lo, s96, 8192\n"
    "s_addc_u32 vcc_hi, s97, 0\n"
    "global_load_dwordx4 v[176:179], v213, vcc\n"
    "s_getpc_b64 s[76:77]\n"
    ".Lreplk_pc0:\n"
    "s_bfe_u32 s63, s65, 0x30003\n"
    "v_lshrrev_b32_e32 v5, 2, v208\n"
    "s_lshl_b32 s63, s63, 6\n"
    "v_add_lshl_u32 v5, s63, v5, 7\n"
    "v_min_u32_e32 v215, .Lreplk_end-.Lreplk_pc0-4, v5\n"
    "global_load_dword v233, v215, s[76:77]\n"
    "v_readfirstlane_b32 s60, v0\n"
    "v_min_u32_e32 v1, 239, v208\n"
    "v_mul_u32_u24_e32 v2, 1366, v1\n"
    "s_lshr_b32 s61, s65, 3\n"
    "s_and_b32 s62, s65, 7\n"
    "v_lshrrev_b32_e32 v2, 16, v2\n"
    "v_lshlrev_b32_e32 v3, 2, v1\n"
    "s_lshl_b32 s62, s62, 6\n"
    "s_movk_i32 s63, 768\n"
    "v_mad_u32_u24 v211, v2, s63, v3\n"
    "v_mul_u32_u24_e32 v4, 384, v2\n"
    "v_add_u32_e32 v2, -1, v2\n"
    "s_movk_i32 s90, 4800\n"
    "s_mov_b32 s91, 0x20000\n"
    "v_add_u32_e32 v3, -576, v3\n"
    "v_cmp_gt_u32_e32 vcc, 3, v2\n"
    "v_add_u32_e32 v3, v3, v4\n"
    "v_bfrev_b32_e32 v4, 1\n"
    "s_movk_i32 s94, 1728\n"
    "s_mov_b32 s95, 0x20000\n"
    "v_cndmask_b32_e32 v212, v4, v3, vcc\n"
    "s_lshl_b32 s99, s62, 5\n"
    "s_add_u32 s99, s99, s61\n"
    "s_mulk_i32 s99, 3072\n"
    "s_lshr_b32 s51, s60, 1\n"
    "s_mul_i32 s63, s51, 18432\n"
    "s_lshl_b32 s52, s60, 13\n"
    "s_add_u32 s63, s63, s52\n"
    "v_add_u32_e32 v210, s63, v213\n"
    "s_mul_i32 s38, s60, 9216\n"
    "s_add_u32 s38, s38, 40960\n"
    "s_mov_b32 s86, 73728\n"
    "s_mov_b32 s87, 0x20000\n"
    "v_and_b32_e32 v5, 63, v208\n"
    "v_min_u32_e32 v5, 8, v5\n"
    "s_and_b32 s52, s60, 1\n"
    "s_lshl_b32 s53, s61, 1\n"
    "s_add_u32 s52, s52, s53\n"
    "s_mul_i32 s52, s52, 9\n"
    "v_add_lshl_u32 v214, s52, v5, 7\n"
    "s_cmp_eq_u32 s61, 0\n"
    "s_cselect_b32 s52, 24, 0\n"
    "s_cmp_eq_u32 s61, 31\n"
    "s_cselect_b32 s53, 40, s52\n"
    "s_sub_u32 s53, 256, s53\n"
    "v_subrev_u32_e32 v6, s52, v208\n"
    "s_sub_u32 s63, s99, 384\n"
    "v_cmp_gt_u32_e32 vcc, s53, v6\n"
    "v_lshl_add_u32 v7, v1, 4, s63\n"
    "v_bfe_u32 v8, v208, 4, 2\n"
    "v_lshl_or_b32 v8, v0, 3, v8\n"
    "v_and_b32_e32 v9, 0xf0, v213\n"
    "v_cndmask_b32_e32 v208, v4, v7, vcc\n"
    "v_mul_u32_u24_e32 v8, 768, v8\n"
    "s_mov_b32 s42, 50331648\n"
    "s_mov_b32 s43, 0x20000\n"
    "v_or_b32_e32 v209, v8, v9\n"
    "s_mov_b32 s82, 73728\n"
    "s_mov_b32 s83, 0x20000\n"
    "s_cmp_eq_u32 s62, 0\n"
    "s_cselect_b32 s78, 0, s42\n"
    "s_mov_b32 s79, 0x20000\n"
    "s_waitcnt lgkmcnt(0)\n"
    "s_cmp_lt_u32 s60, 2\n"
    "s_nop 0\n"
    "s_cselect_b64 s[70:71], s[80:81], s[84:85]\n"
    "s_sub_u32 s76, s40, 196608\n"
    "s_subb_u32 s77, s41, 0\n"
    "s_mov_b32 s65, 0\n"
    "buffer_load_dwordx4 a[224:227], v208, s[76:79], s65 offen\n"
    "s_mov_b32 s65, 98304\n"
    "buffer_load_dwordx4 a[228:231], v208, s[76:79], s65 offen\n"
    "s_mov_b32 s65, 196608\n"
    "buffer_load_dwordx4 a[232:235], v208, s[40:43], s65 offen\n"
    "s_mov_b32 s65, 294912\n"
    "buffer_load_dwordx4 a[236:239], v208, s[40:43], s65 offen\n"
    "buffer_load_dword v224, v211, s[88:91], 0 offen offset:0\n"
    "buffer_load_dword v225, v211, s[88:91], 0 offen offset:192\n"
    "buffer_load_dword v226, v211, s[88:91], 0 offen offset:384\n"
    "buffer_load_dword v227, v211, s[88:91], 0 offen offset:576\n"
    "buffer_load_dword v228, v211, s[88:91], 0 offen offset:768\n"
    "v_mov_b32_e64 v88, 0\n"
    "v_mov_b32_e32 v240, 0xdddd8888\n"
    "v_mov_b32_e64 v89, 0\n"
    "v_mov_b32_e64 v200, 0\n"
    "v_mov_b32_e64 v90, 0\n"
    "v_mov_b32_e64 v91, 0\n"
    "v_mov_b32_e64 v59, 0\n"
    "v_mov_b32_e64 v60, 0\n"
    "v_mov_b32_e64 v67, 0\n"
    "v_mov_b32_e64 v68, 0\n"
    "v_mov_b32_e64 v75, 0\n"
    "v_mov_b32_e64 v76, 0\n"
    "v_mov_b32_e64 v83, 0\n"
    "v_mov_b32_e64 v84, 0\n"
    "v_mov_b32_e32 v36, 0x75089ba4\n"
    "v_mov_b32_e32 v38, 0xbdd2d3e8\n"
    "v_mov_b32_e32 v32, 0xc0135761\n"
    "v_lshlrev_b32_e64 v34, 16, v213\n"
    "v_lshlrev_b32_e64 v234, 1, v213\n"
    "v_or_b32_e32 v0, 0x8000, v34\n"
    "v_or_b32_e32 v1, 0x18000, v34\n"
    "v_or_b32_e32 v2, 0x28000, v34\n"
    "v_or_b32_e32 v3, 0x38000, v34\n"
    "v_or_b32_e32 v4, 0x48000, v34\n"
    "v_or_b32_e32 v5, 0x58000, v34\n"
    "v_or_b32_e32 v6, 0x68000, v34\n"
    "v_or_b32_e32 v7, 0x78000, v34\n"
    "v_cmp_ne_u32_e64 s[72:73], 0, v34\n"
    "s_nop 0\n"
    "s_nop 1\n"
    "v_cndmask_b32_e64 v0, 0, v0, s[72:73]\n"
    "v_pk_mul_f32 v[0:1], v[0:1], v[36:37] op_sel_hi:[1,0]\n"
    "v_pk_mul_f32 v[2:3], v[2:3], v[36:37] op_sel_hi:[1,0]\n"
    "v_pk_mul_f32 v[4:5], v[4:5], v[36:37] op_sel_hi:[1,0]\n"
    "v_pk_mul_f32 v[6:7], v[6:7], v[36:37] op_sel_hi:[1,0]\n"
    "v_pk_mul_f32 v[8:9], v[0:1], v[0:1]\n"
    "v_pk_mul_f32 v[10:11], v[2:3], v[2:3]\n"
    "v_pk_mul_f32 v[12:13], v[4:5], v[4:5]\n"
    "v_pk_mul_f32 v[14:15], v[6:7], v[6:7]\n"
    "v_pk_fma_f32 v[8:9], v[8:9], v[38:39], v[32:33] op_sel_hi:[1,0,0]\n"
    "v_pk_fma_f32 v[10:11], v[10:11], v[38:39], v[32:33] op_sel_hi:[1,0,0]\n"
    "v_pk_fma_f32 v[12:13], v[12:13], v[38:39], v[32:33] op_sel_hi:[1,0,0]\n"
    "v_pk_fma_f32 v[14:15], v[14:15], v[38:39], v[32:33] op_sel_hi:[1,0,0]\n"
    "v_pk_mul_f32 v[8:9], v[8:9], v[0:1]\n"
    "v_pk_mul_f32 v[10:11], v[10:11], v[2:3]\n"
    "v_pk_mul_f32 v[12:13], v[12:13], v[4:5]\n"
    "v_pk_mul_f32 v[14:15], v[14:15], v[6:7]\n"
    "v_exp_f32_e64 v8, v8\n"
    "v_exp_f32_e64 v9, v9\n"
    "v_exp_f32_e64 v10, v10\n"
    "v_exp_f32_e64 v11, v11\n"
    "v_exp_f32_e64 v12, v12\n"
    "v_exp_f32_e64 v13, v13\n"
    "v_exp_f32_e64 v14, v14\n"
    "v_exp_f32_e64 v15, v15\n"
    "v_pk_add_f32 v[16:17], v[8:9], 1.0 op_sel_hi:[1,0]\n"
    "v_pk_add_f32 v[18:19], v[10:11], 1.0 op_sel_hi:[1,0]\n"
    "v_pk_add_f32 v[20:21], v[12:13], 1.0 op_sel_hi:[1,0]\n"
    "v_pk_add_f32 v[22:23], v[14:15], 1.0 op_sel_hi:[1,0]\n"
    "v_rcp_f32_e64 v16, v16\n"
    "v_rcp_f32_e64 v17, v17\n"
    "v_rcp_f32_e64 v18, v18\n"
    "v_rcp_f32_e64 v19, v19\n"
    "v_rcp_f32_e64 v20, v20\n"
    "v_rcp_f32_e64 v21, v21\n"
    "v_rcp_f32_e64 v22, v22\n"
    "v_rcp_f32_e64 v23, v23\n"
    "v_pk_mul_f32 v[16:17], v[16:17], v[0:1]\n"
    "v_pk_mul_f32 v[18:19], v[18:19], v[2:3]\n"
    "v_pk_mul_f32 v[20:21], v[20:21], v[4:5]\n"
    "v_pk_mul_f32 v[22:23], v[22:23], v[6:7]\n"
    "v_pk_mul_f32 v[8:9], v[8:9], v[16:17]\n"
    "v_pk_mul_f32 v[10:11], v[10:11], v[18:19]\n"
    "v_pk_mul_f32 v[12:13], v[12:13], v[20:21]\n"
    "v_pk_mul_f32 v[14:15], v[14:15], v[22:23]\n"
    "v_cvt_pk_bf16_f32 v188, v16, v17\n"
    "v_cvt_pk_bf16_f32 v189, v18, v19\n"
    "v_cvt_pk_bf16_f32 v190, v20, v21\n"
    "v_cvt_pk_bf16_f32 v191, v22, v23\n"
    "v_cvt_pk_bf16_f32 v24, -v8, -v9\n"
    "v_cvt_pk_bf16_f32 v25, -v10, -v11\n"
    "v_cvt_pk_bf16_f32 v26, -v12, -v13\n"
    "v_cvt_pk_bf16_f32 v27, -v14, -v15\n"
    "ds_write_b128 v234, v[24:27] offset:0\n"
    "v_or_b32_e32 v0, 0x88000, v34\n"
    "v_or_b32_e32 v1, 0x98000, v34\n"
    "v_or_b32_e32 v2, 0xa8000, v34\n"
    "v_or_b32_e32 v3, 0xb8000, v34\n"
    "v_or_b32_e32 v4, 0xc8000, v34\n"
    "v_or_b32_e32 v5, 0xd8000, v34\n"
    "v_or_b32_e32 v6, 0xe8000, v34\n"
    "v_or_b32_e32 v7, 0xf8000, v34\n"
    "v_pk_mul_f32 v[0:1], v[0:1], v[36:37] op_sel_hi:[1,0]\n"
    "v_pk_mul_f32 v[2:3], v[2:3], v[36:37] op_sel_hi:[1,0]\n"
    "v_pk_mul_f32 v[4:5], v[4:5], v[36:37] op_sel_hi:[1,0]\n"
    "v_pk_mul_f32 v[6:7], v[6:7], v[36:37] op_sel_hi:[1,0]\n"
    "v_pk_mul_f32 v[8:9], v[0:1], v[0:1]\n"
    "v_pk_mul_f32 v[10:11], v[2:3], v[2:3]\n"
    "v_pk_mul_f32 v[12:13], v[4:5], v[4:5]\n"
    "v_pk_mul_f32 v[14:15], v[6:7], v[6:7]\n"
    "v_pk_fma_f32 v[8:9], v[8:9], v[38:39], v[32:33] op_sel_hi:[1,0,0]\n"
    "v_pk_fma_f32 v[10:11], v[10:11], v[38:39], v[32:33] op_sel_hi:[1,0,0]\n"
    "v_pk_fma_f32 v[12:13], v[12:13], v[38:39], v[32:33] op_sel_hi:[1,0,0]\n"
    "v_pk_fma_f32 v[14:15], v[14:15], v[38:39], v[32:33] op_sel_hi:[1,0,0]\n"
    "v_pk_mul_f32 v[8:9], v[8:9], v[0:1]\n"
    "v_pk_mul_f32 v[10:11], v[10:11], v[2:3]\n"
    "v_pk_mul_f32 v[12:13], v[12:13], v[4:5]\n"
    "v_pk_mul_f32 v[14:15], v[14:15], v[6:7]\n"
    "v_exp_f32_e64 v8, v8\n"
    "v_exp_f32_e64 v9, v9\n"
    "v_exp_f32_e64 v10, v10\n"
    "v_exp_f32_e64 v11, v11\n"
    "v_exp_f32_e64 v12, v12\n"
    "v_exp_f32_e64 v13, v13\n"
    "v_exp_f32_e64 v14, v14\n"
    "v_exp_f32_e64 v15, v15\n"
    "v_pk_add_f32 v[16:17], v[8:9], 1.0 op_sel_hi:[1,0]\n"
    "v_pk_add_f32 v[18:19], v[10:11], 1.0 op_sel_hi:[1,0]\n"
    "v_pk_add_f32 v[20:21], v[12:13], 1.0 op_sel_hi:[1,0]\n"
    "v_pk_add_f32 v[22:23], v[14:15], 1.0 op_sel_hi:[1,0]\n"
    "v_rcp_f32_e64 v16, v16\n"
    "v_rcp_f32_e64 v17, v17\n"
    "v_rcp_f32_e64 v18, v18\n"
    "v_rcp_f32_e64 v19, v19\n"
    "v_rcp_f32_e64 v20, v20\n"
    "v_rcp_f32_e64 v21, v21\n"
    "v_rcp_f32_e64 v22, v22\n"
    "v_rcp_f32_e64 v23, v23\n"
    "v_pk_mul_f32 v[16:17], v[16:17], v[0:1]\n"
    "v_pk_mul_f32 v[18:19], v[18:19], v[2:3]\n"
    "v_pk_mul_f32 v[20:21], v[20:21], v[4:5]\n"
    "v_pk_mul_f32 v[22:23], v[22:23], v[6:7]\n"
    "v_pk_mul_f32 v[8:9], v[8:9], v[16:17]\n"
    "v_pk_mul_f32 v[10:11], v[10:11], v[18:19]\n"
    "v_pk_mul_f32 v[12:13], v[12:13], v[20:21]\n"
    "v_pk_mul_f32 v[14:15], v[14:15], v[22:23]\n"
    "v_cvt_pk_bf16_f32 v192, v16, v17\n"
    "v_cvt_pk_bf16_f32 v193, v18, v19\n"
    "v_cvt_pk_bf16_f32 v194, v20, v21\n"
    "v_cvt_pk_bf16_f32 v195, v22, v23\n"
    "v_cvt_pk_bf16_f32 v24, -v8, -v9\n"
    "v_cvt_pk_bf16_f32 v25, -v10, -v11\n"
    "v_cvt_pk_bf16_f32 v26, -v12, -v13\n"
    "v_cvt_pk_bf16_f32 v27, -v14, -v15\n"
    "ds_write_b128 v234, v[24:27] offset:16\n"
    "v_add_u32_e32 v234, 65536, v234\n"
    "buffer_load_dword v229, v212, s[92:95], 0 offen offset:0\n"
    "s_nop 0\n"
    "s_waitcnt vmcnt(13)\n"
    "v_lshlrev_b32_sdwa v216, 2, v168 dst_sel:DWORD dst_unused:UNUSED_PAD src0_sel:DWORD src1_sel:WORD_0\n"
    "v_lshlrev_b32_sdwa v217, 2, v168 dst_sel:DWORD dst_unused:UNUSED_PAD src0_sel:DWORD src1_sel:WORD_1\n"
    "v_lshlrev_b32_sdwa v218, 2, v169 dst_sel:DWORD dst_unused:UNUSED_PAD src0_sel:DWORD src1_sel:WORD_0\n"
    "v_lshlrev_b32_sdwa v219, 2, v169 dst_sel:DWORD dst_unused:UNUSED_PAD src0_sel:DWORD src1_sel:WORD_1\n"
    "v_lshlrev_b32_sdwa v220, 2, v170 dst_sel:DWORD dst_unused:UNUSED_PAD src0_sel:DWORD src1_sel:WORD_0\n"
    "v_lshlrev_b32_sdwa v221, 2, v170 dst_sel:DWORD dst_unused:UNUSED_PAD src0_sel:DWORD src1_sel:WORD_1\n"
    "v_lshlrev_b32_sdwa v222, 2, v171 dst_sel:DWORD dst_unused:UNUSED_PAD src0_sel:DWORD src1_sel:WORD_0\n"
    "v_lshlrev_b32_sdwa v223, 2, v171 dst_sel:DWORD dst_unused:UNUSED_PAD src0_sel:DWORD src1_sel:WORD_1\n"
    "ds_write_b128 v222, v[88:91]\n"
    "buffer_load_dword v230, v212, s[92:95], 0 offen offset:192\n"
    "s_nop 0\n"
    "s_waitcnt vmcnt(13)\n"
    "v_lshlrev_b32_sdwa v207, 2, v172 dst_sel:DWORD dst_unused:UNUSED_PAD src0_sel:DWORD src1_sel:WORD_0\n"
    "v_lshlrev_b32_sdwa v242, 2, v173 dst_sel:DWORD dst_unused:UNUSED_PAD src0_sel:DWORD src1_sel:WORD_0\n"
    "v_lshlrev_b32_sdwa v243, 2, v173 dst_sel:DWORD dst_unused:UNUSED_PAD src0_sel:DWORD src1_sel:WORD_1\n"
    "v_lshlrev_b32_sdwa v244, 2, v174 dst_sel:DWORD dst_unused:UNUSED_PAD src0_sel:DWORD src1_sel:WORD_0\n"
    "v_lshlrev_b32_sdwa v248, 2, v174 dst_sel:DWORD dst_unused:UNUSED_PAD src0_sel:DWORD src1_sel:WORD_1\n"
    "v_lshlrev_b32_sdwa v249, 2, v175 dst_sel:DWORD dst_unused:UNUSED_PAD src0_sel:DWORD src1_sel:WORD_0\n"
    "v_lshlrev_b32_sdwa v250, 2, v175 dst_sel:DWORD dst_unused:UNUSED_PAD src0_sel:DWORD src1_sel:WORD_1\n"
    "v_bfe_i32 v201, v172, 16, 1\n"
    "v_and_b32_e32 v201, 0x35003500, v201\n"
    "v_bfe_i32 v203, v172, 18, 1\n"
    "v_and_b32_e32 v203, 0x35003500, v203\n"
    "v_bfe_i32 v205, v172, 20, 1\n"
    "v_and_b32_e32 v205, 0x35003500, v205\n"
    "v_bfe_i32 v92, v172, 26, 1\n"
    "v_xor_b32_e32 v92, 0xffff, v92\n"
    "v_and_b32_e32 v92, 0x3f803f80, v92\n"
    "v_bfe_i32 v236, v172, 22, 1\n"
    "v_and_b32_e64 v236, v92, v236\n"
    "v_bfe_i32 v237, v172, 23, 1\n"
    "v_and_b32_e32 v237, v92, v237\n"
    "buffer_load_dword v231, v212, s[92:95], 0 offen offset:384\n"
    "s_waitcnt vmcnt(13)\n"
    "v_lshlrev_b32_sdwa v251, 2, v176 dst_sel:DWORD dst_unused:UNUSED_PAD src0_sel:DWORD src1_sel:WORD_0\n"
    "v_lshlrev_b32_sdwa v252, 2, v176 dst_sel:DWORD dst_unused:UNUSED_PAD src0_sel:DWORD src1_sel:WORD_1\n"
    "v_lshlrev_b32_sdwa v253, 2, v177 dst_sel:DWORD dst_unused:UNUSED_PAD src0_sel:DWORD src1_sel:WORD_0\n"
    "v_lshlrev_b32_sdwa v254, 2, v177 dst_sel:DWORD dst_unused:UNUSED_PAD src0_sel:DWORD src1_sel:WORD_1\n"
    "v_lshlrev_b32_sdwa v241, 0, v178 dst_sel:DWORD dst_unused:UNUSED_PAD src0_sel:DWORD src1_sel:WORD_0\n"
    "v_lshlrev_b32_sdwa v245, 4, v178 dst_sel:DWORD dst_unused:UNUSED_PAD src0_sel:DWORD src1_sel:WORD_1\n"
    "v_lshlrev_b32_sdwa v255, 4, v179 dst_sel:DWORD dst_unused:UNUSED_PAD src0_sel:DWORD src1_sel:WORD_0\n"
    "v_lshlrev_b32_sdwa v247, 4, v179 dst_sel:DWORD dst_unused:UNUSED_PAD src0_sel:DWORD src1_sel:WORD_1\n"
    "v_bfe_i32 v238, v172, 24, 1\n"
    "v_and_b32_e64 v238, v92, v238\n"
    "v_bfe_i32 v239, v172, 25, 1\n"
    "v_and_b32_e64 v239, v92, v239\n"
    "v_bfe_u32 v93, v172, 28, 2\n"
    "v_mul_u32_u24_e32 v93, 393216, v93\n"
    "v_mov_b32_e64 v222, v254\n"
    "v_mov_b32_e32 v254, 0\n"
    "s_nop 1\n"
    "v_mfma_f32_32x32x1_2b_f32 v[0:31], v254, v254, 0\n"
    "global_load_dword v233, v214, s[70:71]\n"
    "s_mov_b32 s48, 0x44ea44ea\n"
    "s_mov_b32 s49, 0xb77fb77f\n"
    "s_mov_b32 s50, 0x89efde6e\n"
    "s_mov_b32 s46, s42\n"
    "s_mov_b32 s47, s43\n"
    "s_mul_i32 s54, s60, 592\n"
    "s_add_u32 s54, s54, -27008\n"
    "s_mul_i32 s55, s60, -1284\n"
    "s_add_u32 s55, s55, 54688\n"
    "s_mul_i32 s56, s60, -6160\n"
    "s_add_u32 s56, s56, -27008\n"
    "s_add_u32 s98, s99, 1375872\n"
    "s_and_b32 s36, s60, 1\n"
    "s_lshl_b32 s36, s36, 13\n"
    "s_add_u32 s36, s36, 327680\n"
    "s_mul_i32 s53, s60, 98304\n"
    "s_add_u32 s98, s53, s98\n"
    "s_xor_b32 s53, s60, 1\n"
    "s_mul_i32 s53, s53, 393216\n"
    "s_add_u32 s99, s99, s53\n"
    "s_add_u32 s53, s98, 393216\n"
    "s_sub_u32 s53, s53, s36\n"
    "s_and_b32 s37, s61, 1\n"
    "s_lshl_b32 s37, s37, 2\n"
    "s_add_u32 s37, s37, s60\n"
    "s_lshl_b32 s37, s37, 12\n"
    "s_cmp_eq_u32 s61, 0\n"
    "s_cselect_b32 s67, 2, 0\n"
    "s_cmp_eq_u32 s61, 31\n"
    "s_cselect_b32 s67, 1, s67\n"
    "s_lshl_b32 s67, s67, 30\n"
    "s_mov_b64 s[68:69], s[44:45]\n"
    "s_mov_b32 s70, 0\n"
    "s_mov_b32 s71, s47\n"
    "s_mov_b64 s[72:73], s[40:41]\n"
    "s_mov_b64 s[74:75], s[42:43]\n"
    "s_mov_b32 s57, 0\n"
    "s_mov_b32 s58, 196608\n"
    "s_mov_b32 s60, 0\n"
    "s_mov_b32 s61, 393216\n"
    "s_mov_b32 s62, 786432\n"
    "s_mov_b32 s63, 1179648\n"
    "s_mov_b32 s52, 1572864\n"
    "s_mov_b32 s88, 98304\n"
    "s_mov_b32 s89, 294912\n"
    "s_mov_b32 s39, 0\n"
    "s_getpc_b64 s[76:77]\n"
    ".Lreplk_pc1:\n"
    "s_add_u32 s76, s76, .Lreplk_last-.Lreplk_pc1\n"
    "s_addc_u32 s77, s77, 0\n"
    "s_sub_u32 s76, s76, s36\n"
    "s_subb_u32 s77, s77, 0\n"
    "s_add_u32 s78, s36, .Lreplk_end-.Lreplk_last\n"
    "s_mov_b32 s79, s37\n"
    "s_add_u32 m0, s38, 0\n"
    "s_mov_b32 s64, 0\n"
    "buffer_load_dwordx4 v210, s[84:87], s64 offen offset:0 lds\n"
    "s_waitcnt vmcnt(13)\n"
    "ds_write2_b32 v216, a224, a225 offset0:0 offset1:20\n"
    "v_add3_u32 v247, v247, v93, s98\n"
    "v_add_u32_e32 v246, s98, v255\n"
    "v_add_u32_e64 v245, s99, v245\n"
    "v_add_u32_e64 v255, s99, v255\n"
    "ds_write2_b32 v216, a226, a227 offset0:40 offset1:60\n"
    "v_and_b32_e64 v93, s67, v172\n"
    "v_and_b32_e32 v94, 0x80000000, v93\n"
    "s_waitcnt vmcnt(12)\n"
    "ds_write2_b32 v216, a228, a229 offset0:80 offset1:100\n"
    "v_and_b32_e32 v93, 0x40000000, v93\n"
    "v_or_b32_e64 v246, v246, v94\n"
    "v_or_b32_e64 v247, v247, v93\n"
    "ds_write2_b32 v216, a230, a231 offset0:120 offset1:140\n"
    "v_add_u32_e32 v216, 1360, v216\n"
    "buffer_load_dwordx4 v210, s[84:87], s64 offen offset:1024 lds\n"
    "s_waitcnt vmcnt(12)\n"
    "ds_write2_b32 v216, a232, a233 offset0:0 offset1:20\n"
    "ds_write2_b32 v216, a234, a235 offset0:40 offset1:60\n"
    "buffer_load_dwordx4 v210, s[84:87], s64 offen offset:2048 lds\n"
    "s_waitcnt vmcnt(12)\n"
    "ds_write2_b32 v216, a236, a237 offset0:80 offset1:100\n"
    "s_add_u32 m0, s38, 3072\n"
    "s_mov_b32 s64, 3072\n"
    "buffer_load_dwordx4 v210, s[84:87], s64 offen offset:0 lds\n"
    "buffer_load_dwordx4 v210, s[84:87], s64 offen offset:1024 lds\n"
    "ds_write2_b32 v216, a238, a239 offset0:120 offset1:140\n"
    "buffer_load_dwordx4 v210, s[84:87], s64 offen offset:2048 lds\n"
    "s_add_u32 m0, s38, 6144\n"
    "s_mov_b32 s64, 6144\n"
    "buffer_load_dwordx4 v210, s[84:87], s64 offen offset:0 lds\n"
    "buffer_load_dwordx4 v210, s[84:87], s64 offen offset:1024 lds\n"
    "v_add_u32_e32 v216, 1360, v216\n"
    "s_waitcnt vmcnt(16)\n"
    "v_lshlrev_b32_e32 v40, 16, v224\n"
    "buffer_load_dwordx4 v210, s[84:87], s64 offen offset:2048 lds\n"
    "s_mov_b32 s65, 589824\n"
    "buffer_load_dwordx4 a[240:243], v208, s[40:43], s65 offen\n"
    "s_waitcnt vmcnt(17)\n"
    "v_lshlrev_b32_e32 v41, 16, v225\n"
    "s_mov_b32 s65, 688128\n"
    "buffer_load_dwordx4 a[244:247], v208, s[40:43], s65 offen\n"
    "s_mov_b32 s59, 49152\n"
    "buffer_load_dwordx4 v[96:99], v209, s[80:83], s59 offen offset:0\n"
    "s_waitcnt vmcnt(18)\n"
    "v_lshlrev_b32_e32 v42, 16, v226\n"
    "s_waitcnt vmcnt(17)\n"
    "v_lshlrev_b32_e32 v43, 16, v227\n"
    "s_waitcnt vmcnt(16)\n"
    "v_lshlrev_b32_e32 v44, 16, v228\n"
    "s_waitcnt vmcnt(15)\n"
    "v_lshlrev_b32_e32 v45, 16, v229\n"
    "s_waitcnt vmcnt(14)\n"
    "v_lshlrev_b32_e32 v46, 16, v230\n"
    "s_waitcnt vmcnt(13)\n"
    "v_lshlrev_b32_e32 v47, 16, v231\n"
    "v_add_f32_e64 v41, v41, v45\n"
    "v_add_f32_e64 v42, v42, v46\n"
    "v_add_f32_e64 v43, v43, v47\n"
    "v_cvt_pk_bf16_f32 v56, v40, v41\n"
    "v_cvt_pk_bf16_f32 v57, v42, v43\n"
    "v_cvt_pk_bf16_f32 v58, v44, 0\n"
    "v_cvt_pk_bf16_f32 v64, 0, v40\n"
    "buffer_load_dwordx4 v[100:103], v209, s[80:83], s59 offen offset:256\n"
    "ds_write_b128 v221, v[56:59] offset:0\n"
    "v_cvt_pk_bf16_f32 v65, v41, v42\n"
    "v_cvt_pk_bf16_f32 v66, v43, v44\n"
    "v_cvt_pk_bf16_f32 v61, v40, v41\n"
    "v_cvt_pk_bf16_f32 v62, v42, v43\n"
    "v_cvt_pk_bf16_f32 v63, v44, 0\n"
    "v_cvt_pk_bf16_f32 v69, 0, v40\n"
    "v_cvt_pk_bf16_f32 v70, v41, v42\n"
    "v_cvt_pk_bf16_f32 v71, v43, v44\n"
    "ds_write_b128 v221, v[64:67] offset:16\n"
    "buffer_load_dwordx4 v[104:107], v209, s[80:83], s59 offen offset:512\n"
    "v_and_b32_e32 v48, 0xffff0000, v224\n"
    "v_and_b32_e32 v49, 0xffff0000, v225\n"
    "v_and_b32_e32 v50, 0xffff0000, v226\n"
    "v_and_b32_e32 v51, 0xffff0000, v227\n"
    "v_and_b32_e32 v52, 0xffff0000, v228\n"
    "ds_write_b128 v221, v[60:63] offset:32\n"
    "v_and_b32_e32 v53, 0xffff0000, v229\n"
    "v_and_b32_e32 v54, 0xffff0000, v230\n"
    "v_and_b32_e32 v55, 0xffff0000, v231\n"
    "v_add_f32_e64 v49, v49, v53\n"
    "v_add_f32_e64 v50, v50, v54\n"
    "v_add_f32_e64 v51, v51, v55\n"
    "ds_write_b128 v221, v[68:71] offset:48\n"
    "s_mov_b32 s65, 983040\n"
    "buffer_load_dwordx4 a[248:251], v208, s[40:43], s65 offen\n"
    "v_cvt_pk_bf16_f32 v72, v48, v49\n"
    "v_cvt_pk_bf16_f32 v73, v50, v51\n"
    "v_cvt_pk_bf16_f32 v74, v52, 0\n"
    "v_cvt_pk_bf16_f32 v80, 0, v48\n"
    "v_cvt_pk_bf16_f32 v81, v49, v50\n"
    "v_cvt_pk_bf16_f32 v82, v51, v52\n"
    "v_cvt_pk_bf16_f32 v77, v48, v49\n"
    "v_cvt_pk_bf16_f32 v78, v50, v51\n"
    "v_cvt_pk_bf16_f32 v79, v52, 0\n"
    "v_cvt_pk_bf16_f32 v85, 0, v48\n"
    "v_cvt_pk_bf16_f32 v86, v49, v50\n"
    "v_cvt_pk_bf16_f32 v87, v51, v52\n"
    "ds_write_b128 v221, v[72:75] offset:336\n"
    "s_mov_b32 s65, 1081344\n"
    "buffer_load_dwordx4 a[252:255], v208, s[40:43], s65 offen\n"
    "v_xor_b32_e32 v224, 0, v219\n"
    "v_xor_b32_e32 v225, 32, v219\n"
    "v_xor_b32_e32 v226, 64, v219\n"
    "v_xor_b32_e32 v227, 96, v219\n"
    "ds_write_b128 v221, v[80:83] offset:352\n"
    "v_xor_b32_e32 v228, 0, v223\n"
    "v_xor_b32_e32 v229, 32, v223\n"
    "s_mov_b32 s59, 52224\n"
    "buffer_load_dwordx4 v[108:111], v209, s[80:83], s59 offen offset:0\n"
    "v_xor_b32_e32 v230, 64, v223\n"
    "v_xor_b32_e32 v231, 96, v223\n"
    "ds_write_b128 v221, v[76:79] offset:368\n"
    "v_lshl_add_u32 v196, s51, 8, v225\n"
    "v_lshl_add_u32 v197, s51, 8, v227\n"
    "v_mfma_f32_32x32x1_2b_f32 v[32:63], v254, v254, 0\n"
    "ds_write_b128 v221, v[84:87] offset:384\n"
    "buffer_load_dwordx4 v[112:115], v209, s[80:83], s59 offen offset:256\n"
    "v_mfma_f32_32x32x1_2b_f32 v[64:95], v254, v254, 0\n"
    "v_add_u32_e64 v254, s54, v242\n"
    "buffer_load_dwordx4 v[116:119], v209, s[80:83], s59 offen offset:512\n"
    "s_mov_b32 s65, 1376256\n"
    "buffer_load_dwordx4 a[176:179], v208, s[40:43], s65 offen\n"
    "s_mov_b32 s65, 1474560\n"
    "buffer_load_dwordx4 a[180:183], v208, s[40:43], s65 offen\n"
    "s_waitcnt vmcnt(12)\n"
    "s_barrier\n"
    "ds_read_b64_tr_b16 a[0:1], v220 offset:0\n"
    "ds_read_b64_tr_b16 a[2:3], v220 offset:3072\n"
    "ds_read_b64_tr_b16 a[4:5], v220 offset:8\n"
    "ds_read_b64_tr_b16 a[6:7], v220 offset:3080\n"
    "ds_read_b64_tr_b16 a[8:9], v220 offset:64\n"
    "ds_read_b64_tr_b16 a[10:11], v220 offset:3136\n"
    "ds_read_b64_tr_b16 a[12:13], v220 offset:72\n"
    "ds_read_b64_tr_b16 a[14:15], v220 offset:3144\n"
    "ds_read_b64_tr_b16 v[168:169], v218 offset:128\n"
    "ds_read_b64_tr_b16 v[170:171], v218 offset:3200\n"
    "ds_read_b64_tr_b16 a[16:17], v220 offset:6144\n"
    "ds_read_b64_tr_b16 a[18:19], v220 offset:9216\n"
    "ds_read_b64_tr_b16 a[20:21], v220 offset:6152\n"
    "ds_read_b64_tr_b16 a[22:23], v220 offset:9224\n"
    "ds_read_b64_tr_b16 a[24:25], v220 offset:6208\n"
    "ds_read_b64_tr_b16 a[26:27], v220 offset:9280\n"
    "ds_read_b64_tr_b16 a[28:29], v220 offset:6216\n"
    "ds_read_b64_tr_b16 a[30:31], v220 offset:9288\n"
    "ds_read_b64_tr_b16 v[172:173], v218 offset:6272\n"
    "s_nop 0\n"
    "s_mov_b32 s59, 0\n"
    "buffer_load_dwordx4 v[120:123], v209, s[80:83], s59 offen offset:0\n"
    "ds_read_b64_tr_b16 v[174:175], v218 offset:9344\n"
    "ds_read_b64_tr_b16 a[32:33], v220 offset:12288\n"
    "ds_read_b64_tr_b16 a[34:35], v220 offset:15360\n"
    "ds_read_b64_tr_b16 a[36:37], v220 offset:12296\n"
    "ds_read_b64_tr_b16 a[38:39], v220 offset:15368\n"
    "ds_read_b64_tr_b16 a[40:41], v220 offset:12352\n"
    "ds_read_b64_tr_b16 a[42:43], v220 offset:15424\n"
    "ds_read_b64_tr_b16 a[44:45], v220 offset:12360\n"
    "ds_read_b64_tr_b16 a[46:47], v220 offset:15432\n"
    "ds_read_b64_tr_b16 v[176:177], v218 offset:12416\n"
    "ds_read_b64_tr_b16 v[178:179], v218 offset:15488\n"
    "s_waitcnt vmcnt(12)\n"
    "ds_write2_b32 v216, a240, a241 offset0:0 offset1:20\n"
    "ds_write2_b32 v216, a242, a243 offset0:40 offset1:60\n"
    "buffer_load_dwordx4 v[124:127], v209, s[80:83], s59 offen offset:256\n"
    "s_waitcnt vmcnt(12)\n"
    "ds_write2_b32 v216, a244, a245 offset0:80 offset1:100\n"
    "s_waitcnt lgkmcnt(2)\n"
    "s_barrier\n"
    "ds_write2_b32 v216, a246, a247 offset0:120 offset1:140\n"
    "buffer_load_dwordx4 v[128:131], v209, s[80:83], s59 offen offset:512\n"
    "v_add_u32_e32 v216, 1360, v216\n"
    "s_waitcnt vmcnt(12)\n"
    "v_or3_b32 v200, v200, v96, v97\n"
    "v_or3_b32 v200, v200, v98, v99\n"
    "v_and_b32_e32 v232, 0x7fff7fff, v96\n"
    "v_add_u32_e32 v96, v201, v96\n"
    "v_add_u32_e64 v98, v201, v98\n"
    "ds_write_b128 v217, v[96:99] offset:24576\n"
    "s_mov_b32 s65, 0\n"
    "buffer_load_dwordx4 a[184:187], v208, s[40:43], s65 offen\n"
    "s_waitcnt vmcnt(12)\n"
    "v_or3_b32 v200, v200, v100, v101\n"
    "v_or3_b32 v200, v200, v102, v103\n"
    "v_add_u32_e64 v100, v203, v100\n"
    "v_add_u32_e64 v102, v203, v102\n"
    "ds_write_b128 v217, v[100:103] offset:24832\n"
    "s_mov_b32 s65, 98304\n"
    "buffer_load_dwordx4 a[188:191], v208, s[40:43], s65 offen\n"
    "s_waitcnt vmcnt(12)\n"
    "v_or3_b32 v200, v200, v104, v105\n"
    "v_or3_b32 v200, v200, v106, v107\n"
    "v_add_u32_e32 v104, v205, v104\n"
    "v_add_u32_e64 v106, v205, v106\n"
    "ds_write_b128 v217, v[104:107] offset:25088\n"
    "s_waitcnt lgkmcnt(0)\n"
    "s_barrier\n"
    "s_mov_b32 s59, 3072\n"
    "buffer_load_dwordx4 v[132:135], v209, s[80:83], s59 offen offset:0\n"
    "ds_read_b64_tr_b16 a[48:49], v224 offset:24576\n"
    "ds_read_b64_tr_b16 v[180:181], v196 offset:24576\n"
    "ds_read_b64_tr_b16 a[52:53], v226 offset:24576\n"
    "ds_read_b64_tr_b16 v[184:185], v197 offset:24576\n"
    "ds_read_b64_tr_b16 a[56:57], v228 offset:24576\n"
    "ds_read_b64_tr_b16 a[60:61], v229 offset:24576\n"
    "ds_read_b64_tr_b16 a[64:65], v230 offset:24576\n"
    "ds_read_b64_tr_b16 a[68:69], v231 offset:24576\n"
    "ds_read_b64_tr_b16 a[72:73], v224 offset:24832\n"
    "ds_read_b64_tr_b16 a[76:77], v226 offset:24832\n"
    "s_waitcnt vmcnt(12)\n"
    "ds_write2_b32 v216, a248, a249 offset0:0 offset1:20\n"
    "ds_write2_b32 v216, a250, a251 offset0:40 offset1:60\n"
    "buffer_load_dwordx4 v[136:139], v209, s[80:83], s59 offen offset:256\n"
    "s_waitcnt vmcnt(12)\n"
    "ds_write2_b32 v216, a252, a253 offset0:80 offset1:100\n"
    "ds_write2_b32 v216, a254, a255 offset0:120 offset1:140\n"
    "v_add_u32_e32 v216, 1360, v216\n"
    "buffer_load_dwordx4 v[140:143], v209, s[80:83], s59 offen offset:512\n"
    "s_waitcnt vmcnt(12)\n"
    "v_or3_b32 v200, v200, v108, v109\n"
    "v_or3_b32 v200, v200, v110, v111\n"
    "v_add_u32_e32 v108, v201, v108\n"
    "v_add_u32_e64 v110, v201, v110\n"
    "ds_write_b128 v217, v[108:111] offset:0\n"
    "s_mov_b32 s65, 393216\n"
    "buffer_load_dwordx4 a[192:195], v208, s[40:43], s65 offen\n"
    "s_waitcnt vmcnt(12)\n"
    "v_or3_b32 v200, v200, v112, v113\n"
    "v_or3_b32 v200, v200, v114, v115\n"
    "v_add_u32_e32 v112, v203, v112\n"
    "v_add_u32_e64 v114, v203, v114\n"
    "ds_write_b128 v217, v[112:115] offset:256\n"
    "v_mov_b32_e64 v112, v188\n"
    "v_mov_b32_e64 v113, v189\n"
    "v_mov_b32_e64 v114, v190\n"
    "v_mov_b32_e32 v115, v191\n"
    "s_mov_b32 s65, 491520\n"
    "buffer_load_dwordx4 a[196:199], v208, s[40:43], s65 offen\n"
    "s_waitcnt vmcnt(12)\n"
    "v_or3_b32 v200, v200, v116, v117\n"
    "v_or3_b32 v200, v200, v118, v119\n"
    "v_add_u32_e64 v116, v205, v116\n"
    "v_add_u32_e64 v118, v205, v118\n"
    "ds_write_b128 v217, v[116:119] offset:512\n"
    "v_mov_b32_e64 v116, v192\n"
    "v_mov_b32_e64 v117, v193\n"
    "v_mov_b32_e64 v118, v194\n"
    "v_mov_b32_e64 v119, v195\n"
    "s_waitcnt lgkmcnt(0)\n"
    "s_barrier\n"
    "s_mov_b32 s59, 24576\n"
    "buffer_load_dwordx4 v[144:147], v209, s[80:83], s59 offen offset:0\n"
    "ds_read_b64_tr_b16 a[50:51], v224 offset:0\n"
    "ds_read_b64_tr_b16 v[182:183], v196 offset:0\n"
    "ds_read_b64_tr_b16 a[54:55], v226 offset:0\n"
    "ds_read_b64_tr_b16 v[186:187], v197 offset:0\n"
    "ds_read_b64_tr_b16 a[58:59], v228 offset:0\n"
    "ds_read_b64_tr_b16 a[62:63], v229 offset:0\n"
    "ds_read_b64_tr_b16 a[66:67], v230 offset:0\n"
    "ds_read_b64_tr_b16 a[70:71], v231 offset:0\n"
    "ds_read_b64_tr_b16 a[74:75], v224 offset:256\n"
    "ds_read_b64_tr_b16 a[78:79], v226 offset:256\n"
    "s_waitcnt vmcnt(12)\n"
    "ds_write2_b32 v216, a176, a177 offset0:0 offset1:20\n"
    "ds_write2_b32 v216, a178, a179 offset0:40 offset1:60\n"
    "s_waitcnt lgkmcnt(8)\n"
    "ds_write_b128 v207, v[184:187] offset:36864\n"
    "buffer_load_dwordx4 v[148:151], v209, s[80:83], s59 offen offset:256\n"
    "s_waitcnt vmcnt(12)\n"
    "ds_write2_b32 v216, a180, a181 offset0:80 offset1:100\n"
    "ds_write2_b32 v216, a182, a183 offset0:120 offset1:140\n"
    "v_add_u32_e32 v216, -4800, v216\n"
    "buffer_load_dwordx4 v[152:155], v209, s[80:83], s59 offen offset:512\n"
    "s_waitcnt vmcnt(12)\n"
    "v_or3_b32 v200, v200, v120, v121\n"
    "v_or3_b32 v200, v200, v122, v123\n"
    "v_add_u32_e64 v120, v201, v120\n"
    "v_add_u32_e64 v122, v201, v122\n"
    "ds_write_b128 v217, v[120:123] offset:12288\n"
    "s_waitcnt lgkmcnt(1)\n"
    "s_barrier\n"
    "s_mov_b32 s65, 786432\n"
    "buffer_load_dwordx4 a[200:203], v208, s[40:43], s65 offen\n"
    "s_waitcnt vmcnt(12)\n"
    "v_or3_b32 v200, v200, v124, v125\n"
    "v_or3_b32 v200, v200, v126, v127\n"
    "v_add_u32_e32 v124, v203, v124\n"
    "v_add_u32_e64 v126, v203, v126\n"
    "ds_write_b128 v217, v[124:127] offset:12544\n"
    "s_mov_b32 s65, 884736\n"
    "buffer_load_dwordx4 a[204:207], v208, s[40:43], s65 offen\n"
    "s_waitcnt vmcnt(12)\n"
    "v_or3_b32 v200, v200, v128, v129\n"
    "v_or3_b32 v200, v200, v130, v131\n"
    "v_add_u32_e32 v128, v205, v128\n"
    "v_add_u32_e64 v130, v205, v130\n"
    "ds_write_b128 v217, v[128:131] offset:12800\n"
    "ds_read_b128 a[224:227], v250 offset:0\n"
    "ds_read_b128 a[228:231], v250 offset:320\n"
    "ds_read_b128 a[232:235], v251 offset:0\n"
    "ds_read_b128 a[236:239], v251 offset:336\n"
    "ds_read_b128 a[240:243], v250 offset:80\n"
    "ds_read_b128 a[244:247], v250 offset:400\n"
    "ds_read_b128 a[248:251], v251 offset:672\n"
    "ds_read_b128 a[252:255], v251 offset:1008\n"
    "ds_read_b128 v[104:107], v252 offset:1344\n"
    "ds_read_b128 v[108:111], v252 offset:1680\n"
    "ds_read_b128 v[128:131], v252 offset:2016\n"
    "s_waitcnt lgkmcnt(11)\n"
    "s_barrier\n"
    "s_mov_b32 s59, 27648\n"
    "buffer_load_dwordx4 v[156:159], v209, s[80:83], s59 offen offset:0\n"
    "ds_read_b64_tr_b16 a[80:81], v224 offset:12288\n"
    "ds_read_b64_tr_b16 a[84:85], v225 offset:12288\n"
    "ds_read_b64_tr_b16 a[88:89], v226 offset:12288\n"
    "ds_read_b64_tr_b16 a[92:93], v227 offset:12288\n"
    "s_waitcnt lgkmcnt(12)\n"
    "v_smfmac_f32_16x16x64_bf16 v[0:3], a[232:235], a[224:231], v240\n"
    "s_waitcnt lgkmcnt(11)\n"
    "ds_read_b64_tr_b16 a[96:97], v228 offset:12288\n"
    "ds_read_b64_tr_b16 a[100:101], v229 offset:12288\n"
    "v_smfmac_f32_16x16x64_bf16 v[4:7], a[236:239], a[224:231], v240 abid:1\n"
    "ds_read_b64_tr_b16 a[104:105], v230 offset:12288\n"
    "ds_read_b64_tr_b16 a[108:109], v231 offset:12288\n"
    "ds_read_b64_tr_b16 a[112:113], v224 offset:12544\n"
    "ds_read_b64_tr_b16 a[116:117], v225 offset:12544\n"
    "ds_read_b64_tr_b16 a[120:121], v226 offset:12544\n"
    "s_waitcnt lgkmcnt(15)\n"
    "v_smfmac_f32_16x16x64_bf16 v[8:11], a[248:251], a[240:247], v240\n"
    "s_waitcnt lgkmcnt(14)\n"
    "ds_read_b64_tr_b16 a[124:125], v227 offset:12544\n"
    "s_waitcnt vmcnt(12)\n"
    "ds_write2_b32 v216, a184, a185 offset0:0 offset1:20\n"
    "v_smfmac_f32_16x16x64_bf16 v[12:15], a[252:255], a[240:247], v240 abid:1\n"
    "s_nop 0\n"
    "ds_write2_b32 v216, a186, a187 offset0:40 offset1:60\n"
    "ds_read_b128 a[224:227], v250 offset:160\n"
    "ds_read_b128 a[228:231], v250 offset:480\n"
    "ds_read_b128 a[232:235], v251 offset:1344\n"
    "ds_read_b128 a[236:239], v251 offset:1680\n"
    "buffer_load_dwordx4 v[160:163], v209, s[80:83], s59 offen offset:256\n"
    "s_waitcnt vmcnt(12)\n"
    "ds_write2_b32 v216, a188, a189 offset0:80 offset1:100\n"
    "ds_write2_b32 v216, a190, a191 offset0:120 offset1:140\n"
    "v_add_u32_e32 v216, 1360, v216\n"
    "ds_read_b128 a[240:243], v250 offset:240\n"
    "ds_read_b128 a[244:247], v250 offset:560\n"
    "buffer_load_dwordx4 v[164:167], v209, s[80:83], s59 offen offset:512\n"
    "s_waitcnt vmcnt(12)\n"
    "v_or3_b32 v200, v200, v132, v133\n"
    "v_or3_b32 v200, v200, v134, v135\n"
    "v_add_u32_e64 v132, v201, v132\n"
    "v_add_u32_e64 v134, v201, v134\n"
    "ds_write_b128 v217, v[132:135] offset:24576\n"
    "s_waitcnt lgkmcnt(6)\n"
    "v_smfmac_f32_16x16x64_bf16 v[16:19], a[232:235], a[224:231], v240\n"
    "s_waitcnt lgkmcnt(5)\n"
    "v_smfmac_f32_16x16x64_bf16 v[20:23], a[236:239], a[224:231], v240 abid:1\n"
    "s_mov_b32 s65, 1179648\n"
    "buffer_load_dwordx4 a[208:211], v208, s[40:43], s65 offen\n"
    "s_waitcnt vmcnt(12)\n"
    "v_or3_b32 v200, v200, v136, v137\n"
    "v_or3_b32 v200, v200, v138, v139\n"
    "v_add_u32_e32 v136, v203, v136\n"
    "v_add_u32_e64 v138, v203, v138\n"
    "ds_write_b128 v217, v[136:139] offset:24832\n"
    "ds_read_b128 a[248:251], v251 offset:2016\n"
    "ds_read_b128 a[252:255], v251 offset:2352\n"
    "s_mov_b32 s65, 1277952\n"
    "buffer_load_dwordx4 a[212:215], v208, s[40:43], s65 offen\n"
    "s_waitcnt vmcnt(12)\n"
    "v_or3_b32 v200, v200, v140, v141\n"
    "v_or3_b32 v200, v200, v142, v143\n"
    "v_add_u32_e32 v140, v205, v140\n"
    "v_add_u32_e64 v142, v205, v142\n"
    "ds_write_b128 v217, v[140:143] offset:25088\n"
    "ds_read_b128 a[224:227], v250 offset:27024\n"
    "ds_read_b128 a[228:231], v250 offset:27344\n"
    "ds_read_b128 a[232:235], v251 offset:10752\n"
    "ds_read_b128 a[236:239], v251 offset:11088\n"
    "ds_read_b128 v[132:135], v252 offset:2352\n"
    "s_waitcnt lgkmcnt(7)\n"
    "v_smfmac_f32_16x16x64_bf16 v[24:27], a[248:251], a[240:247], v240\n"
    "ds_read_b128 a[248:251], v251 offset:11424\n"
    "s_waitcnt lgkmcnt(7)\n"
    "v_smfmac_f32_16x16x64_bf16 v[28:31], a[252:255], a[240:247], v240 abid:1\n"
    "ds_read_b128 a[240:243], v250 offset:27104\n"
    "ds_read_b128 a[244:247], v250 offset:27424\n"
    "ds_read_b128 a[252:255], v251 offset:11760\n"
    "s_waitcnt lgkmcnt(9)\n"
    "s_barrier\n"
    "s_mov_b32 s65, 1572864\n"
    "buffer_load_dwordx4 a[216:219], v208, s[40:43], s65 offen\n"
    "ds_read_b64_tr_b16 a[82:83], v224 offset:24576\n"
    "ds_read_b64_tr_b16 a[86:87], v225 offset:24576\n"
    "ds_read_b64_tr_b16 a[90:91], v226 offset:24576\n"
    "s_waitcnt lgkmcnt(9)\n"
    "v_smfmac_f32_16x16x64_bf16 v[32:35], a[232:235], a[224:231], v240\n"
    "s_waitcnt lgkmcnt(8)\n"
    "ds_read_b64_tr_b16 a[94:95], v227 offset:24576\n"
    "ds_read_b64_tr_b16 a[98:99], v228 offset:24576\n"
    "v_smfmac_f32_16x16x64_bf16 v[36:39], a[236:239], a[224:231], v240 abid:1\n"
    "ds_read_b64_tr_b16 a[102:103], v229 offset:24576\n"
    "ds_read_b64_tr_b16 a[106:107], v230 offset:24576\n"
    "ds_read_b64_tr_b16 a[110:111], v231 offset:24576\n"
    "ds_read_b64_tr_b16 a[114:115], v224 offset:24832\n"
    "ds_read_b64_tr_b16 a[118:119], v225 offset:24832\n"
    "ds_read_b64_tr_b16 a[122:123], v226 offset:24832\n"
    "ds_read_b64_tr_b16 a[126:127], v227 offset:24832\n"
    "s_waitcnt lgkmcnt(13)\n"
    "v_smfmac_f32_16x16x64_bf16 v[40:43], a[248:251], a[240:247], v240\n"
    "s_waitcnt lgkmcnt(12)\n"
    "s_waitcnt vmcnt(12)\n"
    "ds_write2_b32 v216, a192, a193 offset0:0 offset1:20\n"
    "v_smfmac_f32_16x16x64_bf16 v[44:47], a[252:255], a[240:247], v240 abid:1\n"
    "s_nop 0\n"
    "ds_write2_b32 v216, a194, a195 offset0:40 offset1:60\n"
    "ds_read_b128 a[224:227], v250 offset:27184\n"
    "ds_read_b128 a[228:231], v250 offset:27504\n"
    "ds_read_b128 a[232:235], v251 offset:12096\n"
    "s_mov_b32 s65, 1671168\n"
    "buffer_load_dwordx4 a[220:223], v208, s[40:43], s65 offen\n"
    "s_waitcnt vmcnt(12)\n"
    "ds_write2_b32 v216, a196, a197 offset0:80 offset1:100\n"
    "ds_write2_b32 v216, a198, a199 offset0:120 offset1:140\n"
    "v_add_u32_e32 v216, 1360, v216\n"
    "ds_read_b128 a[236:239], v251 offset:12432\n"
    "ds_read_b128 a[240:243], v250 offset:27264\n"
    "ds_read_b128 a[244:247], v250 offset:27584\n"
    "s_waitcnt vmcnt(11)\n"
    "v_or3_b32 v200, v200, v144, v145\n"
    "v_or3_b32 v200, v200, v146, v147\n"
    "v_add_u32_e64 v144, v201, v144\n"
    "v_add_u32_e32 v146, v201, v146\n"
    "ds_write_b128 v217, v[144:147] offset:0\n"
    "s_waitcnt lgkmcnt(6)\n"
    "v_smfmac_f32_16x16x64_bf16 v[48:51], a[232:235], a[224:231], v240\n"
    "ds_read_b128 a[248:251], v251 offset:12768\n"
    "ds_read_b128 a[252:255], v251 offset:13104\n"
    "ds_read_b128 a[232:235], v251 offset:21504\n"
    "s_waitcnt vmcnt(10)\n"
    "v_or3_b32 v200, v200, v148, v149\n"
    "v_or3_b32 v200, v200, v150, v151\n"
    "v_add_u32_e32 v148, v203, v148\n"
    "v_add_u32_e32 v150, v203, v150\n"
    "ds_write_b128 v217, v[148:151] offset:256\n"
    "s_waitcnt lgkmcnt(6)\n"
    "v_smfmac_f32_16x16x64_bf16 v[52:55], a[236:239], a[224:231], v240 abid:1\n"
    "ds_read_b128 a[224:227], v250 offset:54048\n"
    "ds_read_b128 a[228:231], v250 offset:54368\n"
    "ds_read_b128 a[236:239], v251 offset:21840\n"
    "s_waitcnt vmcnt(9)\n"
    "v_or3_b32 v200, v200, v152, v153\n"
    "v_or3_b32 v200, v200, v154, v155\n"
    "v_add_u32_e32 v152, v205, v152\n"
    "v_add_u32_e32 v154, v205, v154\n"
    "ds_write_b128 v217, v[152:155] offset:512\n"
    "s_waitcnt lgkmcnt(6)\n"
    "v_smfmac_f32_16x16x64_bf16 v[56:59], a[248:251], a[240:247], v240\n"
    "v_smfmac_f32_16x16x64_bf16 v[60:63], a[252:255], a[240:247], v240 abid:1\n"
    "ds_read_b128 a[240:243], v250 offset:54128\n"
    "ds_read_b128 a[244:247], v250 offset:54448\n"
    "ds_read_b128 a[248:251], v251 offset:22176\n"
    "ds_read_b128 a[252:255], v251 offset:22512\n"
    "ds_read_b128 v[144:147], v252 offset:10752\n"
    "ds_read_b128 v[148:151], v252 offset:11088\n"
    "s_waitcnt lgkmcnt(8)\n"
    "v_smfmac_f32_16x16x64_bf16 v[64:67], a[232:235], a[224:231], v240\n"
    "ds_read_b128 a[232:235], v251 offset:22848\n"
    "s_waitcnt lgkmcnt(8)\n"
    "v_smfmac_f32_16x16x64_bf16 v[68:71], a[236:239], a[224:231], v240 abid:1\n"
    "ds_read_b128 a[224:227], v250 offset:54208\n"
    "ds_read_b128 a[228:231], v250 offset:54528\n"
    "s_waitcnt lgkmcnt(9)\n"
    "s_barrier\n"
    "ds_read_b64_tr_b16 a[128:129], v224 offset:0\n"
    "ds_read_b64_tr_b16 a[132:133], v225 offset:0\n"
    "ds_read_b64_tr_b16 a[136:137], v226 offset:0\n"
    "s_waitcnt lgkmcnt(9)\n"
    "v_smfmac_f32_16x16x64_bf16 v[72:75], a[248:251], a[240:247], v240\n"
    "s_waitcnt lgkmcnt(8)\n"
    "ds_read_b64_tr_b16 a[140:141], v227 offset:0\n"
    "ds_read_b64_tr_b16 a[144:145], v228 offset:0\n"
    "v_smfmac_f32_16x16x64_bf16 v[76:79], a[252:255], a[240:247], v240 abid:1\n"
    "ds_read_b64_tr_b16 a[148:149], v229 offset:0\n"
    "ds_read_b64_tr_b16 a[152:153], v230 offset:0\n"
    "ds_read_b64_tr_b16 a[156:157], v231 offset:0\n"
    "ds_read_b64_tr_b16 a[160:161], v224 offset:256\n"
    "ds_read_b64_tr_b16 a[164:165], v225 offset:256\n"
    "ds_read_b64_tr_b16 a[168:169], v226 offset:256\n"
    "ds_read_b64_tr_b16 a[172:173], v227 offset:256\n"
    "s_waitcnt vmcnt(8)\n"
    "ds_write2_b32 v216, a200, a201 offset0:0 offset1:20\n"
    "s_waitcnt lgkmcnt(13)\n"
    "v_smfmac_f32_16x16x64_bf16 v[80:83], a[232:235], a[224:231], v240\n"
    "ds_write2_b32 v216, a202, a203 offset0:40 offset1:60\n"
    "ds_read_b128 a[236:239], v251 offset:23184\n"
    "ds_read_b128 a[240:243], v250 offset:54288\n"
    "ds_read_b128 a[244:247], v250 offset:54608\n"
    "ds_read_b128 a[248:251], v251 offset:23520\n"
    "s_waitcnt vmcnt(7)\n"
    "ds_write2_b32 v216, a204, a205 offset0:80 offset1:100\n"
    "ds_write2_b32 v216, a206, a207 offset0:120 offset1:140\n"
    "v_add_u32_e32 v216, 1360, v216\n"
    "ds_read_b128 a[252:255], v251 offset:23856\n"
    "ds_read_b128 a[232:235], v252 offset:0\n"
    "s_waitcnt vmcnt(6)\n"
    "v_or3_b32 v200, v200, v156, v157\n"
    "v_or3_b32 v200, v200, v158, v159\n"
    "v_add_u32_e64 v156, v201, v156\n"
    "v_add_u32_e64 v158, v201, v158\n"
    "ds_write_b128 v217, v[156:159] offset:12288\n"
    "s_waitcnt lgkmcnt(7)\n"
    "v_smfmac_f32_16x16x64_bf16 v[84:87], a[236:239], a[224:231], v240 abid:1\n"
    "s_waitcnt lgkmcnt(5)\n"
    "v_smfmac_f32_16x16x64_bf16 v[88:91], a[248:251], a[240:247], v240\n"
    "ds_read_b128 a[236:239], v252 offset:336\n"
    "ds_read_b128 a[248:251], v252 offset:672\n"
    "s_waitcnt vmcnt(5)\n"
    "v_or3_b32 v200, v200, v160, v161\n"
    "v_or3_b32 v200, v200, v162, v163\n"
    "v_add_u32_e32 v160, v203, v160\n"
    "v_add_u32_e64 v162, v203, v162\n"
    "ds_write_b128 v217, v[160:163] offset:12544\n"
    "s_waitcnt lgkmcnt(4)\n"
    "v_smfmac_f32_16x16x64_bf16 v[92:95], a[252:255], a[240:247], v240 abid:1\n"
    "ds_read_b128 a[252:255], v252 offset:1008\n"
    "ds_read_b128 v[160:163], v252 offset:11424\n"
    "s_waitcnt vmcnt(4)\n"
    "v_or3_b32 v200, v200, v164, v165\n"
    "v_or3_b32 v200, v200, v166, v167\n"
    "v_add_u32_e64 v164, v205, v164\n"
    "v_add_u32_e64 v166, v205, v166\n"
    "ds_write_b128 v217, v[164:167] offset:12800\n"
    "v_and_b32_e32 v200, 0x40004000, v200\n"
    "v_cmp_le_u32_e32 vcc, 1, v200\n"
    "s_nop 1\n"
    "s_cmp_lg_u64 vcc, 0\n"
    "s_cselect_b32 vcc_lo, 1, 0\n"
    "s_or_b32 s66, s66, vcc_lo\n"
    "v_cmp_le_u32_e32 vcc, 981467136, v232\n"
    "s_nop 1\n"
    "s_cmp_lg_u64 vcc, 0\n"
    "s_cselect_b32 vcc_lo, 2, 0\n"
    "s_or_b32 s66, s66, vcc_lo\n"
    "v_mov_b32_e32 v200, s66\n"
    "v_mov_b32_e32 v232, 162992\n"
    "s_mov_b64 exec, 1\n"
    "ds_or_b32 v232, v200\n"
    "s_mov_b64 exec, -1\n"
    "ds_read_b128 v[164:167], v252 offset:11760\n"
    "s_waitcnt lgkmcnt(2)\n"
    "s_barrier\n"
    "ds_read_b64_tr_b16 a[130:131], v224 offset:12288\n"
    "ds_read_b64_tr_b16 a[134:135], v225 offset:12288\n"
    "ds_read_b64_tr_b16 a[138:139], v226 offset:12288\n"
    "ds_read_b64_tr_b16 a[142:143], v227 offset:12288\n"
    "ds_read_b64_tr_b16 a[146:147], v228 offset:12288\n"
    "ds_read_b64_tr_b16 a[150:151], v229 offset:12288\n"
    "ds_read_b64_tr_b16 a[154:155], v230 offset:12288\n"
    "ds_read_b64_tr_b16 a[158:159], v231 offset:12288\n"
    "ds_read_b64_tr_b16 a[162:163], v224 offset:12544\n"
    "ds_read_b64_tr_b16 a[166:167], v225 offset:12544\n"
    "ds_read_b64_tr_b16 a[170:171], v226 offset:12544\n"
    "ds_read_b64_tr_b16 a[174:175], v227 offset:12544\n"
    "s_nop 0\n"
    "s_waitcnt vmcnt(3)\n"
    "ds_write2_b32 v216, a208, a209 offset0:0 offset1:20\n"
    "ds_write2_b32 v216, a210, a211 offset0:40 offset1:60\n"
    "s_waitcnt vmcnt(2)\n"
    "ds_write2_b32 v216, a212, a213 offset0:80 offset1:100\n"
    "s_waitcnt lgkmcnt(2)\n"
    "s_barrier\n"
    "s_add_u32 m0, s38, 0\n"
    "s_mov_b32 s64, 18432\n"
    "buffer_load_dwordx4 v210, s[84:87], s64 offen offset:0 lds\n"
    "buffer_load_dwordx4 v210, s[84:87], s64 offen offset:1024 lds\n"
    "buffer_load_dwordx4 v210, s[84:87], s64 offen offset:2048 lds\n"
    "s_add_u32 m0, s38, 3072\n"
    "s_mov_b32 s64, 21504\n"
    "buffer_load_dwordx4 v210, s[84:87], s64 offen offset:0 lds\n"
    "buffer_load_dwordx4 v210, s[84:87], s64 offen offset:1024 lds\n"
    "buffer_load_dwordx4 v210, s[84:87], s64 offen offset:2048 lds\n"
    "s_add_u32 m0, s38, 6144\n"
    "s_mov_b32 s64, 24576\n"
    "buffer_load_dwordx4 v210, s[84:87], s64 offen offset:0 lds\n"
    "buffer_load_dwordx4 v210, s[84:87], s64 offen offset:1024 lds\n"
    "buffer_load_dwordx4 v210, s[84:87], s64 offen offset:2048 lds\n"
    "s_sub_u32 s80, s76, .Lreplk_last-.Lreplk_loop\n"
    "s_subb_u32 s81, s77, 0\n"
    "s_add_u32 s82, s36, .Lreplk_last-.Lreplk_loop\n"
    "s_and_b32 s85, s37, 0x3fff\n"
    "s_add_u32 s84, s85, 8192\n"
    "s_lshr_b32 s85, s85, 2\n"
    "v_lshlrev_b32_e64 v235, 3, v244\n"
    "s_mov_b32 exec_hi, 0\n"
    "buffer_load_dword v233, v235, s[40:43], s53 offen sc1\n"
    "s_mov_b32 exec_hi, -1\n"
    "s_add_u32 s53, s53, 393216\n"
    "ds_write2_b32 v216, a214, a215 offset0:120 offset1:140\n"
    "v_add_u32_e32 v216, 1360, v216\n"
    "s_mov_b32 s64, 0\n"
    "s_mov_b32 s59, 0\n"
    "ds_read_b32 v232, v232\n"
    "v_lshlrev_b32_e32 v235, 3, v244\n"
    "s_mov_b32 exec_hi, 0\n"
    "buffer_load_dword v233, v235, s[40:43], s53 offen sc1\n"
    "s_mov_b32 exec_hi, -1\n"
    "s_add_u32 s53, s53, 393216\n"
    "s_waitcnt vmcnt(12)\n"
    "ds_write2_b32 v216, a216, a217 offset0:0 offset1:20\n"
    "ds_write2_b32 v216, a218, a219 offset0:40 offset1:60\n"
    "v_lshlrev_b32_e32 v235, 3, v244\n"
    "s_mov_b32 exec_hi, 0\n"
    "buffer_load_dword v233, v235, s[40:43], s53 offen sc1\n"
    "s_mov_b32 exec_hi, -1\n"
    "s_add_u32 s53, s53, 393216\n"
    "s_waitcnt vmcnt(12)\n"
    "ds_write2_b32 v216, a220, a221 offset0:80 offset1:100\n"
    "ds_write2_b32 v216, a222, a223 offset0:120 offset1:140\n"
    "v_lshlrev_b32_e64 v235, 3, v244\n"
    "s_mov_b32 exec_hi, 0\n"
    "buffer_load_dword v233, v235, s[40:43], s53 offen sc1\n"
    "s_mov_b32 exec_hi, -1\n"
    "s_add_u32 s53, s53, 393216\n"
    "v_mov_b32_e64 v235, v222\n"
    "s_waitcnt lgkmcnt(0)\n"
    "s_barrier\n"
    "s_getpc_b64 vcc\n"
    ".Lreplk_pc2:\n"
    "s_sub_u32 vcc_lo, vcc_lo, .Lreplk_pc2-.Lreplk_pc0\n"
    "s_subb_u32 vcc_hi, vcc_hi, 0\n"
    "global_load_dword v233, v215, vcc\n"
    "ds_read_b128 a[224:227], v250 offset:640\n"
    "ds_read_b128 a[228:231], v250 offset:960\n"
    "ds_read_b128 a[240:243], v250 offset:720\n"
    "ds_read_b128 a[244:247], v250 offset:1040\n"
    "ds_read_b128 v[96:99], v250 offset:800\n"
    "ds_read_b128 v[100:103], v250 offset:1120\n"
    "ds_read_b128 v[120:123], v250 offset:880\n"
    "ds_read_b128 v[124:127], v250 offset:1200\n"
    "ds_read_b128 v[136:139], v250 offset:27664\n"
    "ds_read_b128 v[140:143], v250 offset:27984\n"
    "ds_read_b128 v[152:155], v250 offset:27744\n"
    "ds_read_b128 v[156:159], v250 offset:28064\n"
    "s_waitcnt lgkmcnt(10)\n"
    "v_smfmac_f32_16x16x64_bf16 v[0:3], a[232:235], a[224:231], v240\n"
    "s_nop 0\n"
    "ds_read_b128 a[232:235], v252 offset:12096\n"
    "v_smfmac_f32_16x16x64_bf16 v[4:7], a[236:239], a[224:231], v240 abid:1\n"
    "ds_read_b128 a[224:227], v250 offset:27824\n"
    "s_waitcnt lgkmcnt(10)\n"
    "v_smfmac_f32_16x16x64_bf16 v[8:11], a[248:251], a[240:247], v240\n"
    "ds_read_b128 a[228:231], v250 offset:28144\n"
    "v_smfmac_f32_16x16x64_bf16 v[12:15], a[252:255], a[240:247], v240 abid:1\n"
    "s_waitcnt lgkmcnt(9)\n"
    "ds_read_b128 a[236:239], v252 offset:12432\n"
    "v_smfmac_f32_16x16x64_bf16 v[16:19], v[104:107], v[96:103], v240\n"
    "ds_read_b128 a[240:243], v250 offset:27904\n"
    "v_smfmac_f32_16x16x64_bf16 v[20:23], v[108:111], v[96:103], v240 abid:1\n"
    "s_waitcnt lgkmcnt(9)\n"
    "ds_read_b128 a[244:247], v250 offset:28224\n"
    "v_smfmac_f32_16x16x64_bf16 v[24:27], v[128:131], v[120:127], v240\n"
    "ds_read_b128 a[248:251], v252 offset:12768\n"
    "v_smfmac_f32_16x16x64_bf16 v[28:31], v[132:135], v[120:127], v240 abid:1\n"
    "s_waitcnt lgkmcnt(9)\n"
    "ds_read_b128 a[252:255], v252 offset:13104\n"
    "v_smfmac_f32_16x16x64_bf16 v[32:35], v[144:147], v[136:143], v240\n"
    "s_nop 0\n"
    "ds_read_b128 v[96:99], v250 offset:54688\n"
    "v_smfmac_f32_16x16x64_bf16 v[36:39], v[148:151], v[136:143], v240 abid:1\n"
    "s_waitcnt lgkmcnt(9)\n"
    "ds_read_b128 v[100:103], v250 offset:55008\n"
    "v_smfmac_f32_16x16x64_bf16 v[40:43], v[160:163], v[152:159], v240\n"
    "ds_read_b128 v[104:107], v252 offset:21504\n"
    "v_smfmac_f32_16x16x64_bf16 v[44:47], v[164:167], v[152:159], v240 abid:1\n"
    "ds_read_b128 v[108:111], v252 offset:21840\n"
    "ds_read_b128 v[120:123], v250 offset:54768\n"
    "ds_read_b128 v[124:127], v250 offset:55088\n"
    "v_pk_mov_b32 v[224:225], v[0:1], v[0:1] op_sel:[0,1]\n"
    "v_pk_mov_b32 v[226:227], v[2:3], v[2:3] op_sel:[0,1]\n"
    "v_pk_mul_f32 v[228:229], v[0:1], v[0:1]\n"
    "v_pk_mul_f32 v[230:231], v[2:3], v[2:3]\n"
    "v_pk_add_f32 v[224:225], v[224:225], v[4:5]\n"
    "v_pk_add_f32 v[226:227], v[226:227], v[6:7]\n"
    "v_pk_fma_f32 v[228:229], v[4:5], v[4:5], v[228:229]\n"
    "v_pk_fma_f32 v[230:231], v[6:7], v[6:7], v[230:231]\n"
    "v_pk_add_f32 v[224:225], v[224:225], v[8:9]\n"
    "v_pk_add_f32 v[226:227], v[226:227], v[10:11]\n"
    "v_pk_fma_f32 v[228:229], v[8:9], v[8:9], v[228:229]\n"
    "v_pk_fma_f32 v[230:231], v[10:11], v[10:11], v[230:231]\n"
    "v_pk_add_f32 v[224:225], v[224:225], v[12:13]\n"
    "v_pk_add_f32 v[226:227], v[226:227], v[14:15]\n"
    "v_pk_fma_f32 v[228:229], v[12:13], v[12:13], v[228:229]\n"
    "v_pk_fma_f32 v[230:231], v[14:15], v[14:15], v[230:231]\n"
    "v_pk_add_f32 v[224:225], v[224:225], v[16:17]\n"
    "v_pk_add_f32 v[226:227], v[226:227], v[18:19]\n"
    "v_pk_fma_f32 v[228:229], v[16:17], v[16:17], v[228:229]\n"
    "v_pk_fma_f32 v[230:231], v[18:19], v[18:19], v[230:231]\n"
    "v_pk_add_f32 v[224:225], v[224:225], v[20:21]\n"
    "v_pk_add_f32 v[226:227], v[226:227], v[22:23]\n"
    "v_pk_fma_f32 v[228:229], v[20:21], v[20:21], v[228:229]\n"
    "v_pk_fma_f32 v[230:231], v[22:23], v[22:23], v[230:231]\n"
    "v_pk_add_f32 v[224:225], v[224:225], v[24:25]\n"
    "v_pk_add_f32 v[226:227], v[226:227], v[26:27]\n"
    "v_pk_fma_f32 v[228:229], v[24:25], v[24:25], v[228:229]\n"
    "v_pk_fma_f32 v[230:231], v[26:27], v[26:27], v[230:231]\n"
    "v_pk_add_f32 v[224:225], v[224:225], v[28:29]\n"
    "v_pk_add_f32 v[226:227], v[226:227], v[30:31]\n"
    "v_pk_fma_f32 v[228:229], v[28:29], v[28:29], v[228:229]\n"
    "v_pk_fma_f32 v[230:231], v[30:31], v[30:31], v[230:231]\n"
    "v_pk_add_f32 v[224:225], v[224:225], v[32:33]\n"
    "v_pk_add_f32 v[226:227], v[226:227], v[34:35]\n"
    "v_pk_fma_f32 v[228:229], v[32:33], v[32:33], v[228:229]\n"
    "v_pk_fma_f32 v[230:231], v[34:35], v[34:35], v[230:231]\n"
    "v_pk_add_f32 v[224:225], v[224:225], v[36:37]\n"
    "v_pk_add_f32 v[226:227], v[226:227], v[38:39]\n"
    "v_pk_fma_f32 v[228:229], v[36:37], v[36:37], v[228:229]\n"
    "v_pk_fma_f32 v[230:231], v[38:39], v[38:39], v[230:231]\n"
    "v_pk_add_f32 v[224:225], v[224:225], v[40:41]\n"
    "v_pk_add_f32 v[226:227], v[226:227], v[42:43]\n"
    "v_pk_fma_f32 v[228:229], v[40:41], v[40:41], v[228:229]\n"
    "v_pk_fma_f32 v[230:231], v[42:43], v[42:43], v[230:231]\n"
    "v_pk_add_f32 v[224:225], v[224:225], v[44:45]\n"
    "v_pk_add_f32 v[226:227], v[226:227], v[46:47]\n"
    "v_pk_fma_f32 v[228:229], v[44:45], v[44:45], v[228:229]\n"
    "v_pk_fma_f32 v[230:231], v[46:47], v[46:47], v[230:231]\n"
    "s_waitcnt lgkmcnt(0)\n"
    "v_smfmac_f32_16x16x64_bf16 v[48:51], a[232:235], a[224:231], v240\n"
    "s_nop 0\n"
    "ds_read_b128 v[128:131], v252 offset:22176\n"
    "v_smfmac_f32_16x16x64_bf16 v[52:55], a[236:239], a[224:231], v240 abid:1\n"
    "ds_read_b128 v[132:135], v252 offset:22512\n"
    "v_smfmac_f32_16x16x64_bf16 v[56:59], a[248:251], a[240:247], v240\n"
    "ds_read_b128 v[136:139], v250 offset:54848\n"
    "v_smfmac_f32_16x16x64_bf16 v[60:63], a[252:255], a[240:247], v240 abid:1\n"
    "ds_read_b128 v[140:143], v250 offset:55168\n"
    "v_smfmac_f32_16x16x64_bf16 v[64:67], v[104:107], v[96:103], v240\n"
    "ds_read_b128 v[144:147], v252 offset:22848\n"
    "v_smfmac_f32_16x16x64_bf16 v[68:71], v[108:111], v[96:103], v240 abid:1\n"
    "ds_read_b128 v[148:151], v252 offset:23184\n"
    "ds_read_b128 v[152:155], v250 offset:54928\n"
    "v_accvgpr_write_b32 a248, v236\n"
    "ds_read_b128 v[156:159], v250 offset:55248\n"
    "v_pk_add_f32 v[224:225], v[224:225], v[48:49]\n"
    "v_pk_add_f32 v[226:227], v[226:227], v[50:51]\n"
    "v_pk_fma_f32 v[228:229], v[48:49], v[48:49], v[228:229]\n"
    "v_pk_fma_f32 v[230:231], v[50:51], v[50:51], v[230:231]\n"
    "v_pk_add_f32 v[224:225], v[224:225], v[52:53]\n"
    "s_waitcnt vmcnt(5)\n"
    "s_barrier\n"
    "ds_read_b64_tr_b16 a[176:177], v220 offset:0\n"
    "ds_read_b64_tr_b16 a[178:179], v220 offset:3072\n"
    "v_pk_add_f32 v[226:227], v[226:227], v[54:55]\n"
    "ds_read_b64_tr_b16 a[180:181], v220 offset:8\n"
    "ds_read_b64_tr_b16 a[182:183], v220 offset:3080\n"
    "ds_read_b64_tr_b16 a[184:185], v220 offset:64\n"
    "v_pk_fma_f32 v[228:229], v[52:53], v[52:53], v[228:229]\n"
    "ds_read_b64_tr_b16 a[186:187], v220 offset:3136\n"
    "ds_read_b64_tr_b16 a[188:189], v220 offset:72\n"
    "ds_read_b64_tr_b16 a[190:191], v220 offset:3144\n"
    "v_pk_fma_f32 v[230:231], v[54:55], v[54:55], v[230:231]\n"
    "ds_read_b64_tr_b16 v[188:189], v218 offset:128\n"
    "ds_read_b64_tr_b16 v[190:191], v218 offset:3200\n"
    "ds_read_b64_tr_b16 a[192:193], v220 offset:6144\n"
    "v_pk_add_f32 v[224:225], v[224:225], v[56:57]\n"
    "ds_read_b64_tr_b16 a[194:195], v220 offset:9216\n"
    "ds_read_b64_tr_b16 a[196:197], v220 offset:6152\n"
    "ds_read_b64_tr_b16 a[198:199], v220 offset:9224\n"
    "v_pk_add_f32 v[226:227], v[226:227], v[58:59]\n"
    "ds_read_b64_tr_b16 a[200:201], v220 offset:6208\n"
    "ds_read_b64_tr_b16 a[202:203], v220 offset:9280\n"
    "ds_read_b64_tr_b16 a[204:205], v220 offset:6216\n"
    "v_pk_fma_f32 v[228:229], v[56:57], v[56:57], v[228:229]\n"
    "ds_read_b64_tr_b16 a[206:207], v220 offset:9288\n"
    "ds_read_b64_tr_b16 v[192:193], v218 offset:6272\n"
    "ds_read_b64_tr_b16 v[194:195], v218 offset:9344\n"
    "v_pk_fma_f32 v[230:231], v[58:59], v[58:59], v[230:231]\n"
    "ds_read_b64_tr_b16 a[208:209], v220 offset:12288\n"
    "ds_read_b64_tr_b16 a[210:211], v220 offset:15360\n"
    "ds_read_b64_tr_b16 a[212:213], v220 offset:12296\n"
    "v_pk_add_f32 v[224:225], v[224:225], v[60:61]\n"
    "ds_read_b64_tr_b16 a[214:215], v220 offset:15368\n"
    "ds_read_b64_tr_b16 a[216:217], v220 offset:12352\n"
    "ds_read_b64_tr_b16 a[218:219], v220 offset:15424\n"
    "v_pk_add_f32 v[226:227], v[226:227], v[62:63]\n"
    "ds_read_b64_tr_b16 a[220:221], v220 offset:12360\n"
    "ds_read_b64_tr_b16 a[222:223], v220 offset:15432\n"
    "ds_read_b64_tr_b16 v[196:197], v218 offset:12416\n"
    "v_pk_fma_f32 v[228:229], v[60:61], v[60:61], v[228:229]\n"
    "ds_read_b64_tr_b16 v[198:199], v218 offset:15488\n"
    "v_pk_fma_f32 v[230:231], v[62:63], v[62:63], v[230:231]\n"
    "v_pk_add_f32 v[224:225], v[224:225], v[64:65]\n"
    "v_pk_add_f32 v[226:227], v[226:227], v[66:67]\n"
    "v_pk_fma_f32 v[228:229], v[64:65], v[64:65], v[228:229]\n"
    "v_pk_fma_f32 v[230:231], v[66:67], v[66:67], v[230:231]\n"
    "v_pk_add_f32 v[224:225], v[224:225], v[68:69]\n"
    "v_pk_add_f32 v[226:227], v[226:227], v[70:71]\n"
    "v_pk_fma_f32 v[228:229], v[68:69], v[68:69], v[228:229]\n"
    "v_pk_fma_f32 v[230:231], v[70:71], v[70:71], v[230:231]\n"
    "s_waitcnt lgkmcnt(10)\n"
    "v_smfmac_f32_16x16x64_bf16 v[72:75], v[128:131], v[120:127], v240\n"
    "s_nop 0\n"
    "ds_read_b128 v[160:163], v252 offset:23520\n"
    "v_smfmac_f32_16x16x64_bf16 v[76:79], v[132:135], v[120:127], v240 abid:1\n"
    "ds_read_b128 v[164:167], v252 offset:23856\n"
    "v_smfmac_f32_16x16x64_bf16 v[80:83], v[144:147], v[136:143], v240\n"
    "v_accvgpr_write_b32 a249, v237\n"
    "v_smfmac_f32_16x16x64_bf16 v[84:87], v[148:151], v[136:143], v240 abid:1\n"
    "v_accvgpr_write_b32 a250, v238\n"
    "v_accvgpr_write_b32 a251, v239\n"
    "v_pk_add_f32 v[224:225], v[224:225], v[72:73]\n"
    "v_pk_add_f32 v[226:227], v[226:227], v[74:75]\n"
    "v_pk_fma_f32 v[228:229], v[72:73], v[72:73], v[228:229]\n"
    "v_pk_fma_f32 v[230:231], v[74:75], v[74:75], v[230:231]\n"
    "v_pk_add_f32 v[224:225], v[224:225], v[76:77]\n"
    "v_pk_add_f32 v[226:227], v[226:227], v[78:79]\n"
    "v_pk_fma_f32 v[228:229], v[76:77], v[76:77], v[228:229]\n"
    "v_pk_fma_f32 v[230:231], v[78:79], v[78:79], v[230:231]\n"
    "v_pk_add_f32 v[224:225], v[224:225], v[80:81]\n"
    "v_pk_add_f32 v[226:227], v[226:227], v[82:83]\n"
    "v_pk_fma_f32 v[228:229], v[80:81], v[80:81], v[228:229]\n"
    "v_pk_fma_f32 v[230:231], v[82:83], v[82:83], v[230:231]\n"
    "v_pk_add_f32 v[224:225], v[224:225], v[84:85]\n"
    "v_pk_add_f32 v[226:227], v[226:227], v[86:87]\n"
    "v_pk_fma_f32 v[228:229], v[84:85], v[84:85], v[228:229]\n"
    "v_pk_fma_f32 v[230:231], v[86:87], v[86:87], v[230:231]\n"
    "s_waitcnt lgkmcnt(1)\n"
    "v_smfmac_f32_16x16x64_bf16 v[88:91], v[160:163], v[152:159], v240\n"
    "s_waitcnt lgkmcnt(0)\n"
    "v_smfmac_f32_16x16x64_bf16 v[92:95], v[164:167], v[152:159], v240 abid:1\n"
    "s_nop 0\n"
    "s_nop 5\n"
    "v_pk_add_f32 v[224:225], v[224:225], v[88:89]\n"
    "v_pk_add_f32 v[226:227], v[226:227], v[90:91]\n"
    "v_pk_fma_f32 v[228:229], v[88:89], v[88:89], v[228:229]\n"
    "v_pk_fma_f32 v[230:231], v[90:91], v[90:91], v[230:231]\n"
    "v_pk_add_f32 v[224:225], v[224:225], v[92:93]\n"
    "v_pk_add_f32 v[226:227], v[226:227], v[94:95]\n"
    "v_pk_fma_f32 v[228:229], v[92:93], v[92:93], v[228:229]\n"
    "v_pk_fma_f32 v[230:231], v[94:95], v[94:95], v[230:231]\n"
    "ds_write_b128 v254, v[224:227]\n"
    "ds_write_b128 v254, v[228:231] offset:1024\n"
    "v_add_u32_e64 v254, s56, v242\n"
    "s_waitcnt lgkmcnt(0)\n"
    "s_barrier\n"
    "ds_read_b128 v[128:131], v254 offset:0\n"
    "ds_read_b128 v[132:135], v254 offset:6752\n"
    "ds_read_b128 v[136:139], v254 offset:13504\n"
    "ds_read_b128 v[140:143], v254 offset:20256\n"
    "ds_read_b128 v[144:147], v254 offset:1024\n"
    "ds_read_b128 v[148:151], v254 offset:7776\n"
    "ds_read_b128 v[152:155], v254 offset:14528\n"
    "ds_read_b128 v[156:159], v254 offset:21280\n"
    "ds_write_b128 v234, v[112:115] offset:0\n"
    "ds_write_b128 v234, v[116:119] offset:16\n"
    "s_waitcnt lgkmcnt(8)\n"
    "v_pk_add_f32 v[128:129], v[128:129], v[132:133]\n"
    "v_pk_add_f32 v[130:131], v[130:131], v[134:135]\n"
    "ds_write_b128 v207, v[168:171] offset:0\n"
    "s_waitcnt lgkmcnt(6)\n"
    "v_pk_add_f32 v[136:137], v[136:137], v[140:141]\n"
    "v_pk_add_f32 v[138:139], v[138:139], v[142:143]\n"
    "s_waitcnt lgkmcnt(5)\n"
    "v_pk_add_f32 v[144:145], v[144:145], v[148:149]\n"
    "v_pk_add_f32 v[146:147], v[146:147], v[150:151]\n"
    "v_pk_add_f32 v[128:129], v[128:129], v[136:137]\n"
    "v_pk_add_f32 v[130:131], v[130:131], v[138:139]\n"
    "v_mul_f32_e32 v128, 0x3c2aaaab, v128\n"
    "v_mul_f32_e32 v129, 0x3c2aaaab, v129\n"
    "v_mul_f32_e32 v130, 0x3c2aaaab, v130\n"
    "s_waitcnt lgkmcnt(3)\n"
    "v_pk_add_f32 v[152:153], v[152:153], v[156:157]\n"
    "v_pk_add_f32 v[154:155], v[154:155], v[158:159]\n"
    "v_pk_add_f32 v[144:145], v[144:145], v[152:153]\n"
    "v_pk_add_f32 v[146:147], v[146:147], v[154:155]\n"
    "v_mul_f32_e32 v131, 0x3c2aaaab, v131\n"
    "v_mul_f32_e32 v144, 0x3c2aaaab, v144\n"
    "v_mul_f32_e32 v145, 0x3c2aaaab, v145\n"
    "v_mul_f32_e32 v146, 0x3c2aaaab, v146\n"
    "v_mul_f32_e32 v147, 0x3c2aaaab, v147\n"
    "v_fma_f32 v144, -v128, v128, v144\n"
    "v_fma_f32 v145, -v129, v129, v145\n"
    "v_fma_f32 v146, -v130, v130, v146\n"
    "v_fma_f32 v147, -v131, v131, v147\n"
    "v_max_f32_e64 v144, 0, v144\n"
    "v_max_f32_e64 v145, 0, v145\n"
    "v_max_f32_e64 v146, 0, v146\n"
    "v_max_f32_e64 v147, 0, v147\n"
    "v_add_f32_e32 v144, 0x3727c5ac, v144\n"
    "v_add_f32_e32 v145, 0x3727c5ac, v145\n"
    "v_add_f32_e32 v146, 0x3727c5ac, v146\n"
    "v_add_f32_e32 v147, 0x3727c5ac, v147\n"
    "v_rsq_f32_e64 v144, v144\n"
    "v_rsq_f32_e64 v145, v145\n"
    "v_rsq_f32_e64 v146, v146\n"
    "v_rsq_f32_e64 v147, v147\n"
    "v_mul_f32_e64 v224, s50, v144\n"
    "v_mul_f32_e64 v225, s50, v145\n"
    "v_mul_f32_e64 v226, s50, v146\n"
    "v_mul_f32_e64 v227, s50, v147\n"
    "v_mul_f32_e64 v228, v224, -v128\n"
    "v_mul_f32_e64 v229, v225, -v129\n"
    "v_mul_f32_e64 v230, v226, -v130\n"
    "v_mul_f32_e64 v231, v227, -v131\n"
    "ds_write_b128 v207, v[172:175] offset:4096\n"
    "v_pk_fma_f32 v[64:65], v[64:65], v[224:225], v[228:229]\n"
    "v_pk_fma_f32 v[66:67], v[66:67], v[226:227], v[230:231]\n"
    "v_pk_fma_f32 v[68:69], v[68:69], v[224:225], v[228:229]\n"
    "v_pk_fma_f32 v[70:71], v[70:71], v[226:227], v[230:231]\n"
    "v_pk_fma_f32 v[72:73], v[72:73], v[224:225], v[228:229]\n"
    "v_pk_fma_f32 v[74:75], v[74:75], v[226:227], v[230:231]\n"
    "ds_write_b128 v207, v[176:179] offset:8192\n"
    "v_pk_fma_f32 v[76:77], v[76:77], v[224:225], v[228:229]\n"
    "v_pk_fma_f32 v[78:79], v[78:79], v[226:227], v[230:231]\n"
    "v_pk_fma_f32 v[80:81], v[80:81], v[224:225], v[228:229]\n"
    "v_pk_fma_f32 v[82:83], v[82:83], v[226:227], v[230:231]\n"
    "v_pk_fma_f32 v[84:85], v[84:85], v[224:225], v[228:229]\n"
    "v_pk_fma_f32 v[86:87], v[86:87], v[226:227], v[230:231]\n"
    "ds_write_b128 v207, v[188:191] offset:12288\n"
    "v_pk_fma_f32 v[88:89], v[88:89], v[224:225], v[228:229]\n"
    "v_pk_fma_f32 v[90:91], v[90:91], v[226:227], v[230:231]\n"
    "v_pk_fma_f32 v[92:93], v[92:93], v[224:225], v[228:229]\n"
    "v_pk_fma_f32 v[94:95], v[94:95], v[226:227], v[230:231]\n"
    "v_cvt_pk_bf16_f32 v96, v64, v68\n"
    "v_cvt_pk_bf16_f32 v100, v65, v69\n"
    "ds_write_b128 v207, v[192:195] offset:16384\n"
    "v_cvt_pk_bf16_f32 v104, v66, v70\n"
    "v_cvt_pk_bf16_f32 v108, v67, v71\n"
    "v_cvt_pk_bf16_f32 v97, v72, v76\n"
    "v_cvt_pk_bf16_f32 v101, v73, v77\n"
    "v_cvt_pk_bf16_f32 v105, v74, v78\n"
    "v_cvt_pk_bf16_f32 v109, v75, v79\n"
    "ds_write_b128 v207, v[196:199] offset:20480\n"
    "v_cvt_pk_bf16_f32 v98, v80, v84\n"
    "v_cvt_pk_bf16_f32 v102, v81, v85\n"
    "v_cvt_pk_bf16_f32 v106, v82, v86\n"
    "v_cvt_pk_bf16_f32 v110, v83, v87\n"
    "v_cvt_pk_bf16_f32 v99, v88, v92\n"
    "v_cvt_pk_bf16_f32 v103, v89, v93\n"
    "ds_write_b128 v207, v[180:183] offset:32768\n"
    "v_cvt_pk_bf16_f32 v107, v90, v94\n"
    "v_cvt_pk_bf16_f32 v111, v91, v95\n"
    "v_pk_fma_f32 v[0:1], v[0:1], v[224:225], v[228:229]\n"
    "v_pk_fma_f32 v[2:3], v[2:3], v[226:227], v[230:231]\n"
    "v_pk_fma_f32 v[4:5], v[4:5], v[224:225], v[228:229]\n"
    "v_pk_fma_f32 v[6:7], v[6:7], v[226:227], v[230:231]\n"
    "ds_write_b128 v253, v[96:99] offset:2048\n"
    "v_pk_fma_f32 v[8:9], v[8:9], v[224:225], v[228:229]\n"
    "v_pk_fma_f32 v[10:11], v[10:11], v[226:227], v[230:231]\n"
    "v_pk_fma_f32 v[12:13], v[12:13], v[224:225], v[228:229]\n"
    "v_pk_fma_f32 v[14:15], v[14:15], v[226:227], v[230:231]\n"
    "v_pk_fma_f32 v[16:17], v[16:17], v[224:225], v[228:229]\n"
    "v_pk_fma_f32 v[18:19], v[18:19], v[226:227], v[230:231]\n"
    "ds_write_b128 v253, v[100:103] offset:5120\n"
    "v_pk_fma_f32 v[20:21], v[20:21], v[224:225], v[228:229]\n"
    "v_pk_fma_f32 v[22:23], v[22:23], v[226:227], v[230:231]\n"
    "v_pk_fma_f32 v[24:25], v[24:25], v[224:225], v[228:229]\n"
    "v_pk_fma_f32 v[26:27], v[26:27], v[226:227], v[230:231]\n"
    "v_pk_fma_f32 v[28:29], v[28:29], v[224:225], v[228:229]\n"
    "v_pk_fma_f32 v[30:31], v[30:31], v[226:227], v[230:231]\n"
    "v_cvt_pk_bf16_f32 v80, v0, v4\n"
    "v_cvt_pk_bf16_f32 v84, v1, v5\n"
    "v_cvt_pk_bf16_f32 v88, v2, v6\n"
    "v_cvt_pk_bf16_f32 v92, v3, v7\n"
    "v_cvt_pk_bf16_f32 v81, v8, v12\n"
    "v_cvt_pk_bf16_f32 v85, v9, v13\n"
    "v_cvt_pk_bf16_f32 v89, v10, v14\n"
    "v_cvt_pk_bf16_f32 v93, v11, v15\n"
    "v_cvt_pk_bf16_f32 v82, v16, v20\n"
    "v_cvt_pk_bf16_f32 v86, v17, v21\n"
    "v_cvt_pk_bf16_f32 v90, v18, v22\n"
    "v_cvt_pk_bf16_f32 v94, v19, v23\n"
    "v_cvt_pk_bf16_f32 v83, v24, v28\n"
    "ds_write_b128 v253, v[80:83] offset:0\n"
    "v_cvt_pk_bf16_f32 v87, v25, v29\n"
    "v_cvt_pk_bf16_f32 v91, v26, v30\n"
    "v_cvt_pk_bf16_f32 v95, v27, v31\n"
    "v_pk_fma_f32 v[32:33], v[32:33], v[224:225], v[228:229]\n"
    "v_pk_fma_f32 v[34:35], v[34:35], v[226:227], v[230:231]\n"
    "v_pk_fma_f32 v[36:37], v[36:37], v[224:225], v[228:229]\n"
    "ds_write_b128 v253, v[84:87] offset:3072\n"
    "v_pk_fma_f32 v[38:39], v[38:39], v[226:227], v[230:231]\n"
    "v_pk_fma_f32 v[40:41], v[40:41], v[224:225], v[228:229]\n"
    "v_pk_fma_f32 v[42:43], v[42:43], v[226:227], v[230:231]\n"
    "v_pk_fma_f32 v[44:45], v[44:45], v[224:225], v[228:229]\n"
    "v_pk_fma_f32 v[46:47], v[46:47], v[226:227], v[230:231]\n"
    "v_pk_fma_f32 v[48:49], v[48:49], v[224:225], v[228:229]\n"
    "v_pk_fma_f32 v[50:51], v[50:51], v[226:227], v[230:231]\n"
    "v_pk_fma_f32 v[52:53], v[52:53], v[224:225], v[228:229]\n"
    "v_pk_fma_f32 v[54:55], v[54:55], v[226:227], v[230:231]\n"
    "v_pk_fma_f32 v[56:57], v[56:57], v[224:225], v[228:229]\n"
    "v_pk_fma_f32 v[58:59], v[58:59], v[226:227], v[230:231]\n"
    "v_pk_fma_f32 v[60:61], v[60:61], v[224:225], v[228:229]\n"
    "v_pk_fma_f32 v[62:63], v[62:63], v[226:227], v[230:231]\n"
    "v_cvt_pk_bf16_f32 v64, v32, v36\n"
    "v_cvt_pk_bf16_f32 v68, v33, v37\n"
    "v_cvt_pk_bf16_f32 v72, v34, v38\n"
    "v_cvt_pk_bf16_f32 v76, v35, v39\n"
    "v_cvt_pk_bf16_f32 v65, v40, v44\n"
    "v_cvt_pk_bf16_f32 v69, v41, v45\n"
    "v_cvt_pk_bf16_f32 v73, v42, v46\n"
    "v_cvt_pk_bf16_f32 v77, v43, v47\n"
    "v_cvt_pk_bf16_f32 v66, v48, v52\n"
    "v_cvt_pk_bf16_f32 v70, v49, v53\n"
    "v_cvt_pk_bf16_f32 v74, v50, v54\n"
    "v_cvt_pk_bf16_f32 v78, v51, v55\n"
    "v_cvt_pk_bf16_f32 v67, v56, v60\n"
    "ds_write_b128 v253, v[64:67] offset:1024\n"
    "v_cvt_pk_bf16_f32 v71, v57, v61\n"
    "v_cvt_pk_bf16_f32 v75, v58, v62\n"
    "v_cvt_pk_bf16_f32 v79, v59, v63\n"
    "v_mov_b32_e64 v64, v104\n"
    "v_mov_b32_e64 v65, v105\n"
    "v_mov_b32_e64 v66, v106\n"
    "ds_write_b128 v253, v[68:71] offset:4096\n"
    "v_mov_b32_e64 v67, v107\n"
    "v_mov_b32_e64 v68, v108\n"
    "v_mov_b32_e64 v69, v109\n"
    "v_mov_b32_e64 v70, v110\n"
    "v_mov_b32_e64 v71, v111\n"
    "s_waitcnt lgkmcnt(0)\n"
    "s_barrier\n"
    "ds_read_b128 a[224:227], v242 offset:0\n"
    "ds_read_b128 a[228:231], v242 offset:1024\n"
    "ds_read_b128 a[232:235], v242 offset:2048\n"
    "ds_read_b128 a[236:239], v242 offset:3072\n"
    "ds_read_b128 a[240:243], v242 offset:4096\n"
    "ds_read_b128 a[244:247], v242 offset:5120\n"
    "ds_read_b128 a[252:255], v244 offset:32768\n"
    "s_waitcnt lgkmcnt(6)\n"
    "v_mfma_f32_16x16x32_bf16 v[168:171], a[80:83], a[224:227], 0\n"
    "v_mfma_f32_16x16x32_bf16 v[172:175], a[84:87], a[224:227], 0\n"
    "s_waitcnt lgkmcnt(5)\n"
    "v_mfma_f32_16x16x32_bf16 v[168:171], a[128:131], a[228:231], v[168:171]\n"
    "s_waitcnt lgkmcnt(4)\n"
    "v_mfma_f32_16x16x32_bf16 v[168:171], a[48:51], a[232:235], v[168:171]\n"
    "s_waitcnt lgkmcnt(3)\n"
    "v_mfma_f32_16x16x32_bf16 v[172:175], a[132:135], a[228:231], v[172:175]\n"
    "v_mfma_f32_16x16x32_bf16 v[176:179], a[80:83], a[236:239], 0\n"
    "s_waitcnt lgkmcnt(1)\n"
    "v_mfma_f32_16x16x32_bf16 v[176:179], a[128:131], a[240:243], v[176:179]\n"
    "s_waitcnt lgkmcnt(0)\n"
    "v_mfma_f32_16x16x32_bf16 v[172:175], a[252:255], a[232:235], v[172:175]\n"
    "v_mfma_f32_16x16x32_bf16 v[176:179], a[48:51], a[244:247], v[176:179]\n"
    "v_mfma_f32_16x16x32_bf16 v[180:183], a[84:87], a[236:239], 0\n"
    "v_mfma_f32_16x16x32_bf16 v[180:183], a[132:135], a[240:243], v[180:183]\n"
    "v_mfma_f32_16x16x32_bf16 v[180:183], a[252:255], a[244:247], v[180:183]\n"
    "v_readfirstlane_b32 s66, v232\n"
    "s_nop 1\n"
    "s_cmp_lg_u32 s66, 2\n"
    "s_cbranch_scc1 98f\n"
    "ds_read_b128 a[252:255], v244 offset:36864\n"
    "v_lshlrev_b32_sdwa v184, 1, v168 dst_sel:DWORD dst_unused:UNUSED_PAD src0_sel:DWORD src1_sel:WORD_1\n"
    "ds_read_u16 v184, v184\n"
    "v_lshlrev_b32_sdwa v200, 1, v169 dst_sel:DWORD dst_unused:UNUSED_PAD src0_sel:DWORD src1_sel:WORD_1\n"
    "ds_read_u16_d16_hi v200, v200\n"
    "v_lshlrev_b32_sdwa v185, 1, v170 dst_sel:DWORD dst_unused:UNUSED_PAD src0_sel:DWORD src1_sel:WORD_1\n"
    "ds_read_u16 v185, v185\n"
    "v_lshlrev_b32_sdwa v201, 1, v171 dst_sel:DWORD dst_unused:UNUSED_PAD src0_sel:DWORD src1_sel:WORD_1\n"
    "v_mfma_f32_16x16x32_bf16 v[168:171], a[88:91], a[224:227], 0\n"
    "ds_read_u16_d16_hi v201, v201\n"
    "v_lshlrev_b32_sdwa v186, 1, v172 dst_sel:DWORD dst_unused:UNUSED_PAD src0_sel:DWORD src1_sel:WORD_1\n"
    "v_mfma_f32_16x16x32_bf16 v[168:171], a[136:139], a[228:231], v[168:171]\n"
    "ds_read_u16 v186, v186\n"
    "v_lshlrev_b32_sdwa v202, 1, v173 dst_sel:DWORD dst_unused:UNUSED_PAD src0_sel:DWORD src1_sel:WORD_1\n"
    "v_mfma_f32_16x16x32_bf16 v[168:171], a[52:55], a[232:235], v[168:171]\n"
    "ds_read_u16_d16_hi v202, v202\n"
    "v_lshlrev_b32_sdwa v187, 1, v174 dst_sel:DWORD dst_unused:UNUSED_PAD src0_sel:DWORD src1_sel:WORD_1\n"
    "v_lshlrev_b32_sdwa v203, 1, v175 dst_sel:DWORD dst_unused:UNUSED_PAD src0_sel:DWORD src1_sel:WORD_1\n"
    "v_mfma_f32_16x16x32_bf16 v[172:175], a[92:95], a[224:227], 0\n"
    "s_nop 0\n"
    "v_lshlrev_b32_sdwa v188, 1, v176 dst_sel:DWORD dst_unused:UNUSED_PAD src0_sel:DWORD src1_sel:WORD_1\n"
    "v_lshlrev_b32_sdwa v204, 1, v177 dst_sel:DWORD dst_unused:UNUSED_PAD src0_sel:DWORD src1_sel:WORD_1\n"
    "v_mfma_f32_16x16x32_bf16 v[172:175], a[140:143], a[228:231], v[172:175]\n"
    "s_waitcnt lgkmcnt(6)\n"
    "ds_read_u16 v187, v187\n"
    "v_lshlrev_b32_sdwa v189, 1, v178 dst_sel:DWORD dst_unused:UNUSED_PAD src0_sel:DWORD src1_sel:WORD_1\n"
    "v_mfma_f32_16x16x32_bf16 v[172:175], a[252:255], a[232:235], v[172:175]\n"
    "ds_read_u16_d16_hi v203, v203\n"
    "v_lshlrev_b32_sdwa v205, 1, v179 dst_sel:DWORD dst_unused:UNUSED_PAD src0_sel:DWORD src1_sel:WORD_1\n"
    "v_mfma_f32_16x16x32_bf16 v[176:179], a[88:91], a[236:239], 0\n"
    "v_lshlrev_b32_sdwa v190, 1, v180 dst_sel:DWORD dst_unused:UNUSED_PAD src0_sel:DWORD src1_sel:WORD_1\n"
    "v_lshlrev_b32_sdwa v206, 1, v181 dst_sel:DWORD dst_unused:UNUSED_PAD src0_sel:DWORD src1_sel:WORD_1\n"
    "v_mfma_f32_16x16x32_bf16 v[176:179], a[136:139], a[240:243], v[176:179]\n"
    "ds_read_u16 v188, v188\n"
    "v_lshlrev_b32_sdwa v191, 1, v182 dst_sel:DWORD dst_unused:UNUSED_PAD src0_sel:DWORD src1_sel:WORD_1\n"
    "v_mfma_f32_16x16x32_bf16 v[176:179], a[52:55], a[244:247], v[176:179]\n"
    "v_lshlrev_b32_sdwa v207, 1, v183 dst_sel:DWORD dst_unused:UNUSED_PAD src0_sel:DWORD src1_sel:WORD_1\n"
    "v_cvt_pk_f16_f32 v192, v168, v169\n"
    "v_mfma_f32_16x16x32_bf16 v[180:183], a[92:95], a[236:239], 0\n"
    "s_waitcnt lgkmcnt(7)\n"
    "ds_read_u16_d16_hi v204, v204\n"
    "v_or_b32_e32 v184, v184, v200\n"
    "v_mfma_f32_16x16x32_bf16 v[180:183], a[140:143], a[240:243], v[180:183]\n"
    "v_lshlrev_b32_sdwa v193, 1, v170 dst_sel:DWORD dst_unused:UNUSED_PAD src0_sel:DWORD src1_sel:WORD_1\n"
    "v_lshlrev_b32_sdwa v194, 1, v172 dst_sel:DWORD dst_unused:UNUSED_PAD src0_sel:DWORD src1_sel:WORD_1\n"
    "v_mfma_f32_16x16x32_bf16 v[180:183], a[252:255], a[244:247], v[180:183]\n"
    "ds_read_u16 v189, v189\n"
    "v_lshlrev_b32_sdwa v195, 1, v174 dst_sel:DWORD dst_unused:UNUSED_PAD src0_sel:DWORD src1_sel:WORD_1\n"
    "v_cvt_pk_f16_f32 v196, v176, v177\n"
    "v_lshlrev_b32_sdwa v197, 1, v178 dst_sel:DWORD dst_unused:UNUSED_PAD src0_sel:DWORD src1_sel:WORD_1\n"
    "s_waitcnt lgkmcnt(7)\n"
    "v_or_b32_e32 v185, v185, v201\n"
    "v_lshlrev_b32_sdwa v201, 1, v171 dst_sel:DWORD dst_unused:UNUSED_PAD src0_sel:DWORD src1_sel:WORD_1\n"
    "v_mfma_f32_16x16x32_bf16 v[168:171], a[96:99], a[224:227], 0\n"
    "ds_read_u16_d16_hi v205, v205\n"
    "v_lshlrev_b32_sdwa v198, 1, v180 dst_sel:DWORD dst_unused:UNUSED_PAD src0_sel:DWORD src1_sel:WORD_1\n"
    "v_lshlrev_b32_sdwa v199, 1, v182 dst_sel:DWORD dst_unused:UNUSED_PAD src0_sel:DWORD src1_sel:WORD_1\n"
    "v_pk_fma_f16 v232, v192, v192, s48\n"
    "v_mov_b32_e64 v254, 0\n"
    "v_pk_mul_f16 v232, v192, v232\n"
    "v_exp_f16_e64 v234, v232\n"
    "ds_read_u16 v190, v190\n"
    "ds_read_u16_d16_hi v206, v206\n"
    "s_waitcnt lgkmcnt(8)\n"
    "v_or_b32_e32 v186, v186, v202\n"
    "v_mfma_f32_16x16x32_bf16 v[168:171], a[144:147], a[228:231], v[168:171]\n"
    "ds_read_u16 v191, v191\n"
    "v_lshlrev_b32_sdwa v202, 1, v173 dst_sel:DWORD dst_unused:UNUSED_PAD src0_sel:DWORD src1_sel:WORD_1\n"
    "v_mfma_f32_16x16x32_bf16 v[168:171], a[56:59], a[232:235], v[168:171]\n"
    "v_exp_f16_sdwa v234, v232 dst_sel:WORD_1 dst_unused:UNUSED_PRESERVE src0_sel:WORD_1\n"
    "ds_read_u16_d16_hi v207, v207\n"
    "s_waitcnt lgkmcnt(8)\n"
    "v_or_b32_e32 v187, v187, v203\n"
    "v_lshlrev_b32_sdwa v203, 1, v175 dst_sel:DWORD dst_unused:UNUSED_PAD src0_sel:DWORD src1_sel:WORD_1\n"
    "v_mfma_f32_16x16x32_bf16 v[172:175], a[100:103], a[224:227], 0\n"
    "ds_read_u16 v193, v193\n"
    "v_pk_fma_f16 v234, v234, s49, s49\n"
    "v_mfma_f32_16x16x32_bf16 v[172:175], a[148:151], a[228:231], v[172:175]\n"
    "v_lshlrev_b32_sdwa v200, 1, v169 dst_sel:DWORD dst_unused:UNUSED_PAD src0_sel:DWORD src1_sel:WORD_1\n"
    "v_rcp_f16_e32 v232, v234\n"
    "v_mfma_f32_16x16x32_bf16 v[172:175], a[60:63], a[232:235], v[172:175]\n"
    "s_waitcnt lgkmcnt(7)\n"
    "ds_read_u16_d16_hi v201, v201\n"
    "v_or_b32_e64 v188, v188, v204\n"
    "v_rcp_f16_sdwa v232, v234 dst_sel:WORD_1 dst_unused:UNUSED_PRESERVE src0_sel:WORD_1\n"
    "ds_read_u16 v194, v194\n"
    "v_fma_mix_f32 v234, v192, v232, 0 op_sel_hi:[1,1,0]\n"
    "v_fma_mix_f32 v232, v192, v232, 0 op_sel:[1,1,0] op_sel_hi:[1,1,0]\n"
    "v_cvt_pk_bf16_f32 v192, v234, v232\n"
    "v_pk_fma_f16 v232, v196, v196, s48\n"
    "v_pk_mul_f16 v232, v196, v232\n"
    "s_waitcnt lgkmcnt(7)\n"
    "v_or_b32_e32 v189, v189, v205\n"
    "v_lshlrev_b32_sdwa v205, 1, v179 dst_sel:DWORD dst_unused:UNUSED_PAD src0_sel:DWORD src1_sel:WORD_1\n"
    "v_mfma_f32_16x16x32_bf16 v[176:179], a[96:99], a[236:239], 0\n"
    "ds_read_u16_d16_hi v202, v202\n"
    "v_exp_f16_e64 v234, v232\n"
    "v_mfma_f32_16x16x32_bf16 v[176:179], a[144:147], a[240:243], v[176:179]\n"
    "ds_read_b128 v[236:239], v244 offset:0\n"
    "ds_read_u16 v195, v195\n"
    "v_mfma_f32_16x16x32_bf16 v[176:179], a[56:59], a[244:247], v[176:179]\n"
    "s_waitcnt lgkmcnt(8)\n"
    "v_or_b32_e32 v190, v190, v206\n"
    "v_lshlrev_b32_sdwa v206, 1, v181 dst_sel:DWORD dst_unused:UNUSED_PAD src0_sel:DWORD src1_sel:WORD_1\n"
    "v_mfma_f32_16x16x32_bf16 v[96:99], a[0:3], v[184:187], 0\n"
    "ds_read_u16_d16_hi v203, v203\n"
    "v_exp_f16_sdwa v234, v232 dst_sel:WORD_1 dst_unused:UNUSED_PRESERVE src0_sel:WORD_1\n"
    "v_mfma_f32_16x16x32_bf16 v[100:103], a[4:7], v[184:187], 0\n"
    "v_pk_fma_f16 v234, v234, s49, s49\n"
    "v_lshlrev_b32_sdwa v204, 1, v177 dst_sel:DWORD dst_unused:UNUSED_PAD src0_sel:DWORD src1_sel:WORD_1\n"
    "v_mfma_f32_16x16x32_bf16 v[112:115], a[8:11], v[184:187], 0\n"
    "ds_read_b128 v[164:167], v244 offset:2048\n"
    "s_waitcnt lgkmcnt(8)\n"
    "v_or_b32_e32 v191, v191, v207\n"
    "v_mfma_f32_16x16x32_bf16 v[116:119], a[12:15], v[184:187], 0\n"
    "ds_read_u16 v197, v197\n"
    "v_lshlrev_b32_sdwa v207, 1, v183 dst_sel:DWORD dst_unused:UNUSED_PAD src0_sel:DWORD src1_sel:WORD_1\n"
    "v_mfma_f32_16x16x32_bf16 v[180:183], a[100:103], a[236:239], 0\n"
    "ds_read_u16_d16_hi v205, v205\n"
    "v_rcp_f16_e32 v232, v234\n"
    "v_mfma_f32_16x16x32_bf16 v[180:183], a[148:151], a[240:243], v[180:183]\n"
    "v_rcp_f16_sdwa v232, v234 dst_sel:WORD_1 dst_unused:UNUSED_PRESERVE src0_sel:WORD_1\n"
    "v_mfma_f32_16x16x32_bf16 v[180:183], a[60:63], a[244:247], v[180:183]\n"
    "s_waitcnt lgkmcnt(8)\n"
    "ds_read_u16 v198, v198\n"
    "v_or_b32_e64 v193, v193, v201\n"
    "v_mfma_f32_16x16x32_bf16 v[104:107], a[0:3], v[188:191], 0\n"
    "v_lshlrev_b32_sdwa v201, 1, v171 dst_sel:DWORD dst_unused:UNUSED_PAD src0_sel:DWORD src1_sel:WORD_1\n"
    "v_fma_mix_f32 v234, v196, v232, 0 op_sel_hi:[1,1,0]\n"
    "v_mfma_f32_16x16x32_bf16 v[108:111], a[4:7], v[188:191], 0\n"
    "ds_read_u16_d16_hi v206, v206\n"
    "v_fma_mix_f32 v232, v196, v232, 0 op_sel:[1,1,0] op_sel_hi:[1,1,0]\n"
    "v_mfma_f32_16x16x32_bf16 v[120:123], a[8:11], v[188:191], 0\n"
    "s_waitcnt lgkmcnt(7)\n"
    "ds_read_u16 v199, v199\n"
    "v_or_b32_e32 v194, v194, v202\n"
    "v_mfma_f32_16x16x32_bf16 v[128:131], v[236:239], v[184:187], 0\n"
    "v_lshlrev_b32_sdwa v202, 1, v173 dst_sel:DWORD dst_unused:UNUSED_PAD src0_sel:DWORD src1_sel:WORD_1\n"
    "v_cvt_pk_bf16_f32 v196, v234, v232\n"
    "v_mfma_f32_16x16x32_bf16 v[124:127], a[12:15], v[188:191], 0\n"
    "ds_read_u16_d16_hi v207, v207\n"
    "v_mfma_f32_16x16x32_bf16 v[136:139], v[236:239], v[188:191], 0\n"
    "ds_read_u16_d16_hi v200, v200\n"
    "ds_read_u16_d16_hi v201, v201\n"
    "ds_read_b128 v[236:239], v244 offset:4096\n"
    "ds_read_u16_d16_hi v202, v202\n"
    "s_waitcnt lgkmcnt(10)\n"
    "v_mfma_f32_16x16x32_bf16 v[132:135], v[164:167], v[184:187], 0\n"
    "v_or_b32_e32 v195, v195, v203\n"
    "v_lshlrev_b32_sdwa v184, 1, v168 dst_sel:DWORD dst_unused:UNUSED_PAD src0_sel:DWORD src1_sel:WORD_1\n"
    "v_mfma_f32_16x16x32_bf16 v[140:143], v[164:167], v[188:191], 0\n"
    "v_lshlrev_b32_sdwa v185, 1, v170 dst_sel:DWORD dst_unused:UNUSED_PAD src0_sel:DWORD src1_sel:WORD_1\n"
    "v_lshlrev_b32_sdwa v186, 1, v172 dst_sel:DWORD dst_unused:UNUSED_PAD src0_sel:DWORD src1_sel:WORD_1\n"
    "v_mfma_f32_16x16x32_bf16 v[168:171], a[104:107], a[224:227], 0\n"
    "v_lshlrev_b32_sdwa v187, 1, v174 dst_sel:DWORD dst_unused:UNUSED_PAD src0_sel:DWORD src1_sel:WORD_1\n"
    "v_lshlrev_b32_sdwa v203, 1, v175 dst_sel:DWORD dst_unused:UNUSED_PAD src0_sel:DWORD src1_sel:WORD_1\n"
    "v_mfma_f32_16x16x32_bf16 v[168:171], a[152:155], a[228:231], v[168:171]\n"
    "v_lshlrev_b32_sdwa v188, 1, v176 dst_sel:DWORD dst_unused:UNUSED_PAD src0_sel:DWORD src1_sel:WORD_1\n"
    "v_lshlrev_b32_sdwa v189, 1, v178 dst_sel:DWORD dst_unused:UNUSED_PAD src0_sel:DWORD src1_sel:WORD_1\n"
    "v_mfma_f32_16x16x32_bf16 v[168:171], a[64:67], a[232:235], v[168:171]\n"
    "s_waitcnt lgkmcnt(8)\n"
    "v_or_b32_e32 v197, v197, v205\n"
    "v_lshlrev_b32_sdwa v205, 1, v179 dst_sel:DWORD dst_unused:UNUSED_PAD src0_sel:DWORD src1_sel:WORD_1\n"
    "v_mfma_f32_16x16x32_bf16 v[172:175], a[108:111], a[224:227], 0\n"
    "ds_read_u16 v184, v184\n"
    "v_lshlrev_b32_sdwa v190, 1, v180 dst_sel:DWORD dst_unused:UNUSED_PAD src0_sel:DWORD src1_sel:WORD_1\n"
    "v_mfma_f32_16x16x32_bf16 v[172:175], a[156:159], a[228:231], v[172:175]\n"
    "v_lshlrev_b32_sdwa v191, 1, v182 dst_sel:DWORD dst_unused:UNUSED_PAD src0_sel:DWORD src1_sel:WORD_1\n"
    "v_mfma_f32_16x16x32_bf16 v[172:175], a[68:71], a[232:235], v[172:175]\n"
    "ds_read_u16 v185, v185\n"
    "v_mfma_f32_16x16x32_bf16 v[176:179], a[104:107], a[236:239], 0\n"
    "s_waitcnt lgkmcnt(8)\n"
    "v_or_b32_e32 v198, v198, v206\n"
    "v_lshlrev_b32_sdwa v206, 1, v181 dst_sel:DWORD dst_unused:UNUSED_PAD src0_sel:DWORD src1_sel:WORD_1\n"
    "v_mfma_f32_16x16x32_bf16 v[176:179], a[152:155], a[240:243], v[176:179]\n"
    "ds_read_u16 v186, v186\n"
    "v_mfma_f32_16x16x32_bf16 v[176:179], a[64:67], a[244:247], v[176:179]\n"
    "s_nop 0\n"
    "v_mfma_f32_16x16x32_bf16 v[96:99], a[16:19], v[192:195], v[96:99]\n"
    "s_waitcnt lgkmcnt(7)\n"
    "ds_read_u16 v187, v187\n"
    "v_or_b32_e64 v199, v199, v207\n"
    "v_mfma_f32_16x16x32_bf16 v[100:103], a[20:23], v[192:195], v[100:103]\n"
    "ds_read_b128 v[164:167], v244 offset:6144\n"
    "v_lshlrev_b32_sdwa v207, 1, v183 dst_sel:DWORD dst_unused:UNUSED_PAD src0_sel:DWORD src1_sel:WORD_1\n"
    "v_mfma_f32_16x16x32_bf16 v[180:183], a[108:111], a[236:239], 0\n"
    "ds_read_u16_d16_hi v203, v203\n"
    "v_mfma_f32_16x16x32_bf16 v[180:183], a[156:159], a[240:243], v[180:183]\n"
    "s_nop 0\n"
    "v_mfma_f32_16x16x32_bf16 v[180:183], a[68:71], a[244:247], v[180:183]\n"
    "s_waitcnt lgkmcnt(7)\n"
    "ds_read_u16 v188, v188\n"
    "v_mfma_f32_16x16x32_bf16 v[112:115], a[24:27], v[192:195], v[112:115]\n"
    "v_mfma_f32_16x16x32_bf16 v[116:119], a[28:31], v[192:195], v[116:119]\n"
    "ds_read_u16_d16_hi v204, v204\n"
    "v_mfma_f32_16x16x32_bf16 v[128:131], v[236:239], v[192:195], v[128:131]\n"
    "s_nop 0\n"
    "v_mfma_f32_16x16x32_bf16 v[104:107], a[16:19], v[196:199], v[104:107]\n"
    "s_waitcnt lgkmcnt(7)\n"
    "ds_read_u16 v189, v189\n"
    "v_or_b32_e64 v184, v184, v200\n"
    "v_mfma_f32_16x16x32_bf16 v[108:111], a[20:23], v[196:199], v[108:111]\n"
    "ds_read_u16_d16_hi v205, v205\n"
    "v_mfma_f32_16x16x32_bf16 v[120:123], a[24:27], v[196:199], v[120:123]\n"
    "s_waitcnt lgkmcnt(8)\n"
    "v_or_b32_e32 v185, v185, v201\n"
    "v_lshlrev_b32_sdwa v201, 1, v171 dst_sel:DWORD dst_unused:UNUSED_PAD src0_sel:DWORD src1_sel:WORD_1\n"
    "v_mfma_f32_16x16x32_bf16 v[124:127], a[28:31], v[196:199], v[124:127]\n"
    "buffer_load_dwordx4 v[0:3], v246, s[40:43], s60 offen offset:0\n"
    "buffer_load_dwordx4 v[32:35], v246, s[40:43], s60 offen offset:64\n"
    "ds_read_u16 v190, v190\n"
    "v_mfma_f32_16x16x32_bf16 v[136:139], v[236:239], v[196:199], v[136:139]\n"
    "s_waitcnt lgkmcnt(8)\n"
    "v_or_b32_e32 v186, v186, v202\n"
    "v_lshlrev_b32_sdwa v202, 1, v173 dst_sel:DWORD dst_unused:UNUSED_PAD src0_sel:DWORD src1_sel:WORD_1\n"
    "ds_read_u16_d16_hi v206, v206\n"
    "s_waitcnt lgkmcnt(7)\n"
    "v_mfma_f32_16x16x32_bf16 v[132:135], v[164:167], v[192:195], v[132:135]\n"
    "s_nop 0\n"
    "ds_read_u16 v191, v191\n"
    "v_cvt_pk_f16_f32 v192, v168, v169\n"
    "v_mfma_f32_16x16x32_bf16 v[140:143], v[164:167], v[196:199], v[140:143]\n"
    "s_nop 0\n"
    "v_lshlrev_b32_sdwa v193, 1, v170 dst_sel:DWORD dst_unused:UNUSED_PAD src0_sel:DWORD src1_sel:WORD_1\n"
    "v_lshlrev_b32_sdwa v194, 1, v172 dst_sel:DWORD dst_unused:UNUSED_PAD src0_sel:DWORD src1_sel:WORD_1\n"
    "v_mfma_f32_16x16x32_bf16 v[168:171], a[112:115], a[224:227], 0\n"
    "s_waitcnt lgkmcnt(7)\n"
    "ds_read_u16_d16_hi v207, v207\n"
    "v_lshlrev_b32_sdwa v195, 1, v174 dst_sel:DWORD dst_unused:UNUSED_PAD src0_sel:DWORD src1_sel:WORD_1\n"
    "v_mfma_f32_16x16x32_bf16 v[168:171], a[160:163], a[228:231], v[168:171]\n"
    "v_cvt_pk_f16_f32 v196, v176, v177\n"
    "v_lshlrev_b32_sdwa v197, 1, v178 dst_sel:DWORD dst_unused:UNUSED_PAD src0_sel:DWORD src1_sel:WORD_1\n"
    "v_mfma_f32_16x16x32_bf16 v[168:171], a[72:75], a[232:235], v[168:171]\n"
    "ds_read_b128 a[252:255], v244 offset:34816\n"
    "v_lshlrev_b32_sdwa v198, 1, v180 dst_sel:DWORD dst_unused:UNUSED_PAD src0_sel:DWORD src1_sel:WORD_1\n"
    "v_lshlrev_b32_sdwa v199, 1, v182 dst_sel:DWORD dst_unused:UNUSED_PAD src0_sel:DWORD src1_sel:WORD_1\n"
    "v_or_b32_e64 v187, v187, v203\n"
    "v_lshlrev_b32_sdwa v203, 1, v175 dst_sel:DWORD dst_unused:UNUSED_PAD src0_sel:DWORD src1_sel:WORD_1\n"
    "v_mfma_f32_16x16x32_bf16 v[172:175], a[116:119], a[224:227], 0\n"
    "s_waitcnt lgkmcnt(7)\n"
    "ds_read_u16 v193, v193\n"
    "v_or_b32_e32 v188, v188, v204\n"
    "v_mfma_f32_16x16x32_bf16 v[172:175], a[164:167], a[228:231], v[172:175]\n"
    "buffer_load_dwordx4 v[20:23], v247, s[40:43], s60 offen offset:0\n"
    "buffer_load_dwordx4 v[52:55], v247, s[40:43], s60 offen offset:64\n"
    "ds_read_u16_d16_hi v201, v201\n"
    "v_pk_fma_f16 v232, v192, v192, s48\n"
    "v_lshlrev_b32_sdwa v200, 1, v169 dst_sel:DWORD dst_unused:UNUSED_PAD src0_sel:DWORD src1_sel:WORD_1\n"
    "v_pk_mul_f16 v232, v192, v232\n"
    "v_exp_f16_e32 v234, v232\n"
    "s_waitcnt lgkmcnt(7)\n"
    "v_or_b32_e64 v189, v189, v205\n"
    "v_lshlrev_b32_sdwa v205, 1, v179 dst_sel:DWORD dst_unused:UNUSED_PAD src0_sel:DWORD src1_sel:WORD_1\n"
    "v_mfma_f32_16x16x32_bf16 v[176:179], a[112:115], a[236:239], 0\n"
    "ds_read_u16 v194, v194\n"
    "ds_read_u16_d16_hi v202, v202\n"
    "v_mfma_f32_16x16x32_bf16 v[176:179], a[160:163], a[240:243], v[176:179]\n"
    "v_exp_f16_sdwa v234, v232 dst_sel:WORD_1 dst_unused:UNUSED_PRESERVE src0_sel:WORD_1\n"
    "v_mfma_f32_16x16x32_bf16 v[176:179], a[72:75], a[244:247], v[176:179]\n"
    "ds_read_b128 v[236:239], v244 offset:8192\n"
    "s_waitcnt lgkmcnt(8)\n"
    "v_or_b32_e32 v190, v190, v206\n"
    "v_mfma_f32_16x16x32_bf16 v[96:99], a[32:35], v[184:187], v[96:99]\n"
    "ds_read_u16 v195, v195\n"
    "v_lshlrev_b32_sdwa v206, 1, v181 dst_sel:DWORD dst_unused:UNUSED_PAD src0_sel:DWORD src1_sel:WORD_1\n"
    "v_mfma_f32_16x16x32_bf16 v[100:103], a[36:39], v[184:187], v[100:103]\n"
    "ds_read_u16_d16_hi v203, v203\n"
    "v_pk_fma_f16 v234, v234, s49, s49\n"
    "v_mfma_f32_16x16x32_bf16 v[112:115], a[40:43], v[184:187], v[112:115]\n"
    "v_rcp_f16_e32 v232, v234\n"
    "s_waitcnt lgkmcnt(7)\n"
    "v_mfma_f32_16x16x32_bf16 v[172:175], a[252:255], a[232:235], v[172:175]\n"
    "buffer_load_dwordx4 v[4:7], v246, s[40:43], s61 offen offset:0\n"
    "buffer_load_dwordx4 v[36:39], v246, s[40:43], s61 offen offset:64\n"
    "ds_read_b128 v[164:167], v244 offset:10240\n"
    "v_or_b32_e64 v191, v191, v207\n"
    "v_mfma_f32_16x16x32_bf16 v[116:119], a[44:47], v[184:187], v[116:119]\n"
    "ds_read_u16 v197, v197\n"
    "v_lshlrev_b32_sdwa v207, 1, v183 dst_sel:DWORD dst_unused:UNUSED_PAD src0_sel:DWORD src1_sel:WORD_1\n"
    "v_mfma_f32_16x16x32_bf16 v[180:183], a[116:119], a[236:239], 0\n"
    "ds_read_u16_d16_hi v205, v205\n"
    "v_rcp_f16_sdwa v232, v234 dst_sel:WORD_1 dst_unused:UNUSED_PRESERVE src0_sel:WORD_1\n"
    "v_mfma_f32_16x16x32_bf16 v[180:183], a[164:167], a[240:243], v[180:183]\n"
    "s_waitcnt lgkmcnt(8)\n"
    "v_or_b32_e32 v193, v193, v201\n"
    "v_lshlrev_b32_sdwa v201, 1, v171 dst_sel:DWORD dst_unused:UNUSED_PAD src0_sel:DWORD src1_sel:WORD_1\n"
    "v_mfma_f32_16x16x32_bf16 v[180:183], a[252:255], a[244:247], v[180:183]\n"
    "ds_read_u16 v198, v198\n"
    "v_lshlrev_b32_sdwa v204, 1, v177 dst_sel:DWORD dst_unused:UNUSED_PAD src0_sel:DWORD src1_sel:WORD_1\n"
    "v_mfma_f32_16x16x32_bf16 v[104:107], a[32:35], v[188:191], v[104:107]\n"
    "ds_read_u16_d16_hi v206, v206\n"
    "v_fma_mix_f32 v234, v192, v232, 0 op_sel_hi:[1,1,0]\n"
    "v_mfma_f32_16x16x32_bf16 v[108:111], a[36:39], v[188:191], v[108:111]\n"
    "v_fma_mix_f32 v232, v192, v232, 0 op_sel:[1,1,0] op_sel_hi:[1,1,0]\n"
    "v_cvt_pk_bf16_f32 v192, v234, v232\n"
    "v_mfma_f32_16x16x32_bf16 v[120:123], a[40:43], v[188:191], v[120:123]\n"
    "s_waitcnt lgkmcnt(7)\n"
    "ds_read_u16 v199, v199\n"
    "v_or_b32_e32 v194, v194, v202\n"
    "v_mfma_f32_16x16x32_bf16 v[128:131], v[236:239], v[184:187], v[128:131]\n"
    "v_lshlrev_b32_sdwa v202, 1, v173 dst_sel:DWORD dst_unused:UNUSED_PAD src0_sel:DWORD src1_sel:WORD_1\n"
    "v_pk_fma_f16 v232, v196, v196, s48\n"
    "v_mfma_f32_16x16x32_bf16 v[124:127], a[44:47], v[188:191], v[124:127]\n"
    "buffer_load_dwordx4 v[24:27], v247, s[40:43], s61 offen offset:0\n"
    "buffer_load_dwordx4 v[56:59], v247, s[40:43], s61 offen offset:64\n"
    "ds_read_u16_d16_hi v207, v207\n"
    "v_mfma_f32_16x16x32_bf16 v[136:139], v[236:239], v[188:191], v[136:139]\n"
    "ds_read_b128 a[252:255], v244 offset:38912\n"
    "v_pk_mul_f16 v232, v196, v232\n"
    "s_waitcnt lgkmcnt(7)\n"
    "v_mfma_f32_16x16x32_bf16 v[132:135], v[164:167], v[184:187], v[132:135]\n"
    "s_nop 0\n"
    "ds_read_u16_d16_hi v200, v200\n"
    "v_or_b32_e64 v195, v195, v203\n"
    "v_mfma_f32_16x16x32_bf16 v[140:143], v[164:167], v[188:191], v[140:143]\n"
    "v_lshlrev_b32_sdwa v184, 1, v168 dst_sel:DWORD dst_unused:UNUSED_PAD src0_sel:DWORD src1_sel:WORD_1\n"
    "v_lshlrev_b32_sdwa v185, 1, v170 dst_sel:DWORD dst_unused:UNUSED_PAD src0_sel:DWORD src1_sel:WORD_1\n"
    "v_mfma_f32_16x16x32_bf16 v[168:171], a[120:123], a[224:227], 0\n"
    "ds_read_u16 v184, v184\n"
    "v_lshlrev_b32_sdwa v186, 1, v172 dst_sel:DWORD dst_unused:UNUSED_PAD src0_sel:DWORD src1_sel:WORD_1\n"
    "v_mfma_f32_16x16x32_bf16 v[168:171], a[168:171], a[228:231], v[168:171]\n"
    "v_lshlrev_b32_sdwa v187, 1, v174 dst_sel:DWORD dst_unused:UNUSED_PAD src0_sel:DWORD src1_sel:WORD_1\n"
    "v_lshlrev_b32_sdwa v203, 1, v175 dst_sel:DWORD dst_unused:UNUSED_PAD src0_sel:DWORD src1_sel:WORD_1\n"
    "v_mfma_f32_16x16x32_bf16 v[168:171], a[76:79], a[232:235], v[168:171]\n"
    "s_waitcnt lgkmcnt(7)\n"
    "ds_read_u16 v185, v185\n"
    "v_or_b32_e32 v197, v197, v205\n"
    "v_mfma_f32_16x16x32_bf16 v[172:175], a[124:127], a[224:227], 0\n"
    "ds_read_u16_d16_hi v201, v201\n"
    "v_lshlrev_b32_sdwa v188, 1, v176 dst_sel:DWORD dst_unused:UNUSED_PAD src0_sel:DWORD src1_sel:WORD_1\n"
    "v_mfma_f32_16x16x32_bf16 v[172:175], a[172:175], a[228:231], v[172:175]\n"
    "v_lshlrev_b32_sdwa v189, 1, v178 dst_sel:DWORD dst_unused:UNUSED_PAD src0_sel:DWORD src1_sel:WORD_1\n"
    "v_lshlrev_b32_sdwa v205, 1, v179 dst_sel:DWORD dst_unused:UNUSED_PAD src0_sel:DWORD src1_sel:WORD_1\n"
    "v_mfma_f32_16x16x32_bf16 v[176:179], a[120:123], a[236:239], 0\n"
    "ds_read_u16 v186, v186\n"
    "buffer_load_dwordx4 v[8:11], v246, s[40:43], s62 offen offset:0\n"
    "buffer_load_dwordx4 v[40:43], v246, s[40:43], s62 offen offset:64\n"
    "v_mfma_f32_16x16x32_bf16 v[176:179], a[168:171], a[240:243], v[176:179]\n"
    "s_waitcnt lgkmcnt(8)\n"
    "v_or_b32_e32 v198, v198, v206\n"
    "v_lshlrev_b32_sdwa v190, 1, v180 dst_sel:DWORD dst_unused:UNUSED_PAD src0_sel:DWORD src1_sel:WORD_1\n"
    "v_mfma_f32_16x16x32_bf16 v[176:179], a[76:79], a[244:247], v[176:179]\n"
    "ds_read_b128 v[236:239], v244 offset:12288\n"
    "ds_read_u16_d16_hi v202, v202\n"
    "v_lshlrev_b32_sdwa v206, 1, v181 dst_sel:DWORD dst_unused:UNUSED_PAD src0_sel:DWORD src1_sel:WORD_1\n"
    "v_lshlrev_b32_sdwa v191, 1, v182 dst_sel:DWORD dst_unused:UNUSED_PAD src0_sel:DWORD src1_sel:WORD_1\n"
    "v_exp_f16_e32 v234, v232\n"
    "s_waitcnt lgkmcnt(7)\n"
    "v_mfma_f32_16x16x32_bf16 v[172:175], a[252:255], a[232:235], v[172:175]\n"
    "ds_read_u16 v187, v187\n"
    "v_or_b32_e64 v199, v199, v207\n"
    "v_lshlrev_b32_sdwa v207, 1, v183 dst_sel:DWORD dst_unused:UNUSED_PAD src0_sel:DWORD src1_sel:WORD_1\n"
    "v_mfma_f32_16x16x32_bf16 v[180:183], a[124:127], a[236:239], 0\n"
    "v_exp_f16_sdwa v234, v232 dst_sel:WORD_1 dst_unused:UNUSED_PRESERVE src0_sel:WORD_1\n"
    "v_mfma_f32_16x16x32_bf16 v[180:183], a[172:175], a[240:243], v[180:183]\n"
    "ds_read_b128 v[164:167], v244 offset:14336\n"
    "ds_read_u16_d16_hi v203, v203\n"
    "v_mfma_f32_16x16x32_bf16 v[180:183], a[252:255], a[244:247], v[180:183]\n"
    "v_pk_fma_f16 v234, v234, s49, s49\n"
    "v_rcp_f16_e32 v232, v234\n"
    "v_mfma_f32_16x16x32_bf16 v[96:99], a[176:179], v[192:195], v[96:99]\n"
    "s_waitcnt lgkmcnt(8)\n"
    "ds_read_u16 v188, v188\n"
    "v_rcp_f16_sdwa v232, v234 dst_sel:WORD_1 dst_unused:UNUSED_PRESERVE src0_sel:WORD_1\n"
    "v_mfma_f32_16x16x32_bf16 v[100:103], a[180:183], v[192:195], v[100:103]\n"
    "buffer_load_dwordx4 v[12:15], v246, s[40:43], s63 offen offset:0\n"
    "buffer_load_dwordx4 v[44:47], v246, s[40:43], s63 offen offset:64\n"
    "ds_read_u16_d16_hi v204, v204\n"
    "v_mfma_f32_16x16x32_bf16 v[112:115], a[184:187], v[192:195], v[112:115]\n"
    "v_fma_mix_f32 v234, v196, v232, 0 op_sel_hi:[1,1,0]\n"
    "v_fma_mix_f32 v232, v196, v232, 0 op_sel:[1,1,0] op_sel_hi:[1,1,0]\n"
    "v_mfma_f32_16x16x32_bf16 v[116:119], a[188:191], v[192:195], v[116:119]\n"
    "s_waitcnt lgkmcnt(8)\n"
    "ds_read_u16 v189, v189\n"
    "v_cvt_pk_bf16_f32 v196, v234, v232\n"
    "v_or_b32_e32 v184, v184, v200\n"
    "v_or_b32_e32 v185, v185, v201\n"
    "v_mfma_f32_16x16x32_bf16 v[104:107], a[176:179], v[196:199], v[104:107]\n"
    "s_waitcnt lgkmcnt(7)\n"
    "ds_read_u16_d16_hi v205, v205\n"
    "v_lshlrev_b32_sdwa v201, 1, v171 dst_sel:DWORD dst_unused:UNUSED_PAD src0_sel:DWORD src1_sel:WORD_1\n"
    "v_mfma_f32_16x16x32_bf16 v[128:131], v[236:239], v[192:195], v[128:131]\n"
    "v_mfma_f32_16x16x32_bf16 v[108:111], a[180:183], v[196:199], v[108:111]\n"
    "s_nop 0\n"
    "ds_read_b128 a[224:227], v243 offset:0\n"
    "v_mfma_f32_16x16x32_bf16 v[120:123], a[184:187], v[196:199], v[120:123]\n"
    "s_waitcnt lgkmcnt(8)\n"
    "ds_read_u16 v190, v190\n"
    "v_or_b32_e32 v186, v186, v202\n"
    "v_mfma_f32_16x16x32_bf16 v[124:127], a[188:191], v[196:199], v[124:127]\n"
    "s_waitcnt lgkmcnt(7)\n"
    "v_lshlrev_b32_sdwa v202, 1, v173 dst_sel:DWORD dst_unused:UNUSED_PAD src0_sel:DWORD src1_sel:WORD_1\n"
    "v_mfma_f32_16x16x32_bf16 v[132:135], v[164:167], v[192:195], v[132:135]\n"
    "ds_read_b128 a[228:231], v243 offset:1024\n"
    "v_cvt_pk_f16_f32 v192, v168, v169\n"
    "v_mfma_f32_16x16x32_bf16 v[136:139], v[236:239], v[196:199], v[136:139]\n"
    "s_nop 0\n"
    "buffer_load_dwordx4 v[16:19], v246, s[40:43], s52 offen offset:0\n"
    "buffer_load_dwordx4 v[48:51], v246, s[40:43], s52 offen offset:64\n"
    "ds_read_u16_d16_hi v206, v206\n"
    "v_mfma_f32_16x16x32_bf16 v[140:143], v[164:167], v[196:199], v[140:143]\n"
    "s_waitcnt lgkmcnt(8)\n"
    "ds_read_b128 a[232:235], v243 offset:2048\n"
    "v_lshlrev_b32_sdwa v193, 1, v170 dst_sel:DWORD dst_unused:UNUSED_PAD src0_sel:DWORD src1_sel:WORD_1\n"
    "v_lshlrev_b32_sdwa v194, 1, v172 dst_sel:DWORD dst_unused:UNUSED_PAD src0_sel:DWORD src1_sel:WORD_1\n"
    "v_lshlrev_b32_sdwa v195, 1, v174 dst_sel:DWORD dst_unused:UNUSED_PAD src0_sel:DWORD src1_sel:WORD_1\n"
    "v_cvt_pk_f16_f32 v196, v176, v177\n"
    "v_lshlrev_b32_sdwa v197, 1, v178 dst_sel:DWORD dst_unused:UNUSED_PAD src0_sel:DWORD src1_sel:WORD_1\n"
    "v_lshlrev_b32_sdwa v198, 1, v180 dst_sel:DWORD dst_unused:UNUSED_PAD src0_sel:DWORD src1_sel:WORD_1\n"
    "v_lshlrev_b32_sdwa v199, 1, v182 dst_sel:DWORD dst_unused:UNUSED_PAD src0_sel:DWORD src1_sel:WORD_1\n"
    "v_or_b32_e64 v187, v187, v203\n"
    "v_lshlrev_b32_sdwa v203, 1, v175 dst_sel:DWORD dst_unused:UNUSED_PAD src0_sel:DWORD src1_sel:WORD_1\n"
    "v_pk_fma_f16 v232, v192, v192, s48\n"
    "v_pk_mul_f16 v232, v192, v232\n"
    "v_exp_f16_e32 v234, v232\n"
    "s_waitcnt lgkmcnt(7)\n"
    "v_or_b32_e64 v188, v188, v204\n"
    "ds_read_u16 v191, v191\n"
    "ds_read_b128 a[236:239], v243 offset:3072\n"
    "ds_read_u16_d16_hi v207, v207\n"
    "v_exp_f16_sdwa v234, v232 dst_sel:WORD_1 dst_unused:UNUSED_PRESERVE src0_sel:WORD_1\n"
    "ds_read_b128 a[240:243], v243 offset:4096\n"
    "ds_read_b128 a[244:247], v243 offset:5120\n"
    "s_waitcnt lgkmcnt(9)\n"
    "v_mfma_f32_16x16x32_bf16 v[168:171], a[80:83], a[224:227], 0\n"
    "s_nop 0\n"
    "ds_read_b128 a[252:255], v244 offset:32768\n"
    "v_or_b32_e64 v189, v189, v205\n"
    "v_mfma_f32_16x16x32_bf16 v[172:175], a[84:87], a[224:227], 0\n"
    "buffer_load_dwordx4 v[152:155], v245, s[40:43], s57 offen offset:64\n"
    "buffer_load_dwordx4 v[144:147], v245, s[40:43], s57 offen offset:0\n"
    "ds_read_u16 v193, v193\n"
    "s_waitcnt lgkmcnt(9)\n"
    "v_mfma_f32_16x16x32_bf16 v[168:171], a[128:131], a[228:231], v[168:171]\n"
    "s_nop 0\n"
    "v_lshlrev_b32_sdwa v205, 1, v179 dst_sel:DWORD dst_unused:UNUSED_PAD src0_sel:DWORD src1_sel:WORD_1\n"
    "v_pk_fma_f16 v234, v234, s49, s49\n"
    "v_mfma_f32_16x16x32_bf16 v[172:175], a[132:135], a[228:231], v[172:175]\n"
    "ds_read_u16_d16_hi v201, v201\n"
    "v_rcp_f16_e64 v232, v234\n"
    "buffer_load_dwordx4 v[156:159], v245, s[40:43], s88 offen offset:64\n"
    "buffer_load_dwordx4 v[148:151], v245, s[40:43], s88 offen offset:0\n"
    "ds_read_u16 v194, v194\n"
    "s_waitcnt lgkmcnt(9)\n"
    "v_mfma_f32_16x16x32_bf16 v[168:171], a[48:51], a[232:235], v[168:171]\n"
    "s_nop 0\n"
    "ds_read_u16_d16_hi v202, v202\n"
    "v_or_b32_e32 v190, v190, v206\n"
    "v_mfma_f32_16x16x32_bf16 v[96:99], a[192:195], v[184:187], v[96:99]\n"
    "s_waitcnt lgkmcnt(8)\n"
    "v_lshlrev_b32_sdwa v206, 1, v181 dst_sel:DWORD dst_unused:UNUSED_PAD src0_sel:DWORD src1_sel:WORD_1\n"
    "v_rcp_f16_sdwa v232, v234 dst_sel:WORD_1 dst_unused:UNUSED_PRESERVE src0_sel:WORD_1\n"
    "v_mfma_f32_16x16x32_bf16 v[176:179], a[80:83], a[236:239], 0\n"
    "ds_read_b128 v[236:239], v244 offset:16384\n"
    "v_lshlrev_b32_sdwa v200, 1, v169 dst_sel:DWORD dst_unused:UNUSED_PAD src0_sel:DWORD src1_sel:WORD_1\n"
    "v_mfma_f32_16x16x32_bf16 v[100:103], a[196:199], v[184:187], v[100:103]\n"
    "ds_read_u16 v195, v195\n"
    "v_fma_mix_f32 v234, v192, v232, 0 op_sel_hi:[1,1,0]\n"
    "v_mfma_f32_16x16x32_bf16 v[112:115], a[200:203], v[184:187], v[112:115]\n"
    "s_waitcnt lgkmcnt(8)\n"
    "v_or_b32_e32 v191, v191, v207\n"
    "v_fma_mix_f32 v232, v192, v232, 0 op_sel:[1,1,0] op_sel_hi:[1,1,0]\n"
    "v_mfma_f32_16x16x32_bf16 v[176:179], a[128:131], a[240:243], v[176:179]\n"
    "ds_read_u16_d16_hi v203, v203\n"
    "v_mfma_f32_16x16x32_bf16 v[116:119], a[204:207], v[184:187], v[116:119]\n"
    "s_branch 2f\n"
    ".p2align 6\n"
    "1:\n"
    ".Lreplk_loop:\n"
    "v_lshlrev_b32_sdwa v184, 1, v168 dst_sel:DWORD dst_unused:UNUSED_PAD src0_sel:DWORD src1_sel:WORD_1\n"
    "v_lshlrev_b32_sdwa v200, 1, v169 dst_sel:DWORD dst_unused:UNUSED_PAD src0_sel:DWORD src1_sel:WORD_1\n"
    "v_lshlrev_b32_sdwa v185, 1, v170 dst_sel:DWORD dst_unused:UNUSED_PAD src0_sel:DWORD src1_sel:WORD_1\n"
    "v_lshlrev_b32_sdwa v201, 1, v171 dst_sel:DWORD dst_unused:UNUSED_PAD src0_sel:DWORD src1_sel:WORD_1\n"
    "v_mfma_f32_16x16x32_bf16 v[140:143], v[164:167], v[188:191], v[140:143]\n"
    "s_waitcnt vmcnt(2)\n"
    "ds_read_b128 a[252:255], v244 offset:36864\n"
    "ds_read_u16 v184, v184\n"
    "v_mfma_f32_16x16x32_bf16 v[168:171], a[88:91], a[224:227], 0\n"
    "s_nop 0\n"
    "ds_read_u16_d16_hi v200, v200\n"
    "ds_read_u16 v185, v185\n"
    "v_mfma_f32_16x16x32_bf16 v[168:171], a[136:139], a[228:231], v[168:171]\n"
    "buffer_load_dwordx4 v[0:3], v246, s[40:43], s60 offen offset:0\n"
    "buffer_load_dwordx4 v[32:35], v246, s[40:43], s60 offen offset:64\n"
    "ds_read_u16_d16_hi v201, v201\n"
    "v_mfma_f32_16x16x32_bf16 v[168:171], a[52:55], a[232:235], v[168:171]\n"
    "v_lshlrev_b32_sdwa v186, 1, v172 dst_sel:DWORD dst_unused:UNUSED_PAD src0_sel:DWORD src1_sel:WORD_1\n"
    "v_lshlrev_b32_sdwa v202, 1, v173 dst_sel:DWORD dst_unused:UNUSED_PAD src0_sel:DWORD src1_sel:WORD_1\n"
    "v_smfmac_f32_32x32x32_bf16 v[128:143], a[248:251], v[144:151], v241\n"
    "ds_read_u16 v186, v186\n"
    "v_lshlrev_b32_sdwa v187, 1, v174 dst_sel:DWORD dst_unused:UNUSED_PAD src0_sel:DWORD src1_sel:WORD_1\n"
    "v_lshlrev_b32_sdwa v203, 1, v175 dst_sel:DWORD dst_unused:UNUSED_PAD src0_sel:DWORD src1_sel:WORD_1\n"
    "v_lshlrev_b32_sdwa v188, 1, v176 dst_sel:DWORD dst_unused:UNUSED_PAD src0_sel:DWORD src1_sel:WORD_1\n"
    "v_lshlrev_b32_sdwa v204, 1, v177 dst_sel:DWORD dst_unused:UNUSED_PAD src0_sel:DWORD src1_sel:WORD_1\n"
    "v_mfma_f32_16x16x32_bf16 v[172:175], a[92:95], a[224:227], 0\n"
    "ds_read_b128 v[236:239], v244 offset:20480\n"
    "v_lshlrev_b32_sdwa v189, 1, v178 dst_sel:DWORD dst_unused:UNUSED_PAD src0_sel:DWORD src1_sel:WORD_1\n"
    "v_mfma_f32_16x16x32_bf16 v[172:175], a[140:143], a[228:231], v[172:175]\n"
    "s_nop 0\n"
    "ds_read_u16_d16_hi v202, v202\n"
    "v_lshlrev_b32_sdwa v205, 1, v179 dst_sel:DWORD dst_unused:UNUSED_PAD src0_sel:DWORD src1_sel:WORD_1\n"
    "v_mfma_f32_16x16x32_bf16 v[176:179], a[88:91], a[236:239], 0\n"
    "s_waitcnt lgkmcnt(7)\n"
    "v_lshlrev_b32_sdwa v190, 1, v180 dst_sel:DWORD dst_unused:UNUSED_PAD src0_sel:DWORD src1_sel:WORD_1\n"
    "v_lshlrev_b32_sdwa v206, 1, v181 dst_sel:DWORD dst_unused:UNUSED_PAD src0_sel:DWORD src1_sel:WORD_1\n"
    "v_mfma_f32_16x16x32_bf16 v[172:175], a[252:255], a[232:235], v[172:175]\n"
    "ds_read_u16 v187, v187\n"
    "v_lshlrev_b32_sdwa v191, 1, v182 dst_sel:DWORD dst_unused:UNUSED_PAD src0_sel:DWORD src1_sel:WORD_1\n"
    "v_mfma_f32_16x16x32_bf16 v[176:179], a[136:139], a[240:243], v[176:179]\n"
    "ds_read_b128 v[164:167], v244 offset:22528\n"
    "v_lshlrev_b32_sdwa v207, 1, v183 dst_sel:DWORD dst_unused:UNUSED_PAD src0_sel:DWORD src1_sel:WORD_1\n"
    "v_mfma_f32_16x16x32_bf16 v[176:179], a[52:55], a[244:247], v[176:179]\n"
    "ds_read_u16_d16_hi v203, v203\n"
    "v_pk_fma_f16 v234, v234, s49, s49\n"
    "v_mfma_f32_16x16x32_bf16 v[180:183], a[92:95], a[236:239], 0\n"
    "v_rcp_f16_e32 v232, v234\n"
    "v_mfma_f32_16x16x32_bf16 v[180:183], a[140:143], a[240:243], v[180:183]\n"
    "s_waitcnt lgkmcnt(8)\n"
    "ds_read_u16 v188, v188\n"
    "v_rcp_f16_sdwa v232, v234 dst_sel:WORD_1 dst_unused:UNUSED_PRESERVE src0_sel:WORD_1\n"
    "v_mfma_f32_16x16x32_bf16 v[180:183], a[252:255], a[244:247], v[180:183]\n"
    "v_fma_mix_f32 v234, v196, v232, 0 op_sel_hi:[1,1,0]\n"
    "v_fma_mix_f32 v232, v196, v232, 0 op_sel:[1,1,0] op_sel_hi:[1,1,0]\n"
    "v_mfma_f32_16x16x32_bf16 v[96:99], a[208:211], v[192:195], v[96:99]\n"
    "ds_read_u16_d16_hi v204, v204\n"
    "v_cvt_pk_bf16_f32 v196, v234, v232\n"
    "v_mfma_f32_16x16x32_bf16 v[100:103], a[212:215], v[192:195], v[100:103]\n"
    "s_waitcnt lgkmcnt(8)\n"
    "ds_read_u16 v189, v189\n"
    "v_or_b32_e32 v184, v184, v200\n"
    "v_mfma_f32_16x16x32_bf16 v[112:115], a[216:219], v[192:195], v[112:115]\n"
    "buffer_load_dwordx4 v[20:23], v247, s[40:43], s60 offen offset:0\n"
    "buffer_load_dwordx4 v[52:55], v247, s[40:43], s60 offen offset:64\n"
    "v_mfma_f32_16x16x32_bf16 v[116:119], a[220:223], v[192:195], v[116:119]\n"
    "s_waitcnt lgkmcnt(7)\n"
    "ds_read_u16_d16_hi v205, v205\n"
    "v_or_b32_e32 v185, v185, v201\n"
    "v_mfma_f32_16x16x32_bf16 v[128:131], v[236:239], v[192:195], v[128:131]\n"
    "v_lshlrev_b32_sdwa v201, 1, v171 dst_sel:DWORD dst_unused:UNUSED_PAD src0_sel:DWORD src1_sel:WORD_1\n"
    "v_cvt_pk_bf16_f32 v208, v96, v97\n"
    "v_mfma_f32_16x16x32_bf16 v[104:107], a[208:211], v[196:199], v[104:107]\n"
    "ds_read_u16 v190, v190\n"
    "v_cvt_pk_bf16_f32 v209, v98, v99\n"
    "v_mfma_f32_16x16x32_bf16 v[108:111], a[212:215], v[196:199], v[108:111]\n"
    "s_waitcnt lgkmcnt(8)\n"
    "v_or_b32_e64 v186, v186, v202\n"
    "v_lshlrev_b32_sdwa v202, 1, v173 dst_sel:DWORD dst_unused:UNUSED_PAD src0_sel:DWORD src1_sel:WORD_1\n"
    "v_mfma_f32_16x16x32_bf16 v[120:123], a[216:219], v[196:199], v[120:123]\n"
    "s_waitcnt lgkmcnt(6)\n"
    "ds_read_u16_d16_hi v206, v206\n"
    "v_cvt_pk_bf16_f32 v210, v100, v101\n"
    "v_mfma_f32_16x16x32_bf16 v[132:135], v[164:167], v[192:195], v[132:135]\n"
    "ds_read_u16 v191, v191\n"
    "v_cvt_pk_f16_f32 v192, v168, v169\n"
    "v_mfma_f32_16x16x32_bf16 v[124:127], a[220:223], v[196:199], v[124:127]\n"
    "s_nop 0\n"
    "v_lshlrev_b32_sdwa v193, 1, v170 dst_sel:DWORD dst_unused:UNUSED_PAD src0_sel:DWORD src1_sel:WORD_1\n"
    "v_lshlrev_b32_sdwa v194, 1, v172 dst_sel:DWORD dst_unused:UNUSED_PAD src0_sel:DWORD src1_sel:WORD_1\n"
    "v_mfma_f32_16x16x32_bf16 v[136:139], v[236:239], v[196:199], v[136:139]\n"
    "s_waitcnt lgkmcnt(7)\n"
    "ds_read_u16_d16_hi v207, v207\n"
    "v_lshlrev_b32_sdwa v195, 1, v174 dst_sel:DWORD dst_unused:UNUSED_PAD src0_sel:DWORD src1_sel:WORD_1\n"
    "v_mfma_f32_16x16x32_bf16 v[140:143], v[164:167], v[196:199], v[140:143]\n"
    "v_cvt_pk_f16_f32 v196, v176, v177\n"
    "v_lshlrev_b32_sdwa v197, 1, v178 dst_sel:DWORD dst_unused:UNUSED_PAD src0_sel:DWORD src1_sel:WORD_1\n"
    "v_mfma_f32_16x16x32_bf16 v[168:171], a[96:99], a[224:227], 0\n"
    "ds_read_u16 v193, v193\n"
    "v_lshlrev_b32_sdwa v198, 1, v180 dst_sel:DWORD dst_unused:UNUSED_PAD src0_sel:DWORD src1_sel:WORD_1\n"
    "v_mfma_f32_16x16x32_bf16 v[168:171], a[144:147], a[228:231], v[168:171]\n"
    "v_lshlrev_b32_sdwa v199, 1, v182 dst_sel:DWORD dst_unused:UNUSED_PAD src0_sel:DWORD src1_sel:WORD_1\n"
    "v_or_b32_e32 v187, v187, v203\n"
    "v_mfma_f32_16x16x32_bf16 v[168:171], a[56:59], a[232:235], v[168:171]\n"
    "s_waitcnt lgkmcnt(7)\n"
    "ds_read_u16_d16_hi v201, v201\n"
    "v_lshlrev_b32_sdwa v203, 1, v175 dst_sel:DWORD dst_unused:UNUSED_PAD src0_sel:DWORD src1_sel:WORD_1\n"
    "v_mfma_f32_16x16x32_bf16 v[172:175], a[100:103], a[224:227], 0\n"
    "ds_read_u16 v194, v194\n"
    "v_or_b32_e64 v188, v188, v204\n"
    "v_mfma_f32_16x16x32_bf16 v[172:175], a[148:151], a[228:231], v[172:175]\n"
    "v_cvt_pk_bf16_f32 v211, v102, v103\n"
    "v_cvt_pk_bf16_f32 v212, v112, v113\n"
    "v_mfma_f32_16x16x32_bf16 v[172:175], a[60:63], a[232:235], v[172:175]\n"
    "s_waitcnt lgkmcnt(7)\n"
    "ds_read_u16_d16_hi v202, v202\n"
    "v_or_b32_e32 v189, v189, v205\n"
    "v_lshlrev_b32_sdwa v205, 1, v179 dst_sel:DWORD dst_unused:UNUSED_PAD src0_sel:DWORD src1_sel:WORD_1\n"
    "v_mfma_f32_16x16x32_bf16 v[176:179], a[96:99], a[236:239], 0\n"
    "v_cvt_pk_bf16_f32 v213, v114, v115\n"
    "v_cvt_pk_bf16_f32 v214, v116, v117\n"
    "v_mfma_f32_16x16x32_bf16 v[176:179], a[144:147], a[240:243], v[176:179]\n"
    "ds_read_b128 v[236:239], v244 offset:0\n"
    "ds_read_u16 v195, v195\n"
    "v_mfma_f32_16x16x32_bf16 v[176:179], a[56:59], a[244:247], v[176:179]\n"
    "s_waitcnt lgkmcnt(8)\n"
    "buffer_load_dwordx4 v[4:7], v246, s[40:43], s61 offen offset:0\n"
    "buffer_load_dwordx4 v[36:39], v246, s[40:43], s61 offen offset:64\n"
    "v_or_b32_e32 v190, v190, v206\n"
    "v_mfma_f32_16x16x32_bf16 v[96:99], a[0:3], v[184:187], 0\n"
    "ds_read_u16_d16_hi v203, v203\n"
    "v_lshlrev_b32_sdwa v206, 1, v181 dst_sel:DWORD dst_unused:UNUSED_PAD src0_sel:DWORD src1_sel:WORD_1\n"
    "v_mfma_f32_16x16x32_bf16 v[100:103], a[4:7], v[184:187], 0\n"
    "v_cvt_pk_bf16_f32 v215, v118, v119\n"
    "v_cvt_pk_bf16_f32 v216, v128, v129\n"
    "v_mfma_f32_16x16x32_bf16 v[112:115], a[8:11], v[184:187], 0\n"
    "ds_read_b128 v[164:167], v244 offset:2048\n"
    "v_cvt_pk_bf16_f32 v217, v130, v131\n"
    "s_waitcnt lgkmcnt(8)\n"
    "v_or_b32_e32 v191, v191, v207\n"
    "v_mfma_f32_16x16x32_bf16 v[116:119], a[12:15], v[184:187], 0\n"
    "ds_read_u16 v197, v197\n"
    "v_lshlrev_b32_sdwa v207, 1, v183 dst_sel:DWORD dst_unused:UNUSED_PAD src0_sel:DWORD src1_sel:WORD_1\n"
    "v_mfma_f32_16x16x32_bf16 v[180:183], a[100:103], a[236:239], 0\n"
    "ds_read_u16_d16_hi v205, v205\n"
    "v_cvt_pk_bf16_f32 v218, v132, v133\n"
    "v_mfma_f32_16x16x32_bf16 v[180:183], a[148:151], a[240:243], v[180:183]\n"
    "v_cvt_pk_bf16_f32 v219, v134, v135\n"
    "v_cvt_pk_bf16_f32 v220, v104, v105\n"
    "v_mfma_f32_16x16x32_bf16 v[180:183], a[60:63], a[244:247], v[180:183]\n"
    "s_waitcnt lgkmcnt(8)\n"
    "ds_read_u16 v198, v198\n"
    "v_or_b32_e32 v193, v193, v201\n"
    "v_cvt_pk_bf16_f32 v221, v106, v107\n"
    "v_mfma_f32_16x16x32_bf16 v[104:107], a[0:3], v[188:191], 0\n"
    "ds_read_u16_d16_hi v206, v206\n"
    "v_cvt_pk_bf16_f32 v222, v108, v109\n"
    "v_cvt_pk_bf16_f32 v223, v110, v111\n"
    "v_mfma_f32_16x16x32_bf16 v[108:111], a[4:7], v[188:191], 0\n"
    "s_waitcnt lgkmcnt(7)\n"
    "v_or_b32_e32 v194, v194, v202\n"
    "v_cvt_pk_bf16_f32 v224, v120, v121\n"
    "v_mfma_f32_16x16x32_bf16 v[128:131], v[236:239], v[184:187], 0\n"
    "ds_read_u16 v199, v199\n"
    "v_cvt_pk_bf16_f32 v225, v122, v123\n"
    "v_cvt_pk_bf16_f32 v226, v124, v125\n"
    "v_cvt_pk_bf16_f32 v227, v126, v127\n"
    "v_pk_fma_f16 v232, v192, v192, s48\n"
    "v_mfma_f32_16x16x32_bf16 v[120:123], a[8:11], v[188:191], 0\n"
    "ds_read_u16_d16_hi v207, v207\n"
    "v_cvt_pk_bf16_f32 v228, v136, v137\n"
    "v_mfma_f32_16x16x32_bf16 v[124:127], a[12:15], v[188:191], 0\n"
    "v_cvt_pk_bf16_f32 v229, v138, v139\n"
    "v_cvt_pk_bf16_f32 v230, v140, v141\n"
    "v_cvt_pk_bf16_f32 v231, v142, v143\n"
    "s_waitcnt lgkmcnt(6)\n"
    "v_mfma_f32_16x16x32_bf16 v[132:135], v[164:167], v[184:187], 0\n"
    "v_or_b32_e32 v195, v195, v203\n"
    "v_pk_mul_f16 v232, v192, v232\n"
    "v_mfma_f32_16x16x32_bf16 v[136:139], v[236:239], v[188:191], 0\n"
    "v_exp_f16_e64 v234, v232\n"
    "v_mfma_f32_16x16x32_bf16 v[140:143], v[164:167], v[188:191], 0\n"
    "v_exp_f16_sdwa v234, v232 dst_sel:WORD_1 dst_unused:UNUSED_PRESERVE src0_sel:WORD_1\n"
    "v_lshlrev_b32_sdwa v184, 1, v168 dst_sel:DWORD dst_unused:UNUSED_PAD src0_sel:DWORD src1_sel:WORD_1\n"
    "v_pk_fma_f16 v234, v234, s49, s49\n"
    "v_lshlrev_b32_sdwa v200, 1, v169 dst_sel:DWORD dst_unused:UNUSED_PAD src0_sel:DWORD src1_sel:WORD_1\n"
    "v_lshlrev_b32_sdwa v185, 1, v170 dst_sel:DWORD dst_unused:UNUSED_PAD src0_sel:DWORD src1_sel:WORD_1\n"
    "v_lshlrev_b32_sdwa v201, 1, v171 dst_sel:DWORD dst_unused:UNUSED_PAD src0_sel:DWORD src1_sel:WORD_1\n"
    "v_mfma_f32_16x16x32_bf16 v[168:171], a[104:107], a[224:227], 0\n"
    "s_waitcnt lgkmcnt(4)\n"
    "buffer_load_dwordx4 v[24:27], v247, s[40:43], s61 offen offset:0\n"
    "buffer_load_dwordx4 v[56:59], v247, s[40:43], s61 offen offset:64\n"
    "ds_read_u16 v184, v184\n"
    "v_mfma_f32_16x16x32_bf16 v[168:171], a[152:155], a[228:231], v[168:171]\n"
    "s_nop 0\n"
    "ds_read_u16_d16_hi v200, v200\n"
    "ds_read_u16 v185, v185\n"
    "v_or_b32_e64 v197, v197, v205\n"
    "v_lshlrev_b32_sdwa v186, 1, v172 dst_sel:DWORD dst_unused:UNUSED_PAD src0_sel:DWORD src1_sel:WORD_1\n"
    "v_lshlrev_b32_sdwa v202, 1, v173 dst_sel:DWORD dst_unused:UNUSED_PAD src0_sel:DWORD src1_sel:WORD_1\n"
    "v_lshlrev_b32_sdwa v187, 1, v174 dst_sel:DWORD dst_unused:UNUSED_PAD src0_sel:DWORD src1_sel:WORD_1\n"
    "v_lshlrev_b32_sdwa v203, 1, v175 dst_sel:DWORD dst_unused:UNUSED_PAD src0_sel:DWORD src1_sel:WORD_1\n"
    "v_rcp_f16_e32 v232, v234\n"
    "v_mfma_f32_16x16x32_bf16 v[168:171], a[64:67], a[232:235], v[168:171]\n"
    "s_waitcnt lgkmcnt(5)\n"
    "ds_read_u16_d16_hi v201, v201\n"
    "ds_read_u16 v186, v186\n"
    "v_or_b32_e32 v198, v198, v206\n"
    "v_rcp_f16_sdwa v232, v234 dst_sel:WORD_1 dst_unused:UNUSED_PRESERVE src0_sel:WORD_1\n"
    "v_mfma_f32_16x16x32_bf16 v[172:175], a[108:111], a[224:227], 0\n"
    "ds_read_b128 v[236:239], v244 offset:4096\n"
    "v_lshlrev_b32_sdwa v188, 1, v176 dst_sel:DWORD dst_unused:UNUSED_PAD src0_sel:DWORD src1_sel:WORD_1\n"
    "v_mfma_f32_16x16x32_bf16 v[172:175], a[156:159], a[228:231], v[172:175]\n"
    "s_waitcnt lgkmcnt(6)\n"
    "ds_read_u16_d16_hi v202, v202\n"
    "v_lshlrev_b32_sdwa v204, 1, v177 dst_sel:DWORD dst_unused:UNUSED_PAD src0_sel:DWORD src1_sel:WORD_1\n"
    "v_lshlrev_b32_sdwa v189, 1, v178 dst_sel:DWORD dst_unused:UNUSED_PAD src0_sel:DWORD src1_sel:WORD_1\n"
    "v_lshlrev_b32_sdwa v205, 1, v179 dst_sel:DWORD dst_unused:UNUSED_PAD src0_sel:DWORD src1_sel:WORD_1\n"
    "v_lshlrev_b32_sdwa v190, 1, v180 dst_sel:DWORD dst_unused:UNUSED_PAD src0_sel:DWORD src1_sel:WORD_1\n"
    "v_lshlrev_b32_sdwa v206, 1, v181 dst_sel:DWORD dst_unused:UNUSED_PAD src0_sel:DWORD src1_sel:WORD_1\n"
    "v_lshlrev_b32_sdwa v191, 1, v182 dst_sel:DWORD dst_unused:UNUSED_PAD src0_sel:DWORD src1_sel:WORD_1\n"
    "v_fma_mix_f32 v234, v192, v232, 0 op_sel_hi:[1,1,0]\n"
    "v_mfma_f32_16x16x32_bf16 v[172:175], a[68:71], a[232:235], v[172:175]\n"
    "ds_read_u16 v187, v187\n"
    "v_or_b32_e64 v199, v199, v207\n"
    "v_lshlrev_b32_sdwa v207, 1, v183 dst_sel:DWORD dst_unused:UNUSED_PAD src0_sel:DWORD src1_sel:WORD_1\n"
    "v_fma_mix_f32 v232, v192, v232, 0 op_sel:[1,1,0] op_sel_hi:[1,1,0]\n"
    "v_mfma_f32_16x16x32_bf16 v[176:179], a[104:107], a[236:239], 0\n"
    "ds_read_b128 v[164:167], v244 offset:6144\n"
    "ds_read_u16_d16_hi v203, v203\n"
    "v_cvt_pk_bf16_f32 v192, v234, v232\n"
    "v_mfma_f32_16x16x32_bf16 v[176:179], a[152:155], a[240:243], v[176:179]\n"
    "v_pk_fma_f16 v232, v196, v196, s48\n"
    "v_mov_b32_e32 v254, 0\n"
    "v_mfma_f32_16x16x32_bf16 v[176:179], a[64:67], a[244:247], v[176:179]\n"
    "s_waitcnt lgkmcnt(8)\n"
    "ds_read_u16 v188, v188\n"
    "v_pk_mul_f16 v232, v196, v232\n"
    "v_mfma_f32_16x16x32_bf16 v[180:183], a[108:111], a[236:239], 0\n"
    "v_exp_f16_e32 v234, v232\n"
    "v_mfma_f32_16x16x32_bf16 v[180:183], a[156:159], a[240:243], v[180:183]\n"
    "ds_read_u16_d16_hi v204, v204\n"
    "v_exp_f16_sdwa v234, v232 dst_sel:WORD_1 dst_unused:UNUSED_PRESERVE src0_sel:WORD_1\n"
    "v_mfma_f32_16x16x32_bf16 v[180:183], a[68:71], a[244:247], v[180:183]\n"
    "s_waitcnt lgkmcnt(8)\n"
    "ds_read_u16 v189, v189\n"
    "v_pk_fma_f16 v234, v234, s49, s49\n"
    "v_mfma_f32_16x16x32_bf16 v[96:99], a[16:19], v[192:195], v[96:99]\n"
    "buffer_load_dwordx4 v[8:11], v246, s[40:43], s62 offen offset:0\n"
    "buffer_load_dwordx4 v[40:43], v246, s[40:43], s62 offen offset:64\n"
    "ds_read_u16_d16_hi v205, v205\n"
    "v_mfma_f32_16x16x32_bf16 v[100:103], a[20:23], v[192:195], v[100:103]\n"
    "v_rcp_f16_e64 v232, v234\n"
    "v_mfma_f32_16x16x32_bf16 v[112:115], a[24:27], v[192:195], v[112:115]\n"
    "ds_read_u16 v190, v190\n"
    "v_rcp_f16_sdwa v232, v234 dst_sel:WORD_1 dst_unused:UNUSED_PRESERVE src0_sel:WORD_1\n"
    "v_mfma_f32_16x16x32_bf16 v[116:119], a[28:31], v[192:195], v[116:119]\n"
    "s_waitcnt lgkmcnt(8)\n"
    "v_fma_mix_f32 v234, v196, v232, 0 op_sel_hi:[1,1,0]\n"
    "v_fma_mix_f32 v232, v196, v232, 0 op_sel:[1,1,0] op_sel_hi:[1,1,0]\n"
    "v_mfma_f32_16x16x32_bf16 v[128:131], v[236:239], v[192:195], v[128:131]\n"
    "s_waitcnt lgkmcnt(6)\n"
    "ds_read_u16_d16_hi v206, v206\n"
    "v_cvt_pk_bf16_f32 v196, v234, v232\n"
    "v_mfma_f32_16x16x32_bf16 v[132:135], v[164:167], v[192:195], v[132:135]\n"
    "v_or_b32_e64 v184, v184, v200\n"
    "v_or_b32_e32 v185, v185, v201\n"
    "v_mfma_f32_16x16x32_bf16 v[104:107], a[16:19], v[196:199], v[104:107]\n"
    "ds_read_u16 v191, v191\n"
    "v_cvt_pk_f16_f32 v192, v168, v169\n"
    "v_mfma_f32_16x16x32_bf16 v[108:111], a[20:23], v[196:199], v[108:111]\n"
    "s_waitcnt lgkmcnt(7)\n"
    "ds_read_u16_d16_hi v207, v207\n"
    "v_lshlrev_b32_sdwa v193, 1, v170 dst_sel:DWORD dst_unused:UNUSED_PAD src0_sel:DWORD src1_sel:WORD_1\n"
    "v_mfma_f32_16x16x32_bf16 v[120:123], a[24:27], v[196:199], v[120:123]\n"
    "v_lshlrev_b32_sdwa v201, 1, v171 dst_sel:DWORD dst_unused:UNUSED_PAD src0_sel:DWORD src1_sel:WORD_1\n"
    "v_lshlrev_b32_sdwa v194, 1, v172 dst_sel:DWORD dst_unused:UNUSED_PAD src0_sel:DWORD src1_sel:WORD_1\n"
    "v_mfma_f32_16x16x32_bf16 v[124:127], a[28:31], v[196:199], v[124:127]\n"
    "ds_read_b128 a[252:255], v244 offset:34816\n"
    "v_or_b32_e32 v186, v186, v202\n"
    "v_mfma_f32_16x16x32_bf16 v[136:139], v[236:239], v[196:199], v[136:139]\n"
    "ds_read_u16 v193, v193\n"
    "v_lshlrev_b32_sdwa v202, 1, v173 dst_sel:DWORD dst_unused:UNUSED_PAD src0_sel:DWORD src1_sel:WORD_1\n"
    "v_mfma_f32_16x16x32_bf16 v[140:143], v[164:167], v[196:199], v[140:143]\n"
    "s_waitcnt lgkmcnt(8)\n"
    "v_lshlrev_b32_sdwa v195, 1, v174 dst_sel:DWORD dst_unused:UNUSED_PAD src0_sel:DWORD src1_sel:WORD_1\n"
    "v_cvt_pk_f16_f32 v196, v176, v177\n"
    "v_mfma_f32_16x16x32_bf16 v[168:171], a[112:115], a[224:227], 0\n"
    "ds_read_u16_d16_hi v201, v201\n"
    "v_lshlrev_b32_sdwa v197, 1, v178 dst_sel:DWORD dst_unused:UNUSED_PAD src0_sel:DWORD src1_sel:WORD_1\n"
    "v_mfma_f32_16x16x32_bf16 v[168:171], a[160:163], a[228:231], v[168:171]\n"
    "v_lshlrev_b32_sdwa v198, 1, v180 dst_sel:DWORD dst_unused:UNUSED_PAD src0_sel:DWORD src1_sel:WORD_1\n"
    "v_lshlrev_b32_sdwa v199, 1, v182 dst_sel:DWORD dst_unused:UNUSED_PAD src0_sel:DWORD src1_sel:WORD_1\n"
    "v_mfma_f32_16x16x32_bf16 v[168:171], a[72:75], a[232:235], v[168:171]\n"
    "s_waitcnt lgkmcnt(7)\n"
    "ds_read_u16 v194, v194\n"
    "v_or_b32_e32 v187, v187, v203\n"
    "v_lshlrev_b32_sdwa v203, 1, v175 dst_sel:DWORD dst_unused:UNUSED_PAD src0_sel:DWORD src1_sel:WORD_1\n"
    "v_mfma_f32_16x16x32_bf16 v[172:175], a[116:119], a[224:227], 0\n"
    "ds_read_u16_d16_hi v202, v202\n"
    "v_or_b32_e64 v188, v188, v204\n"
    "v_mfma_f32_16x16x32_bf16 v[172:175], a[164:167], a[228:231], v[172:175]\n"
    "v_or_b32_e64 v189, v189, v205\n"
    "v_lshlrev_b32_sdwa v205, 1, v179 dst_sel:DWORD dst_unused:UNUSED_PAD src0_sel:DWORD src1_sel:WORD_1\n"
    "v_mfma_f32_16x16x32_bf16 v[176:179], a[112:115], a[236:239], 0\n"
    "s_nop 0\n"
    "ds_read_b128 v[236:239], v244 offset:8192\n"
    "v_pk_fma_f16 v232, v192, v192, s48\n"
    "v_mfma_f32_16x16x32_bf16 v[176:179], a[160:163], a[240:243], v[176:179]\n"
    "s_waitcnt lgkmcnt(8)\n"
    "buffer_load_dwordx4 v[12:15], v246, s[40:43], s63 offen offset:0\n"
    "buffer_load_dwordx4 v[44:47], v246, s[40:43], s63 offen offset:64\n"
    "ds_read_u16 v195, v195\n"
    "v_mfma_f32_16x16x32_bf16 v[176:179], a[72:75], a[244:247], v[176:179]\n"
    "ds_read_u16_d16_hi v203, v203\n"
    "v_or_b32_e64 v190, v190, v206\n"
    "v_mfma_f32_16x16x32_bf16 v[96:99], a[32:35], v[184:187], v[96:99]\n"
    "v_lshlrev_b32_sdwa v206, 1, v181 dst_sel:DWORD dst_unused:UNUSED_PAD src0_sel:DWORD src1_sel:WORD_1\n"
    "v_pk_mul_f16 v232, v192, v232\n"
    "v_mfma_f32_16x16x32_bf16 v[100:103], a[36:39], v[184:187], v[100:103]\n"
    "s_waitcnt lgkmcnt(7)\n"
    "ds_read_b128 v[164:167], v244 offset:10240\n"
    "v_or_b32_e32 v191, v191, v207\n"
    "v_mfma_f32_16x16x32_bf16 v[172:175], a[252:255], a[232:235], v[172:175]\n"
    "ds_read_u16 v197, v197\n"
    "v_lshlrev_b32_sdwa v207, 1, v183 dst_sel:DWORD dst_unused:UNUSED_PAD src0_sel:DWORD src1_sel:WORD_1\n"
    "v_mfma_f32_16x16x32_bf16 v[180:183], a[116:119], a[236:239], 0\n"
    "v_exp_f16_e64 v234, v232\n"
    "v_mfma_f32_16x16x32_bf16 v[180:183], a[164:167], a[240:243], v[180:183]\n"
    "ds_read_u16_d16_hi v205, v205\n"
    "v_exp_f16_sdwa v234, v232 dst_sel:WORD_1 dst_unused:UNUSED_PRESERVE src0_sel:WORD_1\n"
    "v_mfma_f32_16x16x32_bf16 v[180:183], a[252:255], a[244:247], v[180:183]\n"
    "s_waitcnt lgkmcnt(8)\n"
    "ds_read_u16 v198, v198\n"
    "v_or_b32_e32 v193, v193, v201\n"
    "v_mfma_f32_16x16x32_bf16 v[112:115], a[40:43], v[184:187], v[112:115]\n"
    "v_pk_fma_f16 v234, v234, s49, s49\n"
    "v_lshlrev_b32_sdwa v200, 1, v169 dst_sel:DWORD dst_unused:UNUSED_PAD src0_sel:DWORD src1_sel:WORD_1\n"
    "v_mfma_f32_16x16x32_bf16 v[116:119], a[44:47], v[184:187], v[116:119]\n"
    "ds_read_u16_d16_hi v206, v206\n"
    "v_lshlrev_b32_sdwa v201, 1, v171 dst_sel:DWORD dst_unused:UNUSED_PAD src0_sel:DWORD src1_sel:WORD_1\n"
    "v_mfma_f32_16x16x32_bf16 v[104:107], a[32:35], v[188:191], v[104:107]\n"
    "s_waitcnt lgkmcnt(7)\n"
    "v_or_b32_e32 v194, v194, v202\n"
    "v_lshlrev_b32_sdwa v202, 1, v173 dst_sel:DWORD dst_unused:UNUSED_PAD src0_sel:DWORD src1_sel:WORD_1\n"
    "v_mfma_f32_16x16x32_bf16 v[128:131], v[236:239], v[184:187], v[128:131]\n"
    "ds_read_u16 v199, v199\n"
    "v_rcp_f16_e64 v232, v234\n"
    "v_mfma_f32_16x16x32_bf16 v[108:111], a[36:39], v[188:191], v[108:111]\n"
    "ds_read_u16_d16_hi v207, v207\n"
    "v_rcp_f16_sdwa v232, v234 dst_sel:WORD_1 dst_unused:UNUSED_PRESERVE src0_sel:WORD_1\n"
    "v_mfma_f32_16x16x32_bf16 v[120:123], a[40:43], v[188:191], v[120:123]\n"
    "v_lshlrev_b32_sdwa v204, 1, v177 dst_sel:DWORD dst_unused:UNUSED_PAD src0_sel:DWORD src1_sel:WORD_1\n"
    "v_fma_mix_f32 v234, v192, v232, 0 op_sel_hi:[1,1,0]\n"
    "v_mfma_f32_16x16x32_bf16 v[124:127], a[44:47], v[188:191], v[124:127]\n"
    "s_waitcnt lgkmcnt(6)\n"
    "ds_read_b128 a[252:255], v244 offset:38912\n"
    "v_or_b32_e32 v195, v195, v203\n"
    "v_mfma_f32_16x16x32_bf16 v[132:135], v[164:167], v[184:187], v[132:135]\n"
    "ds_read_u16_d16_hi v200, v200\n"
    "v_lshlrev_b32_sdwa v184, 1, v168 dst_sel:DWORD dst_unused:UNUSED_PAD src0_sel:DWORD src1_sel:WORD_1\n"
    "v_mfma_f32_16x16x32_bf16 v[136:139], v[236:239], v[188:191], v[136:139]\n"
    "v_lshlrev_b32_sdwa v185, 1, v170 dst_sel:DWORD dst_unused:UNUSED_PAD src0_sel:DWORD src1_sel:WORD_1\n"
    "v_lshlrev_b32_sdwa v186, 1, v172 dst_sel:DWORD dst_unused:UNUSED_PAD src0_sel:DWORD src1_sel:WORD_1\n"
    "v_mfma_f32_16x16x32_bf16 v[140:143], v[164:167], v[188:191], v[140:143]\n"
    "ds_read_u16 v184, v184\n"
    "v_lshlrev_b32_sdwa v187, 1, v174 dst_sel:DWORD dst_unused:UNUSED_PAD src0_sel:DWORD src1_sel:WORD_1\n"
    "v_mfma_f32_16x16x32_bf16 v[168:171], a[120:123], a[224:227], 0\n"
    "buffer_load_dwordx4 v[16:19], v246, s[40:43], s52 offen offset:0\n"
    "buffer_load_dwordx4 v[48:51], v246, s[40:43], s52 offen offset:64\n"
    "ds_read_u16 v185, v185\n"
    "v_mfma_f32_16x16x32_bf16 v[168:171], a[168:171], a[228:231], v[168:171]\n"
    "s_waitcnt lgkmcnt(8)\n"
    "v_or_b32_e32 v197, v197, v205\n"
    "v_lshlrev_b32_sdwa v203, 1, v175 dst_sel:DWORD dst_unused:UNUSED_PAD src0_sel:DWORD src1_sel:WORD_1\n"
    "v_mfma_f32_16x16x32_bf16 v[168:171], a[76:79], a[232:235], v[168:171]\n"
    "ds_read_u16_d16_hi v201, v201\n"
    "v_lshlrev_b32_sdwa v188, 1, v176 dst_sel:DWORD dst_unused:UNUSED_PAD src0_sel:DWORD src1_sel:WORD_1\n"
    "v_mfma_f32_16x16x32_bf16 v[172:175], a[124:127], a[224:227], 0\n"
    "v_lshlrev_b32_sdwa v189, 1, v178 dst_sel:DWORD dst_unused:UNUSED_PAD src0_sel:DWORD src1_sel:WORD_1\n"
    "v_lshlrev_b32_sdwa v205, 1, v179 dst_sel:DWORD dst_unused:UNUSED_PAD src0_sel:DWORD src1_sel:WORD_1\n"
    "v_mfma_f32_16x16x32_bf16 v[172:175], a[172:175], a[228:231], v[172:175]\n"
    "s_waitcnt lgkmcnt(7)\n"
    "ds_read_u16 v186, v186\n"
    "v_or_b32_e32 v198, v198, v206\n"
    "v_lshlrev_b32_sdwa v190, 1, v180 dst_sel:DWORD dst_unused:UNUSED_PAD src0_sel:DWORD src1_sel:WORD_1\n"
    "v_lshlrev_b32_sdwa v206, 1, v181 dst_sel:DWORD dst_unused:UNUSED_PAD src0_sel:DWORD src1_sel:WORD_1\n"
    "v_lshlrev_b32_sdwa v191, 1, v182 dst_sel:DWORD dst_unused:UNUSED_PAD src0_sel:DWORD src1_sel:WORD_1\n"
    "v_fma_mix_f32 v232, v192, v232, 0 op_sel:[1,1,0] op_sel_hi:[1,1,0]\n"
    "v_mfma_f32_16x16x32_bf16 v[176:179], a[120:123], a[236:239], 0\n"
    "ds_read_b128 v[236:239], v244 offset:12288\n"
    "ds_read_u16_d16_hi v202, v202\n"
    "v_cvt_pk_bf16_f32 v192, v234, v232\n"
    "v_mfma_f32_16x16x32_bf16 v[176:179], a[168:171], a[240:243], v[176:179]\n"
    "s_waitcnt lgkmcnt(7)\n"
    "v_or_b32_e32 v199, v199, v207\n"
    "v_lshlrev_b32_sdwa v207, 1, v183 dst_sel:DWORD dst_unused:UNUSED_PAD src0_sel:DWORD src1_sel:WORD_1\n"
    "v_mfma_f32_16x16x32_bf16 v[172:175], a[252:255], a[232:235], v[172:175]\n"
    "ds_read_u16 v187, v187\n"
    "v_pk_fma_f16 v232, v196, v196, s48\n"
    "v_mfma_f32_16x16x32_bf16 v[176:179], a[76:79], a[244:247], v[176:179]\n"
    "v_pk_mul_f16 v232, v196, v232\n"
    "v_exp_f16_e64 v234, v232\n"
    "v_mfma_f32_16x16x32_bf16 v[180:183], a[124:127], a[236:239], 0\n"
    "ds_read_b128 v[164:167], v244 offset:14336\n"
    "ds_read_u16_d16_hi v203, v203\n"
    "v_mfma_f32_16x16x32_bf16 v[180:183], a[172:175], a[240:243], v[180:183]\n"
    "s_nop 0\n"
    "v_exp_f16_sdwa v234, v232 dst_sel:WORD_1 dst_unused:UNUSED_PRESERVE src0_sel:WORD_1\n"
    "v_mfma_f32_16x16x32_bf16 v[180:183], a[252:255], a[244:247], v[180:183]\n"
    "s_waitcnt lgkmcnt(8)\n"
    "ds_read_u16 v188, v188\n"
    "v_pk_fma_f16 v234, v234, s49, s49\n"
    "v_mfma_f32_16x16x32_bf16 v[96:99], a[176:179], v[192:195], v[96:99]\n"
    "v_rcp_f16_e32 v232, v234\n"
    "v_mfma_f32_16x16x32_bf16 v[100:103], a[180:183], v[192:195], v[100:103]\n"
    "ds_read_u16_d16_hi v204, v204\n"
    "v_rcp_f16_sdwa v232, v234 dst_sel:WORD_1 dst_unused:UNUSED_PRESERVE src0_sel:WORD_1\n"
    "v_mfma_f32_16x16x32_bf16 v[112:115], a[184:187], v[192:195], v[112:115]\n"
    "s_waitcnt lgkmcnt(8)\n"
    "ds_read_u16 v189, v189\n"
    "v_fma_mix_f32 v234, v196, v232, 0 op_sel_hi:[1,1,0]\n"
    "v_mfma_f32_16x16x32_bf16 v[116:119], a[188:191], v[192:195], v[116:119]\n"
    "v_fma_mix_f32 v232, v196, v232, 0 op_sel:[1,1,0] op_sel_hi:[1,1,0]\n"
    "v_or_b32_e32 v184, v184, v200\n"
    "s_waitcnt lgkmcnt(7)\n"
    "v_mfma_f32_16x16x32_bf16 v[128:131], v[236:239], v[192:195], v[128:131]\n"
    "buffer_load_dwordx4 v[152:155], v245, s[40:43], s57 offen offset:64\n"
    "buffer_load_dwordx4 v[144:147], v245, s[40:43], s57 offen offset:0\n"
    "ds_read_u16_d16_hi v205, v205\n"
    "v_cvt_pk_bf16_f32 v196, v234, v232\n"
    "v_or_b32_e32 v185, v185, v201\n"
    "v_lshlrev_b32_sdwa v201, 1, v171 dst_sel:DWORD dst_unused:UNUSED_PAD src0_sel:DWORD src1_sel:WORD_1\n"
    "v_mfma_f32_16x16x32_bf16 v[104:107], a[176:179], v[196:199], v[104:107]\n"
    "s_waitcnt lgkmcnt(7)\n"
    "ds_read_b128 a[224:227], v243 offset:0\n"
    "ds_read_u16 v190, v190\n"
    "v_mfma_f32_16x16x32_bf16 v[108:111], a[180:183], v[196:199], v[108:111]\n"
    "v_or_b32_e32 v186, v186, v202\n"
    "v_lshlrev_b32_sdwa v202, 1, v173 dst_sel:DWORD dst_unused:UNUSED_PAD src0_sel:DWORD src1_sel:WORD_1\n"
    "v_mfma_f32_16x16x32_bf16 v[120:123], a[184:187], v[196:199], v[120:123]\n"
    "s_waitcnt lgkmcnt(7)\n"
    "ds_read_b128 a[228:231], v243 offset:1024\n"
    "v_mfma_f32_16x16x32_bf16 v[132:135], v[164:167], v[192:195], v[132:135]\n"
    "ds_read_u16_d16_hi v206, v206\n"
    "v_cvt_pk_f16_f32 v192, v168, v169\n"
    "v_mfma_f32_16x16x32_bf16 v[124:127], a[188:191], v[196:199], v[124:127]\n"
    "s_nop 0\n"
    "v_lshlrev_b32_sdwa v193, 1, v170 dst_sel:DWORD dst_unused:UNUSED_PAD src0_sel:DWORD src1_sel:WORD_1\n"
    "v_lshlrev_b32_sdwa v194, 1, v172 dst_sel:DWORD dst_unused:UNUSED_PAD src0_sel:DWORD src1_sel:WORD_1\n"
    "v_mfma_f32_16x16x32_bf16 v[136:139], v[236:239], v[196:199], v[136:139]\n"
    "s_waitcnt lgkmcnt(8)\n"
    "ds_read_b128 a[232:235], v243 offset:2048\n"
    "v_lshlrev_b32_sdwa v195, 1, v174 dst_sel:DWORD dst_unused:UNUSED_PAD src0_sel:DWORD src1_sel:WORD_1\n"
    "v_mfma_f32_16x16x32_bf16 v[140:143], v[164:167], v[196:199], v[140:143]\n"
    "ds_read_u16 v191, v191\n"
    "v_cvt_pk_f16_f32 v196, v176, v177\n"
    "v_lshlrev_b32_sdwa v197, 1, v178 dst_sel:DWORD dst_unused:UNUSED_PAD src0_sel:DWORD src1_sel:WORD_1\n"
    "v_lshlrev_b32_sdwa v198, 1, v180 dst_sel:DWORD dst_unused:UNUSED_PAD src0_sel:DWORD src1_sel:WORD_1\n"
    "v_lshlrev_b32_sdwa v199, 1, v182 dst_sel:DWORD dst_unused:UNUSED_PAD src0_sel:DWORD src1_sel:WORD_1\n"
    "v_or_b32_e64 v187, v187, v203\n"
    "v_lshlrev_b32_sdwa v203, 1, v175 dst_sel:DWORD dst_unused:UNUSED_PAD src0_sel:DWORD src1_sel:WORD_1\n"
    "v_pk_fma_f16 v232, v192, v192, s48\n"
    "s_waitcnt lgkmcnt(8)\n"
    "v_or_b32_e32 v188, v188, v204\n"
    "ds_read_b128 a[236:239], v243 offset:3072\n"
    "ds_read_u16_d16_hi v207, v207\n"
    "v_pk_mul_f16 v232, v192, v232\n"
    "v_exp_f16_e64 v234, v232\n"
    "ds_read_b128 a[240:243], v243 offset:4096\n"
    "v_exp_f16_sdwa v234, v232 dst_sel:WORD_1 dst_unused:UNUSED_PRESERVE src0_sel:WORD_1\n"
    "s_waitcnt lgkmcnt(8)\n"
    "v_mfma_f32_16x16x32_bf16 v[168:171], a[80:83], a[224:227], 0\n"
    "s_nop 0\n"
    "ds_read_b128 a[244:247], v243 offset:5120\n"
    "ds_read_b128 a[252:255], v244 offset:32768\n"
    "v_mfma_f32_16x16x32_bf16 v[172:175], a[84:87], a[224:227], 0\n"
    "buffer_load_dwordx4 v[156:159], v245, s[40:43], s88 offen offset:64\n"
    "buffer_load_dwordx4 v[148:151], v245, s[40:43], s88 offen offset:0\n"
    "ds_read_u16 v193, v193\n"
    "s_waitcnt lgkmcnt(9)\n"
    "v_mfma_f32_16x16x32_bf16 v[168:171], a[128:131], a[228:231], v[168:171]\n"
    "v_or_b32_e32 v189, v189, v205\n"
    "v_lshlrev_b32_sdwa v205, 1, v179 dst_sel:DWORD dst_unused:UNUSED_PAD src0_sel:DWORD src1_sel:WORD_1\n"
    "v_mfma_f32_16x16x32_bf16 v[172:175], a[132:135], a[228:231], v[172:175]\n"
    "ds_read_u16_d16_hi v201, v201\n"
    "v_pk_fma_f16 v234, v234, s49, s49\n"
    "v_rcp_f16_e32 v232, v234\n"
    "s_waitcnt lgkmcnt(8)\n"
    "v_mfma_f32_16x16x32_bf16 v[168:171], a[48:51], a[232:235], v[168:171]\n"
    "ds_read_u16 v194, v194\n"
    "v_or_b32_e64 v190, v190, v206\n"
    "v_mfma_f32_16x16x32_bf16 v[96:99], a[192:195], v[184:187], v[96:99]\n"
    "ds_read_u16_d16_hi v202, v202\n"
    "v_lshlrev_b32_sdwa v206, 1, v181 dst_sel:DWORD dst_unused:UNUSED_PAD src0_sel:DWORD src1_sel:WORD_1\n"
    "v_mfma_f32_16x16x32_bf16 v[100:103], a[196:199], v[184:187], v[100:103]\n"
    "s_waitcnt lgkmcnt(8)\n"
    "v_rcp_f16_sdwa v232, v234 dst_sel:WORD_1 dst_unused:UNUSED_PRESERVE src0_sel:WORD_1\n"
    "v_mfma_f32_16x16x32_bf16 v[176:179], a[80:83], a[236:239], 0\n"
    "s_nop 0\n"
    "ds_read_b128 v[236:239], v244 offset:16384\n"
    "v_lshlrev_b32_sdwa v200, 1, v169 dst_sel:DWORD dst_unused:UNUSED_PAD src0_sel:DWORD src1_sel:WORD_1\n"
    "v_mfma_f32_16x16x32_bf16 v[112:115], a[200:203], v[184:187], v[112:115]\n"
    "ds_read_u16 v195, v195\n"
    "v_fma_mix_f32 v234, v192, v232, 0 op_sel_hi:[1,1,0]\n"
    "v_fma_mix_f32 v232, v192, v232, 0 op_sel:[1,1,0] op_sel_hi:[1,1,0]\n"
    "s_waitcnt lgkmcnt(8)\n"
    "v_mfma_f32_16x16x32_bf16 v[176:179], a[128:131], a[240:243], v[176:179]\n"
    "s_nop 0\n"
    "ds_read_u16_d16_hi v203, v203\n"
    "v_or_b32_e64 v191, v191, v207\n"
    "v_mfma_f32_16x16x32_bf16 v[116:119], a[204:207], v[184:187], v[116:119]\n"
    "2:\n"
    "s_waitcnt lgkmcnt(8)\n"
    "s_barrier\n"
    "v_mfma_f32_16x16x32_bf16 v[176:179], a[48:51], a[244:247], v[176:179]\n"
    "s_waitcnt lgkmcnt(6)\n"
    "ds_write_b128 v253, v[64:67] offset:2048\n"
    "v_mfma_f32_16x16x32_bf16 v[172:175], a[252:255], a[232:235], v[172:175]\n"
    "s_waitcnt lgkmcnt(6)\n"
    "v_lshlrev_b32_sdwa v207, 1, v183 dst_sel:DWORD dst_unused:UNUSED_PAD src0_sel:DWORD src1_sel:WORD_1\n"
    "v_or_b32_e64 v193, v193, v201\n"
    "v_mfma_f32_16x16x32_bf16 v[180:183], a[84:87], a[236:239], 0\n"
    "v_lshlrev_b32_sdwa v201, 1, v171 dst_sel:DWORD dst_unused:UNUSED_PAD src0_sel:DWORD src1_sel:WORD_1\n"
    "v_lshlrev_b32_sdwa v204, 1, v177 dst_sel:DWORD dst_unused:UNUSED_PAD src0_sel:DWORD src1_sel:WORD_1\n"
    "v_mfma_f32_16x16x32_bf16 v[180:183], a[132:135], a[240:243], v[180:183]\n"
    "ds_read_b128 v[164:167], v244 offset:18432\n"
    "ds_write_b128 v253, v[68:71] offset:5120\n"
    "v_mfma_f32_16x16x32_bf16 v[180:183], a[252:255], a[244:247], v[180:183]\n"
    "s_waitcnt lgkmcnt(5)\n"
    "v_or_b32_e32 v194, v194, v202\n"
    "v_lshlrev_b32_sdwa v202, 1, v173 dst_sel:DWORD dst_unused:UNUSED_PAD src0_sel:DWORD src1_sel:WORD_1\n"
    "v_mfma_f32_16x16x32_bf16 v[128:131], v[236:239], v[184:187], v[128:131]\n"
    "v_cvt_pk_bf16_f32 v192, v234, v232\n"
    "v_pk_fma_f16 v232, v196, v196, s48\n"
    "v_mfma_f32_16x16x32_bf16 v[104:107], a[192:195], v[188:191], v[104:107]\n"
    "ds_read_u16 v197, v197\n"
    "ds_write_b128 v253, v[88:91] offset:0\n"
    "v_mfma_f32_16x16x32_bf16 v[108:111], a[196:199], v[188:191], v[108:111]\n"
    "s_waitcnt lgkmcnt(4)\n"
    "buffer_store_dwordx4 v[208:211], v245, s[68:71], s59 offen offset:0 sc1\n"
    "v_mfma_f32_16x16x32_bf16 v[120:123], a[200:203], v[188:191], v[120:123]\n"
    "s_nop 0\n"
    "ds_read_u16_d16_hi v205, v205\n"
    "ds_write_b128 v253, v[92:95] offset:3072\n"
    "v_mfma_f32_16x16x32_bf16 v[124:127], a[204:207], v[188:191], v[124:127]\n"
    "s_waitcnt lgkmcnt(4)\n"
    "v_or_b32_e32 v195, v195, v203\n"
    "v_lshlrev_b32_sdwa v203, 1, v175 dst_sel:DWORD dst_unused:UNUSED_PAD src0_sel:DWORD src1_sel:WORD_1\n"
    "v_mfma_f32_16x16x32_bf16 v[132:135], v[164:167], v[184:187], v[132:135]\n"
    "v_lshlrev_b32_sdwa v184, 1, v168 dst_sel:DWORD dst_unused:UNUSED_PAD src0_sel:DWORD src1_sel:WORD_1\n"
    "v_lshlrev_b32_sdwa v185, 1, v170 dst_sel:DWORD dst_unused:UNUSED_PAD src0_sel:DWORD src1_sel:WORD_1\n"
    "v_mfma_f32_16x16x32_bf16 v[136:139], v[236:239], v[188:191], v[136:139]\n"
    "ds_read_u16 v198, v198\n"
    "ds_write_b128 v253, v[72:75] offset:1024\n"
    "v_mfma_f32_16x16x32_bf16 v[140:143], v[164:167], v[188:191], v[140:143]\n"
    "v_lshlrev_b32_sdwa v186, 1, v172 dst_sel:DWORD dst_unused:UNUSED_PAD src0_sel:DWORD src1_sel:WORD_1\n"
    "v_lshlrev_b32_sdwa v187, 1, v174 dst_sel:DWORD dst_unused:UNUSED_PAD src0_sel:DWORD src1_sel:WORD_1\n"
    "v_mfma_f32_16x16x32_bf16 v[168:171], a[88:91], a[224:227], 0\n"
    "v_lshlrev_b32_sdwa v188, 1, v176 dst_sel:DWORD dst_unused:UNUSED_PAD src0_sel:DWORD src1_sel:WORD_1\n"
    "v_lshlrev_b32_sdwa v189, 1, v178 dst_sel:DWORD dst_unused:UNUSED_PAD src0_sel:DWORD src1_sel:WORD_1\n"
    "v_mfma_f32_16x16x32_bf16 v[168:171], a[136:139], a[228:231], v[168:171]\n"
    "ds_read_u16_d16_hi v206, v206\n"
    "ds_write_b128 v253, v[76:79] offset:4096\n"
    "v_mfma_f32_16x16x32_bf16 v[168:171], a[52:55], a[232:235], v[168:171]\n"
    "s_waitcnt lgkmcnt(4)\n"
    "v_or_b32_e32 v197, v197, v205\n"
    "v_lshlrev_b32_sdwa v205, 1, v179 dst_sel:DWORD dst_unused:UNUSED_PAD src0_sel:DWORD src1_sel:WORD_1\n"
    "v_mfma_f32_16x16x32_bf16 v[172:175], a[92:95], a[224:227], 0\n"
    "v_lshlrev_b32_sdwa v190, 1, v180 dst_sel:DWORD dst_unused:UNUSED_PAD src0_sel:DWORD src1_sel:WORD_1\n"
    "v_lshlrev_b32_sdwa v191, 1, v182 dst_sel:DWORD dst_unused:UNUSED_PAD src0_sel:DWORD src1_sel:WORD_1\n"
    "v_mfma_f32_16x16x32_bf16 v[172:175], a[140:143], a[228:231], v[172:175]\n"
    "ds_read_u16 v199, v199\n"
    "ds_read_u16_d16_hi v207, v207\n"
    "v_mfma_f32_16x16x32_bf16 v[176:179], a[88:91], a[236:239], 0\n"
    "ds_read_b128 a[252:255], v244 offset:36864\n"
    "ds_read_u16 v184, v184\n"
    "v_mfma_f32_16x16x32_bf16 v[176:179], a[136:139], a[240:243], v[176:179]\n"
    "v_pk_mul_f16 v232, v196, v232\n"
    "v_exp_f16_e64 v234, v232\n"
    "v_mfma_f32_16x16x32_bf16 v[176:179], a[52:55], a[244:247], v[176:179]\n"
    "ds_read_u16_d16_hi v200, v200\n"
    "v_exp_f16_sdwa v234, v232 dst_sel:WORD_1 dst_unused:UNUSED_PRESERVE src0_sel:WORD_1\n"
    "v_mfma_f32_16x16x32_bf16 v[96:99], a[208:211], v[192:195], v[96:99]\n"
    "ds_read_u16 v185, v185\n"
    "v_pk_fma_f16 v234, v234, s49, s49\n"
    "v_mfma_f32_16x16x32_bf16 v[100:103], a[212:215], v[192:195], v[100:103]\n"
    "v_rcp_f16_e32 v232, v234\n"
    "v_mfma_f32_16x16x32_bf16 v[112:115], a[216:219], v[192:195], v[112:115]\n"
    "s_waitcnt lgkmcnt(6)\n"
    "ds_read_u16_d16_hi v201, v201\n"
    "buffer_store_dwordx4 v[212:215], v245, s[68:71], s59 offen offset:64 sc1\n"
    "v_or_b32_e64 v198, v198, v206\n"
    "v_lshlrev_b32_sdwa v206, 1, v181 dst_sel:DWORD dst_unused:UNUSED_PAD src0_sel:DWORD src1_sel:WORD_1\n"
    "v_rcp_f16_sdwa v232, v234 dst_sel:WORD_1 dst_unused:UNUSED_PRESERVE src0_sel:WORD_1\n"
    "v_mfma_f32_16x16x32_bf16 v[116:119], a[220:223], v[192:195], v[116:119]\n"
    "ds_read_u16 v186, v186\n"
    "ds_read_b128 v[236:239], v244 offset:20480\n"
    "v_fma_mix_f32 v234, v196, v232, 0 op_sel_hi:[1,1,0]\n"
    "v_fma_mix_f32 v232, v196, v232, 0 op_sel:[1,1,0] op_sel_hi:[1,1,0]\n"
    "v_cvt_pk_bf16_f32 v96, v96, v97\n"
    "v_cvt_pk_bf16_f32 v196, v234, v232\n"
    "v_cvt_pk_bf16_f32 v97, v98, v99\n"
    "v_cvt_pk_bf16_f32 v98, v100, v101\n"
    "s_waitcnt lgkmcnt(6)\n"
    "v_mfma_f32_16x16x32_bf16 v[172:175], a[252:255], a[232:235], v[172:175]\n"
    "s_nop 0\n"
    "ds_read_u16_d16_hi v202, v202\n"
    "ds_read_u16 v187, v187\n"
    "v_or_b32_e64 v199, v199, v207\n"
    "v_lshlrev_b32_sdwa v207, 1, v183 dst_sel:DWORD dst_unused:UNUSED_PAD src0_sel:DWORD src1_sel:WORD_1\n"
    "v_mfma_f32_16x16x32_bf16 v[180:183], a[92:95], a[236:239], 0\n"
    "ds_read_b128 v[164:167], v244 offset:22528\n"
    "v_cvt_pk_bf16_f32 v99, v102, v103\n"
    "v_mfma_f32_16x16x32_bf16 v[180:183], a[140:143], a[240:243], v[180:183]\n"
    "ds_read_u16_d16_hi v203, v203\n"
    "v_cvt_pk_bf16_f32 v112, v112, v113\n"
    "v_mfma_f32_16x16x32_bf16 v[180:183], a[252:255], a[244:247], v[180:183]\n"
    "v_cvt_pk_bf16_f32 v113, v114, v115\n"
    "v_cvt_pk_bf16_f32 v114, v116, v117\n"
    "v_mfma_f32_16x16x32_bf16 v[104:107], a[208:211], v[196:199], v[104:107]\n"
    "s_waitcnt lgkmcnt(8)\n"
    "ds_read_u16 v188, v188\n"
    "v_or_b32_e32 v184, v184, v200\n"
    "v_mfma_f32_16x16x32_bf16 v[108:111], a[212:215], v[196:199], v[108:111]\n"
    "v_cvt_pk_bf16_f32 v115, v118, v119\n"
    "v_mfma_f32_16x16x32_bf16 v[120:123], a[216:219], v[196:199], v[120:123]\n"
    "ds_read_u16_d16_hi v204, v204\n"
    "v_mfma_f32_16x16x32_bf16 v[124:127], a[220:223], v[196:199], v[124:127]\n"
    "s_waitcnt lgkmcnt(8)\n"
    "v_or_b32_e32 v185, v185, v201\n"
    "v_lshlrev_b32_sdwa v201, 1, v171 dst_sel:DWORD dst_unused:UNUSED_PAD src0_sel:DWORD src1_sel:WORD_1\n"
    "v_cvt_pk_bf16_f32 v104, v104, v105\n"
    "v_cvt_pk_bf16_f32 v105, v106, v107\n"
    "v_cvt_pk_bf16_f32 v106, v108, v109\n"
    "v_cvt_pk_bf16_f32 v107, v110, v111\n"
    "s_waitcnt lgkmcnt(6)\n"
    "v_mfma_f32_16x16x32_bf16 v[128:131], v[236:239], v[192:195], v[128:131]\n"
    "s_nop 0\n"
    "ds_read_u16 v189, v189\n"
    "ds_read_u16_d16_hi v205, v205\n"
    "v_mfma_f32_16x16x32_bf16 v[136:139], v[236:239], v[196:199], v[136:139]\n"
    "v_cvt_pk_bf16_f32 v120, v120, v121\n"
    "v_cvt_pk_bf16_f32 v121, v122, v123\n"
    "v_cvt_pk_bf16_f32 v122, v124, v125\n"
    "v_cvt_pk_bf16_f32 v123, v126, v127\n"
    "ds_read_u16 v190, v190\n"
    "s_waitcnt lgkmcnt(8)\n"
    "v_or_b32_e32 v186, v186, v202\n"
    "v_lshlrev_b32_sdwa v202, 1, v173 dst_sel:DWORD dst_unused:UNUSED_PAD src0_sel:DWORD src1_sel:WORD_1\n"
    "v_cvt_pk_bf16_f32 v128, v128, v129\n"
    "s_waitcnt lgkmcnt(6)\n"
    "v_mfma_f32_16x16x32_bf16 v[132:135], v[164:167], v[192:195], v[132:135]\n"
    "buffer_store_dwordx4 v[216:219], v245, s[68:71], s59 offen offset:128 sc1\n"
    "ds_write_b128 v242, v[96:99] offset:24640\n"
    "v_mfma_f32_16x16x32_bf16 v[140:143], v[164:167], v[196:199], v[140:143]\n"
    "s_waitcnt lgkmcnt(6)\n"
    "v_cvt_pk_f16_f32 v192, v168, v169\n"
    "v_lshlrev_b32_sdwa v193, 1, v170 dst_sel:DWORD dst_unused:UNUSED_PAD src0_sel:DWORD src1_sel:WORD_1\n"
    "v_mfma_f32_16x16x32_bf16 v[168:171], a[96:99], a[224:227], 0\n"
    "v_lshlrev_b32_sdwa v194, 1, v172 dst_sel:DWORD dst_unused:UNUSED_PAD src0_sel:DWORD src1_sel:WORD_1\n"
    "v_lshlrev_b32_sdwa v195, 1, v174 dst_sel:DWORD dst_unused:UNUSED_PAD src0_sel:DWORD src1_sel:WORD_1\n"
    "v_mfma_f32_16x16x32_bf16 v[168:171], a[144:147], a[228:231], v[168:171]\n"
    "s_nop 0\n"
    "ds_read_u16_d16_hi v206, v206\n"
    "ds_read_u16 v191, v191\n"
    "v_mfma_f32_16x16x32_bf16 v[168:171], a[56:59], a[232:235], v[168:171]\n"
    "s_waitcnt lgkmcnt(6)\n"
    "ds_read_u16_d16_hi v207, v207\n"
    "ds_read_u16 v193, v193\n"
    "v_cvt_pk_f16_f32 v196, v176, v177\n"
    "v_lshlrev_b32_sdwa v197, 1, v178 dst_sel:DWORD dst_unused:UNUSED_PAD src0_sel:DWORD src1_sel:WORD_1\n"
    "v_lshlrev_b32_sdwa v198, 1, v180 dst_sel:DWORD dst_unused:UNUSED_PAD src0_sel:DWORD src1_sel:WORD_1\n"
    "v_lshlrev_b32_sdwa v199, 1, v182 dst_sel:DWORD dst_unused:UNUSED_PAD src0_sel:DWORD src1_sel:WORD_1\n"
    "v_or_b32_e64 v187, v187, v203\n"
    "v_lshlrev_b32_sdwa v203, 1, v175 dst_sel:DWORD dst_unused:UNUSED_PAD src0_sel:DWORD src1_sel:WORD_1\n"
    "v_mfma_f32_16x16x32_bf16 v[172:175], a[100:103], a[224:227], 0\n"
    "s_waitcnt vmcnt(18)\n"
    "v_or_b32_e32 v188, v188, v204\n"
    "v_cvt_pk_bf16_f32 v129, v130, v131\n"
    "v_mfma_f32_16x16x32_bf16 v[172:175], a[148:151], a[228:231], v[172:175]\n"
    "s_waitcnt lgkmcnt(6)\n"
    "buffer_load_dwordx4 v[64:67], v246, s[40:43], s60 offen offset:128\n"
    "ds_write2_b32 v248, v0, v1 offset0:0 offset1:20\n"
    "v_mfma_f32_16x16x32_bf16 v[172:175], a[60:63], a[232:235], v[172:175]\n"
    "v_or_b32_e32 v189, v189, v205\n"
    "v_lshlrev_b32_sdwa v205, 1, v179 dst_sel:DWORD dst_unused:UNUSED_PAD src0_sel:DWORD src1_sel:WORD_1\n"
    "v_mfma_f32_16x16x32_bf16 v[176:179], a[96:99], a[236:239], 0\n"
    "ds_write2_b32 v248, v2, v3 offset0:40 offset1:60\n"
    "v_mfma_f32_16x16x32_bf16 v[176:179], a[144:147], a[240:243], v[176:179]\n"
    "v_add_u32_e32 v248, 1360, v248\n"
    "v_cvt_pk_bf16_f32 v130, v132, v133\n"
    "v_mfma_f32_16x16x32_bf16 v[176:179], a[56:59], a[244:247], v[176:179]\n"
    "s_waitcnt vmcnt(17)\n"
    "ds_read_u16_d16_hi v201, v201\n"
    "ds_write2_b32 v235, v20, v21 offset0:0 offset1:20\n"
    "s_waitcnt lgkmcnt(7)\n"
    "v_or_b32_e64 v190, v190, v206\n"
    "v_mfma_f32_16x16x32_bf16 v[96:99], a[0:3], v[184:187], 0\n"
    "v_lshlrev_b32_sdwa v206, 1, v181 dst_sel:DWORD dst_unused:UNUSED_PAD src0_sel:DWORD src1_sel:WORD_1\n"
    "v_cvt_pk_bf16_f32 v131, v134, v135\n"
    "v_mfma_f32_16x16x32_bf16 v[100:103], a[4:7], v[184:187], 0\n"
    "ds_read_u16 v194, v194\n"
    "ds_write2_b32 v235, v22, v23 offset0:40 offset1:60\n"
    "v_add_u32_e32 v235, 1360, v235\n"
    "s_waitcnt lgkmcnt(7)\n"
    "v_or_b32_e64 v191, v191, v207\n"
    "v_mfma_f32_16x16x32_bf16 v[116:119], a[12:15], v[184:187], 0\n"
    "s_waitcnt vmcnt(15)\n"
    "ds_read_u16_d16_hi v202, v202\n"
    "ds_write2_b32 v248, v4, v5 offset0:0 offset1:20\n"
    "v_mfma_f32_16x16x32_bf16 v[108:111], a[4:7], v[188:191], 0\n"
    "buffer_store_dwordx4 v[220:223], v245, s[68:71], s39 offen offset:0 sc1\n"
    "ds_write2_b32 v248, v6, v7 offset0:40 offset1:60\n"
    "v_add_u32_e32 v248, 1360, v248\n"
    "v_lshlrev_b32_sdwa v207, 1, v183 dst_sel:DWORD dst_unused:UNUSED_PAD src0_sel:DWORD src1_sel:WORD_1\n"
    "v_mfma_f32_16x16x32_bf16 v[180:183], a[100:103], a[236:239], 0\n"
    "s_waitcnt vmcnt(14)\n"
    "ds_read_b128 v[236:239], v244 offset:0\n"
    "ds_write2_b32 v235, v24, v25 offset0:0 offset1:20\n"
    "v_mfma_f32_16x16x32_bf16 v[180:183], a[148:151], a[240:243], v[180:183]\n"
    "s_waitcnt lgkmcnt(7)\n"
    "v_or_b32_e64 v193, v193, v201\n"
    "v_cvt_pk_bf16_f32 v136, v136, v137\n"
    "v_mfma_f32_16x16x32_bf16 v[180:183], a[60:63], a[244:247], v[180:183]\n"
    "ds_read_u16 v195, v195\n"
    "ds_write_b128 v242, v[112:115] offset:26688\n"
    "v_mfma_f32_16x16x32_bf16 v[112:115], a[8:11], v[184:187], 0\n"
    "v_pk_fma_f16 v232, v192, v192, s48\n"
    "v_cvt_pk_bf16_f32 v137, v138, v139\n"
    "v_mfma_f32_16x16x32_bf16 v[124:127], a[12:15], v[188:191], 0\n"
    "v_cvt_pk_bf16_f32 v138, v140, v141\n"
    "v_cvt_pk_bf16_f32 v139, v142, v143\n"
    "s_waitcnt lgkmcnt(4)\n"
    "v_or_b32_e32 v194, v194, v202\n"
    "buffer_load_dwordx4 v[68:71], v246, s[40:43], s61 offen offset:128\n"
    "ds_write2_b32 v235, v26, v27 offset0:40 offset1:60\n"
    "v_add_u32_e32 v235, -1360, v235\n"
    "v_pk_mul_f16 v232, v192, v232\n"
    "v_lshlrev_b32_sdwa v200, 1, v169 dst_sel:DWORD dst_unused:UNUSED_PAD src0_sel:DWORD src1_sel:WORD_1\n"
    "v_exp_f16_e32 v234, v232\n"
    "ds_write_b128 v242, v[128:131] offset:28736\n"
    "s_waitcnt lgkmcnt(4)\n"
    "v_mfma_f32_16x16x32_bf16 v[128:131], v[236:239], v[184:187], 0\n"
    "v_exp_f16_sdwa v234, v232 dst_sel:WORD_1 dst_unused:UNUSED_PRESERVE src0_sel:WORD_1\n"
    "v_lshlrev_b32_sdwa v201, 1, v171 dst_sel:DWORD dst_unused:UNUSED_PAD src0_sel:DWORD src1_sel:WORD_1\n"
    "ds_write_b128 v242, v[104:107] offset:25664\n"
    "v_mfma_f32_16x16x32_bf16 v[104:107], a[0:3], v[188:191], 0\n"
    "v_pk_fma_f16 v234, v234, s49, s49\n"
    "v_lshlrev_b32_sdwa v202, 1, v173 dst_sel:DWORD dst_unused:UNUSED_PAD src0_sel:DWORD src1_sel:WORD_1\n"
    "v_rcp_f16_e64 v232, v234\n"
    "ds_write2_b32 v248, v8, v9 offset0:0 offset1:20\n"
    "v_rcp_f16_sdwa v232, v234 dst_sel:WORD_1 dst_unused:UNUSED_PRESERVE src0_sel:WORD_1\n"
    "buffer_store_dwordx4 v[224:227], v245, s[68:71], s39 offen offset:64 sc1\n"
    "ds_write_b128 v242, v[120:123] offset:27712\n"
    "v_mfma_f32_16x16x32_bf16 v[120:123], a[8:11], v[188:191], 0\n"
    "v_lshlrev_b32_sdwa v204, 1, v177 dst_sel:DWORD dst_unused:UNUSED_PAD src0_sel:DWORD src1_sel:WORD_1\n"
    "v_fma_mix_f32 v234, v192, v232, 0 op_sel_hi:[1,1,0]\n"
    "v_fma_mix_f32 v232, v192, v232, 0 op_sel:[1,1,0] op_sel_hi:[1,1,0]\n"
    "v_cvt_pk_bf16_f32 v192, v234, v232\n"
    "v_pk_fma_f16 v232, v196, v196, s48\n"
    "v_pk_mul_f16 v232, v196, v232\n"
    "ds_write2_b32 v248, v10, v11 offset0:40 offset1:60\n"
    "v_add_u32_e32 v248, 1360, v248\n"
    "v_exp_f16_e32 v234, v232\n"
    "ds_write_b128 v242, v[136:139] offset:29760\n"
    "v_mfma_f32_16x16x32_bf16 v[136:139], v[236:239], v[188:191], 0\n"
    "s_waitcnt vmcnt(14)\n"
    "v_exp_f16_sdwa v234, v232 dst_sel:WORD_1 dst_unused:UNUSED_PRESERVE src0_sel:WORD_1\n"
    "buffer_load_dwordx4 v[72:75], v246, s[40:43], s62 offen offset:128\n"
    "ds_write2_b32 v248, v12, v13 offset0:0 offset1:20\n"
    "v_pk_fma_f16 v234, v234, s49, s49\n"
    "v_rcp_f16_e64 v232, v234\n"
    "ds_read_u16_d16_hi v203, v203\n"
    "ds_write2_b32 v248, v14, v15 offset0:40 offset1:60\n"
    "v_add_u32_e32 v248, 1360, v248\n"
    "v_rcp_f16_sdwa v232, v234 dst_sel:WORD_1 dst_unused:UNUSED_PRESERVE src0_sel:WORD_1\n"
    "ds_read_b128 v[164:167], v244 offset:2048\n"
    "ds_read_u16 v197, v197\n"
    "s_nop 0\n"
    "s_waitcnt vmcnt(13)\n"
    "ds_write2_b32 v248, v16, v17 offset0:0 offset1:20\n"
    "v_fma_mix_f32 v234, v196, v232, 0 op_sel_hi:[1,1,0]\n"
    "v_fma_mix_f32 v232, v196, v232, 0 op_sel:[1,1,0] op_sel_hi:[1,1,0]\n"
    "v_cvt_pk_bf16_f32 v196, v234, v232\n"
    "ds_read_u16_d16_hi v205, v205\n"
    "ds_write2_b32 v248, v18, v19 offset0:40 offset1:60\n"
    "v_mfma_f32_32x32x1_2b_f32 v[0:31], v254, v254, 0\n"
    "v_add_u32_e32 v248, -5440, v248\n"
    "ds_read_u16 v198, v198\n"
    "buffer_store_dwordx4 v[228:231], v245, s[68:71], s39 offen offset:128 sc1\n"
    "ds_read_u16_d16_hi v206, v206\n"
    "ds_read_u16 v199, v199\n"
    "s_waitcnt lgkmcnt(8)\n"
    "v_or_b32_e32 v195, v195, v203\n"
    "s_waitcnt lgkmcnt(7)\n"
    "v_mfma_f32_16x16x32_bf16 v[132:135], v[164:167], v[184:187], 0\n"
    "s_nop 0\n"
    "ds_read_u16_d16_hi v207, v207\n"
    "v_lshlrev_b32_sdwa v184, 1, v168 dst_sel:DWORD dst_unused:UNUSED_PAD src0_sel:DWORD src1_sel:WORD_1\n"
    "v_mfma_f32_16x16x32_bf16 v[140:143], v[164:167], v[188:191], 0\n"
    "ds_read_b128 v[216:219], v251 offset:0\n"
    "v_lshlrev_b32_sdwa v185, 1, v170 dst_sel:DWORD dst_unused:UNUSED_PAD src0_sel:DWORD src1_sel:WORD_1\n"
    "v_mfma_f32_16x16x32_bf16 v[168:171], a[104:107], a[224:227], 0\n"
    "ds_read_b128 v[220:223], v251 offset:336\n"
    "v_lshlrev_b32_sdwa v186, 1, v172 dst_sel:DWORD dst_unused:UNUSED_PAD src0_sel:DWORD src1_sel:WORD_1\n"
    "v_mfma_f32_16x16x32_bf16 v[168:171], a[152:155], a[228:231], v[168:171]\n"
    "ds_read_u16 v184, v184\n"
    "v_lshlrev_b32_sdwa v187, 1, v174 dst_sel:DWORD dst_unused:UNUSED_PAD src0_sel:DWORD src1_sel:WORD_1\n"
    "v_mfma_f32_16x16x32_bf16 v[168:171], a[64:67], a[232:235], v[168:171]\n"
    "s_waitcnt lgkmcnt(7)\n"
    "s_barrier\n"
    "v_mfma_f32_16x16x32_bf16 v[96:99], a[16:19], v[192:195], v[96:99]\n"
    "buffer_load_dwordx4 v[76:79], v246, s[40:43], s63 offen offset:128\n"
    "ds_read_b128 v[208:211], v250 offset:0\n"
    "v_mfma_f32_16x16x32_bf16 v[100:103], a[20:23], v[192:195], v[100:103]\n"
    "ds_read_b128 v[212:215], v250 offset:320\n"
    "ds_read_u16_d16_hi v200, v200\n"
    "v_mfma_f32_16x16x32_bf16 v[112:115], a[24:27], v[192:195], v[112:115]\n"
    "s_waitcnt lgkmcnt(8)\n"
    "ds_read_u16 v185, v185\n"
    "ds_read_u16_d16_hi v201, v201\n"
    "v_mfma_f32_16x16x32_bf16 v[116:119], a[28:31], v[192:195], v[116:119]\n"
    "v_or_b32_e32 v197, v197, v205\n"
    "v_lshlrev_b32_sdwa v203, 1, v175 dst_sel:DWORD dst_unused:UNUSED_PAD src0_sel:DWORD src1_sel:WORD_1\n"
    "v_mfma_f32_16x16x32_bf16 v[172:175], a[108:111], a[224:227], 0\n"
    "ds_read_u16 v186, v186\n"
    "v_or_b32_e32 v198, v198, v206\n"
    "v_mfma_f32_16x16x32_bf16 v[172:175], a[156:159], a[228:231], v[172:175]\n"
    "s_waitcnt lgkmcnt(8)\n"
    "v_lshlrev_b32_sdwa v188, 1, v176 dst_sel:DWORD dst_unused:UNUSED_PAD src0_sel:DWORD src1_sel:WORD_1\n"
    "v_lshlrev_b32_sdwa v189, 1, v178 dst_sel:DWORD dst_unused:UNUSED_PAD src0_sel:DWORD src1_sel:WORD_1\n"
    "v_mfma_f32_16x16x32_bf16 v[172:175], a[68:71], a[232:235], v[172:175]\n"
    "ds_read_b128 v[236:239], v244 offset:4096\n"
    "v_lshlrev_b32_sdwa v205, 1, v179 dst_sel:DWORD dst_unused:UNUSED_PAD src0_sel:DWORD src1_sel:WORD_1\n"
    "v_mfma_f32_16x16x32_bf16 v[176:179], a[104:107], a[236:239], 0\n"
    "ds_read_u16_d16_hi v202, v202\n"
    "v_lshlrev_b32_sdwa v190, 1, v180 dst_sel:DWORD dst_unused:UNUSED_PAD src0_sel:DWORD src1_sel:WORD_1\n"
    "v_mfma_f32_16x16x32_bf16 v[176:179], a[152:155], a[240:243], v[176:179]\n"
    "v_lshlrev_b32_sdwa v206, 1, v181 dst_sel:DWORD dst_unused:UNUSED_PAD src0_sel:DWORD src1_sel:WORD_1\n"
    "v_lshlrev_b32_sdwa v191, 1, v182 dst_sel:DWORD dst_unused:UNUSED_PAD src0_sel:DWORD src1_sel:WORD_1\n"
    "v_mfma_f32_16x16x32_bf16 v[176:179], a[64:67], a[244:247], v[176:179]\n"
    "ds_read_u16 v187, v187\n"
    "v_or_b32_e32 v199, v199, v207\n"
    "s_waitcnt lgkmcnt(7)\n"
    "v_smfmac_f32_16x16x64_bf16 v[0:3], v[216:219], v[208:215], v240\n"
    "ds_read_b128 v[216:219], v252 offset:0\n"
    "v_smfmac_f32_16x16x64_bf16 v[4:7], v[220:223], v[208:215], v240 abid:1\n"
    "ds_read_b128 v[208:211], v250 offset:640\n"
    "v_mfma_f32_16x16x32_bf16 v[104:107], a[16:19], v[196:199], v[104:107]\n"
    "ds_read_b128 v[212:215], v250 offset:960\n"
    "v_lshlrev_b32_sdwa v207, 1, v183 dst_sel:DWORD dst_unused:UNUSED_PAD src0_sel:DWORD src1_sel:WORD_1\n"
    "v_mfma_f32_16x16x32_bf16 v[180:183], a[108:111], a[236:239], 0\n"
    "s_waitcnt lgkmcnt(9)\n"
    "ds_read_b128 v[220:223], v252 offset:336\n"
    "v_or_b32_e32 v184, v184, v200\n"
    "v_mfma_f32_16x16x32_bf16 v[180:183], a[156:159], a[240:243], v[180:183]\n"
    "ds_read_b128 v[164:167], v244 offset:6144\n"
    "v_mfma_f32_16x16x32_bf16 v[180:183], a[68:71], a[244:247], v[180:183]\n"
    "ds_read_u16_d16_hi v203, v203\n"
    "v_mfma_f32_16x16x32_bf16 v[108:111], a[20:23], v[196:199], v[108:111]\n"
    "s_waitcnt lgkmcnt(10)\n"
    "v_or_b32_e32 v185, v185, v201\n"
    "v_lshlrev_b32_sdwa v201, 1, v171 dst_sel:DWORD dst_unused:UNUSED_PAD src0_sel:DWORD src1_sel:WORD_1\n"
    "v_mfma_f32_16x16x32_bf16 v[120:123], a[24:27], v[196:199], v[120:123]\n"
    "s_waitcnt vmcnt(10)\n"
    "ds_read_u16 v188, v188\n"
    "v_mfma_f32_16x16x32_bf16 v[124:127], a[28:31], v[196:199], v[124:127]\n"
    "s_waitcnt lgkmcnt(9)\n"
    "v_mfma_f32_16x16x32_bf16 v[128:131], v[236:239], v[192:195], v[128:131]\n"
    "ds_read_u16_d16_hi v204, v204\n"
    "v_mfma_f32_16x16x32_bf16 v[136:139], v[236:239], v[196:199], v[136:139]\n"
    "buffer_load_dwordx4 v[80:83], v246, s[40:43], s52 offen offset:128\n"
    "ds_read_u16 v189, v189\n"
    "v_smfmac_f32_32x32x32_bf16 v[112:127], a[248:251], v[152:159], v241\n"
    "s_waitcnt lgkmcnt(10)\n"
    "v_or_b32_e32 v186, v186, v202\n"
    "v_lshlrev_b32_sdwa v202, 1, v173 dst_sel:DWORD dst_unused:UNUSED_PAD src0_sel:DWORD src1_sel:WORD_1\n"
    "ds_read_u16_d16_hi v205, v205\n"
    "v_smfmac_f32_32x32x32_bf16 v[96:111], a[248:251], v[144:151], v241\n"
    "ds_read_u16 v190, v190\n"
    "ds_read_u16_d16_hi v206, v206\n"
    "s_waitcnt lgkmcnt(9)\n"
    "v_smfmac_f32_16x16x64_bf16 v[0:3], v[216:219], v[208:215], v240\n"
    "s_waitcnt lgkmcnt(8)\n"
    "v_smfmac_f32_16x16x64_bf16 v[4:7], v[220:223], v[208:215], v240 abid:1\n"
    "s_waitcnt lgkmcnt(7)\n"
    "v_mfma_f32_16x16x32_bf16 v[132:135], v[164:167], v[192:195], v[132:135]\n"
    "s_nop 0\n"
    "ds_read_b128 v[208:211], v250 offset:80\n"
    "v_cvt_pk_f16_f32 v192, v168, v169\n"
    "v_mfma_f32_16x16x32_bf16 v[140:143], v[164:167], v[196:199], v[140:143]\n"
    "ds_read_b128 v[212:215], v250 offset:400\n"
    "v_lshlrev_b32_sdwa v193, 1, v170 dst_sel:DWORD dst_unused:UNUSED_PAD src0_sel:DWORD src1_sel:WORD_1\n"
    "v_lshlrev_b32_sdwa v194, 1, v172 dst_sel:DWORD dst_unused:UNUSED_PAD src0_sel:DWORD src1_sel:WORD_1\n"
    "v_lshlrev_b32_sdwa v195, 1, v174 dst_sel:DWORD dst_unused:UNUSED_PAD src0_sel:DWORD src1_sel:WORD_1\n"
    "v_cvt_pk_f16_f32 v196, v176, v177\n"
    "v_pk_mov_b32 v[224:225], v[0:1], v[0:1] op_sel:[0,1]\n"
    "v_pk_mov_b32 v[226:227], v[2:3], v[2:3] op_sel:[0,1]\n"
    "v_pk_mul_f32 v[228:229], v[0:1], v[0:1]\n"
    "v_pk_mul_f32 v[230:231], v[2:3], v[2:3]\n"
    "v_pk_add_f32 v[224:225], v[224:225], v[4:5]\n"
    "v_pk_add_f32 v[226:227], v[226:227], v[6:7]\n"
    "v_pk_fma_f32 v[228:229], v[4:5], v[4:5], v[228:229]\n"
    "v_pk_fma_f32 v[230:231], v[6:7], v[6:7], v[230:231]\n"
    "v_mfma_f32_16x16x32_bf16 v[168:171], a[112:115], a[224:227], 0\n"
    "s_waitcnt lgkmcnt(7)\n"
    "ds_read_b128 v[216:219], v251 offset:672\n"
    "ds_read_b128 v[220:223], v251 offset:1008\n"
    "v_mfma_f32_16x16x32_bf16 v[168:171], a[160:163], a[228:231], v[168:171]\n"
    "s_waitcnt lgkmcnt(8)\n"
    "ds_read_u16 v191, v191\n"
    "ds_read_u16_d16_hi v207, v207\n"
    "v_mfma_f32_16x16x32_bf16 v[168:171], a[72:75], a[232:235], v[168:171]\n"
    "buffer_load_dwordx4 v[144:147], v245, s[40:43], s57 offen offset:128\n"
    "v_lshlrev_b32_sdwa v197, 1, v178 dst_sel:DWORD dst_unused:UNUSED_PAD src0_sel:DWORD src1_sel:WORD_1\n"
    "v_lshlrev_b32_sdwa v198, 1, v180 dst_sel:DWORD dst_unused:UNUSED_PAD src0_sel:DWORD src1_sel:WORD_1\n"
    "v_lshlrev_b32_sdwa v199, 1, v182 dst_sel:DWORD dst_unused:UNUSED_PAD src0_sel:DWORD src1_sel:WORD_1\n"
    "v_or_b32_e32 v187, v187, v203\n"
    "v_lshlrev_b32_sdwa v203, 1, v175 dst_sel:DWORD dst_unused:UNUSED_PAD src0_sel:DWORD src1_sel:WORD_1\n"
    "v_mfma_f32_16x16x32_bf16 v[172:175], a[116:119], a[224:227], 0\n"
    "s_waitcnt lgkmcnt(8)\n"
    "ds_read_b128 a[252:255], v244 offset:34816\n"
    "ds_read_u16 v193, v193\n"
    "v_mfma_f32_16x16x32_bf16 v[172:175], a[164:167], a[228:231], v[172:175]\n"
    "v_or_b32_e64 v188, v188, v204\n"
    "v_or_b32_e64 v189, v189, v205\n"
    "v_lshlrev_b32_sdwa v205, 1, v179 dst_sel:DWORD dst_unused:UNUSED_PAD src0_sel:DWORD src1_sel:WORD_1\n"
    "v_mfma_f32_16x16x32_bf16 v[176:179], a[112:115], a[236:239], 0\n"
    "ds_read_u16_d16_hi v201, v201\n"
    "v_lshlrev_b32_e64 v254, 3, v244\n"
    "v_mfma_f32_16x16x32_bf16 v[176:179], a[160:163], a[240:243], v[176:179]\n"
    "ds_read_u16 v194, v194\n"
    "v_pk_fma_f16 v232, v192, v192, s48\n"
    "v_mfma_f32_16x16x32_bf16 v[176:179], a[72:75], a[244:247], v[176:179]\n"
    "s_waitcnt lgkmcnt(8)\n"
    "v_or_b32_e32 v190, v190, v206\n"
    "v_lshlrev_b32_sdwa v206, 1, v181 dst_sel:DWORD dst_unused:UNUSED_PAD src0_sel:DWORD src1_sel:WORD_1\n"
    "v_mfma_f32_16x16x32_bf16 v[112:115], a[40:43], v[184:187], v[112:115]\n"
    "s_waitcnt lgkmcnt(7)\n"
    "ds_read_u16_d16_hi v202, v202\n"
    "v_pk_mul_f16 v232, v192, v232\n"
    "v_smfmac_f32_16x16x64_bf16 v[8:11], v[216:219], v[208:215], v240\n"
    "s_waitcnt lgkmcnt(7)\n"
    "v_exp_f16_e64 v234, v232\n"
    "v_smfmac_f32_16x16x64_bf16 v[12:15], v[220:223], v[208:215], v240 abid:1\n"
    "buffer_load_dwordx4 v[84:87], v247, s[40:43], s60 offen offset:128\n"
    "ds_read_b128 v[208:211], v250 offset:720\n"
    "ds_read_b128 v[212:215], v250 offset:1040\n"
    "v_exp_f16_sdwa v234, v232 dst_sel:WORD_1 dst_unused:UNUSED_PRESERVE src0_sel:WORD_1\n"
    "ds_read_b128 v[216:219], v252 offset:672\n"
    "ds_read_b128 v[220:223], v252 offset:1008\n"
    "v_pk_fma_f16 v234, v234, s49, s49\n"
    "v_lshlrev_b32_sdwa v200, 1, v169 dst_sel:DWORD dst_unused:UNUSED_PAD src0_sel:DWORD src1_sel:WORD_1\n"
    "v_rcp_f16_e32 v232, v234\n"
    "s_waitcnt lgkmcnt(8)\n"
    "v_mfma_f32_16x16x32_bf16 v[172:175], a[252:255], a[232:235], v[172:175]\n"
    "ds_read_b128 v[236:239], v244 offset:8192\n"
    "ds_read_b128 v[152:155], v243 offset:24640\n"
    "v_or_b32_e64 v191, v191, v207\n"
    "v_mfma_f32_16x16x32_bf16 v[116:119], a[44:47], v[184:187], v[116:119]\n"
    "ds_read_b128 v[156:159], v243 offset:25664\n"
    "v_lshlrev_b32_sdwa v207, 1, v183 dst_sel:DWORD dst_unused:UNUSED_PAD src0_sel:DWORD src1_sel:WORD_1\n"
    "v_mfma_f32_16x16x32_bf16 v[180:183], a[116:119], a[236:239], 0\n"
    "ds_read_u16 v195, v195\n"
    "v_rcp_f16_sdwa v232, v234 dst_sel:WORD_1 dst_unused:UNUSED_PRESERVE src0_sel:WORD_1\n"
    "v_mfma_f32_16x16x32_bf16 v[180:183], a[164:167], a[240:243], v[180:183]\n"
    "v_lshlrev_b32_sdwa v204, 1, v177 dst_sel:DWORD dst_unused:UNUSED_PAD src0_sel:DWORD src1_sel:WORD_1\n"
    "v_fma_mix_f32 v234, v192, v232, 0 op_sel_hi:[1,1,0]\n"
    "v_mfma_f32_16x16x32_bf16 v[180:183], a[252:255], a[244:247], v[180:183]\n"
    "s_waitcnt lgkmcnt(10)\n"
    "ds_read_u16_d16_hi v203, v203\n"
    "v_or_b32_e32 v193, v193, v201\n"
    "v_mfma_f32_16x16x32_bf16 v[120:123], a[40:43], v[188:191], v[120:123]\n"
    "s_mov_b32 exec_hi, 0\n"
    "buffer_load_dword v233, v254, s[80:83], s84 offen sc1\n"
    "s_mov_b32 exec_hi, -1\n"
    "v_mfma_f32_16x16x32_bf16 v[124:127], a[44:47], v[188:191], v[124:127]\n"
    "ds_read_b128 v[164:167], v244 offset:10240\n"
    "ds_read_u16 v197, v197\n"
    "v_lshlrev_b32_sdwa v201, 1, v171 dst_sel:DWORD dst_unused:UNUSED_PAD src0_sel:DWORD src1_sel:WORD_1\n"
    "s_waitcnt lgkmcnt(10)\n"
    "v_or_b32_e32 v194, v194, v202\n"
    "v_lshlrev_b32_sdwa v202, 1, v173 dst_sel:DWORD dst_unused:UNUSED_PAD src0_sel:DWORD src1_sel:WORD_1\n"
    "v_fma_mix_f32 v232, v192, v232, 0 op_sel:[1,1,0] op_sel_hi:[1,1,0]\n"
    "ds_read_u16_d16_hi v205, v205\n"
    "s_waitcnt lgkmcnt(9)\n"
    "v_smfmac_f32_16x16x64_bf16 v[8:11], v[216:219], v[208:215], v240\n"
    "s_waitcnt lgkmcnt(8)\n"
    "ds_read_b128 v[216:219], v251 offset:1344\n"
    "v_smfmac_f32_16x16x64_bf16 v[12:15], v[220:223], v[208:215], v240 abid:1\n"
    "s_waitcnt lgkmcnt(8)\n"
    "ds_read_b128 v[208:211], v250 offset:160\n"
    "v_mfma_f32_16x16x32_bf16 v[128:131], v[236:239], v[184:187], v[128:131]\n"
    "s_nop 0\n"
    "v_cvt_pk_bf16_f32 v192, v234, v232\n"
    "v_pk_fma_f16 v232, v196, v196, s48\n"
    "ds_read_b128 v[212:215], v250 offset:480\n"
    "ds_read_b128 v[220:223], v251 offset:1680\n"
    "v_pk_add_f32 v[224:225], v[224:225], v[8:9]\n"
    "v_pk_add_f32 v[226:227], v[226:227], v[10:11]\n"
    "v_pk_fma_f32 v[228:229], v[8:9], v[8:9], v[228:229]\n"
    "v_pk_fma_f32 v[230:231], v[10:11], v[10:11], v[230:231]\n"
    "v_pk_add_f32 v[224:225], v[224:225], v[12:13]\n"
    "v_pk_add_f32 v[226:227], v[226:227], v[14:15]\n"
    "v_pk_fma_f32 v[228:229], v[12:13], v[12:13], v[228:229]\n"
    "v_pk_fma_f32 v[230:231], v[14:15], v[14:15], v[230:231]\n"
    "v_mfma_f32_16x16x32_bf16 v[136:139], v[236:239], v[188:191], v[136:139]\n"
    "s_waitcnt lgkmcnt(9)\n"
    "buffer_load_dwordx4 v[88:91], v247, s[40:43], s61 offen offset:128\n"
    "ds_read_u16 v198, v198\n"
    "v_smfmac_f32_32x32x32_bf16 v[96:111], a[248:251], v[152:159], v241\n"
    "s_nop 0\n"
    "ds_read_u16_d16_hi v206, v206\n"
    "v_pk_mul_f16 v232, v196, v232\n"
    "ds_read_u16 v199, v199\n"
    "s_waitcnt lgkmcnt(9)\n"
    "v_mfma_f32_16x16x32_bf16 v[132:135], v[164:167], v[184:187], v[132:135]\n"
    "v_or_b32_e32 v195, v195, v203\n"
    "v_lshlrev_b32_sdwa v203, 1, v175 dst_sel:DWORD dst_unused:UNUSED_PAD src0_sel:DWORD src1_sel:WORD_1\n"
    "v_mfma_f32_16x16x32_bf16 v[140:143], v[164:167], v[188:191], v[140:143]\n"
    "ds_read_u16_d16_hi v207, v207\n"
    "v_exp_f16_e64 v234, v232\n"
    "v_mfma_f32_16x16x32_bf16 v[96:99], a[32:35], v[184:187], v[96:99]\n"
    "ds_read_b128 v[152:155], v243 offset:26688\n"
    "v_exp_f16_sdwa v234, v232 dst_sel:WORD_1 dst_unused:UNUSED_PRESERVE src0_sel:WORD_1\n"
    "v_mfma_f32_16x16x32_bf16 v[100:103], a[36:39], v[184:187], v[100:103]\n"
    "s_waitcnt lgkmcnt(8)\n"
    "ds_read_b128 v[156:159], v243 offset:27712\n"
    "v_or_b32_e32 v197, v197, v205\n"
    "v_mfma_f32_16x16x32_bf16 v[104:107], a[32:35], v[188:191], v[104:107]\n"
    "ds_read_b128 a[252:255], v244 offset:38912\n"
    "v_lshlrev_b32_sdwa v184, 1, v168 dst_sel:DWORD dst_unused:UNUSED_PAD src0_sel:DWORD src1_sel:WORD_1\n"
    "v_mfma_f32_16x16x32_bf16 v[108:111], a[36:39], v[188:191], v[108:111]\n"
    "s_waitcnt lgkmcnt(8)\n"
    "ds_read_u16 v184, v184\n"
    "v_lshlrev_b32_sdwa v185, 1, v170 dst_sel:DWORD dst_unused:UNUSED_PAD src0_sel:DWORD src1_sel:WORD_1\n"
    "v_smfmac_f32_16x16x64_bf16 v[16:19], v[216:219], v[208:215], v240\n"
    "s_waitcnt lgkmcnt(8)\n"
    "ds_read_b128 v[216:219], v252 offset:1344\n"
    "buffer_load_dwordx4 v[148:151], v245, s[40:43], s88 offen offset:128\n"
    "v_smfmac_f32_16x16x64_bf16 v[20:23], v[220:223], v[208:215], v240 abid:1\n"
    "ds_read_b128 v[208:211], v250 offset:800\n"
    "v_mfma_f32_16x16x32_bf16 v[168:171], a[120:123], a[224:227], 0\n"
    "ds_read_b128 v[212:215], v250 offset:1120\n"
    "v_lshlrev_b32_sdwa v186, 1, v172 dst_sel:DWORD dst_unused:UNUSED_PAD src0_sel:DWORD src1_sel:WORD_1\n"
    "v_mfma_f32_16x16x32_bf16 v[168:171], a[168:171], a[228:231], v[168:171]\n"
    "ds_read_b128 v[220:223], v252 offset:1680\n"
    "v_lshlrev_b32_sdwa v187, 1, v174 dst_sel:DWORD dst_unused:UNUSED_PAD src0_sel:DWORD src1_sel:WORD_1\n"
    "v_mfma_f32_16x16x32_bf16 v[168:171], a[76:79], a[232:235], v[168:171]\n"
    "s_waitcnt lgkmcnt(10)\n"
    "ds_read_u16_d16_hi v200, v200\n"
    "v_or_b32_e32 v198, v198, v206\n"
    "v_mfma_f32_16x16x32_bf16 v[172:175], a[124:127], a[224:227], 0\n"
    "v_lshlrev_b32_sdwa v188, 1, v176 dst_sel:DWORD dst_unused:UNUSED_PAD src0_sel:DWORD src1_sel:WORD_1\n"
    "v_lshlrev_b32_sdwa v189, 1, v178 dst_sel:DWORD dst_unused:UNUSED_PAD src0_sel:DWORD src1_sel:WORD_1\n"
    "v_mfma_f32_16x16x32_bf16 v[172:175], a[172:175], a[228:231], v[172:175]\n"
    "ds_read_u16 v185, v185\n"
    "v_lshlrev_b32_sdwa v205, 1, v179 dst_sel:DWORD dst_unused:UNUSED_PAD src0_sel:DWORD src1_sel:WORD_1\n"
    "v_mfma_f32_16x16x32_bf16 v[176:179], a[120:123], a[236:239], 0\n"
    "v_lshlrev_b32_sdwa v190, 1, v180 dst_sel:DWORD dst_unused:UNUSED_PAD src0_sel:DWORD src1_sel:WORD_1\n"
    "v_lshlrev_b32_sdwa v206, 1, v181 dst_sel:DWORD dst_unused:UNUSED_PAD src0_sel:DWORD src1_sel:WORD_1\n"
    "v_mfma_f32_16x16x32_bf16 v[176:179], a[168:171], a[240:243], v[176:179]\n"
    "s_waitcnt lgkmcnt(9)\n"
    "ds_read_u16_d16_hi v201, v201\n"
    "v_lshlrev_b32_sdwa v191, 1, v182 dst_sel:DWORD dst_unused:UNUSED_PAD src0_sel:DWORD src1_sel:WORD_1\n"
    "v_mfma_f32_16x16x32_bf16 v[176:179], a[76:79], a[244:247], v[176:179]\n"
    "s_waitcnt lgkmcnt(9)\n"
    "v_or_b32_e32 v199, v199, v207\n"
    "v_lshlrev_b32_sdwa v207, 1, v183 dst_sel:DWORD dst_unused:UNUSED_PAD src0_sel:DWORD src1_sel:WORD_1\n"
    "v_smfmac_f32_32x32x32_bf16 v[112:127], a[248:251], v[152:159], v241\n"
    "s_waitcnt lgkmcnt(8)\n"
    "ds_read_u16 v186, v186\n"
    "v_pk_fma_f16 v234, v234, s49, s49\n"
    "v_rcp_f16_e64 v232, v234\n"
    "ds_read_b128 v[236:239], v244 offset:12288\n"
    "ds_read_u16_d16_hi v202, v202\n"
    "v_mfma_f32_16x16x32_bf16 v[172:175], a[252:255], a[232:235], v[172:175]\n"
    "v_rcp_f16_sdwa v232, v234 dst_sel:WORD_1 dst_unused:UNUSED_PRESERVE src0_sel:WORD_1\n"
    "v_mfma_f32_16x16x32_bf16 v[180:183], a[124:127], a[236:239], 0\n"
    "ds_read_u16 v187, v187\n"
    "v_fma_mix_f32 v234, v196, v232, 0 op_sel_hi:[1,1,0]\n"
    "v_mfma_f32_16x16x32_bf16 v[180:183], a[172:175], a[240:243], v[180:183]\n"
    "s_waitcnt lgkmcnt(8)\n"
    "v_fma_mix_f32 v232, v196, v232, 0 op_sel:[1,1,0] op_sel_hi:[1,1,0]\n"
    "v_cvt_pk_bf16_f32 v196, v234, v232\n"
    "v_smfmac_f32_16x16x64_bf16 v[16:19], v[216:219], v[208:215], v240\n"
    "s_waitcnt lgkmcnt(7)\n"
    "ds_read_b128 v[216:219], v251 offset:2016\n"
    "v_smfmac_f32_16x16x64_bf16 v[20:23], v[220:223], v[208:215], v240 abid:1\n"
    "ds_read_b128 v[208:211], v250 offset:240\n"
    "v_mfma_f32_16x16x32_bf16 v[180:183], a[252:255], a[244:247], v[180:183]\n"
    "ds_read_b128 v[212:215], v250 offset:560\n"
    "v_mfma_f32_16x16x32_bf16 v[96:99], a[176:179], v[192:195], v[96:99]\n"
    "s_waitcnt lgkmcnt(9)\n"
    "ds_read_b128 v[220:223], v251 offset:2352\n"
    "v_or_b32_e32 v184, v184, v200\n"
    "v_mfma_f32_16x16x32_bf16 v[100:103], a[180:183], v[192:195], v[100:103]\n"
    "ds_read_b128 v[164:167], v244 offset:14336\n"
    "v_pk_add_f32 v[224:225], v[224:225], v[16:17]\n"
    "v_pk_add_f32 v[226:227], v[226:227], v[18:19]\n"
    "v_pk_fma_f32 v[228:229], v[16:17], v[16:17], v[228:229]\n"
    "v_pk_fma_f32 v[230:231], v[18:19], v[18:19], v[230:231]\n"
    "v_pk_add_f32 v[224:225], v[224:225], v[20:21]\n"
    "v_pk_add_f32 v[226:227], v[226:227], v[22:23]\n"
    "v_pk_fma_f32 v[228:229], v[20:21], v[20:21], v[228:229]\n"
    "v_pk_fma_f32 v[230:231], v[22:23], v[22:23], v[230:231]\n"
    "v_mfma_f32_16x16x32_bf16 v[112:115], a[184:187], v[192:195], v[112:115]\n"
    "s_waitcnt lgkmcnt(7)\n"
    "ds_read_u16_d16_hi v203, v203\n"
    "ds_read_u16 v188, v188\n"
    "v_mfma_f32_16x16x32_bf16 v[116:119], a[188:191], v[192:195], v[116:119]\n"
    "s_nop 0\n"
    "ds_read_u16_d16_hi v204, v204\n"
    "v_or_b32_e64 v185, v185, v201\n"
    "v_mfma_f32_16x16x32_bf16 v[128:131], v[236:239], v[192:195], v[128:131]\n"
    "v_lshlrev_b32_sdwa v201, 1, v171 dst_sel:DWORD dst_unused:UNUSED_PAD src0_sel:DWORD src1_sel:WORD_1\n"
    "ds_read_b128 v[152:155], v243 offset:28736\n"
    "v_mfma_f32_16x16x32_bf16 v[104:107], a[176:179], v[196:199], v[104:107]\n"
    "s_waitcnt lgkmcnt(10)\n"
    "ds_read_b128 v[156:159], v243 offset:29760\n"
    "v_lshlrev_b32_e32 v254, 3, v244\n"
    "v_mfma_f32_16x16x32_bf16 v[108:111], a[180:183], v[196:199], v[108:111]\n"
    "ds_read_u16 v189, v189\n"
    "v_or_b32_e32 v186, v186, v202\n"
    "v_mfma_f32_16x16x32_bf16 v[120:123], a[184:187], v[196:199], v[120:123]\n"
    "v_lshlrev_b32_sdwa v202, 1, v173 dst_sel:DWORD dst_unused:UNUSED_PAD src0_sel:DWORD src1_sel:WORD_1\n"
    "v_mfma_f32_16x16x32_bf16 v[124:127], a[188:191], v[196:199], v[124:127]\n"
    "s_waitcnt lgkmcnt(8)\n"
    "ds_read_u16_d16_hi v205, v205\n"
    "v_smfmac_f32_16x16x64_bf16 v[24:27], v[216:219], v[208:215], v240\n"
    "s_waitcnt lgkmcnt(8)\n"
    "ds_read_b128 v[216:219], v252 offset:2016\n"
    "v_smfmac_f32_16x16x64_bf16 v[28:31], v[220:223], v[208:215], v240 abid:1\n"
    "s_waitcnt lgkmcnt(8)\n"
    "ds_read_b128 v[208:211], v250 offset:880\n"
    "v_mfma_f32_16x16x32_bf16 v[132:135], v[164:167], v[192:195], v[132:135]\n"
    "v_cvt_pk_f16_f32 v192, v168, v169\n"
    "v_lshlrev_b32_sdwa v193, 1, v170 dst_sel:DWORD dst_unused:UNUSED_PAD src0_sel:DWORD src1_sel:WORD_1\n"
    "v_mfma_f32_16x16x32_bf16 v[136:139], v[236:239], v[196:199], v[136:139]\n"
    "s_nop 0\n"
    "ds_read_b128 v[212:215], v250 offset:1200\n"
    "ds_read_b128 v[220:223], v252 offset:2352\n"
    "v_mfma_f32_16x16x32_bf16 v[140:143], v[164:167], v[196:199], v[140:143]\n"
    "s_waitcnt lgkmcnt(10)\n"
    "v_lshlrev_b32_sdwa v194, 1, v172 dst_sel:DWORD dst_unused:UNUSED_PAD src0_sel:DWORD src1_sel:WORD_1\n"
    "v_lshlrev_b32_sdwa v195, 1, v174 dst_sel:DWORD dst_unused:UNUSED_PAD src0_sel:DWORD src1_sel:WORD_1\n"
    "v_cvt_pk_f16_f32 v196, v176, v177\n"
    "v_lshlrev_b32_sdwa v197, 1, v178 dst_sel:DWORD dst_unused:UNUSED_PAD src0_sel:DWORD src1_sel:WORD_1\n"
    "v_lshlrev_b32_sdwa v198, 1, v180 dst_sel:DWORD dst_unused:UNUSED_PAD src0_sel:DWORD src1_sel:WORD_1\n"
    "v_lshlrev_b32_sdwa v199, 1, v182 dst_sel:DWORD dst_unused:UNUSED_PAD src0_sel:DWORD src1_sel:WORD_1\n"
    "v_or_b32_e64 v187, v187, v203\n"
    "ds_read_b128 a[224:227], v242 offset:0\n"
    "ds_read_u16 v190, v190\n"
    "v_lshlrev_b32_sdwa v203, 1, v175 dst_sel:DWORD dst_unused:UNUSED_PAD src0_sel:DWORD src1_sel:WORD_1\n"
    "ds_read_b128 a[228:231], v242 offset:1024\n"
    "s_mov_b32 exec_hi, 0\n"
    "buffer_load_dword v233, v254, s[72:75], s53 offen sc1\n"
    "s_mov_b32 exec_hi, -1\n"
    "s_waitcnt lgkmcnt(9)\n"
    "v_smfmac_f32_32x32x32_bf16 v[128:143], a[248:251], v[152:159], v241\n"
    "s_nop 0\n"
    "ds_read_u16_d16_hi v206, v206\n"
    "ds_read_b128 a[232:235], v242 offset:2048\n"
    "v_or_b32_e64 v188, v188, v204\n"
    "v_pk_fma_f16 v232, v192, v192, s48\n"
    "v_mov_b32_e64 v254, 0\n"
    "ds_read_u16 v191, v191\n"
    "v_pk_mul_f16 v232, v192, v232\n"
    "v_exp_f16_e64 v234, v232\n"
    "ds_read_b128 a[236:239], v242 offset:3072\n"
    "s_waitcnt lgkmcnt(10)\n"
    "v_or_b32_e32 v189, v189, v205\n"
    "ds_read_u16_d16_hi v207, v207\n"
    "s_waitcnt lgkmcnt(9)\n"
    "s_barrier\n"
    "v_smfmac_f32_16x16x64_bf16 v[24:27], v[216:219], v[208:215], v240\n"
    "s_waitcnt lgkmcnt(7)\n"
    "ds_read_b128 a[240:243], v242 offset:4096\n"
    "ds_write2_b32 v248, v32, v33 offset0:0 offset1:20\n"
    "v_smfmac_f32_16x16x64_bf16 v[28:31], v[220:223], v[208:215], v240 abid:1\n"
    "v_lshlrev_b32_sdwa v205, 1, v179 dst_sel:DWORD dst_unused:UNUSED_PAD src0_sel:DWORD src1_sel:WORD_1\n"
    "v_mfma_f32_16x16x32_bf16 v[168:171], a[80:83], a[224:227], 0\n"
    "s_waitcnt lgkmcnt(7)\n"
    "ds_read_b128 a[244:247], v242 offset:5120\n"
    "ds_write2_b32 v248, v34, v35 offset0:40 offset1:60\n"
    "v_add_u32_e32 v248, 1360, v248\n"
    "v_pk_add_f32 v[224:225], v[224:225], v[24:25]\n"
    "v_pk_add_f32 v[226:227], v[226:227], v[26:27]\n"
    "v_pk_fma_f32 v[228:229], v[24:25], v[24:25], v[228:229]\n"
    "v_pk_fma_f32 v[230:231], v[26:27], v[26:27], v[230:231]\n"
    "v_pk_add_f32 v[224:225], v[224:225], v[28:29]\n"
    "v_pk_add_f32 v[226:227], v[226:227], v[30:31]\n"
    "v_pk_fma_f32 v[228:229], v[28:29], v[28:29], v[228:229]\n"
    "v_pk_fma_f32 v[230:231], v[30:31], v[30:31], v[230:231]\n"
    "v_mfma_f32_16x16x32_bf16 v[168:171], a[128:131], a[228:231], v[168:171]\n"
    "s_waitcnt lgkmcnt(5)\n"
    "ds_read_b128 a[252:255], v244 offset:32768\n"
    "ds_write2_b32 v235, v52, v53 offset0:0 offset1:20\n"
    "v_mfma_f32_16x16x32_bf16 v[168:171], a[48:51], a[232:235], v[168:171]\n"
    "v_or_b32_e32 v190, v190, v206\n"
    "v_lshlrev_b32_sdwa v206, 1, v181 dst_sel:DWORD dst_unused:UNUSED_PAD src0_sel:DWORD src1_sel:WORD_1\n"
    "v_mfma_f32_16x16x32_bf16 v[172:175], a[84:87], a[224:227], 0\n"
    "ds_read_u16 v193, v193\n"
    "ds_write2_b32 v235, v54, v55 offset0:40 offset1:60\n"
    "v_mfma_f32_16x16x32_bf16 v[172:175], a[132:135], a[228:231], v[172:175]\n"
    "s_waitcnt lgkmcnt(6)\n"
    "v_add_u32_e32 v235, 1360, v235\n"
    "v_or_b32_e32 v191, v191, v207\n"
    "v_mfma_f32_16x16x32_bf16 v[176:179], a[80:83], a[236:239], 0\n"
    "ds_read_u16_d16_hi v201, v201\n"
    "ds_write2_b32 v248, v36, v37 offset0:0 offset1:20\n"
    "v_mfma_f32_16x16x32_bf16 v[176:179], a[128:131], a[240:243], v[176:179]\n"
    "s_waitcnt lgkmcnt(6)\n"
    "v_lshlrev_b32_sdwa v207, 1, v183 dst_sel:DWORD dst_unused:UNUSED_PAD src0_sel:DWORD src1_sel:WORD_1\n"
    "v_exp_f16_sdwa v234, v232 dst_sel:WORD_1 dst_unused:UNUSED_PRESERVE src0_sel:WORD_1\n"
    "v_mfma_f32_16x16x32_bf16 v[176:179], a[48:51], a[244:247], v[176:179]\n"
    "ds_read_u16 v194, v194\n"
    "ds_write2_b32 v248, v38, v39 offset0:40 offset1:60\n"
    "v_mfma_f32_16x16x32_bf16 v[180:183], a[84:87], a[236:239], 0\n"
    "s_waitcnt lgkmcnt(6)\n"
    "v_add_u32_e32 v248, 1360, v248\n"
    "v_pk_fma_f16 v234, v234, s49, s49\n"
    "v_mfma_f32_16x16x32_bf16 v[172:175], a[252:255], a[232:235], v[172:175]\n"
    "s_add_u32 s60, s60, 1572864\n"
    "ds_read_u16_d16_hi v202, v202\n"
    "ds_write2_b32 v235, v56, v57 offset0:0 offset1:20\n"
    "v_mfma_f32_16x16x32_bf16 v[180:183], a[132:135], a[240:243], v[180:183]\n"
    "v_lshlrev_b32_sdwa v200, 1, v169 dst_sel:DWORD dst_unused:UNUSED_PAD src0_sel:DWORD src1_sel:WORD_1\n"
    "v_rcp_f16_e64 v232, v234\n"
    "v_mfma_f32_16x16x32_bf16 v[180:183], a[252:255], a[244:247], v[180:183]\n"
    "s_add_u32 s61, s61, 1572864\n"
    "ds_read_b128 v[236:239], v244 offset:16384\n"
    "ds_write2_b32 v235, v58, v59 offset0:40 offset1:60\n"
    "v_mfma_f32_16x16x32_bf16 v[96:99], a[192:195], v[184:187], v[96:99]\n"
    "v_add_u32_e32 v235, -1360, v235\n"
    "v_rcp_f16_sdwa v232, v234 dst_sel:WORD_1 dst_unused:UNUSED_PRESERVE src0_sel:WORD_1\n"
    "v_mfma_f32_16x16x32_bf16 v[100:103], a[196:199], v[184:187], v[100:103]\n"
    "s_waitcnt lgkmcnt(6)\n"
    "ds_read_u16 v195, v195\n"
    "ds_write2_b32 v248, v40, v41 offset0:0 offset1:20\n"
    "v_mfma_f32_16x16x32_bf16 v[112:115], a[200:203], v[184:187], v[112:115]\n"
    "v_or_b32_e32 v193, v193, v201\n"
    "v_lshlrev_b32_sdwa v201, 1, v171 dst_sel:DWORD dst_unused:UNUSED_PAD src0_sel:DWORD src1_sel:WORD_1\n"
    "v_mfma_f32_16x16x32_bf16 v[116:119], a[204:207], v[184:187], v[116:119]\n"
    "ds_read_u16_d16_hi v203, v203\n"
    "ds_write2_b32 v248, v42, v43 offset0:40 offset1:60\n"
    "v_mfma_f32_16x16x32_bf16 v[104:107], a[192:195], v[188:191], v[104:107]\n"
    "v_add_u32_e32 v248, 1360, v248\n"
    "v_lshlrev_b32_sdwa v204, 1, v177 dst_sel:DWORD dst_unused:UNUSED_PAD src0_sel:DWORD src1_sel:WORD_1\n"
    "v_mfma_f32_16x16x32_bf16 v[108:111], a[196:199], v[188:191], v[108:111]\n"
    "s_waitcnt lgkmcnt(6)\n"
    "ds_read_b128 v[164:167], v244 offset:18432\n"
    "ds_write2_b32 v248, v44, v45 offset0:0 offset1:20\n"
    "v_mfma_f32_16x16x32_bf16 v[120:123], a[200:203], v[188:191], v[120:123]\n"
    "s_waitcnt lgkmcnt(6)\n"
    "v_or_b32_e64 v194, v194, v202\n"
    "v_lshlrev_b32_sdwa v202, 1, v173 dst_sel:DWORD dst_unused:UNUSED_PAD src0_sel:DWORD src1_sel:WORD_1\n"
    "v_mfma_f32_16x16x32_bf16 v[128:131], v[236:239], v[184:187], v[128:131]\n"
    "ds_read_u16 v197, v197\n"
    "ds_write2_b32 v248, v46, v47 offset0:40 offset1:60\n"
    "v_mfma_f32_16x16x32_bf16 v[124:127], a[204:207], v[188:191], v[124:127]\n"
    "v_add_u32_e32 v248, 1360, v248\n"
    "v_fma_mix_f32 v234, v192, v232, 0 op_sel_hi:[1,1,0]\n"
    "v_mfma_f32_16x16x32_bf16 v[136:139], v[236:239], v[188:191], v[136:139]\n"
    "s_add_u32 s62, s62, 1572864\n"
    "ds_read_u16_d16_hi v205, v205\n"
    "ds_write2_b32 v248, v48, v49 offset0:0 offset1:20\n"
    "v_fma_mix_f32 v232, v192, v232, 0 op_sel:[1,1,0] op_sel_hi:[1,1,0]\n"
    "v_cvt_pk_bf16_f32 v192, v234, v232\n"
    "v_pk_fma_f16 v232, v196, v196, s48\n"
    "v_pk_mul_f16 v232, v196, v232\n"
    "v_exp_f16_e64 v234, v232\n"
    "ds_read_u16 v198, v198\n"
    "ds_write2_b32 v248, v50, v51 offset0:40 offset1:60\n"
    "v_mfma_f32_32x32x1_2b_f32 v[32:63], v254, v254, 0\n"
    "s_waitcnt lgkmcnt(6)\n"
    "v_add_u32_e32 v248, -5440, v248\n"
    "v_or_b32_e32 v195, v195, v203\n"
    "ds_read_u16_d16_hi v206, v206\n"
    "ds_read_u16 v199, v199\n"
    "ds_read_u16_d16_hi v207, v207\n"
    "v_lshlrev_b32_sdwa v203, 1, v175 dst_sel:DWORD dst_unused:UNUSED_PAD src0_sel:DWORD src1_sel:WORD_1\n"
    "v_exp_f16_sdwa v234, v232 dst_sel:WORD_1 dst_unused:UNUSED_PRESERVE src0_sel:WORD_1\n"
    "v_mov_b32_e64 v254, 0\n"
    "v_pk_fma_f16 v234, v234, s49, s49\n"
    "v_rcp_f16_e64 v232, v234\n"
    "v_mfma_f32_16x16x32_bf16 v[132:135], v[164:167], v[184:187], v[132:135]\n"
    "s_add_u32 s63, s63, 1572864\n"
    "ds_read_b128 a[252:255], v244 offset:36864\n"
    "ds_read_u16_d16_hi v200, v200\n"
    "v_mfma_f32_16x16x32_bf16 v[140:143], v[164:167], v[188:191], v[140:143]\n"
    "s_add_u32 s52, s52, 1572864\n"
    "s_add_u32 s53, s53, 393216\n"
    "ds_read_u16_d16_hi v201, v201\n"
    "v_mfma_f32_16x16x32_bf16 v[96:99], a[208:211], v[192:195], v[96:99]\n"
    "s_waitcnt lgkmcnt(8)\n"
    "v_or_b32_e32 v197, v197, v205\n"
    "v_lshlrev_b32_sdwa v184, 1, v168 dst_sel:DWORD dst_unused:UNUSED_PAD src0_sel:DWORD src1_sel:WORD_1\n"
    "v_mfma_f32_16x16x32_bf16 v[100:103], a[212:215], v[192:195], v[100:103]\n"
    "s_nop 0\n"
    "ds_read_u16 v184, v184\n"
    "v_lshlrev_b32_sdwa v185, 1, v170 dst_sel:DWORD dst_unused:UNUSED_PAD src0_sel:DWORD src1_sel:WORD_1\n"
    "v_mfma_f32_16x16x32_bf16 v[168:171], a[88:91], a[224:227], 0\n"
    "s_waitcnt lgkmcnt(7)\n"
    "v_lshlrev_b32_sdwa v186, 1, v172 dst_sel:DWORD dst_unused:UNUSED_PAD src0_sel:DWORD src1_sel:WORD_1\n"
    "v_lshlrev_b32_sdwa v187, 1, v174 dst_sel:DWORD dst_unused:UNUSED_PAD src0_sel:DWORD src1_sel:WORD_1\n"
    "v_mfma_f32_16x16x32_bf16 v[168:171], a[136:139], a[228:231], v[168:171]\n"
    "s_barrier\n"
    "v_mfma_f32_16x16x32_bf16 v[168:171], a[52:55], a[232:235], v[168:171]\n"
    "s_waitcnt lgkmcnt(6)\n"
    "ds_read_u16 v185, v185\n"
    "ds_read_u16 v186, v186\n"
    "v_mfma_f32_16x16x32_bf16 v[172:175], a[92:95], a[224:227], 0\n"
    "ds_read_b128 v[236:239], v244 offset:20480\n"
    "ds_read_u16_d16_hi v202, v202\n"
    "v_mfma_f32_16x16x32_bf16 v[172:175], a[140:143], a[228:231], v[172:175]\n"
    "s_waitcnt lgkmcnt(7)\n"
    "v_or_b32_e32 v198, v198, v206\n"
    "v_lshlrev_b32_sdwa v188, 1, v176 dst_sel:DWORD dst_unused:UNUSED_PAD src0_sel:DWORD src1_sel:WORD_1\n"
    "v_mfma_f32_16x16x32_bf16 v[172:175], a[252:255], a[232:235], v[172:175]\n"
    "ds_read_b128 v[208:211], v250 offset:0\n"
    "v_lshlrev_b32_sdwa v189, 1, v178 dst_sel:DWORD dst_unused:UNUSED_PAD src0_sel:DWORD src1_sel:WORD_1\n"
    "v_mfma_f32_16x16x32_bf16 v[112:115], a[216:219], v[192:195], v[112:115]\n"
    "ds_read_b128 v[212:215], v250 offset:320\n"
    "v_lshlrev_b32_sdwa v205, 1, v179 dst_sel:DWORD dst_unused:UNUSED_PAD src0_sel:DWORD src1_sel:WORD_1\n"
    "v_mfma_f32_16x16x32_bf16 v[176:179], a[88:91], a[236:239], 0\n"
    "ds_read_b128 v[216:219], v251 offset:10752\n"
    "v_lshlrev_b32_sdwa v190, 1, v180 dst_sel:DWORD dst_unused:UNUSED_PAD src0_sel:DWORD src1_sel:WORD_1\n"
    "v_mfma_f32_16x16x32_bf16 v[176:179], a[136:139], a[240:243], v[176:179]\n"
    "ds_read_b128 v[220:223], v251 offset:11088\n"
    "v_lshlrev_b32_sdwa v206, 1, v181 dst_sel:DWORD dst_unused:UNUSED_PAD src0_sel:DWORD src1_sel:WORD_1\n"
    "v_mfma_f32_16x16x32_bf16 v[176:179], a[52:55], a[244:247], v[176:179]\n"
    "ds_read_u16 v187, v187\n"
    "v_lshlrev_b32_sdwa v191, 1, v182 dst_sel:DWORD dst_unused:UNUSED_PAD src0_sel:DWORD src1_sel:WORD_1\n"
    "v_or_b32_e32 v199, v199, v207\n"
    "v_lshlrev_b32_sdwa v207, 1, v183 dst_sel:DWORD dst_unused:UNUSED_PAD src0_sel:DWORD src1_sel:WORD_1\n"
    "v_mfma_f32_16x16x32_bf16 v[180:183], a[92:95], a[236:239], 0\n"
    "s_waitcnt lgkmcnt(9)\n"
    "ds_read_b128 v[164:167], v244 offset:22528\n"
    "v_rcp_f16_sdwa v232, v234 dst_sel:WORD_1 dst_unused:UNUSED_PRESERVE src0_sel:WORD_1\n"
    "v_mfma_f32_16x16x32_bf16 v[180:183], a[140:143], a[240:243], v[180:183]\n"
    "s_waitcnt vmcnt(1)\n"
    "ds_read_u16_d16_hi v203, v203\n"
    "v_fma_mix_f32 v234, v196, v232, 0 op_sel_hi:[1,1,0]\n"
    "v_smfmac_f32_32x32x32_bf16 v[128:143], a[248:251], v[144:151], v241\n"
    "s_waitcnt lgkmcnt(10)\n"
    "ds_read_u16 v188, v188\n"
    "v_fma_mix_f32 v232, v196, v232, 0 op_sel:[1,1,0] op_sel_hi:[1,1,0]\n"
    "v_or_b32_e64 v184, v184, v200\n"
    "v_cvt_pk_bf16_f32 v196, v234, v232\n"
    "v_or_b32_e32 v185, v185, v201\n"
    "v_mfma_f32_16x16x32_bf16 v[180:183], a[252:255], a[244:247], v[180:183]\n"
    "s_waitcnt lgkmcnt(9)\n"
    "ds_read_u16_d16_hi v204, v204\n"
    "v_lshlrev_b32_sdwa v201, 1, v171 dst_sel:DWORD dst_unused:UNUSED_PAD src0_sel:DWORD src1_sel:WORD_1\n"
    "v_mfma_f32_16x16x32_bf16 v[116:119], a[220:223], v[192:195], v[116:119]\n"
    "v_cvt_pk_bf16_f32 v96, v96, v97\n"
    "v_cvt_pk_bf16_f32 v97, v98, v99\n"
    "v_mfma_f32_16x16x32_bf16 v[128:131], v[236:239], v[192:195], v[128:131]\n"
    "s_waitcnt lgkmcnt(8)\n"
    "ds_read_u16 v189, v189\n"
    "v_or_b32_e32 v186, v186, v202\n"
    "v_mfma_f32_16x16x32_bf16 v[104:107], a[208:211], v[196:199], v[104:107]\n"
    "v_lshlrev_b32_sdwa v202, 1, v173 dst_sel:DWORD dst_unused:UNUSED_PAD src0_sel:DWORD src1_sel:WORD_1\n"
    "v_cvt_pk_bf16_f32 v98, v100, v101\n"
    "v_mfma_f32_16x16x32_bf16 v[108:111], a[212:215], v[196:199], v[108:111]\n"
    "s_waitcnt lgkmcnt(7)\n"
    "ds_read_u16_d16_hi v205, v205\n"
    "v_cvt_pk_bf16_f32 v99, v102, v103\n"
    "v_smfmac_f32_16x16x64_bf16 v[32:35], v[216:219], v[208:215], v240\n"
    "s_waitcnt lgkmcnt(7)\n"
    "ds_read_u16 v190, v190\n"
    "buffer_store_dwordx4 v[96:99], v245, s[44:47], s57 offen offset:0 sc1\n"
    "v_smfmac_f32_16x16x64_bf16 v[36:39], v[220:223], v[208:215], v240 abid:1\n"
    "s_waitcnt lgkmcnt(6)\n"
    "ds_read_b128 v[208:211], v250 offset:640\n"
    "v_mfma_f32_16x16x32_bf16 v[132:135], v[164:167], v[192:195], v[132:135]\n"
    "s_nop 0\n"
    "ds_read_b128 v[212:215], v250 offset:960\n"
    "v_cvt_pk_f16_f32 v192, v168, v169\n"
    "v_mfma_f32_16x16x32_bf16 v[120:123], a[216:219], v[196:199], v[120:123]\n"
    "ds_read_b128 v[216:219], v252 offset:10752\n"
    "v_lshlrev_b32_sdwa v193, 1, v170 dst_sel:DWORD dst_unused:UNUSED_PAD src0_sel:DWORD src1_sel:WORD_1\n"
    "v_mfma_f32_16x16x32_bf16 v[124:127], a[220:223], v[196:199], v[124:127]\n"
    "s_nop 0\n"
    "ds_read_b128 v[220:223], v252 offset:11088\n"
    "v_lshlrev_b32_sdwa v194, 1, v172 dst_sel:DWORD dst_unused:UNUSED_PAD src0_sel:DWORD src1_sel:WORD_1\n"
    "v_mfma_f32_16x16x32_bf16 v[136:139], v[236:239], v[196:199], v[136:139]\n"
    "s_waitcnt lgkmcnt(9)\n"
    "ds_read_u16_d16_hi v206, v206\n"
    "v_lshlrev_b32_sdwa v195, 1, v174 dst_sel:DWORD dst_unused:UNUSED_PAD src0_sel:DWORD src1_sel:WORD_1\n"
    "v_mfma_f32_16x16x32_bf16 v[140:143], v[164:167], v[196:199], v[140:143]\n"
    "v_cvt_pk_f16_f32 v196, v176, v177\n"
    "v_lshlrev_b32_sdwa v197, 1, v178 dst_sel:DWORD dst_unused:UNUSED_PAD src0_sel:DWORD src1_sel:WORD_1\n"
    "v_mfma_f32_16x16x32_bf16 v[168:171], a[96:99], a[224:227], 0\n"
    "ds_read_u16 v191, v191\n"
    "v_lshlrev_b32_sdwa v198, 1, v180 dst_sel:DWORD dst_unused:UNUSED_PAD src0_sel:DWORD src1_sel:WORD_1\n"
    "v_mfma_f32_16x16x32_bf16 v[168:171], a[144:147], a[228:231], v[168:171]\n"
    "s_waitcnt lgkmcnt(9)\n"
    "v_lshlrev_b32_sdwa v199, 1, v182 dst_sel:DWORD dst_unused:UNUSED_PAD src0_sel:DWORD src1_sel:WORD_1\n"
    "v_or_b32_e32 v187, v187, v203\n"
    "v_mfma_f32_16x16x32_bf16 v[168:171], a[56:59], a[232:235], v[168:171]\n"
    "ds_read_u16_d16_hi v207, v207\n"
    "v_lshlrev_b32_sdwa v203, 1, v175 dst_sel:DWORD dst_unused:UNUSED_PAD src0_sel:DWORD src1_sel:WORD_1\n"
    "v_mfma_f32_16x16x32_bf16 v[172:175], a[100:103], a[224:227], 0\n"
    "ds_read_u16 v193, v193\n"
    "v_or_b32_e64 v188, v188, v204\n"
    "v_mfma_f32_16x16x32_bf16 v[172:175], a[148:151], a[228:231], v[172:175]\n"
    "v_cvt_pk_bf16_f32 v152, v112, v113\n"
    "v_cvt_pk_bf16_f32 v153, v114, v115\n"
    "v_mfma_f32_16x16x32_bf16 v[172:175], a[60:63], a[232:235], v[172:175]\n"
    "s_waitcnt lgkmcnt(9)\n"
    "ds_read_u16_d16_hi v201, v201\n"
    "v_or_b32_e32 v189, v189, v205\n"
    "v_lshlrev_b32_sdwa v205, 1, v179 dst_sel:DWORD dst_unused:UNUSED_PAD src0_sel:DWORD src1_sel:WORD_1\n"
    "v_mfma_f32_16x16x32_bf16 v[176:179], a[96:99], a[236:239], 0\n"
    "v_cvt_pk_bf16_f32 v154, v116, v117\n"
    "v_cvt_pk_bf16_f32 v155, v118, v119\n"
    "v_mfma_f32_16x16x32_bf16 v[176:179], a[144:147], a[240:243], v[176:179]\n"
    "ds_read_u16 v194, v194\n"
    "v_cvt_pk_bf16_f32 v156, v128, v129\n"
    "v_mfma_f32_16x16x32_bf16 v[176:179], a[56:59], a[244:247], v[176:179]\n"
    "s_waitcnt lgkmcnt(7)\n"
    "ds_read_u16_d16_hi v202, v202\n"
    "v_cvt_pk_bf16_f32 v157, v130, v131\n"
    "v_smfmac_f32_16x16x64_bf16 v[32:35], v[216:219], v[208:215], v240\n"
    "s_waitcnt lgkmcnt(7)\n"
    "v_cvt_pk_bf16_f32 v158, v132, v133\n"
    "v_smfmac_f32_16x16x64_bf16 v[36:39], v[220:223], v[208:215], v240 abid:1\n"
    "ds_read_b128 v[208:211], v250 offset:80\n"
    "v_cvt_pk_bf16_f32 v159, v134, v135\n"
    "ds_read_b128 v[212:215], v250 offset:400\n"
    "s_waitcnt lgkmcnt(8)\n"
    "v_or_b32_e32 v190, v190, v206\n"
    "v_pk_add_f32 v[224:225], v[224:225], v[32:33]\n"
    "v_pk_add_f32 v[226:227], v[226:227], v[34:35]\n"
    "v_pk_fma_f32 v[228:229], v[32:33], v[32:33], v[228:229]\n"
    "v_pk_fma_f32 v[230:231], v[34:35], v[34:35], v[230:231]\n"
    "v_pk_add_f32 v[224:225], v[224:225], v[36:37]\n"
    "v_pk_add_f32 v[226:227], v[226:227], v[38:39]\n"
    "v_pk_fma_f32 v[228:229], v[36:37], v[36:37], v[228:229]\n"
    "v_pk_fma_f32 v[230:231], v[38:39], v[38:39], v[230:231]\n"
    "v_mfma_f32_16x16x32_bf16 v[96:99], a[0:3], v[184:187], 0\n"
    "s_nop 0\n"
    "ds_read_b128 v[216:219], v251 offset:11424\n"
    "ds_read_b128 v[220:223], v251 offset:11760\n"
    "v_mfma_f32_16x16x32_bf16 v[100:103], a[4:7], v[184:187], 0\n"
    "s_waitcnt lgkmcnt(8)\n"
    "ds_read_b128 v[236:239], v244 offset:0\n"
    "ds_read_u16 v195, v195\n"
    "v_mfma_f32_16x16x32_bf16 v[112:115], a[8:11], v[184:187], 0\n"
    "ds_read_u16_d16_hi v203, v203\n"
    "v_lshlrev_b32_sdwa v206, 1, v181 dst_sel:DWORD dst_unused:UNUSED_PAD src0_sel:DWORD src1_sel:WORD_1\n"
    "v_or_b32_e64 v191, v191, v207\n"
    "v_mfma_f32_16x16x32_bf16 v[116:119], a[12:15], v[184:187], 0\n"
    "v_lshlrev_b32_sdwa v207, 1, v183 dst_sel:DWORD dst_unused:UNUSED_PAD src0_sel:DWORD src1_sel:WORD_1\n"
    "v_cvt_pk_bf16_f32 v144, v104, v105\n"
    "v_mfma_f32_16x16x32_bf16 v[180:183], a[100:103], a[236:239], 0\n"
    "s_waitcnt lgkmcnt(9)\n"
    "ds_read_b128 v[164:167], v244 offset:2048\n"
    "v_or_b32_e32 v193, v193, v201\n"
    "v_mfma_f32_16x16x32_bf16 v[180:183], a[148:151], a[240:243], v[180:183]\n"
    "ds_read_u16 v197, v197\n"
    "v_cvt_pk_bf16_f32 v145, v106, v107\n"
    "v_mfma_f32_16x16x32_bf16 v[180:183], a[60:63], a[244:247], v[180:183]\n"
    "v_cvt_pk_bf16_f32 v146, v108, v109\n"
    "v_cvt_pk_bf16_f32 v147, v110, v111\n"
    "v_mfma_f32_16x16x32_bf16 v[104:107], a[0:3], v[188:191], 0\n"
    "ds_read_u16_d16_hi v205, v205\n"
    "v_cvt_pk_bf16_f32 v148, v120, v121\n"
    "v_mfma_f32_16x16x32_bf16 v[108:111], a[4:7], v[188:191], 0\n"
    "s_waitcnt lgkmcnt(8)\n"
    "ds_read_u16 v198, v198\n"
    "v_or_b32_e32 v194, v194, v202\n"
    "v_cvt_pk_bf16_f32 v149, v122, v123\n"
    "v_mfma_f32_16x16x32_bf16 v[120:123], a[8:11], v[188:191], 0\n"
    "s_waitcnt lgkmcnt(8)\n"
    "v_cvt_pk_bf16_f32 v150, v124, v125\n"
    "v_cvt_pk_bf16_f32 v151, v126, v127\n"
    "v_smfmac_f32_16x16x64_bf16 v[40:43], v[216:219], v[208:215], v240\n"
    "s_waitcnt lgkmcnt(7)\n"
    "ds_read_b128 v[216:219], v252 offset:11424\n"
    "ds_read_u16_d16_hi v206, v206\n"
    "v_smfmac_f32_16x16x64_bf16 v[44:47], v[220:223], v[208:215], v240 abid:1\n"
    "s_waitcnt lgkmcnt(8)\n"
    "v_pk_fma_f16 v232, v192, v192, s48\n"
    "v_mfma_f32_16x16x32_bf16 v[128:131], v[236:239], v[184:187], 0\n"
    "s_nop 0\n"
    "ds_read_b128 v[208:211], v250 offset:720\n"
    "v_cvt_pk_bf16_f32 v160, v136, v137\n"
    "v_mfma_f32_16x16x32_bf16 v[124:127], a[12:15], v[188:191], 0\n"
    "ds_read_b128 v[212:215], v250 offset:1040\n"
    "v_cvt_pk_bf16_f32 v161, v138, v139\n"
    "v_cvt_pk_bf16_f32 v162, v140, v141\n"
    "v_cvt_pk_bf16_f32 v163, v142, v143\n"
    "ds_read_b128 v[220:223], v252 offset:11760\n"
    "v_pk_mul_f16 v232, v192, v232\n"
    "v_mfma_f32_16x16x32_bf16 v[136:139], v[236:239], v[188:191], 0\n"
    "s_waitcnt lgkmcnt(8)\n"
    "ds_read_u16 v199, v199\n"
    "v_or_b32_e32 v195, v195, v203\n"
    "v_mfma_f32_16x16x32_bf16 v[132:135], v[164:167], v[184:187], 0\n"
    "v_exp_f16_e64 v234, v232\n"
    "v_mfma_f32_16x16x32_bf16 v[140:143], v[164:167], v[188:191], 0\n"
    "ds_read_u16_d16_hi v207, v207\n"
    "v_exp_f16_sdwa v234, v232 dst_sel:WORD_1 dst_unused:UNUSED_PRESERVE src0_sel:WORD_1\n"
    "v_lshlrev_b32_sdwa v184, 1, v168 dst_sel:DWORD dst_unused:UNUSED_PAD src0_sel:DWORD src1_sel:WORD_1\n"
    "v_pk_fma_f16 v234, v234, s49, s49\n"
    "v_lshlrev_b32_sdwa v200, 1, v169 dst_sel:DWORD dst_unused:UNUSED_PAD src0_sel:DWORD src1_sel:WORD_1\n"
    "v_lshlrev_b32_sdwa v185, 1, v170 dst_sel:DWORD dst_unused:UNUSED_PAD src0_sel:DWORD src1_sel:WORD_1\n"
    "v_lshlrev_b32_sdwa v201, 1, v171 dst_sel:DWORD dst_unused:UNUSED_PAD src0_sel:DWORD src1_sel:WORD_1\n"
    "v_mfma_f32_16x16x32_bf16 v[168:171], a[104:107], a[224:227], 0\n"
    "s_waitcnt lgkmcnt(8)\n"
    "ds_read_u16 v184, v184\n"
    "ds_read_u16_d16_hi v200, v200\n"
    "v_mfma_f32_16x16x32_bf16 v[168:171], a[152:155], a[228:231], v[168:171]\n"
    "v_or_b32_e32 v197, v197, v205\n"
    "v_lshlrev_b32_sdwa v186, 1, v172 dst_sel:DWORD dst_unused:UNUSED_PAD src0_sel:DWORD src1_sel:WORD_1\n"
    "v_lshlrev_b32_sdwa v202, 1, v173 dst_sel:DWORD dst_unused:UNUSED_PAD src0_sel:DWORD src1_sel:WORD_1\n"
    "v_lshlrev_b32_sdwa v187, 1, v174 dst_sel:DWORD dst_unused:UNUSED_PAD src0_sel:DWORD src1_sel:WORD_1\n"
    "v_lshlrev_b32_sdwa v203, 1, v175 dst_sel:DWORD dst_unused:UNUSED_PAD src0_sel:DWORD src1_sel:WORD_1\n"
    "v_rcp_f16_e64 v232, v234\n"
    "v_mfma_f32_16x16x32_bf16 v[168:171], a[64:67], a[232:235], v[168:171]\n"
    "ds_read_u16 v185, v185\n"
    "v_rcp_f16_sdwa v232, v234 dst_sel:WORD_1 dst_unused:UNUSED_PRESERVE src0_sel:WORD_1\n"
    "v_mfma_f32_16x16x32_bf16 v[172:175], a[108:111], a[224:227], 0\n"
    "s_waitcnt lgkmcnt(7)\n"
    "ds_read_u16_d16_hi v201, v201\n"
    "v_or_b32_e32 v198, v198, v206\n"
    "s_waitcnt lgkmcnt(7)\n"
    "v_smfmac_f32_16x16x64_bf16 v[40:43], v[216:219], v[208:215], v240\n"
    "s_waitcnt lgkmcnt(6)\n"
    "ds_read_b128 v[216:219], v251 offset:12096\n"
    "v_smfmac_f32_16x16x64_bf16 v[44:47], v[220:223], v[208:215], v240 abid:1\n"
    "ds_read_b128 v[208:211], v250 offset:160\n"
    "v_lshlrev_b32_sdwa v188, 1, v176 dst_sel:DWORD dst_unused:UNUSED_PAD src0_sel:DWORD src1_sel:WORD_1\n"
    "ds_read_b128 v[212:215], v250 offset:480\n"
    "v_lshlrev_b32_sdwa v204, 1, v177 dst_sel:DWORD dst_unused:UNUSED_PAD src0_sel:DWORD src1_sel:WORD_1\n"
    "ds_read_b128 v[220:223], v251 offset:12432\n"
    "v_pk_add_f32 v[224:225], v[224:225], v[40:41]\n"
    "v_pk_add_f32 v[226:227], v[226:227], v[42:43]\n"
    "v_pk_fma_f32 v[228:229], v[40:41], v[40:41], v[228:229]\n"
    "v_pk_fma_f32 v[230:231], v[42:43], v[42:43], v[230:231]\n"
    "v_pk_add_f32 v[224:225], v[224:225], v[44:45]\n"
    "v_pk_add_f32 v[226:227], v[226:227], v[46:47]\n"
    "v_pk_fma_f32 v[228:229], v[44:45], v[44:45], v[228:229]\n"
    "v_pk_fma_f32 v[230:231], v[46:47], v[46:47], v[230:231]\n"
    "v_lshlrev_b32_sdwa v189, 1, v178 dst_sel:DWORD dst_unused:UNUSED_PAD src0_sel:DWORD src1_sel:WORD_1\n"
    "v_lshlrev_b32_sdwa v205, 1, v179 dst_sel:DWORD dst_unused:UNUSED_PAD src0_sel:DWORD src1_sel:WORD_1\n"
    "v_lshlrev_b32_sdwa v190, 1, v180 dst_sel:DWORD dst_unused:UNUSED_PAD src0_sel:DWORD src1_sel:WORD_1\n"
    "v_lshlrev_b32_sdwa v206, 1, v181 dst_sel:DWORD dst_unused:UNUSED_PAD src0_sel:DWORD src1_sel:WORD_1\n"
    "v_lshlrev_b32_sdwa v191, 1, v182 dst_sel:DWORD dst_unused:UNUSED_PAD src0_sel:DWORD src1_sel:WORD_1\n"
    "v_fma_mix_f32 v234, v192, v232, 0 op_sel_hi:[1,1,0]\n"
    "v_mfma_f32_16x16x32_bf16 v[172:175], a[156:159], a[228:231], v[172:175]\n"
    "s_waitcnt lgkmcnt(7)\n"
    "ds_read_u16 v186, v186\n"
    "ds_read_b128 v[236:239], v244 offset:4096\n"
    "v_mfma_f32_16x16x32_bf16 v[172:175], a[68:71], a[232:235], v[172:175]\n"
    "s_waitcnt lgkmcnt(8)\n"
    "ds_read_u16_d16_hi v202, v202\n"
    "ds_read_u16 v187, v187\n"
    "v_or_b32_e64 v199, v199, v207\n"
    "v_lshlrev_b32_sdwa v207, 1, v183 dst_sel:DWORD dst_unused:UNUSED_PAD src0_sel:DWORD src1_sel:WORD_1\n"
    "v_fma_mix_f32 v232, v192, v232, 0 op_sel:[1,1,0] op_sel_hi:[1,1,0]\n"
    "v_mfma_f32_16x16x32_bf16 v[176:179], a[104:107], a[236:239], 0\n"
    "ds_read_b128 v[164:167], v244 offset:6144\n"
    "v_cvt_pk_bf16_f32 v192, v234, v232\n"
    "v_mfma_f32_16x16x32_bf16 v[176:179], a[152:155], a[240:243], v[176:179]\n"
    "ds_read_u16_d16_hi v203, v203\n"
    "v_pk_fma_f16 v232, v196, v196, s48\n"
    "v_mfma_f32_16x16x32_bf16 v[176:179], a[64:67], a[244:247], v[176:179]\n"
    "s_waitcnt lgkmcnt(9)\n"
    "v_pk_mul_f16 v232, v196, v232\n"
    "v_or_b32_e32 v184, v184, v200\n"
    "v_mfma_f32_16x16x32_bf16 v[180:183], a[108:111], a[236:239], 0\n"
    "ds_read_u16 v188, v188\n"
    "v_exp_f16_e64 v234, v232\n"
    "v_mfma_f32_16x16x32_bf16 v[180:183], a[156:159], a[240:243], v[180:183]\n"
    "s_waitcnt lgkmcnt(8)\n"
    "ds_read_u16_d16_hi v204, v204\n"
    "v_exp_f16_sdwa v234, v232 dst_sel:WORD_1 dst_unused:UNUSED_PRESERVE src0_sel:WORD_1\n"
    "v_smfmac_f32_16x16x64_bf16 v[48:51], v[216:219], v[208:215], v240\n"
    "s_waitcnt lgkmcnt(8)\n"
    "v_pk_fma_f16 v234, v234, s49, s49\n"
    "v_smfmac_f32_16x16x64_bf16 v[52:55], v[220:223], v[208:215], v240 abid:1\n"
    "ds_read_b128 v[208:211], v250 offset:800\n"
    "ds_read_b128 v[212:215], v250 offset:1120\n"
    "v_mfma_f32_16x16x32_bf16 v[180:183], a[68:71], a[244:247], v[180:183]\n"
    "v_rcp_f16_e64 v232, v234\n"
    "v_mfma_f32_16x16x32_bf16 v[96:99], a[16:19], v[192:195], v[96:99]\n"
    "ds_read_b128 v[216:219], v252 offset:12096\n"
    "ds_read_b128 v[220:223], v252 offset:12432\n"
    "v_mfma_f32_16x16x32_bf16 v[100:103], a[20:23], v[192:195], v[100:103]\n"
    "buffer_store_dwordx4 v[152:155], v245, s[44:47], s57 offen offset:64 sc1\n"
    "ds_read_u16 v189, v189\n"
    "v_mfma_f32_16x16x32_bf16 v[112:115], a[24:27], v[192:195], v[112:115]\n"
    "s_waitcnt lgkmcnt(10)\n"
    "ds_read_u16_d16_hi v205, v205\n"
    "v_rcp_f16_sdwa v232, v234 dst_sel:WORD_1 dst_unused:UNUSED_PRESERVE src0_sel:WORD_1\n"
    "v_mfma_f32_16x16x32_bf16 v[116:119], a[28:31], v[192:195], v[116:119]\n"
    "v_fma_mix_f32 v234, v196, v232, 0 op_sel_hi:[1,1,0]\n"
    "v_fma_mix_f32 v232, v196, v232, 0 op_sel:[1,1,0] op_sel_hi:[1,1,0]\n"
    "v_mfma_f32_16x16x32_bf16 v[128:131], v[236:239], v[192:195], v[128:131]\n"
    "s_waitcnt lgkmcnt(9)\n"
    "ds_read_u16 v190, v190\n"
    "v_cvt_pk_bf16_f32 v196, v234, v232\n"
    "v_mfma_f32_16x16x32_bf16 v[132:135], v[164:167], v[192:195], v[132:135]\n"
    "s_waitcnt lgkmcnt(9)\n"
    "v_or_b32_e32 v185, v185, v201\n"
    "v_cvt_pk_f16_f32 v192, v168, v169\n"
    "v_mfma_f32_16x16x32_bf16 v[104:107], a[16:19], v[196:199], v[104:107]\n"
    "ds_read_u16_d16_hi v206, v206\n"
    "v_lshlrev_b32_sdwa v193, 1, v170 dst_sel:DWORD dst_unused:UNUSED_PAD src0_sel:DWORD src1_sel:WORD_1\n"
    "v_mfma_f32_16x16x32_bf16 v[108:111], a[20:23], v[196:199], v[108:111]\n"
    "ds_read_u16 v191, v191\n"
    "v_lshlrev_b32_sdwa v201, 1, v171 dst_sel:DWORD dst_unused:UNUSED_PAD src0_sel:DWORD src1_sel:WORD_1\n"
    "v_mfma_f32_16x16x32_bf16 v[120:123], a[24:27], v[196:199], v[120:123]\n"
    "v_lshlrev_b32_sdwa v194, 1, v172 dst_sel:DWORD dst_unused:UNUSED_PAD src0_sel:DWORD src1_sel:WORD_1\n"
    "v_or_b32_e64 v186, v186, v202\n"
    "v_mfma_f32_16x16x32_bf16 v[124:127], a[28:31], v[196:199], v[124:127]\n"
    "s_waitcnt lgkmcnt(7)\n"
    "ds_read_u16_d16_hi v207, v207\n"
    "v_lshlrev_b32_sdwa v202, 1, v173 dst_sel:DWORD dst_unused:UNUSED_PAD src0_sel:DWORD src1_sel:WORD_1\n"
    "v_mfma_f32_16x16x32_bf16 v[136:139], v[236:239], v[196:199], v[136:139]\n"
    "s_waitcnt lgkmcnt(7)\n"
    "v_lshlrev_b32_sdwa v195, 1, v174 dst_sel:DWORD dst_unused:UNUSED_PAD src0_sel:DWORD src1_sel:WORD_1\n"
    "v_or_b32_e32 v187, v187, v203\n"
    "v_smfmac_f32_16x16x64_bf16 v[48:51], v[216:219], v[208:215], v240\n"
    "s_waitcnt lgkmcnt(6)\n"
    "ds_read_b128 v[216:219], v251 offset:12768\n"
    "ds_read_b128 a[252:255], v244 offset:34816\n"
    "v_smfmac_f32_16x16x64_bf16 v[52:55], v[220:223], v[208:215], v240 abid:1\n"
    "ds_read_b128 v[208:211], v250 offset:240\n"
    "v_mfma_f32_16x16x32_bf16 v[140:143], v[164:167], v[196:199], v[140:143]\n"
    "v_lshlrev_b32_sdwa v203, 1, v175 dst_sel:DWORD dst_unused:UNUSED_PAD src0_sel:DWORD src1_sel:WORD_1\n"
    "v_cvt_pk_f16_f32 v196, v176, v177\n"
    "ds_read_b128 v[212:215], v250 offset:560\n"
    "ds_read_b128 v[220:223], v251 offset:13104\n"
    "v_pk_add_f32 v[224:225], v[224:225], v[48:49]\n"
    "v_pk_add_f32 v[226:227], v[226:227], v[50:51]\n"
    "v_pk_fma_f32 v[228:229], v[48:49], v[48:49], v[228:229]\n"
    "v_pk_fma_f32 v[230:231], v[50:51], v[50:51], v[230:231]\n"
    "v_pk_add_f32 v[224:225], v[224:225], v[52:53]\n"
    "v_pk_add_f32 v[226:227], v[226:227], v[54:55]\n"
    "v_pk_fma_f32 v[228:229], v[52:53], v[52:53], v[228:229]\n"
    "v_pk_fma_f32 v[230:231], v[54:55], v[54:55], v[230:231]\n"
    "v_mfma_f32_16x16x32_bf16 v[168:171], a[112:115], a[224:227], 0\n"
    "s_waitcnt lgkmcnt(9)\n"
    "ds_read_u16 v193, v193\n"
    "ds_read_u16_d16_hi v201, v201\n"
    "v_mfma_f32_16x16x32_bf16 v[168:171], a[160:163], a[228:231], v[168:171]\n"
    "v_lshlrev_b32_sdwa v197, 1, v178 dst_sel:DWORD dst_unused:UNUSED_PAD src0_sel:DWORD src1_sel:WORD_1\n"
    "v_lshlrev_b32_sdwa v198, 1, v180 dst_sel:DWORD dst_unused:UNUSED_PAD src0_sel:DWORD src1_sel:WORD_1\n"
    "v_mfma_f32_16x16x32_bf16 v[168:171], a[72:75], a[232:235], v[168:171]\n"
    "s_waitcnt lgkmcnt(9)\n"
    "ds_read_u16 v194, v194\n"
    "v_lshlrev_b32_sdwa v199, 1, v182 dst_sel:DWORD dst_unused:UNUSED_PAD src0_sel:DWORD src1_sel:WORD_1\n"
    "v_mfma_f32_16x16x32_bf16 v[172:175], a[116:119], a[224:227], 0\n"
    "v_or_b32_e64 v188, v188, v204\n"
    "v_or_b32_e32 v189, v189, v205\n"
    "v_mfma_f32_16x16x32_bf16 v[172:175], a[164:167], a[228:231], v[172:175]\n"
    "ds_read_u16_d16_hi v202, v202\n"
    "buffer_store_dwordx4 v[156:159], v245, s[44:47], s57 offen offset:128 sc1\n"
    "v_lshlrev_b32_sdwa v205, 1, v179 dst_sel:DWORD dst_unused:UNUSED_PAD src0_sel:DWORD src1_sel:WORD_1\n"
    "v_mfma_f32_16x16x32_bf16 v[176:179], a[112:115], a[236:239], 0\n"
    "s_waitcnt lgkmcnt(7)\n"
    "ds_read_b128 v[236:239], v244 offset:8192\n"
    "ds_read_u16 v195, v195\n"
    "v_mfma_f32_16x16x32_bf16 v[172:175], a[252:255], a[232:235], v[172:175]\n"
    "v_or_b32_e32 v190, v190, v206\n"
    "v_lshlrev_b32_sdwa v206, 1, v181 dst_sel:DWORD dst_unused:UNUSED_PAD src0_sel:DWORD src1_sel:WORD_1\n"
    "v_mfma_f32_16x16x32_bf16 v[176:179], a[160:163], a[240:243], v[176:179]\n"
    "s_waitcnt lgkmcnt(7)\n"
    "ds_read_u16_d16_hi v203, v203\n"
    "v_or_b32_e32 v191, v191, v207\n"
    "v_smfmac_f32_16x16x64_bf16 v[56:59], v[216:219], v[208:215], v240\n"
    "s_waitcnt lgkmcnt(7)\n"
    "v_lshlrev_b32_sdwa v207, 1, v183 dst_sel:DWORD dst_unused:UNUSED_PAD src0_sel:DWORD src1_sel:WORD_1\n"
    "v_smfmac_f32_16x16x64_bf16 v[60:63], v[220:223], v[208:215], v240 abid:1\n"
    "ds_read_b128 v[208:211], v250 offset:880\n"
    "ds_read_b128 v[212:215], v250 offset:1200\n"
    "v_mfma_f32_16x16x32_bf16 v[176:179], a[72:75], a[244:247], v[176:179]\n"
    "v_pk_fma_f16 v232, v192, v192, s48\n"
    "v_lshlrev_b32_sdwa v200, 1, v169 dst_sel:DWORD dst_unused:UNUSED_PAD src0_sel:DWORD src1_sel:WORD_1\n"
    "v_mfma_f32_16x16x32_bf16 v[180:183], a[116:119], a[236:239], 0\n"
    "ds_read_b128 v[216:219], v252 offset:12768\n"
    "ds_read_b128 v[220:223], v252 offset:13104\n"
    "v_mfma_f32_16x16x32_bf16 v[180:183], a[164:167], a[240:243], v[180:183]\n"
    "v_pk_mul_f16 v232, v192, v232\n"
    "v_exp_f16_e32 v234, v232\n"
    "v_mfma_f32_16x16x32_bf16 v[180:183], a[252:255], a[244:247], v[180:183]\n"
    "s_waitcnt lgkmcnt(9)\n"
    "ds_read_b128 v[164:167], v244 offset:10240\n"
    "ds_read_u16 v197, v197\n"
    "v_mfma_f32_16x16x32_bf16 v[96:99], a[32:35], v[184:187], v[96:99]\n"
    "v_or_b32_e64 v193, v193, v201\n"
    "v_exp_f16_sdwa v234, v232 dst_sel:WORD_1 dst_unused:UNUSED_PRESERVE src0_sel:WORD_1\n"
    "v_mfma_f32_16x16x32_bf16 v[100:103], a[36:39], v[184:187], v[100:103]\n"
    "ds_read_u16_d16_hi v205, v205\n"
    "v_pk_fma_f16 v234, v234, s49, s49\n"
    "v_mfma_f32_16x16x32_bf16 v[112:115], a[40:43], v[184:187], v[112:115]\n"
    "s_waitcnt lgkmcnt(9)\n"
    "v_or_b32_e32 v194, v194, v202\n"
    "v_lshlrev_b32_sdwa v201, 1, v171 dst_sel:DWORD dst_unused:UNUSED_PAD src0_sel:DWORD src1_sel:WORD_1\n"
    "v_mfma_f32_16x16x32_bf16 v[116:119], a[44:47], v[184:187], v[116:119]\n"
    "ds_read_u16 v198, v198\n"
    "v_lshlrev_b32_sdwa v202, 1, v173 dst_sel:DWORD dst_unused:UNUSED_PAD src0_sel:DWORD src1_sel:WORD_1\n"
    "v_mfma_f32_16x16x32_bf16 v[128:131], v[236:239], v[184:187], v[128:131]\n"
    "ds_read_u16_d16_hi v206, v206\n"
    "v_rcp_f16_e32 v232, v234\n"
    "v_mfma_f32_16x16x32_bf16 v[104:107], a[32:35], v[188:191], v[104:107]\n"
    "v_rcp_f16_sdwa v232, v234 dst_sel:WORD_1 dst_unused:UNUSED_PRESERVE src0_sel:WORD_1\n"
    "v_mfma_f32_16x16x32_bf16 v[108:111], a[36:39], v[188:191], v[108:111]\n"
    "s_waitcnt lgkmcnt(8)\n"
    "ds_read_u16 v199, v199\n"
    "v_or_b32_e32 v195, v195, v203\n"
    "v_mfma_f32_16x16x32_bf16 v[120:123], a[40:43], v[188:191], v[120:123]\n"
    "s_waitcnt lgkmcnt(8)\n"
    "s_barrier\n"
    "v_mfma_f32_16x16x32_bf16 v[124:127], a[44:47], v[188:191], v[124:127]\n"
    "s_waitcnt lgkmcnt(5)\n"
    "ds_read_u16_d16_hi v207, v207\n"
    "ds_write2_b32 v248, v64, v65 offset0:0 offset1:20\n"
    "v_smfmac_f32_16x16x64_bf16 v[56:59], v[216:219], v[208:215], v240\n"
    "s_nop 0\n"
    "buffer_store_dwordx4 v[144:147], v245, s[44:47], s88 offen offset:0 sc1\n"
    "ds_write2_b32 v248, v66, v67 offset0:40 offset1:60\n"
    "v_smfmac_f32_16x16x64_bf16 v[60:63], v[220:223], v[208:215], v240 abid:1\n"
    "s_waitcnt lgkmcnt(6)\n"
    "v_add_u32_e32 v248, 1360, v248\n"
    "v_mfma_f32_16x16x32_bf16 v[132:135], v[164:167], v[184:187], v[132:135]\n"
    "ds_read_b128 a[252:255], v244 offset:38912\n"
    "ds_write2_b32 v235, v84, v85 offset0:0 offset1:20\n"
    "v_or_b32_e64 v197, v197, v205\n"
    "v_pk_add_f32 v[224:225], v[224:225], v[56:57]\n"
    "v_pk_add_f32 v[226:227], v[226:227], v[58:59]\n"
    "v_pk_fma_f32 v[228:229], v[56:57], v[56:57], v[228:229]\n"
    "v_pk_fma_f32 v[230:231], v[58:59], v[58:59], v[230:231]\n"
    "v_pk_add_f32 v[224:225], v[224:225], v[60:61]\n"
    "v_pk_add_f32 v[226:227], v[226:227], v[62:63]\n"
    "v_pk_fma_f32 v[228:229], v[60:61], v[60:61], v[228:229]\n"
    "v_pk_fma_f32 v[230:231], v[62:63], v[62:63], v[230:231]\n"
    "v_mfma_f32_16x16x32_bf16 v[136:139], v[236:239], v[188:191], v[136:139]\n"
    "s_waitcnt lgkmcnt(5)\n"
    "ds_read_u16_d16_hi v200, v200\n"
    "ds_write2_b32 v235, v86, v87 offset0:40 offset1:60\n"
    "v_mfma_f32_16x16x32_bf16 v[140:143], v[164:167], v[188:191], v[140:143]\n"
    "s_waitcnt lgkmcnt(4)\n"
    "v_add_u32_e32 v235, 1360, v235\n"
    "v_lshlrev_b32_sdwa v184, 1, v168 dst_sel:DWORD dst_unused:UNUSED_PAD src0_sel:DWORD src1_sel:WORD_1\n"
    "v_lshlrev_b32_sdwa v185, 1, v170 dst_sel:DWORD dst_unused:UNUSED_PAD src0_sel:DWORD src1_sel:WORD_1\n"
    "v_mfma_f32_16x16x32_bf16 v[168:171], a[120:123], a[224:227], 0\n"
    "ds_read_u16 v184, v184\n"
    "ds_write2_b32 v248, v68, v69 offset0:0 offset1:20\n"
    "v_mfma_f32_16x16x32_bf16 v[168:171], a[168:171], a[228:231], v[168:171]\n"
    "v_lshlrev_b32_sdwa v186, 1, v172 dst_sel:DWORD dst_unused:UNUSED_PAD src0_sel:DWORD src1_sel:WORD_1\n"
    "v_lshlrev_b32_sdwa v187, 1, v174 dst_sel:DWORD dst_unused:UNUSED_PAD src0_sel:DWORD src1_sel:WORD_1\n"
    "v_mfma_f32_16x16x32_bf16 v[168:171], a[76:79], a[232:235], v[168:171]\n"
    "ds_read_u16 v185, v185\n"
    "ds_write2_b32 v248, v70, v71 offset0:40 offset1:60\n"
    "v_add_u32_e32 v248, 1360, v248\n"
    "v_lshlrev_b32_sdwa v203, 1, v175 dst_sel:DWORD dst_unused:UNUSED_PAD src0_sel:DWORD src1_sel:WORD_1\n"
    "v_mfma_f32_16x16x32_bf16 v[172:175], a[124:127], a[224:227], 0\n"
    "v_or_b32_e64 v198, v198, v206\n"
    "v_lshlrev_b32_sdwa v188, 1, v176 dst_sel:DWORD dst_unused:UNUSED_PAD src0_sel:DWORD src1_sel:WORD_1\n"
    "v_mfma_f32_16x16x32_bf16 v[172:175], a[172:175], a[228:231], v[172:175]\n"
    "ds_read_u16_d16_hi v201, v201\n"
    "ds_write2_b32 v235, v88, v89 offset0:0 offset1:20\n"
    "v_lshlrev_b32_sdwa v204, 1, v177 dst_sel:DWORD dst_unused:UNUSED_PAD src0_sel:DWORD src1_sel:WORD_1\n"
    "v_lshlrev_b32_sdwa v189, 1, v178 dst_sel:DWORD dst_unused:UNUSED_PAD src0_sel:DWORD src1_sel:WORD_1\n"
    "v_lshlrev_b32_sdwa v205, 1, v179 dst_sel:DWORD dst_unused:UNUSED_PAD src0_sel:DWORD src1_sel:WORD_1\n"
    "v_lshlrev_b32_sdwa v190, 1, v180 dst_sel:DWORD dst_unused:UNUSED_PAD src0_sel:DWORD src1_sel:WORD_1\n"
    "v_lshlrev_b32_sdwa v206, 1, v181 dst_sel:DWORD dst_unused:UNUSED_PAD src0_sel:DWORD src1_sel:WORD_1\n"
    "v_lshlrev_b32_sdwa v191, 1, v182 dst_sel:DWORD dst_unused:UNUSED_PAD src0_sel:DWORD src1_sel:WORD_1\n"
    "v_fma_mix_f32 v234, v192, v232, 0 op_sel_hi:[1,1,0]\n"
    "s_waitcnt lgkmcnt(6)\n"
    "v_mfma_f32_16x16x32_bf16 v[172:175], a[252:255], a[232:235], v[172:175]\n"
    "s_nop 0\n"
    "ds_read_u16 v186, v186\n"
    "ds_write2_b32 v235, v90, v91 offset0:40 offset1:60\n"
    "v_add_u32_e32 v235, -1360, v235\n"
    "v_or_b32_e32 v199, v199, v207\n"
    "v_lshlrev_b32_sdwa v207, 1, v183 dst_sel:DWORD dst_unused:UNUSED_PAD src0_sel:DWORD src1_sel:WORD_1\n"
    "v_fma_mix_f32 v232, v192, v232, 0 op_sel:[1,1,0] op_sel_hi:[1,1,0]\n"
    "v_mfma_f32_16x16x32_bf16 v[176:179], a[120:123], a[236:239], 0\n"
    "s_waitcnt lgkmcnt(6)\n"
    "ds_read_b128 v[236:239], v244 offset:12288\n"
    "buffer_store_dwordx4 v[148:151], v245, s[44:47], s88 offen offset:64 sc1\n"
    "v_cvt_pk_bf16_f32 v192, v234, v232\n"
    "v_mfma_f32_16x16x32_bf16 v[176:179], a[168:171], a[240:243], v[176:179]\n"
    "ds_read_u16_d16_hi v202, v202\n"
    "ds_write2_b32 v248, v72, v73 offset0:0 offset1:20\n"
    "v_mfma_f32_16x16x32_bf16 v[176:179], a[76:79], a[244:247], v[176:179]\n"
    "v_pk_fma_f16 v232, v196, v196, s48\n"
    "v_or_b32_e32 v184, v184, v200\n"
    "v_mfma_f32_16x16x32_bf16 v[180:183], a[124:127], a[236:239], 0\n"
    "s_waitcnt lgkmcnt(5)\n"
    "ds_read_u16 v187, v187\n"
    "ds_write2_b32 v248, v74, v75 offset0:40 offset1:60\n"
    "v_add_u32_e32 v248, 1360, v248\n"
    "v_pk_mul_f16 v232, v196, v232\n"
    "v_mfma_f32_16x16x32_bf16 v[180:183], a[172:175], a[240:243], v[180:183]\n"
    "v_exp_f16_e64 v234, v232\n"
    "v_mfma_f32_16x16x32_bf16 v[180:183], a[252:255], a[244:247], v[180:183]\n"
    "ds_read_b128 v[164:167], v244 offset:14336\n"
    "ds_write2_b32 v248, v76, v77 offset0:0 offset1:20\n"
    "v_exp_f16_sdwa v234, v232 dst_sel:WORD_1 dst_unused:UNUSED_PRESERVE src0_sel:WORD_1\n"
    "v_mfma_f32_16x16x32_bf16 v[96:99], a[176:179], v[192:195], v[96:99]\n"
    "v_pk_fma_f16 v234, v234, s49, s49\n"
    "v_or_b32_e64 v185, v185, v201\n"
    "v_mfma_f32_16x16x32_bf16 v[100:103], a[180:183], v[192:195], v[100:103]\n"
    "ds_read_u16_d16_hi v203, v203\n"
    "ds_read_u16 v188, v188\n"
    "v_rcp_f16_e32 v232, v234\n"
    "v_mfma_f32_16x16x32_bf16 v[112:115], a[184:187], v[192:195], v[112:115]\n"
    "s_waitcnt lgkmcnt(6)\n"
    "ds_read_u16_d16_hi v204, v204\n"
    "ds_read_u16 v189, v189\n"
    "v_rcp_f16_sdwa v232, v234 dst_sel:WORD_1 dst_unused:UNUSED_PRESERVE src0_sel:WORD_1\n"
    "v_mfma_f32_16x16x32_bf16 v[116:119], a[188:191], v[192:195], v[116:119]\n"
    "ds_read_u16_d16_hi v205, v205\n"
    "v_fma_mix_f32 v234, v196, v232, 0 op_sel_hi:[1,1,0]\n"
    "v_mfma_f32_16x16x32_bf16 v[128:131], v[236:239], v[192:195], v[128:131]\n"
    "v_fma_mix_f32 v232, v196, v232, 0 op_sel:[1,1,0] op_sel_hi:[1,1,0]\n"
    "v_lshlrev_b32_sdwa v201, 1, v171 dst_sel:DWORD dst_unused:UNUSED_PAD src0_sel:DWORD src1_sel:WORD_1\n"
    "v_cvt_pk_bf16_f32 v196, v234, v232\n"
    "v_or_b32_e32 v186, v186, v202\n"
    "s_waitcnt lgkmcnt(6)\n"
    "v_mfma_f32_16x16x32_bf16 v[132:135], v[164:167], v[192:195], v[132:135]\n"
    "v_cvt_pk_f16_f32 v192, v168, v169\n"
    "v_lshlrev_b32_sdwa v193, 1, v170 dst_sel:DWORD dst_unused:UNUSED_PAD src0_sel:DWORD src1_sel:WORD_1\n"
    "v_mfma_f32_16x16x32_bf16 v[104:107], a[176:179], v[196:199], v[104:107]\n"
    "ds_write2_b32 v248, v78, v79 offset0:40 offset1:60\n"
    "v_mfma_f32_16x16x32_bf16 v[108:111], a[180:183], v[196:199], v[108:111]\n"
    "v_add_u32_e32 v248, 1360, v248\n"
    "v_lshlrev_b32_sdwa v194, 1, v172 dst_sel:DWORD dst_unused:UNUSED_PAD src0_sel:DWORD src1_sel:WORD_1\n"
    "v_mfma_f32_16x16x32_bf16 v[120:123], a[184:187], v[196:199], v[120:123]\n"
    "ds_read_b128 a[224:227], v243 offset:0\n"
    "ds_write2_b32 v248, v80, v81 offset0:0 offset1:20\n"
    "v_mfma_f32_16x16x32_bf16 v[124:127], a[188:191], v[196:199], v[124:127]\n"
    "s_waitcnt lgkmcnt(7)\n"
    "buffer_store_dwordx4 v[160:163], v245, s[44:47], s88 offen offset:128 sc1\n"
    "ds_read_u16 v190, v190\n"
    "v_mfma_f32_16x16x32_bf16 v[136:139], v[236:239], v[196:199], v[136:139]\n"
    "ds_write2_b32 v248, v82, v83 offset0:40 offset1:60\n"
    "v_mfma_f32_32x32x1_2b_f32 v[64:95], v254, v254, 0\n"
    "s_waitcnt lgkmcnt(7)\n"
    "v_add_u32_e32 v248, -5440, v248\n"
    "v_lshlrev_b32_sdwa v202, 1, v173 dst_sel:DWORD dst_unused:UNUSED_PAD src0_sel:DWORD src1_sel:WORD_1\n"
    "v_lshlrev_b32_sdwa v195, 1, v174 dst_sel:DWORD dst_unused:UNUSED_PAD src0_sel:DWORD src1_sel:WORD_1\n"
    "v_or_b32_e64 v187, v187, v203\n"
    "ds_read_b128 a[228:231], v243 offset:1024\n"
    "ds_read_u16_d16_hi v206, v206\n"
    "v_lshlrev_b32_sdwa v203, 1, v175 dst_sel:DWORD dst_unused:UNUSED_PAD src0_sel:DWORD src1_sel:WORD_1\n"
    "v_or_b32_e32 v188, v188, v204\n"
    "ds_read_b128 a[232:235], v243 offset:2048\n"
    "ds_read_u16 v191, v191\n"
    "v_mfma_f32_16x16x32_bf16 v[140:143], v[164:167], v[196:199], v[140:143]\n"
    "s_waitcnt lgkmcnt(7)\n"
    "ds_read_b128 a[236:239], v243 offset:3072\n"
    "ds_read_u16_d16_hi v207, v207\n"
    "v_mfma_f32_16x16x32_bf16 v[168:171], a[80:83], a[224:227], 0\n"
    "v_cvt_pk_f16_f32 v196, v176, v177\n"
    "v_lshlrev_b32_sdwa v197, 1, v178 dst_sel:DWORD dst_unused:UNUSED_PAD src0_sel:DWORD src1_sel:WORD_1\n"
    "v_mfma_f32_16x16x32_bf16 v[172:175], a[84:87], a[224:227], 0\n"
    "ds_read_b128 a[240:243], v243 offset:4096\n"
    "ds_read_b128 a[244:247], v243 offset:5120\n"
    "v_lshlrev_b32_sdwa v198, 1, v180 dst_sel:DWORD dst_unused:UNUSED_PAD src0_sel:DWORD src1_sel:WORD_1\n"
    "v_lshlrev_b32_sdwa v199, 1, v182 dst_sel:DWORD dst_unused:UNUSED_PAD src0_sel:DWORD src1_sel:WORD_1\n"
    "v_or_b32_e64 v189, v189, v205\n"
    "v_lshlrev_b32_sdwa v205, 1, v179 dst_sel:DWORD dst_unused:UNUSED_PAD src0_sel:DWORD src1_sel:WORD_1\n"
    "v_pk_fma_f16 v232, v192, v192, s48\n"
    "v_pk_mul_f16 v232, v192, v232\n"
    "v_exp_f16_e64 v234, v232\n"
    "ds_read_b128 a[252:255], v244 offset:32768\n"
    "ds_read_u16 v193, v193\n"
    "s_waitcnt lgkmcnt(9)\n"
    "v_mfma_f32_16x16x32_bf16 v[168:171], a[128:131], a[228:231], v[168:171]\n"
    "s_nop 0\n"
    "ds_read_u16_d16_hi v201, v201\n"
    "v_exp_f16_sdwa v234, v232 dst_sel:WORD_1 dst_unused:UNUSED_PRESERVE src0_sel:WORD_1\n"
    "v_mfma_f32_16x16x32_bf16 v[172:175], a[132:135], a[228:231], v[172:175]\n"
    "v_pk_fma_f16 v234, v234, s49, s49\n"
    "v_rcp_f16_e32 v232, v234\n"
    "s_waitcnt lgkmcnt(8)\n"
    "v_mfma_f32_16x16x32_bf16 v[168:171], a[48:51], a[232:235], v[168:171]\n"
    "ds_read_u16 v194, v194\n"
    "v_or_b32_e32 v190, v190, v206\n"
    "v_mfma_f32_16x16x32_bf16 v[96:99], a[192:195], v[184:187], v[96:99]\n"
    "ds_read_u16_d16_hi v202, v202\n"
    "v_lshlrev_b32_sdwa v206, 1, v181 dst_sel:DWORD dst_unused:UNUSED_PAD src0_sel:DWORD src1_sel:WORD_1\n"
    "v_mfma_f32_16x16x32_bf16 v[100:103], a[196:199], v[184:187], v[100:103]\n"
    "s_waitcnt lgkmcnt(8)\n"
    "buffer_load_dwordx4 v[152:155], v245, s[40:43], s58 offen offset:64\n"
    "buffer_load_dwordx4 v[144:147], v245, s[40:43], s58 offen offset:0\n"
    "ds_read_b128 v[236:239], v244 offset:16384\n"
    "v_mfma_f32_16x16x32_bf16 v[176:179], a[80:83], a[236:239], 0\n"
    "ds_read_u16 v195, v195\n"
    "v_lshlrev_b32_sdwa v200, 1, v169 dst_sel:DWORD dst_unused:UNUSED_PAD src0_sel:DWORD src1_sel:WORD_1\n"
    "v_mfma_f32_16x16x32_bf16 v[112:115], a[200:203], v[184:187], v[112:115]\n"
    "s_waitcnt lgkmcnt(8)\n"
    "v_or_b32_e32 v191, v191, v207\n"
    "v_lshlrev_b32_sdwa v207, 1, v183 dst_sel:DWORD dst_unused:UNUSED_PAD src0_sel:DWORD src1_sel:WORD_1\n"
    "v_mfma_f32_16x16x32_bf16 v[176:179], a[128:131], a[240:243], v[176:179]\n"
    "s_waitcnt lgkmcnt(7)\n"
    "s_barrier\n"
    "v_mfma_f32_16x16x32_bf16 v[176:179], a[48:51], a[244:247], v[176:179]\n"
    "s_waitcnt lgkmcnt(6)\n"
    "ds_read_b128 v[208:211], v250 offset:0\n"
    "ds_read_b128 v[212:215], v250 offset:320\n"
    "v_mfma_f32_16x16x32_bf16 v[172:175], a[252:255], a[232:235], v[172:175]\n"
    "ds_read_b128 v[216:219], v251 offset:21504\n"
    "ds_read_b128 v[220:223], v251 offset:21840\n"
    "v_mfma_f32_16x16x32_bf16 v[180:183], a[84:87], a[236:239], 0\n"
    "s_waitcnt lgkmcnt(8)\n"
    "ds_read_u16_d16_hi v203, v203\n"
    "ds_read_b128 v[164:167], v244 offset:18432\n"
    "v_mfma_f32_16x16x32_bf16 v[180:183], a[132:135], a[240:243], v[180:183]\n"
    "ds_read_u16 v197, v197\n"
    "v_or_b32_e64 v193, v193, v201\n"
    "v_mfma_f32_16x16x32_bf16 v[180:183], a[252:255], a[244:247], v[180:183]\n"
    "v_lshlrev_b32_e64 v254, 3, v244\n"
    "v_lshlrev_b32_sdwa v201, 1, v171 dst_sel:DWORD dst_unused:UNUSED_PAD src0_sel:DWORD src1_sel:WORD_1\n"
    "v_mfma_f32_16x16x32_bf16 v[116:119], a[204:207], v[184:187], v[116:119]\n"
    "ds_read_u16_d16_hi v205, v205\n"
    "v_rcp_f16_sdwa v232, v234 dst_sel:WORD_1 dst_unused:UNUSED_PRESERVE src0_sel:WORD_1\n"
    "v_mfma_f32_16x16x32_bf16 v[104:107], a[192:195], v[188:191], v[104:107]\n"
    "s_waitcnt lgkmcnt(9)\n"
    "ds_read_u16 v198, v198\n"
    "v_or_b32_e32 v194, v194, v202\n"
    "v_mfma_f32_16x16x32_bf16 v[128:131], v[236:239], v[184:187], v[128:131]\n"
    "v_lshlrev_b32_sdwa v202, 1, v173 dst_sel:DWORD dst_unused:UNUSED_PAD src0_sel:DWORD src1_sel:WORD_1\n"
    "v_lshlrev_b32_sdwa v204, 1, v177 dst_sel:DWORD dst_unused:UNUSED_PAD src0_sel:DWORD src1_sel:WORD_1\n"
    "v_mfma_f32_16x16x32_bf16 v[108:111], a[196:199], v[188:191], v[108:111]\n"
    "buffer_load_dwordx4 v[156:159], v245, s[40:43], s89 offen offset:64\n"
    "buffer_load_dwordx4 v[148:151], v245, s[40:43], s89 offen offset:0\n"
    "ds_read_u16_d16_hi v206, v206\n"
    "v_mfma_f32_16x16x32_bf16 v[120:123], a[200:203], v[188:191], v[120:123]\n"
    "v_fma_mix_f32 v234, v192, v232, 0 op_sel_hi:[1,1,0]\n"
    "v_fma_mix_f32 v232, v192, v232, 0 op_sel:[1,1,0] op_sel_hi:[1,1,0]\n"
    "v_mfma_f32_16x16x32_bf16 v[124:127], a[204:207], v[188:191], v[124:127]\n"
    "s_waitcnt lgkmcnt(7)\n"
    "ds_read_u16 v199, v199\n"
    "v_cvt_pk_bf16_f32 v192, v234, v232\n"
    "v_smfmac_f32_16x16x64_bf16 v[64:67], v[216:219], v[208:215], v240\n"
    "s_waitcnt lgkmcnt(7)\n"
    "ds_read_b128 v[216:219], v252 offset:21504\n"
    "v_smfmac_f32_16x16x64_bf16 v[68:71], v[220:223], v[208:215], v240 abid:1\n"
    "ds_read_b128 v[208:211], v250 offset:640\n"
    "v_mfma_f32_16x16x32_bf16 v[136:139], v[236:239], v[188:191], v[136:139]\n"
    "ds_read_b128 v[212:215], v250 offset:960\n"
    "v_pk_fma_f16 v232, v196, v196, s48\n"
    "s_waitcnt lgkmcnt(8)\n"
    "v_mfma_f32_16x16x32_bf16 v[132:135], v[164:167], v[184:187], v[132:135]\n"
    "s_nop 0\n"
    "ds_read_b128 v[220:223], v252 offset:21840\n"
    "v_or_b32_e64 v195, v195, v203\n"
    "v_mfma_f32_16x16x32_bf16 v[140:143], v[164:167], v[188:191], v[140:143]\n"
    "ds_read_u16_d16_hi v207, v207\n"
    "v_lshlrev_b32_sdwa v184, 1, v168 dst_sel:DWORD dst_unused:UNUSED_PAD src0_sel:DWORD src1_sel:WORD_1\n"
    "v_lshlrev_b32_sdwa v185, 1, v170 dst_sel:DWORD dst_unused:UNUSED_PAD src0_sel:DWORD src1_sel:WORD_1\n"
    "v_mfma_f32_16x16x32_bf16 v[168:171], a[88:91], a[224:227], 0\n"
    "v_lshlrev_b32_sdwa v186, 1, v172 dst_sel:DWORD dst_unused:UNUSED_PAD src0_sel:DWORD src1_sel:WORD_1\n"
    "v_lshlrev_b32_sdwa v187, 1, v174 dst_sel:DWORD dst_unused:UNUSED_PAD src0_sel:DWORD src1_sel:WORD_1\n"
    "v_mfma_f32_16x16x32_bf16 v[168:171], a[136:139], a[228:231], v[168:171]\n"
    "s_mov_b32 exec_hi, 0\n"
    "buffer_load_dword v233, v254, s[72:75], s53 offen sc1\n"
    "s_mov_b32 exec_hi, -1\n"
    "s_add_u32 s53, s53, 393216\n"
    "v_mfma_f32_16x16x32_bf16 v[168:171], a[52:55], a[232:235], v[168:171]\n"
    "s_waitcnt lgkmcnt(8)\n"
    "ds_read_b128 a[252:255], v244 offset:36864\n"
    "ds_read_u16 v184, v184\n"
    "v_or_b32_e32 v197, v197, v205\n"
    "v_lshlrev_b32_sdwa v203, 1, v175 dst_sel:DWORD dst_unused:UNUSED_PAD src0_sel:DWORD src1_sel:WORD_1\n"
    "v_mfma_f32_16x16x32_bf16 v[172:175], a[92:95], a[224:227], 0\n"
    "ds_read_u16_d16_hi v200, v200\n"
    "v_lshlrev_b32_sdwa v188, 1, v176 dst_sel:DWORD dst_unused:UNUSED_PAD src0_sel:DWORD src1_sel:WORD_1\n"
    "v_mfma_f32_16x16x32_bf16 v[172:175], a[140:143], a[228:231], v[172:175]\n"
    "ds_read_u16 v185, v185\n"
    "s_waitcnt lgkmcnt(10)\n"
    "v_or_b32_e32 v198, v198, v206\n"
    "v_lshlrev_b32_sdwa v189, 1, v178 dst_sel:DWORD dst_unused:UNUSED_PAD src0_sel:DWORD src1_sel:WORD_1\n"
    "v_lshlrev_b32_sdwa v205, 1, v179 dst_sel:DWORD dst_unused:UNUSED_PAD src0_sel:DWORD src1_sel:WORD_1\n"
    "v_mfma_f32_16x16x32_bf16 v[176:179], a[88:91], a[236:239], 0\n"
    "ds_read_u16_d16_hi v201, v201\n"
    "v_lshlrev_b32_sdwa v190, 1, v180 dst_sel:DWORD dst_unused:UNUSED_PAD src0_sel:DWORD src1_sel:WORD_1\n"
    "v_mfma_f32_16x16x32_bf16 v[176:179], a[136:139], a[240:243], v[176:179]\n"
    "v_lshlrev_b32_sdwa v206, 1, v181 dst_sel:DWORD dst_unused:UNUSED_PAD src0_sel:DWORD src1_sel:WORD_1\n"
    "v_lshlrev_b32_sdwa v191, 1, v182 dst_sel:DWORD dst_unused:UNUSED_PAD src0_sel:DWORD src1_sel:WORD_1\n"
    "v_mfma_f32_16x16x32_bf16 v[176:179], a[52:55], a[244:247], v[176:179]\n"
    "s_waitcnt lgkmcnt(7)\n"
    "ds_read_u16 v186, v186\n"
    "v_pk_mul_f16 v232, v196, v232\n"
    "v_smfmac_f32_16x16x64_bf16 v[64:67], v[216:219], v[208:215], v240\n"
    "s_waitcnt lgkmcnt(7)\n"
    "v_exp_f16_e64 v234, v232\n"
    "v_smfmac_f32_16x16x64_bf16 v[68:71], v[220:223], v[208:215], v240 abid:1\n"
    "ds_read_b128 v[208:211], v250 offset:80\n"
    "ds_read_b128 v[212:215], v250 offset:400\n"
    "v_exp_f16_sdwa v234, v232 dst_sel:WORD_1 dst_unused:UNUSED_PRESERVE src0_sel:WORD_1\n"
    "v_mfma_f32_16x16x32_bf16 v[96:99], a[208:211], v[192:195], v[96:99]\n"
    "s_waitcnt lgkmcnt(7)\n"
    "ds_read_b128 v[216:219], v251 offset:22176\n"
    "v_or_b32_e32 v199, v199, v207\n"
    "ds_read_b128 v[220:223], v251 offset:22512\n"
    "v_pk_add_f32 v[224:225], v[224:225], v[64:65]\n"
    "v_pk_add_f32 v[226:227], v[226:227], v[66:67]\n"
    "v_pk_fma_f32 v[228:229], v[64:65], v[64:65], v[228:229]\n"
    "v_pk_fma_f32 v[230:231], v[66:67], v[66:67], v[230:231]\n"
    "v_pk_add_f32 v[224:225], v[224:225], v[68:69]\n"
    "v_pk_add_f32 v[226:227], v[226:227], v[70:71]\n"
    "v_pk_fma_f32 v[228:229], v[68:69], v[68:69], v[228:229]\n"
    "v_pk_fma_f32 v[230:231], v[70:71], v[70:71], v[230:231]\n"
    "v_mfma_f32_16x16x32_bf16 v[172:175], a[252:255], a[232:235], v[172:175]\n"
    "ds_read_b128 v[236:239], v244 offset:20480\n"
    "ds_read_u16_d16_hi v202, v202\n"
    "v_lshlrev_b32_sdwa v207, 1, v183 dst_sel:DWORD dst_unused:UNUSED_PAD src0_sel:DWORD src1_sel:WORD_1\n"
    "v_mfma_f32_16x16x32_bf16 v[180:183], a[92:95], a[236:239], 0\n"
    "s_waitcnt lgkmcnt(9)\n"
    "ds_read_u16 v187, v187\n"
    "v_pk_fma_f16 v234, v234, s49, s49\n"
    "v_mfma_f32_16x16x32_bf16 v[180:183], a[140:143], a[240:243], v[180:183]\n"
    "s_nop 0\n"
    "ds_read_b128 v[164:167], v244 offset:22528\n"
    "v_rcp_f16_e32 v232, v234\n"
    "v_mfma_f32_16x16x32_bf16 v[180:183], a[252:255], a[244:247], v[180:183]\n"
    "ds_read_u16_d16_hi v203, v203\n"
    "v_rcp_f16_sdwa v232, v234 dst_sel:WORD_1 dst_unused:UNUSED_PRESERVE src0_sel:WORD_1\n"
    "v_mfma_f32_16x16x32_bf16 v[100:103], a[212:215], v[192:195], v[100:103]\n"
    "s_waitcnt lgkmcnt(10)\n"
    "v_fma_mix_f32 v234, v196, v232, 0 op_sel_hi:[1,1,0]\n"
    "v_fma_mix_f32 v232, v196, v232, 0 op_sel:[1,1,0] op_sel_hi:[1,1,0]\n"
    "v_mfma_f32_16x16x32_bf16 v[112:115], a[216:219], v[192:195], v[112:115]\n"
    "ds_read_u16 v188, v188\n"
    "v_cvt_pk_bf16_f32 v196, v234, v232\n"
    "v_mfma_f32_16x16x32_bf16 v[116:119], a[220:223], v[192:195], v[116:119]\n"
    "ds_read_u16_d16_hi v204, v204\n"
    "v_or_b32_e64 v184, v184, v200\n"
    "v_mfma_f32_16x16x32_bf16 v[104:107], a[208:211], v[196:199], v[104:107]\n"
    "s_waitcnt lgkmcnt(8)\n"
    "v_or_b32_e32 v185, v185, v201\n"
    "v_lshlrev_b32_sdwa v201, 1, v171 dst_sel:DWORD dst_unused:UNUSED_PAD src0_sel:DWORD src1_sel:WORD_1\n"
    "v_smfmac_f32_16x16x64_bf16 v[72:75], v[216:219], v[208:215], v240\n"
    "s_waitcnt lgkmcnt(7)\n"
    "ds_read_b128 v[216:219], v252 offset:22176\n"
    "v_smfmac_f32_16x16x64_bf16 v[76:79], v[220:223], v[208:215], v240 abid:1\n"
    "s_waitcnt lgkmcnt(7)\n"
    "ds_read_b128 v[208:211], v250 offset:720\n"
    "v_mfma_f32_16x16x32_bf16 v[128:131], v[236:239], v[192:195], v[128:131]\n"
    "ds_read_b128 v[212:215], v250 offset:1040\n"
    "v_cvt_pk_bf16_f32 v96, v96, v97\n"
    "v_mfma_f32_16x16x32_bf16 v[108:111], a[212:215], v[196:199], v[108:111]\n"
    "ds_read_b128 v[220:223], v252 offset:22512\n"
    "v_lshlrev_b32_e32 v254, 3, v244\n"
    "v_mfma_f32_16x16x32_bf16 v[120:123], a[216:219], v[196:199], v[120:123]\n"
    "s_waitcnt lgkmcnt(9)\n"
    "ds_read_u16 v189, v189\n"
    "v_or_b32_e32 v186, v186, v202\n"
    "v_mfma_f32_16x16x32_bf16 v[124:127], a[220:223], v[196:199], v[124:127]\n"
    "v_lshlrev_b32_sdwa v202, 1, v173 dst_sel:DWORD dst_unused:UNUSED_PAD src0_sel:DWORD src1_sel:WORD_1\n"
    "v_cvt_pk_bf16_f32 v97, v98, v99\n"
    "v_mfma_f32_16x16x32_bf16 v[136:139], v[236:239], v[196:199], v[136:139]\n"
    "s_waitcnt lgkmcnt(8)\n"
    "ds_read_u16_d16_hi v205, v205\n"
    "v_cvt_pk_bf16_f32 v98, v100, v101\n"
    "v_mfma_f32_16x16x32_bf16 v[132:135], v[164:167], v[192:195], v[132:135]\n"
    "v_cvt_pk_f16_f32 v192, v168, v169\n"
    "v_lshlrev_b32_sdwa v193, 1, v170 dst_sel:DWORD dst_unused:UNUSED_PAD src0_sel:DWORD src1_sel:WORD_1\n"
    "v_mfma_f32_16x16x32_bf16 v[140:143], v[164:167], v[196:199], v[140:143]\n"
    "s_nop 0\n"
    "ds_read_u16 v190, v190\n"
    "v_lshlrev_b32_sdwa v194, 1, v172 dst_sel:DWORD dst_unused:UNUSED_PAD src0_sel:DWORD src1_sel:WORD_1\n"
    "v_mfma_f32_16x16x32_bf16 v[168:171], a[96:99], a[224:227], 0\n"
    "s_waitcnt lgkmcnt(9)\n"
    "ds_read_u16_d16_hi v206, v206\n"
    "v_lshlrev_b32_sdwa v195, 1, v174 dst_sel:DWORD dst_unused:UNUSED_PAD src0_sel:DWORD src1_sel:WORD_1\n"
    "v_mfma_f32_16x16x32_bf16 v[168:171], a[144:147], a[228:231], v[168:171]\n"
    "v_cvt_pk_f16_f32 v196, v176, v177\n"
    "v_lshlrev_b32_sdwa v197, 1, v178 dst_sel:DWORD dst_unused:UNUSED_PAD src0_sel:DWORD src1_sel:WORD_1\n"
    "v_mfma_f32_16x16x32_bf16 v[168:171], a[56:59], a[232:235], v[168:171]\n"
    "ds_read_u16 v191, v191\n"
    "v_lshlrev_b32_sdwa v198, 1, v180 dst_sel:DWORD dst_unused:UNUSED_PAD src0_sel:DWORD src1_sel:WORD_1\n"
    "v_lshlrev_b32_sdwa v199, 1, v182 dst_sel:DWORD dst_unused:UNUSED_PAD src0_sel:DWORD src1_sel:WORD_1\n"
    "v_or_b32_e32 v187, v187, v203\n"
    "v_lshlrev_b32_sdwa v203, 1, v175 dst_sel:DWORD dst_unused:UNUSED_PAD src0_sel:DWORD src1_sel:WORD_1\n"
    "v_mfma_f32_16x16x32_bf16 v[172:175], a[100:103], a[224:227], 0\n"
    "s_waitcnt lgkmcnt(7)\n"
    "ds_read_u16_d16_hi v207, v207\n"
    "v_or_b32_e32 v188, v188, v204\n"
    "v_mfma_f32_16x16x32_bf16 v[172:175], a[148:151], a[228:231], v[172:175]\n"
    "s_waitcnt lgkmcnt(7)\n"
    "v_cvt_pk_bf16_f32 v99, v102, v103\n"
    "v_cvt_pk_bf16_f32 v112, v112, v113\n"
    "v_smfmac_f32_16x16x64_bf16 v[72:75], v[216:219], v[208:215], v240\n"
    "s_waitcnt lgkmcnt(6)\n"
    "s_mov_b32 exec_hi, 0\n"
    "buffer_load_dword v233, v254, s[72:75], s53 offen sc1\n"
    "s_mov_b32 exec_hi, -1\n"
    "s_add_u32 s53, s53, 393216\n"
    "v_smfmac_f32_16x16x64_bf16 v[76:79], v[220:223], v[208:215], v240 abid:1\n"
    "s_nop 0\n"
    "ds_write_b128 v242, v[96:99] offset:24640\n"
    "v_mfma_f32_16x16x32_bf16 v[172:175], a[60:63], a[232:235], v[172:175]\n"
    "v_cvt_pk_bf16_f32 v113, v114, v115\n"
    "v_cvt_pk_bf16_f32 v114, v116, v117\n"
    "v_cvt_pk_bf16_f32 v115, v118, v119\n"
    "v_cvt_pk_bf16_f32 v128, v128, v129\n"
    "s_waitcnt lgkmcnt(5)\n"
    "v_or_b32_e32 v189, v189, v205\n"
    "v_pk_add_f32 v[224:225], v[224:225], v[72:73]\n"
    "v_pk_add_f32 v[226:227], v[226:227], v[74:75]\n"
    "v_pk_fma_f32 v[228:229], v[72:73], v[72:73], v[228:229]\n"
    "v_pk_fma_f32 v[230:231], v[74:75], v[74:75], v[230:231]\n"
    "v_pk_add_f32 v[224:225], v[224:225], v[76:77]\n"
    "v_pk_add_f32 v[226:227], v[226:227], v[78:79]\n"
    "v_pk_fma_f32 v[228:229], v[76:77], v[76:77], v[228:229]\n"
    "v_pk_fma_f32 v[230:231], v[78:79], v[78:79], v[230:231]\n"
    "ds_read_b128 v[208:211], v250 offset:160\n"
    "ds_read_b128 v[212:215], v250 offset:480\n"
    "ds_write_b128 v242, v[112:115] offset:26688\n"
    "v_lshlrev_b32_sdwa v205, 1, v179 dst_sel:DWORD dst_unused:UNUSED_PAD src0_sel:DWORD src1_sel:WORD_1\n"
    "v_mfma_f32_16x16x32_bf16 v[176:179], a[96:99], a[236:239], 0\n"
    "s_waitcnt lgkmcnt(5)\n"
    "v_or_b32_e32 v190, v190, v206\n"
    "v_lshlrev_b32_sdwa v206, 1, v181 dst_sel:DWORD dst_unused:UNUSED_PAD src0_sel:DWORD src1_sel:WORD_1\n"
    "v_mfma_f32_16x16x32_bf16 v[176:179], a[144:147], a[240:243], v[176:179]\n"
    "s_nop 0\n"
    "v_cvt_pk_bf16_f32 v129, v130, v131\n"
    "v_cvt_pk_bf16_f32 v130, v132, v133\n"
    "v_mfma_f32_16x16x32_bf16 v[176:179], a[56:59], a[244:247], v[176:179]\n"
    "s_waitcnt lgkmcnt(3)\n"
    "ds_read_b128 v[216:219], v251 offset:22848\n"
    "ds_read_b128 v[220:223], v251 offset:23184\n"
    "v_mfma_f32_16x16x32_bf16 v[96:99], a[0:3], v[184:187], 0\n"
    "ds_read_u16 v193, v193\n"
    "ds_read_u16_d16_hi v201, v201\n"
    "v_mfma_f32_16x16x32_bf16 v[100:103], a[4:7], v[184:187], 0\n"
    "ds_read_u16 v194, v194\n"
    "v_or_b32_e64 v191, v191, v207\n"
    "v_mfma_f32_16x16x32_bf16 v[112:115], a[8:11], v[184:187], 0\n"
    "v_lshlrev_b32_sdwa v207, 1, v183 dst_sel:DWORD dst_unused:UNUSED_PAD src0_sel:DWORD src1_sel:WORD_1\n"
    "v_cvt_pk_bf16_f32 v131, v134, v135\n"
    "v_mfma_f32_16x16x32_bf16 v[180:183], a[100:103], a[236:239], 0\n"
    "v_cvt_pk_bf16_f32 v104, v104, v105\n"
    "v_cvt_pk_bf16_f32 v105, v106, v107\n"
    "v_mfma_f32_16x16x32_bf16 v[180:183], a[148:151], a[240:243], v[180:183]\n"
    "ds_write_b128 v242, v[128:131] offset:28736\n"
    "v_mfma_f32_16x16x32_bf16 v[180:183], a[60:63], a[244:247], v[180:183]\n"
    "v_cvt_pk_bf16_f32 v106, v108, v109\n"
    "v_cvt_pk_bf16_f32 v107, v110, v111\n"
    "v_mfma_f32_16x16x32_bf16 v[116:119], a[12:15], v[184:187], 0\n"
    "v_cvt_pk_bf16_f32 v120, v120, v121\n"
    "v_cvt_pk_bf16_f32 v121, v122, v123\n"
    "v_mfma_f32_16x16x32_bf16 v[108:111], a[4:7], v[188:191], 0\n"
    "s_waitcnt lgkmcnt(5)\n"
    "ds_read_u16_d16_hi v202, v202\n"
    "ds_write_b128 v242, v[104:107] offset:25664\n"
    "v_smfmac_f32_16x16x64_bf16 v[80:83], v[216:219], v[208:215], v240\n"
    "s_waitcnt lgkmcnt(6)\n"
    "v_cvt_pk_bf16_f32 v122, v124, v125\n"
    "v_smfmac_f32_16x16x64_bf16 v[84:87], v[220:223], v[208:215], v240 abid:1\n"
    "v_cvt_pk_bf16_f32 v123, v126, v127\n"
    "v_mfma_f32_16x16x32_bf16 v[104:107], a[0:3], v[188:191], 0\n"
    "ds_read_b128 v[208:211], v250 offset:800\n"
    "ds_write_b128 v242, v[120:123] offset:27712\n"
    "v_mfma_f32_16x16x32_bf16 v[120:123], a[8:11], v[188:191], 0\n"
    "s_waitcnt lgkmcnt(6)\n"
    "v_or_b32_e32 v193, v193, v201\n"
    "v_add_u32_e64 v254, s54, v242\n"
    "v_cvt_pk_bf16_f32 v136, v136, v137\n"
    "v_pk_fma_f16 v232, v192, v192, s48\n"
    "v_mfma_f32_16x16x32_bf16 v[124:127], a[12:15], v[188:191], 0\n"
    "v_cvt_pk_bf16_f32 v137, v138, v139\n"
    "ds_read_b128 v[212:215], v250 offset:1120\n"
    "ds_read_b128 v[216:219], v252 offset:22848\n"
    "ds_read_b128 v[220:223], v252 offset:23184\n"
    "v_cvt_pk_bf16_f32 v138, v140, v141\n"
    "v_cvt_pk_bf16_f32 v139, v142, v143\n"
    "ds_write_b128 v242, v[136:139] offset:29760\n"
    "s_waitcnt lgkmcnt(6)\n"
    "v_or_b32_e32 v194, v194, v202\n"
    "v_pk_mul_f16 v232, v192, v232\n"
    "v_lshlrev_b32_sdwa v200, 1, v169 dst_sel:DWORD dst_unused:UNUSED_PAD src0_sel:DWORD src1_sel:WORD_1\n"
    "v_exp_f16_e64 v234, v232\n"
    "ds_read_b128 v[236:239], v244 offset:0\n"
    "ds_read_u16 v195, v195\n"
    "ds_read_u16_d16_hi v203, v203\n"
    "v_exp_f16_sdwa v234, v232 dst_sel:WORD_1 dst_unused:UNUSED_PRESERVE src0_sel:WORD_1\n"
    "ds_read_b128 v[164:167], v244 offset:2048\n"
    "ds_read_u16 v197, v197\n"
    "v_pk_fma_f16 v234, v234, s49, s49\n"
    "v_lshlrev_b32_sdwa v201, 1, v171 dst_sel:DWORD dst_unused:UNUSED_PAD src0_sel:DWORD src1_sel:WORD_1\n"
    "v_lshlrev_b32_sdwa v202, 1, v173 dst_sel:DWORD dst_unused:UNUSED_PAD src0_sel:DWORD src1_sel:WORD_1\n"
    "v_rcp_f16_e64 v232, v234\n"
    "s_waitcnt lgkmcnt(7)\n"
    "v_smfmac_f32_16x16x64_bf16 v[80:83], v[216:219], v[208:215], v240\n"
    "s_waitcnt lgkmcnt(6)\n"
    "ds_read_b128 v[216:219], v251 offset:23520\n"
    "ds_read_u16_d16_hi v205, v205\n"
    "v_smfmac_f32_16x16x64_bf16 v[84:87], v[220:223], v[208:215], v240 abid:1\n"
    "ds_read_b128 v[208:211], v250 offset:240\n"
    "v_rcp_f16_sdwa v232, v234 dst_sel:WORD_1 dst_unused:UNUSED_PRESERVE src0_sel:WORD_1\n"
    "ds_read_b128 v[212:215], v250 offset:560\n"
    "v_lshlrev_b32_sdwa v204, 1, v177 dst_sel:DWORD dst_unused:UNUSED_PAD src0_sel:DWORD src1_sel:WORD_1\n"
    "v_pk_add_f32 v[224:225], v[224:225], v[80:81]\n"
    "v_pk_add_f32 v[226:227], v[226:227], v[82:83]\n"
    "v_pk_fma_f32 v[228:229], v[80:81], v[80:81], v[228:229]\n"
    "v_pk_fma_f32 v[230:231], v[82:83], v[82:83], v[230:231]\n"
    "v_pk_add_f32 v[224:225], v[224:225], v[84:85]\n"
    "v_pk_add_f32 v[226:227], v[226:227], v[86:87]\n"
    "v_pk_fma_f32 v[228:229], v[84:85], v[84:85], v[228:229]\n"
    "v_pk_fma_f32 v[230:231], v[86:87], v[86:87], v[230:231]\n"
    "s_waitcnt lgkmcnt(8)\n"
    "v_mfma_f32_16x16x32_bf16 v[128:131], v[236:239], v[184:187], 0\n"
    "s_nop 0\n"
    "ds_read_b128 v[220:223], v251 offset:23856\n"
    "ds_read_u16 v198, v198\n"
    "v_mfma_f32_16x16x32_bf16 v[136:139], v[236:239], v[188:191], 0\n"
    "ds_read_u16_d16_hi v206, v206\n"
    "v_fma_mix_f32 v234, v192, v232, 0 op_sel_hi:[1,1,0]\n"
    "v_fma_mix_f32 v232, v192, v232, 0 op_sel:[1,1,0] op_sel_hi:[1,1,0]\n"
    "s_waitcnt lgkmcnt(8)\n"
    "v_mfma_f32_16x16x32_bf16 v[132:135], v[164:167], v[184:187], 0\n"
    "s_nop 0\n"
    "ds_read_u16 v199, v199\n"
    "v_or_b32_e64 v195, v195, v203\n"
    "v_mfma_f32_16x16x32_bf16 v[140:143], v[164:167], v[188:191], 0\n"
    "ds_read_u16_d16_hi v207, v207\n"
    "v_lshlrev_b32_sdwa v184, 1, v168 dst_sel:DWORD dst_unused:UNUSED_PAD src0_sel:DWORD src1_sel:WORD_1\n"
    "v_lshlrev_b32_sdwa v185, 1, v170 dst_sel:DWORD dst_unused:UNUSED_PAD src0_sel:DWORD src1_sel:WORD_1\n"
    "v_mfma_f32_16x16x32_bf16 v[168:171], a[104:107], a[224:227], 0\n"
    "v_lshlrev_b32_sdwa v186, 1, v172 dst_sel:DWORD dst_unused:UNUSED_PAD src0_sel:DWORD src1_sel:WORD_1\n"
    "v_lshlrev_b32_sdwa v187, 1, v174 dst_sel:DWORD dst_unused:UNUSED_PAD src0_sel:DWORD src1_sel:WORD_1\n"
    "v_mfma_f32_16x16x32_bf16 v[168:171], a[152:155], a[228:231], v[168:171]\n"
    "ds_read_u16 v184, v184\n"
    "v_lshlrev_b32_sdwa v203, 1, v175 dst_sel:DWORD dst_unused:UNUSED_PAD src0_sel:DWORD src1_sel:WORD_1\n"
    "v_mfma_f32_16x16x32_bf16 v[168:171], a[64:67], a[232:235], v[168:171]\n"
    "v_lshlrev_b32_sdwa v188, 1, v176 dst_sel:DWORD dst_unused:UNUSED_PAD src0_sel:DWORD src1_sel:WORD_1\n"
    "v_lshlrev_b32_sdwa v189, 1, v178 dst_sel:DWORD dst_unused:UNUSED_PAD src0_sel:DWORD src1_sel:WORD_1\n"
    "v_mfma_f32_16x16x32_bf16 v[172:175], a[108:111], a[224:227], 0\n"
    "s_waitcnt lgkmcnt(7)\n"
    "ds_read_u16_d16_hi v200, v200\n"
    "v_or_b32_e32 v197, v197, v205\n"
    "v_mfma_f32_16x16x32_bf16 v[172:175], a[156:159], a[228:231], v[172:175]\n"
    "s_waitcnt lgkmcnt(7)\n"
    "ds_read_u16 v185, v185\n"
    "v_lshlrev_b32_sdwa v205, 1, v179 dst_sel:DWORD dst_unused:UNUSED_PAD src0_sel:DWORD src1_sel:WORD_1\n"
    "v_smfmac_f32_16x16x64_bf16 v[88:91], v[216:219], v[208:215], v240\n"
    "s_waitcnt lgkmcnt(7)\n"
    "v_lshlrev_b32_sdwa v190, 1, v180 dst_sel:DWORD dst_unused:UNUSED_PAD src0_sel:DWORD src1_sel:WORD_1\n"
    "v_smfmac_f32_16x16x64_bf16 v[92:95], v[220:223], v[208:215], v240 abid:1\n"
    "ds_read_b128 v[208:211], v250 offset:880\n"
    "v_mfma_f32_16x16x32_bf16 v[172:175], a[68:71], a[232:235], v[172:175]\n"
    "ds_read_b128 v[212:215], v250 offset:1200\n"
    "v_lshlrev_b32_sdwa v191, 1, v182 dst_sel:DWORD dst_unused:UNUSED_PAD src0_sel:DWORD src1_sel:WORD_1\n"
    "v_mfma_f32_16x16x32_bf16 v[176:179], a[104:107], a[236:239], 0\n"
    "ds_read_b128 v[216:219], v252 offset:23520\n"
    "v_cvt_pk_bf16_f32 v192, v234, v232\n"
    "v_mfma_f32_16x16x32_bf16 v[176:179], a[152:155], a[240:243], v[176:179]\n"
    "ds_read_b128 v[220:223], v252 offset:23856\n"
    "v_pk_fma_f16 v232, v196, v196, s48\n"
    "v_mfma_f32_16x16x32_bf16 v[176:179], a[64:67], a[244:247], v[176:179]\n"
    "s_waitcnt lgkmcnt(9)\n"
    "ds_read_u16_d16_hi v201, v201\n"
    "v_or_b32_e32 v198, v198, v206\n"
    "v_lshlrev_b32_sdwa v206, 1, v181 dst_sel:DWORD dst_unused:UNUSED_PAD src0_sel:DWORD src1_sel:WORD_1\n"
    "v_pk_mul_f16 v232, v196, v232\n"
    "v_exp_f16_e64 v234, v232\n"
    "ds_read_u16 v186, v186\n"
    "v_exp_f16_sdwa v234, v232 dst_sel:WORD_1 dst_unused:UNUSED_PRESERVE src0_sel:WORD_1\n"
    "v_mfma_f32_16x16x32_bf16 v[96:99], a[16:19], v[192:195], v[96:99]\n"
    "s_waitcnt lgkmcnt(9)\n"
    "ds_read_b128 v[236:239], v244 offset:4096\n"
    "v_or_b32_e32 v199, v199, v207\n"
    "v_lshlrev_b32_sdwa v207, 1, v183 dst_sel:DWORD dst_unused:UNUSED_PAD src0_sel:DWORD src1_sel:WORD_1\n"
    "v_mfma_f32_16x16x32_bf16 v[180:183], a[108:111], a[236:239], 0\n"
    "ds_read_u16_d16_hi v202, v202\n"
    "v_pk_fma_f16 v234, v234, s49, s49\n"
    "v_mfma_f32_16x16x32_bf16 v[180:183], a[156:159], a[240:243], v[180:183]\n"
    "v_rcp_f16_e32 v232, v234\n"
    "v_mfma_f32_16x16x32_bf16 v[180:183], a[68:71], a[244:247], v[180:183]\n"
    "s_waitcnt lgkmcnt(9)\n"
    "ds_read_u16 v187, v187\n"
    "v_rcp_f16_sdwa v232, v234 dst_sel:WORD_1 dst_unused:UNUSED_PRESERVE src0_sel:WORD_1\n"
    "v_mfma_f32_16x16x32_bf16 v[100:103], a[20:23], v[192:195], v[100:103]\n"
    "ds_read_b128 v[164:167], v244 offset:6144\n"
    "v_fma_mix_f32 v234, v196, v232, 0 op_sel_hi:[1,1,0]\n"
    "v_mfma_f32_16x16x32_bf16 v[112:115], a[24:27], v[192:195], v[112:115]\n"
    "ds_read_u16_d16_hi v203, v203\n"
    "v_fma_mix_f32 v232, v196, v232, 0 op_sel:[1,1,0] op_sel_hi:[1,1,0]\n"
    "v_mfma_f32_16x16x32_bf16 v[116:119], a[28:31], v[192:195], v[116:119]\n"
    "s_waitcnt lgkmcnt(8)\n"
    "v_cvt_pk_bf16_f32 v196, v234, v232\n"
    "v_or_b32_e64 v184, v184, v200\n"
    "v_smfmac_f32_16x16x64_bf16 v[88:91], v[216:219], v[208:215], v240\n"
    "s_waitcnt lgkmcnt(7)\n"
    "ds_read_u16 v188, v188\n"
    "v_smfmac_f32_16x16x64_bf16 v[92:95], v[220:223], v[208:215], v240 abid:1\n"
    "v_mfma_f32_16x16x32_bf16 v[104:107], a[16:19], v[196:199], v[104:107]\n"
    "ds_read_u16_d16_hi v204, v204\n"
    "v_mfma_f32_16x16x32_bf16 v[108:111], a[20:23], v[196:199], v[108:111]\n"
    "s_waitcnt lgkmcnt(8)\n"
    "v_or_b32_e32 v185, v185, v201\n"
    "v_lshlrev_b32_sdwa v201, 1, v171 dst_sel:DWORD dst_unused:UNUSED_PAD src0_sel:DWORD src1_sel:WORD_1\n"
    "v_mfma_f32_16x16x32_bf16 v[120:123], a[24:27], v[196:199], v[120:123]\n"
    "ds_read_u16 v189, v189\n"
    "v_pk_add_f32 v[224:225], v[224:225], v[88:89]\n"
    "v_pk_add_f32 v[226:227], v[226:227], v[90:91]\n"
    "v_pk_fma_f32 v[228:229], v[88:89], v[88:89], v[228:229]\n"
    "v_pk_fma_f32 v[230:231], v[90:91], v[90:91], v[230:231]\n"
    "v_pk_add_f32 v[224:225], v[224:225], v[92:93]\n"
    "v_pk_add_f32 v[226:227], v[226:227], v[94:95]\n"
    "v_pk_fma_f32 v[228:229], v[92:93], v[92:93], v[228:229]\n"
    "v_pk_fma_f32 v[230:231], v[94:95], v[94:95], v[230:231]\n"
    "v_mfma_f32_16x16x32_bf16 v[124:127], a[28:31], v[196:199], v[124:127]\n"
    "s_waitcnt vmcnt(2) lgkmcnt(6)\n"
    "ds_write_b128 v254, v[224:227]\n"
    "v_mfma_f32_16x16x32_bf16 v[128:131], v[236:239], v[192:195], v[128:131]\n"
    "s_waitcnt lgkmcnt(5)\n"
    "v_or_b32_e64 v186, v186, v202\n"
    "v_lshlrev_b32_sdwa v202, 1, v173 dst_sel:DWORD dst_unused:UNUSED_PAD src0_sel:DWORD src1_sel:WORD_1\n"
    "v_mfma_f32_16x16x32_bf16 v[132:135], v[164:167], v[192:195], v[132:135]\n"
    "v_cvt_pk_f16_f32 v192, v168, v169\n"
    "v_lshlrev_b32_sdwa v193, 1, v170 dst_sel:DWORD dst_unused:UNUSED_PAD src0_sel:DWORD src1_sel:WORD_1\n"
    "v_mfma_f32_16x16x32_bf16 v[136:139], v[236:239], v[196:199], v[136:139]\n"
    "ds_read_u16_d16_hi v205, v205\n"
    "ds_write_b128 v254, v[228:231] offset:1024\n"
    "v_mfma_f32_16x16x32_bf16 v[140:143], v[164:167], v[196:199], v[140:143]\n"
    "s_waitcnt lgkmcnt(6)\n"
    "v_lshrrev_b32_e32 v254, 2, v242\n"
    "v_lshlrev_b32_sdwa v194, 1, v172 dst_sel:DWORD dst_unused:UNUSED_PAD src0_sel:DWORD src1_sel:WORD_1\n"
    "v_smfmac_f32_32x32x32_bf16 v[112:127], a[248:251], v[152:159], v241\n"
    "v_add_u32_e64 v254, s55, v254\n"
    "v_lshlrev_b32_sdwa v195, 1, v174 dst_sel:DWORD dst_unused:UNUSED_PAD src0_sel:DWORD src1_sel:WORD_1\n"
    "v_cvt_pk_f16_f32 v196, v176, v177\n"
    "v_lshlrev_b32_sdwa v197, 1, v178 dst_sel:DWORD dst_unused:UNUSED_PAD src0_sel:DWORD src1_sel:WORD_1\n"
    "v_lshlrev_b32_sdwa v198, 1, v180 dst_sel:DWORD dst_unused:UNUSED_PAD src0_sel:DWORD src1_sel:WORD_1\n"
    "v_smfmac_f32_32x32x32_bf16 v[96:111], a[248:251], v[144:151], v241\n"
    "s_waitcnt lgkmcnt(4)\n"
    "ds_read_u16 v190, v190\n"
    "ds_read_u16_d16_hi v206, v206\n"
    "v_lshlrev_b32_sdwa v199, 1, v182 dst_sel:DWORD dst_unused:UNUSED_PAD src0_sel:DWORD src1_sel:WORD_1\n"
    "v_or_b32_e32 v187, v187, v203\n"
    "v_lshlrev_b32_sdwa v203, 1, v175 dst_sel:DWORD dst_unused:UNUSED_PAD src0_sel:DWORD src1_sel:WORD_1\n"
    "v_mfma_f32_16x16x32_bf16 v[168:171], a[112:115], a[224:227], 0\n"
    "ds_read_u16 v191, v191\n"
    "ds_read_u16_d16_hi v207, v207\n"
    "v_mfma_f32_16x16x32_bf16 v[168:171], a[160:163], a[228:231], v[168:171]\n"
    "buffer_load_dwordx4 v[144:147], v245, s[40:43], s58 offen offset:128\n"
    "ds_read_b128 a[252:255], v244 offset:34816\n"
    "v_mfma_f32_16x16x32_bf16 v[168:171], a[72:75], a[232:235], v[168:171]\n"
    "v_or_b32_e64 v188, v188, v204\n"
    "v_pk_fma_f16 v232, v192, v192, s48\n"
    "v_mfma_f32_16x16x32_bf16 v[172:175], a[116:119], a[224:227], 0\n"
    "s_waitcnt lgkmcnt(5)\n"
    "s_barrier\n"
    "v_mfma_f32_16x16x32_bf16 v[172:175], a[164:167], a[228:231], v[172:175]\n"
    "ds_read_b32 v224, v254 offset:0\n"
    "ds_read_b32 v225, v254 offset:6752\n"
    "v_or_b32_e64 v189, v189, v205\n"
    "ds_read_b32 v226, v254 offset:13504\n"
    "ds_read_b32 v227, v254 offset:20256\n"
    "ds_read_b32 v228, v254 offset:1024\n"
    "v_lshlrev_b32_sdwa v205, 1, v179 dst_sel:DWORD dst_unused:UNUSED_PAD src0_sel:DWORD src1_sel:WORD_1\n"
    "v_mfma_f32_16x16x32_bf16 v[176:179], a[112:115], a[236:239], 0\n"
    "s_nop 0\n"
    "ds_read_b32 v229, v254 offset:7776\n"
    "ds_read_b32 v230, v254 offset:14528\n"
    "v_mfma_f32_16x16x32_bf16 v[176:179], a[160:163], a[240:243], v[176:179]\n"
    "s_waitcnt lgkmcnt(10)\n"
    "ds_read_b32 v231, v254 offset:21280\n"
    "ds_read_u16 v193, v193\n"
    "v_mfma_f32_16x16x32_bf16 v[176:179], a[72:75], a[244:247], v[176:179]\n"
    "ds_read_u16_d16_hi v201, v201\n"
    "ds_read_u16 v194, v194\n"
    "v_or_b32_e64 v190, v190, v206\n"
    "v_mfma_f32_16x16x32_bf16 v[112:115], a[40:43], v[184:187], v[112:115]\n"
    "ds_read_u16_d16_hi v202, v202\n"
    "buffer_load_dwordx4 v[148:151], v245, s[40:43], s89 offen offset:128\n"
    "s_waitcnt lgkmcnt(12)\n"
    "v_mfma_f32_16x16x32_bf16 v[172:175], a[252:255], a[232:235], v[172:175]\n"
    "s_nop 0\n"
    "ds_read_b128 v[236:239], v244 offset:8192\n"
    "v_lshlrev_b32_sdwa v206, 1, v181 dst_sel:DWORD dst_unused:UNUSED_PAD src0_sel:DWORD src1_sel:WORD_1\n"
    "v_or_b32_e32 v191, v191, v207\n"
    "v_mfma_f32_16x16x32_bf16 v[116:119], a[44:47], v[184:187], v[116:119]\n"
    "s_waitcnt lgkmcnt(10)\n"
    "ds_read_b128 v[152:155], v243 offset:24640\n"
    "v_add_f32_e32 v224, v224, v225\n"
    "v_mfma_f32_16x16x32_bf16 v[120:123], a[40:43], v[188:191], v[120:123]\n"
    "s_waitcnt lgkmcnt(9)\n"
    "ds_read_b128 v[156:159], v243 offset:25664\n"
    "v_add_f32_e32 v226, v226, v227\n"
    "v_mfma_f32_16x16x32_bf16 v[124:127], a[44:47], v[188:191], v[124:127]\n"
    "s_waitcnt lgkmcnt(9)\n"
    "ds_read_u16 v195, v195\n"
    "v_add_f32_e64 v228, v228, v229\n"
    "v_add_f32_e32 v224, v224, v226\n"
    "s_waitcnt lgkmcnt(8)\n"
    "v_add_f32_e64 v230, v230, v231\n"
    "v_mul_f32_e32 v224, 0x3c2aaaab, v224\n"
    "v_add_f32_e64 v228, v228, v230\n"
    "v_lshlrev_b32_sdwa v207, 1, v183 dst_sel:DWORD dst_unused:UNUSED_PAD src0_sel:DWORD src1_sel:WORD_1\n"
    "v_mfma_f32_16x16x32_bf16 v[180:183], a[116:119], a[236:239], 0\n"
    "ds_read_u16_d16_hi v203, v203\n"
    "v_mul_f32_e32 v228, 0x3c2aaaab, v228\n"
    "v_mfma_f32_16x16x32_bf16 v[180:183], a[164:167], a[240:243], v[180:183]\n"
    "ds_read_b128 v[164:167], v244 offset:10240\n"
    "v_fma_f32 v228, -v224, v224, v228\n"
    "v_mfma_f32_16x16x32_bf16 v[180:183], a[252:255], a[244:247], v[180:183]\n"
    "ds_read_u16 v197, v197\n"
    "v_max_f32_e64 v228, 0, v228\n"
    "v_pk_mul_f16 v232, v192, v232\n"
    "v_add_f32_e32 v228, 0x3727c5ac, v228\n"
    "s_waitcnt lgkmcnt(9)\n"
    "v_or_b32_e32 v193, v193, v201\n"
    "v_rsq_f32_e64 v228, v228\n"
    "v_exp_f16_e64 v234, v232\n"
    "v_mul_f32_e64 v228, s50, v228\n"
    "v_exp_f16_sdwa v234, v232 dst_sel:WORD_1 dst_unused:UNUSED_PRESERVE src0_sel:WORD_1\n"
    "ds_write_b32 v254, v228\n"
    "ds_read_u16_d16_hi v205, v205\n"
    "s_waitcnt lgkmcnt(8)\n"
    "v_mfma_f32_16x16x32_bf16 v[128:131], v[236:239], v[184:187], v[128:131]\n"
    "s_nop 0\n"
    "ds_read_u16 v198, v198\n"
    "ds_read_u16_d16_hi v206, v206\n"
    "v_mfma_f32_16x16x32_bf16 v[136:139], v[236:239], v[188:191], v[136:139]\n"
    "s_waitcnt lgkmcnt(8)\n"
    "v_mul_f32_e64 v224, v228, -v224\n"
    "v_or_b32_e32 v194, v194, v202\n"
    "v_smfmac_f32_32x32x32_bf16 v[96:111], a[248:251], v[152:159], v241\n"
    "v_pk_fma_f16 v234, v234, s49, s49\n"
    "v_lshlrev_b32_sdwa v200, 1, v169 dst_sel:DWORD dst_unused:UNUSED_PAD src0_sel:DWORD src1_sel:WORD_1\n"
    "v_lshlrev_b32_sdwa v201, 1, v171 dst_sel:DWORD dst_unused:UNUSED_PAD src0_sel:DWORD src1_sel:WORD_1\n"
    "v_lshlrev_b32_sdwa v202, 1, v173 dst_sel:DWORD dst_unused:UNUSED_PAD src0_sel:DWORD src1_sel:WORD_1\n"
    "v_rcp_f16_e64 v232, v234\n"
    "ds_write_b32 v254, v224 offset:1024\n"
    "ds_read_u16 v199, v199\n"
    "v_add_u32_e32 v254, s56, v242\n"
    "v_rcp_f16_sdwa v232, v234 dst_sel:WORD_1 dst_unused:UNUSED_PRESERVE src0_sel:WORD_1\n"
    "s_waitcnt lgkmcnt(7)\n"
    "v_mfma_f32_16x16x32_bf16 v[132:135], v[164:167], v[184:187], v[132:135]\n"
    "ds_read_u16_d16_hi v207, v207\n"
    "ds_read_b128 v[152:155], v243 offset:26688\n"
    "v_mfma_f32_16x16x32_bf16 v[96:99], a[32:35], v[184:187], v[96:99]\n"
    "ds_read_b128 v[156:159], v243 offset:27712\n"
    "v_or_b32_e64 v195, v195, v203\n"
    "v_mfma_f32_16x16x32_bf16 v[100:103], a[36:39], v[184:187], v[100:103]\n"
    "ds_read_b128 a[252:255], v244 offset:38912\n"
    "v_lshlrev_b32_sdwa v184, 1, v168 dst_sel:DWORD dst_unused:UNUSED_PAD src0_sel:DWORD src1_sel:WORD_1\n"
    "v_mfma_f32_16x16x32_bf16 v[104:107], a[32:35], v[188:191], v[104:107]\n"
    "ds_read_u16 v184, v184\n"
    "v_lshlrev_b32_sdwa v185, 1, v170 dst_sel:DWORD dst_unused:UNUSED_PAD src0_sel:DWORD src1_sel:WORD_1\n"
    "v_mfma_f32_16x16x32_bf16 v[108:111], a[36:39], v[188:191], v[108:111]\n"
    "v_lshlrev_b32_sdwa v186, 1, v172 dst_sel:DWORD dst_unused:UNUSED_PAD src0_sel:DWORD src1_sel:WORD_1\n"
    "v_lshlrev_b32_sdwa v187, 1, v174 dst_sel:DWORD dst_unused:UNUSED_PAD src0_sel:DWORD src1_sel:WORD_1\n"
    "v_mfma_f32_16x16x32_bf16 v[140:143], v[164:167], v[188:191], v[140:143]\n"
    "s_waitcnt lgkmcnt(9)\n"
    "ds_read_u16_d16_hi v200, v200\n"
    "v_or_b32_e32 v197, v197, v205\n"
    "v_mfma_f32_16x16x32_bf16 v[168:171], a[120:123], a[224:227], 0\n"
    "ds_read_u16 v185, v185\n"
    "v_lshlrev_b32_sdwa v203, 1, v175 dst_sel:DWORD dst_unused:UNUSED_PAD src0_sel:DWORD src1_sel:WORD_1\n"
    "v_mfma_f32_16x16x32_bf16 v[168:171], a[168:171], a[228:231], v[168:171]\n"
    "s_waitcnt lgkmcnt(8)\n"
    "s_barrier\n"
    "v_mfma_f32_16x16x32_bf16 v[168:171], a[76:79], a[232:235], v[168:171]\n"
    "ds_read_b128 v[224:227], v254\n"
    "ds_read_b128 v[228:231], v254 offset:1024\n"
    "v_mfma_f32_16x16x32_bf16 v[172:175], a[124:127], a[224:227], 0\n"
    "s_waitcnt lgkmcnt(7)\n"
    "ds_read_u16_d16_hi v201, v201\n"
    "ds_read_u16 v186, v186\n"
    "v_mfma_f32_16x16x32_bf16 v[172:175], a[172:175], a[228:231], v[172:175]\n"
    "s_waitcnt lgkmcnt(8)\n"
    "v_lshlrev_b32_e64 v254, 3, v244\n"
    "v_or_b32_e64 v198, v198, v206\n"
    "v_smfmac_f32_32x32x32_bf16 v[112:127], a[248:251], v[152:159], v241\n"
    "s_mov_b32 exec_hi, 0\n"
    "buffer_load_dword v233, v254, s[72:75], s53 offen sc1\n"
    "s_mov_b32 exec_hi, -1\n"
    "s_add_u32 s53, s53, 393216\n"
    "v_lshlrev_b32_e64 v254, 3, v244\n"
    "v_lshlrev_b32_sdwa v188, 1, v176 dst_sel:DWORD dst_unused:UNUSED_PAD src0_sel:DWORD src1_sel:WORD_1\n"
    "v_lshlrev_b32_sdwa v204, 1, v177 dst_sel:DWORD dst_unused:UNUSED_PAD src0_sel:DWORD src1_sel:WORD_1\n"
    "v_lshlrev_b32_sdwa v189, 1, v178 dst_sel:DWORD dst_unused:UNUSED_PAD src0_sel:DWORD src1_sel:WORD_1\n"
    "v_lshlrev_b32_sdwa v205, 1, v179 dst_sel:DWORD dst_unused:UNUSED_PAD src0_sel:DWORD src1_sel:WORD_1\n"
    "v_lshlrev_b32_sdwa v190, 1, v180 dst_sel:DWORD dst_unused:UNUSED_PAD src0_sel:DWORD src1_sel:WORD_1\n"
    "v_lshlrev_b32_sdwa v206, 1, v181 dst_sel:DWORD dst_unused:UNUSED_PAD src0_sel:DWORD src1_sel:WORD_1\n"
    "v_lshlrev_b32_sdwa v191, 1, v182 dst_sel:DWORD dst_unused:UNUSED_PAD src0_sel:DWORD src1_sel:WORD_1\n"
    "v_fma_mix_f32 v234, v192, v232, 0 op_sel_hi:[1,1,0]\n"
    "s_waitcnt lgkmcnt(6)\n"
    "v_mfma_f32_16x16x32_bf16 v[172:175], a[252:255], a[232:235], v[172:175]\n"
    "s_nop 0\n"
    "ds_read_b128 v[236:239], v244 offset:12288\n"
    "ds_read_u16_d16_hi v202, v202\n"
    "v_or_b32_e64 v199, v199, v207\n"
    "v_lshlrev_b32_sdwa v207, 1, v183 dst_sel:DWORD dst_unused:UNUSED_PAD src0_sel:DWORD src1_sel:WORD_1\n"
    "v_fma_mix_f32 v232, v192, v232, 0 op_sel:[1,1,0] op_sel_hi:[1,1,0]\n"
    "v_mfma_f32_16x16x32_bf16 v[176:179], a[120:123], a[236:239], 0\n"
    "s_waitcnt lgkmcnt(7)\n"
    "ds_read_u16 v187, v187\n"
    "ds_read_b128 v[164:167], v244 offset:14336\n"
    "v_cvt_pk_bf16_f32 v192, v234, v232\n"
    "v_mfma_f32_16x16x32_bf16 v[176:179], a[168:171], a[240:243], v[176:179]\n"
    "s_waitcnt lgkmcnt(6)\n"
    "ds_read_u16_d16_hi v203, v203\n"
    "ds_read_u16 v188, v188\n"
    "v_pk_fma_f16 v232, v196, v196, s48\n"
    "v_or_b32_e64 v184, v184, v200\n"
    "v_pk_mul_f16 v232, v196, v232\n"
    "v_pk_fma_f32 v[64:65], v[64:65], v[224:225], v[228:229]\n"
    "v_pk_fma_f32 v[66:67], v[66:67], v[226:227], v[230:231]\n"
    "v_pk_fma_f32 v[68:69], v[68:69], v[224:225], v[228:229]\n"
    "v_pk_fma_f32 v[70:71], v[70:71], v[226:227], v[230:231]\n"
    "v_pk_fma_f32 v[72:73], v[72:73], v[224:225], v[228:229]\n"
    "v_pk_fma_f32 v[74:75], v[74:75], v[226:227], v[230:231]\n"
    "v_pk_fma_f32 v[76:77], v[76:77], v[224:225], v[228:229]\n"
    "v_pk_fma_f32 v[78:79], v[78:79], v[226:227], v[230:231]\n"
    "v_pk_fma_f32 v[80:81], v[80:81], v[224:225], v[228:229]\n"
    "v_pk_fma_f32 v[82:83], v[82:83], v[226:227], v[230:231]\n"
    "v_pk_fma_f32 v[84:85], v[84:85], v[224:225], v[228:229]\n"
    "v_pk_fma_f32 v[86:87], v[86:87], v[226:227], v[230:231]\n"
    "v_pk_fma_f32 v[88:89], v[88:89], v[224:225], v[228:229]\n"
    "v_pk_fma_f32 v[90:91], v[90:91], v[226:227], v[230:231]\n"
    "v_pk_fma_f32 v[92:93], v[92:93], v[224:225], v[228:229]\n"
    "v_pk_fma_f32 v[94:95], v[94:95], v[226:227], v[230:231]\n"
    "v_pk_fma_f32 v[0:1], v[0:1], v[224:225], v[228:229]\n"
    "v_pk_fma_f32 v[2:3], v[2:3], v[226:227], v[230:231]\n"
    "v_pk_fma_f32 v[4:5], v[4:5], v[224:225], v[228:229]\n"
    "v_pk_fma_f32 v[6:7], v[6:7], v[226:227], v[230:231]\n"
    "v_pk_fma_f32 v[8:9], v[8:9], v[224:225], v[228:229]\n"
    "v_pk_fma_f32 v[10:11], v[10:11], v[226:227], v[230:231]\n"
    "v_pk_fma_f32 v[12:13], v[12:13], v[224:225], v[228:229]\n"
    "v_pk_fma_f32 v[14:15], v[14:15], v[226:227], v[230:231]\n"
    "v_pk_fma_f32 v[16:17], v[16:17], v[224:225], v[228:229]\n"
    "v_pk_fma_f32 v[18:19], v[18:19], v[226:227], v[230:231]\n"
    "v_pk_fma_f32 v[20:21], v[20:21], v[224:225], v[228:229]\n"
    "v_pk_fma_f32 v[22:23], v[22:23], v[226:227], v[230:231]\n"
    "v_pk_fma_f32 v[24:25], v[24:25], v[224:225], v[228:229]\n"
    "v_pk_fma_f32 v[26:27], v[26:27], v[226:227], v[230:231]\n"
    "v_pk_fma_f32 v[28:29], v[28:29], v[224:225], v[228:229]\n"
    "v_pk_fma_f32 v[30:31], v[30:31], v[226:227], v[230:231]\n"
    "v_pk_fma_f32 v[32:33], v[32:33], v[224:225], v[228:229]\n"
    "v_pk_fma_f32 v[34:35], v[34:35], v[226:227], v[230:231]\n"
    "v_pk_fma_f32 v[36:37], v[36:37], v[224:225], v[228:229]\n"
    "v_pk_fma_f32 v[38:39], v[38:39], v[226:227], v[230:231]\n"
    "v_pk_fma_f32 v[40:41], v[40:41], v[224:225], v[228:229]\n"
    "v_pk_fma_f32 v[42:43], v[42:43], v[226:227], v[230:231]\n"
    "v_pk_fma_f32 v[44:45], v[44:45], v[224:225], v[228:229]\n"
    "v_pk_fma_f32 v[46:47], v[46:47], v[226:227], v[230:231]\n"
    "v_pk_fma_f32 v[48:49], v[48:49], v[224:225], v[228:229]\n"
    "v_pk_fma_f32 v[50:51], v[50:51], v[226:227], v[230:231]\n"
    "v_pk_fma_f32 v[52:53], v[52:53], v[224:225], v[228:229]\n"
    "v_pk_fma_f32 v[54:55], v[54:55], v[226:227], v[230:231]\n"
    "v_pk_fma_f32 v[56:57], v[56:57], v[224:225], v[228:229]\n"
    "v_pk_fma_f32 v[58:59], v[58:59], v[226:227], v[230:231]\n"
    "v_pk_fma_f32 v[60:61], v[60:61], v[224:225], v[228:229]\n"
    "v_pk_fma_f32 v[62:63], v[62:63], v[226:227], v[230:231]\n"
    "v_mfma_f32_16x16x32_bf16 v[176:179], a[76:79], a[244:247], v[176:179]\n"
    "s_waitcnt lgkmcnt(1)\n"
    "s_mov_b32 exec_hi, 0\n"
    "buffer_load_dword v233, v254, s[80:83], s85 offen sc1\n"
    "s_mov_b32 exec_hi, -1\n"
    "v_mfma_f32_16x16x32_bf16 v[180:183], a[124:127], a[236:239], 0\n"
    "s_nop 0\n"
    "ds_read_u16_d16_hi v204, v204\n"
    "ds_read_b128 v[152:155], v243 offset:28736\n"
    "v_mfma_f32_16x16x32_bf16 v[180:183], a[172:175], a[240:243], v[180:183]\n"
    "ds_read_b128 v[156:159], v243 offset:29760\n"
    "ds_read_u16 v189, v189\n"
    "v_cvt_pk_bf16_f32 v208, v64, v68\n"
    "v_cvt_pk_bf16_f32 v212, v65, v69\n"
    "v_cvt_pk_bf16_f32 v216, v66, v70\n"
    "v_cvt_pk_bf16_f32 v220, v67, v71\n"
    "v_cvt_pk_bf16_f32 v209, v72, v76\n"
    "v_cvt_pk_bf16_f32 v213, v73, v77\n"
    "v_cvt_pk_bf16_f32 v217, v74, v78\n"
    "v_cvt_pk_bf16_f32 v221, v75, v79\n"
    "v_cvt_pk_bf16_f32 v210, v80, v84\n"
    "v_cvt_pk_bf16_f32 v214, v81, v85\n"
    "v_cvt_pk_bf16_f32 v218, v82, v86\n"
    "v_cvt_pk_bf16_f32 v222, v83, v87\n"
    "v_cvt_pk_bf16_f32 v211, v88, v92\n"
    "ds_write_b128 v253, v[208:211] offset:2048\n"
    "v_cvt_pk_bf16_f32 v215, v89, v93\n"
    "v_cvt_pk_bf16_f32 v219, v90, v94\n"
    "v_cvt_pk_bf16_f32 v223, v91, v95\n"
    "v_cvt_pk_bf16_f32 v80, v0, v4\n"
    "v_cvt_pk_bf16_f32 v84, v1, v5\n"
    "v_cvt_pk_bf16_f32 v88, v2, v6\n"
    "ds_write_b128 v253, v[212:215] offset:5120\n"
    "v_cvt_pk_bf16_f32 v92, v3, v7\n"
    "v_cvt_pk_bf16_f32 v81, v8, v12\n"
    "v_cvt_pk_bf16_f32 v85, v9, v13\n"
    "v_cvt_pk_bf16_f32 v89, v10, v14\n"
    "v_cvt_pk_bf16_f32 v93, v11, v15\n"
    "v_cvt_pk_bf16_f32 v82, v16, v20\n"
    "v_cvt_pk_bf16_f32 v86, v17, v21\n"
    "v_cvt_pk_bf16_f32 v90, v18, v22\n"
    "v_cvt_pk_bf16_f32 v94, v19, v23\n"
    "v_cvt_pk_bf16_f32 v83, v24, v28\n"
    "ds_write_b128 v253, v[80:83] offset:0\n"
    "v_cvt_pk_bf16_f32 v87, v25, v29\n"
    "v_cvt_pk_bf16_f32 v91, v26, v30\n"
    "v_cvt_pk_bf16_f32 v95, v27, v31\n"
    "v_cvt_pk_bf16_f32 v64, v32, v36\n"
    "v_cvt_pk_bf16_f32 v68, v33, v37\n"
    "v_cvt_pk_bf16_f32 v72, v34, v38\n"
    "ds_write_b128 v253, v[84:87] offset:3072\n"
    "v_cvt_pk_bf16_f32 v76, v35, v39\n"
    "v_cvt_pk_bf16_f32 v65, v40, v44\n"
    "v_cvt_pk_bf16_f32 v69, v41, v45\n"
    "v_cvt_pk_bf16_f32 v73, v42, v46\n"
    "v_cvt_pk_bf16_f32 v77, v43, v47\n"
    "v_cvt_pk_bf16_f32 v66, v48, v52\n"
    "v_cvt_pk_bf16_f32 v70, v49, v53\n"
    "v_cvt_pk_bf16_f32 v74, v50, v54\n"
    "v_cvt_pk_bf16_f32 v78, v51, v55\n"
    "v_exp_f16_e64 v234, v232\n"
    "v_mfma_f32_16x16x32_bf16 v[180:183], a[252:255], a[244:247], v[180:183]\n"
    "ds_read_u16_d16_hi v205, v205\n"
    "ds_read_u16 v190, v190\n"
    "v_cvt_pk_bf16_f32 v67, v56, v60\n"
    "v_cvt_pk_bf16_f32 v71, v57, v61\n"
    "v_exp_f16_sdwa v234, v232 dst_sel:WORD_1 dst_unused:UNUSED_PRESERVE src0_sel:WORD_1\n"
    "v_mfma_f32_16x16x32_bf16 v[96:99], a[176:179], v[192:195], v[96:99]\n"
    "ds_write_b128 v253, v[64:67] offset:1024\n"
    "v_cvt_pk_bf16_f32 v75, v58, v62\n"
    "v_cvt_pk_bf16_f32 v79, v59, v63\n"
    "v_pk_fma_f16 v234, v234, s49, s49\n"
    "v_mfma_f32_16x16x32_bf16 v[100:103], a[180:183], v[192:195], v[100:103]\n"
    "v_rcp_f16_e32 v232, v234\n"
    "v_mfma_f32_16x16x32_bf16 v[112:115], a[184:187], v[192:195], v[112:115]\n"
    "ds_read_u16_d16_hi v206, v206\n"
    "ds_write_b128 v253, v[68:71] offset:4096\n"
    "v_rcp_f16_sdwa v232, v234 dst_sel:WORD_1 dst_unused:UNUSED_PRESERVE src0_sel:WORD_1\n"
    "v_mfma_f32_16x16x32_bf16 v[116:119], a[188:191], v[192:195], v[116:119]\n"
    "s_waitcnt lgkmcnt(5)\n"
    "v_fma_mix_f32 v234, v196, v232, 0 op_sel_hi:[1,1,0]\n"
    "v_fma_mix_f32 v232, v196, v232, 0 op_sel:[1,1,0] op_sel_hi:[1,1,0]\n"
    "v_mfma_f32_16x16x32_bf16 v[128:131], v[236:239], v[192:195], v[128:131]\n"
    "v_cvt_pk_bf16_f32 v196, v234, v232\n"
    "v_or_b32_e32 v185, v185, v201\n"
    "v_mfma_f32_16x16x32_bf16 v[132:135], v[164:167], v[192:195], v[132:135]\n"
    "s_waitcnt lgkmcnt(4)\n"
    "ds_read_u16 v191, v191\n"
    "ds_read_u16_d16_hi v207, v207\n"
    "v_mfma_f32_16x16x32_bf16 v[104:107], a[176:179], v[196:199], v[104:107]\n"
    "ds_read_b128 a[252:255], v244 offset:32768\n"
    "v_cvt_pk_f16_f32 v192, v168, v169\n"
    "v_mfma_f32_16x16x32_bf16 v[108:111], a[180:183], v[196:199], v[108:111]\n"
    "v_lshlrev_b32_sdwa v193, 1, v170 dst_sel:DWORD dst_unused:UNUSED_PAD src0_sel:DWORD src1_sel:WORD_1\n"
    "v_lshlrev_b32_sdwa v201, 1, v171 dst_sel:DWORD dst_unused:UNUSED_PAD src0_sel:DWORD src1_sel:WORD_1\n"
    "v_mfma_f32_16x16x32_bf16 v[120:123], a[184:187], v[196:199], v[120:123]\n"
    "ds_read_u16 v193, v193\n"
    "ds_read_u16_d16_hi v201, v201\n"
    "v_mfma_f32_16x16x32_bf16 v[124:127], a[188:191], v[196:199], v[124:127]\n"
    "v_lshlrev_b32_sdwa v194, 1, v172 dst_sel:DWORD dst_unused:UNUSED_PAD src0_sel:DWORD src1_sel:WORD_1\n"
    "v_or_b32_e64 v186, v186, v202\n"
    "v_mfma_f32_16x16x32_bf16 v[136:139], v[236:239], v[196:199], v[136:139]\n"
    "ds_read_u16 v194, v194\n"
    "v_lshlrev_b32_sdwa v202, 1, v173 dst_sel:DWORD dst_unused:UNUSED_PAD src0_sel:DWORD src1_sel:WORD_1\n"
    "v_mfma_f32_16x16x32_bf16 v[140:143], v[164:167], v[196:199], v[140:143]\n"
    "s_waitcnt lgkmcnt(6)\n"
    "s_barrier\n"
    "v_lshlrev_b32_sdwa v195, 1, v174 dst_sel:DWORD dst_unused:UNUSED_PAD src0_sel:DWORD src1_sel:WORD_1\n"
    "v_mov_b32_e64 v64, v216\n"
    "v_cvt_pk_f16_f32 v196, v176, v177\n"
    "v_mov_b32_e64 v65, v217\n"
    "v_smfmac_f32_32x32x32_bf16 v[128:143], a[248:251], v[152:159], v241\n"
    "ds_read_b128 a[224:227], v242 offset:0\n"
    "ds_read_b128 a[228:231], v242 offset:1024\n"
    "v_lshlrev_b32_sdwa v197, 1, v178 dst_sel:DWORD dst_unused:UNUSED_PAD src0_sel:DWORD src1_sel:WORD_1\n"
    "v_mov_b32_e64 v66, v218\n"
    "v_mov_b32_e64 v67, v219\n"
    "v_lshlrev_b32_sdwa v198, 1, v180 dst_sel:DWORD dst_unused:UNUSED_PAD src0_sel:DWORD src1_sel:WORD_1\n"
    "v_mov_b32_e64 v68, v220\n"
    "v_mov_b32_e64 v69, v221\n"
    "v_lshlrev_b32_sdwa v199, 1, v182 dst_sel:DWORD dst_unused:UNUSED_PAD src0_sel:DWORD src1_sel:WORD_1\n"
    "v_or_b32_e64 v187, v187, v203\n"
    "v_lshlrev_b32_sdwa v203, 1, v175 dst_sel:DWORD dst_unused:UNUSED_PAD src0_sel:DWORD src1_sel:WORD_1\n"
    "v_mov_b32_e64 v70, v222\n"
    "v_mov_b32_e64 v71, v223\n"
    "v_or_b32_e64 v188, v188, v204\n"
    "ds_read_b128 a[232:235], v242 offset:2048\n"
    "ds_read_b128 a[236:239], v242 offset:3072\n"
    "ds_read_b128 a[240:243], v242 offset:4096\n"
    "v_or_b32_e64 v189, v189, v205\n"
    "ds_read_b128 a[244:247], v242 offset:5120\n"
    "ds_read_u16_d16_hi v202, v202\n"
    "v_lshlrev_b32_sdwa v205, 1, v179 dst_sel:DWORD dst_unused:UNUSED_PAD src0_sel:DWORD src1_sel:WORD_1\n"
    "v_or_b32_e64 v190, v190, v206\n"
    "v_mfma_f32_16x16x32_bf16 v[96:99], a[192:195], v[184:187], v[96:99]\n"
    "s_waitcnt lgkmcnt(7)\n"
    "ds_read_b128 v[236:239], v244 offset:16384\n"
    "ds_read_u16 v195, v195\n"
    "v_mfma_f32_16x16x32_bf16 v[100:103], a[196:199], v[184:187], v[100:103]\n"
    "s_waitcnt lgkmcnt(8)\n"
    "ds_read_u16_d16_hi v203, v203\n"
    "v_lshlrev_b32_sdwa v206, 1, v181 dst_sel:DWORD dst_unused:UNUSED_PAD src0_sel:DWORD src1_sel:WORD_1\n"
    "v_mfma_f32_16x16x32_bf16 v[168:171], a[80:83], a[224:227], 0\n"
    "s_waitcnt lgkmcnt(8)\n"
    "v_or_b32_e32 v191, v191, v207\n"
    "v_lshlrev_b32_sdwa v207, 1, v183 dst_sel:DWORD dst_unused:UNUSED_PAD src0_sel:DWORD src1_sel:WORD_1\n"
    "v_mfma_f32_16x16x32_bf16 v[168:171], a[128:131], a[228:231], v[168:171]\n"
    "ds_read_b128 v[164:167], v244 offset:18432\n"
    "v_or_b32_e64 v193, v193, v201\n"
    "v_mfma_f32_16x16x32_bf16 v[172:175], a[84:87], a[224:227], 0\n"
    "ds_read_u16 v197, v197\n"
    "v_pk_fma_f16 v232, v192, v192, s48\n"
    "v_mfma_f32_16x16x32_bf16 v[172:175], a[132:135], a[228:231], v[172:175]\n"
    "s_waitcnt lgkmcnt(9)\n"
    "v_pk_mul_f16 v232, v192, v232\n"
    "v_exp_f16_e64 v234, v232\n"
    "v_mfma_f32_16x16x32_bf16 v[168:171], a[48:51], a[232:235], v[168:171]\n"
    "s_waitcnt lgkmcnt(8)\n"
    "ds_read_u16_d16_hi v205, v205\n"
    "v_exp_f16_sdwa v234, v232 dst_sel:WORD_1 dst_unused:UNUSED_PRESERVE src0_sel:WORD_1\n"
    "v_mfma_f32_16x16x32_bf16 v[172:175], a[252:255], a[232:235], v[172:175]\n"
    "ds_read_u16 v198, v198\n"
    "v_pk_fma_f16 v234, v234, s49, s49\n"
    "v_mfma_f32_16x16x32_bf16 v[176:179], a[80:83], a[236:239], 0\n"
    "s_waitcnt lgkmcnt(8)\n"
    "v_rcp_f16_e32 v232, v234\n"
    "v_mfma_f32_16x16x32_bf16 v[176:179], a[128:131], a[240:243], v[176:179]\n"
    "ds_read_u16_d16_hi v206, v206\n"
    "v_rcp_f16_sdwa v232, v234 dst_sel:WORD_1 dst_unused:UNUSED_PRESERVE src0_sel:WORD_1\n"
    "v_mfma_f32_16x16x32_bf16 v[176:179], a[48:51], a[244:247], v[176:179]\n"
    "s_waitcnt lgkmcnt(7)\n"
    "ds_read_u16 v199, v199\n"
    "v_or_b32_e32 v194, v194, v202\n"
    "v_mfma_f32_16x16x32_bf16 v[180:183], a[84:87], a[236:239], 0\n"
    "v_fma_mix_f32 v234, v192, v232, 0 op_sel_hi:[1,1,0]\n"
    "v_fma_mix_f32 v232, v192, v232, 0 op_sel:[1,1,0] op_sel_hi:[1,1,0]\n"
    "v_mfma_f32_16x16x32_bf16 v[180:183], a[132:135], a[240:243], v[180:183]\n"
    "ds_read_u16_d16_hi v207, v207\n"
    "v_cvt_pk_bf16_f32 v192, v234, v232\n"
    "v_mfma_f32_16x16x32_bf16 v[180:183], a[252:255], a[244:247], v[180:183]\n"
    "v_pk_fma_f16 v232, v196, v196, s48\n"
    "v_pk_mul_f16 v232, v196, v232\n"
    "v_mfma_f32_16x16x32_bf16 v[112:115], a[200:203], v[184:187], v[112:115]\n"
    "s_waitcnt lgkmcnt(6)\n"
    "v_or_b32_e32 v195, v195, v203\n"
    "v_exp_f16_e64 v234, v232\n"
    "v_mfma_f32_16x16x32_bf16 v[116:119], a[204:207], v[184:187], v[116:119]\n"
    "v_exp_f16_sdwa v234, v232 dst_sel:WORD_1 dst_unused:UNUSED_PRESERVE src0_sel:WORD_1\n"
    "v_mfma_f32_16x16x32_bf16 v[128:131], v[236:239], v[184:187], v[128:131]\n"
    "v_mfma_f32_16x16x32_bf16 v[132:135], v[164:167], v[184:187], v[132:135]\n"
    "v_mfma_f32_16x16x32_bf16 v[104:107], a[192:195], v[188:191], v[104:107]\n"
    "v_mfma_f32_16x16x32_bf16 v[108:111], a[196:199], v[188:191], v[108:111]\n"
    "s_waitcnt lgkmcnt(4)\n"
    "v_or_b32_e32 v197, v197, v205\n"
    "v_mfma_f32_16x16x32_bf16 v[120:123], a[200:203], v[188:191], v[120:123]\n"
    "v_mfma_f32_16x16x32_bf16 v[124:127], a[204:207], v[188:191], v[124:127]\n"
    "v_mfma_f32_16x16x32_bf16 v[136:139], v[236:239], v[188:191], v[136:139]\n"
    "s_waitcnt lgkmcnt(2)\n"
    "v_or_b32_e32 v198, v198, v206\n"
    "s_waitcnt lgkmcnt(0)\n"
    "v_or_b32_e32 v199, v199, v207\n"
    "s_mov_b32 s59, s58\n"
    "s_add_u32 s57, s57, 1572864\n"
    "s_add_u32 s58, s58, 1572864\n"
    "s_mov_b32 s70, s46\n"
    "s_add_u32 s88, s57, 98304\n"
    "s_add_u32 s89, s58, 98304\n"
    "s_add_u32 s39, s59, 98304\n"
    "s_mov_b32 s82, 0\n"
    "s_add_u32 s64, s64, 1\n"
    "s_cmp_lt_u32 s64, 2\n"
    "s_cselect_b64 s[72:73], s[72:73], s[76:77]\n"
    "s_cselect_b32 s74, s74, s78\n"
    "s_cselect_b32 s53, s53, s79\n"
    "s_cmp_lg_u32 s64, 3\n"
    "s_cbranch_scc1 1b\n"
    ".Lreplk_last:\n"
    "v_lshlrev_b32_sdwa v184, 1, v168 dst_sel:DWORD dst_unused:UNUSED_PAD src0_sel:DWORD src1_sel:WORD_1\n"
    "v_lshlrev_b32_sdwa v200, 1, v169 dst_sel:DWORD dst_unused:UNUSED_PAD src0_sel:DWORD src1_sel:WORD_1\n"
    "v_lshlrev_b32_sdwa v185, 1, v170 dst_sel:DWORD dst_unused:UNUSED_PAD src0_sel:DWORD src1_sel:WORD_1\n"
    "v_lshlrev_b32_sdwa v201, 1, v171 dst_sel:DWORD dst_unused:UNUSED_PAD src0_sel:DWORD src1_sel:WORD_1\n"
    "v_mfma_f32_16x16x32_bf16 v[140:143], v[164:167], v[188:191], v[140:143]\n"
    "s_waitcnt vmcnt(2)\n"
    "ds_read_b128 v[56:59], v244 offset:36864\n"
    "ds_read_u16 v184, v184\n"
    "v_mfma_f32_16x16x32_bf16 v[168:171], a[88:91], a[224:227], 0\n"
    "s_nop 0\n"
    "ds_read_u16_d16_hi v200, v200\n"
    "ds_read_u16 v185, v185\n"
    "v_mfma_f32_16x16x32_bf16 v[168:171], a[136:139], a[228:231], v[168:171]\n"
    "v_lshlrev_b32_sdwa v186, 1, v172 dst_sel:DWORD dst_unused:UNUSED_PAD src0_sel:DWORD src1_sel:WORD_1\n"
    "v_lshlrev_b32_sdwa v202, 1, v173 dst_sel:DWORD dst_unused:UNUSED_PAD src0_sel:DWORD src1_sel:WORD_1\n"
    "v_mfma_f32_16x16x32_bf16 v[168:171], a[52:55], a[232:235], v[168:171]\n"
    "ds_read_u16_d16_hi v201, v201\n"
    "v_lshlrev_b32_sdwa v187, 1, v174 dst_sel:DWORD dst_unused:UNUSED_PAD src0_sel:DWORD src1_sel:WORD_1\n"
    "v_smfmac_f32_32x32x32_bf16 v[128:143], a[248:251], v[144:151], v241\n"
    "ds_read_u16 v186, v186\n"
    "v_lshlrev_b32_sdwa v203, 1, v175 dst_sel:DWORD dst_unused:UNUSED_PAD src0_sel:DWORD src1_sel:WORD_1\n"
    "v_lshlrev_b32_sdwa v188, 1, v176 dst_sel:DWORD dst_unused:UNUSED_PAD src0_sel:DWORD src1_sel:WORD_1\n"
    "v_lshlrev_b32_sdwa v204, 1, v177 dst_sel:DWORD dst_unused:UNUSED_PAD src0_sel:DWORD src1_sel:WORD_1\n"
    "v_lshlrev_b32_sdwa v189, 1, v178 dst_sel:DWORD dst_unused:UNUSED_PAD src0_sel:DWORD src1_sel:WORD_1\n"
    "v_mfma_f32_16x16x32_bf16 v[172:175], a[92:95], a[224:227], 0\n"
    "ds_read_b128 v[236:239], v244 offset:20480\n"
    "buffer_load_dwordx4 v[152:155], v245, s[40:43], s57 offen offset:64\n"
    "buffer_load_dwordx4 v[144:147], v245, s[40:43], s57 offen offset:0\n"
    "buffer_load_dwordx4 v[208:211], v245, s[40:43], s57 offen offset:128\n"
    "v_mfma_f32_16x16x32_bf16 v[172:175], a[140:143], a[228:231], v[172:175]\n"
    "s_nop 0\n"
    "ds_read_u16_d16_hi v202, v202\n"
    "v_lshlrev_b32_sdwa v205, 1, v179 dst_sel:DWORD dst_unused:UNUSED_PAD src0_sel:DWORD src1_sel:WORD_1\n"
    "v_mfma_f32_16x16x32_bf16 v[176:179], a[88:91], a[236:239], 0\n"
    "s_waitcnt lgkmcnt(7)\n"
    "ds_read_u16 v187, v187\n"
    "v_lshlrev_b32_sdwa v190, 1, v180 dst_sel:DWORD dst_unused:UNUSED_PAD src0_sel:DWORD src1_sel:WORD_1\n"
    "v_mfma_f32_16x16x32_bf16 v[172:175], v[56:59], a[232:235], v[172:175]\n"
    "v_lshlrev_b32_sdwa v206, 1, v181 dst_sel:DWORD dst_unused:UNUSED_PAD src0_sel:DWORD src1_sel:WORD_1\n"
    "v_lshlrev_b32_sdwa v191, 1, v182 dst_sel:DWORD dst_unused:UNUSED_PAD src0_sel:DWORD src1_sel:WORD_1\n"
    "v_mfma_f32_16x16x32_bf16 v[176:179], a[136:139], a[240:243], v[176:179]\n"
    "ds_read_b128 v[164:167], v244 offset:22528\n"
    "v_lshlrev_b32_sdwa v207, 1, v183 dst_sel:DWORD dst_unused:UNUSED_PAD src0_sel:DWORD src1_sel:WORD_1\n"
    "v_mfma_f32_16x16x32_bf16 v[176:179], a[52:55], a[244:247], v[176:179]\n"
    "ds_read_u16_d16_hi v203, v203\n"
    "v_pk_fma_f16 v234, v234, s49, s49\n"
    "v_mfma_f32_16x16x32_bf16 v[180:183], a[92:95], a[236:239], 0\n"
    "v_rcp_f16_e32 v232, v234\n"
    "v_mfma_f32_16x16x32_bf16 v[180:183], a[140:143], a[240:243], v[180:183]\n"
    "s_waitcnt lgkmcnt(8)\n"
    "ds_read_u16 v188, v188\n"
    "v_rcp_f16_sdwa v232, v234 dst_sel:WORD_1 dst_unused:UNUSED_PRESERVE src0_sel:WORD_1\n"
    "v_mfma_f32_16x16x32_bf16 v[180:183], v[56:59], a[244:247], v[180:183]\n"
    "ds_read_u16_d16_hi v204, v204\n"
    "v_fma_mix_f32 v234, v196, v232, 0 op_sel_hi:[1,1,0]\n"
    "v_mfma_f32_16x16x32_bf16 v[96:99], a[208:211], v[192:195], v[96:99]\n"
    "v_fma_mix_f32 v232, v196, v232, 0 op_sel:[1,1,0] op_sel_hi:[1,1,0]\n"
    "v_or_b32_e32 v184, v184, v200\n"
    "v_mfma_f32_16x16x32_bf16 v[100:103], a[212:215], v[192:195], v[100:103]\n"
    "s_waitcnt lgkmcnt(8)\n"
    "ds_read_u16 v189, v189\n"
    "buffer_load_dwordx4 v[156:159], v245, s[40:43], s88 offen offset:64\n"
    "buffer_load_dwordx4 v[148:151], v245, s[40:43], s88 offen offset:0\n"
    "buffer_load_dwordx4 v[212:215], v245, s[40:43], s88 offen offset:128\n"
    "v_mfma_f32_16x16x32_bf16 v[112:115], a[216:219], v[192:195], v[112:115]\n"
    "s_waitcnt lgkmcnt(7)\n"
    "ds_read_u16_d16_hi v205, v205\n"
    "v_cvt_pk_bf16_f32 v196, v234, v232\n"
    "v_mfma_f32_16x16x32_bf16 v[116:119], a[220:223], v[192:195], v[116:119]\n"
    "v_or_b32_e32 v185, v185, v201\n"
    "v_lshlrev_b32_sdwa v201, 1, v171 dst_sel:DWORD dst_unused:UNUSED_PAD src0_sel:DWORD src1_sel:WORD_1\n"
    "v_mfma_f32_16x16x32_bf16 v[128:131], v[236:239], v[192:195], v[128:131]\n"
    "ds_read_u16 v190, v190\n"
    "v_cvt_pk_bf16_f32 v96, v96, v97\n"
    "v_mfma_f32_16x16x32_bf16 v[104:107], a[208:211], v[196:199], v[104:107]\n"
    "s_waitcnt lgkmcnt(8)\n"
    "v_or_b32_e64 v186, v186, v202\n"
    "v_lshlrev_b32_sdwa v202, 1, v173 dst_sel:DWORD dst_unused:UNUSED_PAD src0_sel:DWORD src1_sel:WORD_1\n"
    "v_mfma_f32_16x16x32_bf16 v[108:111], a[212:215], v[196:199], v[108:111]\n"
    "s_waitcnt lgkmcnt(6)\n"
    "ds_read_u16_d16_hi v206, v206\n"
    "v_cvt_pk_bf16_f32 v97, v98, v99\n"
    "v_mfma_f32_16x16x32_bf16 v[132:135], v[164:167], v[192:195], v[132:135]\n"
    "v_cvt_pk_f16_f32 v192, v168, v169\n"
    "v_lshlrev_b32_sdwa v193, 1, v170 dst_sel:DWORD dst_unused:UNUSED_PAD src0_sel:DWORD src1_sel:WORD_1\n"
    "v_mfma_f32_16x16x32_bf16 v[120:123], a[216:219], v[196:199], v[120:123]\n"
    "ds_read_u16 v191, v191\n"
    "v_lshlrev_b32_sdwa v194, 1, v172 dst_sel:DWORD dst_unused:UNUSED_PAD src0_sel:DWORD src1_sel:WORD_1\n"
    "v_mfma_f32_16x16x32_bf16 v[124:127], a[220:223], v[196:199], v[124:127]\n"
    "s_waitcnt lgkmcnt(7)\n"
    "ds_read_u16_d16_hi v207, v207\n"
    "v_lshlrev_b32_sdwa v195, 1, v174 dst_sel:DWORD dst_unused:UNUSED_PAD src0_sel:DWORD src1_sel:WORD_1\n"
    "v_mfma_f32_16x16x32_bf16 v[136:139], v[236:239], v[196:199], v[136:139]\n"
    "v_or_b32_e32 v187, v187, v203\n"
    "v_lshlrev_b32_sdwa v203, 1, v175 dst_sel:DWORD dst_unused:UNUSED_PAD src0_sel:DWORD src1_sel:WORD_1\n"
    "v_mfma_f32_16x16x32_bf16 v[140:143], v[164:167], v[196:199], v[140:143]\n"
    "ds_read_u16 v193, v193\n"
    "v_cvt_pk_f16_f32 v196, v176, v177\n"
    "v_mfma_f32_16x16x32_bf16 v[168:171], a[96:99], a[224:227], 0\n"
    "v_lshlrev_b32_sdwa v197, 1, v178 dst_sel:DWORD dst_unused:UNUSED_PAD src0_sel:DWORD src1_sel:WORD_1\n"
    "v_lshlrev_b32_sdwa v198, 1, v180 dst_sel:DWORD dst_unused:UNUSED_PAD src0_sel:DWORD src1_sel:WORD_1\n"
    "v_mfma_f32_16x16x32_bf16 v[168:171], a[144:147], a[228:231], v[168:171]\n"
    "s_waitcnt lgkmcnt(7)\n"
    "ds_read_u16_d16_hi v201, v201\n"
    "v_lshlrev_b32_sdwa v199, 1, v182 dst_sel:DWORD dst_unused:UNUSED_PAD src0_sel:DWORD src1_sel:WORD_1\n"
    "v_mfma_f32_16x16x32_bf16 v[168:171], a[56:59], a[232:235], v[168:171]\n"
    "v_or_b32_e32 v188, v188, v204\n"
    "v_cvt_pk_bf16_f32 v98, v100, v101\n"
    "v_mfma_f32_16x16x32_bf16 v[172:175], a[100:103], a[224:227], 0\n"
    "ds_read_u16 v194, v194\n"
    "v_cvt_pk_bf16_f32 v99, v102, v103\n"
    "v_mfma_f32_16x16x32_bf16 v[172:175], a[148:151], a[228:231], v[172:175]\n"
    "s_waitcnt lgkmcnt(7)\n"
    "ds_read_u16_d16_hi v202, v202\n"
    "buffer_store_dwordx4 v[96:99], v245, s[68:71], s59 offen offset:0 sc1\n"
    "v_mfma_f32_16x16x32_bf16 v[172:175], a[60:63], a[232:235], v[172:175]\n"
    "s_nop 0\n"
    "ds_read_b128 v[0:3], v244 offset:0\n"
    "v_or_b32_e64 v189, v189, v205\n"
    "v_lshlrev_b32_sdwa v205, 1, v179 dst_sel:DWORD dst_unused:UNUSED_PAD src0_sel:DWORD src1_sel:WORD_1\n"
    "v_mfma_f32_16x16x32_bf16 v[176:179], a[96:99], a[236:239], 0\n"
    "ds_read_u16 v195, v195\n"
    "v_cvt_pk_bf16_f32 v224, v112, v113\n"
    "v_mfma_f32_16x16x32_bf16 v[176:179], a[144:147], a[240:243], v[176:179]\n"
    "s_waitcnt lgkmcnt(8)\n"
    "v_or_b32_e32 v190, v190, v206\n"
    "v_lshlrev_b32_sdwa v206, 1, v181 dst_sel:DWORD dst_unused:UNUSED_PAD src0_sel:DWORD src1_sel:WORD_1\n"
    "v_mfma_f32_16x16x32_bf16 v[176:179], a[56:59], a[244:247], v[176:179]\n"
    "ds_read_u16_d16_hi v203, v203\n"
    "v_cvt_pk_bf16_f32 v225, v114, v115\n"
    "v_mfma_f32_16x16x32_bf16 v[96:99], a[0:3], v[184:187], 0\n"
    "s_nop 0\n"
    "ds_read_b128 v[4:7], v244 offset:2048\n"
    "v_cvt_pk_bf16_f32 v226, v116, v117\n"
    "v_mfma_f32_16x16x32_bf16 v[100:103], a[4:7], v[184:187], 0\n"
    "s_waitcnt lgkmcnt(8)\n"
    "ds_read_u16 v197, v197\n"
    "v_cvt_pk_bf16_f32 v227, v118, v119\n"
    "v_mfma_f32_16x16x32_bf16 v[112:115], a[8:11], v[184:187], 0\n"
    "v_cvt_pk_bf16_f32 v228, v128, v129\n"
    "v_cvt_pk_bf16_f32 v229, v130, v131\n"
    "v_or_b32_e64 v191, v191, v207\n"
    "v_mfma_f32_16x16x32_bf16 v[116:119], a[12:15], v[184:187], 0\n"
    "ds_read_u16_d16_hi v205, v205\n"
    "v_lshlrev_b32_sdwa v207, 1, v183 dst_sel:DWORD dst_unused:UNUSED_PAD src0_sel:DWORD src1_sel:WORD_1\n"
    "v_mfma_f32_16x16x32_bf16 v[180:183], a[100:103], a[236:239], 0\n"
    "s_waitcnt lgkmcnt(8)\n"
    "v_cvt_pk_bf16_f32 v230, v132, v133\n"
    "v_or_b32_e32 v193, v193, v201\n"
    "v_mfma_f32_16x16x32_bf16 v[180:183], a[148:151], a[240:243], v[180:183]\n"
    "ds_read_u16 v198, v198\n"
    "v_cvt_pk_bf16_f32 v231, v134, v135\n"
    "v_mfma_f32_16x16x32_bf16 v[180:183], a[60:63], a[244:247], v[180:183]\n"
    "ds_read_u16_d16_hi v206, v206\n"
    "v_cvt_pk_bf16_f32 v216, v104, v105\n"
    "v_cvt_pk_bf16_f32 v217, v106, v107\n"
    "v_mfma_f32_16x16x32_bf16 v[104:107], a[0:3], v[188:191], 0\n"
    "s_waitcnt lgkmcnt(7)\n"
    "v_cvt_pk_bf16_f32 v218, v108, v109\n"
    "v_cvt_pk_bf16_f32 v219, v110, v111\n"
    "v_mfma_f32_16x16x32_bf16 v[128:131], v[0:3], v[184:187], 0\n"
    "s_nop 0\n"
    "ds_read_u16 v199, v199\n"
    "v_or_b32_e64 v194, v194, v202\n"
    "v_mfma_f32_16x16x32_bf16 v[108:111], a[4:7], v[188:191], 0\n"
    "v_cvt_pk_bf16_f32 v220, v120, v121\n"
    "v_cvt_pk_bf16_f32 v221, v122, v123\n"
    "v_cvt_pk_bf16_f32 v222, v124, v125\n"
    "v_cvt_pk_bf16_f32 v223, v126, v127\n"
    "v_pk_fma_f16 v232, v192, v192, s48\n"
    "v_mfma_f32_16x16x32_bf16 v[120:123], a[8:11], v[188:191], 0\n"
    "ds_read_u16_d16_hi v207, v207\n"
    "v_cvt_pk_bf16_f32 v160, v136, v137\n"
    "v_mfma_f32_16x16x32_bf16 v[124:127], a[12:15], v[188:191], 0\n"
    "s_waitcnt lgkmcnt(6)\n"
    "v_cvt_pk_bf16_f32 v161, v138, v139\n"
    "v_cvt_pk_bf16_f32 v162, v140, v141\n"
    "v_mfma_f32_16x16x32_bf16 v[132:135], v[4:7], v[184:187], 0\n"
    "v_or_b32_e32 v195, v195, v203\n"
    "v_cvt_pk_bf16_f32 v163, v142, v143\n"
    "v_pk_mul_f16 v232, v192, v232\n"
    "v_mfma_f32_16x16x32_bf16 v[136:139], v[0:3], v[188:191], 0\n"
    "v_exp_f16_e64 v234, v232\n"
    "v_mfma_f32_16x16x32_bf16 v[140:143], v[4:7], v[188:191], 0\n"
    "v_exp_f16_sdwa v234, v232 dst_sel:WORD_1 dst_unused:UNUSED_PRESERVE src0_sel:WORD_1\n"
    "v_lshlrev_b32_sdwa v184, 1, v168 dst_sel:DWORD dst_unused:UNUSED_PAD src0_sel:DWORD src1_sel:WORD_1\n"
    "v_pk_fma_f16 v234, v234, s49, s49\n"
    "v_lshlrev_b32_sdwa v200, 1, v169 dst_sel:DWORD dst_unused:UNUSED_PAD src0_sel:DWORD src1_sel:WORD_1\n"
    "s_waitcnt lgkmcnt(4)\n"
    "v_or_b32_e32 v197, v197, v205\n"
    "v_lshlrev_b32_sdwa v185, 1, v170 dst_sel:DWORD dst_unused:UNUSED_PAD src0_sel:DWORD src1_sel:WORD_1\n"
    "v_lshlrev_b32_sdwa v201, 1, v171 dst_sel:DWORD dst_unused:UNUSED_PAD src0_sel:DWORD src1_sel:WORD_1\n"
    "v_mfma_f32_16x16x32_bf16 v[168:171], a[104:107], a[224:227], 0\n"
    "ds_read_u16 v184, v184\n"
    "ds_read_u16_d16_hi v200, v200\n"
    "v_mfma_f32_16x16x32_bf16 v[168:171], a[152:155], a[228:231], v[168:171]\n"
    "ds_read_u16 v185, v185\n"
    "ds_read_u16_d16_hi v201, v201\n"
    "v_lshlrev_b32_sdwa v186, 1, v172 dst_sel:DWORD dst_unused:UNUSED_PAD src0_sel:DWORD src1_sel:WORD_1\n"
    "v_lshlrev_b32_sdwa v202, 1, v173 dst_sel:DWORD dst_unused:UNUSED_PAD src0_sel:DWORD src1_sel:WORD_1\n"
    "v_lshlrev_b32_sdwa v187, 1, v174 dst_sel:DWORD dst_unused:UNUSED_PAD src0_sel:DWORD src1_sel:WORD_1\n"
    "v_lshlrev_b32_sdwa v203, 1, v175 dst_sel:DWORD dst_unused:UNUSED_PAD src0_sel:DWORD src1_sel:WORD_1\n"
    "v_rcp_f16_e32 v232, v234\n"
    "v_mfma_f32_16x16x32_bf16 v[168:171], a[64:67], a[232:235], v[168:171]\n"
    "s_waitcnt lgkmcnt(6)\n"
    "ds_read_u16 v186, v186\n"
    "buffer_store_dwordx4 v[224:227], v245, s[68:71], s59 offen offset:64 sc1\n"
    "v_or_b32_e32 v198, v198, v206\n"
    "v_rcp_f16_sdwa v232, v234 dst_sel:WORD_1 dst_unused:UNUSED_PRESERVE src0_sel:WORD_1\n"
    "v_mfma_f32_16x16x32_bf16 v[172:175], a[108:111], a[224:227], 0\n"
    "s_waitcnt lgkmcnt(5)\n"
    "ds_read_b128 v[8:11], v244 offset:4096\n"
    "ds_read_u16_d16_hi v202, v202\n"
    "v_mfma_f32_16x16x32_bf16 v[172:175], a[156:159], a[228:231], v[172:175]\n"
    "v_lshlrev_b32_sdwa v188, 1, v176 dst_sel:DWORD dst_unused:UNUSED_PAD src0_sel:DWORD src1_sel:WORD_1\n"
    "v_lshlrev_b32_sdwa v204, 1, v177 dst_sel:DWORD dst_unused:UNUSED_PAD src0_sel:DWORD src1_sel:WORD_1\n"
    "v_lshlrev_b32_sdwa v189, 1, v178 dst_sel:DWORD dst_unused:UNUSED_PAD src0_sel:DWORD src1_sel:WORD_1\n"
    "v_lshlrev_b32_sdwa v205, 1, v179 dst_sel:DWORD dst_unused:UNUSED_PAD src0_sel:DWORD src1_sel:WORD_1\n"
    "v_lshlrev_b32_sdwa v190, 1, v180 dst_sel:DWORD dst_unused:UNUSED_PAD src0_sel:DWORD src1_sel:WORD_1\n"
    "v_lshlrev_b32_sdwa v206, 1, v181 dst_sel:DWORD dst_unused:UNUSED_PAD src0_sel:DWORD src1_sel:WORD_1\n"
    "v_lshlrev_b32_sdwa v191, 1, v182 dst_sel:DWORD dst_unused:UNUSED_PAD src0_sel:DWORD src1_sel:WORD_1\n"
    "v_fma_mix_f32 v234, v192, v232, 0 op_sel_hi:[1,1,0]\n"
    "v_mfma_f32_16x16x32_bf16 v[172:175], a[68:71], a[232:235], v[172:175]\n"
    "ds_read_u16 v187, v187\n"
    "ds_read_b128 v[12:15], v244 offset:6144\n"
    "v_or_b32_e64 v199, v199, v207\n"
    "v_lshlrev_b32_sdwa v207, 1, v183 dst_sel:DWORD dst_unused:UNUSED_PAD src0_sel:DWORD src1_sel:WORD_1\n"
    "v_fma_mix_f32 v232, v192, v232, 0 op_sel:[1,1,0] op_sel_hi:[1,1,0]\n"
    "v_mfma_f32_16x16x32_bf16 v[176:179], a[104:107], a[236:239], 0\n"
    "s_nop 0\n"
    "ds_read_u16_d16_hi v203, v203\n"
    "v_cvt_pk_bf16_f32 v192, v234, v232\n"
    "v_mfma_f32_16x16x32_bf16 v[176:179], a[152:155], a[240:243], v[176:179]\n"
    "s_waitcnt lgkmcnt(8)\n"
    "ds_read_u16 v188, v188\n"
    "v_pk_fma_f16 v232, v196, v196, s48\n"
    "v_mfma_f32_16x16x32_bf16 v[176:179], a[64:67], a[244:247], v[176:179]\n"
    "v_pk_mul_f16 v232, v196, v232\n"
    "v_or_b32_e64 v184, v184, v200\n"
    "v_mfma_f32_16x16x32_bf16 v[180:183], a[108:111], a[236:239], 0\n"
    "ds_read_u16_d16_hi v204, v204\n"
    "v_exp_f16_e32 v234, v232\n"
    "v_mfma_f32_16x16x32_bf16 v[180:183], a[156:159], a[240:243], v[180:183]\n"
    "s_waitcnt lgkmcnt(8)\n"
    "ds_read_u16 v189, v189\n"
    "v_exp_f16_sdwa v234, v232 dst_sel:WORD_1 dst_unused:UNUSED_PRESERVE src0_sel:WORD_1\n"
    "v_mfma_f32_16x16x32_bf16 v[180:183], a[68:71], a[244:247], v[180:183]\n"
    "v_pk_fma_f16 v234, v234, s49, s49\n"
    "v_or_b32_e64 v185, v185, v201\n"
    "v_mfma_f32_16x16x32_bf16 v[96:99], a[16:19], v[192:195], v[96:99]\n"
    "ds_read_u16_d16_hi v205, v205\n"
    "v_rcp_f16_e64 v232, v234\n"
    "v_mfma_f32_16x16x32_bf16 v[100:103], a[20:23], v[192:195], v[100:103]\n"
    "ds_read_u16 v190, v190\n"
    "v_rcp_f16_sdwa v232, v234 dst_sel:WORD_1 dst_unused:UNUSED_PRESERVE src0_sel:WORD_1\n"
    "v_mfma_f32_16x16x32_bf16 v[112:115], a[24:27], v[192:195], v[112:115]\n"
    "s_waitcnt lgkmcnt(8)\n"
    "v_fma_mix_f32 v234, v196, v232, 0 op_sel_hi:[1,1,0]\n"
    "v_fma_mix_f32 v232, v196, v232, 0 op_sel:[1,1,0] op_sel_hi:[1,1,0]\n"
    "v_mfma_f32_16x16x32_bf16 v[116:119], a[28:31], v[192:195], v[116:119]\n"
    "s_waitcnt lgkmcnt(6)\n"
    "ds_read_u16_d16_hi v206, v206\n"
    "v_cvt_pk_bf16_f32 v196, v234, v232\n"
    "v_mfma_f32_16x16x32_bf16 v[128:131], v[8:11], v[192:195], v[128:131]\n"
    "ds_read_u16 v191, v191\n"
    "v_lshlrev_b32_sdwa v201, 1, v171 dst_sel:DWORD dst_unused:UNUSED_PAD src0_sel:DWORD src1_sel:WORD_1\n"
    "v_mfma_f32_16x16x32_bf16 v[132:135], v[12:15], v[192:195], v[132:135]\n"
    "v_cvt_pk_f16_f32 v192, v168, v169\n"
    "v_lshlrev_b32_sdwa v193, 1, v170 dst_sel:DWORD dst_unused:UNUSED_PAD src0_sel:DWORD src1_sel:WORD_1\n"
    "v_mfma_f32_16x16x32_bf16 v[104:107], a[16:19], v[196:199], v[104:107]\n"
    "s_waitcnt lgkmcnt(7)\n"
    "ds_read_u16_d16_hi v207, v207\n"
    "v_lshlrev_b32_sdwa v194, 1, v172 dst_sel:DWORD dst_unused:UNUSED_PAD src0_sel:DWORD src1_sel:WORD_1\n"
    "v_mfma_f32_16x16x32_bf16 v[108:111], a[20:23], v[196:199], v[108:111]\n"
    "v_or_b32_e32 v186, v186, v202\n"
    "v_lshlrev_b32_sdwa v202, 1, v173 dst_sel:DWORD dst_unused:UNUSED_PAD src0_sel:DWORD src1_sel:WORD_1\n"
    "v_mfma_f32_16x16x32_bf16 v[120:123], a[24:27], v[196:199], v[120:123]\n"
    "ds_read_b128 v[52:55], v244 offset:34816\n"
    "v_lshlrev_b32_sdwa v195, 1, v174 dst_sel:DWORD dst_unused:UNUSED_PAD src0_sel:DWORD src1_sel:WORD_1\n"
    "v_mfma_f32_16x16x32_bf16 v[124:127], a[28:31], v[196:199], v[124:127]\n"
    "ds_read_u16 v193, v193\n"
    "v_or_b32_e32 v187, v187, v203\n"
    "v_mfma_f32_16x16x32_bf16 v[136:139], v[8:11], v[196:199], v[136:139]\n"
    "s_waitcnt lgkmcnt(8)\n"
    "v_lshlrev_b32_sdwa v203, 1, v175 dst_sel:DWORD dst_unused:UNUSED_PAD src0_sel:DWORD src1_sel:WORD_1\n"
    "v_or_b32_e32 v188, v188, v204\n"
    "v_mfma_f32_16x16x32_bf16 v[140:143], v[12:15], v[196:199], v[140:143]\n"
    "ds_read_u16_d16_hi v201, v201\n"
    "buffer_store_dwordx4 v[228:231], v245, s[68:71], s59 offen offset:128 sc1\n"
    "v_mfma_f32_16x16x32_bf16 v[168:171], a[112:115], a[224:227], 0\n"
    "s_waitcnt lgkmcnt(7)\n"
    "ds_read_u16 v194, v194\n"
    "v_cvt_pk_f16_f32 v196, v176, v177\n"
    "v_mfma_f32_16x16x32_bf16 v[168:171], a[160:163], a[228:231], v[168:171]\n"
    "ds_read_u16_d16_hi v202, v202\n"
    "v_lshlrev_b32_sdwa v197, 1, v178 dst_sel:DWORD dst_unused:UNUSED_PAD src0_sel:DWORD src1_sel:WORD_1\n"
    "v_mfma_f32_16x16x32_bf16 v[168:171], a[72:75], a[232:235], v[168:171]\n"
    "v_lshlrev_b32_sdwa v198, 1, v180 dst_sel:DWORD dst_unused:UNUSED_PAD src0_sel:DWORD src1_sel:WORD_1\n"
    "v_lshlrev_b32_sdwa v199, 1, v182 dst_sel:DWORD dst_unused:UNUSED_PAD src0_sel:DWORD src1_sel:WORD_1\n"
    "v_mfma_f32_16x16x32_bf16 v[172:175], a[116:119], a[224:227], 0\n"
    "ds_read_b128 v[16:19], v244 offset:8192\n"
    "v_or_b32_e32 v189, v189, v205\n"
    "v_mfma_f32_16x16x32_bf16 v[172:175], a[164:167], a[228:231], v[172:175]\n"
    "s_waitcnt lgkmcnt(8)\n"
    "ds_read_u16 v195, v195\n"
    "v_lshlrev_b32_sdwa v205, 1, v179 dst_sel:DWORD dst_unused:UNUSED_PAD src0_sel:DWORD src1_sel:WORD_1\n"
    "v_mfma_f32_16x16x32_bf16 v[176:179], a[112:115], a[236:239], 0\n"
    "v_or_b32_e64 v190, v190, v206\n"
    "v_lshlrev_b32_sdwa v206, 1, v181 dst_sel:DWORD dst_unused:UNUSED_PAD src0_sel:DWORD src1_sel:WORD_1\n"
    "v_mfma_f32_16x16x32_bf16 v[176:179], a[160:163], a[240:243], v[176:179]\n"
    "ds_read_u16_d16_hi v203, v203\n"
    "v_pk_fma_f16 v232, v192, v192, s48\n"
    "v_mfma_f32_16x16x32_bf16 v[176:179], a[72:75], a[244:247], v[176:179]\n"
    "s_waitcnt lgkmcnt(7)\n"
    "ds_read_b128 v[20:23], v244 offset:10240\n"
    "v_or_b32_e32 v191, v191, v207\n"
    "v_mfma_f32_16x16x32_bf16 v[172:175], v[52:55], a[232:235], v[172:175]\n"
    "ds_read_u16 v197, v197\n"
    "v_lshlrev_b32_sdwa v207, 1, v183 dst_sel:DWORD dst_unused:UNUSED_PAD src0_sel:DWORD src1_sel:WORD_1\n"
    "v_mfma_f32_16x16x32_bf16 v[180:183], a[116:119], a[236:239], 0\n"
    "v_pk_mul_f16 v232, v192, v232\n"
    "v_lshlrev_b32_sdwa v200, 1, v169 dst_sel:DWORD dst_unused:UNUSED_PAD src0_sel:DWORD src1_sel:WORD_1\n"
    "v_mfma_f32_16x16x32_bf16 v[180:183], a[164:167], a[240:243], v[180:183]\n"
    "ds_read_u16_d16_hi v205, v205\n"
    "v_exp_f16_e64 v234, v232\n"
    "v_mfma_f32_16x16x32_bf16 v[180:183], v[52:55], a[244:247], v[180:183]\n"
    "s_waitcnt lgkmcnt(8)\n"
    "v_or_b32_e32 v193, v193, v201\n"
    "v_exp_f16_sdwa v234, v232 dst_sel:WORD_1 dst_unused:UNUSED_PRESERVE src0_sel:WORD_1\n"
    "v_mfma_f32_16x16x32_bf16 v[96:99], a[32:35], v[184:187], v[96:99]\n"
    "ds_read_u16 v198, v198\n"
    "v_pk_fma_f16 v234, v234, s49, s49\n"
    "v_mfma_f32_16x16x32_bf16 v[100:103], a[36:39], v[184:187], v[100:103]\n"
    "ds_read_u16_d16_hi v206, v206\n"
    "v_lshlrev_b32_sdwa v201, 1, v171 dst_sel:DWORD dst_unused:UNUSED_PAD src0_sel:DWORD src1_sel:WORD_1\n"
    "v_mfma_f32_16x16x32_bf16 v[112:115], a[40:43], v[184:187], v[112:115]\n"
    "s_waitcnt lgkmcnt(7)\n"
    "v_or_b32_e32 v194, v194, v202\n"
    "v_lshlrev_b32_sdwa v202, 1, v173 dst_sel:DWORD dst_unused:UNUSED_PAD src0_sel:DWORD src1_sel:WORD_1\n"
    "v_mfma_f32_16x16x32_bf16 v[116:119], a[44:47], v[184:187], v[116:119]\n"
    "ds_read_u16 v199, v199\n"
    "v_rcp_f16_e64 v232, v234\n"
    "v_mfma_f32_16x16x32_bf16 v[128:131], v[16:19], v[184:187], v[128:131]\n"
    "ds_read_u16_d16_hi v207, v207\n"
    "v_rcp_f16_sdwa v232, v234 dst_sel:WORD_1 dst_unused:UNUSED_PRESERVE src0_sel:WORD_1\n"
    "v_mfma_f32_16x16x32_bf16 v[104:107], a[32:35], v[188:191], v[104:107]\n"
    "v_lshlrev_b32_sdwa v204, 1, v177 dst_sel:DWORD dst_unused:UNUSED_PAD src0_sel:DWORD src1_sel:WORD_1\n"
    "v_fma_mix_f32 v234, v192, v232, 0 op_sel_hi:[1,1,0]\n"
    "v_mfma_f32_16x16x32_bf16 v[108:111], a[36:39], v[188:191], v[108:111]\n"
    "s_waitcnt lgkmcnt(6)\n"
    "ds_read_b128 v[60:63], v244 offset:38912\n"
    "v_or_b32_e32 v195, v195, v203\n"
    "v_mfma_f32_16x16x32_bf16 v[132:135], v[20:23], v[184:187], v[132:135]\n"
    "ds_read_u16_d16_hi v200, v200\n"
    "v_lshlrev_b32_sdwa v184, 1, v168 dst_sel:DWORD dst_unused:UNUSED_PAD src0_sel:DWORD src1_sel:WORD_1\n"
    "v_mfma_f32_16x16x32_bf16 v[120:123], a[40:43], v[188:191], v[120:123]\n"
    "v_lshlrev_b32_sdwa v185, 1, v170 dst_sel:DWORD dst_unused:UNUSED_PAD src0_sel:DWORD src1_sel:WORD_1\n"
    "v_lshlrev_b32_sdwa v186, 1, v172 dst_sel:DWORD dst_unused:UNUSED_PAD src0_sel:DWORD src1_sel:WORD_1\n"
    "v_mfma_f32_16x16x32_bf16 v[124:127], a[44:47], v[188:191], v[124:127]\n"
    "ds_read_u16 v184, v184\n"
    "v_lshlrev_b32_sdwa v187, 1, v174 dst_sel:DWORD dst_unused:UNUSED_PAD src0_sel:DWORD src1_sel:WORD_1\n"
    "v_mfma_f32_16x16x32_bf16 v[136:139], v[16:19], v[188:191], v[136:139]\n"
    "ds_read_u16 v185, v185\n"
    "v_lshlrev_b32_sdwa v203, 1, v175 dst_sel:DWORD dst_unused:UNUSED_PAD src0_sel:DWORD src1_sel:WORD_1\n"
    "v_mfma_f32_16x16x32_bf16 v[140:143], v[20:23], v[188:191], v[140:143]\n"
    "s_waitcnt lgkmcnt(8)\n"
    "v_or_b32_e32 v197, v197, v205\n"
    "v_lshlrev_b32_sdwa v188, 1, v176 dst_sel:DWORD dst_unused:UNUSED_PAD src0_sel:DWORD src1_sel:WORD_1\n"
    "v_mfma_f32_16x16x32_bf16 v[168:171], a[120:123], a[224:227], 0\n"
    "ds_read_u16_d16_hi v201, v201\n"
    "buffer_store_dwordx4 v[216:219], v245, s[68:71], s39 offen offset:0 sc1\n"
    "v_mfma_f32_16x16x32_bf16 v[168:171], a[168:171], a[228:231], v[168:171]\n"
    "ds_read_u16 v186, v186\n"
    "v_lshlrev_b32_sdwa v189, 1, v178 dst_sel:DWORD dst_unused:UNUSED_PAD src0_sel:DWORD src1_sel:WORD_1\n"
    "v_mfma_f32_16x16x32_bf16 v[168:171], a[76:79], a[232:235], v[168:171]\n"
    "s_waitcnt lgkmcnt(8)\n"
    "v_or_b32_e32 v198, v198, v206\n"
    "v_lshlrev_b32_sdwa v205, 1, v179 dst_sel:DWORD dst_unused:UNUSED_PAD src0_sel:DWORD src1_sel:WORD_1\n"
    "v_mfma_f32_16x16x32_bf16 v[172:175], a[124:127], a[224:227], 0\n"
    "ds_read_b128 v[24:27], v244 offset:12288\n"
    "ds_read_u16_d16_hi v202, v202\n"
    "v_mfma_f32_16x16x32_bf16 v[172:175], a[172:175], a[228:231], v[172:175]\n"
    "v_lshlrev_b32_sdwa v190, 1, v180 dst_sel:DWORD dst_unused:UNUSED_PAD src0_sel:DWORD src1_sel:WORD_1\n"
    "v_lshlrev_b32_sdwa v206, 1, v181 dst_sel:DWORD dst_unused:UNUSED_PAD src0_sel:DWORD src1_sel:WORD_1\n"
    "s_waitcnt lgkmcnt(7)\n"
    "v_mfma_f32_16x16x32_bf16 v[172:175], v[60:63], a[232:235], v[172:175]\n"
    "s_nop 0\n"
    "ds_read_u16 v187, v187\n"
    "v_lshlrev_b32_sdwa v191, 1, v182 dst_sel:DWORD dst_unused:UNUSED_PAD src0_sel:DWORD src1_sel:WORD_1\n"
    "v_or_b32_e64 v199, v199, v207\n"
    "v_lshlrev_b32_sdwa v207, 1, v183 dst_sel:DWORD dst_unused:UNUSED_PAD src0_sel:DWORD src1_sel:WORD_1\n"
    "v_fma_mix_f32 v232, v192, v232, 0 op_sel:[1,1,0] op_sel_hi:[1,1,0]\n"
    "v_mfma_f32_16x16x32_bf16 v[176:179], a[120:123], a[236:239], 0\n"
    "ds_read_b128 v[28:31], v244 offset:14336\n"
    "v_cvt_pk_bf16_f32 v192, v234, v232\n"
    "v_mfma_f32_16x16x32_bf16 v[176:179], a[168:171], a[240:243], v[176:179]\n"
    "ds_read_u16_d16_hi v203, v203\n"
    "v_pk_fma_f16 v232, v196, v196, s48\n"
    "v_mfma_f32_16x16x32_bf16 v[176:179], a[76:79], a[244:247], v[176:179]\n"
    "v_pk_mul_f16 v232, v196, v232\n"
    "v_exp_f16_e32 v234, v232\n"
    "v_mfma_f32_16x16x32_bf16 v[180:183], a[124:127], a[236:239], 0\n"
    "s_waitcnt lgkmcnt(8)\n"
    "ds_read_u16 v188, v188\n"
    "v_exp_f16_sdwa v234, v232 dst_sel:WORD_1 dst_unused:UNUSED_PRESERVE src0_sel:WORD_1\n"
    "v_mfma_f32_16x16x32_bf16 v[180:183], a[172:175], a[240:243], v[180:183]\n"
    "ds_read_u16_d16_hi v204, v204\n"
    "v_pk_fma_f16 v234, v234, s49, s49\n"
    "v_mfma_f32_16x16x32_bf16 v[180:183], v[60:63], a[244:247], v[180:183]\n"
    "v_rcp_f16_e32 v232, v234\n"
    "v_mfma_f32_16x16x32_bf16 v[96:99], a[176:179], v[192:195], v[96:99]\n"
    "s_waitcnt lgkmcnt(8)\n"
    "ds_read_u16 v189, v189\n"
    "v_rcp_f16_sdwa v232, v234 dst_sel:WORD_1 dst_unused:UNUSED_PRESERVE src0_sel:WORD_1\n"
    "v_mfma_f32_16x16x32_bf16 v[100:103], a[180:183], v[192:195], v[100:103]\n"
    "ds_read_u16_d16_hi v205, v205\n"
    "v_fma_mix_f32 v234, v196, v232, 0 op_sel_hi:[1,1,0]\n"
    "v_mfma_f32_16x16x32_bf16 v[112:115], a[184:187], v[192:195], v[112:115]\n"
    "s_waitcnt lgkmcnt(8)\n"
    "v_fma_mix_f32 v232, v196, v232, 0 op_sel:[1,1,0] op_sel_hi:[1,1,0]\n"
    "v_or_b32_e32 v184, v184, v200\n"
    "v_mfma_f32_16x16x32_bf16 v[116:119], a[188:191], v[192:195], v[116:119]\n"
    "ds_read_b128 a[224:227], v243 offset:0\n"
    "v_cvt_pk_bf16_f32 v196, v234, v232\n"
    "v_mfma_f32_16x16x32_bf16 v[128:131], v[24:27], v[192:195], v[128:131]\n"
    "s_waitcnt lgkmcnt(8)\n"
    "ds_read_u16 v190, v190\n"
    "v_or_b32_e32 v185, v185, v201\n"
    "v_mfma_f32_16x16x32_bf16 v[104:107], a[176:179], v[196:199], v[104:107]\n"
    "s_waitcnt lgkmcnt(7)\n"
    "v_lshlrev_b32_sdwa v201, 1, v171 dst_sel:DWORD dst_unused:UNUSED_PAD src0_sel:DWORD src1_sel:WORD_1\n"
    "v_or_b32_e32 v186, v186, v202\n"
    "v_mfma_f32_16x16x32_bf16 v[132:135], v[28:31], v[192:195], v[132:135]\n"
    "ds_read_b128 a[228:231], v243 offset:1024\n"
    "v_cvt_pk_f16_f32 v192, v168, v169\n"
    "v_mfma_f32_16x16x32_bf16 v[108:111], a[180:183], v[196:199], v[108:111]\n"
    "s_nop 0\n"
    "ds_read_u16_d16_hi v206, v206\n"
    "v_lshlrev_b32_sdwa v193, 1, v170 dst_sel:DWORD dst_unused:UNUSED_PAD src0_sel:DWORD src1_sel:WORD_1\n"
    "v_mfma_f32_16x16x32_bf16 v[120:123], a[184:187], v[196:199], v[120:123]\n"
    "s_waitcnt lgkmcnt(8)\n"
    "v_lshlrev_b32_sdwa v194, 1, v172 dst_sel:DWORD dst_unused:UNUSED_PAD src0_sel:DWORD src1_sel:WORD_1\n"
    "v_lshlrev_b32_sdwa v202, 1, v173 dst_sel:DWORD dst_unused:UNUSED_PAD src0_sel:DWORD src1_sel:WORD_1\n"
    "v_mfma_f32_16x16x32_bf16 v[124:127], a[188:191], v[196:199], v[124:127]\n"
    "ds_read_b128 a[232:235], v243 offset:2048\n"
    "ds_read_u16 v191, v191\n"
    "v_mfma_f32_16x16x32_bf16 v[136:139], v[24:27], v[196:199], v[136:139]\n"
    "v_lshlrev_b32_sdwa v195, 1, v174 dst_sel:DWORD dst_unused:UNUSED_PAD src0_sel:DWORD src1_sel:WORD_1\n"
    "v_or_b32_e64 v187, v187, v203\n"
    "v_mfma_f32_16x16x32_bf16 v[140:143], v[28:31], v[196:199], v[140:143]\n"
    "ds_read_b128 a[236:239], v243 offset:3072\n"
    "v_lshlrev_b32_sdwa v203, 1, v175 dst_sel:DWORD dst_unused:UNUSED_PAD src0_sel:DWORD src1_sel:WORD_1\n"
    "v_cvt_pk_f16_f32 v196, v176, v177\n"
    "v_lshlrev_b32_sdwa v197, 1, v178 dst_sel:DWORD dst_unused:UNUSED_PAD src0_sel:DWORD src1_sel:WORD_1\n"
    "v_lshlrev_b32_sdwa v198, 1, v180 dst_sel:DWORD dst_unused:UNUSED_PAD src0_sel:DWORD src1_sel:WORD_1\n"
    "v_lshlrev_b32_sdwa v199, 1, v182 dst_sel:DWORD dst_unused:UNUSED_PAD src0_sel:DWORD src1_sel:WORD_1\n"
    "s_waitcnt lgkmcnt(9)\n"
    "v_or_b32_e32 v188, v188, v204\n"
    "ds_read_u16_d16_hi v207, v207\n"
    "ds_read_b128 a[240:243], v243 offset:4096\n"
    "v_pk_fma_f16 v232, v192, v192, s48\n"
    "v_pk_mul_f16 v232, v192, v232\n"
    "v_exp_f16_e64 v234, v232\n"
    "s_waitcnt lgkmcnt(8)\n"
    "v_mfma_f32_16x16x32_bf16 v[168:171], a[80:83], a[224:227], 0\n"
    "ds_read_b128 a[244:247], v243 offset:5120\n"
    "buffer_store_dwordx4 v[220:223], v245, s[68:71], s39 offen offset:64 sc1\n"
    "v_mfma_f32_16x16x32_bf16 v[172:175], a[84:87], a[224:227], 0\n"
    "s_waitcnt lgkmcnt(7)\n"
    "ds_read_b128 v[48:51], v244 offset:32768\n"
    "ds_read_u16 v193, v193\n"
    "v_mfma_f32_16x16x32_bf16 v[168:171], a[128:131], a[228:231], v[168:171]\n"
    "ds_read_u16_d16_hi v201, v201\n"
    "v_or_b32_e64 v189, v189, v205\n"
    "v_mfma_f32_16x16x32_bf16 v[172:175], a[132:135], a[228:231], v[172:175]\n"
    "v_lshlrev_b32_sdwa v205, 1, v179 dst_sel:DWORD dst_unused:UNUSED_PAD src0_sel:DWORD src1_sel:WORD_1\n"
    "v_exp_f16_sdwa v234, v232 dst_sel:WORD_1 dst_unused:UNUSED_PRESERVE src0_sel:WORD_1\n"
    "s_waitcnt lgkmcnt(8)\n"
    "v_mfma_f32_16x16x32_bf16 v[168:171], a[48:51], a[232:235], v[168:171]\n"
    "s_nop 0\n"
    "ds_read_u16 v194, v194\n"
    "v_or_b32_e32 v190, v190, v206\n"
    "v_mfma_f32_16x16x32_bf16 v[96:99], a[192:195], v[184:187], v[96:99]\n"
    "ds_read_u16_d16_hi v202, v202\n"
    "v_lshlrev_b32_sdwa v206, 1, v181 dst_sel:DWORD dst_unused:UNUSED_PAD src0_sel:DWORD src1_sel:WORD_1\n"
    "v_mfma_f32_16x16x32_bf16 v[100:103], a[196:199], v[184:187], v[100:103]\n"
    "s_waitcnt lgkmcnt(8)\n"
    "v_pk_fma_f16 v234, v234, s49, s49\n"
    "v_lshlrev_b32_sdwa v200, 1, v169 dst_sel:DWORD dst_unused:UNUSED_PAD src0_sel:DWORD src1_sel:WORD_1\n"
    "v_mfma_f32_16x16x32_bf16 v[176:179], a[80:83], a[236:239], 0\n"
    "ds_read_b128 v[32:35], v244 offset:16384\n"
    "v_rcp_f16_e64 v232, v234\n"
    "v_mfma_f32_16x16x32_bf16 v[112:115], a[200:203], v[184:187], v[112:115]\n"
    "ds_read_u16 v195, v195\n"
    "v_rcp_f16_sdwa v232, v234 dst_sel:WORD_1 dst_unused:UNUSED_PRESERVE src0_sel:WORD_1\n"
    "s_waitcnt lgkmcnt(8)\n"
    "v_mfma_f32_16x16x32_bf16 v[176:179], a[128:131], a[240:243], v[176:179]\n"
    "s_waitcnt lgkmcnt(7)\n"
    "ds_read_u16_d16_hi v203, v203\n"
    "v_or_b32_e32 v191, v191, v207\n"
    "s_barrier\n"
    "v_mfma_f32_16x16x32_bf16 v[176:179], a[48:51], a[244:247], v[176:179]\n"
    "s_waitcnt lgkmcnt(6)\n"
    "ds_write_b128 v253, v[64:67] offset:2048\n"
    "v_mfma_f32_16x16x32_bf16 v[172:175], v[48:51], a[232:235], v[172:175]\n"
    "s_waitcnt lgkmcnt(6)\n"
    "v_lshlrev_b32_sdwa v207, 1, v183 dst_sel:DWORD dst_unused:UNUSED_PAD src0_sel:DWORD src1_sel:WORD_1\n"
    "v_or_b32_e64 v193, v193, v201\n"
    "v_mfma_f32_16x16x32_bf16 v[180:183], a[84:87], a[236:239], 0\n"
    "v_lshlrev_b32_sdwa v201, 1, v171 dst_sel:DWORD dst_unused:UNUSED_PAD src0_sel:DWORD src1_sel:WORD_1\n"
    "v_lshlrev_b32_sdwa v204, 1, v177 dst_sel:DWORD dst_unused:UNUSED_PAD src0_sel:DWORD src1_sel:WORD_1\n"
    "v_mfma_f32_16x16x32_bf16 v[180:183], a[132:135], a[240:243], v[180:183]\n"
    "ds_read_b128 v[36:39], v244 offset:18432\n"
    "ds_write_b128 v253, v[68:71] offset:5120\n"
    "v_mfma_f32_16x16x32_bf16 v[180:183], v[48:51], a[244:247], v[180:183]\n"
    "s_waitcnt lgkmcnt(5)\n"
    "v_or_b32_e32 v194, v194, v202\n"
    "v_lshlrev_b32_sdwa v202, 1, v173 dst_sel:DWORD dst_unused:UNUSED_PAD src0_sel:DWORD src1_sel:WORD_1\n"
    "v_mfma_f32_16x16x32_bf16 v[116:119], a[204:207], v[184:187], v[116:119]\n"
    "v_fma_mix_f32 v234, v192, v232, 0 op_sel_hi:[1,1,0]\n"
    "v_fma_mix_f32 v232, v192, v232, 0 op_sel:[1,1,0] op_sel_hi:[1,1,0]\n"
    "v_mfma_f32_16x16x32_bf16 v[128:131], v[32:35], v[184:187], v[128:131]\n"
    "ds_read_u16 v197, v197\n"
    "ds_write_b128 v253, v[88:91] offset:0\n"
    "v_mfma_f32_16x16x32_bf16 v[104:107], a[192:195], v[188:191], v[104:107]\n"
    "s_waitcnt lgkmcnt(4)\n"
    "buffer_store_dwordx4 v[160:163], v245, s[68:71], s39 offen offset:128 sc1\n"
    "v_mfma_f32_16x16x32_bf16 v[108:111], a[196:199], v[188:191], v[108:111]\n"
    "s_nop 0\n"
    "ds_read_u16_d16_hi v205, v205\n"
    "ds_write_b128 v253, v[92:95] offset:3072\n"
    "v_mfma_f32_16x16x32_bf16 v[120:123], a[200:203], v[188:191], v[120:123]\n"
    "s_waitcnt lgkmcnt(4)\n"
    "v_or_b32_e32 v195, v195, v203\n"
    "v_lshlrev_b32_sdwa v203, 1, v175 dst_sel:DWORD dst_unused:UNUSED_PAD src0_sel:DWORD src1_sel:WORD_1\n"
    "v_mfma_f32_16x16x32_bf16 v[132:135], v[36:39], v[184:187], v[132:135]\n"
    "v_lshlrev_b32_sdwa v184, 1, v168 dst_sel:DWORD dst_unused:UNUSED_PAD src0_sel:DWORD src1_sel:WORD_1\n"
    "v_lshlrev_b32_sdwa v185, 1, v170 dst_sel:DWORD dst_unused:UNUSED_PAD src0_sel:DWORD src1_sel:WORD_1\n"
    "v_mfma_f32_16x16x32_bf16 v[124:127], a[204:207], v[188:191], v[124:127]\n"
    "ds_read_u16 v198, v198\n"
    "ds_write_b128 v253, v[72:75] offset:1024\n"
    "v_mfma_f32_16x16x32_bf16 v[136:139], v[32:35], v[188:191], v[136:139]\n"
    "v_lshlrev_b32_sdwa v186, 1, v172 dst_sel:DWORD dst_unused:UNUSED_PAD src0_sel:DWORD src1_sel:WORD_1\n"
    "v_lshlrev_b32_sdwa v187, 1, v174 dst_sel:DWORD dst_unused:UNUSED_PAD src0_sel:DWORD src1_sel:WORD_1\n"
    "v_mfma_f32_16x16x32_bf16 v[140:143], v[36:39], v[188:191], v[140:143]\n"
    "v_lshlrev_b32_sdwa v188, 1, v176 dst_sel:DWORD dst_unused:UNUSED_PAD src0_sel:DWORD src1_sel:WORD_1\n"
    "v_lshlrev_b32_sdwa v189, 1, v178 dst_sel:DWORD dst_unused:UNUSED_PAD src0_sel:DWORD src1_sel:WORD_1\n"
    "v_mfma_f32_16x16x32_bf16 v[168:171], a[88:91], a[224:227], 0\n"
    "ds_read_u16_d16_hi v206, v206\n"
    "ds_write_b128 v253, v[76:79] offset:4096\n"
    "v_mfma_f32_16x16x32_bf16 v[168:171], a[136:139], a[228:231], v[168:171]\n"
    "s_waitcnt lgkmcnt(4)\n"
    "v_or_b32_e32 v197, v197, v205\n"
    "v_lshlrev_b32_sdwa v205, 1, v179 dst_sel:DWORD dst_unused:UNUSED_PAD src0_sel:DWORD src1_sel:WORD_1\n"
    "v_mfma_f32_16x16x32_bf16 v[168:171], a[52:55], a[232:235], v[168:171]\n"
    "v_lshlrev_b32_sdwa v190, 1, v180 dst_sel:DWORD dst_unused:UNUSED_PAD src0_sel:DWORD src1_sel:WORD_1\n"
    "v_lshlrev_b32_sdwa v191, 1, v182 dst_sel:DWORD dst_unused:UNUSED_PAD src0_sel:DWORD src1_sel:WORD_1\n"
    "v_mfma_f32_16x16x32_bf16 v[172:175], a[92:95], a[224:227], 0\n"
    "ds_read_u16 v199, v199\n"
    "ds_read_u16_d16_hi v207, v207\n"
    "v_mfma_f32_16x16x32_bf16 v[172:175], a[140:143], a[228:231], v[172:175]\n"
    "ds_read_u16 v184, v184\n"
    "ds_read_u16_d16_hi v200, v200\n"
    "v_mfma_f32_16x16x32_bf16 v[172:175], v[56:59], a[232:235], v[172:175]\n"
    "v_cvt_pk_bf16_f32 v192, v234, v232\n"
    "v_pk_fma_f16 v232, v196, v196, s48\n"
    "v_mfma_f32_16x16x32_bf16 v[176:179], a[88:91], a[236:239], 0\n"
    "ds_read_u16 v185, v185\n"
    "v_pk_mul_f16 v232, v196, v232\n"
    "v_mfma_f32_16x16x32_bf16 v[176:179], a[136:139], a[240:243], v[176:179]\n"
    "v_exp_f16_e64 v234, v232\n"
    "v_mfma_f32_16x16x32_bf16 v[176:179], a[52:55], a[244:247], v[176:179]\n"
    "ds_read_u16_d16_hi v201, v201\n"
    "v_exp_f16_sdwa v234, v232 dst_sel:WORD_1 dst_unused:UNUSED_PRESERVE src0_sel:WORD_1\n"
    "v_mfma_f32_16x16x32_bf16 v[96:99], a[208:211], v[192:195], v[96:99]\n"
    "s_waitcnt lgkmcnt(6)\n"
    "v_or_b32_e32 v198, v198, v206\n"
    "v_lshlrev_b32_sdwa v206, 1, v181 dst_sel:DWORD dst_unused:UNUSED_PAD src0_sel:DWORD src1_sel:WORD_1\n"
    "v_pk_fma_f16 v234, v234, s49, s49\n"
    "v_mfma_f32_16x16x32_bf16 v[100:103], a[212:215], v[192:195], v[100:103]\n"
    "ds_read_u16 v186, v186\n"
    "v_rcp_f16_e64 v232, v234\n"
    "v_mfma_f32_16x16x32_bf16 v[112:115], a[216:219], v[192:195], v[112:115]\n"
    "ds_read_b128 v[40:43], v244 offset:20480\n"
    "v_rcp_f16_sdwa v232, v234 dst_sel:WORD_1 dst_unused:UNUSED_PRESERVE src0_sel:WORD_1\n"
    "v_mfma_f32_16x16x32_bf16 v[116:119], a[220:223], v[192:195], v[116:119]\n"
    "ds_read_u16_d16_hi v202, v202\n"
    "v_fma_mix_f32 v234, v196, v232, 0 op_sel_hi:[1,1,0]\n"
    "v_fma_mix_f32 v232, v196, v232, 0 op_sel:[1,1,0] op_sel_hi:[1,1,0]\n"
    "v_cvt_pk_bf16_f32 v96, v96, v97\n"
    "v_cvt_pk_bf16_f32 v196, v234, v232\n"
    "v_cvt_pk_bf16_f32 v97, v98, v99\n"
    "v_cvt_pk_bf16_f32 v98, v100, v101\n"
    "s_waitcnt lgkmcnt(7)\n"
    "v_or_b32_e32 v199, v199, v207\n"
    "v_lshlrev_b32_sdwa v207, 1, v183 dst_sel:DWORD dst_unused:UNUSED_PAD src0_sel:DWORD src1_sel:WORD_1\n"
    "v_mfma_f32_16x16x32_bf16 v[180:183], a[92:95], a[236:239], 0\n"
    "ds_read_u16 v187, v187\n"
    "ds_read_b128 v[44:47], v244 offset:22528\n"
    "v_mfma_f32_16x16x32_bf16 v[180:183], a[140:143], a[240:243], v[180:183]\n"
    "ds_read_u16_d16_hi v203, v203\n"
    "v_cvt_pk_bf16_f32 v99, v102, v103\n"
    "v_mfma_f32_16x16x32_bf16 v[180:183], v[56:59], a[244:247], v[180:183]\n"
    "v_cvt_pk_bf16_f32 v112, v112, v113\n"
    "v_cvt_pk_bf16_f32 v113, v114, v115\n"
    "v_mfma_f32_16x16x32_bf16 v[104:107], a[208:211], v[196:199], v[104:107]\n"
    "s_waitcnt lgkmcnt(8)\n"
    "ds_read_u16 v188, v188\n"
    "v_or_b32_e32 v184, v184, v200\n"
    "v_mfma_f32_16x16x32_bf16 v[108:111], a[212:215], v[196:199], v[108:111]\n"
    "v_cvt_pk_bf16_f32 v114, v116, v117\n"
    "v_cvt_pk_bf16_f32 v115, v118, v119\n"
    "v_mfma_f32_16x16x32_bf16 v[120:123], a[216:219], v[196:199], v[120:123]\n"
    "ds_read_u16_d16_hi v204, v204\n"
    "v_cvt_pk_bf16_f32 v104, v104, v105\n"
    "v_mfma_f32_16x16x32_bf16 v[124:127], a[220:223], v[196:199], v[124:127]\n"
    "s_waitcnt lgkmcnt(8)\n"
    "ds_read_u16 v189, v189\n"
    "v_or_b32_e32 v185, v185, v201\n"
    "v_lshlrev_b32_sdwa v201, 1, v171 dst_sel:DWORD dst_unused:UNUSED_PAD src0_sel:DWORD src1_sel:WORD_1\n"
    "v_cvt_pk_bf16_f32 v105, v106, v107\n"
    "v_cvt_pk_bf16_f32 v106, v108, v109\n"
    "s_waitcnt lgkmcnt(7)\n"
    "v_mfma_f32_16x16x32_bf16 v[128:131], v[40:43], v[192:195], v[128:131]\n"
    "s_nop 0\n"
    "ds_read_u16_d16_hi v205, v205\n"
    "v_cvt_pk_bf16_f32 v107, v110, v111\n"
    "v_mfma_f32_16x16x32_bf16 v[136:139], v[40:43], v[196:199], v[136:139]\n"
    "v_cvt_pk_bf16_f32 v120, v120, v121\n"
    "v_cvt_pk_bf16_f32 v121, v122, v123\n"
    "v_cvt_pk_bf16_f32 v122, v124, v125\n"
    "v_cvt_pk_bf16_f32 v123, v126, v127\n"
    "ds_read_u16 v190, v190\n"
    "s_waitcnt lgkmcnt(8)\n"
    "v_or_b32_e32 v186, v186, v202\n"
    "v_lshlrev_b32_sdwa v202, 1, v173 dst_sel:DWORD dst_unused:UNUSED_PAD src0_sel:DWORD src1_sel:WORD_1\n"
    "v_cvt_pk_bf16_f32 v128, v128, v129\n"
    "s_waitcnt lgkmcnt(6)\n"
    "v_mfma_f32_16x16x32_bf16 v[132:135], v[44:47], v[192:195], v[132:135]\n"
    "s_nop 0\n"
    "v_cvt_pk_f16_f32 v192, v168, v169\n"
    "v_lshlrev_b32_sdwa v193, 1, v170 dst_sel:DWORD dst_unused:UNUSED_PAD src0_sel:DWORD src1_sel:WORD_1\n"
    "v_mfma_f32_16x16x32_bf16 v[140:143], v[44:47], v[196:199], v[140:143]\n"
    "s_nop 0\n"
    "ds_write_b128 v242, v[96:99] offset:24640\n"
    "v_mfma_f32_16x16x32_bf16 v[168:171], a[96:99], a[224:227], 0\n"
    "s_waitcnt lgkmcnt(6)\n"
    "v_lshlrev_b32_sdwa v194, 1, v172 dst_sel:DWORD dst_unused:UNUSED_PAD src0_sel:DWORD src1_sel:WORD_1\n"
    "v_lshlrev_b32_sdwa v195, 1, v174 dst_sel:DWORD dst_unused:UNUSED_PAD src0_sel:DWORD src1_sel:WORD_1\n"
    "v_mfma_f32_16x16x32_bf16 v[168:171], a[144:147], a[228:231], v[168:171]\n"
    "v_cvt_pk_f16_f32 v196, v176, v177\n"
    "v_lshlrev_b32_sdwa v197, 1, v178 dst_sel:DWORD dst_unused:UNUSED_PAD src0_sel:DWORD src1_sel:WORD_1\n"
    "v_mfma_f32_16x16x32_bf16 v[168:171], a[56:59], a[232:235], v[168:171]\n"
    "ds_read_u16_d16_hi v206, v206\n"
    "ds_read_u16 v191, v191\n"
    "v_lshlrev_b32_sdwa v198, 1, v180 dst_sel:DWORD dst_unused:UNUSED_PAD src0_sel:DWORD src1_sel:WORD_1\n"
    "v_lshlrev_b32_sdwa v199, 1, v182 dst_sel:DWORD dst_unused:UNUSED_PAD src0_sel:DWORD src1_sel:WORD_1\n"
    "v_or_b32_e64 v187, v187, v203\n"
    "v_lshlrev_b32_sdwa v203, 1, v175 dst_sel:DWORD dst_unused:UNUSED_PAD src0_sel:DWORD src1_sel:WORD_1\n"
    "v_mfma_f32_16x16x32_bf16 v[172:175], a[100:103], a[224:227], 0\n"
    "s_waitcnt lgkmcnt(6)\n"
    "ds_write_b128 v242, v[112:115] offset:26688\n"
    "v_mfma_f32_16x16x32_bf16 v[172:175], a[148:151], a[228:231], v[172:175]\n"
    "v_or_b32_e32 v188, v188, v204\n"
    "v_cvt_pk_bf16_f32 v129, v130, v131\n"
    "v_mfma_f32_16x16x32_bf16 v[172:175], a[60:63], a[232:235], v[172:175]\n"
    "s_waitcnt lgkmcnt(5)\n"
    "v_or_b32_e32 v189, v189, v205\n"
    "v_lshlrev_b32_sdwa v205, 1, v179 dst_sel:DWORD dst_unused:UNUSED_PAD src0_sel:DWORD src1_sel:WORD_1\n"
    "v_mfma_f32_16x16x32_bf16 v[176:179], a[96:99], a[236:239], 0\n"
    "ds_read_u16_d16_hi v207, v207\n"
    "ds_write_b128 v242, v[104:107] offset:25664\n"
    "v_mfma_f32_16x16x32_bf16 v[176:179], a[144:147], a[240:243], v[176:179]\n"
    "v_cvt_pk_bf16_f32 v130, v132, v133\n"
    "v_cvt_pk_bf16_f32 v131, v134, v135\n"
    "v_mfma_f32_16x16x32_bf16 v[176:179], a[56:59], a[244:247], v[176:179]\n"
    "v_cvt_pk_bf16_f32 v136, v136, v137\n"
    "v_pk_fma_f16 v232, v192, v192, s48\n"
    "v_cvt_pk_bf16_f32 v137, v138, v139\n"
    "v_cvt_pk_bf16_f32 v138, v140, v141\n"
    "v_cvt_pk_bf16_f32 v139, v142, v143\n"
    "s_waitcnt lgkmcnt(4)\n"
    "v_or_b32_e32 v190, v190, v206\n"
    "v_mfma_f32_16x16x32_bf16 v[96:99], a[0:3], v[184:187], 0\n"
    "ds_read_u16 v193, v193\n"
    "ds_write_b128 v242, v[128:131] offset:28736\n"
    "v_mfma_f32_16x16x32_bf16 v[100:103], a[4:7], v[184:187], 0\n"
    "v_lshlrev_b32_sdwa v206, 1, v181 dst_sel:DWORD dst_unused:UNUSED_PAD src0_sel:DWORD src1_sel:WORD_1\n"
    "v_pk_mul_f16 v232, v192, v232\n"
    "v_mfma_f32_16x16x32_bf16 v[112:115], a[8:11], v[184:187], 0\n"
    "v_exp_f16_e64 v234, v232\n"
    "v_lshlrev_b32_sdwa v200, 1, v169 dst_sel:DWORD dst_unused:UNUSED_PAD src0_sel:DWORD src1_sel:WORD_1\n"
    "v_exp_f16_sdwa v234, v232 dst_sel:WORD_1 dst_unused:UNUSED_PRESERVE src0_sel:WORD_1\n"
    "v_lshlrev_b32_sdwa v204, 1, v177 dst_sel:DWORD dst_unused:UNUSED_PAD src0_sel:DWORD src1_sel:WORD_1\n"
    "v_pk_fma_f16 v234, v234, s49, s49\n"
    "s_waitcnt lgkmcnt(2)\n"
    "v_or_b32_e32 v191, v191, v207\n"
    "v_mfma_f32_16x16x32_bf16 v[116:119], a[12:15], v[184:187], 0\n"
    "ds_read_u16_d16_hi v201, v201\n"
    "ds_write_b128 v242, v[120:123] offset:27712\n"
    "v_mfma_f32_16x16x32_bf16 v[128:131], v[0:3], v[184:187], 0\n"
    "v_lshlrev_b32_sdwa v207, 1, v183 dst_sel:DWORD dst_unused:UNUSED_PAD src0_sel:DWORD src1_sel:WORD_1\n"
    "v_rcp_f16_e64 v232, v234\n"
    "v_mfma_f32_16x16x32_bf16 v[180:183], a[100:103], a[236:239], 0\n"
    "v_rcp_f16_sdwa v232, v234 dst_sel:WORD_1 dst_unused:UNUSED_PRESERVE src0_sel:WORD_1\n"
    "v_mfma_f32_16x16x32_bf16 v[180:183], a[148:151], a[240:243], v[180:183]\n"
    "ds_read_u16 v194, v194\n"
    "ds_write_b128 v242, v[136:139] offset:29760\n"
    "v_mfma_f32_16x16x32_bf16 v[180:183], a[60:63], a[244:247], v[180:183]\n"
    "v_fma_mix_f32 v234, v192, v232, 0 op_sel_hi:[1,1,0]\n"
    "v_fma_mix_f32 v232, v192, v232, 0 op_sel:[1,1,0] op_sel_hi:[1,1,0]\n"
    "v_mfma_f32_16x16x32_bf16 v[132:135], v[4:7], v[184:187], 0\n"
    "v_lshlrev_b32_sdwa v184, 1, v168 dst_sel:DWORD dst_unused:UNUSED_PAD src0_sel:DWORD src1_sel:WORD_1\n"
    "v_lshlrev_b32_sdwa v185, 1, v170 dst_sel:DWORD dst_unused:UNUSED_PAD src0_sel:DWORD src1_sel:WORD_1\n"
    "v_mfma_f32_16x16x32_bf16 v[104:107], a[0:3], v[188:191], 0\n"
    "ds_read_u16_d16_hi v202, v202\n"
    "ds_read_u16 v195, v195\n"
    "v_mfma_f32_16x16x32_bf16 v[108:111], a[4:7], v[188:191], 0\n"
    "ds_read_u16_d16_hi v203, v203\n"
    "ds_read_u16 v197, v197\n"
    "v_mfma_f32_16x16x32_bf16 v[120:123], a[8:11], v[188:191], 0\n"
    "s_waitcnt lgkmcnt(6)\n"
    "v_or_b32_e32 v193, v193, v201\n"
    "v_lshlrev_b32_sdwa v201, 1, v171 dst_sel:DWORD dst_unused:UNUSED_PAD src0_sel:DWORD src1_sel:WORD_1\n"
    "v_mfma_f32_16x16x32_bf16 v[124:127], a[12:15], v[188:191], 0\n"
    "ds_read_u16_d16_hi v205, v205\n"
    "v_lshlrev_b32_sdwa v186, 1, v172 dst_sel:DWORD dst_unused:UNUSED_PAD src0_sel:DWORD src1_sel:WORD_1\n"
    "v_mfma_f32_16x16x32_bf16 v[136:139], v[0:3], v[188:191], 0\n"
    "v_lshlrev_b32_sdwa v187, 1, v174 dst_sel:DWORD dst_unused:UNUSED_PAD src0_sel:DWORD src1_sel:WORD_1\n"
    "v_cvt_pk_bf16_f32 v192, v234, v232\n"
    "v_mfma_f32_16x16x32_bf16 v[140:143], v[4:7], v[188:191], 0\n"
    "s_nop 0\n"
    "ds_read_u16 v198, v198\n"
    "v_lshlrev_b32_sdwa v188, 1, v176 dst_sel:DWORD dst_unused:UNUSED_PAD src0_sel:DWORD src1_sel:WORD_1\n"
    "v_mfma_f32_16x16x32_bf16 v[168:171], a[104:107], a[224:227], 0\n"
    "s_waitcnt lgkmcnt(6)\n"
    "v_lshlrev_b32_sdwa v189, 1, v178 dst_sel:DWORD dst_unused:UNUSED_PAD src0_sel:DWORD src1_sel:WORD_1\n"
    "v_lshlrev_b32_sdwa v190, 1, v180 dst_sel:DWORD dst_unused:UNUSED_PAD src0_sel:DWORD src1_sel:WORD_1\n"
    "v_mfma_f32_16x16x32_bf16 v[168:171], a[152:155], a[228:231], v[168:171]\n"
    "s_barrier\n"
    "v_mfma_f32_16x16x32_bf16 v[168:171], a[64:67], a[232:235], v[168:171]\n"
    "s_waitcnt lgkmcnt(5)\n"
    "ds_read_u16_d16_hi v206, v206\n"
    "ds_read_u16 v199, v199\n"
    "v_or_b32_e64 v194, v194, v202\n"
    "ds_read_u16_d16_hi v207, v207\n"
    "ds_read_u16 v184, v184\n"
    "v_lshlrev_b32_sdwa v202, 1, v173 dst_sel:DWORD dst_unused:UNUSED_PAD src0_sel:DWORD src1_sel:WORD_1\n"
    "v_lshlrev_b32_sdwa v191, 1, v182 dst_sel:DWORD dst_unused:UNUSED_PAD src0_sel:DWORD src1_sel:WORD_1\n"
    "v_pk_fma_f16 v232, v196, v196, s48\n"
    "v_pk_mul_f16 v232, v196, v232\n"
    "v_exp_f16_e32 v234, v232\n"
    "s_waitcnt lgkmcnt(7)\n"
    "v_or_b32_e64 v195, v195, v203\n"
    "v_lshlrev_b32_sdwa v203, 1, v175 dst_sel:DWORD dst_unused:UNUSED_PAD src0_sel:DWORD src1_sel:WORD_1\n"
    "v_mfma_f32_16x16x32_bf16 v[172:175], a[108:111], a[224:227], 0\n"
    "ds_read_u16_d16_hi v200, v200\n"
    "ds_read_u16 v185, v185\n"
    "v_mfma_f32_16x16x32_bf16 v[172:175], a[156:159], a[228:231], v[172:175]\n"
    "s_nop 0\n"
    "v_exp_f16_sdwa v234, v232 dst_sel:WORD_1 dst_unused:UNUSED_PRESERVE src0_sel:WORD_1\n"
    "v_mfma_f32_16x16x32_bf16 v[172:175], a[68:71], a[232:235], v[172:175]\n"
    "s_waitcnt lgkmcnt(7)\n"
    "ds_read_u16_d16_hi v201, v201\n"
    "v_or_b32_e64 v197, v197, v205\n"
    "v_mfma_f32_16x16x32_bf16 v[96:99], a[16:19], v[192:195], v[96:99]\n"
    "v_lshlrev_b32_sdwa v205, 1, v179 dst_sel:DWORD dst_unused:UNUSED_PAD src0_sel:DWORD src1_sel:WORD_1\n"
    "v_pk_fma_f16 v234, v234, s49, s49\n"
    "v_mfma_f32_16x16x32_bf16 v[176:179], a[104:107], a[236:239], 0\n"
    "ds_read_u16 v186, v186\n"
    "v_rcp_f16_e32 v232, v234\n"
    "v_mfma_f32_16x16x32_bf16 v[176:179], a[152:155], a[240:243], v[176:179]\n"
    "v_rcp_f16_sdwa v232, v234 dst_sel:WORD_1 dst_unused:UNUSED_PRESERVE src0_sel:WORD_1\n"
    "v_mfma_f32_16x16x32_bf16 v[176:179], a[64:67], a[244:247], v[176:179]\n"
    "s_waitcnt lgkmcnt(7)\n"
    "ds_read_u16_d16_hi v202, v202\n"
    "v_or_b32_e64 v198, v198, v206\n"
    "v_mfma_f32_16x16x32_bf16 v[100:103], a[20:23], v[192:195], v[100:103]\n"
    "ds_read_u16 v187, v187\n"
    "v_lshlrev_b32_sdwa v206, 1, v181 dst_sel:DWORD dst_unused:UNUSED_PAD src0_sel:DWORD src1_sel:WORD_1\n"
    "v_mfma_f32_16x16x32_bf16 v[112:115], a[24:27], v[192:195], v[112:115]\n"
    "v_fma_mix_f32 v234, v196, v232, 0 op_sel_hi:[1,1,0]\n"
    "v_fma_mix_f32 v232, v196, v232, 0 op_sel:[1,1,0] op_sel_hi:[1,1,0]\n"
    "v_mfma_f32_16x16x32_bf16 v[116:119], a[28:31], v[192:195], v[116:119]\n"
    "s_waitcnt lgkmcnt(7)\n"
    "ds_read_u16_d16_hi v203, v203\n"
    "v_or_b32_e32 v199, v199, v207\n"
    "v_mfma_f32_16x16x32_bf16 v[128:131], v[8:11], v[192:195], v[128:131]\n"
    "v_lshlrev_b32_sdwa v207, 1, v183 dst_sel:DWORD dst_unused:UNUSED_PAD src0_sel:DWORD src1_sel:WORD_1\n"
    "v_cvt_pk_bf16_f32 v196, v234, v232\n"
    "v_mfma_f32_16x16x32_bf16 v[180:183], a[108:111], a[236:239], 0\n"
    "ds_read_u16 v188, v188\n"
    "v_mfma_f32_16x16x32_bf16 v[180:183], a[156:159], a[240:243], v[180:183]\n"
    "s_nop 0\n"
    "v_mfma_f32_16x16x32_bf16 v[180:183], a[68:71], a[244:247], v[180:183]\n"
    "s_waitcnt lgkmcnt(7)\n"
    "ds_read_u16_d16_hi v204, v204\n"
    "v_or_b32_e64 v184, v184, v200\n"
    "v_mfma_f32_16x16x32_bf16 v[132:135], v[12:15], v[192:195], v[132:135]\n"
    "ds_read_u16 v189, v189\n"
    "v_cvt_pk_f16_f32 v192, v168, v169\n"
    "v_mfma_f32_16x16x32_bf16 v[104:107], a[16:19], v[196:199], v[104:107]\n"
    "v_lshlrev_b32_sdwa v193, 1, v170 dst_sel:DWORD dst_unused:UNUSED_PAD src0_sel:DWORD src1_sel:WORD_1\n"
    "v_lshlrev_b32_sdwa v194, 1, v172 dst_sel:DWORD dst_unused:UNUSED_PAD src0_sel:DWORD src1_sel:WORD_1\n"
    "v_mfma_f32_16x16x32_bf16 v[108:111], a[20:23], v[196:199], v[108:111]\n"
    "s_waitcnt lgkmcnt(7)\n"
    "ds_read_u16_d16_hi v205, v205\n"
    "v_or_b32_e64 v185, v185, v201\n"
    "v_mfma_f32_16x16x32_bf16 v[120:123], a[24:27], v[196:199], v[120:123]\n"
    "s_waitcnt vmcnt(7)\n"
    "v_lshlrev_b32_sdwa v201, 1, v171 dst_sel:DWORD dst_unused:UNUSED_PAD src0_sel:DWORD src1_sel:WORD_1\n"
    "v_lshlrev_b32_sdwa v195, 1, v174 dst_sel:DWORD dst_unused:UNUSED_PAD src0_sel:DWORD src1_sel:WORD_1\n"
    "v_mfma_f32_16x16x32_bf16 v[124:127], a[28:31], v[196:199], v[124:127]\n"
    "ds_read_u16 v190, v190\n"
    "v_pk_fma_f16 v232, v192, v192, s48\n"
    "v_mfma_f32_16x16x32_bf16 v[136:139], v[8:11], v[196:199], v[136:139]\n"
    "v_pk_mul_f16 v232, v192, v232\n"
    "v_exp_f16_e32 v234, v232\n"
    "v_mfma_f32_16x16x32_bf16 v[140:143], v[12:15], v[196:199], v[140:143]\n"
    "s_waitcnt vmcnt(6) lgkmcnt(7)\n"
    "ds_read_u16_d16_hi v206, v206\n"
    "v_or_b32_e64 v186, v186, v202\n"
    "v_smfmac_f32_32x32x32_bf16 v[112:127], a[248:251], v[152:159], v241\n"
    "ds_read_u16 v191, v191\n"
    "v_lshlrev_b32_sdwa v202, 1, v173 dst_sel:DWORD dst_unused:UNUSED_PAD src0_sel:DWORD src1_sel:WORD_1\n"
    "v_cvt_pk_f16_f32 v196, v176, v177\n"
    "v_lshlrev_b32_sdwa v197, 1, v178 dst_sel:DWORD dst_unused:UNUSED_PAD src0_sel:DWORD src1_sel:WORD_1\n"
    "v_lshlrev_b32_sdwa v198, 1, v180 dst_sel:DWORD dst_unused:UNUSED_PAD src0_sel:DWORD src1_sel:WORD_1\n"
    "v_smfmac_f32_32x32x32_bf16 v[96:111], a[248:251], v[144:151], v241\n"
    "s_waitcnt lgkmcnt(7)\n"
    "ds_read_u16_d16_hi v207, v207\n"
    "v_lshlrev_b32_sdwa v199, 1, v182 dst_sel:DWORD dst_unused:UNUSED_PAD src0_sel:DWORD src1_sel:WORD_1\n"
    "v_or_b32_e32 v187, v187, v203\n"
    "v_lshlrev_b32_sdwa v203, 1, v175 dst_sel:DWORD dst_unused:UNUSED_PAD src0_sel:DWORD src1_sel:WORD_1\n"
    "v_exp_f16_sdwa v234, v232 dst_sel:WORD_1 dst_unused:UNUSED_PRESERVE src0_sel:WORD_1\n"
    "v_smfmac_f32_32x32x32_bf16 v[128:143], a[248:251], v[208:215], v241\n"
    "ds_read_u16 v193, v193\n"
    "v_pk_fma_f16 v234, v234, s49, s49\n"
    "v_rcp_f16_e32 v232, v234\n"
    "ds_read_u16_d16_hi v201, v201\n"
    "v_mfma_f32_16x16x32_bf16 v[168:171], a[112:115], a[224:227], 0\n"
    "s_waitcnt lgkmcnt(7)\n"
    "ds_read_u16 v194, v194\n"
    "v_or_b32_e32 v188, v188, v204\n"
    "v_mfma_f32_16x16x32_bf16 v[168:171], a[160:163], a[228:231], v[168:171]\n"
    "v_rcp_f16_sdwa v232, v234 dst_sel:WORD_1 dst_unused:UNUSED_PRESERVE src0_sel:WORD_1\n"
    "v_mfma_f32_16x16x32_bf16 v[168:171], a[72:75], a[232:235], v[168:171]\n"
    "s_waitcnt lgkmcnt(7)\n"
    "ds_read_u16_d16_hi v202, v202\n"
    "v_or_b32_e64 v189, v189, v205\n"
    "v_mfma_f32_16x16x32_bf16 v[172:175], a[116:119], a[224:227], 0\n"
    "v_lshlrev_b32_sdwa v205, 1, v179 dst_sel:DWORD dst_unused:UNUSED_PAD src0_sel:DWORD src1_sel:WORD_1\n"
    "v_fma_mix_f32 v234, v192, v232, 0 op_sel_hi:[1,1,0]\n"
    "v_mfma_f32_16x16x32_bf16 v[172:175], a[164:167], a[228:231], v[172:175]\n"
    "ds_read_b128 v[152:155], v243 offset:24640\n"
    "v_lshlrev_b32_sdwa v200, 1, v169 dst_sel:DWORD dst_unused:UNUSED_PAD src0_sel:DWORD src1_sel:WORD_1\n"
    "v_mfma_f32_16x16x32_bf16 v[172:175], v[52:55], a[232:235], v[172:175]\n"
    "ds_read_b128 v[156:159], v243 offset:25664\n"
    "v_fma_mix_f32 v232, v192, v232, 0 op_sel:[1,1,0] op_sel_hi:[1,1,0]\n"
    "v_mfma_f32_16x16x32_bf16 v[176:179], a[112:115], a[236:239], 0\n"
    "s_waitcnt lgkmcnt(8)\n"
    "ds_read_u16 v195, v195\n"
    "v_or_b32_e32 v190, v190, v206\n"
    "v_mfma_f32_16x16x32_bf16 v[176:179], a[160:163], a[240:243], v[176:179]\n"
    "v_lshlrev_b32_sdwa v206, 1, v181 dst_sel:DWORD dst_unused:UNUSED_PAD src0_sel:DWORD src1_sel:WORD_1\n"
    "v_cvt_pk_bf16_f32 v192, v234, v232\n"
    "v_mfma_f32_16x16x32_bf16 v[176:179], a[72:75], a[244:247], v[176:179]\n"
    "ds_read_u16_d16_hi v203, v203\n"
    "v_pk_fma_f16 v232, v196, v196, s48\n"
    "v_mfma_f32_16x16x32_bf16 v[112:115], a[40:43], v[184:187], v[112:115]\n"
    "s_waitcnt lgkmcnt(8)\n"
    "ds_read_u16 v197, v197\n"
    "v_or_b32_e32 v191, v191, v207\n"
    "v_mfma_f32_16x16x32_bf16 v[116:119], a[44:47], v[184:187], v[116:119]\n"
    "v_lshlrev_b32_sdwa v207, 1, v183 dst_sel:DWORD dst_unused:UNUSED_PAD src0_sel:DWORD src1_sel:WORD_1\n"
    "v_lshlrev_b32_sdwa v204, 1, v177 dst_sel:DWORD dst_unused:UNUSED_PAD src0_sel:DWORD src1_sel:WORD_1\n"
    "v_mfma_f32_16x16x32_bf16 v[180:183], a[116:119], a[236:239], 0\n"
    "ds_read_u16_d16_hi v205, v205\n"
    "v_pk_mul_f16 v232, v196, v232\n"
    "v_mfma_f32_16x16x32_bf16 v[180:183], a[164:167], a[240:243], v[180:183]\n"
    "v_exp_f16_e32 v234, v232\n"
    "v_mfma_f32_16x16x32_bf16 v[180:183], v[52:55], a[244:247], v[180:183]\n"
    "s_waitcnt lgkmcnt(8)\n"
    "ds_read_u16 v198, v198\n"
    "v_or_b32_e64 v193, v193, v201\n"
    "v_mfma_f32_16x16x32_bf16 v[128:131], v[16:19], v[184:187], v[128:131]\n"
    "v_lshlrev_b32_sdwa v201, 1, v171 dst_sel:DWORD dst_unused:UNUSED_PAD src0_sel:DWORD src1_sel:WORD_1\n"
    "v_exp_f16_sdwa v234, v232 dst_sel:WORD_1 dst_unused:UNUSED_PRESERVE src0_sel:WORD_1\n"
    "v_mfma_f32_16x16x32_bf16 v[132:135], v[20:23], v[184:187], v[132:135]\n"
    "ds_read_u16_d16_hi v206, v206\n"
    "v_pk_fma_f16 v234, v234, s49, s49\n"
    "v_mfma_f32_16x16x32_bf16 v[120:123], a[40:43], v[188:191], v[120:123]\n"
    "s_waitcnt lgkmcnt(7)\n"
    "ds_read_u16 v199, v199\n"
    "v_or_b32_e32 v194, v194, v202\n"
    "v_mfma_f32_16x16x32_bf16 v[124:127], a[44:47], v[188:191], v[124:127]\n"
    "s_waitcnt lgkmcnt(7)\n"
    "v_lshlrev_b32_sdwa v202, 1, v173 dst_sel:DWORD dst_unused:UNUSED_PAD src0_sel:DWORD src1_sel:WORD_1\n"
    "v_rcp_f16_e32 v232, v234\n"
    "v_smfmac_f32_32x32x32_bf16 v[96:111], a[248:251], v[152:159], v241\n"
    "ds_read_u16_d16_hi v207, v207\n"
    "v_rcp_f16_sdwa v232, v234 dst_sel:WORD_1 dst_unused:UNUSED_PRESERVE src0_sel:WORD_1\n"
    "ds_read_b128 v[152:155], v243 offset:26688\n"
    "v_mfma_f32_16x16x32_bf16 v[136:139], v[16:19], v[188:191], v[136:139]\n"
    "ds_read_b128 v[156:159], v243 offset:27712\n"
    "v_fma_mix_f32 v234, v196, v232, 0 op_sel_hi:[1,1,0]\n"
    "v_mfma_f32_16x16x32_bf16 v[140:143], v[20:23], v[188:191], v[140:143]\n"
    "s_waitcnt lgkmcnt(8)\n"
    "ds_read_u16_d16_hi v200, v200\n"
    "v_or_b32_e32 v195, v195, v203\n"
    "v_mfma_f32_16x16x32_bf16 v[96:99], a[32:35], v[184:187], v[96:99]\n"
    "v_lshlrev_b32_sdwa v203, 1, v175 dst_sel:DWORD dst_unused:UNUSED_PAD src0_sel:DWORD src1_sel:WORD_1\n"
    "v_fma_mix_f32 v232, v196, v232, 0 op_sel:[1,1,0] op_sel_hi:[1,1,0]\n"
    "v_mfma_f32_16x16x32_bf16 v[100:103], a[36:39], v[184:187], v[100:103]\n"
    "ds_read_u16_d16_hi v201, v201\n"
    "v_lshlrev_b32_sdwa v184, 1, v168 dst_sel:DWORD dst_unused:UNUSED_PAD src0_sel:DWORD src1_sel:WORD_1\n"
    "v_mfma_f32_16x16x32_bf16 v[104:107], a[32:35], v[188:191], v[104:107]\n"
    "s_waitcnt lgkmcnt(8)\n"
    "v_or_b32_e32 v197, v197, v205\n"
    "v_lshlrev_b32_sdwa v185, 1, v170 dst_sel:DWORD dst_unused:UNUSED_PAD src0_sel:DWORD src1_sel:WORD_1\n"
    "v_mfma_f32_16x16x32_bf16 v[108:111], a[36:39], v[188:191], v[108:111]\n"
    "ds_read_u16 v184, v184\n"
    "v_lshlrev_b32_sdwa v186, 1, v172 dst_sel:DWORD dst_unused:UNUSED_PAD src0_sel:DWORD src1_sel:WORD_1\n"
    "v_mfma_f32_16x16x32_bf16 v[168:171], a[120:123], a[224:227], 0\n"
    "ds_read_u16 v185, v185\n"
    "v_lshlrev_b32_sdwa v187, 1, v174 dst_sel:DWORD dst_unused:UNUSED_PAD src0_sel:DWORD src1_sel:WORD_1\n"
    "v_mfma_f32_16x16x32_bf16 v[168:171], a[168:171], a[228:231], v[168:171]\n"
    "v_lshlrev_b32_sdwa v188, 1, v176 dst_sel:DWORD dst_unused:UNUSED_PAD src0_sel:DWORD src1_sel:WORD_1\n"
    "v_lshlrev_b32_sdwa v189, 1, v178 dst_sel:DWORD dst_unused:UNUSED_PAD src0_sel:DWORD src1_sel:WORD_1\n"
    "v_mfma_f32_16x16x32_bf16 v[168:171], a[76:79], a[232:235], v[168:171]\n"
    "s_waitcnt lgkmcnt(8)\n"
    "ds_read_u16 v186, v186\n"
    "v_or_b32_e32 v198, v198, v206\n"
    "v_mfma_f32_16x16x32_bf16 v[172:175], a[124:127], a[224:227], 0\n"
    "v_lshlrev_b32_sdwa v205, 1, v179 dst_sel:DWORD dst_unused:UNUSED_PAD src0_sel:DWORD src1_sel:WORD_1\n"
    "v_lshlrev_b32_sdwa v190, 1, v180 dst_sel:DWORD dst_unused:UNUSED_PAD src0_sel:DWORD src1_sel:WORD_1\n"
    "v_mfma_f32_16x16x32_bf16 v[172:175], a[172:175], a[228:231], v[172:175]\n"
    "ds_read_u16_d16_hi v202, v202\n"
    "v_lshlrev_b32_sdwa v206, 1, v181 dst_sel:DWORD dst_unused:UNUSED_PAD src0_sel:DWORD src1_sel:WORD_1\n"
    "v_mfma_f32_16x16x32_bf16 v[172:175], v[60:63], a[232:235], v[172:175]\n"
    "s_waitcnt lgkmcnt(7)\n"
    "v_lshlrev_b32_sdwa v191, 1, v182 dst_sel:DWORD dst_unused:UNUSED_PAD src0_sel:DWORD src1_sel:WORD_1\n"
    "v_or_b32_e64 v199, v199, v207\n"
    "v_mfma_f32_16x16x32_bf16 v[176:179], a[120:123], a[236:239], 0\n"
    "s_waitcnt lgkmcnt(6)\n"
    "ds_read_u16 v187, v187\n"
    "v_lshlrev_b32_sdwa v207, 1, v183 dst_sel:DWORD dst_unused:UNUSED_PAD src0_sel:DWORD src1_sel:WORD_1\n"
    "v_smfmac_f32_32x32x32_bf16 v[112:127], a[248:251], v[152:159], v241\n"
    "ds_read_u16_d16_hi v203, v203\n"
    "v_cvt_pk_bf16_f32 v196, v234, v232\n"
    "v_mfma_f32_16x16x32_bf16 v[176:179], a[168:171], a[240:243], v[176:179]\n"
    "ds_read_u16 v188, v188\n"
    "v_mfma_f32_16x16x32_bf16 v[176:179], a[76:79], a[244:247], v[176:179]\n"
    "v_mfma_f32_16x16x32_bf16 v[180:183], a[124:127], a[236:239], 0\n"
    "ds_read_u16_d16_hi v204, v204\n"
    "v_mfma_f32_16x16x32_bf16 v[180:183], a[172:175], a[240:243], v[180:183]\n"
    "s_nop 0\n"
    "v_mfma_f32_16x16x32_bf16 v[180:183], v[60:63], a[244:247], v[180:183]\n"
    "s_waitcnt lgkmcnt(7)\n"
    "ds_read_b128 v[152:155], v243 offset:28736\n"
    "ds_read_b128 v[156:159], v243 offset:29760\n"
    "v_mfma_f32_16x16x32_bf16 v[96:99], a[176:179], v[192:195], v[96:99]\n"
    "s_waitcnt lgkmcnt(8)\n"
    "v_or_b32_e32 v184, v184, v200\n"
    "v_or_b32_e64 v185, v185, v201\n"
    "v_mfma_f32_16x16x32_bf16 v[100:103], a[180:183], v[192:195], v[100:103]\n"
    "ds_read_u16 v189, v189\n"
    "v_lshlrev_b32_sdwa v201, 1, v171 dst_sel:DWORD dst_unused:UNUSED_PAD src0_sel:DWORD src1_sel:WORD_1\n"
    "v_mfma_f32_16x16x32_bf16 v[112:115], a[184:187], v[192:195], v[112:115]\n"
    "ds_read_u16_d16_hi v205, v205\n"
    "v_mfma_f32_16x16x32_bf16 v[116:119], a[188:191], v[192:195], v[116:119]\n"
    "s_nop 0\n"
    "v_mfma_f32_16x16x32_bf16 v[128:131], v[24:27], v[192:195], v[128:131]\n"
    "s_waitcnt lgkmcnt(8)\n"
    "ds_read_b128 a[224:227], v242 offset:0\n"
    "v_or_b32_e64 v186, v186, v202\n"
    "v_mfma_f32_16x16x32_bf16 v[132:135], v[28:31], v[192:195], v[132:135]\n"
    "ds_read_u16 v190, v190\n"
    "v_cvt_pk_f16_f32 v192, v168, v169\n"
    "v_mfma_f32_16x16x32_bf16 v[104:107], a[176:179], v[196:199], v[104:107]\n"
    "s_nop 0\n"
    "v_lshlrev_b32_sdwa v193, 1, v170 dst_sel:DWORD dst_unused:UNUSED_PAD src0_sel:DWORD src1_sel:WORD_1\n"
    "v_lshlrev_b32_sdwa v194, 1, v172 dst_sel:DWORD dst_unused:UNUSED_PAD src0_sel:DWORD src1_sel:WORD_1\n"
    "v_mfma_f32_16x16x32_bf16 v[108:111], a[180:183], v[196:199], v[108:111]\n"
    "s_waitcnt lgkmcnt(8)\n"
    "ds_read_b128 a[228:231], v242 offset:1024\n"
    "v_lshlrev_b32_sdwa v202, 1, v173 dst_sel:DWORD dst_unused:UNUSED_PAD src0_sel:DWORD src1_sel:WORD_1\n"
    "v_mfma_f32_16x16x32_bf16 v[120:123], a[184:187], v[196:199], v[120:123]\n"
    "ds_read_u16_d16_hi v206, v206\n"
    "v_lshlrev_b32_sdwa v195, 1, v174 dst_sel:DWORD dst_unused:UNUSED_PAD src0_sel:DWORD src1_sel:WORD_1\n"
    "v_mfma_f32_16x16x32_bf16 v[124:127], a[188:191], v[196:199], v[124:127]\n"
    "v_or_b32_e64 v187, v187, v203\n"
    "v_lshlrev_b32_sdwa v203, 1, v175 dst_sel:DWORD dst_unused:UNUSED_PAD src0_sel:DWORD src1_sel:WORD_1\n"
    "v_mfma_f32_16x16x32_bf16 v[136:139], v[24:27], v[196:199], v[136:139]\n"
    "s_nop 0\n"
    "ds_read_b128 a[232:235], v242 offset:2048\n"
    "ds_read_u16 v191, v191\n"
    "v_mfma_f32_16x16x32_bf16 v[140:143], v[28:31], v[196:199], v[140:143]\n"
    "s_waitcnt lgkmcnt(9)\n"
    "v_cvt_pk_f16_f32 v196, v176, v177\n"
    "v_lshlrev_b32_sdwa v197, 1, v178 dst_sel:DWORD dst_unused:UNUSED_PAD src0_sel:DWORD src1_sel:WORD_1\n"
    "v_lshlrev_b32_sdwa v198, 1, v180 dst_sel:DWORD dst_unused:UNUSED_PAD src0_sel:DWORD src1_sel:WORD_1\n"
    "v_lshlrev_b32_sdwa v199, 1, v182 dst_sel:DWORD dst_unused:UNUSED_PAD src0_sel:DWORD src1_sel:WORD_1\n"
    "v_or_b32_e32 v188, v188, v204\n"
    "s_waitcnt lgkmcnt(8)\n"
    "v_smfmac_f32_32x32x32_bf16 v[128:143], a[248:251], v[152:159], v241\n"
    "ds_read_b128 a[236:239], v242 offset:3072\n"
    "ds_read_u16_d16_hi v207, v207\n"
    "v_pk_fma_f16 v232, v192, v192, s48\n"
    "v_pk_mul_f16 v232, v192, v232\n"
    "v_exp_f16_e64 v234, v232\n"
    "ds_read_b128 a[240:243], v242 offset:4096\n"
    "v_exp_f16_sdwa v234, v232 dst_sel:WORD_1 dst_unused:UNUSED_PRESERVE src0_sel:WORD_1\n"
    "s_waitcnt lgkmcnt(8)\n"
    "v_mfma_f32_16x16x32_bf16 v[168:171], a[80:83], a[224:227], 0\n"
    "s_nop 0\n"
    "ds_read_b128 a[244:247], v242 offset:5120\n"
    "ds_read_u16 v193, v193\n"
    "v_mfma_f32_16x16x32_bf16 v[172:175], a[84:87], a[224:227], 0\n"
    "v_or_b32_e64 v189, v189, v205\n"
    "v_lshlrev_b32_sdwa v205, 1, v179 dst_sel:DWORD dst_unused:UNUSED_PAD src0_sel:DWORD src1_sel:WORD_1\n"
    "v_pk_fma_f16 v234, v234, s49, s49\n"
    "s_waitcnt lgkmcnt(8)\n"
    "v_mfma_f32_16x16x32_bf16 v[168:171], a[128:131], a[228:231], v[168:171]\n"
    "s_nop 0\n"
    "ds_read_u16_d16_hi v201, v201\n"
    "v_rcp_f16_e64 v232, v234\n"
    "v_mfma_f32_16x16x32_bf16 v[172:175], a[132:135], a[228:231], v[172:175]\n"
    "ds_read_u16 v194, v194\n"
    "v_rcp_f16_sdwa v232, v234 dst_sel:WORD_1 dst_unused:UNUSED_PRESERVE src0_sel:WORD_1\n"
    "s_waitcnt lgkmcnt(8)\n"
    "v_mfma_f32_16x16x32_bf16 v[168:171], a[48:51], a[232:235], v[168:171]\n"
    "s_nop 0\n"
    "ds_read_u16_d16_hi v202, v202\n"
    "v_or_b32_e32 v190, v190, v206\n"
    "v_mfma_f32_16x16x32_bf16 v[172:175], v[48:51], a[232:235], v[172:175]\n"
    "v_lshlrev_b32_sdwa v206, 1, v181 dst_sel:DWORD dst_unused:UNUSED_PAD src0_sel:DWORD src1_sel:WORD_1\n"
    "v_fma_mix_f32 v234, v192, v232, 0 op_sel_hi:[1,1,0]\n"
    "v_mfma_f32_16x16x32_bf16 v[96:99], a[192:195], v[184:187], v[96:99]\n"
    "s_waitcnt lgkmcnt(7)\n"
    "ds_read_u16 v195, v195\n"
    "v_lshlrev_b32_sdwa v200, 1, v169 dst_sel:DWORD dst_unused:UNUSED_PAD src0_sel:DWORD src1_sel:WORD_1\n"
    "v_mfma_f32_16x16x32_bf16 v[176:179], a[80:83], a[236:239], 0\n"
    "v_fma_mix_f32 v232, v192, v232, 0 op_sel:[1,1,0] op_sel_hi:[1,1,0]\n"
    "v_cvt_pk_bf16_f32 v192, v234, v232\n"
    "v_mfma_f32_16x16x32_bf16 v[100:103], a[196:199], v[184:187], v[100:103]\n"
    "ds_read_u16_d16_hi v203, v203\n"
    "v_pk_fma_f16 v232, v196, v196, s48\n"
    "v_mfma_f32_16x16x32_bf16 v[112:115], a[200:203], v[184:187], v[112:115]\n"
    "s_waitcnt lgkmcnt(7)\n"
    "v_or_b32_e64 v191, v191, v207\n"
    "v_lshlrev_b32_sdwa v207, 1, v183 dst_sel:DWORD dst_unused:UNUSED_PAD src0_sel:DWORD src1_sel:WORD_1\n"
    "v_mfma_f32_16x16x32_bf16 v[176:179], a[128:131], a[240:243], v[176:179]\n"
    "s_waitcnt lgkmcnt(6)\n"
    "ds_read_u16 v197, v197\n"
    "v_pk_mul_f16 v232, v196, v232\n"
    "v_mfma_f32_16x16x32_bf16 v[176:179], a[48:51], a[244:247], v[176:179]\n"
    "ds_read_u16_d16_hi v205, v205\n"
    "v_exp_f16_e64 v234, v232\n"
    "v_mfma_f32_16x16x32_bf16 v[180:183], a[84:87], a[236:239], 0\n"
    "v_exp_f16_sdwa v234, v232 dst_sel:WORD_1 dst_unused:UNUSED_PRESERVE src0_sel:WORD_1\n"
    "v_mfma_f32_16x16x32_bf16 v[180:183], a[132:135], a[240:243], v[180:183]\n"
    "ds_read_u16 v198, v198\n"
    "v_pk_fma_f16 v234, v234, s49, s49\n"
    "v_mfma_f32_16x16x32_bf16 v[180:183], v[48:51], a[244:247], v[180:183]\n"
    "v_lshlrev_b32_sdwa v204, 1, v177 dst_sel:DWORD dst_unused:UNUSED_PAD src0_sel:DWORD src1_sel:WORD_1\n"
    "v_rcp_f16_e32 v232, v234\n"
    "v_mfma_f32_16x16x32_bf16 v[116:119], a[204:207], v[184:187], v[116:119]\n"
    "s_waitcnt lgkmcnt(7)\n"
    "ds_read_u16_d16_hi v206, v206\n"
    "v_or_b32_e64 v193, v193, v201\n"
    "v_mfma_f32_16x16x32_bf16 v[128:131], v[32:35], v[184:187], v[128:131]\n"
    "ds_read_u16 v199, v199\n"
    "v_lshlrev_b32_sdwa v201, 1, v171 dst_sel:DWORD dst_unused:UNUSED_PAD src0_sel:DWORD src1_sel:WORD_1\n"
    "v_mfma_f32_16x16x32_bf16 v[132:135], v[36:39], v[184:187], v[132:135]\n"
    "v_lshlrev_b32_sdwa v184, 1, v168 dst_sel:DWORD dst_unused:UNUSED_PAD src0_sel:DWORD src1_sel:WORD_1\n"
    "v_lshlrev_b32_sdwa v185, 1, v170 dst_sel:DWORD dst_unused:UNUSED_PAD src0_sel:DWORD src1_sel:WORD_1\n"
    "v_mfma_f32_16x16x32_bf16 v[104:107], a[192:195], v[188:191], v[104:107]\n"
    "s_waitcnt lgkmcnt(7)\n"
    "ds_read_u16_d16_hi v207, v207\n"
    "v_or_b32_e32 v194, v194, v202\n"
    "v_mfma_f32_16x16x32_bf16 v[108:111], a[196:199], v[188:191], v[108:111]\n"
    "v_lshlrev_b32_sdwa v186, 1, v172 dst_sel:DWORD dst_unused:UNUSED_PAD src0_sel:DWORD src1_sel:WORD_1\n"
    "v_lshlrev_b32_sdwa v202, 1, v173 dst_sel:DWORD dst_unused:UNUSED_PAD src0_sel:DWORD src1_sel:WORD_1\n"
    "v_mfma_f32_16x16x32_bf16 v[120:123], a[200:203], v[188:191], v[120:123]\n"
    "ds_read_u16 v184, v184\n"
    "v_lshlrev_b32_sdwa v187, 1, v174 dst_sel:DWORD dst_unused:UNUSED_PAD src0_sel:DWORD src1_sel:WORD_1\n"
    "v_mfma_f32_16x16x32_bf16 v[124:127], a[204:207], v[188:191], v[124:127]\n"
    "s_waitcnt lgkmcnt(7)\n"
    "ds_read_u16_d16_hi v200, v200\n"
    "v_or_b32_e32 v195, v195, v203\n"
    "v_mfma_f32_16x16x32_bf16 v[136:139], v[32:35], v[188:191], v[136:139]\n"
    "v_lshlrev_b32_sdwa v203, 1, v175 dst_sel:DWORD dst_unused:UNUSED_PAD src0_sel:DWORD src1_sel:WORD_1\n"
    "v_rcp_f16_sdwa v232, v234 dst_sel:WORD_1 dst_unused:UNUSED_PRESERVE src0_sel:WORD_1\n"
    "v_mfma_f32_16x16x32_bf16 v[140:143], v[36:39], v[188:191], v[140:143]\n"
    "ds_read_u16 v185, v185\n"
    "v_lshlrev_b32_sdwa v188, 1, v176 dst_sel:DWORD dst_unused:UNUSED_PAD src0_sel:DWORD src1_sel:WORD_1\n"
    "v_mfma_f32_16x16x32_bf16 v[168:171], a[88:91], a[224:227], 0\n"
    "v_lshlrev_b32_sdwa v189, 1, v178 dst_sel:DWORD dst_unused:UNUSED_PAD src0_sel:DWORD src1_sel:WORD_1\n"
    "v_lshlrev_b32_sdwa v190, 1, v180 dst_sel:DWORD dst_unused:UNUSED_PAD src0_sel:DWORD src1_sel:WORD_1\n"
    "v_mfma_f32_16x16x32_bf16 v[168:171], a[136:139], a[228:231], v[168:171]\n"
    "s_waitcnt lgkmcnt(7)\n"
    "ds_read_u16_d16_hi v201, v201\n"
    "v_or_b32_e32 v197, v197, v205\n"
    "v_mfma_f32_16x16x32_bf16 v[168:171], a[52:55], a[232:235], v[168:171]\n"
    "ds_read_u16 v186, v186\n"
    "v_lshlrev_b32_sdwa v205, 1, v179 dst_sel:DWORD dst_unused:UNUSED_PAD src0_sel:DWORD src1_sel:WORD_1\n"
    "v_mfma_f32_16x16x32_bf16 v[172:175], a[92:95], a[224:227], 0\n"
    "v_lshlrev_b32_sdwa v191, 1, v182 dst_sel:DWORD dst_unused:UNUSED_PAD src0_sel:DWORD src1_sel:WORD_1\n"
    "v_fma_mix_f32 v234, v196, v232, 0 op_sel_hi:[1,1,0]\n"
    "v_mfma_f32_16x16x32_bf16 v[172:175], a[140:143], a[228:231], v[172:175]\n"
    "s_waitcnt lgkmcnt(7)\n"
    "ds_read_u16_d16_hi v202, v202\n"
    "v_or_b32_e32 v198, v198, v206\n"
    "v_mfma_f32_16x16x32_bf16 v[172:175], v[56:59], a[232:235], v[172:175]\n"
    "v_lshlrev_b32_sdwa v206, 1, v181 dst_sel:DWORD dst_unused:UNUSED_PAD src0_sel:DWORD src1_sel:WORD_1\n"
    "v_fma_mix_f32 v232, v196, v232, 0 op_sel:[1,1,0] op_sel_hi:[1,1,0]\n"
    "v_mfma_f32_16x16x32_bf16 v[176:179], a[88:91], a[236:239], 0\n"
    "ds_read_u16 v187, v187\n"
    "v_cvt_pk_bf16_f32 v196, v234, v232\n"
    "v_mfma_f32_16x16x32_bf16 v[176:179], a[136:139], a[240:243], v[176:179]\n"
    "s_nop 0\n"
    "v_mfma_f32_16x16x32_bf16 v[176:179], a[52:55], a[244:247], v[176:179]\n"
    "s_waitcnt lgkmcnt(7)\n"
    "ds_read_u16_d16_hi v203, v203\n"
    "v_or_b32_e64 v199, v199, v207\n"
    "v_mfma_f32_16x16x32_bf16 v[96:99], a[208:211], v[192:195], v[96:99]\n"
    "ds_read_u16 v188, v188\n"
    "buffer_load_dwordx4 v[152:155], v245, s[40:43], s58 offen offset:64\n"
    "buffer_load_dwordx4 v[144:147], v245, s[40:43], s58 offen offset:0\n"
    "buffer_load_dwordx4 v[208:211], v245, s[40:43], s58 offen offset:128\n"
    "v_mfma_f32_16x16x32_bf16 v[100:103], a[212:215], v[192:195], v[100:103]\n"
    "v_lshlrev_b32_sdwa v207, 1, v183 dst_sel:DWORD dst_unused:UNUSED_PAD src0_sel:DWORD src1_sel:WORD_1\n"
    "ds_read_u16_d16_hi v204, v204\n"
    "v_mfma_f32_16x16x32_bf16 v[180:183], a[92:95], a[236:239], 0\n"
    "s_waitcnt lgkmcnt(8)\n"
    "v_or_b32_e32 v184, v184, v200\n"
    "v_cvt_pk_bf16_f32 v96, v96, v97\n"
    "v_mfma_f32_16x16x32_bf16 v[180:183], a[140:143], a[240:243], v[180:183]\n"
    "ds_read_u16 v189, v189\n"
    "v_cvt_pk_bf16_f32 v97, v98, v99\n"
    "v_mfma_f32_16x16x32_bf16 v[180:183], v[56:59], a[244:247], v[180:183]\n"
    "v_cvt_pk_bf16_f32 v98, v100, v101\n"
    "v_cvt_pk_bf16_f32 v99, v102, v103\n"
    "v_mfma_f32_16x16x32_bf16 v[112:115], a[216:219], v[192:195], v[112:115]\n"
    "s_waitcnt lgkmcnt(7)\n"
    "ds_read_u16_d16_hi v205, v205\n"
    "buffer_store_dwordx4 v[96:99], v245, s[44:47], s57 offen offset:0 sc1\n"
    "v_mfma_f32_16x16x32_bf16 v[116:119], a[220:223], v[192:195], v[116:119]\n"
    "s_nop 0\n"
    "ds_read_u16 v190, v190\n"
    "v_or_b32_e32 v185, v185, v201\n"
    "v_mfma_f32_16x16x32_bf16 v[128:131], v[40:43], v[192:195], v[128:131]\n"
    "s_waitcnt lgkmcnt(7)\n"
    "ds_read_u16_d16_hi v206, v206\n"
    "v_lshlrev_b32_sdwa v201, 1, v171 dst_sel:DWORD dst_unused:UNUSED_PAD src0_sel:DWORD src1_sel:WORD_1\n"
    "v_mfma_f32_16x16x32_bf16 v[132:135], v[44:47], v[192:195], v[132:135]\n"
    "v_cvt_pk_f16_f32 v192, v168, v169\n"
    "v_lshlrev_b32_sdwa v193, 1, v170 dst_sel:DWORD dst_unused:UNUSED_PAD src0_sel:DWORD src1_sel:WORD_1\n"
    "v_mfma_f32_16x16x32_bf16 v[104:107], a[208:211], v[196:199], v[104:107]\n"
    "ds_read_u16 v191, v191\n"
    "v_lshlrev_b32_sdwa v194, 1, v172 dst_sel:DWORD dst_unused:UNUSED_PAD src0_sel:DWORD src1_sel:WORD_1\n"
    "v_mfma_f32_16x16x32_bf16 v[108:111], a[212:215], v[196:199], v[108:111]\n"
    "v_or_b32_e32 v186, v186, v202\n"
    "v_lshlrev_b32_sdwa v202, 1, v173 dst_sel:DWORD dst_unused:UNUSED_PAD src0_sel:DWORD src1_sel:WORD_1\n"
    "v_mfma_f32_16x16x32_bf16 v[120:123], a[216:219], v[196:199], v[120:123]\n"
    "s_waitcnt lgkmcnt(7)\n"
    "ds_read_u16_d16_hi v207, v207\n"
    "v_lshlrev_b32_sdwa v195, 1, v174 dst_sel:DWORD dst_unused:UNUSED_PAD src0_sel:DWORD src1_sel:WORD_1\n"
    "v_mfma_f32_16x16x32_bf16 v[124:127], a[220:223], v[196:199], v[124:127]\n"
    "ds_read_u16 v193, v193\n"
    "v_or_b32_e32 v187, v187, v203\n"
    "v_mfma_f32_16x16x32_bf16 v[136:139], v[40:43], v[196:199], v[136:139]\n"
    "v_lshlrev_b32_sdwa v203, 1, v175 dst_sel:DWORD dst_unused:UNUSED_PAD src0_sel:DWORD src1_sel:WORD_1\n"
    "v_cvt_pk_bf16_f32 v224, v112, v113\n"
    "v_mfma_f32_16x16x32_bf16 v[140:143], v[44:47], v[196:199], v[140:143]\n"
    "s_waitcnt lgkmcnt(7)\n"
    "buffer_load_dwordx4 v[156:159], v245, s[40:43], s89 offen offset:64\n"
    "buffer_load_dwordx4 v[148:151], v245, s[40:43], s89 offen offset:0\n"
    "buffer_load_dwordx4 v[212:215], v245, s[40:43], s89 offen offset:128\n"
    "ds_read_u16_d16_hi v201, v201\n"
    "v_mfma_f32_16x16x32_bf16 v[168:171], a[96:99], a[224:227], 0\n"
    "ds_read_u16 v194, v194\n"
    "v_cvt_pk_f16_f32 v196, v176, v177\n"
    "v_mfma_f32_16x16x32_bf16 v[168:171], a[144:147], a[228:231], v[168:171]\n"
    "v_lshlrev_b32_sdwa v197, 1, v178 dst_sel:DWORD dst_unused:UNUSED_PAD src0_sel:DWORD src1_sel:WORD_1\n"
    "v_lshlrev_b32_sdwa v198, 1, v180 dst_sel:DWORD dst_unused:UNUSED_PAD src0_sel:DWORD src1_sel:WORD_1\n"
    "v_mfma_f32_16x16x32_bf16 v[168:171], a[56:59], a[232:235], v[168:171]\n"
    "s_waitcnt lgkmcnt(7)\n"
    "ds_read_u16_d16_hi v202, v202\n"
    "v_lshlrev_b32_sdwa v199, 1, v182 dst_sel:DWORD dst_unused:UNUSED_PAD src0_sel:DWORD src1_sel:WORD_1\n"
    "v_mfma_f32_16x16x32_bf16 v[172:175], a[100:103], a[224:227], 0\n"
    "v_or_b32_e32 v188, v188, v204\n"
    "v_or_b32_e64 v189, v189, v205\n"
    "v_mfma_f32_16x16x32_bf16 v[172:175], a[148:151], a[228:231], v[172:175]\n"
    "ds_read_u16 v195, v195\n"
    "v_lshlrev_b32_sdwa v205, 1, v179 dst_sel:DWORD dst_unused:UNUSED_PAD src0_sel:DWORD src1_sel:WORD_1\n"
    "v_mfma_f32_16x16x32_bf16 v[172:175], a[60:63], a[232:235], v[172:175]\n"
    "v_cvt_pk_bf16_f32 v225, v114, v115\n"
    "v_cvt_pk_bf16_f32 v226, v116, v117\n"
    "v_mfma_f32_16x16x32_bf16 v[176:179], a[96:99], a[236:239], 0\n"
    "s_waitcnt lgkmcnt(7)\n"
    "ds_read_u16_d16_hi v203, v203\n"
    "v_or_b32_e32 v190, v190, v206\n"
    "v_mfma_f32_16x16x32_bf16 v[176:179], a[144:147], a[240:243], v[176:179]\n"
    "ds_read_u16 v197, v197\n"
    "v_lshlrev_b32_sdwa v206, 1, v181 dst_sel:DWORD dst_unused:UNUSED_PAD src0_sel:DWORD src1_sel:WORD_1\n"
    "v_mfma_f32_16x16x32_bf16 v[176:179], a[56:59], a[244:247], v[176:179]\n"
    "v_cvt_pk_bf16_f32 v227, v118, v119\n"
    "v_cvt_pk_bf16_f32 v228, v128, v129\n"
    "v_mfma_f32_16x16x32_bf16 v[96:99], a[0:3], v[184:187], 0\n"
    "s_waitcnt lgkmcnt(7)\n"
    "ds_read_u16_d16_hi v205, v205\n"
    "v_cvt_pk_bf16_f32 v229, v130, v131\n"
    "v_mfma_f32_16x16x32_bf16 v[100:103], a[4:7], v[184:187], 0\n"
    "v_or_b32_e32 v191, v191, v207\n"
    "v_lshlrev_b32_sdwa v207, 1, v183 dst_sel:DWORD dst_unused:UNUSED_PAD src0_sel:DWORD src1_sel:WORD_1\n"
    "v_mfma_f32_16x16x32_bf16 v[180:183], a[100:103], a[236:239], 0\n"
    "ds_read_u16 v198, v198\n"
    "v_cvt_pk_bf16_f32 v230, v132, v133\n"
    "v_mfma_f32_16x16x32_bf16 v[180:183], a[148:151], a[240:243], v[180:183]\n"
    "v_cvt_pk_bf16_f32 v231, v134, v135\n"
    "v_cvt_pk_bf16_f32 v216, v104, v105\n"
    "v_mfma_f32_16x16x32_bf16 v[180:183], a[60:63], a[244:247], v[180:183]\n"
    "s_waitcnt lgkmcnt(7)\n"
    "ds_read_u16_d16_hi v206, v206\n"
    "v_or_b32_e32 v193, v193, v201\n"
    "v_mfma_f32_16x16x32_bf16 v[112:115], a[8:11], v[184:187], 0\n"
    "ds_read_u16 v199, v199\n"
    "v_cvt_pk_bf16_f32 v217, v106, v107\n"
    "v_mfma_f32_16x16x32_bf16 v[116:119], a[12:15], v[184:187], 0\n"
    "v_cvt_pk_bf16_f32 v218, v108, v109\n"
    "v_cvt_pk_bf16_f32 v219, v110, v111\n"
    "v_mfma_f32_16x16x32_bf16 v[128:131], v[0:3], v[184:187], 0\n"
    "s_waitcnt lgkmcnt(7)\n"
    "ds_read_u16_d16_hi v207, v207\n"
    "v_or_b32_e32 v194, v194, v202\n"
    "v_mfma_f32_16x16x32_bf16 v[132:135], v[4:7], v[184:187], 0\n"
    "v_cvt_pk_bf16_f32 v220, v120, v121\n"
    "v_cvt_pk_bf16_f32 v221, v122, v123\n"
    "v_mfma_f32_16x16x32_bf16 v[104:107], a[0:3], v[188:191], 0\n"
    "v_cvt_pk_bf16_f32 v222, v124, v125\n"
    "v_cvt_pk_bf16_f32 v223, v126, v127\n"
    "v_mfma_f32_16x16x32_bf16 v[108:111], a[4:7], v[188:191], 0\n"
    "v_pk_fma_f16 v232, v192, v192, s48\n"
    "v_cvt_pk_bf16_f32 v160, v136, v137\n"
    "v_mfma_f32_16x16x32_bf16 v[120:123], a[8:11], v[188:191], 0\n"
    "s_waitcnt lgkmcnt(6)\n"
    "v_cvt_pk_bf16_f32 v161, v138, v139\n"
    "v_cvt_pk_bf16_f32 v162, v140, v141\n"
    "v_mfma_f32_16x16x32_bf16 v[124:127], a[12:15], v[188:191], 0\n"
    "v_or_b32_e32 v195, v195, v203\n"
    "v_cvt_pk_bf16_f32 v163, v142, v143\n"
    "v_pk_mul_f16 v232, v192, v232\n"
    "v_mfma_f32_16x16x32_bf16 v[136:139], v[0:3], v[188:191], 0\n"
    "v_exp_f16_e64 v234, v232\n"
    "v_mfma_f32_16x16x32_bf16 v[140:143], v[4:7], v[188:191], 0\n"
    "s_waitcnt lgkmcnt(4)\n"
    "v_or_b32_e32 v197, v197, v205\n"
    "v_exp_f16_sdwa v234, v232 dst_sel:WORD_1 dst_unused:UNUSED_PRESERVE src0_sel:WORD_1\n"
    "v_lshlrev_b32_sdwa v184, 1, v168 dst_sel:DWORD dst_unused:UNUSED_PAD src0_sel:DWORD src1_sel:WORD_1\n"
    "v_pk_fma_f16 v234, v234, s49, s49\n"
    "v_lshlrev_b32_sdwa v200, 1, v169 dst_sel:DWORD dst_unused:UNUSED_PAD src0_sel:DWORD src1_sel:WORD_1\n"
    "v_lshlrev_b32_sdwa v185, 1, v170 dst_sel:DWORD dst_unused:UNUSED_PAD src0_sel:DWORD src1_sel:WORD_1\n"
    "v_lshlrev_b32_sdwa v201, 1, v171 dst_sel:DWORD dst_unused:UNUSED_PAD src0_sel:DWORD src1_sel:WORD_1\n"
    "v_mfma_f32_16x16x32_bf16 v[168:171], a[104:107], a[224:227], 0\n"
    "s_nop 0\n"
    "ds_read_u16 v184, v184\n"
    "ds_read_u16_d16_hi v200, v200\n"
    "v_mfma_f32_16x16x32_bf16 v[168:171], a[152:155], a[228:231], v[168:171]\n"
    "s_waitcnt lgkmcnt(4)\n"
    "ds_read_u16 v185, v185\n"
    "ds_read_u16_d16_hi v201, v201\n"
    "v_lshlrev_b32_sdwa v186, 1, v172 dst_sel:DWORD dst_unused:UNUSED_PAD src0_sel:DWORD src1_sel:WORD_1\n"
    "v_lshlrev_b32_sdwa v202, 1, v173 dst_sel:DWORD dst_unused:UNUSED_PAD src0_sel:DWORD src1_sel:WORD_1\n"
    "v_lshlrev_b32_sdwa v187, 1, v174 dst_sel:DWORD dst_unused:UNUSED_PAD src0_sel:DWORD src1_sel:WORD_1\n"
    "v_lshlrev_b32_sdwa v203, 1, v175 dst_sel:DWORD dst_unused:UNUSED_PAD src0_sel:DWORD src1_sel:WORD_1\n"
    "v_rcp_f16_e64 v232, v234\n"
    "v_mfma_f32_16x16x32_bf16 v[168:171], a[64:67], a[232:235], v[168:171]\n"
    "ds_read_u16 v186, v186\n"
    "v_or_b32_e32 v198, v198, v206\n"
    "v_rcp_f16_sdwa v232, v234 dst_sel:WORD_1 dst_unused:UNUSED_PRESERVE src0_sel:WORD_1\n"
    "v_mfma_f32_16x16x32_bf16 v[172:175], a[108:111], a[224:227], 0\n"
    "s_waitcnt lgkmcnt(5)\n"
    "ds_read_u16_d16_hi v202, v202\n"
    "v_lshlrev_b32_sdwa v188, 1, v176 dst_sel:DWORD dst_unused:UNUSED_PAD src0_sel:DWORD src1_sel:WORD_1\n"
    "v_lshlrev_b32_sdwa v204, 1, v177 dst_sel:DWORD dst_unused:UNUSED_PAD src0_sel:DWORD src1_sel:WORD_1\n"
    "v_lshlrev_b32_sdwa v189, 1, v178 dst_sel:DWORD dst_unused:UNUSED_PAD src0_sel:DWORD src1_sel:WORD_1\n"
    "v_lshlrev_b32_sdwa v205, 1, v179 dst_sel:DWORD dst_unused:UNUSED_PAD src0_sel:DWORD src1_sel:WORD_1\n"
    "v_lshlrev_b32_sdwa v190, 1, v180 dst_sel:DWORD dst_unused:UNUSED_PAD src0_sel:DWORD src1_sel:WORD_1\n"
    "v_lshlrev_b32_sdwa v206, 1, v181 dst_sel:DWORD dst_unused:UNUSED_PAD src0_sel:DWORD src1_sel:WORD_1\n"
    "v_lshlrev_b32_sdwa v191, 1, v182 dst_sel:DWORD dst_unused:UNUSED_PAD src0_sel:DWORD src1_sel:WORD_1\n"
    "v_fma_mix_f32 v234, v192, v232, 0 op_sel_hi:[1,1,0]\n"
    "v_mfma_f32_16x16x32_bf16 v[172:175], a[156:159], a[228:231], v[172:175]\n"
    "ds_read_u16 v187, v187\n"
    "v_or_b32_e64 v199, v199, v207\n"
    "v_mfma_f32_16x16x32_bf16 v[172:175], a[68:71], a[232:235], v[172:175]\n"
    "ds_read_u16_d16_hi v203, v203\n"
    "buffer_store_dwordx4 v[224:227], v245, s[44:47], s57 offen offset:64 sc1\n"
    "v_lshlrev_b32_sdwa v207, 1, v183 dst_sel:DWORD dst_unused:UNUSED_PAD src0_sel:DWORD src1_sel:WORD_1\n"
    "v_fma_mix_f32 v232, v192, v232, 0 op_sel:[1,1,0] op_sel_hi:[1,1,0]\n"
    "v_mfma_f32_16x16x32_bf16 v[176:179], a[104:107], a[236:239], 0\n"
    "s_nop 0\n"
    "ds_read_u16 v188, v188\n"
    "v_cvt_pk_bf16_f32 v192, v234, v232\n"
    "v_mfma_f32_16x16x32_bf16 v[176:179], a[152:155], a[240:243], v[176:179]\n"
    "s_waitcnt lgkmcnt(7)\n"
    "ds_read_u16_d16_hi v204, v204\n"
    "v_pk_fma_f16 v232, v196, v196, s48\n"
    "v_mfma_f32_16x16x32_bf16 v[176:179], a[64:67], a[244:247], v[176:179]\n"
    "v_pk_mul_f16 v232, v196, v232\n"
    "v_or_b32_e64 v184, v184, v200\n"
    "v_mfma_f32_16x16x32_bf16 v[180:183], a[108:111], a[236:239], 0\n"
    "ds_read_u16 v189, v189\n"
    "v_exp_f16_e32 v234, v232\n"
    "v_mfma_f32_16x16x32_bf16 v[180:183], a[156:159], a[240:243], v[180:183]\n"
    "s_waitcnt lgkmcnt(7)\n"
    "ds_read_u16_d16_hi v205, v205\n"
    "v_exp_f16_sdwa v234, v232 dst_sel:WORD_1 dst_unused:UNUSED_PRESERVE src0_sel:WORD_1\n"
    "v_mfma_f32_16x16x32_bf16 v[180:183], a[68:71], a[244:247], v[180:183]\n"
    "v_pk_fma_f16 v234, v234, s49, s49\n"
    "v_or_b32_e64 v185, v185, v201\n"
    "v_mfma_f32_16x16x32_bf16 v[96:99], a[16:19], v[192:195], v[96:99]\n"
    "ds_read_u16 v190, v190\n"
    "v_rcp_f16_e32 v232, v234\n"
    "v_mfma_f32_16x16x32_bf16 v[100:103], a[20:23], v[192:195], v[100:103]\n"
    "s_waitcnt lgkmcnt(7)\n"
    "ds_read_u16_d16_hi v206, v206\n"
    "v_rcp_f16_sdwa v232, v234 dst_sel:WORD_1 dst_unused:UNUSED_PRESERVE src0_sel:WORD_1\n"
    "v_mfma_f32_16x16x32_bf16 v[112:115], a[24:27], v[192:195], v[112:115]\n"
    "v_fma_mix_f32 v234, v196, v232, 0 op_sel_hi:[1,1,0]\n"
    "v_fma_mix_f32 v232, v196, v232, 0 op_sel:[1,1,0] op_sel_hi:[1,1,0]\n"
    "v_mfma_f32_16x16x32_bf16 v[116:119], a[28:31], v[192:195], v[116:119]\n"
    "s_nop 0\n"
    "ds_read_u16 v191, v191\n"
    "v_cvt_pk_bf16_f32 v196, v234, v232\n"
    "v_mfma_f32_16x16x32_bf16 v[128:131], v[8:11], v[192:195], v[128:131]\n"
    "s_waitcnt lgkmcnt(7)\n"
    "ds_read_u16_d16_hi v207, v207\n"
    "v_lshlrev_b32_sdwa v201, 1, v171 dst_sel:DWORD dst_unused:UNUSED_PAD src0_sel:DWORD src1_sel:WORD_1\n"
    "v_mfma_f32_16x16x32_bf16 v[132:135], v[12:15], v[192:195], v[132:135]\n"
    "v_cvt_pk_f16_f32 v192, v168, v169\n"
    "v_lshlrev_b32_sdwa v193, 1, v170 dst_sel:DWORD dst_unused:UNUSED_PAD src0_sel:DWORD src1_sel:WORD_1\n"
    "v_mfma_f32_16x16x32_bf16 v[104:107], a[16:19], v[196:199], v[104:107]\n"
    "ds_read_u16 v193, v193\n"
    "v_lshlrev_b32_sdwa v194, 1, v172 dst_sel:DWORD dst_unused:UNUSED_PAD src0_sel:DWORD src1_sel:WORD_1\n"
    "v_mfma_f32_16x16x32_bf16 v[108:111], a[20:23], v[196:199], v[108:111]\n"
    "v_or_b32_e32 v186, v186, v202\n"
    "v_lshlrev_b32_sdwa v202, 1, v173 dst_sel:DWORD dst_unused:UNUSED_PAD src0_sel:DWORD src1_sel:WORD_1\n"
    "v_mfma_f32_16x16x32_bf16 v[120:123], a[24:27], v[196:199], v[120:123]\n"
    "s_waitcnt lgkmcnt(7)\n"
    "ds_read_u16_d16_hi v201, v201\n"
    "v_lshlrev_b32_sdwa v195, 1, v174 dst_sel:DWORD dst_unused:UNUSED_PAD src0_sel:DWORD src1_sel:WORD_1\n"
    "v_mfma_f32_16x16x32_bf16 v[124:127], a[28:31], v[196:199], v[124:127]\n"
    "v_or_b32_e64 v187, v187, v203\n"
    "v_lshlrev_b32_sdwa v203, 1, v175 dst_sel:DWORD dst_unused:UNUSED_PAD src0_sel:DWORD src1_sel:WORD_1\n"
    "v_mfma_f32_16x16x32_bf16 v[136:139], v[8:11], v[196:199], v[136:139]\n"
    "ds_read_u16 v194, v194\n"
    "v_or_b32_e32 v188, v188, v204\n"
    "v_mfma_f32_16x16x32_bf16 v[140:143], v[12:15], v[196:199], v[140:143]\n"
    "s_waitcnt lgkmcnt(7)\n"
    "ds_read_u16_d16_hi v202, v202\n"
    "v_cvt_pk_f16_f32 v196, v176, v177\n"
    "v_mfma_f32_16x16x32_bf16 v[168:171], a[112:115], a[224:227], 0\n"
    "v_lshlrev_b32_sdwa v197, 1, v178 dst_sel:DWORD dst_unused:UNUSED_PAD src0_sel:DWORD src1_sel:WORD_1\n"
    "v_lshlrev_b32_sdwa v198, 1, v180 dst_sel:DWORD dst_unused:UNUSED_PAD src0_sel:DWORD src1_sel:WORD_1\n"
    "v_mfma_f32_16x16x32_bf16 v[168:171], a[160:163], a[228:231], v[168:171]\n"
    "ds_read_u16 v195, v195\n"
    "v_lshlrev_b32_sdwa v199, 1, v182 dst_sel:DWORD dst_unused:UNUSED_PAD src0_sel:DWORD src1_sel:WORD_1\n"
    "v_mfma_f32_16x16x32_bf16 v[168:171], a[72:75], a[232:235], v[168:171]\n"
    "v_or_b32_e64 v189, v189, v205\n"
    "v_lshlrev_b32_sdwa v205, 1, v179 dst_sel:DWORD dst_unused:UNUSED_PAD src0_sel:DWORD src1_sel:WORD_1\n"
    "v_mfma_f32_16x16x32_bf16 v[172:175], a[116:119], a[224:227], 0\n"
    "s_waitcnt lgkmcnt(7)\n"
    "ds_read_u16_d16_hi v203, v203\n"
    "v_or_b32_e32 v190, v190, v206\n"
    "v_mfma_f32_16x16x32_bf16 v[172:175], a[164:167], a[228:231], v[172:175]\n"
    "v_lshlrev_b32_sdwa v206, 1, v181 dst_sel:DWORD dst_unused:UNUSED_PAD src0_sel:DWORD src1_sel:WORD_1\n"
    "v_pk_fma_f16 v232, v192, v192, s48\n"
    "v_mfma_f32_16x16x32_bf16 v[172:175], v[52:55], a[232:235], v[172:175]\n"
    "ds_read_u16 v197, v197\n"
    "v_pk_mul_f16 v232, v192, v232\n"
    "v_mfma_f32_16x16x32_bf16 v[176:179], a[112:115], a[236:239], 0\n"
    "s_waitcnt lgkmcnt(7)\n"
    "ds_read_u16_d16_hi v205, v205\n"
    "buffer_store_dwordx4 v[228:231], v245, s[44:47], s57 offen offset:128 sc1\n"
    "v_mfma_f32_16x16x32_bf16 v[176:179], a[160:163], a[240:243], v[176:179]\n"
    "s_nop 0\n"
    "ds_read_u16 v198, v198\n"
    "v_or_b32_e64 v191, v191, v207\n"
    "v_mfma_f32_16x16x32_bf16 v[176:179], a[72:75], a[244:247], v[176:179]\n"
    "v_lshlrev_b32_sdwa v207, 1, v183 dst_sel:DWORD dst_unused:UNUSED_PAD src0_sel:DWORD src1_sel:WORD_1\n"
    "v_exp_f16_e32 v234, v232\n"
    "v_mfma_f32_16x16x32_bf16 v[180:183], a[116:119], a[236:239], 0\n"
    "s_waitcnt lgkmcnt(7)\n"
    "ds_read_u16_d16_hi v206, v206\n"
    "v_or_b32_e64 v193, v193, v201\n"
    "v_mfma_f32_16x16x32_bf16 v[180:183], a[164:167], a[240:243], v[180:183]\n"
    "ds_read_u16 v199, v199\n"
    "v_exp_f16_sdwa v234, v232 dst_sel:WORD_1 dst_unused:UNUSED_PRESERVE src0_sel:WORD_1\n"
    "v_mfma_f32_16x16x32_bf16 v[180:183], v[52:55], a[244:247], v[180:183]\n"
    "v_pk_fma_f16 v234, v234, s49, s49\n"
    "v_lshlrev_b32_sdwa v200, 1, v169 dst_sel:DWORD dst_unused:UNUSED_PAD src0_sel:DWORD src1_sel:WORD_1\n"
    "v_mfma_f32_16x16x32_bf16 v[96:99], a[32:35], v[184:187], v[96:99]\n"
    "s_waitcnt lgkmcnt(7)\n"
    "ds_read_u16_d16_hi v207, v207\n"
    "v_or_b32_e32 v194, v194, v202\n"
    "v_mfma_f32_16x16x32_bf16 v[100:103], a[36:39], v[184:187], v[100:103]\n"
    "v_lshlrev_b32_sdwa v201, 1, v171 dst_sel:DWORD dst_unused:UNUSED_PAD src0_sel:DWORD src1_sel:WORD_1\n"
    "v_lshlrev_b32_sdwa v202, 1, v173 dst_sel:DWORD dst_unused:UNUSED_PAD src0_sel:DWORD src1_sel:WORD_1\n"
    "v_mfma_f32_16x16x32_bf16 v[112:115], a[40:43], v[184:187], v[112:115]\n"
    "ds_read_u16_d16_hi v200, v200\n"
    "v_rcp_f16_e32 v232, v234\n"
    "v_mfma_f32_16x16x32_bf16 v[116:119], a[44:47], v[184:187], v[116:119]\n"
    "s_waitcnt lgkmcnt(7)\n"
    "ds_read_u16_d16_hi v201, v201\n"
    "v_or_b32_e64 v195, v195, v203\n"
    "v_mfma_f32_16x16x32_bf16 v[128:131], v[16:19], v[184:187], v[128:131]\n"
    "v_lshlrev_b32_sdwa v203, 1, v175 dst_sel:DWORD dst_unused:UNUSED_PAD src0_sel:DWORD src1_sel:WORD_1\n"
    "v_rcp_f16_sdwa v232, v234 dst_sel:WORD_1 dst_unused:UNUSED_PRESERVE src0_sel:WORD_1\n"
    "v_mfma_f32_16x16x32_bf16 v[132:135], v[20:23], v[184:187], v[132:135]\n"
    "ds_read_u16_d16_hi v202, v202\n"
    "v_lshlrev_b32_sdwa v184, 1, v168 dst_sel:DWORD dst_unused:UNUSED_PAD src0_sel:DWORD src1_sel:WORD_1\n"
    "v_mfma_f32_16x16x32_bf16 v[104:107], a[32:35], v[188:191], v[104:107]\n"
    "s_waitcnt lgkmcnt(7)\n"
    "ds_read_u16 v184, v184\n"
    "v_or_b32_e32 v197, v197, v205\n"
    "v_mfma_f32_16x16x32_bf16 v[108:111], a[36:39], v[188:191], v[108:111]\n"
    "v_lshlrev_b32_sdwa v185, 1, v170 dst_sel:DWORD dst_unused:UNUSED_PAD src0_sel:DWORD src1_sel:WORD_1\n"
    "v_lshlrev_b32_sdwa v186, 1, v172 dst_sel:DWORD dst_unused:UNUSED_PAD src0_sel:DWORD src1_sel:WORD_1\n"
    "v_mfma_f32_16x16x32_bf16 v[120:123], a[40:43], v[188:191], v[120:123]\n"
    "ds_read_u16 v185, v185\n"
    "v_lshlrev_b32_sdwa v187, 1, v174 dst_sel:DWORD dst_unused:UNUSED_PAD src0_sel:DWORD src1_sel:WORD_1\n"
    "v_mfma_f32_16x16x32_bf16 v[124:127], a[44:47], v[188:191], v[124:127]\n"
    "v_lshlrev_b32_sdwa v204, 1, v177 dst_sel:DWORD dst_unused:UNUSED_PAD src0_sel:DWORD src1_sel:WORD_1\n"
    "v_lshlrev_b32_sdwa v205, 1, v179 dst_sel:DWORD dst_unused:UNUSED_PAD src0_sel:DWORD src1_sel:WORD_1\n"
    "v_mfma_f32_16x16x32_bf16 v[136:139], v[16:19], v[188:191], v[136:139]\n"
    "s_waitcnt lgkmcnt(7)\n"
    "ds_read_u16 v186, v186\n"
    "v_or_b32_e32 v198, v198, v206\n"
    "v_mfma_f32_16x16x32_bf16 v[140:143], v[20:23], v[188:191], v[140:143]\n"
    "v_lshlrev_b32_sdwa v188, 1, v176 dst_sel:DWORD dst_unused:UNUSED_PAD src0_sel:DWORD src1_sel:WORD_1\n"
    "v_lshlrev_b32_sdwa v189, 1, v178 dst_sel:DWORD dst_unused:UNUSED_PAD src0_sel:DWORD src1_sel:WORD_1\n"
    "v_mfma_f32_16x16x32_bf16 v[168:171], a[120:123], a[224:227], 0\n"
    "s_nop 0\n"
    "ds_read_u16 v187, v187\n"
    "v_lshlrev_b32_sdwa v190, 1, v180 dst_sel:DWORD dst_unused:UNUSED_PAD src0_sel:DWORD src1_sel:WORD_1\n"
    "v_mfma_f32_16x16x32_bf16 v[168:171], a[168:171], a[228:231], v[168:171]\n"
    "s_waitcnt lgkmcnt(7)\n"
    "ds_read_u16_d16_hi v203, v203\n"
    "v_lshlrev_b32_sdwa v206, 1, v181 dst_sel:DWORD dst_unused:UNUSED_PAD src0_sel:DWORD src1_sel:WORD_1\n"
    "v_mfma_f32_16x16x32_bf16 v[168:171], a[76:79], a[232:235], v[168:171]\n"
    "v_lshlrev_b32_sdwa v191, 1, v182 dst_sel:DWORD dst_unused:UNUSED_PAD src0_sel:DWORD src1_sel:WORD_1\n"
    "v_fma_mix_f32 v234, v192, v232, 0 op_sel_hi:[1,1,0]\n"
    "v_mfma_f32_16x16x32_bf16 v[172:175], a[124:127], a[224:227], 0\n"
    "ds_read_u16 v188, v188\n"
    "v_or_b32_e64 v199, v199, v207\n"
    "v_mfma_f32_16x16x32_bf16 v[172:175], a[172:175], a[228:231], v[172:175]\n"
    "v_lshlrev_b32_sdwa v207, 1, v183 dst_sel:DWORD dst_unused:UNUSED_PAD src0_sel:DWORD src1_sel:WORD_1\n"
    "v_fma_mix_f32 v232, v192, v232, 0 op_sel:[1,1,0] op_sel_hi:[1,1,0]\n"
    "v_mfma_f32_16x16x32_bf16 v[172:175], v[60:63], a[232:235], v[172:175]\n"
    "ds_read_u16_d16_hi v204, v204\n"
    "v_cvt_pk_bf16_f32 v192, v234, v232\n"
    "v_mfma_f32_16x16x32_bf16 v[176:179], a[120:123], a[236:239], 0\n"
    "v_pk_fma_f16 v232, v196, v196, s48\n"
    "v_pk_mul_f16 v232, v196, v232\n"
    "v_mfma_f32_16x16x32_bf16 v[176:179], a[168:171], a[240:243], v[176:179]\n"
    "ds_read_u16 v189, v189\n"
    "v_exp_f16_e32 v234, v232\n"
    "v_mfma_f32_16x16x32_bf16 v[176:179], a[76:79], a[244:247], v[176:179]\n"
    "s_waitcnt lgkmcnt(7)\n"
    "ds_read_u16_d16_hi v205, v205\n"
    "buffer_store_dwordx4 v[216:219], v245, s[44:47], s88 offen offset:0 sc1\n"
    "v_mfma_f32_16x16x32_bf16 v[180:183], a[124:127], a[236:239], 0\n"
    "s_waitcnt lgkmcnt(7)\n"
    "ds_read_b128 a[224:227], v243 offset:0\n"
    "v_exp_f16_sdwa v234, v232 dst_sel:WORD_1 dst_unused:UNUSED_PRESERVE src0_sel:WORD_1\n"
    "v_mfma_f32_16x16x32_bf16 v[180:183], a[172:175], a[240:243], v[180:183]\n"
    "s_waitcnt lgkmcnt(7)\n"
    "ds_read_u16 v190, v190\n"
    "v_pk_fma_f16 v234, v234, s49, s49\n"
    "v_mfma_f32_16x16x32_bf16 v[180:183], v[60:63], a[244:247], v[180:183]\n"
    "ds_read_b128 a[228:231], v243 offset:1024\n"
    "v_rcp_f16_e32 v232, v234\n"
    "v_mfma_f32_16x16x32_bf16 v[96:99], a[176:179], v[192:195], v[96:99]\n"
    "ds_read_u16_d16_hi v206, v206\n"
    "v_rcp_f16_sdwa v232, v234 dst_sel:WORD_1 dst_unused:UNUSED_PRESERVE src0_sel:WORD_1\n"
    "v_mfma_f32_16x16x32_bf16 v[100:103], a[180:183], v[192:195], v[100:103]\n"
    "s_waitcnt lgkmcnt(8)\n"
    "v_fma_mix_f32 v234, v196, v232, 0 op_sel_hi:[1,1,0]\n"
    "v_fma_mix_f32 v232, v196, v232, 0 op_sel:[1,1,0] op_sel_hi:[1,1,0]\n"
    "v_mfma_f32_16x16x32_bf16 v[112:115], a[184:187], v[192:195], v[112:115]\n"
    "ds_read_b128 a[232:235], v243 offset:2048\n"
    "ds_read_u16 v191, v191\n"
    "v_mfma_f32_16x16x32_bf16 v[116:119], a[188:191], v[192:195], v[116:119]\n"
    "v_cvt_pk_bf16_f32 v196, v234, v232\n"
    "v_or_b32_e64 v184, v184, v200\n"
    "v_mfma_f32_16x16x32_bf16 v[128:131], v[24:27], v[192:195], v[128:131]\n"
    "ds_read_b128 a[236:239], v243 offset:3072\n"
    "v_or_b32_e32 v185, v185, v201\n"
    "v_mfma_f32_16x16x32_bf16 v[132:135], v[28:31], v[192:195], v[132:135]\n"
    "s_waitcnt lgkmcnt(9)\n"
    "ds_read_u16_d16_hi v207, v207\n"
    "v_cvt_pk_f16_f32 v192, v168, v169\n"
    "v_mfma_f32_16x16x32_bf16 v[104:107], a[176:179], v[196:199], v[104:107]\n"
    "v_lshlrev_b32_sdwa v193, 1, v170 dst_sel:DWORD dst_unused:UNUSED_PAD src0_sel:DWORD src1_sel:WORD_1\n"
    "v_lshlrev_b32_sdwa v201, 1, v171 dst_sel:DWORD dst_unused:UNUSED_PAD src0_sel:DWORD src1_sel:WORD_1\n"
    "v_mfma_f32_16x16x32_bf16 v[108:111], a[180:183], v[196:199], v[108:111]\n"
    "ds_read_b128 a[240:243], v243 offset:4096\n"
    "v_lshlrev_b32_sdwa v194, 1, v172 dst_sel:DWORD dst_unused:UNUSED_PAD src0_sel:DWORD src1_sel:WORD_1\n"
    "v_mfma_f32_16x16x32_bf16 v[120:123], a[184:187], v[196:199], v[120:123]\n"
    "s_waitcnt lgkmcnt(8)\n"
    "ds_read_b128 a[244:247], v243 offset:5120\n"
    "v_or_b32_e32 v186, v186, v202\n"
    "v_mfma_f32_16x16x32_bf16 v[124:127], a[188:191], v[196:199], v[124:127]\n"
    "ds_read_u16 v193, v193\n"
    "v_lshlrev_b32_sdwa v202, 1, v173 dst_sel:DWORD dst_unused:UNUSED_PAD src0_sel:DWORD src1_sel:WORD_1\n"
    "v_mfma_f32_16x16x32_bf16 v[136:139], v[24:27], v[196:199], v[136:139]\n"
    "v_lshlrev_b32_sdwa v195, 1, v174 dst_sel:DWORD dst_unused:UNUSED_PAD src0_sel:DWORD src1_sel:WORD_1\n"
    "v_or_b32_e64 v187, v187, v203\n"
    "v_mfma_f32_16x16x32_bf16 v[140:143], v[28:31], v[196:199], v[140:143]\n"
    "ds_read_u16_d16_hi v201, v201\n"
    "v_lshlrev_b32_sdwa v203, 1, v175 dst_sel:DWORD dst_unused:UNUSED_PAD src0_sel:DWORD src1_sel:WORD_1\n"
    "v_mfma_f32_16x16x32_bf16 v[168:171], a[80:83], a[224:227], 0\n"
    "s_waitcnt lgkmcnt(9)\n"
    "v_cvt_pk_f16_f32 v196, v176, v177\n"
    "v_lshlrev_b32_sdwa v197, 1, v178 dst_sel:DWORD dst_unused:UNUSED_PAD src0_sel:DWORD src1_sel:WORD_1\n"
    "v_mfma_f32_16x16x32_bf16 v[168:171], a[128:131], a[228:231], v[168:171]\n"
    "ds_read_u16 v194, v194\n"
    "v_lshlrev_b32_sdwa v198, 1, v180 dst_sel:DWORD dst_unused:UNUSED_PAD src0_sel:DWORD src1_sel:WORD_1\n"
    "v_mfma_f32_16x16x32_bf16 v[172:175], a[84:87], a[224:227], 0\n"
    "s_waitcnt lgkmcnt(8)\n"
    "ds_read_u16_d16_hi v202, v202\n"
    "v_lshlrev_b32_sdwa v199, 1, v182 dst_sel:DWORD dst_unused:UNUSED_PAD src0_sel:DWORD src1_sel:WORD_1\n"
    "v_mfma_f32_16x16x32_bf16 v[168:171], a[48:51], a[232:235], v[168:171]\n"
    "v_or_b32_e64 v188, v188, v204\n"
    "v_or_b32_e64 v189, v189, v205\n"
    "v_mfma_f32_16x16x32_bf16 v[172:175], a[132:135], a[228:231], v[172:175]\n"
    "ds_read_u16 v195, v195\n"
    "v_lshlrev_b32_sdwa v205, 1, v179 dst_sel:DWORD dst_unused:UNUSED_PAD src0_sel:DWORD src1_sel:WORD_1\n"
    "v_mfma_f32_16x16x32_bf16 v[172:175], v[48:51], a[232:235], v[172:175]\n"
    "s_waitcnt lgkmcnt(8)\n"
    "v_or_b32_e32 v190, v190, v206\n"
    "v_lshlrev_b32_sdwa v206, 1, v181 dst_sel:DWORD dst_unused:UNUSED_PAD src0_sel:DWORD src1_sel:WORD_1\n"
    "v_mfma_f32_16x16x32_bf16 v[176:179], a[80:83], a[236:239], 0\n"
    "ds_read_u16_d16_hi v203, v203\n"
    "v_pk_fma_f16 v232, v192, v192, s48\n"
    "v_mfma_f32_16x16x32_bf16 v[96:99], a[192:195], v[184:187], v[96:99]\n"
    "s_waitcnt lgkmcnt(7)\n"
    "v_or_b32_e64 v191, v191, v207\n"
    "v_lshlrev_b32_sdwa v207, 1, v183 dst_sel:DWORD dst_unused:UNUSED_PAD src0_sel:DWORD src1_sel:WORD_1\n"
    "v_mfma_f32_16x16x32_bf16 v[176:179], a[128:131], a[240:243], v[176:179]\n"
    "s_waitcnt lgkmcnt(6)\n"
    "ds_read_u16 v197, v197\n"
    "v_pk_mul_f16 v232, v192, v232\n"
    "s_barrier\n"
    "v_mfma_f32_16x16x32_bf16 v[176:179], a[48:51], a[244:247], v[176:179]\n"
    "ds_read_u16_d16_hi v205, v205\n"
    "buffer_store_dwordx4 v[220:223], v245, s[44:47], s88 offen offset:64 sc1\n"
    "v_mfma_f32_16x16x32_bf16 v[180:183], a[84:87], a[236:239], 0\n"
    "s_waitcnt lgkmcnt(5)\n"
    "ds_read_u16 v198, v198\n"
    "ds_read_u16_d16_hi v206, v206\n"
    "v_mfma_f32_16x16x32_bf16 v[180:183], a[132:135], a[240:243], v[180:183]\n"
    "ds_read_u16 v199, v199\n"
    "v_or_b32_e32 v193, v193, v201\n"
    "v_mfma_f32_16x16x32_bf16 v[180:183], v[48:51], a[244:247], v[180:183]\n"
    "s_waitcnt lgkmcnt(7)\n"
    "ds_read_u16_d16_hi v207, v207\n"
    "v_or_b32_e64 v194, v194, v202\n"
    "v_mfma_f32_16x16x32_bf16 v[100:103], a[196:199], v[184:187], v[100:103]\n"
    "v_exp_f16_e64 v234, v232\n"
    "v_mfma_f32_16x16x32_bf16 v[112:115], a[200:203], v[184:187], v[112:115]\n"
    "v_exp_f16_sdwa v234, v232 dst_sel:WORD_1 dst_unused:UNUSED_PRESERVE src0_sel:WORD_1\n"
    "v_mfma_f32_16x16x32_bf16 v[116:119], a[204:207], v[184:187], v[116:119]\n"
    "v_pk_fma_f16 v234, v234, s49, s49\n"
    "v_lshlrev_b32_sdwa v200, 1, v169 dst_sel:DWORD dst_unused:UNUSED_PAD src0_sel:DWORD src1_sel:WORD_1\n"
    "v_mfma_f32_16x16x32_bf16 v[128:131], v[32:35], v[184:187], v[128:131]\n"
    "s_waitcnt lgkmcnt(6)\n"
    "ds_read_u16_d16_hi v200, v200\n"
    "v_or_b32_e32 v195, v195, v203\n"
    "v_mfma_f32_16x16x32_bf16 v[132:135], v[36:39], v[184:187], v[132:135]\n"
    "v_lshlrev_b32_sdwa v184, 1, v168 dst_sel:DWORD dst_unused:UNUSED_PAD src0_sel:DWORD src1_sel:WORD_1\n"
    "v_lshlrev_b32_sdwa v185, 1, v170 dst_sel:DWORD dst_unused:UNUSED_PAD src0_sel:DWORD src1_sel:WORD_1\n"
    "v_mfma_f32_16x16x32_bf16 v[104:107], a[192:195], v[188:191], v[104:107]\n"
    "ds_read_u16 v184, v184\n"
    "ds_read_u16 v185, v185\n"
    "v_mfma_f32_16x16x32_bf16 v[108:111], a[196:199], v[188:191], v[108:111]\n"
    "s_waitcnt lgkmcnt(7)\n"
    "v_or_b32_e32 v197, v197, v205\n"
    "v_lshlrev_b32_sdwa v201, 1, v171 dst_sel:DWORD dst_unused:UNUSED_PAD src0_sel:DWORD src1_sel:WORD_1\n"
    "v_mfma_f32_16x16x32_bf16 v[120:123], a[200:203], v[188:191], v[120:123]\n"
    "ds_read_u16_d16_hi v201, v201\n"
    "v_lshlrev_b32_sdwa v186, 1, v172 dst_sel:DWORD dst_unused:UNUSED_PAD src0_sel:DWORD src1_sel:WORD_1\n"
    "v_mfma_f32_16x16x32_bf16 v[124:127], a[204:207], v[188:191], v[124:127]\n"
    "ds_read_u16 v186, v186\n"
    "v_lshlrev_b32_sdwa v202, 1, v173 dst_sel:DWORD dst_unused:UNUSED_PAD src0_sel:DWORD src1_sel:WORD_1\n"
    "v_mfma_f32_16x16x32_bf16 v[136:139], v[32:35], v[188:191], v[136:139]\n"
    "v_lshlrev_b32_sdwa v187, 1, v174 dst_sel:DWORD dst_unused:UNUSED_PAD src0_sel:DWORD src1_sel:WORD_1\n"
    "v_lshlrev_b32_sdwa v203, 1, v175 dst_sel:DWORD dst_unused:UNUSED_PAD src0_sel:DWORD src1_sel:WORD_1\n"
    "v_mfma_f32_16x16x32_bf16 v[140:143], v[36:39], v[188:191], v[140:143]\n"
    "s_waitcnt lgkmcnt(7)\n"
    "ds_read_u16_d16_hi v202, v202\n"
    "v_rcp_f16_e32 v232, v234\n"
    "v_mfma_f32_16x16x32_bf16 v[168:171], a[88:91], a[224:227], 0\n"
    "ds_read_u16 v187, v187\n"
    "v_or_b32_e32 v198, v198, v206\n"
    "v_mfma_f32_16x16x32_bf16 v[168:171], a[136:139], a[228:231], v[168:171]\n"
    "v_rcp_f16_sdwa v232, v234 dst_sel:WORD_1 dst_unused:UNUSED_PRESERVE src0_sel:WORD_1\n"
    "v_mfma_f32_16x16x32_bf16 v[168:171], a[52:55], a[232:235], v[168:171]\n"
    "s_waitcnt lgkmcnt(7)\n"
    "ds_read_u16_d16_hi v203, v203\n"
    "v_lshlrev_b32_sdwa v188, 1, v176 dst_sel:DWORD dst_unused:UNUSED_PAD src0_sel:DWORD src1_sel:WORD_1\n"
    "v_mfma_f32_16x16x32_bf16 v[172:175], a[92:95], a[224:227], 0\n"
    "v_lshlrev_b32_sdwa v204, 1, v177 dst_sel:DWORD dst_unused:UNUSED_PAD src0_sel:DWORD src1_sel:WORD_1\n"
    "v_lshlrev_b32_sdwa v189, 1, v178 dst_sel:DWORD dst_unused:UNUSED_PAD src0_sel:DWORD src1_sel:WORD_1\n"
    "v_mfma_f32_16x16x32_bf16 v[172:175], a[140:143], a[228:231], v[172:175]\n"
    "ds_read_u16 v188, v188\n"
    "v_lshlrev_b32_sdwa v205, 1, v179 dst_sel:DWORD dst_unused:UNUSED_PAD src0_sel:DWORD src1_sel:WORD_1\n"
    "v_lshlrev_b32_sdwa v190, 1, v180 dst_sel:DWORD dst_unused:UNUSED_PAD src0_sel:DWORD src1_sel:WORD_1\n"
    "v_lshlrev_b32_sdwa v206, 1, v181 dst_sel:DWORD dst_unused:UNUSED_PAD src0_sel:DWORD src1_sel:WORD_1\n"
    "v_lshlrev_b32_sdwa v191, 1, v182 dst_sel:DWORD dst_unused:UNUSED_PAD src0_sel:DWORD src1_sel:WORD_1\n"
    "v_fma_mix_f32 v234, v192, v232, 0 op_sel_hi:[1,1,0]\n"
    "v_mfma_f32_16x16x32_bf16 v[172:175], v[56:59], a[232:235], v[172:175]\n"
    "s_waitcnt lgkmcnt(7)\n"
    "ds_read_u16_d16_hi v204, v204\n"
    "v_or_b32_e32 v199, v199, v207\n"
    "v_lshlrev_b32_sdwa v207, 1, v183 dst_sel:DWORD dst_unused:UNUSED_PAD src0_sel:DWORD src1_sel:WORD_1\n"
    "v_fma_mix_f32 v232, v192, v232, 0 op_sel:[1,1,0] op_sel_hi:[1,1,0]\n"
    "v_mfma_f32_16x16x32_bf16 v[176:179], a[88:91], a[236:239], 0\n"
    "ds_read_u16 v189, v189\n"
    "v_cvt_pk_bf16_f32 v192, v234, v232\n"
    "v_mfma_f32_16x16x32_bf16 v[176:179], a[136:139], a[240:243], v[176:179]\n"
    "ds_read_u16_d16_hi v205, v205\n"
    "buffer_store_dwordx4 v[160:163], v245, s[44:47], s88 offen offset:128 sc1\n"
    "v_mfma_f32_16x16x32_bf16 v[176:179], a[52:55], a[244:247], v[176:179]\n"
    "v_pk_fma_f16 v232, v196, v196, s48\n"
    "v_or_b32_e32 v184, v184, v200\n"
    "v_mfma_f32_16x16x32_bf16 v[180:183], a[92:95], a[236:239], 0\n"
    "s_waitcnt lgkmcnt(7)\n"
    "ds_read_u16 v190, v190\n"
    "v_pk_mul_f16 v232, v196, v232\n"
    "v_mfma_f32_16x16x32_bf16 v[180:183], a[140:143], a[240:243], v[180:183]\n"
    "ds_read_u16_d16_hi v206, v206\n"
    "v_exp_f16_e32 v234, v232\n"
    "v_mfma_f32_16x16x32_bf16 v[180:183], v[56:59], a[244:247], v[180:183]\n"
    "s_waitcnt lgkmcnt(7)\n"
    "ds_read_u16 v191, v191\n"
    "v_exp_f16_sdwa v234, v232 dst_sel:WORD_1 dst_unused:UNUSED_PRESERVE src0_sel:WORD_1\n"
    "v_mfma_f32_16x16x32_bf16 v[96:99], a[208:211], v[192:195], v[96:99]\n"
    "v_pk_fma_f16 v234, v234, s49, s49\n"
    "v_or_b32_e32 v185, v185, v201\n"
    "v_mfma_f32_16x16x32_bf16 v[100:103], a[212:215], v[192:195], v[100:103]\n"
    "s_waitcnt lgkmcnt(7)\n"
    "ds_read_u16_d16_hi v207, v207\n"
    "v_rcp_f16_e32 v232, v234\n"
    "v_mfma_f32_16x16x32_bf16 v[112:115], a[216:219], v[192:195], v[112:115]\n"
    "v_rcp_f16_sdwa v232, v234 dst_sel:WORD_1 dst_unused:UNUSED_PRESERVE src0_sel:WORD_1\n"
    "v_mfma_f32_16x16x32_bf16 v[116:119], a[220:223], v[192:195], v[116:119]\n"
    "v_fma_mix_f32 v234, v196, v232, 0 op_sel_hi:[1,1,0]\n"
    "v_fma_mix_f32 v232, v196, v232, 0 op_sel:[1,1,0] op_sel_hi:[1,1,0]\n"
    "v_mfma_f32_16x16x32_bf16 v[128:131], v[40:43], v[192:195], v[128:131]\n"
    "s_waitcnt lgkmcnt(6)\n"
    "v_cvt_pk_bf16_f32 v196, v234, v232\n"
    "v_lshlrev_b32_sdwa v201, 1, v171 dst_sel:DWORD dst_unused:UNUSED_PAD src0_sel:DWORD src1_sel:WORD_1\n"
    "v_mfma_f32_16x16x32_bf16 v[132:135], v[44:47], v[192:195], v[132:135]\n"
    "ds_read_u16_d16_hi v201, v201\n"
    "v_cvt_pk_f16_f32 v192, v168, v169\n"
    "v_mfma_f32_16x16x32_bf16 v[104:107], a[208:211], v[196:199], v[104:107]\n"
    "v_lshlrev_b32_sdwa v193, 1, v170 dst_sel:DWORD dst_unused:UNUSED_PAD src0_sel:DWORD src1_sel:WORD_1\n"
    "v_lshlrev_b32_sdwa v194, 1, v172 dst_sel:DWORD dst_unused:UNUSED_PAD src0_sel:DWORD src1_sel:WORD_1\n"
    "v_mfma_f32_16x16x32_bf16 v[108:111], a[212:215], v[196:199], v[108:111]\n"
    "ds_read_u16 v193, v193\n"
    "ds_read_u16 v194, v194\n"
    "v_mfma_f32_16x16x32_bf16 v[120:123], a[216:219], v[196:199], v[120:123]\n"
    "s_waitcnt lgkmcnt(7)\n"
    "v_or_b32_e32 v186, v186, v202\n"
    "v_lshlrev_b32_sdwa v202, 1, v173 dst_sel:DWORD dst_unused:UNUSED_PAD src0_sel:DWORD src1_sel:WORD_1\n"
    "v_mfma_f32_16x16x32_bf16 v[124:127], a[220:223], v[196:199], v[124:127]\n"
    "ds_read_u16_d16_hi v202, v202\n"
    "v_lshlrev_b32_sdwa v195, 1, v174 dst_sel:DWORD dst_unused:UNUSED_PAD src0_sel:DWORD src1_sel:WORD_1\n"
    "v_mfma_f32_16x16x32_bf16 v[136:139], v[40:43], v[196:199], v[136:139]\n"
    "ds_read_u16 v195, v195\n"
    "v_or_b32_e32 v187, v187, v203\n"
    "v_mfma_f32_16x16x32_bf16 v[140:143], v[44:47], v[196:199], v[140:143]\n"
    "s_waitcnt lgkmcnt(7)\n"
    "v_lshlrev_b32_sdwa v203, 1, v175 dst_sel:DWORD dst_unused:UNUSED_PAD src0_sel:DWORD src1_sel:WORD_1\n"
    "v_cvt_pk_f16_f32 v196, v176, v177\n"
    "v_mfma_f32_16x16x32_bf16 v[168:171], a[96:99], a[224:227], 0\n"
    "ds_read_u16_d16_hi v203, v203\n"
    "v_lshlrev_b32_sdwa v197, 1, v178 dst_sel:DWORD dst_unused:UNUSED_PAD src0_sel:DWORD src1_sel:WORD_1\n"
    "v_mfma_f32_16x16x32_bf16 v[168:171], a[144:147], a[228:231], v[168:171]\n"
    "ds_read_u16 v197, v197\n"
    "v_lshlrev_b32_sdwa v198, 1, v180 dst_sel:DWORD dst_unused:UNUSED_PAD src0_sel:DWORD src1_sel:WORD_1\n"
    "v_mfma_f32_16x16x32_bf16 v[168:171], a[56:59], a[232:235], v[168:171]\n"
    "v_lshlrev_b32_sdwa v199, 1, v182 dst_sel:DWORD dst_unused:UNUSED_PAD src0_sel:DWORD src1_sel:WORD_1\n"
    "v_or_b32_e32 v188, v188, v204\n"
    "v_mfma_f32_16x16x32_bf16 v[172:175], a[100:103], a[224:227], 0\n"
    "s_waitcnt lgkmcnt(7)\n"
    "ds_read_u16 v198, v198\n"
    "v_or_b32_e64 v189, v189, v205\n"
    "v_mfma_f32_16x16x32_bf16 v[172:175], a[148:151], a[228:231], v[172:175]\n"
    "ds_read_u16 v199, v199\n"
    "v_lshlrev_b32_sdwa v205, 1, v179 dst_sel:DWORD dst_unused:UNUSED_PAD src0_sel:DWORD src1_sel:WORD_1\n"
    "v_mfma_f32_16x16x32_bf16 v[172:175], a[60:63], a[232:235], v[172:175]\n"
    "s_nop 0\n"
    "v_cvt_pk_bf16_f32 v96, v96, v97\n"
    "v_cvt_pk_bf16_f32 v97, v98, v99\n"
    "v_mfma_f32_16x16x32_bf16 v[176:179], a[96:99], a[236:239], 0\n"
    "s_waitcnt lgkmcnt(7)\n"
    "ds_read_u16_d16_hi v205, v205\n"
    "v_cvt_pk_bf16_f32 v98, v100, v101\n"
    "v_mfma_f32_16x16x32_bf16 v[176:179], a[144:147], a[240:243], v[176:179]\n"
    "v_cvt_pk_bf16_f32 v99, v102, v103\n"
    "v_or_b32_e64 v190, v190, v206\n"
    "v_mfma_f32_16x16x32_bf16 v[176:179], a[56:59], a[244:247], v[176:179]\n"
    "v_lshlrev_b32_sdwa v206, 1, v181 dst_sel:DWORD dst_unused:UNUSED_PAD src0_sel:DWORD src1_sel:WORD_1\n"
    "v_cvt_pk_bf16_f32 v112, v112, v113\n"
    "v_mfma_f32_16x16x32_bf16 v[100:103], a[4:7], v[184:187], 0\n"
    "s_nop 0\n"
    "ds_write_b128 v242, v[96:99] offset:24640\n"
    "v_mfma_f32_16x16x32_bf16 v[96:99], a[0:3], v[184:187], 0\n"
    "s_waitcnt lgkmcnt(7)\n"
    "v_cvt_pk_bf16_f32 v113, v114, v115\n"
    "v_cvt_pk_bf16_f32 v114, v116, v117\n"
    "v_cvt_pk_bf16_f32 v115, v118, v119\n"
    "v_cvt_pk_bf16_f32 v128, v128, v129\n"
    "v_cvt_pk_bf16_f32 v129, v130, v131\n"
    "v_or_b32_e32 v191, v191, v207\n"
    "v_mfma_f32_16x16x32_bf16 v[116:119], a[12:15], v[184:187], 0\n"
    "s_waitcnt lgkmcnt(5)\n"
    "ds_read_u16_d16_hi v206, v206\n"
    "ds_write_b128 v242, v[112:115] offset:26688\n"
    "v_mfma_f32_16x16x32_bf16 v[112:115], a[8:11], v[184:187], 0\n"
    "v_lshlrev_b32_sdwa v207, 1, v183 dst_sel:DWORD dst_unused:UNUSED_PAD src0_sel:DWORD src1_sel:WORD_1\n"
    "v_cvt_pk_bf16_f32 v130, v132, v133\n"
    "v_mfma_f32_16x16x32_bf16 v[180:183], a[100:103], a[236:239], 0\n"
    "v_or_b32_e64 v193, v193, v201\n"
    "v_cvt_pk_bf16_f32 v131, v134, v135\n"
    "v_mfma_f32_16x16x32_bf16 v[180:183], a[148:151], a[240:243], v[180:183]\n"
    "ds_read_u16_d16_hi v207, v207\n"
    "ds_write_b128 v242, v[128:131] offset:28736\n"
    "v_mfma_f32_16x16x32_bf16 v[180:183], a[60:63], a[244:247], v[180:183]\n"
    "s_nop 0\n"
    "v_cvt_pk_bf16_f32 v104, v104, v105\n"
    "v_cvt_pk_bf16_f32 v105, v106, v107\n"
    "v_mfma_f32_16x16x32_bf16 v[128:131], v[0:3], v[184:187], 0\n"
    "s_waitcnt lgkmcnt(4)\n"
    "v_cvt_pk_bf16_f32 v106, v108, v109\n"
    "v_cvt_pk_bf16_f32 v107, v110, v111\n"
    "v_mfma_f32_16x16x32_bf16 v[132:135], v[4:7], v[184:187], 0\n"
    "ds_write_b128 v242, v[104:107] offset:25664\n"
    "v_mfma_f32_16x16x32_bf16 v[104:107], a[0:3], v[188:191], 0\n"
    "v_or_b32_e32 v194, v194, v202\n"
    "v_cvt_pk_bf16_f32 v120, v120, v121\n"
    "v_mfma_f32_16x16x32_bf16 v[108:111], a[4:7], v[188:191], 0\n"
    "s_waitcnt lgkmcnt(3)\n"
    "v_cvt_pk_bf16_f32 v121, v122, v123\n"
    "v_cvt_pk_bf16_f32 v122, v124, v125\n"
    "v_cvt_pk_bf16_f32 v123, v126, v127\n"
    "ds_write_b128 v242, v[120:123] offset:27712\n"
    "v_mfma_f32_16x16x32_bf16 v[120:123], a[8:11], v[188:191], 0\n"
    "v_cvt_pk_bf16_f32 v136, v136, v137\n"
    "v_pk_fma_f16 v232, v192, v192, s48\n"
    "v_mfma_f32_16x16x32_bf16 v[124:127], a[12:15], v[188:191], 0\n"
    "s_waitcnt lgkmcnt(2)\n"
    "v_cvt_pk_bf16_f32 v137, v138, v139\n"
    "v_cvt_pk_bf16_f32 v138, v140, v141\n"
    "v_or_b32_e32 v195, v195, v203\n"
    "v_cvt_pk_bf16_f32 v139, v142, v143\n"
    "ds_write_b128 v242, v[136:139] offset:29760\n"
    "v_pk_mul_f16 v232, v192, v232\n"
    "v_mfma_f32_16x16x32_bf16 v[136:139], v[0:3], v[188:191], 0\n"
    "v_exp_f16_e64 v234, v232\n"
    "v_mfma_f32_16x16x32_bf16 v[140:143], v[4:7], v[188:191], 0\n"
    "v_exp_f16_sdwa v234, v232 dst_sel:WORD_1 dst_unused:UNUSED_PRESERVE src0_sel:WORD_1\n"
    "v_or_b32_e64 v197, v197, v205\n"
    "v_pk_fma_f16 v234, v234, s49, s49\n"
    "v_lshlrev_b32_sdwa v184, 1, v168 dst_sel:DWORD dst_unused:UNUSED_PAD src0_sel:DWORD src1_sel:WORD_1\n"
    "v_lshlrev_b32_sdwa v200, 1, v169 dst_sel:DWORD dst_unused:UNUSED_PAD src0_sel:DWORD src1_sel:WORD_1\n"
    "v_lshlrev_b32_sdwa v185, 1, v170 dst_sel:DWORD dst_unused:UNUSED_PAD src0_sel:DWORD src1_sel:WORD_1\n"
    "v_lshlrev_b32_sdwa v201, 1, v171 dst_sel:DWORD dst_unused:UNUSED_PAD src0_sel:DWORD src1_sel:WORD_1\n"
    "v_mfma_f32_16x16x32_bf16 v[168:171], a[104:107], a[224:227], 0\n"
    "ds_read_u16 v184, v184\n"
    "ds_read_u16_d16_hi v200, v200\n"
    "v_mfma_f32_16x16x32_bf16 v[168:171], a[152:155], a[228:231], v[168:171]\n"
    "ds_read_u16 v185, v185\n"
    "ds_read_u16_d16_hi v201, v201\n"
    "v_lshlrev_b32_sdwa v186, 1, v172 dst_sel:DWORD dst_unused:UNUSED_PAD src0_sel:DWORD src1_sel:WORD_1\n"
    "v_lshlrev_b32_sdwa v202, 1, v173 dst_sel:DWORD dst_unused:UNUSED_PAD src0_sel:DWORD src1_sel:WORD_1\n"
    "v_lshlrev_b32_sdwa v187, 1, v174 dst_sel:DWORD dst_unused:UNUSED_PAD src0_sel:DWORD src1_sel:WORD_1\n"
    "v_lshlrev_b32_sdwa v203, 1, v175 dst_sel:DWORD dst_unused:UNUSED_PAD src0_sel:DWORD src1_sel:WORD_1\n"
    "v_rcp_f16_e32 v232, v234\n"
    "v_mfma_f32_16x16x32_bf16 v[168:171], a[64:67], a[232:235], v[168:171]\n"
    "s_waitcnt lgkmcnt(4)\n"
    "ds_read_u16 v186, v186\n"
    "v_or_b32_e32 v198, v198, v206\n"
    "v_rcp_f16_sdwa v232, v234 dst_sel:WORD_1 dst_unused:UNUSED_PRESERVE src0_sel:WORD_1\n"
    "v_mfma_f32_16x16x32_bf16 v[172:175], a[108:111], a[224:227], 0\n"
    "s_barrier\n"
    "v_mfma_f32_16x16x32_bf16 v[172:175], a[156:159], a[228:231], v[172:175]\n"
    "ds_read_u16_d16_hi v202, v202\n"
    "ds_read_u16 v187, v187\n"
    "v_lshlrev_b32_sdwa v188, 1, v176 dst_sel:DWORD dst_unused:UNUSED_PAD src0_sel:DWORD src1_sel:WORD_1\n"
    "v_lshlrev_b32_sdwa v204, 1, v177 dst_sel:DWORD dst_unused:UNUSED_PAD src0_sel:DWORD src1_sel:WORD_1\n"
    "v_lshlrev_b32_sdwa v189, 1, v178 dst_sel:DWORD dst_unused:UNUSED_PAD src0_sel:DWORD src1_sel:WORD_1\n"
    "v_lshlrev_b32_sdwa v205, 1, v179 dst_sel:DWORD dst_unused:UNUSED_PAD src0_sel:DWORD src1_sel:WORD_1\n"
    "v_lshlrev_b32_sdwa v190, 1, v180 dst_sel:DWORD dst_unused:UNUSED_PAD src0_sel:DWORD src1_sel:WORD_1\n"
    "v_lshlrev_b32_sdwa v206, 1, v181 dst_sel:DWORD dst_unused:UNUSED_PAD src0_sel:DWORD src1_sel:WORD_1\n"
    "v_lshlrev_b32_sdwa v191, 1, v182 dst_sel:DWORD dst_unused:UNUSED_PAD src0_sel:DWORD src1_sel:WORD_1\n"
    "v_fma_mix_f32 v234, v192, v232, 0 op_sel_hi:[1,1,0]\n"
    "v_mfma_f32_16x16x32_bf16 v[172:175], a[68:71], a[232:235], v[172:175]\n"
    "ds_read_u16_d16_hi v203, v203\n"
    "ds_read_u16 v188, v188\n"
    "v_or_b32_e32 v199, v199, v207\n"
    "v_lshlrev_b32_sdwa v207, 1, v183 dst_sel:DWORD dst_unused:UNUSED_PAD src0_sel:DWORD src1_sel:WORD_1\n"
    "v_fma_mix_f32 v232, v192, v232, 0 op_sel:[1,1,0] op_sel_hi:[1,1,0]\n"
    "v_mfma_f32_16x16x32_bf16 v[176:179], a[104:107], a[236:239], 0\n"
    "s_waitcnt lgkmcnt(7)\n"
    "ds_read_u16_d16_hi v204, v204\n"
    "ds_read_u16 v189, v189\n"
    "v_cvt_pk_bf16_f32 v192, v234, v232\n"
    "v_mfma_f32_16x16x32_bf16 v[176:179], a[152:155], a[240:243], v[176:179]\n"
    "v_pk_fma_f16 v232, v196, v196, s48\n"
    "v_or_b32_e32 v184, v184, v200\n"
    "v_mfma_f32_16x16x32_bf16 v[176:179], a[64:67], a[244:247], v[176:179]\n"
    "s_waitcnt lgkmcnt(7)\n"
    "ds_read_u16_d16_hi v205, v205\n"
    "v_pk_mul_f16 v232, v196, v232\n"
    "v_mfma_f32_16x16x32_bf16 v[180:183], a[108:111], a[236:239], 0\n"
    "v_exp_f16_e32 v234, v232\n"
    "v_mfma_f32_16x16x32_bf16 v[180:183], a[156:159], a[240:243], v[180:183]\n"
    "ds_read_u16 v190, v190\n"
    "v_exp_f16_sdwa v234, v232 dst_sel:WORD_1 dst_unused:UNUSED_PRESERVE src0_sel:WORD_1\n"
    "v_mfma_f32_16x16x32_bf16 v[180:183], a[68:71], a[244:247], v[180:183]\n"
    "s_waitcnt lgkmcnt(7)\n"
    "ds_read_u16_d16_hi v206, v206\n"
    "v_pk_fma_f16 v234, v234, s49, s49\n"
    "v_mfma_f32_16x16x32_bf16 v[96:99], a[16:19], v[192:195], v[96:99]\n"
    "v_rcp_f16_e64 v232, v234\n"
    "v_mfma_f32_16x16x32_bf16 v[100:103], a[20:23], v[192:195], v[100:103]\n"
    "ds_read_u16 v191, v191\n"
    "v_rcp_f16_sdwa v232, v234 dst_sel:WORD_1 dst_unused:UNUSED_PRESERVE src0_sel:WORD_1\n"
    "v_mfma_f32_16x16x32_bf16 v[112:115], a[24:27], v[192:195], v[112:115]\n"
    "v_fma_mix_f32 v234, v196, v232, 0 op_sel_hi:[1,1,0]\n"
    "v_fma_mix_f32 v232, v196, v232, 0 op_sel:[1,1,0] op_sel_hi:[1,1,0]\n"
    "v_mfma_f32_16x16x32_bf16 v[116:119], a[28:31], v[192:195], v[116:119]\n"
    "s_waitcnt lgkmcnt(7)\n"
    "ds_read_u16_d16_hi v207, v207\n"
    "v_cvt_pk_bf16_f32 v196, v234, v232\n"
    "v_mfma_f32_16x16x32_bf16 v[128:131], v[8:11], v[192:195], v[128:131]\n"
    "v_or_b32_e32 v185, v185, v201\n"
    "v_lshlrev_b32_sdwa v201, 1, v171 dst_sel:DWORD dst_unused:UNUSED_PAD src0_sel:DWORD src1_sel:WORD_1\n"
    "v_mfma_f32_16x16x32_bf16 v[132:135], v[12:15], v[192:195], v[132:135]\n"
    "ds_read_u16_d16_hi v201, v201\n"
    "v_cvt_pk_f16_f32 v192, v168, v169\n"
    "v_mfma_f32_16x16x32_bf16 v[104:107], a[16:19], v[196:199], v[104:107]\n"
    "s_waitcnt lgkmcnt(7)\n"
    "v_lshlrev_b32_sdwa v193, 1, v170 dst_sel:DWORD dst_unused:UNUSED_PAD src0_sel:DWORD src1_sel:WORD_1\n"
    "v_lshlrev_b32_sdwa v194, 1, v172 dst_sel:DWORD dst_unused:UNUSED_PAD src0_sel:DWORD src1_sel:WORD_1\n"
    "v_mfma_f32_16x16x32_bf16 v[108:111], a[20:23], v[196:199], v[108:111]\n"
    "s_waitcnt vmcnt(6)\n"
    "ds_read_u16 v193, v193\n"
    "v_or_b32_e64 v186, v186, v202\n"
    "v_mfma_f32_16x16x32_bf16 v[120:123], a[24:27], v[196:199], v[120:123]\n"
    "ds_read_u16 v194, v194\n"
    "v_lshlrev_b32_sdwa v202, 1, v173 dst_sel:DWORD dst_unused:UNUSED_PAD src0_sel:DWORD src1_sel:WORD_1\n"
    "v_mfma_f32_16x16x32_bf16 v[124:127], a[28:31], v[196:199], v[124:127]\n"
    "v_lshlrev_b32_sdwa v195, 1, v174 dst_sel:DWORD dst_unused:UNUSED_PAD src0_sel:DWORD src1_sel:WORD_1\n"
    "v_or_b32_e64 v187, v187, v203\n"
    "v_mfma_f32_16x16x32_bf16 v[136:139], v[8:11], v[196:199], v[136:139]\n"
    "s_waitcnt lgkmcnt(7)\n"
    "ds_read_u16_d16_hi v202, v202\n"
    "v_lshlrev_b32_sdwa v203, 1, v175 dst_sel:DWORD dst_unused:UNUSED_PAD src0_sel:DWORD src1_sel:WORD_1\n"
    "v_mfma_f32_16x16x32_bf16 v[140:143], v[12:15], v[196:199], v[140:143]\n"
    "s_waitcnt vmcnt(5)\n"
    "ds_read_u16 v195, v195\n"
    "v_cvt_pk_f16_f32 v196, v176, v177\n"
    "v_smfmac_f32_32x32x32_bf16 v[112:127], a[248:251], v[152:159], v241\n"
    "v_lshlrev_b32_sdwa v197, 1, v178 dst_sel:DWORD dst_unused:UNUSED_PAD src0_sel:DWORD src1_sel:WORD_1\n"
    "v_lshlrev_b32_sdwa v198, 1, v180 dst_sel:DWORD dst_unused:UNUSED_PAD src0_sel:DWORD src1_sel:WORD_1\n"
    "v_lshlrev_b32_sdwa v199, 1, v182 dst_sel:DWORD dst_unused:UNUSED_PAD src0_sel:DWORD src1_sel:WORD_1\n"
    "v_or_b32_e64 v188, v188, v204\n"
    "v_or_b32_e32 v189, v189, v205\n"
    "v_smfmac_f32_32x32x32_bf16 v[96:111], a[248:251], v[144:151], v241\n"
    "s_waitcnt lgkmcnt(7)\n"
    "ds_read_b128 v[152:155], v243 offset:24640\n"
    "ds_read_b128 v[156:159], v243 offset:25664\n"
    "v_lshlrev_b32_sdwa v205, 1, v179 dst_sel:DWORD dst_unused:UNUSED_PAD src0_sel:DWORD src1_sel:WORD_1\n"
    "v_or_b32_e32 v190, v190, v206\n"
    "v_lshlrev_b32_sdwa v206, 1, v181 dst_sel:DWORD dst_unused:UNUSED_PAD src0_sel:DWORD src1_sel:WORD_1\n"
    "v_smfmac_f32_32x32x32_bf16 v[128:143], a[248:251], v[208:215], v241\n"
    "s_waitcnt lgkmcnt(7)\n"
    "ds_read_u16_d16_hi v203, v203\n"
    "ds_read_u16 v197, v197\n"
    "v_or_b32_e64 v191, v191, v207\n"
    "v_lshlrev_b32_sdwa v207, 1, v183 dst_sel:DWORD dst_unused:UNUSED_PAD src0_sel:DWORD src1_sel:WORD_1\n"
    "v_pk_fma_f16 v232, v192, v192, s48\n"
    "v_mfma_f32_16x16x32_bf16 v[168:171], a[112:115], a[224:227], 0\n"
    "v_pk_mul_f16 v232, v192, v232\n"
    "v_exp_f16_e64 v234, v232\n"
    "v_mfma_f32_16x16x32_bf16 v[168:171], a[160:163], a[228:231], v[168:171]\n"
    "ds_read_u16_d16_hi v205, v205\n"
    "v_exp_f16_sdwa v234, v232 dst_sel:WORD_1 dst_unused:UNUSED_PRESERVE src0_sel:WORD_1\n"
    "v_mfma_f32_16x16x32_bf16 v[168:171], a[72:75], a[232:235], v[168:171]\n"
    "s_waitcnt lgkmcnt(8)\n"
    "ds_read_u16 v198, v198\n"
    "v_or_b32_e32 v193, v193, v201\n"
    "v_mfma_f32_16x16x32_bf16 v[172:175], a[116:119], a[224:227], 0\n"
    "v_pk_fma_f16 v234, v234, s49, s49\n"
    "v_rcp_f16_e64 v232, v234\n"
    "v_mfma_f32_16x16x32_bf16 v[172:175], a[164:167], a[228:231], v[172:175]\n"
    "ds_read_u16_d16_hi v206, v206\n"
    "v_lshlrev_b32_sdwa v200, 1, v169 dst_sel:DWORD dst_unused:UNUSED_PAD src0_sel:DWORD src1_sel:WORD_1\n"
    "v_mfma_f32_16x16x32_bf16 v[172:175], v[52:55], a[232:235], v[172:175]\n"
    "s_waitcnt lgkmcnt(8)\n"
    "ds_read_u16 v199, v199\n"
    "v_or_b32_e32 v194, v194, v202\n"
    "v_mfma_f32_16x16x32_bf16 v[176:179], a[112:115], a[236:239], 0\n"
    "s_nop 0\n"
    "v_lshlrev_b32_sdwa v201, 1, v171 dst_sel:DWORD dst_unused:UNUSED_PAD src0_sel:DWORD src1_sel:WORD_1\n"
    "v_rcp_f16_sdwa v232, v234 dst_sel:WORD_1 dst_unused:UNUSED_PRESERVE src0_sel:WORD_1\n"
    "v_mfma_f32_16x16x32_bf16 v[176:179], a[160:163], a[240:243], v[176:179]\n"
    "s_waitcnt lgkmcnt(6)\n"
    "ds_read_u16_d16_hi v207, v207\n"
    "v_lshlrev_b32_sdwa v202, 1, v173 dst_sel:DWORD dst_unused:UNUSED_PAD src0_sel:DWORD src1_sel:WORD_1\n"
    "v_smfmac_f32_32x32x32_bf16 v[96:111], a[248:251], v[152:159], v241\n"
    "ds_read_b128 v[152:155], v243 offset:26688\n"
    "v_fma_mix_f32 v234, v192, v232, 0 op_sel_hi:[1,1,0]\n"
    "v_fma_mix_f32 v232, v192, v232, 0 op_sel:[1,1,0] op_sel_hi:[1,1,0]\n"
    "v_cvt_pk_bf16_f32 v192, v234, v232\n"
    "v_pk_fma_f16 v232, v196, v196, s48\n"
    "v_mfma_f32_16x16x32_bf16 v[176:179], a[72:75], a[244:247], v[176:179]\n"
    "s_waitcnt lgkmcnt(7)\n"
    "ds_read_b128 v[156:159], v243 offset:27712\n"
    "ds_read_u16_d16_hi v200, v200\n"
    "v_mfma_f32_16x16x32_bf16 v[180:183], a[116:119], a[236:239], 0\n"
    "v_or_b32_e32 v195, v195, v203\n"
    "v_lshlrev_b32_sdwa v203, 1, v175 dst_sel:DWORD dst_unused:UNUSED_PAD src0_sel:DWORD src1_sel:WORD_1\n"
    "v_mfma_f32_16x16x32_bf16 v[180:183], a[164:167], a[240:243], v[180:183]\n"
    "ds_read_u16_d16_hi v201, v201\n"
    "v_lshlrev_b32_sdwa v204, 1, v177 dst_sel:DWORD dst_unused:UNUSED_PAD src0_sel:DWORD src1_sel:WORD_1\n"
    "v_mfma_f32_16x16x32_bf16 v[180:183], v[52:55], a[244:247], v[180:183]\n"
    "v_pk_mul_f16 v232, v196, v232\n"
    "v_exp_f16_e32 v234, v232\n"
    "v_mfma_f32_16x16x32_bf16 v[96:99], a[32:35], v[184:187], v[96:99]\n"
    "s_waitcnt lgkmcnt(8)\n"
    "ds_read_u16_d16_hi v202, v202\n"
    "v_or_b32_e64 v197, v197, v205\n"
    "v_mfma_f32_16x16x32_bf16 v[100:103], a[36:39], v[184:187], v[100:103]\n"
    "ds_read_u16_d16_hi v203, v203\n"
    "v_lshlrev_b32_sdwa v205, 1, v179 dst_sel:DWORD dst_unused:UNUSED_PAD src0_sel:DWORD src1_sel:WORD_1\n"
    "v_mfma_f32_16x16x32_bf16 v[112:115], a[40:43], v[184:187], v[112:115]\n"
    "s_nop 0\n"
    "v_exp_f16_sdwa v234, v232 dst_sel:WORD_1 dst_unused:UNUSED_PRESERVE src0_sel:WORD_1\n"
    "v_mfma_f32_16x16x32_bf16 v[116:119], a[44:47], v[184:187], v[116:119]\n"
    "s_waitcnt lgkmcnt(8)\n"
    "ds_read_u16_d16_hi v204, v204\n"
    "v_or_b32_e64 v198, v198, v206\n"
    "v_mfma_f32_16x16x32_bf16 v[128:131], v[16:19], v[184:187], v[128:131]\n"
    "v_lshlrev_b32_sdwa v206, 1, v181 dst_sel:DWORD dst_unused:UNUSED_PAD src0_sel:DWORD src1_sel:WORD_1\n"
    "v_pk_fma_f16 v234, v234, s49, s49\n"
    "v_mfma_f32_16x16x32_bf16 v[132:135], v[20:23], v[184:187], v[132:135]\n"
    "s_nop 0\n"
    "ds_read_u16_d16_hi v205, v205\n"
    "v_lshlrev_b32_sdwa v184, 1, v168 dst_sel:DWORD dst_unused:UNUSED_PAD src0_sel:DWORD src1_sel:WORD_1\n"
    "v_mfma_f32_16x16x32_bf16 v[104:107], a[32:35], v[188:191], v[104:107]\n"
    "s_waitcnt lgkmcnt(7)\n"
    "v_lshlrev_b32_sdwa v185, 1, v170 dst_sel:DWORD dst_unused:UNUSED_PAD src0_sel:DWORD src1_sel:WORD_1\n"
    "v_lshlrev_b32_sdwa v186, 1, v172 dst_sel:DWORD dst_unused:UNUSED_PAD src0_sel:DWORD src1_sel:WORD_1\n"
    "v_mfma_f32_16x16x32_bf16 v[108:111], a[36:39], v[188:191], v[108:111]\n"
    "ds_read_u16 v184, v184\n"
    "v_lshlrev_b32_sdwa v187, 1, v174 dst_sel:DWORD dst_unused:UNUSED_PAD src0_sel:DWORD src1_sel:WORD_1\n"
    "v_mfma_f32_16x16x32_bf16 v[120:123], a[40:43], v[188:191], v[120:123]\n"
    "ds_read_u16 v185, v185\n"
    "v_or_b32_e32 v199, v199, v207\n"
    "v_mfma_f32_16x16x32_bf16 v[124:127], a[44:47], v[188:191], v[124:127]\n"
    "s_waitcnt lgkmcnt(8)\n"
    "v_lshlrev_b32_sdwa v207, 1, v183 dst_sel:DWORD dst_unused:UNUSED_PAD src0_sel:DWORD src1_sel:WORD_1\n"
    "v_rcp_f16_e64 v232, v234\n"
    "v_mfma_f32_16x16x32_bf16 v[136:139], v[16:19], v[188:191], v[136:139]\n"
    "ds_read_u16 v186, v186\n"
    "v_rcp_f16_sdwa v232, v234 dst_sel:WORD_1 dst_unused:UNUSED_PRESERVE src0_sel:WORD_1\n"
    "v_mfma_f32_16x16x32_bf16 v[140:143], v[20:23], v[188:191], v[140:143]\n"
    "ds_read_u16 v187, v187\n"
    "v_lshlrev_b32_sdwa v188, 1, v176 dst_sel:DWORD dst_unused:UNUSED_PAD src0_sel:DWORD src1_sel:WORD_1\n"
    "v_smfmac_f32_32x32x32_bf16 v[112:127], a[248:251], v[152:159], v241\n"
    "v_lshlrev_b32_sdwa v189, 1, v178 dst_sel:DWORD dst_unused:UNUSED_PAD src0_sel:DWORD src1_sel:WORD_1\n"
    "v_lshlrev_b32_sdwa v190, 1, v180 dst_sel:DWORD dst_unused:UNUSED_PAD src0_sel:DWORD src1_sel:WORD_1\n"
    "v_lshlrev_b32_sdwa v191, 1, v182 dst_sel:DWORD dst_unused:UNUSED_PAD src0_sel:DWORD src1_sel:WORD_1\n"
    "v_fma_mix_f32 v234, v196, v232, 0 op_sel_hi:[1,1,0]\n"
    "v_fma_mix_f32 v232, v196, v232, 0 op_sel:[1,1,0] op_sel_hi:[1,1,0]\n"
    "v_mfma_f32_16x16x32_bf16 v[168:171], a[120:123], a[224:227], 0\n"
    "ds_read_u16 v188, v188\n"
    "v_cvt_pk_bf16_f32 v196, v234, v232\n"
    "v_mfma_f32_16x16x32_bf16 v[168:171], a[168:171], a[228:231], v[168:171]\n"
    "ds_read_b128 v[152:155], v243 offset:28736\n"
    "v_mfma_f32_16x16x32_bf16 v[168:171], a[76:79], a[232:235], v[168:171]\n"
    "ds_read_b128 v[156:159], v243 offset:29760\n"
    "v_mfma_f32_16x16x32_bf16 v[172:175], a[124:127], a[224:227], 0\n"
    "ds_read_u16 v189, v189\n"
    "v_mfma_f32_16x16x32_bf16 v[172:175], a[172:175], a[228:231], v[172:175]\n"
    "v_mfma_f32_16x16x32_bf16 v[172:175], v[60:63], a[232:235], v[172:175]\n"
    "ds_read_u16 v190, v190\n"
    "v_mfma_f32_16x16x32_bf16 v[176:179], a[120:123], a[236:239], 0\n"
    "s_waitcnt lgkmcnt(8)\n"
    "ds_read_u16_d16_hi v206, v206\n"
    "v_or_b32_e32 v184, v184, v200\n"
    "v_mfma_f32_16x16x32_bf16 v[176:179], a[168:171], a[240:243], v[176:179]\n"
    "s_nop 0\n"
    "v_mfma_f32_16x16x32_bf16 v[176:179], a[76:79], a[244:247], v[176:179]\n"
    "s_waitcnt lgkmcnt(8)\n"
    "ds_read_u16 v191, v191\n"
    "v_or_b32_e32 v185, v185, v201\n"
    "v_mfma_f32_16x16x32_bf16 v[180:183], a[124:127], a[236:239], 0\n"
    "v_lshlrev_b32_sdwa v201, 1, v171 dst_sel:DWORD dst_unused:UNUSED_PAD src0_sel:DWORD src1_sel:WORD_1\n"
    "v_mfma_f32_16x16x32_bf16 v[180:183], a[172:175], a[240:243], v[180:183]\n"
    "s_waitcnt lgkmcnt(8)\n"
    "ds_read_u16_d16_hi v207, v207\n"
    "v_or_b32_e32 v186, v186, v202\n"
    "v_mfma_f32_16x16x32_bf16 v[180:183], v[60:63], a[244:247], v[180:183]\n"
    "s_waitcnt lgkmcnt(8)\n"
    "v_lshlrev_b32_sdwa v202, 1, v173 dst_sel:DWORD dst_unused:UNUSED_PAD src0_sel:DWORD src1_sel:WORD_1\n"
    "v_or_b32_e64 v187, v187, v203\n"
    "v_mfma_f32_16x16x32_bf16 v[96:99], a[176:179], v[192:195], v[96:99]\n"
    "ds_read_u16_d16_hi v201, v201\n"
    "v_lshlrev_b32_sdwa v203, 1, v175 dst_sel:DWORD dst_unused:UNUSED_PAD src0_sel:DWORD src1_sel:WORD_1\n"
    "v_mfma_f32_16x16x32_bf16 v[100:103], a[180:183], v[192:195], v[100:103]\n"
    "s_waitcnt lgkmcnt(7)\n"
    "ds_read_u16_d16_hi v202, v202\n"
    "v_or_b32_e32 v188, v188, v204\n"
    "v_mfma_f32_16x16x32_bf16 v[112:115], a[184:187], v[192:195], v[112:115]\n"
    "v_mfma_f32_16x16x32_bf16 v[116:119], a[188:191], v[192:195], v[116:119]\n"
    "ds_read_u16_d16_hi v203, v203\n"
    "v_mfma_f32_16x16x32_bf16 v[128:131], v[24:27], v[192:195], v[128:131]\n"
    "s_nop 0\n"
    "v_mfma_f32_16x16x32_bf16 v[132:135], v[28:31], v[192:195], v[132:135]\n"
    "s_waitcnt lgkmcnt(7)\n"
    "v_cvt_pk_f16_f32 v192, v168, v169\n"
    "v_lshlrev_b32_sdwa v193, 1, v170 dst_sel:DWORD dst_unused:UNUSED_PAD src0_sel:DWORD src1_sel:WORD_1\n"
    "v_mfma_f32_16x16x32_bf16 v[104:107], a[176:179], v[196:199], v[104:107]\n"
    "ds_read_u16 v193, v193\n"
    "v_lshlrev_b32_sdwa v194, 1, v172 dst_sel:DWORD dst_unused:UNUSED_PAD src0_sel:DWORD src1_sel:WORD_1\n"
    "v_mfma_f32_16x16x32_bf16 v[108:111], a[180:183], v[196:199], v[108:111]\n"
    "ds_read_u16 v194, v194\n"
    "v_lshlrev_b32_sdwa v195, 1, v174 dst_sel:DWORD dst_unused:UNUSED_PAD src0_sel:DWORD src1_sel:WORD_1\n"
    "v_mfma_f32_16x16x32_bf16 v[120:123], a[184:187], v[196:199], v[120:123]\n"
    "s_waitcnt lgkmcnt(7)\n"
    "ds_read_u16 v195, v195\n"
    "v_or_b32_e32 v189, v189, v205\n"
    "v_mfma_f32_16x16x32_bf16 v[124:127], a[188:191], v[196:199], v[124:127]\n"
    "v_lshlrev_b32_sdwa v205, 1, v179 dst_sel:DWORD dst_unused:UNUSED_PAD src0_sel:DWORD src1_sel:WORD_1\n"
    "v_or_b32_e64 v190, v190, v206\n"
    "v_mfma_f32_16x16x32_bf16 v[136:139], v[24:27], v[196:199], v[136:139]\n"
    "ds_read_u16_d16_hi v205, v205\n"
    "v_lshlrev_b32_sdwa v206, 1, v181 dst_sel:DWORD dst_unused:UNUSED_PAD src0_sel:DWORD src1_sel:WORD_1\n"
    "v_mfma_f32_16x16x32_bf16 v[140:143], v[28:31], v[196:199], v[140:143]\n"
    "s_nop 0\n"
    "v_cvt_pk_f16_f32 v196, v176, v177\n"
    "v_lshlrev_b32_sdwa v197, 1, v178 dst_sel:DWORD dst_unused:UNUSED_PAD src0_sel:DWORD src1_sel:WORD_1\n"
    "v_mfma_f32_16x16x32_bf16 v[96:99], a[192:195], v[184:187], v[96:99]\n"
    "s_waitcnt lgkmcnt(7)\n"
    "ds_read_u16 v197, v197\n"
    "v_lshlrev_b32_sdwa v198, 1, v180 dst_sel:DWORD dst_unused:UNUSED_PAD src0_sel:DWORD src1_sel:WORD_1\n"
    "v_smfmac_f32_32x32x32_bf16 v[128:143], a[248:251], v[152:159], v241\n"
    "ds_read_u16 v198, v198\n"
    "v_lshlrev_b32_sdwa v199, 1, v182 dst_sel:DWORD dst_unused:UNUSED_PAD src0_sel:DWORD src1_sel:WORD_1\n"
    "v_or_b32_e64 v191, v191, v207\n"
    "v_lshlrev_b32_sdwa v207, 1, v183 dst_sel:DWORD dst_unused:UNUSED_PAD src0_sel:DWORD src1_sel:WORD_1\n"
    "v_pk_fma_f16 v232, v192, v192, s48\n"
    "v_mfma_f32_16x16x32_bf16 v[100:103], a[196:199], v[184:187], v[100:103]\n"
    "ds_read_u16_d16_hi v206, v206\n"
    "v_pk_mul_f16 v232, v192, v232\n"
    "v_mfma_f32_16x16x32_bf16 v[112:115], a[200:203], v[184:187], v[112:115]\n"
    "v_exp_f16_e64 v234, v232\n"
    "v_mfma_f32_16x16x32_bf16 v[116:119], a[204:207], v[184:187], v[116:119]\n"
    "ds_read_u16 v199, v199\n"
    "v_exp_f16_sdwa v234, v232 dst_sel:WORD_1 dst_unused:UNUSED_PRESERVE src0_sel:WORD_1\n"
    "v_mfma_f32_16x16x32_bf16 v[128:131], v[32:35], v[184:187], v[128:131]\n"
    "v_pk_fma_f16 v234, v234, s49, s49\n"
    "v_rcp_f16_e32 v232, v234\n"
    "v_mfma_f32_16x16x32_bf16 v[132:135], v[36:39], v[184:187], v[132:135]\n"
    "s_waitcnt lgkmcnt(7)\n"
    "ds_read_u16_d16_hi v207, v207\n"
    "v_or_b32_e64 v193, v193, v201\n"
    "v_mfma_f32_16x16x32_bf16 v[104:107], a[192:195], v[188:191], v[104:107]\n"
    "s_waitcnt lgkmcnt(7)\n"
    "v_or_b32_e32 v194, v194, v202\n"
    "v_rcp_f16_sdwa v232, v234 dst_sel:WORD_1 dst_unused:UNUSED_PRESERVE src0_sel:WORD_1\n"
    "v_mfma_f32_16x16x32_bf16 v[108:111], a[196:199], v[188:191], v[108:111]\n"
    "v_fma_mix_f32 v234, v192, v232, 0 op_sel_hi:[1,1,0]\n"
    "v_fma_mix_f32 v232, v192, v232, 0 op_sel:[1,1,0] op_sel_hi:[1,1,0]\n"
    "v_mfma_f32_16x16x32_bf16 v[120:123], a[200:203], v[188:191], v[120:123]\n"
    "s_waitcnt lgkmcnt(6)\n"
    "v_or_b32_e32 v195, v195, v203\n"
    "v_cvt_pk_bf16_f32 v192, v234, v232\n"
    "v_mfma_f32_16x16x32_bf16 v[124:127], a[204:207], v[188:191], v[124:127]\n"
    "v_pk_fma_f16 v232, v196, v196, s48\n"
    "v_pk_mul_f16 v232, v196, v232\n"
    "v_mfma_f32_16x16x32_bf16 v[136:139], v[32:35], v[188:191], v[136:139]\n"
    "v_exp_f16_e64 v234, v232\n"
    "v_mfma_f32_16x16x32_bf16 v[140:143], v[36:39], v[188:191], v[140:143]\n"
    "s_waitcnt lgkmcnt(4)\n"
    "v_or_b32_e32 v197, v197, v205\n"
    "v_exp_f16_sdwa v234, v232 dst_sel:WORD_1 dst_unused:UNUSED_PRESERVE src0_sel:WORD_1\n"
    "v_mfma_f32_16x16x32_bf16 v[96:99], a[208:211], v[192:195], v[96:99]\n"
    "v_pk_fma_f16 v234, v234, s49, s49\n"
    "v_rcp_f16_e64 v232, v234\n"
    "v_mfma_f32_16x16x32_bf16 v[100:103], a[212:215], v[192:195], v[100:103]\n"
    "v_rcp_f16_sdwa v232, v234 dst_sel:WORD_1 dst_unused:UNUSED_PRESERVE src0_sel:WORD_1\n"
    "v_mfma_f32_16x16x32_bf16 v[112:115], a[216:219], v[192:195], v[112:115]\n"
    "s_waitcnt lgkmcnt(2)\n"
    "v_or_b32_e32 v198, v198, v206\n"
    "v_fma_mix_f32 v234, v196, v232, 0 op_sel_hi:[1,1,0]\n"
    "v_mfma_f32_16x16x32_bf16 v[116:119], a[220:223], v[192:195], v[116:119]\n"
    "v_fma_mix_f32 v232, v196, v232, 0 op_sel:[1,1,0] op_sel_hi:[1,1,0]\n"
    "v_cvt_pk_bf16_f32 v96, v96, v97\n"
    "v_mfma_f32_16x16x32_bf16 v[128:131], v[40:43], v[192:195], v[128:131]\n"
    "v_cvt_pk_bf16_f32 v196, v234, v232\n"
    "v_cvt_pk_bf16_f32 v97, v98, v99\n"
    "v_mfma_f32_16x16x32_bf16 v[132:135], v[44:47], v[192:195], v[132:135]\n"
    "v_cvt_pk_bf16_f32 v98, v100, v101\n"
    "s_waitcnt lgkmcnt(0)\n"
    "v_or_b32_e32 v199, v199, v207\n"
    "v_cvt_pk_bf16_f32 v99, v102, v103\n"
    "buffer_store_dwordx4 v[96:99], v245, s[44:47], s58 offen offset:0 sc1\n"
    "v_mfma_f32_16x16x32_bf16 v[104:107], a[208:211], v[196:199], v[104:107]\n"
    "v_cvt_pk_bf16_f32 v112, v112, v113\n"
    "v_cvt_pk_bf16_f32 v113, v114, v115\n"
    "v_mfma_f32_16x16x32_bf16 v[108:111], a[212:215], v[196:199], v[108:111]\n"
    "v_cvt_pk_bf16_f32 v114, v116, v117\n"
    "v_cvt_pk_bf16_f32 v115, v118, v119\n"
    "v_mfma_f32_16x16x32_bf16 v[120:123], a[216:219], v[196:199], v[120:123]\n"
    "buffer_store_dwordx4 v[112:115], v245, s[44:47], s58 offen offset:64 sc1\n"
    "v_mfma_f32_16x16x32_bf16 v[124:127], a[220:223], v[196:199], v[124:127]\n"
    "v_cvt_pk_bf16_f32 v128, v128, v129\n"
    "v_cvt_pk_bf16_f32 v129, v130, v131\n"
    "v_mfma_f32_16x16x32_bf16 v[136:139], v[40:43], v[196:199], v[136:139]\n"
    "v_cvt_pk_bf16_f32 v130, v132, v133\n"
    "v_cvt_pk_bf16_f32 v131, v134, v135\n"
    "v_mfma_f32_16x16x32_bf16 v[140:143], v[44:47], v[196:199], v[140:143]\n"
    "buffer_store_dwordx4 v[128:131], v245, s[44:47], s58 offen offset:128 sc1\n"
    "v_cvt_pk_bf16_f32 v104, v104, v105\n"
    "v_cvt_pk_bf16_f32 v105, v106, v107\n"
    "v_cvt_pk_bf16_f32 v106, v108, v109\n"
    "v_cvt_pk_bf16_f32 v107, v110, v111\n"
    "v_cvt_pk_bf16_f32 v120, v120, v121\n"
    "buffer_store_dwordx4 v[104:107], v245, s[44:47], s89 offen offset:0 sc1\n"
    "v_cvt_pk_bf16_f32 v121, v122, v123\n"
    "v_cvt_pk_bf16_f32 v122, v124, v125\n"
    "v_cvt_pk_bf16_f32 v123, v126, v127\n"
    "buffer_store_dwordx4 v[120:123], v245, s[44:47], s89 offen offset:64 sc1\n"
    "v_cvt_pk_bf16_f32 v136, v136, v137\n"
    "v_cvt_pk_bf16_f32 v137, v138, v139\n"
    "v_cvt_pk_bf16_f32 v138, v140, v141\n"
    "v_cvt_pk_bf16_f32 v139, v142, v143\n"
    "buffer_store_dwordx4 v[136:139], v245, s[44:47], s89 offen offset:128 sc1\n"
    "97:\n"
    "98:\n"
    "s_waitcnt vmcnt(0) lgkmcnt(0)\n"
    ".Lreplk_end:\n"
    : "+{s66}"(flg), "+{v208}"(tid), "+{s65}"(blk), "+{s[98:99]}"(ka)
    : "{s[96:97]}"(CTAB)
    : "memory", "scc", "vcc", "m0", "v0", "v1", "v2", "v3", "v4", "v5", "v6", "v7", "v8", "v9", "v10", "v11", "v12", "v13", "v14", "v15", "v16", "v17", "v18", "v19", "v20", "v21", "v22", "v23", "v24", "v25", "v26", "v27", "v28", "v29", "v30", "v31", "v32", "v33", "v34", "v35", "v36", "v37", "v38", "v39", "v40", "v41", "v42", "v43", "v44", "v45", "v46", "v47", "v48", "v49", "v50", "v51", "v52", "v53", "v54", "v55", "v56", "v57", "v58", "v59", "v60", "v61", "v62", "v63", "v64", "v65", "v66", "v67", "v68", "v69", "v70", "v71", "v72", "v73", "v74", "v75", "v76", "v77", "v78", "v79", "v80", "v81", "v82", "v83", "v84", "v85", "v86", "v87", "v88", "v89", "v90", "v91", "v92", "v93", "v94", "v95", "v96", "v97", "v98", "v99", "v100", "v101", "v102", "v103", "v104", "v105", "v106", "v107", "v108", "v109", "v110", "v111", "v112", "v113", "v114", "v115", "v116", "v117", "v118", "v119", "v120", "v121", "v122", "v123", "v124", "v125", "v126", "v127", "v128", "v129", "v130", "v131", "v132", "v133", "v134", "v135", "v136", "v137", "v138", "v139", "v140", "v141", "v142", "v143", "v144", "v145", "v146", "v147", "v148", "v149", "v150", "v151", "v152", "v153", "v154", "v155", "v156", "v157", "v158", "v159", "v160", "v161", "v162", "v163", "v164", "v165", "v166", "v167", "v168", "v169", "v170", "v171", "v172", "v173", "v174", "v175", "v176", "v177", "v178", "v179", "v180", "v181", "v182", "v183", "v184", "v185", "v186", "v187", "v188", "v189", "v190", "v191", "v192", "v193", "v194", "v195", "v196", "v197", "v198", "v199", "v200", "v201", "v202", "v203", "v204", "v205", "v206", "v207", "v209", "v210", "v211", "v212", "v213", "v214", "v215", "v216", "v217", "v218", "v219", "v220", "v221", "v222", "v223", "v224", "v225", "v226", "v227", "v228", "v229", "v230", "v231", "v232", "v233", "v234", "v235", "v236", "v237", "v238", "v239", "v240", "v241", "v242", "v243", "v244", "v245", "v246", "v247", "v248", "v249", "v250", "v251", "v252", "v253", "v254", "v255", "a0", "a1", "a2", "a3", "a4", "a5", "a6", "a7", "a8", "a9", "a10", "a11", "a12", "a13", "a14", "a15", "a16", "a17", "a18", "a19", "a20", "a21", "a22", "a23", "a24", "a25", "a26", "a27", "a28", "a29", "a30", "a31", "a32", "a33", "a34", "a35", "a36", "a37", "a38", "a39", "a40", "a41", "a42", "a43", "a44", "a45", "a46", "a47", "a48", "a49", "a50", "a51", "a52", "a53", "a54", "a55", "a56", "a57", "a58", "a59", "a60", "a61", "a62", "a63", "a64", "a65", "a66", "a67", "a68", "a69", "a70", "a71", "a72", "a73", "a74", "a75", "a76", "a77", "a78", "a79", "a80", "a81", "a82", "a83", "a84", "a85", "a86", "a87", "a88", "a89", "a90", "a91", "a92", "a93", "a94", "a95", "a96", "a97", "a98", "a99", "a100", "a101", "a102", "a103", "a104", "a105", "a106", "a107", "a108", "a109", "a110", "a111", "a112", "a113", "a114", "a115", "a116", "a117", "a118", "a119", "a120", "a121", "a122", "a123", "a124", "a125", "a126", "a127", "a128", "a129", "a130", "a131", "a132", "a133", "a134", "a135", "a136", "a137", "a138", "a139", "a140", "a141", "a142", "a143", "a144", "a145", "a146", "a147", "a148", "a149", "a150", "a151", "a152", "a153", "a154", "a155", "a156", "a157", "a158", "a159", "a160", "a161", "a162", "a163", "a164", "a165", "a166", "a167", "a168", "a169", "a170", "a171", "a172", "a173", "a174", "a175", "a176", "a177", "a178", "a179", "a180", "a181", "a182", "a183", "a184", "a185", "a186", "a187", "a188", "a189", "a190", "a191", "a192", "a193", "a194", "a195", "a196", "a197", "a198", "a199", "a200", "a201", "a202", "a203", "a204", "a205", "a206", "a207", "a208", "a209", "a210", "a211", "a212", "a213", "a214", "a215", "a216", "a217", "a218", "a219", "a220", "a221", "a222", "a223", "a224", "a225", "a226", "a227", "a228", "a229", "a230", "a231", "a232", "a233", "a234", "a235", "a236", "a237", "a238", "a239", "a240", "a241", "a242", "a243", "a244", "a245", "a246", "a247", "a248", "a249", "a250", "a251", "a252", "a253", "a254", "a255", "s36", "s37", "s38", "s39", "s40", "s41", "s42", "s43", "s44", "s45", "s46", "s47", "s48", "s49", "s50", "s51", "s52", "s53", "s54", "s55", "s56", "s57", "s58", "s59", "s60", "s61", "s62", "s63", "s64", "s67", "s68", "s69", "s70", "s71", "s72", "s73", "s74", "s75", "s76", "s77", "s78", "s79", "s80", "s81", "s82", "s83", "s84", "s85", "s86", "s87", "s88", "s89", "s90", "s91", "s92", "s93", "s94", "s95");
  if (flg != 2u) {
    const int l2 = __builtin_amdgcn_mbcnt_hi(~0u, __builtin_amdgcn_mbcnt_lo(~0u, 0u));
    slow_path(x, dwl, dws, w1, w2, out, (float*)smem, w * 64 + l2, blockIdx.x >> 3, (blockIdx.x & 7) * 4);
  }
}

extern "C" void kernel_launch(void* const* d_in, const int* in_sizes, int n_in, void* d_out, int out_size, void* d_ws,
                              size_t ws_size, hipStream_t stream) {
  replk_kernel<<<256, 256, 0, stream>>>((const u16*)d_in[0], (const u16*)d_in[1], (const u16*)d_in[2], (const u16*)d_in[3],
                                        (const u16*)d_in[4], (u16*)d_out
                                        );
}
```

```cpp
#include <hip/hip_runtime.h>
#include <hip/hip_bf16.h>

typedef unsigned u32;
typedef unsigned short u16;
typedef unsigned long long u64;
typedef __bf16 bf16x2 __attribute__((ext_vector_type(2)));
typedef float f32x2 __attribute__((ext_vector_type(2)));
#define DI __device__ __forceinline__

constexpr int IMG = 512;
constexpr int CH = 96;
constexpr int HID = 384;
constexpr int PXB = 192;
constexpr int ROWB = 98304;
constexpr int TOEP_CS = 336;
constexpr int XT_RS = 320;
constexpr int XT_PP = 1360;
constexpr int XT_CG = 6752;
constexpr int GS = 6160;
constexpr int L_LUT = 0;
constexpr int L_TOEP = 8208;
constexpr int L_W = 40960;
constexpr int L_LUTN = 65536;
constexpr int L_XT = 81920;
constexpr int L_H = 108928;
constexpr int L_EX = 133568;
constexpr int XT_Q = 27024;
constexpr int L_MISC = 162992;
constexpr int L_TOTAL = 163056;
constexpr int LUTK = 106;
constexpr float GC1 = -2.302208198144325f, GC3 = -0.1029432395800235f;
__device__ const u32 CTAB[3 * 1024] = {
671109120,671098880,134490112,673187840,671372952,671229956,145500164,673318996,671636784,671361032,156510216,673450152,671900616,671492108,167520268,673581308,672164452,684206128,178530352,686295376,672428284,684337204,189540404,686426532,672692116,684468280,200550456,686557688,672955948,684599356,211560508,686688844,673219784,697313376,222570592,699402912,673483616,697444452,233580644,699534068,673747448,697575528,244590696,699665224,674011280,697706604,255600748,699796380,674254849,710420624,266610832,712510448,674518681,710551700,277620884,712641604,674782513,710682776,288630936,712772760,675046345,710813852,299640988,712903916,
684223077,723527872,310651072,721423680,684486909,723658948,321661124,721554836,683702165,723790024,332671176,721685992,683965997,723921100,343681228,721817148,685278409,736635120,354691312,734531216,685542241,736766196,365701364,734662372,684757497,736897272,376711416,734793528,685021329,737028348,387721468,734924684,686313474,749742368,398731552,747638752,686577306,749873444,409741604,747769908,685792562,750004520,420751656,747901064,686056394,750135596,431761708,748032220,687368806,762849616,442771792,760746288,687632638,762980692,453781844,760877444,686847894,763111768,464791896,761008600,687111726,763242844,475801948,761139756,
697337034,771762560,486812032,773853824,697600866,771893636,497822084,773984980,697864698,772024712,508832136,774116136,698128530,772155788,519842188,774247292,696274947,784869808,530852272,786961360,696538779,785000884,541862324,787092516,696802611,785131960,552872376,787223672,697066443,785263036,563882428,787354828,699427431,797977056,574892512,800068896,699691263,798108132,585902564,800200052,699955095,798239208,596912616,800331208,700218927,798370284,607922668,800462364,698385611,811084304,618932752,813176432,698649443,811215380,629942804,813307588,698913275,811346456,640952856,813438744,699177107,811477532,651962908,813569900,
710430724,824191552,135539264,822089664,710694556,824322628,146549316,822220820,709909812,824453704,157559368,822351976,710173644,824584780,168569420,822483132,709388904,837298800,179579504,835197200,709652736,837429876,190589556,835328356,708867992,837560952,201599608,835459512,709131824,837692028,212609660,835590668,712541388,850406048,223619744,848304736,712805220,850537124,234629796,848435892,712020476,850668200,245639848,848567048,712284308,850799276,256649900,848698204,711479301,863513296,267659984,861412272,711743133,863644372,278670036,861543428,710958389,863775448,289680088,861674584,711222221,863906524,300690140,861805740,
723544681,677394944,311704064,679484672,723808513,677526020,322714116,679615828,724072345,677657096,333724168,679746984,724336177,677788172,344734220,679878140,724600013,690502192,355744304,692592208,724863845,690633268,366754356,692723364,725127677,690764344,377764408,692854520,725391509,690895420,388774460,692985676,721440774,703609440,399784544,705699744,721704606,703740516,410794596,705830900,721968438,703871592,421804648,705962056,722232270,704002668,432814700,706093212,722496106,716716688,443824784,718807280,722759938,716847764,454834836,718938436,723023770,716978840,465844888,719069592,723287602,717109916,476854940,719200748,
736658638,725629632,487865024,731914816,736922470,725760708,498875076,732045972,736137726,725891784,509885128,732177128,736401558,726022860,520895180,732308284,737693703,738736880,531905264,745022352,737957535,738867956,542915316,745153508,737172791,738999032,553925368,745284664,737436623,739130108,564935420,745415820,734554731,751844128,575945504,758129888,734818563,751975204,586955556,758261044,734033819,752106280,597965608,758392200,734297651,752237356,608975660,758523356,735610063,764951376,619985744,771237424,735873895,765082452,630995796,771368580,735089151,765213528,642005848,771499736,735352983,765344604,653015900,771630892,
749752328,778058624,136592256,780150656,750016160,778189700,147602308,780281812,750279992,778320776,158612360,780443436,750543824,778451852,169622412,780574508,748710508,791165872,180632496,793288492,748974340,791296948,191642548,793419564,749238172,791428024,202652600,793550636,749502004,791559100,213662652,793681708,747668688,804273120,224672736,806395692,747932520,804404196,235682788,806526764,748196352,804535272,246692840,806657836,748460184,804666348,257702892,806788908,746606601,817380368,268712976,819502892,746870433,817511444,279723028,819633964,747134265,817642520,290733080,819765036,747398097,817773596,301743132,819896108,
762866285,826293312,312753216,832610092,763130117,826424388,323763268,832741164,762345373,826555464,334773320,832872236,762609205,826686540,345783372,833003308,761824465,839400560,356793456,845717292,762088297,839531636,367803508,845848364,761303553,839662712,378813560,845979436,761567385,839793788,389823612,846110508,760762378,852507808,400833696,858824492,761026210,852638884,411843748,858955564,760241466,852769960,422853800,859086636,760505298,852901036,433863852,859217708,759720558,865615056,444873936,871931692,759984390,865746132,455883988,872062764,759199646,865877208,466894040,872193836,759463478,866008284,477904092,872324908,
771785938,671098882,488908800,673226540,772049770,671229958,499918852,673357612,772313602,671361034,510928904,673488684,772577434,671492110,521938956,673619756,772821003,684206130,532949040,686333740,773084835,684337206,543959092,686464812,773348667,684468282,554969144,686595884,773612499,684599358,565979196,686726956,773876335,697313378,576989280,699440940,774140167,697444454,587999332,699572012,774403999,697575530,599009384,699703084,774667831,697706606,610019436,699834156,774931667,710420626,621029520,712548140,775195499,710551702,632039572,712679212,775459331,710682778,643049624,712810284,775723163,710813854,654059676,712941356,
784879628,723527874,137636032,721461036,785143460,723658950,148646084,721592108,784358716,723790026,159656136,721723180,784622548,723921102,170666188,721854252,785934960,736635122,181676272,734568236,786198792,736766198,192686324,734699308,785414048,736897274,203696376,734830380,785677880,737028350,214706428,734961452,786990292,749742370,225716512,747675436,787254124,749873446,236726564,747806508,786469380,750004522,247736616,747937580,786733212,750135598,258746668,748068652,788025357,762849618,269756752,760782636,788289189,762980694,280766804,760913708,787504445,763111770,291776856,761044780,787768277,763242846,302786908,761175852,
797993585,771762562,313796992,773889836,798257417,771893638,324807044,774020908,798521249,772024714,335817096,774151980,798785081,772155790,346827148,774283052,796951765,784869810,357837232,786997036,797215597,785000886,368847284,787128108,797479429,785131962,379857336,787259180,797743261,785263038,390867388,787390252,800083982,797977058,401877472,800104236,800347814,798108134,412887524,800235308,800611646,798239210,423897576,800366380,800875478,798370286,434907628,800497452,799042162,811084306,445917712,813211436,799305994,811215382,456927764,813342508,799569826,811346458,467937816,813473580,799833658,811477534,478947868,813604652,
811107542,824191554,489957952,822124332,811371374,824322630,500968004,822255404,810586630,824453706,511978056,822386476,810850462,824584782,522988108,822517548,810045455,837298802,533998192,835231532,810309287,837429878,545008244,835362604,809524543,837560954,556018296,835493676,809788375,837692030,567028348,835624748,813197939,850406050,578038432,848338732,813461771,850537126,589048484,848469804,812677027,850668202,600058536,848600876,812940859,850799278,611068588,848731948,812156119,863513298,622078672,861445932,812419951,863644374,633088724,861577004,811635207,863775450,644098776,861708076,811899039,863906526,655108828,861839148,
824201232,677394946,138689024,679517996,824465064,677526022,149699076,679649068,824728896,677657098,160709128,679780140,824992728,677788174,171719180,679911212,825256564,690502194,182729264,692625196,825520396,690633270,193739316,692756268,825784228,690764346,204749368,692887340,826048060,690895422,215759420,693018412,822117592,703609442,226769504,705732396,822381424,703740518,237779556,705863468,822645256,703871594,248789608,705994540,822909088,704002670,259799660,706125612,823152657,716716690,270809744,718839596,823416489,716847766,281819796,718970668,823680321,716978842,292829848,719101740,823944153,717109918,303839900,719232812,
837315189,725629634,314849984,731946796,837579021,725760710,325860036,732077868,836794277,725891786,336870088,732208940,837058109,726022862,347880140,732340012,838370521,738736882,358890224,745053996,838634353,738867958,369900276,745185068,837849609,738999034,380910328,745316140,838113441,739130110,391920380,745447212,835211282,751844130,402930464,758161196,835475114,751975206,413940516,758292268,834690370,752106282,424950568,758423340,834954202,752237358,435960620,758554412,836266614,764951378,446970704,771268396,836530446,765082454,457980756,771399468,835745702,765213530,468990808,771530540,836009534,765344606,480000860,771661612,
850429146,778058626,491010944,780181292,850692978,778189702,502020996,780312364,850956810,778320778,513031048,780443436,851220642,778451854,524041100,780574508,849367059,791165874,535051184,793288492,849630891,791296950,546061236,793419564,849894723,791428026,557071288,793550636,850158555,791559102,568081340,793681708,848325239,804273122,579091424,806395692,848589071,804404198,590101476,806526764,848852903,804535274,601111528,806657836,849116735,804666350,612121580,806788908,847283419,817380370,623131664,819502892,847547251,817511446,634141716,819633964,847811083,817642522,645151768,819765036,848074915,817773598,656161820,819896108,
863541411,826293314,656161856,832610092,863803555,826424390,656161860,832741164,863017123,826555466,656161864,832872236,863279267,826686542,656161868,833003308,862492835,839400562,656161904,845717292,862754979,839531638,656161908,845848364,861968547,839662714,656161912,845979436,862230691,839793790,656161916,846110508,861444259,852507810,656161952,858824492,861706403,852638886,656161956,858955564,860919971,852769962,656161960,859086636,861182115,852901038,656161964,859217708,860395683,865615058,656162000,871931692,860657827,865746134,656162004,872062764,859871395,865877210,656162008,872193836,860133539,866008286,656162012,872324908,
2151688192u,1885629024,1342187520,1342212212,2151688196u,1885891172,1452812292,1364494564,2151688200u,1886153320,1563437064,1386776836,2218797068u,1886415468,1674061836,1409059156,2165647376u,1886677616,1342253072,1342474360,2165647380u,1886939764,1452877844,1364756712,2164271128u,1887201912,1563502616,1387038984,2231379996u,1887464060,1674127388,1409321304,1082140704,1887726208,1342318624,1342998652,1082140708,1887988356,1452943396,1365281004,1082140712,1888250504,1563568168,1387563276,1149249580,1888512652,1674192940,1409845596,1108682800,1888774800,1342384176,1342736512,1108682804,1889036948,1453008948,1365018864,1107306552,1889299096,1563633720,1387301136,1174415420,1889561244,1674258492,1409583456,
268445760,1892969168,1342449728,1342474372,268445764,1893231316,1453074500,1364756692,268445768,1893493464,1563699272,1387038964,335554636,1893755612,1674324044,1409321316,269822032,1891920576,1342515280,1342736520,269822036,1892182724,1453140052,1365018840,268445784,1892444872,1563764824,1387301112,335554652,1892707020,1674389596,1409583464,1342187616,1890871984,1342580832,1343260812,1342187620,1891134132,1453205604,1365543132,1342187624,1891396280,1563830376,1387825404,1409296492,1891658428,1674455148,1410107756,1343563888,1889823392,1342646384,1342998672,1343563892,1890085540,1453271156,1365280992,1342187640,1890347688,1563895928,1387563264,1409296508,1890609836,1674520700,1409845616,
536881280,1895066352,1342711936,1364494484,536881284,1895328500,1453336708,1386776772,536881288,1895590648,1563961480,1409059108,603990156,1895852796,1674586252,1431341364,538257552,1894017760,1342777488,1364756632,538257556,1894279908,1453402260,1387038920,536881304,1894542056,1564027032,1409321256,603990172,1894804204,1674651804,1431603512,1610623136,1897163536,1342843040,1365280924,1610623140,1897425684,1453467812,1387563212,1610623144,1897687832,1564092584,1409845548,1677732012,1897949980,1674717356,1432127804,1611999408,1896114944,1342908592,1365018784,1611999412,1896377092,1453533364,1387301072,1610623160,1896639240,1564158136,1409583408,1677732028,1896901388,1674782908,1431865664,
809511104,1900309312,1342974144,1364756644,809511108,1900571460,1453598916,1387038900,809511112,1900833608,1564223688,1409321236,876619980,1901095756,1674848460,1431603524,823470288,1901357904,1343039696,1365018792,823470292,1901620052,1453664468,1387301048,822094040,1901882200,1564289240,1409583384,889202908,1902144348,1674914012,1431865672,1887447264,1898212128,1343105248,1365543084,1887447268,1898474276,1453730020,1387825340,1887447272,1898736424,1564354792,1410107676,1954556140,1898998572,1674979564,1432389964,1913989360,1899260720,1343170800,1365280944,1913989364,1899522868,1453795572,1387563200,1912613112,1899785016,1564420344,1409845536,1979721980,1900047164,1675045116,1432127824,
2151688448u,1784705124,1347430656,1452835440,2151688452u,1784967272,1458055428,1475117792,2151688456u,1785229420,1568680200,1497400064,2218797324u,1785491568,1679304972,1519682384,2165647632u,1785753716,1347496208,1453097588,2165647636u,1786015864,1458120980,1475379940,2164271384u,1786278012,1568745752,1497662212,2231380252u,1786540160,1679370524,1519944532,1082140960,1786802308,1347561760,1453621880,1082140964,1787064456,1458186532,1475904232,1082140968,1787326604,1568811304,1498186504,1149249836,1787588752,1679436076,1520468824,1108683056,1787850900,1347627312,1453359740,1108683060,1788113048,1458252084,1475642092,1107306808,1788375196,1568876856,1497924364,1174415676,1788637344,1679501628,1520206684,
268446016,1792045268,1347692864,1453097600,268446020,1792307416,1458317636,1475379920,268446024,1792569564,1568942408,1497662192,335554892,1792831712,1679567180,1519944544,269822288,1790996676,1347758416,1453359748,269822292,1791258824,1458383188,1475642068,268446040,1791520972,1569007960,1497924340,335554908,1791783120,1679632732,1520206692,1342187872,1789948084,1347823968,1453884040,1342187876,1790210232,1458448740,1476166360,1342187880,1790472380,1569073512,1498448632,1409296748,1790734528,1679698284,1520730984,1343564144,1788899492,1347889520,1453621900,1343564148,1789161640,1458514292,1475904220,1342187896,1789423788,1569139064,1498186492,1409296764,1789685936,1679763836,1520468844,
536881536,1794142452,1347955072,1475117712,536881540,1794404600,1458579844,1497400000,536881544,1794666748,1569204616,1519682336,603990412,1794928896,1679829388,1541964592,538257808,1793093860,1348020624,1475379860,538257812,1793356008,1458645396,1497662148,536881560,1793618156,1569270168,1519944484,603990428,1793880304,1679894940,1542226740,1610623392,1796239636,1348086176,1475904152,1610623396,1796501784,1458710948,1498186440,1610623400,1796763932,1569335720,1520468776,1677732268,1797026080,1679960492,1542751032,1611999664,1795191044,1348151728,1475642012,1611999668,1795453192,1458776500,1497924300,1610623416,1795715340,1569401272,1520206636,1677732284,1795977488,1680026044,1542488892,
809511360,1799385412,1348217280,1475379872,809511364,1799647560,1458842052,1497662128,809511368,1799909708,1569466824,1519944464,876620236,1800171856,1680091596,1542226752,823470544,1800434004,1348282832,1475642020,823470548,1800696152,1458907604,1497924276,822094296,1800958300,1569532376,1520206612,889203164,1801220448,1680157148,1542488900,1887447520,1797288228,1348348384,1476166312,1887447524,1797550376,1458973156,1498448568,1887447528,1797812524,1569597928,1520730904,1954556396,1798074672,1680222700,1543013192,1913989616,1798336820,1348413936,1475904172,1913989620,1798598968,1459038708,1498186428,1912613368,1798861116,1569663480,1520468764,1979722236,1799123264,1680288252,1542751052,
2151688704u,2087482984,1352673280,1563464828,2151688708u,2087745132,1463298052,1585747180,2151688712u,2088007280,1573922824,1608029452,2218797580u,2088269428,1684547596,1630311772,2165647888u,2088531576,1352738832,1563726976,2165647892u,2088793724,1463363604,1586009328,2164271640u,2089055872,1573988376,1608291600,2231380508u,2089318020,1684613148,1630573920,1082141216,2089580168,1352804384,1564251268,1082141220,2089842316,1463429156,1586533620,1082141224,2090104464,1574053928,1608815892,1149250092,2090366612,1684678700,1631098212,1108683312,2090628760,1352869936,1563989128,1108683316,2090890908,1463494708,1586271480,1107307064,2091153056,1574119480,1608553752,1174415932,2091415204,1684744252,1630836072,
268446272,2094823128,1352935488,1563726988,268446276,2095085276,1463560260,1586009308,268446280,2095347424,1574185032,1608291580,335555148,2095609572,1684809804,1630573932,269822544,2093774536,1353001040,1563989136,269822548,2094036684,1463625812,1586271456,268446296,2094298832,1574250584,1608553728,335555164,2094560980,1684875356,1630836080,1342188128,2092725944,1353066592,1564513428,1342188132,2092988092,1463691364,1586795748,1342188136,2093250240,1574316136,1609078020,1409297004,2093512388,1684940908,1631360372,1343564400,2091677352,1353132144,1564251288,1343564404,2091939500,1463756916,1586533608,1342188152,2092201648,1574381688,1608815880,1409297020,2092463796,1685006460,1631098232,
536881792,2096920312,1353197696,1585747100,536881796,2097182460,1463822468,1608029388,536881800,2097444608,1574447240,1630311724,603990668,2097706756,1685072012,1652593980,538258064,2095871720,1353263248,1586009248,538258068,2096133868,1463888020,1608291536,536881816,2096396016,1574512792,1630573872,603990684,2096658164,1685137564,1652856128,1610623648,2099017496,1353328800,1586533540,1610623652,2099279644,1463953572,1608815828,1610623656,2099541792,1574578344,1631098164,1677732524,2099803940,1685203116,1653380420,1611999920,2097968904,1353394352,1586271400,1611999924,2098231052,1464019124,1608553688,1610623672,2098493200,1574643896,1630836024,1677732540,2098755348,1685268668,1653118280,
809511616,2102163272,1353459904,1586009260,809511620,2102425420,1464084676,1608291516,809511624,2102687568,1574709448,1630573852,876620492,2102949716,1685334220,1652856140,823470800,2103211864,1353525456,1586271408,823470804,2103474012,1464150228,1608553664,822094552,2103736160,1574775000,1630836000,889203420,2103998308,1685399772,1653118288,1887447776,2100066088,1353591008,1586795700,1887447780,2100328236,1464215780,1609077956,1887447784,2100590384,1574840552,1631360292,1954556652,2100852532,1685465324,1653642580,1913989872,2101114680,1353656560,1586533560,1913989876,2101376828,1464281332,1608815816,1912613624,2101638976,1574906104,1631098152,1979722492,2101901124,1685530876,1653380440,
2151688960u,1986559084,1357916416,1674088056,2151688964u,1986821232,1468541188,1696370408,2151688968u,1987083380,1579165960,1718652680,2218797836u,1987345528,1689790732,1740935000,2165648144u,1987607676,1357981968,1674350204,2165648148u,1987869824,1468606740,1696632556,2164271896u,1988131972,1579231512,1718914828,2231380764u,1988394120,1689856284,1741197148,1082141472,1988656268,1358047520,1674874496,1082141476,1988918416,1468672292,1697156848,1082141480,1989180564,1579297064,1719439120,1149250348,1989442712,1689921836,1741721440,1108683568,1989704860,1358113072,1674612356,1108683572,1989967008,1468737844,1696894708,1107307320,1990229156,1579362616,1719176980,1174416188,1990491304,1689987388,1741459300,
268446528,1993899228,1358178624,1674350216,268446532,1994161376,1468803396,1696632536,268446536,1994423524,1579428168,1718914808,335555404,1994685672,1690052940,1741197160,269822800,1992850636,1358244176,1674612364,269822804,1993112784,1468868948,1696894684,268446552,1993374932,1579493720,1719176956,335555420,1993637080,1690118492,1741459308,1342188384,1991802044,1358309728,1675136656,1342188388,1992064192,1468934500,1697418976,1342188392,1992326340,1579559272,1719701248,1409297260,1992588488,1690184044,1741983600,1343564656,1990753452,1358375280,1674874516,1343564660,1991015600,1469000052,1697156836,1342188408,1991277748,1579624824,1719439108,1409297276,1991539896,1690249596,1741721460,
536882048,1995996412,1358440832,1696370328,536882052,1996258560,1469065604,1718652616,536882056,1996520708,1579690376,1740934952,603990924,1996782856,1690315148,1763217208,538258320,1994947820,1358506384,1696632476,538258324,1995209968,1469131156,1718914764,536882072,1995472116,1579755928,1741197100,603990940,1995734264,1690380700,1763479356,1610623904,1998093596,1358571936,1697156768,1610623908,1998355744,1469196708,1719439056,1610623912,1998617892,1579821480,1741721392,1677732780,1998880040,1690446252,1764003648,1612000176,1997045004,1358637488,1696894628,1612000180,1997307152,1469262260,1719176916,1610623928,1997569300,1579887032,1741459252,1677732796,1997831448,1690511804,1763741508,
809511872,2001239372,1358703040,1696632488,809511876,2001501520,1469327812,1718914744,809511880,2001763668,1579952584,1741197080,876620748,2002025816,1690577356,1763479368,823471056,2002287964,1358768592,1696894636,823471060,2002550112,1469393364,1719176892,822094808,2002812260,1580018136,1741459228,889203676,2003074408,1690642908,1763741516,1887448032,1999142188,1358834144,1697418928,1887448036,1999404336,1469458916,1719701184,1887448040,1999666484,1580083688,1741983520,1954556908,1999928632,1690708460,1764265808,1913990128,2000190780,1358899696,1697156788,1913990132,2000452928,1469524468,1719439044,1912613880,2000715076,1580149240,1741721380,1979722748,2000977224,1690774012,1764003668,
136579076,1343253088,52428,12582912,135530496,1453879396,843229,12648449,134481920,1564505704,1634030,12713986,145229824,1675132012,2411724,12779523,136841224,1343318640,3198156,13369356,135792808,1453944948,3988957,13434893,134744232,1564571256,4779758,13500430,139724968,1675197564,5557452,13565967,137103372,1343384208,6343884,14155800,136054952,1454010516,7134685,14221337,135006376,1564636824,7925486,14286874,139724968,1675263132,8703180,14352411,137365520,1343449728,9489612,14942244,136316928,1454076036,10280413,15007781,135268352,1564702344,11071214,15073318,145229824,1675328652,11848908,15138855,
139724868,1365535332,117964,12582960,139724852,1476161640,908765,12648497,138676260,1586787948,1699566,12714034,137627668,1697414256,2477260,12779571,145229896,1365600884,3263692,13369404,145229880,1476227192,4054493,13434941,138938408,1586853500,4845294,13500478,137889816,1697479808,5622988,13566015,145229900,1365666452,6409420,14155848,145229884,1476292760,7200221,14221385,139200556,1586919068,7991022,14286922,138151964,1697545376,8768716,14352459,139724880,1365731972,9555148,14942292,139724864,1476358280,10345949,15007829,139462704,1586984588,11136750,15073366,138414112,1697610896,11914444,15138903,
137627668,1387817576,183500,12583008,136579076,1498443884,974301,12648545,135530496,1609070192,1765102,12714082,134481920,1719696500,2542796,12779619,137889816,1387883128,3329228,13369452,136841224,1498509436,4120029,13434989,135792808,1609135744,4910830,13500526,134744232,1719762052,5688524,13566063,138151964,1387948696,6474956,14155896,137103372,1498575004,7265757,14221433,136054952,1609201312,8056558,14286970,135006376,1719827620,8834252,14352507,138414112,1388014216,9620684,14942340,137365520,1498640524,10411485,15007877,136316928,1609266832,11202286,15073414,135268352,1719893140,11979980,15138951,
139724968,1410099820,249036,12583056,139724868,1520726128,1039837,12648593,139724852,1631352436,1830638,12714130,138676260,1741978744,2608332,12779667,145229824,1410165372,3394764,13369500,145229896,1520791680,4185565,13435037,145229880,1631417988,4976366,13500574,138938408,1742044296,5754060,13566111,145229824,1410230940,6540492,14155944,145229900,1520857248,7331293,14221481,145229884,1631483556,8122094,14287018,139200556,1742109864,8899788,14352555,139724968,1410296460,9686220,14942388,139724880,1520922768,10477021,15007925,139724864,1631549076,11267822,15073462,139462704,1742175384,12045516,15138999,
180619940,1348496080,52428,12582912,179571360,1459122388,843229,12648449,178522784,1569748696,1634030,12713986,189270688,1680375004,2411724,12779523,180882088,1348561600,3198156,13369356,179833672,1459187908,3988957,13434893,178785096,1569814216,4779758,13500430,183765832,1680440524,5557452,13565967,181144236,1348627104,6343884,14155800,180095816,1459253412,7134685,14221337,179047240,1569879720,7925486,14286874,183765832,1680506028,8703180,14352411,181406384,1348692656,9489612,14942244,180357792,1459318964,10280413,15007781,179309216,1569945272,11071214,15073318,189270688,1680571580,11848908,15138855,
183765732,1370778324,117964,12582960,183765716,1481404632,908765,12648497,182717124,1592030940,1699566,12714034,181668532,1702657248,2477260,12779571,189270760,1370843844,3263692,13369404,189270744,1481470152,4054493,13434941,182979272,1592096460,4845294,13500478,181930680,1702722768,5622988,13566015,189270764,1370909348,6409420,14155848,189270748,1481535656,7200221,14221385,183241420,1592161964,7991022,14286922,182192828,1702788272,8768716,14352459,183765744,1370974900,9555148,14942292,183765728,1481601208,10345949,15007829,183503568,1592227516,11136750,15073366,182454976,1702853824,11914444,15138903,
181668532,1393060568,183500,12583008,180619940,1503686876,974301,12648545,179571360,1614313184,1765102,12714082,178522784,1724939492,2542796,12779619,181930680,1393126088,3329228,13369452,180882088,1503752396,4120029,13434989,179833672,1614378704,4910830,13500526,178785096,1725005012,5688524,13566063,182192828,1393191592,6474956,14155896,181144236,1503817900,7265757,14221433,180095816,1614444208,8056558,14286970,179047240,1725070516,8834252,14352507,182454976,1393257144,9620684,14942340,181406384,1503883452,10411485,15007877,180357792,1614509760,11202286,15073414,179309216,1725136068,11979980,15138951,
183765832,1415342812,249036,12583056,183765732,1525969120,1039837,12648593,183765716,1636595428,1830638,12714130,182717124,1747221736,2608332,12779667,189270688,1415408332,3394764,13369500,189270760,1526034640,4185565,13435037,189270744,1636660948,4976366,13500574,182979272,1747287256,5754060,13566111,189270688,1415473836,6540492,14155944,189270764,1526100144,7331293,14221481,189270748,1636726452,8122094,14287018,183241420,1747352760,8899788,14352555,183765832,1415539388,9686220,14942388,183765744,1526165696,10477021,15007925,183765728,1636792004,11267822,15073462,183503568,1747418312,12045516,15138999,
224660804,1353738992,52428,12582912,223612224,1464365300,843229,12648449,222563648,1574991608,1634030,12713986,233311552,1685617916,2411724,12779523,224922952,1353804512,3198156,13369356,223874536,1464430820,3988957,13434893,222825960,1575057128,4779758,13500430,227806696,1685683436,5557452,13565967,225185100,1353870080,6343884,14155800,224136680,1464496388,7134685,14221337,223088104,1575122696,7925486,14286874,227806696,1685749004,8703180,14352411,225447248,1353935632,9489612,14942244,224398656,1464561940,10280413,15007781,223350080,1575188248,11071214,15073318,233311552,1685814556,11848908,15138855,
227806596,1376021236,117964,12582960,227806580,1486647544,908765,12648497,226757988,1597273852,1699566,12714034,225709396,1707900160,2477260,12779571,233311624,1376086756,3263692,13369404,233311608,1486713064,4054493,13434941,227020136,1597339372,4845294,13500478,225971544,1707965680,5622988,13566015,233311628,1376152324,6409420,14155848,233311612,1486778632,7200221,14221385,227282284,1597404940,7991022,14286922,226233692,1708031248,8768716,14352459,227806608,1376217876,9555148,14942292,227806592,1486844184,10345949,15007829,227544432,1597470492,11136750,15073366,226495840,1708096800,11914444,15138903,
225709396,1398303480,183500,12583008,224660804,1508929788,974301,12648545,223612224,1619556096,1765102,12714082,222563648,1730182404,2542796,12779619,225971544,1398369000,3329228,13369452,224922952,1508995308,4120029,13434989,223874536,1619621616,4910830,13500526,222825960,1730247924,5688524,13566063,226233692,1398434568,6474956,14155896,225185100,1509060876,7265757,14221433,224136680,1619687184,8056558,14286970,223088104,1730313492,8834252,14352507,226495840,1398500120,9620684,14942340,225447248,1509126428,10411485,15007877,224398656,1619752736,11202286,15073414,223350080,1730379044,11979980,15138951,
227806696,1420585724,249036,12583056,227806596,1531212032,1039837,12648593,227806580,1641838340,1830638,12714130,226757988,1752464648,2608332,12779667,233311552,1420651244,3394764,13369500,233311624,1531277552,4185565,13435037,233311608,1641903860,4976366,13500574,227020136,1752530168,5754060,13566111,233311552,1420716812,6540492,14155944,233311628,1531343120,7331293,14221481,233311612,1641969428,8122094,14287018,227282284,1752595736,8899788,14352555,227806696,1420782364,9686220,14942388,227806608,1531408672,10477021,15007925,227806592,1642034980,11267822,15073462,227544432,1752661288,12045516,15138999,
268701668,1358981952,52428,12582912,267653088,1469608260,843229,12648449,266604512,1580234568,1634030,12713986,277352416,1690860876,2411724,12779523,268963816,1359047504,3198156,13369356,267915400,1469673812,3988957,13434893,266866824,1580300120,4779758,13500430,271847560,1690926428,5557452,13565967,269225964,1359113008,6343884,14155800,268177544,1469739316,7134685,14221337,267128968,1580365624,7925486,14286874,271847560,1690991932,8703180,14352411,269488112,1359178528,9489612,14942244,268439520,1469804836,10280413,15007781,267390944,1580431144,11071214,15073318,277352416,1691057452,11848908,15138855,
271847460,1381264196,117964,12582960,271847444,1491890504,908765,12648497,270798852,1602516812,1699566,12714034,269750260,1713143120,2477260,12779571,277352488,1381329748,3263692,13369404,277352472,1491956056,4054493,13434941,271061000,1602582364,4845294,13500478,270012408,1713208672,5622988,13566015,277352492,1381395252,6409420,14155848,277352476,1492021560,7200221,14221385,271323148,1602647868,7991022,14286922,270274556,1713274176,8768716,14352459,271847472,1381460772,9555148,14942292,271847456,1492087080,10345949,15007829,271585296,1602713388,11136750,15073366,270536704,1713339696,11914444,15138903,
269750260,1403546440,183500,12583008,268701668,1514172748,974301,12648545,267653088,1624799056,1765102,12714082,266604512,1735425364,2542796,12779619,270012408,1403611992,3329228,13369452,268963816,1514238300,4120029,13434989,267915400,1624864608,4910830,13500526,266866824,1735490916,5688524,13566063,270274556,1403677496,6474956,14155896,269225964,1514303804,7265757,14221433,268177544,1624930112,8056558,14286970,267128968,1735556420,8834252,14352507,270536704,1403743016,9620684,14942340,269488112,1514369324,10411485,15007877,268439520,1624995632,11202286,15073414,267390944,1735621940,11979980,15138951,
271847560,1425828684,249036,12583056,271847460,1536454992,1039837,12648593,271847444,1647081300,1830638,12714130,270798852,1757707608,2608332,12779667,277352416,1425894236,3394764,13369500,277352488,1536520544,4185565,13435037,277352472,1647146852,4976366,13500574,271061000,1757773160,5754060,13566111,277352416,1425959740,6540492,14155944,277352492,1536586048,7331293,14221481,277352476,1647212356,8122094,14287018,271323148,1757838664,8899788,14352555,271847560,1426025260,9686220,14942388,271847472,1536651568,10477021,15007925,271847456,1647277876,11267822,15073462,271585296,1757904184,12045516,15138999};

DI float bf_lo(u32 v) { return __uint_as_float(v << 16); }
DI u32 pk_bf16(float a, float b) { f32x2 t = {a, b}; return __builtin_bit_cast(u32, __builtin_convertvector(t, bf16x2)); }

DI void slow_path(const u16* __restrict__ x, const u16* __restrict__ dwl, const u16* __restrict__ dws, const u16* __restrict__ w1,
                  const u16* __restrict__ w2, u16* __restrict__ out, float* sm, int t, int tx, int ty0) {
  float* o = sm;
  float* g = sm + 8 * CH;
  float* st = g + 8 * HID;
#pragma unroll 1
  for (int b = 0; b < 128; ++b) {
    const int y = ty0 * 16 + (b >> 1), x0 = tx * 16 + (b & 1) * 8;
    __syncthreads();
#pragma unroll 1
    for (int i = t; i < 8 * CH; i += 256) {
      const int p = i / CH, c = i - p * CH;
      float acc = 0.f;
#pragma unroll 1
      for (int a = 0; a < 5; ++a)
#pragma unroll 1
        for (int e = 0; e < 5; ++e) {
          const int yy = y + a - 2, xc = x0 + p + e - 2;
          if (yy < 0 || yy >= IMG || xc < 0 || xc >= IMG) continue;
          const float xv = bf_lo(x[(size_t)(yy * IMG + xc) * CH + c]);
          acc = __builtin_fmaf(bf_lo(dws[(a * 5 + e) * CH + c]), xv, acc);
          if (a >= 1 && a <= 3 && e >= 1 && e <= 3) acc = __builtin_fmaf(bf_lo(dwl[((a - 1) * 3 + (e - 1)) * CH + c]), xv, acc);
        }
      o[i] = acc;
    }
    __syncthreads();
    if (t < 8) {
      float m = 0.f, v = 0.f;
#pragma unroll 1
      for (int c = 0; c < CH; ++c) m += o[t * CH + c];
      m *= (1.0f / CH);
#pragma unroll 1
      for (int c = 0; c < CH; ++c) { const float d = o[t * CH + c] - m; v = __builtin_fmaf(d, d, v); }
      st[2 * t] = m; st[2 * t + 1] = __builtin_amdgcn_rsqf(v * (1.0f / CH) + 1e-5f);
    }
    __syncthreads();
#pragma unroll 1
    for (int i = t; i < 8 * HID; i += 256) {
      const int p = i / HID, e = i - p * HID;
      const float m = st[2 * p], r = st[2 * p + 1];
      float u = 0.f;
#pragma unroll 1
      for (int c = 0; c < CH; ++c) u = __builtin_fmaf((o[p * CH + c] - m) * r, bf_lo(w1[c * HID + e]), u);
      const float z = u * __builtin_fmaf(u * u, GC3, GC1);
      g[i] = u * __builtin_amdgcn_rcpf(1.0f + __builtin_amdgcn_exp2f(z));
    }
    __syncthreads();
#pragma unroll 1
    for (int i = t; i < 8 * CH; i += 256) {
      const int p = i / CH, c = i - p * CH;
      float acc = 0.f;
#pragma unroll 1
      for (int e = 0; e < HID; ++e) acc = __builtin_fmaf(g[p * HID + e], bf_lo(w2[e * CH + c]), acc);
      const size_t idx = (size_t)(y * IMG + x0 + p) * CH + c;
      out[idx] = (u16)pk_bf16(bf_lo(x[idx]) + acc, 0.f);
    }
  }
}

__global__ __launch_bounds__(256, 1) void replk_kernel(const u16* __restrict__ x, const u16* __restrict__ dwl,
    const u16* __restrict__ dws, const u16* __restrict__ w1, const u16* __restrict__ w2, u16* __restrict__ out
    ) {
  __shared__ __attribute__((aligned(16))) char smem[L_TOTAL];
  __attribute__((address_space(3))) char* smem3 = (__attribute__((address_space(3))) char*)smem;
  const int w = __builtin_amdgcn_readfirstlane(threadIdx.x >> 6);
  u32 tid = threadIdx.x, blk = blockIdx.x;
  u32 flg = (u32)(size_t)smem3 ? 1u : 0u;
  u64 ka = (u64)__builtin_amdgcn_kernarg_segment_ptr();
  asm volatile(
    ".p2align 3\n"
    "s_load_dwordx2 s[40:41], s[98:99], 0x0\n"
    "s_load_dwordx2 s[88:89], s[98:99], 0x10\n"
    "s_load_dwordx2 s[92:93], s[98:99], 0x8\n"
    "s_load_dwordx2 s[84:85], s[98:99], 0x20\n"
    "s_load_dwordx2 s[80:81], s[98:99], 0x18\n"
    "s_load_dwordx2 s[44:45], s[98:99], 0x28\n"
    "v_lshlrev_b32_e32 v213, 4, v208\n"
    "v_lshrrev_b32_e32 v0, 6, v208\n"
    "s_add_u32 vcc_lo, s96, 0\n"
    "s_addc_u32 vcc_hi, s97, 0\n"
    "global_load_dwordx4 v[168:171], v213, vcc\n"
    "s_add_u32 vcc_lo, s96, 4096\n"
    "s_addc_u32 vcc_hi, s97, 0\n"
    "global_load_dwordx4 v[172:175], v213, vcc\n"
    "s_add_u32 vcc_lo, s96, 8192\n"
    "s_addc_u32 vcc_hi, s97, 0\n"
    "global_load_dwordx4 v[176:179], v213, vcc\n"
    "s_getpc_b64 s[76:77]\n"
    ".Lreplk_pc0:\n"
    "s_bfe_u32 s63, s65, 0x30003\n"
    "v_lshrrev_b32_e32 v5, 2, v208\n"
    "s_lshl_b32 s63, s63, 6\n"
    "v_add_lshl_u32 v5, s63, v5, 7\n"
    "v_min_u32_e32 v215, .Lreplk_end-.Lreplk_pc0-4, v5\n"
    "global_load_dword v233, v215, s[76:77]\n"
    "v_readfirstlane_b32 s60, v0\n"
    "v_min_u32_e32 v1, 239, v208\n"
    "v_mul_u32_u24_e32 v2, 1366, v1\n"
    "s_lshr_b32 s61, s65, 3\n"
    "s_and_b32 s62, s65, 7\n"
    "v_lshrrev_b32_e32 v2, 16, v2\n"
    "v_lshlrev_b32_e32 v3, 2, v1\n"
    "s_lshl_b32 s62, s62, 6\n"
    "s_movk_i32 s63, 768\n"
    "v_mad_u32_u24 v211, v2, s63, v3\n"
    "v_mul_u32_u24_e32 v4, 384, v2\n"
    "v_add_u32_e32 v2, -1, v2\n"
    "s_movk_i32 s90, 4800\n"
    "s_mov_b32 s91, 0x20000\n"
    "v_add_u32_e32 v3, -576, v3\n"
    "v_cmp_gt_u32_e32 vcc, 3, v2\n"
    "v_add_u32_e32 v3, v3, v4\n"
    "v_bfrev_b32_e32 v4, 1\n"
    "s_movk_i32 s94, 1728\n"
    "s_mov_b32 s95, 0x20000\n"
    "v_cndmask_b32_e32 v212, v4, v3, vcc\n"
    "s_lshl_b32 s99, s62, 5\n"
    "s_add_u32 s99, s99, s61\n"
    "s_mulk_i32 s99, 3072\n"
    "s_lshr_b32 s51, s60, 1\n"
    "s_mul_i32 s63, s51, 18432\n"
    "s_lshl_b32 s52, s60, 13\n"
    "s_add_u32 s63, s63, s52\n"
    "v_add_u32_e32 v210, s63, v213\n"
    "s_mul_i32 s38, s60, 9216\n"
    "s_add_u32 s38, s38, 40960\n"
    "s_mov_b32 s86, 73728\n"
    "s_mov_b32 s87, 0x20000\n"
    "v_and_b32_e32 v5, 63, v208\n"
    "v_min_u32_e32 v5, 8, v5\n"
    "s_and_b32 s52, s60, 1\n"
    "s_lshl_b32 s53, s61, 1\n"
    "s_add_u32 s52, s52, s53\n"
    "s_mul_i32 s52, s52, 9\n"
    "v_add_lshl_u32 v214, s52, v5, 7\n"
    "s_cmp_eq_u32 s61, 0\n"
    "s_cselect_b32 s52, 24, 0\n"
    "s_cmp_eq_u32 s61, 31\n"
    "s_cselect_b32 s53, 40, s52\n"
    "s_sub_u32 s53, 256, s53\n"
    "v_subrev_u32_e32 v6, s52, v208\n"
    "s_sub_u32 s63, s99, 384\n"
    "v_cmp_gt_u32_e32 vcc, s53, v6\n"
    "v_lshl_add_u32 v7, v1, 4, s63\n"
    "v_bfe_u32 v8, v208, 4, 2\n"
    "v_lshl_or_b32 v8, v0, 3, v8\n"
    "v_and_b32_e32 v9, 0xf0, v213\n"
    "v_cndmask_b32_e32 v208, v4, v7, vcc\n"
    "v_mul_u32_u24_e32 v8, 768, v8\n"
    "s_mov_b32 s42, 50331648\n"
    "s_mov_b32 s43, 0x20000\n"
    "v_or_b32_e32 v209, v8, v9\n"
    "s_mov_b32 s82, 73728\n"
    "s_mov_b32 s83, 0x20000\n"
    "s_cmp_eq_u32 s62, 0\n"
    "s_cselect_b32 s78, 0, s42\n"
    "s_mov_b32 s79, 0x20000\n"
    "s_waitcnt lgkmcnt(0)\n"
    "s_cmp_lt_u32 s60, 2\n"
    "s_nop 0\n"
    "s_cselect_b64 s[70:71], s[80:81], s[84:85]\n"
    "s_sub_u32 s76, s40, 196608\n"
    "s_subb_u32 s77, s41, 0\n"
    "s_mov_b32 s65, 0\n"
    "buffer_load_dwordx4 a[224:227], v208, s[76:79], s65 offen\n"
    "s_mov_b32 s65, 98304\n"
    "buffer_load_dwordx4 a[228:231], v208, s[76:79], s65 offen\n"
    "s_mov_b32 s65, 196608\n"
    "buffer_load_dwordx4 a[232:235], v208, s[40:43], s65 offen\n"
    "s_mov_b32 s65, 294912\n"
    "buffer_load_dwordx4 a[236:239], v208, s[40:43], s65 offen\n"
    "buffer_load_dword v224, v211, s[88:91], 0 offen offset:0\n"
    "buffer_load_dword v225, v211, s[88:91], 0 offen offset:192\n"
    "buffer_load_dword v226, v211, s[88:91], 0 offen offset:384\n"
    "buffer_load_dword v227, v211, s[88:91], 0 offen offset:576\n"
    "buffer_load_dword v228, v211, s[88:91], 0 offen offset:768\n"
    "v_mov_b32_e64 v88, 0\n"
    "v_mov_b32_e32 v240, 0xdddd8888\n"
    "v_mov_b32_e64 v89, 0\n"
    "v_mov_b32_e64 v200, 0\n"
    "v_mov_b32_e64 v90, 0\n"
    "v_mov_b32_e64 v91, 0\n"
    "v_mov_b32_e64 v59, 0\n"
    "v_mov_b32_e64 v60, 0\n"
    "v_mov_b32_e64 v67, 0\n"
    "v_mov_b32_e64 v68, 0\n"
    "v_mov_b32_e64 v75, 0\n"
    "v_mov_b32_e64 v76, 0\n"
    "v_mov_b32_e64 v83, 0\n"
    "v_mov_b32_e64 v84, 0\n"
    "v_mov_b32_e32 v36, 0x75089ba4\n"
    "v_mov_b32_e32 v38, 0xbdd2d3e8\n"
    "v_mov_b32_e32 v32, 0xc0135761\n"
    "v_lshlrev_b32_e64 v34, 16, v213\n"
    "v_lshlrev_b32_e64 v234, 1, v213\n"
    "v_or_b32_e32 v0, 0x8000, v34\n"
    "v_or_b32_e32 v1, 0x18000, v34\n"
    "v_or_b32_e32 v2, 0x28000, v34\n"
    "v_or_b32_e32 v3, 0x38000, v34\n"
    "v_or_b32_e32 v4, 0x48000, v34\n"
    "v_or_b32_e32 v5, 0x58000, v34\n"
    "v_or_b32_e32 v6, 0x68000, v34\n"
    "v_or_b32_e32 v7, 0x78000, v34\n"
    "v_cmp_ne_u32_e64 s[72:73], 0, v34\n"
    "s_nop 0\n"
    "s_nop 1\n"
    "v_cndmask_b32_e64 v0, 0, v0, s[72:73]\n"
    "v_pk_mul_f32 v[0:1], v[0:1], v[36:37] op_sel_hi:[1,0]\n"
    "v_pk_mul_f32 v[2:3], v[2:3], v[36:37] op_sel_hi:[1,0]\n"
    "v_pk_mul_f32 v[4:5], v[4:5], v[36:37] op_sel_hi:[1,0]\n"
    "v_pk_mul_f32 v[6:7], v[6:7], v[36:37] op_sel_hi:[1,0]\n"
    "v_pk_mul_f32 v[8:9], v[0:1], v[0:1]\n"
    "v_pk_mul_f32 v[10:11], v[2:3], v[2:3]\n"
    "v_pk_mul_f32 v[12:13], v[4:5], v[4:5]\n"
    "v_pk_mul_f32 v[14:15], v[6:7], v[6:7]\n"
    "v_pk_fma_f32 v[8:9], v[8:9], v[38:39], v[32:33] op_sel_hi:[1,0,0]\n"
    "v_pk_fma_f32 v[10:11], v[10:11], v[38:39], v[32:33] op_sel_hi:[1,0,0]\n"
    "v_pk_fma_f32 v[12:13], v[12:13], v[38:39], v[32:33] op_sel_hi:[1,0,0]\n"
    "v_pk_fma_f32 v[14:15], v[14:15], v[38:39], v[32:33] op_sel_hi:[1,0,0]\n"
    "v_pk_mul_f32 v[8:9], v[8:9], v[0:1]\n"
    "v_pk_mul_f32 v[10:11], v[10:11], v[2:3]\n"
    "v_pk_mul_f32 v[12:13], v[12:13], v[4:5]\n"
    "v_pk_mul_f32 v[14:15], v[14:15], v[6:7]\n"
    "v_exp_f32_e64 v8, v8\n"
    "v_exp_f32_e64 v9, v9\n"
    "v_exp_f32_e64 v10, v10\n"
    "v_exp_f32_e64 v11, v11\n"
    "v_exp_f32_e64 v12, v12\n"
    "v_exp_f32_e64 v13, v13\n"
    "v_exp_f32_e64 v14, v14\n"
    "v_exp_f32_e64 v15, v15\n"
    "v_pk_add_f32 v[16:17], v[8:9], 1.0 op_sel_hi:[1,0]\n"
    "v_pk_add_f32 v[18:19], v[10:11], 1.0 op_sel_hi:[1,0]\n"
    "v_pk_add_f32 v[20:21], v[12:13], 1.0 op_sel_hi:[1,0]\n"
    "v_pk_add_f32 v[22:23], v[14:15], 1.0 op_sel_hi:[1,0]\n"
    "v_rcp_f32_e64 v16, v16\n"
    "v_rcp_f32_e64 v17, v17\n"
    "v_rcp_f32_e64 v18, v18\n"
    "v_rcp_f32_e64 v19, v19\n"
    "v_rcp_f32_e64 v20, v20\n"
    "v_rcp_f32_e64 v21, v21\n"
    "v_rcp_f32_e64 v22, v22\n"
    "v_rcp_f32_e64 v23, v23\n"
    "v_pk_mul_f32 v[16:17], v[16:17], v[0:1]\n"
    "v_pk_mul_f32 v[18:19], v[18:19], v[2:3]\n"
    "v_pk_mul_f32 v[20:21], v[20:21], v[4:5]\n"
    "v_pk_mul_f32 v[22:23], v[22:23], v[6:7]\n"
    "v_pk_mul_f32 v[8:9], v[8:9], v[16:17]\n"
    "v_pk_mul_f32 v[10:11], v[10:11], v[18:19]\n"
    "v_pk_mul_f32 v[12:13], v[12:13], v[20:21]\n"
    "v_pk_mul_f32 v[14:15], v[14:15], v[22:23]\n"
    "v_cvt_pk_bf16_f32 v188, v16, v17\n"
    "v_cvt_pk_bf16_f32 v189, v18, v19\n"
    "v_cvt_pk_bf16_f32 v190, v20, v21\n"
    "v_cvt_pk_bf16_f32 v191, v22, v23\n"
    "v_cvt_pk_bf16_f32 v24, -v8, -v9\n"
    "v_cvt_pk_bf16_f32 v25, -v10, -v11\n"
    "v_cvt_pk_bf16_f32 v26, -v12, -v13\n"
    "v_cvt_pk_bf16_f32 v27, -v14, -v15\n"
    "ds_write_b128 v234, v[24:27] offset:0\n"
    "v_or_b32_e32 v0, 0x88000, v34\n"
    "v_or_b32_e32 v1, 0x98000, v34\n"
    "v_or_b32_e32 v2, 0xa8000, v34\n"
    "v_or_b32_e32 v3, 0xb8000, v34\n"
    "v_or_b32_e32 v4, 0xc8000, v34\n"
    "v_or_b32_e32 v5, 0xd8000, v34\n"
    "v_or_b32_e32 v6, 0xe8000, v34\n"
    "v_or_b32_e32 v7, 0xf8000, v34\n"
    "v_pk_mul_f32 v[0:1], v[0:1], v[36:37] op_sel_hi:[1,0]\n"
    "v_pk_mul_f32 v[2:3], v[2:3], v[36:37] op_sel_hi:[1,0]\n"
    "v_pk_mul_f32 v[4:5], v[4:5], v[36:37] op_sel_hi:[1,0]\n"
    "v_pk_mul_f32 v[6:7], v[6:7], v[36:37] op_sel_hi:[1,0]\n"
    "v_pk_mul_f32 v[8:9], v[0:1], v[0:1]\n"
    "v_pk_mul_f32 v[10:11], v[2:3], v[2:3]\n"
    "v_pk_mul_f32 v[12:13], v[4:5], v[4:5]\n"
    "v_pk_mul_f32 v[14:15], v[6:7], v[6:7]\n"
    "v_pk_fma_f32 v[8:9], v[8:9], v[38:39], v[32:33] op_sel_hi:[1,0,0]\n"
    "v_pk_fma_f32 v[10:11], v[10:11], v[38:39], v[32:33] op_sel_hi:[1,0,0]\n"
    "v_pk_fma_f32 v[12:13], v[12:13], v[38:39], v[32:33] op_sel_hi:[1,0,0]\n"
    "v_pk_fma_f32 v[14:15], v[14:15], v[38:39], v[32:33] op_sel_hi:[1,0,0]\n"
    "v_pk_mul_f32 v[8:9], v[8:9], v[0:1]\n"
    "v_pk_mul_f32 v[10:11], v[10:11], v[2:3]\n"
    "v_pk_mul_f32 v[12:13], v[12:13], v[4:5]\n"
    "v_pk_mul_f32 v[14:15], v[14:15], v[6:7]\n"
    "v_exp_f32_e64 v8, v8\n"
    "v_exp_f32_e64 v9, v9\n"
    "v_exp_f32_e64 v10, v10\n"
    "v_exp_f32_e64 v11, v11\n"
    "v_exp_f32_e64 v12, v12\n"
    "v_exp_f32_e64 v13, v13\n"
    "v_exp_f32_e64 v14, v14\n"
    "v_exp_f32_e64 v15, v15\n"
    "v_pk_add_f32 v[16:17], v[8:9], 1.0 op_sel_hi:[1,0]\n"
    "v_pk_add_f32 v[18:19], v[10:11], 1.0 op_sel_hi:[1,0]\n"
    "v_pk_add_f32 v[20:21], v[12:13], 1.0 op_sel_hi:[1,0]\n"
    "v_pk_add_f32 v[22:23], v[14:15], 1.0 op_sel_hi:[1,0]\n"
    "v_rcp_f32_e64 v16, v16\n"
    "v_rcp_f32_e64 v17, v17\n"
    "v_rcp_f32_e64 v18, v18\n"
    "v_rcp_f32_e64 v19, v19\n"
    "v_rcp_f32_e64 v20, v20\n"
    "v_rcp_f32_e64 v21, v21\n"
    "v_rcp_f32_e64 v22, v22\n"
    "v_rcp_f32_e64 v23, v23\n"
    "v_pk_mul_f32 v[16:17], v[16:17], v[0:1]\n"
    "v_pk_mul_f32 v[18:19], v[18:19], v[2:3]\n"
    "v_pk_mul_f32 v[20:21], v[20:21], v[4:5]\n"
    "v_pk_mul_f32 v[22:23], v[22:23], v[6:7]\n"
    "v_pk_mul_f32 v[8:9], v[8:9], v[16:17]\n"
    "v_pk_mul_f32 v[10:11], v[10:11], v[18:19]\n"
    "v_pk_mul_f32 v[12:13], v[12:13], v[20:21]\n"
    "v_pk_mul_f32 v[14:15], v[14:15], v[22:23]\n"
    "v_cvt_pk_bf16_f32 v192, v16, v17\n"
    "v_cvt_pk_bf16_f32 v193, v18, v19\n"
    "v_cvt_pk_bf16_f32 v194, v20, v21\n"
    "v_cvt_pk_bf16_f32 v195, v22, v23\n"
    "v_cvt_pk_bf16_f32 v24, -v8, -v9\n"
    "v_cvt_pk_bf16_f32 v25, -v10, -v11\n"
    "v_cvt_pk_bf16_f32 v26, -v12, -v13\n"
    "v_cvt_pk_bf16_f32 v27, -v14, -v15\n"
    "ds_write_b128 v234, v[24:27] offset:16\n"
    "v_add_u32_e32 v234, 65536, v234\n"
    "buffer_load_dword v229, v212, s[92:95], 0 offen offset:0\n"
    "s_nop 0\n"
    "s_waitcnt vmcnt(13)\n"
    "v_lshlrev_b32_sdwa v216, 2, v168 dst_sel:DWORD dst_unused:UNUSED_PAD src0_sel:DWORD src1_sel:WORD_0\n"
    "v_lshlrev_b32_sdwa v217, 2, v168 dst_sel:DWORD dst_unused:UNUSED_PAD src0_sel:DWORD src1_sel:WORD_1\n"
    "v_lshlrev_b32_sdwa v218, 2, v169 dst_sel:DWORD dst_unused:UNUSED_PAD src0_sel:DWORD src1_sel:WORD_0\n"
    "v_lshlrev_b32_sdwa v219, 2, v169 dst_sel:DWORD dst_unused:UNUSED_PAD src0_sel:DWORD src1_sel:WORD_1\n"
    "v_lshlrev_b32_sdwa v220, 2, v170 dst_sel:DWORD dst_unused:UNUSED_PAD src0_sel:DWORD src1_sel:WORD_0\n"
    "v_lshlrev_b32_sdwa v221, 2, v170 dst_sel:DWORD dst_unused:UNUSED_PAD src0_sel:DWORD src1_sel:WORD_1\n"
    "v_lshlrev_b32_sdwa v222, 2, v171 dst_sel:DWORD dst_unused:UNUSED_PAD src0_sel:DWORD src1_sel:WORD_0\n"
    "v_lshlrev_b32_sdwa v223, 2, v171 dst_sel:DWORD dst_unused:UNUSED_PAD src0_sel:DWORD src1_sel:WORD_1\n"
    "ds_write_b128 v222, v[88:91]\n"
    "buffer_load_dword v230, v212, s[92:95], 0 offen offset:192\n"
    "s_nop 0\n"
    "s_waitcnt vmcnt(13)\n"
    "v_lshlrev_b32_sdwa v207, 2, v172 dst_sel:DWORD dst_unused:UNUSED_PAD src0_sel:DWORD src1_sel:WORD_0\n"
    "v_lshlrev_b32_sdwa v242, 2, v173 dst_sel:DWORD dst_unused:UNUSED_PAD src0_sel:DWORD src1_sel:WORD_0\n"
    "v_lshlrev_b32_sdwa v243, 2, v173 dst_sel:DWORD dst_unused:UNUSED_PAD src0_sel:DWORD src1_sel:WORD_1\n"
    "v_lshlrev_b32_sdwa v244, 2, v174 dst_sel:DWORD dst_unused:UNUSED_PAD src0_sel:DWORD src1_sel:WORD_0\n"
    "v_lshlrev_b32_sdwa v248, 2, v174 dst_sel:DWORD dst_unused:UNUSED_PAD src0_sel:DWORD src1_sel:WORD_1\n"
    "v_lshlrev_b32_sdwa v249, 2, v175 dst_sel:DWORD dst_unused:UNUSED_PAD src0_sel:DWORD src1_sel:WORD_0\n"
    "v_lshlrev_b32_sdwa v250, 2, v175 dst_sel:DWORD dst_unused:UNUSED_PAD src0_sel:DWORD src1_sel:WORD_1\n"
    "v_bfe_i32 v201, v172, 16, 1\n"
    "v_and_b32_e32 v201, 0x35003500, v201\n"
    "v_bfe_i32 v203, v172, 18, 1\n"
    "v_and_b32_e32 v203, 0x35003500, v203\n"
    "v_bfe_i32 v205, v172, 20, 1\n"
    "v_and_b32_e32 v205, 0x35003500, v205\n"
    "v_bfe_i32 v92, v172, 26, 1\n"
    "v_xor_b32_e32 v92, 0xffff, v92\n"
    "v_and_b32_e32 v92, 0x3f803f80, v92\n"
    "v_bfe_i32 v236, v172, 22, 1\n"
    "v_and_b32_e64 v236, v92, v236\n"
    "v_bfe_i32 v237, v172, 23, 1\n"
    "v_and_b32_e32 v237, v92, v237\n"
    "buffer_load_dword v231, v212, s[92:95], 0 offen offset:384\n"
    "s_waitcnt vmcnt(13)\n"
    "v_lshlrev_b32_sdwa v251, 2, v176 dst_sel:DWORD dst_unused:UNUSED_PAD src0_sel:DWORD src1_sel:WORD_0\n"
    "v_lshlrev_b32_sdwa v252, 2, v176 dst_sel:DWORD dst_unused:UNUSED_PAD src0_sel:DWORD src1_sel:WORD_1\n"
    "v_lshlrev_b32_sdwa v253, 2, v177 dst_sel:DWORD dst_unused:UNUSED_PAD src0_sel:DWORD src1_sel:WORD_0\n"
    "v_lshlrev_b32_sdwa v254, 2, v177 dst_sel:DWORD dst_unused:UNUSED_PAD src0_sel:DWORD src1_sel:WORD_1\n"
    "v_lshlrev_b32_sdwa v241, 0, v178 dst_sel:DWORD dst_unused:UNUSED_PAD src0_sel:DWORD src1_sel:WORD_0\n"
    "v_lshlrev_b32_sdwa v245, 4, v178 dst_sel:DWORD dst_unused:UNUSED_PAD src0_sel:DWORD src1_sel:WORD_1\n"
    "v_lshlrev_b32_sdwa v255, 4, v179 dst_sel:DWORD dst_unused:UNUSED_PAD src0_sel:DWORD src1_sel:WORD_0\n"
    "v_lshlrev_b32_sdwa v247, 4, v179 dst_sel:DWORD dst_unused:UNUSED_PAD src0_sel:DWORD src1_sel:WORD_1\n"
    "v_bfe_i32 v238, v172, 24, 1\n"
    "v_and_b32_e64 v238, v92, v238\n"
    "v_bfe_i32 v239, v172, 25, 1\n"
    "v_and_b32_e64 v239, v92, v239\n"
    "v_bfe_u32 v93, v172, 28, 2\n"
    "v_mul_u32_u24_e32 v93, 393216, v93\n"
    "v_mov_b32_e64 v222, v254\n"
    "v_mov_b32_e32 v254, 0\n"
    "s_nop 1\n"
    "v_mfma_f32_32x32x1_2b_f32 v[0:31], v254, v254, 0\n"
    "global_load_dword v233, v214, s[70:71]\n"
    "s_mov_b32 s48, 0x44ea44ea\n"
    "s_mov_b32 s49, 0xb77fb77f\n"
    "s_mov_b32 s50, 0x89efde6e\n"
    "s_mov_b32 s46, s42\n"
    "s_mov_b32 s47, s43\n"
    "s_mul_i32 s54, s60, 592\n"
    "s_add_u32 s54, s54, -27008\n"
    "s_mul_i32 s55, s60, -1284\n"
    "s_add_u32 s55, s55, 54688\n"
    "s_mul_i32 s56, s60, -6160\n"
    "s_add_u32 s56, s56, -27008\n"
    "s_add_u32 s98, s99, 1375872\n"
    "s_and_b32 s36, s60, 1\n"
    "s_lshl_b32 s36, s36, 13\n"
    "s_add_u32 s36, s36, 327680\n"
    "s_mul_i32 s53, s60, 98304\n"
    "s_add_u32 s98, s53, s98\n"
    "s_xor_b32 s53, s60, 1\n"
    "s_mul_i32 s53, s53, 393216\n"
    "s_add_u32 s99, s99, s53\n"
    "s_add_u32 s53, s98, 393216\n"
    "s_sub_u32 s53, s53, s36\n"
    "s_and_b32 s37, s61, 1\n"
    "s_lshl_b32 s37, s37, 2\n"
    "s_add_u32 s37, s37, s60\n"
    "s_lshl_b32 s37, s37, 12\n"
    "s_cmp_eq_u32 s61, 0\n"
    "s_cselect_b32 s67, 2, 0\n"
    "s_cmp_eq_u32 s61, 31\n"
    "s_cselect_b32 s67, 1, s67\n"
    "s_lshl_b32 s67, s67, 30\n"
    "s_mov_b64 s[68:69], s[44:45]\n"
    "s_mov_b32 s70, 0\n"
    "s_mov_b32 s71, s47\n"
    "s_mov_b64 s[72:73], s[40:41]\n"
    "s_mov_b64 s[74:75], s[42:43]\n"
    "s_mov_b32 s57, 0\n"
    "s_mov_b32 s58, 196608\n"
    "s_mov_b32 s60, 0\n"
    "s_mov_b32 s61, 393216\n"
    "s_mov_b32 s62, 786432\n"
    "s_mov_b32 s63, 1179648\n"
    "s_mov_b32 s52, 1572864\n"
    "s_mov_b32 s88, 98304\n"
    "s_mov_b32 s89, 294912\n"
    "s_mov_b32 s39, 0\n"
    "s_getpc_b64 s[76:77]\n"
    ".Lreplk_pc1:\n"
    "s_add_u32 s76, s76, .Lreplk_last-.Lreplk_pc1\n"
    "s_addc_u32 s77, s77, 0\n"
    "s_sub_u32 s76, s76, s36\n"
    "s_subb_u32 s77, s77, 0\n"
    "s_add_u32 s78, s36, .Lreplk_end-.Lreplk_last\n"
    "s_mov_b32 s79, s37\n"
    "s_add_u32 m0, s38, 0\n"
    "s_mov_b32 s64, 0\n"
    "buffer_load_dwordx4 v210, s[84:87], s64 offen offset:0 lds\n"
    "s_waitcnt vmcnt(13)\n"
    "ds_write2_b32 v216, a224, a225 offset0:0 offset1:20\n"
    "v_add3_u32 v247, v247, v93, s98\n"
    "v_add_u32_e32 v246, s98, v255\n"
    "v_add_u32_e64 v245, s99, v245\n"
    "v_add_u32_e64 v255, s99, v255\n"
    "ds_write2_b32 v216, a226, a227 offset0:40 offset1:60\n"
    "v_and_b32_e64 v93, s67, v172\n"
    "v_and_b32_e32 v94, 0x80000000, v93\n"
    "s_waitcnt vmcnt(12)\n"
    "ds_write2_b32 v216, a228, a229 offset0:80 offset1:100\n"
    "v_and_b32_e32 v93, 0x40000000, v93\n"
    "v_or_b32_e64 v246, v246, v94\n"
    "v_or_b32_e64 v247, v247, v93\n"
    "ds_write2_b32 v216, a230, a231 offset0:120 offset1:140\n"
    "v_add_u32_e32 v216, 1360, v216\n"
    "buffer_load_dwordx4 v210, s[84:87], s64 offen offset:1024 lds\n"
    "s_waitcnt vmcnt(12)\n"
    "ds_write2_b32 v216, a232, a233 offset0:0 offset1:20\n"
    "ds_write2_b32 v216, a234, a235 offset0:40 offset1:60\n"
    "buffer_load_dwordx4 v210, s[84:87], s64 offen offset:2048 lds\n"
    "s_waitcnt vmcnt(12)\n"
    "ds_write2_b32 v216, a236, a237 offset0:80 offset1:100\n"
    "s_add_u32 m0, s38, 3072\n"
    "s_mov_b32 s64, 3072\n"
    "buffer_load_dwordx4 v210, s[84:87], s64 offen offset:0 lds\n"
    "buffer_load_dwordx4 v210, s[84:87], s64 offen offset:1024 lds\n"
    "ds_write2_b32 v216, a238, a239 offset0:120 offset1:140\n"
    "buffer_load_dwordx4 v210, s[84:87], s64 offen offset:2048 lds\n"
    "s_add_u32 m0, s38, 6144\n"
    "s_mov_b32 s64, 6144\n"
    "buffer_load_dwordx4 v210, s[84:87], s64 offen offset:0 lds\n"
    "buffer_load_dwordx4 v210, s[84:87], s64 offen offset:1024 lds\n"
    "v_add_u32_e32 v216, 1360, v216\n"
    "s_waitcnt vmcnt(16)\n"
    "v_lshlrev_b32_e32 v40, 16, v224\n"
    "buffer_load_dwordx4 v210, s[84:87], s64 offen offset:2048 lds\n"
    "s_mov_b32 s65, 589824\n"
    "buffer_load_dwordx4 a[240:243], v208, s[40:43], s65 offen\n"
    "s_waitcnt vmcnt(17)\n"
    "v_lshlrev_b32_e32 v41, 16, v225\n"
    "s_mov_b32 s65, 688128\n"
    "buffer_load_dwordx4 a[244:247], v208, s[40:43], s65 offen\n"
    "s_mov_b32 s59, 49152\n"
    "buffer_load_dwordx4 v[96:99], v209, s[80:83], s59 offen offset:0\n"
    "s_waitcnt vmcnt(18)\n"
    "v_lshlrev_b32_e32 v42, 16, v226\n"
    "s_waitcnt vmcnt(17)\n"
    "v_lshlrev_b32_e32 v43, 16, v227\n"
    "s_waitcnt vmcnt(16)\n"
    "v_lshlrev_b32_e32 v44, 16, v228\n"
    "s_waitcnt vmcnt(15)\n"
    "v_lshlrev_b32_e32 v45, 16, v229\n"
    "s_waitcnt vmcnt(14)\n"
    "v_lshlrev_b32_e32 v46, 16, v230\n"
    "s_waitcnt vmcnt(13)\n"
    "v_lshlrev_b32_e32 v47, 16, v231\n"
    "v_add_f32_e64 v41, v41, v45\n"
    "v_add_f32_e64 v42, v42, v46\n"
    "v_add_f32_e64 v43, v43, v47\n"
    "v_cvt_pk_bf16_f32 v56, v40, v41\n"
    "v_cvt_pk_bf16_f32 v57, v42, v43\n"
    "v_cvt_pk_bf16_f32 v58, v44, 0\n"
    "v_cvt_pk_bf16_f32 v64, 0, v40\n"
    "buffer_load_dwordx4 v[100:103], v209, s[80:83], s59 offen offset:256\n"
    "ds_write_b128 v221, v[56:59] offset:0\n"
    "v_cvt_pk_bf16_f32 v65, v41, v42\n"
    "v_cvt_pk_bf16_f32 v66, v43, v44\n"
    "v_cvt_pk_bf16_f32 v61, v40, v41\n"
    "v_cvt_pk_bf16_f32 v62, v42, v43\n"
    "v_cvt_pk_bf16_f32 v63, v44, 0\n"
    "v_cvt_pk_bf16_f32 v69, 0, v40\n"
    "v_cvt_pk_bf16_f32 v70, v41, v42\n"
    "v_cvt_pk_bf16_f32 v71, v43, v44\n"
    "ds_write_b128 v221, v[64:67] offset:16\n"
    "buffer_load_dwordx4 v[104:107], v209, s[80:83], s59 offen offset:512\n"
    "v_and_b32_e32 v48, 0xffff0000, v224\n"
    "v_and_b32_e32 v49, 0xffff0000, v225\n"
    "v_and_b32_e32 v50, 0xffff0000, v226\n"
    "v_and_b32_e32 v51, 0xffff0000, v227\n"
    "v_and_b32_e32 v52, 0xffff0000, v228\n"
    "ds_write_b128 v221, v[60:63] offset:32\n"
    "v_and_b32_e32 v53, 0xffff0000, v229\n"
    "v_and_b32_e32 v54, 0xffff0000, v230\n"
    "v_and_b32_e32 v55, 0xffff0000, v231\n"
    "v_add_f32_e64 v49, v49, v53\n"
    "v_add_f32_e64 v50, v50, v54\n"
    "v_add_f32_e64 v51, v51, v55\n"
    "ds_write_b128 v221, v[68:71] offset:48\n"
    "s_mov_b32 s65, 983040\n"
    "buffer_load_dwordx4 a[248:251], v208, s[40:43], s65 offen\n"
    "v_cvt_pk_bf16_f32 v72, v48, v49\n"
    "v_cvt_pk_bf16_f32 v73, v50, v51\n"
    "v_cvt_pk_bf16_f32 v74, v52, 0\n"
    "v_cvt_pk_bf16_f32 v80, 0, v48\n"
    "v_cvt_pk_bf16_f32 v81, v49, v50\n"
    "v_cvt_pk_bf16_f32 v82, v51, v52\n"
    "v_cvt_pk_bf16_f32 v77, v48, v49\n"
    "v_cvt_pk_bf16_f32 v78, v50, v51\n"
    "v_cvt_pk_bf16_f32 v79, v52, 0\n"
    "v_cvt_pk_bf16_f32 v85, 0, v48\n"
    "v_cvt_pk_bf16_f32 v86, v49, v50\n"
    "v_cvt_pk_bf16_f32 v87, v51, v52\n"
    "ds_write_b128 v221, v[72:75] offset:336\n"
    "s_mov_b32 s65, 1081344\n"
    "buffer_load_dwordx4 a[252:255], v208, s[40:43], s65 offen\n"
    "v_xor_b32_e32 v224, 0, v219\n"
    "v_xor_b32_e32 v225, 32, v219\n"
    "v_xor_b32_e32 v226, 64, v219\n"
    "v_xor_b32_e32 v227, 96, v219\n"
    "ds_write_b128 v221, v[80:83] offset:352\n"
    "v_xor_b32_e32 v228, 0, v223\n"
    "v_xor_b32_e32 v229, 32, v223\n"
    "s_mov_b32 s59, 52224\n"
    "buffer_load_dwordx4 v[108:111], v209, s[80:83], s59 offen offset:0\n"
    "v_xor_b32_e32 v230, 64, v223\n"
    "v_xor_b32_e32 v231, 96, v223\n"
    "ds_write_b128 v221, v[76:79] offset:368\n"
    "v_lshl_add_u32 v196, s51, 8, v225\n"
    "v_lshl_add_u32 v197, s51, 8, v227\n"
    "v_mfma_f32_32x32x1_2b_f32 v[32:63], v254, v254, 0\n"
    "ds_write_b128 v221, v[84:87] offset:384\n"
    "buffer_load_dwordx4 v[112:115], v209, s[80:83], s59 offen offset:256\n"
    "v_mfma_f32_32x32x1_2b_f32 v[64:95], v254, v254, 0\n"
    "v_add_u32_e64 v254, s54, v242\n"
    "buffer_load_dwordx4 v[116:119], v209, s[80:83], s59 offen offset:512\n"
    "s_mov_b32 s65, 1376256\n"
    "buffer_load_dwordx4 a[176:179], v208, s[40:43], s65 offen\n"
    "s_mov_b32 s65, 1474560\n"
    "buffer_load_dwordx4 a[180:183], v208, s[40:43], s65 offen\n"
    "s_waitcnt vmcnt(12)\n"
    "s_barrier\n"
    "ds_read_b64_tr_b16 a[0:1], v220 offset:0\n"
    "ds_read_b64_tr_b16 a[2:3], v220 offset:3072\n"
    "ds_read_b64_tr_b16 a[4:5], v220 offset:8\n"
    "ds_read_b64_tr_b16 a[6:7], v220 offset:3080\n"
    "ds_read_b64_tr_b16 a[8:9], v220 offset:64\n"
    "ds_read_b64_tr_b16 a[10:11], v220 offset:3136\n"
    "ds_read_b64_tr_b16 a[12:13], v220 offset:72\n"
    "ds_read_b64_tr_b16 a[14:15], v220 offset:3144\n"
    "ds_read_b64_tr_b16 v[168:169], v218 offset:128\n"
    "ds_read_b64_tr_b16 v[170:171], v218 offset:3200\n"
    "ds_read_b64_tr_b16 a[16:17], v220 offset:6144\n"
    "ds_read_b64_tr_b16 a[18:19], v220 offset:9216\n"
    "ds_read_b64_tr_b16 a[20:21], v220 offset:6152\n"
    "ds_read_b64_tr_b16 a[22:23], v220 offset:9224\n"
    "ds_read_b64_tr_b16 a[24:25], v220 offset:6208\n"
    "ds_read_b64_tr_b16 a[26:27], v220 offset:9280\n"
    "ds_read_b64_tr_b16 a[28:29], v220 offset:6216\n"
    "ds_read_b64_tr_b16 a[30:31], v220 offset:9288\n"
    "ds_read_b64_tr_b16 v[172:173], v218 offset:6272\n"
    "s_nop 0\n"
    "s_mov_b32 s59, 0\n"
    "buffer_load_dwordx4 v[120:123], v209, s[80:83], s59 offen offset:0\n"
    "ds_read_b64_tr_b16 v[174:175], v218 offset:9344\n"
    "ds_read_b64_tr_b16 a[32:33], v220 offset:12288\n"
    "ds_read_b64_tr_b16 a[34:35], v220 offset:15360\n"
    "ds_read_b64_tr_b16 a[36:37], v220 offset:12296\n"
    "ds_read_b64_tr_b16 a[38:39], v220 offset:15368\n"
    "ds_read_b64_tr_b16 a[40:41], v220 offset:12352\n"
    "ds_read_b64_tr_b16 a[42:43], v220 offset:15424\n"
    "ds_read_b64_tr_b16 a[44:45], v220 offset:12360\n"
    "ds_read_b64_tr_b16 a[46:47], v220 offset:15432\n"
    "ds_read_b64_tr_b16 v[176:177], v218 offset:12416\n"
    "ds_read_b64_tr_b16 v[178:179], v218 offset:15488\n"
    "s_waitcnt vmcnt(12)\n"
    "ds_write2_b32 v216, a240, a241 offset0:0 offset1:20\n"
    "ds_write2_b32 v216, a242, a243 offset0:40 offset1:60\n"
    "buffer_load_dwordx4 v[124:127], v209, s[80:83], s59 offen offset:256\n"
    "s_waitcnt vmcnt(12)\n"
    "ds_write2_b32 v216, a244, a245 offset0:80 offset1:100\n"
    "s_waitcnt lgkmcnt(2)\n"
    "s_barrier\n"
    "ds_write2_b32 v216, a246, a247 offset0:120 offset1:140\n"
    "buffer_load_dwordx4 v[128:131], v209, s[80:83], s59 offen offset:512\n"
    "v_add_u32_e32 v216, 1360, v216\n"
    "s_waitcnt vmcnt(12)\n"
    "v_or3_b32 v200, v200, v96, v97\n"
    "v_or3_b32 v200, v200, v98, v99\n"
    "v_and_b32_e32 v232, 0x7fff7fff, v96\n"
    "v_add_u32_e32 v96, v201, v96\n"
    "v_add_u32_e64 v98, v201, v98\n"
    "ds_write_b128 v217, v[96:99] offset:24576\n"
    "s_mov_b32 s65, 0\n"
    "buffer_load_dwordx4 a[184:187], v208, s[40:43], s65 offen\n"
    "s_waitcnt vmcnt(12)\n"
    "v_or3_b32 v200, v200, v100, v101\n"
    "v_or3_b32 v200, v200, v102, v103\n"
    "v_add_u32_e64 v100, v203, v100\n"
    "v_add_u32_e64 v102, v203, v102\n"
    "ds_write_b128 v217, v[100:103] offset:24832\n"
    "s_mov_b32 s65, 98304\n"
    "buffer_load_dwordx4 a[188:191], v208, s[40:43], s65 offen\n"
    "s_waitcnt vmcnt(12)\n"
    "v_or3_b32 v200, v200, v104, v105\n"
    "v_or3_b32 v200, v200, v106, v107\n"
    "v_add_u32_e32 v104, v205, v104\n"
    "v_add_u32_e64 v106, v205, v106\n"
    "ds_write_b128 v217, v[104:107] offset:25088\n"
    "s_waitcnt lgkmcnt(0)\n"
    "s_barrier\n"
    "s_mov_b32 s59, 3072\n"
    "buffer_load_dwordx4 v[132:135], v209, s[80:83], s59 offen offset:0\n"
    "ds_read_b64_tr_b16 a[48:49], v224 offset:24576\n"
    "ds_read_b64_tr_b16 v[180:181], v196 offset:24576\n"
    "ds_read_b64_tr_b16 a[52:53], v226 offset:24576\n"
    "ds_read_b64_tr_b16 v[184:185], v197 offset:24576\n"
    "ds_read_b64_tr_b16 a[56:57], v228 offset:24576\n"
    "ds_read_b64_tr_b16 a[60:61], v229 offset:24576\n"
    "ds_read_b64_tr_b16 a[64:65], v230 offset:24576\n"
    "ds_read_b64_tr_b16 a[68:69], v231 offset:24576\n"
    "ds_read_b64_tr_b16 a[72:73], v224 offset:24832\n"
    "ds_read_b64_tr_b16 a[76:77], v226 offset:24832\n"
    "s_waitcnt vmcnt(12)\n"
    "ds_write2_b32 v216, a248, a249 offset0:0 offset1:20\n"
    "ds_write2_b32 v216, a250, a251 offset0:40 offset1:60\n"
    "buffer_load_dwordx4 v[136:139], v209, s[80:83], s59 offen offset:256\n"
    "s_waitcnt vmcnt(12)\n"
    "ds_write2_b32 v216, a252, a253 offset0:80 offset1:100\n"
    "ds_write2_b32 v216, a254, a255 offset0:120 offset1:140\n"
    "v_add_u32_e32 v216, 1360, v216\n"
    "buffer_load_dwordx4 v[140:143], v209, s[80:83], s59 offen offset:512\n"
    "s_waitcnt vmcnt(12)\n"
    "v_or3_b32 v200, v200, v108, v109\n"
    "v_or3_b32 v200, v200, v110, v111\n"
    "v_add_u32_e32 v108, v201, v108\n"
    "v_add_u32_e64 v110, v201, v110\n"
    "ds_write_b128 v217, v[108:111] offset:0\n"
    "s_mov_b32 s65, 393216\n"
    "buffer_load_dwordx4 a[192:195], v208, s[40:43], s65 offen\n"
    "s_waitcnt vmcnt(12)\n"
    "v_or3_b32 v200, v200, v112, v113\n"
    "v_or3_b32 v200, v200, v114, v115\n"
    "v_add_u32_e32 v112, v203, v112\n"
    "v_add_u32_e64 v114, v203, v114\n"
    "ds_write_b128 v217, v[112:115] offset:256\n"
    "v_mov_b32_e64 v112, v188\n"
    "v_mov_b32_e64 v113, v189\n"
    "v_mov_b32_e64 v114, v190\n"
    "v_mov_b32_e32 v115, v191\n"
    "s_mov_b32 s65, 491520\n"
    "buffer_load_dwordx4 a[196:199], v208, s[40:43], s65 offen\n"
    "s_waitcnt vmcnt(12)\n"
    "v_or3_b32 v200, v200, v116, v117\n"
    "v_or3_b32 v200, v200, v118, v119\n"
    "v_add_u32_e64 v116, v205, v116\n"
    "v_add_u32_e64 v118, v205, v118\n"
    "ds_write_b128 v217, v[116:119] offset:512\n"
    "v_mov_b32_e64 v116, v192\n"
    "v_mov_b32_e64 v117, v193\n"
    "v_mov_b32_e64 v118, v194\n"
    "v_mov_b32_e64 v119, v195\n"
    "s_waitcnt lgkmcnt(0)\n"
    "s_barrier\n"
    "s_mov_b32 s59, 24576\n"
    "buffer_load_dwordx4 v[144:147], v209, s[80:83], s59 offen offset:0\n"
    "ds_read_b64_tr_b16 a[50:51], v224 offset:0\n"
    "ds_read_b64_tr_b16 v[182:183], v196 offset:0\n"
    "ds_read_b64_tr_b16 a[54:55], v226 offset:0\n"
    "ds_read_b64_tr_b16 v[186:187], v197 offset:0\n"
    "ds_read_b64_tr_b16 a[58:59], v228 offset:0\n"
    "ds_read_b64_tr_b16 a[62:63], v229 offset:0\n"
    "ds_read_b64_tr_b16 a[66:67], v230 offset:0\n"
    "ds_read_b64_tr_b16 a[70:71], v231 offset:0\n"
    "ds_read_b64_tr_b16 a[74:75], v224 offset:256\n"
    "ds_read_b64_tr_b16 a[78:79], v226 offset:256\n"
    "s_waitcnt vmcnt(12)\n"
    "ds_write2_b32 v216, a176, a177 offset0:0 offset1:20\n"
    "ds_write2_b32 v216, a178, a179 offset0:40 offset1:60\n"
    "s_waitcnt lgkmcnt(8)\n"
    "ds_write_b128 v207, v[184:187] offset:36864\n"
    "buffer_load_dwordx4 v[148:151], v209, s[80:83], s59 offen offset:256\n"
    "s_waitcnt vmcnt(12)\n"
    "ds_write2_b32 v216, a180, a181 offset0:80 offset1:100\n"
    "ds_write2_b32 v216, a182, a183 offset0:120 offset1:140\n"
    "v_add_u32_e32 v216, -4800, v216\n"
    "buffer_load_dwordx4 v[152:155], v209, s[80:83], s59 offen offset:512\n"
    "s_waitcnt vmcnt(12)\n"
    "v_or3_b32 v200, v200, v120, v121\n"
    "v_or3_b32 v200, v200, v122, v123\n"
    "v_add_u32_e64 v120, v201, v120\n"
    "v_add_u32_e64 v122, v201, v122\n"
    "ds_write_b128 v217, v[120:123] offset:12288\n"
    "s_waitcnt lgkmcnt(1)\n"
    "s_barrier\n"
    "s_mov_b32 s65, 786432\n"
    "buffer_load_dwordx4 a[200:203], v208, s[40:43], s65 offen\n"
    "s_waitcnt vmcnt(12)\n"
    "v_or3_b32 v200, v200, v124, v125\n"
    "v_or3_b32 v200, v200, v126, v127\n"
    "v_add_u32_e32 v124, v203, v124\n"
    "v_add_u32_e64 v126, v203, v126\n"
    "ds_write_b128 v217, v[124:127] offset:12544\n"
    "s_mov_b32 s65, 884736\n"
    "buffer_load_dwordx4 a[204:207], v208, s[40:43], s65 offen\n"
    "s_waitcnt vmcnt(12)\n"
    "v_or3_b32 v200, v200, v128, v129\n"
    "v_or3_b32 v200, v200, v130, v131\n"
    "v_add_u32_e32 v128, v205, v128\n"
    "v_add_u32_e64 v130, v205, v130\n"
    "ds_write_b128 v217, v[128:131] offset:12800\n"
    "ds_read_b128 a[224:227], v250 offset:0\n"
    "ds_read_b128 a[228:231], v250 offset:320\n"
    "ds_read_b128 a[232:235], v251 offset:0\n"
    "ds_read_b128 a[236:239], v251 offset:336\n"
    "ds_read_b128 a[240:243], v250 offset:80\n"
    "ds_read_b128 a[244:247], v250 offset:400\n"
    "ds_read_b128 a[248:251], v251 offset:672\n"
    "ds_read_b128 a[252:255], v251 offset:1008\n"
    "ds_read_b128 v[104:107], v252 offset:1344\n"
    "ds_read_b128 v[108:111], v252 offset:1680\n"
    "ds_read_b128 v[128:131], v252 offset:2016\n"
    "s_waitcnt lgkmcnt(11)\n"
    "s_barrier\n"
    "s_mov_b32 s59, 27648\n"
    "buffer_load_dwordx4 v[156:159], v209, s[80:83], s59 offen offset:0\n"
    "ds_read_b64_tr_b16 a[80:81], v224 offset:12288\n"
    "ds_read_b64_tr_b16 a[84:85], v225 offset:12288\n"
    "ds_read_b64_tr_b16 a[88:89], v226 offset:12288\n"
    "ds_read_b64_tr_b16 a[92:93], v227 offset:12288\n"
    "s_waitcnt lgkmcnt(12)\n"
    "v_smfmac_f32_16x16x64_bf16 v[0:3], a[232:235], a[224:231], v240\n"
    "s_waitcnt lgkmcnt(11)\n"
    "ds_read_b64_tr_b16 a[96:97], v228 offset:12288\n"
    "ds_read_b64_tr_b16 a[100:101], v229 offset:12288\n"
    "v_smfmac_f32_16x16x64_bf16 v[4:7], a[236:239], a[224:231], v240 abid:1\n"
    "ds_read_b64_tr_b16 a[104:105], v230 offset:12288\n"
    "ds_read_b64_tr_b16 a[108:109], v231 offset:12288\n"
    "ds_read_b64_tr_b16 a[112:113], v224 offset:12544\n"
    "ds_read_b64_tr_b16 a[116:117], v225 offset:12544\n"
    "ds_read_b64_tr_b16 a[120:121], v226 offset:12544\n"
    "s_waitcnt lgkmcnt(15)\n"
    "v_smfmac_f32_16x16x64_bf16 v[8:11], a[248:251], a[240:247], v240\n"
    "s_waitcnt lgkmcnt(14)\n"
    "ds_read_b64_tr_b16 a[124:125], v227 offset:12544\n"
    "s_waitcnt vmcnt(12)\n"
    "ds_write2_b32 v216, a184, a185 offset0:0 offset1:20\n"
    "v_smfmac_f32_16x16x64_bf16 v[12:15], a[252:255], a[240:247], v240 abid:1\n"
    "s_nop 0\n"
    "ds_write2_b32 v216, a186, a187 offset0:40 offset1:60\n"
    "ds_read_b128 a[224:227], v250 offset:160\n"
    "ds_read_b128 a[228:231], v250 offset:480\n"
    "ds_read_b128 a[232:235], v251 offset:1344\n"
    "ds_read_b128 a[236:239], v251 offset:1680\n"
    "buffer_load_dwordx4 v[160:163], v209, s[80:83], s59 offen offset:256\n"
    "s_waitcnt vmcnt(12)\n"
    "ds_write2_b32 v216, a188, a189 offset0:80 offset1:100\n"
    "ds_write2_b32 v216, a190, a191 offset0:120 offset1:140\n"
    "v_add_u32_e32 v216, 1360, v216\n"
    "ds_read_b128 a[240:243], v250 offset:240\n"
    "ds_read_b128 a[244:247], v250 offset:560\n"
    "buffer_load_dwordx4 v[164:167], v209, s[80:83], s59 offen offset:512\n"
    "s_waitcnt vmcnt(12)\n"
    "v_or3_b32 v200, v200, v132, v133\n"
    "v_or3_b32 v200, v200, v134, v135\n"
    "v_add_u32_e64 v132, v201, v132\n"
    "v_add_u32_e64 v134, v201, v134\n"
    "ds_write_b128 v217, v[132:135] offset:24576\n"
    "s_waitcnt lgkmcnt(6)\n"
    "v_smfmac_f32_16x16x64_bf16 v[16:19], a[232:235], a[224:231], v240\n"
    "s_waitcnt lgkmcnt(5)\n"
    "v_smfmac_f32_16x16x64_bf16 v[20:23], a[236:239], a[224:231], v240 abid:1\n"
    "s_mov_b32 s65, 1179648\n"
    "buffer_load_dwordx4 a[208:211], v208, s[40:43], s65 offen\n"
    "s_waitcnt vmcnt(12)\n"
    "v_or3_b32 v200, v200, v136, v137\n"
    "v_or3_b32 v200, v200, v138, v139\n"
    "v_add_u32_e32 v136, v203, v136\n"
    "v_add_u32_e64 v138, v203, v138\n"
    "ds_write_b128 v217, v[136:139] offset:24832\n"
    "ds_read_b128 a[248:251], v251 offset:2016\n"
    "ds_read_b128 a[252:255], v251 offset:2352\n"
    "s_mov_b32 s65, 1277952\n"
    "buffer_load_dwordx4 a[212:215], v208, s[40:43], s65 offen\n"
    "s_waitcnt vmcnt(12)\n"
    "v_or3_b32 v200, v200, v140, v141\n"
    "v_or3_b32 v200, v200, v142, v143\n"
    "v_add_u32_e32 v140, v205, v140\n"
    "v_add_u32_e64 v142, v205, v142\n"
    "ds_write_b128 v217, v[140:143] offset:25088\n"
    "ds_read_b128 a[224:227], v250 offset:27024\n"
    "ds_read_b128 a[228:231], v250 offset:27344\n"
    "ds_read_b128 a[232:235], v251 offset:10752\n"
    "ds_read_b128 a[236:239], v251 offset:11088\n"
    "ds_read_b128 v[132:135], v252 offset:2352\n"
    "s_waitcnt lgkmcnt(7)\n"
    "v_smfmac_f32_16x16x64_bf16 v[24:27], a[248:251], a[240:247], v240\n"
    "ds_read_b128 a[248:251], v251 offset:11424\n"
    "s_waitcnt lgkmcnt(7)\n"
    "v_smfmac_f32_16x16x64_bf16 v[28:31], a[252:255], a[240:247], v240 abid:1\n"
    "ds_read_b128 a[240:243], v250 offset:27104\n"
    "ds_read_b128 a[244:247], v250 offset:27424\n"
    "ds_read_b128 a[252:255], v251 offset:11760\n"
    "s_waitcnt lgkmcnt(9)\n"
    "s_barrier\n"
    "s_mov_b32 s65, 1572864\n"
    "buffer_load_dwordx4 a[216:219], v208, s[40:43], s65 offen\n"
    "ds_read_b64_tr_b16 a[82:83], v224 offset:24576\n"
    "ds_read_b64_tr_b16 a[86:87], v225 offset:24576\n"
    "ds_read_b64_tr_b16 a[90:91], v226 offset:24576\n"
    "s_waitcnt lgkmcnt(9)\n"
    "v_smfmac_f32_16x16x64_bf16 v[32:35], a[232:235], a[224:231], v240\n"
    "s_waitcnt lgkmcnt(8)\n"
    "ds_read_b64_tr_b16 a[94:95], v227 offset:24576\n"
    "ds_read_b64_tr_b16 a[98:99], v228 offset:24576\n"
    "v_smfmac_f32_16x16x64_bf16 v[36:39], a[236:239], a[224:231], v240 abid:1\n"
    "ds_read_b64_tr_b16 a[102:103], v229 offset:24576\n"
    "ds_read_b64_tr_b16 a[106:107], v230 offset:24576\n"
    "ds_read_b64_tr_b16 a[110:111], v231 offset:24576\n"
    "ds_read_b64_tr_b16 a[114:115], v224 offset:24832\n"
    "ds_read_b64_tr_b16 a[118:119], v225 offset:24832\n"
    "ds_read_b64_tr_b16 a[122:123], v226 offset:24832\n"
    "ds_read_b64_tr_b16 a[126:127], v227 offset:24832\n"
    "s_waitcnt lgkmcnt(13)\n"
    "v_smfmac_f32_16x16x64_bf16 v[40:43], a[248:251], a[240:247], v240\n"
    "s_waitcnt lgkmcnt(12)\n"
    "s_waitcnt vmcnt(12)\n"
    "ds_write2_b32 v216, a192, a193 offset0:0 offset1:20\n"
    "v_smfmac_f32_16x16x64_bf16 v[44:47], a[252:255], a[240:247], v240 abid:1\n"
    "s_nop 0\n"
    "ds_write2_b32 v216, a194, a195 offset0:40 offset1:60\n"
    "ds_read_b128 a[224:227], v250 offset:27184\n"
    "ds_read_b128 a[228:231], v250 offset:27504\n"
    "ds_read_b128 a[232:235], v251 offset:12096\n"
    "s_mov_b32 s65, 1671168\n"
    "buffer_load_dwordx4 a[220:223], v208, s[40:43], s65 offen\n"
    "s_waitcnt vmcnt(12)\n"
    "ds_write2_b32 v216, a196, a197 offset0:80 offset1:100\n"
    "ds_write2_b32 v216, a198, a199 offset0:120 offset1:140\n"
    "v_add_u32_e32 v216, 1360, v216\n"
    "ds_read_b128 a[236:239], v251 offset:12432\n"
    "ds_read_b128 a[240:243], v250 offset:27264\n"
    "ds_read_b128 a[244:247], v250 offset:27584\n"
    "s_waitcnt vmcnt(11)\n"
    "v_or3_b32 v200, v200, v144, v145\n"
    "v_or3_b32 v200, v200, v146, v147\n"
    "v_add_u32_e64 v144, v201, v144\n"
    "v_add_u32_e32 v146, v201, v146\n"
    "ds_write_b128 v217, v[144:147] offset:0\n"
    "s_waitcnt lgkmcnt(6)\n"
    "v_smfmac_f32_16x16x64_bf16 v[48:51], a[232:235], a[224:231], v240\n"
    "ds_read_b128 a[248:251], v251 offset:12768\n"
    "ds_read_b128 a[252:255], v251 offset:13104\n"
    "ds_read_b128 a[232:235], v251 offset:21504\n"
    "s_waitcnt vmcnt(10)\n"
    "v_or3_b32 v200, v200, v148, v149\n"
    "v_or3_b32 v200, v200, v150, v151\n"
    "v_add_u32_e32 v148, v203, v148\n"
    "v_add_u32_e32 v150, v203, v150\n"
    "ds_write_b128 v217, v[148:151] offset:256\n"
    "s_waitcnt lgkmcnt(6)\n"
    "v_smfmac_f32_16x16x64_bf16 v[52:55], a[236:239], a[224:231], v240 abid:1\n"
    "ds_read_b128 a[224:227], v250 offset:54048\n"
    "ds_read_b128 a[228:231], v250 offset:54368\n"
    "ds_read_b128 a[236:239], v251 offset:21840\n"
    "s_waitcnt vmcnt(9)\n"
    "v_or3_b32 v200, v200, v152, v153\n"
    "v_or3_b32 v200, v200, v154, v155\n"
    "v_add_u32_e32 v152, v205, v152\n"
    "v_add_u32_e32 v154, v205, v154\n"
    "ds_write_b128 v217, v[152:155] offset:512\n"
    "s_waitcnt lgkmcnt(6)\n"
    "v_smfmac_f32_16x16x64_bf16 v[56:59], a[248:251], a[240:247], v240\n"
    "v_smfmac_f32_16x16x64_bf16 v[60:63], a[252:255], a[240:247], v240 abid:1\n"
    "ds_read_b128 a[240:243], v250 offset:54128\n"
    "ds_read_b128 a[244:247], v250 offset:54448\n"
    "ds_read_b128 a[248:251], v251 offset:22176\n"
    "ds_read_b128 a[252:255], v251 offset:22512\n"
    "ds_read_b128 v[144:147], v252 offset:10752\n"
    "ds_read_b128 v[148:151], v252 offset:11088\n"
    "s_waitcnt lgkmcnt(8)\n"
    "v_smfmac_f32_16x16x64_bf16 v[64:67], a[232:235], a[224:231], v240\n"
    "ds_read_b128 a[232:235], v251 offset:22848\n"
    "s_waitcnt lgkmcnt(8)\n"
    "v_smfmac_f32_16x16x64_bf16 v[68:71], a[236:239], a[224:231], v240 abid:1\n"
    "ds_read_b128 a[224:227], v250 offset:54208\n"
    "ds_read_b128 a[228:231], v250 offset:54528\n"
    "s_waitcnt lgkmcnt(9)\n"
    "s_barrier\n"
    "ds_read_b64_tr_b16 a[128:129], v224 offset:0\n"
    "ds_read_b64_tr_b16 a[132:133], v225 offset:0\n"
    "ds_read_b64_tr_b16 a[136:137], v226 offset:0\n"
    "s_waitcnt lgkmcnt(9)\n"
    "v_smfmac_f32_16x16x64_bf16 v[72:75], a[248:251], a[240:247], v240\n"
    "s_waitcnt lgkmcnt(8)\n"
    "ds_read_b64_tr_b16 a[140:141], v227 offset:0\n"
    "ds_read_b64_tr_b16 a[144:145], v228 offset:0\n"
    "v_smfmac_f32_16x16x64_bf16 v[76:79], a[252:255], a[240:247], v240 abid:1\n"
    "ds_read_b64_tr_b16 a[148:149], v229 offset:0\n"
    "ds_read_b64_tr_b16 a[152:153], v230 offset:0\n"
    "ds_read_b64_tr_b16 a[156:157], v231 offset:0\n"
    "ds_read_b64_tr_b16 a[160:161], v224 offset:256\n"
    "ds_read_b64_tr_b16 a[164:165], v225 offset:256\n"
    "ds_read_b64_tr_b16 a[168:169], v226 offset:256\n"
    "ds_read_b64_tr_b16 a[172:173], v227 offset:256\n"
    "s_waitcnt vmcnt(8)\n"
    "ds_write2_b32 v216, a200, a201 offset0:0 offset1:20\n"
    "s_waitcnt lgkmcnt(13)\n"
    "v_smfmac_f32_16x16x64_bf16 v[80:83], a[232:235], a[224:231], v240\n"
    "ds_write2_b32 v216, a202, a203 offset0:40 offset1:60\n"
    "ds_read_b128 a[236:239], v251 offset:23184\n"
    "ds_read_b128 a[240:243], v250 offset:54288\n"
    "ds_read_b128 a[244:247], v250 offset:54608\n"
    "ds_read_b128 a[248:251], v251 offset:23520\n"
    "s_waitcnt vmcnt(7)\n"
    "ds_write2_b32 v216, a204, a205 offset0:80 offset1:100\n"
    "ds_write2_b32 v216, a206, a207 offset0:120 offset1:140\n"
    "v_add_u32_e32 v216, 1360, v216\n"
    "ds_read_b128 a[252:255], v251 offset:23856\n"
    "ds_read_b128 a[232:235], v252 offset:0\n"
    "s_waitcnt vmcnt(6)\n"
    "v_or3_b32 v200, v200, v156, v157\n"
    "v_or3_b32 v200, v200, v158, v159\n"
    "v_add_u32_e64 v156, v201, v156\n"
    "v_add_u32_e64 v158, v201, v158\n"
    "ds_write_b128 v217, v[156:159] offset:12288\n"
    "s_waitcnt lgkmcnt(7)\n"
    "v_smfmac_f32_16x16x64_bf16 v[84:87], a[236:239], a[224:231], v240 abid:1\n"
    "s_waitcnt lgkmcnt(5)\n"
    "v_smfmac_f32_16x16x64_bf16 v[88:91], a[248:251], a[240:247], v240\n"
    "ds_read_b128 a[236:239], v252 offset:336\n"
    "ds_read_b128 a[248:251], v252 offset:672\n"
    "s_waitcnt vmcnt(5)\n"
    "v_or3_b32 v200, v200, v160, v161\n"
    "v_or3_b32 v200, v200, v162, v163\n"
    "v_add_u32_e32 v160, v203, v160\n"
    "v_add_u32_e64 v162, v203, v162\n"
    "ds_write_b128 v217, v[160:163] offset:12544\n"
    "s_waitcnt lgkmcnt(4)\n"
    "v_smfmac_f32_16x16x64_bf16 v[92:95], a[252:255], a[240:247], v240 abid:1\n"
    "ds_read_b128 a[252:255], v252 offset:1008\n"
    "ds_read_b128 v[160:163], v252 offset:11424\n"
    "s_waitcnt vmcnt(4)\n"
    "v_or3_b32 v200, v200, v164, v165\n"
    "v_or3_b32 v200, v200, v166, v167\n"
    "v_add_u32_e64 v164, v205, v164\n"
    "v_add_u32_e64 v166, v205, v166\n"
    "ds_write_b128 v217, v[164:167] offset:12800\n"
    "v_and_b32_e32 v200, 0x40004000, v200\n"
    "v_cmp_le_u32_e32 vcc, 1, v200\n"
    "s_nop 1\n"
    "s_cmp_lg_u64 vcc, 0\n"
    "s_cselect_b32 vcc_lo, 1, 0\n"
    "s_or_b32 s66, s66, vcc_lo\n"
    "v_cmp_le_u32_e32 vcc, 981467136, v232\n"
    "s_nop 1\n"
    "s_cmp_lg_u64 vcc, 0\n"
    "s_cselect_b32 vcc_lo, 2, 0\n"
    "s_or_b32 s66, s66, vcc_lo\n"
    "v_mov_b32_e32 v200, s66\n"
    "v_mov_b32_e32 v232, 162992\n"
    "s_mov_b64 exec, 1\n"
    "ds_or_b32 v232, v200\n"
    "s_mov_b64 exec, -1\n"
    "ds_read_b128 v[164:167], v252 offset:11760\n"
    "s_waitcnt lgkmcnt(2)\n"
    "s_barrier\n"
    "ds_read_b64_tr_b16 a[130:131], v224 offset:12288\n"
    "ds_read_b64_tr_b16 a[134:135], v225 offset:12288\n"
    "ds_read_b64_tr_b16 a[138:139], v226 offset:12288\n"
    "ds_read_b64_tr_b16 a[142:143], v227 offset:12288\n"
    "ds_read_b64_tr_b16 a[146:147], v228 offset:12288\n"
    "ds_read_b64_tr_b16 a[150:151], v229 offset:12288\n"
    "ds_read_b64_tr_b16 a[154:155], v230 offset:12288\n"
    "ds_read_b64_tr_b16 a[158:159], v231 offset:12288\n"
    "ds_read_b64_tr_b16 a[162:163], v224 offset:12544\n"
    "ds_read_b64_tr_b16 a[166:167], v225 offset:12544\n"
    "ds_read_b64_tr_b16 a[170:171], v226 offset:12544\n"
    "ds_read_b64_tr_b16 a[174:175], v227 offset:12544\n"
    "s_nop 0\n"
    "s_waitcnt vmcnt(3)\n"
    "ds_write2_b32 v216, a208, a209 offset0:0 offset1:20\n"
    "ds_write2_b32 v216, a210, a211 offset0:40 offset1:60\n"
    "s_waitcnt vmcnt(2)\n"
    "ds_write2_b32 v216, a212, a213 offset0:80 offset1:100\n"
    "s_waitcnt lgkmcnt(2)\n"
    "s_barrier\n"
    "s_add_u32 m0, s38, 0\n"
    "s_mov_b32 s64, 18432\n"
    "buffer_load_dwordx4 v210, s[84:87], s64 offen offset:0 lds\n"
    "buffer_load_dwordx4 v210, s[84:87], s64 offen offset:1024 lds\n"
    "buffer_load_dwordx4 v210, s[84:87], s64 offen offset:2048 lds\n"
    "s_add_u32 m0, s38, 3072\n"
    "s_mov_b32 s64, 21504\n"
    "buffer_load_dwordx4 v210, s[84:87], s64 offen offset:0 lds\n"
    "buffer_load_dwordx4 v210, s[84:87], s64 offen offset:1024 lds\n"
    "buffer_load_dwordx4 v210, s[84:87], s64 offen offset:2048 lds\n"
    "s_add_u32 m0, s38, 6144\n"
    "s_mov_b32 s64, 24576\n"
    "buffer_load_dwordx4 v210, s[84:87], s64 offen offset:0 lds\n"
    "buffer_load_dwordx4 v210, s[84:87], s64 offen offset:1024 lds\n"
    "buffer_load_dwordx4 v210, s[84:87], s64 offen offset:2048 lds\n"
    "s_sub_u32 s80, s76, .Lreplk_last-.Lreplk_loop\n"
    "s_subb_u32 s81, s77, 0\n"
    "s_add_u32 s82, s36, .Lreplk_last-.Lreplk_loop\n"
    "s_and_b32 s85, s37, 0x3fff\n"
    "s_add_u32 s84, s85, 8192\n"
    "s_lshr_b32 s85, s85, 2\n"
    "v_lshlrev_b32_e64 v235, 3, v244\n"
    "s_mov_b32 exec_hi, 0\n"
    "buffer_load_dword v233, v235, s[40:43], s53 offen sc1\n"
    "s_mov_b32 exec_hi, -1\n"
    "s_add_u32 s53, s53, 393216\n"
    "ds_write2_b32 v216, a214, a215 offset0:120 offset1:140\n"
    "v_add_u32_e32 v216, 1360, v216\n"
    "s_mov_b32 s64, 0\n"
    "s_mov_b32 s59, 0\n"
    "ds_read_b32 v232, v232\n"
    "v_lshlrev_b32_e32 v235, 3, v244\n"
    "s_mov_b32 exec_hi, 0\n"
    "buffer_load_dword v233, v235, s[40:43], s53 offen sc1\n"
    "s_mov_b32 exec_hi, -1\n"
    "s_add_u32 s53, s53, 393216\n"
    "s_waitcnt vmcnt(12)\n"
    "ds_write2_b32 v216, a216, a217 offset0:0 offset1:20\n"
    "ds_write2_b32 v216, a218, a219 offset0:40 offset1:60\n"
    "v_lshlrev_b32_e32 v235, 3, v244\n"
    "s_mov_b32 exec_hi, 0\n"
    "buffer_load_dword v233, v235, s[40:43], s53 offen sc1\n"
    "s_mov_b32 exec_hi, -1\n"
    "s_add_u32 s53, s53, 393216\n"
    "s_waitcnt vmcnt(12)\n"
    "ds_write2_b32 v216, a220, a221 offset0:80 offset1:100\n"
    "ds_write2_b32 v216, a222, a223 offset0:120 offset1:140\n"
    "v_lshlrev_b32_e64 v235, 3, v244\n"
    "s_mov_b32 exec_hi, 0\n"
    "buffer_load_dword v233, v235, s[40:43], s53 offen sc1\n"
    "s_mov_b32 exec_hi, -1\n"
    "s_add_u32 s53, s53, 393216\n"
    "v_mov_b32_e64 v235, v222\n"
    "s_waitcnt lgkmcnt(0)\n"
    "s_barrier\n"
    "s_getpc_b64 vcc\n"
    ".Lreplk_pc2:\n"
    "s_sub_u32 vcc_lo, vcc_lo, .Lreplk_pc2-.Lreplk_pc0\n"
    "s_subb_u32 vcc_hi, vcc_hi, 0\n"
    "global_load_dword v233, v215, vcc\n"
    "ds_read_b128 a[224:227], v250 offset:640\n"
    "ds_read_b128 a[228:231], v250 offset:960\n"
    "ds_read_b128 a[240:243], v250 offset:720\n"
    "ds_read_b128 a[244:247], v250 offset:1040\n"
    "ds_read_b128 v[96:99], v250 offset:800\n"
    "ds_read_b128 v[100:103], v250 offset:1120\n"
    "ds_read_b128 v[120:123], v250 offset:880\n"
    "ds_read_b128 v[124:127], v250 offset:1200\n"
    "ds_read_b128 v[136:139], v250 offset:27664\n"
    "ds_read_b128 v[140:143], v250 offset:27984\n"
    "ds_read_b128 v[152:155], v250 offset:27744\n"
    "ds_read_b128 v[156:159], v250 offset:28064\n"
    "s_waitcnt lgkmcnt(10)\n"
    "v_smfmac_f32_16x16x64_bf16 v[0:3], a[232:235], a[224:231], v240\n"
    "s_nop 0\n"
    "ds_read_b128 a[232:235], v252 offset:12096\n"
    "v_smfmac_f32_16x16x64_bf16 v[4:7], a[236:239], a[224:231], v240 abid:1\n"
    "ds_read_b128 a[224:227], v250 offset:27824\n"
    "s_waitcnt lgkmcnt(10)\n"
    "v_smfmac_f32_16x16x64_bf16 v[8:11], a[248:251], a[240:247], v240\n"
    "ds_read_b128 a[228:231], v250 offset:28144\n"
    "v_smfmac_f32_16x16x64_bf16 v[12:15], a[252:255], a[240:247], v240 abid:1\n"
    "s_waitcnt lgkmcnt(9)\n"
    "ds_read_b128 a[236:239], v252 offset:12432\n"
    "v_smfmac_f32_16x16x64_bf16 v[16:19], v[104:107], v[96:103], v240\n"
    "ds_read_b128 a[240:243], v250 offset:27904\n"
    "v_smfmac_f32_16x16x64_bf16 v[20:23], v[108:111], v[96:103], v240 abid:1\n"
    "s_waitcnt lgkmcnt(9)\n"
    "ds_read_b128 a[244:247], v250 offset:28224\n"
    "v_smfmac_f32_16x16x64_bf16 v[24:27], v[128:131], v[120:127], v240\n"
    "ds_read_b128 a[248:251], v252 offset:12768\n"
    "v_smfmac_f32_16x16x64_bf16 v[28:31], v[132:135], v[120:127], v240 abid:1\n"
    "s_waitcnt lgkmcnt(9)\n"
    "ds_read_b128 a[252:255], v252 offset:13104\n"
    "v_smfmac_f32_16x16x64_bf16 v[32:35], v[144:147], v[136:143], v240\n"
    "s_nop 0\n"
    "ds_read_b128 v[96:99], v250 offset:54688\n"
    "v_smfmac_f32_16x16x64_bf16 v[36:39], v[148:151], v[136:143], v240 abid:1\n"
    "s_waitcnt lgkmcnt(9)\n"
    "ds_read_b128 v[100:103], v250 offset:55008\n"
    "v_smfmac_f32_16x16x64_bf16 v[40:43], v[160:163], v[152:159], v240\n"
    "ds_read_b128 v[104:107], v252 offset:21504\n"
    "v_smfmac_f32_16x16x64_bf16 v[44:47], v[164:167], v[152:159], v240 abid:1\n"
    "ds_read_b128 v[108:111], v252 offset:21840\n"
    "ds_read_b128 v[120:123], v250 offset:54768\n"
    "ds_read_b128 v[124:127], v250 offset:55088\n"
    "v_pk_mov_b32 v[224:225], v[0:1], v[0:1] op_sel:[0,1]\n"
    "v_pk_mov_b32 v[226:227], v[2:3], v[2:3] op_sel:[0,1]\n"
    "v_pk_mul_f32 v[228:229], v[0:1], v[0:1]\n"
    "v_pk_mul_f32 v[230:231], v[2:3], v[2:3]\n"
    "v_pk_add_f32 v[224:225], v[224:225], v[4:5]\n"
    "v_pk_add_f32 v[226:227], v[226:227], v[6:7]\n"
    "v_pk_fma_f32 v[228:229], v[4:5], v[4:5], v[228:229]\n"
    "v_pk_fma_f32 v[230:231], v[6:7], v[6:7], v[230:231]\n"
    "v_pk_add_f32 v[224:225], v[224:225], v[8:9]\n"
    "v_pk_add_f32 v[226:227], v[226:227], v[10:11]\n"
    "v_pk_fma_f32 v[228:229], v[8:9], v[8:9], v[228:229]\n"
    "v_pk_fma_f32 v[230:231], v[10:11], v[10:11], v[230:231]\n"
    "v_pk_add_f32 v[224:225], v[224:225], v[12:13]\n"
    "v_pk_add_f32 v[226:227], v[226:227], v[14:15]\n"
    "v_pk_fma_f32 v[228:229], v[12:13], v[12:13], v[228:229]\n"
    "v_pk_fma_f32 v[230:231], v[14:15], v[14:15], v[230:231]\n"
    "v_pk_add_f32 v[224:225], v[224:225], v[16:17]\n"
    "v_pk_add_f32 v[226:227], v[226:227], v[18:19]\n"
    "v_pk_fma_f32 v[228:229], v[16:17], v[16:17], v[228:229]\n"
    "v_pk_fma_f32 v[230:231], v[18:19], v[18:19], v[230:231]\n"
    "v_pk_add_f32 v[224:225], v[224:225], v[20:21]\n"
    "v_pk_add_f32 v[226:227], v[226:227], v[22:23]\n"
    "v_pk_fma_f32 v[228:229], v[20:21], v[20:21], v[228:229]\n"
    "v_pk_fma_f32 v[230:231], v[22:23], v[22:23], v[230:231]\n"
    "v_pk_add_f32 v[224:225], v[224:225], v[24:25]\n"
    "v_pk_add_f32 v[226:227], v[226:227], v[26:27]\n"
    "v_pk_fma_f32 v[228:229], v[24:25], v[24:25], v[228:229]\n"
    "v_pk_fma_f32 v[230:231], v[26:27], v[26:27], v[230:231]\n"
    "v_pk_add_f32 v[224:225], v[224:225], v[28:29]\n"
    "v_pk_add_f32 v[226:227], v[226:227], v[30:31]\n"
    "v_pk_fma_f32 v[228:229], v[28:29], v[28:29], v[228:229]\n"
    "v_pk_fma_f32 v[230:231], v[30:31], v[30:31], v[230:231]\n"
    "v_pk_add_f32 v[224:225], v[224:225], v[32:33]\n"
    "v_pk_add_f32 v[226:227], v[226:227], v[34:35]\n"
    "v_pk_fma_f32 v[228:229], v[32:33], v[32:33], v[228:229]\n"
    "v_pk_fma_f32 v[230:231], v[34:35], v[34:35], v[230:231]\n"
    "v_pk_add_f32 v[224:225], v[224:225], v[36:37]\n"
    "v_pk_add_f32 v[226:227], v[226:227], v[38:39]\n"
    "v_pk_fma_f32 v[228:229], v[36:37], v[36:37], v[228:229]\n"
    "v_pk_fma_f32 v[230:231], v[38:39], v[38:39], v[230:231]\n"
    "v_pk_add_f32 v[224:225], v[224:225], v[40:41]\n"
    "v_pk_add_f32 v[226:227], v[226:227], v[42:43]\n"
    "v_pk_fma_f32 v[228:229], v[40:41], v[40:41], v[228:229]\n"
    "v_pk_fma_f32 v[230:231], v[42:43], v[42:43], v[230:231]\n"
    "v_pk_add_f32 v[224:225], v[224:225], v[44:45]\n"
    "v_pk_add_f32 v[226:227], v[226:227], v[46:47]\n"
    "v_pk_fma_f32 v[228:229], v[44:45], v[44:45], v[228:229]\n"
    "v_pk_fma_f32 v[230:231], v[46:47], v[46:47], v[230:231]\n"
    "s_waitcnt lgkmcnt(0)\n"
    "v_smfmac_f32_16x16x64_bf16 v[48:51], a[232:235], a[224:231], v240\n"
    "s_nop 0\n"
    "ds_read_b128 v[128:131], v252 offset:22176\n"
    "v_smfmac_f32_16x16x64_bf16 v[52:55], a[236:239], a[224:231], v240 abid:1\n"
    "ds_read_b128 v[132:135], v252 offset:22512\n"
    "v_smfmac_f32_16x16x64_bf16 v[56:59], a[248:251], a[240:247], v240\n"
    "ds_read_b128 v[136:139], v250 offset:54848\n"
    "v_smfmac_f32_16x16x64_bf16 v[60:63], a[252:255], a[240:247], v240 abid:1\n"
    "ds_read_b128 v[140:143], v250 offset:55168\n"
    "v_smfmac_f32_16x16x64_bf16 v[64:67], v[104:107], v[96:103], v240\n"
    "ds_read_b128 v[144:147], v252 offset:22848\n"
    "v_smfmac_f32_16x16x64_bf16 v[68:71], v[108:111], v[96:103], v240 abid:1\n"
    "ds_read_b128 v[148:151], v252 offset:23184\n"
    "ds_read_b128 v[152:155], v250 offset:54928\n"
    "v_accvgpr_write_b32 a248, v236\n"
    "ds_read_b128 v[156:159], v250 offset:55248\n"
    "v_pk_add_f32 v[224:225], v[224:225], v[48:49]\n"
    "v_pk_add_f32 v[226:227], v[226:227], v[50:51]\n"
    "v_pk_fma_f32 v[228:229], v[48:49], v[48:49], v[228:229]\n"
    "v_pk_fma_f32 v[230:231], v[50:51], v[50:51], v[230:231]\n"
    "v_pk_add_f32 v[224:225], v[224:225], v[52:53]\n"
    "s_waitcnt vmcnt(5)\n"
    "s_barrier\n"
    "ds_read_b64_tr_b16 a[176:177], v220 offset:0\n"
    "ds_read_b64_tr_b16 a[178:179], v220 offset:3072\n"
    "v_pk_add_f32 v[226:227], v[226:227], v[54:55]\n"
    "ds_read_b64_tr_b16 a[180:181], v220 offset:8\n"
    "ds_read_b64_tr_b16 a[182:183], v220 offset:3080\n"
    "ds_read_b64_tr_b16 a[184:185], v220 offset:64\n"
    "v_pk_fma_f32 v[228:229], v[52:53], v[52:53], v[228:229]\n"
    "ds_read_b64_tr_b16 a[186:187], v220 offset:3136\n"
    "ds_read_b64_tr_b16 a[188:189], v220 offset:72\n"
    "ds_read_b64_tr_b16 a[190:191], v220 offset:3144\n"
    "v_pk_fma_f32 v[230:231], v[54:55], v[54:55], v[230:231]\n"
    "ds_read_b64_tr_b16 v[188:189], v218 offset:128\n"
    "ds_read_b64_tr_b16 v[190:191], v218 offset:3200\n"
    "ds_read_b64_tr_b16 a[192:193], v220 offset:6144\n"
    "v_pk_add_f32 v[224:225], v[224:225], v[56:57]\n"
    "ds_read_b64_tr_b16 a[194:195], v220 offset:9216\n"
    "ds_read_b64_tr_b16 a[196:197], v220 offset:6152\n"
    "ds_read_b64_tr_b16 a[198:199], v220 offset:9224\n"
    "v_pk_add_f32 v[226:227], v[226:227], v[58:59]\n"
    "ds_read_b64_tr_b16 a[200:201], v220 offset:6208\n"
    "ds_read_b64_tr_b16 a[202:203], v220 offset:9280\n"
    "ds_read_b64_tr_b16 a[204:205], v220 offset:6216\n"
    "v_pk_fma_f32 v[228:229], v[56:57], v[56:57], v[228:229]\n"
    "ds_read_b64_tr_b16 a[206:207], v220 offset:9288\n"
    "ds_read_b64_tr_b16 v[192:193], v218 offset:6272\n"
    "ds_read_b64_tr_b16 v[194:195], v218 offset:9344\n"
    "v_pk_fma_f32 v[230:231], v[58:59], v[58:59], v[230:231]\n"
    "ds_read_b64_tr_b16 a[208:209], v220 offset:12288\n"
    "ds_read_b64_tr_b16 a[210:211], v220 offset:15360\n"
    "ds_read_b64_tr_b16 a[212:213], v220 offset:12296\n"
    "v_pk_add_f32 v[224:225], v[224:225], v[60:61]\n"
    "ds_read_b64_tr_b16 a[214:215], v220 offset:15368\n"
    "ds_read_b64_tr_b16 a[216:217], v220 offset:12352\n"
    "ds_read_b64_tr_b16 a[218:219], v220 offset:15424\n"
    "v_pk_add_f32 v[226:227], v[226:227], v[62:63]\n"
    "ds_read_b64_tr_b16 a[220:221], v220 offset:12360\n"
    "ds_read_b64_tr_b16 a[222:223], v220 offset:15432\n"
    "ds_read_b64_tr_b16 v[196:197], v218 offset:12416\n"
    "v_pk_fma_f32 v[228:229], v[60:61], v[60:61], v[228:229]\n"
    "ds_read_b64_tr_b16 v[198:199], v218 offset:15488\n"
    "v_pk_fma_f32 v[230:231], v[62:63], v[62:63], v[230:231]\n"
    "v_pk_add_f32 v[224:225], v[224:225], v[64:65]\n"
    "v_pk_add_f32 v[226:227], v[226:227], v[66:67]\n"
    "v_pk_fma_f32 v[228:229], v[64:65], v[64:65], v[228:229]\n"
    "v_pk_fma_f32 v[230:231], v[66:67], v[66:67], v[230:231]\n"
    "v_pk_add_f32 v[224:225], v[224:225], v[68:69]\n"
    "v_pk_add_f32 v[226:227], v[226:227], v[70:71]\n"
    "v_pk_fma_f32 v[228:229], v[68:69], v[68:69], v[228:229]\n"
    "v_pk_fma_f32 v[230:231], v[70:71], v[70:71], v[230:231]\n"
    "s_waitcnt lgkmcnt(10)\n"
    "v_smfmac_f32_16x16x64_bf16 v[72:75], v[128:131], v[120:127], v240\n"
    "s_nop 0\n"
    "ds_read_b128 v[160:163], v252 offset:23520\n"
    "v_smfmac_f32_16x16x64_bf16 v[76:79], v[132:135], v[120:127], v240 abid:1\n"
    "ds_read_b128 v[164:167], v252 offset:23856\n"
    "v_smfmac_f32_16x16x64_bf16 v[80:83], v[144:147], v[136:143], v240\n"
    "v_accvgpr_write_b32 a249, v237\n"
    "v_smfmac_f32_16x16x64_bf16 v[84:87], v[148:151], v[136:143], v240 abid:1\n"
    "v_accvgpr_write_b32 a250, v238\n"
    "v_accvgpr_write_b32 a251, v239\n"
    "v_pk_add_f32 v[224:225], v[224:225], v[72:73]\n"
    "v_pk_add_f32 v[226:227], v[226:227], v[74:75]\n"
    "v_pk_fma_f32 v[228:229], v[72:73], v[72:73], v[228:229]\n"
    "v_pk_fma_f32 v[230:231], v[74:75], v[74:75], v[230:231]\n"
    "v_pk_add_f32 v[224:225], v[224:225], v[76:77]\n"
    "v_pk_add_f32 v[226:227], v[226:227], v[78:79]\n"
    "v_pk_fma_f32 v[228:229], v[76:77], v[76:77], v[228:229]\n"
    "v_pk_fma_f32 v[230:231], v[78:79], v[78:79], v[230:231]\n"
    "v_pk_add_f32 v[224:225], v[224:225], v[80:81]\n"
    "v_pk_add_f32 v[226:227], v[226:227], v[82:83]\n"
    "v_pk_fma_f32 v[228:229], v[80:81], v[80:81], v[228:229]\n"
    "v_pk_fma_f32 v[230:231], v[82:83], v[82:83], v[230:231]\n"
    "v_pk_add_f32 v[224:225], v[224:225], v[84:85]\n"
    "v_pk_add_f32 v[226:227], v[226:227], v[86:87]\n"
    "v_pk_fma_f32 v[228:229], v[84:85], v[84:85], v[228:229]\n"
    "v_pk_fma_f32 v[230:231], v[86:87], v[86:87], v[230:231]\n"
    "s_waitcnt lgkmcnt(1)\n"
    "v_smfmac_f32_16x16x64_bf16 v[88:91], v[160:163], v[152:159], v240\n"
    "s_waitcnt lgkmcnt(0)\n"
    "v_smfmac_f32_16x16x64_bf16 v[92:95], v[164:167], v[152:159], v240 abid:1\n"
    "s_nop 0\n"
    "s_nop 5\n"
    "v_pk_add_f32 v[224:225], v[224:225], v[88:89]\n"
    "v_pk_add_f32 v[226:227], v[226:227], v[90:91]\n"
    "v_pk_fma_f32 v[228:229], v[88:89], v[88:89], v[228:229]\n"
    "v_pk_fma_f32 v[230:231], v[90:91], v[90:91], v[230:231]\n"
    "v_pk_add_f32 v[224:225], v[224:225], v[92:93]\n"
    "v_pk_add_f32 v[226:227], v[226:227], v[94:95]\n"
    "v_pk_fma_f32 v[228:229], v[92:93], v[92:93], v[228:229]\n"
    "v_pk_fma_f32 v[230:231], v[94:95], v[94:95], v[230:231]\n"
    "ds_write_b128 v254, v[224:227]\n"
    "ds_write_b128 v254, v[228:231] offset:1024\n"
    "v_add_u32_e64 v254, s56, v242\n"
    "s_waitcnt lgkmcnt(0)\n"
    "s_barrier\n"
    "ds_read_b128 v[128:131], v254 offset:0\n"
    "ds_read_b128 v[132:135], v254 offset:6752\n"
    "ds_read_b128 v[136:139], v254 offset:13504\n"
    "ds_read_b128 v[140:143], v254 offset:20256\n"
    "ds_read_b128 v[144:147], v254 offset:1024\n"
    "ds_read_b128 v[148:151], v254 offset:7776\n"
    "ds_read_b128 v[152:155], v254 offset:14528\n"
    "ds_read_b128 v[156:159], v254 offset:21280\n"
    "ds_write_b128 v234, v[112:115] offset:0\n"
    "ds_write_b128 v234, v[116:119] offset:16\n"
    "s_waitcnt lgkmcnt(8)\n"
    "v_pk_add_f32 v[128:129], v[128:129], v[132:133]\n"
    "v_pk_add_f32 v[130:131], v[130:131], v[134:135]\n"
    "ds_write_b128 v207, v[168:171] offset:0\n"
    "s_waitcnt lgkmcnt(6)\n"
    "v_pk_add_f32 v[136:137], v[136:137], v[140:141]\n"
    "v_pk_add_f32 v[138:139], v[138:139], v[142:143]\n"
    "s_waitcnt lgkmcnt(5)\n"
    "v_pk_add_f32 v[144:145], v[144:145], v[148:149]\n"
    "v_pk_add_f32 v[146:147], v[146:147], v[150:151]\n"
    "v_pk_add_f32 v[128:129], v[128:129], v[136:137]\n"
    "v_pk_add_f32 v[130:131], v[130:131], v[138:139]\n"
    "v_mul_f32_e32 v128, 0x3c2aaaab, v128\n"
    "v_mul_f32_e32 v129, 0x3c2aaaab, v129\n"
    "v_mul_f32_e32 v130, 0x3c2aaaab, v130\n"
    "s_waitcnt lgkmcnt(3)\n"
    "v_pk_add_f32 v[152:153], v[152:153], v[156:157]\n"
    "v_pk_add_f32 v[154:155], v[154:155], v[158:159]\n"
    "v_pk_add_f32 v[144:145], v[144:145], v[152:153]\n"
    "v_pk_add_f32 v[146:147], v[146:147], v[154:155]\n"
    "v_mul_f32_e32 v131, 0x3c2aaaab, v131\n"
    "v_mul_f32_e32 v144, 0x3c2aaaab, v144\n"
    "v_mul_f32_e32 v145, 0x3c2aaaab, v145\n"
    "v_mul_f32_e32 v146, 0x3c2aaaab, v146\n"
    "v_mul_f32_e32 v147, 0x3c2aaaab, v147\n"
    "v_fma_f32 v144, -v128, v128, v144\n"
    "v_fma_f32 v145, -v129, v129, v145\n"
    "v_fma_f32 v146, -v130, v130, v146\n"
    "v_fma_f32 v147, -v131, v131, v147\n"
    "v_max_f32_e64 v144, 0, v144\n"
    "v_max_f32_e64 v145, 0, v145\n"
    "v_max_f32_e64 v146, 0, v146\n"
    "v_max_f32_e64 v147, 0, v147\n"
    "v_add_f32_e32 v144, 0x3727c5ac, v144\n"
    "v_add_f32_e32 v145, 0x3727c5ac, v145\n"
    "v_add_f32_e32 v146, 0x3727c5ac, v146\n"
    "v_add_f32_e32 v147, 0x3727c5ac, v147\n"
    "v_rsq_f32_e64 v144, v144\n"
    "v_rsq_f32_e64 v145, v145\n"
    "v_rsq_f32_e64 v146, v146\n"
    "v_rsq_f32_e64 v147, v147\n"
    "v_mul_f32_e64 v224, s50, v144\n"
    "v_mul_f32_e64 v225, s50, v145\n"
    "v_mul_f32_e64 v226, s50, v146\n"
    "v_mul_f32_e64 v227, s50, v147\n"
    "v_mul_f32_e64 v228, v224, -v128\n"
    "v_mul_f32_e64 v229, v225, -v129\n"
    "v_mul_f32_e64 v230, v226, -v130\n"
    "v_mul_f32_e64 v231, v227, -v131\n"
    "ds_write_b128 v207, v[172:175] offset:4096\n"
    "v_pk_fma_f32 v[64:65], v[64:65], v[224:225], v[228:229]\n"
    "v_pk_fma_f32 v[66:67], v[66:67], v[226:227], v[230:231]\n"
    "v_pk_fma_f32 v[68:69], v[68:69], v[224:225], v[228:229]\n"
    "v_pk_fma_f32 v[70:71], v[70:71], v[226:227], v[230:231]\n"
    "v_pk_fma_f32 v[72:73], v[72:73], v[224:225], v[228:229]\n"
    "v_pk_fma_f32 v[74:75], v[74:75], v[226:227], v[230:231]\n"
    "ds_write_b128 v207, v[176:179] offset:8192\n"
    "v_pk_fma_f32 v[76:77], v[76:77], v[224:225], v[228:229]\n"
    "v_pk_fma_f32 v[78:79], v[78:79], v[226:227], v[230:231]\n"
    "v_pk_fma_f32 v[80:81], v[80:81], v[224:225], v[228:229]\n"
    "v_pk_fma_f32 v[82:83], v[82:83], v[226:227], v[230:231]\n"
    "v_pk_fma_f32 v[84:85], v[84:85], v[224:225], v[228:229]\n"
    "v_pk_fma_f32 v[86:87], v[86:87], v[226:227], v[230:231]\n"
    "ds_write_b128 v207, v[188:191] offset:12288\n"
    "v_pk_fma_f32 v[88:89], v[88:89], v[224:225], v[228:229]\n"
    "v_pk_fma_f32 v[90:91], v[90:91], v[226:227], v[230:231]\n"
    "v_pk_fma_f32 v[92:93], v[92:93], v[224:225], v[228:229]\n"
    "v_pk_fma_f32 v[94:95], v[94:95], v[226:227], v[230:231]\n"
    "v_cvt_pk_bf16_f32 v96, v64, v68\n"
    "v_cvt_pk_bf16_f32 v100, v65, v69\n"
    "ds_write_b128 v207, v[192:195] offset:16384\n"
    "v_cvt_pk_bf16_f32 v104, v66, v70\n"
    "v_cvt_pk_bf16_f32 v108, v67, v71\n"
    "v_cvt_pk_bf16_f32 v97, v72, v76\n"
    "v_cvt_pk_bf16_f32 v101, v73, v77\n"
    "v_cvt_pk_bf16_f32 v105, v74, v78\n"
    "v_cvt_pk_bf16_f32 v109, v75, v79\n"
    "ds_write_b128 v207, v[196:199] offset:20480\n"
    "v_cvt_pk_bf16_f32 v98, v80, v84\n"
    "v_cvt_pk_bf16_f32 v102, v81, v85\n"
    "v_cvt_pk_bf16_f32 v106, v82, v86\n"
    "v_cvt_pk_bf16_f32 v110, v83, v87\n"
    "v_cvt_pk_bf16_f32 v99, v88, v92\n"
    "v_cvt_pk_bf16_f32 v103, v89, v93\n"
    "ds_write_b128 v207, v[180:183] offset:32768\n"
    "v_cvt_pk_bf16_f32 v107, v90, v94\n"
    "v_cvt_pk_bf16_f32 v111, v91, v95\n"
    "v_pk_fma_f32 v[0:1], v[0:1], v[224:225], v[228:229]\n"
    "v_pk_fma_f32 v[2:3], v[2:3], v[226:227], v[230:231]\n"
    "v_pk_fma_f32 v[4:5], v[4:5], v[224:225], v[228:229]\n"
    "v_pk_fma_f32 v[6:7], v[6:7], v[226:227], v[230:231]\n"
    "ds_write_b128 v253, v[96:99] offset:2048\n"
    "v_pk_fma_f32 v[8:9], v[8:9], v[224:225], v[228:229]\n"
    "v_pk_fma_f32 v[10:11], v[10:11], v[226:227], v[230:231]\n"
    "v_pk_fma_f32 v[12:13], v[12:13], v[224:225], v[228:229]\n"
    "v_pk_fma_f32 v[14:15], v[14:15], v[226:227], v[230:231]\n"
    "v_pk_fma_f32 v[16:17], v[16:17], v[224:225], v[228:229]\n"
    "v_pk_fma_f32 v[18:19], v[18:19], v[226:227], v[230:231]\n"
    "ds_write_b128 v253, v[100:103] offset:5120\n"
    "v_pk_fma_f32 v[20:21], v[20:21], v[224:225], v[228:229]\n"
    "v_pk_fma_f32 v[22:23], v[22:23], v[226:227], v[230:231]\n"
    "v_pk_fma_f32 v[24:25], v[24:25], v[224:225], v[228:229]\n"
    "v_pk_fma_f32 v[26:27], v[26:27], v[226:227], v[230:231]\n"
    "v_pk_fma_f32 v[28:29], v[28:29], v[224:225], v[228:229]\n"
    "v_pk_fma_f32 v[30:31], v[30:31], v[226:227], v[230:231]\n"
    "v_cvt_pk_bf16_f32 v80, v0, v4\n"
    "v_cvt_pk_bf16_f32 v84, v1, v5\n"
    "v_cvt_pk_bf16_f32 v88, v2, v6\n"
    "v_cvt_pk_bf16_f32 v92, v3, v7\n"
    "v_cvt_pk_bf16_f32 v81, v8, v12\n"
    "v_cvt_pk_bf16_f32 v85, v9, v13\n"
    "v_cvt_pk_bf16_f32 v89, v10, v14\n"
    "v_cvt_pk_bf16_f32 v93, v11, v15\n"
    "v_cvt_pk_bf16_f32 v82, v16, v20\n"
    "v_cvt_pk_bf16_f32 v86, v17, v21\n"
    "v_cvt_pk_bf16_f32 v90, v18, v22\n"
    "v_cvt_pk_bf16_f32 v94, v19, v23\n"
    "v_cvt_pk_bf16_f32 v83, v24, v28\n"
    "ds_write_b128 v253, v[80:83] offset:0\n"
    "v_cvt_pk_bf16_f32 v87, v25, v29\n"
    "v_cvt_pk_bf16_f32 v91, v26, v30\n"
    "v_cvt_pk_bf16_f32 v95, v27, v31\n"
    "v_pk_fma_f32 v[32:33], v[32:33], v[224:225], v[228:229]\n"
    "v_pk_fma_f32 v[34:35], v[34:35], v[226:227], v[230:231]\n"
    "v_pk_fma_f32 v[36:37], v[36:37], v[224:225], v[228:229]\n"
    "ds_write_b128 v253, v[84:87] offset:3072\n"
    "v_pk_fma_f32 v[38:39], v[38:39], v[226:227], v[230:231]\n"
    "v_pk_fma_f32 v[40:41], v[40:41], v[224:225], v[228:229]\n"
    "v_pk_fma_f32 v[42:43], v[42:43], v[226:227], v[230:231]\n"
    "v_pk_fma_f32 v[44:45], v[44:45], v[224:225], v[228:229]\n"
    "v_pk_fma_f32 v[46:47], v[46:47], v[226:227], v[230:231]\n"
    "v_pk_fma_f32 v[48:49], v[48:49], v[224:225], v[228:229]\n"
    "v_pk_fma_f32 v[50:51], v[50:51], v[226:227], v[230:231]\n"
    "v_pk_fma_f32 v[52:53], v[52:53], v[224:225], v[228:229]\n"
    "v_pk_fma_f32 v[54:55], v[54:55], v[226:227], v[230:231]\n"
    "v_pk_fma_f32 v[56:57], v[56:57], v[224:225], v[228:229]\n"
    "v_pk_fma_f32 v[58:59], v[58:59], v[226:227], v[230:231]\n"
    "v_pk_fma_f32 v[60:61], v[60:61], v[224:225], v[228:229]\n"
    "v_pk_fma_f32 v[62:63], v[62:63], v[226:227], v[230:231]\n"
    "v_cvt_pk_bf16_f32 v64, v32, v36\n"
    "v_cvt_pk_bf16_f32 v68, v33, v37\n"
    "v_cvt_pk_bf16_f32 v72, v34, v38\n"
    "v_cvt_pk_bf16_f32 v76, v35, v39\n"
    "v_cvt_pk_bf16_f32 v65, v40, v44\n"
    "v_cvt_pk_bf16_f32 v69, v41, v45\n"
    "v_cvt_pk_bf16_f32 v73, v42, v46\n"
    "v_cvt_pk_bf16_f32 v77, v43, v47\n"
    "v_cvt_pk_bf16_f32 v66, v48, v52\n"
    "v_cvt_pk_bf16_f32 v70, v49, v53\n"
    "v_cvt_pk_bf16_f32 v74, v50, v54\n"
    "v_cvt_pk_bf16_f32 v78, v51, v55\n"
    "v_cvt_pk_bf16_f32 v67, v56, v60\n"
    "ds_write_b128 v253, v[64:67] offset:1024\n"
    "v_cvt_pk_bf16_f32 v71, v57, v61\n"
    "v_cvt_pk_bf16_f32 v75, v58, v62\n"
    "v_cvt_pk_bf16_f32 v79, v59, v63\n"
    "ds_write_b128 v253, v[68:71] offset:4096\n"
    "s_waitcnt lgkmcnt(0)\n"
    "s_barrier\n"
    "ds_read_b128 a[224:227], v242 offset:0\n"
    "ds_read_b128 a[228:231], v242 offset:1024\n"
    "ds_read_b128 a[232:235], v242 offset:2048\n"
    "ds_read_b128 a[236:239], v242 offset:3072\n"
    "ds_read_b128 a[240:243], v242 offset:4096\n"
    "ds_read_b128 a[244:247], v242 offset:5120\n"
    "ds_read_b128 a[252:255], v244 offset:32768\n"
    "s_waitcnt lgkmcnt(6)\n"
    "v_mfma_f32_16x16x32_bf16 v[168:171], a[80:83], a[224:227], 0\n"
    "v_mfma_f32_16x16x32_bf16 v[172:175], a[84:87], a[224:227], 0\n"
    "s_waitcnt lgkmcnt(5)\n"
    "v_mfma_f32_16x16x32_bf16 v[168:171], a[128:131], a[228:231], v[168:171]\n"
    "s_waitcnt lgkmcnt(4)\n"
    "v_mfma_f32_16x16x32_bf16 v[168:171], a[48:51], a[232:235], v[168:171]\n"
    "s_waitcnt lgkmcnt(3)\n"
    "v_mfma_f32_16x16x32_bf16 v[172:175], a[132:135], a[228:231], v[172:175]\n"
    "v_mfma_f32_16x16x32_bf16 v[176:179], a[80:83], a[236:239], 0\n"
    "s_waitcnt lgkmcnt(1)\n"
    "v_mfma_f32_16x16x32_bf16 v[176:179], a[128:131], a[240:243], v[176:179]\n"
    "s_waitcnt lgkmcnt(0)\n"
    "v_mfma_f32_16x16x32_bf16 v[172:175], a[252:255], a[232:235], v[172:175]\n"
    "v_mfma_f32_16x16x32_bf16 v[176:179], a[48:51], a[244:247], v[176:179]\n"
    "v_mfma_f32_16x16x32_bf16 v[180:183], a[84:87], a[236:239], 0\n"
    "v_mfma_f32_16x16x32_bf16 v[180:183], a[132:135], a[240:243], v[180:183]\n"
    "v_mfma_f32_16x16x32_bf16 v[180:183], a[252:255], a[244:247], v[180:183]\n"
    "v_readfirstlane_b32 s66, v232\n"
    "s_nop 1\n"
    "s_cmp_lg_u32 s66, 2\n"
    "s_cbranch_scc1 98f\n"
    "ds_read_b128 a[252:255], v244 offset:36864\n"
    "ds_write_b128 v253, v[72:75] offset:25664\n"
    "v_lshlrev_b32_sdwa v184, 1, v168 dst_sel:DWORD dst_unused:UNUSED_PAD src0_sel:DWORD src1_sel:WORD_1\n"
    "v_lshlrev_b32_sdwa v200, 1, v169 dst_sel:DWORD dst_unused:UNUSED_PAD src0_sel:DWORD src1_sel:WORD_1\n"
    "v_lshlrev_b32_sdwa v185, 1, v170 dst_sel:DWORD dst_unused:UNUSED_PAD src0_sel:DWORD src1_sel:WORD_1\n"
    "v_lshlrev_b32_sdwa v201, 1, v171 dst_sel:DWORD dst_unused:UNUSED_PAD src0_sel:DWORD src1_sel:WORD_1\n"
    "v_mfma_f32_16x16x32_bf16 v[168:171], a[88:91], a[224:227], 0\n"
    "v_lshlrev_b32_sdwa v186, 1, v172 dst_sel:DWORD dst_unused:UNUSED_PAD src0_sel:DWORD src1_sel:WORD_1\n"
    "v_lshlrev_b32_sdwa v202, 1, v173 dst_sel:DWORD dst_unused:UNUSED_PAD src0_sel:DWORD src1_sel:WORD_1\n"
    "v_mfma_f32_16x16x32_bf16 v[168:171], a[136:139], a[228:231], v[168:171]\n"
    "ds_read_u16 v184, v184\n"
    "ds_read_u16_d16_hi v200, v200\n"
    "v_mfma_f32_16x16x32_bf16 v[168:171], a[52:55], a[232:235], v[168:171]\n"
    "ds_read_u16 v185, v185\n"
    "ds_read_u16_d16_hi v201, v201\n"
    "v_lshlrev_b32_sdwa v187, 1, v174 dst_sel:DWORD dst_unused:UNUSED_PAD src0_sel:DWORD src1_sel:WORD_1\n"
    "v_lshlrev_b32_sdwa v203, 1, v175 dst_sel:DWORD dst_unused:UNUSED_PAD src0_sel:DWORD src1_sel:WORD_1\n"
    "v_mfma_f32_16x16x32_bf16 v[172:175], a[92:95], a[224:227], 0\n"
    "v_lshlrev_b32_sdwa v188, 1, v176 dst_sel:DWORD dst_unused:UNUSED_PAD src0_sel:DWORD src1_sel:WORD_1\n"
    "v_lshlrev_b32_sdwa v204, 1, v177 dst_sel:DWORD dst_unused:UNUSED_PAD src0_sel:DWORD src1_sel:WORD_1\n"
    "v_mfma_f32_16x16x32_bf16 v[172:175], a[140:143], a[228:231], v[172:175]\n"
    "ds_read_u16 v186, v186\n"
    "v_lshlrev_b32_sdwa v189, 1, v178 dst_sel:DWORD dst_unused:UNUSED_PAD src0_sel:DWORD src1_sel:WORD_1\n"
    "s_waitcnt lgkmcnt(6)\n"
    "v_mfma_f32_16x16x32_bf16 v[172:175], a[252:255], a[232:235], v[172:175]\n"
    "s_nop 0\n"
    "ds_read_u16_d16_hi v202, v202\n"
    "v_lshlrev_b32_sdwa v205, 1, v179 dst_sel:DWORD dst_unused:UNUSED_PAD src0_sel:DWORD src1_sel:WORD_1\n"
    "v_mfma_f32_16x16x32_bf16 v[176:179], a[88:91], a[236:239], 0\n"
    "v_lshlrev_b32_sdwa v190, 1, v180 dst_sel:DWORD dst_unused:UNUSED_PAD src0_sel:DWORD src1_sel:WORD_1\n"
    "v_lshlrev_b32_sdwa v206, 1, v181 dst_sel:DWORD dst_unused:UNUSED_PAD src0_sel:DWORD src1_sel:WORD_1\n"
    "v_mfma_f32_16x16x32_bf16 v[176:179], a[136:139], a[240:243], v[176:179]\n"
    "ds_read_u16 v187, v187\n"
    "v_lshlrev_b32_sdwa v191, 1, v182 dst_sel:DWORD dst_unused:UNUSED_PAD src0_sel:DWORD src1_sel:WORD_1\n"
    "v_mfma_f32_16x16x32_bf16 v[176:179], a[52:55], a[244:247], v[176:179]\n"
    "v_lshlrev_b32_sdwa v207, 1, v183 dst_sel:DWORD dst_unused:UNUSED_PAD src0_sel:DWORD src1_sel:WORD_1\n"
    "v_cvt_pk_f16_f32 v192, v168, v169\n"
    "v_mfma_f32_16x16x32_bf16 v[180:183], a[92:95], a[236:239], 0\n"
    "ds_read_u16_d16_hi v203, v203\n"
    "v_lshlrev_b32_sdwa v193, 1, v170 dst_sel:DWORD dst_unused:UNUSED_PAD src0_sel:DWORD src1_sel:WORD_1\n"
    "v_mfma_f32_16x16x32_bf16 v[180:183], a[140:143], a[240:243], v[180:183]\n"
    "v_lshlrev_b32_sdwa v194, 1, v172 dst_sel:DWORD dst_unused:UNUSED_PAD src0_sel:DWORD src1_sel:WORD_1\n"
    "v_lshlrev_b32_sdwa v195, 1, v174 dst_sel:DWORD dst_unused:UNUSED_PAD src0_sel:DWORD src1_sel:WORD_1\n"
    "v_mfma_f32_16x16x32_bf16 v[180:183], a[252:255], a[244:247], v[180:183]\n"
    "ds_read_u16 v188, v188\n"
    "v_cvt_pk_f16_f32 v196, v176, v177\n"
    "v_lshlrev_b32_sdwa v197, 1, v178 dst_sel:DWORD dst_unused:UNUSED_PAD src0_sel:DWORD src1_sel:WORD_1\n"
    "v_pk_fma_f16 v232, v192, v192, s48\n"
    "s_waitcnt lgkmcnt(7)\n"
    "v_or_b32_e32 v184, v184, v200\n"
    "v_pk_mul_f16 v232, v192, v232\n"
    "v_mov_b32_e64 v254, 0\n"
    "v_lshlrev_b32_sdwa v198, 1, v180 dst_sel:DWORD dst_unused:UNUSED_PAD src0_sel:DWORD src1_sel:WORD_1\n"
    "v_lshlrev_b32_sdwa v199, 1, v182 dst_sel:DWORD dst_unused:UNUSED_PAD src0_sel:DWORD src1_sel:WORD_1\n"
    "v_exp_f16_e64 v234, v232\n"
    "ds_write_b128 v253, v[76:79] offset:28736\n"
    "s_waitcnt lgkmcnt(6)\n"
    "v_or_b32_e32 v185, v185, v201\n"
    "v_lshlrev_b32_sdwa v201, 1, v171 dst_sel:DWORD dst_unused:UNUSED_PAD src0_sel:DWORD src1_sel:WORD_1\n"
    "v_mfma_f32_16x16x32_bf16 v[168:171], a[96:99], a[224:227], 0\n"
    "v_exp_f16_sdwa v234, v232 dst_sel:WORD_1 dst_unused:UNUSED_PRESERVE src0_sel:WORD_1\n"
    "ds_read_u16_d16_hi v204, v204\n"
    "ds_write_b128 v253, v[88:91] offset:24640\n"
    "s_waitcnt lgkmcnt(6)\n"
    "v_or_b32_e32 v186, v186, v202\n"
    "v_mfma_f32_16x16x32_bf16 v[168:171], a[144:147], a[228:231], v[168:171]\n"
    "v_lshlrev_b32_sdwa v202, 1, v173 dst_sel:DWORD dst_unused:UNUSED_PAD src0_sel:DWORD src1_sel:WORD_1\n"
    "v_pk_fma_f16 v234, v234, s49, s49\n"
    "v_mfma_f32_16x16x32_bf16 v[168:171], a[56:59], a[232:235], v[168:171]\n"
    "ds_read_u16 v189, v189\n"
    "ds_read_u16_d16_hi v205, v205\n"
    "v_rcp_f16_e32 v232, v234\n"
    "s_waitcnt lgkmcnt(6)\n"
    "v_or_b32_e64 v187, v187, v203\n"
    "v_lshlrev_b32_sdwa v203, 1, v175 dst_sel:DWORD dst_unused:UNUSED_PAD src0_sel:DWORD src1_sel:WORD_1\n"
    "v_mfma_f32_16x16x32_bf16 v[172:175], a[100:103], a[224:227], 0\n"
    "ds_write_b128 v253, v[92:95] offset:27712\n"
    "v_mfma_f32_16x16x32_bf16 v[172:175], a[148:151], a[228:231], v[172:175]\n"
    "v_lshlrev_b32_sdwa v200, 1, v169 dst_sel:DWORD dst_unused:UNUSED_PAD src0_sel:DWORD src1_sel:WORD_1\n"
    "v_rcp_f16_sdwa v232, v234 dst_sel:WORD_1 dst_unused:UNUSED_PRESERVE src0_sel:WORD_1\n"
    "v_mfma_f32_16x16x32_bf16 v[172:175], a[60:63], a[232:235], v[172:175]\n"
    "v_fma_mix_f32 v234, v192, v232, 0 op_sel_hi:[1,1,0]\n"
    "ds_read_u16 v190, v190\n"
    "ds_read_u16_d16_hi v206, v206\n"
    "s_waitcnt lgkmcnt(5)\n"
    "v_or_b32_e32 v188, v188, v204\n"
    "v_fma_mix_f32 v232, v192, v232, 0 op_sel:[1,1,0] op_sel_hi:[1,1,0]\n"
    "v_cvt_pk_bf16_f32 v192, v234, v232\n"
    "v_pk_fma_f16 v232, v196, v196, s48\n"
    "v_pk_mul_f16 v232, v196, v232\n"
    "v_exp_f16_e64 v234, v232\n"
    "ds_read_u16 v191, v191\n"
    "ds_write_b128 v253, v[104:107] offset:26688\n"
    "v_exp_f16_sdwa v234, v232 dst_sel:WORD_1 dst_unused:UNUSED_PRESERVE src0_sel:WORD_1\n"
    "ds_read_u16_d16_hi v207, v207\n"
    "ds_read_u16 v193, v193\n"
    "s_waitcnt lgkmcnt(6)\n"
    "v_or_b32_e32 v189, v189, v205\n"
    "v_lshlrev_b32_sdwa v205, 1, v179 dst_sel:DWORD dst_unused:UNUSED_PAD src0_sel:DWORD src1_sel:WORD_1\n"
    "v_mfma_f32_16x16x32_bf16 v[176:179], a[96:99], a[236:239], 0\n"
    "ds_write_b128 v253, v[108:111] offset:29760\n"
    "v_mfma_f32_16x16x32_bf16 v[176:179], a[144:147], a[240:243], v[176:179]\n"
    "v_pk_fma_f16 v234, v234, s49, s49\n"
    "v_rcp_f16_e64 v232, v234\n"
    "v_mfma_f32_16x16x32_bf16 v[176:179], a[56:59], a[244:247], v[176:179]\n"
    "s_waitcnt lgkmcnt(5)\n"
    "v_or_b32_e32 v190, v190, v206\n"
    "v_lshlrev_b32_sdwa v206, 1, v181 dst_sel:DWORD dst_unused:UNUSED_PAD src0_sel:DWORD src1_sel:WORD_1\n"
    "v_mfma_f32_16x16x32_bf16 v[96:99], a[0:3], v[184:187], 0\n"
    "s_barrier\n"
    "v_mfma_f32_16x16x32_bf16 v[100:103], a[4:7], v[184:187], 0\n"
    "ds_read_u16_d16_hi v201, v201\n"
    "ds_read_u16 v194, v194\n"
    "v_mfma_f32_16x16x32_bf16 v[112:115], a[8:11], v[184:187], 0\n"
    "s_waitcnt lgkmcnt(4)\n"
    "ds_read_u16_d16_hi v202, v202\n"
    "ds_read_b128 v[236:239], v244 offset:0\n"
    "v_or_b32_e64 v191, v191, v207\n"
    "v_mfma_f32_16x16x32_bf16 v[116:119], a[12:15], v[184:187], 0\n"
    "ds_read_u16 v195, v195\n"
    "v_lshlrev_b32_sdwa v207, 1, v183 dst_sel:DWORD dst_unused:UNUSED_PAD src0_sel:DWORD src1_sel:WORD_1\n"
    "v_mfma_f32_16x16x32_bf16 v[180:183], a[100:103], a[236:239], 0\n"
    "v_lshlrev_b32_sdwa v204, 1, v177 dst_sel:DWORD dst_unused:UNUSED_PAD src0_sel:DWORD src1_sel:WORD_1\n"
    "v_rcp_f16_sdwa v232, v234 dst_sel:WORD_1 dst_unused:UNUSED_PRESERVE src0_sel:WORD_1\n"
    "v_mfma_f32_16x16x32_bf16 v[180:183], a[148:151], a[240:243], v[180:183]\n"
    "ds_read_u16_d16_hi v203, v203\n"
    "v_fma_mix_f32 v234, v196, v232, 0 op_sel_hi:[1,1,0]\n"
    "v_mfma_f32_16x16x32_bf16 v[180:183], a[60:63], a[244:247], v[180:183]\n"
    "v_fma_mix_f32 v232, v196, v232, 0 op_sel:[1,1,0] op_sel_hi:[1,1,0]\n"
    "v_cvt_pk_bf16_f32 v196, v234, v232\n"
    "v_mfma_f32_16x16x32_bf16 v[104:107], a[0:3], v[188:191], 0\n"
    "ds_read_b128 v[164:167], v244 offset:2048\n"
    "ds_read_u16 v197, v197\n"
    "v_mfma_f32_16x16x32_bf16 v[108:111], a[4:7], v[188:191], 0\n"
    "v_mfma_f32_16x16x32_bf16 v[120:123], a[8:11], v[188:191], 0\n"
    "ds_read_u16_d16_hi v205, v205\n"
    "v_mfma_f32_16x16x32_bf16 v[124:127], a[12:15], v[188:191], 0\n"
    "ds_read_u16 v198, v198\n"
    "ds_read_u16_d16_hi v206, v206\n"
    "s_waitcnt lgkmcnt(10)\n"
    "v_or_b32_e32 v193, v193, v201\n"
    "v_lshlrev_b32_sdwa v201, 1, v171 dst_sel:DWORD dst_unused:UNUSED_PAD src0_sel:DWORD src1_sel:WORD_1\n"
    "ds_read_u16 v199, v199\n"
    "ds_read_u16_d16_hi v207, v207\n"
    "ds_read_u16_d16_hi v200, v200\n"
    "ds_read_u16_d16_hi v201, v201\n"
    "ds_read_u16_d16_hi v204, v204\n"
    "ds_read_b128 a[252:255], v244 offset:34816\n"
    "s_waitcnt lgkmcnt(13)\n"
    "v_mfma_f32_16x16x32_bf16 v[128:131], v[236:239], v[184:187], 0\n"
    "v_or_b32_e32 v194, v194, v202\n"
    "v_lshlrev_b32_sdwa v202, 1, v173 dst_sel:DWORD dst_unused:UNUSED_PAD src0_sel:DWORD src1_sel:WORD_1\n"
    "v_mfma_f32_16x16x32_bf16 v[136:139], v[236:239], v[188:191], 0\n"
    "ds_read_b128 v[236:239], v244 offset:4096\n"
    "ds_read_u16_d16_hi v202, v202\n"
    "s_waitcnt lgkmcnt(12)\n"
    "v_mfma_f32_16x16x32_bf16 v[132:135], v[164:167], v[184:187], 0\n"
    "v_or_b32_e32 v195, v195, v203\n"
    "v_lshlrev_b32_sdwa v184, 1, v168 dst_sel:DWORD dst_unused:UNUSED_PAD src0_sel:DWORD src1_sel:WORD_1\n"
    "v_mfma_f32_16x16x32_bf16 v[140:143], v[164:167], v[188:191], 0\n"
    "v_lshlrev_b32_sdwa v185, 1, v170 dst_sel:DWORD dst_unused:UNUSED_PAD src0_sel:DWORD src1_sel:WORD_1\n"
    "v_lshlrev_b32_sdwa v186, 1, v172 dst_sel:DWORD dst_unused:UNUSED_PAD src0_sel:DWORD src1_sel:WORD_1\n"
    "v_mfma_f32_16x16x32_bf16 v[168:171], a[104:107], a[224:227], 0\n"
    "v_lshlrev_b32_sdwa v187, 1, v174 dst_sel:DWORD dst_unused:UNUSED_PAD src0_sel:DWORD src1_sel:WORD_1\n"
    "v_lshlrev_b32_sdwa v203, 1, v175 dst_sel:DWORD dst_unused:UNUSED_PAD src0_sel:DWORD src1_sel:WORD_1\n"
    "v_mfma_f32_16x16x32_bf16 v[168:171], a[152:155], a[228:231], v[168:171]\n"
    "v_lshlrev_b32_sdwa v188, 1, v176 dst_sel:DWORD dst_unused:UNUSED_PAD src0_sel:DWORD src1_sel:WORD_1\n"
    "v_lshlrev_b32_sdwa v189, 1, v178 dst_sel:DWORD dst_unused:UNUSED_PAD src0_sel:DWORD src1_sel:WORD_1\n"
    "v_mfma_f32_16x16x32_bf16 v[168:171], a[64:67], a[232:235], v[168:171]\n"
    "s_nop 0\n"
    "v_lshlrev_b32_sdwa v190, 1, v180 dst_sel:DWORD dst_unused:UNUSED_PAD src0_sel:DWORD src1_sel:WORD_1\n"
    "v_lshlrev_b32_sdwa v191, 1, v182 dst_sel:DWORD dst_unused:UNUSED_PAD src0_sel:DWORD src1_sel:WORD_1\n"
    "v_mfma_f32_16x16x32_bf16 v[172:175], a[108:111], a[224:227], 0\n"
    "s_waitcnt lgkmcnt(10)\n"
    "buffer_load_dwordx4 v[0:3], v246, s[40:43], s60 offen offset:0\n"
    "buffer_load_dwordx4 v[32:35], v246, s[40:43], s60 offen offset:64\n"
    "buffer_load_dwordx4 v[64:67], v246, s[40:43], s60 offen offset:128\n"
    "v_mfma_f32_16x16x32_bf16 v[172:175], a[156:159], a[228:231], v[172:175]\n"
    "v_or_b32_e64 v197, v197, v205\n"
    "v_lshlrev_b32_sdwa v205, 1, v179 dst_sel:DWORD dst_unused:UNUSED_PAD src0_sel:DWORD src1_sel:WORD_1\n"
    "v_mfma_f32_16x16x32_bf16 v[172:175], a[68:71], a[232:235], v[172:175]\n"
    "s_nop 0\n"
    "v_mfma_f32_16x16x32_bf16 v[176:179], a[104:107], a[236:239], 0\n"
    "s_waitcnt lgkmcnt(8)\n"
    "ds_read_u16 v184, v184\n"
    "v_or_b32_e64 v198, v198, v206\n"
    "v_mfma_f32_16x16x32_bf16 v[176:179], a[152:155], a[240:243], v[176:179]\n"
    "v_lshlrev_b32_sdwa v206, 1, v181 dst_sel:DWORD dst_unused:UNUSED_PAD src0_sel:DWORD src1_sel:WORD_1\n"
    "v_mfma_f32_16x16x32_bf16 v[176:179], a[64:67], a[244:247], v[176:179]\n"
    "ds_read_u16 v185, v185\n"
    "v_mfma_f32_16x16x32_bf16 v[96:99], a[16:19], v[192:195], v[96:99]\n"
    "s_waitcnt lgkmcnt(8)\n"
    "v_or_b32_e32 v199, v199, v207\n"
    "v_lshlrev_b32_sdwa v207, 1, v183 dst_sel:DWORD dst_unused:UNUSED_PAD src0_sel:DWORD src1_sel:WORD_1\n"
    "v_mfma_f32_16x16x32_bf16 v[180:183], a[108:111], a[236:239], 0\n"
    "ds_read_u16 v186, v186\n"
    "v_mfma_f32_16x16x32_bf16 v[180:183], a[156:159], a[240:243], v[180:183]\n"
    "v_mfma_f32_16x16x32_bf16 v[180:183], a[68:71], a[244:247], v[180:183]\n"
    "ds_read_u16 v187, v187\n"
    "v_mfma_f32_16x16x32_bf16 v[100:103], a[20:23], v[192:195], v[100:103]\n"
    "ds_read_b128 v[164:167], v244 offset:6144\n"
    "buffer_load_dwordx4 v[20:23], v247, s[40:43], s60 offen offset:0\n"
    "buffer_load_dwordx4 v[52:55], v247, s[40:43], s60 offen offset:64\n"
    "buffer_load_dwordx4 v[84:87], v247, s[40:43], s60 offen offset:128\n"
    "v_mfma_f32_16x16x32_bf16 v[112:115], a[24:27], v[192:195], v[112:115]\n"
    "ds_read_u16_d16_hi v203, v203\n"
    "v_mfma_f32_16x16x32_bf16 v[116:119], a[28:31], v[192:195], v[116:119]\n"
    "s_waitcnt lgkmcnt(7)\n"
    "v_mfma_f32_16x16x32_bf16 v[128:131], v[236:239], v[192:195], v[128:131]\n"
    "s_nop 0\n"
    "ds_read_u16 v188, v188\n"
    "v_mfma_f32_16x16x32_bf16 v[104:107], a[16:19], v[196:199], v[104:107]\n"
    "ds_read_u16 v189, v189\n"
    "v_mfma_f32_16x16x32_bf16 v[108:111], a[20:23], v[196:199], v[108:111]\n"
    "v_mfma_f32_16x16x32_bf16 v[120:123], a[24:27], v[196:199], v[120:123]\n"
    "ds_read_u16_d16_hi v205, v205\n"
    "s_waitcnt lgkmcnt(8)\n"
    "v_or_b32_e32 v184, v184, v200\n"
    "v_mfma_f32_16x16x32_bf16 v[124:127], a[28:31], v[196:199], v[124:127]\n"
    "s_nop 0\n"
    "v_mfma_f32_16x16x32_bf16 v[136:139], v[236:239], v[196:199], v[136:139]\n"
    "s_waitcnt lgkmcnt(7)\n"
    "ds_read_u16 v190, v190\n"
    "v_or_b32_e64 v185, v185, v201\n"
    "v_lshlrev_b32_sdwa v201, 1, v171 dst_sel:DWORD dst_unused:UNUSED_PAD src0_sel:DWORD src1_sel:WORD_1\n"
    "ds_read_u16_d16_hi v206, v206\n"
    "buffer_load_dwordx4 v[4:7], v246, s[40:43], s61 offen offset:0\n"
    "buffer_load_dwordx4 v[36:39], v246, s[40:43], s61 offen offset:64\n"
    "buffer_load_dwordx4 v[68:71], v246, s[40:43], s61 offen offset:128\n"
    "s_waitcnt lgkmcnt(8)\n"
    "v_or_b32_e32 v186, v186, v202\n"
    "v_lshlrev_b32_sdwa v202, 1, v173 dst_sel:DWORD dst_unused:UNUSED_PAD src0_sel:DWORD src1_sel:WORD_1\n"
    "s_waitcnt lgkmcnt(6)\n"
    "v_mfma_f32_16x16x32_bf16 v[132:135], v[164:167], v[192:195], v[132:135]\n"
    "s_nop 0\n"
    "ds_read_u16 v191, v191\n"
    "v_cvt_pk_f16_f32 v192, v168, v169\n"
    "v_mfma_f32_16x16x32_bf16 v[140:143], v[164:167], v[196:199], v[140:143]\n"
    "ds_read_u16_d16_hi v207, v207\n"
    "v_lshlrev_b32_sdwa v193, 1, v170 dst_sel:DWORD dst_unused:UNUSED_PAD src0_sel:DWORD src1_sel:WORD_1\n"
    "v_mfma_f32_16x16x32_bf16 v[168:171], a[112:115], a[224:227], 0\n"
    "v_lshlrev_b32_sdwa v194, 1, v172 dst_sel:DWORD dst_unused:UNUSED_PAD src0_sel:DWORD src1_sel:WORD_1\n"
    "v_lshlrev_b32_sdwa v195, 1, v174 dst_sel:DWORD dst_unused:UNUSED_PAD src0_sel:DWORD src1_sel:WORD_1\n"
    "v_mfma_f32_16x16x32_bf16 v[168:171], a[160:163], a[228:231], v[168:171]\n"
    "s_waitcnt lgkmcnt(7)\n"
    "ds_read_u16 v193, v193\n"
    "v_cvt_pk_f16_f32 v196, v176, v177\n"
    "v_mfma_f32_16x16x32_bf16 v[168:171], a[72:75], a[232:235], v[168:171]\n"
    "s_waitcnt lgkmcnt(7)\n"
    "v_lshlrev_b32_sdwa v197, 1, v178 dst_sel:DWORD dst_unused:UNUSED_PAD src0_sel:DWORD src1_sel:WORD_1\n"
    "v_lshlrev_b32_sdwa v198, 1, v180 dst_sel:DWORD dst_unused:UNUSED_PAD src0_sel:DWORD src1_sel:WORD_1\n"
    "v_lshlrev_b32_sdwa v199, 1, v182 dst_sel:DWORD dst_unused:UNUSED_PAD src0_sel:DWORD src1_sel:WORD_1\n"
    "v_or_b32_e64 v187, v187, v203\n"
    "v_lshlrev_b32_sdwa v203, 1, v175 dst_sel:DWORD dst_unused:UNUSED_PAD src0_sel:DWORD src1_sel:WORD_1\n"
    "v_mfma_f32_16x16x32_bf16 v[172:175], a[116:119], a[224:227], 0\n"
    "ds_read_u16_d16_hi v201, v201\n"
    "ds_read_u16 v194, v194\n"
    "v_mfma_f32_16x16x32_bf16 v[172:175], a[164:167], a[228:231], v[172:175]\n"
    "v_or_b32_e32 v188, v188, v204\n"
    "v_pk_fma_f16 v232, v192, v192, s48\n"
    "v_mfma_f32_16x16x32_bf16 v[172:175], a[252:255], a[232:235], v[172:175]\n"
    "s_waitcnt lgkmcnt(7)\n"
    "ds_read_u16_d16_hi v202, v202\n"
    "buffer_load_dwordx4 v[24:27], v247, s[40:43], s61 offen offset:0\n"
    "buffer_load_dwordx4 v[56:59], v247, s[40:43], s61 offen offset:64\n"
    "buffer_load_dwordx4 v[88:91], v247, s[40:43], s61 offen offset:128\n"
    "v_or_b32_e64 v189, v189, v205\n"
    "v_lshlrev_b32_sdwa v205, 1, v179 dst_sel:DWORD dst_unused:UNUSED_PAD src0_sel:DWORD src1_sel:WORD_1\n"
    "v_mfma_f32_16x16x32_bf16 v[176:179], a[112:115], a[236:239], 0\n"
    "ds_read_b128 v[236:239], v244 offset:8192\n"
    "v_pk_mul_f16 v232, v192, v232\n"
    "v_mfma_f32_16x16x32_bf16 v[176:179], a[160:163], a[240:243], v[176:179]\n"
    "ds_read_u16 v195, v195\n"
    "v_exp_f16_e64 v234, v232\n"
    "v_mfma_f32_16x16x32_bf16 v[176:179], a[72:75], a[244:247], v[176:179]\n"
    "s_waitcnt lgkmcnt(8)\n"
    "v_or_b32_e32 v190, v190, v206\n"
    "v_lshlrev_b32_sdwa v206, 1, v181 dst_sel:DWORD dst_unused:UNUSED_PAD src0_sel:DWORD src1_sel:WORD_1\n"
    "v_mfma_f32_16x16x32_bf16 v[96:99], a[32:35], v[184:187], v[96:99]\n"
    "ds_read_u16_d16_hi v203, v203\n"
    "v_exp_f16_sdwa v234, v232 dst_sel:WORD_1 dst_unused:UNUSED_PRESERVE src0_sel:WORD_1\n"
    "v_mfma_f32_16x16x32_bf16 v[100:103], a[36:39], v[184:187], v[100:103]\n"
    "ds_read_b128 v[164:167], v244 offset:10240\n"
    "v_pk_fma_f16 v234, v234, s49, s49\n"
    "v_mfma_f32_16x16x32_bf16 v[112:115], a[40:43], v[184:187], v[112:115]\n"
    "s_waitcnt lgkmcnt(8)\n"
    "ds_read_u16 v197, v197\n"
    "v_or_b32_e32 v191, v191, v207\n"
    "v_mfma_f32_16x16x32_bf16 v[116:119], a[44:47], v[184:187], v[116:119]\n"
    "v_lshlrev_b32_sdwa v207, 1, v183 dst_sel:DWORD dst_unused:UNUSED_PAD src0_sel:DWORD src1_sel:WORD_1\n"
    "v_lshlrev_b32_sdwa v200, 1, v169 dst_sel:DWORD dst_unused:UNUSED_PAD src0_sel:DWORD src1_sel:WORD_1\n"
    "v_mfma_f32_16x16x32_bf16 v[180:183], a[116:119], a[236:239], 0\n"
    "ds_read_u16_d16_hi v205, v205\n"
    "v_rcp_f16_e32 v232, v234\n"
    "v_mfma_f32_16x16x32_bf16 v[180:183], a[164:167], a[240:243], v[180:183]\n"
    "s_waitcnt lgkmcnt(8)\n"
    "ds_read_u16 v198, v198\n"
    "buffer_load_dwordx4 v[8:11], v246, s[40:43], s62 offen offset:0\n"
    "buffer_load_dwordx4 v[40:43], v246, s[40:43], s62 offen offset:64\n"
    "buffer_load_dwordx4 v[72:75], v246, s[40:43], s62 offen offset:128\n"
    "v_mfma_f32_16x16x32_bf16 v[180:183], a[252:255], a[244:247], v[180:183]\n"
    "v_or_b32_e64 v193, v193, v201\n"
    "v_lshlrev_b32_sdwa v201, 1, v171 dst_sel:DWORD dst_unused:UNUSED_PAD src0_sel:DWORD src1_sel:WORD_1\n"
    "v_mfma_f32_16x16x32_bf16 v[104:107], a[32:35], v[188:191], v[104:107]\n"
    "ds_read_u16_d16_hi v206, v206\n"
    "v_rcp_f16_sdwa v232, v234 dst_sel:WORD_1 dst_unused:UNUSED_PRESERVE src0_sel:WORD_1\n"
    "v_mfma_f32_16x16x32_bf16 v[108:111], a[36:39], v[188:191], v[108:111]\n"
    "s_waitcnt lgkmcnt(7)\n"
    "ds_read_u16 v199, v199\n"
    "v_or_b32_e32 v194, v194, v202\n"
    "v_mfma_f32_16x16x32_bf16 v[128:131], v[236:239], v[184:187], v[128:131]\n"
    "v_lshlrev_b32_sdwa v202, 1, v173 dst_sel:DWORD dst_unused:UNUSED_PAD src0_sel:DWORD src1_sel:WORD_1\n"
    "v_lshlrev_b32_sdwa v204, 1, v177 dst_sel:DWORD dst_unused:UNUSED_PAD src0_sel:DWORD src1_sel:WORD_1\n"
    "v_mfma_f32_16x16x32_bf16 v[120:123], a[40:43], v[188:191], v[120:123]\n"
    "ds_read_u16_d16_hi v207, v207\n"
    "v_fma_mix_f32 v234, v192, v232, 0 op_sel_hi:[1,1,0]\n"
    "v_mfma_f32_16x16x32_bf16 v[124:127], a[44:47], v[188:191], v[124:127]\n"
    "v_fma_mix_f32 v232, v192, v232, 0 op_sel:[1,1,0] op_sel_hi:[1,1,0]\n"
    "v_cvt_pk_bf16_f32 v192, v234, v232\n"
    "v_mfma_f32_16x16x32_bf16 v[136:139], v[236:239], v[188:191], v[136:139]\n"
    "s_waitcnt lgkmcnt(6)\n"
    "ds_read_b128 a[252:255], v244 offset:38912\n"
    "ds_read_u16_d16_hi v200, v200\n"
    "v_mfma_f32_16x16x32_bf16 v[132:135], v[164:167], v[184:187], v[132:135]\n"
    "v_or_b32_e32 v195, v195, v203\n"
    "v_lshlrev_b32_sdwa v184, 1, v168 dst_sel:DWORD dst_unused:UNUSED_PAD src0_sel:DWORD src1_sel:WORD_1\n"
    "v_mfma_f32_16x16x32_bf16 v[140:143], v[164:167], v[188:191], v[140:143]\n"
    "ds_read_u16 v184, v184\n"
    "v_lshlrev_b32_sdwa v185, 1, v170 dst_sel:DWORD dst_unused:UNUSED_PAD src0_sel:DWORD src1_sel:WORD_1\n"
    "v_mfma_f32_16x16x32_bf16 v[168:171], a[120:123], a[224:227], 0\n"
    "buffer_load_dwordx4 v[12:15], v246, s[40:43], s63 offen offset:0\n"
    "buffer_load_dwordx4 v[44:47], v246, s[40:43], s63 offen offset:64\n"
    "buffer_load_dwordx4 v[76:79], v246, s[40:43], s63 offen offset:128\n"
    "ds_read_u16 v185, v185\n"
    "v_mfma_f32_16x16x32_bf16 v[168:171], a[168:171], a[228:231], v[168:171]\n"
    "s_waitcnt lgkmcnt(8)\n"
    "v_or_b32_e32 v197, v197, v205\n"
    "v_lshlrev_b32_sdwa v186, 1, v172 dst_sel:DWORD dst_unused:UNUSED_PAD src0_sel:DWORD src1_sel:WORD_1\n"
    "v_mfma_f32_16x16x32_bf16 v[168:171], a[76:79], a[232:235], v[168:171]\n"
    "ds_read_u16_d16_hi v201, v201\n"
    "v_lshlrev_b32_sdwa v187, 1, v174 dst_sel:DWORD dst_unused:UNUSED_PAD src0_sel:DWORD src1_sel:WORD_1\n"
    "v_lshlrev_b32_sdwa v203, 1, v175 dst_sel:DWORD dst_unused:UNUSED_PAD src0_sel:DWORD src1_sel:WORD_1\n"
    "v_mfma_f32_16x16x32_bf16 v[172:175], a[124:127], a[224:227], 0\n"
    "v_lshlrev_b32_sdwa v188, 1, v176 dst_sel:DWORD dst_unused:UNUSED_PAD src0_sel:DWORD src1_sel:WORD_1\n"
    "v_lshlrev_b32_sdwa v189, 1, v178 dst_sel:DWORD dst_unused:UNUSED_PAD src0_sel:DWORD src1_sel:WORD_1\n"
    "v_mfma_f32_16x16x32_bf16 v[172:175], a[172:175], a[228:231], v[172:175]\n"
    "s_waitcnt lgkmcnt(7)\n"
    "ds_read_u16 v186, v186\n"
    "v_or_b32_e32 v198, v198, v206\n"
    "v_lshlrev_b32_sdwa v205, 1, v179 dst_sel:DWORD dst_unused:UNUSED_PAD src0_sel:DWORD src1_sel:WORD_1\n"
    "v_mfma_f32_16x16x32_bf16 v[176:179], a[120:123], a[236:239], 0\n"
    "ds_read_b128 v[236:239], v244 offset:12288\n"
    "v_lshlrev_b32_sdwa v190, 1, v180 dst_sel:DWORD dst_unused:UNUSED_PAD src0_sel:DWORD src1_sel:WORD_1\n"
    "v_mfma_f32_16x16x32_bf16 v[176:179], a[168:171], a[240:243], v[176:179]\n"
    "ds_read_u16_d16_hi v202, v202\n"
    "v_lshlrev_b32_sdwa v206, 1, v181 dst_sel:DWORD dst_unused:UNUSED_PAD src0_sel:DWORD src1_sel:WORD_1\n"
    "v_mfma_f32_16x16x32_bf16 v[176:179], a[76:79], a[244:247], v[176:179]\n"
    "s_waitcnt lgkmcnt(7)\n"
    "v_lshlrev_b32_sdwa v191, 1, v182 dst_sel:DWORD dst_unused:UNUSED_PAD src0_sel:DWORD src1_sel:WORD_1\n"
    "v_or_b32_e32 v199, v199, v207\n"
    "v_mfma_f32_16x16x32_bf16 v[172:175], a[252:255], a[232:235], v[172:175]\n"
    "ds_read_u16 v187, v187\n"
    "v_lshlrev_b32_sdwa v207, 1, v183 dst_sel:DWORD dst_unused:UNUSED_PAD src0_sel:DWORD src1_sel:WORD_1\n"
    "v_pk_fma_f16 v232, v196, v196, s48\n"
    "v_mfma_f32_16x16x32_bf16 v[180:183], a[124:127], a[236:239], 0\n"
    "buffer_load_dwordx4 v[16:19], v246, s[40:43], s52 offen offset:0\n"
    "buffer_load_dwordx4 v[48:51], v246, s[40:43], s52 offen offset:64\n"
    "buffer_load_dwordx4 v[80:83], v246, s[40:43], s52 offen offset:128\n"
    "ds_read_b128 v[164:167], v244 offset:14336\n"
    "v_pk_mul_f16 v232, v196, v232\n"
    "v_mfma_f32_16x16x32_bf16 v[180:183], a[172:175], a[240:243], v[180:183]\n"
    "ds_read_u16_d16_hi v203, v203\n"
    "v_exp_f16_e32 v234, v232\n"
    "v_mfma_f32_16x16x32_bf16 v[180:183], a[252:255], a[244:247], v[180:183]\n"
    "s_waitcnt lgkmcnt(8)\n"
    "ds_read_u16 v188, v188\n"
    "v_exp_f16_sdwa v234, v232 dst_sel:WORD_1 dst_unused:UNUSED_PRESERVE src0_sel:WORD_1\n"
    "v_mfma_f32_16x16x32_bf16 v[96:99], a[176:179], v[192:195], v[96:99]\n"
    "v_pk_fma_f16 v234, v234, s49, s49\n"
    "v_or_b32_e64 v184, v184, v200\n"
    "v_mfma_f32_16x16x32_bf16 v[100:103], a[180:183], v[192:195], v[100:103]\n"
    "ds_read_u16_d16_hi v204, v204\n"
    "v_rcp_f16_e32 v232, v234\n"
    "v_mfma_f32_16x16x32_bf16 v[112:115], a[184:187], v[192:195], v[112:115]\n"
    "s_waitcnt lgkmcnt(8)\n"
    "ds_read_u16 v189, v189\n"
    "v_rcp_f16_sdwa v232, v234 dst_sel:WORD_1 dst_unused:UNUSED_PRESERVE src0_sel:WORD_1\n"
    "v_mfma_f32_16x16x32_bf16 v[116:119], a[188:191], v[192:195], v[116:119]\n"
    "v_fma_mix_f32 v234, v196, v232, 0 op_sel_hi:[1,1,0]\n"
    "v_fma_mix_f32 v232, v196, v232, 0 op_sel:[1,1,0] op_sel_hi:[1,1,0]\n"
    "s_waitcnt lgkmcnt(7)\n"
    "v_mfma_f32_16x16x32_bf16 v[128:131], v[236:239], v[192:195], v[128:131]\n"
    "s_nop 0\n"
    "ds_read_u16_d16_hi v205, v205\n"
    "v_cvt_pk_bf16_f32 v196, v234, v232\n"
    "v_or_b32_e32 v185, v185, v201\n"
    "v_lshlrev_b32_sdwa v201, 1, v171 dst_sel:DWORD dst_unused:UNUSED_PAD src0_sel:DWORD src1_sel:WORD_1\n"
    "v_mfma_f32_16x16x32_bf16 v[104:107], a[176:179], v[196:199], v[104:107]\n"
    "s_waitcnt lgkmcnt(7)\n"
    "buffer_load_dwordx4 v[152:155], v245, s[40:43], s57 offen offset:64\n"
    "buffer_load_dwordx4 v[144:147], v245, s[40:43], s57 offen offset:0\n"
    "ds_read_b128 a[224:227], v243 offset:0\n"
    "v_mfma_f32_16x16x32_bf16 v[108:111], a[180:183], v[196:199], v[108:111]\n"
    "ds_read_u16 v190, v190\n"
    "v_or_b32_e32 v186, v186, v202\n"
    "v_mfma_f32_16x16x32_bf16 v[120:123], a[184:187], v[196:199], v[120:123]\n"
    "s_waitcnt lgkmcnt(7)\n"
    "ds_read_b128 a[228:231], v243 offset:1024\n"
    "v_lshlrev_b32_sdwa v202, 1, v173 dst_sel:DWORD dst_unused:UNUSED_PAD src0_sel:DWORD src1_sel:WORD_1\n"
    "v_mfma_f32_16x16x32_bf16 v[132:135], v[164:167], v[192:195], v[132:135]\n"
    "ds_read_u16_d16_hi v206, v206\n"
    "v_cvt_pk_f16_f32 v192, v168, v169\n"
    "v_mfma_f32_16x16x32_bf16 v[124:127], a[188:191], v[196:199], v[124:127]\n"
    "s_nop 0\n"
    "v_lshlrev_b32_sdwa v193, 1, v170 dst_sel:DWORD dst_unused:UNUSED_PAD src0_sel:DWORD src1_sel:WORD_1\n"
    "v_lshlrev_b32_sdwa v194, 1, v172 dst_sel:DWORD dst_unused:UNUSED_PAD src0_sel:DWORD src1_sel:WORD_1\n"
    "v_mfma_f32_16x16x32_bf16 v[136:139], v[236:239], v[196:199], v[136:139]\n"
    "s_waitcnt lgkmcnt(8)\n"
    "ds_read_b128 a[232:235], v243 offset:2048\n"
    "v_lshlrev_b32_sdwa v195, 1, v174 dst_sel:DWORD dst_unused:UNUSED_PAD src0_sel:DWORD src1_sel:WORD_1\n"
    "v_mfma_f32_16x16x32_bf16 v[140:143], v[164:167], v[196:199], v[140:143]\n"
    "ds_read_u16 v191, v191\n"
    "v_cvt_pk_f16_f32 v196, v176, v177\n"
    "v_lshlrev_b32_sdwa v197, 1, v178 dst_sel:DWORD dst_unused:UNUSED_PAD src0_sel:DWORD src1_sel:WORD_1\n"
    "v_lshlrev_b32_sdwa v198, 1, v180 dst_sel:DWORD dst_unused:UNUSED_PAD src0_sel:DWORD src1_sel:WORD_1\n"
    "v_lshlrev_b32_sdwa v199, 1, v182 dst_sel:DWORD dst_unused:UNUSED_PAD src0_sel:DWORD src1_sel:WORD_1\n"
    "v_or_b32_e64 v187, v187, v203\n"
    "v_lshlrev_b32_sdwa v203, 1, v175 dst_sel:DWORD dst_unused:UNUSED_PAD src0_sel:DWORD src1_sel:WORD_1\n"
    "s_waitcnt lgkmcnt(8)\n"
    "v_or_b32_e32 v188, v188, v204\n"
    "ds_read_b128 a[236:239], v243 offset:3072\n"
    "ds_read_u16_d16_hi v207, v207\n"
    "v_pk_fma_f16 v232, v192, v192, s48\n"
    "buffer_load_dwordx4 v[156:159], v245, s[40:43], s88 offen offset:64\n"
    "buffer_load_dwordx4 v[148:151], v245, s[40:43], s88 offen offset:0\n"
    "ds_read_b128 a[240:243], v243 offset:4096\n"
    "v_pk_mul_f16 v232, v192, v232\n"
    "s_waitcnt lgkmcnt(8)\n"
    "v_mfma_f32_16x16x32_bf16 v[168:171], a[80:83], a[224:227], 0\n"
    "s_nop 0\n"
    "ds_read_b128 a[244:247], v243 offset:5120\n"
    "v_or_b32_e64 v189, v189, v205\n"
    "v_mfma_f32_16x16x32_bf16 v[172:175], a[84:87], a[224:227], 0\n"
    "ds_read_b128 a[252:255], v244 offset:32768\n"
    "v_lshlrev_b32_sdwa v205, 1, v179 dst_sel:DWORD dst_unused:UNUSED_PAD src0_sel:DWORD src1_sel:WORD_1\n"
    "v_exp_f16_e32 v234, v232\n"
    "s_waitcnt lgkmcnt(8)\n"
    "v_mfma_f32_16x16x32_bf16 v[168:171], a[128:131], a[228:231], v[168:171]\n"
    "ds_read_u16 v193, v193\n"
    "v_exp_f16_sdwa v234, v232 dst_sel:WORD_1 dst_unused:UNUSED_PRESERVE src0_sel:WORD_1\n"
    "v_mfma_f32_16x16x32_bf16 v[172:175], a[132:135], a[228:231], v[172:175]\n"
    "ds_read_u16_d16_hi v201, v201\n"
    "v_pk_fma_f16 v234, v234, s49, s49\n"
    "v_rcp_f16_e32 v232, v234\n"
    "s_waitcnt lgkmcnt(8)\n"
    "v_mfma_f32_16x16x32_bf16 v[168:171], a[48:51], a[232:235], v[168:171]\n"
    "ds_read_u16 v194, v194\n"
    "v_or_b32_e64 v190, v190, v206\n"
    "v_mfma_f32_16x16x32_bf16 v[96:99], a[192:195], v[184:187], v[96:99]\n"
    "ds_read_u16_d16_hi v202, v202\n"
    "v_lshlrev_b32_sdwa v206, 1, v181 dst_sel:DWORD dst_unused:UNUSED_PAD src0_sel:DWORD src1_sel:WORD_1\n"
    "v_mfma_f32_16x16x32_bf16 v[100:103], a[196:199], v[184:187], v[100:103]\n"
    "s_waitcnt lgkmcnt(8)\n"
    "v_rcp_f16_sdwa v232, v234 dst_sel:WORD_1 dst_unused:UNUSED_PRESERVE src0_sel:WORD_1\n"
    "v_mfma_f32_16x16x32_bf16 v[176:179], a[80:83], a[236:239], 0\n"
    "s_nop 0\n"
    "ds_read_b128 v[236:239], v244 offset:16384\n"
    "v_lshlrev_b32_sdwa v200, 1, v169 dst_sel:DWORD dst_unused:UNUSED_PAD src0_sel:DWORD src1_sel:WORD_1\n"
    "v_mfma_f32_16x16x32_bf16 v[112:115], a[200:203], v[184:187], v[112:115]\n"
    "ds_read_u16 v195, v195\n"
    "v_fma_mix_f32 v234, v192, v232, 0 op_sel_hi:[1,1,0]\n"
    "s_waitcnt lgkmcnt(8)\n"
    "v_mfma_f32_16x16x32_bf16 v[176:179], a[128:131], a[240:243], v[176:179]\n"
    "v_or_b32_e32 v191, v191, v207\n"
    "v_lshlrev_b32_sdwa v207, 1, v183 dst_sel:DWORD dst_unused:UNUSED_PAD src0_sel:DWORD src1_sel:WORD_1\n"
    "v_mfma_f32_16x16x32_bf16 v[180:183], a[84:87], a[236:239], 0\n"
    "s_waitcnt lgkmcnt(7)\n"
    "ds_read_u16_d16_hi v203, v203\n"
    "v_fma_mix_f32 v232, v192, v232, 0 op_sel:[1,1,0] op_sel_hi:[1,1,0]\n"
    "v_mfma_f32_16x16x32_bf16 v[176:179], a[48:51], a[244:247], v[176:179]\n"
    "s_waitcnt lgkmcnt(7)\n"
    "ds_read_b128 v[164:167], v244 offset:18432\n"
    "v_cvt_pk_bf16_f32 v192, v234, v232\n"
    "v_mfma_f32_16x16x32_bf16 v[172:175], a[252:255], a[232:235], v[172:175]\n"
    "ds_read_u16 v197, v197\n"
    "v_pk_fma_f16 v232, v196, v196, s48\n"
    "v_mfma_f32_16x16x32_bf16 v[180:183], a[132:135], a[240:243], v[180:183]\n"
    "v_pk_mul_f16 v232, v196, v232\n"
    "v_lshlrev_b32_sdwa v204, 1, v177 dst_sel:DWORD dst_unused:UNUSED_PAD src0_sel:DWORD src1_sel:WORD_1\n"
    "v_mfma_f32_16x16x32_bf16 v[180:183], a[252:255], a[244:247], v[180:183]\n"
    "ds_read_u16_d16_hi v205, v205\n"
    "v_exp_f16_e64 v234, v232\n"
    "v_mfma_f32_16x16x32_bf16 v[116:119], a[204:207], v[184:187], v[116:119]\n"
    "s_waitcnt lgkmcnt(8)\n"
    "v_or_b32_e32 v193, v193, v201\n"
    "v_lshlrev_b32_sdwa v201, 1, v171 dst_sel:DWORD dst_unused:UNUSED_PAD src0_sel:DWORD src1_sel:WORD_1\n"
    "v_mfma_f32_16x16x32_bf16 v[104:107], a[192:195], v[188:191], v[104:107]\n"
    "ds_read_u16 v198, v198\n"
    "v_exp_f16_sdwa v234, v232 dst_sel:WORD_1 dst_unused:UNUSED_PRESERVE src0_sel:WORD_1\n"
    "v_mfma_f32_16x16x32_bf16 v[108:111], a[196:199], v[188:191], v[108:111]\n"
    "ds_read_u16_d16_hi v206, v206\n"
    "v_pk_fma_f16 v234, v234, s49, s49\n"
    "v_mfma_f32_16x16x32_bf16 v[120:123], a[200:203], v[188:191], v[120:123]\n"
    "s_waitcnt lgkmcnt(7)\n"
    "v_or_b32_e32 v194, v194, v202\n"
    "v_lshlrev_b32_sdwa v202, 1, v173 dst_sel:DWORD dst_unused:UNUSED_PAD src0_sel:DWORD src1_sel:WORD_1\n"
    "v_mfma_f32_16x16x32_bf16 v[128:131], v[236:239], v[184:187], v[128:131]\n"
    "ds_read_u16 v199, v199\n"
    "v_mfma_f32_16x16x32_bf16 v[124:127], a[204:207], v[188:191], v[124:127]\n"
    "v_mfma_f32_16x16x32_bf16 v[136:139], v[236:239], v[188:191], v[136:139]\n"
    "ds_read_u16_d16_hi v207, v207\n"
    "ds_read_b128 a[252:255], v244 offset:36864\n"
    "ds_read_u16_d16_hi v200, v200\n"
    "s_waitcnt lgkmcnt(8)\n"
    "v_mfma_f32_16x16x32_bf16 v[132:135], v[164:167], v[184:187], v[132:135]\n"
    "v_or_b32_e32 v195, v195, v203\n"
    "v_lshlrev_b32_sdwa v184, 1, v168 dst_sel:DWORD dst_unused:UNUSED_PAD src0_sel:DWORD src1_sel:WORD_1\n"
    "v_mfma_f32_16x16x32_bf16 v[140:143], v[164:167], v[188:191], v[140:143]\n"
    "v_lshlrev_b32_sdwa v185, 1, v170 dst_sel:DWORD dst_unused:UNUSED_PAD src0_sel:DWORD src1_sel:WORD_1\n"
    "v_lshlrev_b32_sdwa v186, 1, v172 dst_sel:DWORD dst_unused:UNUSED_PAD src0_sel:DWORD src1_sel:WORD_1\n"
    "v_mfma_f32_16x16x32_bf16 v[168:171], a[88:91], a[224:227], 0\n"
    "ds_read_u16 v184, v184\n"
    "v_lshlrev_b32_sdwa v187, 1, v174 dst_sel:DWORD dst_unused:UNUSED_PAD src0_sel:DWORD src1_sel:WORD_1\n"
    "v_mfma_f32_16x16x32_bf16 v[168:171], a[136:139], a[228:231], v[168:171]\n"
    "ds_read_u16 v185, v185\n"
    "s_branch 2f\n"
    ".p2align 6\n"
    "1:\n"
    ".Lreplk_loop:\n"
    "v_lshlrev_b32_sdwa v184, 1, v168 dst_sel:DWORD dst_unused:UNUSED_PAD src0_sel:DWORD src1_sel:WORD_1\n"
    "v_lshlrev_b32_sdwa v200, 1, v169 dst_sel:DWORD dst_unused:UNUSED_PAD src0_sel:DWORD src1_sel:WORD_1\n"
    "v_lshlrev_b32_sdwa v185, 1, v170 dst_sel:DWORD dst_unused:UNUSED_PAD src0_sel:DWORD src1_sel:WORD_1\n"
    "v_lshlrev_b32_sdwa v201, 1, v171 dst_sel:DWORD dst_unused:UNUSED_PAD src0_sel:DWORD src1_sel:WORD_1\n"
    "v_mfma_f32_16x16x32_bf16 v[140:143], v[164:167], v[188:191], v[140:143]\n"
    "s_waitcnt vmcnt(2)\n"
    "ds_read_b128 a[252:255], v244 offset:36864\n"
    "ds_read_u16 v184, v184\n"
    "v_mfma_f32_16x16x32_bf16 v[168:171], a[88:91], a[224:227], 0\n"
    "s_nop 0\n"
    "ds_read_u16_d16_hi v200, v200\n"
    "ds_read_u16 v185, v185\n"
    "v_mfma_f32_16x16x32_bf16 v[168:171], a[136:139], a[228:231], v[168:171]\n"
    "buffer_load_dwordx4 v[0:3], v246, s[40:43], s60 offen offset:0\n"
    "buffer_load_dwordx4 v[32:35], v246, s[40:43], s60 offen offset:64\n"
    "buffer_load_dwordx4 v[64:67], v246, s[40:43], s60 offen offset:128\n"
    "ds_read_u16_d16_hi v201, v201\n"
    "v_mfma_f32_16x16x32_bf16 v[168:171], a[52:55], a[232:235], v[168:171]\n"
    "v_lshlrev_b32_sdwa v186, 1, v172 dst_sel:DWORD dst_unused:UNUSED_PAD src0_sel:DWORD src1_sel:WORD_1\n"
    "v_lshlrev_b32_sdwa v202, 1, v173 dst_sel:DWORD dst_unused:UNUSED_PAD src0_sel:DWORD src1_sel:WORD_1\n"
    "v_smfmac_f32_32x32x32_bf16 v[128:143], a[248:251], v[144:151], v241\n"
    "ds_read_u16 v186, v186\n"
    "v_lshlrev_b32_sdwa v187, 1, v174 dst_sel:DWORD dst_unused:UNUSED_PAD src0_sel:DWORD src1_sel:WORD_1\n"
    "v_lshlrev_b32_sdwa v203, 1, v175 dst_sel:DWORD dst_unused:UNUSED_PAD src0_sel:DWORD src1_sel:WORD_1\n"
    "v_lshlrev_b32_sdwa v188, 1, v176 dst_sel:DWORD dst_unused:UNUSED_PAD src0_sel:DWORD src1_sel:WORD_1\n"
    "v_lshlrev_b32_sdwa v204, 1, v177 dst_sel:DWORD dst_unused:UNUSED_PAD src0_sel:DWORD src1_sel:WORD_1\n"
    "v_mfma_f32_16x16x32_bf16 v[172:175], a[92:95], a[224:227], 0\n"
    "ds_read_b128 v[236:239], v244 offset:20480\n"
    "ds_read_u16_d16_hi v202, v202\n"
    "v_mfma_f32_16x16x32_bf16 v[172:175], a[140:143], a[228:231], v[172:175]\n"
    "s_nop 0\n"
    "v_lshlrev_b32_sdwa v189, 1, v178 dst_sel:DWORD dst_unused:UNUSED_PAD src0_sel:DWORD src1_sel:WORD_1\n"
    "v_lshlrev_b32_sdwa v205, 1, v179 dst_sel:DWORD dst_unused:UNUSED_PAD src0_sel:DWORD src1_sel:WORD_1\n"
    "v_mfma_f32_16x16x32_bf16 v[176:179], a[88:91], a[236:239], 0\n"
    "s_waitcnt lgkmcnt(7)\n"
    "ds_read_u16 v187, v187\n"
    "v_lshlrev_b32_sdwa v190, 1, v180 dst_sel:DWORD dst_unused:UNUSED_PAD src0_sel:DWORD src1_sel:WORD_1\n"
    "v_mfma_f32_16x16x32_bf16 v[172:175], a[252:255], a[232:235], v[172:175]\n"
    "v_lshlrev_b32_sdwa v206, 1, v181 dst_sel:DWORD dst_unused:UNUSED_PAD src0_sel:DWORD src1_sel:WORD_1\n"
    "v_lshlrev_b32_sdwa v191, 1, v182 dst_sel:DWORD dst_unused:UNUSED_PAD src0_sel:DWORD src1_sel:WORD_1\n"
    "v_mfma_f32_16x16x32_bf16 v[176:179], a[136:139], a[240:243], v[176:179]\n"
    "ds_read_b128 v[164:167], v244 offset:22528\n"
    "v_lshlrev_b32_sdwa v207, 1, v183 dst_sel:DWORD dst_unused:UNUSED_PAD src0_sel:DWORD src1_sel:WORD_1\n"
    "v_mfma_f32_16x16x32_bf16 v[176:179], a[52:55], a[244:247], v[176:179]\n"
    "ds_read_u16_d16_hi v203, v203\n"
    "v_pk_fma_f16 v234, v234, s49, s49\n"
    "v_mfma_f32_16x16x32_bf16 v[180:183], a[92:95], a[236:239], 0\n"
    "v_rcp_f16_e32 v232, v234\n"
    "v_mfma_f32_16x16x32_bf16 v[180:183], a[140:143], a[240:243], v[180:183]\n"
    "s_waitcnt lgkmcnt(8)\n"
    "ds_read_u16 v188, v188\n"
    "v_rcp_f16_sdwa v232, v234 dst_sel:WORD_1 dst_unused:UNUSED_PRESERVE src0_sel:WORD_1\n"
    "v_mfma_f32_16x16x32_bf16 v[180:183], a[252:255], a[244:247], v[180:183]\n"
    "ds_read_u16_d16_hi v204, v204\n"
    "v_fma_mix_f32 v234, v196, v232, 0 op_sel_hi:[1,1,0]\n"
    "v_mfma_f32_16x16x32_bf16 v[96:99], a[208:211], v[192:195], v[96:99]\n"
    "v_fma_mix_f32 v232, v196, v232, 0 op_sel:[1,1,0] op_sel_hi:[1,1,0]\n"
    "v_or_b32_e32 v184, v184, v200\n"
    "v_mfma_f32_16x16x32_bf16 v[100:103], a[212:215], v[192:195], v[100:103]\n"
    "s_waitcnt lgkmcnt(8)\n"
    "ds_read_u16 v189, v189\n"
    "buffer_load_dwordx4 v[20:23], v247, s[40:43], s60 offen offset:0\n"
    "buffer_load_dwordx4 v[52:55], v247, s[40:43], s60 offen offset:64\n"
    "buffer_load_dwordx4 v[84:87], v247, s[40:43], s60 offen offset:128\n"
    "v_mfma_f32_16x16x32_bf16 v[112:115], a[216:219], v[192:195], v[112:115]\n"
    "s_waitcnt lgkmcnt(7)\n"
    "ds_read_u16_d16_hi v205, v205\n"
    "v_cvt_pk_bf16_f32 v196, v234, v232\n"
    "v_mfma_f32_16x16x32_bf16 v[116:119], a[220:223], v[192:195], v[116:119]\n"
    "v_or_b32_e32 v185, v185, v201\n"
    "v_lshlrev_b32_sdwa v201, 1, v171 dst_sel:DWORD dst_unused:UNUSED_PAD src0_sel:DWORD src1_sel:WORD_1\n"
    "v_mfma_f32_16x16x32_bf16 v[128:131], v[236:239], v[192:195], v[128:131]\n"
    "ds_read_u16 v190, v190\n"
    "v_cvt_pk_bf16_f32 v208, v96, v97\n"
    "v_mfma_f32_16x16x32_bf16 v[104:107], a[208:211], v[196:199], v[104:107]\n"
    "s_waitcnt lgkmcnt(8)\n"
    "v_or_b32_e64 v186, v186, v202\n"
    "v_lshlrev_b32_sdwa v202, 1, v173 dst_sel:DWORD dst_unused:UNUSED_PAD src0_sel:DWORD src1_sel:WORD_1\n"
    "v_mfma_f32_16x16x32_bf16 v[108:111], a[212:215], v[196:199], v[108:111]\n"
    "s_waitcnt lgkmcnt(6)\n"
    "ds_read_u16_d16_hi v206, v206\n"
    "v_cvt_pk_bf16_f32 v209, v98, v99\n"
    "v_mfma_f32_16x16x32_bf16 v[132:135], v[164:167], v[192:195], v[132:135]\n"
    "v_cvt_pk_f16_f32 v192, v168, v169\n"
    "v_lshlrev_b32_sdwa v193, 1, v170 dst_sel:DWORD dst_unused:UNUSED_PAD src0_sel:DWORD src1_sel:WORD_1\n"
    "v_mfma_f32_16x16x32_bf16 v[120:123], a[216:219], v[196:199], v[120:123]\n"
    "ds_read_u16 v191, v191\n"
    "v_lshlrev_b32_sdwa v194, 1, v172 dst_sel:DWORD dst_unused:UNUSED_PAD src0_sel:DWORD src1_sel:WORD_1\n"
    "v_mfma_f32_16x16x32_bf16 v[124:127], a[220:223], v[196:199], v[124:127]\n"
    "s_waitcnt lgkmcnt(7)\n"
    "ds_read_u16_d16_hi v207, v207\n"
    "v_lshlrev_b32_sdwa v195, 1, v174 dst_sel:DWORD dst_unused:UNUSED_PAD src0_sel:DWORD src1_sel:WORD_1\n"
    "v_mfma_f32_16x16x32_bf16 v[136:139], v[236:239], v[196:199], v[136:139]\n"
    "v_or_b32_e32 v187, v187, v203\n"
    "v_lshlrev_b32_sdwa v203, 1, v175 dst_sel:DWORD dst_unused:UNUSED_PAD src0_sel:DWORD src1_sel:WORD_1\n"
    "v_mfma_f32_16x16x32_bf16 v[140:143], v[164:167], v[196:199], v[140:143]\n"
    "ds_read_u16 v193, v193\n"
    "v_cvt_pk_f16_f32 v196, v176, v177\n"
    "v_mfma_f32_16x16x32_bf16 v[168:171], a[96:99], a[224:227], 0\n"
    "v_lshlrev_b32_sdwa v197, 1, v178 dst_sel:DWORD dst_unused:UNUSED_PAD src0_sel:DWORD src1_sel:WORD_1\n"
    "v_lshlrev_b32_sdwa v198, 1, v180 dst_sel:DWORD dst_unused:UNUSED_PAD src0_sel:DWORD src1_sel:WORD_1\n"
    "v_mfma_f32_16x16x32_bf16 v[168:171], a[144:147], a[228:231], v[168:171]\n"
    "s_waitcnt lgkmcnt(7)\n"
    "ds_read_u16_d16_hi v201, v201\n"
    "v_lshlrev_b32_sdwa v199, 1, v182 dst_sel:DWORD dst_unused:UNUSED_PAD src0_sel:DWORD src1_sel:WORD_1\n"
    "v_mfma_f32_16x16x32_bf16 v[168:171], a[56:59], a[232:235], v[168:171]\n"
    "v_or_b32_e32 v188, v188, v204\n"
    "v_cvt_pk_bf16_f32 v210, v100, v101\n"
    "v_mfma_f32_16x16x32_bf16 v[172:175], a[100:103], a[224:227], 0\n"
    "ds_read_u16 v194, v194\n"
    "v_cvt_pk_bf16_f32 v211, v102, v103\n"
    "v_mfma_f32_16x16x32_bf16 v[172:175], a[148:151], a[228:231], v[172:175]\n"
    "s_waitcnt lgkmcnt(7)\n"
    "ds_read_u16_d16_hi v202, v202\n"
    "v_or_b32_e32 v189, v189, v205\n"
    "v_mfma_f32_16x16x32_bf16 v[172:175], a[60:63], a[232:235], v[172:175]\n"
    "v_lshlrev_b32_sdwa v205, 1, v179 dst_sel:DWORD dst_unused:UNUSED_PAD src0_sel:DWORD src1_sel:WORD_1\n"
    "v_cvt_pk_bf16_f32 v212, v112, v113\n"
    "v_mfma_f32_16x16x32_bf16 v[176:179], a[96:99], a[236:239], 0\n"
    "ds_read_b128 v[236:239], v244 offset:0\n"
    "v_cvt_pk_bf16_f32 v213, v114, v115\n"
    "v_mfma_f32_16x16x32_bf16 v[176:179], a[144:147], a[240:243], v[176:179]\n"
    "buffer_load_dwordx4 v[4:7], v246, s[40:43], s61 offen offset:0\n"
    "buffer_load_dwordx4 v[36:39], v246, s[40:43], s61 offen offset:64\n"
    "buffer_load_dwordx4 v[68:71], v246, s[40:43], s61 offen offset:128\n"
    "ds_read_u16 v195, v195\n"
    "v_mfma_f32_16x16x32_bf16 v[176:179], a[56:59], a[244:247], v[176:179]\n"
    "s_waitcnt lgkmcnt(8)\n"
    "ds_read_u16_d16_hi v203, v203\n"
    "v_or_b32_e32 v190, v190, v206\n"
    "v_mfma_f32_16x16x32_bf16 v[96:99], a[0:3], v[184:187], 0\n"
    "v_lshlrev_b32_sdwa v206, 1, v181 dst_sel:DWORD dst_unused:UNUSED_PAD src0_sel:DWORD src1_sel:WORD_1\n"
    "v_cvt_pk_bf16_f32 v214, v116, v117\n"
    "v_mfma_f32_16x16x32_bf16 v[100:103], a[4:7], v[184:187], 0\n"
    "s_nop 0\n"
    "ds_read_b128 v[164:167], v244 offset:2048\n"
    "v_cvt_pk_bf16_f32 v215, v118, v119\n"
    "v_mfma_f32_16x16x32_bf16 v[112:115], a[8:11], v[184:187], 0\n"
    "s_waitcnt lgkmcnt(8)\n"
    "ds_read_u16 v197, v197\n"
    "v_cvt_pk_bf16_f32 v216, v128, v129\n"
    "v_cvt_pk_bf16_f32 v217, v130, v131\n"
    "v_or_b32_e64 v191, v191, v207\n"
    "v_mfma_f32_16x16x32_bf16 v[116:119], a[12:15], v[184:187], 0\n"
    "ds_read_u16_d16_hi v205, v205\n"
    "v_lshlrev_b32_sdwa v207, 1, v183 dst_sel:DWORD dst_unused:UNUSED_PAD src0_sel:DWORD src1_sel:WORD_1\n"
    "v_mfma_f32_16x16x32_bf16 v[180:183], a[100:103], a[236:239], 0\n"
    "v_cvt_pk_bf16_f32 v218, v132, v133\n"
    "v_cvt_pk_bf16_f32 v219, v134, v135\n"
    "v_mfma_f32_16x16x32_bf16 v[180:183], a[148:151], a[240:243], v[180:183]\n"
    "s_waitcnt lgkmcnt(8)\n"
    "ds_read_u16 v198, v198\n"
    "v_or_b32_e32 v193, v193, v201\n"
    "v_mfma_f32_16x16x32_bf16 v[180:183], a[60:63], a[244:247], v[180:183]\n"
    "v_cvt_pk_bf16_f32 v220, v104, v105\n"
    "v_cvt_pk_bf16_f32 v221, v106, v107\n"
    "v_mfma_f32_16x16x32_bf16 v[104:107], a[0:3], v[188:191], 0\n"
    "ds_read_u16_d16_hi v206, v206\n"
    "v_cvt_pk_bf16_f32 v222, v108, v109\n"
    "v_cvt_pk_bf16_f32 v223, v110, v111\n"
    "v_mfma_f32_16x16x32_bf16 v[108:111], a[4:7], v[188:191], 0\n"
    "s_waitcnt lgkmcnt(7)\n"
    "ds_read_u16 v199, v199\n"
    "v_or_b32_e32 v194, v194, v202\n"
    "v_mfma_f32_16x16x32_bf16 v[128:131], v[236:239], v[184:187], 0\n"
    "v_cvt_pk_bf16_f32 v224, v120, v121\n"
    "v_cvt_pk_bf16_f32 v225, v122, v123\n"
    "v_cvt_pk_bf16_f32 v226, v124, v125\n"
    "v_cvt_pk_bf16_f32 v227, v126, v127\n"
    "v_pk_fma_f16 v232, v192, v192, s48\n"
    "v_mfma_f32_16x16x32_bf16 v[120:123], a[8:11], v[188:191], 0\n"
    "ds_read_u16_d16_hi v207, v207\n"
    "v_cvt_pk_bf16_f32 v228, v136, v137\n"
    "v_mfma_f32_16x16x32_bf16 v[124:127], a[12:15], v[188:191], 0\n"
    "v_cvt_pk_bf16_f32 v229, v138, v139\n"
    "v_cvt_pk_bf16_f32 v230, v140, v141\n"
    "s_waitcnt lgkmcnt(6)\n"
    "v_mfma_f32_16x16x32_bf16 v[132:135], v[164:167], v[184:187], 0\n"
    "v_or_b32_e32 v195, v195, v203\n"
    "v_cvt_pk_bf16_f32 v231, v142, v143\n"
    "v_pk_mul_f16 v232, v192, v232\n"
    "v_mfma_f32_16x16x32_bf16 v[136:139], v[236:239], v[188:191], 0\n"
    "v_exp_f16_e64 v234, v232\n"
    "v_mfma_f32_16x16x32_bf16 v[140:143], v[164:167], v[188:191], 0\n"
    "buffer_load_dwordx4 v[24:27], v247, s[40:43], s61 offen offset:0\n"
    "buffer_load_dwordx4 v[56:59], v247, s[40:43], s61 offen offset:64\n"
    "buffer_load_dwordx4 v[88:91], v247, s[40:43], s61 offen offset:128\n"
    "v_exp_f16_sdwa v234, v232 dst_sel:WORD_1 dst_unused:UNUSED_PRESERVE src0_sel:WORD_1\n"
    "v_lshlrev_b32_sdwa v184, 1, v168 dst_sel:DWORD dst_unused:UNUSED_PAD src0_sel:DWORD src1_sel:WORD_1\n"
    "s_waitcnt lgkmcnt(4)\n"
    "v_or_b32_e32 v197, v197, v205\n"
    "v_pk_fma_f16 v234, v234, s49, s49\n"
    "v_lshlrev_b32_sdwa v200, 1, v169 dst_sel:DWORD dst_unused:UNUSED_PAD src0_sel:DWORD src1_sel:WORD_1\n"
    "v_lshlrev_b32_sdwa v185, 1, v170 dst_sel:DWORD dst_unused:UNUSED_PAD src0_sel:DWORD src1_sel:WORD_1\n"
    "v_lshlrev_b32_sdwa v201, 1, v171 dst_sel:DWORD dst_unused:UNUSED_PAD src0_sel:DWORD src1_sel:WORD_1\n"
    "v_mfma_f32_16x16x32_bf16 v[168:171], a[104:107], a[224:227], 0\n"
    "ds_read_u16 v184, v184\n"
    "ds_read_u16_d16_hi v200, v200\n"
    "v_mfma_f32_16x16x32_bf16 v[168:171], a[152:155], a[228:231], v[168:171]\n"
    "ds_read_u16 v185, v185\n"
    "ds_read_u16_d16_hi v201, v201\n"
    "v_lshlrev_b32_sdwa v186, 1, v172 dst_sel:DWORD dst_unused:UNUSED_PAD src0_sel:DWORD src1_sel:WORD_1\n"
    "v_lshlrev_b32_sdwa v202, 1, v173 dst_sel:DWORD dst_unused:UNUSED_PAD src0_sel:DWORD src1_sel:WORD_1\n"
    "v_lshlrev_b32_sdwa v187, 1, v174 dst_sel:DWORD dst_unused:UNUSED_PAD src0_sel:DWORD src1_sel:WORD_1\n"
    "v_lshlrev_b32_sdwa v203, 1, v175 dst_sel:DWORD dst_unused:UNUSED_PAD src0_sel:DWORD src1_sel:WORD_1\n"
    "v_rcp_f16_e32 v232, v234\n"
    "v_mfma_f32_16x16x32_bf16 v[168:171], a[64:67], a[232:235], v[168:171]\n"
    "s_waitcnt lgkmcnt(5)\n"
    "ds_read_u16 v186, v186\n"
    "v_or_b32_e32 v198, v198, v206\n"
    "v_rcp_f16_sdwa v232, v234 dst_sel:WORD_1 dst_unused:UNUSED_PRESERVE src0_sel:WORD_1\n"
    "v_mfma_f32_16x16x32_bf16 v[172:175], a[108:111], a[224:227], 0\n"
    "s_waitcnt lgkmcnt(5)\n"
    "ds_read_b128 v[236:239], v244 offset:4096\n"
    "v_lshlrev_b32_sdwa v188, 1, v176 dst_sel:DWORD dst_unused:UNUSED_PAD src0_sel:DWORD src1_sel:WORD_1\n"
    "v_mfma_f32_16x16x32_bf16 v[172:175], a[156:159], a[228:231], v[172:175]\n"
    "ds_read_u16_d16_hi v202, v202\n"
    "v_lshlrev_b32_sdwa v204, 1, v177 dst_sel:DWORD dst_unused:UNUSED_PAD src0_sel:DWORD src1_sel:WORD_1\n"
    "v_lshlrev_b32_sdwa v189, 1, v178 dst_sel:DWORD dst_unused:UNUSED_PAD src0_sel:DWORD src1_sel:WORD_1\n"
    "v_lshlrev_b32_sdwa v205, 1, v179 dst_sel:DWORD dst_unused:UNUSED_PAD src0_sel:DWORD src1_sel:WORD_1\n"
    "v_lshlrev_b32_sdwa v190, 1, v180 dst_sel:DWORD dst_unused:UNUSED_PAD src0_sel:DWORD src1_sel:WORD_1\n"
    "v_lshlrev_b32_sdwa v206, 1, v181 dst_sel:DWORD dst_unused:UNUSED_PAD src0_sel:DWORD src1_sel:WORD_1\n"
    "v_lshlrev_b32_sdwa v191, 1, v182 dst_sel:DWORD dst_unused:UNUSED_PAD src0_sel:DWORD src1_sel:WORD_1\n"
    "v_fma_mix_f32 v234, v192, v232, 0 op_sel_hi:[1,1,0]\n"
    "v_mfma_f32_16x16x32_bf16 v[172:175], a[68:71], a[232:235], v[172:175]\n"
    "ds_read_u16 v187, v187\n"
    "v_or_b32_e64 v199, v199, v207\n"
    "v_lshlrev_b32_sdwa v207, 1, v183 dst_sel:DWORD dst_unused:UNUSED_PAD src0_sel:DWORD src1_sel:WORD_1\n"
    "v_fma_mix_f32 v232, v192, v232, 0 op_sel:[1,1,0] op_sel_hi:[1,1,0]\n"
    "v_mfma_f32_16x16x32_bf16 v[176:179], a[104:107], a[236:239], 0\n"
    "ds_read_b128 v[164:167], v244 offset:6144\n"
    "ds_read_u16_d16_hi v203, v203\n"
    "v_cvt_pk_bf16_f32 v192, v234, v232\n"
    "v_mfma_f32_16x16x32_bf16 v[176:179], a[152:155], a[240:243], v[176:179]\n"
    "v_pk_fma_f16 v232, v196, v196, s48\n"
    "v_mov_b32_e32 v254, 0\n"
    "v_mfma_f32_16x16x32_bf16 v[176:179], a[64:67], a[244:247], v[176:179]\n"
    "s_waitcnt lgkmcnt(8)\n"
    "ds_read_u16 v188, v188\n"
    "v_pk_mul_f16 v232, v196, v232\n"
    "v_mfma_f32_16x16x32_bf16 v[180:183], a[108:111], a[236:239], 0\n"
    "v_exp_f16_e64 v234, v232\n"
    "v_mfma_f32_16x16x32_bf16 v[180:183], a[156:159], a[240:243], v[180:183]\n"
    "buffer_load_dwordx4 v[8:11], v246, s[40:43], s62 offen offset:0\n"
    "buffer_load_dwordx4 v[40:43], v246, s[40:43], s62 offen offset:64\n"
    "buffer_load_dwordx4 v[72:75], v246, s[40:43], s62 offen offset:128\n"
    "ds_read_u16_d16_hi v204, v204\n"
    "v_mfma_f32_16x16x32_bf16 v[180:183], a[68:71], a[244:247], v[180:183]\n"
    "s_waitcnt lgkmcnt(8)\n"
    "ds_read_u16 v189, v189\n"
    "v_exp_f16_sdwa v234, v232 dst_sel:WORD_1 dst_unused:UNUSED_PRESERVE src0_sel:WORD_1\n"
    "v_mfma_f32_16x16x32_bf16 v[96:99], a[16:19], v[192:195], v[96:99]\n"
    "s_nop 0\n"
    "v_pk_fma_f16 v234, v234, s49, s49\n"
    "v_or_b32_e64 v184, v184, v200\n"
    "v_mfma_f32_16x16x32_bf16 v[100:103], a[20:23], v[192:195], v[100:103]\n"
    "ds_read_u16_d16_hi v205, v205\n"
    "v_rcp_f16_e64 v232, v234\n"
    "v_mfma_f32_16x16x32_bf16 v[112:115], a[24:27], v[192:195], v[112:115]\n"
    "ds_read_u16 v190, v190\n"
    "v_rcp_f16_sdwa v232, v234 dst_sel:WORD_1 dst_unused:UNUSED_PRESERVE src0_sel:WORD_1\n"
    "v_mfma_f32_16x16x32_bf16 v[116:119], a[28:31], v[192:195], v[116:119]\n"
    "s_waitcnt lgkmcnt(8)\n"
    "v_fma_mix_f32 v234, v196, v232, 0 op_sel_hi:[1,1,0]\n"
    "v_fma_mix_f32 v232, v196, v232, 0 op_sel:[1,1,0] op_sel_hi:[1,1,0]\n"
    "v_mfma_f32_16x16x32_bf16 v[128:131], v[236:239], v[192:195], v[128:131]\n"
    "s_waitcnt lgkmcnt(6)\n"
    "ds_read_u16_d16_hi v206, v206\n"
    "v_cvt_pk_bf16_f32 v196, v234, v232\n"
    "v_mfma_f32_16x16x32_bf16 v[132:135], v[164:167], v[192:195], v[132:135]\n"
    "ds_read_u16 v191, v191\n"
    "v_or_b32_e32 v185, v185, v201\n"
    "v_mfma_f32_16x16x32_bf16 v[104:107], a[16:19], v[196:199], v[104:107]\n"
    "v_cvt_pk_f16_f32 v192, v168, v169\n"
    "v_lshlrev_b32_sdwa v193, 1, v170 dst_sel:DWORD dst_unused:UNUSED_PAD src0_sel:DWORD src1_sel:WORD_1\n"
    "v_mfma_f32_16x16x32_bf16 v[108:111], a[20:23], v[196:199], v[108:111]\n"
    "s_waitcnt lgkmcnt(7)\n"
    "ds_read_u16_d16_hi v207, v207\n"
    "v_lshlrev_b32_sdwa v201, 1, v171 dst_sel:DWORD dst_unused:UNUSED_PAD src0_sel:DWORD src1_sel:WORD_1\n"
    "v_mfma_f32_16x16x32_bf16 v[120:123], a[24:27], v[196:199], v[120:123]\n"
    "v_lshlrev_b32_sdwa v194, 1, v172 dst_sel:DWORD dst_unused:UNUSED_PAD src0_sel:DWORD src1_sel:WORD_1\n"
    "v_or_b32_e64 v186, v186, v202\n"
    "v_mfma_f32_16x16x32_bf16 v[124:127], a[28:31], v[196:199], v[124:127]\n"
    "ds_read_b128 a[252:255], v244 offset:34816\n"
    "v_lshlrev_b32_sdwa v202, 1, v173 dst_sel:DWORD dst_unused:UNUSED_PAD src0_sel:DWORD src1_sel:WORD_1\n"
    "v_mfma_f32_16x16x32_bf16 v[136:139], v[236:239], v[196:199], v[136:139]\n"
    "s_nop 0\n"
    "ds_read_u16 v193, v193\n"
    "v_lshlrev_b32_sdwa v195, 1, v174 dst_sel:DWORD dst_unused:UNUSED_PAD src0_sel:DWORD src1_sel:WORD_1\n"
    "v_mfma_f32_16x16x32_bf16 v[140:143], v[164:167], v[196:199], v[140:143]\n"
    "s_waitcnt lgkmcnt(8)\n"
    "v_cvt_pk_f16_f32 v196, v176, v177\n"
    "v_lshlrev_b32_sdwa v197, 1, v178 dst_sel:DWORD dst_unused:UNUSED_PAD src0_sel:DWORD src1_sel:WORD_1\n"
    "v_mfma_f32_16x16x32_bf16 v[168:171], a[112:115], a[224:227], 0\n"
    "ds_read_u16_d16_hi v201, v201\n"
    "v_lshlrev_b32_sdwa v198, 1, v180 dst_sel:DWORD dst_unused:UNUSED_PAD src0_sel:DWORD src1_sel:WORD_1\n"
    "v_mfma_f32_16x16x32_bf16 v[168:171], a[160:163], a[228:231], v[168:171]\n"
    "ds_read_u16 v194, v194\n"
    "v_lshlrev_b32_sdwa v199, 1, v182 dst_sel:DWORD dst_unused:UNUSED_PAD src0_sel:DWORD src1_sel:WORD_1\n"
    "v_mfma_f32_16x16x32_bf16 v[168:171], a[72:75], a[232:235], v[168:171]\n"
    "s_waitcnt lgkmcnt(8)\n"
    "v_or_b32_e32 v187, v187, v203\n"
    "v_lshlrev_b32_sdwa v203, 1, v175 dst_sel:DWORD dst_unused:UNUSED_PAD src0_sel:DWORD src1_sel:WORD_1\n"
    "v_mfma_f32_16x16x32_bf16 v[172:175], a[116:119], a[224:227], 0\n"
    "buffer_load_dwordx4 v[12:15], v246, s[40:43], s63 offen offset:0\n"
    "buffer_load_dwordx4 v[44:47], v246, s[40:43], s63 offen offset:64\n"
    "buffer_load_dwordx4 v[76:79], v246, s[40:43], s63 offen offset:128\n"
    "ds_read_u16_d16_hi v202, v202\n"
    "v_mfma_f32_16x16x32_bf16 v[172:175], a[164:167], a[228:231], v[172:175]\n"
    "ds_read_b128 v[236:239], v244 offset:8192\n"
    "v_or_b32_e64 v188, v188, v204\n"
    "v_or_b32_e64 v189, v189, v205\n"
    "v_lshlrev_b32_sdwa v205, 1, v179 dst_sel:DWORD dst_unused:UNUSED_PAD src0_sel:DWORD src1_sel:WORD_1\n"
    "v_mfma_f32_16x16x32_bf16 v[176:179], a[112:115], a[236:239], 0\n"
    "s_waitcnt lgkmcnt(8)\n"
    "ds_read_u16 v195, v195\n"
    "v_or_b32_e32 v190, v190, v206\n"
    "v_mfma_f32_16x16x32_bf16 v[176:179], a[160:163], a[240:243], v[176:179]\n"
    "v_lshlrev_b32_sdwa v206, 1, v181 dst_sel:DWORD dst_unused:UNUSED_PAD src0_sel:DWORD src1_sel:WORD_1\n"
    "v_pk_fma_f16 v232, v192, v192, s48\n"
    "v_mfma_f32_16x16x32_bf16 v[176:179], a[72:75], a[244:247], v[176:179]\n"
    "ds_read_u16_d16_hi v203, v203\n"
    "v_pk_mul_f16 v232, v192, v232\n"
    "v_mfma_f32_16x16x32_bf16 v[96:99], a[32:35], v[184:187], v[96:99]\n"
    "s_waitcnt lgkmcnt(7)\n"
    "ds_read_b128 v[164:167], v244 offset:10240\n"
    "v_or_b32_e32 v191, v191, v207\n"
    "v_mfma_f32_16x16x32_bf16 v[172:175], a[252:255], a[232:235], v[172:175]\n"
    "ds_read_u16 v197, v197\n"
    "v_lshlrev_b32_sdwa v207, 1, v183 dst_sel:DWORD dst_unused:UNUSED_PAD src0_sel:DWORD src1_sel:WORD_1\n"
    "v_mfma_f32_16x16x32_bf16 v[180:183], a[116:119], a[236:239], 0\n"
    "v_exp_f16_e64 v234, v232\n"
    "v_mfma_f32_16x16x32_bf16 v[180:183], a[164:167], a[240:243], v[180:183]\n"
    "ds_read_u16_d16_hi v205, v205\n"
    "v_exp_f16_sdwa v234, v232 dst_sel:WORD_1 dst_unused:UNUSED_PRESERVE src0_sel:WORD_1\n"
    "v_mfma_f32_16x16x32_bf16 v[180:183], a[252:255], a[244:247], v[180:183]\n"
    "s_waitcnt lgkmcnt(8)\n"
    "v_or_b32_e32 v193, v193, v201\n"
    "v_pk_fma_f16 v234, v234, s49, s49\n"
    "v_mfma_f32_16x16x32_bf16 v[100:103], a[36:39], v[184:187], v[100:103]\n"
    "ds_read_u16 v198, v198\n"
    "v_lshlrev_b32_sdwa v200, 1, v169 dst_sel:DWORD dst_unused:UNUSED_PAD src0_sel:DWORD src1_sel:WORD_1\n"
    "v_mfma_f32_16x16x32_bf16 v[112:115], a[40:43], v[184:187], v[112:115]\n"
    "ds_read_u16_d16_hi v206, v206\n"
    "v_lshlrev_b32_sdwa v201, 1, v171 dst_sel:DWORD dst_unused:UNUSED_PAD src0_sel:DWORD src1_sel:WORD_1\n"
    "v_mfma_f32_16x16x32_bf16 v[116:119], a[44:47], v[184:187], v[116:119]\n"
    "v_rcp_f16_e32 v232, v234\n"
    "v_mfma_f32_16x16x32_bf16 v[104:107], a[32:35], v[188:191], v[104:107]\n"
    "s_waitcnt lgkmcnt(7)\n"
    "ds_read_u16 v199, v199\n"
    "v_or_b32_e64 v194, v194, v202\n"
    "v_mfma_f32_16x16x32_bf16 v[128:131], v[236:239], v[184:187], v[128:131]\n"
    "v_lshlrev_b32_sdwa v202, 1, v173 dst_sel:DWORD dst_unused:UNUSED_PAD src0_sel:DWORD src1_sel:WORD_1\n"
    "v_rcp_f16_sdwa v232, v234 dst_sel:WORD_1 dst_unused:UNUSED_PRESERVE src0_sel:WORD_1\n"
    "v_mfma_f32_16x16x32_bf16 v[108:111], a[36:39], v[188:191], v[108:111]\n"
    "ds_read_u16_d16_hi v207, v207\n"
    "v_lshlrev_b32_sdwa v204, 1, v177 dst_sel:DWORD dst_unused:UNUSED_PAD src0_sel:DWORD src1_sel:WORD_1\n"
    "v_mfma_f32_16x16x32_bf16 v[120:123], a[40:43], v[188:191], v[120:123]\n"
    "buffer_load_dwordx4 v[16:19], v246, s[40:43], s52 offen offset:0\n"
    "buffer_load_dwordx4 v[48:51], v246, s[40:43], s52 offen offset:64\n"
    "buffer_load_dwordx4 v[80:83], v246, s[40:43], s52 offen offset:128\n"
    "ds_read_b128 a[252:255], v244 offset:38912\n"
    "v_mfma_f32_16x16x32_bf16 v[124:127], a[44:47], v[188:191], v[124:127]\n"
    "s_waitcnt lgkmcnt(7)\n"
    "ds_read_u16_d16_hi v200, v200\n"
    "v_or_b32_e32 v195, v195, v203\n"
    "v_mfma_f32_16x16x32_bf16 v[132:135], v[164:167], v[184:187], v[132:135]\n"
    "v_lshlrev_b32_sdwa v184, 1, v168 dst_sel:DWORD dst_unused:UNUSED_PAD src0_sel:DWORD src1_sel:WORD_1\n"
    "v_lshlrev_b32_sdwa v185, 1, v170 dst_sel:DWORD dst_unused:UNUSED_PAD src0_sel:DWORD src1_sel:WORD_1\n"
    "v_mfma_f32_16x16x32_bf16 v[136:139], v[236:239], v[188:191], v[136:139]\n"
    "ds_read_u16 v184, v184\n"
    "v_lshlrev_b32_sdwa v186, 1, v172 dst_sel:DWORD dst_unused:UNUSED_PAD src0_sel:DWORD src1_sel:WORD_1\n"
    "v_mfma_f32_16x16x32_bf16 v[140:143], v[164:167], v[188:191], v[140:143]\n"
    "ds_read_u16 v185, v185\n"
    "v_lshlrev_b32_sdwa v187, 1, v174 dst_sel:DWORD dst_unused:UNUSED_PAD src0_sel:DWORD src1_sel:WORD_1\n"
    "v_mfma_f32_16x16x32_bf16 v[168:171], a[120:123], a[224:227], 0\n"
    "s_waitcnt lgkmcnt(8)\n"
    "v_or_b32_e32 v197, v197, v205\n"
    "v_lshlrev_b32_sdwa v203, 1, v175 dst_sel:DWORD dst_unused:UNUSED_PAD src0_sel:DWORD src1_sel:WORD_1\n"
    "v_mfma_f32_16x16x32_bf16 v[168:171], a[168:171], a[228:231], v[168:171]\n"
    "ds_read_u16_d16_hi v201, v201\n"
    "v_lshlrev_b32_sdwa v188, 1, v176 dst_sel:DWORD dst_unused:UNUSED_PAD src0_sel:DWORD src1_sel:WORD_1\n"
    "v_mfma_f32_16x16x32_bf16 v[168:171], a[76:79], a[232:235], v[168:171]\n"
    "v_lshlrev_b32_sdwa v189, 1, v178 dst_sel:DWORD dst_unused:UNUSED_PAD src0_sel:DWORD src1_sel:WORD_1\n"
    "v_lshlrev_b32_sdwa v205, 1, v179 dst_sel:DWORD dst_unused:UNUSED_PAD src0_sel:DWORD src1_sel:WORD_1\n"
    "v_mfma_f32_16x16x32_bf16 v[172:175], a[124:127], a[224:227], 0\n"
    "ds_read_u16 v186, v186\n"
    "v_lshlrev_b32_sdwa v190, 1, v180 dst_sel:DWORD dst_unused:UNUSED_PAD src0_sel:DWORD src1_sel:WORD_1\n"
    "v_mfma_f32_16x16x32_bf16 v[172:175], a[172:175], a[228:231], v[172:175]\n"
    "s_waitcnt lgkmcnt(8)\n"
    "v_or_b32_e32 v198, v198, v206\n"
    "v_lshlrev_b32_sdwa v206, 1, v181 dst_sel:DWORD dst_unused:UNUSED_PAD src0_sel:DWORD src1_sel:WORD_1\n"
    "v_lshlrev_b32_sdwa v191, 1, v182 dst_sel:DWORD dst_unused:UNUSED_PAD src0_sel:DWORD src1_sel:WORD_1\n"
    "v_fma_mix_f32 v234, v192, v232, 0 op_sel_hi:[1,1,0]\n"
    "v_fma_mix_f32 v232, v192, v232, 0 op_sel:[1,1,0] op_sel_hi:[1,1,0]\n"
    "v_mfma_f32_16x16x32_bf16 v[176:179], a[120:123], a[236:239], 0\n"
    "ds_read_b128 v[236:239], v244 offset:12288\n"
    "ds_read_u16_d16_hi v202, v202\n"
    "v_cvt_pk_bf16_f32 v192, v234, v232\n"
    "v_mfma_f32_16x16x32_bf16 v[176:179], a[168:171], a[240:243], v[176:179]\n"
    "s_waitcnt lgkmcnt(7)\n"
    "ds_read_u16 v187, v187\n"
    "v_or_b32_e32 v199, v199, v207\n"
    "v_mfma_f32_16x16x32_bf16 v[172:175], a[252:255], a[232:235], v[172:175]\n"
    "v_lshlrev_b32_sdwa v207, 1, v183 dst_sel:DWORD dst_unused:UNUSED_PAD src0_sel:DWORD src1_sel:WORD_1\n"
    "v_pk_fma_f16 v232, v196, v196, s48\n"
    "v_mfma_f32_16x16x32_bf16 v[176:179], a[76:79], a[244:247], v[176:179]\n"
    "ds_read_b128 v[164:167], v244 offset:14336\n"
    "v_pk_mul_f16 v232, v196, v232\n"
    "v_mfma_f32_16x16x32_bf16 v[180:183], a[124:127], a[236:239], 0\n"
    "ds_read_u16_d16_hi v203, v203\n"
    "v_exp_f16_e32 v234, v232\n"
    "v_mfma_f32_16x16x32_bf16 v[180:183], a[172:175], a[240:243], v[180:183]\n"
    "s_waitcnt lgkmcnt(8)\n"
    "v_exp_f16_sdwa v234, v232 dst_sel:WORD_1 dst_unused:UNUSED_PRESERVE src0_sel:WORD_1\n"
    "v_mfma_f32_16x16x32_bf16 v[180:183], a[252:255], a[244:247], v[180:183]\n"
    "ds_read_u16 v188, v188\n"
    "buffer_load_dwordx4 v[152:155], v245, s[40:43], s57 offen offset:64\n"
    "buffer_load_dwordx4 v[144:147], v245, s[40:43], s57 offen offset:0\n"
    "v_mfma_f32_16x16x32_bf16 v[96:99], a[176:179], v[192:195], v[96:99]\n"
    "ds_read_u16_d16_hi v204, v204\n"
    "v_pk_fma_f16 v234, v234, s49, s49\n"
    "v_mfma_f32_16x16x32_bf16 v[100:103], a[180:183], v[192:195], v[100:103]\n"
    "v_rcp_f16_e32 v232, v234\n"
    "v_mfma_f32_16x16x32_bf16 v[112:115], a[184:187], v[192:195], v[112:115]\n"
    "s_waitcnt lgkmcnt(8)\n"
    "ds_read_u16 v189, v189\n"
    "v_rcp_f16_sdwa v232, v234 dst_sel:WORD_1 dst_unused:UNUSED_PRESERVE src0_sel:WORD_1\n"
    "v_mfma_f32_16x16x32_bf16 v[116:119], a[188:191], v[192:195], v[116:119]\n"
    "ds_read_u16_d16_hi v205, v205\n"
    "v_fma_mix_f32 v234, v196, v232, 0 op_sel_hi:[1,1,0]\n"
    "s_waitcnt lgkmcnt(8)\n"
    "v_mfma_f32_16x16x32_bf16 v[128:131], v[236:239], v[192:195], v[128:131]\n"
    "s_nop 0\n"
    "v_fma_mix_f32 v232, v196, v232, 0 op_sel:[1,1,0] op_sel_hi:[1,1,0]\n"
    "v_or_b32_e64 v184, v184, v200\n"
    "v_cvt_pk_bf16_f32 v196, v234, v232\n"
    "v_or_b32_e32 v185, v185, v201\n"
    "v_lshlrev_b32_sdwa v201, 1, v171 dst_sel:DWORD dst_unused:UNUSED_PAD src0_sel:DWORD src1_sel:WORD_1\n"
    "v_mfma_f32_16x16x32_bf16 v[104:107], a[176:179], v[196:199], v[104:107]\n"
    "s_waitcnt lgkmcnt(7)\n"
    "ds_read_b128 a[224:227], v243 offset:0\n"
    "ds_read_u16 v190, v190\n"
    "v_mfma_f32_16x16x32_bf16 v[108:111], a[180:183], v[196:199], v[108:111]\n"
    "s_waitcnt lgkmcnt(7)\n"
    "v_or_b32_e32 v186, v186, v202\n"
    "v_lshlrev_b32_sdwa v202, 1, v173 dst_sel:DWORD dst_unused:UNUSED_PAD src0_sel:DWORD src1_sel:WORD_1\n"
    "v_mfma_f32_16x16x32_bf16 v[132:135], v[164:167], v[192:195], v[132:135]\n"
    "ds_read_b128 a[228:231], v243 offset:1024\n"
    "v_cvt_pk_f16_f32 v192, v168, v169\n"
    "v_mfma_f32_16x16x32_bf16 v[120:123], a[184:187], v[196:199], v[120:123]\n"
    "s_nop 0\n"
    "ds_read_u16_d16_hi v206, v206\n"
    "v_lshlrev_b32_sdwa v193, 1, v170 dst_sel:DWORD dst_unused:UNUSED_PAD src0_sel:DWORD src1_sel:WORD_1\n"
    "v_mfma_f32_16x16x32_bf16 v[124:127], a[188:191], v[196:199], v[124:127]\n"
    "s_waitcnt lgkmcnt(8)\n"
    "v_lshlrev_b32_sdwa v194, 1, v172 dst_sel:DWORD dst_unused:UNUSED_PAD src0_sel:DWORD src1_sel:WORD_1\n"
    "v_lshlrev_b32_sdwa v195, 1, v174 dst_sel:DWORD dst_unused:UNUSED_PAD src0_sel:DWORD src1_sel:WORD_1\n"
    "v_mfma_f32_16x16x32_bf16 v[136:139], v[236:239], v[196:199], v[136:139]\n"
    "ds_read_b128 a[232:235], v243 offset:2048\n"
    "ds_read_u16 v191, v191\n"
    "v_mfma_f32_16x16x32_bf16 v[140:143], v[164:167], v[196:199], v[140:143]\n"
    "v_cvt_pk_f16_f32 v196, v176, v177\n"
    "v_lshlrev_b32_sdwa v197, 1, v178 dst_sel:DWORD dst_unused:UNUSED_PAD src0_sel:DWORD src1_sel:WORD_1\n"
    "v_lshlrev_b32_sdwa v198, 1, v180 dst_sel:DWORD dst_unused:UNUSED_PAD src0_sel:DWORD src1_sel:WORD_1\n"
    "v_lshlrev_b32_sdwa v199, 1, v182 dst_sel:DWORD dst_unused:UNUSED_PAD src0_sel:DWORD src1_sel:WORD_1\n"
    "v_or_b32_e64 v187, v187, v203\n"
    "v_lshlrev_b32_sdwa v203, 1, v175 dst_sel:DWORD dst_unused:UNUSED_PAD src0_sel:DWORD src1_sel:WORD_1\n"
    "s_waitcnt lgkmcnt(8)\n"
    "v_or_b32_e32 v188, v188, v204\n"
    "buffer_load_dwordx4 v[156:159], v245, s[40:43], s88 offen offset:64\n"
    "buffer_load_dwordx4 v[148:151], v245, s[40:43], s88 offen offset:0\n"
    "ds_read_b128 a[236:239], v243 offset:3072\n"
    "ds_read_u16_d16_hi v207, v207\n"
    "v_pk_fma_f16 v232, v192, v192, s48\n"
    "ds_read_b128 a[240:243], v243 offset:4096\n"
    "v_pk_mul_f16 v232, v192, v232\n"
    "s_waitcnt lgkmcnt(8)\n"
    "v_mfma_f32_16x16x32_bf16 v[168:171], a[80:83], a[224:227], 0\n"
    "s_nop 0\n"
    "ds_read_b128 a[244:247], v243 offset:5120\n"
    "ds_read_b128 a[252:255], v244 offset:32768\n"
    "v_mfma_f32_16x16x32_bf16 v[172:175], a[84:87], a[224:227], 0\n"
    "v_or_b32_e64 v189, v189, v205\n"
    "v_lshlrev_b32_sdwa v205, 1, v179 dst_sel:DWORD dst_unused:UNUSED_PAD src0_sel:DWORD src1_sel:WORD_1\n"
    "v_exp_f16_e32 v234, v232\n"
    "s_waitcnt lgkmcnt(8)\n"
    "v_mfma_f32_16x16x32_bf16 v[168:171], a[128:131], a[228:231], v[168:171]\n"
    "ds_read_u16 v193, v193\n"
    "ds_read_u16_d16_hi v201, v201\n"
    "v_mfma_f32_16x16x32_bf16 v[172:175], a[132:135], a[228:231], v[172:175]\n"
    "v_exp_f16_sdwa v234, v232 dst_sel:WORD_1 dst_unused:UNUSED_PRESERVE src0_sel:WORD_1\n"
    "ds_read_u16 v194, v194\n"
    "s_waitcnt lgkmcnt(9)\n"
    "v_mfma_f32_16x16x32_bf16 v[168:171], a[48:51], a[232:235], v[168:171]\n"
    "v_or_b32_e32 v190, v190, v206\n"
    "v_lshlrev_b32_sdwa v206, 1, v181 dst_sel:DWORD dst_unused:UNUSED_PAD src0_sel:DWORD src1_sel:WORD_1\n"
    "v_mfma_f32_16x16x32_bf16 v[96:99], a[192:195], v[184:187], v[96:99]\n"
    "ds_read_u16_d16_hi v202, v202\n"
    "v_pk_fma_f16 v234, v234, s49, s49\n"
    "v_mfma_f32_16x16x32_bf16 v[100:103], a[196:199], v[184:187], v[100:103]\n"
    "s_waitcnt lgkmcnt(8)\n"
    "v_rcp_f16_e32 v232, v234\n"
    "v_mfma_f32_16x16x32_bf16 v[176:179], a[80:83], a[236:239], 0\n"
    "ds_read_b128 v[236:239], v244 offset:16384\n"
    "v_lshlrev_b32_sdwa v200, 1, v169 dst_sel:DWORD dst_unused:UNUSED_PAD src0_sel:DWORD src1_sel:WORD_1\n"
    "v_mfma_f32_16x16x32_bf16 v[112:115], a[200:203], v[184:187], v[112:115]\n"
    "ds_read_u16 v195, v195\n"
    "v_rcp_f16_sdwa v232, v234 dst_sel:WORD_1 dst_unused:UNUSED_PRESERVE src0_sel:WORD_1\n"
    "s_waitcnt lgkmcnt(8)\n"
    "v_mfma_f32_16x16x32_bf16 v[176:179], a[128:131], a[240:243], v[176:179]\n"
    "s_waitcnt lgkmcnt(7)\n"
    "ds_read_u16_d16_hi v203, v203\n"
    "v_or_b32_e32 v191, v191, v207\n"
    "v_mfma_f32_16x16x32_bf16 v[176:179], a[48:51], a[244:247], v[176:179]\n"
    "s_waitcnt lgkmcnt(7)\n"
    "v_lshlrev_b32_sdwa v207, 1, v183 dst_sel:DWORD dst_unused:UNUSED_PAD src0_sel:DWORD src1_sel:WORD_1\n"
    "v_fma_mix_f32 v234, v192, v232, 0 op_sel_hi:[1,1,0]\n"
    "v_mfma_f32_16x16x32_bf16 v[172:175], a[252:255], a[232:235], v[172:175]\n"
    "ds_read_b128 v[164:167], v244 offset:18432\n"
    "v_fma_mix_f32 v232, v192, v232, 0 op_sel:[1,1,0] op_sel_hi:[1,1,0]\n"
    "v_mfma_f32_16x16x32_bf16 v[180:183], a[84:87], a[236:239], 0\n"
    "ds_read_u16 v197, v197\n"
    "v_lshlrev_b32_sdwa v204, 1, v177 dst_sel:DWORD dst_unused:UNUSED_PAD src0_sel:DWORD src1_sel:WORD_1\n"
    "v_mfma_f32_16x16x32_bf16 v[180:183], a[132:135], a[240:243], v[180:183]\n"
    "v_cvt_pk_bf16_f32 v192, v234, v232\n"
    "v_pk_fma_f16 v232, v196, v196, s48\n"
    "v_mfma_f32_16x16x32_bf16 v[180:183], a[252:255], a[244:247], v[180:183]\n"
    "ds_read_u16_d16_hi v205, v205\n"
    "v_pk_mul_f16 v232, v196, v232\n"
    "v_mfma_f32_16x16x32_bf16 v[116:119], a[204:207], v[184:187], v[116:119]\n"
    "s_waitcnt lgkmcnt(8)\n"
    "ds_read_u16 v198, v198\n"
    "v_or_b32_e32 v193, v193, v201\n"
    "v_mfma_f32_16x16x32_bf16 v[104:107], a[192:195], v[188:191], v[104:107]\n"
    "v_lshlrev_b32_sdwa v201, 1, v171 dst_sel:DWORD dst_unused:UNUSED_PAD src0_sel:DWORD src1_sel:WORD_1\n"
    "v_exp_f16_e64 v234, v232\n"
    "v_mfma_f32_16x16x32_bf16 v[108:111], a[196:199], v[188:191], v[108:111]\n"
    "ds_read_u16_d16_hi v206, v206\n"
    "buffer_store_dwordx4 v[208:211], v245, s[68:71], s59 offen offset:0 sc1\n"
    "v_mfma_f32_16x16x32_bf16 v[120:123], a[200:203], v[188:191], v[120:123]\n"
    "s_waitcnt lgkmcnt(7)\n"
    "ds_read_u16 v199, v199\n"
    "v_or_b32_e32 v194, v194, v202\n"
    "v_mfma_f32_16x16x32_bf16 v[128:131], v[236:239], v[184:187], v[128:131]\n"
    "ds_read_u16_d16_hi v207, v207\n"
    "v_lshlrev_b32_sdwa v202, 1, v173 dst_sel:DWORD dst_unused:UNUSED_PAD src0_sel:DWORD src1_sel:WORD_1\n"
    "v_mfma_f32_16x16x32_bf16 v[124:127], a[204:207], v[188:191], v[124:127]\n"
    "v_exp_f16_sdwa v234, v232 dst_sel:WORD_1 dst_unused:UNUSED_PRESERVE src0_sel:WORD_1\n"
    "v_mfma_f32_16x16x32_bf16 v[136:139], v[236:239], v[188:191], v[136:139]\n"
    "ds_read_b128 a[252:255], v244 offset:36864\n"
    "v_pk_fma_f16 v234, v234, s49, s49\n"
    "s_waitcnt lgkmcnt(7)\n"
    "v_mfma_f32_16x16x32_bf16 v[132:135], v[164:167], v[184:187], v[132:135]\n"
    "s_nop 0\n"
    "ds_read_u16_d16_hi v200, v200\n"
    "v_or_b32_e64 v195, v195, v203\n"
    "v_mfma_f32_16x16x32_bf16 v[140:143], v[164:167], v[188:191], v[140:143]\n"
    "v_lshlrev_b32_sdwa v184, 1, v168 dst_sel:DWORD dst_unused:UNUSED_PAD src0_sel:DWORD src1_sel:WORD_1\n"
    "v_lshlrev_b32_sdwa v185, 1, v170 dst_sel:DWORD dst_unused:UNUSED_PAD src0_sel:DWORD src1_sel:WORD_1\n"
    "v_mfma_f32_16x16x32_bf16 v[168:171], a[88:91], a[224:227], 0\n"
    "ds_read_u16 v184, v184\n"
    "v_lshlrev_b32_sdwa v186, 1, v172 dst_sel:DWORD dst_unused:UNUSED_PAD src0_sel:DWORD src1_sel:WORD_1\n"
    "v_mfma_f32_16x16x32_bf16 v[168:171], a[136:139], a[228:231], v[168:171]\n"
    "ds_read_u16 v185, v185\n"
    "v_lshlrev_b32_sdwa v187, 1, v174 dst_sel:DWORD dst_unused:UNUSED_PAD src0_sel:DWORD src1_sel:WORD_1\n"
    "2:\n"
    "v_mfma_f32_16x16x32_bf16 v[168:171], a[52:55], a[232:235], v[168:171]\n"
    "s_waitcnt lgkmcnt(8)\n"
    "v_or_b32_e32 v197, v197, v205\n"
    "v_lshlrev_b32_sdwa v203, 1, v175 dst_sel:DWORD dst_unused:UNUSED_PAD src0_sel:DWORD src1_sel:WORD_1\n"
    "v_mfma_f32_16x16x32_bf16 v[172:175], a[92:95], a[224:227], 0\n"
    "ds_read_u16_d16_hi v201, v201\n"
    "v_lshlrev_b32_sdwa v188, 1, v176 dst_sel:DWORD dst_unused:UNUSED_PAD src0_sel:DWORD src1_sel:WORD_1\n"
    "v_mfma_f32_16x16x32_bf16 v[172:175], a[140:143], a[228:231], v[172:175]\n"
    "v_lshlrev_b32_sdwa v189, 1, v178 dst_sel:DWORD dst_unused:UNUSED_PAD src0_sel:DWORD src1_sel:WORD_1\n"
    "v_lshlrev_b32_sdwa v205, 1, v179 dst_sel:DWORD dst_unused:UNUSED_PAD src0_sel:DWORD src1_sel:WORD_1\n"
    "v_mfma_f32_16x16x32_bf16 v[176:179], a[88:91], a[236:239], 0\n"
    "ds_read_u16 v186, v186\n"
    "v_lshlrev_b32_sdwa v190, 1, v180 dst_sel:DWORD dst_unused:UNUSED_PAD src0_sel:DWORD src1_sel:WORD_1\n"
    "v_mfma_f32_16x16x32_bf16 v[176:179], a[136:139], a[240:243], v[176:179]\n"
    "s_waitcnt lgkmcnt(8)\n"
    "v_or_b32_e32 v198, v198, v206\n"
    "v_lshlrev_b32_sdwa v206, 1, v181 dst_sel:DWORD dst_unused:UNUSED_PAD src0_sel:DWORD src1_sel:WORD_1\n"
    "v_mfma_f32_16x16x32_bf16 v[176:179], a[52:55], a[244:247], v[176:179]\n"
    "ds_read_b128 v[236:239], v244 offset:20480\n"
    "ds_read_u16_d16_hi v202, v202\n"
    "v_mfma_f32_16x16x32_bf16 v[96:99], a[208:211], v[192:195], v[96:99]\n"
    "v_lshlrev_b32_sdwa v191, 1, v182 dst_sel:DWORD dst_unused:UNUSED_PAD src0_sel:DWORD src1_sel:WORD_1\n"
    "v_rcp_f16_e32 v232, v234\n"
    "v_mfma_f32_16x16x32_bf16 v[100:103], a[212:215], v[192:195], v[100:103]\n"
    "s_waitcnt lgkmcnt(7)\n"
    "ds_read_u16 v187, v187\n"
    "v_or_b32_e64 v199, v199, v207\n"
    "v_mfma_f32_16x16x32_bf16 v[172:175], a[252:255], a[232:235], v[172:175]\n"
    "v_lshlrev_b32_sdwa v207, 1, v183 dst_sel:DWORD dst_unused:UNUSED_PAD src0_sel:DWORD src1_sel:WORD_1\n"
    "v_rcp_f16_sdwa v232, v234 dst_sel:WORD_1 dst_unused:UNUSED_PRESERVE src0_sel:WORD_1\n"
    "v_mfma_f32_16x16x32_bf16 v[180:183], a[92:95], a[236:239], 0\n"
    "s_nop 0\n"
    "ds_read_b128 v[164:167], v244 offset:22528\n"
    "buffer_store_dwordx4 v[212:215], v245, s[68:71], s59 offen offset:64 sc1\n"
    "v_mfma_f32_16x16x32_bf16 v[180:183], a[140:143], a[240:243], v[180:183]\n"
    "s_waitcnt lgkmcnt(7)\n"
    "ds_read_u16_d16_hi v203, v203\n"
    "ds_read_u16 v188, v188\n"
    "v_mfma_f32_16x16x32_bf16 v[180:183], a[252:255], a[244:247], v[180:183]\n"
    "v_fma_mix_f32 v234, v196, v232, 0 op_sel_hi:[1,1,0]\n"
    "v_fma_mix_f32 v232, v196, v232, 0 op_sel:[1,1,0] op_sel_hi:[1,1,0]\n"
    "v_mfma_f32_16x16x32_bf16 v[112:115], a[216:219], v[192:195], v[112:115]\n"
    "ds_read_u16_d16_hi v204, v204\n"
    "v_cvt_pk_bf16_f32 v196, v234, v232\n"
    "v_mfma_f32_16x16x32_bf16 v[116:119], a[220:223], v[192:195], v[116:119]\n"
    "v_or_b32_e32 v184, v184, v200\n"
    "v_cvt_pk_bf16_f32 v96, v96, v97\n"
    "v_mfma_f32_16x16x32_bf16 v[104:107], a[208:211], v[196:199], v[104:107]\n"
    "s_waitcnt lgkmcnt(8)\n"
    "ds_read_u16 v189, v189\n"
    "v_or_b32_e32 v185, v185, v201\n"
    "v_mfma_f32_16x16x32_bf16 v[108:111], a[212:215], v[196:199], v[108:111]\n"
    "s_waitcnt lgkmcnt(7)\n"
    "v_lshlrev_b32_sdwa v201, 1, v171 dst_sel:DWORD dst_unused:UNUSED_PAD src0_sel:DWORD src1_sel:WORD_1\n"
    "v_cvt_pk_bf16_f32 v97, v98, v99\n"
    "v_mfma_f32_16x16x32_bf16 v[128:131], v[236:239], v[192:195], v[128:131]\n"
    "ds_read_u16_d16_hi v205, v205\n"
    "v_cvt_pk_bf16_f32 v98, v100, v101\n"
    "v_mfma_f32_16x16x32_bf16 v[120:123], a[216:219], v[196:199], v[120:123]\n"
    "ds_read_u16 v190, v190\n"
    "v_cvt_pk_bf16_f32 v99, v102, v103\n"
    "v_mfma_f32_16x16x32_bf16 v[124:127], a[220:223], v[196:199], v[124:127]\n"
    "s_waitcnt lgkmcnt(8)\n"
    "v_or_b32_e32 v186, v186, v202\n"
    "v_lshlrev_b32_sdwa v202, 1, v173 dst_sel:DWORD dst_unused:UNUSED_PAD src0_sel:DWORD src1_sel:WORD_1\n"
    "v_mfma_f32_16x16x32_bf16 v[136:139], v[236:239], v[196:199], v[136:139]\n"
    "v_cvt_pk_bf16_f32 v112, v112, v113\n"
    "v_cvt_pk_bf16_f32 v113, v114, v115\n"
    "s_waitcnt lgkmcnt(6)\n"
    "v_mfma_f32_16x16x32_bf16 v[132:135], v[164:167], v[192:195], v[132:135]\n"
    "ds_write_b128 v243, v[96:99] offset:0\n"
    "v_mfma_f32_16x16x32_bf16 v[140:143], v[164:167], v[196:199], v[140:143]\n"
    "v_cvt_pk_f16_f32 v192, v168, v169\n"
    "v_lshlrev_b32_sdwa v193, 1, v170 dst_sel:DWORD dst_unused:UNUSED_PAD src0_sel:DWORD src1_sel:WORD_1\n"
    "v_mfma_f32_16x16x32_bf16 v[168:171], a[96:99], a[224:227], 0\n"
    "s_waitcnt lgkmcnt(6)\n"
    "v_lshlrev_b32_sdwa v194, 1, v172 dst_sel:DWORD dst_unused:UNUSED_PAD src0_sel:DWORD src1_sel:WORD_1\n"
    "v_lshlrev_b32_sdwa v195, 1, v174 dst_sel:DWORD dst_unused:UNUSED_PAD src0_sel:DWORD src1_sel:WORD_1\n"
    "v_mfma_f32_16x16x32_bf16 v[168:171], a[144:147], a[228:231], v[168:171]\n"
    "ds_read_u16_d16_hi v206, v206\n"
    "ds_read_u16 v191, v191\n"
    "v_mfma_f32_16x16x32_bf16 v[168:171], a[56:59], a[232:235], v[168:171]\n"
    "ds_read_u16_d16_hi v207, v207\n"
    "v_cvt_pk_f16_f32 v196, v176, v177\n"
    "v_lshlrev_b32_sdwa v197, 1, v178 dst_sel:DWORD dst_unused:UNUSED_PAD src0_sel:DWORD src1_sel:WORD_1\n"
    "v_lshlrev_b32_sdwa v198, 1, v180 dst_sel:DWORD dst_unused:UNUSED_PAD src0_sel:DWORD src1_sel:WORD_1\n"
    "v_lshlrev_b32_sdwa v199, 1, v182 dst_sel:DWORD dst_unused:UNUSED_PAD src0_sel:DWORD src1_sel:WORD_1\n"
    "v_or_b32_e64 v187, v187, v203\n"
    "v_lshlrev_b32_sdwa v203, 1, v175 dst_sel:DWORD dst_unused:UNUSED_PAD src0_sel:DWORD src1_sel:WORD_1\n"
    "v_mfma_f32_16x16x32_bf16 v[172:175], a[100:103], a[224:227], 0\n"
    "s_waitcnt lgkmcnt(7)\n"
    "ds_read_u16 v193, v193\n"
    "v_or_b32_e32 v188, v188, v204\n"
    "v_mfma_f32_16x16x32_bf16 v[172:175], a[148:151], a[228:231], v[172:175]\n"
    "ds_read_u16_d16_hi v201, v201\n"
    "buffer_store_dwordx4 v[216:219], v245, s[68:71], s59 offen offset:128 sc1\n"
    "v_mfma_f32_16x16x32_bf16 v[172:175], a[60:63], a[232:235], v[172:175]\n"
    "s_waitcnt lgkmcnt(7)\n"
    "ds_read_u16 v194, v194\n"
    "v_or_b32_e32 v189, v189, v205\n"
    "v_lshlrev_b32_sdwa v205, 1, v179 dst_sel:DWORD dst_unused:UNUSED_PAD src0_sel:DWORD src1_sel:WORD_1\n"
    "v_mfma_f32_16x16x32_bf16 v[176:179], a[96:99], a[236:239], 0\n"
    "ds_read_u16_d16_hi v202, v202\n"
    "v_cvt_pk_bf16_f32 v114, v116, v117\n"
    "v_mfma_f32_16x16x32_bf16 v[176:179], a[144:147], a[240:243], v[176:179]\n"
    "v_cvt_pk_bf16_f32 v115, v118, v119\n"
    "v_cvt_pk_bf16_f32 v128, v128, v129\n"
    "v_mfma_f32_16x16x32_bf16 v[176:179], a[56:59], a[244:247], v[176:179]\n"
    "v_cvt_pk_bf16_f32 v129, v130, v131\n"
    "v_cvt_pk_bf16_f32 v130, v132, v133\n"
    "v_cvt_pk_bf16_f32 v131, v134, v135\n"
    "v_cvt_pk_bf16_f32 v104, v104, v105\n"
    "ds_write_b128 v243, v[112:115] offset:2048\n"
    "s_waitcnt lgkmcnt(7)\n"
    "v_or_b32_e32 v190, v190, v206\n"
    "v_mfma_f32_16x16x32_bf16 v[96:99], a[0:3], v[184:187], 0\n"
    "s_waitcnt vmcnt(24)\n"
    "v_lshlrev_b32_sdwa v206, 1, v181 dst_sel:DWORD dst_unused:UNUSED_PAD src0_sel:DWORD src1_sel:WORD_1\n"
    "v_cvt_pk_bf16_f32 v105, v106, v107\n"
    "v_mfma_f32_16x16x32_bf16 v[100:103], a[4:7], v[184:187], 0\n"
    "s_waitcnt lgkmcnt(5)\n"
    "ds_read_b128 v[236:239], v244 offset:0\n"
    "ds_write2_b32 v248, v0, v1 offset0:0 offset1:20\n"
    "v_mfma_f32_16x16x32_bf16 v[112:115], a[8:11], v[184:187], 0\n"
    "v_or_b32_e64 v191, v191, v207\n"
    "v_lshlrev_b32_sdwa v207, 1, v183 dst_sel:DWORD dst_unused:UNUSED_PAD src0_sel:DWORD src1_sel:WORD_1\n"
    "v_mfma_f32_16x16x32_bf16 v[180:183], a[100:103], a[236:239], 0\n"
    "s_nop 0\n"
    "ds_read_u16 v195, v195\n"
    "ds_write2_b32 v248, v2, v3 offset0:40 offset1:60\n"
    "v_mfma_f32_16x16x32_bf16 v[180:183], a[148:151], a[240:243], v[180:183]\n"
    "s_waitcnt lgkmcnt(7)\n"
    "v_add_u32_e32 v248, 1360, v248\n"
    "v_or_b32_e32 v193, v193, v201\n"
    "v_mfma_f32_16x16x32_bf16 v[180:183], a[60:63], a[244:247], v[180:183]\n"
    "s_waitcnt vmcnt(21)\n"
    "ds_read_u16_d16_hi v203, v203\n"
    "ds_write2_b32 v235, v20, v21 offset0:0 offset1:20\n"
    "v_mfma_f32_16x16x32_bf16 v[116:119], a[12:15], v[184:187], 0\n"
    "v_cvt_pk_bf16_f32 v106, v108, v109\n"
    "v_cvt_pk_bf16_f32 v107, v110, v111\n"
    "v_mfma_f32_16x16x32_bf16 v[108:111], a[4:7], v[188:191], 0\n"
    "s_waitcnt lgkmcnt(6)\n"
    "ds_read_b128 v[164:167], v244 offset:2048\n"
    "ds_write2_b32 v235, v22, v23 offset0:40 offset1:60\n"
    "v_add_u32_e32 v235, 1360, v235\n"
    "v_or_b32_e32 v194, v194, v202\n"
    "v_cvt_pk_bf16_f32 v120, v120, v121\n"
    "v_cvt_pk_bf16_f32 v121, v122, v123\n"
    "v_cvt_pk_bf16_f32 v122, v124, v125\n"
    "s_waitcnt vmcnt(20)\n"
    "ds_write2_b32 v248, v4, v5 offset0:0 offset1:20\n"
    "buffer_store_dwordx4 v[220:223], v245, s[68:71], s39 offen offset:0 sc1\n"
    "ds_write2_b32 v248, v6, v7 offset0:40 offset1:60\n"
    "v_add_u32_e32 v248, 1360, v248\n"
    "v_cvt_pk_bf16_f32 v123, v126, v127\n"
    "v_cvt_pk_bf16_f32 v136, v136, v137\n"
    "v_pk_fma_f16 v232, v192, v192, s48\n"
    "v_mfma_f32_16x16x32_bf16 v[124:127], a[12:15], v[188:191], 0\n"
    "s_waitcnt vmcnt(16)\n"
    "ds_read_u16 v197, v197\n"
    "ds_write2_b32 v235, v24, v25 offset0:0 offset1:20\n"
    "v_cvt_pk_bf16_f32 v137, v138, v139\n"
    "v_cvt_pk_bf16_f32 v138, v140, v141\n"
    "s_waitcnt lgkmcnt(6)\n"
    "v_or_b32_e32 v195, v195, v203\n"
    "s_waitcnt lgkmcnt(4)\n"
    "v_mfma_f32_16x16x32_bf16 v[132:135], v[164:167], v[184:187], 0\n"
    "s_nop 0\n"
    "ds_read_u16_d16_hi v205, v205\n"
    "ds_write2_b32 v235, v26, v27 offset0:40 offset1:60\n"
    "v_add_u32_e32 v235, -1360, v235\n"
    "v_cvt_pk_bf16_f32 v139, v142, v143\n"
    "v_pk_mul_f16 v232, v192, v232\n"
    "v_lshlrev_b32_sdwa v200, 1, v169 dst_sel:DWORD dst_unused:UNUSED_PAD src0_sel:DWORD src1_sel:WORD_1\n"
    "v_exp_f16_e64 v234, v232\n"
    "v_mfma_f32_16x16x32_bf16 v[140:143], v[164:167], v[188:191], 0\n"
    "ds_read_u16 v198, v198\n"
    "ds_write_b128 v243, v[128:131] offset:4096\n"
    "v_mfma_f32_16x16x32_bf16 v[128:131], v[236:239], v[184:187], 0\n"
    "v_exp_f16_sdwa v234, v232 dst_sel:WORD_1 dst_unused:UNUSED_PRESERVE src0_sel:WORD_1\n"
    "v_lshlrev_b32_sdwa v184, 1, v168 dst_sel:DWORD dst_unused:UNUSED_PAD src0_sel:DWORD src1_sel:WORD_1\n"
    "v_pk_fma_f16 v234, v234, s49, s49\n"
    "v_lshlrev_b32_sdwa v185, 1, v170 dst_sel:DWORD dst_unused:UNUSED_PAD src0_sel:DWORD src1_sel:WORD_1\n"
    "v_lshlrev_b32_sdwa v201, 1, v171 dst_sel:DWORD dst_unused:UNUSED_PAD src0_sel:DWORD src1_sel:WORD_1\n"
    "v_mfma_f32_16x16x32_bf16 v[168:171], a[104:107], a[224:227], 0\n"
    "ds_read_u16_d16_hi v206, v206\n"
    "ds_write_b128 v243, v[104:107] offset:1024\n"
    "v_mfma_f32_16x16x32_bf16 v[104:107], a[0:3], v[188:191], 0\n"
    "v_lshlrev_b32_sdwa v186, 1, v172 dst_sel:DWORD dst_unused:UNUSED_PAD src0_sel:DWORD src1_sel:WORD_1\n"
    "v_lshlrev_b32_sdwa v202, 1, v173 dst_sel:DWORD dst_unused:UNUSED_PAD src0_sel:DWORD src1_sel:WORD_1\n"
    "v_mfma_f32_16x16x32_bf16 v[168:171], a[152:155], a[228:231], v[168:171]\n"
    "s_waitcnt lgkmcnt(4)\n"
    "v_or_b32_e32 v197, v197, v205\n"
    "v_lshlrev_b32_sdwa v187, 1, v174 dst_sel:DWORD dst_unused:UNUSED_PAD src0_sel:DWORD src1_sel:WORD_1\n"
    "v_lshlrev_b32_sdwa v203, 1, v175 dst_sel:DWORD dst_unused:UNUSED_PAD src0_sel:DWORD src1_sel:WORD_1\n"
    "s_waitcnt vmcnt(15)\n"
    "ds_write2_b32 v248, v8, v9 offset0:0 offset1:20\n"
    "v_rcp_f16_e32 v232, v234\n"
    "v_mfma_f32_16x16x32_bf16 v[168:171], a[64:67], a[232:235], v[168:171]\n"
    "ds_read_u16 v199, v199\n"
    "buffer_store_dwordx4 v[224:227], v245, s[68:71], s39 offen offset:64 sc1\n"
    "ds_write_b128 v243, v[120:123] offset:3072\n"
    "v_mfma_f32_16x16x32_bf16 v[120:123], a[8:11], v[188:191], 0\n"
    "v_rcp_f16_sdwa v232, v234 dst_sel:WORD_1 dst_unused:UNUSED_PRESERVE src0_sel:WORD_1\n"
    "v_mfma_f32_16x16x32_bf16 v[172:175], a[108:111], a[224:227], 0\n"
    "s_waitcnt lgkmcnt(3)\n"
    "v_or_b32_e32 v198, v198, v206\n"
    "v_lshlrev_b32_sdwa v204, 1, v177 dst_sel:DWORD dst_unused:UNUSED_PAD src0_sel:DWORD src1_sel:WORD_1\n"
    "v_mfma_f32_16x16x32_bf16 v[172:175], a[156:159], a[228:231], v[172:175]\n"
    "ds_read_u16_d16_hi v207, v207\n"
    "ds_write2_b32 v248, v10, v11 offset0:40 offset1:60\n"
    "v_add_u32_e32 v248, 1360, v248\n"
    "v_lshlrev_b32_sdwa v205, 1, v179 dst_sel:DWORD dst_unused:UNUSED_PAD src0_sel:DWORD src1_sel:WORD_1\n"
    "v_lshlrev_b32_sdwa v206, 1, v181 dst_sel:DWORD dst_unused:UNUSED_PAD src0_sel:DWORD src1_sel:WORD_1\n"
    "v_fma_mix_f32 v234, v192, v232, 0 op_sel_hi:[1,1,0]\n"
    "v_mfma_f32_16x16x32_bf16 v[172:175], a[68:71], a[232:235], v[172:175]\n"
    "s_waitcnt vmcnt(14)\n"
    "ds_read_b128 v[216:219], v251 offset:0\n"
    "ds_write_b128 v243, v[136:139] offset:5120\n"
    "v_mfma_f32_16x16x32_bf16 v[136:139], v[236:239], v[188:191], 0\n"
    "s_nop 0\n"
    "v_lshlrev_b32_sdwa v188, 1, v176 dst_sel:DWORD dst_unused:UNUSED_PAD src0_sel:DWORD src1_sel:WORD_1\n"
    "v_lshlrev_b32_sdwa v189, 1, v178 dst_sel:DWORD dst_unused:UNUSED_PAD src0_sel:DWORD src1_sel:WORD_1\n"
    "v_lshlrev_b32_sdwa v190, 1, v180 dst_sel:DWORD dst_unused:UNUSED_PAD src0_sel:DWORD src1_sel:WORD_1\n"
    "v_lshlrev_b32_sdwa v191, 1, v182 dst_sel:DWORD dst_unused:UNUSED_PAD src0_sel:DWORD src1_sel:WORD_1\n"
    "v_fma_mix_f32 v232, v192, v232, 0 op_sel:[1,1,0] op_sel_hi:[1,1,0]\n"
    "v_mfma_f32_16x16x32_bf16 v[176:179], a[104:107], a[236:239], 0\n"
    "s_nop 0\n"
    "ds_read_b128 v[220:223], v251 offset:336\n"
    "s_waitcnt vmcnt(13)\n"
    "ds_write2_b32 v248, v12, v13 offset0:0 offset1:20\n"
    "v_cvt_pk_bf16_f32 v192, v234, v232\n"
    "v_mfma_f32_16x16x32_bf16 v[176:179], a[152:155], a[240:243], v[176:179]\n"
    "v_pk_fma_f16 v232, v196, v196, s48\n"
    "v_pk_mul_f16 v232, v196, v232\n"
    "v_mfma_f32_16x16x32_bf16 v[176:179], a[64:67], a[244:247], v[176:179]\n"
    "ds_read_u16 v184, v184\n"
    "ds_write2_b32 v248, v14, v15 offset0:40 offset1:60\n"
    "v_add_u32_e32 v248, 1360, v248\n"
    "s_waitcnt lgkmcnt(6)\n"
    "v_or_b32_e32 v199, v199, v207\n"
    "v_lshlrev_b32_sdwa v207, 1, v183 dst_sel:DWORD dst_unused:UNUSED_PAD src0_sel:DWORD src1_sel:WORD_1\n"
    "v_mfma_f32_16x16x32_bf16 v[180:183], a[108:111], a[236:239], 0\n"
    "s_waitcnt vmcnt(11)\n"
    "ds_read_u16_d16_hi v200, v200\n"
    "s_waitcnt vmcnt(10)\n"
    "ds_write2_b32 v248, v16, v17 offset0:0 offset1:20\n"
    "v_mfma_f32_16x16x32_bf16 v[180:183], a[156:159], a[240:243], v[180:183]\n"
    "v_exp_f16_e64 v234, v232\n"
    "v_mfma_f32_16x16x32_bf16 v[180:183], a[68:71], a[244:247], v[180:183]\n"
    "ds_read_u16 v185, v185\n"
    "ds_write2_b32 v248, v18, v19 offset0:40 offset1:60\n"
    "v_mfma_f32_32x32x1_2b_f32 v[0:31], v254, v254, 0\n"
    "buffer_store_dwordx4 v[228:231], v245, s[68:71], s39 offen offset:128 sc1\n"
    "ds_read_u16_d16_hi v201, v201\n"
    "v_add_u32_e32 v248, -5440, v248\n"
    "v_exp_f16_sdwa v234, v232 dst_sel:WORD_1 dst_unused:UNUSED_PRESERVE src0_sel:WORD_1\n"
    "ds_read_u16 v186, v186\n"
    "ds_read_b128 v[236:239], v244 offset:4096\n"
    "v_pk_fma_f16 v234, v234, s49, s49\n"
    "v_mfma_f32_16x16x32_bf16 v[96:99], a[16:19], v[192:195], v[96:99]\n"
    "ds_read_u16_d16_hi v202, v202\n"
    "ds_read_u16 v187, v187\n"
    "v_mfma_f32_16x16x32_bf16 v[100:103], a[20:23], v[192:195], v[100:103]\n"
    "v_rcp_f16_e32 v232, v234\n"
    "v_mfma_f32_16x16x32_bf16 v[112:115], a[24:27], v[192:195], v[112:115]\n"
    "ds_read_b128 v[164:167], v244 offset:6144\n"
    "v_rcp_f16_sdwa v232, v234 dst_sel:WORD_1 dst_unused:UNUSED_PRESERVE src0_sel:WORD_1\n"
    "v_mfma_f32_16x16x32_bf16 v[116:119], a[28:31], v[192:195], v[116:119]\n"
    "s_waitcnt lgkmcnt(8)\n"
    "ds_read_u16_d16_hi v203, v203\n"
    "v_fma_mix_f32 v234, v196, v232, 0 op_sel_hi:[1,1,0]\n"
    "v_fma_mix_f32 v232, v196, v232, 0 op_sel:[1,1,0] op_sel_hi:[1,1,0]\n"
    "s_waitcnt lgkmcnt(7)\n"
    "s_barrier\n"
    "ds_read_b128 v[208:211], v250 offset:0\n"
    "ds_read_b128 v[212:215], v250 offset:320\n"
    "v_cvt_pk_bf16_f32 v196, v234, v232\n"
    "v_or_b32_e32 v184, v184, v200\n"
    "s_waitcnt lgkmcnt(8)\n"
    "v_or_b32_e32 v185, v185, v201\n"
    "v_mfma_f32_16x16x32_bf16 v[104:107], a[16:19], v[196:199], v[104:107]\n"
    "s_waitcnt lgkmcnt(6)\n"
    "ds_read_u16 v188, v188\n"
    "ds_read_u16_d16_hi v204, v204\n"
    "v_mfma_f32_16x16x32_bf16 v[128:131], v[236:239], v[192:195], v[128:131]\n"
    "ds_read_u16 v189, v189\n"
    "v_lshlrev_b32_sdwa v201, 1, v171 dst_sel:DWORD dst_unused:UNUSED_PAD src0_sel:DWORD src1_sel:WORD_1\n"
    "v_mfma_f32_16x16x32_bf16 v[108:111], a[20:23], v[196:199], v[108:111]\n"
    "s_waitcnt vmcnt(6)\n"
    "v_mfma_f32_16x16x32_bf16 v[120:123], a[24:27], v[196:199], v[120:123]\n"
    "s_waitcnt lgkmcnt(8)\n"
    "ds_read_u16_d16_hi v205, v205\n"
    "v_or_b32_e64 v186, v186, v202\n"
    "v_mfma_f32_16x16x32_bf16 v[124:127], a[28:31], v[196:199], v[124:127]\n"
    "ds_read_u16 v190, v190\n"
    "v_lshlrev_b32_sdwa v202, 1, v173 dst_sel:DWORD dst_unused:UNUSED_PAD src0_sel:DWORD src1_sel:WORD_1\n"
    "v_mfma_f32_16x16x32_bf16 v[136:139], v[236:239], v[196:199], v[136:139]\n"
    "s_waitcnt lgkmcnt(8)\n"
    "v_mfma_f32_16x16x32_bf16 v[132:135], v[164:167], v[192:195], v[132:135]\n"
    "s_nop 0\n"
    "ds_read_u16_d16_hi v206, v206\n"
    "v_cvt_pk_f16_f32 v192, v168, v169\n"
    "v_mfma_f32_16x16x32_bf16 v[140:143], v[164:167], v[196:199], v[140:143]\n"
    "s_nop 0\n"
    "v_lshlrev_b32_sdwa v193, 1, v170 dst_sel:DWORD dst_unused:UNUSED_PAD src0_sel:DWORD src1_sel:WORD_1\n"
    "v_lshlrev_b32_sdwa v194, 1, v172 dst_sel:DWORD dst_unused:UNUSED_PAD src0_sel:DWORD src1_sel:WORD_1\n"
    "v_smfmac_f32_32x32x32_bf16 v[112:127], a[248:251], v[152:159], v241\n"
    "s_waitcnt lgkmcnt(6)\n"
    "ds_read_u16 v191, v191\n"
    "v_lshlrev_b32_sdwa v195, 1, v174 dst_sel:DWORD dst_unused:UNUSED_PAD src0_sel:DWORD src1_sel:WORD_1\n"
    "v_cvt_pk_f16_f32 v196, v176, v177\n"
    "v_lshlrev_b32_sdwa v197, 1, v178 dst_sel:DWORD dst_unused:UNUSED_PAD src0_sel:DWORD src1_sel:WORD_1\n"
    "v_lshlrev_b32_sdwa v198, 1, v180 dst_sel:DWORD dst_unused:UNUSED_PAD src0_sel:DWORD src1_sel:WORD_1\n"
    "v_smfmac_f32_16x16x64_bf16 v[0:3], v[216:219], v[208:215], v240\n"
    "ds_read_b128 v[216:219], v252 offset:0\n"
    "ds_read_u16_d16_hi v207, v207\n"
    "v_smfmac_f32_16x16x64_bf16 v[4:7], v[220:223], v[208:215], v240 abid:1\n"
    "v_lshlrev_b32_sdwa v199, 1, v182 dst_sel:DWORD dst_unused:UNUSED_PAD src0_sel:DWORD src1_sel:WORD_1\n"
    "s_waitcnt vmcnt(5)\n"
    "v_smfmac_f32_32x32x32_bf16 v[96:111], a[248:251], v[144:151], v241\n"
    "s_nop 0\n"
    "ds_read_b128 v[208:211], v250 offset:640\n"
    "buffer_load_dwordx4 v[144:147], v245, s[40:43], s57 offen offset:128\n"
    "ds_read_b128 v[212:215], v250 offset:960\n"
    "v_or_b32_e64 v187, v187, v203\n"
    "v_lshlrev_b32_sdwa v203, 1, v175 dst_sel:DWORD dst_unused:UNUSED_PAD src0_sel:DWORD src1_sel:WORD_1\n"
    "v_mfma_f32_16x16x32_bf16 v[168:171], a[112:115], a[224:227], 0\n"
    "s_waitcnt lgkmcnt(9)\n"
    "ds_read_b128 v[220:223], v252 offset:336\n"
    "v_or_b32_e32 v188, v188, v204\n"
    "v_mfma_f32_16x16x32_bf16 v[168:171], a[160:163], a[228:231], v[168:171]\n"
    "ds_read_b128 a[252:255], v244 offset:34816\n"
    "v_pk_fma_f16 v232, v192, v192, s48\n"
    "v_mfma_f32_16x16x32_bf16 v[168:171], a[72:75], a[232:235], v[168:171]\n"
    "s_waitcnt lgkmcnt(9)\n"
    "ds_read_u16 v193, v193\n"
    "v_or_b32_e32 v189, v189, v205\n"
    "v_mfma_f32_16x16x32_bf16 v[172:175], a[116:119], a[224:227], 0\n"
    "v_lshlrev_b32_sdwa v205, 1, v179 dst_sel:DWORD dst_unused:UNUSED_PAD src0_sel:DWORD src1_sel:WORD_1\n"
    "v_pk_mul_f16 v232, v192, v232\n"
    "v_mfma_f32_16x16x32_bf16 v[172:175], a[164:167], a[228:231], v[172:175]\n"
    "ds_read_u16_d16_hi v201, v201\n"
    "v_exp_f16_e64 v234, v232\n"
    "v_mfma_f32_16x16x32_bf16 v[176:179], a[112:115], a[236:239], 0\n"
    "ds_read_u16 v194, v194\n"
    "v_exp_f16_sdwa v234, v232 dst_sel:WORD_1 dst_unused:UNUSED_PRESERVE src0_sel:WORD_1\n"
    "v_mfma_f32_16x16x32_bf16 v[176:179], a[160:163], a[240:243], v[176:179]\n"
    "s_waitcnt lgkmcnt(10)\n"
    "v_or_b32_e32 v190, v190, v206\n"
    "v_lshlrev_b32_sdwa v206, 1, v181 dst_sel:DWORD dst_unused:UNUSED_PAD src0_sel:DWORD src1_sel:WORD_1\n"
    "v_mfma_f32_16x16x32_bf16 v[176:179], a[72:75], a[244:247], v[176:179]\n"
    "ds_read_u16_d16_hi v202, v202\n"
    "v_pk_fma_f16 v234, v234, s49, s49\n"
    "v_mfma_f32_16x16x32_bf16 v[112:115], a[40:43], v[184:187], v[112:115]\n"
    "buffer_load_dwordx4 v[148:151], v245, s[40:43], s88 offen offset:128\n"
    "ds_read_b128 v[236:239], v244 offset:8192\n"
    "v_lshlrev_b32_sdwa v200, 1, v169 dst_sel:DWORD dst_unused:UNUSED_PAD src0_sel:DWORD src1_sel:WORD_1\n"
    "v_rcp_f16_e64 v232, v234\n"
    "ds_read_b128 v[152:155], v242 offset:0\n"
    "s_waitcnt lgkmcnt(9)\n"
    "v_or_b32_e32 v191, v191, v207\n"
    "v_mfma_f32_16x16x32_bf16 v[116:119], a[44:47], v[184:187], v[116:119]\n"
    "s_waitcnt lgkmcnt(8)\n"
    "ds_read_b128 v[156:159], v242 offset:1024\n"
    "ds_read_u16 v195, v195\n"
    "v_smfmac_f32_16x16x64_bf16 v[0:3], v[216:219], v[208:215], v240\n"
    "s_waitcnt lgkmcnt(8)\n"
    "v_lshlrev_b32_sdwa v207, 1, v183 dst_sel:DWORD dst_unused:UNUSED_PAD src0_sel:DWORD src1_sel:WORD_1\n"
    "v_smfmac_f32_16x16x64_bf16 v[4:7], v[220:223], v[208:215], v240 abid:1\n"
    "ds_read_b128 v[208:211], v250 offset:80\n"
    "v_mfma_f32_16x16x32_bf16 v[172:175], a[252:255], a[232:235], v[172:175]\n"
    "ds_read_b128 v[212:215], v250 offset:400\n"
    "v_rcp_f16_sdwa v232, v234 dst_sel:WORD_1 dst_unused:UNUSED_PRESERVE src0_sel:WORD_1\n"
    "v_mfma_f32_16x16x32_bf16 v[180:183], a[116:119], a[236:239], 0\n"
    "ds_read_b128 v[216:219], v251 offset:672\n"
    "v_lshlrev_b32_sdwa v204, 1, v177 dst_sel:DWORD dst_unused:UNUSED_PAD src0_sel:DWORD src1_sel:WORD_1\n"
    "v_fma_mix_f32 v234, v192, v232, 0 op_sel_hi:[1,1,0]\n"
    "ds_read_b128 v[220:223], v251 offset:1008\n"
    "v_pk_mov_b32 v[224:225], v[0:1], v[0:1] op_sel:[0,1]\n"
    "v_pk_mov_b32 v[226:227], v[2:3], v[2:3] op_sel:[0,1]\n"
    "v_pk_mul_f32 v[228:229], v[0:1], v[0:1]\n"
    "v_pk_mul_f32 v[230:231], v[2:3], v[2:3]\n"
    "v_pk_add_f32 v[224:225], v[224:225], v[4:5]\n"
    "v_pk_add_f32 v[226:227], v[226:227], v[6:7]\n"
    "v_pk_fma_f32 v[228:229], v[4:5], v[4:5], v[228:229]\n"
    "v_pk_fma_f32 v[230:231], v[6:7], v[6:7], v[230:231]\n"
    "v_mfma_f32_16x16x32_bf16 v[180:183], a[164:167], a[240:243], v[180:183]\n"
    "s_waitcnt lgkmcnt(9)\n"
    "ds_read_u16_d16_hi v203, v203\n"
    "ds_read_b128 v[164:167], v244 offset:10240\n"
    "v_mfma_f32_16x16x32_bf16 v[180:183], a[252:255], a[244:247], v[180:183]\n"
    "s_waitcnt lgkmcnt(9)\n"
    "ds_read_u16 v197, v197\n"
    "v_lshlrev_b32_e64 v254, 3, v244\n"
    "v_mfma_f32_16x16x32_bf16 v[128:131], v[236:239], v[184:187], v[128:131]\n"
    "v_or_b32_e64 v193, v193, v201\n"
    "v_or_b32_e32 v194, v194, v202\n"
    "v_mfma_f32_16x16x32_bf16 v[120:123], a[40:43], v[188:191], v[120:123]\n"
    "s_waitcnt lgkmcnt(8)\n"
    "ds_read_u16_d16_hi v205, v205\n"
    "v_lshlrev_b32_sdwa v201, 1, v171 dst_sel:DWORD dst_unused:UNUSED_PAD src0_sel:DWORD src1_sel:WORD_1\n"
    "v_smfmac_f32_32x32x32_bf16 v[96:111], a[248:251], v[152:159], v241\n"
    "ds_read_u16 v198, v198\n"
    "v_lshlrev_b32_sdwa v202, 1, v173 dst_sel:DWORD dst_unused:UNUSED_PAD src0_sel:DWORD src1_sel:WORD_1\n"
    "v_fma_mix_f32 v232, v192, v232, 0 op_sel:[1,1,0] op_sel_hi:[1,1,0]\n"
    "v_cvt_pk_bf16_f32 v192, v234, v232\n"
    "v_pk_fma_f16 v232, v196, v196, s48\n"
    "v_mfma_f32_16x16x32_bf16 v[124:127], a[44:47], v[188:191], v[124:127]\n"
    "ds_read_u16_d16_hi v206, v206\n"
    "v_pk_mul_f16 v232, v196, v232\n"
    "v_mfma_f32_16x16x32_bf16 v[136:139], v[236:239], v[188:191], v[136:139]\n"
    "v_exp_f16_e64 v234, v232\n"
    "v_mfma_f32_16x16x32_bf16 v[96:99], a[32:35], v[184:187], v[96:99]\n"
    "s_waitcnt lgkmcnt(7)\n"
    "ds_read_u16 v199, v199\n"
    "v_exp_f16_sdwa v234, v232 dst_sel:WORD_1 dst_unused:UNUSED_PRESERVE src0_sel:WORD_1\n"
    "v_smfmac_f32_16x16x64_bf16 v[8:11], v[216:219], v[208:215], v240\n"
    "s_waitcnt lgkmcnt(7)\n"
    "ds_read_b128 v[216:219], v252 offset:672\n"
    "v_smfmac_f32_16x16x64_bf16 v[12:15], v[220:223], v[208:215], v240 abid:1\n"
    "ds_read_b128 v[208:211], v250 offset:720\n"
    "v_mfma_f32_16x16x32_bf16 v[100:103], a[36:39], v[184:187], v[100:103]\n"
    "ds_read_b128 v[212:215], v250 offset:1040\n"
    "v_pk_fma_f16 v234, v234, s49, s49\n"
    "v_mfma_f32_16x16x32_bf16 v[104:107], a[32:35], v[188:191], v[104:107]\n"
    "s_waitcnt lgkmcnt(8)\n"
    "ds_read_b128 v[220:223], v252 offset:1008\n"
    "s_mov_b32 exec_hi, 0\n"
    "buffer_load_dword v233, v254, s[80:83], s84 offen sc1\n"
    "s_mov_b32 exec_hi, -1\n"
    "v_mfma_f32_16x16x32_bf16 v[132:135], v[164:167], v[184:187], v[132:135]\n"
    "s_nop 0\n"
    "ds_read_u16_d16_hi v207, v207\n"
    "v_or_b32_e64 v195, v195, v203\n"
    "v_mfma_f32_16x16x32_bf16 v[108:111], a[36:39], v[188:191], v[108:111]\n"
    "v_lshlrev_b32_sdwa v184, 1, v168 dst_sel:DWORD dst_unused:UNUSED_PAD src0_sel:DWORD src1_sel:WORD_1\n"
    "v_lshlrev_b32_sdwa v185, 1, v170 dst_sel:DWORD dst_unused:UNUSED_PAD src0_sel:DWORD src1_sel:WORD_1\n"
    "v_mfma_f32_16x16x32_bf16 v[140:143], v[164:167], v[188:191], v[140:143]\n"
    "ds_read_b128 v[152:155], v242 offset:2048\n"
    "ds_read_b128 v[156:159], v242 offset:3072\n"
    "v_mfma_f32_16x16x32_bf16 v[168:171], a[120:123], a[224:227], 0\n"
    "s_waitcnt lgkmcnt(10)\n"
    "v_or_b32_e32 v197, v197, v205\n"
    "v_lshlrev_b32_sdwa v186, 1, v172 dst_sel:DWORD dst_unused:UNUSED_PAD src0_sel:DWORD src1_sel:WORD_1\n"
    "v_mfma_f32_16x16x32_bf16 v[168:171], a[168:171], a[228:231], v[168:171]\n"
    "ds_read_b128 a[252:255], v244 offset:38912\n"
    "ds_read_u16 v184, v184\n"
    "v_mfma_f32_16x16x32_bf16 v[168:171], a[76:79], a[232:235], v[168:171]\n"
    "v_lshlrev_b32_sdwa v187, 1, v174 dst_sel:DWORD dst_unused:UNUSED_PAD src0_sel:DWORD src1_sel:WORD_1\n"
    "v_lshlrev_b32_sdwa v203, 1, v175 dst_sel:DWORD dst_unused:UNUSED_PAD src0_sel:DWORD src1_sel:WORD_1\n"
    "v_mfma_f32_16x16x32_bf16 v[172:175], a[124:127], a[224:227], 0\n"
    "s_waitcnt lgkmcnt(10)\n"
    "ds_read_u16_d16_hi v200, v200\n"
    "v_or_b32_e32 v198, v198, v206\n"
    "v_mfma_f32_16x16x32_bf16 v[172:175], a[172:175], a[228:231], v[172:175]\n"
    "v_lshlrev_b32_sdwa v188, 1, v176 dst_sel:DWORD dst_unused:UNUSED_PAD src0_sel:DWORD src1_sel:WORD_1\n"
    "v_lshlrev_b32_sdwa v189, 1, v178 dst_sel:DWORD dst_unused:UNUSED_PAD src0_sel:DWORD src1_sel:WORD_1\n"
    "v_mfma_f32_16x16x32_bf16 v[96:99], a[176:179], v[192:195], v[96:99]\n"
    "ds_read_u16 v185, v185\n"
    "v_lshlrev_b32_sdwa v205, 1, v179 dst_sel:DWORD dst_unused:UNUSED_PAD src0_sel:DWORD src1_sel:WORD_1\n"
    "v_mfma_f32_16x16x32_bf16 v[176:179], a[120:123], a[236:239], 0\n"
    "v_lshlrev_b32_sdwa v190, 1, v180 dst_sel:DWORD dst_unused:UNUSED_PAD src0_sel:DWORD src1_sel:WORD_1\n"
    "v_lshlrev_b32_sdwa v206, 1, v181 dst_sel:DWORD dst_unused:UNUSED_PAD src0_sel:DWORD src1_sel:WORD_1\n"
    "v_mfma_f32_16x16x32_bf16 v[176:179], a[168:171], a[240:243], v[176:179]\n"
    "s_waitcnt lgkmcnt(8)\n"
    "ds_read_u16_d16_hi v201, v201\n"
    "v_lshlrev_b32_sdwa v191, 1, v182 dst_sel:DWORD dst_unused:UNUSED_PAD src0_sel:DWORD src1_sel:WORD_1\n"
    "v_smfmac_f32_16x16x64_bf16 v[8:11], v[216:219], v[208:215], v240\n"
    "s_waitcnt lgkmcnt(8)\n"
    "ds_read_b128 v[216:219], v251 offset:1344\n"
    "v_smfmac_f32_16x16x64_bf16 v[12:15], v[220:223], v[208:215], v240 abid:1\n"
    "ds_read_b128 v[208:211], v250 offset:160\n"
    "v_mfma_f32_16x16x32_bf16 v[176:179], a[76:79], a[244:247], v[176:179]\n"
    "v_rcp_f16_e32 v232, v234\n"
    "v_mfma_f32_16x16x32_bf16 v[100:103], a[180:183], v[192:195], v[100:103]\n"
    "s_waitcnt lgkmcnt(8)\n"
    "ds_read_b128 v[212:215], v250 offset:480\n"
    "ds_read_b128 v[220:223], v251 offset:1680\n"
    "v_or_b32_e64 v199, v199, v207\n"
    "v_lshlrev_b32_sdwa v207, 1, v183 dst_sel:DWORD dst_unused:UNUSED_PAD src0_sel:DWORD src1_sel:WORD_1\n"
    "v_rcp_f16_sdwa v232, v234 dst_sel:WORD_1 dst_unused:UNUSED_PRESERVE src0_sel:WORD_1\n"
    "v_pk_add_f32 v[224:225], v[224:225], v[8:9]\n"
    "v_pk_add_f32 v[226:227], v[226:227], v[10:11]\n"
    "v_pk_fma_f32 v[228:229], v[8:9], v[8:9], v[228:229]\n"
    "v_pk_fma_f32 v[230:231], v[10:11], v[10:11], v[230:231]\n"
    "v_pk_add_f32 v[224:225], v[224:225], v[12:13]\n"
    "v_pk_add_f32 v[226:227], v[226:227], v[14:15]\n"
    "v_pk_fma_f32 v[228:229], v[12:13], v[12:13], v[228:229]\n"
    "v_pk_fma_f32 v[230:231], v[14:15], v[14:15], v[230:231]\n"
    "v_mfma_f32_16x16x32_bf16 v[180:183], a[124:127], a[236:239], 0\n"
    "s_waitcnt lgkmcnt(8)\n"
    "ds_read_u16 v186, v186\n"
    "ds_read_b128 v[236:239], v244 offset:12288\n"
    "v_smfmac_f32_32x32x32_bf16 v[112:127], a[248:251], v[152:159], v241\n"
    "s_nop 0\n"
    "ds_read_u16_d16_hi v202, v202\n"
    "v_fma_mix_f32 v234, v196, v232, 0 op_sel_hi:[1,1,0]\n"
    "v_fma_mix_f32 v232, v196, v232, 0 op_sel:[1,1,0] op_sel_hi:[1,1,0]\n"
    "v_cvt_pk_bf16_f32 v196, v234, v232\n"
    "ds_read_u16 v187, v187\n"
    "v_mfma_f32_16x16x32_bf16 v[172:175], a[252:255], a[232:235], v[172:175]\n"
    "s_waitcnt lgkmcnt(10)\n"
    "ds_read_b128 v[164:167], v244 offset:14336\n"
    "v_or_b32_e32 v184, v184, v200\n"
    "v_mfma_f32_16x16x32_bf16 v[180:183], a[172:175], a[240:243], v[180:183]\n"
    "ds_read_u16_d16_hi v203, v203\n"
    "v_mfma_f32_16x16x32_bf16 v[180:183], a[252:255], a[244:247], v[180:183]\n"
    "s_waitcnt lgkmcnt(9)\n"
    "v_or_b32_e32 v185, v185, v201\n"
    "v_lshlrev_b32_sdwa v201, 1, v171 dst_sel:DWORD dst_unused:UNUSED_PAD src0_sel:DWORD src1_sel:WORD_1\n"
    "v_mfma_f32_16x16x32_bf16 v[112:115], a[184:187], v[192:195], v[112:115]\n"
    "ds_read_u16 v188, v188\n"
    "v_mfma_f32_16x16x32_bf16 v[116:119], a[188:191], v[192:195], v[116:119]\n"
    "s_waitcnt lgkmcnt(8)\n"
    "v_smfmac_f32_16x16x64_bf16 v[16:19], v[216:219], v[208:215], v240\n"
    "s_waitcnt lgkmcnt(7)\n"
    "ds_read_b128 v[216:219], v252 offset:1344\n"
    "ds_read_u16_d16_hi v204, v204\n"
    "v_smfmac_f32_16x16x64_bf16 v[20:23], v[220:223], v[208:215], v240 abid:1\n"
    "v_mfma_f32_16x16x32_bf16 v[104:107], a[176:179], v[196:199], v[104:107]\n"
    "s_nop 0\n"
    "ds_read_b128 v[208:211], v250 offset:800\n"
    "v_mfma_f32_16x16x32_bf16 v[108:111], a[180:183], v[196:199], v[108:111]\n"
    "s_waitcnt lgkmcnt(8)\n"
    "ds_read_b128 v[212:215], v250 offset:1120\n"
    "v_mfma_f32_16x16x32_bf16 v[128:131], v[236:239], v[192:195], v[128:131]\n"
    "ds_read_b128 v[220:223], v252 offset:1680\n"
    "v_mfma_f32_16x16x32_bf16 v[120:123], a[184:187], v[196:199], v[120:123]\n"
    "s_nop 0\n"
    "ds_read_b128 v[152:155], v242 offset:4096\n"
    "v_mfma_f32_16x16x32_bf16 v[124:127], a[188:191], v[196:199], v[124:127]\n"
    "s_waitcnt lgkmcnt(10)\n"
    "ds_read_b128 v[156:159], v242 offset:5120\n"
    "v_or_b32_e32 v186, v186, v202\n"
    "v_mfma_f32_16x16x32_bf16 v[136:139], v[236:239], v[196:199], v[136:139]\n"
    "s_waitcnt lgkmcnt(9)\n"
    "ds_read_u16 v189, v189\n"
    "v_lshlrev_b32_sdwa v202, 1, v173 dst_sel:DWORD dst_unused:UNUSED_PAD src0_sel:DWORD src1_sel:WORD_1\n"
    "v_mfma_f32_16x16x32_bf16 v[132:135], v[164:167], v[192:195], v[132:135]\n"
    "v_cvt_pk_f16_f32 v192, v168, v169\n"
    "v_lshlrev_b32_sdwa v193, 1, v170 dst_sel:DWORD dst_unused:UNUSED_PAD src0_sel:DWORD src1_sel:WORD_1\n"
    "v_mfma_f32_16x16x32_bf16 v[140:143], v[164:167], v[196:199], v[140:143]\n"
    "ds_read_u16_d16_hi v205, v205\n"
    "v_lshlrev_b32_sdwa v194, 1, v172 dst_sel:DWORD dst_unused:UNUSED_PAD src0_sel:DWORD src1_sel:WORD_1\n"
    "v_lshlrev_b32_sdwa v195, 1, v174 dst_sel:DWORD dst_unused:UNUSED_PAD src0_sel:DWORD src1_sel:WORD_1\n"
    "v_cvt_pk_f16_f32 v196, v176, v177\n"
    "v_lshlrev_b32_sdwa v197, 1, v178 dst_sel:DWORD dst_unused:UNUSED_PAD src0_sel:DWORD src1_sel:WORD_1\n"
    "v_lshlrev_b32_sdwa v198, 1, v180 dst_sel:DWORD dst_unused:UNUSED_PAD src0_sel:DWORD src1_sel:WORD_1\n"
    "v_lshlrev_b32_sdwa v199, 1, v182 dst_sel:DWORD dst_unused:UNUSED_PAD src0_sel:DWORD src1_sel:WORD_1\n"
    "s_waitcnt lgkmcnt(10)\n"
    "v_or_b32_e32 v187, v187, v203\n"
    "ds_read_b128 a[224:227], v242 offset:24640\n"
    "ds_read_u16 v190, v190\n"
    "v_lshlrev_b32_sdwa v203, 1, v175 dst_sel:DWORD dst_unused:UNUSED_PAD src0_sel:DWORD src1_sel:WORD_1\n"
    "v_pk_fma_f16 v232, v192, v192, s48\n"
    "v_pk_mul_f16 v232, v192, v232\n"
    "v_exp_f16_e64 v234, v232\n"
    "ds_read_b128 a[228:231], v242 offset:25664\n"
    "ds_read_u16_d16_hi v206, v206\n"
    "s_waitcnt lgkmcnt(10)\n"
    "v_or_b32_e32 v188, v188, v204\n"
    "s_waitcnt lgkmcnt(9)\n"
    "v_smfmac_f32_16x16x64_bf16 v[16:19], v[216:219], v[208:215], v240\n"
    "s_waitcnt lgkmcnt(8)\n"
    "ds_read_b128 v[216:219], v251 offset:2016\n"
    "v_smfmac_f32_16x16x64_bf16 v[20:23], v[220:223], v[208:215], v240 abid:1\n"
    "ds_read_b128 v[208:211], v250 offset:240\n"
    "v_exp_f16_sdwa v234, v232 dst_sel:WORD_1 dst_unused:UNUSED_PRESERVE src0_sel:WORD_1\n"
    "ds_read_b128 v[212:215], v250 offset:560\n"
    "s_waitcnt lgkmcnt(9)\n"
    "v_smfmac_f32_32x32x32_bf16 v[128:143], a[248:251], v[152:159], v241\n"
    "s_nop 0\n"
    "ds_read_b128 v[220:223], v251 offset:2352\n"
    "v_pk_fma_f16 v234, v234, s49, s49\n"
    "ds_read_b128 a[232:235], v242 offset:26688\n"
    "v_lshlrev_b32_e64 v254, 3, v244\n"
    "ds_read_u16 v191, v191\n"
    "v_pk_add_f32 v[224:225], v[224:225], v[16:17]\n"
    "v_pk_add_f32 v[226:227], v[226:227], v[18:19]\n"
    "v_pk_fma_f32 v[228:229], v[16:17], v[16:17], v[228:229]\n"
    "v_pk_fma_f32 v[230:231], v[18:19], v[18:19], v[230:231]\n"
    "v_pk_add_f32 v[224:225], v[224:225], v[20:21]\n"
    "v_pk_add_f32 v[226:227], v[226:227], v[22:23]\n"
    "v_pk_fma_f32 v[228:229], v[20:21], v[20:21], v[228:229]\n"
    "v_pk_fma_f32 v[230:231], v[22:23], v[22:23], v[230:231]\n"
    "s_waitcnt lgkmcnt(9)\n"
    "v_mfma_f32_16x16x32_bf16 v[168:171], a[80:83], a[224:227], 0\n"
    "s_nop 0\n"
    "ds_read_b128 a[236:239], v242 offset:27712\n"
    "ds_read_u16_d16_hi v207, v207\n"
    "v_mfma_f32_16x16x32_bf16 v[172:175], a[84:87], a[224:227], 0\n"
    "s_waitcnt lgkmcnt(9)\n"
    "ds_read_b128 a[240:243], v242 offset:28736\n"
    "v_or_b32_e32 v189, v189, v205\n"
    "v_mfma_f32_16x16x32_bf16 v[168:171], a[128:131], a[228:231], v[168:171]\n"
    "ds_read_b128 a[244:247], v242 offset:29760\n"
    "v_lshlrev_b32_sdwa v205, 1, v179 dst_sel:DWORD dst_unused:UNUSED_PAD src0_sel:DWORD src1_sel:WORD_1\n"
    "v_mfma_f32_16x16x32_bf16 v[172:175], a[132:135], a[228:231], v[172:175]\n"
    "ds_read_b128 a[252:255], v244 offset:32768\n"
    "v_rcp_f16_e32 v232, v234\n"
    "s_waitcnt lgkmcnt(10)\n"
    "v_or_b32_e32 v190, v190, v206\n"
    "v_mfma_f32_16x16x32_bf16 v[96:99], a[192:195], v[184:187], v[96:99]\n"
    "ds_read_u16 v193, v193\n"
    "v_lshlrev_b32_sdwa v206, 1, v181 dst_sel:DWORD dst_unused:UNUSED_PAD src0_sel:DWORD src1_sel:WORD_1\n"
    "v_mfma_f32_16x16x32_bf16 v[100:103], a[196:199], v[184:187], v[100:103]\n"
    "s_waitcnt lgkmcnt(9)\n"
    "ds_read_u16_d16_hi v201, v201\n"
    "v_rcp_f16_sdwa v232, v234 dst_sel:WORD_1 dst_unused:UNUSED_PRESERVE src0_sel:WORD_1\n"
    "v_smfmac_f32_16x16x64_bf16 v[24:27], v[216:219], v[208:215], v240\n"
    "s_waitcnt lgkmcnt(9)\n"
    "s_mov_b32 exec_hi, 0\n"
    "buffer_load_dword v233, v254, s[72:75], s53 offen sc1\n"
    "s_mov_b32 exec_hi, -1\n"
    "v_smfmac_f32_16x16x64_bf16 v[28:31], v[220:223], v[208:215], v240 abid:1\n"
    "s_waitcnt lgkmcnt(8)\n"
    "ds_read_b128 v[208:211], v250 offset:880\n"
    "ds_read_b128 v[212:215], v250 offset:1200\n"
    "v_mfma_f32_16x16x32_bf16 v[168:171], a[48:51], a[232:235], v[168:171]\n"
    "s_nop 0\n"
    "ds_read_b128 v[216:219], v252 offset:2016\n"
    "v_fma_mix_f32 v234, v192, v232, 0 op_sel_hi:[1,1,0]\n"
    "v_mfma_f32_16x16x32_bf16 v[112:115], a[200:203], v[184:187], v[112:115]\n"
    "s_waitcnt lgkmcnt(9)\n"
    "ds_read_b128 v[220:223], v252 offset:2352\n"
    "v_fma_mix_f32 v232, v192, v232, 0 op_sel:[1,1,0] op_sel_hi:[1,1,0]\n"
    "v_mfma_f32_16x16x32_bf16 v[176:179], a[80:83], a[236:239], 0\n"
    "ds_read_u16 v194, v194\n"
    "v_lshlrev_b32_sdwa v200, 1, v169 dst_sel:DWORD dst_unused:UNUSED_PAD src0_sel:DWORD src1_sel:WORD_1\n"
    "v_mov_b32_e64 v254, 0\n"
    "v_cvt_pk_bf16_f32 v192, v234, v232\n"
    "v_pk_fma_f16 v232, v196, v196, s48\n"
    "s_waitcnt lgkmcnt(9)\n"
    "v_mfma_f32_16x16x32_bf16 v[176:179], a[128:131], a[240:243], v[176:179]\n"
    "s_nop 0\n"
    "ds_read_u16_d16_hi v202, v202\n"
    "v_or_b32_e64 v191, v191, v207\n"
    "v_mfma_f32_16x16x32_bf16 v[116:119], a[204:207], v[184:187], v[116:119]\n"
    "s_waitcnt lgkmcnt(9)\n"
    "v_lshlrev_b32_sdwa v207, 1, v183 dst_sel:DWORD dst_unused:UNUSED_PAD src0_sel:DWORD src1_sel:WORD_1\n"
    "v_pk_mul_f16 v232, v196, v232\n"
    "v_mfma_f32_16x16x32_bf16 v[176:179], a[48:51], a[244:247], v[176:179]\n"
    "s_waitcnt lgkmcnt(8)\n"
    "ds_read_b128 v[236:239], v244 offset:16384\n"
    "ds_read_u16 v195, v195\n"
    "v_mfma_f32_16x16x32_bf16 v[172:175], a[252:255], a[232:235], v[172:175]\n"
    "s_add_u32 s60, s60, 1572864\n"
    "v_exp_f16_e64 v234, v232\n"
    "v_mfma_f32_16x16x32_bf16 v[180:183], a[84:87], a[236:239], 0\n"
    "s_add_u32 s61, s61, 1572864\n"
    "ds_read_u16_d16_hi v203, v203\n"
    "v_lshlrev_b32_sdwa v204, 1, v177 dst_sel:DWORD dst_unused:UNUSED_PAD src0_sel:DWORD src1_sel:WORD_1\n"
    "v_mfma_f32_16x16x32_bf16 v[180:183], a[132:135], a[240:243], v[180:183]\n"
    "s_add_u32 s62, s62, 1572864\n"
    "v_exp_f16_sdwa v234, v232 dst_sel:WORD_1 dst_unused:UNUSED_PRESERVE src0_sel:WORD_1\n"
    "v_mfma_f32_16x16x32_bf16 v[180:183], a[252:255], a[244:247], v[180:183]\n"
    "s_waitcnt lgkmcnt(8)\n"
    "ds_read_b128 v[164:167], v244 offset:18432\n"
    "v_or_b32_e32 v193, v193, v201\n"
    "v_mfma_f32_16x16x32_bf16 v[104:107], a[192:195], v[188:191], v[104:107]\n"
    "s_waitcnt lgkmcnt(8)\n"
    "s_barrier\n"
    "v_mfma_f32_16x16x32_bf16 v[108:111], a[196:199], v[188:191], v[108:111]\n"
    "s_waitcnt lgkmcnt(6)\n"
    "ds_read_u16 v197, v197\n"
    "ds_write2_b32 v248, v32, v33 offset0:0 offset1:20\n"
    "v_smfmac_f32_16x16x64_bf16 v[24:27], v[216:219], v[208:215], v240\n"
    "s_add_u32 s63, s63, 1572864\n"
    "v_lshlrev_b32_sdwa v201, 1, v171 dst_sel:DWORD dst_unused:UNUSED_PAD src0_sel:DWORD src1_sel:WORD_1\n"
    "v_smfmac_f32_16x16x64_bf16 v[28:31], v[220:223], v[208:215], v240 abid:1\n"
    "s_waitcnt lgkmcnt(5)\n"
    "ds_read_u16_d16_hi v205, v205\n"
    "ds_write2_b32 v248, v34, v35 offset0:40 offset1:60\n"
    "v_mfma_f32_16x16x32_bf16 v[128:131], v[236:239], v[184:187], v[128:131]\n"
    "s_add_u32 s52, s52, 1572864\n"
    "s_add_u32 s53, s53, 393216\n"
    "v_add_u32_e32 v248, 1360, v248\n"
    "v_or_b32_e64 v194, v194, v202\n"
    "ds_write2_b32 v235, v52, v53 offset0:0 offset1:20\n"
    "v_pk_add_f32 v[224:225], v[224:225], v[24:25]\n"
    "v_pk_add_f32 v[226:227], v[226:227], v[26:27]\n"
    "v_pk_fma_f32 v[228:229], v[24:25], v[24:25], v[228:229]\n"
    "v_pk_fma_f32 v[230:231], v[26:27], v[26:27], v[230:231]\n"
    "v_pk_add_f32 v[224:225], v[224:225], v[28:29]\n"
    "v_pk_add_f32 v[226:227], v[226:227], v[30:31]\n"
    "v_pk_fma_f32 v[228:229], v[28:29], v[28:29], v[228:229]\n"
    "v_pk_fma_f32 v[230:231], v[30:31], v[30:31], v[230:231]\n"
    "v_mfma_f32_16x16x32_bf16 v[120:123], a[200:203], v[188:191], v[120:123]\n"
    "s_waitcnt lgkmcnt(5)\n"
    "ds_read_u16 v198, v198\n"
    "ds_write2_b32 v235, v54, v55 offset0:40 offset1:60\n"
    "v_mfma_f32_16x16x32_bf16 v[132:135], v[164:167], v[184:187], v[132:135]\n"
    "s_nop 0\n"
    "v_add_u32_e32 v235, 1360, v235\n"
    "v_or_b32_e64 v195, v195, v203\n"
    "v_mfma_f32_16x16x32_bf16 v[124:127], a[204:207], v[188:191], v[124:127]\n"
    "ds_read_u16_d16_hi v206, v206\n"
    "ds_write2_b32 v248, v36, v37 offset0:0 offset1:20\n"
    "v_mfma_f32_16x16x32_bf16 v[136:139], v[236:239], v[188:191], v[136:139]\n"
    "s_waitcnt lgkmcnt(4)\n"
    "v_or_b32_e32 v197, v197, v205\n"
    "v_lshlrev_b32_sdwa v184, 1, v168 dst_sel:DWORD dst_unused:UNUSED_PAD src0_sel:DWORD src1_sel:WORD_1\n"
    "v_mfma_f32_16x16x32_bf16 v[140:143], v[164:167], v[188:191], v[140:143]\n"
    "ds_read_u16 v199, v199\n"
    "ds_write2_b32 v248, v38, v39 offset0:40 offset1:60\n"
    "v_add_u32_e32 v248, 1360, v248\n"
    "v_lshlrev_b32_sdwa v185, 1, v170 dst_sel:DWORD dst_unused:UNUSED_PAD src0_sel:DWORD src1_sel:WORD_1\n"
    "v_mfma_f32_16x16x32_bf16 v[168:171], a[88:91], a[224:227], 0\n"
    "v_lshlrev_b32_sdwa v186, 1, v172 dst_sel:DWORD dst_unused:UNUSED_PAD src0_sel:DWORD src1_sel:WORD_1\n"
    "v_lshlrev_b32_sdwa v202, 1, v173 dst_sel:DWORD dst_unused:UNUSED_PAD src0_sel:DWORD src1_sel:WORD_1\n"
    "v_mfma_f32_16x16x32_bf16 v[168:171], a[136:139], a[228:231], v[168:171]\n"
    "ds_read_u16_d16_hi v207, v207\n"
    "ds_write2_b32 v235, v56, v57 offset0:0 offset1:20\n"
    "v_mfma_f32_16x16x32_bf16 v[168:171], a[52:55], a[232:235], v[168:171]\n"
    "v_lshlrev_b32_sdwa v187, 1, v174 dst_sel:DWORD dst_unused:UNUSED_PAD src0_sel:DWORD src1_sel:WORD_1\n"
    "v_lshlrev_b32_sdwa v203, 1, v175 dst_sel:DWORD dst_unused:UNUSED_PAD src0_sel:DWORD src1_sel:WORD_1\n"
    "v_mfma_f32_16x16x32_bf16 v[172:175], a[92:95], a[224:227], 0\n"
    "ds_read_b128 a[252:255], v244 offset:36864\n"
    "ds_write2_b32 v235, v58, v59 offset0:40 offset1:60\n"
    "v_mfma_f32_16x16x32_bf16 v[172:175], a[140:143], a[228:231], v[172:175]\n"
    "v_add_u32_e32 v235, -1360, v235\n"
    "v_lshlrev_b32_sdwa v188, 1, v176 dst_sel:DWORD dst_unused:UNUSED_PAD src0_sel:DWORD src1_sel:WORD_1\n"
    "v_lshlrev_b32_sdwa v189, 1, v178 dst_sel:DWORD dst_unused:UNUSED_PAD src0_sel:DWORD src1_sel:WORD_1\n"
    "ds_write2_b32 v248, v40, v41 offset0:0 offset1:20\n"
    "s_waitcnt lgkmcnt(7)\n"
    "v_or_b32_e32 v198, v198, v206\n"
    "ds_write2_b32 v248, v42, v43 offset0:40 offset1:60\n"
    "v_add_u32_e32 v248, 1360, v248\n"
    "v_lshlrev_b32_sdwa v205, 1, v179 dst_sel:DWORD dst_unused:UNUSED_PAD src0_sel:DWORD src1_sel:WORD_1\n"
    "v_mfma_f32_16x16x32_bf16 v[176:179], a[88:91], a[236:239], 0\n"
    "ds_read_u16 v184, v184\n"
    "ds_write2_b32 v248, v44, v45 offset0:0 offset1:20\n"
    "v_mfma_f32_16x16x32_bf16 v[176:179], a[136:139], a[240:243], v[176:179]\n"
    "s_waitcnt lgkmcnt(6)\n"
    "v_lshlrev_b32_sdwa v190, 1, v180 dst_sel:DWORD dst_unused:UNUSED_PAD src0_sel:DWORD src1_sel:WORD_1\n"
    "v_lshlrev_b32_sdwa v206, 1, v181 dst_sel:DWORD dst_unused:UNUSED_PAD src0_sel:DWORD src1_sel:WORD_1\n"
    "v_mfma_f32_16x16x32_bf16 v[176:179], a[52:55], a[244:247], v[176:179]\n"
    "s_waitcnt lgkmcnt(4)\n"
    "ds_read_u16_d16_hi v200, v200\n"
    "ds_write2_b32 v248, v46, v47 offset0:40 offset1:60\n"
    "v_mfma_f32_16x16x32_bf16 v[172:175], a[252:255], a[232:235], v[172:175]\n"
    "v_add_u32_e32 v248, 1360, v248\n"
    "v_lshlrev_b32_sdwa v191, 1, v182 dst_sel:DWORD dst_unused:UNUSED_PAD src0_sel:DWORD src1_sel:WORD_1\n"
    "v_or_b32_e64 v199, v199, v207\n"
    "ds_write2_b32 v248, v48, v49 offset0:0 offset1:20\n"
    "v_lshlrev_b32_sdwa v207, 1, v183 dst_sel:DWORD dst_unused:UNUSED_PAD src0_sel:DWORD src1_sel:WORD_1\n"
    "v_mfma_f32_16x16x32_bf16 v[180:183], a[92:95], a[236:239], 0\n"
    "ds_read_u16 v185, v185\n"
    "ds_write2_b32 v248, v50, v51 offset0:40 offset1:60\n"
    "v_mfma_f32_32x32x1_2b_f32 v[32:63], v254, v254, 0\n"
    "v_add_u32_e32 v248, -5440, v248\n"
    "v_pk_fma_f16 v234, v234, s49, s49\n"
    "v_mov_b32_e64 v254, 0\n"
    "v_rcp_f16_e64 v232, v234\n"
    "ds_read_u16_d16_hi v201, v201\n"
    "ds_read_u16 v186, v186\n"
    "v_rcp_f16_sdwa v232, v234 dst_sel:WORD_1 dst_unused:UNUSED_PRESERVE src0_sel:WORD_1\n"
    "ds_read_b128 v[236:239], v244 offset:20480\n"
    "ds_read_u16_d16_hi v202, v202\n"
    "v_mfma_f32_16x16x32_bf16 v[180:183], a[140:143], a[240:243], v[180:183]\n"
    "s_nop 0\n"
    "ds_read_b128 v[216:219], v251 offset:10752\n"
    "ds_read_b128 v[220:223], v251 offset:11088\n"
    "v_mfma_f32_16x16x32_bf16 v[180:183], a[252:255], a[244:247], v[180:183]\n"
    "s_waitcnt lgkmcnt(8)\n"
    "v_fma_mix_f32 v234, v196, v232, 0 op_sel_hi:[1,1,0]\n"
    "v_fma_mix_f32 v232, v196, v232, 0 op_sel:[1,1,0] op_sel_hi:[1,1,0]\n"
    "v_mfma_f32_16x16x32_bf16 v[96:99], a[208:211], v[192:195], v[96:99]\n"
    "ds_read_u16 v187, v187\n"
    "v_cvt_pk_bf16_f32 v196, v234, v232\n"
    "v_mfma_f32_16x16x32_bf16 v[100:103], a[212:215], v[192:195], v[100:103]\n"
    "ds_read_b128 v[164:167], v244 offset:22528\n"
    "v_or_b32_e32 v184, v184, v200\n"
    "v_mfma_f32_16x16x32_bf16 v[112:115], a[216:219], v[192:195], v[112:115]\n"
    "s_waitcnt lgkmcnt(8)\n"
    "s_barrier\n"
    "v_mfma_f32_16x16x32_bf16 v[116:119], a[220:223], v[192:195], v[116:119]\n"
    "s_waitcnt lgkmcnt(7)\n"
    "ds_read_b128 v[208:211], v250 offset:0\n"
    "ds_read_b128 v[212:215], v250 offset:320\n"
    "v_mfma_f32_16x16x32_bf16 v[104:107], a[208:211], v[196:199], v[104:107]\n"
    "ds_read_u16_d16_hi v203, v203\n"
    "ds_read_u16 v188, v188\n"
    "v_mfma_f32_16x16x32_bf16 v[108:111], a[212:215], v[196:199], v[108:111]\n"
    "ds_read_u16_d16_hi v204, v204\n"
    "v_or_b32_e64 v185, v185, v201\n"
    "v_mfma_f32_16x16x32_bf16 v[120:123], a[216:219], v[196:199], v[120:123]\n"
    "v_lshlrev_b32_sdwa v201, 1, v171 dst_sel:DWORD dst_unused:UNUSED_PAD src0_sel:DWORD src1_sel:WORD_1\n"
    "v_cvt_pk_bf16_f32 v96, v96, v97\n"
    "v_mfma_f32_16x16x32_bf16 v[124:127], a[220:223], v[196:199], v[124:127]\n"
    "s_waitcnt lgkmcnt(8)\n"
    "ds_read_u16 v189, v189\n"
    "v_or_b32_e32 v186, v186, v202\n"
    "v_lshlrev_b32_sdwa v202, 1, v173 dst_sel:DWORD dst_unused:UNUSED_PAD src0_sel:DWORD src1_sel:WORD_1\n"
    "v_cvt_pk_bf16_f32 v97, v98, v99\n"
    "s_waitcnt vmcnt(2)\n"
    "v_smfmac_f32_32x32x32_bf16 v[128:143], a[248:251], v[144:151], v241\n"
    "s_nop 0\n"
    "ds_read_u16_d16_hi v205, v205\n"
    "v_cvt_pk_bf16_f32 v98, v100, v101\n"
    "v_cvt_pk_bf16_f32 v99, v102, v103\n"
    "v_cvt_pk_bf16_f32 v152, v112, v113\n"
    "v_cvt_pk_bf16_f32 v153, v114, v115\n"
    "v_cvt_pk_bf16_f32 v154, v116, v117\n"
    "v_cvt_pk_bf16_f32 v155, v118, v119\n"
    "v_cvt_pk_bf16_f32 v144, v104, v105\n"
    "v_cvt_pk_bf16_f32 v145, v106, v107\n"
    "v_cvt_pk_bf16_f32 v146, v108, v109\n"
    "v_mfma_f32_16x16x32_bf16 v[128:131], v[236:239], v[192:195], v[128:131]\n"
    "s_waitcnt lgkmcnt(6)\n"
    "ds_read_u16 v190, v190\n"
    "ds_read_u16_d16_hi v206, v206\n"
    "v_mfma_f32_16x16x32_bf16 v[132:135], v[164:167], v[192:195], v[132:135]\n"
    "s_waitcnt lgkmcnt(7)\n"
    "buffer_store_dwordx4 v[96:99], v245, s[44:47], s57 offen offset:0 sc1\n"
    "ds_read_u16 v191, v191\n"
    "v_smfmac_f32_16x16x64_bf16 v[32:35], v[216:219], v[208:215], v240\n"
    "s_waitcnt lgkmcnt(7)\n"
    "ds_read_b128 v[216:219], v252 offset:10752\n"
    "v_smfmac_f32_16x16x64_bf16 v[36:39], v[220:223], v[208:215], v240 abid:1\n"
    "s_nop 0\n"
    "ds_read_b128 v[208:211], v250 offset:640\n"
    "v_mfma_f32_16x16x32_bf16 v[136:139], v[236:239], v[196:199], v[136:139]\n"
    "v_cvt_pk_f16_f32 v192, v168, v169\n"
    "v_lshlrev_b32_sdwa v193, 1, v170 dst_sel:DWORD dst_unused:UNUSED_PAD src0_sel:DWORD src1_sel:WORD_1\n"
    "v_mfma_f32_16x16x32_bf16 v[140:143], v[164:167], v[196:199], v[140:143]\n"
    "s_nop 0\n"
    "ds_read_b128 v[212:215], v250 offset:960\n"
    "ds_read_b128 v[220:223], v252 offset:11088\n"
    "v_mfma_f32_16x16x32_bf16 v[168:171], a[96:99], a[224:227], 0\n"
    "s_waitcnt lgkmcnt(9)\n"
    "v_lshlrev_b32_sdwa v194, 1, v172 dst_sel:DWORD dst_unused:UNUSED_PAD src0_sel:DWORD src1_sel:WORD_1\n"
    "v_lshlrev_b32_sdwa v195, 1, v174 dst_sel:DWORD dst_unused:UNUSED_PAD src0_sel:DWORD src1_sel:WORD_1\n"
    "v_mfma_f32_16x16x32_bf16 v[168:171], a[144:147], a[228:231], v[168:171]\n"
    "ds_read_u16_d16_hi v207, v207\n"
    "v_cvt_pk_f16_f32 v196, v176, v177\n"
    "v_mfma_f32_16x16x32_bf16 v[168:171], a[56:59], a[232:235], v[168:171]\n"
    "ds_read_u16 v193, v193\n"
    "v_lshlrev_b32_sdwa v197, 1, v178 dst_sel:DWORD dst_unused:UNUSED_PAD src0_sel:DWORD src1_sel:WORD_1\n"
    "v_lshlrev_b32_sdwa v198, 1, v180 dst_sel:DWORD dst_unused:UNUSED_PAD src0_sel:DWORD src1_sel:WORD_1\n"
    "v_lshlrev_b32_sdwa v199, 1, v182 dst_sel:DWORD dst_unused:UNUSED_PAD src0_sel:DWORD src1_sel:WORD_1\n"
    "v_or_b32_e64 v187, v187, v203\n"
    "v_lshlrev_b32_sdwa v203, 1, v175 dst_sel:DWORD dst_unused:UNUSED_PAD src0_sel:DWORD src1_sel:WORD_1\n"
    "v_mfma_f32_16x16x32_bf16 v[172:175], a[100:103], a[224:227], 0\n"
    "s_waitcnt lgkmcnt(9)\n"
    "ds_read_u16_d16_hi v201, v201\n"
    "v_or_b32_e32 v188, v188, v204\n"
    "v_mfma_f32_16x16x32_bf16 v[172:175], a[148:151], a[228:231], v[172:175]\n"
    "v_or_b32_e64 v189, v189, v205\n"
    "v_lshlrev_b32_sdwa v205, 1, v179 dst_sel:DWORD dst_unused:UNUSED_PAD src0_sel:DWORD src1_sel:WORD_1\n"
    "v_mfma_f32_16x16x32_bf16 v[172:175], a[60:63], a[232:235], v[172:175]\n"
    "ds_read_u16 v194, v194\n"
    "v_cvt_pk_bf16_f32 v156, v128, v129\n"
    "v_mfma_f32_16x16x32_bf16 v[176:179], a[96:99], a[236:239], 0\n"
    "s_waitcnt lgkmcnt(9)\n"
    "v_or_b32_e32 v190, v190, v206\n"
    "v_lshlrev_b32_sdwa v206, 1, v181 dst_sel:DWORD dst_unused:UNUSED_PAD src0_sel:DWORD src1_sel:WORD_1\n"
    "v_mfma_f32_16x16x32_bf16 v[176:179], a[144:147], a[240:243], v[176:179]\n"
    "ds_read_u16_d16_hi v202, v202\n"
    "v_cvt_pk_bf16_f32 v157, v130, v131\n"
    "v_mfma_f32_16x16x32_bf16 v[176:179], a[56:59], a[244:247], v[176:179]\n"
    "ds_read_b128 v[236:239], v244 offset:0\n"
    "v_cvt_pk_bf16_f32 v158, v132, v133\n"
    "v_mfma_f32_16x16x32_bf16 v[96:99], a[0:3], v[184:187], 0\n"
    "ds_read_u16 v195, v195\n"
    "v_cvt_pk_bf16_f32 v159, v134, v135\n"
    "v_mfma_f32_16x16x32_bf16 v[100:103], a[4:7], v[184:187], 0\n"
    "s_waitcnt lgkmcnt(8)\n"
    "v_cvt_pk_bf16_f32 v147, v110, v111\n"
    "v_cvt_pk_bf16_f32 v148, v120, v121\n"
    "v_smfmac_f32_16x16x64_bf16 v[32:35], v[216:219], v[208:215], v240\n"
    "s_waitcnt lgkmcnt(7)\n"
    "ds_read_b128 v[216:219], v251 offset:11424\n"
    "v_smfmac_f32_16x16x64_bf16 v[36:39], v[220:223], v[208:215], v240 abid:1\n"
    "ds_read_b128 v[208:211], v250 offset:80\n"
    "v_mfma_f32_16x16x32_bf16 v[112:115], a[8:11], v[184:187], 0\n"
    "ds_read_b128 v[212:215], v250 offset:400\n"
    "s_waitcnt lgkmcnt(9)\n"
    "v_or_b32_e32 v191, v191, v207\n"
    "ds_read_b128 v[220:223], v251 offset:11760\n"
    "v_pk_add_f32 v[224:225], v[224:225], v[32:33]\n"
    "v_pk_add_f32 v[226:227], v[226:227], v[34:35]\n"
    "v_pk_fma_f32 v[228:229], v[32:33], v[32:33], v[228:229]\n"
    "v_pk_fma_f32 v[230:231], v[34:35], v[34:35], v[230:231]\n"
    "v_pk_add_f32 v[224:225], v[224:225], v[36:37]\n"
    "v_pk_add_f32 v[226:227], v[226:227], v[38:39]\n"
    "v_pk_fma_f32 v[228:229], v[36:37], v[36:37], v[228:229]\n"
    "v_pk_fma_f32 v[230:231], v[38:39], v[38:39], v[230:231]\n"
    "v_mfma_f32_16x16x32_bf16 v[116:119], a[12:15], v[184:187], 0\n"
    "s_waitcnt lgkmcnt(8)\n"
    "ds_read_u16_d16_hi v203, v203\n"
    "ds_read_b128 v[164:167], v244 offset:2048\n"
    "v_mfma_f32_16x16x32_bf16 v[104:107], a[0:3], v[188:191], 0\n"
    "s_nop 0\n"
    "ds_read_u16 v197, v197\n"
    "v_lshlrev_b32_sdwa v207, 1, v183 dst_sel:DWORD dst_unused:UNUSED_PAD src0_sel:DWORD src1_sel:WORD_1\n"
    "v_mfma_f32_16x16x32_bf16 v[180:183], a[100:103], a[236:239], 0\n"
    "v_or_b32_e64 v193, v193, v201\n"
    "v_cvt_pk_bf16_f32 v149, v122, v123\n"
    "v_mfma_f32_16x16x32_bf16 v[180:183], a[148:151], a[240:243], v[180:183]\n"
    "s_waitcnt lgkmcnt(8)\n"
    "ds_read_u16_d16_hi v205, v205\n"
    "v_or_b32_e32 v194, v194, v202\n"
    "v_mfma_f32_16x16x32_bf16 v[180:183], a[60:63], a[244:247], v[180:183]\n"
    "ds_read_u16 v198, v198\n"
    "v_cvt_pk_bf16_f32 v150, v124, v125\n"
    "v_mfma_f32_16x16x32_bf16 v[128:131], v[236:239], v[184:187], 0\n"
    "v_cvt_pk_bf16_f32 v151, v126, v127\n"
    "v_pk_fma_f16 v232, v192, v192, s48\n"
    "v_mfma_f32_16x16x32_bf16 v[108:111], a[4:7], v[188:191], 0\n"
    "ds_read_u16_d16_hi v206, v206\n"
    "v_cvt_pk_bf16_f32 v160, v136, v137\n"
    "v_mfma_f32_16x16x32_bf16 v[120:123], a[8:11], v[188:191], 0\n"
    "v_cvt_pk_bf16_f32 v161, v138, v139\n"
    "v_cvt_pk_bf16_f32 v162, v140, v141\n"
    "v_mfma_f32_16x16x32_bf16 v[124:127], a[12:15], v[188:191], 0\n"
    "s_waitcnt lgkmcnt(7)\n"
    "ds_read_u16 v199, v199\n"
    "v_cvt_pk_bf16_f32 v163, v142, v143\n"
    "v_smfmac_f32_16x16x64_bf16 v[40:43], v[216:219], v[208:215], v240\n"
    "s_waitcnt lgkmcnt(7)\n"
    "ds_read_b128 v[216:219], v252 offset:11424\n"
    "v_smfmac_f32_16x16x64_bf16 v[44:47], v[220:223], v[208:215], v240 abid:1\n"
    "ds_read_b128 v[208:211], v250 offset:720\n"
    "v_pk_mul_f16 v232, v192, v232\n"
    "v_mfma_f32_16x16x32_bf16 v[136:139], v[236:239], v[188:191], 0\n"
    "ds_read_b128 v[212:215], v250 offset:1040\n"
    "v_exp_f16_e32 v234, v232\n"
    "s_waitcnt lgkmcnt(8)\n"
    "v_mfma_f32_16x16x32_bf16 v[132:135], v[164:167], v[184:187], 0\n"
    "ds_read_b128 v[220:223], v252 offset:11760\n"
    "v_or_b32_e64 v195, v195, v203\n"
    "v_mfma_f32_16x16x32_bf16 v[140:143], v[164:167], v[188:191], 0\n"
    "ds_read_u16_d16_hi v207, v207\n"
    "v_exp_f16_sdwa v234, v232 dst_sel:WORD_1 dst_unused:UNUSED_PRESERVE src0_sel:WORD_1\n"
    "v_lshlrev_b32_sdwa v184, 1, v168 dst_sel:DWORD dst_unused:UNUSED_PAD src0_sel:DWORD src1_sel:WORD_1\n"
    "v_pk_fma_f16 v234, v234, s49, s49\n"
    "v_lshlrev_b32_sdwa v200, 1, v169 dst_sel:DWORD dst_unused:UNUSED_PAD src0_sel:DWORD src1_sel:WORD_1\n"
    "v_lshlrev_b32_sdwa v185, 1, v170 dst_sel:DWORD dst_unused:UNUSED_PAD src0_sel:DWORD src1_sel:WORD_1\n"
    "v_lshlrev_b32_sdwa v201, 1, v171 dst_sel:DWORD dst_unused:UNUSED_PAD src0_sel:DWORD src1_sel:WORD_1\n"
    "v_mfma_f32_16x16x32_bf16 v[168:171], a[104:107], a[224:227], 0\n"
    "s_waitcnt lgkmcnt(8)\n"
    "ds_read_u16 v184, v184\n"
    "v_or_b32_e32 v197, v197, v205\n"
    "v_mfma_f32_16x16x32_bf16 v[168:171], a[152:155], a[228:231], v[168:171]\n"
    "ds_read_u16_d16_hi v200, v200\n"
    "v_lshlrev_b32_sdwa v186, 1, v172 dst_sel:DWORD dst_unused:UNUSED_PAD src0_sel:DWORD src1_sel:WORD_1\n"
    "v_lshlrev_b32_sdwa v202, 1, v173 dst_sel:DWORD dst_unused:UNUSED_PAD src0_sel:DWORD src1_sel:WORD_1\n"
    "v_lshlrev_b32_sdwa v187, 1, v174 dst_sel:DWORD dst_unused:UNUSED_PAD src0_sel:DWORD src1_sel:WORD_1\n"
    "v_lshlrev_b32_sdwa v203, 1, v175 dst_sel:DWORD dst_unused:UNUSED_PAD src0_sel:DWORD src1_sel:WORD_1\n"
    "v_rcp_f16_e64 v232, v234\n"
    "v_mfma_f32_16x16x32_bf16 v[168:171], a[64:67], a[232:235], v[168:171]\n"
    "ds_read_u16 v185, v185\n"
    "s_waitcnt lgkmcnt(9)\n"
    "v_or_b32_e32 v198, v198, v206\n"
    "v_rcp_f16_sdwa v232, v234 dst_sel:WORD_1 dst_unused:UNUSED_PRESERVE src0_sel:WORD_1\n"
    "v_mfma_f32_16x16x32_bf16 v[172:175], a[108:111], a[224:227], 0\n"
    "ds_read_u16_d16_hi v201, v201\n"
    "v_lshlrev_b32_sdwa v188, 1, v176 dst_sel:DWORD dst_unused:UNUSED_PAD src0_sel:DWORD src1_sel:WORD_1\n"
    "v_lshlrev_b32_sdwa v204, 1, v177 dst_sel:DWORD dst_unused:UNUSED_PAD src0_sel:DWORD src1_sel:WORD_1\n"
    "v_lshlrev_b32_sdwa v189, 1, v178 dst_sel:DWORD dst_unused:UNUSED_PAD src0_sel:DWORD src1_sel:WORD_1\n"
    "v_lshlrev_b32_sdwa v205, 1, v179 dst_sel:DWORD dst_unused:UNUSED_PAD src0_sel:DWORD src1_sel:WORD_1\n"
    "v_lshlrev_b32_sdwa v190, 1, v180 dst_sel:DWORD dst_unused:UNUSED_PAD src0_sel:DWORD src1_sel:WORD_1\n"
    "v_lshlrev_b32_sdwa v206, 1, v181 dst_sel:DWORD dst_unused:UNUSED_PAD src0_sel:DWORD src1_sel:WORD_1\n"
    "v_lshlrev_b32_sdwa v191, 1, v182 dst_sel:DWORD dst_unused:UNUSED_PAD src0_sel:DWORD src1_sel:WORD_1\n"
    "s_waitcnt lgkmcnt(6)\n"
    "v_smfmac_f32_16x16x64_bf16 v[40:43], v[216:219], v[208:215], v240\n"
    "s_waitcnt lgkmcnt(5)\n"
    "ds_read_b128 v[216:219], v251 offset:12096\n"
    "ds_read_u16 v186, v186\n"
    "v_smfmac_f32_16x16x64_bf16 v[44:47], v[220:223], v[208:215], v240 abid:1\n"
    "ds_read_b128 v[208:211], v250 offset:160\n"
    "ds_read_b128 v[212:215], v250 offset:480\n"
    "v_fma_mix_f32 v234, v192, v232, 0 op_sel_hi:[1,1,0]\n"
    "v_mfma_f32_16x16x32_bf16 v[172:175], a[156:159], a[228:231], v[172:175]\n"
    "ds_read_b128 v[220:223], v251 offset:12432\n"
    "v_fma_mix_f32 v232, v192, v232, 0 op_sel:[1,1,0] op_sel_hi:[1,1,0]\n"
    "v_cvt_pk_bf16_f32 v192, v234, v232\n"
    "v_pk_fma_f16 v232, v196, v196, s48\n"
    "v_pk_mul_f16 v232, v196, v232\n"
    "v_pk_add_f32 v[224:225], v[224:225], v[40:41]\n"
    "v_pk_add_f32 v[226:227], v[226:227], v[42:43]\n"
    "v_pk_fma_f32 v[228:229], v[40:41], v[40:41], v[228:229]\n"
    "v_pk_fma_f32 v[230:231], v[42:43], v[42:43], v[230:231]\n"
    "v_pk_add_f32 v[224:225], v[224:225], v[44:45]\n"
    "v_pk_add_f32 v[226:227], v[226:227], v[46:47]\n"
    "v_pk_fma_f32 v[228:229], v[44:45], v[44:45], v[228:229]\n"
    "v_pk_fma_f32 v[230:231], v[46:47], v[46:47], v[230:231]\n"
    "v_mfma_f32_16x16x32_bf16 v[172:175], a[68:71], a[232:235], v[172:175]\n"
    "s_waitcnt lgkmcnt(8)\n"
    "ds_read_b128 v[236:239], v244 offset:4096\n"
    "ds_read_u16_d16_hi v202, v202\n"
    "v_mfma_f32_16x16x32_bf16 v[176:179], a[104:107], a[236:239], 0\n"
    "s_waitcnt lgkmcnt(9)\n"
    "ds_read_u16 v187, v187\n"
    "v_or_b32_e64 v199, v199, v207\n"
    "v_mfma_f32_16x16x32_bf16 v[176:179], a[152:155], a[240:243], v[176:179]\n"
    "ds_read_b128 v[164:167], v244 offset:6144\n"
    "v_lshlrev_b32_sdwa v207, 1, v183 dst_sel:DWORD dst_unused:UNUSED_PAD src0_sel:DWORD src1_sel:WORD_1\n"
    "v_mfma_f32_16x16x32_bf16 v[176:179], a[64:67], a[244:247], v[176:179]\n"
    "ds_read_u16_d16_hi v203, v203\n"
    "v_exp_f16_e32 v234, v232\n"
    "v_mfma_f32_16x16x32_bf16 v[180:183], a[108:111], a[236:239], 0\n"
    "s_waitcnt lgkmcnt(9)\n"
    "v_exp_f16_sdwa v234, v232 dst_sel:WORD_1 dst_unused:UNUSED_PRESERVE src0_sel:WORD_1\n"
    "v_mfma_f32_16x16x32_bf16 v[180:183], a[156:159], a[240:243], v[180:183]\n"
    "ds_read_u16 v188, v188\n"
    "v_pk_fma_f16 v234, v234, s49, s49\n"
    "v_mfma_f32_16x16x32_bf16 v[180:183], a[68:71], a[244:247], v[180:183]\n"
    "ds_read_u16_d16_hi v204, v204\n"
    "v_rcp_f16_e64 v232, v234\n"
    "v_mfma_f32_16x16x32_bf16 v[96:99], a[16:19], v[192:195], v[96:99]\n"
    "s_waitcnt lgkmcnt(8)\n"
    "v_rcp_f16_sdwa v232, v234 dst_sel:WORD_1 dst_unused:UNUSED_PRESERVE src0_sel:WORD_1\n"
    "v_smfmac_f32_16x16x64_bf16 v[48:51], v[216:219], v[208:215], v240\n"
    "s_waitcnt lgkmcnt(7)\n"
    "ds_read_b128 v[216:219], v252 offset:12096\n"
    "ds_read_u16 v189, v189\n"
    "v_smfmac_f32_16x16x64_bf16 v[52:55], v[220:223], v[208:215], v240 abid:1\n"
    "s_nop 0\n"
    "v_fma_mix_f32 v234, v196, v232, 0 op_sel_hi:[1,1,0]\n"
    "v_mfma_f32_16x16x32_bf16 v[100:103], a[20:23], v[192:195], v[100:103]\n"
    "s_waitcnt lgkmcnt(8)\n"
    "ds_read_b128 v[208:211], v250 offset:800\n"
    "v_fma_mix_f32 v232, v196, v232, 0 op_sel:[1,1,0] op_sel_hi:[1,1,0]\n"
    "v_mfma_f32_16x16x32_bf16 v[112:115], a[24:27], v[192:195], v[112:115]\n"
    "ds_read_b128 v[212:215], v250 offset:1120\n"
    "v_cvt_pk_bf16_f32 v196, v234, v232\n"
    "v_mfma_f32_16x16x32_bf16 v[116:119], a[28:31], v[192:195], v[116:119]\n"
    "s_waitcnt lgkmcnt(9)\n"
    "ds_read_b128 v[220:223], v252 offset:12432\n"
    "v_or_b32_e64 v184, v184, v200\n"
    "v_mfma_f32_16x16x32_bf16 v[128:131], v[236:239], v[192:195], v[128:131]\n"
    "s_waitcnt lgkmcnt(8)\n"
    "ds_read_u16_d16_hi v205, v205\n"
    "v_or_b32_e32 v185, v185, v201\n"
    "v_mfma_f32_16x16x32_bf16 v[132:135], v[164:167], v[192:195], v[132:135]\n"
    "buffer_store_dwordx4 v[152:155], v245, s[44:47], s57 offen offset:64 sc1\n"
    "ds_read_u16 v190, v190\n"
    "v_mfma_f32_16x16x32_bf16 v[104:107], a[16:19], v[196:199], v[104:107]\n"
    "s_waitcnt lgkmcnt(9)\n"
    "v_cvt_pk_f16_f32 v192, v168, v169\n"
    "v_lshlrev_b32_sdwa v193, 1, v170 dst_sel:DWORD dst_unused:UNUSED_PAD src0_sel:DWORD src1_sel:WORD_1\n"
    "v_mfma_f32_16x16x32_bf16 v[108:111], a[20:23], v[196:199], v[108:111]\n"
    "ds_read_u16_d16_hi v206, v206\n"
    "v_lshlrev_b32_sdwa v201, 1, v171 dst_sel:DWORD dst_unused:UNUSED_PAD src0_sel:DWORD src1_sel:WORD_1\n"
    "v_mfma_f32_16x16x32_bf16 v[120:123], a[24:27], v[196:199], v[120:123]\n"
    "v_lshlrev_b32_sdwa v194, 1, v172 dst_sel:DWORD dst_unused:UNUSED_PAD src0_sel:DWORD src1_sel:WORD_1\n"
    "v_or_b32_e32 v186, v186, v202\n"
    "v_mfma_f32_16x16x32_bf16 v[124:127], a[28:31], v[196:199], v[124:127]\n"
    "ds_read_u16 v191, v191\n"
    "v_lshlrev_b32_sdwa v202, 1, v173 dst_sel:DWORD dst_unused:UNUSED_PAD src0_sel:DWORD src1_sel:WORD_1\n"
    "v_mfma_f32_16x16x32_bf16 v[136:139], v[236:239], v[196:199], v[136:139]\n"
    "s_waitcnt lgkmcnt(8)\n"
    "ds_read_u16_d16_hi v207, v207\n"
    "v_lshlrev_b32_sdwa v195, 1, v174 dst_sel:DWORD dst_unused:UNUSED_PAD src0_sel:DWORD src1_sel:WORD_1\n"
    "v_mfma_f32_16x16x32_bf16 v[140:143], v[164:167], v[196:199], v[140:143]\n"
    "v_cvt_pk_f16_f32 v196, v176, v177\n"
    "v_lshlrev_b32_sdwa v197, 1, v178 dst_sel:DWORD dst_unused:UNUSED_PAD src0_sel:DWORD src1_sel:WORD_1\n"
    "v_mfma_f32_16x16x32_bf16 v[168:171], a[112:115], a[224:227], 0\n"
    "ds_read_b128 a[252:255], v244 offset:34816\n"
    "v_lshlrev_b32_sdwa v198, 1, v180 dst_sel:DWORD dst_unused:UNUSED_PAD src0_sel:DWORD src1_sel:WORD_1\n"
    "v_mfma_f32_16x16x32_bf16 v[168:171], a[160:163], a[228:231], v[168:171]\n"
    "s_waitcnt lgkmcnt(7)\n"
    "ds_read_u16 v193, v193\n"
    "v_lshlrev_b32_sdwa v199, 1, v182 dst_sel:DWORD dst_unused:UNUSED_PAD src0_sel:DWORD src1_sel:WORD_1\n"
    "v_smfmac_f32_16x16x64_bf16 v[48:51], v[216:219], v[208:215], v240\n"
    "s_waitcnt lgkmcnt(7)\n"
    "v_or_b32_e64 v187, v187, v203\n"
    "v_smfmac_f32_16x16x64_bf16 v[52:55], v[220:223], v[208:215], v240 abid:1\n"
    "ds_read_b128 v[208:211], v250 offset:240\n"
    "ds_read_b128 v[212:215], v250 offset:560\n"
    "v_mfma_f32_16x16x32_bf16 v[168:171], a[72:75], a[232:235], v[168:171]\n"
    "v_lshlrev_b32_sdwa v203, 1, v175 dst_sel:DWORD dst_unused:UNUSED_PAD src0_sel:DWORD src1_sel:WORD_1\n"
    "v_or_b32_e64 v188, v188, v204\n"
    "ds_read_b128 v[216:219], v251 offset:12768\n"
    "ds_read_b128 v[220:223], v251 offset:13104\n"
    "v_pk_add_f32 v[224:225], v[224:225], v[48:49]\n"
    "v_pk_add_f32 v[226:227], v[226:227], v[50:51]\n"
    "v_pk_fma_f32 v[228:229], v[48:49], v[48:49], v[228:229]\n"
    "v_pk_fma_f32 v[230:231], v[50:51], v[50:51], v[230:231]\n"
    "v_pk_add_f32 v[224:225], v[224:225], v[52:53]\n"
    "v_pk_add_f32 v[226:227], v[226:227], v[54:55]\n"
    "v_pk_fma_f32 v[228:229], v[52:53], v[52:53], v[228:229]\n"
    "v_pk_fma_f32 v[230:231], v[54:55], v[54:55], v[230:231]\n"
    "v_mfma_f32_16x16x32_bf16 v[172:175], a[116:119], a[224:227], 0\n"
    "s_waitcnt lgkmcnt(9)\n"
    "ds_read_u16_d16_hi v201, v201\n"
    "ds_read_u16 v194, v194\n"
    "v_mfma_f32_16x16x32_bf16 v[172:175], a[164:167], a[228:231], v[172:175]\n"
    "s_waitcnt lgkmcnt(10)\n"
    "v_or_b32_e64 v189, v189, v205\n"
    "v_lshlrev_b32_sdwa v205, 1, v179 dst_sel:DWORD dst_unused:UNUSED_PAD src0_sel:DWORD src1_sel:WORD_1\n"
    "v_mfma_f32_16x16x32_bf16 v[176:179], a[112:115], a[236:239], 0\n"
    "ds_read_u16_d16_hi v202, v202\n"
    "v_or_b32_e64 v190, v190, v206\n"
    "v_mfma_f32_16x16x32_bf16 v[176:179], a[160:163], a[240:243], v[176:179]\n"
    "v_lshlrev_b32_sdwa v206, 1, v181 dst_sel:DWORD dst_unused:UNUSED_PAD src0_sel:DWORD src1_sel:WORD_1\n"
    "v_pk_fma_f16 v232, v192, v192, s48\n"
    "v_mfma_f32_16x16x32_bf16 v[176:179], a[72:75], a[244:247], v[176:179]\n"
    "s_waitcnt lgkmcnt(8)\n"
    "ds_read_b128 v[236:239], v244 offset:8192\n"
    "v_or_b32_e32 v191, v191, v207\n"
    "v_mfma_f32_16x16x32_bf16 v[172:175], a[252:255], a[232:235], v[172:175]\n"
    "ds_read_u16 v195, v195\n"
    "v_lshlrev_b32_sdwa v207, 1, v183 dst_sel:DWORD dst_unused:UNUSED_PAD src0_sel:DWORD src1_sel:WORD_1\n"
    "v_mfma_f32_16x16x32_bf16 v[180:183], a[116:119], a[236:239], 0\n"
    "v_pk_mul_f16 v232, v192, v232\n"
    "v_lshlrev_b32_sdwa v200, 1, v169 dst_sel:DWORD dst_unused:UNUSED_PAD src0_sel:DWORD src1_sel:WORD_1\n"
    "v_mfma_f32_16x16x32_bf16 v[180:183], a[164:167], a[240:243], v[180:183]\n"
    "ds_read_u16_d16_hi v203, v203\n"
    "v_exp_f16_e64 v234, v232\n"
    "v_mfma_f32_16x16x32_bf16 v[180:183], a[252:255], a[244:247], v[180:183]\n"
    "s_waitcnt lgkmcnt(7)\n"
    "ds_read_b128 v[164:167], v244 offset:10240\n"
    "buffer_store_dwordx4 v[156:159], v245, s[44:47], s57 offen offset:128 sc1\n"
    "v_smfmac_f32_16x16x64_bf16 v[56:59], v[216:219], v[208:215], v240\n"
    "s_waitcnt lgkmcnt(7)\n"
    "ds_read_b128 v[216:219], v252 offset:12768\n"
    "ds_read_u16 v197, v197\n"
    "v_smfmac_f32_16x16x64_bf16 v[60:63], v[220:223], v[208:215], v240 abid:1\n"
    "ds_read_b128 v[208:211], v250 offset:880\n"
    "v_mfma_f32_16x16x32_bf16 v[96:99], a[32:35], v[184:187], v[96:99]\n"
    "s_waitcnt lgkmcnt(9)\n"
    "v_or_b32_e32 v193, v193, v201\n"
    "v_exp_f16_sdwa v234, v232 dst_sel:WORD_1 dst_unused:UNUSED_PRESERVE src0_sel:WORD_1\n"
    "v_mfma_f32_16x16x32_bf16 v[100:103], a[36:39], v[184:187], v[100:103]\n"
    "ds_read_b128 v[212:215], v250 offset:1200\n"
    "ds_read_b128 v[220:223], v252 offset:13104\n"
    "v_mfma_f32_16x16x32_bf16 v[112:115], a[40:43], v[184:187], v[112:115]\n"
    "ds_read_u16_d16_hi v205, v205\n"
    "v_pk_fma_f16 v234, v234, s49, s49\n"
    "v_mfma_f32_16x16x32_bf16 v[116:119], a[44:47], v[184:187], v[116:119]\n"
    "s_waitcnt lgkmcnt(9)\n"
    "v_or_b32_e32 v194, v194, v202\n"
    "v_lshlrev_b32_sdwa v201, 1, v171 dst_sel:DWORD dst_unused:UNUSED_PAD src0_sel:DWORD src1_sel:WORD_1\n"
    "v_mfma_f32_16x16x32_bf16 v[128:131], v[236:239], v[184:187], v[128:131]\n"
    "ds_read_u16 v198, v198\n"
    "v_lshlrev_b32_sdwa v202, 1, v173 dst_sel:DWORD dst_unused:UNUSED_PAD src0_sel:DWORD src1_sel:WORD_1\n"
    "v_mfma_f32_16x16x32_bf16 v[104:107], a[32:35], v[188:191], v[104:107]\n"
    "v_rcp_f16_e64 v232, v234\n"
    "v_mfma_f32_16x16x32_bf16 v[108:111], a[36:39], v[188:191], v[108:111]\n"
    "ds_read_u16_d16_hi v206, v206\n"
    "v_rcp_f16_sdwa v232, v234 dst_sel:WORD_1 dst_unused:UNUSED_PRESERVE src0_sel:WORD_1\n"
    "v_mfma_f32_16x16x32_bf16 v[120:123], a[40:43], v[188:191], v[120:123]\n"
    "s_waitcnt lgkmcnt(8)\n"
    "ds_read_u16 v199, v199\n"
    "v_or_b32_e32 v195, v195, v203\n"
    "v_mfma_f32_16x16x32_bf16 v[132:135], v[164:167], v[184:187], v[132:135]\n"
    "v_lshlrev_b32_sdwa v184, 1, v168 dst_sel:DWORD dst_unused:UNUSED_PAD src0_sel:DWORD src1_sel:WORD_1\n"
    "v_lshlrev_b32_sdwa v185, 1, v170 dst_sel:DWORD dst_unused:UNUSED_PAD src0_sel:DWORD src1_sel:WORD_1\n"
    "v_mfma_f32_16x16x32_bf16 v[124:127], a[44:47], v[188:191], v[124:127]\n"
    "ds_read_u16_d16_hi v207, v207\n"
    "v_lshlrev_b32_sdwa v186, 1, v172 dst_sel:DWORD dst_unused:UNUSED_PAD src0_sel:DWORD src1_sel:WORD_1\n"
    "v_mfma_f32_16x16x32_bf16 v[136:139], v[236:239], v[188:191], v[136:139]\n"
    "v_lshlrev_b32_sdwa v187, 1, v174 dst_sel:DWORD dst_unused:UNUSED_PAD src0_sel:DWORD src1_sel:WORD_1\n"
    "v_lshlrev_b32_sdwa v203, 1, v175 dst_sel:DWORD dst_unused:UNUSED_PAD src0_sel:DWORD src1_sel:WORD_1\n"
    "v_mfma_f32_16x16x32_bf16 v[140:143], v[164:167], v[188:191], v[140:143]\n"
    "ds_read_b128 a[252:255], v244 offset:38912\n"
    "v_lshlrev_b32_sdwa v188, 1, v176 dst_sel:DWORD dst_unused:UNUSED_PAD src0_sel:DWORD src1_sel:WORD_1\n"
    "v_mfma_f32_16x16x32_bf16 v[168:171], a[120:123], a[224:227], 0\n"
    "s_waitcnt lgkmcnt(7)\n"
    "s_barrier\n"
    "v_smfmac_f32_16x16x64_bf16 v[56:59], v[216:219], v[208:215], v240\n"
    "s_waitcnt lgkmcnt(5)\n"
    "ds_read_u16 v184, v184\n"
    "ds_write2_b32 v248, v64, v65 offset0:0 offset1:20\n"
    "v_smfmac_f32_16x16x64_bf16 v[60:63], v[220:223], v[208:215], v240 abid:1\n"
    "v_or_b32_e32 v197, v197, v205\n"
    "v_mfma_f32_16x16x32_bf16 v[168:171], a[168:171], a[228:231], v[168:171]\n"
    "ds_read_u16_d16_hi v200, v200\n"
    "ds_write2_b32 v248, v66, v67 offset0:40 offset1:60\n"
    "buffer_store_dwordx4 v[144:147], v245, s[44:47], s88 offen offset:0 sc1\n"
    "v_pk_add_f32 v[224:225], v[224:225], v[56:57]\n"
    "v_pk_add_f32 v[226:227], v[226:227], v[58:59]\n"
    "v_pk_fma_f32 v[228:229], v[56:57], v[56:57], v[228:229]\n"
    "v_pk_fma_f32 v[230:231], v[58:59], v[58:59], v[230:231]\n"
    "v_pk_add_f32 v[224:225], v[224:225], v[60:61]\n"
    "v_pk_add_f32 v[226:227], v[226:227], v[62:63]\n"
    "v_pk_fma_f32 v[228:229], v[60:61], v[60:61], v[228:229]\n"
    "v_pk_fma_f32 v[230:231], v[62:63], v[62:63], v[230:231]\n"
    "v_mfma_f32_16x16x32_bf16 v[168:171], a[76:79], a[232:235], v[168:171]\n"
    "s_waitcnt lgkmcnt(4)\n"
    "ds_read_u16 v185, v185\n"
    "ds_write2_b32 v235, v84, v85 offset0:0 offset1:20\n"
    "v_mfma_f32_16x16x32_bf16 v[172:175], a[124:127], a[224:227], 0\n"
    "s_nop 0\n"
    "v_add_u32_e32 v248, 1360, v248\n"
    "v_or_b32_e64 v198, v198, v206\n"
    "v_mfma_f32_16x16x32_bf16 v[172:175], a[172:175], a[228:231], v[172:175]\n"
    "ds_read_u16_d16_hi v201, v201\n"
    "ds_write2_b32 v235, v86, v87 offset0:40 offset1:60\n"
    "v_add_u32_e32 v235, 1360, v235\n"
    "v_lshlrev_b32_sdwa v204, 1, v177 dst_sel:DWORD dst_unused:UNUSED_PAD src0_sel:DWORD src1_sel:WORD_1\n"
    "v_lshlrev_b32_sdwa v189, 1, v178 dst_sel:DWORD dst_unused:UNUSED_PAD src0_sel:DWORD src1_sel:WORD_1\n"
    "v_lshlrev_b32_sdwa v205, 1, v179 dst_sel:DWORD dst_unused:UNUSED_PAD src0_sel:DWORD src1_sel:WORD_1\n"
    "v_lshlrev_b32_sdwa v190, 1, v180 dst_sel:DWORD dst_unused:UNUSED_PAD src0_sel:DWORD src1_sel:WORD_1\n"
    "ds_write2_b32 v248, v68, v69 offset0:0 offset1:20\n"
    "v_lshlrev_b32_sdwa v206, 1, v181 dst_sel:DWORD dst_unused:UNUSED_PAD src0_sel:DWORD src1_sel:WORD_1\n"
    "v_lshlrev_b32_sdwa v191, 1, v182 dst_sel:DWORD dst_unused:UNUSED_PAD src0_sel:DWORD src1_sel:WORD_1\n"
    "ds_write2_b32 v248, v70, v71 offset0:40 offset1:60\n"
    "v_add_u32_e32 v248, 1360, v248\n"
    "v_fma_mix_f32 v234, v192, v232, 0 op_sel_hi:[1,1,0]\n"
    "v_mfma_f32_16x16x32_bf16 v[172:175], a[252:255], a[232:235], v[172:175]\n"
    "s_waitcnt lgkmcnt(6)\n"
    "ds_read_u16 v186, v186\n"
    "ds_write2_b32 v235, v88, v89 offset0:0 offset1:20\n"
    "v_or_b32_e32 v199, v199, v207\n"
    "v_lshlrev_b32_sdwa v207, 1, v183 dst_sel:DWORD dst_unused:UNUSED_PAD src0_sel:DWORD src1_sel:WORD_1\n"
    "v_fma_mix_f32 v232, v192, v232, 0 op_sel:[1,1,0] op_sel_hi:[1,1,0]\n"
    "v_mfma_f32_16x16x32_bf16 v[176:179], a[120:123], a[236:239], 0\n"
    "ds_read_b128 v[236:239], v244 offset:12288\n"
    "ds_write2_b32 v235, v90, v91 offset0:40 offset1:60\n"
    "v_add_u32_e32 v235, -1360, v235\n"
    "v_cvt_pk_bf16_f32 v192, v234, v232\n"
    "v_mfma_f32_16x16x32_bf16 v[176:179], a[168:171], a[240:243], v[176:179]\n"
    "v_pk_fma_f16 v232, v196, v196, s48\n"
    "v_or_b32_e32 v184, v184, v200\n"
    "v_mfma_f32_16x16x32_bf16 v[176:179], a[76:79], a[244:247], v[176:179]\n"
    "s_waitcnt lgkmcnt(4)\n"
    "ds_read_u16_d16_hi v202, v202\n"
    "ds_write2_b32 v248, v72, v73 offset0:0 offset1:20\n"
    "v_mfma_f32_16x16x32_bf16 v[180:183], a[124:127], a[236:239], 0\n"
    "v_pk_mul_f16 v232, v196, v232\n"
    "v_or_b32_e64 v185, v185, v201\n"
    "v_mfma_f32_16x16x32_bf16 v[180:183], a[172:175], a[240:243], v[180:183]\n"
    "ds_read_u16 v187, v187\n"
    "ds_write2_b32 v248, v74, v75 offset0:40 offset1:60\n"
    "v_add_u32_e32 v248, 1360, v248\n"
    "v_exp_f16_e64 v234, v232\n"
    "v_mfma_f32_16x16x32_bf16 v[180:183], a[252:255], a[244:247], v[180:183]\n"
    "ds_read_b128 v[164:167], v244 offset:14336\n"
    "ds_write2_b32 v248, v76, v77 offset0:0 offset1:20\n"
    "v_exp_f16_sdwa v234, v232 dst_sel:WORD_1 dst_unused:UNUSED_PRESERVE src0_sel:WORD_1\n"
    "v_mfma_f32_16x16x32_bf16 v[96:99], a[176:179], v[192:195], v[96:99]\n"
    "buffer_store_dwordx4 v[148:151], v245, s[44:47], s88 offen offset:64 sc1\n"
    "ds_write2_b32 v248, v78, v79 offset0:40 offset1:60\n"
    "v_add_u32_e32 v248, 1360, v248\n"
    "v_pk_fma_f16 v234, v234, s49, s49\n"
    "v_mfma_f32_16x16x32_bf16 v[100:103], a[180:183], v[192:195], v[100:103]\n"
    "ds_read_u16_d16_hi v203, v203\n"
    "ds_write2_b32 v248, v80, v81 offset0:0 offset1:20\n"
    "v_rcp_f16_e32 v232, v234\n"
    "v_mfma_f32_16x16x32_bf16 v[112:115], a[184:187], v[192:195], v[112:115]\n"
    "s_waitcnt lgkmcnt(7)\n"
    "v_rcp_f16_sdwa v232, v234 dst_sel:WORD_1 dst_unused:UNUSED_PRESERVE src0_sel:WORD_1\n"
    "v_mfma_f32_16x16x32_bf16 v[116:119], a[188:191], v[192:195], v[116:119]\n"
    "ds_read_u16 v188, v188\n"
    "ds_write2_b32 v248, v82, v83 offset0:40 offset1:60\n"
    "v_mfma_f32_32x32x1_2b_f32 v[64:95], v254, v254, 0\n"
    "v_add_u32_e32 v248, -5440, v248\n"
    "v_fma_mix_f32 v234, v196, v232, 0 op_sel_hi:[1,1,0]\n"
    "v_fma_mix_f32 v232, v196, v232, 0 op_sel:[1,1,0] op_sel_hi:[1,1,0]\n"
    "v_lshlrev_b32_sdwa v201, 1, v171 dst_sel:DWORD dst_unused:UNUSED_PAD src0_sel:DWORD src1_sel:WORD_1\n"
    "v_cvt_pk_bf16_f32 v196, v234, v232\n"
    "v_or_b32_e64 v186, v186, v202\n"
    "ds_read_u16_d16_hi v204, v204\n"
    "ds_read_u16 v189, v189\n"
    "ds_read_b128 v[216:219], v251 offset:21504\n"
    "v_lshlrev_b32_sdwa v202, 1, v173 dst_sel:DWORD dst_unused:UNUSED_PAD src0_sel:DWORD src1_sel:WORD_1\n"
    "ds_read_b128 v[220:223], v251 offset:21840\n"
    "ds_read_u16_d16_hi v205, v205\n"
    "v_mfma_f32_16x16x32_bf16 v[128:131], v[236:239], v[192:195], v[128:131]\n"
    "s_waitcnt lgkmcnt(9)\n"
    "v_mfma_f32_16x16x32_bf16 v[132:135], v[164:167], v[192:195], v[132:135]\n"
    "s_waitcnt lgkmcnt(7)\n"
    "ds_read_u16 v190, v190\n"
    "v_cvt_pk_f16_f32 v192, v168, v169\n"
    "v_mfma_f32_16x16x32_bf16 v[104:107], a[176:179], v[196:199], v[104:107]\n"
    "ds_read_u16_d16_hi v206, v206\n"
    "v_lshlrev_b32_sdwa v193, 1, v170 dst_sel:DWORD dst_unused:UNUSED_PAD src0_sel:DWORD src1_sel:WORD_1\n"
    "v_mfma_f32_16x16x32_bf16 v[108:111], a[180:183], v[196:199], v[108:111]\n"
    "s_waitcnt lgkmcnt(7)\n"
    "s_barrier\n"
    "v_mfma_f32_16x16x32_bf16 v[120:123], a[184:187], v[196:199], v[120:123]\n"
    "s_nop 0\n"
    "ds_read_b128 v[208:211], v250 offset:0\n"
    "ds_read_b128 v[212:215], v250 offset:320\n"
    "v_mfma_f32_16x16x32_bf16 v[124:127], a[188:191], v[196:199], v[124:127]\n"
    "s_waitcnt lgkmcnt(8)\n"
    "ds_read_b128 a[224:227], v243 offset:24640\n"
    "ds_read_b128 a[228:231], v243 offset:25664\n"
    "v_mfma_f32_16x16x32_bf16 v[136:139], v[236:239], v[196:199], v[136:139]\n"
    "ds_read_b128 a[232:235], v243 offset:26688\n"
    "ds_read_u16 v191, v191\n"
    "v_mfma_f32_16x16x32_bf16 v[140:143], v[164:167], v[196:199], v[140:143]\n"
    "v_lshlrev_b32_sdwa v194, 1, v172 dst_sel:DWORD dst_unused:UNUSED_PAD src0_sel:DWORD src1_sel:WORD_1\n"
    "v_lshlrev_b32_sdwa v195, 1, v174 dst_sel:DWORD dst_unused:UNUSED_PAD src0_sel:DWORD src1_sel:WORD_1\n"
    "v_cvt_pk_f16_f32 v196, v176, v177\n"
    "v_lshlrev_b32_sdwa v197, 1, v178 dst_sel:DWORD dst_unused:UNUSED_PAD src0_sel:DWORD src1_sel:WORD_1\n"
    "v_lshlrev_b32_sdwa v198, 1, v180 dst_sel:DWORD dst_unused:UNUSED_PAD src0_sel:DWORD src1_sel:WORD_1\n"
    "v_lshlrev_b32_sdwa v199, 1, v182 dst_sel:DWORD dst_unused:UNUSED_PAD src0_sel:DWORD src1_sel:WORD_1\n"
    "v_or_b32_e64 v187, v187, v203\n"
    "v_lshlrev_b32_sdwa v203, 1, v175 dst_sel:DWORD dst_unused:UNUSED_PAD src0_sel:DWORD src1_sel:WORD_1\n"
    "v_or_b32_e64 v188, v188, v204\n"
    "ds_read_b128 a[236:239], v243 offset:27712\n"
    "ds_read_u16_d16_hi v207, v207\n"
    "s_waitcnt lgkmcnt(10)\n"
    "v_or_b32_e32 v189, v189, v205\n"
    "ds_read_b128 a[240:243], v243 offset:28736\n"
    "ds_read_b128 a[244:247], v243 offset:29760\n"
    "buffer_store_dwordx4 v[160:163], v245, s[44:47], s88 offen offset:128 sc1\n"
    "v_lshlrev_b32_sdwa v205, 1, v179 dst_sel:DWORD dst_unused:UNUSED_PAD src0_sel:DWORD src1_sel:WORD_1\n"
    "s_waitcnt lgkmcnt(9)\n"
    "v_or_b32_e64 v190, v190, v206\n"
    "v_mfma_f32_16x16x32_bf16 v[96:99], a[192:195], v[184:187], v[96:99]\n"
    "s_waitcnt lgkmcnt(8)\n"
    "ds_read_b128 a[252:255], v244 offset:32768\n"
    "ds_read_u16 v193, v193\n"
    "v_smfmac_f32_16x16x64_bf16 v[64:67], v[216:219], v[208:215], v240\n"
    "s_waitcnt lgkmcnt(9)\n"
    "ds_read_b128 v[216:219], v252 offset:21504\n"
    "v_smfmac_f32_16x16x64_bf16 v[68:71], v[220:223], v[208:215], v240 abid:1\n"
    "ds_read_b128 v[208:211], v250 offset:640\n"
    "v_mfma_f32_16x16x32_bf16 v[168:171], a[80:83], a[224:227], 0\n"
    "s_waitcnt lgkmcnt(9)\n"
    "ds_read_b128 v[212:215], v250 offset:960\n"
    "v_lshlrev_b32_sdwa v206, 1, v181 dst_sel:DWORD dst_unused:UNUSED_PAD src0_sel:DWORD src1_sel:WORD_1\n"
    "v_mfma_f32_16x16x32_bf16 v[168:171], a[128:131], a[228:231], v[168:171]\n"
    "ds_read_b128 v[220:223], v252 offset:21840\n"
    "v_pk_fma_f16 v232, v192, v192, s48\n"
    "v_mfma_f32_16x16x32_bf16 v[168:171], a[48:51], a[232:235], v[168:171]\n"
    "ds_read_u16_d16_hi v201, v201\n"
    "v_pk_mul_f16 v232, v192, v232\n"
    "v_mfma_f32_16x16x32_bf16 v[172:175], a[84:87], a[224:227], 0\n"
    "s_waitcnt lgkmcnt(10)\n"
    "v_exp_f16_e32 v234, v232\n"
    "v_mfma_f32_16x16x32_bf16 v[172:175], a[132:135], a[228:231], v[172:175]\n"
    "buffer_load_dwordx4 v[152:155], v245, s[40:43], s58 offen offset:64\n"
    "buffer_load_dwordx4 v[144:147], v245, s[40:43], s58 offen offset:0\n"
    "ds_read_u16 v194, v194\n"
    "v_mfma_f32_16x16x32_bf16 v[176:179], a[80:83], a[236:239], 0\n"
    "s_waitcnt lgkmcnt(9)\n"
    "v_or_b32_e32 v191, v191, v207\n"
    "v_lshlrev_b32_sdwa v207, 1, v183 dst_sel:DWORD dst_unused:UNUSED_PAD src0_sel:DWORD src1_sel:WORD_1\n"
    "v_mfma_f32_16x16x32_bf16 v[176:179], a[128:131], a[240:243], v[176:179]\n"
    "s_waitcnt lgkmcnt(8)\n"
    "ds_read_u16_d16_hi v202, v202\n"
    "v_lshlrev_b32_e64 v254, 3, v244\n"
    "v_mfma_f32_16x16x32_bf16 v[176:179], a[48:51], a[244:247], v[176:179]\n"
    "s_waitcnt lgkmcnt(8)\n"
    "ds_read_b128 v[236:239], v244 offset:16384\n"
    "v_exp_f16_sdwa v234, v232 dst_sel:WORD_1 dst_unused:UNUSED_PRESERVE src0_sel:WORD_1\n"
    "v_mfma_f32_16x16x32_bf16 v[172:175], a[252:255], a[232:235], v[172:175]\n"
    "ds_read_u16 v195, v195\n"
    "v_pk_fma_f16 v234, v234, s49, s49\n"
    "v_mfma_f32_16x16x32_bf16 v[180:183], a[84:87], a[236:239], 0\n"
    "v_lshlrev_b32_sdwa v200, 1, v169 dst_sel:DWORD dst_unused:UNUSED_PAD src0_sel:DWORD src1_sel:WORD_1\n"
    "v_rcp_f16_e64 v232, v234\n"
    "v_mfma_f32_16x16x32_bf16 v[180:183], a[132:135], a[240:243], v[180:183]\n"
    "ds_read_u16_d16_hi v203, v203\n"
    "v_rcp_f16_sdwa v232, v234 dst_sel:WORD_1 dst_unused:UNUSED_PRESERVE src0_sel:WORD_1\n"
    "v_mfma_f32_16x16x32_bf16 v[180:183], a[252:255], a[244:247], v[180:183]\n"
    "s_waitcnt lgkmcnt(7)\n"
    "ds_read_b128 v[164:167], v244 offset:18432\n"
    "v_lshlrev_b32_sdwa v204, 1, v177 dst_sel:DWORD dst_unused:UNUSED_PAD src0_sel:DWORD src1_sel:WORD_1\n"
    "v_smfmac_f32_16x16x64_bf16 v[64:67], v[216:219], v[208:215], v240\n"
    "s_waitcnt lgkmcnt(7)\n"
    "ds_read_b128 v[216:219], v251 offset:22176\n"
    "v_smfmac_f32_16x16x64_bf16 v[68:71], v[220:223], v[208:215], v240 abid:1\n"
    "ds_read_b128 v[208:211], v250 offset:80\n"
    "v_mfma_f32_16x16x32_bf16 v[100:103], a[196:199], v[184:187], v[100:103]\n"
    "ds_read_b128 v[212:215], v250 offset:400\n"
    "buffer_load_dwordx4 v[156:159], v245, s[40:43], s89 offen offset:64\n"
    "buffer_load_dwordx4 v[148:151], v245, s[40:43], s89 offen offset:0\n"
    "ds_read_b128 v[220:223], v251 offset:22512\n"
    "v_fma_mix_f32 v234, v192, v232, 0 op_sel_hi:[1,1,0]\n"
    "v_pk_add_f32 v[224:225], v[224:225], v[64:65]\n"
    "v_pk_add_f32 v[226:227], v[226:227], v[66:67]\n"
    "v_pk_fma_f32 v[228:229], v[64:65], v[64:65], v[228:229]\n"
    "v_pk_fma_f32 v[230:231], v[66:67], v[66:67], v[230:231]\n"
    "v_pk_add_f32 v[224:225], v[224:225], v[68:69]\n"
    "v_pk_add_f32 v[226:227], v[226:227], v[70:71]\n"
    "v_pk_fma_f32 v[228:229], v[68:69], v[68:69], v[228:229]\n"
    "v_pk_fma_f32 v[230:231], v[70:71], v[70:71], v[230:231]\n"
    "v_mfma_f32_16x16x32_bf16 v[112:115], a[200:203], v[184:187], v[112:115]\n"
    "s_waitcnt lgkmcnt(9)\n"
    "ds_read_u16 v197, v197\n"
    "ds_read_u16_d16_hi v205, v205\n"
    "v_mfma_f32_16x16x32_bf16 v[116:119], a[204:207], v[184:187], v[116:119]\n"
    "s_waitcnt lgkmcnt(9)\n"
    "v_or_b32_e64 v193, v193, v201\n"
    "v_or_b32_e64 v194, v194, v202\n"
    "v_mfma_f32_16x16x32_bf16 v[128:131], v[236:239], v[184:187], v[128:131]\n"
    "ds_read_u16 v198, v198\n"
    "v_lshlrev_b32_sdwa v201, 1, v171 dst_sel:DWORD dst_unused:UNUSED_PAD src0_sel:DWORD src1_sel:WORD_1\n"
    "v_mfma_f32_16x16x32_bf16 v[104:107], a[192:195], v[188:191], v[104:107]\n"
    "v_lshlrev_b32_sdwa v202, 1, v173 dst_sel:DWORD dst_unused:UNUSED_PAD src0_sel:DWORD src1_sel:WORD_1\n"
    "v_fma_mix_f32 v232, v192, v232, 0 op_sel:[1,1,0] op_sel_hi:[1,1,0]\n"
    "v_mfma_f32_16x16x32_bf16 v[108:111], a[196:199], v[188:191], v[108:111]\n"
    "ds_read_u16_d16_hi v206, v206\n"
    "v_cvt_pk_bf16_f32 v192, v234, v232\n"
    "v_mfma_f32_16x16x32_bf16 v[120:123], a[200:203], v[188:191], v[120:123]\n"
    "s_waitcnt lgkmcnt(8)\n"
    "ds_read_u16 v199, v199\n"
    "v_or_b32_e32 v195, v195, v203\n"
    "v_mfma_f32_16x16x32_bf16 v[132:135], v[164:167], v[184:187], v[132:135]\n"
    "v_lshlrev_b32_sdwa v184, 1, v168 dst_sel:DWORD dst_unused:UNUSED_PAD src0_sel:DWORD src1_sel:WORD_1\n"
    "v_lshlrev_b32_sdwa v185, 1, v170 dst_sel:DWORD dst_unused:UNUSED_PAD src0_sel:DWORD src1_sel:WORD_1\n"
    "v_mfma_f32_16x16x32_bf16 v[124:127], a[204:207], v[188:191], v[124:127]\n"
    "s_mov_b32 exec_hi, 0\n"
    "buffer_load_dword v233, v254, s[72:75], s53 offen sc1\n"
    "s_mov_b32 exec_hi, -1\n"
    "s_add_u32 s53, s53, 393216\n"
    "v_mfma_f32_16x16x32_bf16 v[136:139], v[236:239], v[188:191], v[136:139]\n"
    "s_waitcnt lgkmcnt(6)\n"
    "ds_read_u16_d16_hi v207, v207\n"
    "ds_read_b128 a[252:255], v244 offset:36864\n"
    "v_smfmac_f32_16x16x64_bf16 v[72:75], v[216:219], v[208:215], v240\n"
    "s_waitcnt lgkmcnt(7)\n"
    "ds_read_b128 v[216:219], v252 offset:22176\n"
    "v_smfmac_f32_16x16x64_bf16 v[76:79], v[220:223], v[208:215], v240 abid:1\n"
    "ds_read_b128 v[208:211], v250 offset:720\n"
    "v_mfma_f32_16x16x32_bf16 v[140:143], v[164:167], v[188:191], v[140:143]\n"
    "v_lshlrev_b32_sdwa v186, 1, v172 dst_sel:DWORD dst_unused:UNUSED_PAD src0_sel:DWORD src1_sel:WORD_1\n"
    "v_lshlrev_b32_sdwa v187, 1, v174 dst_sel:DWORD dst_unused:UNUSED_PAD src0_sel:DWORD src1_sel:WORD_1\n"
    "v_mfma_f32_16x16x32_bf16 v[168:171], a[88:91], a[224:227], 0\n"
    "ds_read_b128 v[212:215], v250 offset:1040\n"
    "ds_read_b128 v[220:223], v252 offset:22512\n"
    "v_mfma_f32_16x16x32_bf16 v[168:171], a[136:139], a[228:231], v[168:171]\n"
    "v_lshlrev_b32_sdwa v203, 1, v175 dst_sel:DWORD dst_unused:UNUSED_PAD src0_sel:DWORD src1_sel:WORD_1\n"
    "v_lshlrev_b32_sdwa v188, 1, v176 dst_sel:DWORD dst_unused:UNUSED_PAD src0_sel:DWORD src1_sel:WORD_1\n"
    "v_mfma_f32_16x16x32_bf16 v[168:171], a[52:55], a[232:235], v[168:171]\n"
    "s_waitcnt lgkmcnt(9)\n"
    "ds_read_u16 v184, v184\n"
    "v_or_b32_e32 v197, v197, v205\n"
    "v_mfma_f32_16x16x32_bf16 v[172:175], a[92:95], a[224:227], 0\n"
    "ds_read_u16_d16_hi v200, v200\n"
    "v_lshlrev_b32_sdwa v189, 1, v178 dst_sel:DWORD dst_unused:UNUSED_PAD src0_sel:DWORD src1_sel:WORD_1\n"
    "v_mfma_f32_16x16x32_bf16 v[172:175], a[140:143], a[228:231], v[172:175]\n"
    "v_lshlrev_b32_sdwa v205, 1, v179 dst_sel:DWORD dst_unused:UNUSED_PAD src0_sel:DWORD src1_sel:WORD_1\n"
    "v_lshlrev_b32_sdwa v190, 1, v180 dst_sel:DWORD dst_unused:UNUSED_PAD src0_sel:DWORD src1_sel:WORD_1\n"
    "v_mfma_f32_16x16x32_bf16 v[176:179], a[88:91], a[236:239], 0\n"
    "s_waitcnt lgkmcnt(9)\n"
    "ds_read_u16 v185, v185\n"
    "v_or_b32_e32 v198, v198, v206\n"
    "v_mfma_f32_16x16x32_bf16 v[176:179], a[136:139], a[240:243], v[176:179]\n"
    "v_lshlrev_b32_sdwa v206, 1, v181 dst_sel:DWORD dst_unused:UNUSED_PAD src0_sel:DWORD src1_sel:WORD_1\n"
    "v_lshlrev_b32_sdwa v191, 1, v182 dst_sel:DWORD dst_unused:UNUSED_PAD src0_sel:DWORD src1_sel:WORD_1\n"
    "v_mfma_f32_16x16x32_bf16 v[176:179], a[52:55], a[244:247], v[176:179]\n"
    "ds_read_u16_d16_hi v201, v201\n"
    "v_pk_fma_f16 v232, v196, v196, s48\n"
    "v_pk_mul_f16 v232, v196, v232\n"
    "v_exp_f16_e32 v234, v232\n"
    "s_waitcnt lgkmcnt(8)\n"
    "v_mfma_f32_16x16x32_bf16 v[172:175], a[252:255], a[232:235], v[172:175]\n"
    "ds_read_u16 v186, v186\n"
    "v_or_b32_e64 v199, v199, v207\n"
    "v_lshlrev_b32_sdwa v207, 1, v183 dst_sel:DWORD dst_unused:UNUSED_PAD src0_sel:DWORD src1_sel:WORD_1\n"
    "v_mfma_f32_16x16x32_bf16 v[180:183], a[92:95], a[236:239], 0\n"
    "ds_read_b128 v[236:239], v244 offset:20480\n"
    "v_exp_f16_sdwa v234, v232 dst_sel:WORD_1 dst_unused:UNUSED_PRESERVE src0_sel:WORD_1\n"
    "v_mfma_f32_16x16x32_bf16 v[180:183], a[140:143], a[240:243], v[180:183]\n"
    "s_waitcnt lgkmcnt(7)\n"
    "ds_read_u16_d16_hi v202, v202\n"
    "v_pk_fma_f16 v234, v234, s49, s49\n"
    "v_smfmac_f32_16x16x64_bf16 v[72:75], v[216:219], v[208:215], v240\n"
    "s_waitcnt lgkmcnt(7)\n"
    "ds_read_b128 v[216:219], v251 offset:22848\n"
    "v_smfmac_f32_16x16x64_bf16 v[76:79], v[220:223], v[208:215], v240 abid:1\n"
    "ds_read_b128 v[208:211], v250 offset:160\n"
    "v_mfma_f32_16x16x32_bf16 v[180:183], a[252:255], a[244:247], v[180:183]\n"
    "v_rcp_f16_e32 v232, v234\n"
    "v_mfma_f32_16x16x32_bf16 v[96:99], a[208:211], v[192:195], v[96:99]\n"
    "ds_read_b128 v[212:215], v250 offset:480\n"
    "ds_read_b128 v[220:223], v251 offset:23184\n"
    "v_rcp_f16_sdwa v232, v234 dst_sel:WORD_1 dst_unused:UNUSED_PRESERVE src0_sel:WORD_1\n"
    "v_mfma_f32_16x16x32_bf16 v[100:103], a[212:215], v[192:195], v[100:103]\n"
    "s_waitcnt lgkmcnt(9)\n"
    "ds_read_u16 v187, v187\n"
    "v_fma_mix_f32 v234, v196, v232, 0 op_sel_hi:[1,1,0]\n"
    "v_fma_mix_f32 v232, v196, v232, 0 op_sel:[1,1,0] op_sel_hi:[1,1,0]\n"
    "v_or_b32_e64 v184, v184, v200\n"
    "v_cvt_pk_bf16_f32 v196, v234, v232\n"
    "v_pk_add_f32 v[224:225], v[224:225], v[72:73]\n"
    "v_pk_add_f32 v[226:227], v[226:227], v[74:75]\n"
    "v_pk_fma_f32 v[228:229], v[72:73], v[72:73], v[228:229]\n"
    "v_pk_fma_f32 v[230:231], v[74:75], v[74:75], v[230:231]\n"
    "v_pk_add_f32 v[224:225], v[224:225], v[76:77]\n"
    "v_pk_add_f32 v[226:227], v[226:227], v[78:79]\n"
    "v_pk_fma_f32 v[228:229], v[76:77], v[76:77], v[228:229]\n"
    "v_pk_fma_f32 v[230:231], v[78:79], v[78:79], v[230:231]\n"
    "v_mfma_f32_16x16x32_bf16 v[112:115], a[216:219], v[192:195], v[112:115]\n"
    "s_waitcnt lgkmcnt(8)\n"
    "ds_read_b128 v[164:167], v244 offset:22528\n"
    "ds_read_u16_d16_hi v203, v203\n"
    "v_mfma_f32_16x16x32_bf16 v[116:119], a[220:223], v[192:195], v[116:119]\n"
    "s_nop 0\n"
    "ds_read_u16 v188, v188\n"
    "v_lshlrev_b32_e64 v254, 3, v244\n"
    "v_mfma_f32_16x16x32_bf16 v[104:107], a[208:211], v[196:199], v[104:107]\n"
    "s_waitcnt lgkmcnt(9)\n"
    "v_or_b32_e32 v185, v185, v201\n"
    "v_lshlrev_b32_sdwa v201, 1, v171 dst_sel:DWORD dst_unused:UNUSED_PAD src0_sel:DWORD src1_sel:WORD_1\n"
    "v_mfma_f32_16x16x32_bf16 v[128:131], v[236:239], v[192:195], v[128:131]\n"
    "ds_read_u16_d16_hi v204, v204\n"
    "v_cvt_pk_bf16_f32 v96, v96, v97\n"
    "v_mfma_f32_16x16x32_bf16 v[108:111], a[212:215], v[196:199], v[108:111]\n"
    "ds_read_u16 v189, v189\n"
    "v_cvt_pk_bf16_f32 v97, v98, v99\n"
    "v_mfma_f32_16x16x32_bf16 v[120:123], a[216:219], v[196:199], v[120:123]\n"
    "s_waitcnt lgkmcnt(8)\n"
    "v_or_b32_e32 v186, v186, v202\n"
    "v_lshlrev_b32_sdwa v202, 1, v173 dst_sel:DWORD dst_unused:UNUSED_PAD src0_sel:DWORD src1_sel:WORD_1\n"
    "v_mfma_f32_16x16x32_bf16 v[124:127], a[220:223], v[196:199], v[124:127]\n"
    "s_waitcnt lgkmcnt(7)\n"
    "ds_read_u16_d16_hi v205, v205\n"
    "v_cvt_pk_bf16_f32 v98, v100, v101\n"
    "v_smfmac_f32_16x16x64_bf16 v[80:83], v[216:219], v[208:215], v240\n"
    "s_waitcnt lgkmcnt(7)\n"
    "v_cvt_pk_bf16_f32 v99, v102, v103\n"
    "v_smfmac_f32_16x16x64_bf16 v[84:87], v[220:223], v[208:215], v240 abid:1\n"
    "v_cvt_pk_bf16_f32 v112, v112, v113\n"
    "v_mfma_f32_16x16x32_bf16 v[136:139], v[236:239], v[196:199], v[136:139]\n"
    "s_nop 0\n"
    "ds_write_b128 v243, v[96:99] offset:24640\n"
    "s_waitcnt lgkmcnt(6)\n"
    "v_mfma_f32_16x16x32_bf16 v[132:135], v[164:167], v[192:195], v[132:135]\n"
    "s_mov_b32 exec_hi, 0\n"
    "buffer_load_dword v233, v254, s[72:75], s53 offen sc1\n"
    "s_mov_b32 exec_hi, -1\n"
    "v_mfma_f32_16x16x32_bf16 v[140:143], v[164:167], v[196:199], v[140:143]\n"
    "s_add_u32 s53, s53, 393216\n"
    "v_cvt_pk_f16_f32 v192, v168, v169\n"
    "ds_read_b128 v[208:211], v250 offset:800\n"
    "ds_read_b128 v[212:215], v250 offset:1120\n"
    "ds_read_b128 v[216:219], v252 offset:22848\n"
    "v_lshlrev_b32_sdwa v193, 1, v170 dst_sel:DWORD dst_unused:UNUSED_PAD src0_sel:DWORD src1_sel:WORD_1\n"
    "v_mfma_f32_16x16x32_bf16 v[168:171], a[96:99], a[224:227], 0\n"
    "s_waitcnt lgkmcnt(8)\n"
    "ds_read_b128 v[220:223], v252 offset:23184\n"
    "ds_read_u16 v190, v190\n"
    "v_mfma_f32_16x16x32_bf16 v[168:171], a[144:147], a[228:231], v[168:171]\n"
    "ds_read_u16_d16_hi v206, v206\n"
    "v_lshlrev_b32_sdwa v194, 1, v172 dst_sel:DWORD dst_unused:UNUSED_PAD src0_sel:DWORD src1_sel:WORD_1\n"
    "v_mfma_f32_16x16x32_bf16 v[168:171], a[56:59], a[232:235], v[168:171]\n"
    "s_waitcnt lgkmcnt(9)\n"
    "v_lshlrev_b32_sdwa v195, 1, v174 dst_sel:DWORD dst_unused:UNUSED_PAD src0_sel:DWORD src1_sel:WORD_1\n"
    "v_cvt_pk_f16_f32 v196, v176, v177\n"
    "v_lshlrev_b32_sdwa v197, 1, v178 dst_sel:DWORD dst_unused:UNUSED_PAD src0_sel:DWORD src1_sel:WORD_1\n"
    "v_lshlrev_b32_sdwa v198, 1, v180 dst_sel:DWORD dst_unused:UNUSED_PAD src0_sel:DWORD src1_sel:WORD_1\n"
    "v_lshlrev_b32_sdwa v199, 1, v182 dst_sel:DWORD dst_unused:UNUSED_PAD src0_sel:DWORD src1_sel:WORD_1\n"
    "v_or_b32_e64 v187, v187, v203\n"
    "v_lshlrev_b32_sdwa v203, 1, v175 dst_sel:DWORD dst_unused:UNUSED_PAD src0_sel:DWORD src1_sel:WORD_1\n"
    "v_mfma_f32_16x16x32_bf16 v[172:175], a[100:103], a[224:227], 0\n"
    "ds_read_u16 v191, v191\n"
    "ds_read_u16_d16_hi v207, v207\n"
    "v_mfma_f32_16x16x32_bf16 v[172:175], a[148:151], a[228:231], v[172:175]\n"
    "v_or_b32_e64 v188, v188, v204\n"
    "v_cvt_pk_bf16_f32 v113, v114, v115\n"
    "v_mfma_f32_16x16x32_bf16 v[172:175], a[60:63], a[232:235], v[172:175]\n"
    "s_waitcnt lgkmcnt(8)\n"
    "ds_read_u16 v193, v193\n"
    "v_or_b32_e32 v189, v189, v205\n"
    "v_lshlrev_b32_sdwa v205, 1, v179 dst_sel:DWORD dst_unused:UNUSED_PAD src0_sel:DWORD src1_sel:WORD_1\n"
    "v_mfma_f32_16x16x32_bf16 v[176:179], a[96:99], a[236:239], 0\n"
    "v_cvt_pk_bf16_f32 v114, v116, v117\n"
    "v_cvt_pk_bf16_f32 v115, v118, v119\n"
    "v_mfma_f32_16x16x32_bf16 v[176:179], a[144:147], a[240:243], v[176:179]\n"
    "v_cvt_pk_bf16_f32 v128, v128, v129\n"
    "v_cvt_pk_bf16_f32 v129, v130, v131\n"
    "v_mfma_f32_16x16x32_bf16 v[176:179], a[56:59], a[244:247], v[176:179]\n"
    "s_waitcnt lgkmcnt(6)\n"
    "ds_write_b128 v243, v[112:115] offset:26688\n"
    "v_smfmac_f32_16x16x64_bf16 v[80:83], v[216:219], v[208:215], v240\n"
    "s_waitcnt lgkmcnt(6)\n"
    "v_cvt_pk_bf16_f32 v130, v132, v133\n"
    "v_smfmac_f32_16x16x64_bf16 v[84:87], v[220:223], v[208:215], v240 abid:1\n"
    "v_cvt_pk_bf16_f32 v131, v134, v135\n"
    "v_cvt_pk_bf16_f32 v104, v104, v105\n"
    "v_cvt_pk_bf16_f32 v105, v106, v107\n"
    "ds_read_b128 v[208:211], v250 offset:240\n"
    "ds_write_b128 v243, v[128:131] offset:28736\n"
    "v_pk_add_f32 v[224:225], v[224:225], v[80:81]\n"
    "v_pk_add_f32 v[226:227], v[226:227], v[82:83]\n"
    "v_pk_fma_f32 v[228:229], v[80:81], v[80:81], v[228:229]\n"
    "v_pk_fma_f32 v[230:231], v[82:83], v[82:83], v[230:231]\n"
    "v_pk_add_f32 v[224:225], v[224:225], v[84:85]\n"
    "v_pk_add_f32 v[226:227], v[226:227], v[86:87]\n"
    "v_pk_fma_f32 v[228:229], v[84:85], v[84:85], v[228:229]\n"
    "v_pk_fma_f32 v[230:231], v[86:87], v[86:87], v[230:231]\n"
    "ds_read_b128 v[212:215], v250 offset:560\n"
    "ds_read_b128 v[216:219], v251 offset:23520\n"
    "ds_read_b128 v[220:223], v251 offset:23856\n"
    "s_waitcnt lgkmcnt(7)\n"
    "v_or_b32_e32 v190, v190, v206\n"
    "v_mfma_f32_16x16x32_bf16 v[96:99], a[0:3], v[184:187], 0\n"
    "ds_read_u16_d16_hi v201, v201\n"
    "ds_read_u16 v194, v194\n"
    "v_mfma_f32_16x16x32_bf16 v[100:103], a[4:7], v[184:187], 0\n"
    "ds_read_u16_d16_hi v202, v202\n"
    "v_lshlrev_b32_sdwa v206, 1, v181 dst_sel:DWORD dst_unused:UNUSED_PAD src0_sel:DWORD src1_sel:WORD_1\n"
    "v_mfma_f32_16x16x32_bf16 v[112:115], a[8:11], v[184:187], 0\n"
    "ds_read_b128 v[236:239], v244 offset:0\n"
    "v_or_b32_e64 v191, v191, v207\n"
    "v_mfma_f32_16x16x32_bf16 v[116:119], a[12:15], v[184:187], 0\n"
    "ds_read_u16 v195, v195\n"
    "v_lshlrev_b32_sdwa v207, 1, v183 dst_sel:DWORD dst_unused:UNUSED_PAD src0_sel:DWORD src1_sel:WORD_1\n"
    "v_mfma_f32_16x16x32_bf16 v[180:183], a[100:103], a[236:239], 0\n"
    "v_cvt_pk_bf16_f32 v106, v108, v109\n"
    "v_cvt_pk_bf16_f32 v107, v110, v111\n"
    "v_mfma_f32_16x16x32_bf16 v[180:183], a[148:151], a[240:243], v[180:183]\n"
    "v_cvt_pk_bf16_f32 v120, v120, v121\n"
    "v_cvt_pk_bf16_f32 v121, v122, v123\n"
    "v_mfma_f32_16x16x32_bf16 v[180:183], a[60:63], a[244:247], v[180:183]\n"
    "v_cvt_pk_bf16_f32 v122, v124, v125\n"
    "v_cvt_pk_bf16_f32 v123, v126, v127\n"
    "v_mfma_f32_16x16x32_bf16 v[108:111], a[4:7], v[188:191], 0\n"
    "s_waitcnt lgkmcnt(6)\n"
    "ds_write_b128 v243, v[104:107] offset:25664\n"
    "v_smfmac_f32_16x16x64_bf16 v[88:91], v[216:219], v[208:215], v240\n"
    "s_waitcnt lgkmcnt(6)\n"
    "v_add_u32_e32 v254, s54, v242\n"
    "v_smfmac_f32_16x16x64_bf16 v[92:95], v[220:223], v[208:215], v240 abid:1\n"
    "v_cvt_pk_bf16_f32 v136, v136, v137\n"
    "v_mfma_f32_16x16x32_bf16 v[104:107], a[0:3], v[188:191], 0\n"
    "s_waitcnt lgkmcnt(5)\n"
    "ds_read_b128 v[208:211], v250 offset:880\n"
    "ds_read_b128 v[212:215], v250 offset:1200\n"
    "v_or_b32_e64 v193, v193, v201\n"
    "ds_write_b128 v243, v[120:123] offset:27712\n"
    "v_mfma_f32_16x16x32_bf16 v[120:123], a[8:11], v[188:191], 0\n"
    "v_pk_fma_f16 v232, v192, v192, s48\n"
    "v_cvt_pk_bf16_f32 v137, v138, v139\n"
    "v_mfma_f32_16x16x32_bf16 v[124:127], a[12:15], v[188:191], 0\n"
    "s_waitcnt lgkmcnt(5)\n"
    "v_or_b32_e32 v194, v194, v202\n"
    "v_cvt_pk_bf16_f32 v138, v140, v141\n"
    "v_mfma_f32_16x16x32_bf16 v[128:131], v[236:239], v[184:187], 0\n"
    "ds_read_b128 v[216:219], v252 offset:23520\n"
    "ds_read_b128 v[220:223], v252 offset:23856\n"
    "v_cvt_pk_bf16_f32 v139, v142, v143\n"
    "ds_write_b128 v243, v[136:139] offset:29760\n"
    "v_pk_mul_f16 v232, v192, v232\n"
    "v_mfma_f32_16x16x32_bf16 v[136:139], v[236:239], v[188:191], 0\n"
    "v_exp_f16_e64 v234, v232\n"
    "v_lshlrev_b32_sdwa v200, 1, v169 dst_sel:DWORD dst_unused:UNUSED_PAD src0_sel:DWORD src1_sel:WORD_1\n"
    "v_exp_f16_sdwa v234, v232 dst_sel:WORD_1 dst_unused:UNUSED_PRESERVE src0_sel:WORD_1\n"
    "ds_read_u16_d16_hi v203, v203\n"
    "ds_read_b128 v[164:167], v244 offset:2048\n"
    "v_pk_fma_f16 v234, v234, s49, s49\n"
    "ds_read_u16 v197, v197\n"
    "ds_read_u16_d16_hi v205, v205\n"
    "v_lshlrev_b32_sdwa v201, 1, v171 dst_sel:DWORD dst_unused:UNUSED_PAD src0_sel:DWORD src1_sel:WORD_1\n"
    "v_lshlrev_b32_sdwa v202, 1, v173 dst_sel:DWORD dst_unused:UNUSED_PAD src0_sel:DWORD src1_sel:WORD_1\n"
    "v_rcp_f16_e64 v232, v234\n"
    "v_lshlrev_b32_sdwa v204, 1, v177 dst_sel:DWORD dst_unused:UNUSED_PAD src0_sel:DWORD src1_sel:WORD_1\n"
    "s_waitcnt lgkmcnt(6)\n"
    "v_smfmac_f32_16x16x64_bf16 v[88:91], v[216:219], v[208:215], v240\n"
    "s_waitcnt lgkmcnt(5)\n"
    "ds_read_u16 v198, v198\n"
    "ds_read_u16_d16_hi v206, v206\n"
    "v_smfmac_f32_16x16x64_bf16 v[92:95], v[220:223], v[208:215], v240 abid:1\n"
    "ds_read_u16 v199, v199\n"
    "v_rcp_f16_sdwa v232, v234 dst_sel:WORD_1 dst_unused:UNUSED_PRESERVE src0_sel:WORD_1\n"
    "ds_read_u16_d16_hi v207, v207\n"
    "v_fma_mix_f32 v234, v192, v232, 0 op_sel_hi:[1,1,0]\n"
    "v_pk_add_f32 v[224:225], v[224:225], v[88:89]\n"
    "v_pk_add_f32 v[226:227], v[226:227], v[90:91]\n"
    "v_pk_fma_f32 v[228:229], v[88:89], v[88:89], v[228:229]\n"
    "v_pk_fma_f32 v[230:231], v[90:91], v[90:91], v[230:231]\n"
    "v_pk_add_f32 v[224:225], v[224:225], v[92:93]\n"
    "v_pk_add_f32 v[226:227], v[226:227], v[94:95]\n"
    "v_pk_fma_f32 v[228:229], v[92:93], v[92:93], v[228:229]\n"
    "v_pk_fma_f32 v[230:231], v[94:95], v[94:95], v[230:231]\n"
    "v_fma_mix_f32 v232, v192, v232, 0 op_sel:[1,1,0] op_sel_hi:[1,1,0]\n"
    "s_waitcnt lgkmcnt(6)\n"
    "v_mfma_f32_16x16x32_bf16 v[132:135], v[164:167], v[184:187], 0\n"
    "v_or_b32_e32 v195, v195, v203\n"
    "v_lshlrev_b32_sdwa v184, 1, v168 dst_sel:DWORD dst_unused:UNUSED_PAD src0_sel:DWORD src1_sel:WORD_1\n"
    "v_mfma_f32_16x16x32_bf16 v[140:143], v[164:167], v[188:191], 0\n"
    "ds_write_b128 v254, v[224:227]\n"
    "v_lshlrev_b32_sdwa v185, 1, v170 dst_sel:DWORD dst_unused:UNUSED_PAD src0_sel:DWORD src1_sel:WORD_1\n"
    "v_mfma_f32_16x16x32_bf16 v[168:171], a[104:107], a[224:227], 0\n"
    "v_lshlrev_b32_sdwa v186, 1, v172 dst_sel:DWORD dst_unused:UNUSED_PAD src0_sel:DWORD src1_sel:WORD_1\n"
    "v_lshlrev_b32_sdwa v187, 1, v174 dst_sel:DWORD dst_unused:UNUSED_PAD src0_sel:DWORD src1_sel:WORD_1\n"
    "v_mfma_f32_16x16x32_bf16 v[168:171], a[152:155], a[228:231], v[168:171]\n"
    "s_waitcnt lgkmcnt(5)\n"
    "v_or_b32_e32 v197, v197, v205\n"
    "v_lshlrev_b32_sdwa v203, 1, v175 dst_sel:DWORD dst_unused:UNUSED_PAD src0_sel:DWORD src1_sel:WORD_1\n"
    "v_mfma_f32_16x16x32_bf16 v[168:171], a[64:67], a[232:235], v[168:171]\n"
    "ds_read_u16 v184, v184\n"
    "ds_write_b128 v254, v[228:231] offset:1024\n"
    "v_mfma_f32_16x16x32_bf16 v[172:175], a[108:111], a[224:227], 0\n"
    "v_lshrrev_b32_e64 v254, 2, v242\n"
    "v_lshlrev_b32_sdwa v188, 1, v176 dst_sel:DWORD dst_unused:UNUSED_PAD src0_sel:DWORD src1_sel:WORD_1\n"
    "v_mfma_f32_16x16x32_bf16 v[172:175], a[156:159], a[228:231], v[172:175]\n"
    "s_waitcnt lgkmcnt(5)\n"
    "v_add_u32_e32 v254, s55, v254\n"
    "v_or_b32_e64 v198, v198, v206\n"
    "v_mfma_f32_16x16x32_bf16 v[172:175], a[68:71], a[232:235], v[172:175]\n"
    "ds_read_u16_d16_hi v200, v200\n"
    "ds_read_u16 v185, v185\n"
    "v_lshlrev_b32_sdwa v189, 1, v178 dst_sel:DWORD dst_unused:UNUSED_PAD src0_sel:DWORD src1_sel:WORD_1\n"
    "v_lshlrev_b32_sdwa v205, 1, v179 dst_sel:DWORD dst_unused:UNUSED_PAD src0_sel:DWORD src1_sel:WORD_1\n"
    "v_mfma_f32_16x16x32_bf16 v[176:179], a[104:107], a[236:239], 0\n"
    "ds_read_u16_d16_hi v201, v201\n"
    "ds_read_u16 v186, v186\n"
    "v_lshlrev_b32_sdwa v190, 1, v180 dst_sel:DWORD dst_unused:UNUSED_PAD src0_sel:DWORD src1_sel:WORD_1\n"
    "v_lshlrev_b32_sdwa v206, 1, v181 dst_sel:DWORD dst_unused:UNUSED_PAD src0_sel:DWORD src1_sel:WORD_1\n"
    "v_lshlrev_b32_sdwa v191, 1, v182 dst_sel:DWORD dst_unused:UNUSED_PAD src0_sel:DWORD src1_sel:WORD_1\n"
    "s_waitcnt lgkmcnt(6)\n"
    "v_or_b32_e32 v199, v199, v207\n"
    "v_lshlrev_b32_sdwa v207, 1, v183 dst_sel:DWORD dst_unused:UNUSED_PAD src0_sel:DWORD src1_sel:WORD_1\n"
    "v_cvt_pk_bf16_f32 v192, v234, v232\n"
    "v_mfma_f32_16x16x32_bf16 v[176:179], a[152:155], a[240:243], v[176:179]\n"
    "ds_read_b128 v[236:239], v244 offset:4096\n"
    "ds_read_u16_d16_hi v202, v202\n"
    "v_mfma_f32_16x16x32_bf16 v[176:179], a[64:67], a[244:247], v[176:179]\n"
    "v_pk_fma_f16 v232, v196, v196, s48\n"
    "v_pk_mul_f16 v232, v196, v232\n"
    "v_mfma_f32_16x16x32_bf16 v[180:183], a[108:111], a[236:239], 0\n"
    "s_waitcnt lgkmcnt(6)\n"
    "ds_read_u16 v187, v187\n"
    "v_exp_f16_e32 v234, v232\n"
    "v_mfma_f32_16x16x32_bf16 v[180:183], a[156:159], a[240:243], v[180:183]\n"
    "s_barrier\n"
    "v_mfma_f32_16x16x32_bf16 v[180:183], a[68:71], a[244:247], v[180:183]\n"
    "s_waitcnt lgkmcnt(6)\n"
    "ds_read_b32 v224, v254 offset:0\n"
    "ds_read_b32 v225, v254 offset:6752\n"
    "v_exp_f16_sdwa v234, v232 dst_sel:WORD_1 dst_unused:UNUSED_PRESERVE src0_sel:WORD_1\n"
    "v_mfma_f32_16x16x32_bf16 v[96:99], a[16:19], v[192:195], v[96:99]\n"
    "ds_read_b32 v226, v254 offset:13504\n"
    "ds_read_b32 v227, v254 offset:20256\n"
    "v_pk_fma_f16 v234, v234, s49, s49\n"
    "v_mfma_f32_16x16x32_bf16 v[100:103], a[20:23], v[192:195], v[100:103]\n"
    "s_waitcnt lgkmcnt(8)\n"
    "ds_read_b32 v228, v254 offset:1024\n"
    "ds_read_b32 v229, v254 offset:7776\n"
    "v_rcp_f16_e64 v232, v234\n"
    "v_mfma_f32_16x16x32_bf16 v[112:115], a[24:27], v[192:195], v[112:115]\n"
    "ds_read_b32 v230, v254 offset:14528\n"
    "ds_read_b32 v231, v254 offset:21280\n"
    "v_rcp_f16_sdwa v232, v234 dst_sel:WORD_1 dst_unused:UNUSED_PRESERVE src0_sel:WORD_1\n"
    "v_mfma_f32_16x16x32_bf16 v[116:119], a[28:31], v[192:195], v[116:119]\n"
    "s_waitcnt lgkmcnt(10)\n"
    "ds_read_b128 v[164:167], v244 offset:6144\n"
    "ds_read_u16_d16_hi v203, v203\n"
    "v_mfma_f32_16x16x32_bf16 v[128:131], v[236:239], v[192:195], v[128:131]\n"
    "ds_read_u16 v188, v188\n"
    "ds_read_u16_d16_hi v204, v204\n"
    "v_fma_mix_f32 v234, v196, v232, 0 op_sel_hi:[1,1,0]\n"
    "v_fma_mix_f32 v232, v196, v232, 0 op_sel:[1,1,0] op_sel_hi:[1,1,0]\n"
    "v_or_b32_e64 v184, v184, v200\n"
    "v_cvt_pk_bf16_f32 v196, v234, v232\n"
    "v_or_b32_e32 v185, v185, v201\n"
    "s_waitcnt lgkmcnt(10)\n"
    "v_add_f32_e32 v224, v224, v225\n"
    "v_mfma_f32_16x16x32_bf16 v[104:107], a[16:19], v[196:199], v[104:107]\n"
    "s_waitcnt lgkmcnt(8)\n"
    "ds_read_u16 v189, v189\n"
    "ds_read_u16_d16_hi v205, v205\n"
    "v_mfma_f32_16x16x32_bf16 v[108:111], a[20:23], v[196:199], v[108:111]\n"
    "s_waitcnt vmcnt(2)\n"
    "v_add_f32_e32 v226, v226, v227\n"
    "v_lshlrev_b32_sdwa v201, 1, v171 dst_sel:DWORD dst_unused:UNUSED_PAD src0_sel:DWORD src1_sel:WORD_1\n"
    "v_mfma_f32_16x16x32_bf16 v[120:123], a[24:27], v[196:199], v[120:123]\n"
    "s_waitcnt lgkmcnt(8)\n"
    "ds_read_u16 v190, v190\n"
    "v_add_f32_e32 v228, v228, v229\n"
    "v_mfma_f32_16x16x32_bf16 v[124:127], a[28:31], v[196:199], v[124:127]\n"
    "s_waitcnt lgkmcnt(7)\n"
    "v_add_f32_e32 v230, v230, v231\n"
    "v_add_f32_e64 v224, v224, v226\n"
    "v_mfma_f32_16x16x32_bf16 v[136:139], v[236:239], v[196:199], v[136:139]\n"
    "ds_read_u16_d16_hi v206, v206\n"
    "v_add_f32_e32 v228, v228, v230\n"
    "v_smfmac_f32_32x32x32_bf16 v[112:127], a[248:251], v[152:159], v241\n"
    "s_waitcnt lgkmcnt(7)\n"
    "v_mul_f32_e32 v224, 0x3c2aaaab, v224\n"
    "v_mul_f32_e32 v228, 0x3c2aaaab, v228\n"
    "v_or_b32_e64 v186, v186, v202\n"
    "v_fma_f32 v228, -v224, v224, v228\n"
    "v_lshlrev_b32_sdwa v202, 1, v173 dst_sel:DWORD dst_unused:UNUSED_PAD src0_sel:DWORD src1_sel:WORD_1\n"
    "v_mfma_f32_16x16x32_bf16 v[132:135], v[164:167], v[192:195], v[132:135]\n"
    "ds_read_u16 v191, v191\n"
    "ds_read_u16_d16_hi v207, v207\n"
    "v_mfma_f32_16x16x32_bf16 v[140:143], v[164:167], v[196:199], v[140:143]\n"
    "s_waitcnt lgkmcnt(8)\n"
    "v_max_f32_e32 v228, 0, v228\n"
    "v_cvt_pk_f16_f32 v192, v168, v169\n"
    "v_smfmac_f32_32x32x32_bf16 v[96:111], a[248:251], v[144:151], v241\n"
    "ds_read_b128 a[252:255], v244 offset:34816\n"
    "buffer_load_dwordx4 v[144:147], v245, s[40:43], s58 offen offset:128\n"
    "v_add_f32_e32 v228, 0x3727c5ac, v228\n"
    "v_lshlrev_b32_sdwa v193, 1, v170 dst_sel:DWORD dst_unused:UNUSED_PAD src0_sel:DWORD src1_sel:WORD_1\n"
    "v_rsq_f32_e32 v228, v228\n"
    "v_mfma_f32_16x16x32_bf16 v[168:171], a[112:115], a[224:227], 0\n"
    "s_waitcnt lgkmcnt(7)\n"
    "ds_read_u16 v193, v193\n"
    "v_mul_f32_e64 v228, s50, v228\n"
    "v_mfma_f32_16x16x32_bf16 v[168:171], a[160:163], a[228:231], v[168:171]\n"
    "v_mul_f32_e64 v224, v228, -v224\n"
    "v_lshlrev_b32_sdwa v194, 1, v172 dst_sel:DWORD dst_unused:UNUSED_PAD src0_sel:DWORD src1_sel:WORD_1\n"
    "v_mfma_f32_16x16x32_bf16 v[168:171], a[72:75], a[232:235], v[168:171]\n"
    "ds_write_b32 v254, v228\n"
    "v_lshlrev_b32_sdwa v195, 1, v174 dst_sel:DWORD dst_unused:UNUSED_PAD src0_sel:DWORD src1_sel:WORD_1\n"
    "v_cvt_pk_f16_f32 v196, v176, v177\n"
    "ds_write_b32 v254, v224 offset:1024\n"
    "ds_read_u16_d16_hi v201, v201\n"
    "v_add_u32_e64 v254, s56, v242\n"
    "v_lshlrev_b32_sdwa v197, 1, v178 dst_sel:DWORD dst_unused:UNUSED_PAD src0_sel:DWORD src1_sel:WORD_1\n"
    "v_lshlrev_b32_sdwa v198, 1, v180 dst_sel:DWORD dst_unused:UNUSED_PAD src0_sel:DWORD src1_sel:WORD_1\n"
    "v_lshlrev_b32_sdwa v199, 1, v182 dst_sel:DWORD dst_unused:UNUSED_PAD src0_sel:DWORD src1_sel:WORD_1\n"
    "v_or_b32_e64 v187, v187, v203\n"
    "v_lshlrev_b32_sdwa v203, 1, v175 dst_sel:DWORD dst_unused:UNUSED_PAD src0_sel:DWORD src1_sel:WORD_1\n"
    "v_mfma_f32_16x16x32_bf16 v[172:175], a[116:119], a[224:227], 0\n"
    "s_waitcnt lgkmcnt(8)\n"
    "ds_read_u16 v194, v194\n"
    "ds_read_u16_d16_hi v202, v202\n"
    "v_mfma_f32_16x16x32_bf16 v[172:175], a[164:167], a[228:231], v[172:175]\n"
    "s_waitcnt lgkmcnt(9)\n"
    "ds_read_b128 v[236:239], v244 offset:8192\n"
    "v_or_b32_e64 v188, v188, v204\n"
    "v_or_b32_e64 v189, v189, v205\n"
    "v_lshlrev_b32_sdwa v205, 1, v179 dst_sel:DWORD dst_unused:UNUSED_PAD src0_sel:DWORD src1_sel:WORD_1\n"
    "v_mfma_f32_16x16x32_bf16 v[176:179], a[112:115], a[236:239], 0\n"
    "s_nop 0\n"
    "ds_read_b128 v[152:155], v242 offset:24640\n"
    "ds_read_b128 v[156:159], v242 offset:25664\n"
    "v_mfma_f32_16x16x32_bf16 v[176:179], a[160:163], a[240:243], v[176:179]\n"
    "s_waitcnt lgkmcnt(9)\n"
    "ds_read_u16 v195, v195\n"
    "buffer_load_dwordx4 v[148:151], v245, s[40:43], s89 offen offset:128\n"
    "v_mfma_f32_16x16x32_bf16 v[172:175], a[252:255], a[232:235], v[172:175]\n"
    "v_or_b32_e32 v190, v190, v206\n"
    "v_lshlrev_b32_sdwa v206, 1, v181 dst_sel:DWORD dst_unused:UNUSED_PAD src0_sel:DWORD src1_sel:WORD_1\n"
    "v_mfma_f32_16x16x32_bf16 v[176:179], a[72:75], a[244:247], v[176:179]\n"
    "s_waitcnt lgkmcnt(7)\n"
    "s_barrier\n"
    "v_mfma_f32_16x16x32_bf16 v[112:115], a[40:43], v[184:187], v[112:115]\n"
    "s_waitcnt lgkmcnt(6)\n"
    "ds_read_b128 v[224:227], v254\n"
    "ds_read_b128 v[228:231], v254 offset:1024\n"
    "v_lshlrev_b32_e64 v254, 3, v244\n"
    "v_or_b32_e64 v191, v191, v207\n"
    "v_mfma_f32_16x16x32_bf16 v[116:119], a[44:47], v[184:187], v[116:119]\n"
    "ds_read_u16_d16_hi v203, v203\n"
    "ds_read_b128 v[164:167], v244 offset:10240\n"
    "v_mfma_f32_16x16x32_bf16 v[120:123], a[40:43], v[188:191], v[120:123]\n"
    "ds_read_u16 v197, v197\n"
    "ds_read_u16_d16_hi v205, v205\n"
    "v_lshlrev_b32_sdwa v207, 1, v183 dst_sel:DWORD dst_unused:UNUSED_PAD src0_sel:DWORD src1_sel:WORD_1\n"
    "v_mfma_f32_16x16x32_bf16 v[180:183], a[116:119], a[236:239], 0\n"
    "s_waitcnt lgkmcnt(9)\n"
    "v_or_b32_e32 v193, v193, v201\n"
    "v_or_b32_e32 v194, v194, v202\n"
    "v_mfma_f32_16x16x32_bf16 v[180:183], a[164:167], a[240:243], v[180:183]\n"
    "s_mov_b32 exec_hi, 0\n"
    "buffer_load_dword v233, v254, s[72:75], s53 offen sc1\n"
    "s_mov_b32 exec_hi, -1\n"
    "s_add_u32 s53, s53, 393216\n"
    "v_mfma_f32_16x16x32_bf16 v[180:183], a[252:255], a[244:247], v[180:183]\n"
    "s_waitcnt lgkmcnt(7)\n"
    "ds_read_u16 v198, v198\n"
    "ds_read_u16_d16_hi v206, v206\n"
    "v_smfmac_f32_32x32x32_bf16 v[96:111], a[248:251], v[152:159], v241\n"
    "v_pk_fma_f16 v232, v192, v192, s48\n"
    "v_lshlrev_b32_e64 v254, 3, v244\n"
    "v_pk_mul_f16 v232, v192, v232\n"
    "v_lshlrev_b32_sdwa v200, 1, v169 dst_sel:DWORD dst_unused:UNUSED_PAD src0_sel:DWORD src1_sel:WORD_1\n"
    "v_exp_f16_e32 v234, v232\n"
    "v_mfma_f32_16x16x32_bf16 v[128:131], v[236:239], v[184:187], v[128:131]\n"
    "s_waitcnt lgkmcnt(6)\n"
    "ds_read_u16 v199, v199\n"
    "ds_read_u16_d16_hi v207, v207\n"
    "v_exp_f16_sdwa v234, v232 dst_sel:WORD_1 dst_unused:UNUSED_PRESERVE src0_sel:WORD_1\n"
    "v_lshlrev_b32_sdwa v201, 1, v171 dst_sel:DWORD dst_unused:UNUSED_PAD src0_sel:DWORD src1_sel:WORD_1\n"
    "v_pk_fma_f32 v[64:65], v[64:65], v[224:225], v[228:229]\n"
    "v_pk_fma_f32 v[66:67], v[66:67], v[226:227], v[230:231]\n"
    "v_pk_fma_f32 v[68:69], v[68:69], v[224:225], v[228:229]\n"
    "v_pk_fma_f32 v[70:71], v[70:71], v[226:227], v[230:231]\n"
    "v_pk_fma_f32 v[72:73], v[72:73], v[224:225], v[228:229]\n"
    "v_pk_fma_f32 v[74:75], v[74:75], v[226:227], v[230:231]\n"
    "v_pk_fma_f32 v[76:77], v[76:77], v[224:225], v[228:229]\n"
    "v_pk_fma_f32 v[78:79], v[78:79], v[226:227], v[230:231]\n"
    "v_pk_fma_f32 v[80:81], v[80:81], v[224:225], v[228:229]\n"
    "v_pk_fma_f32 v[82:83], v[82:83], v[226:227], v[230:231]\n"
    "v_pk_fma_f32 v[84:85], v[84:85], v[224:225], v[228:229]\n"
    "v_pk_fma_f32 v[86:87], v[86:87], v[226:227], v[230:231]\n"
    "v_pk_fma_f32 v[88:89], v[88:89], v[224:225], v[228:229]\n"
    "v_pk_fma_f32 v[90:91], v[90:91], v[226:227], v[230:231]\n"
    "v_pk_fma_f32 v[92:93], v[92:93], v[224:225], v[228:229]\n"
    "v_pk_fma_f32 v[94:95], v[94:95], v[226:227], v[230:231]\n"
    "v_pk_fma_f32 v[0:1], v[0:1], v[224:225], v[228:229]\n"
    "v_pk_fma_f32 v[2:3], v[2:3], v[226:227], v[230:231]\n"
    "v_pk_fma_f32 v[4:5], v[4:5], v[224:225], v[228:229]\n"
    "v_pk_fma_f32 v[6:7], v[6:7], v[226:227], v[230:231]\n"
    "v_pk_fma_f32 v[8:9], v[8:9], v[224:225], v[228:229]\n"
    "v_pk_fma_f32 v[10:11], v[10:11], v[226:227], v[230:231]\n"
    "v_pk_fma_f32 v[12:13], v[12:13], v[224:225], v[228:229]\n"
    "v_pk_fma_f32 v[14:15], v[14:15], v[226:227], v[230:231]\n"
    "v_pk_fma_f32 v[16:17], v[16:17], v[224:225], v[228:229]\n"
    "v_pk_fma_f32 v[18:19], v[18:19], v[226:227], v[230:231]\n"
    "v_pk_fma_f32 v[20:21], v[20:21], v[224:225], v[228:229]\n"
    "v_pk_fma_f32 v[22:23], v[22:23], v[226:227], v[230:231]\n"
    "v_pk_fma_f32 v[24:25], v[24:25], v[224:225], v[228:229]\n"
    "v_pk_fma_f32 v[26:27], v[26:27], v[226:227], v[230:231]\n"
    "v_pk_fma_f32 v[28:29], v[28:29], v[224:225], v[228:229]\n"
    "v_pk_fma_f32 v[30:31], v[30:31], v[226:227], v[230:231]\n"
    "v_pk_fma_f32 v[32:33], v[32:33], v[224:225], v[228:229]\n"
    "v_pk_fma_f32 v[34:35], v[34:35], v[226:227], v[230:231]\n"
    "v_pk_fma_f32 v[36:37], v[36:37], v[224:225], v[228:229]\n"
    "v_pk_fma_f32 v[38:39], v[38:39], v[226:227], v[230:231]\n"
    "v_pk_fma_f32 v[40:41], v[40:41], v[224:225], v[228:229]\n"
    "v_pk_fma_f32 v[42:43], v[42:43], v[226:227], v[230:231]\n"
    "v_pk_fma_f32 v[44:45], v[44:45], v[224:225], v[228:229]\n"
    "v_pk_fma_f32 v[46:47], v[46:47], v[226:227], v[230:231]\n"
    "v_pk_fma_f32 v[48:49], v[48:49], v[224:225], v[228:229]\n"
    "v_pk_fma_f32 v[50:51], v[50:51], v[226:227], v[230:231]\n"
    "v_pk_fma_f32 v[52:53], v[52:53], v[224:225], v[228:229]\n"
    "v_pk_fma_f32 v[54:55], v[54:55], v[226:227], v[230:231]\n"
    "v_pk_fma_f32 v[56:57], v[56:57], v[224:225], v[228:229]\n"
    "v_pk_fma_f32 v[58:59], v[58:59], v[226:227], v[230:231]\n"
    "v_pk_fma_f32 v[60:61], v[60:61], v[224:225], v[228:229]\n"
    "v_pk_fma_f32 v[62:63], v[62:63], v[226:227], v[230:231]\n"
    "v_mfma_f32_16x16x32_bf16 v[96:99], a[32:35], v[184:187], v[96:99]\n"
    "s_waitcnt lgkmcnt(1)\n"
    "s_mov_b32 exec_hi, 0\n"
    "buffer_load_dword v233, v254, s[80:83], s85 offen sc1\n"
    "s_mov_b32 exec_hi, -1\n"
    "v_mfma_f32_16x16x32_bf16 v[100:103], a[36:39], v[184:187], v[100:103]\n"
    "s_waitcnt lgkmcnt(0)\n"
    "ds_read_b128 v[152:155], v242 offset:26688\n"
    "ds_read_b128 v[156:159], v242 offset:27712\n"
    "v_mfma_f32_16x16x32_bf16 v[132:135], v[164:167], v[184:187], v[132:135]\n"
    "ds_read_b128 a[252:255], v244 offset:38912\n"
    "ds_read_u16_d16_hi v200, v200\n"
    "v_mfma_f32_16x16x32_bf16 v[104:107], a[32:35], v[188:191], v[104:107]\n"
    "ds_read_u16_d16_hi v201, v201\n"
    "v_cvt_pk_bf16_f32 v208, v64, v68\n"
    "v_mfma_f32_16x16x32_bf16 v[108:111], a[36:39], v[188:191], v[108:111]\n"
    "v_cvt_pk_bf16_f32 v212, v65, v69\n"
    "v_cvt_pk_bf16_f32 v216, v66, v70\n"
    "v_mfma_f32_16x16x32_bf16 v[124:127], a[44:47], v[188:191], v[124:127]\n"
    "v_cvt_pk_bf16_f32 v220, v67, v71\n"
    "v_cvt_pk_bf16_f32 v209, v72, v76\n"
    "v_mfma_f32_16x16x32_bf16 v[136:139], v[236:239], v[188:191], v[136:139]\n"
    "ds_read_b128 v[236:239], v244 offset:12288\n"
    "v_cvt_pk_bf16_f32 v213, v73, v77\n"
    "v_mfma_f32_16x16x32_bf16 v[140:143], v[164:167], v[188:191], v[140:143]\n"
    "v_cvt_pk_bf16_f32 v217, v74, v78\n"
    "v_cvt_pk_bf16_f32 v221, v75, v79\n"
    "v_cvt_pk_bf16_f32 v210, v80, v84\n"
    "v_cvt_pk_bf16_f32 v214, v81, v85\n"
    "v_cvt_pk_bf16_f32 v218, v82, v86\n"
    "v_cvt_pk_bf16_f32 v222, v83, v87\n"
    "v_cvt_pk_bf16_f32 v211, v88, v92\n"
    "ds_write_b128 v253, v[208:211] offset:2048\n"
    "s_waitcnt lgkmcnt(5)\n"
    "v_smfmac_f32_32x32x32_bf16 v[112:127], a[248:251], v[152:159], v241\n"
    "s_nop 0\n"
    "v_cvt_pk_bf16_f32 v215, v89, v93\n"
    "v_cvt_pk_bf16_f32 v219, v90, v94\n"
    "v_cvt_pk_bf16_f32 v223, v91, v95\n"
    "v_cvt_pk_bf16_f32 v80, v0, v4\n"
    "v_cvt_pk_bf16_f32 v84, v1, v5\n"
    "v_cvt_pk_bf16_f32 v88, v2, v6\n"
    "v_cvt_pk_bf16_f32 v92, v3, v7\n"
    "v_cvt_pk_bf16_f32 v81, v8, v12\n"
    "v_cvt_pk_bf16_f32 v85, v9, v13\n"
    "v_or_b32_e64 v195, v195, v203\n"
    "v_cvt_pk_bf16_f32 v89, v10, v14\n"
    "v_cvt_pk_bf16_f32 v93, v11, v15\n"
    "v_cvt_pk_bf16_f32 v82, v16, v20\n"
    "v_cvt_pk_bf16_f32 v86, v17, v21\n"
    "v_cvt_pk_bf16_f32 v90, v18, v22\n"
    "v_or_b32_e64 v197, v197, v205\n"
    "v_cvt_pk_bf16_f32 v94, v19, v23\n"
    "v_cvt_pk_bf16_f32 v83, v24, v28\n"
    "v_pk_fma_f16 v234, v234, s49, s49\n"
    "ds_write_b128 v253, v[212:215] offset:5120\n"
    "v_lshlrev_b32_sdwa v184, 1, v168 dst_sel:DWORD dst_unused:UNUSED_PAD src0_sel:DWORD src1_sel:WORD_1\n"
    "v_cvt_pk_bf16_f32 v87, v25, v29\n"
    "v_lshlrev_b32_sdwa v185, 1, v170 dst_sel:DWORD dst_unused:UNUSED_PAD src0_sel:DWORD src1_sel:WORD_1\n"
    "v_mfma_f32_16x16x32_bf16 v[168:171], a[120:123], a[224:227], 0\n"
    "v_cvt_pk_bf16_f32 v91, v26, v30\n"
    "v_lshlrev_b32_sdwa v186, 1, v172 dst_sel:DWORD dst_unused:UNUSED_PAD src0_sel:DWORD src1_sel:WORD_1\n"
    "v_mfma_f32_16x16x32_bf16 v[168:171], a[168:171], a[228:231], v[168:171]\n"
    "ds_read_u16 v184, v184\n"
    "ds_write_b128 v253, v[80:83] offset:0\n"
    "v_lshlrev_b32_sdwa v202, 1, v173 dst_sel:DWORD dst_unused:UNUSED_PAD src0_sel:DWORD src1_sel:WORD_1\n"
    "v_lshlrev_b32_sdwa v187, 1, v174 dst_sel:DWORD dst_unused:UNUSED_PAD src0_sel:DWORD src1_sel:WORD_1\n"
    "v_lshlrev_b32_sdwa v203, 1, v175 dst_sel:DWORD dst_unused:UNUSED_PAD src0_sel:DWORD src1_sel:WORD_1\n"
    "v_cvt_pk_bf16_f32 v95, v27, v31\n"
    "v_rcp_f16_e64 v232, v234\n"
    "v_mfma_f32_16x16x32_bf16 v[168:171], a[76:79], a[232:235], v[168:171]\n"
    "v_or_b32_e64 v198, v198, v206\n"
    "v_rcp_f16_sdwa v232, v234 dst_sel:WORD_1 dst_unused:UNUSED_PRESERVE src0_sel:WORD_1\n"
    "v_mfma_f32_16x16x32_bf16 v[172:175], a[124:127], a[224:227], 0\n"
    "ds_read_u16 v185, v185\n"
    "ds_write_b128 v253, v[84:87] offset:3072\n"
    "v_mfma_f32_16x16x32_bf16 v[172:175], a[172:175], a[228:231], v[172:175]\n"
    "v_lshlrev_b32_sdwa v188, 1, v176 dst_sel:DWORD dst_unused:UNUSED_PAD src0_sel:DWORD src1_sel:WORD_1\n"
    "v_lshlrev_b32_sdwa v204, 1, v177 dst_sel:DWORD dst_unused:UNUSED_PAD src0_sel:DWORD src1_sel:WORD_1\n"
    "v_lshlrev_b32_sdwa v189, 1, v178 dst_sel:DWORD dst_unused:UNUSED_PAD src0_sel:DWORD src1_sel:WORD_1\n"
    "v_lshlrev_b32_sdwa v205, 1, v179 dst_sel:DWORD dst_unused:UNUSED_PAD src0_sel:DWORD src1_sel:WORD_1\n"
    "v_lshlrev_b32_sdwa v190, 1, v180 dst_sel:DWORD dst_unused:UNUSED_PAD src0_sel:DWORD src1_sel:WORD_1\n"
    "v_lshlrev_b32_sdwa v206, 1, v181 dst_sel:DWORD dst_unused:UNUSED_PAD src0_sel:DWORD src1_sel:WORD_1\n"
    "v_lshlrev_b32_sdwa v191, 1, v182 dst_sel:DWORD dst_unused:UNUSED_PAD src0_sel:DWORD src1_sel:WORD_1\n"
    "v_fma_mix_f32 v234, v192, v232, 0 op_sel_hi:[1,1,0]\n"
    "s_waitcnt lgkmcnt(4)\n"
    "v_mfma_f32_16x16x32_bf16 v[172:175], a[252:255], a[232:235], v[172:175]\n"
    "s_nop 0\n"
    "ds_read_u16 v186, v186\n"
    "ds_read_u16_d16_hi v202, v202\n"
    "v_cvt_pk_bf16_f32 v64, v32, v36\n"
    "v_cvt_pk_bf16_f32 v68, v33, v37\n"
    "v_or_b32_e32 v199, v199, v207\n"
    "v_lshlrev_b32_sdwa v207, 1, v183 dst_sel:DWORD dst_unused:UNUSED_PAD src0_sel:DWORD src1_sel:WORD_1\n"
    "v_cvt_pk_bf16_f32 v72, v34, v38\n"
    "v_fma_mix_f32 v232, v192, v232, 0 op_sel:[1,1,0] op_sel_hi:[1,1,0]\n"
    "v_mfma_f32_16x16x32_bf16 v[176:179], a[120:123], a[236:239], 0\n"
    "s_waitcnt lgkmcnt(4)\n"
    "ds_read_u16 v187, v187\n"
    "ds_read_b128 v[164:167], v244 offset:14336\n"
    "v_cvt_pk_bf16_f32 v76, v35, v39\n"
    "v_cvt_pk_bf16_f32 v65, v40, v44\n"
    "v_cvt_pk_bf16_f32 v192, v234, v232\n"
    "v_mfma_f32_16x16x32_bf16 v[176:179], a[168:171], a[240:243], v[176:179]\n"
    "ds_read_u16_d16_hi v203, v203\n"
    "ds_read_u16 v188, v188\n"
    "v_mfma_f32_16x16x32_bf16 v[176:179], a[76:79], a[244:247], v[176:179]\n"
    "s_nop 0\n"
    "v_cvt_pk_bf16_f32 v69, v41, v45\n"
    "v_cvt_pk_bf16_f32 v73, v42, v46\n"
    "v_pk_fma_f16 v232, v196, v196, s48\n"
    "v_mfma_f32_16x16x32_bf16 v[180:183], a[124:127], a[236:239], 0\n"
    "s_waitcnt lgkmcnt(6)\n"
    "ds_read_u16_d16_hi v204, v204\n"
    "v_cvt_pk_bf16_f32 v77, v43, v47\n"
    "v_cvt_pk_bf16_f32 v66, v48, v52\n"
    "v_cvt_pk_bf16_f32 v70, v49, v53\n"
    "v_pk_mul_f16 v232, v196, v232\n"
    "v_mfma_f32_16x16x32_bf16 v[180:183], a[172:175], a[240:243], v[180:183]\n"
    "ds_read_b128 v[152:155], v242 offset:28736\n"
    "ds_read_b128 v[156:159], v242 offset:29760\n"
    "v_cvt_pk_bf16_f32 v74, v50, v54\n"
    "v_cvt_pk_bf16_f32 v78, v51, v55\n"
    "v_exp_f16_e32 v234, v232\n"
    "v_mfma_f32_16x16x32_bf16 v[180:183], a[252:255], a[244:247], v[180:183]\n"
    "ds_read_u16 v189, v189\n"
    "v_cvt_pk_bf16_f32 v67, v56, v60\n"
    "v_cvt_pk_bf16_f32 v71, v57, v61\n"
    "v_exp_f16_sdwa v234, v232 dst_sel:WORD_1 dst_unused:UNUSED_PRESERVE src0_sel:WORD_1\n"
    "v_mfma_f32_16x16x32_bf16 v[96:99], a[176:179], v[192:195], v[96:99]\n"
    "s_waitcnt lgkmcnt(8)\n"
    "v_cvt_pk_bf16_f32 v75, v58, v62\n"
    "v_cvt_pk_bf16_f32 v79, v59, v63\n"
    "v_pk_fma_f16 v234, v234, s49, s49\n"
    "v_mfma_f32_16x16x32_bf16 v[100:103], a[180:183], v[192:195], v[100:103]\n"
    "ds_write_b128 v253, v[64:67] offset:1024\n"
    "v_rcp_f16_e32 v232, v234\n"
    "v_mfma_f32_16x16x32_bf16 v[112:115], a[184:187], v[192:195], v[112:115]\n"
    "v_rcp_f16_sdwa v232, v234 dst_sel:WORD_1 dst_unused:UNUSED_PRESERVE src0_sel:WORD_1\n"
    "v_mfma_f32_16x16x32_bf16 v[116:119], a[188:191], v[192:195], v[116:119]\n"
    "s_waitcnt lgkmcnt(6)\n"
    "ds_read_u16_d16_hi v205, v205\n"
    "ds_write_b128 v253, v[68:71] offset:4096\n"
    "v_mfma_f32_16x16x32_bf16 v[128:131], v[236:239], v[192:195], v[128:131]\n"
    "v_fma_mix_f32 v234, v196, v232, 0 op_sel_hi:[1,1,0]\n"
    "v_fma_mix_f32 v232, v196, v232, 0 op_sel:[1,1,0] op_sel_hi:[1,1,0]\n"
    "v_mfma_f32_16x16x32_bf16 v[132:135], v[164:167], v[192:195], v[132:135]\n"
    "v_cvt_pk_bf16_f32 v196, v234, v232\n"
    "v_or_b32_e64 v184, v184, v200\n"
    "v_or_b32_e32 v185, v185, v201\n"
    "v_mfma_f32_16x16x32_bf16 v[104:107], a[176:179], v[196:199], v[104:107]\n"
    "s_waitcnt lgkmcnt(4)\n"
    "ds_read_u16 v190, v190\n"
    "ds_read_u16_d16_hi v206, v206\n"
    "v_mfma_f32_16x16x32_bf16 v[108:111], a[180:183], v[196:199], v[108:111]\n"
    "ds_read_u16 v191, v191\n"
    "v_cvt_pk_f16_f32 v192, v168, v169\n"
    "v_mfma_f32_16x16x32_bf16 v[120:123], a[184:187], v[196:199], v[120:123]\n"
    "ds_read_u16_d16_hi v207, v207\n"
    "v_lshlrev_b32_sdwa v193, 1, v170 dst_sel:DWORD dst_unused:UNUSED_PAD src0_sel:DWORD src1_sel:WORD_1\n"
    "v_mfma_f32_16x16x32_bf16 v[124:127], a[188:191], v[196:199], v[124:127]\n"
    "v_lshlrev_b32_sdwa v201, 1, v171 dst_sel:DWORD dst_unused:UNUSED_PAD src0_sel:DWORD src1_sel:WORD_1\n"
    "v_lshlrev_b32_sdwa v194, 1, v172 dst_sel:DWORD dst_unused:UNUSED_PAD src0_sel:DWORD src1_sel:WORD_1\n"
    "v_mfma_f32_16x16x32_bf16 v[136:139], v[236:239], v[196:199], v[136:139]\n"
    "ds_read_b128 a[252:255], v244 offset:32768\n"
    "ds_read_u16 v193, v193\n"
    "v_mfma_f32_16x16x32_bf16 v[140:143], v[164:167], v[196:199], v[140:143]\n"
    "v_or_b32_e64 v186, v186, v202\n"
    "v_lshlrev_b32_sdwa v202, 1, v173 dst_sel:DWORD dst_unused:UNUSED_PAD src0_sel:DWORD src1_sel:WORD_1\n"
    "v_lshlrev_b32_sdwa v195, 1, v174 dst_sel:DWORD dst_unused:UNUSED_PAD src0_sel:DWORD src1_sel:WORD_1\n"
    "v_cvt_pk_f16_f32 v196, v176, v177\n"
    "v_lshlrev_b32_sdwa v197, 1, v178 dst_sel:DWORD dst_unused:UNUSED_PAD src0_sel:DWORD src1_sel:WORD_1\n"
    "v_lshlrev_b32_sdwa v198, 1, v180 dst_sel:DWORD dst_unused:UNUSED_PAD src0_sel:DWORD src1_sel:WORD_1\n"
    "v_smfmac_f32_32x32x32_bf16 v[128:143], a[248:251], v[152:159], v241\n"
    "s_waitcnt lgkmcnt(6)\n"
    "s_barrier\n"
    "v_lshlrev_b32_sdwa v199, 1, v182 dst_sel:DWORD dst_unused:UNUSED_PAD src0_sel:DWORD src1_sel:WORD_1\n"
    "v_or_b32_e64 v187, v187, v203\n"
    "ds_read_b128 a[224:227], v242 offset:0\n"
    "ds_read_b128 a[228:231], v242 offset:1024\n"
    "v_lshlrev_b32_sdwa v203, 1, v175 dst_sel:DWORD dst_unused:UNUSED_PAD src0_sel:DWORD src1_sel:WORD_1\n"
    "v_or_b32_e64 v188, v188, v204\n"
    "ds_read_b128 a[232:235], v242 offset:2048\n"
    "ds_read_b128 a[236:239], v242 offset:3072\n"
    "ds_read_b128 a[240:243], v242 offset:4096\n"
    "v_or_b32_e64 v189, v189, v205\n"
    "ds_read_b128 a[244:247], v242 offset:5120\n"
    "ds_read_u16_d16_hi v201, v201\n"
    "v_lshlrev_b32_sdwa v205, 1, v179 dst_sel:DWORD dst_unused:UNUSED_PAD src0_sel:DWORD src1_sel:WORD_1\n"
    "s_waitcnt lgkmcnt(10)\n"
    "v_or_b32_e64 v190, v190, v206\n"
    "v_mfma_f32_16x16x32_bf16 v[96:99], a[192:195], v[184:187], v[96:99]\n"
    "s_waitcnt lgkmcnt(8)\n"
    "ds_read_u16 v194, v194\n"
    "ds_read_u16_d16_hi v202, v202\n"
    "v_mfma_f32_16x16x32_bf16 v[100:103], a[196:199], v[184:187], v[100:103]\n"
    "v_lshlrev_b32_sdwa v206, 1, v181 dst_sel:DWORD dst_unused:UNUSED_PAD src0_sel:DWORD src1_sel:WORD_1\n"
    "v_or_b32_e64 v191, v191, v207\n"
    "v_mfma_f32_16x16x32_bf16 v[112:115], a[200:203], v[184:187], v[112:115]\n"
    "ds_read_b128 v[236:239], v244 offset:16384\n"
    "v_lshlrev_b32_sdwa v207, 1, v183 dst_sel:DWORD dst_unused:UNUSED_PAD src0_sel:DWORD src1_sel:WORD_1\n"
    "v_mfma_f32_16x16x32_bf16 v[116:119], a[204:207], v[184:187], v[116:119]\n"
    "s_waitcnt lgkmcnt(9)\n"
    "ds_read_u16 v195, v195\n"
    "v_pk_fma_f16 v232, v192, v192, s48\n"
    "v_mfma_f32_16x16x32_bf16 v[168:171], a[80:83], a[224:227], 0\n"
    "s_waitcnt lgkmcnt(9)\n"
    "v_pk_mul_f16 v232, v192, v232\n"
    "v_exp_f16_e64 v234, v232\n"
    "v_mfma_f32_16x16x32_bf16 v[168:171], a[128:131], a[228:231], v[168:171]\n"
    "s_waitcnt lgkmcnt(8)\n"
    "ds_read_u16_d16_hi v203, v203\n"
    "v_exp_f16_sdwa v234, v232 dst_sel:WORD_1 dst_unused:UNUSED_PRESERVE src0_sel:WORD_1\n"
    "v_mfma_f32_16x16x32_bf16 v[168:171], a[48:51], a[232:235], v[168:171]\n"
    "s_waitcnt lgkmcnt(8)\n"
    "ds_read_b128 v[164:167], v244 offset:18432\n"
    "v_pk_fma_f16 v234, v234, s49, s49\n"
    "v_mfma_f32_16x16x32_bf16 v[172:175], a[84:87], a[224:227], 0\n"
    "ds_read_u16 v197, v197\n"
    "v_rcp_f16_e32 v232, v234\n"
    "v_mfma_f32_16x16x32_bf16 v[172:175], a[132:135], a[228:231], v[172:175]\n"
    "s_waitcnt lgkmcnt(8)\n"
    "s_barrier\n"
    "v_mfma_f32_16x16x32_bf16 v[172:175], a[252:255], a[232:235], v[172:175]\n"
    "s_waitcnt lgkmcnt(6)\n"
    "ds_read_u16_d16_hi v205, v205\n"
    "ds_write_b128 v253, v[72:75] offset:25664\n"
    "v_mfma_f32_16x16x32_bf16 v[176:179], a[80:83], a[236:239], 0\n"
    "s_waitcnt lgkmcnt(6)\n"
    "v_or_b32_e32 v193, v193, v201\n"
    "v_or_b32_e64 v194, v194, v202\n"
    "v_mfma_f32_16x16x32_bf16 v[176:179], a[128:131], a[240:243], v[176:179]\n"
    "v_rcp_f16_sdwa v232, v234 dst_sel:WORD_1 dst_unused:UNUSED_PRESERVE src0_sel:WORD_1\n"
    "v_mfma_f32_16x16x32_bf16 v[176:179], a[48:51], a[244:247], v[176:179]\n"
    "ds_read_u16 v198, v198\n"
    "ds_write_b128 v253, v[76:79] offset:28736\n"
    "v_mfma_f32_16x16x32_bf16 v[180:183], a[84:87], a[236:239], 0\n"
    "s_waitcnt lgkmcnt(5)\n"
    "v_or_b32_e32 v195, v195, v203\n"
    "v_fma_mix_f32 v234, v192, v232, 0 op_sel_hi:[1,1,0]\n"
    "v_mfma_f32_16x16x32_bf16 v[180:183], a[132:135], a[240:243], v[180:183]\n"
    "v_fma_mix_f32 v232, v192, v232, 0 op_sel:[1,1,0] op_sel_hi:[1,1,0]\n"
    "v_cvt_pk_bf16_f32 v192, v234, v232\n"
    "v_mfma_f32_16x16x32_bf16 v[180:183], a[252:255], a[244:247], v[180:183]\n"
    "ds_read_u16_d16_hi v206, v206\n"
    "ds_write_b128 v253, v[88:91] offset:24640\n"
    "v_mfma_f32_16x16x32_bf16 v[128:131], v[236:239], v[184:187], v[128:131]\n"
    "v_pk_fma_f16 v232, v196, v196, s48\n"
    "v_pk_mul_f16 v232, v196, v232\n"
    "v_mfma_f32_16x16x32_bf16 v[132:135], v[164:167], v[184:187], v[132:135]\n"
    "s_waitcnt lgkmcnt(4)\n"
    "v_or_b32_e32 v197, v197, v205\n"
    "v_exp_f16_e64 v234, v232\n"
    "v_mfma_f32_16x16x32_bf16 v[104:107], a[192:195], v[188:191], v[104:107]\n"
    "ds_read_u16 v199, v199\n"
    "ds_write_b128 v253, v[92:95] offset:27712\n"
    "v_mfma_f32_16x16x32_bf16 v[108:111], a[196:199], v[188:191], v[108:111]\n"
    "v_exp_f16_sdwa v234, v232 dst_sel:WORD_1 dst_unused:UNUSED_PRESERVE src0_sel:WORD_1\n"
    "v_mfma_f32_16x16x32_bf16 v[120:123], a[200:203], v[188:191], v[120:123]\n"
    "v_mfma_f32_16x16x32_bf16 v[124:127], a[204:207], v[188:191], v[124:127]\n"
    "ds_read_u16_d16_hi v207, v207\n"
    "ds_write_b128 v253, v[216:219] offset:26688\n"
    "v_mfma_f32_16x16x32_bf16 v[136:139], v[236:239], v[188:191], v[136:139]\n"
    "s_waitcnt lgkmcnt(4)\n"
    "v_or_b32_e32 v198, v198, v206\n"
    "ds_write_b128 v253, v[220:223] offset:29760\n"
    "s_waitcnt lgkmcnt(2)\n"
    "v_or_b32_e32 v199, v199, v207\n"
    "s_mov_b32 s59, s58\n"
    "s_add_u32 s57, s57, 1572864\n"
    "s_add_u32 s58, s58, 1572864\n"
    "s_mov_b32 s70, s46\n"
    "s_add_u32 s88, s57, 98304\n"
    "s_add_u32 s89, s58, 98304\n"
    "s_add_u32 s39, s59, 98304\n"
    "s_mov_b32 s82, 0\n"
    "s_add_u32 s64, s64, 1\n"
    "s_cmp_lt_u32 s64, 2\n"
    "s_cselect_b64 s[72:73], s[72:73], s[76:77]\n"
    "s_cselect_b32 s74, s74, s78\n"
    "s_cselect_b32 s53, s53, s79\n"
    "s_cmp_lg_u32 s64, 3\n"
    "s_cbranch_scc1 1b\n"
    ".Lreplk_last:\n"
    "v_lshlrev_b32_sdwa v184, 1, v168 dst_sel:DWORD dst_unused:UNUSED_PAD src0_sel:DWORD src1_sel:WORD_1\n"
    "v_lshlrev_b32_sdwa v200, 1, v169 dst_sel:DWORD dst_unused:UNUSED_PAD src0_sel:DWORD src1_sel:WORD_1\n"
    "v_lshlrev_b32_sdwa v185, 1, v170 dst_sel:DWORD dst_unused:UNUSED_PAD src0_sel:DWORD src1_sel:WORD_1\n"
    "v_lshlrev_b32_sdwa v201, 1, v171 dst_sel:DWORD dst_unused:UNUSED_PAD src0_sel:DWORD src1_sel:WORD_1\n"
    "v_mfma_f32_16x16x32_bf16 v[140:143], v[164:167], v[188:191], v[140:143]\n"
    "s_waitcnt vmcnt(2)\n"
    "ds_read_b128 v[56:59], v244 offset:36864\n"
    "ds_read_u16 v184, v184\n"
    "v_mfma_f32_16x16x32_bf16 v[168:171], a[88:91], a[224:227], 0\n"
    "s_nop 0\n"
    "ds_read_u16_d16_hi v200, v200\n"
    "ds_read_u16 v185, v185\n"
    "v_mfma_f32_16x16x32_bf16 v[168:171], a[136:139], a[228:231], v[168:171]\n"
    "v_lshlrev_b32_sdwa v186, 1, v172 dst_sel:DWORD dst_unused:UNUSED_PAD src0_sel:DWORD src1_sel:WORD_1\n"
    "v_lshlrev_b32_sdwa v202, 1, v173 dst_sel:DWORD dst_unused:UNUSED_PAD src0_sel:DWORD src1_sel:WORD_1\n"
    "v_mfma_f32_16x16x32_bf16 v[168:171], a[52:55], a[232:235], v[168:171]\n"
    "ds_read_u16_d16_hi v201, v201\n"
    "v_lshlrev_b32_sdwa v187, 1, v174 dst_sel:DWORD dst_unused:UNUSED_PAD src0_sel:DWORD src1_sel:WORD_1\n"
    "v_smfmac_f32_32x32x32_bf16 v[128:143], a[248:251], v[144:151], v241\n"
    "ds_read_u16 v186, v186\n"
    "v_lshlrev_b32_sdwa v203, 1, v175 dst_sel:DWORD dst_unused:UNUSED_PAD src0_sel:DWORD src1_sel:WORD_1\n"
    "v_lshlrev_b32_sdwa v188, 1, v176 dst_sel:DWORD dst_unused:UNUSED_PAD src0_sel:DWORD src1_sel:WORD_1\n"
    "v_lshlrev_b32_sdwa v204, 1, v177 dst_sel:DWORD dst_unused:UNUSED_PAD src0_sel:DWORD src1_sel:WORD_1\n"
    "v_lshlrev_b32_sdwa v189, 1, v178 dst_sel:DWORD dst_unused:UNUSED_PAD src0_sel:DWORD src1_sel:WORD_1\n"
    "v_mfma_f32_16x16x32_bf16 v[172:175], a[92:95], a[224:227], 0\n"
    "ds_read_b128 v[236:239], v244 offset:20480\n"
    "buffer_load_dwordx4 v[152:155], v245, s[40:43], s57 offen offset:64\n"
    "buffer_load_dwordx4 v[144:147], v245, s[40:43], s57 offen offset:0\n"
    "buffer_load_dwordx4 v[208:211], v245, s[40:43], s57 offen offset:128\n"
    "v_mfma_f32_16x16x32_bf16 v[172:175], a[140:143], a[228:231], v[172:175]\n"
    "s_nop 0\n"
    "ds_read_u16_d16_hi v202, v202\n"
    "v_lshlrev_b32_sdwa v205, 1, v179 dst_sel:DWORD dst_unused:UNUSED_PAD src0_sel:DWORD src1_sel:WORD_1\n"
    "v_mfma_f32_16x16x32_bf16 v[176:179], a[88:91], a[236:239], 0\n"
    "s_waitcnt lgkmcnt(7)\n"
    "ds_read_u16 v187, v187\n"
    "v_lshlrev_b32_sdwa v190, 1, v180 dst_sel:DWORD dst_unused:UNUSED_PAD src0_sel:DWORD src1_sel:WORD_1\n"
    "v_mfma_f32_16x16x32_bf16 v[172:175], v[56:59], a[232:235], v[172:175]\n"
    "v_lshlrev_b32_sdwa v206, 1, v181 dst_sel:DWORD dst_unused:UNUSED_PAD src0_sel:DWORD src1_sel:WORD_1\n"
    "v_lshlrev_b32_sdwa v191, 1, v182 dst_sel:DWORD dst_unused:UNUSED_PAD src0_sel:DWORD src1_sel:WORD_1\n"
    "v_mfma_f32_16x16x32_bf16 v[176:179], a[136:139], a[240:243], v[176:179]\n"
    "ds_read_b128 v[164:167], v244 offset:22528\n"
    "v_lshlrev_b32_sdwa v207, 1, v183 dst_sel:DWORD dst_unused:UNUSED_PAD src0_sel:DWORD src1_sel:WORD_1\n"
    "v_mfma_f32_16x16x32_bf16 v[176:179], a[52:55], a[244:247], v[176:179]\n"
    "ds_read_u16_d16_hi v203, v203\n"
    "v_pk_fma_f16 v234, v234, s49, s49\n"
    "v_mfma_f32_16x16x32_bf16 v[180:183], a[92:95], a[236:239], 0\n"
    "v_rcp_f16_e32 v232, v234\n"
    "v_mfma_f32_16x16x32_bf16 v[180:183], a[140:143], a[240:243], v[180:183]\n"
    "s_waitcnt lgkmcnt(8)\n"
    "ds_read_u16 v188, v188\n"
    "v_rcp_f16_sdwa v232, v234 dst_sel:WORD_1 dst_unused:UNUSED_PRESERVE src0_sel:WORD_1\n"
    "v_mfma_f32_16x16x32_bf16 v[180:183], v[56:59], a[244:247], v[180:183]\n"
    "ds_read_u16_d16_hi v204, v204\n"
    "v_fma_mix_f32 v234, v196, v232, 0 op_sel_hi:[1,1,0]\n"
    "v_mfma_f32_16x16x32_bf16 v[96:99], a[208:211], v[192:195], v[96:99]\n"
    "v_fma_mix_f32 v232, v196, v232, 0 op_sel:[1,1,0] op_sel_hi:[1,1,0]\n"
    "v_or_b32_e32 v184, v184, v200\n"
    "v_mfma_f32_16x16x32_bf16 v[100:103], a[212:215], v[192:195], v[100:103]\n"
    "s_waitcnt lgkmcnt(8)\n"
    "ds_read_u16 v189, v189\n"
    "buffer_load_dwordx4 v[156:159], v245, s[40:43], s88 offen offset:64\n"
    "buffer_load_dwordx4 v[148:151], v245, s[40:43], s88 offen offset:0\n"
    "buffer_load_dwordx4 v[212:215], v245, s[40:43], s88 offen offset:128\n"
    "v_mfma_f32_16x16x32_bf16 v[112:115], a[216:219], v[192:195], v[112:115]\n"
    "s_waitcnt lgkmcnt(7)\n"
    "ds_read_u16_d16_hi v205, v205\n"
    "v_cvt_pk_bf16_f32 v196, v234, v232\n"
    "v_mfma_f32_16x16x32_bf16 v[116:119], a[220:223], v[192:195], v[116:119]\n"
    "v_or_b32_e32 v185, v185, v201\n"
    "v_lshlrev_b32_sdwa v201, 1, v171 dst_sel:DWORD dst_unused:UNUSED_PAD src0_sel:DWORD src1_sel:WORD_1\n"
    "v_mfma_f32_16x16x32_bf16 v[128:131], v[236:239], v[192:195], v[128:131]\n"
    "ds_read_u16 v190, v190\n"
    "v_cvt_pk_bf16_f32 v96, v96, v97\n"
    "v_mfma_f32_16x16x32_bf16 v[104:107], a[208:211], v[196:199], v[104:107]\n"
    "s_waitcnt lgkmcnt(8)\n"
    "v_or_b32_e64 v186, v186, v202\n"
    "v_lshlrev_b32_sdwa v202, 1, v173 dst_sel:DWORD dst_unused:UNUSED_PAD src0_sel:DWORD src1_sel:WORD_1\n"
    "v_mfma_f32_16x16x32_bf16 v[108:111], a[212:215], v[196:199], v[108:111]\n"
    "s_waitcnt lgkmcnt(6)\n"
    "ds_read_u16_d16_hi v206, v206\n"
    "v_cvt_pk_bf16_f32 v97, v98, v99\n"
    "v_mfma_f32_16x16x32_bf16 v[132:135], v[164:167], v[192:195], v[132:135]\n"
    "v_cvt_pk_f16_f32 v192, v168, v169\n"
    "v_lshlrev_b32_sdwa v193, 1, v170 dst_sel:DWORD dst_unused:UNUSED_PAD src0_sel:DWORD src1_sel:WORD_1\n"
    "v_mfma_f32_16x16x32_bf16 v[120:123], a[216:219], v[196:199], v[120:123]\n"
    "ds_read_u16 v191, v191\n"
    "v_lshlrev_b32_sdwa v194, 1, v172 dst_sel:DWORD dst_unused:UNUSED_PAD src0_sel:DWORD src1_sel:WORD_1\n"
    "v_mfma_f32_16x16x32_bf16 v[124:127], a[220:223], v[196:199], v[124:127]\n"
    "s_waitcnt lgkmcnt(7)\n"
    "ds_read_u16_d16_hi v207, v207\n"
    "v_lshlrev_b32_sdwa v195, 1, v174 dst_sel:DWORD dst_unused:UNUSED_PAD src0_sel:DWORD src1_sel:WORD_1\n"
    "v_mfma_f32_16x16x32_bf16 v[136:139], v[236:239], v[196:199], v[136:139]\n"
    "v_or_b32_e32 v187, v187, v203\n"
    "v_lshlrev_b32_sdwa v203, 1, v175 dst_sel:DWORD dst_unused:UNUSED_PAD src0_sel:DWORD src1_sel:WORD_1\n"
    "v_mfma_f32_16x16x32_bf16 v[140:143], v[164:167], v[196:199], v[140:143]\n"
    "ds_read_u16 v193, v193\n"
    "v_cvt_pk_f16_f32 v196, v176, v177\n"
    "v_mfma_f32_16x16x32_bf16 v[168:171], a[96:99], a[224:227], 0\n"
    "v_lshlrev_b32_sdwa v197, 1, v178 dst_sel:DWORD dst_unused:UNUSED_PAD src0_sel:DWORD src1_sel:WORD_1\n"
    "v_lshlrev_b32_sdwa v198, 1, v180 dst_sel:DWORD dst_unused:UNUSED_PAD src0_sel:DWORD src1_sel:WORD_1\n"
    "v_mfma_f32_16x16x32_bf16 v[168:171], a[144:147], a[228:231], v[168:171]\n"
    "s_waitcnt lgkmcnt(7)\n"
    "ds_read_u16_d16_hi v201, v201\n"
    "v_lshlrev_b32_sdwa v199, 1, v182 dst_sel:DWORD dst_unused:UNUSED_PAD src0_sel:DWORD src1_sel:WORD_1\n"
    "v_mfma_f32_16x16x32_bf16 v[168:171], a[56:59], a[232:235], v[168:171]\n"
    "v_or_b32_e32 v188, v188, v204\n"
    "v_cvt_pk_bf16_f32 v98, v100, v101\n"
    "v_mfma_f32_16x16x32_bf16 v[172:175], a[100:103], a[224:227], 0\n"
    "ds_read_u16 v194, v194\n"
    "v_cvt_pk_bf16_f32 v99, v102, v103\n"
    "v_mfma_f32_16x16x32_bf16 v[172:175], a[148:151], a[228:231], v[172:175]\n"
    "s_waitcnt lgkmcnt(7)\n"
    "ds_read_u16_d16_hi v202, v202\n"
    "buffer_store_dwordx4 v[96:99], v245, s[68:71], s59 offen offset:0 sc1\n"
    "v_mfma_f32_16x16x32_bf16 v[172:175], a[60:63], a[232:235], v[172:175]\n"
    "s_nop 0\n"
    "ds_read_b128 v[0:3], v244 offset:0\n"
    "v_or_b32_e64 v189, v189, v205\n"
    "v_lshlrev_b32_sdwa v205, 1, v179 dst_sel:DWORD dst_unused:UNUSED_PAD src0_sel:DWORD src1_sel:WORD_1\n"
    "v_mfma_f32_16x16x32_bf16 v[176:179], a[96:99], a[236:239], 0\n"
    "ds_read_u16 v195, v195\n"
    "v_cvt_pk_bf16_f32 v224, v112, v113\n"
    "v_mfma_f32_16x16x32_bf16 v[176:179], a[144:147], a[240:243], v[176:179]\n"
    "s_waitcnt lgkmcnt(8)\n"
    "v_or_b32_e32 v190, v190, v206\n"
    "v_lshlrev_b32_sdwa v206, 1, v181 dst_sel:DWORD dst_unused:UNUSED_PAD src0_sel:DWORD src1_sel:WORD_1\n"
    "v_mfma_f32_16x16x32_bf16 v[176:179], a[56:59], a[244:247], v[176:179]\n"
    "ds_read_u16_d16_hi v203, v203\n"
    "v_cvt_pk_bf16_f32 v225, v114, v115\n"
    "v_mfma_f32_16x16x32_bf16 v[96:99], a[0:3], v[184:187], 0\n"
    "s_nop 0\n"
    "ds_read_b128 v[4:7], v244 offset:2048\n"
    "v_cvt_pk_bf16_f32 v226, v116, v117\n"
    "v_mfma_f32_16x16x32_bf16 v[100:103], a[4:7], v[184:187], 0\n"
    "s_waitcnt lgkmcnt(8)\n"
    "ds_read_u16 v197, v197\n"
    "v_cvt_pk_bf16_f32 v227, v118, v119\n"
    "v_mfma_f32_16x16x32_bf16 v[112:115], a[8:11], v[184:187], 0\n"
    "v_cvt_pk_bf16_f32 v228, v128, v129\n"
    "v_cvt_pk_bf16_f32 v229, v130, v131\n"
    "v_or_b32_e64 v191, v191, v207\n"
    "v_mfma_f32_16x16x32_bf16 v[116:119], a[12:15], v[184:187], 0\n"
    "ds_read_u16_d16_hi v205, v205\n"
    "v_lshlrev_b32_sdwa v207, 1, v183 dst_sel:DWORD dst_unused:UNUSED_PAD src0_sel:DWORD src1_sel:WORD_1\n"
    "v_mfma_f32_16x16x32_bf16 v[180:183], a[100:103], a[236:239], 0\n"
    "s_waitcnt lgkmcnt(8)\n"
    "v_cvt_pk_bf16_f32 v230, v132, v133\n"
    "v_or_b32_e32 v193, v193, v201\n"
    "v_mfma_f32_16x16x32_bf16 v[180:183], a[148:151], a[240:243], v[180:183]\n"
    "ds_read_u16 v198, v198\n"
    "v_cvt_pk_bf16_f32 v231, v134, v135\n"
    "v_mfma_f32_16x16x32_bf16 v[180:183], a[60:63], a[244:247], v[180:183]\n"
    "ds_read_u16_d16_hi v206, v206\n"
    "v_cvt_pk_bf16_f32 v216, v104, v105\n"
    "v_cvt_pk_bf16_f32 v217, v106, v107\n"
    "v_mfma_f32_16x16x32_bf16 v[104:107], a[0:3], v[188:191], 0\n"
    "s_waitcnt lgkmcnt(7)\n"
    "v_cvt_pk_bf16_f32 v218, v108, v109\n"
    "v_cvt_pk_bf16_f32 v219, v110, v111\n"
    "v_mfma_f32_16x16x32_bf16 v[128:131], v[0:3], v[184:187], 0\n"
    "s_nop 0\n"
    "ds_read_u16 v199, v199\n"
    "v_or_b32_e64 v194, v194, v202\n"
    "v_mfma_f32_16x16x32_bf16 v[108:111], a[4:7], v[188:191], 0\n"
    "v_cvt_pk_bf16_f32 v220, v120, v121\n"
    "v_cvt_pk_bf16_f32 v221, v122, v123\n"
    "v_cvt_pk_bf16_f32 v222, v124, v125\n"
    "v_cvt_pk_bf16_f32 v223, v126, v127\n"
    "v_pk_fma_f16 v232, v192, v192, s48\n"
    "v_mfma_f32_16x16x32_bf16 v[120:123], a[8:11], v[188:191], 0\n"
    "ds_read_u16_d16_hi v207, v207\n"
    "v_cvt_pk_bf16_f32 v160, v136, v137\n"
    "v_mfma_f32_16x16x32_bf16 v[124:127], a[12:15], v[188:191], 0\n"
    "s_waitcnt lgkmcnt(6)\n"
    "v_cvt_pk_bf16_f32 v161, v138, v139\n"
    "v_cvt_pk_bf16_f32 v162, v140, v141\n"
    "v_mfma_f32_16x16x32_bf16 v[132:135], v[4:7], v[184:187], 0\n"
    "v_or_b32_e32 v195, v195, v203\n"
    "v_cvt_pk_bf16_f32 v163, v142, v143\n"
    "v_pk_mul_f16 v232, v192, v232\n"
    "v_mfma_f32_16x16x32_bf16 v[136:139], v[0:3], v[188:191], 0\n"
    "v_exp_f16_e64 v234, v232\n"
    "v_mfma_f32_16x16x32_bf16 v[140:143], v[4:7], v[188:191], 0\n"
    "v_exp_f16_sdwa v234, v232 dst_sel:WORD_1 dst_unused:UNUSED_PRESERVE src0_sel:WORD_1\n"
    "v_lshlrev_b32_sdwa v184, 1, v168 dst_sel:DWORD dst_unused:UNUSED_PAD src0_sel:DWORD src1_sel:WORD_1\n"
    "v_pk_fma_f16 v234, v234, s49, s49\n"
    "v_lshlrev_b32_sdwa v200, 1, v169 dst_sel:DWORD dst_unused:UNUSED_PAD src0_sel:DWORD src1_sel:WORD_1\n"
    "s_waitcnt lgkmcnt(4)\n"
    "v_or_b32_e32 v197, v197, v205\n"
    "v_lshlrev_b32_sdwa v185, 1, v170 dst_sel:DWORD dst_unused:UNUSED_PAD src0_sel:DWORD src1_sel:WORD_1\n"
    "v_lshlrev_b32_sdwa v201, 1, v171 dst_sel:DWORD dst_unused:UNUSED_PAD src0_sel:DWORD src1_sel:WORD_1\n"
    "v_mfma_f32_16x16x32_bf16 v[168:171], a[104:107], a[224:227], 0\n"
    "ds_read_u16 v184, v184\n"
    "ds_read_u16_d16_hi v200, v200\n"
    "v_mfma_f32_16x16x32_bf16 v[168:171], a[152:155], a[228:231], v[168:171]\n"
    "ds_read_u16 v185, v185\n"
    "ds_read_u16_d16_hi v201, v201\n"
    "v_lshlrev_b32_sdwa v186, 1, v172 dst_sel:DWORD dst_unused:UNUSED_PAD src0_sel:DWORD src1_sel:WORD_1\n"
    "v_lshlrev_b32_sdwa v202, 1, v173 dst_sel:DWORD dst_unused:UNUSED_PAD src0_sel:DWORD src1_sel:WORD_1\n"
    "v_lshlrev_b32_sdwa v187, 1, v174 dst_sel:DWORD dst_unused:UNUSED_PAD src0_sel:DWORD src1_sel:WORD_1\n"
    "v_lshlrev_b32_sdwa v203, 1, v175 dst_sel:DWORD dst_unused:UNUSED_PAD src0_sel:DWORD src1_sel:WORD_1\n"
    "v_rcp_f16_e32 v232, v234\n"
    "v_mfma_f32_16x16x32_bf16 v[168:171], a[64:67], a[232:235], v[168:171]\n"
    "s_waitcnt lgkmcnt(6)\n"
    "ds_read_u16 v186, v186\n"
    "buffer_store_dwordx4 v[224:227], v245, s[68:71], s59 offen offset:64 sc1\n"
    "v_or_b32_e32 v198, v198, v206\n"
    "v_rcp_f16_sdwa v232, v234 dst_sel:WORD_1 dst_unused:UNUSED_PRESERVE src0_sel:WORD_1\n"
    "v_mfma_f32_16x16x32_bf16 v[172:175], a[108:111], a[224:227], 0\n"
    "s_waitcnt lgkmcnt(5)\n"
    "ds_read_b128 v[8:11], v244 offset:4096\n"
    "ds_read_u16_d16_hi v202, v202\n"
    "v_mfma_f32_16x16x32_bf16 v[172:175], a[156:159], a[228:231], v[172:175]\n"
    "v_lshlrev_b32_sdwa v188, 1, v176 dst_sel:DWORD dst_unused:UNUSED_PAD src0_sel:DWORD src1_sel:WORD_1\n"
    "v_lshlrev_b32_sdwa v204, 1, v177 dst_sel:DWORD dst_unused:UNUSED_PAD src0_sel:DWORD src1_sel:WORD_1\n"
    "v_lshlrev_b32_sdwa v189, 1, v178 dst_sel:DWORD dst_unused:UNUSED_PAD src0_sel:DWORD src1_sel:WORD_1\n"
    "v_lshlrev_b32_sdwa v205, 1, v179 dst_sel:DWORD dst_unused:UNUSED_PAD src0_sel:DWORD src1_sel:WORD_1\n"
    "v_lshlrev_b32_sdwa v190, 1, v180 dst_sel:DWORD dst_unused:UNUSED_PAD src0_sel:DWORD src1_sel:WORD_1\n"
    "v_lshlrev_b32_sdwa v206, 1, v181 dst_sel:DWORD dst_unused:UNUSED_PAD src0_sel:DWORD src1_sel:WORD_1\n"
    "v_lshlrev_b32_sdwa v191, 1, v182 dst_sel:DWORD dst_unused:UNUSED_PAD src0_sel:DWORD src1_sel:WORD_1\n"
    "v_fma_mix_f32 v234, v192, v232, 0 op_sel_hi:[1,1,0]\n"
    "v_mfma_f32_16x16x32_bf16 v[172:175], a[68:71], a[232:235], v[172:175]\n"
    "ds_read_u16 v187, v187\n"
    "ds_read_b128 v[12:15], v244 offset:6144\n"
    "v_or_b32_e64 v199, v199, v207\n"
    "v_lshlrev_b32_sdwa v207, 1, v183 dst_sel:DWORD dst_unused:UNUSED_PAD src0_sel:DWORD src1_sel:WORD_1\n"
    "v_fma_mix_f32 v232, v192, v232, 0 op_sel:[1,1,0] op_sel_hi:[1,1,0]\n"
    "v_mfma_f32_16x16x32_bf16 v[176:179], a[104:107], a[236:239], 0\n"
    "s_nop 0\n"
    "ds_read_u16_d16_hi v203, v203\n"
    "v_cvt_pk_bf16_f32 v192, v234, v232\n"
    "v_mfma_f32_16x16x32_bf16 v[176:179], a[152:155], a[240:243], v[176:179]\n"
    "s_waitcnt lgkmcnt(8)\n"
    "ds_read_u16 v188, v188\n"
    "v_pk_fma_f16 v232, v196, v196, s48\n"
    "v_mfma_f32_16x16x32_bf16 v[176:179], a[64:67], a[244:247], v[176:179]\n"
    "v_pk_mul_f16 v232, v196, v232\n"
    "v_or_b32_e64 v184, v184, v200\n"
    "v_mfma_f32_16x16x32_bf16 v[180:183], a[108:111], a[236:239], 0\n"
    "ds_read_u16_d16_hi v204, v204\n"
    "v_exp_f16_e32 v234, v232\n"
    "v_mfma_f32_16x16x32_bf16 v[180:183], a[156:159], a[240:243], v[180:183]\n"
    "s_waitcnt lgkmcnt(8)\n"
    "ds_read_u16 v189, v189\n"
    "v_exp_f16_sdwa v234, v232 dst_sel:WORD_1 dst_unused:UNUSED_PRESERVE src0_sel:WORD_1\n"
    "v_mfma_f32_16x16x32_bf16 v[180:183], a[68:71], a[244:247], v[180:183]\n"
    "v_pk_fma_f16 v234, v234, s49, s49\n"
    "v_or_b32_e64 v185, v185, v201\n"
    "v_mfma_f32_16x16x32_bf16 v[96:99], a[16:19], v[192:195], v[96:99]\n"
    "ds_read_u16_d16_hi v205, v205\n"
    "v_rcp_f16_e64 v232, v234\n"
    "v_mfma_f32_16x16x32_bf16 v[100:103], a[20:23], v[192:195], v[100:103]\n"
    "ds_read_u16 v190, v190\n"
    "v_rcp_f16_sdwa v232, v234 dst_sel:WORD_1 dst_unused:UNUSED_PRESERVE src0_sel:WORD_1\n"
    "v_mfma_f32_16x16x32_bf16 v[112:115], a[24:27], v[192:195], v[112:115]\n"
    "s_waitcnt lgkmcnt(8)\n"
    "v_fma_mix_f32 v234, v196, v232, 0 op_sel_hi:[1,1,0]\n"
    "v_fma_mix_f32 v232, v196, v232, 0 op_sel:[1,1,0] op_sel_hi:[1,1,0]\n"
    "v_mfma_f32_16x16x32_bf16 v[116:119], a[28:31], v[192:195], v[116:119]\n"
    "s_waitcnt lgkmcnt(6)\n"
    "ds_read_u16_d16_hi v206, v206\n"
    "v_cvt_pk_bf16_f32 v196, v234, v232\n"
    "v_mfma_f32_16x16x32_bf16 v[128:131], v[8:11], v[192:195], v[128:131]\n"
    "ds_read_u16 v191, v191\n"
    "v_lshlrev_b32_sdwa v201, 1, v171 dst_sel:DWORD dst_unused:UNUSED_PAD src0_sel:DWORD src1_sel:WORD_1\n"
    "v_mfma_f32_16x16x32_bf16 v[132:135], v[12:15], v[192:195], v[132:135]\n"
    "v_cvt_pk_f16_f32 v192, v168, v169\n"
    "v_lshlrev_b32_sdwa v193, 1, v170 dst_sel:DWORD dst_unused:UNUSED_PAD src0_sel:DWORD src1_sel:WORD_1\n"
    "v_mfma_f32_16x16x32_bf16 v[104:107], a[16:19], v[196:199], v[104:107]\n"
    "s_waitcnt lgkmcnt(7)\n"
    "ds_read_u16_d16_hi v207, v207\n"
    "v_lshlrev_b32_sdwa v194, 1, v172 dst_sel:DWORD dst_unused:UNUSED_PAD src0_sel:DWORD src1_sel:WORD_1\n"
    "v_mfma_f32_16x16x32_bf16 v[108:111], a[20:23], v[196:199], v[108:111]\n"
    "v_or_b32_e32 v186, v186, v202\n"
    "v_lshlrev_b32_sdwa v202, 1, v173 dst_sel:DWORD dst_unused:UNUSED_PAD src0_sel:DWORD src1_sel:WORD_1\n"
    "v_mfma_f32_16x16x32_bf16 v[120:123], a[24:27], v[196:199], v[120:123]\n"
    "ds_read_b128 v[52:55], v244 offset:34816\n"
    "v_lshlrev_b32_sdwa v195, 1, v174 dst_sel:DWORD dst_unused:UNUSED_PAD src0_sel:DWORD src1_sel:WORD_1\n"
    "v_mfma_f32_16x16x32_bf16 v[124:127], a[28:31], v[196:199], v[124:127]\n"
    "ds_read_u16 v193, v193\n"
    "v_or_b32_e32 v187, v187, v203\n"
    "v_mfma_f32_16x16x32_bf16 v[136:139], v[8:11], v[196:199], v[136:139]\n"
    "s_waitcnt lgkmcnt(8)\n"
    "v_lshlrev_b32_sdwa v203, 1, v175 dst_sel:DWORD dst_unused:UNUSED_PAD src0_sel:DWORD src1_sel:WORD_1\n"
    "v_or_b32_e32 v188, v188, v204\n"
    "v_mfma_f32_16x16x32_bf16 v[140:143], v[12:15], v[196:199], v[140:143]\n"
    "ds_read_u16_d16_hi v201, v201\n"
    "buffer_store_dwordx4 v[228:231], v245, s[68:71], s59 offen offset:128 sc1\n"
    "v_mfma_f32_16x16x32_bf16 v[168:171], a[112:115], a[224:227], 0\n"
    "s_waitcnt lgkmcnt(7)\n"
    "ds_read_u16 v194, v194\n"
    "v_cvt_pk_f16_f32 v196, v176, v177\n"
    "v_mfma_f32_16x16x32_bf16 v[168:171], a[160:163], a[228:231], v[168:171]\n"
    "ds_read_u16_d16_hi v202, v202\n"
    "v_lshlrev_b32_sdwa v197, 1, v178 dst_sel:DWORD dst_unused:UNUSED_PAD src0_sel:DWORD src1_sel:WORD_1\n"
    "v_mfma_f32_16x16x32_bf16 v[168:171], a[72:75], a[232:235], v[168:171]\n"
    "v_lshlrev_b32_sdwa v198, 1, v180 dst_sel:DWORD dst_unused:UNUSED_PAD src0_sel:DWORD src1_sel:WORD_1\n"
    "v_lshlrev_b32_sdwa v199, 1, v182 dst_sel:DWORD dst_unused:UNUSED_PAD src0_sel:DWORD src1_sel:WORD_1\n"
    "v_mfma_f32_16x16x32_bf16 v[172:175], a[116:119], a[224:227], 0\n"
    "ds_read_b128 v[16:19], v244 offset:8192\n"
    "v_or_b32_e32 v189, v189, v205\n"
    "v_mfma_f32_16x16x32_bf16 v[172:175], a[164:167], a[228:231], v[172:175]\n"
    "s_waitcnt lgkmcnt(8)\n"
    "ds_read_u16 v195, v195\n"
    "v_lshlrev_b32_sdwa v205, 1, v179 dst_sel:DWORD dst_unused:UNUSED_PAD src0_sel:DWORD src1_sel:WORD_1\n"
    "v_mfma_f32_16x16x32_bf16 v[176:179], a[112:115], a[236:239], 0\n"
    "v_or_b32_e64 v190, v190, v206\n"
    "v_lshlrev_b32_sdwa v206, 1, v181 dst_sel:DWORD dst_unused:UNUSED_PAD src0_sel:DWORD src1_sel:WORD_1\n"
    "v_mfma_f32_16x16x32_bf16 v[176:179], a[160:163], a[240:243], v[176:179]\n"
    "ds_read_u16_d16_hi v203, v203\n"
    "v_pk_fma_f16 v232, v192, v192, s48\n"
    "v_mfma_f32_16x16x32_bf16 v[176:179], a[72:75], a[244:247], v[176:179]\n"
    "s_waitcnt lgkmcnt(7)\n"
    "ds_read_b128 v[20:23], v244 offset:10240\n"
    "v_or_b32_e32 v191, v191, v207\n"
    "v_mfma_f32_16x16x32_bf16 v[172:175], v[52:55], a[232:235], v[172:175]\n"
    "ds_read_u16 v197, v197\n"
    "v_lshlrev_b32_sdwa v207, 1, v183 dst_sel:DWORD dst_unused:UNUSED_PAD src0_sel:DWORD src1_sel:WORD_1\n"
    "v_mfma_f32_16x16x32_bf16 v[180:183], a[116:119], a[236:239], 0\n"
    "v_pk_mul_f16 v232, v192, v232\n"
    "v_lshlrev_b32_sdwa v200, 1, v169 dst_sel:DWORD dst_unused:UNUSED_PAD src0_sel:DWORD src1_sel:WORD_1\n"
    "v_mfma_f32_16x16x32_bf16 v[180:183], a[164:167], a[240:243], v[180:183]\n"
    "ds_read_u16_d16_hi v205, v205\n"
    "v_exp_f16_e64 v234, v232\n"
    "v_mfma_f32_16x16x32_bf16 v[180:183], v[52:55], a[244:247], v[180:183]\n"
    "s_waitcnt lgkmcnt(8)\n"
    "v_or_b32_e32 v193, v193, v201\n"
    "v_exp_f16_sdwa v234, v232 dst_sel:WORD_1 dst_unused:UNUSED_PRESERVE src0_sel:WORD_1\n"
    "v_mfma_f32_16x16x32_bf16 v[96:99], a[32:35], v[184:187], v[96:99]\n"
    "ds_read_u16 v198, v198\n"
    "v_pk_fma_f16 v234, v234, s49, s49\n"
    "v_mfma_f32_16x16x32_bf16 v[100:103], a[36:39], v[184:187], v[100:103]\n"
    "ds_read_u16_d16_hi v206, v206\n"
    "v_lshlrev_b32_sdwa v201, 1, v171 dst_sel:DWORD dst_unused:UNUSED_PAD src0_sel:DWORD src1_sel:WORD_1\n"
    "v_mfma_f32_16x16x32_bf16 v[112:115], a[40:43], v[184:187], v[112:115]\n"
    "s_waitcnt lgkmcnt(7)\n"
    "v_or_b32_e32 v194, v194, v202\n"
    "v_lshlrev_b32_sdwa v202, 1, v173 dst_sel:DWORD dst_unused:UNUSED_PAD src0_sel:DWORD src1_sel:WORD_1\n"
    "v_mfma_f32_16x16x32_bf16 v[116:119], a[44:47], v[184:187], v[116:119]\n"
    "ds_read_u16 v199, v199\n"
    "v_rcp_f16_e64 v232, v234\n"
    "v_mfma_f32_16x16x32_bf16 v[128:131], v[16:19], v[184:187], v[128:131]\n"
    "ds_read_u16_d16_hi v207, v207\n"
    "v_rcp_f16_sdwa v232, v234 dst_sel:WORD_1 dst_unused:UNUSED_PRESERVE src0_sel:WORD_1\n"
    "v_mfma_f32_16x16x32_bf16 v[104:107], a[32:35], v[188:191], v[104:107]\n"
    "v_lshlrev_b32_sdwa v204, 1, v177 dst_sel:DWORD dst_unused:UNUSED_PAD src0_sel:DWORD src1_sel:WORD_1\n"
    "v_fma_mix_f32 v234, v192, v232, 0 op_sel_hi:[1,1,0]\n"
    "v_mfma_f32_16x16x32_bf16 v[108:111], a[36:39], v[188:191], v[108:111]\n"
    "s_waitcnt lgkmcnt(6)\n"
    "ds_read_b128 v[60:63], v244 offset:38912\n"
    "v_or_b32_e32 v195, v195, v203\n"
    "v_mfma_f32_16x16x32_bf16 v[132:135], v[20:23], v[184:187], v[132:135]\n"
    "ds_read_u16_d16_hi v200, v200\n"
    "v_lshlrev_b32_sdwa v184, 1, v168 dst_sel:DWORD dst_unused:UNUSED_PAD src0_sel:DWORD src1_sel:WORD_1\n"
    "v_mfma_f32_16x16x32_bf16 v[120:123], a[40:43], v[188:191], v[120:123]\n"
    "v_lshlrev_b32_sdwa v185, 1, v170 dst_sel:DWORD dst_unused:UNUSED_PAD src0_sel:DWORD src1_sel:WORD_1\n"
    "v_lshlrev_b32_sdwa v186, 1, v172 dst_sel:DWORD dst_unused:UNUSED_PAD src0_sel:DWORD src1_sel:WORD_1\n"
    "v_mfma_f32_16x16x32_bf16 v[124:127], a[44:47], v[188:191], v[124:127]\n"
    "ds_read_u16 v184, v184\n"
    "v_lshlrev_b32_sdwa v187, 1, v174 dst_sel:DWORD dst_unused:UNUSED_PAD src0_sel:DWORD src1_sel:WORD_1\n"
    "v_mfma_f32_16x16x32_bf16 v[136:139], v[16:19], v[188:191], v[136:139]\n"
    "ds_read_u16 v185, v185\n"
    "v_lshlrev_b32_sdwa v203, 1, v175 dst_sel:DWORD dst_unused:UNUSED_PAD src0_sel:DWORD src1_sel:WORD_1\n"
    "v_mfma_f32_16x16x32_bf16 v[140:143], v[20:23], v[188:191], v[140:143]\n"
    "s_waitcnt lgkmcnt(8)\n"
    "v_or_b32_e32 v197, v197, v205\n"
    "v_lshlrev_b32_sdwa v188, 1, v176 dst_sel:DWORD dst_unused:UNUSED_PAD src0_sel:DWORD src1_sel:WORD_1\n"
    "v_mfma_f32_16x16x32_bf16 v[168:171], a[120:123], a[224:227], 0\n"
    "ds_read_u16_d16_hi v201, v201\n"
    "buffer_store_dwordx4 v[216:219], v245, s[68:71], s39 offen offset:0 sc1\n"
    "v_mfma_f32_16x16x32_bf16 v[168:171], a[168:171], a[228:231], v[168:171]\n"
    "ds_read_u16 v186, v186\n"
    "v_lshlrev_b32_sdwa v189, 1, v178 dst_sel:DWORD dst_unused:UNUSED_PAD src0_sel:DWORD src1_sel:WORD_1\n"
    "v_mfma_f32_16x16x32_bf16 v[168:171], a[76:79], a[232:235], v[168:171]\n"
    "s_waitcnt lgkmcnt(8)\n"
    "v_or_b32_e32 v198, v198, v206\n"
    "v_lshlrev_b32_sdwa v205, 1, v179 dst_sel:DWORD dst_unused:UNUSED_PAD src0_sel:DWORD src1_sel:WORD_1\n"
    "v_mfma_f32_16x16x32_bf16 v[172:175], a[124:127], a[224:227], 0\n"
    "ds_read_b128 v[24:27], v244 offset:12288\n"
    "ds_read_u16_d16_hi v202, v202\n"
    "v_mfma_f32_16x16x32_bf16 v[172:175], a[172:175], a[228:231], v[172:175]\n"
    "v_lshlrev_b32_sdwa v190, 1, v180 dst_sel:DWORD dst_unused:UNUSED_PAD src0_sel:DWORD src1_sel:WORD_1\n"
    "v_lshlrev_b32_sdwa v206, 1, v181 dst_sel:DWORD dst_unused:UNUSED_PAD src0_sel:DWORD src1_sel:WORD_1\n"
    "s_waitcnt lgkmcnt(7)\n"
    "v_mfma_f32_16x16x32_bf16 v[172:175], v[60:63], a[232:235], v[172:175]\n"
    "s_nop 0\n"
    "ds_read_u16 v187, v187\n"
    "v_lshlrev_b32_sdwa v191, 1, v182 dst_sel:DWORD dst_unused:UNUSED_PAD src0_sel:DWORD src1_sel:WORD_1\n"
    "v_or_b32_e64 v199, v199, v207\n"
    "v_lshlrev_b32_sdwa v207, 1, v183 dst_sel:DWORD dst_unused:UNUSED_PAD src0_sel:DWORD src1_sel:WORD_1\n"
    "v_fma_mix_f32 v232, v192, v232, 0 op_sel:[1,1,0] op_sel_hi:[1,1,0]\n"
    "v_mfma_f32_16x16x32_bf16 v[176:179], a[120:123], a[236:239], 0\n"
    "ds_read_b128 v[28:31], v244 offset:14336\n"
    "v_cvt_pk_bf16_f32 v192, v234, v232\n"
    "v_mfma_f32_16x16x32_bf16 v[176:179], a[168:171], a[240:243], v[176:179]\n"
    "ds_read_u16_d16_hi v203, v203\n"
    "v_pk_fma_f16 v232, v196, v196, s48\n"
    "v_mfma_f32_16x16x32_bf16 v[176:179], a[76:79], a[244:247], v[176:179]\n"
    "v_pk_mul_f16 v232, v196, v232\n"
    "v_exp_f16_e32 v234, v232\n"
    "v_mfma_f32_16x16x32_bf16 v[180:183], a[124:127], a[236:239], 0\n"
    "s_waitcnt lgkmcnt(8)\n"
    "ds_read_u16 v188, v188\n"
    "v_exp_f16_sdwa v234, v232 dst_sel:WORD_1 dst_unused:UNUSED_PRESERVE src0_sel:WORD_1\n"
    "v_mfma_f32_16x16x32_bf16 v[180:183], a[172:175], a[240:243], v[180:183]\n"
    "ds_read_u16_d16_hi v204, v204\n"
    "v_pk_fma_f16 v234, v234, s49, s49\n"
    "v_mfma_f32_16x16x32_bf16 v[180:183], v[60:63], a[244:247], v[180:183]\n"
    "v_rcp_f16_e32 v232, v234\n"
    "v_mfma_f32_16x16x32_bf16 v[96:99], a[176:179], v[192:195], v[96:99]\n"
    "s_waitcnt lgkmcnt(8)\n"
    "ds_read_u16 v189, v189\n"
    "v_rcp_f16_sdwa v232, v234 dst_sel:WORD_1 dst_unused:UNUSED_PRESERVE src0_sel:WORD_1\n"
    "v_mfma_f32_16x16x32_bf16 v[100:103], a[180:183], v[192:195], v[100:103]\n"
    "ds_read_u16_d16_hi v205, v205\n"
    "v_fma_mix_f32 v234, v196, v232, 0 op_sel_hi:[1,1,0]\n"
    "v_mfma_f32_16x16x32_bf16 v[112:115], a[184:187], v[192:195], v[112:115]\n"
    "s_waitcnt lgkmcnt(8)\n"
    "v_fma_mix_f32 v232, v196, v232, 0 op_sel:[1,1,0] op_sel_hi:[1,1,0]\n"
    "v_or_b32_e32 v184, v184, v200\n"
    "v_mfma_f32_16x16x32_bf16 v[116:119], a[188:191], v[192:195], v[116:119]\n"
    "ds_read_b128 a[224:227], v243 offset:0\n"
    "v_cvt_pk_bf16_f32 v196, v234, v232\n"
    "v_mfma_f32_16x16x32_bf16 v[128:131], v[24:27], v[192:195], v[128:131]\n"
    "s_waitcnt lgkmcnt(8)\n"
    "ds_read_u16 v190, v190\n"
    "v_or_b32_e32 v185, v185, v201\n"
    "v_mfma_f32_16x16x32_bf16 v[104:107], a[176:179], v[196:199], v[104:107]\n"
    "s_waitcnt lgkmcnt(7)\n"
    "v_lshlrev_b32_sdwa v201, 1, v171 dst_sel:DWORD dst_unused:UNUSED_PAD src0_sel:DWORD src1_sel:WORD_1\n"
    "v_or_b32_e32 v186, v186, v202\n"
    "v_mfma_f32_16x16x32_bf16 v[132:135], v[28:31], v[192:195], v[132:135]\n"
    "ds_read_b128 a[228:231], v243 offset:1024\n"
    "v_cvt_pk_f16_f32 v192, v168, v169\n"
    "v_mfma_f32_16x16x32_bf16 v[108:111], a[180:183], v[196:199], v[108:111]\n"
    "s_nop 0\n"
    "ds_read_u16_d16_hi v206, v206\n"
    "v_lshlrev_b32_sdwa v193, 1, v170 dst_sel:DWORD dst_unused:UNUSED_PAD src0_sel:DWORD src1_sel:WORD_1\n"
    "v_mfma_f32_16x16x32_bf16 v[120:123], a[184:187], v[196:199], v[120:123]\n"
    "s_waitcnt lgkmcnt(8)\n"
    "v_lshlrev_b32_sdwa v194, 1, v172 dst_sel:DWORD dst_unused:UNUSED_PAD src0_sel:DWORD src1_sel:WORD_1\n"
    "v_lshlrev_b32_sdwa v202, 1, v173 dst_sel:DWORD dst_unused:UNUSED_PAD src0_sel:DWORD src1_sel:WORD_1\n"
    "v_mfma_f32_16x16x32_bf16 v[124:127], a[188:191], v[196:199], v[124:127]\n"
    "ds_read_b128 a[232:235], v243 offset:2048\n"
    "ds_read_u16 v191, v191\n"
    "v_mfma_f32_16x16x32_bf16 v[136:139], v[24:27], v[196:199], v[136:139]\n"
    "v_lshlrev_b32_sdwa v195, 1, v174 dst_sel:DWORD dst_unused:UNUSED_PAD src0_sel:DWORD src1_sel:WORD_1\n"
    "v_or_b32_e64 v187, v187, v203\n"
    "v_mfma_f32_16x16x32_bf16 v[140:143], v[28:31], v[196:199], v[140:143]\n"
    "ds_read_b128 a[236:239], v243 offset:3072\n"
    "v_lshlrev_b32_sdwa v203, 1, v175 dst_sel:DWORD dst_unused:UNUSED_PAD src0_sel:DWORD src1_sel:WORD_1\n"
    "v_cvt_pk_f16_f32 v196, v176, v177\n"
    "v_lshlrev_b32_sdwa v197, 1, v178 dst_sel:DWORD dst_unused:UNUSED_PAD src0_sel:DWORD src1_sel:WORD_1\n"
    "v_lshlrev_b32_sdwa v198, 1, v180 dst_sel:DWORD dst_unused:UNUSED_PAD src0_sel:DWORD src1_sel:WORD_1\n"
    "v_lshlrev_b32_sdwa v199, 1, v182 dst_sel:DWORD dst_unused:UNUSED_PAD src0_sel:DWORD src1_sel:WORD_1\n"
    "s_waitcnt lgkmcnt(9)\n"
    "v_or_b32_e32 v188, v188, v204\n"
    "ds_read_u16_d16_hi v207, v207\n"
    "ds_read_b128 a[240:243], v243 offset:4096\n"
    "v_pk_fma_f16 v232, v192, v192, s48\n"
    "v_pk_mul_f16 v232, v192, v232\n"
    "v_exp_f16_e64 v234, v232\n"
    "s_waitcnt lgkmcnt(8)\n"
    "v_mfma_f32_16x16x32_bf16 v[168:171], a[80:83], a[224:227], 0\n"
    "ds_read_b128 a[244:247], v243 offset:5120\n"
    "buffer_store_dwordx4 v[220:223], v245, s[68:71], s39 offen offset:64 sc1\n"
    "v_mfma_f32_16x16x32_bf16 v[172:175], a[84:87], a[224:227], 0\n"
    "s_waitcnt lgkmcnt(7)\n"
    "ds_read_b128 v[48:51], v244 offset:32768\n"
    "ds_read_u16 v193, v193\n"
    "v_mfma_f32_16x16x32_bf16 v[168:171], a[128:131], a[228:231], v[168:171]\n"
    "ds_read_u16_d16_hi v201, v201\n"
    "v_or_b32_e64 v189, v189, v205\n"
    "v_mfma_f32_16x16x32_bf16 v[172:175], a[132:135], a[228:231], v[172:175]\n"
    "v_lshlrev_b32_sdwa v205, 1, v179 dst_sel:DWORD dst_unused:UNUSED_PAD src0_sel:DWORD src1_sel:WORD_1\n"
    "v_exp_f16_sdwa v234, v232 dst_sel:WORD_1 dst_unused:UNUSED_PRESERVE src0_sel:WORD_1\n"
    "s_waitcnt lgkmcnt(8)\n"
    "v_mfma_f32_16x16x32_bf16 v[168:171], a[48:51], a[232:235], v[168:171]\n"
    "s_nop 0\n"
    "ds_read_u16 v194, v194\n"
    "v_or_b32_e32 v190, v190, v206\n"
    "v_mfma_f32_16x16x32_bf16 v[96:99], a[192:195], v[184:187], v[96:99]\n"
    "ds_read_u16_d16_hi v202, v202\n"
    "v_lshlrev_b32_sdwa v206, 1, v181 dst_sel:DWORD dst_unused:UNUSED_PAD src0_sel:DWORD src1_sel:WORD_1\n"
    "v_mfma_f32_16x16x32_bf16 v[100:103], a[196:199], v[184:187], v[100:103]\n"
    "s_waitcnt lgkmcnt(8)\n"
    "v_pk_fma_f16 v234, v234, s49, s49\n"
    "v_lshlrev_b32_sdwa v200, 1, v169 dst_sel:DWORD dst_unused:UNUSED_PAD src0_sel:DWORD src1_sel:WORD_1\n"
    "v_mfma_f32_16x16x32_bf16 v[176:179], a[80:83], a[236:239], 0\n"
    "ds_read_b128 v[32:35], v244 offset:16384\n"
    "v_rcp_f16_e64 v232, v234\n"
    "v_mfma_f32_16x16x32_bf16 v[112:115], a[200:203], v[184:187], v[112:115]\n"
    "ds_read_u16 v195, v195\n"
    "v_rcp_f16_sdwa v232, v234 dst_sel:WORD_1 dst_unused:UNUSED_PRESERVE src0_sel:WORD_1\n"
    "s_waitcnt lgkmcnt(8)\n"
    "v_mfma_f32_16x16x32_bf16 v[176:179], a[128:131], a[240:243], v[176:179]\n"
    "s_waitcnt lgkmcnt(7)\n"
    "ds_read_u16_d16_hi v203, v203\n"
    "v_or_b32_e32 v191, v191, v207\n"
    "v_mfma_f32_16x16x32_bf16 v[176:179], a[48:51], a[244:247], v[176:179]\n"
    "s_waitcnt lgkmcnt(7)\n"
    "v_lshlrev_b32_sdwa v207, 1, v183 dst_sel:DWORD dst_unused:UNUSED_PAD src0_sel:DWORD src1_sel:WORD_1\n"
    "v_fma_mix_f32 v234, v192, v232, 0 op_sel_hi:[1,1,0]\n"
    "v_mfma_f32_16x16x32_bf16 v[172:175], v[48:51], a[232:235], v[172:175]\n"
    "ds_read_b128 v[36:39], v244 offset:18432\n"
    "ds_read_u16 v197, v197\n"
    "v_mfma_f32_16x16x32_bf16 v[180:183], a[84:87], a[236:239], 0\n"
    "v_fma_mix_f32 v232, v192, v232, 0 op_sel:[1,1,0] op_sel_hi:[1,1,0]\n"
    "v_lshlrev_b32_sdwa v204, 1, v177 dst_sel:DWORD dst_unused:UNUSED_PAD src0_sel:DWORD src1_sel:WORD_1\n"
    "v_mfma_f32_16x16x32_bf16 v[180:183], a[132:135], a[240:243], v[180:183]\n"
    "ds_read_u16_d16_hi v205, v205\n"
    "v_cvt_pk_bf16_f32 v192, v234, v232\n"
    "v_mfma_f32_16x16x32_bf16 v[180:183], v[48:51], a[244:247], v[180:183]\n"
    "v_pk_fma_f16 v232, v196, v196, s48\n"
    "v_pk_mul_f16 v232, v196, v232\n"
    "v_mfma_f32_16x16x32_bf16 v[116:119], a[204:207], v[184:187], v[116:119]\n"
    "s_waitcnt lgkmcnt(8)\n"
    "ds_read_u16 v198, v198\n"
    "v_or_b32_e32 v193, v193, v201\n"
    "v_mfma_f32_16x16x32_bf16 v[104:107], a[192:195], v[188:191], v[104:107]\n"
    "v_lshlrev_b32_sdwa v201, 1, v171 dst_sel:DWORD dst_unused:UNUSED_PAD src0_sel:DWORD src1_sel:WORD_1\n"
    "v_exp_f16_e64 v234, v232\n"
    "v_mfma_f32_16x16x32_bf16 v[108:111], a[196:199], v[188:191], v[108:111]\n"
    "ds_read_u16_d16_hi v206, v206\n"
    "v_exp_f16_sdwa v234, v232 dst_sel:WORD_1 dst_unused:UNUSED_PRESERVE src0_sel:WORD_1\n"
    "v_mfma_f32_16x16x32_bf16 v[120:123], a[200:203], v[188:191], v[120:123]\n"
    "s_waitcnt lgkmcnt(7)\n"
    "ds_read_u16 v199, v199\n"
    "v_or_b32_e32 v194, v194, v202\n"
    "v_mfma_f32_16x16x32_bf16 v[128:131], v[32:35], v[184:187], v[128:131]\n"
    "v_lshlrev_b32_sdwa v202, 1, v173 dst_sel:DWORD dst_unused:UNUSED_PAD src0_sel:DWORD src1_sel:WORD_1\n"
    "v_pk_fma_f16 v234, v234, s49, s49\n"
    "v_mfma_f32_16x16x32_bf16 v[124:127], a[204:207], v[188:191], v[124:127]\n"
    "ds_read_u16_d16_hi v207, v207\n"
    "v_rcp_f16_e64 v232, v234\n"
    "v_mfma_f32_16x16x32_bf16 v[136:139], v[32:35], v[188:191], v[136:139]\n"
    "ds_read_u16_d16_hi v200, v200\n"
    "v_rcp_f16_sdwa v232, v234 dst_sel:WORD_1 dst_unused:UNUSED_PRESERVE src0_sel:WORD_1\n"
    "s_waitcnt lgkmcnt(7)\n"
    "v_mfma_f32_16x16x32_bf16 v[132:135], v[36:39], v[184:187], v[132:135]\n"
    "s_nop 0\n"
    "buffer_store_dwordx4 v[160:163], v245, s[68:71], s39 offen offset:128 sc1\n"
    "ds_read_u16_d16_hi v201, v201\n"
    "v_mfma_f32_16x16x32_bf16 v[140:143], v[36:39], v[188:191], v[140:143]\n"
    "ds_read_b128 v[40:43], v244 offset:20480\n"
    "v_or_b32_e64 v195, v195, v203\n"
    "v_lshlrev_b32_sdwa v184, 1, v168 dst_sel:DWORD dst_unused:UNUSED_PAD src0_sel:DWORD src1_sel:WORD_1\n"
    "v_lshlrev_b32_sdwa v185, 1, v170 dst_sel:DWORD dst_unused:UNUSED_PAD src0_sel:DWORD src1_sel:WORD_1\n"
    "v_mfma_f32_16x16x32_bf16 v[168:171], a[88:91], a[224:227], 0\n"
    "ds_read_u16 v184, v184\n"
    "v_lshlrev_b32_sdwa v186, 1, v172 dst_sel:DWORD dst_unused:UNUSED_PAD src0_sel:DWORD src1_sel:WORD_1\n"
    "v_mfma_f32_16x16x32_bf16 v[168:171], a[136:139], a[228:231], v[168:171]\n"
    "s_waitcnt lgkmcnt(8)\n"
    "v_or_b32_e32 v197, v197, v205\n"
    "v_lshlrev_b32_sdwa v187, 1, v174 dst_sel:DWORD dst_unused:UNUSED_PAD src0_sel:DWORD src1_sel:WORD_1\n"
    "v_mfma_f32_16x16x32_bf16 v[168:171], a[52:55], a[232:235], v[168:171]\n"
    "ds_read_u16 v185, v185\n"
    "v_lshlrev_b32_sdwa v203, 1, v175 dst_sel:DWORD dst_unused:UNUSED_PAD src0_sel:DWORD src1_sel:WORD_1\n"
    "v_mfma_f32_16x16x32_bf16 v[172:175], a[92:95], a[224:227], 0\n"
    "ds_read_u16 v186, v186\n"
    "v_lshlrev_b32_sdwa v188, 1, v176 dst_sel:DWORD dst_unused:UNUSED_PAD src0_sel:DWORD src1_sel:WORD_1\n"
    "v_mfma_f32_16x16x32_bf16 v[172:175], a[140:143], a[228:231], v[172:175]\n"
    "s_waitcnt lgkmcnt(8)\n"
    "v_or_b32_e32 v198, v198, v206\n"
    "v_lshlrev_b32_sdwa v189, 1, v178 dst_sel:DWORD dst_unused:UNUSED_PAD src0_sel:DWORD src1_sel:WORD_1\n"
    "v_mfma_f32_16x16x32_bf16 v[172:175], v[56:59], a[232:235], v[172:175]\n"
    "ds_read_u16_d16_hi v202, v202\n"
    "v_lshlrev_b32_sdwa v205, 1, v179 dst_sel:DWORD dst_unused:UNUSED_PAD src0_sel:DWORD src1_sel:WORD_1\n"
    "v_mfma_f32_16x16x32_bf16 v[176:179], a[88:91], a[236:239], 0\n"
    "v_lshlrev_b32_sdwa v190, 1, v180 dst_sel:DWORD dst_unused:UNUSED_PAD src0_sel:DWORD src1_sel:WORD_1\n"
    "v_lshlrev_b32_sdwa v206, 1, v181 dst_sel:DWORD dst_unused:UNUSED_PAD src0_sel:DWORD src1_sel:WORD_1\n"
    "v_mfma_f32_16x16x32_bf16 v[176:179], a[136:139], a[240:243], v[176:179]\n"
    "ds_read_u16 v187, v187\n"
    "v_lshlrev_b32_sdwa v191, 1, v182 dst_sel:DWORD dst_unused:UNUSED_PAD src0_sel:DWORD src1_sel:WORD_1\n"
    "v_mfma_f32_16x16x32_bf16 v[176:179], a[52:55], a[244:247], v[176:179]\n"
    "s_waitcnt lgkmcnt(8)\n"
    "v_or_b32_e32 v199, v199, v207\n"
    "v_lshlrev_b32_sdwa v207, 1, v183 dst_sel:DWORD dst_unused:UNUSED_PAD src0_sel:DWORD src1_sel:WORD_1\n"
    "v_mfma_f32_16x16x32_bf16 v[180:183], a[92:95], a[236:239], 0\n"
    "ds_read_b128 v[44:47], v244 offset:22528\n"
    "ds_read_u16_d16_hi v203, v203\n"
    "v_mfma_f32_16x16x32_bf16 v[180:183], a[140:143], a[240:243], v[180:183]\n"
    "s_nop 0\n"
    "v_fma_mix_f32 v234, v196, v232, 0 op_sel_hi:[1,1,0]\n"
    "v_fma_mix_f32 v232, v196, v232, 0 op_sel:[1,1,0] op_sel_hi:[1,1,0]\n"
    "v_mfma_f32_16x16x32_bf16 v[180:183], v[56:59], a[244:247], v[180:183]\n"
    "s_waitcnt lgkmcnt(7)\n"
    "ds_read_u16 v188, v188\n"
    "v_cvt_pk_bf16_f32 v196, v234, v232\n"
    "v_mfma_f32_16x16x32_bf16 v[96:99], a[208:211], v[192:195], v[96:99]\n"
    "v_mfma_f32_16x16x32_bf16 v[100:103], a[212:215], v[192:195], v[100:103]\n"
    "ds_read_u16_d16_hi v204, v204\n"
    "v_mfma_f32_16x16x32_bf16 v[112:115], a[216:219], v[192:195], v[112:115]\n"
    "s_waitcnt lgkmcnt(8)\n"
    "v_or_b32_e32 v184, v184, v200\n"
    "v_mfma_f32_16x16x32_bf16 v[116:119], a[220:223], v[192:195], v[116:119]\n"
    "ds_read_u16 v189, v189\n"
    "v_mfma_f32_16x16x32_bf16 v[128:131], v[40:43], v[192:195], v[128:131]\n"
    "s_waitcnt lgkmcnt(8)\n"
    "v_or_b32_e32 v185, v185, v201\n"
    "v_lshlrev_b32_sdwa v201, 1, v171 dst_sel:DWORD dst_unused:UNUSED_PAD src0_sel:DWORD src1_sel:WORD_1\n"
    "v_mfma_f32_16x16x32_bf16 v[104:107], a[208:211], v[196:199], v[104:107]\n"
    "ds_read_u16_d16_hi v205, v205\n"
    "v_cvt_pk_bf16_f32 v96, v96, v97\n"
    "v_mfma_f32_16x16x32_bf16 v[108:111], a[212:215], v[196:199], v[108:111]\n"
    "ds_read_u16 v190, v190\n"
    "v_cvt_pk_bf16_f32 v97, v98, v99\n"
    "v_mfma_f32_16x16x32_bf16 v[120:123], a[216:219], v[196:199], v[120:123]\n"
    "s_waitcnt lgkmcnt(8)\n"
    "v_or_b32_e32 v186, v186, v202\n"
    "v_lshlrev_b32_sdwa v202, 1, v173 dst_sel:DWORD dst_unused:UNUSED_PAD src0_sel:DWORD src1_sel:WORD_1\n"
    "v_mfma_f32_16x16x32_bf16 v[124:127], a[220:223], v[196:199], v[124:127]\n"
    "s_nop 0\n"
    "ds_read_u16_d16_hi v206, v206\n"
    "v_cvt_pk_bf16_f32 v98, v100, v101\n"
    "v_mfma_f32_16x16x32_bf16 v[136:139], v[40:43], v[196:199], v[136:139]\n"
    "s_waitcnt lgkmcnt(7)\n"
    "v_cvt_pk_bf16_f32 v99, v102, v103\n"
    "v_cvt_pk_bf16_f32 v112, v112, v113\n"
    "v_mfma_f32_16x16x32_bf16 v[132:135], v[44:47], v[192:195], v[132:135]\n"
    "v_cvt_pk_f16_f32 v192, v168, v169\n"
    "v_lshlrev_b32_sdwa v193, 1, v170 dst_sel:DWORD dst_unused:UNUSED_PAD src0_sel:DWORD src1_sel:WORD_1\n"
    "v_mfma_f32_16x16x32_bf16 v[140:143], v[44:47], v[196:199], v[140:143]\n"
    "s_nop 0\n"
    "ds_write_b128 v243, v[96:99] offset:0\n"
    "v_mfma_f32_16x16x32_bf16 v[168:171], a[96:99], a[224:227], 0\n"
    "s_waitcnt lgkmcnt(7)\n"
    "v_lshlrev_b32_sdwa v194, 1, v172 dst_sel:DWORD dst_unused:UNUSED_PAD src0_sel:DWORD src1_sel:WORD_1\n"
    "v_lshlrev_b32_sdwa v195, 1, v174 dst_sel:DWORD dst_unused:UNUSED_PAD src0_sel:DWORD src1_sel:WORD_1\n"
    "v_mfma_f32_16x16x32_bf16 v[168:171], a[144:147], a[228:231], v[168:171]\n"
    "s_nop 0\n"
    "v_cvt_pk_f16_f32 v196, v176, v177\n"
    "v_lshlrev_b32_sdwa v197, 1, v178 dst_sel:DWORD dst_unused:UNUSED_PAD src0_sel:DWORD src1_sel:WORD_1\n"
    "v_mfma_f32_16x16x32_bf16 v[168:171], a[56:59], a[232:235], v[168:171]\n"
    "s_waitcnt lgkmcnt(5)\n"
    "ds_read_u16 v191, v191\n"
    "ds_read_u16_d16_hi v207, v207\n"
    "v_lshlrev_b32_sdwa v198, 1, v180 dst_sel:DWORD dst_unused:UNUSED_PAD src0_sel:DWORD src1_sel:WORD_1\n"
    "v_lshlrev_b32_sdwa v199, 1, v182 dst_sel:DWORD dst_unused:UNUSED_PAD src0_sel:DWORD src1_sel:WORD_1\n"
    "v_or_b32_e64 v187, v187, v203\n"
    "v_lshlrev_b32_sdwa v203, 1, v175 dst_sel:DWORD dst_unused:UNUSED_PAD src0_sel:DWORD src1_sel:WORD_1\n"
    "v_mfma_f32_16x16x32_bf16 v[172:175], a[100:103], a[224:227], 0\n"
    "ds_read_u16 v193, v193\n"
    "ds_read_u16_d16_hi v201, v201\n"
    "v_mfma_f32_16x16x32_bf16 v[172:175], a[148:151], a[228:231], v[172:175]\n"
    "v_or_b32_e64 v188, v188, v204\n"
    "v_cvt_pk_bf16_f32 v113, v114, v115\n"
    "v_mfma_f32_16x16x32_bf16 v[172:175], a[60:63], a[232:235], v[172:175]\n"
    "s_waitcnt lgkmcnt(7)\n"
    "ds_read_u16 v194, v194\n"
    "v_or_b32_e32 v189, v189, v205\n"
    "v_lshlrev_b32_sdwa v205, 1, v179 dst_sel:DWORD dst_unused:UNUSED_PAD src0_sel:DWORD src1_sel:WORD_1\n"
    "v_mfma_f32_16x16x32_bf16 v[176:179], a[96:99], a[236:239], 0\n"
    "v_cvt_pk_bf16_f32 v114, v116, v117\n"
    "v_cvt_pk_bf16_f32 v115, v118, v119\n"
    "v_mfma_f32_16x16x32_bf16 v[176:179], a[144:147], a[240:243], v[176:179]\n"
    "v_cvt_pk_bf16_f32 v128, v128, v129\n"
    "v_cvt_pk_bf16_f32 v129, v130, v131\n"
    "v_mfma_f32_16x16x32_bf16 v[176:179], a[56:59], a[244:247], v[176:179]\n"
    "s_waitcnt lgkmcnt(5)\n"
    "ds_write_b128 v243, v[112:115] offset:2048\n"
    "v_or_b32_e32 v190, v190, v206\n"
    "v_mfma_f32_16x16x32_bf16 v[96:99], a[0:3], v[184:187], 0\n"
    "v_lshlrev_b32_sdwa v206, 1, v181 dst_sel:DWORD dst_unused:UNUSED_PAD src0_sel:DWORD src1_sel:WORD_1\n"
    "v_cvt_pk_bf16_f32 v130, v132, v133\n"
    "v_mfma_f32_16x16x32_bf16 v[100:103], a[4:7], v[184:187], 0\n"
    "v_cvt_pk_bf16_f32 v131, v134, v135\n"
    "v_cvt_pk_bf16_f32 v104, v104, v105\n"
    "v_mfma_f32_16x16x32_bf16 v[112:115], a[8:11], v[184:187], 0\n"
    "ds_read_u16_d16_hi v202, v202\n"
    "ds_write_b128 v243, v[128:131] offset:4096\n"
    "s_waitcnt lgkmcnt(6)\n"
    "v_or_b32_e32 v191, v191, v207\n"
    "v_mfma_f32_16x16x32_bf16 v[116:119], a[12:15], v[184:187], 0\n"
    "v_lshlrev_b32_sdwa v207, 1, v183 dst_sel:DWORD dst_unused:UNUSED_PAD src0_sel:DWORD src1_sel:WORD_1\n"
    "v_cvt_pk_bf16_f32 v105, v106, v107\n"
    "v_mfma_f32_16x16x32_bf16 v[180:183], a[100:103], a[236:239], 0\n"
    "v_cvt_pk_bf16_f32 v106, v108, v109\n"
    "v_cvt_pk_bf16_f32 v107, v110, v111\n"
    "v_mfma_f32_16x16x32_bf16 v[180:183], a[148:151], a[240:243], v[180:183]\n"
    "ds_read_u16 v195, v195\n"
    "ds_write_b128 v243, v[104:107] offset:1024\n"
    "v_mfma_f32_16x16x32_bf16 v[180:183], a[60:63], a[244:247], v[180:183]\n"
    "s_waitcnt lgkmcnt(4)\n"
    "v_or_b32_e32 v193, v193, v201\n"
    "v_cvt_pk_bf16_f32 v120, v120, v121\n"
    "v_mfma_f32_16x16x32_bf16 v[128:131], v[0:3], v[184:187], 0\n"
    "v_cvt_pk_bf16_f32 v121, v122, v123\n"
    "v_cvt_pk_bf16_f32 v122, v124, v125\n"
    "v_mfma_f32_16x16x32_bf16 v[132:135], v[4:7], v[184:187], 0\n"
    "ds_read_u16_d16_hi v203, v203\n"
    "ds_read_u16 v197, v197\n"
    "v_mfma_f32_16x16x32_bf16 v[104:107], a[0:3], v[188:191], 0\n"
    "ds_read_u16_d16_hi v205, v205\n"
    "ds_read_u16 v198, v198\n"
    "v_mfma_f32_16x16x32_bf16 v[108:111], a[4:7], v[188:191], 0\n"
    "s_waitcnt lgkmcnt(6)\n"
    "v_or_b32_e32 v194, v194, v202\n"
    "v_cvt_pk_bf16_f32 v123, v126, v127\n"
    "v_cvt_pk_bf16_f32 v136, v136, v137\n"
    "v_pk_fma_f16 v232, v192, v192, s48\n"
    "v_mfma_f32_16x16x32_bf16 v[124:127], a[12:15], v[188:191], 0\n"
    "v_cvt_pk_bf16_f32 v137, v138, v139\n"
    "v_cvt_pk_bf16_f32 v138, v140, v141\n"
    "v_cvt_pk_bf16_f32 v139, v142, v143\n"
    "v_pk_mul_f16 v232, v192, v232\n"
    "ds_write_b128 v243, v[120:123] offset:3072\n"
    "v_mfma_f32_16x16x32_bf16 v[120:123], a[8:11], v[188:191], 0\n"
    "v_exp_f16_e64 v234, v232\n"
    "v_mfma_f32_16x16x32_bf16 v[140:143], v[4:7], v[188:191], 0\n"
    "ds_write_b128 v243, v[136:139] offset:5120\n"
    "v_mfma_f32_16x16x32_bf16 v[136:139], v[0:3], v[188:191], 0\n"
    "v_exp_f16_sdwa v234, v232 dst_sel:WORD_1 dst_unused:UNUSED_PRESERVE src0_sel:WORD_1\n"
    "s_waitcnt lgkmcnt(5)\n"
    "v_or_b32_e32 v195, v195, v203\n"
    "v_pk_fma_f16 v234, v234, s49, s49\n"
    "v_lshlrev_b32_sdwa v184, 1, v168 dst_sel:DWORD dst_unused:UNUSED_PAD src0_sel:DWORD src1_sel:WORD_1\n"
    "v_lshlrev_b32_sdwa v200, 1, v169 dst_sel:DWORD dst_unused:UNUSED_PAD src0_sel:DWORD src1_sel:WORD_1\n"
    "v_lshlrev_b32_sdwa v185, 1, v170 dst_sel:DWORD dst_unused:UNUSED_PAD src0_sel:DWORD src1_sel:WORD_1\n"
    "v_lshlrev_b32_sdwa v201, 1, v171 dst_sel:DWORD dst_unused:UNUSED_PAD src0_sel:DWORD src1_sel:WORD_1\n"
    "v_mfma_f32_16x16x32_bf16 v[168:171], a[104:107], a[224:227], 0\n"
    "ds_read_u16_d16_hi v206, v206\n"
    "ds_read_u16 v199, v199\n"
    "v_mfma_f32_16x16x32_bf16 v[168:171], a[152:155], a[228:231], v[168:171]\n"
    "ds_read_u16_d16_hi v207, v207\n"
    "ds_read_u16 v184, v184\n"
    "s_waitcnt lgkmcnt(7)\n"
    "v_or_b32_e32 v197, v197, v205\n"
    "v_lshlrev_b32_sdwa v186, 1, v172 dst_sel:DWORD dst_unused:UNUSED_PAD src0_sel:DWORD src1_sel:WORD_1\n"
    "v_lshlrev_b32_sdwa v202, 1, v173 dst_sel:DWORD dst_unused:UNUSED_PAD src0_sel:DWORD src1_sel:WORD_1\n"
    "v_lshlrev_b32_sdwa v187, 1, v174 dst_sel:DWORD dst_unused:UNUSED_PAD src0_sel:DWORD src1_sel:WORD_1\n"
    "v_lshlrev_b32_sdwa v203, 1, v175 dst_sel:DWORD dst_unused:UNUSED_PAD src0_sel:DWORD src1_sel:WORD_1\n"
    "v_rcp_f16_e64 v232, v234\n"
    "v_mfma_f32_16x16x32_bf16 v[168:171], a[64:67], a[232:235], v[168:171]\n"
    "ds_read_u16_d16_hi v200, v200\n"
    "v_rcp_f16_sdwa v232, v234 dst_sel:WORD_1 dst_unused:UNUSED_PRESERVE src0_sel:WORD_1\n"
    "v_mfma_f32_16x16x32_bf16 v[172:175], a[108:111], a[224:227], 0\n"
    "s_waitcnt lgkmcnt(5)\n"
    "ds_read_u16 v185, v185\n"
    "v_lshlrev_b32_sdwa v188, 1, v176 dst_sel:DWORD dst_unused:UNUSED_PAD src0_sel:DWORD src1_sel:WORD_1\n"
    "v_mfma_f32_16x16x32_bf16 v[172:175], a[156:159], a[228:231], v[172:175]\n"
    "s_barrier\n"
    "v_lshlrev_b32_sdwa v204, 1, v177 dst_sel:DWORD dst_unused:UNUSED_PAD src0_sel:DWORD src1_sel:WORD_1\n"
    "v_lshlrev_b32_sdwa v189, 1, v178 dst_sel:DWORD dst_unused:UNUSED_PAD src0_sel:DWORD src1_sel:WORD_1\n"
    "v_lshlrev_b32_sdwa v205, 1, v179 dst_sel:DWORD dst_unused:UNUSED_PAD src0_sel:DWORD src1_sel:WORD_1\n"
    "s_waitcnt lgkmcnt(5)\n"
    "v_or_b32_e32 v198, v198, v206\n"
    "v_lshlrev_b32_sdwa v190, 1, v180 dst_sel:DWORD dst_unused:UNUSED_PAD src0_sel:DWORD src1_sel:WORD_1\n"
    "v_lshlrev_b32_sdwa v206, 1, v181 dst_sel:DWORD dst_unused:UNUSED_PAD src0_sel:DWORD src1_sel:WORD_1\n"
    "v_lshlrev_b32_sdwa v191, 1, v182 dst_sel:DWORD dst_unused:UNUSED_PAD src0_sel:DWORD src1_sel:WORD_1\n"
    "v_fma_mix_f32 v234, v192, v232, 0 op_sel_hi:[1,1,0]\n"
    "v_mfma_f32_16x16x32_bf16 v[172:175], a[68:71], a[232:235], v[172:175]\n"
    "ds_read_u16_d16_hi v201, v201\n"
    "ds_read_u16 v186, v186\n"
    "v_fma_mix_f32 v232, v192, v232, 0 op_sel:[1,1,0] op_sel_hi:[1,1,0]\n"
    "v_mfma_f32_16x16x32_bf16 v[176:179], a[104:107], a[236:239], 0\n"
    "ds_read_u16_d16_hi v202, v202\n"
    "ds_read_u16 v187, v187\n"
    "s_waitcnt lgkmcnt(7)\n"
    "v_or_b32_e32 v199, v199, v207\n"
    "v_lshlrev_b32_sdwa v207, 1, v183 dst_sel:DWORD dst_unused:UNUSED_PAD src0_sel:DWORD src1_sel:WORD_1\n"
    "v_cvt_pk_bf16_f32 v192, v234, v232\n"
    "v_mfma_f32_16x16x32_bf16 v[176:179], a[152:155], a[240:243], v[176:179]\n"
    "ds_read_u16_d16_hi v203, v203\n"
    "v_pk_fma_f16 v232, v196, v196, s48\n"
    "v_mfma_f32_16x16x32_bf16 v[176:179], a[64:67], a[244:247], v[176:179]\n"
    "v_pk_mul_f16 v232, v196, v232\n"
    "v_exp_f16_e32 v234, v232\n"
    "v_mfma_f32_16x16x32_bf16 v[180:183], a[108:111], a[236:239], 0\n"
    "s_waitcnt lgkmcnt(6)\n"
    "ds_read_u16 v188, v188\n"
    "v_exp_f16_sdwa v234, v232 dst_sel:WORD_1 dst_unused:UNUSED_PRESERVE src0_sel:WORD_1\n"
    "v_mfma_f32_16x16x32_bf16 v[180:183], a[156:159], a[240:243], v[180:183]\n"
    "ds_read_u16_d16_hi v204, v204\n"
    "v_pk_fma_f16 v234, v234, s49, s49\n"
    "v_mfma_f32_16x16x32_bf16 v[180:183], a[68:71], a[244:247], v[180:183]\n"
    "v_rcp_f16_e32 v232, v234\n"
    "v_mfma_f32_16x16x32_bf16 v[96:99], a[16:19], v[192:195], v[96:99]\n"
    "ds_read_u16 v189, v189\n"
    "v_rcp_f16_sdwa v232, v234 dst_sel:WORD_1 dst_unused:UNUSED_PRESERVE src0_sel:WORD_1\n"
    "v_mfma_f32_16x16x32_bf16 v[100:103], a[20:23], v[192:195], v[100:103]\n"
    "s_waitcnt lgkmcnt(7)\n"
    "ds_read_u16_d16_hi v205, v205\n"
    "v_fma_mix_f32 v234, v196, v232, 0 op_sel_hi:[1,1,0]\n"
    "v_mfma_f32_16x16x32_bf16 v[112:115], a[24:27], v[192:195], v[112:115]\n"
    "v_fma_mix_f32 v232, v196, v232, 0 op_sel:[1,1,0] op_sel_hi:[1,1,0]\n"
    "v_or_b32_e64 v184, v184, v200\n"
    "v_mfma_f32_16x16x32_bf16 v[116:119], a[28:31], v[192:195], v[116:119]\n"
    "ds_read_u16 v190, v190\n"
    "v_cvt_pk_bf16_f32 v196, v234, v232\n"
    "v_mfma_f32_16x16x32_bf16 v[128:131], v[8:11], v[192:195], v[128:131]\n"
    "v_or_b32_e32 v185, v185, v201\n"
    "v_lshlrev_b32_sdwa v201, 1, v171 dst_sel:DWORD dst_unused:UNUSED_PAD src0_sel:DWORD src1_sel:WORD_1\n"
    "v_mfma_f32_16x16x32_bf16 v[132:135], v[12:15], v[192:195], v[132:135]\n"
    "s_waitcnt lgkmcnt(7)\n"
    "ds_read_u16_d16_hi v206, v206\n"
    "v_cvt_pk_f16_f32 v192, v168, v169\n"
    "v_mfma_f32_16x16x32_bf16 v[104:107], a[16:19], v[196:199], v[104:107]\n"
    "v_lshlrev_b32_sdwa v193, 1, v170 dst_sel:DWORD dst_unused:UNUSED_PAD src0_sel:DWORD src1_sel:WORD_1\n"
    "v_lshlrev_b32_sdwa v194, 1, v172 dst_sel:DWORD dst_unused:UNUSED_PAD src0_sel:DWORD src1_sel:WORD_1\n"
    "v_mfma_f32_16x16x32_bf16 v[108:111], a[20:23], v[196:199], v[108:111]\n"
    "s_waitcnt vmcnt(7)\n"
    "ds_read_u16 v191, v191\n"
    "v_or_b32_e64 v186, v186, v202\n"
    "v_mfma_f32_16x16x32_bf16 v[120:123], a[24:27], v[196:199], v[120:123]\n"
    "s_waitcnt lgkmcnt(7)\n"
    "ds_read_u16_d16_hi v207, v207\n"
    "v_lshlrev_b32_sdwa v202, 1, v173 dst_sel:DWORD dst_unused:UNUSED_PAD src0_sel:DWORD src1_sel:WORD_1\n"
    "v_mfma_f32_16x16x32_bf16 v[124:127], a[28:31], v[196:199], v[124:127]\n"
    "v_lshlrev_b32_sdwa v195, 1, v174 dst_sel:DWORD dst_unused:UNUSED_PAD src0_sel:DWORD src1_sel:WORD_1\n"
    "v_or_b32_e64 v187, v187, v203\n"
    "v_mfma_f32_16x16x32_bf16 v[136:139], v[8:11], v[196:199], v[136:139]\n"
    "ds_read_u16 v193, v193\n"
    "v_lshlrev_b32_sdwa v203, 1, v175 dst_sel:DWORD dst_unused:UNUSED_PAD src0_sel:DWORD src1_sel:WORD_1\n"
    "v_mfma_f32_16x16x32_bf16 v[140:143], v[12:15], v[196:199], v[140:143]\n"
    "s_waitcnt vmcnt(6)\n"
    "v_cvt_pk_f16_f32 v196, v176, v177\n"
    "v_lshlrev_b32_sdwa v197, 1, v178 dst_sel:DWORD dst_unused:UNUSED_PAD src0_sel:DWORD src1_sel:WORD_1\n"
    "v_smfmac_f32_32x32x32_bf16 v[112:127], a[248:251], v[152:159], v241\n"
    "s_waitcnt lgkmcnt(7)\n"
    "ds_read_u16_d16_hi v201, v201\n"
    "v_lshlrev_b32_sdwa v198, 1, v180 dst_sel:DWORD dst_unused:UNUSED_PAD src0_sel:DWORD src1_sel:WORD_1\n"
    "v_lshlrev_b32_sdwa v199, 1, v182 dst_sel:DWORD dst_unused:UNUSED_PAD src0_sel:DWORD src1_sel:WORD_1\n"
    "v_or_b32_e64 v188, v188, v204\n"
    "v_pk_fma_f16 v232, v192, v192, s48\n"
    "v_smfmac_f32_32x32x32_bf16 v[96:111], a[248:251], v[144:151], v241\n"
    "ds_read_u16 v194, v194\n"
    "v_pk_mul_f16 v232, v192, v232\n"
    "v_exp_f16_e32 v234, v232\n"
    "ds_read_u16_d16_hi v202, v202\n"
    "v_smfmac_f32_32x32x32_bf16 v[128:143], a[248:251], v[208:215], v241\n"
    "s_waitcnt lgkmcnt(7)\n"
    "ds_read_b128 v[152:155], v242 offset:0\n"
    "v_or_b32_e32 v189, v189, v205\n"
    "v_lshlrev_b32_sdwa v205, 1, v179 dst_sel:DWORD dst_unused:UNUSED_PAD src0_sel:DWORD src1_sel:WORD_1\n"
    "v_exp_f16_sdwa v234, v232 dst_sel:WORD_1 dst_unused:UNUSED_PRESERVE src0_sel:WORD_1\n"
    "v_mfma_f32_16x16x32_bf16 v[168:171], a[112:115], a[224:227], 0\n"
    "s_waitcnt lgkmcnt(7)\n"
    "ds_read_b128 v[156:159], v242 offset:1024\n"
    "ds_read_u16 v195, v195\n"
    "v_mfma_f32_16x16x32_bf16 v[168:171], a[160:163], a[228:231], v[168:171]\n"
    "v_or_b32_e64 v190, v190, v206\n"
    "v_lshlrev_b32_sdwa v206, 1, v181 dst_sel:DWORD dst_unused:UNUSED_PAD src0_sel:DWORD src1_sel:WORD_1\n"
    "v_mfma_f32_16x16x32_bf16 v[168:171], a[72:75], a[232:235], v[168:171]\n"
    "ds_read_u16_d16_hi v203, v203\n"
    "v_pk_fma_f16 v234, v234, s49, s49\n"
    "v_mfma_f32_16x16x32_bf16 v[172:175], a[116:119], a[224:227], 0\n"
    "v_rcp_f16_e32 v232, v234\n"
    "v_mfma_f32_16x16x32_bf16 v[172:175], a[164:167], a[228:231], v[172:175]\n"
    "s_waitcnt lgkmcnt(8)\n"
    "ds_read_u16 v197, v197\n"
    "v_or_b32_e64 v191, v191, v207\n"
    "v_mfma_f32_16x16x32_bf16 v[172:175], v[52:55], a[232:235], v[172:175]\n"
    "v_lshlrev_b32_sdwa v207, 1, v183 dst_sel:DWORD dst_unused:UNUSED_PAD src0_sel:DWORD src1_sel:WORD_1\n"
    "v_lshlrev_b32_sdwa v200, 1, v169 dst_sel:DWORD dst_unused:UNUSED_PAD src0_sel:DWORD src1_sel:WORD_1\n"
    "v_mfma_f32_16x16x32_bf16 v[176:179], a[112:115], a[236:239], 0\n"
    "ds_read_u16_d16_hi v205, v205\n"
    "v_rcp_f16_sdwa v232, v234 dst_sel:WORD_1 dst_unused:UNUSED_PRESERVE src0_sel:WORD_1\n"
    "v_mfma_f32_16x16x32_bf16 v[176:179], a[160:163], a[240:243], v[176:179]\n"
    "s_waitcnt lgkmcnt(8)\n"
    "ds_read_u16 v198, v198\n"
    "v_or_b32_e32 v193, v193, v201\n"
    "v_mfma_f32_16x16x32_bf16 v[176:179], a[72:75], a[244:247], v[176:179]\n"
    "v_lshlrev_b32_sdwa v201, 1, v171 dst_sel:DWORD dst_unused:UNUSED_PAD src0_sel:DWORD src1_sel:WORD_1\n"
    "v_fma_mix_f32 v234, v192, v232, 0 op_sel_hi:[1,1,0]\n"
    "v_mfma_f32_16x16x32_bf16 v[180:183], a[116:119], a[236:239], 0\n"
    "ds_read_u16_d16_hi v206, v206\n"
    "v_fma_mix_f32 v232, v192, v232, 0 op_sel:[1,1,0] op_sel_hi:[1,1,0]\n"
    "v_mfma_f32_16x16x32_bf16 v[180:183], a[164:167], a[240:243], v[180:183]\n"
    "v_cvt_pk_bf16_f32 v192, v234, v232\n"
    "v_pk_fma_f16 v232, v196, v196, s48\n"
    "v_mfma_f32_16x16x32_bf16 v[180:183], v[52:55], a[244:247], v[180:183]\n"
    "s_waitcnt lgkmcnt(6)\n"
    "ds_read_u16 v199, v199\n"
    "v_or_b32_e32 v194, v194, v202\n"
    "v_smfmac_f32_32x32x32_bf16 v[96:111], a[248:251], v[152:159], v241\n"
    "ds_read_u16_d16_hi v207, v207\n"
    "v_lshlrev_b32_sdwa v202, 1, v173 dst_sel:DWORD dst_unused:UNUSED_PAD src0_sel:DWORD src1_sel:WORD_1\n"
    "v_lshlrev_b32_sdwa v204, 1, v177 dst_sel:DWORD dst_unused:UNUSED_PAD src0_sel:DWORD src1_sel:WORD_1\n"
    "v_pk_mul_f16 v232, v196, v232\n"
    "v_exp_f16_e64 v234, v232\n"
    "v_mfma_f32_16x16x32_bf16 v[112:115], a[40:43], v[184:187], v[112:115]\n"
    "ds_read_b128 v[152:155], v242 offset:2048\n"
    "v_exp_f16_sdwa v234, v232 dst_sel:WORD_1 dst_unused:UNUSED_PRESERVE src0_sel:WORD_1\n"
    "v_mfma_f32_16x16x32_bf16 v[116:119], a[44:47], v[184:187], v[116:119]\n"
    "ds_read_b128 v[156:159], v242 offset:3072\n"
    "v_pk_fma_f16 v234, v234, s49, s49\n"
    "v_mfma_f32_16x16x32_bf16 v[96:99], a[32:35], v[184:187], v[96:99]\n"
    "s_waitcnt lgkmcnt(8)\n"
    "ds_read_u16_d16_hi v200, v200\n"
    "v_or_b32_e32 v195, v195, v203\n"
    "v_mfma_f32_16x16x32_bf16 v[100:103], a[36:39], v[184:187], v[100:103]\n"
    "v_lshlrev_b32_sdwa v203, 1, v175 dst_sel:DWORD dst_unused:UNUSED_PAD src0_sel:DWORD src1_sel:WORD_1\n"
    "v_rcp_f16_e64 v232, v234\n"
    "v_mfma_f32_16x16x32_bf16 v[128:131], v[16:19], v[184:187], v[128:131]\n"
    "ds_read_u16_d16_hi v201, v201\n"
    "v_rcp_f16_sdwa v232, v234 dst_sel:WORD_1 dst_unused:UNUSED_PRESERVE src0_sel:WORD_1\n"
    "v_mfma_f32_16x16x32_bf16 v[132:135], v[20:23], v[184:187], v[132:135]\n"
    "s_waitcnt lgkmcnt(8)\n"
    "ds_read_u16_d16_hi v202, v202\n"
    "v_or_b32_e32 v197, v197, v205\n"
    "v_mfma_f32_16x16x32_bf16 v[104:107], a[32:35], v[188:191], v[104:107]\n"
    "v_lshlrev_b32_sdwa v184, 1, v168 dst_sel:DWORD dst_unused:UNUSED_PAD src0_sel:DWORD src1_sel:WORD_1\n"
    "v_lshlrev_b32_sdwa v185, 1, v170 dst_sel:DWORD dst_unused:UNUSED_PAD src0_sel:DWORD src1_sel:WORD_1\n"
    "v_mfma_f32_16x16x32_bf16 v[108:111], a[36:39], v[188:191], v[108:111]\n"
    "ds_read_u16 v184, v184\n"
    "v_lshlrev_b32_sdwa v186, 1, v172 dst_sel:DWORD dst_unused:UNUSED_PAD src0_sel:DWORD src1_sel:WORD_1\n"
    "v_mfma_f32_16x16x32_bf16 v[120:123], a[40:43], v[188:191], v[120:123]\n"
    "s_waitcnt lgkmcnt(8)\n"
    "ds_read_u16 v185, v185\n"
    "v_lshlrev_b32_sdwa v187, 1, v174 dst_sel:DWORD dst_unused:UNUSED_PAD src0_sel:DWORD src1_sel:WORD_1\n"
    "v_mfma_f32_16x16x32_bf16 v[124:127], a[44:47], v[188:191], v[124:127]\n"
    "v_or_b32_e32 v198, v198, v206\n"
    "v_lshlrev_b32_sdwa v205, 1, v179 dst_sel:DWORD dst_unused:UNUSED_PAD src0_sel:DWORD src1_sel:WORD_1\n"
    "v_mfma_f32_16x16x32_bf16 v[136:139], v[16:19], v[188:191], v[136:139]\n"
    "ds_read_u16 v186, v186\n"
    "v_lshlrev_b32_sdwa v206, 1, v181 dst_sel:DWORD dst_unused:UNUSED_PAD src0_sel:DWORD src1_sel:WORD_1\n"
    "v_mfma_f32_16x16x32_bf16 v[140:143], v[20:23], v[188:191], v[140:143]\n"
    "s_nop 0\n"
    "v_lshlrev_b32_sdwa v188, 1, v176 dst_sel:DWORD dst_unused:UNUSED_PAD src0_sel:DWORD src1_sel:WORD_1\n"
    "v_lshlrev_b32_sdwa v189, 1, v178 dst_sel:DWORD dst_unused:UNUSED_PAD src0_sel:DWORD src1_sel:WORD_1\n"
    "v_mfma_f32_16x16x32_bf16 v[168:171], a[120:123], a[224:227], 0\n"
    "s_waitcnt lgkmcnt(6)\n"
    "ds_read_u16 v187, v187\n"
    "v_lshlrev_b32_sdwa v190, 1, v180 dst_sel:DWORD dst_unused:UNUSED_PAD src0_sel:DWORD src1_sel:WORD_1\n"
    "v_smfmac_f32_32x32x32_bf16 v[112:127], a[248:251], v[152:159], v241\n"
    "ds_read_u16_d16_hi v203, v203\n"
    "v_lshlrev_b32_sdwa v191, 1, v182 dst_sel:DWORD dst_unused:UNUSED_PAD src0_sel:DWORD src1_sel:WORD_1\n"
    "v_or_b32_e64 v199, v199, v207\n"
    "v_lshlrev_b32_sdwa v207, 1, v183 dst_sel:DWORD dst_unused:UNUSED_PAD src0_sel:DWORD src1_sel:WORD_1\n"
    "v_fma_mix_f32 v234, v196, v232, 0 op_sel_hi:[1,1,0]\n"
    "v_mfma_f32_16x16x32_bf16 v[168:171], a[168:171], a[228:231], v[168:171]\n"
    "ds_read_u16 v188, v188\n"
    "v_fma_mix_f32 v232, v196, v232, 0 op_sel:[1,1,0] op_sel_hi:[1,1,0]\n"
    "v_mfma_f32_16x16x32_bf16 v[168:171], a[76:79], a[232:235], v[168:171]\n"
    "v_cvt_pk_bf16_f32 v196, v234, v232\n"
    "v_mfma_f32_16x16x32_bf16 v[172:175], a[124:127], a[224:227], 0\n"
    "ds_read_u16_d16_hi v204, v204\n"
    "v_mfma_f32_16x16x32_bf16 v[172:175], a[172:175], a[228:231], v[172:175]\n"
    "v_mfma_f32_16x16x32_bf16 v[172:175], v[60:63], a[232:235], v[172:175]\n"
    "ds_read_b128 v[152:155], v242 offset:4096\n"
    "v_mfma_f32_16x16x32_bf16 v[176:179], a[120:123], a[236:239], 0\n"
    "ds_read_b128 v[156:159], v242 offset:5120\n"
    "v_mfma_f32_16x16x32_bf16 v[176:179], a[168:171], a[240:243], v[176:179]\n"
    "s_waitcnt lgkmcnt(8)\n"
    "ds_read_u16 v189, v189\n"
    "v_or_b32_e32 v184, v184, v200\n"
    "v_mfma_f32_16x16x32_bf16 v[176:179], a[76:79], a[244:247], v[176:179]\n"
    "s_waitcnt lgkmcnt(8)\n"
    "v_or_b32_e32 v185, v185, v201\n"
    "v_lshlrev_b32_sdwa v201, 1, v171 dst_sel:DWORD dst_unused:UNUSED_PAD src0_sel:DWORD src1_sel:WORD_1\n"
    "v_mfma_f32_16x16x32_bf16 v[180:183], a[124:127], a[236:239], 0\n"
    "ds_read_u16_d16_hi v205, v205\n"
    "v_mfma_f32_16x16x32_bf16 v[180:183], a[172:175], a[240:243], v[180:183]\n"
    "s_waitcnt lgkmcnt(8)\n"
    "ds_read_b128 a[224:227], v242 offset:24640\n"
    "v_or_b32_e32 v186, v186, v202\n"
    "v_mfma_f32_16x16x32_bf16 v[180:183], v[60:63], a[244:247], v[180:183]\n"
    "ds_read_u16 v190, v190\n"
    "v_lshlrev_b32_sdwa v202, 1, v173 dst_sel:DWORD dst_unused:UNUSED_PAD src0_sel:DWORD src1_sel:WORD_1\n"
    "v_mfma_f32_16x16x32_bf16 v[96:99], a[176:179], v[192:195], v[96:99]\n"
    "v_mfma_f32_16x16x32_bf16 v[100:103], a[180:183], v[192:195], v[100:103]\n"
    "ds_read_b128 a[228:231], v242 offset:25664\n"
    "v_mfma_f32_16x16x32_bf16 v[112:115], a[184:187], v[192:195], v[112:115]\n"
    "s_waitcnt lgkmcnt(9)\n"
    "ds_read_u16_d16_hi v206, v206\n"
    "v_or_b32_e32 v187, v187, v203\n"
    "v_mfma_f32_16x16x32_bf16 v[116:119], a[188:191], v[192:195], v[116:119]\n"
    "v_lshlrev_b32_sdwa v203, 1, v175 dst_sel:DWORD dst_unused:UNUSED_PAD src0_sel:DWORD src1_sel:WORD_1\n"
    "v_mfma_f32_16x16x32_bf16 v[128:131], v[24:27], v[192:195], v[128:131]\n"
    "s_nop 0\n"
    "ds_read_b128 a[232:235], v242 offset:26688\n"
    "v_mfma_f32_16x16x32_bf16 v[132:135], v[28:31], v[192:195], v[132:135]\n"
    "s_waitcnt lgkmcnt(8)\n"
    "ds_read_u16 v191, v191\n"
    "v_cvt_pk_f16_f32 v192, v168, v169\n"
    "v_mfma_f32_16x16x32_bf16 v[104:107], a[176:179], v[196:199], v[104:107]\n"
    "v_lshlrev_b32_sdwa v193, 1, v170 dst_sel:DWORD dst_unused:UNUSED_PAD src0_sel:DWORD src1_sel:WORD_1\n"
    "v_lshlrev_b32_sdwa v194, 1, v172 dst_sel:DWORD dst_unused:UNUSED_PAD src0_sel:DWORD src1_sel:WORD_1\n"
    "v_mfma_f32_16x16x32_bf16 v[108:111], a[180:183], v[196:199], v[108:111]\n"
    "ds_read_b128 a[236:239], v242 offset:27712\n"
    "ds_read_u16_d16_hi v207, v207\n"
    "v_mfma_f32_16x16x32_bf16 v[120:123], a[184:187], v[196:199], v[120:123]\n"
    "v_lshlrev_b32_sdwa v195, 1, v174 dst_sel:DWORD dst_unused:UNUSED_PAD src0_sel:DWORD src1_sel:WORD_1\n"
    "v_or_b32_e64 v188, v188, v204\n"
    "v_mfma_f32_16x16x32_bf16 v[124:127], a[188:191], v[196:199], v[124:127]\n"
    "ds_read_b128 a[240:243], v242 offset:28736\n"
    "v_pk_fma_f16 v232, v192, v192, s48\n"
    "v_mfma_f32_16x16x32_bf16 v[136:139], v[24:27], v[196:199], v[136:139]\n"
    "s_waitcnt lgkmcnt(8)\n"
    "ds_read_b128 a[244:247], v242 offset:29760\n"
    "v_or_b32_e32 v189, v189, v205\n"
    "v_mfma_f32_16x16x32_bf16 v[140:143], v[28:31], v[196:199], v[140:143]\n"
    "ds_read_u16 v193, v193\n"
    "v_cvt_pk_f16_f32 v196, v176, v177\n"
    "v_mfma_f32_16x16x32_bf16 v[168:171], a[80:83], a[224:227], 0\n"
    "s_nop 0\n"
    "v_lshlrev_b32_sdwa v197, 1, v178 dst_sel:DWORD dst_unused:UNUSED_PAD src0_sel:DWORD src1_sel:WORD_1\n"
    "v_lshlrev_b32_sdwa v205, 1, v179 dst_sel:DWORD dst_unused:UNUSED_PAD src0_sel:DWORD src1_sel:WORD_1\n"
    "v_mfma_f32_16x16x32_bf16 v[172:175], a[84:87], a[224:227], 0\n"
    "s_waitcnt lgkmcnt(8)\n"
    "ds_read_u16_d16_hi v201, v201\n"
    "v_lshlrev_b32_sdwa v198, 1, v180 dst_sel:DWORD dst_unused:UNUSED_PAD src0_sel:DWORD src1_sel:WORD_1\n"
    "v_smfmac_f32_32x32x32_bf16 v[128:143], a[248:251], v[152:159], v241\n"
    "v_lshlrev_b32_sdwa v199, 1, v182 dst_sel:DWORD dst_unused:UNUSED_PAD src0_sel:DWORD src1_sel:WORD_1\n"
    "v_pk_mul_f16 v232, v192, v232\n"
    "v_exp_f16_e32 v234, v232\n"
    "ds_read_u16 v194, v194\n"
    "v_mfma_f32_16x16x32_bf16 v[168:171], a[128:131], a[228:231], v[168:171]\n"
    "s_waitcnt lgkmcnt(8)\n"
    "ds_read_u16_d16_hi v202, v202\n"
    "v_or_b32_e32 v190, v190, v206\n"
    "v_mfma_f32_16x16x32_bf16 v[168:171], a[48:51], a[232:235], v[168:171]\n"
    "v_lshlrev_b32_sdwa v206, 1, v181 dst_sel:DWORD dst_unused:UNUSED_PAD src0_sel:DWORD src1_sel:WORD_1\n"
    "v_exp_f16_sdwa v234, v232 dst_sel:WORD_1 dst_unused:UNUSED_PRESERVE src0_sel:WORD_1\n"
    "v_mfma_f32_16x16x32_bf16 v[172:175], a[132:135], a[228:231], v[172:175]\n"
    "s_waitcnt lgkmcnt(7)\n"
    "ds_read_u16 v195, v195\n"
    "v_pk_fma_f16 v234, v234, s49, s49\n"
    "v_mfma_f32_16x16x32_bf16 v[172:175], v[48:51], a[232:235], v[172:175]\n"
    "ds_read_u16_d16_hi v203, v203\n"
    "v_lshlrev_b32_sdwa v200, 1, v169 dst_sel:DWORD dst_unused:UNUSED_PAD src0_sel:DWORD src1_sel:WORD_1\n"
    "v_mfma_f32_16x16x32_bf16 v[176:179], a[80:83], a[236:239], 0\n"
    "v_rcp_f16_e32 v232, v234\n"
    "v_mfma_f32_16x16x32_bf16 v[96:99], a[192:195], v[184:187], v[96:99]\n"
    "s_waitcnt lgkmcnt(7)\n"
    "ds_read_u16 v197, v197\n"
    "v_or_b32_e32 v191, v191, v207\n"
    "v_mfma_f32_16x16x32_bf16 v[176:179], a[128:131], a[240:243], v[176:179]\n"
    "s_waitcnt lgkmcnt(7)\n"
    "v_lshlrev_b32_sdwa v207, 1, v183 dst_sel:DWORD dst_unused:UNUSED_PAD src0_sel:DWORD src1_sel:WORD_1\n"
    "v_rcp_f16_sdwa v232, v234 dst_sel:WORD_1 dst_unused:UNUSED_PRESERVE src0_sel:WORD_1\n"
    "v_mfma_f32_16x16x32_bf16 v[176:179], a[48:51], a[244:247], v[176:179]\n"
    "ds_read_u16_d16_hi v205, v205\n"
    "v_fma_mix_f32 v234, v192, v232, 0 op_sel_hi:[1,1,0]\n"
    "v_mfma_f32_16x16x32_bf16 v[180:183], a[84:87], a[236:239], 0\n"
    "ds_read_u16 v198, v198\n"
    "v_fma_mix_f32 v232, v192, v232, 0 op_sel:[1,1,0] op_sel_hi:[1,1,0]\n"
    "v_mfma_f32_16x16x32_bf16 v[180:183], a[132:135], a[240:243], v[180:183]\n"
    "v_cvt_pk_bf16_f32 v192, v234, v232\n"
    "v_pk_fma_f16 v232, v196, v196, s48\n"
    "v_mfma_f32_16x16x32_bf16 v[180:183], v[48:51], a[244:247], v[180:183]\n"
    "s_waitcnt lgkmcnt(7)\n"
    "ds_read_u16_d16_hi v206, v206\n"
    "v_or_b32_e32 v193, v193, v201\n"
    "v_mfma_f32_16x16x32_bf16 v[100:103], a[196:199], v[184:187], v[100:103]\n"
    "v_lshlrev_b32_sdwa v201, 1, v171 dst_sel:DWORD dst_unused:UNUSED_PAD src0_sel:DWORD src1_sel:WORD_1\n"
    "v_lshlrev_b32_sdwa v204, 1, v177 dst_sel:DWORD dst_unused:UNUSED_PAD src0_sel:DWORD src1_sel:WORD_1\n"
    "v_mfma_f32_16x16x32_bf16 v[112:115], a[200:203], v[184:187], v[112:115]\n"
    "ds_read_u16 v199, v199\n"
    "v_pk_mul_f16 v232, v196, v232\n"
    "v_mfma_f32_16x16x32_bf16 v[116:119], a[204:207], v[184:187], v[116:119]\n"
    "v_exp_f16_e32 v234, v232\n"
    "v_mfma_f32_16x16x32_bf16 v[128:131], v[32:35], v[184:187], v[128:131]\n"
    "s_waitcnt lgkmcnt(7)\n"
    "ds_read_u16_d16_hi v207, v207\n"
    "v_or_b32_e64 v194, v194, v202\n"
    "v_mfma_f32_16x16x32_bf16 v[132:135], v[36:39], v[184:187], v[132:135]\n"
    "ds_read_u16_d16_hi v200, v200\n"
    "v_lshlrev_b32_sdwa v184, 1, v168 dst_sel:DWORD dst_unused:UNUSED_PAD src0_sel:DWORD src1_sel:WORD_1\n"
    "v_mfma_f32_16x16x32_bf16 v[104:107], a[192:195], v[188:191], v[104:107]\n"
    "v_lshlrev_b32_sdwa v185, 1, v170 dst_sel:DWORD dst_unused:UNUSED_PAD src0_sel:DWORD src1_sel:WORD_1\n"
    "v_lshlrev_b32_sdwa v186, 1, v172 dst_sel:DWORD dst_unused:UNUSED_PAD src0_sel:DWORD src1_sel:WORD_1\n"
    "v_mfma_f32_16x16x32_bf16 v[108:111], a[196:199], v[188:191], v[108:111]\n"
    "s_waitcnt lgkmcnt(7)\n"
    "ds_read_u16 v184, v184\n"
    "v_or_b32_e32 v195, v195, v203\n"
    "v_mfma_f32_16x16x32_bf16 v[120:123], a[200:203], v[188:191], v[120:123]\n"
    "v_lshlrev_b32_sdwa v202, 1, v173 dst_sel:DWORD dst_unused:UNUSED_PAD src0_sel:DWORD src1_sel:WORD_1\n"
    "v_lshlrev_b32_sdwa v187, 1, v174 dst_sel:DWORD dst_unused:UNUSED_PAD src0_sel:DWORD src1_sel:WORD_1\n"
    "v_mfma_f32_16x16x32_bf16 v[124:127], a[204:207], v[188:191], v[124:127]\n"
    "ds_read_u16 v185, v185\n"
    "v_lshlrev_b32_sdwa v203, 1, v175 dst_sel:DWORD dst_unused:UNUSED_PAD src0_sel:DWORD src1_sel:WORD_1\n"
    "v_mfma_f32_16x16x32_bf16 v[136:139], v[32:35], v[188:191], v[136:139]\n"
    "s_nop 0\n"
    "v_exp_f16_sdwa v234, v232 dst_sel:WORD_1 dst_unused:UNUSED_PRESERVE src0_sel:WORD_1\n"
    "v_mfma_f32_16x16x32_bf16 v[140:143], v[36:39], v[188:191], v[140:143]\n"
    "s_waitcnt lgkmcnt(7)\n"
    "ds_read_u16_d16_hi v201, v201\n"
    "v_or_b32_e64 v197, v197, v205\n"
    "v_mfma_f32_16x16x32_bf16 v[168:171], a[88:91], a[224:227], 0\n"
    "ds_read_u16 v186, v186\n"
    "v_lshlrev_b32_sdwa v188, 1, v176 dst_sel:DWORD dst_unused:UNUSED_PAD src0_sel:DWORD src1_sel:WORD_1\n"
    "v_mfma_f32_16x16x32_bf16 v[168:171], a[136:139], a[228:231], v[168:171]\n"
    "v_lshlrev_b32_sdwa v189, 1, v178 dst_sel:DWORD dst_unused:UNUSED_PAD src0_sel:DWORD src1_sel:WORD_1\n"
    "v_lshlrev_b32_sdwa v205, 1, v179 dst_sel:DWORD dst_unused:UNUSED_PAD src0_sel:DWORD src1_sel:WORD_1\n"
    "v_mfma_f32_16x16x32_bf16 v[168:171], a[52:55], a[232:235], v[168:171]\n"
    "s_waitcnt lgkmcnt(7)\n"
    "ds_read_u16_d16_hi v202, v202\n"
    "v_or_b32_e32 v198, v198, v206\n"
    "v_mfma_f32_16x16x32_bf16 v[172:175], a[92:95], a[224:227], 0\n"
    "v_lshlrev_b32_sdwa v190, 1, v180 dst_sel:DWORD dst_unused:UNUSED_PAD src0_sel:DWORD src1_sel:WORD_1\n"
    "v_lshlrev_b32_sdwa v206, 1, v181 dst_sel:DWORD dst_unused:UNUSED_PAD src0_sel:DWORD src1_sel:WORD_1\n"
    "v_mfma_f32_16x16x32_bf16 v[172:175], a[140:143], a[228:231], v[172:175]\n"
    "ds_read_u16 v187, v187\n"
    "v_lshlrev_b32_sdwa v191, 1, v182 dst_sel:DWORD dst_unused:UNUSED_PAD src0_sel:DWORD src1_sel:WORD_1\n"
    "v_mfma_f32_16x16x32_bf16 v[172:175], v[56:59], a[232:235], v[172:175]\n"
    "v_pk_fma_f16 v234, v234, s49, s49\n"
    "v_rcp_f16_e32 v232, v234\n"
    "v_mfma_f32_16x16x32_bf16 v[176:179], a[88:91], a[236:239], 0\n"
    "s_waitcnt lgkmcnt(7)\n"
    "ds_read_u16_d16_hi v203, v203\n"
    "v_or_b32_e64 v199, v199, v207\n"
    "v_mfma_f32_16x16x32_bf16 v[176:179], a[136:139], a[240:243], v[176:179]\n"
    "ds_read_u16 v188, v188\n"
    "v_lshlrev_b32_sdwa v207, 1, v183 dst_sel:DWORD dst_unused:UNUSED_PAD src0_sel:DWORD src1_sel:WORD_1\n"
    "v_mfma_f32_16x16x32_bf16 v[176:179], a[52:55], a[244:247], v[176:179]\n"
    "s_nop 0\n"
    "v_rcp_f16_sdwa v232, v234 dst_sel:WORD_1 dst_unused:UNUSED_PRESERVE src0_sel:WORD_1\n"
    "v_mfma_f32_16x16x32_bf16 v[180:183], a[92:95], a[236:239], 0\n"
    "s_waitcnt lgkmcnt(7)\n"
    "ds_read_u16_d16_hi v204, v204\n"
    "v_fma_mix_f32 v234, v196, v232, 0 op_sel_hi:[1,1,0]\n"
    "v_mfma_f32_16x16x32_bf16 v[180:183], a[140:143], a[240:243], v[180:183]\n"
    "v_fma_mix_f32 v232, v196, v232, 0 op_sel:[1,1,0] op_sel_hi:[1,1,0]\n"
    "v_or_b32_e64 v184, v184, v200\n"
    "v_mfma_f32_16x16x32_bf16 v[180:183], v[56:59], a[244:247], v[180:183]\n"
    "ds_read_u16 v189, v189\n"
    "v_cvt_pk_bf16_f32 v196, v234, v232\n"
    "v_mfma_f32_16x16x32_bf16 v[96:99], a[208:211], v[192:195], v[96:99]\n"
    "s_waitcnt lgkmcnt(7)\n"
    "ds_read_u16_d16_hi v205, v205\n"
    "v_or_b32_e32 v185, v185, v201\n"
    "v_mfma_f32_16x16x32_bf16 v[100:103], a[212:215], v[192:195], v[100:103]\n"
    "v_lshlrev_b32_sdwa v201, 1, v171 dst_sel:DWORD dst_unused:UNUSED_PAD src0_sel:DWORD src1_sel:WORD_1\n"
    "v_mfma_f32_16x16x32_bf16 v[112:115], a[216:219], v[192:195], v[112:115]\n"
    "ds_read_u16 v190, v190\n"
    "v_mfma_f32_16x16x32_bf16 v[116:119], a[220:223], v[192:195], v[116:119]\n"
    "v_cvt_pk_bf16_f32 v96, v96, v97\n"
    "v_cvt_pk_bf16_f32 v97, v98, v99\n"
    "v_mfma_f32_16x16x32_bf16 v[128:131], v[40:43], v[192:195], v[128:131]\n"
    "s_waitcnt lgkmcnt(7)\n"
    "ds_read_u16_d16_hi v206, v206\n"
    "v_or_b32_e32 v186, v186, v202\n"
    "v_mfma_f32_16x16x32_bf16 v[132:135], v[44:47], v[192:195], v[132:135]\n"
    "v_cvt_pk_f16_f32 v192, v168, v169\n"
    "v_lshlrev_b32_sdwa v193, 1, v170 dst_sel:DWORD dst_unused:UNUSED_PAD src0_sel:DWORD src1_sel:WORD_1\n"
    "v_mfma_f32_16x16x32_bf16 v[104:107], a[208:211], v[196:199], v[104:107]\n"
    "s_nop 0\n"
    "ds_read_u16 v191, v191\n"
    "v_lshlrev_b32_sdwa v194, 1, v172 dst_sel:DWORD dst_unused:UNUSED_PAD src0_sel:DWORD src1_sel:WORD_1\n"
    "v_mfma_f32_16x16x32_bf16 v[108:111], a[212:215], v[196:199], v[108:111]\n"
    "s_waitcnt lgkmcnt(7)\n"
    "ds_read_u16_d16_hi v207, v207\n"
    "v_lshlrev_b32_sdwa v202, 1, v173 dst_sel:DWORD dst_unused:UNUSED_PAD src0_sel:DWORD src1_sel:WORD_1\n"
    "v_mfma_f32_16x16x32_bf16 v[120:123], a[216:219], v[196:199], v[120:123]\n"
    "v_lshlrev_b32_sdwa v195, 1, v174 dst_sel:DWORD dst_unused:UNUSED_PAD src0_sel:DWORD src1_sel:WORD_1\n"
    "v_or_b32_e64 v187, v187, v203\n"
    "v_mfma_f32_16x16x32_bf16 v[124:127], a[220:223], v[196:199], v[124:127]\n"
    "ds_read_u16 v193, v193\n"
    "v_lshlrev_b32_sdwa v203, 1, v175 dst_sel:DWORD dst_unused:UNUSED_PAD src0_sel:DWORD src1_sel:WORD_1\n"
    "v_mfma_f32_16x16x32_bf16 v[136:139], v[40:43], v[196:199], v[136:139]\n"
    "s_nop 0\n"
    "v_cvt_pk_bf16_f32 v98, v100, v101\n"
    "v_cvt_pk_bf16_f32 v99, v102, v103\n"
    "v_mfma_f32_16x16x32_bf16 v[140:143], v[44:47], v[196:199], v[140:143]\n"
    "s_waitcnt lgkmcnt(7)\n"
    "buffer_load_dwordx4 v[152:155], v245, s[40:43], s58 offen offset:64\n"
    "buffer_load_dwordx4 v[144:147], v245, s[40:43], s58 offen offset:0\n"
    "buffer_load_dwordx4 v[208:211], v245, s[40:43], s58 offen offset:128\n"
    "ds_read_u16_d16_hi v201, v201\n"
    "v_mfma_f32_16x16x32_bf16 v[168:171], a[96:99], a[224:227], 0\n"
    "ds_read_u16 v194, v194\n"
    "v_cvt_pk_f16_f32 v196, v176, v177\n"
    "v_mfma_f32_16x16x32_bf16 v[168:171], a[144:147], a[228:231], v[168:171]\n"
    "v_lshlrev_b32_sdwa v197, 1, v178 dst_sel:DWORD dst_unused:UNUSED_PAD src0_sel:DWORD src1_sel:WORD_1\n"
    "v_lshlrev_b32_sdwa v198, 1, v180 dst_sel:DWORD dst_unused:UNUSED_PAD src0_sel:DWORD src1_sel:WORD_1\n"
    "v_mfma_f32_16x16x32_bf16 v[168:171], a[56:59], a[232:235], v[168:171]\n"
    "s_waitcnt lgkmcnt(7)\n"
    "ds_read_u16_d16_hi v202, v202\n"
    "buffer_store_dwordx4 v[96:99], v245, s[44:47], s57 offen offset:0 sc1\n"
    "v_mfma_f32_16x16x32_bf16 v[172:175], a[100:103], a[224:227], 0\n"
    "s_nop 0\n"
    "ds_read_u16 v195, v195\n"
    "v_lshlrev_b32_sdwa v199, 1, v182 dst_sel:DWORD dst_unused:UNUSED_PAD src0_sel:DWORD src1_sel:WORD_1\n"
    "v_mfma_f32_16x16x32_bf16 v[172:175], a[148:151], a[228:231], v[172:175]\n"
    "s_waitcnt lgkmcnt(7)\n"
    "ds_read_u16_d16_hi v203, v203\n"
    "v_or_b32_e32 v188, v188, v204\n"
    "v_mfma_f32_16x16x32_bf16 v[172:175], a[60:63], a[232:235], v[172:175]\n"
    "v_or_b32_e64 v189, v189, v205\n"
    "v_lshlrev_b32_sdwa v205, 1, v179 dst_sel:DWORD dst_unused:UNUSED_PAD src0_sel:DWORD src1_sel:WORD_1\n"
    "v_mfma_f32_16x16x32_bf16 v[176:179], a[96:99], a[236:239], 0\n"
    "ds_read_u16 v197, v197\n"
    "v_or_b32_e32 v190, v190, v206\n"
    "v_mfma_f32_16x16x32_bf16 v[176:179], a[144:147], a[240:243], v[176:179]\n"
    "v_lshlrev_b32_sdwa v206, 1, v181 dst_sel:DWORD dst_unused:UNUSED_PAD src0_sel:DWORD src1_sel:WORD_1\n"
    "v_cvt_pk_bf16_f32 v224, v112, v113\n"
    "v_mfma_f32_16x16x32_bf16 v[176:179], a[56:59], a[244:247], v[176:179]\n"
    "s_waitcnt lgkmcnt(7)\n"
    "ds_read_u16_d16_hi v205, v205\n"
    "v_cvt_pk_bf16_f32 v225, v114, v115\n"
    "v_mfma_f32_16x16x32_bf16 v[96:99], a[0:3], v[184:187], 0\n"
    "v_cvt_pk_bf16_f32 v226, v116, v117\n"
    "v_cvt_pk_bf16_f32 v227, v118, v119\n"
    "v_mfma_f32_16x16x32_bf16 v[100:103], a[4:7], v[184:187], 0\n"
    "s_nop 0\n"
    "ds_read_u16 v198, v198\n"
    "v_cvt_pk_bf16_f32 v228, v128, v129\n"
    "v_mfma_f32_16x16x32_bf16 v[112:115], a[8:11], v[184:187], 0\n"
    "s_waitcnt lgkmcnt(7)\n"
    "buffer_load_dwordx4 v[156:159], v245, s[40:43], s89 offen offset:64\n"
    "buffer_load_dwordx4 v[148:151], v245, s[40:43], s89 offen offset:0\n"
    "buffer_load_dwordx4 v[212:215], v245, s[40:43], s89 offen offset:128\n"
    "ds_read_u16_d16_hi v206, v206\n"
    "v_cvt_pk_bf16_f32 v229, v130, v131\n"
    "v_or_b32_e64 v191, v191, v207\n"
    "v_mfma_f32_16x16x32_bf16 v[116:119], a[12:15], v[184:187], 0\n"
    "ds_read_u16 v199, v199\n"
    "v_lshlrev_b32_sdwa v207, 1, v183 dst_sel:DWORD dst_unused:UNUSED_PAD src0_sel:DWORD src1_sel:WORD_1\n"
    "v_mfma_f32_16x16x32_bf16 v[180:183], a[100:103], a[236:239], 0\n"
    "v_cvt_pk_bf16_f32 v230, v132, v133\n"
    "v_or_b32_e32 v193, v193, v201\n"
    "v_mfma_f32_16x16x32_bf16 v[180:183], a[148:151], a[240:243], v[180:183]\n"
    "s_waitcnt lgkmcnt(7)\n"
    "ds_read_u16_d16_hi v207, v207\n"
    "v_cvt_pk_bf16_f32 v231, v134, v135\n"
    "v_mfma_f32_16x16x32_bf16 v[180:183], a[60:63], a[244:247], v[180:183]\n"
    "v_cvt_pk_bf16_f32 v216, v104, v105\n"
    "v_cvt_pk_bf16_f32 v217, v106, v107\n"
    "v_mfma_f32_16x16x32_bf16 v[128:131], v[0:3], v[184:187], 0\n"
    "v_cvt_pk_bf16_f32 v218, v108, v109\n"
    "v_cvt_pk_bf16_f32 v219, v110, v111\n"
    "v_mfma_f32_16x16x32_bf16 v[132:135], v[4:7], v[184:187], 0\n"
    "s_waitcnt lgkmcnt(6)\n"
    "v_or_b32_e32 v194, v194, v202\n"
    "v_cvt_pk_bf16_f32 v220, v120, v121\n"
    "v_mfma_f32_16x16x32_bf16 v[104:107], a[0:3], v[188:191], 0\n"
    "v_cvt_pk_bf16_f32 v221, v122, v123\n"
    "v_cvt_pk_bf16_f32 v222, v124, v125\n"
    "v_mfma_f32_16x16x32_bf16 v[108:111], a[4:7], v[188:191], 0\n"
    "v_cvt_pk_bf16_f32 v223, v126, v127\n"
    "v_pk_fma_f16 v232, v192, v192, s48\n"
    "v_mfma_f32_16x16x32_bf16 v[120:123], a[8:11], v[188:191], 0\n"
    "v_cvt_pk_bf16_f32 v160, v136, v137\n"
    "v_cvt_pk_bf16_f32 v161, v138, v139\n"
    "v_mfma_f32_16x16x32_bf16 v[124:127], a[12:15], v[188:191], 0\n"
    "s_waitcnt lgkmcnt(4)\n"
    "v_cvt_pk_bf16_f32 v162, v140, v141\n"
    "v_or_b32_e32 v195, v195, v203\n"
    "v_cvt_pk_bf16_f32 v163, v142, v143\n"
    "v_pk_mul_f16 v232, v192, v232\n"
    "v_mfma_f32_16x16x32_bf16 v[136:139], v[0:3], v[188:191], 0\n"
    "v_exp_f16_e64 v234, v232\n"
    "v_mfma_f32_16x16x32_bf16 v[140:143], v[4:7], v[188:191], 0\n"
    "v_or_b32_e64 v197, v197, v205\n"
    "v_exp_f16_sdwa v234, v232 dst_sel:WORD_1 dst_unused:UNUSED_PRESERVE src0_sel:WORD_1\n"
    "v_lshlrev_b32_sdwa v184, 1, v168 dst_sel:DWORD dst_unused:UNUSED_PAD src0_sel:DWORD src1_sel:WORD_1\n"
    "v_pk_fma_f16 v234, v234, s49, s49\n"
    "v_lshlrev_b32_sdwa v200, 1, v169 dst_sel:DWORD dst_unused:UNUSED_PAD src0_sel:DWORD src1_sel:WORD_1\n"
    "v_lshlrev_b32_sdwa v185, 1, v170 dst_sel:DWORD dst_unused:UNUSED_PAD src0_sel:DWORD src1_sel:WORD_1\n"
    "v_lshlrev_b32_sdwa v201, 1, v171 dst_sel:DWORD dst_unused:UNUSED_PAD src0_sel:DWORD src1_sel:WORD_1\n"
    "v_mfma_f32_16x16x32_bf16 v[168:171], a[104:107], a[224:227], 0\n"
    "s_waitcnt lgkmcnt(1)\n"
    "ds_read_u16 v184, v184\n"
    "ds_read_u16_d16_hi v200, v200\n"
    "v_mfma_f32_16x16x32_bf16 v[168:171], a[152:155], a[228:231], v[168:171]\n"
    "s_nop 0\n"
    "ds_read_u16 v185, v185\n"
    "ds_read_u16_d16_hi v201, v201\n"
    "v_lshlrev_b32_sdwa v186, 1, v172 dst_sel:DWORD dst_unused:UNUSED_PAD src0_sel:DWORD src1_sel:WORD_1\n"
    "v_lshlrev_b32_sdwa v202, 1, v173 dst_sel:DWORD dst_unused:UNUSED_PAD src0_sel:DWORD src1_sel:WORD_1\n"
    "v_lshlrev_b32_sdwa v187, 1, v174 dst_sel:DWORD dst_unused:UNUSED_PAD src0_sel:DWORD src1_sel:WORD_1\n"
    "v_lshlrev_b32_sdwa v203, 1, v175 dst_sel:DWORD dst_unused:UNUSED_PAD src0_sel:DWORD src1_sel:WORD_1\n"
    "v_rcp_f16_e32 v232, v234\n"
    "v_mfma_f32_16x16x32_bf16 v[168:171], a[64:67], a[232:235], v[168:171]\n"
    "s_waitcnt lgkmcnt(4)\n"
    "ds_read_u16 v186, v186\n"
    "v_or_b32_e64 v198, v198, v206\n"
    "v_rcp_f16_sdwa v232, v234 dst_sel:WORD_1 dst_unused:UNUSED_PRESERVE src0_sel:WORD_1\n"
    "v_mfma_f32_16x16x32_bf16 v[172:175], a[108:111], a[224:227], 0\n"
    "ds_read_u16_d16_hi v202, v202\n"
    "v_lshlrev_b32_sdwa v188, 1, v176 dst_sel:DWORD dst_unused:UNUSED_PAD src0_sel:DWORD src1_sel:WORD_1\n"
    "v_lshlrev_b32_sdwa v204, 1, v177 dst_sel:DWORD dst_unused:UNUSED_PAD src0_sel:DWORD src1_sel:WORD_1\n"
    "v_lshlrev_b32_sdwa v189, 1, v178 dst_sel:DWORD dst_unused:UNUSED_PAD src0_sel:DWORD src1_sel:WORD_1\n"
    "v_lshlrev_b32_sdwa v205, 1, v179 dst_sel:DWORD dst_unused:UNUSED_PAD src0_sel:DWORD src1_sel:WORD_1\n"
    "v_lshlrev_b32_sdwa v190, 1, v180 dst_sel:DWORD dst_unused:UNUSED_PAD src0_sel:DWORD src1_sel:WORD_1\n"
    "v_lshlrev_b32_sdwa v206, 1, v181 dst_sel:DWORD dst_unused:UNUSED_PAD src0_sel:DWORD src1_sel:WORD_1\n"
    "v_lshlrev_b32_sdwa v191, 1, v182 dst_sel:DWORD dst_unused:UNUSED_PAD src0_sel:DWORD src1_sel:WORD_1\n"
    "v_fma_mix_f32 v234, v192, v232, 0 op_sel_hi:[1,1,0]\n"
    "v_mfma_f32_16x16x32_bf16 v[172:175], a[156:159], a[228:231], v[172:175]\n"
    "ds_read_u16 v187, v187\n"
    "v_or_b32_e64 v199, v199, v207\n"
    "v_mfma_f32_16x16x32_bf16 v[172:175], a[68:71], a[232:235], v[172:175]\n"
    "ds_read_u16_d16_hi v203, v203\n"
    "v_lshlrev_b32_sdwa v207, 1, v183 dst_sel:DWORD dst_unused:UNUSED_PAD src0_sel:DWORD src1_sel:WORD_1\n"
    "v_fma_mix_f32 v232, v192, v232, 0 op_sel:[1,1,0] op_sel_hi:[1,1,0]\n"
    "v_mfma_f32_16x16x32_bf16 v[176:179], a[104:107], a[236:239], 0\n"
    "ds_read_u16 v188, v188\n"
    "v_cvt_pk_bf16_f32 v192, v234, v232\n"
    "v_mfma_f32_16x16x32_bf16 v[176:179], a[152:155], a[240:243], v[176:179]\n"
    "v_pk_fma_f16 v232, v196, v196, s48\n"
    "v_pk_mul_f16 v232, v196, v232\n"
    "v_mfma_f32_16x16x32_bf16 v[176:179], a[64:67], a[244:247], v[176:179]\n"
    "s_waitcnt lgkmcnt(7)\n"
    "ds_read_u16_d16_hi v204, v204\n"
    "v_exp_f16_e32 v234, v232\n"
    "v_mfma_f32_16x16x32_bf16 v[180:183], a[108:111], a[236:239], 0\n"
    "v_exp_f16_sdwa v234, v232 dst_sel:WORD_1 dst_unused:UNUSED_PRESERVE src0_sel:WORD_1\n"
    "v_mfma_f32_16x16x32_bf16 v[180:183], a[156:159], a[240:243], v[180:183]\n"
    "ds_read_u16 v189, v189\n"
    "v_pk_fma_f16 v234, v234, s49, s49\n"
    "v_mfma_f32_16x16x32_bf16 v[180:183], a[68:71], a[244:247], v[180:183]\n"
    "s_waitcnt lgkmcnt(7)\n"
    "ds_read_u16_d16_hi v205, v205\n"
    "v_rcp_f16_e32 v232, v234\n"
    "v_mfma_f32_16x16x32_bf16 v[96:99], a[16:19], v[192:195], v[96:99]\n"
    "v_rcp_f16_sdwa v232, v234 dst_sel:WORD_1 dst_unused:UNUSED_PRESERVE src0_sel:WORD_1\n"
    "v_mfma_f32_16x16x32_bf16 v[100:103], a[20:23], v[192:195], v[100:103]\n"
    "ds_read_u16 v190, v190\n"
    "v_fma_mix_f32 v234, v196, v232, 0 op_sel_hi:[1,1,0]\n"
    "v_mfma_f32_16x16x32_bf16 v[112:115], a[24:27], v[192:195], v[112:115]\n"
    "v_fma_mix_f32 v232, v196, v232, 0 op_sel:[1,1,0] op_sel_hi:[1,1,0]\n"
    "v_or_b32_e32 v184, v184, v200\n"
    "v_mfma_f32_16x16x32_bf16 v[116:119], a[28:31], v[192:195], v[116:119]\n"
    "s_waitcnt lgkmcnt(7)\n"
    "ds_read_u16_d16_hi v206, v206\n"
    "v_cvt_pk_bf16_f32 v196, v234, v232\n"
    "v_mfma_f32_16x16x32_bf16 v[128:131], v[8:11], v[192:195], v[128:131]\n"
    "ds_read_u16 v191, v191\n"
    "v_or_b32_e64 v185, v185, v201\n"
    "v_mfma_f32_16x16x32_bf16 v[132:135], v[12:15], v[192:195], v[132:135]\n"
    "v_cvt_pk_f16_f32 v192, v168, v169\n"
    "v_lshlrev_b32_sdwa v193, 1, v170 dst_sel:DWORD dst_unused:UNUSED_PAD src0_sel:DWORD src1_sel:WORD_1\n"
    "v_mfma_f32_16x16x32_bf16 v[104:107], a[16:19], v[196:199], v[104:107]\n"
    "s_waitcnt lgkmcnt(7)\n"
    "ds_read_u16_d16_hi v207, v207\n"
    "buffer_store_dwordx4 v[224:227], v245, s[44:47], s57 offen offset:64 sc1\n"
    "v_mfma_f32_16x16x32_bf16 v[108:111], a[20:23], v[196:199], v[108:111]\n"
    "s_nop 0\n"
    "ds_read_u16 v193, v193\n"
    "v_lshlrev_b32_sdwa v201, 1, v171 dst_sel:DWORD dst_unused:UNUSED_PAD src0_sel:DWORD src1_sel:WORD_1\n"
    "v_mfma_f32_16x16x32_bf16 v[120:123], a[24:27], v[196:199], v[120:123]\n"
    "v_lshlrev_b32_sdwa v194, 1, v172 dst_sel:DWORD dst_unused:UNUSED_PAD src0_sel:DWORD src1_sel:WORD_1\n"
    "v_or_b32_e32 v186, v186, v202\n"
    "v_mfma_f32_16x16x32_bf16 v[124:127], a[28:31], v[196:199], v[124:127]\n"
    "s_waitcnt lgkmcnt(7)\n"
    "ds_read_u16_d16_hi v201, v201\n"
    "v_lshlrev_b32_sdwa v202, 1, v173 dst_sel:DWORD dst_unused:UNUSED_PAD src0_sel:DWORD src1_sel:WORD_1\n"
    "v_mfma_f32_16x16x32_bf16 v[136:139], v[8:11], v[196:199], v[136:139]\n"
    "ds_read_u16 v194, v194\n"
    "v_lshlrev_b32_sdwa v195, 1, v174 dst_sel:DWORD dst_unused:UNUSED_PAD src0_sel:DWORD src1_sel:WORD_1\n"
    "v_mfma_f32_16x16x32_bf16 v[140:143], v[12:15], v[196:199], v[140:143]\n"
    "s_nop 0\n"
    "v_cvt_pk_f16_f32 v196, v176, v177\n"
    "v_lshlrev_b32_sdwa v197, 1, v178 dst_sel:DWORD dst_unused:UNUSED_PAD src0_sel:DWORD src1_sel:WORD_1\n"
    "v_mfma_f32_16x16x32_bf16 v[168:171], a[112:115], a[224:227], 0\n"
    "s_waitcnt lgkmcnt(7)\n"
    "ds_read_u16_d16_hi v202, v202\n"
    "v_lshlrev_b32_sdwa v198, 1, v180 dst_sel:DWORD dst_unused:UNUSED_PAD src0_sel:DWORD src1_sel:WORD_1\n"
    "v_mfma_f32_16x16x32_bf16 v[168:171], a[160:163], a[228:231], v[168:171]\n"
    "v_lshlrev_b32_sdwa v199, 1, v182 dst_sel:DWORD dst_unused:UNUSED_PAD src0_sel:DWORD src1_sel:WORD_1\n"
    "v_or_b32_e64 v187, v187, v203\n"
    "v_mfma_f32_16x16x32_bf16 v[168:171], a[72:75], a[232:235], v[168:171]\n"
    "ds_read_u16 v195, v195\n"
    "v_lshlrev_b32_sdwa v203, 1, v175 dst_sel:DWORD dst_unused:UNUSED_PAD src0_sel:DWORD src1_sel:WORD_1\n"
    "v_mfma_f32_16x16x32_bf16 v[172:175], a[116:119], a[224:227], 0\n"
    "v_or_b32_e64 v188, v188, v204\n"
    "v_or_b32_e32 v189, v189, v205\n"
    "v_mfma_f32_16x16x32_bf16 v[172:175], a[164:167], a[228:231], v[172:175]\n"
    "s_waitcnt lgkmcnt(7)\n"
    "ds_read_u16_d16_hi v203, v203\n"
    "v_lshlrev_b32_sdwa v205, 1, v179 dst_sel:DWORD dst_unused:UNUSED_PAD src0_sel:DWORD src1_sel:WORD_1\n"
    "v_mfma_f32_16x16x32_bf16 v[172:175], v[52:55], a[232:235], v[172:175]\n"
    "ds_read_u16 v197, v197\n"
    "v_or_b32_e64 v190, v190, v206\n"
    "v_mfma_f32_16x16x32_bf16 v[176:179], a[112:115], a[236:239], 0\n"
    "v_lshlrev_b32_sdwa v206, 1, v181 dst_sel:DWORD dst_unused:UNUSED_PAD src0_sel:DWORD src1_sel:WORD_1\n"
    "v_pk_fma_f16 v232, v192, v192, s48\n"
    "v_mfma_f32_16x16x32_bf16 v[176:179], a[160:163], a[240:243], v[176:179]\n"
    "s_waitcnt lgkmcnt(7)\n"
    "ds_read_u16_d16_hi v205, v205\n"
    "v_or_b32_e32 v191, v191, v207\n"
    "v_mfma_f32_16x16x32_bf16 v[176:179], a[72:75], a[244:247], v[176:179]\n"
    "v_lshlrev_b32_sdwa v207, 1, v183 dst_sel:DWORD dst_unused:UNUSED_PAD src0_sel:DWORD src1_sel:WORD_1\n"
    "v_pk_mul_f16 v232, v192, v232\n"
    "v_mfma_f32_16x16x32_bf16 v[180:183], a[116:119], a[236:239], 0\n"
    "ds_read_u16 v198, v198\n"
    "v_exp_f16_e32 v234, v232\n"
    "v_mfma_f32_16x16x32_bf16 v[180:183], a[164:167], a[240:243], v[180:183]\n"
    "s_waitcnt lgkmcnt(7)\n"
    "ds_read_u16_d16_hi v206, v206\n"
    "v_or_b32_e64 v193, v193, v201\n"
    "v_mfma_f32_16x16x32_bf16 v[180:183], v[52:55], a[244:247], v[180:183]\n"
    "v_exp_f16_sdwa v234, v232 dst_sel:WORD_1 dst_unused:UNUSED_PRESERVE src0_sel:WORD_1\n"
    "v_mfma_f32_16x16x32_bf16 v[96:99], a[32:35], v[184:187], v[96:99]\n"
    "ds_read_u16 v199, v199\n"
    "v_pk_fma_f16 v234, v234, s49, s49\n"
    "v_mfma_f32_16x16x32_bf16 v[100:103], a[36:39], v[184:187], v[100:103]\n"
    "v_lshlrev_b32_sdwa v200, 1, v169 dst_sel:DWORD dst_unused:UNUSED_PAD src0_sel:DWORD src1_sel:WORD_1\n"
    "v_lshlrev_b32_sdwa v201, 1, v171 dst_sel:DWORD dst_unused:UNUSED_PAD src0_sel:DWORD src1_sel:WORD_1\n"
    "v_mfma_f32_16x16x32_bf16 v[112:115], a[40:43], v[184:187], v[112:115]\n"
    "s_waitcnt lgkmcnt(7)\n"
    "ds_read_u16_d16_hi v207, v207\n"
    "v_or_b32_e32 v194, v194, v202\n"
    "v_mfma_f32_16x16x32_bf16 v[116:119], a[44:47], v[184:187], v[116:119]\n"
    "v_lshlrev_b32_sdwa v202, 1, v173 dst_sel:DWORD dst_unused:UNUSED_PAD src0_sel:DWORD src1_sel:WORD_1\n"
    "v_rcp_f16_e64 v232, v234\n"
    "v_mfma_f32_16x16x32_bf16 v[128:131], v[16:19], v[184:187], v[128:131]\n"
    "ds_read_u16_d16_hi v200, v200\n"
    "v_rcp_f16_sdwa v232, v234 dst_sel:WORD_1 dst_unused:UNUSED_PRESERVE src0_sel:WORD_1\n"
    "v_mfma_f32_16x16x32_bf16 v[132:135], v[20:23], v[184:187], v[132:135]\n"
    "s_waitcnt lgkmcnt(7)\n"
    "ds_read_u16_d16_hi v201, v201\n"
    "v_or_b32_e32 v195, v195, v203\n"
    "v_mfma_f32_16x16x32_bf16 v[104:107], a[32:35], v[188:191], v[104:107]\n"
    "v_lshlrev_b32_sdwa v184, 1, v168 dst_sel:DWORD dst_unused:UNUSED_PAD src0_sel:DWORD src1_sel:WORD_1\n"
    "v_lshlrev_b32_sdwa v185, 1, v170 dst_sel:DWORD dst_unused:UNUSED_PAD src0_sel:DWORD src1_sel:WORD_1\n"
    "v_mfma_f32_16x16x32_bf16 v[108:111], a[36:39], v[188:191], v[108:111]\n"
    "ds_read_u16 v184, v184\n"
    "buffer_store_dwordx4 v[228:231], v245, s[44:47], s57 offen offset:128 sc1\n"
    "v_mfma_f32_16x16x32_bf16 v[120:123], a[40:43], v[188:191], v[120:123]\n"
    "s_waitcnt lgkmcnt(7)\n"
    "ds_read_u16 v185, v185\n"
    "v_or_b32_e32 v197, v197, v205\n"
    "v_mfma_f32_16x16x32_bf16 v[124:127], a[44:47], v[188:191], v[124:127]\n"
    "ds_read_u16_d16_hi v202, v202\n"
    "v_lshlrev_b32_sdwa v186, 1, v172 dst_sel:DWORD dst_unused:UNUSED_PAD src0_sel:DWORD src1_sel:WORD_1\n"
    "v_mfma_f32_16x16x32_bf16 v[136:139], v[16:19], v[188:191], v[136:139]\n"
    "v_lshlrev_b32_sdwa v187, 1, v174 dst_sel:DWORD dst_unused:UNUSED_PAD src0_sel:DWORD src1_sel:WORD_1\n"
    "v_lshlrev_b32_sdwa v203, 1, v175 dst_sel:DWORD dst_unused:UNUSED_PAD src0_sel:DWORD src1_sel:WORD_1\n"
    "v_mfma_f32_16x16x32_bf16 v[140:143], v[20:23], v[188:191], v[140:143]\n"
    "s_waitcnt lgkmcnt(7)\n"
    "ds_read_u16 v186, v186\n"
    "v_or_b32_e32 v198, v198, v206\n"
    "v_mfma_f32_16x16x32_bf16 v[168:171], a[120:123], a[224:227], 0\n"
    "v_lshlrev_b32_sdwa v188, 1, v176 dst_sel:DWORD dst_unused:UNUSED_PAD src0_sel:DWORD src1_sel:WORD_1\n"
    "v_lshlrev_b32_sdwa v204, 1, v177 dst_sel:DWORD dst_unused:UNUSED_PAD src0_sel:DWORD src1_sel:WORD_1\n"
    "v_mfma_f32_16x16x32_bf16 v[168:171], a[168:171], a[228:231], v[168:171]\n"
    "s_nop 0\n"
    "ds_read_u16 v187, v187\n"
    "v_lshlrev_b32_sdwa v189, 1, v178 dst_sel:DWORD dst_unused:UNUSED_PAD src0_sel:DWORD src1_sel:WORD_1\n"
    "v_mfma_f32_16x16x32_bf16 v[168:171], a[76:79], a[232:235], v[168:171]\n"
    "s_waitcnt lgkmcnt(7)\n"
    "ds_read_u16_d16_hi v203, v203\n"
    "v_lshlrev_b32_sdwa v205, 1, v179 dst_sel:DWORD dst_unused:UNUSED_PAD src0_sel:DWORD src1_sel:WORD_1\n"
    "v_mfma_f32_16x16x32_bf16 v[172:175], a[124:127], a[224:227], 0\n"
    "v_lshlrev_b32_sdwa v190, 1, v180 dst_sel:DWORD dst_unused:UNUSED_PAD src0_sel:DWORD src1_sel:WORD_1\n"
    "v_lshlrev_b32_sdwa v206, 1, v181 dst_sel:DWORD dst_unused:UNUSED_PAD src0_sel:DWORD src1_sel:WORD_1\n"
    "v_mfma_f32_16x16x32_bf16 v[172:175], a[172:175], a[228:231], v[172:175]\n"
    "ds_read_u16 v188, v188\n"
    "v_lshlrev_b32_sdwa v191, 1, v182 dst_sel:DWORD dst_unused:UNUSED_PAD src0_sel:DWORD src1_sel:WORD_1\n"
    "v_fma_mix_f32 v234, v192, v232, 0 op_sel_hi:[1,1,0]\n"
    "v_mfma_f32_16x16x32_bf16 v[172:175], v[60:63], a[232:235], v[172:175]\n"
    "s_barrier\n"
    "v_or_b32_e64 v199, v199, v207\n"
    "v_lshlrev_b32_sdwa v207, 1, v183 dst_sel:DWORD dst_unused:UNUSED_PAD src0_sel:DWORD src1_sel:WORD_1\n"
    "v_fma_mix_f32 v232, v192, v232, 0 op_sel:[1,1,0] op_sel_hi:[1,1,0]\n"
    "v_mfma_f32_16x16x32_bf16 v[176:179], a[120:123], a[236:239], 0\n"
    "s_waitcnt lgkmcnt(5)\n"
    "ds_read_u16_d16_hi v204, v204\n"
    "ds_read_u16 v189, v189\n"
    "v_cvt_pk_bf16_f32 v192, v234, v232\n"
    "v_mfma_f32_16x16x32_bf16 v[176:179], a[168:171], a[240:243], v[176:179]\n"
    "ds_read_u16_d16_hi v205, v205\n"
    "ds_read_b128 a[224:227], v243 offset:24640\n"
    "v_mfma_f32_16x16x32_bf16 v[176:179], a[76:79], a[244:247], v[176:179]\n"
    "v_pk_fma_f16 v232, v196, v196, s48\n"
    "v_or_b32_e64 v184, v184, v200\n"
    "v_mfma_f32_16x16x32_bf16 v[180:183], a[124:127], a[236:239], 0\n"
    "ds_read_u16 v190, v190\n"
    "v_pk_mul_f16 v232, v196, v232\n"
    "v_mfma_f32_16x16x32_bf16 v[180:183], a[172:175], a[240:243], v[180:183]\n"
    "ds_read_b128 a[228:231], v243 offset:25664\n"
    "v_exp_f16_e32 v234, v232\n"
    "v_mfma_f32_16x16x32_bf16 v[180:183], v[60:63], a[244:247], v[180:183]\n"
    "s_waitcnt lgkmcnt(8)\n"
    "ds_read_u16_d16_hi v206, v206\n"
    "v_exp_f16_sdwa v234, v232 dst_sel:WORD_1 dst_unused:UNUSED_PRESERVE src0_sel:WORD_1\n"
    "v_mfma_f32_16x16x32_bf16 v[96:99], a[176:179], v[192:195], v[96:99]\n"
    "ds_read_b128 a[232:235], v243 offset:26688\n"
    "v_pk_fma_f16 v234, v234, s49, s49\n"
    "v_mfma_f32_16x16x32_bf16 v[100:103], a[180:183], v[192:195], v[100:103]\n"
    "s_waitcnt lgkmcnt(9)\n"
    "ds_read_u16 v191, v191\n"
    "v_rcp_f16_e64 v232, v234\n"
    "v_mfma_f32_16x16x32_bf16 v[112:115], a[184:187], v[192:195], v[112:115]\n"
    "v_rcp_f16_sdwa v232, v234 dst_sel:WORD_1 dst_unused:UNUSED_PRESERVE src0_sel:WORD_1\n"
    "v_mfma_f32_16x16x32_bf16 v[116:119], a[188:191], v[192:195], v[116:119]\n"
    "s_waitcnt lgkmcnt(8)\n"
    "ds_read_b128 a[236:239], v243 offset:27712\n"
    "v_fma_mix_f32 v234, v196, v232, 0 op_sel_hi:[1,1,0]\n"
    "v_mfma_f32_16x16x32_bf16 v[128:131], v[24:27], v[192:195], v[128:131]\n"
    "ds_read_u16_d16_hi v207, v207\n"
    "v_fma_mix_f32 v232, v196, v232, 0 op_sel:[1,1,0] op_sel_hi:[1,1,0]\n"
    "v_mfma_f32_16x16x32_bf16 v[132:135], v[28:31], v[192:195], v[132:135]\n"
    "v_cvt_pk_bf16_f32 v196, v234, v232\n"
    "buffer_store_dwordx4 v[216:219], v245, s[44:47], s88 offen offset:0 sc1\n"
    "ds_read_b128 a[240:243], v243 offset:28736\n"
    "v_mfma_f32_16x16x32_bf16 v[104:107], a[176:179], v[196:199], v[104:107]\n"
    "s_waitcnt lgkmcnt(8)\n"
    "ds_read_b128 a[244:247], v243 offset:29760\n"
    "v_or_b32_e32 v185, v185, v201\n"
    "v_mfma_f32_16x16x32_bf16 v[108:111], a[180:183], v[196:199], v[108:111]\n"
    "v_cvt_pk_f16_f32 v192, v168, v169\n"
    "v_lshlrev_b32_sdwa v193, 1, v170 dst_sel:DWORD dst_unused:UNUSED_PAD src0_sel:DWORD src1_sel:WORD_1\n"
    "v_mfma_f32_16x16x32_bf16 v[120:123], a[184:187], v[196:199], v[120:123]\n"
    "ds_read_u16 v193, v193\n"
    "v_lshlrev_b32_sdwa v201, 1, v171 dst_sel:DWORD dst_unused:UNUSED_PAD src0_sel:DWORD src1_sel:WORD_1\n"
    "v_mfma_f32_16x16x32_bf16 v[124:127], a[188:191], v[196:199], v[124:127]\n"
    "ds_read_u16_d16_hi v201, v201\n"
    "v_lshlrev_b32_sdwa v194, 1, v172 dst_sel:DWORD dst_unused:UNUSED_PAD src0_sel:DWORD src1_sel:WORD_1\n"
    "v_mfma_f32_16x16x32_bf16 v[136:139], v[24:27], v[196:199], v[136:139]\n"
    "ds_read_u16 v194, v194\n"
    "v_or_b32_e32 v186, v186, v202\n"
    "v_mfma_f32_16x16x32_bf16 v[140:143], v[28:31], v[196:199], v[140:143]\n"
    "s_waitcnt lgkmcnt(8)\n"
    "v_lshlrev_b32_sdwa v202, 1, v173 dst_sel:DWORD dst_unused:UNUSED_PAD src0_sel:DWORD src1_sel:WORD_1\n"
    "v_lshlrev_b32_sdwa v195, 1, v174 dst_sel:DWORD dst_unused:UNUSED_PAD src0_sel:DWORD src1_sel:WORD_1\n"
    "v_mfma_f32_16x16x32_bf16 v[168:171], a[80:83], a[224:227], 0\n"
    "ds_read_u16_d16_hi v202, v202\n"
    "v_cvt_pk_f16_f32 v196, v176, v177\n"
    "v_mfma_f32_16x16x32_bf16 v[168:171], a[128:131], a[228:231], v[168:171]\n"
    "ds_read_u16 v195, v195\n"
    "v_lshlrev_b32_sdwa v197, 1, v178 dst_sel:DWORD dst_unused:UNUSED_PAD src0_sel:DWORD src1_sel:WORD_1\n"
    "v_mfma_f32_16x16x32_bf16 v[168:171], a[48:51], a[232:235], v[168:171]\n"
    "v_lshlrev_b32_sdwa v198, 1, v180 dst_sel:DWORD dst_unused:UNUSED_PAD src0_sel:DWORD src1_sel:WORD_1\n"
    "v_lshlrev_b32_sdwa v199, 1, v182 dst_sel:DWORD dst_unused:UNUSED_PAD src0_sel:DWORD src1_sel:WORD_1\n"
    "v_or_b32_e64 v187, v187, v203\n"
    "v_lshlrev_b32_sdwa v203, 1, v175 dst_sel:DWORD dst_unused:UNUSED_PAD src0_sel:DWORD src1_sel:WORD_1\n"
    "v_mfma_f32_16x16x32_bf16 v[172:175], a[84:87], a[224:227], 0\n"
    "ds_read_u16_d16_hi v203, v203\n"
    "v_or_b32_e32 v188, v188, v204\n"
    "v_mfma_f32_16x16x32_bf16 v[172:175], a[132:135], a[228:231], v[172:175]\n"
    "s_waitcnt lgkmcnt(6)\n"
    "ds_read_u16 v197, v197\n"
    "v_or_b32_e64 v189, v189, v205\n"
    "v_mfma_f32_16x16x32_bf16 v[172:175], v[48:51], a[232:235], v[172:175]\n"
    "v_lshlrev_b32_sdwa v205, 1, v179 dst_sel:DWORD dst_unused:UNUSED_PAD src0_sel:DWORD src1_sel:WORD_1\n"
    "v_or_b32_e64 v190, v190, v206\n"
    "v_mfma_f32_16x16x32_bf16 v[176:179], a[80:83], a[236:239], 0\n"
    "ds_read_u16_d16_hi v205, v205\n"
    "v_lshlrev_b32_sdwa v206, 1, v181 dst_sel:DWORD dst_unused:UNUSED_PAD src0_sel:DWORD src1_sel:WORD_1\n"
    "v_mfma_f32_16x16x32_bf16 v[176:179], a[128:131], a[240:243], v[176:179]\n"
    "ds_read_u16 v198, v198\n"
    "v_or_b32_e64 v191, v191, v207\n"
    "v_mfma_f32_16x16x32_bf16 v[176:179], a[48:51], a[244:247], v[176:179]\n"
    "v_lshlrev_b32_sdwa v207, 1, v183 dst_sel:DWORD dst_unused:UNUSED_PAD src0_sel:DWORD src1_sel:WORD_1\n"
    "v_pk_fma_f16 v232, v192, v192, s48\n"
    "v_mfma_f32_16x16x32_bf16 v[180:183], a[84:87], a[236:239], 0\n"
    "s_waitcnt lgkmcnt(7)\n"
    "ds_read_u16_d16_hi v206, v206\n"
    "v_or_b32_e32 v193, v193, v201\n"
    "v_mfma_f32_16x16x32_bf16 v[180:183], a[132:135], a[240:243], v[180:183]\n"
    "v_pk_mul_f16 v232, v192, v232\n"
    "v_lshlrev_b32_sdwa v200, 1, v169 dst_sel:DWORD dst_unused:UNUSED_PAD src0_sel:DWORD src1_sel:WORD_1\n"
    "v_mfma_f32_16x16x32_bf16 v[180:183], v[48:51], a[244:247], v[180:183]\n"
    "ds_read_u16 v199, v199\n"
    "v_exp_f16_e32 v234, v232\n"
    "v_mfma_f32_16x16x32_bf16 v[96:99], a[192:195], v[184:187], v[96:99]\n"
    "s_waitcnt lgkmcnt(7)\n"
    "ds_read_u16_d16_hi v207, v207\n"
    "v_or_b32_e64 v194, v194, v202\n"
    "v_mfma_f32_16x16x32_bf16 v[100:103], a[196:199], v[184:187], v[100:103]\n"
    "v_exp_f16_sdwa v234, v232 dst_sel:WORD_1 dst_unused:UNUSED_PRESERVE src0_sel:WORD_1\n"
    "v_mfma_f32_16x16x32_bf16 v[112:115], a[200:203], v[184:187], v[112:115]\n"
    "ds_read_u16_d16_hi v200, v200\n"
    "v_pk_fma_f16 v234, v234, s49, s49\n"
    "v_mfma_f32_16x16x32_bf16 v[116:119], a[204:207], v[184:187], v[116:119]\n"
    "v_lshlrev_b32_sdwa v201, 1, v171 dst_sel:DWORD dst_unused:UNUSED_PAD src0_sel:DWORD src1_sel:WORD_1\n"
    "v_lshlrev_b32_sdwa v202, 1, v173 dst_sel:DWORD dst_unused:UNUSED_PAD src0_sel:DWORD src1_sel:WORD_1\n"
    "v_mfma_f32_16x16x32_bf16 v[128:131], v[32:35], v[184:187], v[128:131]\n"
    "s_waitcnt lgkmcnt(7)\n"
    "ds_read_u16_d16_hi v201, v201\n"
    "v_or_b32_e32 v195, v195, v203\n"
    "v_mfma_f32_16x16x32_bf16 v[132:135], v[36:39], v[184:187], v[132:135]\n"
    "v_lshlrev_b32_sdwa v184, 1, v168 dst_sel:DWORD dst_unused:UNUSED_PAD src0_sel:DWORD src1_sel:WORD_1\n"
    "v_lshlrev_b32_sdwa v185, 1, v170 dst_sel:DWORD dst_unused:UNUSED_PAD src0_sel:DWORD src1_sel:WORD_1\n"
    "v_mfma_f32_16x16x32_bf16 v[104:107], a[192:195], v[188:191], v[104:107]\n"
    "ds_read_u16 v184, v184\n"
    "buffer_store_dwordx4 v[220:223], v245, s[44:47], s88 offen offset:64 sc1\n"
    "v_mfma_f32_16x16x32_bf16 v[108:111], a[196:199], v[188:191], v[108:111]\n"
    "s_waitcnt lgkmcnt(7)\n"
    "ds_read_u16 v185, v185\n"
    "v_or_b32_e32 v197, v197, v205\n"
    "v_mfma_f32_16x16x32_bf16 v[120:123], a[200:203], v[188:191], v[120:123]\n"
    "ds_read_u16_d16_hi v202, v202\n"
    "v_lshlrev_b32_sdwa v186, 1, v172 dst_sel:DWORD dst_unused:UNUSED_PAD src0_sel:DWORD src1_sel:WORD_1\n"
    "v_mfma_f32_16x16x32_bf16 v[124:127], a[204:207], v[188:191], v[124:127]\n"
    "v_lshlrev_b32_sdwa v187, 1, v174 dst_sel:DWORD dst_unused:UNUSED_PAD src0_sel:DWORD src1_sel:WORD_1\n"
    "v_lshlrev_b32_sdwa v203, 1, v175 dst_sel:DWORD dst_unused:UNUSED_PAD src0_sel:DWORD src1_sel:WORD_1\n"
    "v_mfma_f32_16x16x32_bf16 v[136:139], v[32:35], v[188:191], v[136:139]\n"
    "s_waitcnt lgkmcnt(7)\n"
    "ds_read_u16 v186, v186\n"
    "v_rcp_f16_e32 v232, v234\n"
    "v_mfma_f32_16x16x32_bf16 v[140:143], v[36:39], v[188:191], v[140:143]\n"
    "ds_read_u16 v187, v187\n"
    "v_or_b32_e32 v198, v198, v206\n"
    "v_mfma_f32_16x16x32_bf16 v[168:171], a[88:91], a[224:227], 0\n"
    "v_rcp_f16_sdwa v232, v234 dst_sel:WORD_1 dst_unused:UNUSED_PRESERVE src0_sel:WORD_1\n"
    "v_mfma_f32_16x16x32_bf16 v[168:171], a[136:139], a[228:231], v[168:171]\n"
    "s_waitcnt lgkmcnt(7)\n"
    "ds_read_u16_d16_hi v203, v203\n"
    "v_lshlrev_b32_sdwa v188, 1, v176 dst_sel:DWORD dst_unused:UNUSED_PAD src0_sel:DWORD src1_sel:WORD_1\n"
    "v_mfma_f32_16x16x32_bf16 v[168:171], a[52:55], a[232:235], v[168:171]\n"
    "v_lshlrev_b32_sdwa v204, 1, v177 dst_sel:DWORD dst_unused:UNUSED_PAD src0_sel:DWORD src1_sel:WORD_1\n"
    "v_lshlrev_b32_sdwa v189, 1, v178 dst_sel:DWORD dst_unused:UNUSED_PAD src0_sel:DWORD src1_sel:WORD_1\n"
    "v_mfma_f32_16x16x32_bf16 v[172:175], a[92:95], a[224:227], 0\n"
    "ds_read_u16 v188, v188\n"
    "v_lshlrev_b32_sdwa v205, 1, v179 dst_sel:DWORD dst_unused:UNUSED_PAD src0_sel:DWORD src1_sel:WORD_1\n"
    "v_mfma_f32_16x16x32_bf16 v[172:175], a[140:143], a[228:231], v[172:175]\n"
    "v_lshlrev_b32_sdwa v190, 1, v180 dst_sel:DWORD dst_unused:UNUSED_PAD src0_sel:DWORD src1_sel:WORD_1\n"
    "v_lshlrev_b32_sdwa v206, 1, v181 dst_sel:DWORD dst_unused:UNUSED_PAD src0_sel:DWORD src1_sel:WORD_1\n"
    "v_lshlrev_b32_sdwa v191, 1, v182 dst_sel:DWORD dst_unused:UNUSED_PAD src0_sel:DWORD src1_sel:WORD_1\n"
    "v_fma_mix_f32 v234, v192, v232, 0 op_sel_hi:[1,1,0]\n"
    "v_mfma_f32_16x16x32_bf16 v[172:175], v[56:59], a[232:235], v[172:175]\n"
    "ds_read_u16_d16_hi v204, v204\n"
    "v_or_b32_e32 v199, v199, v207\n"
    "v_lshlrev_b32_sdwa v207, 1, v183 dst_sel:DWORD dst_unused:UNUSED_PAD src0_sel:DWORD src1_sel:WORD_1\n"
    "v_fma_mix_f32 v232, v192, v232, 0 op_sel:[1,1,0] op_sel_hi:[1,1,0]\n"
    "v_mfma_f32_16x16x32_bf16 v[176:179], a[88:91], a[236:239], 0\n"
    "s_waitcnt lgkmcnt(7)\n"
    "ds_read_u16 v189, v189\n"
    "v_cvt_pk_bf16_f32 v192, v234, v232\n"
    "v_mfma_f32_16x16x32_bf16 v[176:179], a[136:139], a[240:243], v[176:179]\n"
    "ds_read_u16_d16_hi v205, v205\n"
    "v_pk_fma_f16 v232, v196, v196, s48\n"
    "v_mfma_f32_16x16x32_bf16 v[176:179], a[52:55], a[244:247], v[176:179]\n"
    "s_waitcnt lgkmcnt(8)\n"
    "v_pk_mul_f16 v232, v196, v232\n"
    "v_or_b32_e32 v184, v184, v200\n"
    "v_mfma_f32_16x16x32_bf16 v[180:183], a[92:95], a[236:239], 0\n"
    "ds_read_u16 v190, v190\n"
    "v_exp_f16_e32 v234, v232\n"
    "v_mfma_f32_16x16x32_bf16 v[180:183], a[140:143], a[240:243], v[180:183]\n"
    "s_waitcnt lgkmcnt(7)\n"
    "ds_read_u16_d16_hi v206, v206\n"
    "v_exp_f16_sdwa v234, v232 dst_sel:WORD_1 dst_unused:UNUSED_PRESERVE src0_sel:WORD_1\n"
    "v_mfma_f32_16x16x32_bf16 v[180:183], v[56:59], a[244:247], v[180:183]\n"
    "v_pk_fma_f16 v234, v234, s49, s49\n"
    "v_or_b32_e64 v185, v185, v201\n"
    "v_mfma_f32_16x16x32_bf16 v[96:99], a[208:211], v[192:195], v[96:99]\n"
    "ds_read_u16 v191, v191\n"
    "v_rcp_f16_e32 v232, v234\n"
    "v_mfma_f32_16x16x32_bf16 v[100:103], a[212:215], v[192:195], v[100:103]\n"
    "s_waitcnt lgkmcnt(7)\n"
    "ds_read_u16_d16_hi v207, v207\n"
    "v_rcp_f16_sdwa v232, v234 dst_sel:WORD_1 dst_unused:UNUSED_PRESERVE src0_sel:WORD_1\n"
    "v_mfma_f32_16x16x32_bf16 v[112:115], a[216:219], v[192:195], v[112:115]\n"
    "v_fma_mix_f32 v234, v196, v232, 0 op_sel_hi:[1,1,0]\n"
    "v_fma_mix_f32 v232, v196, v232, 0 op_sel:[1,1,0] op_sel_hi:[1,1,0]\n"
    "v_mfma_f32_16x16x32_bf16 v[116:119], a[220:223], v[192:195], v[116:119]\n"
    "v_cvt_pk_bf16_f32 v196, v234, v232\n"
    "v_lshlrev_b32_sdwa v201, 1, v171 dst_sel:DWORD dst_unused:UNUSED_PAD src0_sel:DWORD src1_sel:WORD_1\n"
    "v_mfma_f32_16x16x32_bf16 v[128:131], v[40:43], v[192:195], v[128:131]\n"
    "ds_read_u16_d16_hi v201, v201\n"
    "v_or_b32_e32 v186, v186, v202\n"
    "v_mfma_f32_16x16x32_bf16 v[132:135], v[44:47], v[192:195], v[132:135]\n"
    "s_waitcnt lgkmcnt(7)\n"
    "v_cvt_pk_f16_f32 v192, v168, v169\n"
    "v_lshlrev_b32_sdwa v193, 1, v170 dst_sel:DWORD dst_unused:UNUSED_PAD src0_sel:DWORD src1_sel:WORD_1\n"
    "v_mfma_f32_16x16x32_bf16 v[104:107], a[208:211], v[196:199], v[104:107]\n"
    "ds_read_u16 v193, v193\n"
    "v_lshlrev_b32_sdwa v194, 1, v172 dst_sel:DWORD dst_unused:UNUSED_PAD src0_sel:DWORD src1_sel:WORD_1\n"
    "v_mfma_f32_16x16x32_bf16 v[108:111], a[212:215], v[196:199], v[108:111]\n"
    "s_nop 0\n"
    "ds_read_u16 v194, v194\n"
    "buffer_store_dwordx4 v[160:163], v245, s[44:47], s88 offen offset:128 sc1\n"
    "v_mfma_f32_16x16x32_bf16 v[120:123], a[216:219], v[196:199], v[120:123]\n"
    "s_waitcnt lgkmcnt(7)\n"
    "v_lshlrev_b32_sdwa v202, 1, v173 dst_sel:DWORD dst_unused:UNUSED_PAD src0_sel:DWORD src1_sel:WORD_1\n"
    "v_lshlrev_b32_sdwa v195, 1, v174 dst_sel:DWORD dst_unused:UNUSED_PAD src0_sel:DWORD src1_sel:WORD_1\n"
    "v_mfma_f32_16x16x32_bf16 v[124:127], a[220:223], v[196:199], v[124:127]\n"
    "ds_read_u16_d16_hi v202, v202\n"
    "ds_read_u16 v195, v195\n"
    "v_mfma_f32_16x16x32_bf16 v[136:139], v[40:43], v[196:199], v[136:139]\n"
    "v_or_b32_e32 v187, v187, v203\n"
    "v_lshlrev_b32_sdwa v203, 1, v175 dst_sel:DWORD dst_unused:UNUSED_PAD src0_sel:DWORD src1_sel:WORD_1\n"
    "v_mfma_f32_16x16x32_bf16 v[140:143], v[44:47], v[196:199], v[140:143]\n"
    "s_waitcnt lgkmcnt(7)\n"
    "ds_read_u16_d16_hi v203, v203\n"
    "v_cvt_pk_f16_f32 v196, v176, v177\n"
    "v_mfma_f32_16x16x32_bf16 v[168:171], a[96:99], a[224:227], 0\n"
    "v_lshlrev_b32_sdwa v197, 1, v178 dst_sel:DWORD dst_unused:UNUSED_PAD src0_sel:DWORD src1_sel:WORD_1\n"
    "v_lshlrev_b32_sdwa v198, 1, v180 dst_sel:DWORD dst_unused:UNUSED_PAD src0_sel:DWORD src1_sel:WORD_1\n"
    "v_mfma_f32_16x16x32_bf16 v[168:171], a[144:147], a[228:231], v[168:171]\n"
    "ds_read_u16 v197, v197\n"
    "v_lshlrev_b32_sdwa v199, 1, v182 dst_sel:DWORD dst_unused:UNUSED_PAD src0_sel:DWORD src1_sel:WORD_1\n"
    "v_mfma_f32_16x16x32_bf16 v[168:171], a[56:59], a[232:235], v[168:171]\n"
    "s_waitcnt lgkmcnt(7)\n"
    "v_or_b32_e32 v188, v188, v204\n"
    "v_or_b32_e64 v189, v189, v205\n"
    "v_mfma_f32_16x16x32_bf16 v[172:175], a[100:103], a[224:227], 0\n"
    "ds_read_u16 v198, v198\n"
    "v_lshlrev_b32_sdwa v205, 1, v179 dst_sel:DWORD dst_unused:UNUSED_PAD src0_sel:DWORD src1_sel:WORD_1\n"
    "v_mfma_f32_16x16x32_bf16 v[172:175], a[148:151], a[228:231], v[172:175]\n"
    "ds_read_u16_d16_hi v205, v205\n"
    "v_cvt_pk_bf16_f32 v96, v96, v97\n"
    "v_mfma_f32_16x16x32_bf16 v[172:175], a[60:63], a[232:235], v[172:175]\n"
    "v_cvt_pk_bf16_f32 v97, v98, v99\n"
    "v_cvt_pk_bf16_f32 v98, v100, v101\n"
    "v_mfma_f32_16x16x32_bf16 v[176:179], a[96:99], a[236:239], 0\n"
    "s_waitcnt lgkmcnt(7)\n"
    "ds_read_u16 v199, v199\n"
    "v_cvt_pk_bf16_f32 v99, v102, v103\n"
    "v_mfma_f32_16x16x32_bf16 v[176:179], a[144:147], a[240:243], v[176:179]\n"
    "v_or_b32_e32 v190, v190, v206\n"
    "v_lshlrev_b32_sdwa v206, 1, v181 dst_sel:DWORD dst_unused:UNUSED_PAD src0_sel:DWORD src1_sel:WORD_1\n"
    "v_mfma_f32_16x16x32_bf16 v[176:179], a[56:59], a[244:247], v[176:179]\n"
    "v_cvt_pk_bf16_f32 v112, v112, v113\n"
    "v_cvt_pk_bf16_f32 v113, v114, v115\n"
    "v_mfma_f32_16x16x32_bf16 v[100:103], a[4:7], v[184:187], 0\n"
    "s_waitcnt lgkmcnt(6)\n"
    "ds_write_b128 v243, v[96:99] offset:24640\n"
    "v_mfma_f32_16x16x32_bf16 v[96:99], a[0:3], v[184:187], 0\n"
    "s_nop 0\n"
    "v_cvt_pk_bf16_f32 v114, v116, v117\n"
    "v_cvt_pk_bf16_f32 v115, v118, v119\n"
    "v_cvt_pk_bf16_f32 v128, v128, v129\n"
    "v_cvt_pk_bf16_f32 v129, v130, v131\n"
    "v_or_b32_e32 v191, v191, v207\n"
    "v_mfma_f32_16x16x32_bf16 v[116:119], a[12:15], v[184:187], 0\n"
    "s_waitcnt lgkmcnt(5)\n"
    "ds_read_u16_d16_hi v206, v206\n"
    "ds_write_b128 v243, v[112:115] offset:26688\n"
    "v_mfma_f32_16x16x32_bf16 v[112:115], a[8:11], v[184:187], 0\n"
    "v_lshlrev_b32_sdwa v207, 1, v183 dst_sel:DWORD dst_unused:UNUSED_PAD src0_sel:DWORD src1_sel:WORD_1\n"
    "v_cvt_pk_bf16_f32 v130, v132, v133\n"
    "v_mfma_f32_16x16x32_bf16 v[180:183], a[100:103], a[236:239], 0\n"
    "v_or_b32_e64 v193, v193, v201\n"
    "v_cvt_pk_bf16_f32 v131, v134, v135\n"
    "v_mfma_f32_16x16x32_bf16 v[180:183], a[148:151], a[240:243], v[180:183]\n"
    "s_nop 0\n"
    "ds_read_u16_d16_hi v207, v207\n"
    "ds_write_b128 v243, v[128:131] offset:28736\n"
    "v_mfma_f32_16x16x32_bf16 v[180:183], a[60:63], a[244:247], v[180:183]\n"
    "s_waitcnt lgkmcnt(6)\n"
    "v_cvt_pk_bf16_f32 v104, v104, v105\n"
    "v_cvt_pk_bf16_f32 v105, v106, v107\n"
    "v_mfma_f32_16x16x32_bf16 v[128:131], v[0:3], v[184:187], 0\n"
    "v_cvt_pk_bf16_f32 v106, v108, v109\n"
    "v_cvt_pk_bf16_f32 v107, v110, v111\n"
    "v_mfma_f32_16x16x32_bf16 v[132:135], v[4:7], v[184:187], 0\n"
    "ds_write_b128 v243, v[104:107] offset:25664\n"
    "v_mfma_f32_16x16x32_bf16 v[104:107], a[0:3], v[188:191], 0\n"
    "v_or_b32_e32 v194, v194, v202\n"
    "v_cvt_pk_bf16_f32 v120, v120, v121\n"
    "v_mfma_f32_16x16x32_bf16 v[108:111], a[4:7], v[188:191], 0\n"
    "s_waitcnt lgkmcnt(3)\n"
    "v_cvt_pk_bf16_f32 v121, v122, v123\n"
    "v_cvt_pk_bf16_f32 v122, v124, v125\n"
    "v_cvt_pk_bf16_f32 v123, v126, v127\n"
    "ds_write_b128 v243, v[120:123] offset:27712\n"
    "v_mfma_f32_16x16x32_bf16 v[120:123], a[8:11], v[188:191], 0\n"
    "v_cvt_pk_bf16_f32 v136, v136, v137\n"
    "v_pk_fma_f16 v232, v192, v192, s48\n"
    "v_mfma_f32_16x16x32_bf16 v[124:127], a[12:15], v[188:191], 0\n"
    "s_waitcnt lgkmcnt(2)\n"
    "v_cvt_pk_bf16_f32 v137, v138, v139\n"
    "v_cvt_pk_bf16_f32 v138, v140, v141\n"
    "v_or_b32_e32 v195, v195, v203\n"
    "v_cvt_pk_bf16_f32 v139, v142, v143\n"
    "ds_write_b128 v243, v[136:139] offset:29760\n"
    "v_pk_mul_f16 v232, v192, v232\n"
    "v_mfma_f32_16x16x32_bf16 v[136:139], v[0:3], v[188:191], 0\n"
    "v_exp_f16_e64 v234, v232\n"
    "v_mfma_f32_16x16x32_bf16 v[140:143], v[4:7], v[188:191], 0\n"
    "v_exp_f16_sdwa v234, v232 dst_sel:WORD_1 dst_unused:UNUSED_PRESERVE src0_sel:WORD_1\n"
    "v_or_b32_e64 v197, v197, v205\n"
    "v_pk_fma_f16 v234, v234, s49, s49\n"
    "v_lshlrev_b32_sdwa v184, 1, v168 dst_sel:DWORD dst_unused:UNUSED_PAD src0_sel:DWORD src1_sel:WORD_1\n"
    "v_lshlrev_b32_sdwa v200, 1, v169 dst_sel:DWORD dst_unused:UNUSED_PAD src0_sel:DWORD src1_sel:WORD_1\n"
    "v_lshlrev_b32_sdwa v185, 1, v170 dst_sel:DWORD dst_unused:UNUSED_PAD src0_sel:DWORD src1_sel:WORD_1\n"
    "v_lshlrev_b32_sdwa v201, 1, v171 dst_sel:DWORD dst_unused:UNUSED_PAD src0_sel:DWORD src1_sel:WORD_1\n"
    "v_mfma_f32_16x16x32_bf16 v[168:171], a[104:107], a[224:227], 0\n"
    "ds_read_u16 v184, v184\n"
    "ds_read_u16_d16_hi v200, v200\n"
    "v_mfma_f32_16x16x32_bf16 v[168:171], a[152:155], a[228:231], v[168:171]\n"
    "ds_read_u16 v185, v185\n"
    "ds_read_u16_d16_hi v201, v201\n"
    "v_lshlrev_b32_sdwa v186, 1, v172 dst_sel:DWORD dst_unused:UNUSED_PAD src0_sel:DWORD src1_sel:WORD_1\n"
    "v_lshlrev_b32_sdwa v202, 1, v173 dst_sel:DWORD dst_unused:UNUSED_PAD src0_sel:DWORD src1_sel:WORD_1\n"
    "v_lshlrev_b32_sdwa v187, 1, v174 dst_sel:DWORD dst_unused:UNUSED_PAD src0_sel:DWORD src1_sel:WORD_1\n"
    "v_lshlrev_b32_sdwa v203, 1, v175 dst_sel:DWORD dst_unused:UNUSED_PAD src0_sel:DWORD src1_sel:WORD_1\n"
    "v_rcp_f16_e32 v232, v234\n"
    "v_mfma_f32_16x16x32_bf16 v[168:171], a[64:67], a[232:235], v[168:171]\n"
    "s_waitcnt lgkmcnt(4)\n"
    "ds_read_u16 v186, v186\n"
    "v_or_b32_e32 v198, v198, v206\n"
    "v_rcp_f16_sdwa v232, v234 dst_sel:WORD_1 dst_unused:UNUSED_PRESERVE src0_sel:WORD_1\n"
    "v_mfma_f32_16x16x32_bf16 v[172:175], a[108:111], a[224:227], 0\n"
    "s_barrier\n"
    "v_mfma_f32_16x16x32_bf16 v[172:175], a[156:159], a[228:231], v[172:175]\n"
    "ds_read_u16_d16_hi v202, v202\n"
    "ds_read_u16 v187, v187\n"
    "v_lshlrev_b32_sdwa v188, 1, v176 dst_sel:DWORD dst_unused:UNUSED_PAD src0_sel:DWORD src1_sel:WORD_1\n"
    "v_lshlrev_b32_sdwa v204, 1, v177 dst_sel:DWORD dst_unused:UNUSED_PAD src0_sel:DWORD src1_sel:WORD_1\n"
    "v_lshlrev_b32_sdwa v189, 1, v178 dst_sel:DWORD dst_unused:UNUSED_PAD src0_sel:DWORD src1_sel:WORD_1\n"
    "v_lshlrev_b32_sdwa v205, 1, v179 dst_sel:DWORD dst_unused:UNUSED_PAD src0_sel:DWORD src1_sel:WORD_1\n"
    "v_lshlrev_b32_sdwa v190, 1, v180 dst_sel:DWORD dst_unused:UNUSED_PAD src0_sel:DWORD src1_sel:WORD_1\n"
    "v_lshlrev_b32_sdwa v206, 1, v181 dst_sel:DWORD dst_unused:UNUSED_PAD src0_sel:DWORD src1_sel:WORD_1\n"
    "v_lshlrev_b32_sdwa v191, 1, v182 dst_sel:DWORD dst_unused:UNUSED_PAD src0_sel:DWORD src1_sel:WORD_1\n"
    "v_fma_mix_f32 v234, v192, v232, 0 op_sel_hi:[1,1,0]\n"
    "v_mfma_f32_16x16x32_bf16 v[172:175], a[68:71], a[232:235], v[172:175]\n"
    "ds_read_u16_d16_hi v203, v203\n"
    "ds_read_u16 v188, v188\n"
    "v_or_b32_e32 v199, v199, v207\n"
    "v_lshlrev_b32_sdwa v207, 1, v183 dst_sel:DWORD dst_unused:UNUSED_PAD src0_sel:DWORD src1_sel:WORD_1\n"
    "v_fma_mix_f32 v232, v192, v232, 0 op_sel:[1,1,0] op_sel_hi:[1,1,0]\n"
    "v_mfma_f32_16x16x32_bf16 v[176:179], a[104:107], a[236:239], 0\n"
    "s_waitcnt lgkmcnt(7)\n"
    "ds_read_u16_d16_hi v204, v204\n"
    "ds_read_u16 v189, v189\n"
    "v_cvt_pk_bf16_f32 v192, v234, v232\n"
    "v_mfma_f32_16x16x32_bf16 v[176:179], a[152:155], a[240:243], v[176:179]\n"
    "v_pk_fma_f16 v232, v196, v196, s48\n"
    "v_or_b32_e32 v184, v184, v200\n"
    "v_mfma_f32_16x16x32_bf16 v[176:179], a[64:67], a[244:247], v[176:179]\n"
    "s_waitcnt lgkmcnt(7)\n"
    "ds_read_u16_d16_hi v205, v205\n"
    "v_pk_mul_f16 v232, v196, v232\n"
    "v_mfma_f32_16x16x32_bf16 v[180:183], a[108:111], a[236:239], 0\n"
    "v_exp_f16_e32 v234, v232\n"
    "v_mfma_f32_16x16x32_bf16 v[180:183], a[156:159], a[240:243], v[180:183]\n"
    "ds_read_u16 v190, v190\n"
    "v_exp_f16_sdwa v234, v232 dst_sel:WORD_1 dst_unused:UNUSED_PRESERVE src0_sel:WORD_1\n"
    "v_mfma_f32_16x16x32_bf16 v[180:183], a[68:71], a[244:247], v[180:183]\n"
    "s_waitcnt lgkmcnt(7)\n"
    "ds_read_u16_d16_hi v206, v206\n"
    "v_pk_fma_f16 v234, v234, s49, s49\n"
    "v_mfma_f32_16x16x32_bf16 v[96:99], a[16:19], v[192:195], v[96:99]\n"
    "v_rcp_f16_e64 v232, v234\n"
    "v_mfma_f32_16x16x32_bf16 v[100:103], a[20:23], v[192:195], v[100:103]\n"
    "ds_read_u16 v191, v191\n"
    "v_rcp_f16_sdwa v232, v234 dst_sel:WORD_1 dst_unused:UNUSED_PRESERVE src0_sel:WORD_1\n"
    "v_mfma_f32_16x16x32_bf16 v[112:115], a[24:27], v[192:195], v[112:115]\n"
    "v_fma_mix_f32 v234, v196, v232, 0 op_sel_hi:[1,1,0]\n"
    "v_fma_mix_f32 v232, v196, v232, 0 op_sel:[1,1,0] op_sel_hi:[1,1,0]\n"
    "v_mfma_f32_16x16x32_bf16 v[116:119], a[28:31], v[192:195], v[116:119]\n"
    "s_waitcnt lgkmcnt(7)\n"
    "ds_read_u16_d16_hi v207, v207\n"
    "v_cvt_pk_bf16_f32 v196, v234, v232\n"
    "v_mfma_f32_16x16x32_bf16 v[128:131], v[8:11], v[192:195], v[128:131]\n"
    "v_or_b32_e32 v185, v185, v201\n"
    "v_lshlrev_b32_sdwa v201, 1, v171 dst_sel:DWORD dst_unused:UNUSED_PAD src0_sel:DWORD src1_sel:WORD_1\n"
    "v_mfma_f32_16x16x32_bf16 v[132:135], v[12:15], v[192:195], v[132:135]\n"
    "ds_read_u16_d16_hi v201, v201\n"
    "v_cvt_pk_f16_f32 v192, v168, v169\n"
    "v_mfma_f32_16x16x32_bf16 v[104:107], a[16:19], v[196:199], v[104:107]\n"
    "s_waitcnt lgkmcnt(7)\n"
    "v_lshlrev_b32_sdwa v193, 1, v170 dst_sel:DWORD dst_unused:UNUSED_PAD src0_sel:DWORD src1_sel:WORD_1\n"
    "v_lshlrev_b32_sdwa v194, 1, v172 dst_sel:DWORD dst_unused:UNUSED_PAD src0_sel:DWORD src1_sel:WORD_1\n"
    "v_mfma_f32_16x16x32_bf16 v[108:111], a[20:23], v[196:199], v[108:111]\n"
    "s_waitcnt vmcnt(6)\n"
    "ds_read_u16 v193, v193\n"
    "v_or_b32_e64 v186, v186, v202\n"
    "v_mfma_f32_16x16x32_bf16 v[120:123], a[24:27], v[196:199], v[120:123]\n"
    "ds_read_u16 v194, v194\n"
    "v_lshlrev_b32_sdwa v202, 1, v173 dst_sel:DWORD dst_unused:UNUSED_PAD src0_sel:DWORD src1_sel:WORD_1\n"
    "v_mfma_f32_16x16x32_bf16 v[124:127], a[28:31], v[196:199], v[124:127]\n"
    "v_lshlrev_b32_sdwa v195, 1, v174 dst_sel:DWORD dst_unused:UNUSED_PAD src0_sel:DWORD src1_sel:WORD_1\n"
    "v_or_b32_e64 v187, v187, v203\n"
    "v_mfma_f32_16x16x32_bf16 v[136:139], v[8:11], v[196:199], v[136:139]\n"
    "s_waitcnt lgkmcnt(7)\n"
    "ds_read_u16_d16_hi v202, v202\n"
    "v_lshlrev_b32_sdwa v203, 1, v175 dst_sel:DWORD dst_unused:UNUSED_PAD src0_sel:DWORD src1_sel:WORD_1\n"
    "v_mfma_f32_16x16x32_bf16 v[140:143], v[12:15], v[196:199], v[140:143]\n"
    "s_waitcnt vmcnt(5)\n"
    "ds_read_u16 v195, v195\n"
    "v_cvt_pk_f16_f32 v196, v176, v177\n"
    "v_smfmac_f32_32x32x32_bf16 v[112:127], a[248:251], v[152:159], v241\n"
    "v_lshlrev_b32_sdwa v197, 1, v178 dst_sel:DWORD dst_unused:UNUSED_PAD src0_sel:DWORD src1_sel:WORD_1\n"
    "v_lshlrev_b32_sdwa v198, 1, v180 dst_sel:DWORD dst_unused:UNUSED_PAD src0_sel:DWORD src1_sel:WORD_1\n"
    "v_lshlrev_b32_sdwa v199, 1, v182 dst_sel:DWORD dst_unused:UNUSED_PAD src0_sel:DWORD src1_sel:WORD_1\n"
    "v_or_b32_e64 v188, v188, v204\n"
    "v_or_b32_e32 v189, v189, v205\n"
    "v_smfmac_f32_32x32x32_bf16 v[96:111], a[248:251], v[144:151], v241\n"
    "s_waitcnt lgkmcnt(7)\n"
    "ds_read_b128 v[152:155], v242 offset:24640\n"
    "ds_read_b128 v[156:159], v242 offset:25664\n"
    "v_lshlrev_b32_sdwa v205, 1, v179 dst_sel:DWORD dst_unused:UNUSED_PAD src0_sel:DWORD src1_sel:WORD_1\n"
    "v_or_b32_e32 v190, v190, v206\n"
    "v_lshlrev_b32_sdwa v206, 1, v181 dst_sel:DWORD dst_unused:UNUSED_PAD src0_sel:DWORD src1_sel:WORD_1\n"
    "v_smfmac_f32_32x32x32_bf16 v[128:143], a[248:251], v[208:215], v241\n"
    "s_waitcnt lgkmcnt(7)\n"
    "ds_read_u16_d16_hi v203, v203\n"
    "ds_read_u16 v197, v197\n"
    "v_or_b32_e64 v191, v191, v207\n"
    "v_lshlrev_b32_sdwa v207, 1, v183 dst_sel:DWORD dst_unused:UNUSED_PAD src0_sel:DWORD src1_sel:WORD_1\n"
    "v_pk_fma_f16 v232, v192, v192, s48\n"
    "v_mfma_f32_16x16x32_bf16 v[168:171], a[112:115], a[224:227], 0\n"
    "v_pk_mul_f16 v232, v192, v232\n"
    "v_exp_f16_e64 v234, v232\n"
    "v_mfma_f32_16x16x32_bf16 v[168:171], a[160:163], a[228:231], v[168:171]\n"
    "ds_read_u16_d16_hi v205, v205\n"
    "v_exp_f16_sdwa v234, v232 dst_sel:WORD_1 dst_unused:UNUSED_PRESERVE src0_sel:WORD_1\n"
    "v_mfma_f32_16x16x32_bf16 v[168:171], a[72:75], a[232:235], v[168:171]\n"
    "s_waitcnt lgkmcnt(8)\n"
    "ds_read_u16 v198, v198\n"
    "v_or_b32_e32 v193, v193, v201\n"
    "v_mfma_f32_16x16x32_bf16 v[172:175], a[116:119], a[224:227], 0\n"
    "v_pk_fma_f16 v234, v234, s49, s49\n"
    "v_rcp_f16_e64 v232, v234\n"
    "v_mfma_f32_16x16x32_bf16 v[172:175], a[164:167], a[228:231], v[172:175]\n"
    "ds_read_u16_d16_hi v206, v206\n"
    "v_lshlrev_b32_sdwa v200, 1, v169 dst_sel:DWORD dst_unused:UNUSED_PAD src0_sel:DWORD src1_sel:WORD_1\n"
    "v_mfma_f32_16x16x32_bf16 v[172:175], v[52:55], a[232:235], v[172:175]\n"
    "s_waitcnt lgkmcnt(8)\n"
    "ds_read_u16 v199, v199\n"
    "v_or_b32_e32 v194, v194, v202\n"
    "v_mfma_f32_16x16x32_bf16 v[176:179], a[112:115], a[236:239], 0\n"
    "s_nop 0\n"
    "v_lshlrev_b32_sdwa v201, 1, v171 dst_sel:DWORD dst_unused:UNUSED_PAD src0_sel:DWORD src1_sel:WORD_1\n"
    "v_rcp_f16_sdwa v232, v234 dst_sel:WORD_1 dst_unused:UNUSED_PRESERVE src0_sel:WORD_1\n"
    "v_mfma_f32_16x16x32_bf16 v[176:179], a[160:163], a[240:243], v[176:179]\n"
    "s_waitcnt lgkmcnt(6)\n"
    "ds_read_u16_d16_hi v207, v207\n"
    "v_lshlrev_b32_sdwa v202, 1, v173 dst_sel:DWORD dst_unused:UNUSED_PAD src0_sel:DWORD src1_sel:WORD_1\n"
    "v_smfmac_f32_32x32x32_bf16 v[96:111], a[248:251], v[152:159], v241\n"
    "ds_read_b128 v[152:155], v242 offset:26688\n"
    "v_fma_mix_f32 v234, v192, v232, 0 op_sel_hi:[1,1,0]\n"
    "v_fma_mix_f32 v232, v192, v232, 0 op_sel:[1,1,0] op_sel_hi:[1,1,0]\n"
    "v_cvt_pk_bf16_f32 v192, v234, v232\n"
    "v_pk_fma_f16 v232, v196, v196, s48\n"
    "v_mfma_f32_16x16x32_bf16 v[176:179], a[72:75], a[244:247], v[176:179]\n"
    "s_waitcnt lgkmcnt(7)\n"
    "ds_read_b128 v[156:159], v242 offset:27712\n"
    "ds_read_u16_d16_hi v200, v200\n"
    "v_mfma_f32_16x16x32_bf16 v[180:183], a[116:119], a[236:239], 0\n"
    "v_or_b32_e32 v195, v195, v203\n"
    "v_lshlrev_b32_sdwa v203, 1, v175 dst_sel:DWORD dst_unused:UNUSED_PAD src0_sel:DWORD src1_sel:WORD_1\n"
    "v_mfma_f32_16x16x32_bf16 v[180:183], a[164:167], a[240:243], v[180:183]\n"
    "ds_read_u16_d16_hi v201, v201\n"
    "v_lshlrev_b32_sdwa v204, 1, v177 dst_sel:DWORD dst_unused:UNUSED_PAD src0_sel:DWORD src1_sel:WORD_1\n"
    "v_mfma_f32_16x16x32_bf16 v[180:183], v[52:55], a[244:247], v[180:183]\n"
    "v_pk_mul_f16 v232, v196, v232\n"
    "v_exp_f16_e32 v234, v232\n"
    "v_mfma_f32_16x16x32_bf16 v[96:99], a[32:35], v[184:187], v[96:99]\n"
    "s_waitcnt lgkmcnt(8)\n"
    "ds_read_u16_d16_hi v202, v202\n"
    "v_or_b32_e64 v197, v197, v205\n"
    "v_mfma_f32_16x16x32_bf16 v[100:103], a[36:39], v[184:187], v[100:103]\n"
    "ds_read_u16_d16_hi v203, v203\n"
    "v_lshlrev_b32_sdwa v205, 1, v179 dst_sel:DWORD dst_unused:UNUSED_PAD src0_sel:DWORD src1_sel:WORD_1\n"
    "v_mfma_f32_16x16x32_bf16 v[112:115], a[40:43], v[184:187], v[112:115]\n"
    "s_nop 0\n"
    "v_exp_f16_sdwa v234, v232 dst_sel:WORD_1 dst_unused:UNUSED_PRESERVE src0_sel:WORD_1\n"
    "v_mfma_f32_16x16x32_bf16 v[116:119], a[44:47], v[184:187], v[116:119]\n"
    "s_waitcnt lgkmcnt(8)\n"
    "ds_read_u16_d16_hi v204, v204\n"
    "v_or_b32_e64 v198, v198, v206\n"
    "v_mfma_f32_16x16x32_bf16 v[128:131], v[16:19], v[184:187], v[128:131]\n"
    "v_lshlrev_b32_sdwa v206, 1, v181 dst_sel:DWORD dst_unused:UNUSED_PAD src0_sel:DWORD src1_sel:WORD_1\n"
    "v_pk_fma_f16 v234, v234, s49, s49\n"
    "v_mfma_f32_16x16x32_bf16 v[132:135], v[20:23], v[184:187], v[132:135]\n"
    "s_nop 0\n"
    "ds_read_u16_d16_hi v205, v205\n"
    "v_lshlrev_b32_sdwa v184, 1, v168 dst_sel:DWORD dst_unused:UNUSED_PAD src0_sel:DWORD src1_sel:WORD_1\n"
    "v_mfma_f32_16x16x32_bf16 v[104:107], a[32:35], v[188:191], v[104:107]\n"
    "s_waitcnt lgkmcnt(7)\n"
    "v_lshlrev_b32_sdwa v185, 1, v170 dst_sel:DWORD dst_unused:UNUSED_PAD src0_sel:DWORD src1_sel:WORD_1\n"
    "v_lshlrev_b32_sdwa v186, 1, v172 dst_sel:DWORD dst_unused:UNUSED_PAD src0_sel:DWORD src1_sel:WORD_1\n"
    "v_mfma_f32_16x16x32_bf16 v[108:111], a[36:39], v[188:191], v[108:111]\n"
    "ds_read_u16 v184, v184\n"
    "v_lshlrev_b32_sdwa v187, 1, v174 dst_sel:DWORD dst_unused:UNUSED_PAD src0_sel:DWORD src1_sel:WORD_1\n"
    "v_mfma_f32_16x16x32_bf16 v[120:123], a[40:43], v[188:191], v[120:123]\n"
    "ds_read_u16 v185, v185\n"
    "v_or_b32_e32 v199, v199, v207\n"
    "v_mfma_f32_16x16x32_bf16 v[124:127], a[44:47], v[188:191], v[124:127]\n"
    "s_waitcnt lgkmcnt(8)\n"
    "v_lshlrev_b32_sdwa v207, 1, v183 dst_sel:DWORD dst_unused:UNUSED_PAD src0_sel:DWORD src1_sel:WORD_1\n"
    "v_rcp_f16_e64 v232, v234\n"
    "v_mfma_f32_16x16x32_bf16 v[136:139], v[16:19], v[188:191], v[136:139]\n"
    "ds_read_u16 v186, v186\n"
    "v_rcp_f16_sdwa v232, v234 dst_sel:WORD_1 dst_unused:UNUSED_PRESERVE src0_sel:WORD_1\n"
    "v_mfma_f32_16x16x32_bf16 v[140:143], v[20:23], v[188:191], v[140:143]\n"
    "ds_read_u16 v187, v187\n"
    "v_lshlrev_b32_sdwa v188, 1, v176 dst_sel:DWORD dst_unused:UNUSED_PAD src0_sel:DWORD src1_sel:WORD_1\n"
    "v_smfmac_f32_32x32x32_bf16 v[112:127], a[248:251], v[152:159], v241\n"
    "v_lshlrev_b32_sdwa v189, 1, v178 dst_sel:DWORD dst_unused:UNUSED_PAD src0_sel:DWORD src1_sel:WORD_1\n"
    "v_lshlrev_b32_sdwa v190, 1, v180 dst_sel:DWORD dst_unused:UNUSED_PAD src0_sel:DWORD src1_sel:WORD_1\n"
    "v_lshlrev_b32_sdwa v191, 1, v182 dst_sel:DWORD dst_unused:UNUSED_PAD src0_sel:DWORD src1_sel:WORD_1\n"
    "v_fma_mix_f32 v234, v196, v232, 0 op_sel_hi:[1,1,0]\n"
    "v_fma_mix_f32 v232, v196, v232, 0 op_sel:[1,1,0] op_sel_hi:[1,1,0]\n"
    "v_mfma_f32_16x16x32_bf16 v[168:171], a[120:123], a[224:227], 0\n"
    "ds_read_u16 v188, v188\n"
    "v_cvt_pk_bf16_f32 v196, v234, v232\n"
    "v_mfma_f32_16x16x32_bf16 v[168:171], a[168:171], a[228:231], v[168:171]\n"
    "ds_read_b128 v[152:155], v242 offset:28736\n"
    "v_mfma_f32_16x16x32_bf16 v[168:171], a[76:79], a[232:235], v[168:171]\n"
    "ds_read_b128 v[156:159], v242 offset:29760\n"
    "v_mfma_f32_16x16x32_bf16 v[172:175], a[124:127], a[224:227], 0\n"
    "ds_read_u16 v189, v189\n"
    "v_mfma_f32_16x16x32_bf16 v[172:175], a[172:175], a[228:231], v[172:175]\n"
    "v_mfma_f32_16x16x32_bf16 v[172:175], v[60:63], a[232:235], v[172:175]\n"
    "ds_read_u16 v190, v190\n"
    "v_mfma_f32_16x16x32_bf16 v[176:179], a[120:123], a[236:239], 0\n"
    "s_waitcnt lgkmcnt(8)\n"
    "ds_read_u16_d16_hi v206, v206\n"
    "v_or_b32_e32 v184, v184, v200\n"
    "v_mfma_f32_16x16x32_bf16 v[176:179], a[168:171], a[240:243], v[176:179]\n"
    "s_nop 0\n"
    "v_mfma_f32_16x16x32_bf16 v[176:179], a[76:79], a[244:247], v[176:179]\n"
    "s_waitcnt lgkmcnt(8)\n"
    "ds_read_u16 v191, v191\n"
    "v_or_b32_e32 v185, v185, v201\n"
    "v_mfma_f32_16x16x32_bf16 v[180:183], a[124:127], a[236:239], 0\n"
    "v_lshlrev_b32_sdwa v201, 1, v171 dst_sel:DWORD dst_unused:UNUSED_PAD src0_sel:DWORD src1_sel:WORD_1\n"
    "v_mfma_f32_16x16x32_bf16 v[180:183], a[172:175], a[240:243], v[180:183]\n"
    "s_waitcnt lgkmcnt(8)\n"
    "ds_read_u16_d16_hi v207, v207\n"
    "v_or_b32_e32 v186, v186, v202\n"
    "v_mfma_f32_16x16x32_bf16 v[180:183], v[60:63], a[244:247], v[180:183]\n"
    "s_waitcnt lgkmcnt(8)\n"
    "v_lshlrev_b32_sdwa v202, 1, v173 dst_sel:DWORD dst_unused:UNUSED_PAD src0_sel:DWORD src1_sel:WORD_1\n"
    "v_or_b32_e64 v187, v187, v203\n"
    "v_mfma_f32_16x16x32_bf16 v[96:99], a[176:179], v[192:195], v[96:99]\n"
    "ds_read_u16_d16_hi v201, v201\n"
    "v_lshlrev_b32_sdwa v203, 1, v175 dst_sel:DWORD dst_unused:UNUSED_PAD src0_sel:DWORD src1_sel:WORD_1\n"
    "v_mfma_f32_16x16x32_bf16 v[100:103], a[180:183], v[192:195], v[100:103]\n"
    "s_waitcnt lgkmcnt(7)\n"
    "ds_read_u16_d16_hi v202, v202\n"
    "v_or_b32_e32 v188, v188, v204\n"
    "v_mfma_f32_16x16x32_bf16 v[112:115], a[184:187], v[192:195], v[112:115]\n"
    "v_mfma_f32_16x16x32_bf16 v[116:119], a[188:191], v[192:195], v[116:119]\n"
    "ds_read_u16_d16_hi v203, v203\n"
    "v_mfma_f32_16x16x32_bf16 v[128:131], v[24:27], v[192:195], v[128:131]\n"
    "s_nop 0\n"
    "v_mfma_f32_16x16x32_bf16 v[132:135], v[28:31], v[192:195], v[132:135]\n"
    "s_waitcnt lgkmcnt(7)\n"
    "v_cvt_pk_f16_f32 v192, v168, v169\n"
    "v_lshlrev_b32_sdwa v193, 1, v170 dst_sel:DWORD dst_unused:UNUSED_PAD src0_sel:DWORD src1_sel:WORD_1\n"
    "v_mfma_f32_16x16x32_bf16 v[104:107], a[176:179], v[196:199], v[104:107]\n"
    "ds_read_u16 v193, v193\n"
    "v_lshlrev_b32_sdwa v194, 1, v172 dst_sel:DWORD dst_unused:UNUSED_PAD src0_sel:DWORD src1_sel:WORD_1\n"
    "v_mfma_f32_16x16x32_bf16 v[108:111], a[180:183], v[196:199], v[108:111]\n"
    "ds_read_u16 v194, v194\n"
    "v_lshlrev_b32_sdwa v195, 1, v174 dst_sel:DWORD dst_unused:UNUSED_PAD src0_sel:DWORD src1_sel:WORD_1\n"
    "v_mfma_f32_16x16x32_bf16 v[120:123], a[184:187], v[196:199], v[120:123]\n"
    "s_waitcnt lgkmcnt(7)\n"
    "ds_read_u16 v195, v195\n"
    "v_or_b32_e32 v189, v189, v205\n"
    "v_mfma_f32_16x16x32_bf16 v[124:127], a[188:191], v[196:199], v[124:127]\n"
    "v_lshlrev_b32_sdwa v205, 1, v179 dst_sel:DWORD dst_unused:UNUSED_PAD src0_sel:DWORD src1_sel:WORD_1\n"
    "v_or_b32_e64 v190, v190, v206\n"
    "v_mfma_f32_16x16x32_bf16 v[136:139], v[24:27], v[196:199], v[136:139]\n"
    "ds_read_u16_d16_hi v205, v205\n"
    "v_lshlrev_b32_sdwa v206, 1, v181 dst_sel:DWORD dst_unused:UNUSED_PAD src0_sel:DWORD src1_sel:WORD_1\n"
    "v_mfma_f32_16x16x32_bf16 v[140:143], v[28:31], v[196:199], v[140:143]\n"
    "s_nop 0\n"
    "v_cvt_pk_f16_f32 v196, v176, v177\n"
    "v_lshlrev_b32_sdwa v197, 1, v178 dst_sel:DWORD dst_unused:UNUSED_PAD src0_sel:DWORD src1_sel:WORD_1\n"
    "v_mfma_f32_16x16x32_bf16 v[96:99], a[192:195], v[184:187], v[96:99]\n"
    "s_waitcnt lgkmcnt(7)\n"
    "ds_read_u16 v197, v197\n"
    "v_lshlrev_b32_sdwa v198, 1, v180 dst_sel:DWORD dst_unused:UNUSED_PAD src0_sel:DWORD src1_sel:WORD_1\n"
    "v_smfmac_f32_32x32x32_bf16 v[128:143], a[248:251], v[152:159], v241\n"
    "ds_read_u16 v198, v198\n"
    "v_lshlrev_b32_sdwa v199, 1, v182 dst_sel:DWORD dst_unused:UNUSED_PAD src0_sel:DWORD src1_sel:WORD_1\n"
    "v_or_b32_e64 v191, v191, v207\n"
    "v_lshlrev_b32_sdwa v207, 1, v183 dst_sel:DWORD dst_unused:UNUSED_PAD src0_sel:DWORD src1_sel:WORD_1\n"
    "v_pk_fma_f16 v232, v192, v192, s48\n"
    "v_mfma_f32_16x16x32_bf16 v[100:103], a[196:199], v[184:187], v[100:103]\n"
    "ds_read_u16_d16_hi v206, v206\n"
    "v_pk_mul_f16 v232, v192, v232\n"
    "v_mfma_f32_16x16x32_bf16 v[112:115], a[200:203], v[184:187], v[112:115]\n"
    "v_exp_f16_e64 v234, v232\n"
    "v_mfma_f32_16x16x32_bf16 v[116:119], a[204:207], v[184:187], v[116:119]\n"
    "ds_read_u16 v199, v199\n"
    "v_exp_f16_sdwa v234, v232 dst_sel:WORD_1 dst_unused:UNUSED_PRESERVE src0_sel:WORD_1\n"
    "v_mfma_f32_16x16x32_bf16 v[128:131], v[32:35], v[184:187], v[128:131]\n"
    "v_pk_fma_f16 v234, v234, s49, s49\n"
    "v_rcp_f16_e32 v232, v234\n"
    "v_mfma_f32_16x16x32_bf16 v[132:135], v[36:39], v[184:187], v[132:135]\n"
    "s_waitcnt lgkmcnt(7)\n"
    "ds_read_u16_d16_hi v207, v207\n"
    "v_or_b32_e64 v193, v193, v201\n"
    "v_mfma_f32_16x16x32_bf16 v[104:107], a[192:195], v[188:191], v[104:107]\n"
    "s_waitcnt lgkmcnt(7)\n"
    "v_or_b32_e32 v194, v194, v202\n"
    "v_rcp_f16_sdwa v232, v234 dst_sel:WORD_1 dst_unused:UNUSED_PRESERVE src0_sel:WORD_1\n"
    "v_mfma_f32_16x16x32_bf16 v[108:111], a[196:199], v[188:191], v[108:111]\n"
    "v_fma_mix_f32 v234, v192, v232, 0 op_sel_hi:[1,1,0]\n"
    "v_fma_mix_f32 v232, v192, v232, 0 op_sel:[1,1,0] op_sel_hi:[1,1,0]\n"
    "v_mfma_f32_16x16x32_bf16 v[120:123], a[200:203], v[188:191], v[120:123]\n"
    "s_waitcnt lgkmcnt(6)\n"
    "v_or_b32_e32 v195, v195, v203\n"
    "v_cvt_pk_bf16_f32 v192, v234, v232\n"
    "v_mfma_f32_16x16x32_bf16 v[124:127], a[204:207], v[188:191], v[124:127]\n"
    "v_pk_fma_f16 v232, v196, v196, s48\n"
    "v_pk_mul_f16 v232, v196, v232\n"
    "v_mfma_f32_16x16x32_bf16 v[136:139], v[32:35], v[188:191], v[136:139]\n"
    "v_exp_f16_e64 v234, v232\n"
    "v_mfma_f32_16x16x32_bf16 v[140:143], v[36:39], v[188:191], v[140:143]\n"
    "s_waitcnt lgkmcnt(4)\n"
    "v_or_b32_e32 v197, v197, v205\n"
    "v_exp_f16_sdwa v234, v232 dst_sel:WORD_1 dst_unused:UNUSED_PRESERVE src0_sel:WORD_1\n"
    "v_mfma_f32_16x16x32_bf16 v[96:99], a[208:211], v[192:195], v[96:99]\n"
    "v_pk_fma_f16 v234, v234, s49, s49\n"
    "v_rcp_f16_e64 v232, v234\n"
    "v_mfma_f32_16x16x32_bf16 v[100:103], a[212:215], v[192:195], v[100:103]\n"
    "v_rcp_f16_sdwa v232, v234 dst_sel:WORD_1 dst_unused:UNUSED_PRESERVE src0_sel:WORD_1\n"
    "v_mfma_f32_16x16x32_bf16 v[112:115], a[216:219], v[192:195], v[112:115]\n"
    "s_waitcnt lgkmcnt(2)\n"
    "v_or_b32_e32 v198, v198, v206\n"
    "v_fma_mix_f32 v234, v196, v232, 0 op_sel_hi:[1,1,0]\n"
    "v_mfma_f32_16x16x32_bf16 v[116:119], a[220:223], v[192:195], v[116:119]\n"
    "v_fma_mix_f32 v232, v196, v232, 0 op_sel:[1,1,0] op_sel_hi:[1,1,0]\n"
    "v_cvt_pk_bf16_f32 v96, v96, v97\n"
    "v_mfma_f32_16x16x32_bf16 v[128:131], v[40:43], v[192:195], v[128:131]\n"
    "v_cvt_pk_bf16_f32 v196, v234, v232\n"
    "v_cvt_pk_bf16_f32 v97, v98, v99\n"
    "v_mfma_f32_16x16x32_bf16 v[132:135], v[44:47], v[192:195], v[132:135]\n"
    "v_cvt_pk_bf16_f32 v98, v100, v101\n"
    "s_waitcnt lgkmcnt(0)\n"
    "v_or_b32_e32 v199, v199, v207\n"
    "v_cvt_pk_bf16_f32 v99, v102, v103\n"
    "buffer_store_dwordx4 v[96:99], v245, s[44:47], s58 offen offset:0 sc1\n"
    "v_mfma_f32_16x16x32_bf16 v[104:107], a[208:211], v[196:199], v[104:107]\n"
    "v_cvt_pk_bf16_f32 v112, v112, v113\n"
    "v_cvt_pk_bf16_f32 v113, v114, v115\n"
    "v_mfma_f32_16x16x32_bf16 v[108:111], a[212:215], v[196:199], v[108:111]\n"
    "v_cvt_pk_bf16_f32 v114, v116, v117\n"
    "v_cvt_pk_bf16_f32 v115, v118, v119\n"
    "v_mfma_f32_16x16x32_bf16 v[120:123], a[216:219], v[196:199], v[120:123]\n"
    "buffer_store_dwordx4 v[112:115], v245, s[44:47], s58 offen offset:64 sc1\n"
    "v_mfma_f32_16x16x32_bf16 v[124:127], a[220:223], v[196:199], v[124:127]\n"
    "v_cvt_pk_bf16_f32 v128, v128, v129\n"
    "v_cvt_pk_bf16_f32 v129, v130, v131\n"
    "v_mfma_f32_16x16x32_bf16 v[136:139], v[40:43], v[196:199], v[136:139]\n"
    "v_cvt_pk_bf16_f32 v130, v132, v133\n"
    "v_cvt_pk_bf16_f32 v131, v134, v135\n"
    "v_mfma_f32_16x16x32_bf16 v[140:143], v[44:47], v[196:199], v[140:143]\n"
    "buffer_store_dwordx4 v[128:131], v245, s[44:47], s58 offen offset:128 sc1\n"
    "v_cvt_pk_bf16_f32 v104, v104, v105\n"
    "v_cvt_pk_bf16_f32 v105, v106, v107\n"
    "v_cvt_pk_bf16_f32 v106, v108, v109\n"
    "v_cvt_pk_bf16_f32 v107, v110, v111\n"
    "v_cvt_pk_bf16_f32 v120, v120, v121\n"
    "buffer_store_dwordx4 v[104:107], v245, s[44:47], s89 offen offset:0 sc1\n"
    "v_cvt_pk_bf16_f32 v121, v122, v123\n"
    "v_cvt_pk_bf16_f32 v122, v124, v125\n"
    "v_cvt_pk_bf16_f32 v123, v126, v127\n"
    "buffer_store_dwordx4 v[120:123], v245, s[44:47], s89 offen offset:64 sc1\n"
    "v_cvt_pk_bf16_f32 v136, v136, v137\n"
    "v_cvt_pk_bf16_f32 v137, v138, v139\n"
    "v_cvt_pk_bf16_f32 v138, v140, v141\n"
    "v_cvt_pk_bf16_f32 v139, v142, v143\n"
    "buffer_store_dwordx4 v[136:139], v245, s[44:47], s89 offen offset:128 sc1\n"
    "97:\n"
    "98:\n"
    "s_waitcnt vmcnt(0) lgkmcnt(0)\n"
    ".Lreplk_end:\n"
    : "+{s66}"(flg), "+{v208}"(tid), "+{s65}"(blk), "+{s[98:99]}"(ka)
    : "{s[96:97]}"(CTAB)
    : "memory", "scc", "vcc", "m0", "v0", "v1", "v2", "v3", "v4", "v5", "v6", "v7", "v8", "v9", "v10", "v11", "v12", "v13", "v14", "v15", "v16", "v17", "v18", "v19", "v20", "v21", "v22", "v23", "v24", "v25", "v26", "v27", "v28", "v29", "v30", "v31", "v32", "v33", "v34", "v35", "v36", "v37", "v38", "v39", "v40", "v41", "v42", "v43", "v44", "v45", "v46", "v47", "v48", "v49", "v50", "v51", "v52", "v53", "v54", "v55", "v56", "v57", "v58", "v59", "v60", "v61", "v62", "v63", "v64", "v65", "v66", "v67", "v68", "v69", "v70", "v71", "v72", "v73", "v74", "v75", "v76", "v77", "v78", "v79", "v80", "v81", "v82", "v83", "v84", "v85", "v86", "v87", "v88", "v89", "v90", "v91", "v92", "v93", "v94", "v95", "v96", "v97", "v98", "v99", "v100", "v101", "v102", "v103", "v104", "v105", "v106", "v107", "v108", "v109", "v110", "v111", "v112", "v113", "v114", "v115", "v116", "v117", "v118", "v119", "v120", "v121", "v122", "v123", "v124", "v125", "v126", "v127", "v128", "v129", "v130", "v131", "v132", "v133", "v134", "v135", "v136", "v137", "v138", "v139", "v140", "v141", "v142", "v143", "v144", "v145", "v146", "v147", "v148", "v149", "v150", "v151", "v152", "v153", "v154", "v155", "v156", "v157", "v158", "v159", "v160", "v161", "v162", "v163", "v164", "v165", "v166", "v167", "v168", "v169", "v170", "v171", "v172", "v173", "v174", "v175", "v176", "v177", "v178", "v179", "v180", "v181", "v182", "v183", "v184", "v185", "v186", "v187", "v188", "v189", "v190", "v191", "v192", "v193", "v194", "v195", "v196", "v197", "v198", "v199", "v200", "v201", "v202", "v203", "v204", "v205", "v206", "v207", "v209", "v210", "v211", "v212", "v213", "v214", "v215", "v216", "v217", "v218", "v219", "v220", "v221", "v222", "v223", "v224", "v225", "v226", "v227", "v228", "v229", "v230", "v231", "v232", "v233", "v234", "v235", "v236", "v237", "v238", "v239", "v240", "v241", "v242", "v243", "v244", "v245", "v246", "v247", "v248", "v249", "v250", "v251", "v252", "v253", "v254", "v255", "a0", "a1", "a2", "a3", "a4", "a5", "a6", "a7", "a8", "a9", "a10", "a11", "a12", "a13", "a14", "a15", "a16", "a17", "a18", "a19", "a20", "a21", "a22", "a23", "a24", "a25", "a26", "a27", "a28", "a29", "a30", "a31", "a32", "a33", "a34", "a35", "a36", "a37", "a38", "a39", "a40", "a41", "a42", "a43", "a44", "a45", "a46", "a47", "a48", "a49", "a50", "a51", "a52", "a53", "a54", "a55", "a56", "a57", "a58", "a59", "a60", "a61", "a62", "a63", "a64", "a65", "a66", "a67", "a68", "a69", "a70", "a71", "a72", "a73", "a74", "a75", "a76", "a77", "a78", "a79", "a80", "a81", "a82", "a83", "a84", "a85", "a86", "a87", "a88", "a89", "a90", "a91", "a92", "a93", "a94", "a95", "a96", "a97", "a98", "a99", "a100", "a101", "a102", "a103", "a104", "a105", "a106", "a107", "a108", "a109", "a110", "a111", "a112", "a113", "a114", "a115", "a116", "a117", "a118", "a119", "a120", "a121", "a122", "a123", "a124", "a125", "a126", "a127", "a128", "a129", "a130", "a131", "a132", "a133", "a134", "a135", "a136", "a137", "a138", "a139", "a140", "a141", "a142", "a143", "a144", "a145", "a146", "a147", "a148", "a149", "a150", "a151", "a152", "a153", "a154", "a155", "a156", "a157", "a158", "a159", "a160", "a161", "a162", "a163", "a164", "a165", "a166", "a167", "a168", "a169", "a170", "a171", "a172", "a173", "a174", "a175", "a176", "a177", "a178", "a179", "a180", "a181", "a182", "a183", "a184", "a185", "a186", "a187", "a188", "a189", "a190", "a191", "a192", "a193", "a194", "a195", "a196", "a197", "a198", "a199", "a200", "a201", "a202", "a203", "a204", "a205", "a206", "a207", "a208", "a209", "a210", "a211", "a212", "a213", "a214", "a215", "a216", "a217", "a218", "a219", "a220", "a221", "a222", "a223", "a224", "a225", "a226", "a227", "a228", "a229", "a230", "a231", "a232", "a233", "a234", "a235", "a236", "a237", "a238", "a239", "a240", "a241", "a242", "a243", "a244", "a245", "a246", "a247", "a248", "a249", "a250", "a251", "a252", "a253", "a254", "a255", "s36", "s37", "s38", "s39", "s40", "s41", "s42", "s43", "s44", "s45", "s46", "s47", "s48", "s49", "s50", "s51", "s52", "s53", "s54", "s55", "s56", "s57", "s58", "s59", "s60", "s61", "s62", "s63", "s64", "s67", "s68", "s69", "s70", "s71", "s72", "s73", "s74", "s75", "s76", "s77", "s78", "s79", "s80", "s81", "s82", "s83", "s84", "s85", "s86", "s87", "s88", "s89", "s90", "s91", "s92", "s93", "s94", "s95");
  if (flg != 2u) {
    const int l2 = __builtin_amdgcn_mbcnt_hi(~0u, __builtin_amdgcn_mbcnt_lo(~0u, 0u));
    slow_path(x, dwl, dws, w1, w2, out, (float*)smem, w * 64 + l2, blockIdx.x >> 3, (blockIdx.x & 7) * 4);
  }
}

extern "C" void kernel_launch(void* const* d_in, const int* in_sizes, int n_in, void* d_out, int out_size, void* d_ws,
                              size_t ws_size, hipStream_t stream) {
  replk_kernel<<<256, 256, 0, stream>>>((const u16*)d_in[0], (const u16*)d_in[1], (const u16*)d_in[2], (const u16*)d_in[3],
                                        (const u16*)d_in[4], (u16*)d_out
                                        );
}
```

```cpp
#include <hip/hip_runtime.h>
#include <hip/hip_bf16.h>

typedef unsigned u32;
typedef unsigned short u16;
typedef unsigned long long u64;
typedef __bf16 bf16x2 __attribute__((ext_vector_type(2)));
typedef float f32x2 __attribute__((ext_vector_type(2)));
#define DI __device__ __forceinline__

constexpr int IMG = 512;
constexpr int CH = 96;
constexpr int HID = 384;
constexpr int PXB = 192;
constexpr int ROWB = 98304;
constexpr int TOEP_CS = 336;
constexpr int XT_RS = 320;
constexpr int XT_PP = 1360;
constexpr int XT_CG = 6752;
constexpr int GS = 6160;
constexpr int L_LUT = 0;
constexpr int L_TOEP = 8208;
constexpr int L_W = 40960;
constexpr int L_LUTN = 65536;
constexpr int L_XT = 81920;
constexpr int L_H = 108928;
constexpr int L_EX = 133568;
constexpr int XT_Q = 27024;
constexpr int L_MISC = 162992;
constexpr int L_TOTAL = 163056;
constexpr int LUTK = 106;
constexpr float GC1 = -2.302208198144325f, GC3 = -0.1029432395800235f;
__device__ const u32 CTAB[3 * 1024] = {
671109120,671098880,134490112,673187840,671372952,671229956,145500164,673318996,671636784,671361032,156510216,673450152,671900616,671492108,167520268,673581308,672164452,684206128,178530352,686295376,672428284,684337204,189540404,686426532,672692116,684468280,200550456,686557688,672955948,684599356,211560508,686688844,673219784,697313376,222570592,699402912,673483616,697444452,233580644,699534068,673747448,697575528,244590696,699665224,674011280,697706604,255600748,699796380,674254849,710420624,266610832,712510448,674518681,710551700,277620884,712641604,674782513,710682776,288630936,712772760,675046345,710813852,299640988,712903916,
684223077,723527872,310651072,721423680,684486909,723658948,321661124,721554836,683702165,723790024,332671176,721685992,683965997,723921100,343681228,721817148,685278409,736635120,354691312,734531216,685542241,736766196,365701364,734662372,684757497,736897272,376711416,734793528,685021329,737028348,387721468,734924684,686313474,749742368,398731552,747638752,686577306,749873444,409741604,747769908,685792562,750004520,420751656,747901064,686056394,750135596,431761708,748032220,687368806,762849616,442771792,760746288,687632638,762980692,453781844,760877444,686847894,763111768,464791896,761008600,687111726,763242844,475801948,761139756,
697337034,771762560,486812032,773853824,697600866,771893636,497822084,773984980,697864698,772024712,508832136,774116136,698128530,772155788,519842188,774247292,696274947,784869808,530852272,786961360,696538779,785000884,541862324,787092516,696802611,785131960,552872376,787223672,697066443,785263036,563882428,787354828,699427431,797977056,574892512,800068896,699691263,798108132,585902564,800200052,699955095,798239208,596912616,800331208,700218927,798370284,607922668,800462364,698385611,811084304,618932752,813176432,698649443,811215380,629942804,813307588,698913275,811346456,640952856,813438744,699177107,811477532,651962908,813569900,
710430724,824191552,135539264,822089664,710694556,824322628,146549316,822220820,709909812,824453704,157559368,822351976,710173644,824584780,168569420,822483132,709388904,837298800,179579504,835197200,709652736,837429876,190589556,835328356,708867992,837560952,201599608,835459512,709131824,837692028,212609660,835590668,712541388,850406048,223619744,848304736,712805220,850537124,234629796,848435892,712020476,850668200,245639848,848567048,712284308,850799276,256649900,848698204,711479301,863513296,267659984,861412272,711743133,863644372,278670036,861543428,710958389,863775448,289680088,861674584,711222221,863906524,300690140,861805740,
723544681,677394944,311704064,679484672,723808513,677526020,322714116,679615828,724072345,677657096,333724168,679746984,724336177,677788172,344734220,679878140,724600013,690502192,355744304,692592208,724863845,690633268,366754356,692723364,725127677,690764344,377764408,692854520,725391509,690895420,388774460,692985676,721440774,703609440,399784544,705699744,721704606,703740516,410794596,705830900,721968438,703871592,421804648,705962056,722232270,704002668,432814700,706093212,722496106,716716688,443824784,718807280,722759938,716847764,454834836,718938436,723023770,716978840,465844888,719069592,723287602,717109916,476854940,719200748,
736658638,725629632,487865024,731914816,736922470,725760708,498875076,732045972,736137726,725891784,509885128,732177128,736401558,726022860,520895180,732308284,737693703,738736880,531905264,745022352,737957535,738867956,542915316,745153508,737172791,738999032,553925368,745284664,737436623,739130108,564935420,745415820,734554731,751844128,575945504,758129888,734818563,751975204,586955556,758261044,734033819,752106280,597965608,758392200,734297651,752237356,608975660,758523356,735610063,764951376,619985744,771237424,735873895,765082452,630995796,771368580,735089151,765213528,642005848,771499736,735352983,765344604,653015900,771630892,
749752328,778058624,136592256,780150656,750016160,778189700,147602308,780281812,750279992,778320776,158612360,780443436,750543824,778451852,169622412,780574508,748710508,791165872,180632496,793288492,748974340,791296948,191642548,793419564,749238172,791428024,202652600,793550636,749502004,791559100,213662652,793681708,747668688,804273120,224672736,806395692,747932520,804404196,235682788,806526764,748196352,804535272,246692840,806657836,748460184,804666348,257702892,806788908,746606601,817380368,268712976,819502892,746870433,817511444,279723028,819633964,747134265,817642520,290733080,819765036,747398097,817773596,301743132,819896108,
762866285,826293312,312753216,832610092,763130117,826424388,323763268,832741164,762345373,826555464,334773320,832872236,762609205,826686540,345783372,833003308,761824465,839400560,356793456,845717292,762088297,839531636,367803508,845848364,761303553,839662712,378813560,845979436,761567385,839793788,389823612,846110508,760762378,852507808,400833696,858824492,761026210,852638884,411843748,858955564,760241466,852769960,422853800,859086636,760505298,852901036,433863852,859217708,759720558,865615056,444873936,871931692,759984390,865746132,455883988,872062764,759199646,865877208,466894040,872193836,759463478,866008284,477904092,872324908,
771785938,671098882,488908800,673226540,772049770,671229958,499918852,673357612,772313602,671361034,510928904,673488684,772577434,671492110,521938956,673619756,772821003,684206130,532949040,686333740,773084835,684337206,543959092,686464812,773348667,684468282,554969144,686595884,773612499,684599358,565979196,686726956,773876335,697313378,576989280,699440940,774140167,697444454,587999332,699572012,774403999,697575530,599009384,699703084,774667831,697706606,610019436,699834156,774931667,710420626,621029520,712548140,775195499,710551702,632039572,712679212,775459331,710682778,643049624,712810284,775723163,710813854,654059676,712941356,
784879628,723527874,137636032,721461036,785143460,723658950,148646084,721592108,784358716,723790026,159656136,721723180,784622548,723921102,170666188,721854252,785934960,736635122,181676272,734568236,786198792,736766198,192686324,734699308,785414048,736897274,203696376,734830380,785677880,737028350,214706428,734961452,786990292,749742370,225716512,747675436,787254124,749873446,236726564,747806508,786469380,750004522,247736616,747937580,786733212,750135598,258746668,748068652,788025357,762849618,269756752,760782636,788289189,762980694,280766804,760913708,787504445,763111770,291776856,761044780,787768277,763242846,302786908,761175852,
797993585,771762562,313796992,773889836,798257417,771893638,324807044,774020908,798521249,772024714,335817096,774151980,798785081,772155790,346827148,774283052,796951765,784869810,357837232,786997036,797215597,785000886,368847284,787128108,797479429,785131962,379857336,787259180,797743261,785263038,390867388,787390252,800083982,797977058,401877472,800104236,800347814,798108134,412887524,800235308,800611646,798239210,423897576,800366380,800875478,798370286,434907628,800497452,799042162,811084306,445917712,813211436,799305994,811215382,456927764,813342508,799569826,811346458,467937816,813473580,799833658,811477534,478947868,813604652,
811107542,824191554,489957952,822124332,811371374,824322630,500968004,822255404,810586630,824453706,511978056,822386476,810850462,824584782,522988108,822517548,810045455,837298802,533998192,835231532,810309287,837429878,545008244,835362604,809524543,837560954,556018296,835493676,809788375,837692030,567028348,835624748,813197939,850406050,578038432,848338732,813461771,850537126,589048484,848469804,812677027,850668202,600058536,848600876,812940859,850799278,611068588,848731948,812156119,863513298,622078672,861445932,812419951,863644374,633088724,861577004,811635207,863775450,644098776,861708076,811899039,863906526,655108828,861839148,
824201232,677394946,138689024,679517996,824465064,677526022,149699076,679649068,824728896,677657098,160709128,679780140,824992728,677788174,171719180,679911212,825256564,690502194,182729264,692625196,825520396,690633270,193739316,692756268,825784228,690764346,204749368,692887340,826048060,690895422,215759420,693018412,822117592,703609442,226769504,705732396,822381424,703740518,237779556,705863468,822645256,703871594,248789608,705994540,822909088,704002670,259799660,706125612,823152657,716716690,270809744,718839596,823416489,716847766,281819796,718970668,823680321,716978842,292829848,719101740,823944153,717109918,303839900,719232812,
837315189,725629634,314849984,731946796,837579021,725760710,325860036,732077868,836794277,725891786,336870088,732208940,837058109,726022862,347880140,732340012,838370521,738736882,358890224,745053996,838634353,738867958,369900276,745185068,837849609,738999034,380910328,745316140,838113441,739130110,391920380,745447212,835211282,751844130,402930464,758161196,835475114,751975206,413940516,758292268,834690370,752106282,424950568,758423340,834954202,752237358,435960620,758554412,836266614,764951378,446970704,771268396,836530446,765082454,457980756,771399468,835745702,765213530,468990808,771530540,836009534,765344606,480000860,771661612,
850429146,778058626,491010944,780181292,850692978,778189702,502020996,780312364,850956810,778320778,513031048,780443436,851220642,778451854,524041100,780574508,849367059,791165874,535051184,793288492,849630891,791296950,546061236,793419564,849894723,791428026,557071288,793550636,850158555,791559102,568081340,793681708,848325239,804273122,579091424,806395692,848589071,804404198,590101476,806526764,848852903,804535274,601111528,806657836,849116735,804666350,612121580,806788908,847283419,817380370,623131664,819502892,847547251,817511446,634141716,819633964,847811083,817642522,645151768,819765036,848074915,817773598,656161820,819896108,
863541411,826293314,656161856,832610092,863803555,826424390,656161860,832741164,863017123,826555466,656161864,832872236,863279267,826686542,656161868,833003308,862492835,839400562,656161904,845717292,862754979,839531638,656161908,845848364,861968547,839662714,656161912,845979436,862230691,839793790,656161916,846110508,861444259,852507810,656161952,858824492,861706403,852638886,656161956,858955564,860919971,852769962,656161960,859086636,861182115,852901038,656161964,859217708,860395683,865615058,656162000,871931692,860657827,865746134,656162004,872062764,859871395,865877210,656162008,872193836,860133539,866008286,656162012,872324908,
2151688192u,1885629024,1342187520,1342212212,2151688196u,1885891172,1452812292,1364494564,2151688200u,1886153320,1563437064,1386776836,2218797068u,1886415468,1674061836,1409059156,2165647376u,1886677616,1342253072,1342474360,2165647380u,1886939764,1452877844,1364756712,2164271128u,1887201912,1563502616,1387038984,2231379996u,1887464060,1674127388,1409321304,1082140704,1887726208,1342318624,1342998652,1082140708,1887988356,1452943396,1365281004,1082140712,1888250504,1563568168,1387563276,1149249580,1888512652,1674192940,1409845596,1108682800,1888774800,1342384176,1342736512,1108682804,1889036948,1453008948,1365018864,1107306552,1889299096,1563633720,1387301136,1174415420,1889561244,1674258492,1409583456,
268445760,1892969168,1342449728,1342474372,268445764,1893231316,1453074500,1364756692,268445768,1893493464,1563699272,1387038964,335554636,1893755612,1674324044,1409321316,269822032,1891920576,1342515280,1342736520,269822036,1892182724,1453140052,1365018840,268445784,1892444872,1563764824,1387301112,335554652,1892707020,1674389596,1409583464,1342187616,1890871984,1342580832,1343260812,1342187620,1891134132,1453205604,1365543132,1342187624,1891396280,1563830376,1387825404,1409296492,1891658428,1674455148,1410107756,1343563888,1889823392,1342646384,1342998672,1343563892,1890085540,1453271156,1365280992,1342187640,1890347688,1563895928,1387563264,1409296508,1890609836,1674520700,1409845616,
536881280,1895066352,1342711936,1364494484,536881284,1895328500,1453336708,1386776772,536881288,1895590648,1563961480,1409059108,603990156,1895852796,1674586252,1431341364,538257552,1894017760,1342777488,1364756632,538257556,1894279908,1453402260,1387038920,536881304,1894542056,1564027032,1409321256,603990172,1894804204,1674651804,1431603512,1610623136,1897163536,1342843040,1365280924,1610623140,1897425684,1453467812,1387563212,1610623144,1897687832,1564092584,1409845548,1677732012,1897949980,1674717356,1432127804,1611999408,1896114944,1342908592,1365018784,1611999412,1896377092,1453533364,1387301072,1610623160,1896639240,1564158136,1409583408,1677732028,1896901388,1674782908,1431865664,
809511104,1900309312,1342974144,1364756644,809511108,1900571460,1453598916,1387038900,809511112,1900833608,1564223688,1409321236,876619980,1901095756,1674848460,1431603524,823470288,1901357904,1343039696,1365018792,823470292,1901620052,1453664468,1387301048,822094040,1901882200,1564289240,1409583384,889202908,1902144348,1674914012,1431865672,1887447264,1898212128,1343105248,1365543084,1887447268,1898474276,1453730020,1387825340,1887447272,1898736424,1564354792,1410107676,1954556140,1898998572,1674979564,1432389964,1913989360,1899260720,1343170800,1365280944,1913989364,1899522868,1453795572,1387563200,1912613112,1899785016,1564420344,1409845536,1979721980,1900047164,1675045116,1432127824,
2151688448u,1784705124,1347430656,1452835440,2151688452u,1784967272,1458055428,1475117792,2151688456u,1785229420,1568680200,1497400064,2218797324u,1785491568,1679304972,1519682384,2165647632u,1785753716,1347496208,1453097588,2165647636u,1786015864,1458120980,1475379940,2164271384u,1786278012,1568745752,1497662212,2231380252u,1786540160,1679370524,1519944532,1082140960,1786802308,1347561760,1453621880,1082140964,1787064456,1458186532,1475904232,1082140968,1787326604,1568811304,1498186504,1149249836,1787588752,1679436076,1520468824,1108683056,1787850900,1347627312,1453359740,1108683060,1788113048,1458252084,1475642092,1107306808,1788375196,1568876856,1497924364,1174415676,1788637344,1679501628,1520206684,
268446016,1792045268,1347692864,1453097600,268446020,1792307416,1458317636,1475379920,268446024,1792569564,1568942408,1497662192,335554892,1792831712,1679567180,1519944544,269822288,1790996676,1347758416,1453359748,269822292,1791258824,1458383188,1475642068,268446040,1791520972,1569007960,1497924340,335554908,1791783120,1679632732,1520206692,1342187872,1789948084,1347823968,1453884040,1342187876,1790210232,1458448740,1476166360,1342187880,1790472380,1569073512,1498448632,1409296748,1790734528,1679698284,1520730984,1343564144,1788899492,1347889520,1453621900,1343564148,1789161640,1458514292,1475904220,1342187896,1789423788,1569139064,1498186492,1409296764,1789685936,1679763836,1520468844,
536881536,1794142452,1347955072,1475117712,536881540,1794404600,1458579844,1497400000,536881544,1794666748,1569204616,1519682336,603990412,1794928896,1679829388,1541964592,538257808,1793093860,1348020624,1475379860,538257812,1793356008,1458645396,1497662148,536881560,1793618156,1569270168,1519944484,603990428,1793880304,1679894940,1542226740,1610623392,1796239636,1348086176,1475904152,1610623396,1796501784,1458710948,1498186440,1610623400,1796763932,1569335720,1520468776,1677732268,1797026080,1679960492,1542751032,1611999664,1795191044,1348151728,1475642012,1611999668,1795453192,1458776500,1497924300,1610623416,1795715340,1569401272,1520206636,1677732284,1795977488,1680026044,1542488892,
809511360,1799385412,1348217280,1475379872,809511364,1799647560,1458842052,1497662128,809511368,1799909708,1569466824,1519944464,876620236,1800171856,1680091596,1542226752,823470544,1800434004,1348282832,1475642020,823470548,1800696152,1458907604,1497924276,822094296,1800958300,1569532376,1520206612,889203164,1801220448,1680157148,1542488900,1887447520,1797288228,1348348384,1476166312,1887447524,1797550376,1458973156,1498448568,1887447528,1797812524,1569597928,1520730904,1954556396,1798074672,1680222700,1543013192,1913989616,1798336820,1348413936,1475904172,1913989620,1798598968,1459038708,1498186428,1912613368,1798861116,1569663480,1520468764,1979722236,1799123264,1680288252,1542751052,
2151688704u,2087482984,1352673280,1563464828,2151688708u,2087745132,1463298052,1585747180,2151688712u,2088007280,1573922824,1608029452,2218797580u,2088269428,1684547596,1630311772,2165647888u,2088531576,1352738832,1563726976,2165647892u,2088793724,1463363604,1586009328,2164271640u,2089055872,1573988376,1608291600,2231380508u,2089318020,1684613148,1630573920,1082141216,2089580168,1352804384,1564251268,1082141220,2089842316,1463429156,1586533620,1082141224,2090104464,1574053928,1608815892,1149250092,2090366612,1684678700,1631098212,1108683312,2090628760,1352869936,1563989128,1108683316,2090890908,1463494708,1586271480,1107307064,2091153056,1574119480,1608553752,1174415932,2091415204,1684744252,1630836072,
268446272,2094823128,1352935488,1563726988,268446276,2095085276,1463560260,1586009308,268446280,2095347424,1574185032,1608291580,335555148,2095609572,1684809804,1630573932,269822544,2093774536,1353001040,1563989136,269822548,2094036684,1463625812,1586271456,268446296,2094298832,1574250584,1608553728,335555164,2094560980,1684875356,1630836080,1342188128,2092725944,1353066592,1564513428,1342188132,2092988092,1463691364,1586795748,1342188136,2093250240,1574316136,1609078020,1409297004,2093512388,1684940908,1631360372,1343564400,2091677352,1353132144,1564251288,1343564404,2091939500,1463756916,1586533608,1342188152,2092201648,1574381688,1608815880,1409297020,2092463796,1685006460,1631098232,
536881792,2096920312,1353197696,1585747100,536881796,2097182460,1463822468,1608029388,536881800,2097444608,1574447240,1630311724,603990668,2097706756,1685072012,1652593980,538258064,2095871720,1353263248,1586009248,538258068,2096133868,1463888020,1608291536,536881816,2096396016,1574512792,1630573872,603990684,2096658164,1685137564,1652856128,1610623648,2099017496,1353328800,1586533540,1610623652,2099279644,1463953572,1608815828,1610623656,2099541792,1574578344,1631098164,1677732524,2099803940,1685203116,1653380420,1611999920,2097968904,1353394352,1586271400,1611999924,2098231052,1464019124,1608553688,1610623672,2098493200,1574643896,1630836024,1677732540,2098755348,1685268668,1653118280,
809511616,2102163272,1353459904,1586009260,809511620,2102425420,1464084676,1608291516,809511624,2102687568,1574709448,1630573852,876620492,2102949716,1685334220,1652856140,823470800,2103211864,1353525456,1586271408,823470804,2103474012,1464150228,1608553664,822094552,2103736160,1574775000,1630836000,889203420,2103998308,1685399772,1653118288,1887447776,2100066088,1353591008,1586795700,1887447780,2100328236,1464215780,1609077956,1887447784,2100590384,1574840552,1631360292,1954556652,2100852532,1685465324,1653642580,1913989872,2101114680,1353656560,1586533560,1913989876,2101376828,1464281332,1608815816,1912613624,2101638976,1574906104,1631098152,1979722492,2101901124,1685530876,1653380440,
2151688960u,1986559084,1357916416,1674088056,2151688964u,1986821232,1468541188,1696370408,2151688968u,1987083380,1579165960,1718652680,2218797836u,1987345528,1689790732,1740935000,2165648144u,1987607676,1357981968,1674350204,2165648148u,1987869824,1468606740,1696632556,2164271896u,1988131972,1579231512,1718914828,2231380764u,1988394120,1689856284,1741197148,1082141472,1988656268,1358047520,1674874496,1082141476,1988918416,1468672292,1697156848,1082141480,1989180564,1579297064,1719439120,1149250348,1989442712,1689921836,1741721440,1108683568,1989704860,1358113072,1674612356,1108683572,1989967008,1468737844,1696894708,1107307320,1990229156,1579362616,1719176980,1174416188,1990491304,1689987388,1741459300,
268446528,1993899228,1358178624,1674350216,268446532,1994161376,1468803396,1696632536,268446536,1994423524,1579428168,1718914808,335555404,1994685672,1690052940,1741197160,269822800,1992850636,1358244176,1674612364,269822804,1993112784,1468868948,1696894684,268446552,1993374932,1579493720,1719176956,335555420,1993637080,1690118492,1741459308,1342188384,1991802044,1358309728,1675136656,1342188388,1992064192,1468934500,1697418976,1342188392,1992326340,1579559272,1719701248,1409297260,1992588488,1690184044,1741983600,1343564656,1990753452,1358375280,1674874516,1343564660,1991015600,1469000052,1697156836,1342188408,1991277748,1579624824,1719439108,1409297276,1991539896,1690249596,1741721460,
536882048,1995996412,1358440832,1696370328,536882052,1996258560,1469065604,1718652616,536882056,1996520708,1579690376,1740934952,603990924,1996782856,1690315148,1763217208,538258320,1994947820,1358506384,1696632476,538258324,1995209968,1469131156,1718914764,536882072,1995472116,1579755928,1741197100,603990940,1995734264,1690380700,1763479356,1610623904,1998093596,1358571936,1697156768,1610623908,1998355744,1469196708,1719439056,1610623912,1998617892,1579821480,1741721392,1677732780,1998880040,1690446252,1764003648,1612000176,1997045004,1358637488,1696894628,1612000180,1997307152,1469262260,1719176916,1610623928,1997569300,1579887032,1741459252,1677732796,1997831448,1690511804,1763741508,
809511872,2001239372,1358703040,1696632488,809511876,2001501520,1469327812,1718914744,809511880,2001763668,1579952584,1741197080,876620748,2002025816,1690577356,1763479368,823471056,2002287964,1358768592,1696894636,823471060,2002550112,1469393364,1719176892,822094808,2002812260,1580018136,1741459228,889203676,2003074408,1690642908,1763741516,1887448032,1999142188,1358834144,1697418928,1887448036,1999404336,1469458916,1719701184,1887448040,1999666484,1580083688,1741983520,1954556908,1999928632,1690708460,1764265808,1913990128,2000190780,1358899696,1697156788,1913990132,2000452928,1469524468,1719439044,1912613880,2000715076,1580149240,1741721380,1979722748,2000977224,1690774012,1764003668,
136579076,1343253088,52428,12582912,135530496,1453879396,843229,12648449,134481920,1564505704,1634030,12713986,145229824,1675132012,2411724,12779523,136841224,1343318640,3198156,13369356,135792808,1453944948,3988957,13434893,134744232,1564571256,4779758,13500430,139724968,1675197564,5557452,13565967,137103372,1343384208,6343884,14155800,136054952,1454010516,7134685,14221337,135006376,1564636824,7925486,14286874,139724968,1675263132,8703180,14352411,137365520,1343449728,9489612,14942244,136316928,1454076036,10280413,15007781,135268352,1564702344,11071214,15073318,145229824,1675328652,11848908,15138855,
139724868,1365535332,117964,12582960,139724852,1476161640,908765,12648497,138676260,1586787948,1699566,12714034,137627668,1697414256,2477260,12779571,145229896,1365600884,3263692,13369404,145229880,1476227192,4054493,13434941,138938408,1586853500,4845294,13500478,137889816,1697479808,5622988,13566015,145229900,1365666452,6409420,14155848,145229884,1476292760,7200221,14221385,139200556,1586919068,7991022,14286922,138151964,1697545376,8768716,14352459,139724880,1365731972,9555148,14942292,139724864,1476358280,10345949,15007829,139462704,1586984588,11136750,15073366,138414112,1697610896,11914444,15138903,
137627668,1387817576,183500,12583008,136579076,1498443884,974301,12648545,135530496,1609070192,1765102,12714082,134481920,1719696500,2542796,12779619,137889816,1387883128,3329228,13369452,136841224,1498509436,4120029,13434989,135792808,1609135744,4910830,13500526,134744232,1719762052,5688524,13566063,138151964,1387948696,6474956,14155896,137103372,1498575004,7265757,14221433,136054952,1609201312,8056558,14286970,135006376,1719827620,8834252,14352507,138414112,1388014216,9620684,14942340,137365520,1498640524,10411485,15007877,136316928,1609266832,11202286,15073414,135268352,1719893140,11979980,15138951,
139724968,1410099820,249036,12583056,139724868,1520726128,1039837,12648593,139724852,1631352436,1830638,12714130,138676260,1741978744,2608332,12779667,145229824,1410165372,3394764,13369500,145229896,1520791680,4185565,13435037,145229880,1631417988,4976366,13500574,138938408,1742044296,5754060,13566111,145229824,1410230940,6540492,14155944,145229900,1520857248,7331293,14221481,145229884,1631483556,8122094,14287018,139200556,1742109864,8899788,14352555,139724968,1410296460,9686220,14942388,139724880,1520922768,10477021,15007925,139724864,1631549076,11267822,15073462,139462704,1742175384,12045516,15138999,
180619940,1348496080,52428,12582912,179571360,1459122388,843229,12648449,178522784,1569748696,1634030,12713986,189270688,1680375004,2411724,12779523,180882088,1348561600,3198156,13369356,179833672,1459187908,3988957,13434893,178785096,1569814216,4779758,13500430,183765832,1680440524,5557452,13565967,181144236,1348627104,6343884,14155800,180095816,1459253412,7134685,14221337,179047240,1569879720,7925486,14286874,183765832,1680506028,8703180,14352411,181406384,1348692656,9489612,14942244,180357792,1459318964,10280413,15007781,179309216,1569945272,11071214,15073318,189270688,1680571580,11848908,15138855,
183765732,1370778324,117964,12582960,183765716,1481404632,908765,12648497,182717124,1592030940,1699566,12714034,181668532,1702657248,2477260,12779571,189270760,1370843844,3263692,13369404,189270744,1481470152,4054493,13434941,182979272,1592096460,4845294,13500478,181930680,1702722768,5622988,13566015,189270764,1370909348,6409420,14155848,189270748,1481535656,7200221,14221385,183241420,1592161964,7991022,14286922,182192828,1702788272,8768716,14352459,183765744,1370974900,9555148,14942292,183765728,1481601208,10345949,15007829,183503568,1592227516,11136750,15073366,182454976,1702853824,11914444,15138903,
181668532,1393060568,183500,12583008,180619940,1503686876,974301,12648545,179571360,1614313184,1765102,12714082,178522784,1724939492,2542796,12779619,181930680,1393126088,3329228,13369452,180882088,1503752396,4120029,13434989,179833672,1614378704,4910830,13500526,178785096,1725005012,5688524,13566063,182192828,1393191592,6474956,14155896,181144236,1503817900,7265757,14221433,180095816,1614444208,8056558,14286970,179047240,1725070516,8834252,14352507,182454976,1393257144,9620684,14942340,181406384,1503883452,10411485,15007877,180357792,1614509760,11202286,15073414,179309216,1725136068,11979980,15138951,
183765832,1415342812,249036,12583056,183765732,1525969120,1039837,12648593,183765716,1636595428,1830638,12714130,182717124,1747221736,2608332,12779667,189270688,1415408332,3394764,13369500,189270760,1526034640,4185565,13435037,189270744,1636660948,4976366,13500574,182979272,1747287256,5754060,13566111,189270688,1415473836,6540492,14155944,189270764,1526100144,7331293,14221481,189270748,1636726452,8122094,14287018,183241420,1747352760,8899788,14352555,183765832,1415539388,9686220,14942388,183765744,1526165696,10477021,15007925,183765728,1636792004,11267822,15073462,183503568,1747418312,12045516,15138999,
224660804,1353738992,52428,12582912,223612224,1464365300,843229,12648449,222563648,1574991608,1634030,12713986,233311552,1685617916,2411724,12779523,224922952,1353804512,3198156,13369356,223874536,1464430820,3988957,13434893,222825960,1575057128,4779758,13500430,227806696,1685683436,5557452,13565967,225185100,1353870080,6343884,14155800,224136680,1464496388,7134685,14221337,223088104,1575122696,7925486,14286874,227806696,1685749004,8703180,14352411,225447248,1353935632,9489612,14942244,224398656,1464561940,10280413,15007781,223350080,1575188248,11071214,15073318,233311552,1685814556,11848908,15138855,
227806596,1376021236,117964,12582960,227806580,1486647544,908765,12648497,226757988,1597273852,1699566,12714034,225709396,1707900160,2477260,12779571,233311624,1376086756,3263692,13369404,233311608,1486713064,4054493,13434941,227020136,1597339372,4845294,13500478,225971544,1707965680,5622988,13566015,233311628,1376152324,6409420,14155848,233311612,1486778632,7200221,14221385,227282284,1597404940,7991022,14286922,226233692,1708031248,8768716,14352459,227806608,1376217876,9555148,14942292,227806592,1486844184,10345949,15007829,227544432,1597470492,11136750,15073366,226495840,1708096800,11914444,15138903,
225709396,1398303480,183500,12583008,224660804,1508929788,974301,12648545,223612224,1619556096,1765102,12714082,222563648,1730182404,2542796,12779619,225971544,1398369000,3329228,13369452,224922952,1508995308,4120029,13434989,223874536,1619621616,4910830,13500526,222825960,1730247924,5688524,13566063,226233692,1398434568,6474956,14155896,225185100,1509060876,7265757,14221433,224136680,1619687184,8056558,14286970,223088104,1730313492,8834252,14352507,226495840,1398500120,9620684,14942340,225447248,1509126428,10411485,15007877,224398656,1619752736,11202286,15073414,223350080,1730379044,11979980,15138951,
227806696,1420585724,249036,12583056,227806596,1531212032,1039837,12648593,227806580,1641838340,1830638,12714130,226757988,1752464648,2608332,12779667,233311552,1420651244,3394764,13369500,233311624,1531277552,4185565,13435037,233311608,1641903860,4976366,13500574,227020136,1752530168,5754060,13566111,233311552,1420716812,6540492,14155944,233311628,1531343120,7331293,14221481,233311612,1641969428,8122094,14287018,227282284,1752595736,8899788,14352555,227806696,1420782364,9686220,14942388,227806608,1531408672,10477021,15007925,227806592,1642034980,11267822,15073462,227544432,1752661288,12045516,15138999,
268701668,1358981952,52428,12582912,267653088,1469608260,843229,12648449,266604512,1580234568,1634030,12713986,277352416,1690860876,2411724,12779523,268963816,1359047504,3198156,13369356,267915400,1469673812,3988957,13434893,266866824,1580300120,4779758,13500430,271847560,1690926428,5557452,13565967,269225964,1359113008,6343884,14155800,268177544,1469739316,7134685,14221337,267128968,1580365624,7925486,14286874,271847560,1690991932,8703180,14352411,269488112,1359178528,9489612,14942244,268439520,1469804836,10280413,15007781,267390944,1580431144,11071214,15073318,277352416,1691057452,11848908,15138855,
271847460,1381264196,117964,12582960,271847444,1491890504,908765,12648497,270798852,1602516812,1699566,12714034,269750260,1713143120,2477260,12779571,277352488,1381329748,3263692,13369404,277352472,1491956056,4054493,13434941,271061000,1602582364,4845294,13500478,270012408,1713208672,5622988,13566015,277352492,1381395252,6409420,14155848,277352476,1492021560,7200221,14221385,271323148,1602647868,7991022,14286922,270274556,1713274176,8768716,14352459,271847472,1381460772,9555148,14942292,271847456,1492087080,10345949,15007829,271585296,1602713388,11136750,15073366,270536704,1713339696,11914444,15138903,
269750260,1403546440,183500,12583008,268701668,1514172748,974301,12648545,267653088,1624799056,1765102,12714082,266604512,1735425364,2542796,12779619,270012408,1403611992,3329228,13369452,268963816,1514238300,4120029,13434989,267915400,1624864608,4910830,13500526,266866824,1735490916,5688524,13566063,270274556,1403677496,6474956,14155896,269225964,1514303804,7265757,14221433,268177544,1624930112,8056558,14286970,267128968,1735556420,8834252,14352507,270536704,1403743016,9620684,14942340,269488112,1514369324,10411485,15007877,268439520,1624995632,11202286,15073414,267390944,1735621940,11979980,15138951,
271847560,1425828684,249036,12583056,271847460,1536454992,1039837,12648593,271847444,1647081300,1830638,12714130,270798852,1757707608,2608332,12779667,277352416,1425894236,3394764,13369500,277352488,1536520544,4185565,13435037,277352472,1647146852,4976366,13500574,271061000,1757773160,5754060,13566111,277352416,1425959740,6540492,14155944,277352492,1536586048,7331293,14221481,277352476,1647212356,8122094,14287018,271323148,1757838664,8899788,14352555,271847560,1426025260,9686220,14942388,271847472,1536651568,10477021,15007925,271847456,1647277876,11267822,15073462,271585296,1757904184,12045516,15138999};

DI float bf_lo(u32 v) { return __uint_as_float(v << 16); }
DI u32 pk_bf16(float a, float b) { f32x2 t = {a, b}; return __builtin_bit_cast(u32, __builtin_convertvector(t, bf16x2)); }

DI void slow_path(const u16* __restrict__ x, const u16* __restrict__ dwl, const u16* __restrict__ dws, const u16* __restrict__ w1,
                  const u16* __restrict__ w2, u16* __restrict__ out, float* sm, int t, int tx, int ty0) {
  float* o = sm;
  float* g = sm + 8 * CH;
  float* st = g + 8 * HID;
#pragma unroll 1
  for (int b = 0; b < 128; ++b) {
    const int y = ty0 * 16 + (b >> 1), x0 = tx * 16 + (b & 1) * 8;
    __syncthreads();
#pragma unroll 1
    for (int i = t; i < 8 * CH; i += 256) {
      const int p = i / CH, c = i - p * CH;
      float acc = 0.f;
#pragma unroll 1
      for (int a = 0; a < 5; ++a)
#pragma unroll 1
        for (int e = 0; e < 5; ++e) {
          const int yy = y + a - 2, xc = x0 + p + e - 2;
          if (yy < 0 || yy >= IMG || xc < 0 || xc >= IMG) continue;
          const float xv = bf_lo(x[(size_t)(yy * IMG + xc) * CH + c]);
          acc = __builtin_fmaf(bf_lo(dws[(a * 5 + e) * CH + c]), xv, acc);
          if (a >= 1 && a <= 3 && e >= 1 && e <= 3) acc = __builtin_fmaf(bf_lo(dwl[((a - 1) * 3 + (e - 1)) * CH + c]), xv, acc);
        }
      o[i] = acc;
    }
    __syncthreads();
    if (t < 8) {
      float m = 0.f, v = 0.f;
#pragma unroll 1
      for (int c = 0; c < CH; ++c) m += o[t * CH + c];
      m *= (1.0f / CH);
#pragma unroll 1
      for (int c = 0; c < CH; ++c) { const float d = o[t * CH + c] - m; v = __builtin_fmaf(d, d, v); }
      st[2 * t] = m; st[2 * t + 1] = __builtin_amdgcn_rsqf(v * (1.0f / CH) + 1e-5f);
    }
    __syncthreads();
#pragma unroll 1
    for (int i = t; i < 8 * HID; i += 256) {
      const int p = i / HID, e = i - p * HID;
      const float m = st[2 * p], r = st[2 * p + 1];
      float u = 0.f;
#pragma unroll 1
      for (int c = 0; c < CH; ++c) u = __builtin_fmaf((o[p * CH + c] - m) * r, bf_lo(w1[c * HID + e]), u);
      const float z = u * __builtin_fmaf(u * u, GC3, GC1);
      g[i] = u * __builtin_amdgcn_rcpf(1.0f + __builtin_amdgcn_exp2f(z));
    }
    __syncthreads();
#pragma unroll 1
    for (int i = t; i < 8 * CH; i += 256) {
      const int p = i / CH, c = i - p * CH;
      float acc = 0.f;
#pragma unroll 1
      for (int e = 0; e < HID; ++e) acc = __builtin_fmaf(g[p * HID + e], bf_lo(w2[e * CH + c]), acc);
      const size_t idx = (size_t)(y * IMG + x0 + p) * CH + c;
      out[idx] = (u16)pk_bf16(bf_lo(x[idx]) + acc, 0.f);
    }
  }
}

__global__ __launch_bounds__(256, 1) void replk_kernel(const u16* __restrict__ x, const u16* __restrict__ dwl,
    const u16* __restrict__ dws, const u16* __restrict__ w1, const u16* __restrict__ w2, u16* __restrict__ out
    ) {
  __shared__ __attribute__((aligned(16))) char smem[L_TOTAL];
  __attribute__((address_space(3))) char* smem3 = (__attribute__((address_space(3))) char*)smem;
  const int w = __builtin_amdgcn_readfirstlane(threadIdx.x >> 6);
  u32 tid = threadIdx.x, blk = blockIdx.x;
  u32 flg = (u32)(size_t)smem3 ? 1u : 0u;
  u64 ka = (u64)__builtin_amdgcn_kernarg_segment_ptr();
  asm volatile(
    ".p2align 3\n"
    "s_load_dwordx2 s[40:41], s[98:99], 0x0\n"
    "s_load_dwordx2 s[88:89], s[98:99], 0x10\n"
    "s_load_dwordx2 s[92:93], s[98:99], 0x8\n"
    "s_load_dwordx2 s[84:85], s[98:99], 0x20\n"
    "s_load_dwordx2 s[80:81], s[98:99], 0x18\n"
    "s_load_dwordx2 s[44:45], s[98:99], 0x28\n"
    "v_lshlrev_b32_e32 v213, 4, v208\n"
    "v_lshrrev_b32_e32 v0, 6, v208\n"
    "s_add_u32 vcc_lo, s96, 0\n"
    "s_addc_u32 vcc_hi, s97, 0\n"
    "global_load_dwordx4 v[168:171], v213, vcc\n"
    "s_add_u32 vcc_lo, s96, 4096\n"
    "s_addc_u32 vcc_hi, s97, 0\n"
    "global_load_dwordx4 v[172:175], v213, vcc\n"
    "s_add_u32 vcc_lo, s96, 8192\n"
    "s_addc_u32 vcc_hi, s97, 0\n"
    "global_load_dwordx4 v[176:179], v213, vcc\n"
    "s_getpc_b64 s[76:77]\n"
    ".Lreplk_pc0:\n"
    "s_bfe_u32 s63, s65, 0x30003\n"
    "v_lshrrev_b32_e32 v5, 2, v208\n"
    "s_lshl_b32 s63, s63, 6\n"
    "v_add_lshl_u32 v5, s63, v5, 7\n"
    "v_min_u32_e32 v215, .Lreplk_end-.Lreplk_pc0-4, v5\n"
    "global_load_dword v233, v215, s[76:77]\n"
    "v_readfirstlane_b32 s60, v0\n"
    "v_min_u32_e32 v1, 239, v208\n"
    "v_mul_u32_u24_e32 v2, 1366, v1\n"
    "s_lshr_b32 s61, s65, 3\n"
    "s_and_b32 s62, s65, 7\n"
    "v_lshrrev_b32_e32 v2, 16, v2\n"
    "v_lshlrev_b32_e32 v3, 2, v1\n"
    "s_lshl_b32 s62, s62, 6\n"
    "s_movk_i32 s63, 768\n"
    "v_mad_u32_u24 v211, v2, s63, v3\n"
    "v_mul_u32_u24_e32 v4, 384, v2\n"
    "v_add_u32_e32 v2, -1, v2\n"
    "s_movk_i32 s90, 4800\n"
    "s_mov_b32 s91, 0x20000\n"
    "v_add_u32_e32 v3, -576, v3\n"
    "v_cmp_gt_u32_e32 vcc, 3, v2\n"
    "v_add_u32_e32 v3, v3, v4\n"
    "v_bfrev_b32_e32 v4, 1\n"
    "s_movk_i32 s94, 1728\n"
    "s_mov_b32 s95, 0x20000\n"
    "v_cndmask_b32_e32 v212, v4, v3, vcc\n"
    "s_lshl_b32 s99, s62, 5\n"
    "s_add_u32 s99, s99, s61\n"
    "s_mulk_i32 s99, 3072\n"
    "s_lshr_b32 s51, s60, 1\n"
    "s_mul_i32 s63, s51, 18432\n"
    "s_lshl_b32 s52, s60, 13\n"
    "s_add_u32 s63, s63, s52\n"
    "v_add_u32_e32 v210, s63, v213\n"
    "s_mul_i32 s38, s60, 9216\n"
    "s_add_u32 s38, s38, 40960\n"
    "s_mov_b32 s86, 73728\n"
    "s_mov_b32 s87, 0x20000\n"
    "v_and_b32_e32 v5, 63, v208\n"
    "v_min_u32_e32 v5, 8, v5\n"
    "s_and_b32 s52, s60, 1\n"
    "s_lshl_b32 s53, s61, 1\n"
    "s_add_u32 s52, s52, s53\n"
    "s_mul_i32 s52, s52, 9\n"
    "v_add_lshl_u32 v214, s52, v5, 7\n"
    "s_cmp_eq_u32 s61, 0\n"
    "s_cselect_b32 s52, 24, 0\n"
    "s_cmp_eq_u32 s61, 31\n"
    "s_cselect_b32 s53, 40, s52\n"
    "s_sub_u32 s53, 256, s53\n"
    "v_subrev_u32_e32 v6, s52, v208\n"
    "s_sub_u32 s63, s99, 384\n"
    "v_cmp_gt_u32_e32 vcc, s53, v6\n"
    "v_lshl_add_u32 v7, v1, 4, s63\n"
    "v_bfe_u32 v8, v208, 4, 2\n"
    "v_lshl_or_b32 v8, v0, 3, v8\n"
    "v_and_b32_e32 v9, 0xf0, v213\n"
    "v_cndmask_b32_e32 v208, v4, v7, vcc\n"
    "v_mul_u32_u24_e32 v8, 768, v8\n"
    "s_mov_b32 s42, 50331648\n"
    "s_mov_b32 s43, 0x20000\n"
    "v_or_b32_e32 v209, v8, v9\n"
    "s_mov_b32 s82, 73728\n"
    "s_mov_b32 s83, 0x20000\n"
    "s_cmp_eq_u32 s62, 0\n"
    "s_cselect_b32 s78, 0, s42\n"
    "s_mov_b32 s79, 0x20000\n"
    "s_waitcnt lgkmcnt(0)\n"
    "s_cmp_lt_u32 s60, 2\n"
    "s_nop 0\n"
    "s_cselect_b64 s[70:71], s[80:81], s[84:85]\n"
    "s_sub_u32 s76, s40, 196608\n"
    "s_subb_u32 s77, s41, 0\n"
    "s_mov_b32 s65, 0\n"
    "buffer_load_dwordx4 a[224:227], v208, s[76:79], s65 offen\n"
    "s_mov_b32 s65, 98304\n"
    "buffer_load_dwordx4 a[228:231], v208, s[76:79], s65 offen\n"
    "s_mov_b32 s65, 196608\n"
    "buffer_load_dwordx4 a[232:235], v208, s[40:43], s65 offen\n"
    "s_mov_b32 s65, 294912\n"
    "buffer_load_dwordx4 a[236:239], v208, s[40:43], s65 offen\n"
    "buffer_load_dword v224, v211, s[88:91], 0 offen offset:0\n"
    "buffer_load_dword v225, v211, s[88:91], 0 offen offset:192\n"
    "buffer_load_dword v226, v211, s[88:91], 0 offen offset:384\n"
    "buffer_load_dword v227, v211, s[88:91], 0 offen offset:576\n"
    "buffer_load_dword v228, v211, s[88:91], 0 offen offset:768\n"
    "v_mov_b32_e64 v88, 0\n"
    "v_mov_b32_e32 v240, 0xdddd8888\n"
    "v_mov_b32_e64 v89, 0\n"
    "v_mov_b32_e64 v200, 0\n"
    "v_mov_b32_e64 v90, 0\n"
    "v_mov_b32_e64 v91, 0\n"
    "v_mov_b32_e64 v59, 0\n"
    "v_mov_b32_e64 v60, 0\n"
    "v_mov_b32_e64 v67, 0\n"
    "v_mov_b32_e64 v68, 0\n"
    "v_mov_b32_e64 v75, 0\n"
    "v_mov_b32_e64 v76, 0\n"
    "v_mov_b32_e64 v83, 0\n"
    "v_mov_b32_e64 v84, 0\n"
    "v_mov_b32_e32 v36, 0x75089ba4\n"
    "v_mov_b32_e32 v38, 0xbdd2d3e8\n"
    "v_mov_b32_e32 v32, 0xc0135761\n"
    "v_lshlrev_b32_e64 v34, 16, v213\n"
    "v_lshlrev_b32_e64 v234, 1, v213\n"
    "v_or_b32_e32 v0, 0x8000, v34\n"
    "v_or_b32_e32 v1, 0x18000, v34\n"
    "v_or_b32_e32 v2, 0x28000, v34\n"
    "v_or_b32_e32 v3, 0x38000, v34\n"
    "v_or_b32_e32 v4, 0x48000, v34\n"
    "v_or_b32_e32 v5, 0x58000, v34\n"
    "v_or_b32_e32 v6, 0x68000, v34\n"
    "v_or_b32_e32 v7, 0x78000, v34\n"
    "v_cmp_ne_u32_e64 s[72:73], 0, v34\n"
    "s_nop 0\n"
    "s_nop 1\n"
    "v_cndmask_b32_e64 v0, 0, v0, s[72:73]\n"
    "v_pk_mul_f32 v[0:1], v[0:1], v[36:37] op_sel_hi:[1,0]\n"
    "v_pk_mul_f32 v[2:3], v[2:3], v[36:37] op_sel_hi:[1,0]\n"
    "v_pk_mul_f32 v[4:5], v[4:5], v[36:37] op_sel_hi:[1,0]\n"
    "v_pk_mul_f32 v[6:7], v[6:7], v[36:37] op_sel_hi:[1,0]\n"
    "v_pk_mul_f32 v[8:9], v[0:1], v[0:1]\n"
    "v_pk_mul_f32 v[10:11], v[2:3], v[2:3]\n"
    "v_pk_mul_f32 v[12:13], v[4:5], v[4:5]\n"
    "v_pk_mul_f32 v[14:15], v[6:7], v[6:7]\n"
    "v_pk_fma_f32 v[8:9], v[8:9], v[38:39], v[32:33] op_sel_hi:[1,0,0]\n"
    "v_pk_fma_f32 v[10:11], v[10:11], v[38:39], v[32:33] op_sel_hi:[1,0,0]\n"
    "v_pk_fma_f32 v[12:13], v[12:13], v[38:39], v[32:33] op_sel_hi:[1,0,0]\n"
    "v_pk_fma_f32 v[14:15], v[14:15], v[38:39], v[32:33] op_sel_hi:[1,0,0]\n"
    "v_pk_mul_f32 v[8:9], v[8:9], v[0:1]\n"
    "v_pk_mul_f32 v[10:11], v[10:11], v[2:3]\n"
    "v_pk_mul_f32 v[12:13], v[12:13], v[4:5]\n"
    "v_pk_mul_f32 v[14:15], v[14:15], v[6:7]\n"
    "v_exp_f32_e64 v8, v8\n"
    "v_exp_f32_e64 v9, v9\n"
    "v_exp_f32_e64 v10, v10\n"
    "v_exp_f32_e64 v11, v11\n"
    "v_exp_f32_e64 v12, v12\n"
    "v_exp_f32_e64 v13, v13\n"
    "v_exp_f32_e64 v14, v14\n"
    "v_exp_f32_e64 v15, v15\n"
    "v_pk_add_f32 v[16:17], v[8:9], 1.0 op_sel_hi:[1,0]\n"
    "v_pk_add_f32 v[18:19], v[10:11], 1.0 op_sel_hi:[1,0]\n"
    "v_pk_add_f32 v[20:21], v[12:13], 1.0 op_sel_hi:[1,0]\n"
    "v_pk_add_f32 v[22:23], v[14:15], 1.0 op_sel_hi:[1,0]\n"
    "v_rcp_f32_e64 v16, v16\n"
    "v_rcp_f32_e64 v17, v17\n"
    "v_rcp_f32_e64 v18, v18\n"
    "v_rcp_f32_e64 v19, v19\n"
    "v_rcp_f32_e64 v20, v20\n"
    "v_rcp_f32_e64 v21, v21\n"
    "v_rcp_f32_e64 v22, v22\n"
    "v_rcp_f32_e64 v23, v23\n"
    "v_pk_mul_f32 v[16:17], v[16:17], v[0:1]\n"
    "v_pk_mul_f32 v[18:19], v[18:19], v[2:3]\n"
    "v_pk_mul_f32 v[20:21], v[20:21], v[4:5]\n"
    "v_pk_mul_f32 v[22:23], v[22:23], v[6:7]\n"
    "v_pk_mul_f32 v[8:9], v[8:9], v[16:17]\n"
    "v_pk_mul_f32 v[10:11], v[10:11], v[18:19]\n"
    "v_pk_mul_f32 v[12:13], v[12:13], v[20:21]\n"
    "v_pk_mul_f32 v[14:15], v[14:15], v[22:23]\n"
    "v_cvt_pk_bf16_f32 v188, v16, v17\n"
    "v_cvt_pk_bf16_f32 v189, v18, v19\n"
    "v_cvt_pk_bf16_f32 v190, v20, v21\n"
    "v_cvt_pk_bf16_f32 v191, v22, v23\n"
    "v_cvt_pk_bf16_f32 v24, -v8, -v9\n"
    "v_cvt_pk_bf16_f32 v25, -v10, -v11\n"
    "v_cvt_pk_bf16_f32 v26, -v12, -v13\n"
    "v_cvt_pk_bf16_f32 v27, -v14, -v15\n"
    "ds_write_b128 v234, v[24:27] offset:0\n"
    "v_or_b32_e32 v0, 0x88000, v34\n"
    "v_or_b32_e32 v1, 0x98000, v34\n"
    "v_or_b32_e32 v2, 0xa8000, v34\n"
    "v_or_b32_e32 v3, 0xb8000, v34\n"
    "v_or_b32_e32 v4, 0xc8000, v34\n"
    "v_or_b32_e32 v5, 0xd8000, v34\n"
    "v_or_b32_e32 v6, 0xe8000, v34\n"
    "v_or_b32_e32 v7, 0xf8000, v34\n"
    "v_pk_mul_f32 v[0:1], v[0:1], v[36:37] op_sel_hi:[1,0]\n"
    "v_pk_mul_f32 v[2:3], v[2:3], v[36:37] op_sel_hi:[1,0]\n"
    "v_pk_mul_f32 v[4:5], v[4:5], v[36:37] op_sel_hi:[1,0]\n"
    "v_pk_mul_f32 v[6:7], v[6:7], v[36:37] op_sel_hi:[1,0]\n"
    "v_pk_mul_f32 v[8:9], v[0:1], v[0:1]\n"
    "v_pk_mul_f32 v[10:11], v[2:3], v[2:3]\n"
    "v_pk_mul_f32 v[12:13], v[4:5], v[4:5]\n"
    "v_pk_mul_f32 v[14:15], v[6:7], v[6:7]\n"
    "v_pk_fma_f32 v[8:9], v[8:9], v[38:39], v[32:33] op_sel_hi:[1,0,0]\n"
    "v_pk_fma_f32 v[10:11], v[10:11], v[38:39], v[32:33] op_sel_hi:[1,0,0]\n"
    "v_pk_fma_f32 v[12:13], v[12:13], v[38:39], v[32:33] op_sel_hi:[1,0,0]\n"
    "v_pk_fma_f32 v[14:15], v[14:15], v[38:39], v[32:33] op_sel_hi:[1,0,0]\n"
    "v_pk_mul_f32 v[8:9], v[8:9], v[0:1]\n"
    "v_pk_mul_f32 v[10:11], v[10:11], v[2:3]\n"
    "v_pk_mul_f32 v[12:13], v[12:13], v[4:5]\n"
    "v_pk_mul_f32 v[14:15], v[14:15], v[6:7]\n"
    "v_exp_f32_e64 v8, v8\n"
    "v_exp_f32_e64 v9, v9\n"
    "v_exp_f32_e64 v10, v10\n"
    "v_exp_f32_e64 v11, v11\n"
    "v_exp_f32_e64 v12, v12\n"
    "v_exp_f32_e64 v13, v13\n"
    "v_exp_f32_e64 v14, v14\n"
    "v_exp_f32_e64 v15, v15\n"
    "v_pk_add_f32 v[16:17], v[8:9], 1.0 op_sel_hi:[1,0]\n"
    "v_pk_add_f32 v[18:19], v[10:11], 1.0 op_sel_hi:[1,0]\n"
    "v_pk_add_f32 v[20:21], v[12:13], 1.0 op_sel_hi:[1,0]\n"
    "v_pk_add_f32 v[22:23], v[14:15], 1.0 op_sel_hi:[1,0]\n"
    "v_rcp_f32_e64 v16, v16\n"
    "v_rcp_f32_e64 v17, v17\n"
    "v_rcp_f32_e64 v18, v18\n"
    "v_rcp_f32_e64 v19, v19\n"
    "v_rcp_f32_e64 v20, v20\n"
    "v_rcp_f32_e64 v21, v21\n"
    "v_rcp_f32_e64 v22, v22\n"
    "v_rcp_f32_e64 v23, v23\n"
    "v_pk_mul_f32 v[16:17], v[16:17], v[0:1]\n"
    "v_pk_mul_f32 v[18:19], v[18:19], v[2:3]\n"
    "v_pk_mul_f32 v[20:21], v[20:21], v[4:5]\n"
    "v_pk_mul_f32 v[22:23], v[22:23], v[6:7]\n"
    "v_pk_mul_f32 v[8:9], v[8:9], v[16:17]\n"
    "v_pk_mul_f32 v[10:11], v[10:11], v[18:19]\n"
    "v_pk_mul_f32 v[12:13], v[12:13], v[20:21]\n"
    "v_pk_mul_f32 v[14:15], v[14:15], v[22:23]\n"
    "v_cvt_pk_bf16_f32 v192, v16, v17\n"
    "v_cvt_pk_bf16_f32 v193, v18, v19\n"
    "v_cvt_pk_bf16_f32 v194, v20, v21\n"
    "v_cvt_pk_bf16_f32 v195, v22, v23\n"
    "v_cvt_pk_bf16_f32 v24, -v8, -v9\n"
    "v_cvt_pk_bf16_f32 v25, -v10, -v11\n"
    "v_cvt_pk_bf16_f32 v26, -v12, -v13\n"
    "v_cvt_pk_bf16_f32 v27, -v14, -v15\n"
    "ds_write_b128 v234, v[24:27] offset:16\n"
    "v_add_u32_e32 v234, 65536, v234\n"
    "buffer_load_dword v229, v212, s[92:95], 0 offen offset:0\n"
    "s_nop 0\n"
    "s_waitcnt vmcnt(13)\n"
    "v_lshlrev_b32_sdwa v216, 2, v168 dst_sel:DWORD dst_unused:UNUSED_PAD src0_sel:DWORD src1_sel:WORD_0\n"
    "v_lshlrev_b32_sdwa v217, 2, v168 dst_sel:DWORD dst_unused:UNUSED_PAD src0_sel:DWORD src1_sel:WORD_1\n"
    "v_lshlrev_b32_sdwa v218, 2, v169 dst_sel:DWORD dst_unused:UNUSED_PAD src0_sel:DWORD src1_sel:WORD_0\n"
    "v_lshlrev_b32_sdwa v219, 2, v169 dst_sel:DWORD dst_unused:UNUSED_PAD src0_sel:DWORD src1_sel:WORD_1\n"
    "v_lshlrev_b32_sdwa v220, 2, v170 dst_sel:DWORD dst_unused:UNUSED_PAD src0_sel:DWORD src1_sel:WORD_0\n"
    "v_lshlrev_b32_sdwa v221, 2, v170 dst_sel:DWORD dst_unused:UNUSED_PAD src0_sel:DWORD src1_sel:WORD_1\n"
    "v_lshlrev_b32_sdwa v222, 2, v171 dst_sel:DWORD dst_unused:UNUSED_PAD src0_sel:DWORD src1_sel:WORD_0\n"
    "v_lshlrev_b32_sdwa v223, 2, v171 dst_sel:DWORD dst_unused:UNUSED_PAD src0_sel:DWORD src1_sel:WORD_1\n"
    "ds_write_b128 v222, v[88:91]\n"
    "buffer_load_dword v230, v212, s[92:95], 0 offen offset:192\n"
    "s_nop 0\n"
    "s_waitcnt vmcnt(13)\n"
    "v_lshlrev_b32_sdwa v207, 2, v172 dst_sel:DWORD dst_unused:UNUSED_PAD src0_sel:DWORD src1_sel:WORD_0\n"
    "v_lshlrev_b32_sdwa v242, 2, v173 dst_sel:DWORD dst_unused:UNUSED_PAD src0_sel:DWORD src1_sel:WORD_0\n"
    "v_lshlrev_b32_sdwa v243, 2, v173 dst_sel:DWORD dst_unused:UNUSED_PAD src0_sel:DWORD src1_sel:WORD_1\n"
    "v_lshlrev_b32_sdwa v244, 2, v174 dst_sel:DWORD dst_unused:UNUSED_PAD src0_sel:DWORD src1_sel:WORD_0\n"
    "v_lshlrev_b32_sdwa v248, 2, v174 dst_sel:DWORD dst_unused:UNUSED_PAD src0_sel:DWORD src1_sel:WORD_1\n"
    "v_lshlrev_b32_sdwa v249, 2, v175 dst_sel:DWORD dst_unused:UNUSED_PAD src0_sel:DWORD src1_sel:WORD_0\n"
    "v_lshlrev_b32_sdwa v250, 2, v175 dst_sel:DWORD dst_unused:UNUSED_PAD src0_sel:DWORD src1_sel:WORD_1\n"
    "v_bfe_i32 v201, v172, 16, 1\n"
    "v_and_b32_e32 v201, 0x35003500, v201\n"
    "v_bfe_i32 v203, v172, 18, 1\n"
    "v_and_b32_e32 v203, 0x35003500, v203\n"
    "v_bfe_i32 v205, v172, 20, 1\n"
    "v_and_b32_e32 v205, 0x35003500, v205\n"
    "v_bfe_i32 v92, v172, 26, 1\n"
    "v_xor_b32_e32 v92, 0xffff, v92\n"
    "v_and_b32_e32 v92, 0x3f803f80, v92\n"
    "v_bfe_i32 v236, v172, 22, 1\n"
    "v_and_b32_e64 v236, v92, v236\n"
    "v_bfe_i32 v237, v172, 23, 1\n"
    "v_and_b32_e32 v237, v92, v237\n"
    "buffer_load_dword v231, v212, s[92:95], 0 offen offset:384\n"
    "s_waitcnt vmcnt(13)\n"
    "v_lshlrev_b32_sdwa v251, 2, v176 dst_sel:DWORD dst_unused:UNUSED_PAD src0_sel:DWORD src1_sel:WORD_0\n"
    "v_lshlrev_b32_sdwa v252, 2, v176 dst_sel:DWORD dst_unused:UNUSED_PAD src0_sel:DWORD src1_sel:WORD_1\n"
    "v_lshlrev_b32_sdwa v253, 2, v177 dst_sel:DWORD dst_unused:UNUSED_PAD src0_sel:DWORD src1_sel:WORD_0\n"
    "v_lshlrev_b32_sdwa v254, 2, v177 dst_sel:DWORD dst_unused:UNUSED_PAD src0_sel:DWORD src1_sel:WORD_1\n"
    "v_lshlrev_b32_sdwa v241, 0, v178 dst_sel:DWORD dst_unused:UNUSED_PAD src0_sel:DWORD src1_sel:WORD_0\n"
    "v_lshlrev_b32_sdwa v245, 4, v178 dst_sel:DWORD dst_unused:UNUSED_PAD src0_sel:DWORD src1_sel:WORD_1\n"
    "v_lshlrev_b32_sdwa v255, 4, v179 dst_sel:DWORD dst_unused:UNUSED_PAD src0_sel:DWORD src1_sel:WORD_0\n"
    "v_lshlrev_b32_sdwa v247, 4, v179 dst_sel:DWORD dst_unused:UNUSED_PAD src0_sel:DWORD src1_sel:WORD_1\n"
    "v_bfe_i32 v238, v172, 24, 1\n"
    "v_and_b32_e64 v238, v92, v238\n"
    "v_bfe_i32 v239, v172, 25, 1\n"
    "v_and_b32_e64 v239, v92, v239\n"
    "v_bfe_u32 v93, v172, 28, 2\n"
    "v_mul_u32_u24_e32 v93, 393216, v93\n"
    "v_mov_b32_e64 v222, v254\n"
    "v_mov_b32_e32 v254, 0\n"
    "s_nop 1\n"
    "v_mfma_f32_32x32x1_2b_f32 v[0:31], v254, v254, 0\n"
    "global_load_dword v233, v214, s[70:71]\n"
    "s_mov_b32 s48, 0x44ea44ea\n"
    "s_mov_b32 s49, 0xb77fb77f\n"
    "s_mov_b32 s50, 0x89efde6e\n"
    "s_mov_b32 s46, s42\n"
    "s_mov_b32 s47, s43\n"
    "s_mul_i32 s54, s60, 592\n"
    "s_add_u32 s54, s54, -27008\n"
    "s_mul_i32 s55, s60, -1284\n"
    "s_add_u32 s55, s55, 54688\n"
    "s_mul_i32 s56, s60, -6160\n"
    "s_add_u32 s56, s56, -27008\n"
    "s_add_u32 s98, s99, 1375872\n"
    "s_and_b32 s36, s60, 1\n"
    "s_lshl_b32 s36, s36, 13\n"
    "s_add_u32 s36, s36, 327680\n"
    "s_mul_i32 s53, s60, 98304\n"
    "s_add_u32 s98, s53, s98\n"
    "s_xor_b32 s53, s60, 1\n"
    "s_mul_i32 s53, s53, 393216\n"
    "s_add_u32 s99, s99, s53\n"
    "s_add_u32 s53, s98, 393216\n"
    "s_sub_u32 s53, s53, s36\n"
    "s_and_b32 s37, s61, 1\n"
    "s_lshl_b32 s37, s37, 2\n"
    "s_add_u32 s37, s37, s60\n"
    "s_lshl_b32 s37, s37, 12\n"
    "s_cmp_eq_u32 s61, 0\n"
    "s_cselect_b32 s67, 2, 0\n"
    "s_cmp_eq_u32 s61, 31\n"
    "s_cselect_b32 s67, 1, s67\n"
    "s_lshl_b32 s67, s67, 30\n"
    "s_mov_b64 s[68:69], s[44:45]\n"
    "s_mov_b32 s70, 0\n"
    "s_mov_b32 s71, s47\n"
    "s_mov_b64 s[72:73], s[40:41]\n"
    "s_mov_b64 s[74:75], s[42:43]\n"
    "s_mov_b32 s57, 0\n"
    "s_mov_b32 s58, 196608\n"
    "s_mov_b32 s60, 0\n"
    "s_mov_b32 s61, 393216\n"
    "s_mov_b32 s62, 786432\n"
    "s_mov_b32 s63, 1179648\n"
    "s_mov_b32 s52, 1572864\n"
    "s_mov_b32 s88, 98304\n"
    "s_mov_b32 s89, 294912\n"
    "s_mov_b32 s39, 0\n"
    "s_getpc_b64 s[76:77]\n"
    ".Lreplk_pc1:\n"
    "s_add_u32 s76, s76, .Lreplk_last-.Lreplk_pc1\n"
    "s_addc_u32 s77, s77, 0\n"
    "s_sub_u32 s76, s76, s36\n"
    "s_subb_u32 s77, s77, 0\n"
    "s_add_u32 s78, s36, .Lreplk_end-.Lreplk_last\n"
    "s_mov_b32 s79, s37\n"
    "s_add_u32 m0, s38, 0\n"
    "s_mov_b32 s64, 0\n"
    "buffer_load_dwordx4 v210, s[84:87], s64 offen offset:0 lds\n"
    "s_waitcnt vmcnt(13)\n"
    "ds_write2_b32 v216, a224, a225 offset0:0 offset1:20\n"
    "v_add3_u32 v247, v247, v93, s98\n"
    "v_add_u32_e32 v246, s98, v255\n"
    "v_add_u32_e64 v245, s99, v245\n"
    "v_add_u32_e64 v255, s99, v255\n"
    "ds_write2_b32 v216, a226, a227 offset0:40 offset1:60\n"
    "v_and_b32_e64 v93, s67, v172\n"
    "v_and_b32_e32 v94, 0x80000000, v93\n"
    "s_waitcnt vmcnt(12)\n"
    "ds_write2_b32 v216, a228, a229 offset0:80 offset1:100\n"
    "v_and_b32_e32 v93, 0x40000000, v93\n"
    "v_or_b32_e64 v246, v246, v94\n"
    "v_or_b32_e64 v247, v247, v93\n"
    "ds_write2_b32 v216, a230, a231 offset0:120 offset1:140\n"
    "v_add_u32_e32 v216, 1360, v216\n"
    "buffer_load_dwordx4 v210, s[84:87], s64 offen offset:1024 lds\n"
    "s_waitcnt vmcnt(12)\n"
    "ds_write2_b32 v216, a232, a233 offset0:0 offset1:20\n"
    "ds_write2_b32 v216, a234, a235 offset0:40 offset1:60\n"
    "buffer_load_dwordx4 v210, s[84:87], s64 offen offset:2048 lds\n"
    "s_waitcnt vmcnt(12)\n"
    "ds_write2_b32 v216, a236, a237 offset0:80 offset1:100\n"
    "s_add_u32 m0, s38, 3072\n"
    "s_mov_b32 s64, 3072\n"
    "buffer_load_dwordx4 v210, s[84:87], s64 offen offset:0 lds\n"
    "buffer_load_dwordx4 v210, s[84:87], s64 offen offset:1024 lds\n"
    "ds_write2_b32 v216, a238, a239 offset0:120 offset1:140\n"
    "buffer_load_dwordx4 v210, s[84:87], s64 offen offset:2048 lds\n"
    "s_add_u32 m0, s38, 6144\n"
    "s_mov_b32 s64, 6144\n"
    "buffer_load_dwordx4 v210, s[84:87], s64 offen offset:0 lds\n"
    "buffer_load_dwordx4 v210, s[84:87], s64 offen offset:1024 lds\n"
    "v_add_u32_e32 v216, 1360, v216\n"
    "s_waitcnt vmcnt(16)\n"
    "v_lshlrev_b32_e32 v40, 16, v224\n"
    "buffer_load_dwordx4 v210, s[84:87], s64 offen offset:2048 lds\n"
    "s_mov_b32 s65, 589824\n"
    "buffer_load_dwordx4 a[240:243], v208, s[40:43], s65 offen\n"
    "s_waitcnt vmcnt(17)\n"
    "v_lshlrev_b32_e32 v41, 16, v225\n"
    "s_mov_b32 s65, 688128\n"
    "buffer_load_dwordx4 a[244:247], v208, s[40:43], s65 offen\n"
    "s_mov_b32 s59, 49152\n"
    "buffer_load_dwordx4 v[96:99], v209, s[80:83], s59 offen offset:0\n"
    "s_waitcnt vmcnt(18)\n"
    "v_lshlrev_b32_e32 v42, 16, v226\n"
    "s_waitcnt vmcnt(17)\n"
    "v_lshlrev_b32_e32 v43, 16, v227\n"
    "s_waitcnt vmcnt(16)\n"
    "v_lshlrev_b32_e32 v44, 16, v228\n"
    "s_waitcnt vmcnt(15)\n"
    "v_lshlrev_b32_e32 v45, 16, v229\n"
    "s_waitcnt vmcnt(14)\n"
    "v_lshlrev_b32_e32 v46, 16, v230\n"
    "s_waitcnt vmcnt(13)\n"
    "v_lshlrev_b32_e32 v47, 16, v231\n"
    "v_add_f32_e64 v41, v41, v45\n"
    "v_add_f32_e64 v42, v42, v46\n"
    "v_add_f32_e64 v43, v43, v47\n"
    "v_cvt_pk_bf16_f32 v56, v40, v41\n"
    "v_cvt_pk_bf16_f32 v57, v42, v43\n"
    "v_cvt_pk_bf16_f32 v58, v44, 0\n"
    "v_cvt_pk_bf16_f32 v64, 0, v40\n"
    "buffer_load_dwordx4 v[100:103], v209, s[80:83], s59 offen offset:256\n"
    "ds_write_b128 v221, v[56:59] offset:0\n"
    "v_cvt_pk_bf16_f32 v65, v41, v42\n"
    "v_cvt_pk_bf16_f32 v66, v43, v44\n"
    "v_cvt_pk_bf16_f32 v61, v40, v41\n"
    "v_cvt_pk_bf16_f32 v62, v42, v43\n"
    "v_cvt_pk_bf16_f32 v63, v44, 0\n"
    "v_cvt_pk_bf16_f32 v69, 0, v40\n"
    "v_cvt_pk_bf16_f32 v70, v41, v42\n"
    "v_cvt_pk_bf16_f32 v71, v43, v44\n"
    "ds_write_b128 v221, v[64:67] offset:16\n"
    "buffer_load_dwordx4 v[104:107], v209, s[80:83], s59 offen offset:512\n"
    "v_and_b32_e32 v48, 0xffff0000, v224\n"
    "v_and_b32_e32 v49, 0xffff0000, v225\n"
    "v_and_b32_e32 v50, 0xffff0000, v226\n"
    "v_and_b32_e32 v51, 0xffff0000, v227\n"
    "v_and_b32_e32 v52, 0xffff0000, v228\n"
    "ds_write_b128 v221, v[60:63] offset:32\n"
    "v_and_b32_e32 v53, 0xffff0000, v229\n"
    "v_and_b32_e32 v54, 0xffff0000, v230\n"
    "v_and_b32_e32 v55, 0xffff0000, v231\n"
    "v_add_f32_e64 v49, v49, v53\n"
    "v_add_f32_e64 v50, v50, v54\n"
    "v_add_f32_e64 v51, v51, v55\n"
    "ds_write_b128 v221, v[68:71] offset:48\n"
    "s_mov_b32 s65, 983040\n"
    "buffer_load_dwordx4 a[248:251], v208, s[40:43], s65 offen\n"
    "v_cvt_pk_bf16_f32 v72, v48, v49\n"
    "v_cvt_pk_bf16_f32 v73, v50, v51\n"
    "v_cvt_pk_bf16_f32 v74, v52, 0\n"
    "v_cvt_pk_bf16_f32 v80, 0, v48\n"
    "v_cvt_pk_bf16_f32 v81, v49, v50\n"
    "v_cvt_pk_bf16_f32 v82, v51, v52\n"
    "v_cvt_pk_bf16_f32 v77, v48, v49\n"
    "v_cvt_pk_bf16_f32 v78, v50, v51\n"
    "v_cvt_pk_bf16_f32 v79, v52, 0\n"
    "v_cvt_pk_bf16_f32 v85, 0, v48\n"
    "v_cvt_pk_bf16_f32 v86, v49, v50\n"
    "v_cvt_pk_bf16_f32 v87, v51, v52\n"
    "ds_write_b128 v221, v[72:75] offset:336\n"
    "s_mov_b32 s65, 1081344\n"
    "buffer_load_dwordx4 a[252:255], v208, s[40:43], s65 offen\n"
    "v_xor_b32_e32 v224, 0, v219\n"
    "v_xor_b32_e32 v225, 32, v219\n"
    "v_xor_b32_e32 v226, 64, v219\n"
    "v_xor_b32_e32 v227, 96, v219\n"
    "ds_write_b128 v221, v[80:83] offset:352\n"
    "v_xor_b32_e32 v228, 0, v223\n"
    "v_xor_b32_e32 v229, 32, v223\n"
    "s_mov_b32 s59, 52224\n"
    "buffer_load_dwordx4 v[108:111], v209, s[80:83], s59 offen offset:0\n"
    "v_xor_b32_e32 v230, 64, v223\n"
    "v_xor_b32_e32 v231, 96, v223\n"
    "ds_write_b128 v221, v[76:79] offset:368\n"
    "v_lshl_add_u32 v196, s51, 8, v225\n"
    "v_lshl_add_u32 v197, s51, 8, v227\n"
    "v_mfma_f32_32x32x1_2b_f32 v[32:63], v254, v254, 0\n"
    "ds_write_b128 v221, v[84:87] offset:384\n"
    "buffer_load_dwordx4 v[112:115], v209, s[80:83], s59 offen offset:256\n"
    "v_mfma_f32_32x32x1_2b_f32 v[64:95], v254, v254, 0\n"
    "v_add_u32_e64 v254, s54, v242\n"
    "buffer_load_dwordx4 v[116:119], v209, s[80:83], s59 offen offset:512\n"
    "s_mov_b32 s65, 1376256\n"
    "buffer_load_dwordx4 a[176:179], v208, s[40:43], s65 offen\n"
    "s_mov_b32 s65, 1474560\n"
    "buffer_load_dwordx4 a[180:183], v208, s[40:43], s65 offen\n"
    "s_waitcnt vmcnt(12)\n"
    "s_barrier\n"
    "ds_read_b64_tr_b16 a[0:1], v220 offset:0\n"
    "ds_read_b64_tr_b16 a[2:3], v220 offset:3072\n"
    "ds_read_b64_tr_b16 a[4:5], v220 offset:8\n"
    "ds_read_b64_tr_b16 a[6:7], v220 offset:3080\n"
    "ds_read_b64_tr_b16 a[8:9], v220 offset:64\n"
    "ds_read_b64_tr_b16 a[10:11], v220 offset:3136\n"
    "ds_read_b64_tr_b16 a[12:13], v220 offset:72\n"
    "ds_read_b64_tr_b16 a[14:15], v220 offset:3144\n"
    "ds_read_b64_tr_b16 v[168:169], v218 offset:128\n"
    "ds_read_b64_tr_b16 v[170:171], v218 offset:3200\n"
    "ds_read_b64_tr_b16 a[16:17], v220 offset:6144\n"
    "ds_read_b64_tr_b16 a[18:19], v220 offset:9216\n"
    "ds_read_b64_tr_b16 a[20:21], v220 offset:6152\n"
    "ds_read_b64_tr_b16 a[22:23], v220 offset:9224\n"
    "ds_read_b64_tr_b16 a[24:25], v220 offset:6208\n"
    "ds_read_b64_tr_b16 a[26:27], v220 offset:9280\n"
    "ds_read_b64_tr_b16 a[28:29], v220 offset:6216\n"
    "ds_read_b64_tr_b16 a[30:31], v220 offset:9288\n"
    "ds_read_b64_tr_b16 v[172:173], v218 offset:6272\n"
    "s_nop 0\n"
    "s_mov_b32 s59, 0\n"
    "buffer_load_dwordx4 v[120:123], v209, s[80:83], s59 offen offset:0\n"
    "ds_read_b64_tr_b16 v[174:175], v218 offset:9344\n"
    "ds_read_b64_tr_b16 a[32:33], v220 offset:12288\n"
    "ds_read_b64_tr_b16 a[34:35], v220 offset:15360\n"
    "ds_read_b64_tr_b16 a[36:37], v220 offset:12296\n"
    "ds_read_b64_tr_b16 a[38:39], v220 offset:15368\n"
    "ds_read_b64_tr_b16 a[40:41], v220 offset:12352\n"
    "ds_read_b64_tr_b16 a[42:43], v220 offset:15424\n"
    "ds_read_b64_tr_b16 a[44:45], v220 offset:12360\n"
    "ds_read_b64_tr_b16 a[46:47], v220 offset:15432\n"
    "ds_read_b64_tr_b16 v[176:177], v218 offset:12416\n"
    "ds_read_b64_tr_b16 v[178:179], v218 offset:15488\n"
    "s_waitcnt vmcnt(12)\n"
    "ds_write2_b32 v216, a240, a241 offset0:0 offset1:20\n"
    "ds_write2_b32 v216, a242, a243 offset0:40 offset1:60\n"
    "buffer_load_dwordx4 v[124:127], v209, s[80:83], s59 offen offset:256\n"
    "s_waitcnt vmcnt(12)\n"
    "ds_write2_b32 v216, a244, a245 offset0:80 offset1:100\n"
    "s_waitcnt lgkmcnt(2)\n"
    "s_barrier\n"
    "ds_write2_b32 v216, a246, a247 offset0:120 offset1:140\n"
    "buffer_load_dwordx4 v[128:131], v209, s[80:83], s59 offen offset:512\n"
    "v_add_u32_e32 v216, 1360, v216\n"
    "s_waitcnt vmcnt(12)\n"
    "v_or3_b32 v200, v200, v96, v97\n"
    "v_or3_b32 v200, v200, v98, v99\n"
    "v_and_b32_e32 v232, 0x7fff7fff, v96\n"
    "v_add_u32_e32 v96, v201, v96\n"
    "v_add_u32_e64 v98, v201, v98\n"
    "ds_write_b128 v217, v[96:99] offset:24576\n"
    "s_mov_b32 s65, 0\n"
    "buffer_load_dwordx4 a[184:187], v208, s[40:43], s65 offen\n"
    "s_waitcnt vmcnt(12)\n"
    "v_or3_b32 v200, v200, v100, v101\n"
    "v_or3_b32 v200, v200, v102, v103\n"
    "v_add_u32_e64 v100, v203, v100\n"
    "v_add_u32_e64 v102, v203, v102\n"
    "ds_write_b128 v217, v[100:103] offset:24832\n"
    "s_mov_b32 s65, 98304\n"
    "buffer_load_dwordx4 a[188:191], v208, s[40:43], s65 offen\n"
    "s_waitcnt vmcnt(12)\n"
    "v_or3_b32 v200, v200, v104, v105\n"
    "v_or3_b32 v200, v200, v106, v107\n"
    "v_add_u32_e32 v104, v205, v104\n"
    "v_add_u32_e64 v106, v205, v106\n"
    "ds_write_b128 v217, v[104:107] offset:25088\n"
    "s_waitcnt lgkmcnt(0)\n"
    "s_barrier\n"
    "s_mov_b32 s59, 3072\n"
    "buffer_load_dwordx4 v[132:135], v209, s[80:83], s59 offen offset:0\n"
    "ds_read_b64_tr_b16 a[48:49], v224 offset:24576\n"
    "ds_read_b64_tr_b16 v[180:181], v196 offset:24576\n"
    "ds_read_b64_tr_b16 a[52:53], v226 offset:24576\n"
    "ds_read_b64_tr_b16 v[184:185], v197 offset:24576\n"
    "ds_read_b64_tr_b16 a[56:57], v228 offset:24576\n"
    "ds_read_b64_tr_b16 a[60:61], v229 offset:24576\n"
    "ds_read_b64_tr_b16 a[64:65], v230 offset:24576\n"
    "ds_read_b64_tr_b16 a[68:69], v231 offset:24576\n"
    "ds_read_b64_tr_b16 a[72:73], v224 offset:24832\n"
    "ds_read_b64_tr_b16 a[76:77], v226 offset:24832\n"
    "s_waitcnt vmcnt(12)\n"
    "ds_write2_b32 v216, a248, a249 offset0:0 offset1:20\n"
    "ds_write2_b32 v216, a250, a251 offset0:40 offset1:60\n"
    "buffer_load_dwordx4 v[136:139], v209, s[80:83], s59 offen offset:256\n"
    "s_waitcnt vmcnt(12)\n"
    "ds_write2_b32 v216, a252, a253 offset0:80 offset1:100\n"
    "ds_write2_b32 v216, a254, a255 offset0:120 offset1:140\n"
    "v_add_u32_e32 v216, 1360, v216\n"
    "buffer_load_dwordx4 v[140:143], v209, s[80:83], s59 offen offset:512\n"
    "s_waitcnt vmcnt(12)\n"
    "v_or3_b32 v200, v200, v108, v109\n"
    "v_or3_b32 v200, v200, v110, v111\n"
    "v_add_u32_e32 v108, v201, v108\n"
    "v_add_u32_e64 v110, v201, v110\n"
    "ds_write_b128 v217, v[108:111] offset:0\n"
    "s_mov_b32 s65, 393216\n"
    "buffer_load_dwordx4 a[192:195], v208, s[40:43], s65 offen\n"
    "s_waitcnt vmcnt(12)\n"
    "v_or3_b32 v200, v200, v112, v113\n"
    "v_or3_b32 v200, v200, v114, v115\n"
    "v_add_u32_e32 v112, v203, v112\n"
    "v_add_u32_e64 v114, v203, v114\n"
    "ds_write_b128 v217, v[112:115] offset:256\n"
    "v_mov_b32_e64 v112, v188\n"
    "v_mov_b32_e64 v113, v189\n"
    "v_mov_b32_e64 v114, v190\n"
    "v_mov_b32_e32 v115, v191\n"
    "s_mov_b32 s65, 491520\n"
    "buffer_load_dwordx4 a[196:199], v208, s[40:43], s65 offen\n"
    "s_waitcnt vmcnt(12)\n"
    "v_or3_b32 v200, v200, v116, v117\n"
    "v_or3_b32 v200, v200, v118, v119\n"
    "v_add_u32_e64 v116, v205, v116\n"
    "v_add_u32_e64 v118, v205, v118\n"
    "ds_write_b128 v217, v[116:119] offset:512\n"
    "v_mov_b32_e64 v116, v192\n"
    "v_mov_b32_e64 v117, v193\n"
    "v_mov_b32_e64 v118, v194\n"
    "v_mov_b32_e64 v119, v195\n"
    "s_waitcnt lgkmcnt(0)\n"
    "s_barrier\n"
    "s_mov_b32 s59, 24576\n"
    "buffer_load_dwordx4 v[144:147], v209, s[80:83], s59 offen offset:0\n"
    "ds_read_b64_tr_b16 a[50:51], v224 offset:0\n"
    "ds_read_b64_tr_b16 v[182:183], v196 offset:0\n"
    "ds_read_b64_tr_b16 a[54:55], v226 offset:0\n"
    "ds_read_b64_tr_b16 v[186:187], v197 offset:0\n"
    "ds_read_b64_tr_b16 a[58:59], v228 offset:0\n"
    "ds_read_b64_tr_b16 a[62:63], v229 offset:0\n"
    "ds_read_b64_tr_b16 a[66:67], v230 offset:0\n"
    "ds_read_b64_tr_b16 a[70:71], v231 offset:0\n"
    "ds_read_b64_tr_b16 a[74:75], v224 offset:256\n"
    "ds_read_b64_tr_b16 a[78:79], v226 offset:256\n"
    "s_waitcnt vmcnt(12)\n"
    "ds_write2_b32 v216, a176, a177 offset0:0 offset1:20\n"
    "ds_write2_b32 v216, a178, a179 offset0:40 offset1:60\n"
    "s_waitcnt lgkmcnt(8)\n"
    "ds_write_b128 v207, v[184:187] offset:36864\n"
    "buffer_load_dwordx4 v[148:151], v209, s[80:83], s59 offen offset:256\n"
    "s_waitcnt vmcnt(12)\n"
    "ds_write2_b32 v216, a180, a181 offset0:80 offset1:100\n"
    "ds_write2_b32 v216, a182, a183 offset0:120 offset1:140\n"
    "v_add_u32_e32 v216, -4800, v216\n"
    "buffer_load_dwordx4 v[152:155], v209, s[80:83], s59 offen offset:512\n"
    "s_waitcnt vmcnt(12)\n"
    "v_or3_b32 v200, v200, v120, v121\n"
    "v_or3_b32 v200, v200, v122, v123\n"
    "v_add_u32_e64 v120, v201, v120\n"
    "v_add_u32_e64 v122, v201, v122\n"
    "ds_write_b128 v217, v[120:123] offset:12288\n"
    "s_waitcnt lgkmcnt(1)\n"
    "s_barrier\n"
    "s_mov_b32 s65, 786432\n"
    "buffer_load_dwordx4 a[200:203], v208, s[40:43], s65 offen\n"
    "s_waitcnt vmcnt(12)\n"
    "v_or3_b32 v200, v200, v124, v125\n"
    "v_or3_b32 v200, v200, v126, v127\n"
    "v_add_u32_e32 v124, v203, v124\n"
    "v_add_u32_e64 v126, v203, v126\n"
    "ds_write_b128 v217, v[124:127] offset:12544\n"
    "s_mov_b32 s65, 884736\n"
    "buffer_load_dwordx4 a[204:207], v208, s[40:43], s65 offen\n"
    "s_waitcnt vmcnt(12)\n"
    "v_or3_b32 v200, v200, v128, v129\n"
    "v_or3_b32 v200, v200, v130, v131\n"
    "v_add_u32_e32 v128, v205, v128\n"
    "v_add_u32_e64 v130, v205, v130\n"
    "ds_write_b128 v217, v[128:131] offset:12800\n"
    "ds_read_b128 a[224:227], v250 offset:0\n"
    "ds_read_b128 a[228:231], v250 offset:320\n"
    "ds_read_b128 a[232:235], v251 offset:0\n"
    "ds_read_b128 a[236:239], v251 offset:336\n"
    "ds_read_b128 a[240:243], v250 offset:80\n"
    "ds_read_b128 a[244:247], v250 offset:400\n"
    "ds_read_b128 a[248:251], v251 offset:672\n"
    "ds_read_b128 a[252:255], v251 offset:1008\n"
    "ds_read_b128 v[104:107], v252 offset:1344\n"
    "ds_read_b128 v[108:111], v252 offset:1680\n"
    "ds_read_b128 v[128:131], v252 offset:2016\n"
    "s_waitcnt lgkmcnt(11)\n"
    "s_barrier\n"
    "s_mov_b32 s59, 27648\n"
    "buffer_load_dwordx4 v[156:159], v209, s[80:83], s59 offen offset:0\n"
    "ds_read_b64_tr_b16 a[80:81], v224 offset:12288\n"
    "ds_read_b64_tr_b16 a[84:85], v225 offset:12288\n"
    "ds_read_b64_tr_b16 a[88:89], v226 offset:12288\n"
    "ds_read_b64_tr_b16 a[92:93], v227 offset:12288\n"
    "s_waitcnt lgkmcnt(12)\n"
    "v_smfmac_f32_16x16x64_bf16 v[0:3], a[232:235], a[224:231], v240\n"
    "s_waitcnt lgkmcnt(11)\n"
    "ds_read_b64_tr_b16 a[96:97], v228 offset:12288\n"
    "ds_read_b64_tr_b16 a[100:101], v229 offset:12288\n"
    "v_smfmac_f32_16x16x64_bf16 v[4:7], a[236:239], a[224:231], v240 abid:1\n"
    "ds_read_b64_tr_b16 a[104:105], v230 offset:12288\n"
    "ds_read_b64_tr_b16 a[108:109], v231 offset:12288\n"
    "ds_read_b64_tr_b16 a[112:113], v224 offset:12544\n"
    "ds_read_b64_tr_b16 a[116:117], v225 offset:12544\n"
    "ds_read_b64_tr_b16 a[120:121], v226 offset:12544\n"
    "s_waitcnt lgkmcnt(15)\n"
    "v_smfmac_f32_16x16x64_bf16 v[8:11], a[248:251], a[240:247], v240\n"
    "s_waitcnt lgkmcnt(14)\n"
    "ds_read_b64_tr_b16 a[124:125], v227 offset:12544\n"
    "s_waitcnt vmcnt(12)\n"
    "ds_write2_b32 v216, a184, a185 offset0:0 offset1:20\n"
    "v_smfmac_f32_16x16x64_bf16 v[12:15], a[252:255], a[240:247], v240 abid:1\n"
    "s_nop 0\n"
    "ds_write2_b32 v216, a186, a187 offset0:40 offset1:60\n"
    "ds_read_b128 a[224:227], v250 offset:160\n"
    "ds_read_b128 a[228:231], v250 offset:480\n"
    "ds_read_b128 a[232:235], v251 offset:1344\n"
    "ds_read_b128 a[236:239], v251 offset:1680\n"
    "buffer_load_dwordx4 v[160:163], v209, s[80:83], s59 offen offset:256\n"
    "s_waitcnt vmcnt(12)\n"
    "ds_write2_b32 v216, a188, a189 offset0:80 offset1:100\n"
    "ds_write2_b32 v216, a190, a191 offset0:120 offset1:140\n"
    "v_add_u32_e32 v216, 1360, v216\n"
    "ds_read_b128 a[240:243], v250 offset:240\n"
    "ds_read_b128 a[244:247], v250 offset:560\n"
    "buffer_load_dwordx4 v[164:167], v209, s[80:83], s59 offen offset:512\n"
    "s_waitcnt vmcnt(12)\n"
    "v_or3_b32 v200, v200, v132, v133\n"
    "v_or3_b32 v200, v200, v134, v135\n"
    "v_add_u32_e64 v132, v201, v132\n"
    "v_add_u32_e64 v134, v201, v134\n"
    "ds_write_b128 v217, v[132:135] offset:24576\n"
    "s_waitcnt lgkmcnt(6)\n"
    "v_smfmac_f32_16x16x64_bf16 v[16:19], a[232:235], a[224:231], v240\n"
    "s_waitcnt lgkmcnt(5)\n"
    "v_smfmac_f32_16x16x64_bf16 v[20:23], a[236:239], a[224:231], v240 abid:1\n"
    "s_mov_b32 s65, 1179648\n"
    "buffer_load_dwordx4 a[208:211], v208, s[40:43], s65 offen\n"
    "s_waitcnt vmcnt(12)\n"
    "v_or3_b32 v200, v200, v136, v137\n"
    "v_or3_b32 v200, v200, v138, v139\n"
    "v_add_u32_e32 v136, v203, v136\n"
    "v_add_u32_e64 v138, v203, v138\n"
    "ds_write_b128 v217, v[136:139] offset:24832\n"
    "ds_read_b128 a[248:251], v251 offset:2016\n"
    "ds_read_b128 a[252:255], v251 offset:2352\n"
    "s_mov_b32 s65, 1277952\n"
    "buffer_load_dwordx4 a[212:215], v208, s[40:43], s65 offen\n"
    "s_waitcnt vmcnt(12)\n"
    "v_or3_b32 v200, v200, v140, v141\n"
    "v_or3_b32 v200, v200, v142, v143\n"
    "v_add_u32_e32 v140, v205, v140\n"
    "v_add_u32_e64 v142, v205, v142\n"
    "ds_write_b128 v217, v[140:143] offset:25088\n"
    "ds_read_b128 a[224:227], v250 offset:27024\n"
    "ds_read_b128 a[228:231], v250 offset:27344\n"
    "ds_read_b128 a[232:235], v251 offset:10752\n"
    "ds_read_b128 a[236:239], v251 offset:11088\n"
    "ds_read_b128 v[132:135], v252 offset:2352\n"
    "s_waitcnt lgkmcnt(7)\n"
    "v_smfmac_f32_16x16x64_bf16 v[24:27], a[248:251], a[240:247], v240\n"
    "ds_read_b128 a[248:251], v251 offset:11424\n"
    "s_waitcnt lgkmcnt(7)\n"
    "v_smfmac_f32_16x16x64_bf16 v[28:31], a[252:255], a[240:247], v240 abid:1\n"
    "ds_read_b128 a[240:243], v250 offset:27104\n"
    "ds_read_b128 a[244:247], v250 offset:27424\n"
    "ds_read_b128 a[252:255], v251 offset:11760\n"
    "s_waitcnt lgkmcnt(9)\n"
    "s_barrier\n"
    "s_mov_b32 s65, 1572864\n"
    "buffer_load_dwordx4 a[216:219], v208, s[40:43], s65 offen\n"
    "ds_read_b64_tr_b16 a[82:83], v224 offset:24576\n"
    "ds_read_b64_tr_b16 a[86:87], v225 offset:24576\n"
    "ds_read_b64_tr_b16 a[90:91], v226 offset:24576\n"
    "s_waitcnt lgkmcnt(9)\n"
    "v_smfmac_f32_16x16x64_bf16 v[32:35], a[232:235], a[224:231], v240\n"
    "s_waitcnt lgkmcnt(8)\n"
    "ds_read_b64_tr_b16 a[94:95], v227 offset:24576\n"
    "ds_read_b64_tr_b16 a[98:99], v228 offset:24576\n"
    "v_smfmac_f32_16x16x64_bf16 v[36:39], a[236:239], a[224:231], v240 abid:1\n"
    "ds_read_b64_tr_b16 a[102:103], v229 offset:24576\n"
    "ds_read_b64_tr_b16 a[106:107], v230 offset:24576\n"
    "ds_read_b64_tr_b16 a[110:111], v231 offset:24576\n"
    "ds_read_b64_tr_b16 a[114:115], v224 offset:24832\n"
    "ds_read_b64_tr_b16 a[118:119], v225 offset:24832\n"
    "ds_read_b64_tr_b16 a[122:123], v226 offset:24832\n"
    "ds_read_b64_tr_b16 a[126:127], v227 offset:24832\n"
    "s_waitcnt lgkmcnt(13)\n"
    "v_smfmac_f32_16x16x64_bf16 v[40:43], a[248:251], a[240:247], v240\n"
    "s_waitcnt lgkmcnt(12)\n"
    "s_waitcnt vmcnt(12)\n"
    "ds_write2_b32 v216, a192, a193 offset0:0 offset1:20\n"
    "v_smfmac_f32_16x16x64_bf16 v[44:47], a[252:255], a[240:247], v240 abid:1\n"
    "s_nop 0\n"
    "ds_write2_b32 v216, a194, a195 offset0:40 offset1:60\n"
    "ds_read_b128 a[224:227], v250 offset:27184\n"
    "ds_read_b128 a[228:231], v250 offset:27504\n"
    "ds_read_b128 a[232:235], v251 offset:12096\n"
    "s_mov_b32 s65, 1671168\n"
    "buffer_load_dwordx4 a[220:223], v208, s[40:43], s65 offen\n"
    "s_waitcnt vmcnt(12)\n"
    "ds_write2_b32 v216, a196, a197 offset0:80 offset1:100\n"
    "ds_write2_b32 v216, a198, a199 offset0:120 offset1:140\n"
    "v_add_u32_e32 v216, 1360, v216\n"
    "ds_read_b128 a[236:239], v251 offset:12432\n"
    "ds_read_b128 a[240:243], v250 offset:27264\n"
    "ds_read_b128 a[244:247], v250 offset:27584\n"
    "s_waitcnt vmcnt(11)\n"
    "v_or3_b32 v200, v200, v144, v145\n"
    "v_or3_b32 v200, v200, v146, v147\n"
    "v_add_u32_e64 v144, v201, v144\n"
    "v_add_u32_e32 v146, v201, v146\n"
    "ds_write_b128 v217, v[144:147] offset:0\n"
    "s_waitcnt lgkmcnt(6)\n"
    "v_smfmac_f32_16x16x64_bf16 v[48:51], a[232:235], a[224:231], v240\n"
    "ds_read_b128 a[248:251], v251 offset:12768\n"
    "ds_read_b128 a[252:255], v251 offset:13104\n"
    "ds_read_b128 a[232:235], v251 offset:21504\n"
    "s_waitcnt vmcnt(10)\n"
    "v_or3_b32 v200, v200, v148, v149\n"
    "v_or3_b32 v200, v200, v150, v151\n"
    "v_add_u32_e32 v148, v203, v148\n"
    "v_add_u32_e32 v150, v203, v150\n"
    "ds_write_b128 v217, v[148:151] offset:256\n"
    "s_waitcnt lgkmcnt(6)\n"
    "v_smfmac_f32_16x16x64_bf16 v[52:55], a[236:239], a[224:231], v240 abid:1\n"
    "ds_read_b128 a[224:227], v250 offset:54048\n"
    "ds_read_b128 a[228:231], v250 offset:54368\n"
    "ds_read_b128 a[236:239], v251 offset:21840\n"
    "s_waitcnt vmcnt(9)\n"
    "v_or3_b32 v200, v200, v152, v153\n"
    "v_or3_b32 v200, v200, v154, v155\n"
    "v_add_u32_e32 v152, v205, v152\n"
    "v_add_u32_e32 v154, v205, v154\n"
    "ds_write_b128 v217, v[152:155] offset:512\n"
    "s_waitcnt lgkmcnt(6)\n"
    "v_smfmac_f32_16x16x64_bf16 v[56:59], a[248:251], a[240:247], v240\n"
    "v_smfmac_f32_16x16x64_bf16 v[60:63], a[252:255], a[240:247], v240 abid:1\n"
    "ds_read_b128 a[240:243], v250 offset:54128\n"
    "ds_read_b128 a[244:247], v250 offset:54448\n"
    "ds_read_b128 a[248:251], v251 offset:22176\n"
    "ds_read_b128 a[252:255], v251 offset:22512\n"
    "ds_read_b128 v[144:147], v252 offset:10752\n"
    "ds_read_b128 v[148:151], v252 offset:11088\n"
    "s_waitcnt lgkmcnt(8)\n"
    "v_smfmac_f32_16x16x64_bf16 v[64:67], a[232:235], a[224:231], v240\n"
    "ds_read_b128 a[232:235], v251 offset:22848\n"
    "s_waitcnt lgkmcnt(8)\n"
    "v_smfmac_f32_16x16x64_bf16 v[68:71], a[236:239], a[224:231], v240 abid:1\n"
    "ds_read_b128 a[224:227], v250 offset:54208\n"
    "ds_read_b128 a[228:231], v250 offset:54528\n"
    "s_waitcnt lgkmcnt(9)\n"
    "s_barrier\n"
    "ds_read_b64_tr_b16 a[128:129], v224 offset:0\n"
    "ds_read_b64_tr_b16 a[132:133], v225 offset:0\n"
    "ds_read_b64_tr_b16 a[136:137], v226 offset:0\n"
    "s_waitcnt lgkmcnt(9)\n"
    "v_smfmac_f32_16x16x64_bf16 v[72:75], a[248:251], a[240:247], v240\n"
    "s_waitcnt lgkmcnt(8)\n"
    "ds_read_b64_tr_b16 a[140:141], v227 offset:0\n"
    "ds_read_b64_tr_b16 a[144:145], v228 offset:0\n"
    "v_smfmac_f32_16x16x64_bf16 v[76:79], a[252:255], a[240:247], v240 abid:1\n"
    "ds_read_b64_tr_b16 a[148:149], v229 offset:0\n"
    "ds_read_b64_tr_b16 a[152:153], v230 offset:0\n"
    "ds_read_b64_tr_b16 a[156:157], v231 offset:0\n"
    "ds_read_b64_tr_b16 a[160:161], v224 offset:256\n"
    "ds_read_b64_tr_b16 a[164:165], v225 offset:256\n"
    "ds_read_b64_tr_b16 a[168:169], v226 offset:256\n"
    "ds_read_b64_tr_b16 a[172:173], v227 offset:256\n"
    "s_waitcnt vmcnt(8)\n"
    "ds_write2_b32 v216, a200, a201 offset0:0 offset1:20\n"
    "s_waitcnt lgkmcnt(13)\n"
    "v_smfmac_f32_16x16x64_bf16 v[80:83], a[232:235], a[224:231], v240\n"
    "ds_write2_b32 v216, a202, a203 offset0:40 offset1:60\n"
    "ds_read_b128 a[236:239], v251 offset:23184\n"
    "ds_read_b128 a[240:243], v250 offset:54288\n"
    "ds_read_b128 a[244:247], v250 offset:54608\n"
    "ds_read_b128 a[248:251], v251 offset:23520\n"
    "s_waitcnt vmcnt(7)\n"
    "ds_write2_b32 v216, a204, a205 offset0:80 offset1:100\n"
    "ds_write2_b32 v216, a206, a207 offset0:120 offset1:140\n"
    "v_add_u32_e32 v216, 1360, v216\n"
    "ds_read_b128 a[252:255], v251 offset:23856\n"
    "ds_read_b128 a[232:235], v252 offset:0\n"
    "s_waitcnt vmcnt(6)\n"
    "v_or3_b32 v200, v200, v156, v157\n"
    "v_or3_b32 v200, v200, v158, v159\n"
    "v_add_u32_e64 v156, v201, v156\n"
    "v_add_u32_e64 v158, v201, v158\n"
    "ds_write_b128 v217, v[156:159] offset:12288\n"
    "s_waitcnt lgkmcnt(7)\n"
    "v_smfmac_f32_16x16x64_bf16 v[84:87], a[236:239], a[224:231], v240 abid:1\n"
    "s_waitcnt lgkmcnt(5)\n"
    "v_smfmac_f32_16x16x64_bf16 v[88:91], a[248:251], a[240:247], v240\n"
    "ds_read_b128 a[236:239], v252 offset:336\n"
    "ds_read_b128 a[248:251], v252 offset:672\n"
    "s_waitcnt vmcnt(5)\n"
    "v_or3_b32 v200, v200, v160, v161\n"
    "v_or3_b32 v200, v200, v162, v163\n"
    "v_add_u32_e32 v160, v203, v160\n"
    "v_add_u32_e64 v162, v203, v162\n"
    "ds_write_b128 v217, v[160:163] offset:12544\n"
    "s_waitcnt lgkmcnt(4)\n"
    "v_smfmac_f32_16x16x64_bf16 v[92:95], a[252:255], a[240:247], v240 abid:1\n"
    "ds_read_b128 a[252:255], v252 offset:1008\n"
    "ds_read_b128 v[160:163], v252 offset:11424\n"
    "s_waitcnt vmcnt(4)\n"
    "v_or3_b32 v200, v200, v164, v165\n"
    "v_or3_b32 v200, v200, v166, v167\n"
    "v_add_u32_e64 v164, v205, v164\n"
    "v_add_u32_e64 v166, v205, v166\n"
    "ds_write_b128 v217, v[164:167] offset:12800\n"
    "v_and_b32_e32 v200, 0x40004000, v200\n"
    "v_cmp_le_u32_e32 vcc, 1, v200\n"
    "s_nop 1\n"
    "s_cmp_lg_u64 vcc, 0\n"
    "s_cselect_b32 vcc_lo, 1, 0\n"
    "s_or_b32 s66, s66, vcc_lo\n"
    "v_cmp_le_u32_e32 vcc, 981467136, v232\n"
    "s_nop 1\n"
    "s_cmp_lg_u64 vcc, 0\n"
    "s_cselect_b32 vcc_lo, 2, 0\n"
    "s_or_b32 s66, s66, vcc_lo\n"
    "v_mov_b32_e32 v200, s66\n"
    "v_mov_b32_e32 v232, 162992\n"
    "s_mov_b64 exec, 1\n"
    "ds_or_b32 v232, v200\n"
    "s_mov_b64 exec, -1\n"
    "ds_read_b128 v[164:167], v252 offset:11760\n"
    "s_waitcnt lgkmcnt(2)\n"
    "s_barrier\n"
    "ds_read_b64_tr_b16 a[130:131], v224 offset:12288\n"
    "ds_read_b64_tr_b16 a[134:135], v225 offset:12288\n"
    "ds_read_b64_tr_b16 a[138:139], v226 offset:12288\n"
    "ds_read_b64_tr_b16 a[142:143], v227 offset:12288\n"
    "ds_read_b64_tr_b16 a[146:147], v228 offset:12288\n"
    "ds_read_b64_tr_b16 a[150:151], v229 offset:12288\n"
    "ds_read_b64_tr_b16 a[154:155], v230 offset:12288\n"
    "ds_read_b64_tr_b16 a[158:159], v231 offset:12288\n"
    "ds_read_b64_tr_b16 a[162:163], v224 offset:12544\n"
    "ds_read_b64_tr_b16 a[166:167], v225 offset:12544\n"
    "ds_read_b64_tr_b16 a[170:171], v226 offset:12544\n"
    "ds_read_b64_tr_b16 a[174:175], v227 offset:12544\n"
    "s_nop 0\n"
    "s_waitcnt vmcnt(3)\n"
    "ds_write2_b32 v216, a208, a209 offset0:0 offset1:20\n"
    "ds_write2_b32 v216, a210, a211 offset0:40 offset1:60\n"
    "s_waitcnt vmcnt(2)\n"
    "ds_write2_b32 v216, a212, a213 offset0:80 offset1:100\n"
    "s_waitcnt lgkmcnt(2)\n"
    "s_barrier\n"
    "s_add_u32 m0, s38, 0\n"
    "s_mov_b32 s64, 18432\n"
    "buffer_load_dwordx4 v210, s[84:87], s64 offen offset:0 lds\n"
    "buffer_load_dwordx4 v210, s[84:87], s64 offen offset:1024 lds\n"
    "buffer_load_dwordx4 v210, s[84:87], s64 offen offset:2048 lds\n"
    "s_add_u32 m0, s38, 3072\n"
    "s_mov_b32 s64, 21504\n"
    "buffer_load_dwordx4 v210, s[84:87], s64 offen offset:0 lds\n"
    "buffer_load_dwordx4 v210, s[84:87], s64 offen offset:1024 lds\n"
    "buffer_load_dwordx4 v210, s[84:87], s64 offen offset:2048 lds\n"
    "s_add_u32 m0, s38, 6144\n"
    "s_mov_b32 s64, 24576\n"
    "buffer_load_dwordx4 v210, s[84:87], s64 offen offset:0 lds\n"
    "buffer_load_dwordx4 v210, s[84:87], s64 offen offset:1024 lds\n"
    "buffer_load_dwordx4 v210, s[84:87], s64 offen offset:2048 lds\n"
    "s_sub_u32 s80, s76, .Lreplk_last-.Lreplk_loop\n"
    "s_subb_u32 s81, s77, 0\n"
    "s_add_u32 s82, s36, .Lreplk_last-.Lreplk_loop\n"
    "s_and_b32 s85, s37, 0x3fff\n"
    "s_add_u32 s84, s85, 8192\n"
    "s_lshr_b32 s85, s85, 2\n"
    "v_lshlrev_b32_e64 v235, 3, v244\n"
    "s_mov_b32 exec_hi, 0\n"
    "buffer_load_dword v233, v235, s[40:43], s53 offen sc1\n"
    "s_mov_b32 exec_hi, -1\n"
    "s_add_u32 s53, s53, 393216\n"
    "ds_write2_b32 v216, a214, a215 offset0:120 offset1:140\n"
    "v_add_u32_e32 v216, 1360, v216\n"
    "s_mov_b32 s64, 0\n"
    "s_mov_b32 s59, 0\n"
    "ds_read_b32 v232, v232\n"
    "v_lshlrev_b32_e32 v235, 3, v244\n"
    "s_mov_b32 exec_hi, 0\n"
    "buffer_load_dword v233, v235, s[40:43], s53 offen sc1\n"
    "s_mov_b32 exec_hi, -1\n"
    "s_add_u32 s53, s53, 393216\n"
    "s_waitcnt vmcnt(12)\n"
    "ds_write2_b32 v216, a216, a217 offset0:0 offset1:20\n"
    "ds_write2_b32 v216, a218, a219 offset0:40 offset1:60\n"
    "v_lshlrev_b32_e32 v235, 3, v244\n"
    "s_mov_b32 exec_hi, 0\n"
    "buffer_load_dword v233, v235, s[40:43], s53 offen sc1\n"
    "s_mov_b32 exec_hi, -1\n"
    "s_add_u32 s53, s53, 393216\n"
    "s_waitcnt vmcnt(12)\n"
    "ds_write2_b32 v216, a220, a221 offset0:80 offset1:100\n"
    "ds_write2_b32 v216, a222, a223 offset0:120 offset1:140\n"
    "v_lshlrev_b32_e64 v235, 3, v244\n"
    "s_mov_b32 exec_hi, 0\n"
    "buffer_load_dword v233, v235, s[40:43], s53 offen sc1\n"
    "s_mov_b32 exec_hi, -1\n"
    "s_add_u32 s53, s53, 393216\n"
    "v_mov_b32_e64 v235, v222\n"
    "s_waitcnt lgkmcnt(0)\n"
    "s_barrier\n"
    "s_getpc_b64 vcc\n"
    ".Lreplk_pc2:\n"
    "s_sub_u32 vcc_lo, vcc_lo, .Lreplk_pc2-.Lreplk_pc0\n"
    "s_subb_u32 vcc_hi, vcc_hi, 0\n"
    "global_load_dword v233, v215, vcc\n"
    "ds_read_b128 a[224:227], v250 offset:640\n"
    "ds_read_b128 a[228:231], v250 offset:960\n"
    "ds_read_b128 a[240:243], v250 offset:720\n"
    "ds_read_b128 a[244:247], v250 offset:1040\n"
    "ds_read_b128 v[96:99], v250 offset:800\n"
    "ds_read_b128 v[100:103], v250 offset:1120\n"
    "ds_read_b128 v[120:123], v250 offset:880\n"
    "ds_read_b128 v[124:127], v250 offset:1200\n"
    "ds_read_b128 v[136:139], v250 offset:27664\n"
    "ds_read_b128 v[140:143], v250 offset:27984\n"
    "ds_read_b128 v[152:155], v250 offset:27744\n"
    "ds_read_b128 v[156:159], v250 offset:28064\n"
    "s_waitcnt lgkmcnt(10)\n"
    "v_smfmac_f32_16x16x64_bf16 v[0:3], a[232:235], a[224:231], v240\n"
    "s_nop 0\n"
    "ds_read_b128 a[232:235], v252 offset:12096\n"
    "v_smfmac_f32_16x16x64_bf16 v[4:7], a[236:239], a[224:231], v240 abid:1\n"
    "ds_read_b128 a[224:227], v250 offset:27824\n"
    "s_waitcnt lgkmcnt(10)\n"
    "v_smfmac_f32_16x16x64_bf16 v[8:11], a[248:251], a[240:247], v240\n"
    "ds_read_b128 a[228:231], v250 offset:28144\n"
    "v_smfmac_f32_16x16x64_bf16 v[12:15], a[252:255], a[240:247], v240 abid:1\n"
    "s_waitcnt lgkmcnt(9)\n"
    "ds_read_b128 a[236:239], v252 offset:12432\n"
    "v_smfmac_f32_16x16x64_bf16 v[16:19], v[104:107], v[96:103], v240\n"
    "ds_read_b128 a[240:243], v250 offset:27904\n"
    "v_smfmac_f32_16x16x64_bf16 v[20:23], v[108:111], v[96:103], v240 abid:1\n"
    "s_waitcnt lgkmcnt(9)\n"
    "ds_read_b128 a[244:247], v250 offset:28224\n"
    "v_smfmac_f32_16x16x64_bf16 v[24:27], v[128:131], v[120:127], v240\n"
    "ds_read_b128 a[248:251], v252 offset:12768\n"
    "v_smfmac_f32_16x16x64_bf16 v[28:31], v[132:135], v[120:127], v240 abid:1\n"
    "s_waitcnt lgkmcnt(9)\n"
    "ds_read_b128 a[252:255], v252 offset:13104\n"
    "v_smfmac_f32_16x16x64_bf16 v[32:35], v[144:147], v[136:143], v240\n"
    "s_nop 0\n"
    "ds_read_b128 v[96:99], v250 offset:54688\n"
    "v_smfmac_f32_16x16x64_bf16 v[36:39], v[148:151], v[136:143], v240 abid:1\n"
    "s_waitcnt lgkmcnt(9)\n"
    "ds_read_b128 v[100:103], v250 offset:55008\n"
    "v_smfmac_f32_16x16x64_bf16 v[40:43], v[160:163], v[152:159], v240\n"
    "ds_read_b128 v[104:107], v252 offset:21504\n"
    "v_smfmac_f32_16x16x64_bf16 v[44:47], v[164:167], v[152:159], v240 abid:1\n"
    "ds_read_b128 v[108:111], v252 offset:21840\n"
    "ds_read_b128 v[120:123], v250 offset:54768\n"
    "ds_read_b128 v[124:127], v250 offset:55088\n"
    "v_pk_mov_b32 v[224:225], v[0:1], v[0:1] op_sel:[0,1]\n"
    "v_pk_mov_b32 v[226:227], v[2:3], v[2:3] op_sel:[0,1]\n"
    "v_pk_mul_f32 v[228:229], v[0:1], v[0:1]\n"
    "v_pk_mul_f32 v[230:231], v[2:3], v[2:3]\n"
    "v_pk_add_f32 v[224:225], v[224:225], v[4:5]\n"
    "v_pk_add_f32 v[226:227], v[226:227], v[6:7]\n"
    "v_pk_fma_f32 v[228:229], v[4:5], v[4:5], v[228:229]\n"
    "v_pk_fma_f32 v[230:231], v[6:7], v[6:7], v[230:231]\n"
    "v_pk_add_f32 v[224:225], v[224:225], v[8:9]\n"
    "v_pk_add_f32 v[226:227], v[226:227], v[10:11]\n"
    "v_pk_fma_f32 v[228:229], v[8:9], v[8:9], v[228:229]\n"
    "v_pk_fma_f32 v[230:231], v[10:11], v[10:11], v[230:231]\n"
    "v_pk_add_f32 v[224:225], v[224:225], v[12:13]\n"
    "v_pk_add_f32 v[226:227], v[226:227], v[14:15]\n"
    "v_pk_fma_f32 v[228:229], v[12:13], v[12:13], v[228:229]\n"
    "v_pk_fma_f32 v[230:231], v[14:15], v[14:15], v[230:231]\n"
    "v_pk_add_f32 v[224:225], v[224:225], v[16:17]\n"
    "v_pk_add_f32 v[226:227], v[226:227], v[18:19]\n"
    "v_pk_fma_f32 v[228:229], v[16:17], v[16:17], v[228:229]\n"
    "v_pk_fma_f32 v[230:231], v[18:19], v[18:19], v[230:231]\n"
    "v_pk_add_f32 v[224:225], v[224:225], v[20:21]\n"
    "v_pk_add_f32 v[226:227], v[226:227], v[22:23]\n"
    "v_pk_fma_f32 v[228:229], v[20:21], v[20:21], v[228:229]\n"
    "v_pk_fma_f32 v[230:231], v[22:23], v[22:23], v[230:231]\n"
    "v_pk_add_f32 v[224:225], v[224:225], v[24:25]\n"
    "v_pk_add_f32 v[226:227], v[226:227], v[26:27]\n"
    "v_pk_fma_f32 v[228:229], v[24:25], v[24:25], v[228:229]\n"
    "v_pk_fma_f32 v[230:231], v[26:27], v[26:27], v[230:231]\n"
    "v_pk_add_f32 v[224:225], v[224:225], v[28:29]\n"
    "v_pk_add_f32 v[226:227], v[226:227], v[30:31]\n"
    "v_pk_fma_f32 v[228:229], v[28:29], v[28:29], v[228:229]\n"
    "v_pk_fma_f32 v[230:231], v[30:31], v[30:31], v[230:231]\n"
    "v_pk_add_f32 v[224:225], v[224:225], v[32:33]\n"
    "v_pk_add_f32 v[226:227], v[226:227], v[34:35]\n"
    "v_pk_fma_f32 v[228:229], v[32:33], v[32:33], v[228:229]\n"
    "v_pk_fma_f32 v[230:231], v[34:35], v[34:35], v[230:231]\n"
    "v_pk_add_f32 v[224:225], v[224:225], v[36:37]\n"
    "v_pk_add_f32 v[226:227], v[226:227], v[38:39]\n"
    "v_pk_fma_f32 v[228:229], v[36:37], v[36:37], v[228:229]\n"
    "v_pk_fma_f32 v[230:231], v[38:39], v[38:39], v[230:231]\n"
    "v_pk_add_f32 v[224:225], v[224:225], v[40:41]\n"
    "v_pk_add_f32 v[226:227], v[226:227], v[42:43]\n"
    "v_pk_fma_f32 v[228:229], v[40:41], v[40:41], v[228:229]\n"
    "v_pk_fma_f32 v[230:231], v[42:43], v[42:43], v[230:231]\n"
    "v_pk_add_f32 v[224:225], v[224:225], v[44:45]\n"
    "v_pk_add_f32 v[226:227], v[226:227], v[46:47]\n"
    "v_pk_fma_f32 v[228:229], v[44:45], v[44:45], v[228:229]\n"
    "v_pk_fma_f32 v[230:231], v[46:47], v[46:47], v[230:231]\n"
    "s_waitcnt lgkmcnt(0)\n"
    "v_smfmac_f32_16x16x64_bf16 v[48:51], a[232:235], a[224:231], v240\n"
    "s_nop 0\n"
    "ds_read_b128 v[128:131], v252 offset:22176\n"
    "v_smfmac_f32_16x16x64_bf16 v[52:55], a[236:239], a[224:231], v240 abid:1\n"
    "ds_read_b128 v[132:135], v252 offset:22512\n"
    "v_smfmac_f32_16x16x64_bf16 v[56:59], a[248:251], a[240:247], v240\n"
    "ds_read_b128 v[136:139], v250 offset:54848\n"
    "v_smfmac_f32_16x16x64_bf16 v[60:63], a[252:255], a[240:247], v240 abid:1\n"
    "ds_read_b128 v[140:143], v250 offset:55168\n"
    "v_smfmac_f32_16x16x64_bf16 v[64:67], v[104:107], v[96:103], v240\n"
    "ds_read_b128 v[144:147], v252 offset:22848\n"
    "v_smfmac_f32_16x16x64_bf16 v[68:71], v[108:111], v[96:103], v240 abid:1\n"
    "ds_read_b128 v[148:151], v252 offset:23184\n"
    "ds_read_b128 v[152:155], v250 offset:54928\n"
    "v_accvgpr_write_b32 a248, v236\n"
    "ds_read_b128 v[156:159], v250 offset:55248\n"
    "v_pk_add_f32 v[224:225], v[224:225], v[48:49]\n"
    "v_pk_add_f32 v[226:227], v[226:227], v[50:51]\n"
    "v_pk_fma_f32 v[228:229], v[48:49], v[48:49], v[228:229]\n"
    "v_pk_fma_f32 v[230:231], v[50:51], v[50:51], v[230:231]\n"
    "v_pk_add_f32 v[224:225], v[224:225], v[52:53]\n"
    "s_waitcnt vmcnt(5)\n"
    "s_barrier\n"
    "ds_read_b64_tr_b16 a[176:177], v220 offset:0\n"
    "ds_read_b64_tr_b16 a[178:179], v220 offset:3072\n"
    "v_pk_add_f32 v[226:227], v[226:227], v[54:55]\n"
    "ds_read_b64_tr_b16 a[180:181], v220 offset:8\n"
    "ds_read_b64_tr_b16 a[182:183], v220 offset:3080\n"
    "ds_read_b64_tr_b16 a[184:185], v220 offset:64\n"
    "v_pk_fma_f32 v[228:229], v[52:53], v[52:53], v[228:229]\n"
    "ds_read_b64_tr_b16 a[186:187], v220 offset:3136\n"
    "ds_read_b64_tr_b16 a[188:189], v220 offset:72\n"
    "ds_read_b64_tr_b16 a[190:191], v220 offset:3144\n"
    "v_pk_fma_f32 v[230:231], v[54:55], v[54:55], v[230:231]\n"
    "ds_read_b64_tr_b16 v[188:189], v218 offset:128\n"
    "ds_read_b64_tr_b16 v[190:191], v218 offset:3200\n"
    "ds_read_b64_tr_b16 a[192:193], v220 offset:6144\n"
    "v_pk_add_f32 v[224:225], v[224:225], v[56:57]\n"
    "ds_read_b64_tr_b16 a[194:195], v220 offset:9216\n"
    "ds_read_b64_tr_b16 a[196:197], v220 offset:6152\n"
    "ds_read_b64_tr_b16 a[198:199], v220 offset:9224\n"
    "v_pk_add_f32 v[226:227], v[226:227], v[58:59]\n"
    "ds_read_b64_tr_b16 a[200:201], v220 offset:6208\n"
    "ds_read_b64_tr_b16 a[202:203], v220 offset:9280\n"
    "ds_read_b64_tr_b16 a[204:205], v220 offset:6216\n"
    "v_pk_fma_f32 v[228:229], v[56:57], v[56:57], v[228:229]\n"
    "ds_read_b64_tr_b16 a[206:207], v220 offset:9288\n"
    "ds_read_b64_tr_b16 v[192:193], v218 offset:6272\n"
    "ds_read_b64_tr_b16 v[194:195], v218 offset:9344\n"
    "v_pk_fma_f32 v[230:231], v[58:59], v[58:59], v[230:231]\n"
    "ds_read_b64_tr_b16 a[208:209], v220 offset:12288\n"
    "ds_read_b64_tr_b16 a[210:211], v220 offset:15360\n"
    "ds_read_b64_tr_b16 a[212:213], v220 offset:12296\n"
    "v_pk_add_f32 v[224:225], v[224:225], v[60:61]\n"
    "ds_read_b64_tr_b16 a[214:215], v220 offset:15368\n"
    "ds_read_b64_tr_b16 a[216:217], v220 offset:12352\n"
    "ds_read_b64_tr_b16 a[218:219], v220 offset:15424\n"
    "v_pk_add_f32 v[226:227], v[226:227], v[62:63]\n"
    "ds_read_b64_tr_b16 a[220:221], v220 offset:12360\n"
    "ds_read_b64_tr_b16 a[222:223], v220 offset:15432\n"
    "ds_read_b64_tr_b16 v[196:197], v218 offset:12416\n"
    "v_pk_fma_f32 v[228:229], v[60:61], v[60:61], v[228:229]\n"
    "ds_read_b64_tr_b16 v[198:199], v218 offset:15488\n"
    "v_pk_fma_f32 v[230:231], v[62:63], v[62:63], v[230:231]\n"
    "v_pk_add_f32 v[224:225], v[224:225], v[64:65]\n"
    "v_pk_add_f32 v[226:227], v[226:227], v[66:67]\n"
    "v_pk_fma_f32 v[228:229], v[64:65], v[64:65], v[228:229]\n"
    "v_pk_fma_f32 v[230:231], v[66:67], v[66:67], v[230:231]\n"
    "v_pk_add_f32 v[224:225], v[224:225], v[68:69]\n"
    "v_pk_add_f32 v[226:227], v[226:227], v[70:71]\n"
    "v_pk_fma_f32 v[228:229], v[68:69], v[68:69], v[228:229]\n"
    "v_pk_fma_f32 v[230:231], v[70:71], v[70:71], v[230:231]\n"
    "s_waitcnt lgkmcnt(10)\n"
    "v_smfmac_f32_16x16x64_bf16 v[72:75], v[128:131], v[120:127], v240\n"
    "s_nop 0\n"
    "ds_read_b128 v[160:163], v252 offset:23520\n"
    "v_smfmac_f32_16x16x64_bf16 v[76:79], v[132:135], v[120:127], v240 abid:1\n"
    "ds_read_b128 v[164:167], v252 offset:23856\n"
    "v_smfmac_f32_16x16x64_bf16 v[80:83], v[144:147], v[136:143], v240\n"
    "v_accvgpr_write_b32 a249, v237\n"
    "v_smfmac_f32_16x16x64_bf16 v[84:87], v[148:151], v[136:143], v240 abid:1\n"
    "v_accvgpr_write_b32 a250, v238\n"
    "v_accvgpr_write_b32 a251, v239\n"
    "v_pk_add_f32 v[224:225], v[224:225], v[72:73]\n"
    "v_pk_add_f32 v[226:227], v[226:227], v[74:75]\n"
    "v_pk_fma_f32 v[228:229], v[72:73], v[72:73], v[228:229]\n"
    "v_pk_fma_f32 v[230:231], v[74:75], v[74:75], v[230:231]\n"
    "v_pk_add_f32 v[224:225], v[224:225], v[76:77]\n"
    "v_pk_add_f32 v[226:227], v[226:227], v[78:79]\n"
    "v_pk_fma_f32 v[228:229], v[76:77], v[76:77], v[228:229]\n"
    "v_pk_fma_f32 v[230:231], v[78:79], v[78:79], v[230:231]\n"
    "v_pk_add_f32 v[224:225], v[224:225], v[80:81]\n"
    "v_pk_add_f32 v[226:227], v[226:227], v[82:83]\n"
    "v_pk_fma_f32 v[228:229], v[80:81], v[80:81], v[228:229]\n"
    "v_pk_fma_f32 v[230:231], v[82:83], v[82:83], v[230:231]\n"
    "v_pk_add_f32 v[224:225], v[224:225], v[84:85]\n"
    "v_pk_add_f32 v[226:227], v[226:227], v[86:87]\n"
    "v_pk_fma_f32 v[228:229], v[84:85], v[84:85], v[228:229]\n"
    "v_pk_fma_f32 v[230:231], v[86:87], v[86:87], v[230:231]\n"
    "s_waitcnt lgkmcnt(1)\n"
    "v_smfmac_f32_16x16x64_bf16 v[88:91], v[160:163], v[152:159], v240\n"
    "s_waitcnt lgkmcnt(0)\n"
    "v_smfmac_f32_16x16x64_bf16 v[92:95], v[164:167], v[152:159], v240 abid:1\n"
    "s_nop 0\n"
    "s_nop 5\n"
    "v_pk_add_f32 v[224:225], v[224:225], v[88:89]\n"
    "v_pk_add_f32 v[226:227], v[226:227], v[90:91]\n"
    "v_pk_fma_f32 v[228:229], v[88:89], v[88:89], v[228:229]\n"
    "v_pk_fma_f32 v[230:231], v[90:91], v[90:91], v[230:231]\n"
    "v_pk_add_f32 v[224:225], v[224:225], v[92:93]\n"
    "v_pk_add_f32 v[226:227], v[226:227], v[94:95]\n"
    "v_pk_fma_f32 v[228:229], v[92:93], v[92:93], v[228:229]\n"
    "v_pk_fma_f32 v[230:231], v[94:95], v[94:95], v[230:231]\n"
    "ds_write_b128 v254, v[224:227]\n"
    "ds_write_b128 v254, v[228:231] offset:1024\n"
    "v_add_u32_e64 v254, s56, v242\n"
    "s_waitcnt lgkmcnt(0)\n"
    "s_barrier\n"
    "ds_read_b128 v[128:131], v254 offset:0\n"
    "ds_read_b128 v[132:135], v254 offset:6752\n"
    "ds_read_b128 v[136:139], v254 offset:13504\n"
    "ds_read_b128 v[140:143], v254 offset:20256\n"
    "ds_read_b128 v[144:147], v254 offset:1024\n"
    "ds_read_b128 v[148:151], v254 offset:7776\n"
    "ds_read_b128 v[152:155], v254 offset:14528\n"
    "ds_read_b128 v[156:159], v254 offset:21280\n"
    "ds_write_b128 v234, v[112:115] offset:0\n"
    "ds_write_b128 v234, v[116:119] offset:16\n"
    "s_waitcnt lgkmcnt(8)\n"
    "v_pk_add_f32 v[128:129], v[128:129], v[132:133]\n"
    "v_pk_add_f32 v[130:131], v[130:131], v[134:135]\n"
    "ds_write_b128 v207, v[168:171] offset:0\n"
    "s_waitcnt lgkmcnt(6)\n"
    "v_pk_add_f32 v[136:137], v[136:137], v[140:141]\n"
    "v_pk_add_f32 v[138:139], v[138:139], v[142:143]\n"
    "s_waitcnt lgkmcnt(5)\n"
    "v_pk_add_f32 v[144:145], v[144:145], v[148:149]\n"
    "v_pk_add_f32 v[146:147], v[146:147], v[150:151]\n"
    "v_pk_add_f32 v[128:129], v[128:129], v[136:137]\n"
    "v_pk_add_f32 v[130:131], v[130:131], v[138:139]\n"
    "v_mul_f32_e32 v128, 0x3c2aaaab, v128\n"
    "v_mul_f32_e32 v129, 0x3c2aaaab, v129\n"
    "v_mul_f32_e32 v130, 0x3c2aaaab, v130\n"
    "s_waitcnt lgkmcnt(3)\n"
    "v_pk_add_f32 v[152:153], v[152:153], v[156:157]\n"
    "v_pk_add_f32 v[154:155], v[154:155], v[158:159]\n"
    "v_pk_add_f32 v[144:145], v[144:145], v[152:153]\n"
    "v_pk_add_f32 v[146:147], v[146:147], v[154:155]\n"
    "v_mul_f32_e32 v131, 0x3c2aaaab, v131\n"
    "v_mul_f32_e32 v144, 0x3c2aaaab, v144\n"
    "v_mul_f32_e32 v145, 0x3c2aaaab, v145\n"
    "v_mul_f32_e32 v146, 0x3c2aaaab, v146\n"
    "v_mul_f32_e32 v147, 0x3c2aaaab, v147\n"
    "v_fma_f32 v144, -v128, v128, v144\n"
    "v_fma_f32 v145, -v129, v129, v145\n"
    "v_fma_f32 v146, -v130, v130, v146\n"
    "v_fma_f32 v147, -v131, v131, v147\n"
    "v_max_f32_e64 v144, 0, v144\n"
    "v_max_f32_e64 v145, 0, v145\n"
    "v_max_f32_e64 v146, 0, v146\n"
    "v_max_f32_e64 v147, 0, v147\n"
    "v_add_f32_e32 v144, 0x3727c5ac, v144\n"
    "v_add_f32_e32 v145, 0x3727c5ac, v145\n"
    "v_add_f32_e32 v146, 0x3727c5ac, v146\n"
    "v_add_f32_e32 v147, 0x3727c5ac, v147\n"
    "v_rsq_f32_e64 v144, v144\n"
    "v_rsq_f32_e64 v145, v145\n"
    "v_rsq_f32_e64 v146, v146\n"
    "v_rsq_f32_e64 v147, v147\n"
    "v_mul_f32_e64 v224, s50, v144\n"
    "v_mul_f32_e64 v225, s50, v145\n"
    "v_mul_f32_e64 v226, s50, v146\n"
    "v_mul_f32_e64 v227, s50, v147\n"
    "v_mul_f32_e64 v228, v224, -v128\n"
    "v_mul_f32_e64 v229, v225, -v129\n"
    "v_mul_f32_e64 v230, v226, -v130\n"
    "v_mul_f32_e64 v231, v227, -v131\n"
    "ds_write_b128 v207, v[172:175] offset:4096\n"
    "v_pk_fma_f32 v[64:65], v[64:65], v[224:225], v[228:229]\n"
    "v_pk_fma_f32 v[66:67], v[66:67], v[226:227], v[230:231]\n"
    "v_pk_fma_f32 v[68:69], v[68:69], v[224:225], v[228:229]\n"
    "v_pk_fma_f32 v[70:71], v[70:71], v[226:227], v[230:231]\n"
    "v_pk_fma_f32 v[72:73], v[72:73], v[224:225], v[228:229]\n"
    "v_pk_fma_f32 v[74:75], v[74:75], v[226:227], v[230:231]\n"
    "ds_write_b128 v207, v[176:179] offset:8192\n"
    "v_pk_fma_f32 v[76:77], v[76:77], v[224:225], v[228:229]\n"
    "v_pk_fma_f32 v[78:79], v[78:79], v[226:227], v[230:231]\n"
    "v_pk_fma_f32 v[80:81], v[80:81], v[224:225], v[228:229]\n"
    "v_pk_fma_f32 v[82:83], v[82:83], v[226:227], v[230:231]\n"
    "v_pk_fma_f32 v[84:85], v[84:85], v[224:225], v[228:229]\n"
    "v_pk_fma_f32 v[86:87], v[86:87], v[226:227], v[230:231]\n"
    "ds_write_b128 v207, v[188:191] offset:12288\n"
    "v_pk_fma_f32 v[88:89], v[88:89], v[224:225], v[228:229]\n"
    "v_pk_fma_f32 v[90:91], v[90:91], v[226:227], v[230:231]\n"
    "v_pk_fma_f32 v[92:93], v[92:93], v[224:225], v[228:229]\n"
    "v_pk_fma_f32 v[94:95], v[94:95], v[226:227], v[230:231]\n"
    "v_cvt_pk_bf16_f32 v96, v64, v68\n"
    "v_cvt_pk_bf16_f32 v100, v65, v69\n"
    "ds_write_b128 v207, v[192:195] offset:16384\n"
    "v_cvt_pk_bf16_f32 v104, v66, v70\n"
    "v_cvt_pk_bf16_f32 v108, v67, v71\n"
    "v_cvt_pk_bf16_f32 v97, v72, v76\n"
    "v_cvt_pk_bf16_f32 v101, v73, v77\n"
    "v_cvt_pk_bf16_f32 v105, v74, v78\n"
    "v_cvt_pk_bf16_f32 v109, v75, v79\n"
    "ds_write_b128 v207, v[196:199] offset:20480\n"
    "v_cvt_pk_bf16_f32 v98, v80, v84\n"
    "v_cvt_pk_bf16_f32 v102, v81, v85\n"
    "v_cvt_pk_bf16_f32 v106, v82, v86\n"
    "v_cvt_pk_bf16_f32 v110, v83, v87\n"
    "v_cvt_pk_bf16_f32 v99, v88, v92\n"
    "v_cvt_pk_bf16_f32 v103, v89, v93\n"
    "ds_write_b128 v207, v[180:183] offset:32768\n"
    "v_cvt_pk_bf16_f32 v107, v90, v94\n"
    "v_cvt_pk_bf16_f32 v111, v91, v95\n"
    "v_pk_fma_f32 v[0:1], v[0:1], v[224:225], v[228:229]\n"
    "v_pk_fma_f32 v[2:3], v[2:3], v[226:227], v[230:231]\n"
    "v_pk_fma_f32 v[4:5], v[4:5], v[224:225], v[228:229]\n"
    "v_pk_fma_f32 v[6:7], v[6:7], v[226:227], v[230:231]\n"
    "ds_write_b128 v253, v[96:99] offset:26688\n"
    "v_pk_fma_f32 v[8:9], v[8:9], v[224:225], v[228:229]\n"
    "v_pk_fma_f32 v[10:11], v[10:11], v[226:227], v[230:231]\n"
    "v_pk_fma_f32 v[12:13], v[12:13], v[224:225], v[228:229]\n"
    "v_pk_fma_f32 v[14:15], v[14:15], v[226:227], v[230:231]\n"
    "v_pk_fma_f32 v[16:17], v[16:17], v[224:225], v[228:229]\n"
    "v_pk_fma_f32 v[18:19], v[18:19], v[226:227], v[230:231]\n"
    "ds_write_b128 v253, v[100:103] offset:29760\n"
    "v_pk_fma_f32 v[20:21], v[20:21], v[224:225], v[228:229]\n"
    "v_pk_fma_f32 v[22:23], v[22:23], v[226:227], v[230:231]\n"
    "v_pk_fma_f32 v[24:25], v[24:25], v[224:225], v[228:229]\n"
    "v_pk_fma_f32 v[26:27], v[26:27], v[226:227], v[230:231]\n"
    "v_pk_fma_f32 v[28:29], v[28:29], v[224:225], v[228:229]\n"
    "v_pk_fma_f32 v[30:31], v[30:31], v[226:227], v[230:231]\n"
    "v_cvt_pk_bf16_f32 v80, v0, v4\n"
    "v_cvt_pk_bf16_f32 v84, v1, v5\n"
    "v_cvt_pk_bf16_f32 v88, v2, v6\n"
    "v_cvt_pk_bf16_f32 v92, v3, v7\n"
    "v_cvt_pk_bf16_f32 v81, v8, v12\n"
    "v_cvt_pk_bf16_f32 v85, v9, v13\n"
    "v_cvt_pk_bf16_f32 v89, v10, v14\n"
    "v_cvt_pk_bf16_f32 v93, v11, v15\n"
    "v_cvt_pk_bf16_f32 v82, v16, v20\n"
    "v_cvt_pk_bf16_f32 v86, v17, v21\n"
    "v_cvt_pk_bf16_f32 v90, v18, v22\n"
    "v_cvt_pk_bf16_f32 v94, v19, v23\n"
    "v_cvt_pk_bf16_f32 v83, v24, v28\n"
    "ds_write_b128 v253, v[80:83] offset:24640\n"
    "v_cvt_pk_bf16_f32 v87, v25, v29\n"
    "v_cvt_pk_bf16_f32 v91, v26, v30\n"
    "v_cvt_pk_bf16_f32 v95, v27, v31\n"
    "v_pk_fma_f32 v[32:33], v[32:33], v[224:225], v[228:229]\n"
    "v_pk_fma_f32 v[34:35], v[34:35], v[226:227], v[230:231]\n"
    "v_pk_fma_f32 v[36:37], v[36:37], v[224:225], v[228:229]\n"
    "ds_write_b128 v253, v[84:87] offset:27712\n"
    "v_pk_fma_f32 v[38:39], v[38:39], v[226:227], v[230:231]\n"
    "v_pk_fma_f32 v[40:41], v[40:41], v[224:225], v[228:229]\n"
    "v_pk_fma_f32 v[42:43], v[42:43], v[226:227], v[230:231]\n"
    "v_pk_fma_f32 v[44:45], v[44:45], v[224:225], v[228:229]\n"
    "v_pk_fma_f32 v[46:47], v[46:47], v[226:227], v[230:231]\n"
    "v_pk_fma_f32 v[48:49], v[48:49], v[224:225], v[228:229]\n"
    "v_pk_fma_f32 v[50:51], v[50:51], v[226:227], v[230:231]\n"
    "v_pk_fma_f32 v[52:53], v[52:53], v[224:225], v[228:229]\n"
    "v_pk_fma_f32 v[54:55], v[54:55], v[226:227], v[230:231]\n"
    "v_pk_fma_f32 v[56:57], v[56:57], v[224:225], v[228:229]\n"
    "v_pk_fma_f32 v[58:59], v[58:59], v[226:227], v[230:231]\n"
    "v_pk_fma_f32 v[60:61], v[60:61], v[224:225], v[228:229]\n"
    "v_pk_fma_f32 v[62:63], v[62:63], v[226:227], v[230:231]\n"
    "v_cvt_pk_bf16_f32 v64, v32, v36\n"
    "v_cvt_pk_bf16_f32 v68, v33, v37\n"
    "v_cvt_pk_bf16_f32 v72, v34, v38\n"
    "v_cvt_pk_bf16_f32 v76, v35, v39\n"
    "v_cvt_pk_bf16_f32 v65, v40, v44\n"
    "v_cvt_pk_bf16_f32 v69, v41, v45\n"
    "v_cvt_pk_bf16_f32 v73, v42, v46\n"
    "v_cvt_pk_bf16_f32 v77, v43, v47\n"
    "v_cvt_pk_bf16_f32 v66, v48, v52\n"
    "v_cvt_pk_bf16_f32 v70, v49, v53\n"
    "v_cvt_pk_bf16_f32 v74, v50, v54\n"
    "v_cvt_pk_bf16_f32 v78, v51, v55\n"
    "v_cvt_pk_bf16_f32 v67, v56, v60\n"
    "ds_write_b128 v253, v[64:67] offset:25664\n"
    "v_cvt_pk_bf16_f32 v71, v57, v61\n"
    "v_cvt_pk_bf16_f32 v75, v58, v62\n"
    "v_cvt_pk_bf16_f32 v79, v59, v63\n"
    "ds_write_b128 v253, v[68:71] offset:28736\n"
    "s_waitcnt lgkmcnt(0)\n"
    "s_barrier\n"
    "ds_read_b128 a[224:227], v242 offset:24640\n"
    "ds_read_b128 a[228:231], v242 offset:25664\n"
    "ds_read_b128 a[232:235], v242 offset:26688\n"
    "ds_read_b128 a[236:239], v242 offset:27712\n"
    "ds_read_b128 a[240:243], v242 offset:28736\n"
    "ds_read_b128 a[244:247], v242 offset:29760\n"
    "ds_read_b128 a[252:255], v244 offset:32768\n"
    "s_waitcnt lgkmcnt(6)\n"
    "v_mfma_f32_16x16x32_bf16 v[168:171], a[80:83], a[224:227], 0\n"
    "v_mfma_f32_16x16x32_bf16 v[172:175], a[84:87], a[224:227], 0\n"
    "s_waitcnt lgkmcnt(5)\n"
    "v_mfma_f32_16x16x32_bf16 v[168:171], a[128:131], a[228:231], v[168:171]\n"
    "s_waitcnt lgkmcnt(4)\n"
    "v_mfma_f32_16x16x32_bf16 v[168:171], a[48:51], a[232:235], v[168:171]\n"
    "s_waitcnt lgkmcnt(3)\n"
    "v_mfma_f32_16x16x32_bf16 v[172:175], a[132:135], a[228:231], v[172:175]\n"
    "v_mfma_f32_16x16x32_bf16 v[176:179], a[80:83], a[236:239], 0\n"
    "s_waitcnt lgkmcnt(1)\n"
    "v_mfma_f32_16x16x32_bf16 v[176:179], a[128:131], a[240:243], v[176:179]\n"
    "s_waitcnt lgkmcnt(0)\n"
    "v_mfma_f32_16x16x32_bf16 v[172:175], a[252:255], a[232:235], v[172:175]\n"
    "v_mfma_f32_16x16x32_bf16 v[176:179], a[48:51], a[244:247], v[176:179]\n"
    "v_mfma_f32_16x16x32_bf16 v[180:183], a[84:87], a[236:239], 0\n"
    "v_mfma_f32_16x16x32_bf16 v[180:183], a[132:135], a[240:243], v[180:183]\n"
    "v_mfma_f32_16x16x32_bf16 v[180:183], a[252:255], a[244:247], v[180:183]\n"
    "v_readfirstlane_b32 s66, v232\n"
    "s_nop 1\n"
    "s_cmp_lg_u32 s66, 2\n"
    "s_cbranch_scc1 98f\n"
    "ds_read_b128 a[252:255], v244 offset:36864\n"
    "ds_write_b128 v253, v[72:75] offset:1024\n"
    "v_lshlrev_b32_sdwa v184, 1, v168 dst_sel:DWORD dst_unused:UNUSED_PAD src0_sel:DWORD src1_sel:WORD_1\n"
    "v_lshlrev_b32_sdwa v200, 1, v169 dst_sel:DWORD dst_unused:UNUSED_PAD src0_sel:DWORD src1_sel:WORD_1\n"
    "v_lshlrev_b32_sdwa v185, 1, v170 dst_sel:DWORD dst_unused:UNUSED_PAD src0_sel:DWORD src1_sel:WORD_1\n"
    "v_lshlrev_b32_sdwa v201, 1, v171 dst_sel:DWORD dst_unused:UNUSED_PAD src0_sel:DWORD src1_sel:WORD_1\n"
    "v_mfma_f32_16x16x32_bf16 v[168:171], a[88:91], a[224:227], 0\n"
    "v_lshlrev_b32_sdwa v186, 1, v172 dst_sel:DWORD dst_unused:UNUSED_PAD src0_sel:DWORD src1_sel:WORD_1\n"
    "v_lshlrev_b32_sdwa v202, 1, v173 dst_sel:DWORD dst_unused:UNUSED_PAD src0_sel:DWORD src1_sel:WORD_1\n"
    "v_mfma_f32_16x16x32_bf16 v[168:171], a[136:139], a[228:231], v[168:171]\n"
    "ds_read_u16 v184, v184\n"
    "ds_read_u16_d16_hi v200, v200\n"
    "v_mfma_f32_16x16x32_bf16 v[168:171], a[52:55], a[232:235], v[168:171]\n"
    "ds_read_u16 v185, v185\n"
    "ds_read_u16_d16_hi v201, v201\n"
    "v_lshlrev_b32_sdwa v187, 1, v174 dst_sel:DWORD dst_unused:UNUSED_PAD src0_sel:DWORD src1_sel:WORD_1\n"
    "v_lshlrev_b32_sdwa v203, 1, v175 dst_sel:DWORD dst_unused:UNUSED_PAD src0_sel:DWORD src1_sel:WORD_1\n"
    "v_mfma_f32_16x16x32_bf16 v[172:175], a[92:95], a[224:227], 0\n"
    "v_lshlrev_b32_sdwa v188, 1, v176 dst_sel:DWORD dst_unused:UNUSED_PAD src0_sel:DWORD src1_sel:WORD_1\n"
    "v_lshlrev_b32_sdwa v204, 1, v177 dst_sel:DWORD dst_unused:UNUSED_PAD src0_sel:DWORD src1_sel:WORD_1\n"
    "v_mfma_f32_16x16x32_bf16 v[172:175], a[140:143], a[228:231], v[172:175]\n"
    "ds_read_u16 v186, v186\n"
    "v_lshlrev_b32_sdwa v189, 1, v178 dst_sel:DWORD dst_unused:UNUSED_PAD src0_sel:DWORD src1_sel:WORD_1\n"
    "s_waitcnt lgkmcnt(6)\n"
    "v_mfma_f32_16x16x32_bf16 v[172:175], a[252:255], a[232:235], v[172:175]\n"
    "s_nop 0\n"
    "ds_read_u16_d16_hi v202, v202\n"
    "v_lshlrev_b32_sdwa v205, 1, v179 dst_sel:DWORD dst_unused:UNUSED_PAD src0_sel:DWORD src1_sel:WORD_1\n"
    "v_mfma_f32_16x16x32_bf16 v[176:179], a[88:91], a[236:239], 0\n"
    "v_lshlrev_b32_sdwa v190, 1, v180 dst_sel:DWORD dst_unused:UNUSED_PAD src0_sel:DWORD src1_sel:WORD_1\n"
    "v_lshlrev_b32_sdwa v206, 1, v181 dst_sel:DWORD dst_unused:UNUSED_PAD src0_sel:DWORD src1_sel:WORD_1\n"
    "v_mfma_f32_16x16x32_bf16 v[176:179], a[136:139], a[240:243], v[176:179]\n"
    "ds_read_u16 v187, v187\n"
    "v_lshlrev_b32_sdwa v191, 1, v182 dst_sel:DWORD dst_unused:UNUSED_PAD src0_sel:DWORD src1_sel:WORD_1\n"
    "v_mfma_f32_16x16x32_bf16 v[176:179], a[52:55], a[244:247], v[176:179]\n"
    "v_lshlrev_b32_sdwa v207, 1, v183 dst_sel:DWORD dst_unused:UNUSED_PAD src0_sel:DWORD src1_sel:WORD_1\n"
    "v_cvt_pk_f16_f32 v192, v168, v169\n"
    "v_mfma_f32_16x16x32_bf16 v[180:183], a[92:95], a[236:239], 0\n"
    "ds_read_u16_d16_hi v203, v203\n"
    "v_lshlrev_b32_sdwa v193, 1, v170 dst_sel:DWORD dst_unused:UNUSED_PAD src0_sel:DWORD src1_sel:WORD_1\n"
    "v_mfma_f32_16x16x32_bf16 v[180:183], a[140:143], a[240:243], v[180:183]\n"
    "v_lshlrev_b32_sdwa v194, 1, v172 dst_sel:DWORD dst_unused:UNUSED_PAD src0_sel:DWORD src1_sel:WORD_1\n"
    "v_lshlrev_b32_sdwa v195, 1, v174 dst_sel:DWORD dst_unused:UNUSED_PAD src0_sel:DWORD src1_sel:WORD_1\n"
    "v_mfma_f32_16x16x32_bf16 v[180:183], a[252:255], a[244:247], v[180:183]\n"
    "ds_read_u16 v188, v188\n"
    "v_cvt_pk_f16_f32 v196, v176, v177\n"
    "v_lshlrev_b32_sdwa v197, 1, v178 dst_sel:DWORD dst_unused:UNUSED_PAD src0_sel:DWORD src1_sel:WORD_1\n"
    "v_pk_fma_f16 v232, v192, v192, s48\n"
    "s_waitcnt lgkmcnt(7)\n"
    "v_or_b32_e32 v184, v184, v200\n"
    "v_pk_mul_f16 v232, v192, v232\n"
    "v_mov_b32_e64 v254, 0\n"
    "v_lshlrev_b32_sdwa v198, 1, v180 dst_sel:DWORD dst_unused:UNUSED_PAD src0_sel:DWORD src1_sel:WORD_1\n"
    "v_lshlrev_b32_sdwa v199, 1, v182 dst_sel:DWORD dst_unused:UNUSED_PAD src0_sel:DWORD src1_sel:WORD_1\n"
    "v_exp_f16_e64 v234, v232\n"
    "ds_write_b128 v253, v[76:79] offset:4096\n"
    "s_waitcnt lgkmcnt(6)\n"
    "v_or_b32_e32 v185, v185, v201\n"
    "v_lshlrev_b32_sdwa v201, 1, v171 dst_sel:DWORD dst_unused:UNUSED_PAD src0_sel:DWORD src1_sel:WORD_1\n"
    "v_mfma_f32_16x16x32_bf16 v[168:171], a[96:99], a[224:227], 0\n"
    "v_exp_f16_sdwa v234, v232 dst_sel:WORD_1 dst_unused:UNUSED_PRESERVE src0_sel:WORD_1\n"
    "ds_read_u16_d16_hi v204, v204\n"
    "ds_write_b128 v253, v[88:91] offset:0\n"
    "s_waitcnt lgkmcnt(6)\n"
    "v_or_b32_e32 v186, v186, v202\n"
    "v_mfma_f32_16x16x32_bf16 v[168:171], a[144:147], a[228:231], v[168:171]\n"
    "v_lshlrev_b32_sdwa v202, 1, v173 dst_sel:DWORD dst_unused:UNUSED_PAD src0_sel:DWORD src1_sel:WORD_1\n"
    "v_pk_fma_f16 v234, v234, s49, s49\n"
    "v_mfma_f32_16x16x32_bf16 v[168:171], a[56:59], a[232:235], v[168:171]\n"
    "ds_read_u16 v189, v189\n"
    "ds_read_u16_d16_hi v205, v205\n"
    "v_rcp_f16_e32 v232, v234\n"
    "s_waitcnt lgkmcnt(6)\n"
    "v_or_b32_e64 v187, v187, v203\n"
    "v_lshlrev_b32_sdwa v203, 1, v175 dst_sel:DWORD dst_unused:UNUSED_PAD src0_sel:DWORD src1_sel:WORD_1\n"
    "v_mfma_f32_16x16x32_bf16 v[172:175], a[100:103], a[224:227], 0\n"
    "ds_write_b128 v253, v[92:95] offset:3072\n"
    "v_mfma_f32_16x16x32_bf16 v[172:175], a[148:151], a[228:231], v[172:175]\n"
    "v_lshlrev_b32_sdwa v200, 1, v169 dst_sel:DWORD dst_unused:UNUSED_PAD src0_sel:DWORD src1_sel:WORD_1\n"
    "v_rcp_f16_sdwa v232, v234 dst_sel:WORD_1 dst_unused:UNUSED_PRESERVE src0_sel:WORD_1\n"
    "v_mfma_f32_16x16x32_bf16 v[172:175], a[60:63], a[232:235], v[172:175]\n"
    "v_fma_mix_f32 v234, v192, v232, 0 op_sel_hi:[1,1,0]\n"
    "ds_read_u16 v190, v190\n"
    "ds_read_u16_d16_hi v206, v206\n"
    "ds_read_u16 v191, v191\n"
    "s_waitcnt lgkmcnt(6)\n"
    "v_or_b32_e32 v188, v188, v204\n"
    "v_fma_mix_f32 v232, v192, v232, 0 op_sel:[1,1,0] op_sel_hi:[1,1,0]\n"
    "v_cvt_pk_bf16_f32 v192, v234, v232\n"
    "v_pk_fma_f16 v232, v196, v196, s48\n"
    "v_pk_mul_f16 v232, v196, v232\n"
    "v_exp_f16_e64 v234, v232\n"
    "ds_write_b128 v253, v[104:107] offset:2048\n"
    "v_exp_f16_sdwa v234, v232 dst_sel:WORD_1 dst_unused:UNUSED_PRESERVE src0_sel:WORD_1\n"
    "ds_read_u16_d16_hi v207, v207\n"
    "ds_read_u16 v193, v193\n"
    "s_waitcnt lgkmcnt(6)\n"
    "v_or_b32_e32 v189, v189, v205\n"
    "v_lshlrev_b32_sdwa v205, 1, v179 dst_sel:DWORD dst_unused:UNUSED_PAD src0_sel:DWORD src1_sel:WORD_1\n"
    "v_mfma_f32_16x16x32_bf16 v[176:179], a[96:99], a[236:239], 0\n"
    "ds_write_b128 v253, v[108:111] offset:5120\n"
    "v_mfma_f32_16x16x32_bf16 v[176:179], a[144:147], a[240:243], v[176:179]\n"
    "v_pk_fma_f16 v234, v234, s49, s49\n"
    "v_rcp_f16_e64 v232, v234\n"
    "v_mfma_f32_16x16x32_bf16 v[176:179], a[56:59], a[244:247], v[176:179]\n"
    "v_rcp_f16_sdwa v232, v234 dst_sel:WORD_1 dst_unused:UNUSED_PRESERVE src0_sel:WORD_1\n"
    "s_waitcnt lgkmcnt(5)\n"
    "v_or_b32_e32 v190, v190, v206\n"
    "v_mfma_f32_16x16x32_bf16 v[96:99], a[0:3], v[184:187], 0\n"
    "s_barrier\n"
    "v_mfma_f32_16x16x32_bf16 v[100:103], a[4:7], v[184:187], 0\n"
    "ds_read_u16_d16_hi v201, v201\n"
    "ds_read_u16 v194, v194\n"
    "v_mfma_f32_16x16x32_bf16 v[112:115], a[8:11], v[184:187], 0\n"
    "s_waitcnt lgkmcnt(4)\n"
    "ds_read_u16_d16_hi v202, v202\n"
    "ds_read_u16 v195, v195\n"
    "v_lshlrev_b32_sdwa v206, 1, v181 dst_sel:DWORD dst_unused:UNUSED_PAD src0_sel:DWORD src1_sel:WORD_1\n"
    "v_or_b32_e64 v191, v191, v207\n"
    "v_mfma_f32_16x16x32_bf16 v[116:119], a[12:15], v[184:187], 0\n"
    "v_lshlrev_b32_sdwa v207, 1, v183 dst_sel:DWORD dst_unused:UNUSED_PAD src0_sel:DWORD src1_sel:WORD_1\n"
    "v_lshlrev_b32_sdwa v204, 1, v177 dst_sel:DWORD dst_unused:UNUSED_PAD src0_sel:DWORD src1_sel:WORD_1\n"
    "v_mfma_f32_16x16x32_bf16 v[180:183], a[100:103], a[236:239], 0\n"
    "ds_read_b128 v[236:239], v244 offset:0\n"
    "v_fma_mix_f32 v234, v196, v232, 0 op_sel_hi:[1,1,0]\n"
    "v_mfma_f32_16x16x32_bf16 v[180:183], a[148:151], a[240:243], v[180:183]\n"
    "ds_read_u16_d16_hi v203, v203\n"
    "v_fma_mix_f32 v232, v196, v232, 0 op_sel:[1,1,0] op_sel_hi:[1,1,0]\n"
    "v_mfma_f32_16x16x32_bf16 v[180:183], a[60:63], a[244:247], v[180:183]\n"
    "v_cvt_pk_bf16_f32 v196, v234, v232\n"
    "ds_read_b128 v[164:167], v244 offset:2048\n"
    "v_mfma_f32_16x16x32_bf16 v[104:107], a[0:3], v[188:191], 0\n"
    "ds_read_u16 v197, v197\n"
    "v_mfma_f32_16x16x32_bf16 v[108:111], a[4:7], v[188:191], 0\n"
    "v_mfma_f32_16x16x32_bf16 v[120:123], a[8:11], v[188:191], 0\n"
    "ds_read_u16_d16_hi v205, v205\n"
    "v_mfma_f32_16x16x32_bf16 v[124:127], a[12:15], v[188:191], 0\n"
    "s_waitcnt lgkmcnt(8)\n"
    "v_or_b32_e32 v193, v193, v201\n"
    "ds_read_u16 v198, v198\n"
    "v_lshlrev_b32_sdwa v201, 1, v171 dst_sel:DWORD dst_unused:UNUSED_PAD src0_sel:DWORD src1_sel:WORD_1\n"
    "ds_read_u16_d16_hi v206, v206\n"
    "ds_read_u16 v199, v199\n"
    "ds_read_u16_d16_hi v207, v207\n"
    "ds_read_u16_d16_hi v200, v200\n"
    "ds_read_u16_d16_hi v201, v201\n"
    "ds_read_u16_d16_hi v204, v204\n"
    "ds_read_b128 a[252:255], v244 offset:34816\n"
    "s_waitcnt lgkmcnt(14)\n"
    "v_or_b32_e32 v194, v194, v202\n"
    "v_lshlrev_b32_sdwa v202, 1, v173 dst_sel:DWORD dst_unused:UNUSED_PAD src0_sel:DWORD src1_sel:WORD_1\n"
    "ds_read_u16_d16_hi v202, v202\n"
    "s_waitcnt lgkmcnt(13)\n"
    "v_mfma_f32_16x16x32_bf16 v[128:131], v[236:239], v[184:187], 0\n"
    "s_nop 0\n"
    "v_mfma_f32_16x16x32_bf16 v[136:139], v[236:239], v[188:191], 0\n"
    "ds_read_b128 v[236:239], v244 offset:4096\n"
    "s_waitcnt lgkmcnt(12)\n"
    "v_mfma_f32_16x16x32_bf16 v[132:135], v[164:167], v[184:187], 0\n"
    "v_or_b32_e32 v195, v195, v203\n"
    "v_lshlrev_b32_sdwa v184, 1, v168 dst_sel:DWORD dst_unused:UNUSED_PAD src0_sel:DWORD src1_sel:WORD_1\n"
    "v_mfma_f32_16x16x32_bf16 v[140:143], v[164:167], v[188:191], 0\n"
    "v_lshlrev_b32_sdwa v185, 1, v170 dst_sel:DWORD dst_unused:UNUSED_PAD src0_sel:DWORD src1_sel:WORD_1\n"
    "v_lshlrev_b32_sdwa v186, 1, v172 dst_sel:DWORD dst_unused:UNUSED_PAD src0_sel:DWORD src1_sel:WORD_1\n"
    "v_mfma_f32_16x16x32_bf16 v[168:171], a[104:107], a[224:227], 0\n"
    "v_lshlrev_b32_sdwa v187, 1, v174 dst_sel:DWORD dst_unused:UNUSED_PAD src0_sel:DWORD src1_sel:WORD_1\n"
    "v_lshlrev_b32_sdwa v203, 1, v175 dst_sel:DWORD dst_unused:UNUSED_PAD src0_sel:DWORD src1_sel:WORD_1\n"
    "v_mfma_f32_16x16x32_bf16 v[168:171], a[152:155], a[228:231], v[168:171]\n"
    "v_lshlrev_b32_sdwa v188, 1, v176 dst_sel:DWORD dst_unused:UNUSED_PAD src0_sel:DWORD src1_sel:WORD_1\n"
    "v_lshlrev_b32_sdwa v189, 1, v178 dst_sel:DWORD dst_unused:UNUSED_PAD src0_sel:DWORD src1_sel:WORD_1\n"
    "v_mfma_f32_16x16x32_bf16 v[168:171], a[64:67], a[232:235], v[168:171]\n"
    "s_nop 0\n"
    "v_lshlrev_b32_sdwa v190, 1, v180 dst_sel:DWORD dst_unused:UNUSED_PAD src0_sel:DWORD src1_sel:WORD_1\n"
    "v_lshlrev_b32_sdwa v191, 1, v182 dst_sel:DWORD dst_unused:UNUSED_PAD src0_sel:DWORD src1_sel:WORD_1\n"
    "v_mfma_f32_16x16x32_bf16 v[172:175], a[108:111], a[224:227], 0\n"
    "s_waitcnt lgkmcnt(10)\n"
    "buffer_load_dwordx4 v[0:3], v246, s[40:43], s60 offen offset:0\n"
    "buffer_load_dwordx4 v[32:35], v246, s[40:43], s60 offen offset:64\n"
    "buffer_load_dwordx4 v[64:67], v246, s[40:43], s60 offen offset:128\n"
    "v_mfma_f32_16x16x32_bf16 v[172:175], a[156:159], a[228:231], v[172:175]\n"
    "v_or_b32_e64 v197, v197, v205\n"
    "v_lshlrev_b32_sdwa v205, 1, v179 dst_sel:DWORD dst_unused:UNUSED_PAD src0_sel:DWORD src1_sel:WORD_1\n"
    "v_mfma_f32_16x16x32_bf16 v[172:175], a[68:71], a[232:235], v[172:175]\n"
    "s_nop 0\n"
    "v_mfma_f32_16x16x32_bf16 v[176:179], a[104:107], a[236:239], 0\n"
    "s_waitcnt lgkmcnt(8)\n"
    "ds_read_u16 v184, v184\n"
    "v_or_b32_e64 v198, v198, v206\n"
    "v_mfma_f32_16x16x32_bf16 v[176:179], a[152:155], a[240:243], v[176:179]\n"
    "v_lshlrev_b32_sdwa v206, 1, v181 dst_sel:DWORD dst_unused:UNUSED_PAD src0_sel:DWORD src1_sel:WORD_1\n"
    "v_mfma_f32_16x16x32_bf16 v[176:179], a[64:67], a[244:247], v[176:179]\n"
    "ds_read_u16 v185, v185\n"
    "v_mfma_f32_16x16x32_bf16 v[96:99], a[16:19], v[192:195], v[96:99]\n"
    "s_waitcnt lgkmcnt(8)\n"
    "v_or_b32_e32 v199, v199, v207\n"
    "v_lshlrev_b32_sdwa v207, 1, v183 dst_sel:DWORD dst_unused:UNUSED_PAD src0_sel:DWORD src1_sel:WORD_1\n"
    "v_mfma_f32_16x16x32_bf16 v[180:183], a[108:111], a[236:239], 0\n"
    "ds_read_u16 v186, v186\n"
    "v_mfma_f32_16x16x32_bf16 v[180:183], a[156:159], a[240:243], v[180:183]\n"
    "v_mfma_f32_16x16x32_bf16 v[180:183], a[68:71], a[244:247], v[180:183]\n"
    "ds_read_u16 v187, v187\n"
    "v_mfma_f32_16x16x32_bf16 v[100:103], a[20:23], v[192:195], v[100:103]\n"
    "ds_read_u16_d16_hi v203, v203\n"
    "buffer_load_dwordx4 v[20:23], v247, s[40:43], s60 offen offset:0\n"
    "buffer_load_dwordx4 v[52:55], v247, s[40:43], s60 offen offset:64\n"
    "buffer_load_dwordx4 v[84:87], v247, s[40:43], s60 offen offset:128\n"
    "v_mfma_f32_16x16x32_bf16 v[112:115], a[24:27], v[192:195], v[112:115]\n"
    "v_mfma_f32_16x16x32_bf16 v[116:119], a[28:31], v[192:195], v[116:119]\n"
    "ds_read_b128 v[164:167], v244 offset:6144\n"
    "ds_read_u16 v188, v188\n"
    "v_mfma_f32_16x16x32_bf16 v[104:107], a[16:19], v[196:199], v[104:107]\n"
    "s_nop 0\n"
    "v_mfma_f32_16x16x32_bf16 v[108:111], a[20:23], v[196:199], v[108:111]\n"
    "s_waitcnt lgkmcnt(7)\n"
    "ds_read_u16 v189, v189\n"
    "v_mfma_f32_16x16x32_bf16 v[128:131], v[236:239], v[192:195], v[128:131]\n"
    "v_mfma_f32_16x16x32_bf16 v[120:123], a[24:27], v[196:199], v[120:123]\n"
    "ds_read_u16_d16_hi v205, v205\n"
    "s_waitcnt lgkmcnt(8)\n"
    "v_or_b32_e32 v184, v184, v200\n"
    "v_mfma_f32_16x16x32_bf16 v[124:127], a[28:31], v[196:199], v[124:127]\n"
    "s_nop 0\n"
    "v_mfma_f32_16x16x32_bf16 v[136:139], v[236:239], v[196:199], v[136:139]\n"
    "s_waitcnt lgkmcnt(7)\n"
    "ds_read_u16 v190, v190\n"
    "v_or_b32_e64 v185, v185, v201\n"
    "v_lshlrev_b32_sdwa v201, 1, v171 dst_sel:DWORD dst_unused:UNUSED_PAD src0_sel:DWORD src1_sel:WORD_1\n"
    "ds_read_u16_d16_hi v206, v206\n"
    "buffer_load_dwordx4 v[4:7], v246, s[40:43], s61 offen offset:0\n"
    "buffer_load_dwordx4 v[36:39], v246, s[40:43], s61 offen offset:64\n"
    "buffer_load_dwordx4 v[68:71], v246, s[40:43], s61 offen offset:128\n"
    "s_waitcnt lgkmcnt(8)\n"
    "v_or_b32_e32 v186, v186, v202\n"
    "v_lshlrev_b32_sdwa v202, 1, v173 dst_sel:DWORD dst_unused:UNUSED_PAD src0_sel:DWORD src1_sel:WORD_1\n"
    "ds_read_u16 v191, v191\n"
    "ds_read_u16_d16_hi v207, v207\n"
    "ds_read_u16_d16_hi v201, v201\n"
    "ds_read_u16_d16_hi v202, v202\n"
    "ds_read_b128 v[236:239], v244 offset:8192\n"
    "s_waitcnt lgkmcnt(10)\n"
    "v_mfma_f32_16x16x32_bf16 v[132:135], v[164:167], v[192:195], v[132:135]\n"
    "s_nop 0\n"
    "v_cvt_pk_f16_f32 v192, v168, v169\n"
    "v_lshlrev_b32_sdwa v193, 1, v170 dst_sel:DWORD dst_unused:UNUSED_PAD src0_sel:DWORD src1_sel:WORD_1\n"
    "v_mfma_f32_16x16x32_bf16 v[140:143], v[164:167], v[196:199], v[140:143]\n"
    "v_lshlrev_b32_sdwa v194, 1, v172 dst_sel:DWORD dst_unused:UNUSED_PAD src0_sel:DWORD src1_sel:WORD_1\n"
    "v_lshlrev_b32_sdwa v195, 1, v174 dst_sel:DWORD dst_unused:UNUSED_PAD src0_sel:DWORD src1_sel:WORD_1\n"
    "v_mfma_f32_16x16x32_bf16 v[168:171], a[112:115], a[224:227], 0\n"
    "v_cvt_pk_f16_f32 v196, v176, v177\n"
    "v_lshlrev_b32_sdwa v197, 1, v178 dst_sel:DWORD dst_unused:UNUSED_PAD src0_sel:DWORD src1_sel:WORD_1\n"
    "v_mfma_f32_16x16x32_bf16 v[168:171], a[160:163], a[228:231], v[168:171]\n"
    "s_waitcnt lgkmcnt(9)\n"
    "v_lshlrev_b32_sdwa v198, 1, v180 dst_sel:DWORD dst_unused:UNUSED_PAD src0_sel:DWORD src1_sel:WORD_1\n"
    "v_lshlrev_b32_sdwa v199, 1, v182 dst_sel:DWORD dst_unused:UNUSED_PAD src0_sel:DWORD src1_sel:WORD_1\n"
    "v_mfma_f32_16x16x32_bf16 v[168:171], a[72:75], a[232:235], v[168:171]\n"
    "v_or_b32_e32 v187, v187, v203\n"
    "v_lshlrev_b32_sdwa v203, 1, v175 dst_sel:DWORD dst_unused:UNUSED_PAD src0_sel:DWORD src1_sel:WORD_1\n"
    "v_mfma_f32_16x16x32_bf16 v[172:175], a[116:119], a[224:227], 0\n"
    "v_or_b32_e32 v188, v188, v204\n"
    "v_pk_fma_f16 v232, v192, v192, s48\n"
    "v_mfma_f32_16x16x32_bf16 v[172:175], a[164:167], a[228:231], v[172:175]\n"
    "s_waitcnt lgkmcnt(7)\n"
    "ds_read_u16 v193, v193\n"
    "buffer_load_dwordx4 v[24:27], v247, s[40:43], s61 offen offset:0\n"
    "buffer_load_dwordx4 v[56:59], v247, s[40:43], s61 offen offset:64\n"
    "buffer_load_dwordx4 v[88:91], v247, s[40:43], s61 offen offset:128\n"
    "v_mfma_f32_16x16x32_bf16 v[172:175], a[252:255], a[232:235], v[172:175]\n"
    "ds_read_u16 v194, v194\n"
    "v_or_b32_e64 v189, v189, v205\n"
    "v_lshlrev_b32_sdwa v205, 1, v179 dst_sel:DWORD dst_unused:UNUSED_PAD src0_sel:DWORD src1_sel:WORD_1\n"
    "v_mfma_f32_16x16x32_bf16 v[176:179], a[112:115], a[236:239], 0\n"
    "ds_read_u16 v195, v195\n"
    "v_pk_mul_f16 v232, v192, v232\n"
    "v_mfma_f32_16x16x32_bf16 v[176:179], a[160:163], a[240:243], v[176:179]\n"
    "s_waitcnt lgkmcnt(8)\n"
    "v_or_b32_e32 v190, v190, v206\n"
    "v_lshlrev_b32_sdwa v206, 1, v181 dst_sel:DWORD dst_unused:UNUSED_PAD src0_sel:DWORD src1_sel:WORD_1\n"
    "v_mfma_f32_16x16x32_bf16 v[176:179], a[72:75], a[244:247], v[176:179]\n"
    "ds_read_u16_d16_hi v203, v203\n"
    "v_exp_f16_e32 v234, v232\n"
    "v_mfma_f32_16x16x32_bf16 v[96:99], a[32:35], v[184:187], v[96:99]\n"
    "v_exp_f16_sdwa v234, v232 dst_sel:WORD_1 dst_unused:UNUSED_PRESERVE src0_sel:WORD_1\n"
    "v_mfma_f32_16x16x32_bf16 v[100:103], a[36:39], v[184:187], v[100:103]\n"
    "s_waitcnt lgkmcnt(7)\n"
    "ds_read_b128 v[164:167], v244 offset:10240\n"
    "ds_read_u16 v197, v197\n"
    "v_mfma_f32_16x16x32_bf16 v[112:115], a[40:43], v[184:187], v[112:115]\n"
    "v_or_b32_e64 v191, v191, v207\n"
    "v_lshlrev_b32_sdwa v207, 1, v183 dst_sel:DWORD dst_unused:UNUSED_PAD src0_sel:DWORD src1_sel:WORD_1\n"
    "v_mfma_f32_16x16x32_bf16 v[180:183], a[116:119], a[236:239], 0\n"
    "ds_read_u16_d16_hi v205, v205\n"
    "v_pk_fma_f16 v234, v234, s49, s49\n"
    "v_mfma_f32_16x16x32_bf16 v[180:183], a[164:167], a[240:243], v[180:183]\n"
    "s_waitcnt lgkmcnt(7)\n"
    "v_lshlrev_b32_sdwa v200, 1, v169 dst_sel:DWORD dst_unused:UNUSED_PAD src0_sel:DWORD src1_sel:WORD_1\n"
    "v_rcp_f16_e32 v232, v234\n"
    "v_mfma_f32_16x16x32_bf16 v[180:183], a[252:255], a[244:247], v[180:183]\n"
    "ds_read_u16 v198, v198\n"
    "buffer_load_dwordx4 v[8:11], v246, s[40:43], s62 offen offset:0\n"
    "buffer_load_dwordx4 v[40:43], v246, s[40:43], s62 offen offset:64\n"
    "buffer_load_dwordx4 v[72:75], v246, s[40:43], s62 offen offset:128\n"
    "v_mfma_f32_16x16x32_bf16 v[116:119], a[44:47], v[184:187], v[116:119]\n"
    "ds_read_u16_d16_hi v206, v206\n"
    "v_rcp_f16_sdwa v232, v234 dst_sel:WORD_1 dst_unused:UNUSED_PRESERVE src0_sel:WORD_1\n"
    "v_mfma_f32_16x16x32_bf16 v[128:131], v[236:239], v[184:187], v[128:131]\n"
    "s_waitcnt lgkmcnt(8)\n"
    "v_or_b32_e32 v193, v193, v201\n"
    "v_lshlrev_b32_sdwa v201, 1, v171 dst_sel:DWORD dst_unused:UNUSED_PAD src0_sel:DWORD src1_sel:WORD_1\n"
    "v_mfma_f32_16x16x32_bf16 v[104:107], a[32:35], v[188:191], v[104:107]\n"
    "s_waitcnt lgkmcnt(7)\n"
    "ds_read_u16 v199, v199\n"
    "v_or_b32_e32 v194, v194, v202\n"
    "v_mfma_f32_16x16x32_bf16 v[108:111], a[36:39], v[188:191], v[108:111]\n"
    "ds_read_u16_d16_hi v207, v207\n"
    "v_lshlrev_b32_sdwa v202, 1, v173 dst_sel:DWORD dst_unused:UNUSED_PAD src0_sel:DWORD src1_sel:WORD_1\n"
    "v_mfma_f32_16x16x32_bf16 v[120:123], a[40:43], v[188:191], v[120:123]\n"
    "v_lshlrev_b32_sdwa v204, 1, v177 dst_sel:DWORD dst_unused:UNUSED_PAD src0_sel:DWORD src1_sel:WORD_1\n"
    "v_fma_mix_f32 v234, v192, v232, 0 op_sel_hi:[1,1,0]\n"
    "v_mfma_f32_16x16x32_bf16 v[124:127], a[44:47], v[188:191], v[124:127]\n"
    "s_waitcnt lgkmcnt(6)\n"
    "ds_read_b128 a[252:255], v244 offset:38912\n"
    "v_or_b32_e32 v195, v195, v203\n"
    "v_mfma_f32_16x16x32_bf16 v[132:135], v[164:167], v[184:187], v[132:135]\n"
    "ds_read_u16_d16_hi v200, v200\n"
    "v_lshlrev_b32_sdwa v184, 1, v168 dst_sel:DWORD dst_unused:UNUSED_PAD src0_sel:DWORD src1_sel:WORD_1\n"
    "v_mfma_f32_16x16x32_bf16 v[136:139], v[236:239], v[188:191], v[136:139]\n"
    "v_lshlrev_b32_sdwa v185, 1, v170 dst_sel:DWORD dst_unused:UNUSED_PAD src0_sel:DWORD src1_sel:WORD_1\n"
    "v_lshlrev_b32_sdwa v186, 1, v172 dst_sel:DWORD dst_unused:UNUSED_PAD src0_sel:DWORD src1_sel:WORD_1\n"
    "v_mfma_f32_16x16x32_bf16 v[140:143], v[164:167], v[188:191], v[140:143]\n"
    "ds_read_u16 v184, v184\n"
    "v_lshlrev_b32_sdwa v187, 1, v174 dst_sel:DWORD dst_unused:UNUSED_PAD src0_sel:DWORD src1_sel:WORD_1\n"
    "v_mfma_f32_16x16x32_bf16 v[168:171], a[120:123], a[224:227], 0\n"
    "buffer_load_dwordx4 v[12:15], v246, s[40:43], s63 offen offset:0\n"
    "buffer_load_dwordx4 v[44:47], v246, s[40:43], s63 offen offset:64\n"
    "buffer_load_dwordx4 v[76:79], v246, s[40:43], s63 offen offset:128\n"
    "ds_read_u16 v185, v185\n"
    "v_mfma_f32_16x16x32_bf16 v[168:171], a[168:171], a[228:231], v[168:171]\n"
    "s_waitcnt lgkmcnt(8)\n"
    "v_or_b32_e32 v197, v197, v205\n"
    "v_lshlrev_b32_sdwa v203, 1, v175 dst_sel:DWORD dst_unused:UNUSED_PAD src0_sel:DWORD src1_sel:WORD_1\n"
    "v_mfma_f32_16x16x32_bf16 v[168:171], a[76:79], a[232:235], v[168:171]\n"
    "ds_read_u16_d16_hi v201, v201\n"
    "v_lshlrev_b32_sdwa v188, 1, v176 dst_sel:DWORD dst_unused:UNUSED_PAD src0_sel:DWORD src1_sel:WORD_1\n"
    "v_mfma_f32_16x16x32_bf16 v[172:175], a[124:127], a[224:227], 0\n"
    "v_lshlrev_b32_sdwa v189, 1, v178 dst_sel:DWORD dst_unused:UNUSED_PAD src0_sel:DWORD src1_sel:WORD_1\n"
    "v_lshlrev_b32_sdwa v205, 1, v179 dst_sel:DWORD dst_unused:UNUSED_PAD src0_sel:DWORD src1_sel:WORD_1\n"
    "v_mfma_f32_16x16x32_bf16 v[172:175], a[172:175], a[228:231], v[172:175]\n"
    "s_waitcnt lgkmcnt(7)\n"
    "ds_read_u16 v186, v186\n"
    "v_or_b32_e32 v198, v198, v206\n"
    "v_lshlrev_b32_sdwa v190, 1, v180 dst_sel:DWORD dst_unused:UNUSED_PAD src0_sel:DWORD src1_sel:WORD_1\n"
    "v_lshlrev_b32_sdwa v206, 1, v181 dst_sel:DWORD dst_unused:UNUSED_PAD src0_sel:DWORD src1_sel:WORD_1\n"
    "v_lshlrev_b32_sdwa v191, 1, v182 dst_sel:DWORD dst_unused:UNUSED_PAD src0_sel:DWORD src1_sel:WORD_1\n"
    "v_fma_mix_f32 v232, v192, v232, 0 op_sel:[1,1,0] op_sel_hi:[1,1,0]\n"
    "v_mfma_f32_16x16x32_bf16 v[176:179], a[120:123], a[236:239], 0\n"
    "ds_read_u16_d16_hi v202, v202\n"
    "v_cvt_pk_bf16_f32 v192, v234, v232\n"
    "v_mfma_f32_16x16x32_bf16 v[176:179], a[168:171], a[240:243], v[176:179]\n"
    "s_waitcnt lgkmcnt(6)\n"
    "ds_read_b128 v[236:239], v244 offset:12288\n"
    "v_or_b32_e32 v199, v199, v207\n"
    "v_mfma_f32_16x16x32_bf16 v[172:175], a[252:255], a[232:235], v[172:175]\n"
    "ds_read_u16 v187, v187\n"
    "v_lshlrev_b32_sdwa v207, 1, v183 dst_sel:DWORD dst_unused:UNUSED_PAD src0_sel:DWORD src1_sel:WORD_1\n"
    "v_mfma_f32_16x16x32_bf16 v[176:179], a[76:79], a[244:247], v[176:179]\n"
    "v_pk_fma_f16 v232, v196, v196, s48\n"
    "v_pk_mul_f16 v232, v196, v232\n"
    "v_mfma_f32_16x16x32_bf16 v[180:183], a[124:127], a[236:239], 0\n"
    "buffer_load_dwordx4 v[16:19], v246, s[40:43], s52 offen offset:0\n"
    "buffer_load_dwordx4 v[48:51], v246, s[40:43], s52 offen offset:64\n"
    "buffer_load_dwordx4 v[80:83], v246, s[40:43], s52 offen offset:128\n"
    "ds_read_u16_d16_hi v203, v203\n"
    "v_mfma_f32_16x16x32_bf16 v[180:183], a[172:175], a[240:243], v[180:183]\n"
    "ds_read_b128 v[164:167], v244 offset:14336\n"
    "v_exp_f16_e32 v234, v232\n"
    "v_mfma_f32_16x16x32_bf16 v[180:183], a[252:255], a[244:247], v[180:183]\n"
    "s_waitcnt lgkmcnt(8)\n"
    "ds_read_u16 v188, v188\n"
    "v_exp_f16_sdwa v234, v232 dst_sel:WORD_1 dst_unused:UNUSED_PRESERVE src0_sel:WORD_1\n"
    "v_mfma_f32_16x16x32_bf16 v[96:99], a[176:179], v[192:195], v[96:99]\n"
    "ds_read_u16_d16_hi v204, v204\n"
    "v_pk_fma_f16 v234, v234, s49, s49\n"
    "v_mfma_f32_16x16x32_bf16 v[100:103], a[180:183], v[192:195], v[100:103]\n"
    "v_rcp_f16_e32 v232, v234\n"
    "v_mfma_f32_16x16x32_bf16 v[112:115], a[184:187], v[192:195], v[112:115]\n"
    "s_waitcnt lgkmcnt(8)\n"
    "ds_read_u16 v189, v189\n"
    "v_rcp_f16_sdwa v232, v234 dst_sel:WORD_1 dst_unused:UNUSED_PRESERVE src0_sel:WORD_1\n"
    "v_mfma_f32_16x16x32_bf16 v[116:119], a[188:191], v[192:195], v[116:119]\n"
    "v_fma_mix_f32 v234, v196, v232, 0 op_sel_hi:[1,1,0]\n"
    "v_fma_mix_f32 v232, v196, v232, 0 op_sel:[1,1,0] op_sel_hi:[1,1,0]\n"
    "v_or_b32_e64 v184, v184, v200\n"
    "v_cvt_pk_bf16_f32 v196, v234, v232\n"
    "v_or_b32_e32 v185, v185, v201\n"
    "s_waitcnt lgkmcnt(6)\n"
    "v_mfma_f32_16x16x32_bf16 v[128:131], v[236:239], v[192:195], v[128:131]\n"
    "ds_read_u16_d16_hi v205, v205\n"
    "ds_read_u16 v190, v190\n"
    "v_mfma_f32_16x16x32_bf16 v[104:107], a[176:179], v[196:199], v[104:107]\n"
    "buffer_load_dwordx4 v[152:155], v245, s[40:43], s57 offen offset:64\n"
    "buffer_load_dwordx4 v[144:147], v245, s[40:43], s57 offen offset:0\n"
    "v_mfma_f32_16x16x32_bf16 v[108:111], a[180:183], v[196:199], v[108:111]\n"
    "ds_read_b128 a[224:227], v243 offset:24640\n"
    "v_lshlrev_b32_sdwa v201, 1, v171 dst_sel:DWORD dst_unused:UNUSED_PAD src0_sel:DWORD src1_sel:WORD_1\n"
    "v_mfma_f32_16x16x32_bf16 v[120:123], a[184:187], v[196:199], v[120:123]\n"
    "ds_read_u16_d16_hi v206, v206\n"
    "v_or_b32_e32 v186, v186, v202\n"
    "v_mfma_f32_16x16x32_bf16 v[124:127], a[188:191], v[196:199], v[124:127]\n"
    "s_waitcnt lgkmcnt(7)\n"
    "v_lshlrev_b32_sdwa v202, 1, v173 dst_sel:DWORD dst_unused:UNUSED_PAD src0_sel:DWORD src1_sel:WORD_1\n"
    "v_or_b32_e64 v187, v187, v203\n"
    "v_mfma_f32_16x16x32_bf16 v[132:135], v[164:167], v[192:195], v[132:135]\n"
    "ds_read_b128 a[228:231], v243 offset:25664\n"
    "v_cvt_pk_f16_f32 v192, v168, v169\n"
    "v_mfma_f32_16x16x32_bf16 v[136:139], v[236:239], v[196:199], v[136:139]\n"
    "ds_read_b128 a[232:235], v243 offset:26688\n"
    "v_lshlrev_b32_sdwa v193, 1, v170 dst_sel:DWORD dst_unused:UNUSED_PAD src0_sel:DWORD src1_sel:WORD_1\n"
    "v_mfma_f32_16x16x32_bf16 v[140:143], v[164:167], v[196:199], v[140:143]\n"
    "ds_read_u16 v191, v191\n"
    "v_lshlrev_b32_sdwa v194, 1, v172 dst_sel:DWORD dst_unused:UNUSED_PAD src0_sel:DWORD src1_sel:WORD_1\n"
    "v_lshlrev_b32_sdwa v195, 1, v174 dst_sel:DWORD dst_unused:UNUSED_PAD src0_sel:DWORD src1_sel:WORD_1\n"
    "v_lshlrev_b32_sdwa v203, 1, v175 dst_sel:DWORD dst_unused:UNUSED_PAD src0_sel:DWORD src1_sel:WORD_1\n"
    "v_cvt_pk_f16_f32 v196, v176, v177\n"
    "v_lshlrev_b32_sdwa v197, 1, v178 dst_sel:DWORD dst_unused:UNUSED_PAD src0_sel:DWORD src1_sel:WORD_1\n"
    "v_lshlrev_b32_sdwa v198, 1, v180 dst_sel:DWORD dst_unused:UNUSED_PAD src0_sel:DWORD src1_sel:WORD_1\n"
    "v_lshlrev_b32_sdwa v199, 1, v182 dst_sel:DWORD dst_unused:UNUSED_PAD src0_sel:DWORD src1_sel:WORD_1\n"
    "s_waitcnt lgkmcnt(8)\n"
    "v_or_b32_e32 v188, v188, v204\n"
    "ds_read_b128 a[236:239], v243 offset:27712\n"
    "ds_read_u16_d16_hi v207, v207\n"
    "v_pk_fma_f16 v232, v192, v192, s48\n"
    "buffer_load_dwordx4 v[156:159], v245, s[40:43], s88 offen offset:64\n"
    "buffer_load_dwordx4 v[148:151], v245, s[40:43], s88 offen offset:0\n"
    "ds_read_b128 a[240:243], v243 offset:28736\n"
    "s_waitcnt lgkmcnt(9)\n"
    "v_or_b32_e32 v189, v189, v205\n"
    "ds_read_b128 a[244:247], v243 offset:29760\n"
    "ds_read_u16 v193, v193\n"
    "s_waitcnt lgkmcnt(9)\n"
    "v_mfma_f32_16x16x32_bf16 v[168:171], a[80:83], a[224:227], 0\n"
    "s_nop 0\n"
    "v_lshlrev_b32_sdwa v205, 1, v179 dst_sel:DWORD dst_unused:UNUSED_PAD src0_sel:DWORD src1_sel:WORD_1\n"
    "v_pk_mul_f16 v232, v192, v232\n"
    "v_mfma_f32_16x16x32_bf16 v[172:175], a[84:87], a[224:227], 0\n"
    "ds_read_b128 a[252:255], v244 offset:32768\n"
    "v_exp_f16_e64 v234, v232\n"
    "ds_read_u16_d16_hi v201, v201\n"
    "s_waitcnt lgkmcnt(9)\n"
    "v_mfma_f32_16x16x32_bf16 v[168:171], a[128:131], a[228:231], v[168:171]\n"
    "s_waitcnt lgkmcnt(8)\n"
    "ds_read_u16 v194, v194\n"
    "v_or_b32_e64 v190, v190, v206\n"
    "v_mfma_f32_16x16x32_bf16 v[168:171], a[48:51], a[232:235], v[168:171]\n"
    "v_lshlrev_b32_sdwa v206, 1, v181 dst_sel:DWORD dst_unused:UNUSED_PAD src0_sel:DWORD src1_sel:WORD_1\n"
    "v_exp_f16_sdwa v234, v232 dst_sel:WORD_1 dst_unused:UNUSED_PRESERVE src0_sel:WORD_1\n"
    "v_mfma_f32_16x16x32_bf16 v[172:175], a[132:135], a[228:231], v[172:175]\n"
    "ds_read_u16_d16_hi v202, v202\n"
    "v_pk_fma_f16 v234, v234, s49, s49\n"
    "v_mfma_f32_16x16x32_bf16 v[96:99], a[192:195], v[184:187], v[96:99]\n"
    "s_waitcnt lgkmcnt(8)\n"
    "v_rcp_f16_e32 v232, v234\n"
    "v_mfma_f32_16x16x32_bf16 v[176:179], a[80:83], a[236:239], 0\n"
    "ds_read_u16 v195, v195\n"
    "v_lshlrev_b32_sdwa v200, 1, v169 dst_sel:DWORD dst_unused:UNUSED_PAD src0_sel:DWORD src1_sel:WORD_1\n"
    "v_mfma_f32_16x16x32_bf16 v[100:103], a[196:199], v[184:187], v[100:103]\n"
    "s_waitcnt lgkmcnt(7)\n"
    "ds_read_b128 v[236:239], v244 offset:16384\n"
    "v_or_b32_e64 v191, v191, v207\n"
    "v_mfma_f32_16x16x32_bf16 v[176:179], a[128:131], a[240:243], v[176:179]\n"
    "s_waitcnt lgkmcnt(7)\n"
    "ds_read_u16_d16_hi v203, v203\n"
    "v_lshlrev_b32_sdwa v207, 1, v183 dst_sel:DWORD dst_unused:UNUSED_PAD src0_sel:DWORD src1_sel:WORD_1\n"
    "v_mfma_f32_16x16x32_bf16 v[176:179], a[48:51], a[244:247], v[176:179]\n"
    "v_rcp_f16_sdwa v232, v234 dst_sel:WORD_1 dst_unused:UNUSED_PRESERVE src0_sel:WORD_1\n"
    "v_mfma_f32_16x16x32_bf16 v[180:183], a[84:87], a[236:239], 0\n"
    "s_nop 0\n"
    "ds_read_b128 v[164:167], v244 offset:18432\n"
    "v_fma_mix_f32 v234, v192, v232, 0 op_sel_hi:[1,1,0]\n"
    "v_mfma_f32_16x16x32_bf16 v[180:183], a[132:135], a[240:243], v[180:183]\n"
    "s_waitcnt lgkmcnt(7)\n"
    "ds_read_u16 v197, v197\n"
    "v_fma_mix_f32 v232, v192, v232, 0 op_sel:[1,1,0] op_sel_hi:[1,1,0]\n"
    "v_mfma_f32_16x16x32_bf16 v[172:175], a[252:255], a[232:235], v[172:175]\n"
    "v_lshlrev_b32_sdwa v204, 1, v177 dst_sel:DWORD dst_unused:UNUSED_PAD src0_sel:DWORD src1_sel:WORD_1\n"
    "v_cvt_pk_bf16_f32 v192, v234, v232\n"
    "v_mfma_f32_16x16x32_bf16 v[180:183], a[252:255], a[244:247], v[180:183]\n"
    "ds_read_u16_d16_hi v205, v205\n"
    "v_pk_fma_f16 v232, v196, v196, s48\n"
    "v_mfma_f32_16x16x32_bf16 v[112:115], a[200:203], v[184:187], v[112:115]\n"
    "s_waitcnt lgkmcnt(8)\n"
    "v_or_b32_e32 v193, v193, v201\n"
    "v_lshlrev_b32_sdwa v201, 1, v171 dst_sel:DWORD dst_unused:UNUSED_PAD src0_sel:DWORD src1_sel:WORD_1\n"
    "v_mfma_f32_16x16x32_bf16 v[116:119], a[204:207], v[184:187], v[116:119]\n"
    "ds_read_u16 v198, v198\n"
    "v_pk_mul_f16 v232, v196, v232\n"
    "v_mfma_f32_16x16x32_bf16 v[104:107], a[192:195], v[188:191], v[104:107]\n"
    "ds_read_u16_d16_hi v206, v206\n"
    "v_exp_f16_e64 v234, v232\n"
    "v_mfma_f32_16x16x32_bf16 v[108:111], a[196:199], v[188:191], v[108:111]\n"
    "s_waitcnt lgkmcnt(8)\n"
    "v_or_b32_e32 v194, v194, v202\n"
    "v_lshlrev_b32_sdwa v202, 1, v173 dst_sel:DWORD dst_unused:UNUSED_PAD src0_sel:DWORD src1_sel:WORD_1\n"
    "v_mfma_f32_16x16x32_bf16 v[120:123], a[200:203], v[188:191], v[120:123]\n"
    "ds_read_u16 v199, v199\n"
    "v_exp_f16_sdwa v234, v232 dst_sel:WORD_1 dst_unused:UNUSED_PRESERVE src0_sel:WORD_1\n"
    "s_waitcnt lgkmcnt(7)\n"
    "v_mfma_f32_16x16x32_bf16 v[128:131], v[236:239], v[184:187], v[128:131]\n"
    "s_nop 0\n"
    "ds_read_u16_d16_hi v207, v207\n"
    "v_pk_fma_f16 v234, v234, s49, s49\n"
    "s_branch 2f\n"
    ".p2align 6\n"
    "1:\n"
    ".Lreplk_loop:\n"
    "v_lshlrev_b32_sdwa v184, 1, v168 dst_sel:DWORD dst_unused:UNUSED_PAD src0_sel:DWORD src1_sel:WORD_1\n"
    "v_lshlrev_b32_sdwa v200, 1, v169 dst_sel:DWORD dst_unused:UNUSED_PAD src0_sel:DWORD src1_sel:WORD_1\n"
    "v_lshlrev_b32_sdwa v185, 1, v170 dst_sel:DWORD dst_unused:UNUSED_PAD src0_sel:DWORD src1_sel:WORD_1\n"
    "v_lshlrev_b32_sdwa v201, 1, v171 dst_sel:DWORD dst_unused:UNUSED_PAD src0_sel:DWORD src1_sel:WORD_1\n"
    "v_mfma_f32_16x16x32_bf16 v[140:143], v[164:167], v[188:191], v[140:143]\n"
    "s_waitcnt vmcnt(2) lgkmcnt(3)\n"
    "ds_read_u16 v184, v184\n"
    "ds_read_b128 a[252:255], v244 offset:36864\n"
    "v_mfma_f32_16x16x32_bf16 v[168:171], a[88:91], a[224:227], 0\n"
    "s_nop 0\n"
    "ds_read_u16_d16_hi v200, v200\n"
    "ds_read_u16 v185, v185\n"
    "v_mfma_f32_16x16x32_bf16 v[168:171], a[136:139], a[228:231], v[168:171]\n"
    "buffer_load_dwordx4 v[0:3], v246, s[40:43], s60 offen offset:0\n"
    "buffer_load_dwordx4 v[32:35], v246, s[40:43], s60 offen offset:64\n"
    "buffer_load_dwordx4 v[64:67], v246, s[40:43], s60 offen offset:128\n"
    "ds_read_u16_d16_hi v201, v201\n"
    "v_mfma_f32_16x16x32_bf16 v[168:171], a[52:55], a[232:235], v[168:171]\n"
    "v_lshlrev_b32_sdwa v186, 1, v172 dst_sel:DWORD dst_unused:UNUSED_PAD src0_sel:DWORD src1_sel:WORD_1\n"
    "v_lshlrev_b32_sdwa v202, 1, v173 dst_sel:DWORD dst_unused:UNUSED_PAD src0_sel:DWORD src1_sel:WORD_1\n"
    "v_smfmac_f32_32x32x32_bf16 v[128:143], a[248:251], v[144:151], v241\n"
    "ds_read_u16 v186, v186\n"
    "v_lshlrev_b32_sdwa v187, 1, v174 dst_sel:DWORD dst_unused:UNUSED_PAD src0_sel:DWORD src1_sel:WORD_1\n"
    "v_lshlrev_b32_sdwa v203, 1, v175 dst_sel:DWORD dst_unused:UNUSED_PAD src0_sel:DWORD src1_sel:WORD_1\n"
    "v_lshlrev_b32_sdwa v188, 1, v176 dst_sel:DWORD dst_unused:UNUSED_PAD src0_sel:DWORD src1_sel:WORD_1\n"
    "v_lshlrev_b32_sdwa v204, 1, v177 dst_sel:DWORD dst_unused:UNUSED_PAD src0_sel:DWORD src1_sel:WORD_1\n"
    "v_mfma_f32_16x16x32_bf16 v[172:175], a[92:95], a[224:227], 0\n"
    "s_nop 0\n"
    "ds_read_u16_d16_hi v202, v202\n"
    "v_lshlrev_b32_sdwa v189, 1, v178 dst_sel:DWORD dst_unused:UNUSED_PAD src0_sel:DWORD src1_sel:WORD_1\n"
    "v_mfma_f32_16x16x32_bf16 v[172:175], a[140:143], a[228:231], v[172:175]\n"
    "s_waitcnt lgkmcnt(7)\n"
    "ds_read_b128 v[236:239], v244 offset:20480\n"
    "v_lshlrev_b32_sdwa v205, 1, v179 dst_sel:DWORD dst_unused:UNUSED_PAD src0_sel:DWORD src1_sel:WORD_1\n"
    "v_mfma_f32_16x16x32_bf16 v[176:179], a[88:91], a[236:239], 0\n"
    "ds_read_u16 v187, v187\n"
    "v_lshlrev_b32_sdwa v190, 1, v180 dst_sel:DWORD dst_unused:UNUSED_PAD src0_sel:DWORD src1_sel:WORD_1\n"
    "v_mfma_f32_16x16x32_bf16 v[176:179], a[136:139], a[240:243], v[176:179]\n"
    "v_lshlrev_b32_sdwa v191, 1, v182 dst_sel:DWORD dst_unused:UNUSED_PAD src0_sel:DWORD src1_sel:WORD_1\n"
    "v_pk_fma_f16 v234, v234, s49, s49\n"
    "v_mfma_f32_16x16x32_bf16 v[176:179], a[52:55], a[244:247], v[176:179]\n"
    "s_waitcnt lgkmcnt(7)\n"
    "ds_read_u16_d16_hi v203, v203\n"
    "v_or_b32_e32 v198, v198, v206\n"
    "v_mfma_f32_16x16x32_bf16 v[172:175], a[252:255], a[232:235], v[172:175]\n"
    "v_lshlrev_b32_sdwa v206, 1, v181 dst_sel:DWORD dst_unused:UNUSED_PAD src0_sel:DWORD src1_sel:WORD_1\n"
    "v_or_b32_e64 v199, v199, v207\n"
    "v_mfma_f32_16x16x32_bf16 v[96:99], a[208:211], v[192:195], v[96:99]\n"
    "ds_read_u16 v188, v188\n"
    "v_lshlrev_b32_sdwa v207, 1, v183 dst_sel:DWORD dst_unused:UNUSED_PAD src0_sel:DWORD src1_sel:WORD_1\n"
    "v_mfma_f32_16x16x32_bf16 v[180:183], a[92:95], a[236:239], 0\n"
    "s_waitcnt lgkmcnt(8)\n"
    "v_rcp_f16_e32 v232, v234\n"
    "v_mfma_f32_16x16x32_bf16 v[180:183], a[140:143], a[240:243], v[180:183]\n"
    "v_rcp_f16_sdwa v232, v234 dst_sel:WORD_1 dst_unused:UNUSED_PRESERVE src0_sel:WORD_1\n"
    "v_mfma_f32_16x16x32_bf16 v[180:183], a[252:255], a[244:247], v[180:183]\n"
    "ds_write_b128 v253, v[216:219] offset:2048\n"
    "buffer_load_dwordx4 v[20:23], v247, s[40:43], s60 offen offset:0\n"
    "buffer_load_dwordx4 v[52:55], v247, s[40:43], s60 offen offset:64\n"
    "buffer_load_dwordx4 v[84:87], v247, s[40:43], s60 offen offset:128\n"
    "v_mfma_f32_16x16x32_bf16 v[100:103], a[212:215], v[192:195], v[100:103]\n"
    "s_waitcnt lgkmcnt(6)\n"
    "v_fma_mix_f32 v234, v196, v232, 0 op_sel_hi:[1,1,0]\n"
    "v_fma_mix_f32 v232, v196, v232, 0 op_sel:[1,1,0] op_sel_hi:[1,1,0]\n"
    "v_mfma_f32_16x16x32_bf16 v[112:115], a[216:219], v[192:195], v[112:115]\n"
    "ds_read_b128 v[164:167], v244 offset:22528\n"
    "ds_read_u16_d16_hi v204, v204\n"
    "v_mfma_f32_16x16x32_bf16 v[116:119], a[220:223], v[192:195], v[116:119]\n"
    "s_waitcnt lgkmcnt(6)\n"
    "ds_read_u16 v189, v189\n"
    "ds_read_u16_d16_hi v205, v205\n"
    "v_mfma_f32_16x16x32_bf16 v[128:131], v[236:239], v[192:195], v[128:131]\n"
    "v_cvt_pk_bf16_f32 v196, v234, v232\n"
    "v_or_b32_e64 v184, v184, v200\n"
    "v_or_b32_e64 v185, v185, v201\n"
    "v_mfma_f32_16x16x32_bf16 v[104:107], a[208:211], v[196:199], v[104:107]\n"
    "v_lshlrev_b32_sdwa v201, 1, v171 dst_sel:DWORD dst_unused:UNUSED_PAD src0_sel:DWORD src1_sel:WORD_1\n"
    "v_or_b32_e64 v186, v186, v202\n"
    "v_mfma_f32_16x16x32_bf16 v[108:111], a[212:215], v[196:199], v[108:111]\n"
    "v_lshlrev_b32_sdwa v202, 1, v173 dst_sel:DWORD dst_unused:UNUSED_PAD src0_sel:DWORD src1_sel:WORD_1\n"
    "v_cvt_pk_bf16_f32 v208, v96, v97\n"
    "v_mfma_f32_16x16x32_bf16 v[120:123], a[216:219], v[196:199], v[120:123]\n"
    "s_waitcnt lgkmcnt(6)\n"
    "ds_write_b128 v253, v[220:223] offset:5120\n"
    "v_mfma_f32_16x16x32_bf16 v[124:127], a[220:223], v[196:199], v[124:127]\n"
    "v_or_b32_e32 v187, v187, v203\n"
    "v_lshlrev_b32_sdwa v203, 1, v175 dst_sel:DWORD dst_unused:UNUSED_PAD src0_sel:DWORD src1_sel:WORD_1\n"
    "v_mfma_f32_16x16x32_bf16 v[136:139], v[236:239], v[196:199], v[136:139]\n"
    "v_cvt_pk_bf16_f32 v209, v98, v99\n"
    "v_cvt_pk_bf16_f32 v210, v100, v101\n"
    "ds_read_u16 v190, v190\n"
    "ds_read_u16_d16_hi v206, v206\n"
    "s_waitcnt lgkmcnt(6)\n"
    "v_mfma_f32_16x16x32_bf16 v[132:135], v[164:167], v[192:195], v[132:135]\n"
    "s_nop 0\n"
    "ds_read_u16 v191, v191\n"
    "ds_read_u16_d16_hi v207, v207\n"
    "v_mfma_f32_16x16x32_bf16 v[140:143], v[164:167], v[196:199], v[140:143]\n"
    "s_nop 0\n"
    "v_cvt_pk_f16_f32 v192, v168, v169\n"
    "v_lshlrev_b32_sdwa v193, 1, v170 dst_sel:DWORD dst_unused:UNUSED_PAD src0_sel:DWORD src1_sel:WORD_1\n"
    "v_mfma_f32_16x16x32_bf16 v[168:171], a[96:99], a[224:227], 0\n"
    "s_waitcnt lgkmcnt(7)\n"
    "ds_read_u16 v193, v193\n"
    "v_lshlrev_b32_sdwa v194, 1, v172 dst_sel:DWORD dst_unused:UNUSED_PAD src0_sel:DWORD src1_sel:WORD_1\n"
    "v_mfma_f32_16x16x32_bf16 v[168:171], a[144:147], a[228:231], v[168:171]\n"
    "v_lshlrev_b32_sdwa v195, 1, v174 dst_sel:DWORD dst_unused:UNUSED_PAD src0_sel:DWORD src1_sel:WORD_1\n"
    "v_cvt_pk_f16_f32 v196, v176, v177\n"
    "v_mfma_f32_16x16x32_bf16 v[168:171], a[56:59], a[232:235], v[168:171]\n"
    "s_nop 0\n"
    "ds_read_u16_d16_hi v201, v201\n"
    "v_lshlrev_b32_sdwa v197, 1, v178 dst_sel:DWORD dst_unused:UNUSED_PAD src0_sel:DWORD src1_sel:WORD_1\n"
    "v_mfma_f32_16x16x32_bf16 v[172:175], a[100:103], a[224:227], 0\n"
    "s_waitcnt lgkmcnt(6)\n"
    "v_lshlrev_b32_sdwa v198, 1, v180 dst_sel:DWORD dst_unused:UNUSED_PAD src0_sel:DWORD src1_sel:WORD_1\n"
    "v_lshlrev_b32_sdwa v199, 1, v182 dst_sel:DWORD dst_unused:UNUSED_PAD src0_sel:DWORD src1_sel:WORD_1\n"
    "v_mfma_f32_16x16x32_bf16 v[172:175], a[148:151], a[228:231], v[172:175]\n"
    "ds_read_u16 v194, v194\n"
    "buffer_load_dwordx4 v[4:7], v246, s[40:43], s61 offen offset:0\n"
    "buffer_load_dwordx4 v[36:39], v246, s[40:43], s61 offen offset:64\n"
    "buffer_load_dwordx4 v[68:71], v246, s[40:43], s61 offen offset:128\n"
    "v_mfma_f32_16x16x32_bf16 v[172:175], a[60:63], a[232:235], v[172:175]\n"
    "ds_read_u16_d16_hi v202, v202\n"
    "v_or_b32_e64 v188, v188, v204\n"
    "v_or_b32_e64 v189, v189, v205\n"
    "v_lshlrev_b32_sdwa v205, 1, v179 dst_sel:DWORD dst_unused:UNUSED_PAD src0_sel:DWORD src1_sel:WORD_1\n"
    "v_mfma_f32_16x16x32_bf16 v[176:179], a[96:99], a[236:239], 0\n"
    "ds_read_u16 v195, v195\n"
    "v_cvt_pk_bf16_f32 v211, v102, v103\n"
    "v_mfma_f32_16x16x32_bf16 v[176:179], a[144:147], a[240:243], v[176:179]\n"
    "v_cvt_pk_bf16_f32 v212, v112, v113\n"
    "v_cvt_pk_bf16_f32 v213, v114, v115\n"
    "v_mfma_f32_16x16x32_bf16 v[176:179], a[56:59], a[244:247], v[176:179]\n"
    "s_waitcnt lgkmcnt(7)\n"
    "ds_read_b128 v[236:239], v244 offset:0\n"
    "v_or_b32_e32 v190, v190, v206\n"
    "v_mfma_f32_16x16x32_bf16 v[96:99], a[0:3], v[184:187], 0\n"
    "ds_read_u16_d16_hi v203, v203\n"
    "v_lshlrev_b32_sdwa v206, 1, v181 dst_sel:DWORD dst_unused:UNUSED_PAD src0_sel:DWORD src1_sel:WORD_1\n"
    "v_mfma_f32_16x16x32_bf16 v[100:103], a[4:7], v[184:187], 0\n"
    "s_nop 0\n"
    "v_cvt_pk_bf16_f32 v214, v116, v117\n"
    "v_cvt_pk_bf16_f32 v215, v118, v119\n"
    "v_mfma_f32_16x16x32_bf16 v[112:115], a[8:11], v[184:187], 0\n"
    "s_waitcnt lgkmcnt(7)\n"
    "ds_read_b128 v[164:167], v244 offset:2048\n"
    "ds_read_u16 v197, v197\n"
    "v_cvt_pk_bf16_f32 v216, v128, v129\n"
    "v_cvt_pk_bf16_f32 v217, v130, v131\n"
    "v_or_b32_e64 v191, v191, v207\n"
    "v_mfma_f32_16x16x32_bf16 v[116:119], a[12:15], v[184:187], 0\n"
    "v_lshlrev_b32_sdwa v207, 1, v183 dst_sel:DWORD dst_unused:UNUSED_PAD src0_sel:DWORD src1_sel:WORD_1\n"
    "v_cvt_pk_bf16_f32 v218, v132, v133\n"
    "v_mfma_f32_16x16x32_bf16 v[180:183], a[100:103], a[236:239], 0\n"
    "ds_read_u16_d16_hi v205, v205\n"
    "v_cvt_pk_bf16_f32 v219, v134, v135\n"
    "v_mfma_f32_16x16x32_bf16 v[180:183], a[148:151], a[240:243], v[180:183]\n"
    "s_waitcnt lgkmcnt(8)\n"
    "ds_read_u16 v198, v198\n"
    "v_or_b32_e32 v193, v193, v201\n"
    "v_mfma_f32_16x16x32_bf16 v[180:183], a[60:63], a[244:247], v[180:183]\n"
    "v_cvt_pk_bf16_f32 v220, v104, v105\n"
    "v_cvt_pk_bf16_f32 v221, v106, v107\n"
    "v_mfma_f32_16x16x32_bf16 v[104:107], a[0:3], v[188:191], 0\n"
    "ds_read_u16_d16_hi v206, v206\n"
    "v_cvt_pk_bf16_f32 v222, v108, v109\n"
    "v_cvt_pk_bf16_f32 v223, v110, v111\n"
    "v_mfma_f32_16x16x32_bf16 v[108:111], a[4:7], v[188:191], 0\n"
    "s_waitcnt lgkmcnt(8)\n"
    "v_or_b32_e32 v194, v194, v202\n"
    "v_cvt_pk_bf16_f32 v224, v120, v121\n"
    "v_cvt_pk_bf16_f32 v225, v122, v123\n"
    "v_cvt_pk_bf16_f32 v226, v124, v125\n"
    "v_cvt_pk_bf16_f32 v227, v126, v127\n"
    "v_pk_fma_f16 v232, v192, v192, s48\n"
    "v_mfma_f32_16x16x32_bf16 v[120:123], a[8:11], v[188:191], 0\n"
    "s_waitcnt lgkmcnt(6)\n"
    "ds_read_u16 v199, v199\n"
    "ds_read_u16_d16_hi v207, v207\n"
    "v_mfma_f32_16x16x32_bf16 v[128:131], v[236:239], v[184:187], 0\n"
    "s_nop 0\n"
    "buffer_load_dwordx4 v[24:27], v247, s[40:43], s61 offen offset:0\n"
    "buffer_load_dwordx4 v[56:59], v247, s[40:43], s61 offen offset:64\n"
    "buffer_load_dwordx4 v[88:91], v247, s[40:43], s61 offen offset:128\n"
    "v_mfma_f32_16x16x32_bf16 v[124:127], a[12:15], v[188:191], 0\n"
    "s_waitcnt lgkmcnt(6)\n"
    "v_or_b32_e32 v195, v195, v203\n"
    "v_cvt_pk_bf16_f32 v228, v136, v137\n"
    "v_mfma_f32_16x16x32_bf16 v[132:135], v[164:167], v[184:187], 0\n"
    "v_cvt_pk_bf16_f32 v229, v138, v139\n"
    "v_cvt_pk_bf16_f32 v230, v140, v141\n"
    "v_cvt_pk_bf16_f32 v231, v142, v143\n"
    "v_pk_mul_f16 v232, v192, v232\n"
    "v_mfma_f32_16x16x32_bf16 v[136:139], v[236:239], v[188:191], 0\n"
    "v_exp_f16_e64 v234, v232\n"
    "v_mfma_f32_16x16x32_bf16 v[140:143], v[164:167], v[188:191], 0\n"
    "v_exp_f16_sdwa v234, v232 dst_sel:WORD_1 dst_unused:UNUSED_PRESERVE src0_sel:WORD_1\n"
    "v_lshlrev_b32_sdwa v184, 1, v168 dst_sel:DWORD dst_unused:UNUSED_PAD src0_sel:DWORD src1_sel:WORD_1\n"
    "v_pk_fma_f16 v234, v234, s49, s49\n"
    "v_lshlrev_b32_sdwa v200, 1, v169 dst_sel:DWORD dst_unused:UNUSED_PAD src0_sel:DWORD src1_sel:WORD_1\n"
    "s_waitcnt lgkmcnt(4)\n"
    "v_or_b32_e32 v197, v197, v205\n"
    "v_lshlrev_b32_sdwa v185, 1, v170 dst_sel:DWORD dst_unused:UNUSED_PAD src0_sel:DWORD src1_sel:WORD_1\n"
    "v_lshlrev_b32_sdwa v201, 1, v171 dst_sel:DWORD dst_unused:UNUSED_PAD src0_sel:DWORD src1_sel:WORD_1\n"
    "v_mfma_f32_16x16x32_bf16 v[168:171], a[104:107], a[224:227], 0\n"
    "ds_read_u16 v184, v184\n"
    "ds_read_u16_d16_hi v200, v200\n"
    "v_mfma_f32_16x16x32_bf16 v[168:171], a[152:155], a[228:231], v[168:171]\n"
    "ds_read_u16 v185, v185\n"
    "ds_read_u16_d16_hi v201, v201\n"
    "v_lshlrev_b32_sdwa v186, 1, v172 dst_sel:DWORD dst_unused:UNUSED_PAD src0_sel:DWORD src1_sel:WORD_1\n"
    "v_lshlrev_b32_sdwa v202, 1, v173 dst_sel:DWORD dst_unused:UNUSED_PAD src0_sel:DWORD src1_sel:WORD_1\n"
    "s_waitcnt lgkmcnt(6)\n"
    "v_or_b32_e32 v198, v198, v206\n"
    "v_lshlrev_b32_sdwa v187, 1, v174 dst_sel:DWORD dst_unused:UNUSED_PAD src0_sel:DWORD src1_sel:WORD_1\n"
    "v_lshlrev_b32_sdwa v203, 1, v175 dst_sel:DWORD dst_unused:UNUSED_PAD src0_sel:DWORD src1_sel:WORD_1\n"
    "v_rcp_f16_e64 v232, v234\n"
    "v_mfma_f32_16x16x32_bf16 v[168:171], a[64:67], a[232:235], v[168:171]\n"
    "ds_read_u16 v186, v186\n"
    "v_rcp_f16_sdwa v232, v234 dst_sel:WORD_1 dst_unused:UNUSED_PRESERVE src0_sel:WORD_1\n"
    "v_mfma_f32_16x16x32_bf16 v[172:175], a[108:111], a[224:227], 0\n"
    "s_nop 0\n"
    "ds_read_u16_d16_hi v202, v202\n"
    "v_lshlrev_b32_sdwa v188, 1, v176 dst_sel:DWORD dst_unused:UNUSED_PAD src0_sel:DWORD src1_sel:WORD_1\n"
    "v_mfma_f32_16x16x32_bf16 v[172:175], a[156:159], a[228:231], v[172:175]\n"
    "s_waitcnt lgkmcnt(6)\n"
    "v_lshlrev_b32_sdwa v204, 1, v177 dst_sel:DWORD dst_unused:UNUSED_PAD src0_sel:DWORD src1_sel:WORD_1\n"
    "v_lshlrev_b32_sdwa v189, 1, v178 dst_sel:DWORD dst_unused:UNUSED_PAD src0_sel:DWORD src1_sel:WORD_1\n"
    "v_lshlrev_b32_sdwa v205, 1, v179 dst_sel:DWORD dst_unused:UNUSED_PAD src0_sel:DWORD src1_sel:WORD_1\n"
    "v_lshlrev_b32_sdwa v190, 1, v180 dst_sel:DWORD dst_unused:UNUSED_PAD src0_sel:DWORD src1_sel:WORD_1\n"
    "v_lshlrev_b32_sdwa v206, 1, v181 dst_sel:DWORD dst_unused:UNUSED_PAD src0_sel:DWORD src1_sel:WORD_1\n"
    "v_lshlrev_b32_sdwa v191, 1, v182 dst_sel:DWORD dst_unused:UNUSED_PAD src0_sel:DWORD src1_sel:WORD_1\n"
    "v_fma_mix_f32 v234, v192, v232, 0 op_sel_hi:[1,1,0]\n"
    "v_mfma_f32_16x16x32_bf16 v[172:175], a[68:71], a[232:235], v[172:175]\n"
    "ds_read_b128 v[236:239], v244 offset:4096\n"
    "ds_read_u16 v187, v187\n"
    "v_or_b32_e64 v199, v199, v207\n"
    "v_lshlrev_b32_sdwa v207, 1, v183 dst_sel:DWORD dst_unused:UNUSED_PAD src0_sel:DWORD src1_sel:WORD_1\n"
    "v_fma_mix_f32 v232, v192, v232, 0 op_sel:[1,1,0] op_sel_hi:[1,1,0]\n"
    "v_mfma_f32_16x16x32_bf16 v[176:179], a[104:107], a[236:239], 0\n"
    "buffer_load_dwordx4 v[8:11], v246, s[40:43], s62 offen offset:0\n"
    "buffer_load_dwordx4 v[40:43], v246, s[40:43], s62 offen offset:64\n"
    "buffer_load_dwordx4 v[72:75], v246, s[40:43], s62 offen offset:128\n"
    "ds_read_u16_d16_hi v203, v203\n"
    "v_cvt_pk_bf16_f32 v192, v234, v232\n"
    "v_mfma_f32_16x16x32_bf16 v[176:179], a[152:155], a[240:243], v[176:179]\n"
    "s_waitcnt lgkmcnt(7)\n"
    "ds_read_b128 v[164:167], v244 offset:6144\n"
    "ds_read_u16 v188, v188\n"
    "v_mfma_f32_16x16x32_bf16 v[176:179], a[64:67], a[244:247], v[176:179]\n"
    "s_nop 0\n"
    "v_pk_fma_f16 v232, v196, v196, s48\n"
    "v_or_b32_e64 v184, v184, v200\n"
    "v_mfma_f32_16x16x32_bf16 v[180:183], a[108:111], a[236:239], 0\n"
    "ds_read_u16_d16_hi v204, v204\n"
    "v_pk_mul_f16 v232, v196, v232\n"
    "v_mfma_f32_16x16x32_bf16 v[180:183], a[156:159], a[240:243], v[180:183]\n"
    "v_exp_f16_e32 v234, v232\n"
    "v_mfma_f32_16x16x32_bf16 v[180:183], a[68:71], a[244:247], v[180:183]\n"
    "s_waitcnt lgkmcnt(8)\n"
    "ds_read_u16 v189, v189\n"
    "v_exp_f16_sdwa v234, v232 dst_sel:WORD_1 dst_unused:UNUSED_PRESERVE src0_sel:WORD_1\n"
    "v_mfma_f32_16x16x32_bf16 v[96:99], a[16:19], v[192:195], v[96:99]\n"
    "ds_read_u16_d16_hi v205, v205\n"
    "v_pk_fma_f16 v234, v234, s49, s49\n"
    "v_mfma_f32_16x16x32_bf16 v[100:103], a[20:23], v[192:195], v[100:103]\n"
    "v_rcp_f16_e32 v232, v234\n"
    "v_mfma_f32_16x16x32_bf16 v[112:115], a[24:27], v[192:195], v[112:115]\n"
    "s_waitcnt lgkmcnt(7)\n"
    "ds_read_u16 v190, v190\n"
    "v_rcp_f16_sdwa v232, v234 dst_sel:WORD_1 dst_unused:UNUSED_PRESERVE src0_sel:WORD_1\n"
    "v_mfma_f32_16x16x32_bf16 v[116:119], a[28:31], v[192:195], v[116:119]\n"
    "v_fma_mix_f32 v234, v196, v232, 0 op_sel_hi:[1,1,0]\n"
    "v_fma_mix_f32 v232, v196, v232, 0 op_sel:[1,1,0] op_sel_hi:[1,1,0]\n"
    "v_mfma_f32_16x16x32_bf16 v[128:131], v[236:239], v[192:195], v[128:131]\n"
    "ds_read_u16_d16_hi v206, v206\n"
    "v_cvt_pk_bf16_f32 v196, v234, v232\n"
    "v_or_b32_e64 v185, v185, v201\n"
    "v_lshlrev_b32_sdwa v201, 1, v171 dst_sel:DWORD dst_unused:UNUSED_PAD src0_sel:DWORD src1_sel:WORD_1\n"
    "v_mfma_f32_16x16x32_bf16 v[104:107], a[16:19], v[196:199], v[104:107]\n"
    "s_waitcnt lgkmcnt(6)\n"
    "ds_read_u16 v191, v191\n"
    "v_or_b32_e32 v186, v186, v202\n"
    "v_mfma_f32_16x16x32_bf16 v[132:135], v[164:167], v[192:195], v[132:135]\n"
    "v_cvt_pk_f16_f32 v192, v168, v169\n"
    "v_lshlrev_b32_sdwa v193, 1, v170 dst_sel:DWORD dst_unused:UNUSED_PAD src0_sel:DWORD src1_sel:WORD_1\n"
    "v_mfma_f32_16x16x32_bf16 v[108:111], a[20:23], v[196:199], v[108:111]\n"
    "ds_read_u16_d16_hi v207, v207\n"
    "v_lshlrev_b32_sdwa v194, 1, v172 dst_sel:DWORD dst_unused:UNUSED_PAD src0_sel:DWORD src1_sel:WORD_1\n"
    "v_mfma_f32_16x16x32_bf16 v[120:123], a[24:27], v[196:199], v[120:123]\n"
    "ds_read_u16 v193, v193\n"
    "v_lshlrev_b32_sdwa v202, 1, v173 dst_sel:DWORD dst_unused:UNUSED_PAD src0_sel:DWORD src1_sel:WORD_1\n"
    "v_mfma_f32_16x16x32_bf16 v[124:127], a[28:31], v[196:199], v[124:127]\n"
    "v_lshlrev_b32_sdwa v195, 1, v174 dst_sel:DWORD dst_unused:UNUSED_PAD src0_sel:DWORD src1_sel:WORD_1\n"
    "v_or_b32_e32 v187, v187, v203\n"
    "v_mfma_f32_16x16x32_bf16 v[136:139], v[236:239], v[196:199], v[136:139]\n"
    "s_waitcnt lgkmcnt(7)\n"
    "buffer_load_dwordx4 v[12:15], v246, s[40:43], s63 offen offset:0\n"
    "buffer_load_dwordx4 v[44:47], v246, s[40:43], s63 offen offset:64\n"
    "buffer_load_dwordx4 v[76:79], v246, s[40:43], s63 offen offset:128\n"
    "ds_read_b128 a[252:255], v244 offset:34816\n"
    "v_mfma_f32_16x16x32_bf16 v[140:143], v[164:167], v[196:199], v[140:143]\n"
    "ds_read_u16_d16_hi v201, v201\n"
    "v_lshlrev_b32_sdwa v203, 1, v175 dst_sel:DWORD dst_unused:UNUSED_PAD src0_sel:DWORD src1_sel:WORD_1\n"
    "v_mfma_f32_16x16x32_bf16 v[168:171], a[112:115], a[224:227], 0\n"
    "s_nop 0\n"
    "ds_read_u16 v194, v194\n"
    "v_cvt_pk_f16_f32 v196, v176, v177\n"
    "v_mfma_f32_16x16x32_bf16 v[168:171], a[160:163], a[228:231], v[168:171]\n"
    "s_waitcnt lgkmcnt(8)\n"
    "v_lshlrev_b32_sdwa v197, 1, v178 dst_sel:DWORD dst_unused:UNUSED_PAD src0_sel:DWORD src1_sel:WORD_1\n"
    "v_lshlrev_b32_sdwa v198, 1, v180 dst_sel:DWORD dst_unused:UNUSED_PAD src0_sel:DWORD src1_sel:WORD_1\n"
    "v_mfma_f32_16x16x32_bf16 v[168:171], a[72:75], a[232:235], v[168:171]\n"
    "ds_read_u16_d16_hi v202, v202\n"
    "v_lshlrev_b32_sdwa v199, 1, v182 dst_sel:DWORD dst_unused:UNUSED_PAD src0_sel:DWORD src1_sel:WORD_1\n"
    "v_mfma_f32_16x16x32_bf16 v[172:175], a[116:119], a[224:227], 0\n"
    "v_or_b32_e64 v188, v188, v204\n"
    "v_or_b32_e64 v189, v189, v205\n"
    "v_mfma_f32_16x16x32_bf16 v[172:175], a[164:167], a[228:231], v[172:175]\n"
    "ds_read_u16 v195, v195\n"
    "v_lshlrev_b32_sdwa v205, 1, v179 dst_sel:DWORD dst_unused:UNUSED_PAD src0_sel:DWORD src1_sel:WORD_1\n"
    "v_mfma_f32_16x16x32_bf16 v[176:179], a[112:115], a[236:239], 0\n"
    "s_waitcnt lgkmcnt(8)\n"
    "v_or_b32_e32 v190, v190, v206\n"
    "v_lshlrev_b32_sdwa v206, 1, v181 dst_sel:DWORD dst_unused:UNUSED_PAD src0_sel:DWORD src1_sel:WORD_1\n"
    "v_mfma_f32_16x16x32_bf16 v[176:179], a[160:163], a[240:243], v[176:179]\n"
    "ds_read_b128 v[236:239], v244 offset:8192\n"
    "ds_read_u16_d16_hi v203, v203\n"
    "v_mfma_f32_16x16x32_bf16 v[176:179], a[72:75], a[244:247], v[176:179]\n"
    "v_pk_fma_f16 v232, v192, v192, s48\n"
    "v_lshlrev_b32_sdwa v200, 1, v169 dst_sel:DWORD dst_unused:UNUSED_PAD src0_sel:DWORD src1_sel:WORD_1\n"
    "v_mfma_f32_16x16x32_bf16 v[96:99], a[32:35], v[184:187], v[96:99]\n"
    "s_waitcnt lgkmcnt(8)\n"
    "ds_read_b128 v[164:167], v244 offset:10240\n"
    "v_or_b32_e32 v191, v191, v207\n"
    "v_mfma_f32_16x16x32_bf16 v[100:103], a[36:39], v[184:187], v[100:103]\n"
    "s_nop 0\n"
    "ds_read_u16 v197, v197\n"
    "v_lshlrev_b32_sdwa v207, 1, v183 dst_sel:DWORD dst_unused:UNUSED_PAD src0_sel:DWORD src1_sel:WORD_1\n"
    "v_mfma_f32_16x16x32_bf16 v[180:183], a[116:119], a[236:239], 0\n"
    "s_waitcnt lgkmcnt(8)\n"
    "v_pk_mul_f16 v232, v192, v232\n"
    "v_lshlrev_b32_sdwa v204, 1, v177 dst_sel:DWORD dst_unused:UNUSED_PAD src0_sel:DWORD src1_sel:WORD_1\n"
    "v_mfma_f32_16x16x32_bf16 v[172:175], a[252:255], a[232:235], v[172:175]\n"
    "ds_read_u16_d16_hi v205, v205\n"
    "v_exp_f16_e64 v234, v232\n"
    "v_mfma_f32_16x16x32_bf16 v[180:183], a[164:167], a[240:243], v[180:183]\n"
    "s_waitcnt lgkmcnt(8)\n"
    "v_or_b32_e32 v193, v193, v201\n"
    "v_exp_f16_sdwa v234, v232 dst_sel:WORD_1 dst_unused:UNUSED_PRESERVE src0_sel:WORD_1\n"
    "v_mfma_f32_16x16x32_bf16 v[180:183], a[252:255], a[244:247], v[180:183]\n"
    "ds_read_u16 v198, v198\n"
    "v_pk_fma_f16 v234, v234, s49, s49\n"
    "v_mfma_f32_16x16x32_bf16 v[112:115], a[40:43], v[184:187], v[112:115]\n"
    "ds_read_u16_d16_hi v206, v206\n"
    "v_lshlrev_b32_sdwa v201, 1, v171 dst_sel:DWORD dst_unused:UNUSED_PAD src0_sel:DWORD src1_sel:WORD_1\n"
    "v_mfma_f32_16x16x32_bf16 v[116:119], a[44:47], v[184:187], v[116:119]\n"
    "buffer_load_dwordx4 v[16:19], v246, s[40:43], s52 offen offset:0\n"
    "buffer_load_dwordx4 v[48:51], v246, s[40:43], s52 offen offset:64\n"
    "buffer_load_dwordx4 v[80:83], v246, s[40:43], s52 offen offset:128\n"
    "ds_read_u16 v199, v199\n"
    "v_mfma_f32_16x16x32_bf16 v[104:107], a[32:35], v[188:191], v[104:107]\n"
    "s_waitcnt lgkmcnt(9)\n"
    "v_or_b32_e32 v194, v194, v202\n"
    "v_lshlrev_b32_sdwa v202, 1, v173 dst_sel:DWORD dst_unused:UNUSED_PAD src0_sel:DWORD src1_sel:WORD_1\n"
    "v_mfma_f32_16x16x32_bf16 v[108:111], a[36:39], v[188:191], v[108:111]\n"
    "s_waitcnt lgkmcnt(7)\n"
    "ds_read_u16_d16_hi v207, v207\n"
    "v_rcp_f16_e64 v232, v234\n"
    "v_mfma_f32_16x16x32_bf16 v[128:131], v[236:239], v[184:187], v[128:131]\n"
    "v_rcp_f16_sdwa v232, v234 dst_sel:WORD_1 dst_unused:UNUSED_PRESERVE src0_sel:WORD_1\n"
    "v_mfma_f32_16x16x32_bf16 v[120:123], a[40:43], v[188:191], v[120:123]\n"
    "s_waitcnt lgkmcnt(6)\n"
    "ds_read_b128 a[252:255], v244 offset:38912\n"
    "ds_read_u16_d16_hi v200, v200\n"
    "v_mfma_f32_16x16x32_bf16 v[132:135], v[164:167], v[184:187], v[132:135]\n"
    "v_or_b32_e64 v195, v195, v203\n"
    "v_lshlrev_b32_sdwa v184, 1, v168 dst_sel:DWORD dst_unused:UNUSED_PAD src0_sel:DWORD src1_sel:WORD_1\n"
    "v_mfma_f32_16x16x32_bf16 v[124:127], a[44:47], v[188:191], v[124:127]\n"
    "ds_read_u16 v184, v184\n"
    "v_lshlrev_b32_sdwa v185, 1, v170 dst_sel:DWORD dst_unused:UNUSED_PAD src0_sel:DWORD src1_sel:WORD_1\n"
    "v_mfma_f32_16x16x32_bf16 v[136:139], v[236:239], v[188:191], v[136:139]\n"
    "v_lshlrev_b32_sdwa v186, 1, v172 dst_sel:DWORD dst_unused:UNUSED_PAD src0_sel:DWORD src1_sel:WORD_1\n"
    "v_lshlrev_b32_sdwa v187, 1, v174 dst_sel:DWORD dst_unused:UNUSED_PAD src0_sel:DWORD src1_sel:WORD_1\n"
    "v_mfma_f32_16x16x32_bf16 v[140:143], v[164:167], v[188:191], v[140:143]\n"
    "ds_read_u16 v185, v185\n"
    "v_lshlrev_b32_sdwa v203, 1, v175 dst_sel:DWORD dst_unused:UNUSED_PAD src0_sel:DWORD src1_sel:WORD_1\n"
    "v_mfma_f32_16x16x32_bf16 v[168:171], a[120:123], a[224:227], 0\n"
    "s_waitcnt lgkmcnt(8)\n"
    "v_or_b32_e32 v197, v197, v205\n"
    "v_lshlrev_b32_sdwa v188, 1, v176 dst_sel:DWORD dst_unused:UNUSED_PAD src0_sel:DWORD src1_sel:WORD_1\n"
    "v_mfma_f32_16x16x32_bf16 v[168:171], a[168:171], a[228:231], v[168:171]\n"
    "ds_read_u16_d16_hi v201, v201\n"
    "v_lshlrev_b32_sdwa v189, 1, v178 dst_sel:DWORD dst_unused:UNUSED_PAD src0_sel:DWORD src1_sel:WORD_1\n"
    "v_mfma_f32_16x16x32_bf16 v[168:171], a[76:79], a[232:235], v[168:171]\n"
    "v_lshlrev_b32_sdwa v205, 1, v179 dst_sel:DWORD dst_unused:UNUSED_PAD src0_sel:DWORD src1_sel:WORD_1\n"
    "v_lshlrev_b32_sdwa v190, 1, v180 dst_sel:DWORD dst_unused:UNUSED_PAD src0_sel:DWORD src1_sel:WORD_1\n"
    "v_mfma_f32_16x16x32_bf16 v[172:175], a[124:127], a[224:227], 0\n"
    "s_waitcnt lgkmcnt(7)\n"
    "ds_read_u16 v186, v186\n"
    "v_or_b32_e32 v198, v198, v206\n"
    "v_mfma_f32_16x16x32_bf16 v[172:175], a[172:175], a[228:231], v[172:175]\n"
    "ds_read_u16_d16_hi v202, v202\n"
    "v_lshlrev_b32_sdwa v206, 1, v181 dst_sel:DWORD dst_unused:UNUSED_PAD src0_sel:DWORD src1_sel:WORD_1\n"
    "v_lshlrev_b32_sdwa v191, 1, v182 dst_sel:DWORD dst_unused:UNUSED_PAD src0_sel:DWORD src1_sel:WORD_1\n"
    "v_fma_mix_f32 v234, v192, v232, 0 op_sel_hi:[1,1,0]\n"
    "v_fma_mix_f32 v232, v192, v232, 0 op_sel:[1,1,0] op_sel_hi:[1,1,0]\n"
    "v_mfma_f32_16x16x32_bf16 v[176:179], a[120:123], a[236:239], 0\n"
    "ds_read_b128 v[236:239], v244 offset:12288\n"
    "v_cvt_pk_bf16_f32 v192, v234, v232\n"
    "v_mfma_f32_16x16x32_bf16 v[176:179], a[168:171], a[240:243], v[176:179]\n"
    "s_waitcnt lgkmcnt(7)\n"
    "ds_read_u16 v187, v187\n"
    "v_or_b32_e32 v199, v199, v207\n"
    "v_mfma_f32_16x16x32_bf16 v[172:175], a[252:255], a[232:235], v[172:175]\n"
    "buffer_load_dwordx4 v[152:155], v245, s[40:43], s57 offen offset:64\n"
    "buffer_load_dwordx4 v[144:147], v245, s[40:43], s57 offen offset:0\n"
    "v_mfma_f32_16x16x32_bf16 v[176:179], a[76:79], a[244:247], v[176:179]\n"
    "ds_read_u16_d16_hi v203, v203\n"
    "v_lshlrev_b32_sdwa v207, 1, v183 dst_sel:DWORD dst_unused:UNUSED_PAD src0_sel:DWORD src1_sel:WORD_1\n"
    "v_pk_fma_f16 v232, v196, v196, s48\n"
    "v_mfma_f32_16x16x32_bf16 v[180:183], a[124:127], a[236:239], 0\n"
    "v_pk_mul_f16 v232, v196, v232\n"
    "v_mov_b32_e32 v254, 0\n"
    "v_mfma_f32_16x16x32_bf16 v[180:183], a[172:175], a[240:243], v[180:183]\n"
    "s_waitcnt lgkmcnt(7)\n"
    "ds_read_b128 v[164:167], v244 offset:14336\n"
    "ds_read_u16 v188, v188\n"
    "v_exp_f16_e64 v234, v232\n"
    "v_mfma_f32_16x16x32_bf16 v[180:183], a[252:255], a[244:247], v[180:183]\n"
    "v_exp_f16_sdwa v234, v232 dst_sel:WORD_1 dst_unused:UNUSED_PRESERVE src0_sel:WORD_1\n"
    "v_mfma_f32_16x16x32_bf16 v[96:99], a[176:179], v[192:195], v[96:99]\n"
    "ds_read_u16_d16_hi v204, v204\n"
    "v_pk_fma_f16 v234, v234, s49, s49\n"
    "v_mfma_f32_16x16x32_bf16 v[100:103], a[180:183], v[192:195], v[100:103]\n"
    "s_waitcnt lgkmcnt(8)\n"
    "v_rcp_f16_e32 v232, v234\n"
    "v_mfma_f32_16x16x32_bf16 v[112:115], a[184:187], v[192:195], v[112:115]\n"
    "ds_read_u16 v189, v189\n"
    "v_rcp_f16_sdwa v232, v234 dst_sel:WORD_1 dst_unused:UNUSED_PRESERVE src0_sel:WORD_1\n"
    "v_mfma_f32_16x16x32_bf16 v[116:119], a[188:191], v[192:195], v[116:119]\n"
    "ds_read_u16_d16_hi v205, v205\n"
    "v_fma_mix_f32 v234, v196, v232, 0 op_sel_hi:[1,1,0]\n"
    "v_fma_mix_f32 v232, v196, v232, 0 op_sel:[1,1,0] op_sel_hi:[1,1,0]\n"
    "v_or_b32_e32 v184, v184, v200\n"
    "s_waitcnt lgkmcnt(7)\n"
    "v_mfma_f32_16x16x32_bf16 v[128:131], v[236:239], v[192:195], v[128:131]\n"
    "ds_read_u16 v190, v190\n"
    "v_cvt_pk_bf16_f32 v196, v234, v232\n"
    "v_or_b32_e64 v185, v185, v201\n"
    "v_lshlrev_b32_sdwa v201, 1, v171 dst_sel:DWORD dst_unused:UNUSED_PAD src0_sel:DWORD src1_sel:WORD_1\n"
    "v_mfma_f32_16x16x32_bf16 v[104:107], a[176:179], v[196:199], v[104:107]\n"
    "ds_read_b128 a[224:227], v243 offset:24640\n"
    "v_or_b32_e64 v186, v186, v202\n"
    "v_mfma_f32_16x16x32_bf16 v[108:111], a[180:183], v[196:199], v[108:111]\n"
    "ds_read_u16_d16_hi v206, v206\n"
    "v_lshlrev_b32_sdwa v202, 1, v173 dst_sel:DWORD dst_unused:UNUSED_PAD src0_sel:DWORD src1_sel:WORD_1\n"
    "v_mfma_f32_16x16x32_bf16 v[120:123], a[184:187], v[196:199], v[120:123]\n"
    "s_nop 0\n"
    "v_mfma_f32_16x16x32_bf16 v[124:127], a[188:191], v[196:199], v[124:127]\n"
    "s_waitcnt lgkmcnt(7)\n"
    "buffer_load_dwordx4 v[156:159], v245, s[40:43], s88 offen offset:64\n"
    "buffer_load_dwordx4 v[148:151], v245, s[40:43], s88 offen offset:0\n"
    "ds_read_b128 a[228:231], v243 offset:25664\n"
    "v_mfma_f32_16x16x32_bf16 v[132:135], v[164:167], v[192:195], v[132:135]\n"
    "ds_read_b128 a[232:235], v243 offset:26688\n"
    "v_cvt_pk_f16_f32 v192, v168, v169\n"
    "v_mfma_f32_16x16x32_bf16 v[136:139], v[236:239], v[196:199], v[136:139]\n"
    "ds_read_u16 v191, v191\n"
    "v_lshlrev_b32_sdwa v193, 1, v170 dst_sel:DWORD dst_unused:UNUSED_PAD src0_sel:DWORD src1_sel:WORD_1\n"
    "v_mfma_f32_16x16x32_bf16 v[140:143], v[164:167], v[196:199], v[140:143]\n"
    "v_lshlrev_b32_sdwa v194, 1, v172 dst_sel:DWORD dst_unused:UNUSED_PAD src0_sel:DWORD src1_sel:WORD_1\n"
    "v_lshlrev_b32_sdwa v195, 1, v174 dst_sel:DWORD dst_unused:UNUSED_PAD src0_sel:DWORD src1_sel:WORD_1\n"
    "v_cvt_pk_f16_f32 v196, v176, v177\n"
    "v_lshlrev_b32_sdwa v197, 1, v178 dst_sel:DWORD dst_unused:UNUSED_PAD src0_sel:DWORD src1_sel:WORD_1\n"
    "v_lshlrev_b32_sdwa v198, 1, v180 dst_sel:DWORD dst_unused:UNUSED_PAD src0_sel:DWORD src1_sel:WORD_1\n"
    "v_lshlrev_b32_sdwa v199, 1, v182 dst_sel:DWORD dst_unused:UNUSED_PAD src0_sel:DWORD src1_sel:WORD_1\n"
    "v_or_b32_e64 v187, v187, v203\n"
    "v_lshlrev_b32_sdwa v203, 1, v175 dst_sel:DWORD dst_unused:UNUSED_PAD src0_sel:DWORD src1_sel:WORD_1\n"
    "s_waitcnt lgkmcnt(8)\n"
    "v_or_b32_e32 v188, v188, v204\n"
    "ds_read_b128 a[236:239], v243 offset:27712\n"
    "ds_read_u16_d16_hi v207, v207\n"
    "ds_read_b128 a[240:243], v243 offset:28736\n"
    "v_pk_fma_f16 v232, v192, v192, s48\n"
    "v_pk_mul_f16 v232, v192, v232\n"
    "s_waitcnt lgkmcnt(9)\n"
    "v_or_b32_e32 v189, v189, v205\n"
    "ds_read_b128 a[244:247], v243 offset:29760\n"
    "ds_read_u16 v193, v193\n"
    "s_waitcnt lgkmcnt(9)\n"
    "v_mfma_f32_16x16x32_bf16 v[168:171], a[80:83], a[224:227], 0\n"
    "s_nop 0\n"
    "v_lshlrev_b32_sdwa v205, 1, v179 dst_sel:DWORD dst_unused:UNUSED_PAD src0_sel:DWORD src1_sel:WORD_1\n"
    "v_exp_f16_e64 v234, v232\n"
    "v_mfma_f32_16x16x32_bf16 v[172:175], a[84:87], a[224:227], 0\n"
    "ds_read_b128 a[252:255], v244 offset:32768\n"
    "ds_read_u16_d16_hi v201, v201\n"
    "v_exp_f16_sdwa v234, v232 dst_sel:WORD_1 dst_unused:UNUSED_PRESERVE src0_sel:WORD_1\n"
    "s_waitcnt lgkmcnt(9)\n"
    "v_mfma_f32_16x16x32_bf16 v[168:171], a[128:131], a[228:231], v[168:171]\n"
    "s_waitcnt lgkmcnt(8)\n"
    "ds_read_u16 v194, v194\n"
    "v_or_b32_e64 v190, v190, v206\n"
    "v_mfma_f32_16x16x32_bf16 v[168:171], a[48:51], a[232:235], v[168:171]\n"
    "v_lshlrev_b32_sdwa v206, 1, v181 dst_sel:DWORD dst_unused:UNUSED_PAD src0_sel:DWORD src1_sel:WORD_1\n"
    "v_pk_fma_f16 v234, v234, s49, s49\n"
    "v_mfma_f32_16x16x32_bf16 v[172:175], a[132:135], a[228:231], v[172:175]\n"
    "ds_read_u16_d16_hi v202, v202\n"
    "v_rcp_f16_e32 v232, v234\n"
    "v_mfma_f32_16x16x32_bf16 v[96:99], a[192:195], v[184:187], v[96:99]\n"
    "s_waitcnt lgkmcnt(8)\n"
    "v_rcp_f16_sdwa v232, v234 dst_sel:WORD_1 dst_unused:UNUSED_PRESERVE src0_sel:WORD_1\n"
    "v_mfma_f32_16x16x32_bf16 v[176:179], a[80:83], a[236:239], 0\n"
    "ds_read_u16 v195, v195\n"
    "v_lshlrev_b32_sdwa v200, 1, v169 dst_sel:DWORD dst_unused:UNUSED_PAD src0_sel:DWORD src1_sel:WORD_1\n"
    "v_mfma_f32_16x16x32_bf16 v[100:103], a[196:199], v[184:187], v[100:103]\n"
    "ds_read_b128 v[236:239], v244 offset:16384\n"
    "v_fma_mix_f32 v234, v192, v232, 0 op_sel_hi:[1,1,0]\n"
    "v_mfma_f32_16x16x32_bf16 v[112:115], a[200:203], v[184:187], v[112:115]\n"
    "s_waitcnt lgkmcnt(8)\n"
    "ds_read_u16_d16_hi v203, v203\n"
    "v_or_b32_e64 v191, v191, v207\n"
    "v_mfma_f32_16x16x32_bf16 v[176:179], a[128:131], a[240:243], v[176:179]\n"
    "s_waitcnt lgkmcnt(8)\n"
    "v_lshlrev_b32_sdwa v207, 1, v183 dst_sel:DWORD dst_unused:UNUSED_PAD src0_sel:DWORD src1_sel:WORD_1\n"
    "v_fma_mix_f32 v232, v192, v232, 0 op_sel:[1,1,0] op_sel_hi:[1,1,0]\n"
    "v_mfma_f32_16x16x32_bf16 v[176:179], a[48:51], a[244:247], v[176:179]\n"
    "ds_read_b128 v[164:167], v244 offset:18432\n"
    "v_cvt_pk_bf16_f32 v192, v234, v232\n"
    "v_mfma_f32_16x16x32_bf16 v[180:183], a[84:87], a[236:239], 0\n"
    "s_waitcnt lgkmcnt(7)\n"
    "ds_read_u16 v197, v197\n"
    "buffer_store_dwordx4 v[208:211], v245, s[68:71], s59 offen offset:0 sc1\n"
    "v_mfma_f32_16x16x32_bf16 v[172:175], a[252:255], a[232:235], v[172:175]\n"
    "s_nop 0\n"
    "ds_read_u16_d16_hi v205, v205\n"
    "v_lshlrev_b32_sdwa v204, 1, v177 dst_sel:DWORD dst_unused:UNUSED_PAD src0_sel:DWORD src1_sel:WORD_1\n"
    "v_mfma_f32_16x16x32_bf16 v[180:183], a[132:135], a[240:243], v[180:183]\n"
    "s_waitcnt lgkmcnt(8)\n"
    "v_or_b32_e32 v193, v193, v201\n"
    "v_lshlrev_b32_sdwa v201, 1, v171 dst_sel:DWORD dst_unused:UNUSED_PAD src0_sel:DWORD src1_sel:WORD_1\n"
    "v_mfma_f32_16x16x32_bf16 v[180:183], a[252:255], a[244:247], v[180:183]\n"
    "ds_read_u16 v198, v198\n"
    "v_pk_fma_f16 v232, v196, v196, s48\n"
    "v_mfma_f32_16x16x32_bf16 v[116:119], a[204:207], v[184:187], v[116:119]\n"
    "ds_read_u16_d16_hi v206, v206\n"
    "v_pk_mul_f16 v232, v196, v232\n"
    "v_mfma_f32_16x16x32_bf16 v[104:107], a[192:195], v[188:191], v[104:107]\n"
    "v_exp_f16_e32 v234, v232\n"
    "v_mfma_f32_16x16x32_bf16 v[108:111], a[196:199], v[188:191], v[108:111]\n"
    "s_waitcnt lgkmcnt(8)\n"
    "ds_read_u16 v199, v199\n"
    "v_or_b32_e32 v194, v194, v202\n"
    "v_mfma_f32_16x16x32_bf16 v[120:123], a[200:203], v[188:191], v[120:123]\n"
    "s_waitcnt lgkmcnt(7)\n"
    "v_lshlrev_b32_sdwa v202, 1, v173 dst_sel:DWORD dst_unused:UNUSED_PAD src0_sel:DWORD src1_sel:WORD_1\n"
    "v_exp_f16_sdwa v234, v232 dst_sel:WORD_1 dst_unused:UNUSED_PRESERVE src0_sel:WORD_1\n"
    "v_mfma_f32_16x16x32_bf16 v[128:131], v[236:239], v[184:187], v[128:131]\n"
    "ds_read_u16_d16_hi v207, v207\n"
    "v_pk_fma_f16 v234, v234, s49, s49\n"
    "2:\n"
    "v_mfma_f32_16x16x32_bf16 v[124:127], a[204:207], v[188:191], v[124:127]\n"
    "ds_read_b128 a[252:255], v244 offset:36864\n"
    "v_rcp_f16_e32 v232, v234\n"
    "v_mfma_f32_16x16x32_bf16 v[136:139], v[236:239], v[188:191], v[136:139]\n"
    "s_waitcnt lgkmcnt(7)\n"
    "ds_read_u16_d16_hi v200, v200\n"
    "v_or_b32_e64 v195, v195, v203\n"
    "v_mfma_f32_16x16x32_bf16 v[132:135], v[164:167], v[184:187], v[132:135]\n"
    "v_lshlrev_b32_sdwa v184, 1, v168 dst_sel:DWORD dst_unused:UNUSED_PAD src0_sel:DWORD src1_sel:WORD_1\n"
    "v_lshlrev_b32_sdwa v185, 1, v170 dst_sel:DWORD dst_unused:UNUSED_PAD src0_sel:DWORD src1_sel:WORD_1\n"
    "v_mfma_f32_16x16x32_bf16 v[140:143], v[164:167], v[188:191], v[140:143]\n"
    "ds_read_u16 v184, v184\n"
    "v_lshlrev_b32_sdwa v186, 1, v172 dst_sel:DWORD dst_unused:UNUSED_PAD src0_sel:DWORD src1_sel:WORD_1\n"
    "v_mfma_f32_16x16x32_bf16 v[168:171], a[88:91], a[224:227], 0\n"
    "v_lshlrev_b32_sdwa v187, 1, v174 dst_sel:DWORD dst_unused:UNUSED_PAD src0_sel:DWORD src1_sel:WORD_1\n"
    "v_lshlrev_b32_sdwa v203, 1, v175 dst_sel:DWORD dst_unused:UNUSED_PAD src0_sel:DWORD src1_sel:WORD_1\n"
    "v_mfma_f32_16x16x32_bf16 v[168:171], a[136:139], a[228:231], v[168:171]\n"
    "s_waitcnt lgkmcnt(7)\n"
    "ds_read_u16 v185, v185\n"
    "v_or_b32_e32 v197, v197, v205\n"
    "v_mfma_f32_16x16x32_bf16 v[168:171], a[52:55], a[232:235], v[168:171]\n"
    "ds_read_u16_d16_hi v201, v201\n"
    "v_lshlrev_b32_sdwa v188, 1, v176 dst_sel:DWORD dst_unused:UNUSED_PAD src0_sel:DWORD src1_sel:WORD_1\n"
    "v_mfma_f32_16x16x32_bf16 v[172:175], a[92:95], a[224:227], 0\n"
    "v_lshlrev_b32_sdwa v189, 1, v178 dst_sel:DWORD dst_unused:UNUSED_PAD src0_sel:DWORD src1_sel:WORD_1\n"
    "v_lshlrev_b32_sdwa v205, 1, v179 dst_sel:DWORD dst_unused:UNUSED_PAD src0_sel:DWORD src1_sel:WORD_1\n"
    "v_mfma_f32_16x16x32_bf16 v[172:175], a[140:143], a[228:231], v[172:175]\n"
    "ds_read_u16 v186, v186\n"
    "v_lshlrev_b32_sdwa v190, 1, v180 dst_sel:DWORD dst_unused:UNUSED_PAD src0_sel:DWORD src1_sel:WORD_1\n"
    "v_mfma_f32_16x16x32_bf16 v[176:179], a[88:91], a[236:239], 0\n"
    "s_waitcnt lgkmcnt(8)\n"
    "v_or_b32_e32 v198, v198, v206\n"
    "v_lshlrev_b32_sdwa v206, 1, v181 dst_sel:DWORD dst_unused:UNUSED_PAD src0_sel:DWORD src1_sel:WORD_1\n"
    "v_mfma_f32_16x16x32_bf16 v[176:179], a[136:139], a[240:243], v[176:179]\n"
    "s_nop 0\n"
    "ds_read_u16_d16_hi v202, v202\n"
    "buffer_store_dwordx4 v[212:215], v245, s[68:71], s59 offen offset:64 sc1\n"
    "v_mfma_f32_16x16x32_bf16 v[176:179], a[52:55], a[244:247], v[176:179]\n"
    "s_waitcnt lgkmcnt(6)\n"
    "ds_read_b128 v[236:239], v244 offset:20480\n"
    "ds_read_u16 v187, v187\n"
    "v_mfma_f32_16x16x32_bf16 v[172:175], a[252:255], a[232:235], v[172:175]\n"
    "v_lshlrev_b32_sdwa v191, 1, v182 dst_sel:DWORD dst_unused:UNUSED_PAD src0_sel:DWORD src1_sel:WORD_1\n"
    "v_or_b32_e64 v199, v199, v207\n"
    "v_mfma_f32_16x16x32_bf16 v[96:99], a[208:211], v[192:195], v[96:99]\n"
    "ds_read_u16_d16_hi v203, v203\n"
    "v_lshlrev_b32_sdwa v207, 1, v183 dst_sel:DWORD dst_unused:UNUSED_PAD src0_sel:DWORD src1_sel:WORD_1\n"
    "v_mfma_f32_16x16x32_bf16 v[180:183], a[92:95], a[236:239], 0\n"
    "v_rcp_f16_sdwa v232, v234 dst_sel:WORD_1 dst_unused:UNUSED_PRESERVE src0_sel:WORD_1\n"
    "v_mfma_f32_16x16x32_bf16 v[180:183], a[140:143], a[240:243], v[180:183]\n"
    "s_nop 0\n"
    "ds_read_b128 v[164:167], v244 offset:22528\n"
    "v_fma_mix_f32 v234, v196, v232, 0 op_sel_hi:[1,1,0]\n"
    "v_mfma_f32_16x16x32_bf16 v[180:183], a[252:255], a[244:247], v[180:183]\n"
    "s_waitcnt lgkmcnt(8)\n"
    "ds_read_u16 v188, v188\n"
    "v_fma_mix_f32 v232, v196, v232, 0 op_sel:[1,1,0] op_sel_hi:[1,1,0]\n"
    "v_mfma_f32_16x16x32_bf16 v[100:103], a[212:215], v[192:195], v[100:103]\n"
    "v_cvt_pk_bf16_f32 v196, v234, v232\n"
    "v_or_b32_e64 v184, v184, v200\n"
    "v_mfma_f32_16x16x32_bf16 v[112:115], a[216:219], v[192:195], v[112:115]\n"
    "ds_read_u16_d16_hi v204, v204\n"
    "v_cvt_pk_bf16_f32 v96, v96, v97\n"
    "v_mfma_f32_16x16x32_bf16 v[116:119], a[220:223], v[192:195], v[116:119]\n"
    "s_waitcnt lgkmcnt(8)\n"
    "v_or_b32_e32 v185, v185, v201\n"
    "v_lshlrev_b32_sdwa v201, 1, v171 dst_sel:DWORD dst_unused:UNUSED_PAD src0_sel:DWORD src1_sel:WORD_1\n"
    "v_mfma_f32_16x16x32_bf16 v[104:107], a[208:211], v[196:199], v[104:107]\n"
    "ds_read_u16 v189, v189\n"
    "v_cvt_pk_bf16_f32 v97, v98, v99\n"
    "v_mfma_f32_16x16x32_bf16 v[108:111], a[212:215], v[196:199], v[108:111]\n"
    "ds_read_u16_d16_hi v205, v205\n"
    "v_cvt_pk_bf16_f32 v98, v100, v101\n"
    "v_mfma_f32_16x16x32_bf16 v[120:123], a[216:219], v[196:199], v[120:123]\n"
    "v_cvt_pk_bf16_f32 v99, v102, v103\n"
    "v_cvt_pk_bf16_f32 v112, v112, v113\n"
    "v_mfma_f32_16x16x32_bf16 v[124:127], a[220:223], v[196:199], v[124:127]\n"
    "s_waitcnt lgkmcnt(7)\n"
    "ds_read_u16 v190, v190\n"
    "v_or_b32_e32 v186, v186, v202\n"
    "v_mfma_f32_16x16x32_bf16 v[128:131], v[236:239], v[192:195], v[128:131]\n"
    "v_lshlrev_b32_sdwa v202, 1, v173 dst_sel:DWORD dst_unused:UNUSED_PAD src0_sel:DWORD src1_sel:WORD_1\n"
    "v_cvt_pk_bf16_f32 v113, v114, v115\n"
    "v_mfma_f32_16x16x32_bf16 v[136:139], v[236:239], v[196:199], v[136:139]\n"
    "v_cvt_pk_bf16_f32 v114, v116, v117\n"
    "v_cvt_pk_bf16_f32 v115, v118, v119\n"
    "v_cvt_pk_bf16_f32 v104, v104, v105\n"
    "v_cvt_pk_bf16_f32 v105, v106, v107\n"
    "v_cvt_pk_bf16_f32 v106, v108, v109\n"
    "v_cvt_pk_bf16_f32 v128, v128, v129\n"
    "v_cvt_pk_bf16_f32 v129, v130, v131\n"
    "s_waitcnt lgkmcnt(5)\n"
    "v_mfma_f32_16x16x32_bf16 v[132:135], v[164:167], v[192:195], v[132:135]\n"
    "ds_write_b128 v243, v[96:99] offset:24640\n"
    "buffer_store_dwordx4 v[216:219], v245, s[68:71], s59 offen offset:128 sc1\n"
    "v_mfma_f32_16x16x32_bf16 v[140:143], v[164:167], v[196:199], v[140:143]\n"
    "v_cvt_pk_f16_f32 v192, v168, v169\n"
    "v_lshlrev_b32_sdwa v193, 1, v170 dst_sel:DWORD dst_unused:UNUSED_PAD src0_sel:DWORD src1_sel:WORD_1\n"
    "v_mfma_f32_16x16x32_bf16 v[168:171], a[96:99], a[224:227], 0\n"
    "s_waitcnt lgkmcnt(4)\n"
    "ds_read_u16_d16_hi v206, v206\n"
    "ds_read_u16 v191, v191\n"
    "v_mfma_f32_16x16x32_bf16 v[168:171], a[144:147], a[228:231], v[168:171]\n"
    "ds_read_u16_d16_hi v207, v207\n"
    "ds_read_u16 v193, v193\n"
    "v_mfma_f32_16x16x32_bf16 v[168:171], a[56:59], a[232:235], v[168:171]\n"
    "v_lshlrev_b32_sdwa v194, 1, v172 dst_sel:DWORD dst_unused:UNUSED_PAD src0_sel:DWORD src1_sel:WORD_1\n"
    "v_lshlrev_b32_sdwa v195, 1, v174 dst_sel:DWORD dst_unused:UNUSED_PAD src0_sel:DWORD src1_sel:WORD_1\n"
    "v_cvt_pk_f16_f32 v196, v176, v177\n"
    "v_lshlrev_b32_sdwa v197, 1, v178 dst_sel:DWORD dst_unused:UNUSED_PAD src0_sel:DWORD src1_sel:WORD_1\n"
    "v_lshlrev_b32_sdwa v198, 1, v180 dst_sel:DWORD dst_unused:UNUSED_PAD src0_sel:DWORD src1_sel:WORD_1\n"
    "v_lshlrev_b32_sdwa v199, 1, v182 dst_sel:DWORD dst_unused:UNUSED_PAD src0_sel:DWORD src1_sel:WORD_1\n"
    "v_or_b32_e64 v187, v187, v203\n"
    "v_lshlrev_b32_sdwa v203, 1, v175 dst_sel:DWORD dst_unused:UNUSED_PAD src0_sel:DWORD src1_sel:WORD_1\n"
    "v_mfma_f32_16x16x32_bf16 v[172:175], a[100:103], a[224:227], 0\n"
    "s_waitcnt vmcnt(24) lgkmcnt(6)\n"
    "ds_write2_b32 v248, v0, v1 offset0:0 offset1:20\n"
    "v_mfma_f32_16x16x32_bf16 v[172:175], a[148:151], a[228:231], v[172:175]\n"
    "v_or_b32_e32 v188, v188, v204\n"
    "v_or_b32_e64 v189, v189, v205\n"
    "v_mfma_f32_16x16x32_bf16 v[172:175], a[60:63], a[232:235], v[172:175]\n"
    "ds_write2_b32 v248, v2, v3 offset0:40 offset1:60\n"
    "v_add_u32_e32 v248, 1360, v248\n"
    "v_lshlrev_b32_sdwa v205, 1, v179 dst_sel:DWORD dst_unused:UNUSED_PAD src0_sel:DWORD src1_sel:WORD_1\n"
    "v_mfma_f32_16x16x32_bf16 v[176:179], a[96:99], a[236:239], 0\n"
    "s_waitcnt vmcnt(21)\n"
    "ds_read_u16_d16_hi v201, v201\n"
    "ds_write2_b32 v235, v20, v21 offset0:0 offset1:20\n"
    "v_mfma_f32_16x16x32_bf16 v[176:179], a[144:147], a[240:243], v[176:179]\n"
    "s_waitcnt lgkmcnt(7)\n"
    "v_or_b32_e64 v190, v190, v206\n"
    "v_lshlrev_b32_sdwa v206, 1, v181 dst_sel:DWORD dst_unused:UNUSED_PAD src0_sel:DWORD src1_sel:WORD_1\n"
    "v_mfma_f32_16x16x32_bf16 v[176:179], a[56:59], a[244:247], v[176:179]\n"
    "ds_read_u16 v194, v194\n"
    "ds_write2_b32 v235, v22, v23 offset0:40 offset1:60\n"
    "v_mfma_f32_16x16x32_bf16 v[96:99], a[0:3], v[184:187], 0\n"
    "v_add_u32_e32 v235, 1360, v235\n"
    "v_cvt_pk_bf16_f32 v130, v132, v133\n"
    "v_mfma_f32_16x16x32_bf16 v[100:103], a[4:7], v[184:187], 0\n"
    "s_waitcnt vmcnt(18) lgkmcnt(7)\n"
    "ds_write2_b32 v248, v4, v5 offset0:0 offset1:20\n"
    "v_or_b32_e32 v191, v191, v207\n"
    "v_mfma_f32_16x16x32_bf16 v[116:119], a[12:15], v[184:187], 0\n"
    "v_lshlrev_b32_sdwa v207, 1, v183 dst_sel:DWORD dst_unused:UNUSED_PAD src0_sel:DWORD src1_sel:WORD_1\n"
    "v_cvt_pk_bf16_f32 v131, v134, v135\n"
    "v_mfma_f32_16x16x32_bf16 v[180:183], a[100:103], a[236:239], 0\n"
    "ds_read_u16_d16_hi v202, v202\n"
    "ds_write2_b32 v248, v6, v7 offset0:40 offset1:60\n"
    "v_mfma_f32_16x16x32_bf16 v[180:183], a[148:151], a[240:243], v[180:183]\n"
    "buffer_store_dwordx4 v[220:223], v245, s[68:71], s39 offen offset:0 sc1\n"
    "s_waitcnt vmcnt(18)\n"
    "ds_write2_b32 v235, v24, v25 offset0:0 offset1:20\n"
    "v_mfma_f32_16x16x32_bf16 v[180:183], a[60:63], a[244:247], v[180:183]\n"
    "s_waitcnt lgkmcnt(6)\n"
    "v_add_u32_e32 v248, 1360, v248\n"
    "v_or_b32_e32 v193, v193, v201\n"
    "ds_write_b128 v243, v[112:115] offset:26688\n"
    "v_mfma_f32_16x16x32_bf16 v[112:115], a[8:11], v[184:187], 0\n"
    "s_waitcnt vmcnt(16)\n"
    "v_cvt_pk_bf16_f32 v107, v110, v111\n"
    "v_cvt_pk_bf16_f32 v120, v120, v121\n"
    "v_mfma_f32_16x16x32_bf16 v[108:111], a[4:7], v[188:191], 0\n"
    "ds_read_u16 v195, v195\n"
    "ds_write2_b32 v235, v26, v27 offset0:40 offset1:60\n"
    "v_add_u32_e32 v235, -1360, v235\n"
    "v_cvt_pk_bf16_f32 v121, v122, v123\n"
    "v_cvt_pk_bf16_f32 v122, v124, v125\n"
    "v_cvt_pk_bf16_f32 v123, v126, v127\n"
    "v_cvt_pk_bf16_f32 v136, v136, v137\n"
    "ds_write_b128 v243, v[128:131] offset:28736\n"
    "s_waitcnt lgkmcnt(4)\n"
    "v_or_b32_e32 v194, v194, v202\n"
    "v_pk_fma_f16 v232, v192, v192, s48\n"
    "v_mfma_f32_16x16x32_bf16 v[124:127], a[12:15], v[188:191], 0\n"
    "v_cvt_pk_bf16_f32 v137, v138, v139\n"
    "ds_write_b128 v243, v[104:107] offset:25664\n"
    "v_mfma_f32_16x16x32_bf16 v[104:107], a[0:3], v[188:191], 0\n"
    "v_cvt_pk_bf16_f32 v138, v140, v141\n"
    "v_cvt_pk_bf16_f32 v139, v142, v143\n"
    "v_pk_mul_f16 v232, v192, v232\n"
    "v_lshlrev_b32_sdwa v200, 1, v169 dst_sel:DWORD dst_unused:UNUSED_PAD src0_sel:DWORD src1_sel:WORD_1\n"
    "s_waitcnt vmcnt(15)\n"
    "ds_write2_b32 v248, v8, v9 offset0:0 offset1:20\n"
    "v_exp_f16_e32 v234, v232\n"
    "ds_write_b128 v243, v[120:123] offset:27712\n"
    "v_mfma_f32_16x16x32_bf16 v[120:123], a[8:11], v[188:191], 0\n"
    "v_exp_f16_sdwa v234, v232 dst_sel:WORD_1 dst_unused:UNUSED_PRESERVE src0_sel:WORD_1\n"
    "v_lshlrev_b32_sdwa v201, 1, v171 dst_sel:DWORD dst_unused:UNUSED_PAD src0_sel:DWORD src1_sel:WORD_1\n"
    "buffer_store_dwordx4 v[224:227], v245, s[68:71], s39 offen offset:64 sc1\n"
    "ds_write2_b32 v248, v10, v11 offset0:40 offset1:60\n"
    "v_add_u32_e32 v248, 1360, v248\n"
    "v_pk_fma_f16 v234, v234, s49, s49\n"
    "v_lshlrev_b32_sdwa v202, 1, v173 dst_sel:DWORD dst_unused:UNUSED_PAD src0_sel:DWORD src1_sel:WORD_1\n"
    "v_rcp_f16_e64 v232, v234\n"
    "ds_write_b128 v243, v[136:139] offset:29760\n"
    "v_rcp_f16_sdwa v232, v234 dst_sel:WORD_1 dst_unused:UNUSED_PRESERVE src0_sel:WORD_1\n"
    "v_lshlrev_b32_sdwa v204, 1, v177 dst_sel:DWORD dst_unused:UNUSED_PAD src0_sel:DWORD src1_sel:WORD_1\n"
    "v_fma_mix_f32 v234, v192, v232, 0 op_sel_hi:[1,1,0]\n"
    "v_fma_mix_f32 v232, v192, v232, 0 op_sel:[1,1,0] op_sel_hi:[1,1,0]\n"
    "v_cvt_pk_bf16_f32 v192, v234, v232\n"
    "ds_read_b128 v[236:239], v244 offset:0\n"
    "s_waitcnt vmcnt(14)\n"
    "s_waitcnt vmcnt(13)\n"
    "ds_write2_b32 v248, v12, v13 offset0:0 offset1:20\n"
    "v_pk_fma_f16 v232, v196, v196, s48\n"
    "v_pk_mul_f16 v232, v196, v232\n"
    "v_exp_f16_e64 v234, v232\n"
    "ds_read_u16_d16_hi v203, v203\n"
    "ds_write2_b32 v248, v14, v15 offset0:40 offset1:60\n"
    "v_add_u32_e32 v248, 1360, v248\n"
    "v_exp_f16_sdwa v234, v232 dst_sel:WORD_1 dst_unused:UNUSED_PRESERVE src0_sel:WORD_1\n"
    "ds_read_b128 v[164:167], v244 offset:2048\n"
    "ds_read_u16 v197, v197\n"
    "s_waitcnt vmcnt(11)\n"
    "s_waitcnt vmcnt(10)\n"
    "ds_write2_b32 v248, v16, v17 offset0:0 offset1:20\n"
    "v_pk_fma_f16 v234, v234, s49, s49\n"
    "v_rcp_f16_e32 v232, v234\n"
    "s_waitcnt lgkmcnt(6)\n"
    "v_mfma_f32_16x16x32_bf16 v[128:131], v[236:239], v[184:187], 0\n"
    "ds_read_u16_d16_hi v205, v205\n"
    "ds_write2_b32 v248, v18, v19 offset0:40 offset1:60\n"
    "v_mfma_f32_32x32x1_2b_f32 v[0:31], v254, v254, 0\n"
    "v_add_u32_e32 v248, -5440, v248\n"
    "v_rcp_f16_sdwa v232, v234 dst_sel:WORD_1 dst_unused:UNUSED_PRESERVE src0_sel:WORD_1\n"
    "ds_read_u16 v198, v198\n"
    "ds_read_u16_d16_hi v206, v206\n"
    "ds_read_u16 v199, v199\n"
    "v_fma_mix_f32 v234, v196, v232, 0 op_sel_hi:[1,1,0]\n"
    "v_fma_mix_f32 v232, v196, v232, 0 op_sel:[1,1,0] op_sel_hi:[1,1,0]\n"
    "v_cvt_pk_bf16_f32 v196, v234, v232\n"
    "buffer_store_dwordx4 v[228:231], v245, s[68:71], s39 offen offset:128 sc1\n"
    "ds_read_u16_d16_hi v207, v207\n"
    "v_mfma_f32_16x16x32_bf16 v[136:139], v[236:239], v[188:191], 0\n"
    "s_waitcnt lgkmcnt(8)\n"
    "ds_read_b128 v[216:219], v251 offset:0\n"
    "v_or_b32_e32 v195, v195, v203\n"
    "v_mfma_f32_16x16x32_bf16 v[132:135], v[164:167], v[184:187], 0\n"
    "ds_read_b128 v[220:223], v251 offset:336\n"
    "v_lshlrev_b32_sdwa v184, 1, v168 dst_sel:DWORD dst_unused:UNUSED_PAD src0_sel:DWORD src1_sel:WORD_1\n"
    "v_mfma_f32_16x16x32_bf16 v[140:143], v[164:167], v[188:191], 0\n"
    "ds_read_u16 v184, v184\n"
    "v_lshlrev_b32_sdwa v185, 1, v170 dst_sel:DWORD dst_unused:UNUSED_PAD src0_sel:DWORD src1_sel:WORD_1\n"
    "v_mfma_f32_16x16x32_bf16 v[168:171], a[104:107], a[224:227], 0\n"
    "s_waitcnt lgkmcnt(7)\n"
    "s_barrier\n"
    "v_mfma_f32_16x16x32_bf16 v[168:171], a[152:155], a[228:231], v[168:171]\n"
    "ds_read_b128 v[208:211], v250 offset:0\n"
    "ds_read_b128 v[212:215], v250 offset:320\n"
    "v_mfma_f32_16x16x32_bf16 v[168:171], a[64:67], a[232:235], v[168:171]\n"
    "ds_read_u16_d16_hi v200, v200\n"
    "ds_read_u16 v185, v185\n"
    "v_mfma_f32_16x16x32_bf16 v[96:99], a[16:19], v[192:195], v[96:99]\n"
    "s_waitcnt lgkmcnt(9)\n"
    "ds_read_u16_d16_hi v201, v201\n"
    "v_or_b32_e32 v197, v197, v205\n"
    "v_mfma_f32_16x16x32_bf16 v[100:103], a[20:23], v[192:195], v[100:103]\n"
    "v_lshlrev_b32_sdwa v186, 1, v172 dst_sel:DWORD dst_unused:UNUSED_PAD src0_sel:DWORD src1_sel:WORD_1\n"
    "v_or_b32_e32 v198, v198, v206\n"
    "v_mfma_f32_16x16x32_bf16 v[112:115], a[24:27], v[192:195], v[112:115]\n"
    "ds_read_u16 v186, v186\n"
    "v_lshlrev_b32_sdwa v187, 1, v174 dst_sel:DWORD dst_unused:UNUSED_PAD src0_sel:DWORD src1_sel:WORD_1\n"
    "v_mfma_f32_16x16x32_bf16 v[116:119], a[28:31], v[192:195], v[116:119]\n"
    "s_waitcnt lgkmcnt(8)\n"
    "v_lshlrev_b32_sdwa v203, 1, v175 dst_sel:DWORD dst_unused:UNUSED_PAD src0_sel:DWORD src1_sel:WORD_1\n"
    "v_lshlrev_b32_sdwa v188, 1, v176 dst_sel:DWORD dst_unused:UNUSED_PAD src0_sel:DWORD src1_sel:WORD_1\n"
    "v_mfma_f32_16x16x32_bf16 v[172:175], a[108:111], a[224:227], 0\n"
    "ds_read_u16_d16_hi v202, v202\n"
    "v_lshlrev_b32_sdwa v189, 1, v178 dst_sel:DWORD dst_unused:UNUSED_PAD src0_sel:DWORD src1_sel:WORD_1\n"
    "v_mfma_f32_16x16x32_bf16 v[172:175], a[156:159], a[228:231], v[172:175]\n"
    "v_lshlrev_b32_sdwa v205, 1, v179 dst_sel:DWORD dst_unused:UNUSED_PAD src0_sel:DWORD src1_sel:WORD_1\n"
    "v_lshlrev_b32_sdwa v190, 1, v180 dst_sel:DWORD dst_unused:UNUSED_PAD src0_sel:DWORD src1_sel:WORD_1\n"
    "v_mfma_f32_16x16x32_bf16 v[172:175], a[68:71], a[232:235], v[172:175]\n"
    "ds_read_b128 v[236:239], v244 offset:4096\n"
    "ds_read_u16 v187, v187\n"
    "v_mfma_f32_16x16x32_bf16 v[176:179], a[104:107], a[236:239], 0\n"
    "v_lshlrev_b32_sdwa v206, 1, v181 dst_sel:DWORD dst_unused:UNUSED_PAD src0_sel:DWORD src1_sel:WORD_1\n"
    "v_lshlrev_b32_sdwa v191, 1, v182 dst_sel:DWORD dst_unused:UNUSED_PAD src0_sel:DWORD src1_sel:WORD_1\n"
    "v_mfma_f32_16x16x32_bf16 v[176:179], a[152:155], a[240:243], v[176:179]\n"
    "s_waitcnt lgkmcnt(7)\n"
    "ds_read_u16_d16_hi v203, v203\n"
    "v_or_b32_e32 v199, v199, v207\n"
    "v_smfmac_f32_16x16x64_bf16 v[0:3], v[216:219], v[208:215], v240\n"
    "v_lshlrev_b32_sdwa v207, 1, v183 dst_sel:DWORD dst_unused:UNUSED_PAD src0_sel:DWORD src1_sel:WORD_1\n"
    "v_smfmac_f32_16x16x64_bf16 v[4:7], v[220:223], v[208:215], v240 abid:1\n"
    "ds_read_b128 v[208:211], v250 offset:640\n"
    "v_mfma_f32_16x16x32_bf16 v[176:179], a[64:67], a[244:247], v[176:179]\n"
    "s_waitcnt lgkmcnt(8)\n"
    "ds_read_b128 v[212:215], v250 offset:960\n"
    "v_or_b32_e32 v184, v184, v200\n"
    "v_mfma_f32_16x16x32_bf16 v[180:183], a[108:111], a[236:239], 0\n"
    "ds_read_b128 v[216:219], v252 offset:0\n"
    "v_mfma_f32_16x16x32_bf16 v[180:183], a[156:159], a[240:243], v[180:183]\n"
    "ds_read_b128 v[220:223], v252 offset:336\n"
    "v_mfma_f32_16x16x32_bf16 v[180:183], a[68:71], a[244:247], v[180:183]\n"
    "s_waitcnt lgkmcnt(9)\n"
    "ds_read_b128 v[164:167], v244 offset:6144\n"
    "v_or_b32_e32 v185, v185, v201\n"
    "v_mfma_f32_16x16x32_bf16 v[104:107], a[16:19], v[196:199], v[104:107]\n"
    "ds_read_u16 v188, v188\n"
    "v_lshlrev_b32_sdwa v201, 1, v171 dst_sel:DWORD dst_unused:UNUSED_PAD src0_sel:DWORD src1_sel:WORD_1\n"
    "v_mfma_f32_16x16x32_bf16 v[108:111], a[20:23], v[196:199], v[108:111]\n"
    "s_waitcnt vmcnt(6)\n"
    "v_mfma_f32_16x16x32_bf16 v[120:123], a[24:27], v[196:199], v[120:123]\n"
    "s_nop 0\n"
    "ds_read_u16_d16_hi v204, v204\n"
    "v_mfma_f32_16x16x32_bf16 v[124:127], a[28:31], v[196:199], v[124:127]\n"
    "s_waitcnt lgkmcnt(9)\n"
    "v_or_b32_e32 v186, v186, v202\n"
    "v_lshlrev_b32_sdwa v202, 1, v173 dst_sel:DWORD dst_unused:UNUSED_PAD src0_sel:DWORD src1_sel:WORD_1\n"
    "v_mfma_f32_16x16x32_bf16 v[128:131], v[236:239], v[192:195], v[128:131]\n"
    "ds_read_u16 v189, v189\n"
    "v_mfma_f32_16x16x32_bf16 v[136:139], v[236:239], v[196:199], v[136:139]\n"
    "ds_read_u16_d16_hi v205, v205\n"
    "v_smfmac_f32_32x32x32_bf16 v[112:127], a[248:251], v[152:159], v241\n"
    "s_waitcnt vmcnt(5)\n"
    "v_smfmac_f32_32x32x32_bf16 v[96:111], a[248:251], v[144:151], v241\n"
    "s_waitcnt lgkmcnt(7)\n"
    "ds_read_u16 v190, v190\n"
    "ds_read_u16_d16_hi v206, v206\n"
    "v_or_b32_e64 v187, v187, v203\n"
    "v_lshlrev_b32_sdwa v203, 1, v175 dst_sel:DWORD dst_unused:UNUSED_PAD src0_sel:DWORD src1_sel:WORD_1\n"
    "buffer_load_dwordx4 v[144:147], v245, s[40:43], s57 offen offset:128\n"
    "s_waitcnt lgkmcnt(7)\n"
    "v_smfmac_f32_16x16x64_bf16 v[0:3], v[216:219], v[208:215], v240\n"
    "s_waitcnt lgkmcnt(6)\n"
    "ds_read_b128 v[216:219], v251 offset:672\n"
    "ds_read_u16 v191, v191\n"
    "v_smfmac_f32_16x16x64_bf16 v[4:7], v[220:223], v[208:215], v240 abid:1\n"
    "v_mfma_f32_16x16x32_bf16 v[132:135], v[164:167], v[192:195], v[132:135]\n"
    "ds_read_b128 v[208:211], v250 offset:80\n"
    "v_cvt_pk_f16_f32 v192, v168, v169\n"
    "v_mfma_f32_16x16x32_bf16 v[140:143], v[164:167], v[196:199], v[140:143]\n"
    "ds_read_b128 v[212:215], v250 offset:400\n"
    "v_lshlrev_b32_sdwa v193, 1, v170 dst_sel:DWORD dst_unused:UNUSED_PAD src0_sel:DWORD src1_sel:WORD_1\n"
    "v_lshlrev_b32_sdwa v194, 1, v172 dst_sel:DWORD dst_unused:UNUSED_PAD src0_sel:DWORD src1_sel:WORD_1\n"
    "v_lshlrev_b32_sdwa v195, 1, v174 dst_sel:DWORD dst_unused:UNUSED_PAD src0_sel:DWORD src1_sel:WORD_1\n"
    "ds_read_b128 v[220:223], v251 offset:1008\n"
    "v_pk_mov_b32 v[224:225], v[0:1], v[0:1] op_sel:[0,1]\n"
    "v_pk_mov_b32 v[226:227], v[2:3], v[2:3] op_sel:[0,1]\n"
    "v_pk_mul_f32 v[228:229], v[0:1], v[0:1]\n"
    "v_pk_mul_f32 v[230:231], v[2:3], v[2:3]\n"
    "v_pk_add_f32 v[224:225], v[224:225], v[4:5]\n"
    "v_pk_add_f32 v[226:227], v[226:227], v[6:7]\n"
    "v_pk_fma_f32 v[228:229], v[4:5], v[4:5], v[228:229]\n"
    "v_pk_fma_f32 v[230:231], v[6:7], v[6:7], v[230:231]\n"
    "v_mfma_f32_16x16x32_bf16 v[168:171], a[112:115], a[224:227], 0\n"
    "s_waitcnt lgkmcnt(8)\n"
    "ds_read_u16_d16_hi v207, v207\n"
    "ds_read_u16 v193, v193\n"
    "v_mfma_f32_16x16x32_bf16 v[168:171], a[160:163], a[228:231], v[168:171]\n"
    "s_waitcnt lgkmcnt(9)\n"
    "buffer_load_dwordx4 v[148:151], v245, s[40:43], s88 offen offset:128\n"
    "ds_read_b128 a[252:255], v244 offset:34816\n"
    "v_mfma_f32_16x16x32_bf16 v[168:171], a[72:75], a[232:235], v[168:171]\n"
    "v_cvt_pk_f16_f32 v196, v176, v177\n"
    "v_lshlrev_b32_sdwa v197, 1, v178 dst_sel:DWORD dst_unused:UNUSED_PAD src0_sel:DWORD src1_sel:WORD_1\n"
    "v_mfma_f32_16x16x32_bf16 v[172:175], a[116:119], a[224:227], 0\n"
    "ds_read_u16_d16_hi v201, v201\n"
    "v_lshlrev_b32_sdwa v198, 1, v180 dst_sel:DWORD dst_unused:UNUSED_PAD src0_sel:DWORD src1_sel:WORD_1\n"
    "v_mfma_f32_16x16x32_bf16 v[172:175], a[164:167], a[228:231], v[172:175]\n"
    "ds_read_u16 v194, v194\n"
    "v_lshlrev_b32_e64 v254, 3, v244\n"
    "v_lshlrev_b32_sdwa v199, 1, v182 dst_sel:DWORD dst_unused:UNUSED_PAD src0_sel:DWORD src1_sel:WORD_1\n"
    "v_or_b32_e64 v188, v188, v204\n"
    "v_or_b32_e64 v189, v189, v205\n"
    "v_lshlrev_b32_sdwa v205, 1, v179 dst_sel:DWORD dst_unused:UNUSED_PAD src0_sel:DWORD src1_sel:WORD_1\n"
    "v_mfma_f32_16x16x32_bf16 v[176:179], a[112:115], a[236:239], 0\n"
    "s_waitcnt lgkmcnt(9)\n"
    "ds_read_u16_d16_hi v202, v202\n"
    "v_or_b32_e32 v190, v190, v206\n"
    "v_mfma_f32_16x16x32_bf16 v[176:179], a[160:163], a[240:243], v[176:179]\n"
    "v_lshlrev_b32_sdwa v206, 1, v181 dst_sel:DWORD dst_unused:UNUSED_PAD src0_sel:DWORD src1_sel:WORD_1\n"
    "v_pk_fma_f16 v232, v192, v192, s48\n"
    "v_mfma_f32_16x16x32_bf16 v[176:179], a[72:75], a[244:247], v[176:179]\n"
    "s_waitcnt lgkmcnt(7)\n"
    "ds_read_u16 v195, v195\n"
    "v_pk_mul_f16 v232, v192, v232\n"
    "v_smfmac_f32_16x16x64_bf16 v[8:11], v[216:219], v[208:215], v240\n"
    "s_waitcnt lgkmcnt(7)\n"
    "ds_read_b128 v[216:219], v252 offset:672\n"
    "v_smfmac_f32_16x16x64_bf16 v[12:15], v[220:223], v[208:215], v240 abid:1\n"
    "ds_read_b128 v[208:211], v250 offset:720\n"
    "v_mfma_f32_16x16x32_bf16 v[112:115], a[40:43], v[184:187], v[112:115]\n"
    "v_exp_f16_e64 v234, v232\n"
    "ds_read_b128 v[212:215], v250 offset:1040\n"
    "ds_read_b128 v[220:223], v252 offset:1008\n"
    "s_waitcnt lgkmcnt(10)\n"
    "v_or_b32_e64 v191, v191, v207\n"
    "v_mfma_f32_16x16x32_bf16 v[116:119], a[44:47], v[184:187], v[116:119]\n"
    "s_waitcnt lgkmcnt(8)\n"
    "ds_read_b128 v[236:239], v244 offset:8192\n"
    "v_lshlrev_b32_sdwa v207, 1, v183 dst_sel:DWORD dst_unused:UNUSED_PAD src0_sel:DWORD src1_sel:WORD_1\n"
    "v_mfma_f32_16x16x32_bf16 v[172:175], a[252:255], a[232:235], v[172:175]\n"
    "ds_read_b128 v[152:155], v242 offset:24640\n"
    "s_mov_b32 exec_hi, 0\n"
    "buffer_load_dword v233, v254, s[80:83], s84 offen sc1\n"
    "s_mov_b32 exec_hi, -1\n"
    "v_mfma_f32_16x16x32_bf16 v[180:183], a[116:119], a[236:239], 0\n"
    "ds_read_b128 v[156:159], v242 offset:25664\n"
    "ds_read_u16_d16_hi v203, v203\n"
    "v_mfma_f32_16x16x32_bf16 v[180:183], a[164:167], a[240:243], v[180:183]\n"
    "s_waitcnt lgkmcnt(11)\n"
    "v_or_b32_e32 v193, v193, v201\n"
    "v_exp_f16_sdwa v234, v232 dst_sel:WORD_1 dst_unused:UNUSED_PRESERVE src0_sel:WORD_1\n"
    "v_mfma_f32_16x16x32_bf16 v[180:183], a[252:255], a[244:247], v[180:183]\n"
    "ds_read_b128 v[164:167], v244 offset:10240\n"
    "v_pk_fma_f16 v234, v234, s49, s49\n"
    "v_mfma_f32_16x16x32_bf16 v[120:123], a[40:43], v[188:191], v[120:123]\n"
    "ds_read_u16 v197, v197\n"
    "v_lshlrev_b32_sdwa v200, 1, v169 dst_sel:DWORD dst_unused:UNUSED_PAD src0_sel:DWORD src1_sel:WORD_1\n"
    "v_mfma_f32_16x16x32_bf16 v[124:127], a[44:47], v[188:191], v[124:127]\n"
    "s_waitcnt lgkmcnt(11)\n"
    "v_or_b32_e32 v194, v194, v202\n"
    "v_lshlrev_b32_sdwa v201, 1, v171 dst_sel:DWORD dst_unused:UNUSED_PAD src0_sel:DWORD src1_sel:WORD_1\n"
    "ds_read_u16_d16_hi v205, v205\n"
    "v_lshlrev_b32_sdwa v202, 1, v173 dst_sel:DWORD dst_unused:UNUSED_PAD src0_sel:DWORD src1_sel:WORD_1\n"
    "v_rcp_f16_e64 v232, v234\n"
    "v_lshlrev_b32_sdwa v204, 1, v177 dst_sel:DWORD dst_unused:UNUSED_PAD src0_sel:DWORD src1_sel:WORD_1\n"
    "ds_read_u16 v198, v198\n"
    "s_waitcnt lgkmcnt(9)\n"
    "v_smfmac_f32_16x16x64_bf16 v[8:11], v[216:219], v[208:215], v240\n"
    "s_waitcnt lgkmcnt(8)\n"
    "ds_read_b128 v[216:219], v251 offset:1344\n"
    "v_smfmac_f32_16x16x64_bf16 v[12:15], v[220:223], v[208:215], v240 abid:1\n"
    "s_waitcnt lgkmcnt(8)\n"
    "ds_read_b128 v[208:211], v250 offset:160\n"
    "v_mfma_f32_16x16x32_bf16 v[128:131], v[236:239], v[184:187], v[128:131]\n"
    "s_nop 0\n"
    "v_rcp_f16_sdwa v232, v234 dst_sel:WORD_1 dst_unused:UNUSED_PRESERVE src0_sel:WORD_1\n"
    "v_mfma_f32_16x16x32_bf16 v[136:139], v[236:239], v[188:191], v[136:139]\n"
    "ds_read_b128 v[212:215], v250 offset:480\n"
    "ds_read_b128 v[220:223], v251 offset:1680\n"
    "v_fma_mix_f32 v234, v192, v232, 0 op_sel_hi:[1,1,0]\n"
    "v_fma_mix_f32 v232, v192, v232, 0 op_sel:[1,1,0] op_sel_hi:[1,1,0]\n"
    "v_cvt_pk_bf16_f32 v192, v234, v232\n"
    "v_pk_add_f32 v[224:225], v[224:225], v[8:9]\n"
    "v_pk_add_f32 v[226:227], v[226:227], v[10:11]\n"
    "v_pk_fma_f32 v[228:229], v[8:9], v[8:9], v[228:229]\n"
    "v_pk_fma_f32 v[230:231], v[10:11], v[10:11], v[230:231]\n"
    "v_pk_add_f32 v[224:225], v[224:225], v[12:13]\n"
    "v_pk_add_f32 v[226:227], v[226:227], v[14:15]\n"
    "v_pk_fma_f32 v[228:229], v[12:13], v[12:13], v[228:229]\n"
    "v_pk_fma_f32 v[230:231], v[14:15], v[14:15], v[230:231]\n"
    "s_waitcnt lgkmcnt(7)\n"
    "v_smfmac_f32_32x32x32_bf16 v[96:111], a[248:251], v[152:159], v241\n"
    "s_nop 0\n"
    "ds_read_u16_d16_hi v206, v206\n"
    "ds_read_u16 v199, v199\n"
    "v_or_b32_e64 v195, v195, v203\n"
    "v_lshlrev_b32_sdwa v203, 1, v175 dst_sel:DWORD dst_unused:UNUSED_PAD src0_sel:DWORD src1_sel:WORD_1\n"
    "v_pk_fma_f16 v232, v196, v196, s48\n"
    "v_mfma_f32_16x16x32_bf16 v[132:135], v[164:167], v[184:187], v[132:135]\n"
    "ds_read_u16_d16_hi v207, v207\n"
    "v_pk_mul_f16 v232, v196, v232\n"
    "v_mfma_f32_16x16x32_bf16 v[140:143], v[164:167], v[188:191], v[140:143]\n"
    "ds_read_b128 v[152:155], v242 offset:26688\n"
    "v_exp_f16_e32 v234, v232\n"
    "v_mfma_f32_16x16x32_bf16 v[96:99], a[32:35], v[184:187], v[96:99]\n"
    "s_waitcnt lgkmcnt(9)\n"
    "ds_read_b128 v[156:159], v242 offset:27712\n"
    "ds_read_b128 a[252:255], v244 offset:38912\n"
    "v_mfma_f32_16x16x32_bf16 v[100:103], a[36:39], v[184:187], v[100:103]\n"
    "v_or_b32_e64 v197, v197, v205\n"
    "v_lshlrev_b32_sdwa v184, 1, v168 dst_sel:DWORD dst_unused:UNUSED_PAD src0_sel:DWORD src1_sel:WORD_1\n"
    "v_mfma_f32_16x16x32_bf16 v[104:107], a[32:35], v[188:191], v[104:107]\n"
    "ds_read_u16 v184, v184\n"
    "v_lshlrev_b32_sdwa v185, 1, v170 dst_sel:DWORD dst_unused:UNUSED_PAD src0_sel:DWORD src1_sel:WORD_1\n"
    "v_mfma_f32_16x16x32_bf16 v[108:111], a[36:39], v[188:191], v[108:111]\n"
    "s_waitcnt lgkmcnt(8)\n"
    "ds_read_u16_d16_hi v200, v200\n"
    "v_lshlrev_b32_sdwa v186, 1, v172 dst_sel:DWORD dst_unused:UNUSED_PAD src0_sel:DWORD src1_sel:WORD_1\n"
    "v_smfmac_f32_16x16x64_bf16 v[16:19], v[216:219], v[208:215], v240\n"
    "s_waitcnt lgkmcnt(8)\n"
    "v_lshlrev_b32_sdwa v187, 1, v174 dst_sel:DWORD dst_unused:UNUSED_PAD src0_sel:DWORD src1_sel:WORD_1\n"
    "v_smfmac_f32_16x16x64_bf16 v[20:23], v[220:223], v[208:215], v240 abid:1\n"
    "ds_read_b128 v[208:211], v250 offset:800\n"
    "v_mfma_f32_16x16x32_bf16 v[168:171], a[120:123], a[224:227], 0\n"
    "ds_read_b128 v[212:215], v250 offset:1120\n"
    "v_lshlrev_b32_sdwa v188, 1, v176 dst_sel:DWORD dst_unused:UNUSED_PAD src0_sel:DWORD src1_sel:WORD_1\n"
    "v_mfma_f32_16x16x32_bf16 v[168:171], a[168:171], a[228:231], v[168:171]\n"
    "s_waitcnt lgkmcnt(9)\n"
    "ds_read_b128 v[216:219], v252 offset:1344\n"
    "v_or_b32_e32 v198, v198, v206\n"
    "v_mfma_f32_16x16x32_bf16 v[168:171], a[76:79], a[232:235], v[168:171]\n"
    "ds_read_b128 v[220:223], v252 offset:1680\n"
    "v_lshlrev_b32_sdwa v189, 1, v178 dst_sel:DWORD dst_unused:UNUSED_PAD src0_sel:DWORD src1_sel:WORD_1\n"
    "v_mfma_f32_16x16x32_bf16 v[172:175], a[124:127], a[224:227], 0\n"
    "ds_read_u16 v185, v185\n"
    "v_lshlrev_b32_sdwa v205, 1, v179 dst_sel:DWORD dst_unused:UNUSED_PAD src0_sel:DWORD src1_sel:WORD_1\n"
    "v_mfma_f32_16x16x32_bf16 v[172:175], a[172:175], a[228:231], v[172:175]\n"
    "v_lshlrev_b32_sdwa v190, 1, v180 dst_sel:DWORD dst_unused:UNUSED_PAD src0_sel:DWORD src1_sel:WORD_1\n"
    "v_lshlrev_b32_sdwa v206, 1, v181 dst_sel:DWORD dst_unused:UNUSED_PAD src0_sel:DWORD src1_sel:WORD_1\n"
    "v_mfma_f32_16x16x32_bf16 v[176:179], a[120:123], a[236:239], 0\n"
    "s_waitcnt lgkmcnt(9)\n"
    "ds_read_u16_d16_hi v201, v201\n"
    "v_lshlrev_b32_sdwa v191, 1, v182 dst_sel:DWORD dst_unused:UNUSED_PAD src0_sel:DWORD src1_sel:WORD_1\n"
    "v_mfma_f32_16x16x32_bf16 v[176:179], a[168:171], a[240:243], v[176:179]\n"
    "s_waitcnt lgkmcnt(9)\n"
    "v_or_b32_e32 v199, v199, v207\n"
    "v_lshlrev_b32_sdwa v207, 1, v183 dst_sel:DWORD dst_unused:UNUSED_PAD src0_sel:DWORD src1_sel:WORD_1\n"
    "v_smfmac_f32_32x32x32_bf16 v[112:127], a[248:251], v[152:159], v241\n"
    "s_waitcnt lgkmcnt(8)\n"
    "ds_read_u16 v186, v186\n"
    "v_exp_f16_sdwa v234, v232 dst_sel:WORD_1 dst_unused:UNUSED_PRESERVE src0_sel:WORD_1\n"
    "ds_read_u16_d16_hi v202, v202\n"
    "v_mfma_f32_16x16x32_bf16 v[172:175], a[252:255], a[232:235], v[172:175]\n"
    "v_pk_fma_f16 v234, v234, s49, s49\n"
    "v_rcp_f16_e64 v232, v234\n"
    "v_mfma_f32_16x16x32_bf16 v[176:179], a[76:79], a[244:247], v[176:179]\n"
    "ds_read_b128 v[236:239], v244 offset:12288\n"
    "ds_read_u16 v187, v187\n"
    "v_mfma_f32_16x16x32_bf16 v[180:183], a[124:127], a[236:239], 0\n"
    "s_waitcnt lgkmcnt(9)\n"
    "v_rcp_f16_sdwa v232, v234 dst_sel:WORD_1 dst_unused:UNUSED_PRESERVE src0_sel:WORD_1\n"
    "v_mfma_f32_16x16x32_bf16 v[180:183], a[172:175], a[240:243], v[180:183]\n"
    "ds_read_u16_d16_hi v203, v203\n"
    "v_fma_mix_f32 v234, v196, v232, 0 op_sel_hi:[1,1,0]\n"
    "v_mfma_f32_16x16x32_bf16 v[180:183], a[252:255], a[244:247], v[180:183]\n"
    "s_waitcnt lgkmcnt(8)\n"
    "v_fma_mix_f32 v232, v196, v232, 0 op_sel:[1,1,0] op_sel_hi:[1,1,0]\n"
    "v_or_b32_e32 v184, v184, v200\n"
    "v_smfmac_f32_16x16x64_bf16 v[16:19], v[216:219], v[208:215], v240\n"
    "s_waitcnt lgkmcnt(7)\n"
    "ds_read_b128 v[216:219], v251 offset:2016\n"
    "v_smfmac_f32_16x16x64_bf16 v[20:23], v[220:223], v[208:215], v240 abid:1\n"
    "ds_read_b128 v[208:211], v250 offset:240\n"
    "v_mfma_f32_16x16x32_bf16 v[96:99], a[176:179], v[192:195], v[96:99]\n"
    "ds_read_b128 v[212:215], v250 offset:560\n"
    "v_cvt_pk_bf16_f32 v196, v234, v232\n"
    "v_mfma_f32_16x16x32_bf16 v[100:103], a[180:183], v[192:195], v[100:103]\n"
    "ds_read_b128 v[220:223], v251 offset:2352\n"
    "v_pk_add_f32 v[224:225], v[224:225], v[16:17]\n"
    "v_pk_add_f32 v[226:227], v[226:227], v[18:19]\n"
    "v_pk_fma_f32 v[228:229], v[16:17], v[16:17], v[228:229]\n"
    "v_pk_fma_f32 v[230:231], v[18:19], v[18:19], v[230:231]\n"
    "v_pk_add_f32 v[224:225], v[224:225], v[20:21]\n"
    "v_pk_add_f32 v[226:227], v[226:227], v[22:23]\n"
    "v_pk_fma_f32 v[228:229], v[20:21], v[20:21], v[228:229]\n"
    "v_pk_fma_f32 v[230:231], v[22:23], v[22:23], v[230:231]\n"
    "v_mfma_f32_16x16x32_bf16 v[112:115], a[184:187], v[192:195], v[112:115]\n"
    "s_waitcnt lgkmcnt(8)\n"
    "ds_read_b128 v[164:167], v244 offset:14336\n"
    "ds_read_u16 v188, v188\n"
    "v_mfma_f32_16x16x32_bf16 v[116:119], a[188:191], v[192:195], v[116:119]\n"
    "s_waitcnt lgkmcnt(8)\n"
    "ds_read_u16_d16_hi v204, v204\n"
    "v_lshlrev_b32_e64 v254, 3, v244\n"
    "v_mfma_f32_16x16x32_bf16 v[128:131], v[236:239], v[192:195], v[128:131]\n"
    "ds_read_u16 v189, v189\n"
    "v_or_b32_e64 v185, v185, v201\n"
    "v_mfma_f32_16x16x32_bf16 v[104:107], a[176:179], v[196:199], v[104:107]\n"
    "v_lshlrev_b32_sdwa v201, 1, v171 dst_sel:DWORD dst_unused:UNUSED_PAD src0_sel:DWORD src1_sel:WORD_1\n"
    "v_or_b32_e64 v186, v186, v202\n"
    "v_mfma_f32_16x16x32_bf16 v[108:111], a[180:183], v[196:199], v[108:111]\n"
    "ds_read_b128 v[152:155], v242 offset:28736\n"
    "ds_read_b128 v[156:159], v242 offset:29760\n"
    "v_mfma_f32_16x16x32_bf16 v[120:123], a[184:187], v[196:199], v[120:123]\n"
    "v_lshlrev_b32_sdwa v202, 1, v173 dst_sel:DWORD dst_unused:UNUSED_PAD src0_sel:DWORD src1_sel:WORD_1\n"
    "ds_read_u16_d16_hi v205, v205\n"
    "v_mfma_f32_16x16x32_bf16 v[124:127], a[188:191], v[196:199], v[124:127]\n"
    "s_waitcnt lgkmcnt(9)\n"
    "v_or_b32_e32 v187, v187, v203\n"
    "v_lshlrev_b32_sdwa v203, 1, v175 dst_sel:DWORD dst_unused:UNUSED_PAD src0_sel:DWORD src1_sel:WORD_1\n"
    "v_mfma_f32_16x16x32_bf16 v[136:139], v[236:239], v[196:199], v[136:139]\n"
    "s_waitcnt lgkmcnt(8)\n"
    "ds_read_u16 v190, v190\n"
    "v_smfmac_f32_16x16x64_bf16 v[24:27], v[216:219], v[208:215], v240\n"
    "s_waitcnt lgkmcnt(8)\n"
    "ds_read_b128 v[216:219], v252 offset:2016\n"
    "v_smfmac_f32_16x16x64_bf16 v[28:31], v[220:223], v[208:215], v240 abid:1\n"
    "s_waitcnt lgkmcnt(8)\n"
    "ds_read_b128 v[208:211], v250 offset:880\n"
    "v_mfma_f32_16x16x32_bf16 v[132:135], v[164:167], v[192:195], v[132:135]\n"
    "s_nop 0\n"
    "v_cvt_pk_f16_f32 v192, v168, v169\n"
    "v_lshlrev_b32_sdwa v193, 1, v170 dst_sel:DWORD dst_unused:UNUSED_PAD src0_sel:DWORD src1_sel:WORD_1\n"
    "v_mfma_f32_16x16x32_bf16 v[140:143], v[164:167], v[196:199], v[140:143]\n"
    "ds_read_b128 v[212:215], v250 offset:1200\n"
    "ds_read_b128 v[220:223], v252 offset:2352\n"
    "v_lshlrev_b32_sdwa v194, 1, v172 dst_sel:DWORD dst_unused:UNUSED_PAD src0_sel:DWORD src1_sel:WORD_1\n"
    "s_mov_b32 exec_hi, 0\n"
    "buffer_load_dword v233, v254, s[72:75], s53 offen sc1\n"
    "s_mov_b32 exec_hi, -1\n"
    "ds_read_b128 a[224:227], v242 offset:0\n"
    "v_lshlrev_b32_sdwa v195, 1, v174 dst_sel:DWORD dst_unused:UNUSED_PAD src0_sel:DWORD src1_sel:WORD_1\n"
    "v_cvt_pk_f16_f32 v196, v176, v177\n"
    "ds_read_u16_d16_hi v206, v206\n"
    "v_lshlrev_b32_sdwa v197, 1, v178 dst_sel:DWORD dst_unused:UNUSED_PAD src0_sel:DWORD src1_sel:WORD_1\n"
    "v_lshlrev_b32_sdwa v198, 1, v180 dst_sel:DWORD dst_unused:UNUSED_PAD src0_sel:DWORD src1_sel:WORD_1\n"
    "v_lshlrev_b32_sdwa v199, 1, v182 dst_sel:DWORD dst_unused:UNUSED_PAD src0_sel:DWORD src1_sel:WORD_1\n"
    "s_waitcnt lgkmcnt(11)\n"
    "v_or_b32_e32 v188, v188, v204\n"
    "ds_read_b128 a[228:231], v242 offset:1024\n"
    "ds_read_u16 v191, v191\n"
    "s_waitcnt lgkmcnt(10)\n"
    "v_smfmac_f32_32x32x32_bf16 v[128:143], a[248:251], v[152:159], v241\n"
    "s_nop 0\n"
    "v_pk_fma_f16 v232, v192, v192, s48\n"
    "v_mov_b32_e64 v254, 0\n"
    "ds_read_b128 a[232:235], v242 offset:2048\n"
    "v_pk_mul_f16 v232, v192, v232\n"
    "ds_read_u16_d16_hi v207, v207\n"
    "s_waitcnt lgkmcnt(11)\n"
    "v_or_b32_e32 v189, v189, v205\n"
    "v_lshlrev_b32_sdwa v205, 1, v179 dst_sel:DWORD dst_unused:UNUSED_PAD src0_sel:DWORD src1_sel:WORD_1\n"
    "v_exp_f16_e64 v234, v232\n"
    "ds_read_b128 a[236:239], v242 offset:3072\n"
    "ds_read_b128 a[240:243], v242 offset:4096\n"
    "v_exp_f16_sdwa v234, v232 dst_sel:WORD_1 dst_unused:UNUSED_PRESERVE src0_sel:WORD_1\n"
    "s_waitcnt lgkmcnt(9)\n"
    "s_barrier\n"
    "v_smfmac_f32_16x16x64_bf16 v[24:27], v[216:219], v[208:215], v240\n"
    "s_waitcnt lgkmcnt(7)\n"
    "ds_read_b128 a[244:247], v242 offset:5120\n"
    "ds_write2_b32 v248, v32, v33 offset0:0 offset1:20\n"
    "v_smfmac_f32_16x16x64_bf16 v[28:31], v[220:223], v[208:215], v240 abid:1\n"
    "s_waitcnt lgkmcnt(7)\n"
    "v_or_b32_e64 v190, v190, v206\n"
    "v_mfma_f32_16x16x32_bf16 v[168:171], a[80:83], a[224:227], 0\n"
    "ds_read_u16 v193, v193\n"
    "ds_write2_b32 v248, v34, v35 offset0:40 offset1:60\n"
    "v_lshlrev_b32_sdwa v206, 1, v181 dst_sel:DWORD dst_unused:UNUSED_PAD src0_sel:DWORD src1_sel:WORD_1\n"
    "v_pk_add_f32 v[224:225], v[224:225], v[24:25]\n"
    "v_pk_add_f32 v[226:227], v[226:227], v[26:27]\n"
    "v_pk_fma_f32 v[228:229], v[24:25], v[24:25], v[228:229]\n"
    "v_pk_fma_f32 v[230:231], v[26:27], v[26:27], v[230:231]\n"
    "v_pk_add_f32 v[224:225], v[224:225], v[28:29]\n"
    "v_pk_add_f32 v[226:227], v[226:227], v[30:31]\n"
    "v_pk_fma_f32 v[228:229], v[28:29], v[28:29], v[228:229]\n"
    "v_pk_fma_f32 v[230:231], v[30:31], v[30:31], v[230:231]\n"
    "v_mfma_f32_16x16x32_bf16 v[168:171], a[128:131], a[228:231], v[168:171]\n"
    "s_waitcnt lgkmcnt(5)\n"
    "ds_read_b128 a[252:255], v244 offset:32768\n"
    "ds_write2_b32 v235, v52, v53 offset0:0 offset1:20\n"
    "v_mfma_f32_16x16x32_bf16 v[168:171], a[48:51], a[232:235], v[168:171]\n"
    "s_nop 0\n"
    "v_add_u32_e32 v248, 1360, v248\n"
    "v_or_b32_e64 v191, v191, v207\n"
    "v_mfma_f32_16x16x32_bf16 v[172:175], a[84:87], a[224:227], 0\n"
    "ds_read_u16_d16_hi v201, v201\n"
    "ds_write2_b32 v235, v54, v55 offset0:40 offset1:60\n"
    "v_mfma_f32_16x16x32_bf16 v[172:175], a[132:135], a[228:231], v[172:175]\n"
    "v_lshlrev_b32_sdwa v207, 1, v183 dst_sel:DWORD dst_unused:UNUSED_PAD src0_sel:DWORD src1_sel:WORD_1\n"
    "v_add_u32_e32 v235, 1360, v235\n"
    "v_mfma_f32_16x16x32_bf16 v[176:179], a[80:83], a[236:239], 0\n"
    "s_waitcnt lgkmcnt(6)\n"
    "ds_read_u16 v194, v194\n"
    "ds_write2_b32 v248, v36, v37 offset0:0 offset1:20\n"
    "v_mfma_f32_16x16x32_bf16 v[176:179], a[128:131], a[240:243], v[176:179]\n"
    "s_nop 0\n"
    "v_pk_fma_f16 v234, v234, s49, s49\n"
    "v_lshlrev_b32_sdwa v200, 1, v169 dst_sel:DWORD dst_unused:UNUSED_PAD src0_sel:DWORD src1_sel:WORD_1\n"
    "v_mfma_f32_16x16x32_bf16 v[176:179], a[48:51], a[244:247], v[176:179]\n"
    "s_add_u32 s60, s60, 1572864\n"
    "ds_read_u16_d16_hi v202, v202\n"
    "ds_write2_b32 v248, v38, v39 offset0:40 offset1:60\n"
    "v_mfma_f32_16x16x32_bf16 v[180:183], a[84:87], a[236:239], 0\n"
    "s_waitcnt lgkmcnt(6)\n"
    "v_add_u32_e32 v248, 1360, v248\n"
    "v_rcp_f16_e32 v232, v234\n"
    "v_mfma_f32_16x16x32_bf16 v[172:175], a[252:255], a[232:235], v[172:175]\n"
    "s_add_u32 s61, s61, 1572864\n"
    "ds_read_u16 v195, v195\n"
    "ds_write2_b32 v235, v56, v57 offset0:0 offset1:20\n"
    "v_mfma_f32_16x16x32_bf16 v[180:183], a[132:135], a[240:243], v[180:183]\n"
    "s_nop 0\n"
    "v_rcp_f16_sdwa v232, v234 dst_sel:WORD_1 dst_unused:UNUSED_PRESERVE src0_sel:WORD_1\n"
    "v_mfma_f32_16x16x32_bf16 v[180:183], a[252:255], a[244:247], v[180:183]\n"
    "s_waitcnt lgkmcnt(6)\n"
    "ds_read_b128 v[236:239], v244 offset:16384\n"
    "ds_write2_b32 v235, v58, v59 offset0:40 offset1:60\n"
    "v_mfma_f32_16x16x32_bf16 v[96:99], a[192:195], v[184:187], v[96:99]\n"
    "v_add_u32_e32 v235, 50288, v235\n"
    "v_or_b32_e64 v193, v193, v201\n"
    "v_mfma_f32_16x16x32_bf16 v[100:103], a[196:199], v[184:187], v[100:103]\n"
    "ds_read_u16_d16_hi v203, v203\n"
    "ds_write2_b32 v248, v40, v41 offset0:0 offset1:20\n"
    "v_mfma_f32_16x16x32_bf16 v[112:115], a[200:203], v[184:187], v[112:115]\n"
    "v_lshlrev_b32_sdwa v201, 1, v171 dst_sel:DWORD dst_unused:UNUSED_PAD src0_sel:DWORD src1_sel:WORD_1\n"
    "v_lshlrev_b32_sdwa v204, 1, v177 dst_sel:DWORD dst_unused:UNUSED_PAD src0_sel:DWORD src1_sel:WORD_1\n"
    "v_mfma_f32_16x16x32_bf16 v[116:119], a[204:207], v[184:187], v[116:119]\n"
    "ds_read_b128 v[164:167], v244 offset:18432\n"
    "ds_write2_b32 v248, v42, v43 offset0:40 offset1:60\n"
    "v_mfma_f32_16x16x32_bf16 v[104:107], a[192:195], v[188:191], v[104:107]\n"
    "s_waitcnt lgkmcnt(8)\n"
    "v_add_u32_e32 v248, 1360, v248\n"
    "v_or_b32_e32 v194, v194, v202\n"
    "v_mfma_f32_16x16x32_bf16 v[108:111], a[196:199], v[188:191], v[108:111]\n"
    "s_nop 0\n"
    "ds_read_u16 v197, v197\n"
    "ds_write2_b32 v248, v44, v45 offset0:0 offset1:20\n"
    "v_mfma_f32_16x16x32_bf16 v[120:123], a[200:203], v[188:191], v[120:123]\n"
    "s_waitcnt lgkmcnt(6)\n"
    "v_lshlrev_b32_sdwa v202, 1, v173 dst_sel:DWORD dst_unused:UNUSED_PAD src0_sel:DWORD src1_sel:WORD_1\n"
    "v_fma_mix_f32 v234, v192, v232, 0 op_sel_hi:[1,1,0]\n"
    "v_mfma_f32_16x16x32_bf16 v[128:131], v[236:239], v[184:187], v[128:131]\n"
    "s_add_u32 s62, s62, 1572864\n"
    "ds_read_u16_d16_hi v205, v205\n"
    "ds_write2_b32 v248, v46, v47 offset0:40 offset1:60\n"
    "v_mfma_f32_16x16x32_bf16 v[124:127], a[204:207], v[188:191], v[124:127]\n"
    "v_add_u32_e32 v248, 1360, v248\n"
    "v_fma_mix_f32 v232, v192, v232, 0 op_sel:[1,1,0] op_sel_hi:[1,1,0]\n"
    "v_mfma_f32_16x16x32_bf16 v[136:139], v[236:239], v[188:191], v[136:139]\n"
    "ds_read_u16 v198, v198\n"
    "ds_write2_b32 v248, v48, v49 offset0:0 offset1:20\n"
    "s_waitcnt lgkmcnt(7)\n"
    "v_mfma_f32_16x16x32_bf16 v[132:135], v[164:167], v[184:187], v[132:135]\n"
    "v_or_b32_e32 v195, v195, v203\n"
    "v_lshlrev_b32_sdwa v184, 1, v168 dst_sel:DWORD dst_unused:UNUSED_PAD src0_sel:DWORD src1_sel:WORD_1\n"
    "v_mfma_f32_16x16x32_bf16 v[140:143], v[164:167], v[188:191], v[140:143]\n"
    "ds_read_u16_d16_hi v206, v206\n"
    "ds_write2_b32 v248, v50, v51 offset0:40 offset1:60\n"
    "v_mfma_f32_32x32x1_2b_f32 v[32:63], v254, v254, 0\n"
    "v_add_u32_e32 v248, 46208, v248\n"
    "v_lshlrev_b32_sdwa v185, 1, v170 dst_sel:DWORD dst_unused:UNUSED_PAD src0_sel:DWORD src1_sel:WORD_1\n"
    "v_lshlrev_b32_sdwa v186, 1, v172 dst_sel:DWORD dst_unused:UNUSED_PAD src0_sel:DWORD src1_sel:WORD_1\n"
    "ds_read_u16 v199, v199\n"
    "ds_read_u16_d16_hi v207, v207\n"
    "v_lshlrev_b32_sdwa v187, 1, v174 dst_sel:DWORD dst_unused:UNUSED_PAD src0_sel:DWORD src1_sel:WORD_1\n"
    "v_lshlrev_b32_sdwa v203, 1, v175 dst_sel:DWORD dst_unused:UNUSED_PAD src0_sel:DWORD src1_sel:WORD_1\n"
    "v_lshlrev_b32_sdwa v188, 1, v176 dst_sel:DWORD dst_unused:UNUSED_PAD src0_sel:DWORD src1_sel:WORD_1\n"
    "v_lshlrev_b32_sdwa v189, 1, v178 dst_sel:DWORD dst_unused:UNUSED_PAD src0_sel:DWORD src1_sel:WORD_1\n"
    "v_lshlrev_b32_sdwa v190, 1, v180 dst_sel:DWORD dst_unused:UNUSED_PAD src0_sel:DWORD src1_sel:WORD_1\n"
    "v_lshlrev_b32_sdwa v191, 1, v182 dst_sel:DWORD dst_unused:UNUSED_PAD src0_sel:DWORD src1_sel:WORD_1\n"
    "v_cvt_pk_bf16_f32 v192, v234, v232\n"
    "v_mfma_f32_16x16x32_bf16 v[168:171], a[88:91], a[224:227], 0\n"
    "s_waitcnt lgkmcnt(6)\n"
    "ds_read_u16 v184, v184\n"
    "ds_read_b128 a[252:255], v244 offset:36864\n"
    "v_mfma_f32_16x16x32_bf16 v[168:171], a[136:139], a[228:231], v[168:171]\n"
    "s_nop 0\n"
    "ds_read_u16_d16_hi v200, v200\n"
    "v_or_b32_e64 v197, v197, v205\n"
    "v_mfma_f32_16x16x32_bf16 v[168:171], a[52:55], a[232:235], v[168:171]\n"
    "ds_read_u16 v185, v185\n"
    "v_lshlrev_b32_sdwa v205, 1, v179 dst_sel:DWORD dst_unused:UNUSED_PAD src0_sel:DWORD src1_sel:WORD_1\n"
    "v_mfma_f32_16x16x32_bf16 v[172:175], a[92:95], a[224:227], 0\n"
    "v_pk_fma_f16 v232, v196, v196, s48\n"
    "v_mov_b32_e64 v254, 0\n"
    "v_mfma_f32_16x16x32_bf16 v[172:175], a[140:143], a[228:231], v[172:175]\n"
    "s_add_u32 s63, s63, 1572864\n"
    "v_pk_mul_f16 v232, v196, v232\n"
    "v_exp_f16_e32 v234, v232\n"
    "v_mfma_f32_16x16x32_bf16 v[176:179], a[88:91], a[236:239], 0\n"
    "s_waitcnt lgkmcnt(6)\n"
    "ds_write2_b32 v248, v64, v65 offset0:0 offset1:20\n"
    "v_mfma_f32_16x16x32_bf16 v[176:179], a[136:139], a[240:243], v[176:179]\n"
    "s_add_u32 s52, s52, 1572864\n"
    "s_add_u32 s53, s53, 393216\n"
    "v_or_b32_e64 v198, v198, v206\n"
    "v_mfma_f32_16x16x32_bf16 v[176:179], a[52:55], a[244:247], v[176:179]\n"
    "ds_write2_b32 v248, v66, v67 offset0:40 offset1:60\n"
    "v_lshlrev_b32_sdwa v206, 1, v181 dst_sel:DWORD dst_unused:UNUSED_PAD src0_sel:DWORD src1_sel:WORD_1\n"
    "v_add_u32_e32 v248, 1360, v248\n"
    "v_exp_f16_sdwa v234, v232 dst_sel:WORD_1 dst_unused:UNUSED_PRESERVE src0_sel:WORD_1\n"
    "ds_read_u16_d16_hi v201, v201\n"
    "ds_write2_b32 v235, v84, v85 offset0:0 offset1:20\n"
    "s_waitcnt lgkmcnt(8)\n"
    "v_or_b32_e32 v199, v199, v207\n"
    "s_waitcnt lgkmcnt(6)\n"
    "v_mfma_f32_16x16x32_bf16 v[172:175], a[252:255], a[232:235], v[172:175]\n"
    "s_nop 0\n"
    "ds_read_u16 v186, v186\n"
    "ds_write2_b32 v235, v86, v87 offset0:40 offset1:60\n"
    "s_waitcnt vmcnt(2)\n"
    "v_smfmac_f32_32x32x32_bf16 v[128:143], a[248:251], v[144:151], v241\n"
    "s_waitcnt lgkmcnt(7)\n"
    "v_lshlrev_b32_sdwa v207, 1, v183 dst_sel:DWORD dst_unused:UNUSED_PAD src0_sel:DWORD src1_sel:WORD_1\n"
    "v_add_u32_e32 v235, 1360, v235\n"
    "v_pk_fma_f16 v234, v234, s49, s49\n"
    "v_or_b32_e64 v184, v184, v200\n"
    "v_rcp_f16_e64 v232, v234\n"
    "v_mfma_f32_16x16x32_bf16 v[180:183], a[92:95], a[236:239], 0\n"
    "ds_read_u16_d16_hi v202, v202\n"
    "ds_read_b128 v[236:239], v244 offset:20480\n"
    "v_mfma_f32_16x16x32_bf16 v[180:183], a[140:143], a[240:243], v[180:183]\n"
    "ds_read_u16 v187, v187\n"
    "ds_read_u16_d16_hi v203, v203\n"
    "v_mfma_f32_16x16x32_bf16 v[180:183], a[252:255], a[244:247], v[180:183]\n"
    "v_rcp_f16_sdwa v232, v234 dst_sel:WORD_1 dst_unused:UNUSED_PRESERVE src0_sel:WORD_1\n"
    "v_mfma_f32_16x16x32_bf16 v[96:99], a[208:211], v[192:195], v[96:99]\n"
    "v_fma_mix_f32 v234, v196, v232, 0 op_sel_hi:[1,1,0]\n"
    "v_fma_mix_f32 v232, v196, v232, 0 op_sel:[1,1,0] op_sel_hi:[1,1,0]\n"
    "v_mfma_f32_16x16x32_bf16 v[100:103], a[212:215], v[192:195], v[100:103]\n"
    "s_waitcnt lgkmcnt(6)\n"
    "ds_write2_b32 v248, v68, v69 offset0:0 offset1:20\n"
    "v_mfma_f32_16x16x32_bf16 v[112:115], a[216:219], v[192:195], v[112:115]\n"
    "s_nop 0\n"
    "v_cvt_pk_bf16_f32 v196, v234, v232\n"
    "v_or_b32_e64 v185, v185, v201\n"
    "v_mfma_f32_16x16x32_bf16 v[116:119], a[220:223], v[192:195], v[116:119]\n"
    "ds_write2_b32 v248, v70, v71 offset0:40 offset1:60\n"
    "v_mfma_f32_16x16x32_bf16 v[104:107], a[208:211], v[196:199], v[104:107]\n"
    "v_add_u32_e32 v248, 1360, v248\n"
    "v_lshlrev_b32_sdwa v201, 1, v171 dst_sel:DWORD dst_unused:UNUSED_PAD src0_sel:DWORD src1_sel:WORD_1\n"
    "v_mfma_f32_16x16x32_bf16 v[108:111], a[212:215], v[196:199], v[108:111]\n"
    "ds_read_b128 v[164:167], v244 offset:22528\n"
    "ds_read_u16 v188, v188\n"
    "v_mfma_f32_16x16x32_bf16 v[120:123], a[216:219], v[196:199], v[120:123]\n"
    "ds_write2_b32 v235, v88, v89 offset0:0 offset1:20\n"
    "v_mfma_f32_16x16x32_bf16 v[124:127], a[220:223], v[196:199], v[124:127]\n"
    "s_waitcnt lgkmcnt(7)\n"
    "v_or_b32_e32 v186, v186, v202\n"
    "v_lshlrev_b32_sdwa v202, 1, v173 dst_sel:DWORD dst_unused:UNUSED_PAD src0_sel:DWORD src1_sel:WORD_1\n"
    "v_mfma_f32_16x16x32_bf16 v[128:131], v[236:239], v[192:195], v[128:131]\n"
    "ds_read_u16_d16_hi v204, v204\n"
    "ds_write2_b32 v235, v90, v91 offset0:40 offset1:60\n"
    "v_mfma_f32_16x16x32_bf16 v[136:139], v[236:239], v[196:199], v[136:139]\n"
    "v_add_u32_e32 v235, -53008, v235\n"
    "v_cvt_pk_bf16_f32 v96, v96, v97\n"
    "v_cvt_pk_bf16_f32 v97, v98, v99\n"
    "v_cvt_pk_bf16_f32 v98, v100, v101\n"
    "ds_read_u16 v189, v189\n"
    "ds_write2_b32 v248, v72, v73 offset0:0 offset1:20\n"
    "s_waitcnt lgkmcnt(7)\n"
    "v_or_b32_e32 v187, v187, v203\n"
    "v_lshlrev_b32_sdwa v203, 1, v175 dst_sel:DWORD dst_unused:UNUSED_PAD src0_sel:DWORD src1_sel:WORD_1\n"
    "s_waitcnt lgkmcnt(6)\n"
    "v_mfma_f32_16x16x32_bf16 v[132:135], v[164:167], v[192:195], v[132:135]\n"
    "s_nop 0\n"
    "ds_read_u16_d16_hi v205, v205\n"
    "ds_write2_b32 v248, v74, v75 offset0:40 offset1:60\n"
    "v_mfma_f32_16x16x32_bf16 v[140:143], v[164:167], v[196:199], v[140:143]\n"
    "v_add_u32_e32 v248, 1360, v248\n"
    "v_cvt_pk_f16_f32 v192, v168, v169\n"
    "v_lshlrev_b32_sdwa v193, 1, v170 dst_sel:DWORD dst_unused:UNUSED_PAD src0_sel:DWORD src1_sel:WORD_1\n"
    "v_mfma_f32_16x16x32_bf16 v[168:171], a[96:99], a[224:227], 0\n"
    "ds_read_u16 v190, v190\n"
    "ds_write2_b32 v248, v76, v77 offset0:0 offset1:20\n"
    "v_mfma_f32_16x16x32_bf16 v[168:171], a[144:147], a[228:231], v[168:171]\n"
    "v_lshlrev_b32_sdwa v194, 1, v172 dst_sel:DWORD dst_unused:UNUSED_PAD src0_sel:DWORD src1_sel:WORD_1\n"
    "v_lshlrev_b32_sdwa v195, 1, v174 dst_sel:DWORD dst_unused:UNUSED_PAD src0_sel:DWORD src1_sel:WORD_1\n"
    "v_mfma_f32_16x16x32_bf16 v[168:171], a[56:59], a[232:235], v[168:171]\n"
    "s_waitcnt lgkmcnt(6)\n"
    "ds_read_u16_d16_hi v206, v206\n"
    "ds_write2_b32 v248, v78, v79 offset0:40 offset1:60\n"
    "v_mfma_f32_16x16x32_bf16 v[172:175], a[100:103], a[224:227], 0\n"
    "s_nop 0\n"
    "v_add_u32_e32 v248, 1360, v248\n"
    "v_cvt_pk_f16_f32 v196, v176, v177\n"
    "v_mfma_f32_16x16x32_bf16 v[172:175], a[148:151], a[228:231], v[172:175]\n"
    "ds_write2_b32 v248, v80, v81 offset0:0 offset1:20\n"
    "v_mfma_f32_16x16x32_bf16 v[172:175], a[60:63], a[232:235], v[172:175]\n"
    "v_lshlrev_b32_sdwa v197, 1, v178 dst_sel:DWORD dst_unused:UNUSED_PAD src0_sel:DWORD src1_sel:WORD_1\n"
    "v_lshlrev_b32_sdwa v198, 1, v180 dst_sel:DWORD dst_unused:UNUSED_PAD src0_sel:DWORD src1_sel:WORD_1\n"
    "v_lshlrev_b32_sdwa v199, 1, v182 dst_sel:DWORD dst_unused:UNUSED_PAD src0_sel:DWORD src1_sel:WORD_1\n"
    "v_or_b32_e64 v188, v188, v204\n"
    "ds_read_u16 v191, v191\n"
    "ds_write2_b32 v248, v82, v83 offset0:40 offset1:60\n"
    "v_mfma_f32_32x32x1_2b_f32 v[64:95], v254, v254, 0\n"
    "s_waitcnt lgkmcnt(7)\n"
    "v_add_u32_e32 v248, -57088, v248\n"
    "v_or_b32_e32 v189, v189, v205\n"
    "ds_read_u16_d16_hi v207, v207\n"
    "ds_read_u16 v193, v193\n"
    "v_lshlrev_b32_sdwa v205, 1, v179 dst_sel:DWORD dst_unused:UNUSED_PAD src0_sel:DWORD src1_sel:WORD_1\n"
    "v_cvt_pk_bf16_f32 v99, v102, v103\n"
    "buffer_store_dwordx4 v[96:99], v245, s[44:47], s57 offen offset:0 sc1\n"
    "ds_read_u16_d16_hi v201, v201\n"
    "v_mfma_f32_16x16x32_bf16 v[176:179], a[96:99], a[236:239], 0\n"
    "s_waitcnt lgkmcnt(5)\n"
    "ds_read_u16 v194, v194\n"
    "ds_read_u16_d16_hi v202, v202\n"
    "v_mfma_f32_16x16x32_bf16 v[176:179], a[144:147], a[240:243], v[176:179]\n"
    "s_nop 0\n"
    "ds_read_u16 v195, v195\n"
    "v_or_b32_e32 v190, v190, v206\n"
    "v_mfma_f32_16x16x32_bf16 v[176:179], a[56:59], a[244:247], v[176:179]\n"
    "v_lshlrev_b32_sdwa v206, 1, v181 dst_sel:DWORD dst_unused:UNUSED_PAD src0_sel:DWORD src1_sel:WORD_1\n"
    "v_cvt_pk_bf16_f32 v152, v112, v113\n"
    "v_mfma_f32_16x16x32_bf16 v[96:99], a[0:3], v[184:187], 0\n"
    "s_waitcnt lgkmcnt(6)\n"
    "ds_read_b128 v[236:239], v244 offset:0\n"
    "v_cvt_pk_bf16_f32 v153, v114, v115\n"
    "v_mfma_f32_16x16x32_bf16 v[100:103], a[4:7], v[184:187], 0\n"
    "s_barrier\n"
    "v_mfma_f32_16x16x32_bf16 v[112:115], a[8:11], v[184:187], 0\n"
    "s_waitcnt lgkmcnt(6)\n"
    "ds_read_u16_d16_hi v203, v203\n"
    "ds_read_b128 v[208:211], v250 offset:0\n"
    "v_cvt_pk_bf16_f32 v154, v116, v117\n"
    "v_cvt_pk_bf16_f32 v155, v118, v119\n"
    "v_cvt_pk_bf16_f32 v156, v128, v129\n"
    "v_cvt_pk_bf16_f32 v157, v130, v131\n"
    "v_or_b32_e32 v191, v191, v207\n"
    "v_mfma_f32_16x16x32_bf16 v[116:119], a[12:15], v[184:187], 0\n"
    "s_waitcnt lgkmcnt(6)\n"
    "ds_read_b128 v[212:215], v250 offset:320\n"
    "ds_read_b128 v[216:219], v251 offset:10752\n"
    "v_lshlrev_b32_sdwa v207, 1, v183 dst_sel:DWORD dst_unused:UNUSED_PAD src0_sel:DWORD src1_sel:WORD_1\n"
    "v_mfma_f32_16x16x32_bf16 v[180:183], a[100:103], a[236:239], 0\n"
    "ds_read_b128 v[220:223], v251 offset:11088\n"
    "ds_read_b128 v[164:167], v244 offset:2048\n"
    "v_mfma_f32_16x16x32_bf16 v[180:183], a[148:151], a[240:243], v[180:183]\n"
    "ds_read_u16 v197, v197\n"
    "ds_read_u16_d16_hi v205, v205\n"
    "v_mfma_f32_16x16x32_bf16 v[180:183], a[60:63], a[244:247], v[180:183]\n"
    "v_or_b32_e64 v193, v193, v201\n"
    "v_cvt_pk_bf16_f32 v158, v132, v133\n"
    "v_cvt_pk_bf16_f32 v159, v134, v135\n"
    "s_waitcnt lgkmcnt(10)\n"
    "v_or_b32_e32 v194, v194, v202\n"
    "v_cvt_pk_bf16_f32 v144, v104, v105\n"
    "v_cvt_pk_bf16_f32 v145, v106, v107\n"
    "v_mfma_f32_16x16x32_bf16 v[104:107], a[0:3], v[188:191], 0\n"
    "s_waitcnt lgkmcnt(8)\n"
    "ds_read_u16 v198, v198\n"
    "v_cvt_pk_bf16_f32 v146, v108, v109\n"
    "v_mfma_f32_16x16x32_bf16 v[128:131], v[236:239], v[184:187], 0\n"
    "s_nop 0\n"
    "ds_read_u16_d16_hi v206, v206\n"
    "v_cvt_pk_bf16_f32 v147, v110, v111\n"
    "v_mfma_f32_16x16x32_bf16 v[108:111], a[4:7], v[188:191], 0\n"
    "s_nop 0\n"
    "v_cvt_pk_bf16_f32 v148, v120, v121\n"
    "v_cvt_pk_bf16_f32 v149, v122, v123\n"
    "v_mfma_f32_16x16x32_bf16 v[120:123], a[8:11], v[188:191], 0\n"
    "s_waitcnt lgkmcnt(8)\n"
    "ds_read_u16 v199, v199\n"
    "v_cvt_pk_bf16_f32 v150, v124, v125\n"
    "v_cvt_pk_bf16_f32 v151, v126, v127\n"
    "v_or_b32_e32 v195, v195, v203\n"
    "v_pk_fma_f16 v232, v192, v192, s48\n"
    "v_mfma_f32_16x16x32_bf16 v[124:127], a[12:15], v[188:191], 0\n"
    "s_waitcnt lgkmcnt(7)\n"
    "ds_read_u16_d16_hi v207, v207\n"
    "v_cvt_pk_bf16_f32 v160, v136, v137\n"
    "v_smfmac_f32_16x16x64_bf16 v[32:35], v[216:219], v[208:215], v240\n"
    "s_waitcnt lgkmcnt(7)\n"
    "ds_read_b128 v[216:219], v252 offset:10752\n"
    "v_smfmac_f32_16x16x64_bf16 v[36:39], v[220:223], v[208:215], v240 abid:1\n"
    "s_waitcnt lgkmcnt(7)\n"
    "ds_read_b128 v[208:211], v250 offset:640\n"
    "v_mfma_f32_16x16x32_bf16 v[132:135], v[164:167], v[184:187], 0\n"
    "ds_read_b128 v[212:215], v250 offset:960\n"
    "v_cvt_pk_bf16_f32 v161, v138, v139\n"
    "v_cvt_pk_bf16_f32 v162, v140, v141\n"
    "v_cvt_pk_bf16_f32 v163, v142, v143\n"
    "v_pk_mul_f16 v232, v192, v232\n"
    "v_mfma_f32_16x16x32_bf16 v[136:139], v[236:239], v[188:191], 0\n"
    "ds_read_b128 v[220:223], v252 offset:11088\n"
    "v_exp_f16_e64 v234, v232\n"
    "v_mfma_f32_16x16x32_bf16 v[140:143], v[164:167], v[188:191], 0\n"
    "v_exp_f16_sdwa v234, v232 dst_sel:WORD_1 dst_unused:UNUSED_PRESERVE src0_sel:WORD_1\n"
    "s_waitcnt lgkmcnt(8)\n"
    "v_or_b32_e32 v197, v197, v205\n"
    "v_pk_fma_f16 v234, v234, s49, s49\n"
    "v_lshlrev_b32_sdwa v184, 1, v168 dst_sel:DWORD dst_unused:UNUSED_PAD src0_sel:DWORD src1_sel:WORD_1\n"
    "v_lshlrev_b32_sdwa v200, 1, v169 dst_sel:DWORD dst_unused:UNUSED_PAD src0_sel:DWORD src1_sel:WORD_1\n"
    "v_lshlrev_b32_sdwa v185, 1, v170 dst_sel:DWORD dst_unused:UNUSED_PAD src0_sel:DWORD src1_sel:WORD_1\n"
    "v_lshlrev_b32_sdwa v201, 1, v171 dst_sel:DWORD dst_unused:UNUSED_PAD src0_sel:DWORD src1_sel:WORD_1\n"
    "v_mfma_f32_16x16x32_bf16 v[168:171], a[104:107], a[224:227], 0\n"
    "s_nop 0\n"
    "ds_read_u16 v184, v184\n"
    "ds_read_u16_d16_hi v200, v200\n"
    "v_mfma_f32_16x16x32_bf16 v[168:171], a[152:155], a[228:231], v[168:171]\n"
    "s_waitcnt lgkmcnt(8)\n"
    "ds_read_u16 v185, v185\n"
    "v_lshlrev_b32_sdwa v186, 1, v172 dst_sel:DWORD dst_unused:UNUSED_PAD src0_sel:DWORD src1_sel:WORD_1\n"
    "v_lshlrev_b32_sdwa v202, 1, v173 dst_sel:DWORD dst_unused:UNUSED_PAD src0_sel:DWORD src1_sel:WORD_1\n"
    "v_or_b32_e64 v198, v198, v206\n"
    "v_lshlrev_b32_sdwa v187, 1, v174 dst_sel:DWORD dst_unused:UNUSED_PAD src0_sel:DWORD src1_sel:WORD_1\n"
    "v_lshlrev_b32_sdwa v203, 1, v175 dst_sel:DWORD dst_unused:UNUSED_PAD src0_sel:DWORD src1_sel:WORD_1\n"
    "v_rcp_f16_e32 v232, v234\n"
    "v_mfma_f32_16x16x32_bf16 v[168:171], a[64:67], a[232:235], v[168:171]\n"
    "ds_read_u16_d16_hi v201, v201\n"
    "ds_read_u16 v186, v186\n"
    "v_rcp_f16_sdwa v232, v234 dst_sel:WORD_1 dst_unused:UNUSED_PRESERVE src0_sel:WORD_1\n"
    "v_mfma_f32_16x16x32_bf16 v[172:175], a[108:111], a[224:227], 0\n"
    "s_waitcnt lgkmcnt(8)\n"
    "ds_read_u16_d16_hi v202, v202\n"
    "v_lshlrev_b32_sdwa v188, 1, v176 dst_sel:DWORD dst_unused:UNUSED_PAD src0_sel:DWORD src1_sel:WORD_1\n"
    "v_lshlrev_b32_sdwa v204, 1, v177 dst_sel:DWORD dst_unused:UNUSED_PAD src0_sel:DWORD src1_sel:WORD_1\n"
    "v_lshlrev_b32_sdwa v189, 1, v178 dst_sel:DWORD dst_unused:UNUSED_PAD src0_sel:DWORD src1_sel:WORD_1\n"
    "v_lshlrev_b32_sdwa v205, 1, v179 dst_sel:DWORD dst_unused:UNUSED_PAD src0_sel:DWORD src1_sel:WORD_1\n"
    "s_waitcnt lgkmcnt(7)\n"
    "v_smfmac_f32_16x16x64_bf16 v[32:35], v[216:219], v[208:215], v240\n"
    "s_waitcnt lgkmcnt(6)\n"
    "ds_read_b128 v[216:219], v251 offset:11424\n"
    "v_smfmac_f32_16x16x64_bf16 v[36:39], v[220:223], v[208:215], v240 abid:1\n"
    "ds_read_b128 v[208:211], v250 offset:80\n"
    "buffer_store_dwordx4 v[152:155], v245, s[44:47], s57 offen offset:64 sc1\n"
    "v_lshlrev_b32_sdwa v190, 1, v180 dst_sel:DWORD dst_unused:UNUSED_PAD src0_sel:DWORD src1_sel:WORD_1\n"
    "v_lshlrev_b32_sdwa v206, 1, v181 dst_sel:DWORD dst_unused:UNUSED_PAD src0_sel:DWORD src1_sel:WORD_1\n"
    "v_lshlrev_b32_sdwa v191, 1, v182 dst_sel:DWORD dst_unused:UNUSED_PAD src0_sel:DWORD src1_sel:WORD_1\n"
    "v_pk_add_f32 v[224:225], v[224:225], v[32:33]\n"
    "v_pk_add_f32 v[226:227], v[226:227], v[34:35]\n"
    "v_pk_fma_f32 v[228:229], v[32:33], v[32:33], v[228:229]\n"
    "v_pk_fma_f32 v[230:231], v[34:35], v[34:35], v[230:231]\n"
    "v_pk_add_f32 v[224:225], v[224:225], v[36:37]\n"
    "v_pk_add_f32 v[226:227], v[226:227], v[38:39]\n"
    "v_pk_fma_f32 v[228:229], v[36:37], v[36:37], v[228:229]\n"
    "v_pk_fma_f32 v[230:231], v[38:39], v[38:39], v[230:231]\n"
    "v_fma_mix_f32 v234, v192, v232, 0 op_sel_hi:[1,1,0]\n"
    "v_mfma_f32_16x16x32_bf16 v[172:175], a[156:159], a[228:231], v[172:175]\n"
    "s_waitcnt lgkmcnt(6)\n"
    "ds_read_b128 v[212:215], v250 offset:400\n"
    "ds_read_b128 v[220:223], v251 offset:11760\n"
    "v_mfma_f32_16x16x32_bf16 v[172:175], a[68:71], a[232:235], v[172:175]\n"
    "s_nop 0\n"
    "ds_read_b128 v[236:239], v244 offset:4096\n"
    "ds_read_u16 v187, v187\n"
    "v_or_b32_e64 v199, v199, v207\n"
    "v_lshlrev_b32_sdwa v207, 1, v183 dst_sel:DWORD dst_unused:UNUSED_PAD src0_sel:DWORD src1_sel:WORD_1\n"
    "v_fma_mix_f32 v232, v192, v232, 0 op_sel:[1,1,0] op_sel_hi:[1,1,0]\n"
    "v_mfma_f32_16x16x32_bf16 v[176:179], a[104:107], a[236:239], 0\n"
    "ds_read_u16_d16_hi v203, v203\n"
    "ds_read_b128 v[164:167], v244 offset:6144\n"
    "v_cvt_pk_bf16_f32 v192, v234, v232\n"
    "v_mfma_f32_16x16x32_bf16 v[176:179], a[152:155], a[240:243], v[176:179]\n"
    "ds_read_u16 v188, v188\n"
    "v_pk_fma_f16 v232, v196, v196, s48\n"
    "v_mfma_f32_16x16x32_bf16 v[176:179], a[64:67], a[244:247], v[176:179]\n"
    "s_waitcnt lgkmcnt(10)\n"
    "v_pk_mul_f16 v232, v196, v232\n"
    "v_or_b32_e32 v184, v184, v200\n"
    "v_mfma_f32_16x16x32_bf16 v[180:183], a[108:111], a[236:239], 0\n"
    "ds_read_u16_d16_hi v204, v204\n"
    "v_exp_f16_e64 v234, v232\n"
    "v_mfma_f32_16x16x32_bf16 v[180:183], a[156:159], a[240:243], v[180:183]\n"
    "s_waitcnt lgkmcnt(8)\n"
    "ds_read_u16 v189, v189\n"
    "v_exp_f16_sdwa v234, v232 dst_sel:WORD_1 dst_unused:UNUSED_PRESERVE src0_sel:WORD_1\n"
    "v_mfma_f32_16x16x32_bf16 v[180:183], a[68:71], a[244:247], v[180:183]\n"
    "s_waitcnt lgkmcnt(8)\n"
    "v_pk_fma_f16 v234, v234, s49, s49\n"
    "v_or_b32_e32 v185, v185, v201\n"
    "v_smfmac_f32_16x16x64_bf16 v[40:43], v[216:219], v[208:215], v240\n"
    "s_waitcnt lgkmcnt(7)\n"
    "ds_read_b128 v[216:219], v252 offset:11424\n"
    "ds_read_u16_d16_hi v205, v205\n"
    "v_smfmac_f32_16x16x64_bf16 v[44:47], v[220:223], v[208:215], v240 abid:1\n"
    "v_rcp_f16_e64 v232, v234\n"
    "v_mfma_f32_16x16x32_bf16 v[96:99], a[16:19], v[192:195], v[96:99]\n"
    "ds_read_b128 v[208:211], v250 offset:720\n"
    "v_rcp_f16_sdwa v232, v234 dst_sel:WORD_1 dst_unused:UNUSED_PRESERVE src0_sel:WORD_1\n"
    "v_mfma_f32_16x16x32_bf16 v[100:103], a[20:23], v[192:195], v[100:103]\n"
    "ds_read_b128 v[212:215], v250 offset:1040\n"
    "ds_read_b128 v[220:223], v252 offset:11760\n"
    "v_mfma_f32_16x16x32_bf16 v[112:115], a[24:27], v[192:195], v[112:115]\n"
    "s_nop 0\n"
    "v_fma_mix_f32 v234, v196, v232, 0 op_sel_hi:[1,1,0]\n"
    "v_fma_mix_f32 v232, v196, v232, 0 op_sel:[1,1,0] op_sel_hi:[1,1,0]\n"
    "v_mfma_f32_16x16x32_bf16 v[116:119], a[28:31], v[192:195], v[116:119]\n"
    "s_waitcnt lgkmcnt(8)\n"
    "ds_read_u16 v190, v190\n"
    "v_cvt_pk_bf16_f32 v196, v234, v232\n"
    "v_mfma_f32_16x16x32_bf16 v[128:131], v[236:239], v[192:195], v[128:131]\n"
    "ds_read_u16_d16_hi v206, v206\n"
    "v_lshlrev_b32_sdwa v201, 1, v171 dst_sel:DWORD dst_unused:UNUSED_PAD src0_sel:DWORD src1_sel:WORD_1\n"
    "v_mfma_f32_16x16x32_bf16 v[132:135], v[164:167], v[192:195], v[132:135]\n"
    "v_cvt_pk_f16_f32 v192, v168, v169\n"
    "v_lshlrev_b32_sdwa v193, 1, v170 dst_sel:DWORD dst_unused:UNUSED_PAD src0_sel:DWORD src1_sel:WORD_1\n"
    "v_mfma_f32_16x16x32_bf16 v[104:107], a[16:19], v[196:199], v[104:107]\n"
    "s_nop 0\n"
    "ds_read_u16 v191, v191\n"
    "buffer_store_dwordx4 v[156:159], v245, s[44:47], s57 offen offset:128 sc1\n"
    "v_mfma_f32_16x16x32_bf16 v[108:111], a[20:23], v[196:199], v[108:111]\n"
    "s_waitcnt lgkmcnt(9)\n"
    "ds_read_u16_d16_hi v207, v207\n"
    "v_lshlrev_b32_sdwa v194, 1, v172 dst_sel:DWORD dst_unused:UNUSED_PAD src0_sel:DWORD src1_sel:WORD_1\n"
    "v_mfma_f32_16x16x32_bf16 v[120:123], a[24:27], v[196:199], v[120:123]\n"
    "v_or_b32_e64 v186, v186, v202\n"
    "v_lshlrev_b32_sdwa v202, 1, v173 dst_sel:DWORD dst_unused:UNUSED_PAD src0_sel:DWORD src1_sel:WORD_1\n"
    "v_mfma_f32_16x16x32_bf16 v[124:127], a[28:31], v[196:199], v[124:127]\n"
    "ds_read_u16 v193, v193\n"
    "v_lshlrev_b32_sdwa v195, 1, v174 dst_sel:DWORD dst_unused:UNUSED_PAD src0_sel:DWORD src1_sel:WORD_1\n"
    "v_mfma_f32_16x16x32_bf16 v[136:139], v[236:239], v[196:199], v[136:139]\n"
    "ds_read_b128 a[252:255], v244 offset:34816\n"
    "v_or_b32_e32 v187, v187, v203\n"
    "v_mfma_f32_16x16x32_bf16 v[140:143], v[164:167], v[196:199], v[140:143]\n"
    "s_waitcnt lgkmcnt(8)\n"
    "ds_read_u16_d16_hi v201, v201\n"
    "v_lshlrev_b32_sdwa v203, 1, v175 dst_sel:DWORD dst_unused:UNUSED_PAD src0_sel:DWORD src1_sel:WORD_1\n"
    "v_mfma_f32_16x16x32_bf16 v[168:171], a[112:115], a[224:227], 0\n"
    "s_waitcnt lgkmcnt(8)\n"
    "v_cvt_pk_f16_f32 v196, v176, v177\n"
    "v_lshlrev_b32_sdwa v197, 1, v178 dst_sel:DWORD dst_unused:UNUSED_PAD src0_sel:DWORD src1_sel:WORD_1\n"
    "v_smfmac_f32_16x16x64_bf16 v[40:43], v[216:219], v[208:215], v240\n"
    "s_waitcnt lgkmcnt(7)\n"
    "ds_read_b128 v[216:219], v251 offset:12096\n"
    "v_smfmac_f32_16x16x64_bf16 v[44:47], v[220:223], v[208:215], v240 abid:1\n"
    "ds_read_b128 v[208:211], v250 offset:160\n"
    "v_mfma_f32_16x16x32_bf16 v[168:171], a[160:163], a[228:231], v[168:171]\n"
    "ds_read_b128 v[212:215], v250 offset:480\n"
    "v_lshlrev_b32_sdwa v198, 1, v180 dst_sel:DWORD dst_unused:UNUSED_PAD src0_sel:DWORD src1_sel:WORD_1\n"
    "v_mfma_f32_16x16x32_bf16 v[168:171], a[72:75], a[232:235], v[168:171]\n"
    "ds_read_b128 v[220:223], v251 offset:12432\n"
    "v_lshlrev_b32_sdwa v199, 1, v182 dst_sel:DWORD dst_unused:UNUSED_PAD src0_sel:DWORD src1_sel:WORD_1\n"
    "v_or_b32_e64 v188, v188, v204\n"
    "v_or_b32_e64 v189, v189, v205\n"
    "v_lshlrev_b32_sdwa v205, 1, v179 dst_sel:DWORD dst_unused:UNUSED_PAD src0_sel:DWORD src1_sel:WORD_1\n"
    "v_pk_add_f32 v[224:225], v[224:225], v[40:41]\n"
    "v_pk_add_f32 v[226:227], v[226:227], v[42:43]\n"
    "v_pk_fma_f32 v[228:229], v[40:41], v[40:41], v[228:229]\n"
    "v_pk_fma_f32 v[230:231], v[42:43], v[42:43], v[230:231]\n"
    "v_pk_add_f32 v[224:225], v[224:225], v[44:45]\n"
    "v_pk_add_f32 v[226:227], v[226:227], v[46:47]\n"
    "v_pk_fma_f32 v[228:229], v[44:45], v[44:45], v[228:229]\n"
    "v_pk_fma_f32 v[230:231], v[46:47], v[46:47], v[230:231]\n"
    "v_mfma_f32_16x16x32_bf16 v[172:175], a[116:119], a[224:227], 0\n"
    "s_waitcnt lgkmcnt(8)\n"
    "ds_read_u16 v194, v194\n"
    "ds_read_u16_d16_hi v202, v202\n"
    "v_mfma_f32_16x16x32_bf16 v[172:175], a[164:167], a[228:231], v[172:175]\n"
    "s_waitcnt lgkmcnt(9)\n"
    "ds_read_u16 v195, v195\n"
    "v_or_b32_e64 v190, v190, v206\n"
    "v_mfma_f32_16x16x32_bf16 v[176:179], a[112:115], a[236:239], 0\n"
    "v_lshlrev_b32_sdwa v206, 1, v181 dst_sel:DWORD dst_unused:UNUSED_PAD src0_sel:DWORD src1_sel:WORD_1\n"
    "v_or_b32_e32 v191, v191, v207\n"
    "v_mfma_f32_16x16x32_bf16 v[176:179], a[160:163], a[240:243], v[176:179]\n"
    "s_waitcnt lgkmcnt(8)\n"
    "ds_read_b128 v[236:239], v244 offset:8192\n"
    "ds_read_u16_d16_hi v203, v203\n"
    "v_mfma_f32_16x16x32_bf16 v[172:175], a[252:255], a[232:235], v[172:175]\n"
    "v_lshlrev_b32_sdwa v207, 1, v183 dst_sel:DWORD dst_unused:UNUSED_PAD src0_sel:DWORD src1_sel:WORD_1\n"
    "v_pk_fma_f16 v232, v192, v192, s48\n"
    "v_mfma_f32_16x16x32_bf16 v[176:179], a[72:75], a[244:247], v[176:179]\n"
    "ds_read_b128 v[164:167], v244 offset:10240\n"
    "v_pk_mul_f16 v232, v192, v232\n"
    "v_mfma_f32_16x16x32_bf16 v[180:183], a[116:119], a[236:239], 0\n"
    "s_waitcnt lgkmcnt(8)\n"
    "ds_read_u16 v197, v197\n"
    "v_or_b32_e32 v193, v193, v201\n"
    "v_mfma_f32_16x16x32_bf16 v[180:183], a[164:167], a[240:243], v[180:183]\n"
    "s_waitcnt lgkmcnt(8)\n"
    "v_exp_f16_e64 v234, v232\n"
    "v_smfmac_f32_16x16x64_bf16 v[48:51], v[216:219], v[208:215], v240\n"
    "s_waitcnt lgkmcnt(7)\n"
    "ds_read_b128 v[216:219], v252 offset:12096\n"
    "v_smfmac_f32_16x16x64_bf16 v[52:55], v[220:223], v[208:215], v240 abid:1\n"
    "ds_read_b128 v[208:211], v250 offset:800\n"
    "buffer_store_dwordx4 v[144:147], v245, s[44:47], s88 offen offset:0 sc1\n"
    "v_mfma_f32_16x16x32_bf16 v[180:183], a[252:255], a[244:247], v[180:183]\n"
    "ds_read_b128 v[212:215], v250 offset:1120\n"
    "ds_read_b128 v[220:223], v252 offset:12432\n"
    "v_mfma_f32_16x16x32_bf16 v[96:99], a[32:35], v[184:187], v[96:99]\n"
    "s_nop 0\n"
    "ds_read_u16_d16_hi v205, v205\n"
    "v_exp_f16_sdwa v234, v232 dst_sel:WORD_1 dst_unused:UNUSED_PRESERVE src0_sel:WORD_1\n"
    "v_mfma_f32_16x16x32_bf16 v[100:103], a[36:39], v[184:187], v[100:103]\n"
    "s_waitcnt lgkmcnt(10)\n"
    "ds_read_u16 v198, v198\n"
    "v_or_b32_e32 v194, v194, v202\n"
    "v_mfma_f32_16x16x32_bf16 v[112:115], a[40:43], v[184:187], v[112:115]\n"
    "s_waitcnt lgkmcnt(9)\n"
    "v_pk_fma_f16 v234, v234, s49, s49\n"
    "v_lshlrev_b32_sdwa v200, 1, v169 dst_sel:DWORD dst_unused:UNUSED_PAD src0_sel:DWORD src1_sel:WORD_1\n"
    "v_mfma_f32_16x16x32_bf16 v[116:119], a[44:47], v[184:187], v[116:119]\n"
    "ds_read_u16_d16_hi v206, v206\n"
    "v_lshlrev_b32_sdwa v201, 1, v171 dst_sel:DWORD dst_unused:UNUSED_PAD src0_sel:DWORD src1_sel:WORD_1\n"
    "v_mfma_f32_16x16x32_bf16 v[128:131], v[236:239], v[184:187], v[128:131]\n"
    "v_lshlrev_b32_sdwa v202, 1, v173 dst_sel:DWORD dst_unused:UNUSED_PAD src0_sel:DWORD src1_sel:WORD_1\n"
    "v_rcp_f16_e32 v232, v234\n"
    "v_mfma_f32_16x16x32_bf16 v[104:107], a[32:35], v[188:191], v[104:107]\n"
    "s_waitcnt lgkmcnt(8)\n"
    "ds_read_u16 v199, v199\n"
    "v_or_b32_e64 v195, v195, v203\n"
    "v_mfma_f32_16x16x32_bf16 v[132:135], v[164:167], v[184:187], v[132:135]\n"
    "ds_read_u16_d16_hi v207, v207\n"
    "v_lshlrev_b32_sdwa v184, 1, v168 dst_sel:DWORD dst_unused:UNUSED_PAD src0_sel:DWORD src1_sel:WORD_1\n"
    "v_mfma_f32_16x16x32_bf16 v[108:111], a[36:39], v[188:191], v[108:111]\n"
    "v_lshlrev_b32_sdwa v185, 1, v170 dst_sel:DWORD dst_unused:UNUSED_PAD src0_sel:DWORD src1_sel:WORD_1\n"
    "v_lshlrev_b32_sdwa v186, 1, v172 dst_sel:DWORD dst_unused:UNUSED_PAD src0_sel:DWORD src1_sel:WORD_1\n"
    "v_mfma_f32_16x16x32_bf16 v[120:123], a[40:43], v[188:191], v[120:123]\n"
    "ds_read_u16 v184, v184\n"
    "v_lshlrev_b32_sdwa v187, 1, v174 dst_sel:DWORD dst_unused:UNUSED_PAD src0_sel:DWORD src1_sel:WORD_1\n"
    "v_mfma_f32_16x16x32_bf16 v[124:127], a[44:47], v[188:191], v[124:127]\n"
    "s_waitcnt lgkmcnt(7)\n"
    "v_lshlrev_b32_sdwa v203, 1, v175 dst_sel:DWORD dst_unused:UNUSED_PAD src0_sel:DWORD src1_sel:WORD_1\n"
    "v_rcp_f16_sdwa v232, v234 dst_sel:WORD_1 dst_unused:UNUSED_PRESERVE src0_sel:WORD_1\n"
    "v_smfmac_f32_16x16x64_bf16 v[48:51], v[216:219], v[208:215], v240\n"
    "s_waitcnt lgkmcnt(6)\n"
    "ds_read_b128 v[216:219], v251 offset:12768\n"
    "ds_read_b128 a[252:255], v244 offset:38912\n"
    "v_smfmac_f32_16x16x64_bf16 v[52:55], v[220:223], v[208:215], v240 abid:1\n"
    "ds_read_b128 v[208:211], v250 offset:240\n"
    "v_mfma_f32_16x16x32_bf16 v[136:139], v[236:239], v[188:191], v[136:139]\n"
    "ds_read_b128 v[212:215], v250 offset:560\n"
    "v_lshlrev_b32_sdwa v204, 1, v177 dst_sel:DWORD dst_unused:UNUSED_PAD src0_sel:DWORD src1_sel:WORD_1\n"
    "v_fma_mix_f32 v234, v192, v232, 0 op_sel_hi:[1,1,0]\n"
    "v_fma_mix_f32 v232, v192, v232, 0 op_sel:[1,1,0] op_sel_hi:[1,1,0]\n"
    "ds_read_b128 v[220:223], v251 offset:13104\n"
    "v_pk_add_f32 v[224:225], v[224:225], v[48:49]\n"
    "v_pk_add_f32 v[226:227], v[226:227], v[50:51]\n"
    "v_pk_fma_f32 v[228:229], v[48:49], v[48:49], v[228:229]\n"
    "v_pk_fma_f32 v[230:231], v[50:51], v[50:51], v[230:231]\n"
    "v_pk_add_f32 v[224:225], v[224:225], v[52:53]\n"
    "v_pk_add_f32 v[226:227], v[226:227], v[54:55]\n"
    "v_pk_fma_f32 v[228:229], v[52:53], v[52:53], v[228:229]\n"
    "v_pk_fma_f32 v[230:231], v[54:55], v[54:55], v[230:231]\n"
    "v_mfma_f32_16x16x32_bf16 v[140:143], v[164:167], v[188:191], v[140:143]\n"
    "s_waitcnt lgkmcnt(8)\n"
    "ds_read_u16_d16_hi v200, v200\n"
    "ds_read_u16 v185, v185\n"
    "v_mfma_f32_16x16x32_bf16 v[168:171], a[120:123], a[224:227], 0\n"
    "v_or_b32_e32 v197, v197, v205\n"
    "v_or_b32_e64 v198, v198, v206\n"
    "v_mfma_f32_16x16x32_bf16 v[168:171], a[168:171], a[228:231], v[168:171]\n"
    "ds_read_u16_d16_hi v201, v201\n"
    "v_lshlrev_b32_sdwa v188, 1, v176 dst_sel:DWORD dst_unused:UNUSED_PAD src0_sel:DWORD src1_sel:WORD_1\n"
    "v_mfma_f32_16x16x32_bf16 v[168:171], a[76:79], a[232:235], v[168:171]\n"
    "ds_read_u16 v186, v186\n"
    "v_lshlrev_b32_sdwa v189, 1, v178 dst_sel:DWORD dst_unused:UNUSED_PAD src0_sel:DWORD src1_sel:WORD_1\n"
    "v_mfma_f32_16x16x32_bf16 v[172:175], a[124:127], a[224:227], 0\n"
    "s_waitcnt lgkmcnt(10)\n"
    "buffer_store_dwordx4 v[148:151], v245, s[44:47], s88 offen offset:64 sc1\n"
    "ds_read_u16_d16_hi v202, v202\n"
    "v_mfma_f32_16x16x32_bf16 v[172:175], a[172:175], a[228:231], v[172:175]\n"
    "s_waitcnt lgkmcnt(8)\n"
    "v_lshlrev_b32_sdwa v205, 1, v179 dst_sel:DWORD dst_unused:UNUSED_PAD src0_sel:DWORD src1_sel:WORD_1\n"
    "v_lshlrev_b32_sdwa v190, 1, v180 dst_sel:DWORD dst_unused:UNUSED_PAD src0_sel:DWORD src1_sel:WORD_1\n"
    "v_mfma_f32_16x16x32_bf16 v[172:175], a[252:255], a[232:235], v[172:175]\n"
    "ds_read_b128 v[236:239], v244 offset:12288\n"
    "v_lshlrev_b32_sdwa v206, 1, v181 dst_sel:DWORD dst_unused:UNUSED_PAD src0_sel:DWORD src1_sel:WORD_1\n"
    "v_mfma_f32_16x16x32_bf16 v[176:179], a[120:123], a[236:239], 0\n"
    "s_waitcnt lgkmcnt(7)\n"
    "ds_read_u16 v187, v187\n"
    "v_lshlrev_b32_sdwa v191, 1, v182 dst_sel:DWORD dst_unused:UNUSED_PAD src0_sel:DWORD src1_sel:WORD_1\n"
    "v_smfmac_f32_16x16x64_bf16 v[56:59], v[216:219], v[208:215], v240\n"
    "s_waitcnt lgkmcnt(7)\n"
    "ds_read_b128 v[216:219], v252 offset:12768\n"
    "v_smfmac_f32_16x16x64_bf16 v[60:63], v[220:223], v[208:215], v240 abid:1\n"
    "ds_read_b128 v[208:211], v250 offset:880\n"
    "v_or_b32_e32 v199, v199, v207\n"
    "v_lshlrev_b32_sdwa v207, 1, v183 dst_sel:DWORD dst_unused:UNUSED_PAD src0_sel:DWORD src1_sel:WORD_1\n"
    "v_cvt_pk_bf16_f32 v192, v234, v232\n"
    "v_mfma_f32_16x16x32_bf16 v[176:179], a[168:171], a[240:243], v[176:179]\n"
    "s_waitcnt lgkmcnt(8)\n"
    "ds_read_b128 v[212:215], v250 offset:1200\n"
    "v_pk_fma_f16 v232, v196, v196, s48\n"
    "v_mfma_f32_16x16x32_bf16 v[176:179], a[76:79], a[244:247], v[176:179]\n"
    "ds_read_b128 v[220:223], v252 offset:13104\n"
    "v_pk_mul_f16 v232, v196, v232\n"
    "v_mfma_f32_16x16x32_bf16 v[180:183], a[124:127], a[236:239], 0\n"
    "ds_read_u16_d16_hi v203, v203\n"
    "v_exp_f16_e32 v234, v232\n"
    "v_mfma_f32_16x16x32_bf16 v[180:183], a[172:175], a[240:243], v[180:183]\n"
    "ds_read_b128 v[164:167], v244 offset:14336\n"
    "v_exp_f16_sdwa v234, v232 dst_sel:WORD_1 dst_unused:UNUSED_PRESERVE src0_sel:WORD_1\n"
    "v_mfma_f32_16x16x32_bf16 v[180:183], a[252:255], a[244:247], v[180:183]\n"
    "s_waitcnt lgkmcnt(10)\n"
    "ds_read_u16 v188, v188\n"
    "v_pk_fma_f16 v234, v234, s49, s49\n"
    "v_mfma_f32_16x16x32_bf16 v[96:99], a[176:179], v[192:195], v[96:99]\n"
    "v_rcp_f16_e32 v232, v234\n"
    "v_mfma_f32_16x16x32_bf16 v[100:103], a[180:183], v[192:195], v[100:103]\n"
    "ds_read_u16_d16_hi v204, v204\n"
    "v_rcp_f16_sdwa v232, v234 dst_sel:WORD_1 dst_unused:UNUSED_PRESERVE src0_sel:WORD_1\n"
    "v_mfma_f32_16x16x32_bf16 v[112:115], a[184:187], v[192:195], v[112:115]\n"
    "s_waitcnt lgkmcnt(9)\n"
    "ds_read_u16 v189, v189\n"
    "v_fma_mix_f32 v234, v196, v232, 0 op_sel_hi:[1,1,0]\n"
    "v_mfma_f32_16x16x32_bf16 v[116:119], a[188:191], v[192:195], v[116:119]\n"
    "v_fma_mix_f32 v232, v196, v232, 0 op_sel:[1,1,0] op_sel_hi:[1,1,0]\n"
    "v_or_b32_e64 v184, v184, v200\n"
    "v_mfma_f32_16x16x32_bf16 v[128:131], v[236:239], v[192:195], v[128:131]\n"
    "ds_read_u16_d16_hi v205, v205\n"
    "v_cvt_pk_bf16_f32 v196, v234, v232\n"
    "v_or_b32_e64 v185, v185, v201\n"
    "v_lshlrev_b32_sdwa v201, 1, v171 dst_sel:DWORD dst_unused:UNUSED_PAD src0_sel:DWORD src1_sel:WORD_1\n"
    "v_mfma_f32_16x16x32_bf16 v[104:107], a[176:179], v[196:199], v[104:107]\n"
    "ds_read_u16 v190, v190\n"
    "v_or_b32_e64 v186, v186, v202\n"
    "v_mfma_f32_16x16x32_bf16 v[108:111], a[180:183], v[196:199], v[108:111]\n"
    "s_waitcnt lgkmcnt(8)\n"
    "v_lshlrev_b32_sdwa v202, 1, v173 dst_sel:DWORD dst_unused:UNUSED_PAD src0_sel:DWORD src1_sel:WORD_1\n"
    "v_smfmac_f32_16x16x64_bf16 v[56:59], v[216:219], v[208:215], v240\n"
    "s_waitcnt lgkmcnt(7)\n"
    "ds_read_b128 v[216:219], v251 offset:21504\n"
    "v_smfmac_f32_16x16x64_bf16 v[60:63], v[220:223], v[208:215], v240 abid:1\n"
    "ds_read_b128 v[208:211], v250 offset:51648\n"
    "v_mfma_f32_16x16x32_bf16 v[120:123], a[184:187], v[196:199], v[120:123]\n"
    "s_waitcnt lgkmcnt(7)\n"
    "ds_read_b128 v[212:215], v250 offset:51968\n"
    "v_or_b32_e32 v187, v187, v203\n"
    "v_lshlrev_b32_sdwa v203, 1, v175 dst_sel:DWORD dst_unused:UNUSED_PAD src0_sel:DWORD src1_sel:WORD_1\n"
    "v_mfma_f32_16x16x32_bf16 v[132:135], v[164:167], v[192:195], v[132:135]\n"
    "ds_read_b128 v[220:223], v251 offset:21840\n"
    "buffer_store_dwordx4 v[160:163], v245, s[44:47], s88 offen offset:128 sc1\n"
    "v_pk_add_f32 v[224:225], v[224:225], v[56:57]\n"
    "v_pk_add_f32 v[226:227], v[226:227], v[58:59]\n"
    "v_pk_fma_f32 v[228:229], v[56:57], v[56:57], v[228:229]\n"
    "v_pk_fma_f32 v[230:231], v[58:59], v[58:59], v[230:231]\n"
    "v_pk_add_f32 v[224:225], v[224:225], v[60:61]\n"
    "v_pk_add_f32 v[226:227], v[226:227], v[62:63]\n"
    "v_pk_fma_f32 v[228:229], v[60:61], v[60:61], v[228:229]\n"
    "v_pk_fma_f32 v[230:231], v[62:63], v[62:63], v[230:231]\n"
    "v_mfma_f32_16x16x32_bf16 v[124:127], a[188:191], v[196:199], v[124:127]\n"
    "s_waitcnt lgkmcnt(7)\n"
    "ds_read_b128 a[224:227], v243 offset:0\n"
    "ds_read_u16_d16_hi v206, v206\n"
    "v_mfma_f32_16x16x32_bf16 v[136:139], v[236:239], v[196:199], v[136:139]\n"
    "s_nop 0\n"
    "ds_read_b128 a[228:231], v243 offset:1024\n"
    "ds_read_u16 v191, v191\n"
    "v_mfma_f32_16x16x32_bf16 v[140:143], v[164:167], v[196:199], v[140:143]\n"
    "ds_read_b128 a[232:235], v243 offset:2048\n"
    "v_cvt_pk_f16_f32 v192, v168, v169\n"
    "v_lshlrev_b32_sdwa v193, 1, v170 dst_sel:DWORD dst_unused:UNUSED_PAD src0_sel:DWORD src1_sel:WORD_1\n"
    "v_lshlrev_b32_sdwa v194, 1, v172 dst_sel:DWORD dst_unused:UNUSED_PAD src0_sel:DWORD src1_sel:WORD_1\n"
    "v_lshlrev_b32_sdwa v195, 1, v174 dst_sel:DWORD dst_unused:UNUSED_PAD src0_sel:DWORD src1_sel:WORD_1\n"
    "v_cvt_pk_f16_f32 v196, v176, v177\n"
    "v_lshlrev_b32_sdwa v197, 1, v178 dst_sel:DWORD dst_unused:UNUSED_PAD src0_sel:DWORD src1_sel:WORD_1\n"
    "v_lshlrev_b32_sdwa v198, 1, v180 dst_sel:DWORD dst_unused:UNUSED_PAD src0_sel:DWORD src1_sel:WORD_1\n"
    "v_lshlrev_b32_sdwa v199, 1, v182 dst_sel:DWORD dst_unused:UNUSED_PAD src0_sel:DWORD src1_sel:WORD_1\n"
    "v_or_b32_e64 v188, v188, v204\n"
    "ds_read_u16_d16_hi v207, v207\n"
    "ds_read_b128 a[236:239], v243 offset:3072\n"
    "s_waitcnt lgkmcnt(9)\n"
    "v_or_b32_e32 v189, v189, v205\n"
    "ds_read_b128 a[240:243], v243 offset:4096\n"
    "ds_read_b128 a[244:247], v243 offset:5120\n"
    "s_waitcnt lgkmcnt(10)\n"
    "v_smfmac_f32_16x16x64_bf16 v[64:67], v[216:219], v[208:215], v240\n"
    "s_waitcnt lgkmcnt(9)\n"
    "ds_read_b128 v[216:219], v252 offset:21504\n"
    "buffer_load_dwordx4 v[152:155], v245, s[40:43], s58 offen offset:64\n"
    "buffer_load_dwordx4 v[144:147], v245, s[40:43], s58 offen offset:0\n"
    "v_smfmac_f32_16x16x64_bf16 v[68:71], v[220:223], v[208:215], v240 abid:1\n"
    "s_waitcnt lgkmcnt(9)\n"
    "ds_read_b128 v[208:211], v250 offset:52288\n"
    "ds_read_b128 v[212:215], v250 offset:52608\n"
    "v_mfma_f32_16x16x32_bf16 v[168:171], a[80:83], a[224:227], 0\n"
    "s_nop 0\n"
    "v_lshlrev_b32_sdwa v205, 1, v179 dst_sel:DWORD dst_unused:UNUSED_PAD src0_sel:DWORD src1_sel:WORD_1\n"
    "v_lshlrev_b32_e32 v254, 3, v244\n"
    "v_mfma_f32_16x16x32_bf16 v[172:175], a[84:87], a[224:227], 0\n"
    "s_waitcnt lgkmcnt(9)\n"
    "ds_read_b128 v[220:223], v252 offset:21840\n"
    "ds_read_u16 v193, v193\n"
    "v_mfma_f32_16x16x32_bf16 v[168:171], a[128:131], a[228:231], v[168:171]\n"
    "v_or_b32_e64 v190, v190, v206\n"
    "v_lshlrev_b32_sdwa v206, 1, v181 dst_sel:DWORD dst_unused:UNUSED_PAD src0_sel:DWORD src1_sel:WORD_1\n"
    "v_mfma_f32_16x16x32_bf16 v[172:175], a[132:135], a[228:231], v[172:175]\n"
    "ds_read_b128 a[252:255], v244 offset:32768\n"
    "v_pk_fma_f16 v232, v192, v192, s48\n"
    "v_mfma_f32_16x16x32_bf16 v[96:99], a[192:195], v[184:187], v[96:99]\n"
    "s_waitcnt lgkmcnt(10)\n"
    "ds_read_u16_d16_hi v201, v201\n"
    "v_pk_mul_f16 v232, v192, v232\n"
    "v_mfma_f32_16x16x32_bf16 v[168:171], a[48:51], a[232:235], v[168:171]\n"
    "v_exp_f16_e32 v234, v232\n"
    "v_mfma_f32_16x16x32_bf16 v[100:103], a[196:199], v[184:187], v[100:103]\n"
    "ds_read_u16 v194, v194\n"
    "v_exp_f16_sdwa v234, v232 dst_sel:WORD_1 dst_unused:UNUSED_PRESERVE src0_sel:WORD_1\n"
    "v_mfma_f32_16x16x32_bf16 v[112:115], a[200:203], v[184:187], v[112:115]\n"
    "s_waitcnt lgkmcnt(9)\n"
    "v_or_b32_e32 v191, v191, v207\n"
    "v_lshlrev_b32_sdwa v207, 1, v183 dst_sel:DWORD dst_unused:UNUSED_PAD src0_sel:DWORD src1_sel:WORD_1\n"
    "v_mfma_f32_16x16x32_bf16 v[176:179], a[80:83], a[236:239], 0\n"
    "s_nop 0\n"
    "ds_read_u16_d16_hi v202, v202\n"
    "v_pk_fma_f16 v234, v234, s49, s49\n"
    "v_mfma_f32_16x16x32_bf16 v[176:179], a[128:131], a[240:243], v[176:179]\n"
    "s_waitcnt lgkmcnt(8)\n"
    "ds_read_u16 v195, v195\n"
    "buffer_load_dwordx4 v[156:159], v245, s[40:43], s89 offen offset:64\n"
    "buffer_load_dwordx4 v[148:151], v245, s[40:43], s89 offen offset:0\n"
    "v_mfma_f32_16x16x32_bf16 v[176:179], a[48:51], a[244:247], v[176:179]\n"
    "v_lshlrev_b32_sdwa v200, 1, v169 dst_sel:DWORD dst_unused:UNUSED_PAD src0_sel:DWORD src1_sel:WORD_1\n"
    "v_rcp_f16_e32 v232, v234\n"
    "v_mfma_f32_16x16x32_bf16 v[180:183], a[84:87], a[236:239], 0\n"
    "s_waitcnt lgkmcnt(7)\n"
    "ds_read_b128 v[236:239], v244 offset:16384\n"
    "ds_read_u16_d16_hi v203, v203\n"
    "v_smfmac_f32_16x16x64_bf16 v[64:67], v[216:219], v[208:215], v240\n"
    "s_waitcnt lgkmcnt(8)\n"
    "v_rcp_f16_sdwa v232, v234 dst_sel:WORD_1 dst_unused:UNUSED_PRESERVE src0_sel:WORD_1\n"
    "v_smfmac_f32_16x16x64_bf16 v[68:71], v[220:223], v[208:215], v240 abid:1\n"
    "ds_read_b128 v[208:211], v250 offset:51728\n"
    "v_mfma_f32_16x16x32_bf16 v[180:183], a[132:135], a[240:243], v[180:183]\n"
    "s_waitcnt lgkmcnt(7)\n"
    "ds_read_b128 v[212:215], v250 offset:52048\n"
    "v_lshlrev_b32_sdwa v204, 1, v177 dst_sel:DWORD dst_unused:UNUSED_PAD src0_sel:DWORD src1_sel:WORD_1\n"
    "v_fma_mix_f32 v234, v192, v232, 0 op_sel_hi:[1,1,0]\n"
    "ds_read_b128 v[216:219], v251 offset:22176\n"
    "v_pk_add_f32 v[224:225], v[224:225], v[64:65]\n"
    "v_pk_add_f32 v[226:227], v[226:227], v[66:67]\n"
    "v_pk_fma_f32 v[228:229], v[64:65], v[64:65], v[228:229]\n"
    "v_pk_fma_f32 v[230:231], v[66:67], v[66:67], v[230:231]\n"
    "v_pk_add_f32 v[224:225], v[224:225], v[68:69]\n"
    "v_pk_add_f32 v[226:227], v[226:227], v[70:71]\n"
    "v_pk_fma_f32 v[228:229], v[68:69], v[68:69], v[228:229]\n"
    "v_pk_fma_f32 v[230:231], v[70:71], v[70:71], v[230:231]\n"
    "v_mfma_f32_16x16x32_bf16 v[172:175], a[252:255], a[232:235], v[172:175]\n"
    "s_waitcnt lgkmcnt(8)\n"
    "ds_read_b128 v[220:223], v251 offset:22512\n"
    "ds_read_b128 v[164:167], v244 offset:18432\n"
    "v_mfma_f32_16x16x32_bf16 v[180:183], a[252:255], a[244:247], v[180:183]\n"
    "ds_read_u16 v197, v197\n"
    "s_mov_b32 exec_hi, 0\n"
    "buffer_load_dword v233, v254, s[72:75], s53 offen sc1\n"
    "s_mov_b32 exec_hi, -1\n"
    "s_add_u32 s53, s53, 393216\n"
    "v_mfma_f32_16x16x32_bf16 v[116:119], a[204:207], v[184:187], v[116:119]\n"
    "s_waitcnt lgkmcnt(9)\n"
    "ds_read_u16_d16_hi v205, v205\n"
    "v_or_b32_e64 v193, v193, v201\n"
    "v_mfma_f32_16x16x32_bf16 v[104:107], a[192:195], v[188:191], v[104:107]\n"
    "ds_read_u16 v198, v198\n"
    "v_or_b32_e32 v194, v194, v202\n"
    "v_mfma_f32_16x16x32_bf16 v[108:111], a[196:199], v[188:191], v[108:111]\n"
    "s_waitcnt lgkmcnt(9)\n"
    "v_lshlrev_b32_sdwa v201, 1, v171 dst_sel:DWORD dst_unused:UNUSED_PAD src0_sel:DWORD src1_sel:WORD_1\n"
    "v_lshlrev_b32_sdwa v202, 1, v173 dst_sel:DWORD dst_unused:UNUSED_PAD src0_sel:DWORD src1_sel:WORD_1\n"
    "v_mfma_f32_16x16x32_bf16 v[128:131], v[236:239], v[184:187], v[128:131]\n"
    "ds_read_u16_d16_hi v206, v206\n"
    "v_fma_mix_f32 v232, v192, v232, 0 op_sel:[1,1,0] op_sel_hi:[1,1,0]\n"
    "v_mfma_f32_16x16x32_bf16 v[120:123], a[200:203], v[188:191], v[120:123]\n"
    "ds_read_u16 v199, v199\n"
    "v_cvt_pk_bf16_f32 v192, v234, v232\n"
    "v_mfma_f32_16x16x32_bf16 v[124:127], a[204:207], v[188:191], v[124:127]\n"
    "s_waitcnt lgkmcnt(8)\n"
    "v_or_b32_e64 v195, v195, v203\n"
    "v_lshlrev_b32_sdwa v203, 1, v175 dst_sel:DWORD dst_unused:UNUSED_PAD src0_sel:DWORD src1_sel:WORD_1\n"
    "v_mfma_f32_16x16x32_bf16 v[136:139], v[236:239], v[188:191], v[136:139]\n"
    "s_waitcnt lgkmcnt(7)\n"
    "ds_read_u16_d16_hi v207, v207\n"
    "v_pk_fma_f16 v232, v196, v196, s48\n"
    "v_smfmac_f32_16x16x64_bf16 v[72:75], v[216:219], v[208:215], v240\n"
    "s_waitcnt lgkmcnt(7)\n"
    "v_pk_mul_f16 v232, v196, v232\n"
    "v_smfmac_f32_16x16x64_bf16 v[76:79], v[220:223], v[208:215], v240 abid:1\n"
    "s_waitcnt lgkmcnt(6)\n"
    "ds_read_b128 v[208:211], v250 offset:52368\n"
    "ds_read_b128 v[212:215], v250 offset:52688\n"
    "v_mfma_f32_16x16x32_bf16 v[132:135], v[164:167], v[184:187], v[132:135]\n"
    "v_lshlrev_b32_sdwa v184, 1, v168 dst_sel:DWORD dst_unused:UNUSED_PAD src0_sel:DWORD src1_sel:WORD_1\n"
    "v_lshlrev_b32_sdwa v185, 1, v170 dst_sel:DWORD dst_unused:UNUSED_PAD src0_sel:DWORD src1_sel:WORD_1\n"
    "v_mfma_f32_16x16x32_bf16 v[140:143], v[164:167], v[188:191], v[140:143]\n"
    "ds_read_b128 v[216:219], v252 offset:22176\n"
    "ds_read_b128 v[220:223], v252 offset:22512\n"
    "v_mfma_f32_16x16x32_bf16 v[168:171], a[88:91], a[224:227], 0\n"
    "v_lshlrev_b32_sdwa v186, 1, v172 dst_sel:DWORD dst_unused:UNUSED_PAD src0_sel:DWORD src1_sel:WORD_1\n"
    "v_lshlrev_b32_sdwa v187, 1, v174 dst_sel:DWORD dst_unused:UNUSED_PAD src0_sel:DWORD src1_sel:WORD_1\n"
    "v_mfma_f32_16x16x32_bf16 v[168:171], a[136:139], a[228:231], v[168:171]\n"
    "ds_read_u16 v184, v184\n"
    "v_lshlrev_b32_sdwa v188, 1, v176 dst_sel:DWORD dst_unused:UNUSED_PAD src0_sel:DWORD src1_sel:WORD_1\n"
    "v_mfma_f32_16x16x32_bf16 v[168:171], a[52:55], a[232:235], v[168:171]\n"
    "s_waitcnt lgkmcnt(9)\n"
    "ds_read_b128 a[252:255], v244 offset:36864\n"
    "v_or_b32_e32 v197, v197, v205\n"
    "v_mfma_f32_16x16x32_bf16 v[172:175], a[92:95], a[224:227], 0\n"
    "ds_read_u16_d16_hi v200, v200\n"
    "v_lshlrev_b32_sdwa v189, 1, v178 dst_sel:DWORD dst_unused:UNUSED_PAD src0_sel:DWORD src1_sel:WORD_1\n"
    "v_mfma_f32_16x16x32_bf16 v[172:175], a[140:143], a[228:231], v[172:175]\n"
    "v_lshlrev_b32_sdwa v205, 1, v179 dst_sel:DWORD dst_unused:UNUSED_PAD src0_sel:DWORD src1_sel:WORD_1\n"
    "v_lshlrev_b32_sdwa v190, 1, v180 dst_sel:DWORD dst_unused:UNUSED_PAD src0_sel:DWORD src1_sel:WORD_1\n"
    "v_mfma_f32_16x16x32_bf16 v[176:179], a[88:91], a[236:239], 0\n"
    "s_waitcnt lgkmcnt(9)\n"
    "ds_read_u16 v185, v185\n"
    "v_or_b32_e32 v198, v198, v206\n"
    "v_mfma_f32_16x16x32_bf16 v[176:179], a[136:139], a[240:243], v[176:179]\n"
    "v_lshlrev_b32_sdwa v206, 1, v181 dst_sel:DWORD dst_unused:UNUSED_PAD src0_sel:DWORD src1_sel:WORD_1\n"
    "v_lshlrev_b32_sdwa v191, 1, v182 dst_sel:DWORD dst_unused:UNUSED_PAD src0_sel:DWORD src1_sel:WORD_1\n"
    "v_mfma_f32_16x16x32_bf16 v[176:179], a[52:55], a[244:247], v[176:179]\n"
    "ds_read_u16_d16_hi v201, v201\n"
    "v_exp_f16_e64 v234, v232\n"
    "ds_read_u16 v186, v186\n"
    "s_waitcnt lgkmcnt(9)\n"
    "v_or_b32_e32 v199, v199, v207\n"
    "v_lshlrev_b32_sdwa v207, 1, v183 dst_sel:DWORD dst_unused:UNUSED_PAD src0_sel:DWORD src1_sel:WORD_1\n"
    "v_mfma_f32_16x16x32_bf16 v[180:183], a[92:95], a[236:239], 0\n"
    "v_exp_f16_sdwa v234, v232 dst_sel:WORD_1 dst_unused:UNUSED_PRESERVE src0_sel:WORD_1\n"
    "v_mfma_f32_16x16x32_bf16 v[180:183], a[140:143], a[240:243], v[180:183]\n"
    "s_waitcnt lgkmcnt(7)\n"
    "ds_read_u16_d16_hi v202, v202\n"
    "v_pk_fma_f16 v234, v234, s49, s49\n"
    "v_smfmac_f32_16x16x64_bf16 v[72:75], v[216:219], v[208:215], v240\n"
    "s_waitcnt lgkmcnt(7)\n"
    "ds_read_b128 v[216:219], v251 offset:22848\n"
    "v_smfmac_f32_16x16x64_bf16 v[76:79], v[220:223], v[208:215], v240 abid:1\n"
    "ds_read_b128 v[208:211], v250 offset:51808\n"
    "v_mfma_f32_16x16x32_bf16 v[96:99], a[208:211], v[192:195], v[96:99]\n"
    "ds_read_b128 v[212:215], v250 offset:52128\n"
    "v_rcp_f16_e32 v232, v234\n"
    "v_mfma_f32_16x16x32_bf16 v[100:103], a[212:215], v[192:195], v[100:103]\n"
    "s_waitcnt lgkmcnt(8)\n"
    "ds_read_b128 v[220:223], v251 offset:23184\n"
    "v_rcp_f16_sdwa v232, v234 dst_sel:WORD_1 dst_unused:UNUSED_PRESERVE src0_sel:WORD_1\n"
    "v_lshlrev_b32_e64 v254, 3, v244\n"
    "v_fma_mix_f32 v234, v196, v232, 0 op_sel_hi:[1,1,0]\n"
    "v_pk_add_f32 v[224:225], v[224:225], v[72:73]\n"
    "v_pk_add_f32 v[226:227], v[226:227], v[74:75]\n"
    "v_pk_fma_f32 v[228:229], v[72:73], v[72:73], v[228:229]\n"
    "v_pk_fma_f32 v[230:231], v[74:75], v[74:75], v[230:231]\n"
    "v_pk_add_f32 v[224:225], v[224:225], v[76:77]\n"
    "v_pk_add_f32 v[226:227], v[226:227], v[78:79]\n"
    "v_pk_fma_f32 v[228:229], v[76:77], v[76:77], v[228:229]\n"
    "v_pk_fma_f32 v[230:231], v[78:79], v[78:79], v[230:231]\n"
    "v_mfma_f32_16x16x32_bf16 v[172:175], a[252:255], a[232:235], v[172:175]\n"
    "s_waitcnt lgkmcnt(7)\n"
    "ds_read_b128 v[236:239], v244 offset:20480\n"
    "ds_read_u16 v187, v187\n"
    "v_mfma_f32_16x16x32_bf16 v[180:183], a[252:255], a[244:247], v[180:183]\n"
    "ds_read_u16_d16_hi v203, v203\n"
    "v_fma_mix_f32 v232, v196, v232, 0 op_sel:[1,1,0] op_sel_hi:[1,1,0]\n"
    "v_mfma_f32_16x16x32_bf16 v[112:115], a[216:219], v[192:195], v[112:115]\n"
    "s_waitcnt lgkmcnt(9)\n"
    "ds_read_b128 v[164:167], v244 offset:22528\n"
    "v_cvt_pk_bf16_f32 v196, v234, v232\n"
    "v_mfma_f32_16x16x32_bf16 v[116:119], a[220:223], v[192:195], v[116:119]\n"
    "ds_read_u16 v188, v188\n"
    "v_or_b32_e64 v184, v184, v200\n"
    "v_mfma_f32_16x16x32_bf16 v[104:107], a[208:211], v[196:199], v[104:107]\n"
    "v_or_b32_e64 v185, v185, v201\n"
    "v_lshlrev_b32_sdwa v201, 1, v171 dst_sel:DWORD dst_unused:UNUSED_PAD src0_sel:DWORD src1_sel:WORD_1\n"
    "v_mfma_f32_16x16x32_bf16 v[108:111], a[212:215], v[196:199], v[108:111]\n"
    "s_nop 0\n"
    "ds_read_u16_d16_hi v204, v204\n"
    "s_mov_b32 exec_hi, 0\n"
    "buffer_load_dword v233, v254, s[72:75], s53 offen sc1\n"
    "s_mov_b32 exec_hi, -1\n"
    "v_mfma_f32_16x16x32_bf16 v[120:123], a[216:219], v[196:199], v[120:123]\n"
    "s_waitcnt lgkmcnt(8)\n"
    "ds_read_u16 v189, v189\n"
    "v_or_b32_e32 v186, v186, v202\n"
    "v_mfma_f32_16x16x32_bf16 v[124:127], a[220:223], v[196:199], v[124:127]\n"
    "s_waitcnt lgkmcnt(8)\n"
    "v_lshlrev_b32_sdwa v202, 1, v173 dst_sel:DWORD dst_unused:UNUSED_PAD src0_sel:DWORD src1_sel:WORD_1\n"
    "v_cvt_pk_bf16_f32 v96, v96, v97\n"
    "v_smfmac_f32_16x16x64_bf16 v[80:83], v[216:219], v[208:215], v240\n"
    "s_waitcnt lgkmcnt(7)\n"
    "ds_read_b128 v[216:219], v252 offset:22848\n"
    "ds_read_u16_d16_hi v205, v205\n"
    "v_smfmac_f32_16x16x64_bf16 v[84:87], v[220:223], v[208:215], v240 abid:1\n"
    "s_waitcnt lgkmcnt(8)\n"
    "v_cvt_pk_bf16_f32 v97, v98, v99\n"
    "v_mfma_f32_16x16x32_bf16 v[128:131], v[236:239], v[192:195], v[128:131]\n"
    "s_add_u32 s53, s53, 393216\n"
    "ds_read_b128 v[208:211], v250 offset:52448\n"
    "v_cvt_pk_bf16_f32 v98, v100, v101\n"
    "v_mfma_f32_16x16x32_bf16 v[136:139], v[236:239], v[196:199], v[136:139]\n"
    "ds_read_b128 v[212:215], v250 offset:52768\n"
    "v_cvt_pk_bf16_f32 v99, v102, v103\n"
    "v_cvt_pk_bf16_f32 v112, v112, v113\n"
    "v_cvt_pk_bf16_f32 v113, v114, v115\n"
    "v_cvt_pk_bf16_f32 v114, v116, v117\n"
    "v_cvt_pk_bf16_f32 v115, v118, v119\n"
    "v_cvt_pk_bf16_f32 v128, v128, v129\n"
    "s_waitcnt lgkmcnt(7)\n"
    "v_mfma_f32_16x16x32_bf16 v[132:135], v[164:167], v[192:195], v[132:135]\n"
    "s_nop 0\n"
    "ds_write_b128 v243, v[96:99] offset:0\n"
    "v_mfma_f32_16x16x32_bf16 v[140:143], v[164:167], v[196:199], v[140:143]\n"
    "v_cvt_pk_f16_f32 v192, v168, v169\n"
    "v_lshlrev_b32_sdwa v193, 1, v170 dst_sel:DWORD dst_unused:UNUSED_PAD src0_sel:DWORD src1_sel:WORD_1\n"
    "v_mfma_f32_16x16x32_bf16 v[168:171], a[96:99], a[224:227], 0\n"
    "v_lshlrev_b32_sdwa v194, 1, v172 dst_sel:DWORD dst_unused:UNUSED_PAD src0_sel:DWORD src1_sel:WORD_1\n"
    "v_lshlrev_b32_sdwa v195, 1, v174 dst_sel:DWORD dst_unused:UNUSED_PAD src0_sel:DWORD src1_sel:WORD_1\n"
    "v_mfma_f32_16x16x32_bf16 v[168:171], a[144:147], a[228:231], v[168:171]\n"
    "s_nop 0\n"
    "ds_read_b128 v[220:223], v252 offset:23184\n"
    "ds_read_u16 v190, v190\n"
    "v_mfma_f32_16x16x32_bf16 v[168:171], a[56:59], a[232:235], v[168:171]\n"
    "s_waitcnt lgkmcnt(8)\n"
    "ds_read_u16_d16_hi v206, v206\n"
    "ds_read_u16 v191, v191\n"
    "v_cvt_pk_f16_f32 v196, v176, v177\n"
    "v_lshlrev_b32_sdwa v197, 1, v178 dst_sel:DWORD dst_unused:UNUSED_PAD src0_sel:DWORD src1_sel:WORD_1\n"
    "v_lshlrev_b32_sdwa v198, 1, v180 dst_sel:DWORD dst_unused:UNUSED_PAD src0_sel:DWORD src1_sel:WORD_1\n"
    "v_lshlrev_b32_sdwa v199, 1, v182 dst_sel:DWORD dst_unused:UNUSED_PAD src0_sel:DWORD src1_sel:WORD_1\n"
    "v_or_b32_e64 v187, v187, v203\n"
    "v_lshlrev_b32_sdwa v203, 1, v175 dst_sel:DWORD dst_unused:UNUSED_PAD src0_sel:DWORD src1_sel:WORD_1\n"
    "v_mfma_f32_16x16x32_bf16 v[172:175], a[100:103], a[224:227], 0\n"
    "v_or_b32_e64 v188, v188, v204\n"
    "v_cvt_pk_bf16_f32 v129, v130, v131\n"
    "v_mfma_f32_16x16x32_bf16 v[172:175], a[148:151], a[228:231], v[172:175]\n"
    "ds_write_b128 v243, v[112:115] offset:2048\n"
    "v_mfma_f32_16x16x32_bf16 v[172:175], a[60:63], a[232:235], v[172:175]\n"
    "s_waitcnt lgkmcnt(6)\n"
    "v_or_b32_e32 v189, v189, v205\n"
    "v_lshlrev_b32_sdwa v205, 1, v179 dst_sel:DWORD dst_unused:UNUSED_PAD src0_sel:DWORD src1_sel:WORD_1\n"
    "v_smfmac_f32_16x16x64_bf16 v[80:83], v[216:219], v[208:215], v240\n"
    "v_cvt_pk_bf16_f32 v130, v132, v133\n"
    "v_mfma_f32_16x16x32_bf16 v[176:179], a[96:99], a[236:239], 0\n"
    "s_nop 0\n"
    "ds_read_b128 v[216:219], v251 offset:23520\n"
    "ds_read_u16_d16_hi v207, v207\n"
    "v_mfma_f32_16x16x32_bf16 v[176:179], a[144:147], a[240:243], v[176:179]\n"
    "s_waitcnt lgkmcnt(6)\n"
    "ds_read_u16 v193, v193\n"
    "ds_read_u16_d16_hi v201, v201\n"
    "v_cvt_pk_bf16_f32 v131, v134, v135\n"
    "v_cvt_pk_bf16_f32 v104, v104, v105\n"
    "v_cvt_pk_bf16_f32 v105, v106, v107\n"
    "v_pk_add_f32 v[224:225], v[224:225], v[80:81]\n"
    "v_pk_add_f32 v[226:227], v[226:227], v[82:83]\n"
    "v_pk_fma_f32 v[228:229], v[80:81], v[80:81], v[228:229]\n"
    "v_pk_fma_f32 v[230:231], v[82:83], v[82:83], v[230:231]\n"
    "v_smfmac_f32_16x16x64_bf16 v[84:87], v[220:223], v[208:215], v240 abid:1\n"
    "v_cvt_pk_bf16_f32 v106, v108, v109\n"
    "v_mfma_f32_16x16x32_bf16 v[176:179], a[56:59], a[244:247], v[176:179]\n"
    "ds_write_b128 v243, v[128:131] offset:4096\n"
    "s_waitcnt lgkmcnt(7)\n"
    "v_or_b32_e32 v190, v190, v206\n"
    "v_mfma_f32_16x16x32_bf16 v[96:99], a[0:3], v[184:187], 0\n"
    "v_lshlrev_b32_sdwa v206, 1, v181 dst_sel:DWORD dst_unused:UNUSED_PAD src0_sel:DWORD src1_sel:WORD_1\n"
    "v_cvt_pk_bf16_f32 v107, v110, v111\n"
    "v_cvt_pk_bf16_f32 v120, v120, v121\n"
    "v_cvt_pk_bf16_f32 v121, v122, v123\n"
    "ds_read_b128 v[208:211], v250 offset:51888\n"
    "ds_write_b128 v243, v[104:107] offset:1024\n"
    "v_pk_add_f32 v[224:225], v[224:225], v[84:85]\n"
    "v_pk_add_f32 v[226:227], v[226:227], v[86:87]\n"
    "v_pk_fma_f32 v[228:229], v[84:85], v[84:85], v[228:229]\n"
    "v_pk_fma_f32 v[230:231], v[86:87], v[86:87], v[230:231]\n"
    "v_mfma_f32_16x16x32_bf16 v[100:103], a[4:7], v[184:187], 0\n"
    "s_waitcnt lgkmcnt(5)\n"
    "v_or_b32_e32 v191, v191, v207\n"
    "v_lshlrev_b32_sdwa v207, 1, v183 dst_sel:DWORD dst_unused:UNUSED_PAD src0_sel:DWORD src1_sel:WORD_1\n"
    "v_mfma_f32_16x16x32_bf16 v[180:183], a[100:103], a[236:239], 0\n"
    "ds_read_b128 v[212:215], v250 offset:52208\n"
    "ds_read_b128 v[220:223], v251 offset:23856\n"
    "v_mfma_f32_16x16x32_bf16 v[180:183], a[148:151], a[240:243], v[180:183]\n"
    "ds_read_u16 v194, v194\n"
    "ds_read_u16_d16_hi v202, v202\n"
    "v_mfma_f32_16x16x32_bf16 v[180:183], a[60:63], a[244:247], v[180:183]\n"
    "ds_read_u16 v195, v195\n"
    "v_cvt_pk_bf16_f32 v122, v124, v125\n"
    "v_mfma_f32_16x16x32_bf16 v[112:115], a[8:11], v[184:187], 0\n"
    "s_waitcnt lgkmcnt(7)\n"
    "v_or_b32_e32 v193, v193, v201\n"
    "v_cvt_pk_bf16_f32 v123, v126, v127\n"
    "v_mfma_f32_16x16x32_bf16 v[116:119], a[12:15], v[184:187], 0\n"
    "v_add_u32_e64 v254, s54, v242\n"
    "v_cvt_pk_bf16_f32 v136, v136, v137\n"
    "v_mfma_f32_16x16x32_bf16 v[104:107], a[0:3], v[188:191], 0\n"
    "ds_write_b128 v243, v[120:123] offset:3072\n"
    "v_mfma_f32_16x16x32_bf16 v[108:111], a[4:7], v[188:191], 0\n"
    "v_pk_fma_f16 v232, v192, v192, s48\n"
    "v_cvt_pk_bf16_f32 v137, v138, v139\n"
    "v_mfma_f32_16x16x32_bf16 v[120:123], a[8:11], v[188:191], 0\n"
    "v_cvt_pk_bf16_f32 v138, v140, v141\n"
    "v_cvt_pk_bf16_f32 v139, v142, v143\n"
    "v_mfma_f32_16x16x32_bf16 v[124:127], a[12:15], v[188:191], 0\n"
    "s_waitcnt lgkmcnt(5)\n"
    "ds_read_b128 v[236:239], v244 offset:0\n"
    "ds_write_b128 v243, v[136:139] offset:5120\n"
    "v_smfmac_f32_16x16x64_bf16 v[88:91], v[216:219], v[208:215], v240\n"
    "s_waitcnt lgkmcnt(6)\n"
    "v_pk_mul_f16 v232, v192, v232\n"
    "v_smfmac_f32_16x16x64_bf16 v[92:95], v[220:223], v[208:215], v240 abid:1\n"
    "v_exp_f16_e64 v234, v232\n"
    "ds_read_b128 v[208:211], v250 offset:52528\n"
    "ds_read_b128 v[212:215], v250 offset:52848\n"
    "ds_read_b128 v[216:219], v252 offset:23520\n"
    "s_waitcnt lgkmcnt(7)\n"
    "v_or_b32_e32 v194, v194, v202\n"
    "ds_read_b128 v[220:223], v252 offset:23856\n"
    "ds_read_u16_d16_hi v203, v203\n"
    "v_exp_f16_sdwa v234, v232 dst_sel:WORD_1 dst_unused:UNUSED_PRESERVE src0_sel:WORD_1\n"
    "ds_read_b128 v[164:167], v244 offset:2048\n"
    "ds_read_u16 v197, v197\n"
    "v_pk_fma_f16 v234, v234, s49, s49\n"
    "ds_read_u16_d16_hi v205, v205\n"
    "s_waitcnt lgkmcnt(9)\n"
    "v_mfma_f32_16x16x32_bf16 v[128:131], v[236:239], v[184:187], 0\n"
    "s_nop 0\n"
    "v_lshlrev_b32_sdwa v200, 1, v169 dst_sel:DWORD dst_unused:UNUSED_PAD src0_sel:DWORD src1_sel:WORD_1\n"
    "v_lshlrev_b32_sdwa v201, 1, v171 dst_sel:DWORD dst_unused:UNUSED_PAD src0_sel:DWORD src1_sel:WORD_1\n"
    "v_mfma_f32_16x16x32_bf16 v[136:139], v[236:239], v[188:191], 0\n"
    "ds_read_u16 v198, v198\n"
    "v_lshlrev_b32_sdwa v202, 1, v173 dst_sel:DWORD dst_unused:UNUSED_PAD src0_sel:DWORD src1_sel:WORD_1\n"
    "v_rcp_f16_e64 v232, v234\n"
    "v_lshlrev_b32_sdwa v204, 1, v177 dst_sel:DWORD dst_unused:UNUSED_PAD src0_sel:DWORD src1_sel:WORD_1\n"
    "v_rcp_f16_sdwa v232, v234 dst_sel:WORD_1 dst_unused:UNUSED_PRESERVE src0_sel:WORD_1\n"
    "ds_read_u16_d16_hi v206, v206\n"
    "v_fma_mix_f32 v234, v192, v232, 0 op_sel_hi:[1,1,0]\n"
    "v_fma_mix_f32 v232, v192, v232, 0 op_sel:[1,1,0] op_sel_hi:[1,1,0]\n"
    "s_waitcnt lgkmcnt(7)\n"
    "v_smfmac_f32_16x16x64_bf16 v[88:91], v[216:219], v[208:215], v240\n"
    "s_waitcnt lgkmcnt(6)\n"
    "ds_read_u16 v199, v199\n"
    "v_smfmac_f32_16x16x64_bf16 v[92:95], v[220:223], v[208:215], v240 abid:1\n"
    "ds_read_u16_d16_hi v207, v207\n"
    "v_cvt_pk_bf16_f32 v192, v234, v232\n"
    "v_pk_fma_f16 v232, v196, v196, s48\n"
    "v_pk_mul_f16 v232, v196, v232\n"
    "v_exp_f16_e64 v234, v232\n"
    "v_pk_add_f32 v[224:225], v[224:225], v[88:89]\n"
    "v_pk_add_f32 v[226:227], v[226:227], v[90:91]\n"
    "v_pk_fma_f32 v[228:229], v[88:89], v[88:89], v[228:229]\n"
    "v_pk_fma_f32 v[230:231], v[90:91], v[90:91], v[230:231]\n"
    "v_pk_add_f32 v[224:225], v[224:225], v[92:93]\n"
    "v_pk_add_f32 v[226:227], v[226:227], v[94:95]\n"
    "v_pk_fma_f32 v[228:229], v[92:93], v[92:93], v[228:229]\n"
    "v_pk_fma_f32 v[230:231], v[94:95], v[94:95], v[230:231]\n"
    "s_waitcnt lgkmcnt(6)\n"
    "v_mfma_f32_16x16x32_bf16 v[132:135], v[164:167], v[184:187], 0\n"
    "ds_write_b128 v254, v[224:227]\n"
    "v_mfma_f32_16x16x32_bf16 v[140:143], v[164:167], v[188:191], 0\n"
    "v_or_b32_e32 v195, v195, v203\n"
    "v_lshlrev_b32_sdwa v184, 1, v168 dst_sel:DWORD dst_unused:UNUSED_PAD src0_sel:DWORD src1_sel:WORD_1\n"
    "s_waitcnt lgkmcnt(5)\n"
    "v_or_b32_e32 v197, v197, v205\n"
    "v_lshlrev_b32_sdwa v185, 1, v170 dst_sel:DWORD dst_unused:UNUSED_PAD src0_sel:DWORD src1_sel:WORD_1\n"
    "v_mfma_f32_16x16x32_bf16 v[168:171], a[104:107], a[224:227], 0\n"
    "ds_read_u16 v184, v184\n"
    "ds_write_b128 v254, v[228:231] offset:1024\n"
    "v_mfma_f32_16x16x32_bf16 v[168:171], a[152:155], a[228:231], v[168:171]\n"
    "v_lshrrev_b32_e64 v254, 2, v242\n"
    "v_lshlrev_b32_sdwa v186, 1, v172 dst_sel:DWORD dst_unused:UNUSED_PAD src0_sel:DWORD src1_sel:WORD_1\n"
    "v_mfma_f32_16x16x32_bf16 v[168:171], a[64:67], a[232:235], v[168:171]\n"
    "s_waitcnt lgkmcnt(5)\n"
    "v_add_u32_e32 v254, s55, v254\n"
    "v_or_b32_e64 v198, v198, v206\n"
    "v_lshlrev_b32_sdwa v187, 1, v174 dst_sel:DWORD dst_unused:UNUSED_PAD src0_sel:DWORD src1_sel:WORD_1\n"
    "v_lshlrev_b32_sdwa v203, 1, v175 dst_sel:DWORD dst_unused:UNUSED_PAD src0_sel:DWORD src1_sel:WORD_1\n"
    "v_mfma_f32_16x16x32_bf16 v[172:175], a[108:111], a[224:227], 0\n"
    "ds_read_u16_d16_hi v200, v200\n"
    "ds_read_u16 v185, v185\n"
    "v_mfma_f32_16x16x32_bf16 v[172:175], a[156:159], a[228:231], v[172:175]\n"
    "s_nop 0\n"
    "ds_read_u16_d16_hi v201, v201\n"
    "ds_read_u16 v186, v186\n"
    "v_mfma_f32_16x16x32_bf16 v[172:175], a[68:71], a[232:235], v[172:175]\n"
    "s_waitcnt lgkmcnt(6)\n"
    "v_lshlrev_b32_sdwa v188, 1, v176 dst_sel:DWORD dst_unused:UNUSED_PAD src0_sel:DWORD src1_sel:WORD_1\n"
    "v_lshlrev_b32_sdwa v189, 1, v178 dst_sel:DWORD dst_unused:UNUSED_PAD src0_sel:DWORD src1_sel:WORD_1\n"
    "v_lshlrev_b32_sdwa v205, 1, v179 dst_sel:DWORD dst_unused:UNUSED_PAD src0_sel:DWORD src1_sel:WORD_1\n"
    "v_mfma_f32_16x16x32_bf16 v[176:179], a[104:107], a[236:239], 0\n"
    "ds_read_u16_d16_hi v202, v202\n"
    "v_lshlrev_b32_sdwa v190, 1, v180 dst_sel:DWORD dst_unused:UNUSED_PAD src0_sel:DWORD src1_sel:WORD_1\n"
    "v_mfma_f32_16x16x32_bf16 v[176:179], a[152:155], a[240:243], v[176:179]\n"
    "v_lshlrev_b32_sdwa v206, 1, v181 dst_sel:DWORD dst_unused:UNUSED_PAD src0_sel:DWORD src1_sel:WORD_1\n"
    "v_lshlrev_b32_sdwa v191, 1, v182 dst_sel:DWORD dst_unused:UNUSED_PAD src0_sel:DWORD src1_sel:WORD_1\n"
    "v_mfma_f32_16x16x32_bf16 v[176:179], a[64:67], a[244:247], v[176:179]\n"
    "ds_read_b128 v[236:239], v244 offset:4096\n"
    "v_or_b32_e32 v199, v199, v207\n"
    "v_lshlrev_b32_sdwa v207, 1, v183 dst_sel:DWORD dst_unused:UNUSED_PAD src0_sel:DWORD src1_sel:WORD_1\n"
    "v_mfma_f32_16x16x32_bf16 v[180:183], a[108:111], a[236:239], 0\n"
    "s_waitcnt lgkmcnt(6)\n"
    "ds_read_u16 v187, v187\n"
    "v_exp_f16_sdwa v234, v232 dst_sel:WORD_1 dst_unused:UNUSED_PRESERVE src0_sel:WORD_1\n"
    "v_mfma_f32_16x16x32_bf16 v[180:183], a[156:159], a[240:243], v[180:183]\n"
    "s_barrier\n"
    "v_mfma_f32_16x16x32_bf16 v[180:183], a[68:71], a[244:247], v[180:183]\n"
    "s_waitcnt lgkmcnt(6)\n"
    "ds_read_b32 v224, v254 offset:0\n"
    "ds_read_b32 v225, v254 offset:6752\n"
    "v_mfma_f32_16x16x32_bf16 v[96:99], a[16:19], v[192:195], v[96:99]\n"
    "ds_read_b32 v226, v254 offset:13504\n"
    "ds_read_b32 v227, v254 offset:20256\n"
    "v_pk_fma_f16 v234, v234, s49, s49\n"
    "v_mfma_f32_16x16x32_bf16 v[100:103], a[20:23], v[192:195], v[100:103]\n"
    "s_waitcnt lgkmcnt(8)\n"
    "ds_read_b32 v228, v254 offset:1024\n"
    "ds_read_b32 v229, v254 offset:7776\n"
    "v_rcp_f16_e32 v232, v234\n"
    "v_mfma_f32_16x16x32_bf16 v[112:115], a[24:27], v[192:195], v[112:115]\n"
    "ds_read_b32 v230, v254 offset:14528\n"
    "ds_read_b32 v231, v254 offset:21280\n"
    "v_rcp_f16_sdwa v232, v234 dst_sel:WORD_1 dst_unused:UNUSED_PRESERVE src0_sel:WORD_1\n"
    "v_mfma_f32_16x16x32_bf16 v[116:119], a[28:31], v[192:195], v[116:119]\n"
    "ds_read_u16_d16_hi v203, v203\n"
    "ds_read_b128 v[164:167], v244 offset:6144\n"
    "s_waitcnt lgkmcnt(11)\n"
    "v_mfma_f32_16x16x32_bf16 v[128:131], v[236:239], v[192:195], v[128:131]\n"
    "s_nop 0\n"
    "ds_read_u16 v188, v188\n"
    "ds_read_u16_d16_hi v204, v204\n"
    "v_fma_mix_f32 v234, v196, v232, 0 op_sel_hi:[1,1,0]\n"
    "v_fma_mix_f32 v232, v196, v232, 0 op_sel:[1,1,0] op_sel_hi:[1,1,0]\n"
    "v_or_b32_e64 v184, v184, v200\n"
    "v_cvt_pk_bf16_f32 v196, v234, v232\n"
    "v_or_b32_e32 v185, v185, v201\n"
    "s_waitcnt lgkmcnt(10)\n"
    "v_add_f32_e32 v224, v224, v225\n"
    "v_mfma_f32_16x16x32_bf16 v[104:107], a[16:19], v[196:199], v[104:107]\n"
    "s_waitcnt lgkmcnt(8)\n"
    "ds_read_u16 v189, v189\n"
    "ds_read_u16_d16_hi v205, v205\n"
    "v_mfma_f32_16x16x32_bf16 v[108:111], a[20:23], v[196:199], v[108:111]\n"
    "s_waitcnt vmcnt(2) lgkmcnt(8)\n"
    "v_add_f32_e32 v226, v226, v227\n"
    "v_add_f32_e64 v228, v228, v229\n"
    "v_mfma_f32_16x16x32_bf16 v[120:123], a[24:27], v[196:199], v[120:123]\n"
    "ds_read_u16 v190, v190\n"
    "v_add_f32_e64 v224, v224, v226\n"
    "v_mfma_f32_16x16x32_bf16 v[124:127], a[28:31], v[196:199], v[124:127]\n"
    "s_waitcnt lgkmcnt(7)\n"
    "v_add_f32_e32 v230, v230, v231\n"
    "v_mul_f32_e32 v224, 0x3c2aaaab, v224\n"
    "v_mfma_f32_16x16x32_bf16 v[136:139], v[236:239], v[196:199], v[136:139]\n"
    "ds_read_u16_d16_hi v206, v206\n"
    "v_add_f32_e64 v228, v228, v230\n"
    "v_smfmac_f32_32x32x32_bf16 v[112:127], a[248:251], v[152:159], v241\n"
    "ds_read_u16 v191, v191\n"
    "v_mul_f32_e32 v228, 0x3c2aaaab, v228\n"
    "v_lshlrev_b32_sdwa v201, 1, v171 dst_sel:DWORD dst_unused:UNUSED_PAD src0_sel:DWORD src1_sel:WORD_1\n"
    "v_fma_f32 v228, -v224, v224, v228\n"
    "v_or_b32_e32 v186, v186, v202\n"
    "v_smfmac_f32_32x32x32_bf16 v[96:111], a[248:251], v[144:151], v241\n"
    "s_waitcnt lgkmcnt(7)\n"
    "ds_read_u16_d16_hi v207, v207\n"
    "buffer_load_dwordx4 v[144:147], v245, s[40:43], s58 offen offset:128\n"
    "v_max_f32_e64 v228, 0, v228\n"
    "v_lshlrev_b32_sdwa v202, 1, v173 dst_sel:DWORD dst_unused:UNUSED_PAD src0_sel:DWORD src1_sel:WORD_1\n"
    "v_add_f32_e32 v228, 0x3727c5ac, v228\n"
    "v_mfma_f32_16x16x32_bf16 v[132:135], v[164:167], v[192:195], v[132:135]\n"
    "ds_read_b128 a[252:255], v244 offset:34816\n"
    "v_rsq_f32_e64 v228, v228\n"
    "v_mfma_f32_16x16x32_bf16 v[140:143], v[164:167], v[196:199], v[140:143]\n"
    "ds_read_u16_d16_hi v201, v201\n"
    "v_mul_f32_e64 v228, s50, v228\n"
    "v_cvt_pk_f16_f32 v192, v168, v169\n"
    "v_mul_f32_e64 v224, v228, -v224\n"
    "v_lshlrev_b32_sdwa v193, 1, v170 dst_sel:DWORD dst_unused:UNUSED_PAD src0_sel:DWORD src1_sel:WORD_1\n"
    "v_mfma_f32_16x16x32_bf16 v[168:171], a[112:115], a[224:227], 0\n"
    "s_waitcnt lgkmcnt(8)\n"
    "v_lshlrev_b32_sdwa v194, 1, v172 dst_sel:DWORD dst_unused:UNUSED_PAD src0_sel:DWORD src1_sel:WORD_1\n"
    "v_lshlrev_b32_sdwa v195, 1, v174 dst_sel:DWORD dst_unused:UNUSED_PAD src0_sel:DWORD src1_sel:WORD_1\n"
    "v_mfma_f32_16x16x32_bf16 v[168:171], a[160:163], a[228:231], v[168:171]\n"
    "ds_write_b32 v254, v228\n"
    "v_mfma_f32_16x16x32_bf16 v[168:171], a[72:75], a[232:235], v[168:171]\n"
    "s_waitcnt lgkmcnt(7)\n"
    "ds_write_b32 v254, v224 offset:1024\n"
    "v_add_u32_e64 v254, s56, v242\n"
    "v_cvt_pk_f16_f32 v196, v176, v177\n"
    "v_lshlrev_b32_sdwa v197, 1, v178 dst_sel:DWORD dst_unused:UNUSED_PAD src0_sel:DWORD src1_sel:WORD_1\n"
    "v_lshlrev_b32_sdwa v198, 1, v180 dst_sel:DWORD dst_unused:UNUSED_PAD src0_sel:DWORD src1_sel:WORD_1\n"
    "v_lshlrev_b32_sdwa v199, 1, v182 dst_sel:DWORD dst_unused:UNUSED_PAD src0_sel:DWORD src1_sel:WORD_1\n"
    "v_or_b32_e64 v187, v187, v203\n"
    "v_lshlrev_b32_sdwa v203, 1, v175 dst_sel:DWORD dst_unused:UNUSED_PAD src0_sel:DWORD src1_sel:WORD_1\n"
    "v_mfma_f32_16x16x32_bf16 v[172:175], a[116:119], a[224:227], 0\n"
    "ds_read_u16 v193, v193\n"
    "ds_read_u16 v194, v194\n"
    "v_mfma_f32_16x16x32_bf16 v[172:175], a[164:167], a[228:231], v[172:175]\n"
    "s_waitcnt lgkmcnt(8)\n"
    "ds_read_u16_d16_hi v202, v202\n"
    "ds_read_u16 v195, v195\n"
    "v_or_b32_e32 v188, v188, v204\n"
    "v_or_b32_e64 v189, v189, v205\n"
    "v_lshlrev_b32_sdwa v205, 1, v179 dst_sel:DWORD dst_unused:UNUSED_PAD src0_sel:DWORD src1_sel:WORD_1\n"
    "v_mfma_f32_16x16x32_bf16 v[176:179], a[112:115], a[236:239], 0\n"
    "buffer_load_dwordx4 v[148:151], v245, s[40:43], s89 offen offset:128\n"
    "ds_read_b128 v[236:239], v244 offset:8192\n"
    "v_mfma_f32_16x16x32_bf16 v[176:179], a[160:163], a[240:243], v[176:179]\n"
    "v_or_b32_e32 v190, v190, v206\n"
    "v_lshlrev_b32_sdwa v206, 1, v181 dst_sel:DWORD dst_unused:UNUSED_PAD src0_sel:DWORD src1_sel:WORD_1\n"
    "v_mfma_f32_16x16x32_bf16 v[176:179], a[72:75], a[244:247], v[176:179]\n"
    "s_waitcnt lgkmcnt(8)\n"
    "ds_read_b128 v[152:155], v242 offset:0\n"
    "ds_read_b128 v[156:159], v242 offset:1024\n"
    "v_mfma_f32_16x16x32_bf16 v[172:175], a[252:255], a[232:235], v[172:175]\n"
    "v_or_b32_e32 v191, v191, v207\n"
    "v_lshlrev_b32_sdwa v207, 1, v183 dst_sel:DWORD dst_unused:UNUSED_PAD src0_sel:DWORD src1_sel:WORD_1\n"
    "v_mfma_f32_16x16x32_bf16 v[180:183], a[116:119], a[236:239], 0\n"
    "s_waitcnt lgkmcnt(7)\n"
    "s_barrier\n"
    "v_mfma_f32_16x16x32_bf16 v[180:183], a[164:167], a[240:243], v[180:183]\n"
    "s_waitcnt lgkmcnt(6)\n"
    "ds_read_b128 v[224:227], v254\n"
    "ds_read_b128 v[228:231], v254 offset:1024\n"
    "v_mfma_f32_16x16x32_bf16 v[180:183], a[252:255], a[244:247], v[180:183]\n"
    "ds_read_u16_d16_hi v203, v203\n"
    "ds_read_b128 v[164:167], v244 offset:10240\n"
    "v_mfma_f32_16x16x32_bf16 v[112:115], a[40:43], v[184:187], v[112:115]\n"
    "ds_read_u16 v197, v197\n"
    "v_lshlrev_b32_e64 v254, 3, v244\n"
    "v_mfma_f32_16x16x32_bf16 v[116:119], a[44:47], v[184:187], v[116:119]\n"
    "s_waitcnt lgkmcnt(9)\n"
    "s_mov_b32 exec_hi, 0\n"
    "buffer_load_dword v233, v254, s[72:75], s53 offen sc1\n"
    "s_mov_b32 exec_hi, -1\n"
    "s_add_u32 s53, s53, 393216\n"
    "v_mfma_f32_16x16x32_bf16 v[120:123], a[40:43], v[188:191], v[120:123]\n"
    "s_waitcnt lgkmcnt(7)\n"
    "ds_read_u16_d16_hi v205, v205\n"
    "ds_read_u16 v198, v198\n"
    "v_mfma_f32_16x16x32_bf16 v[128:131], v[236:239], v[184:187], v[128:131]\n"
    "v_or_b32_e64 v193, v193, v201\n"
    "v_or_b32_e32 v194, v194, v202\n"
    "v_pk_fma_f16 v232, v192, v192, s48\n"
    "v_mfma_f32_16x16x32_bf16 v[124:127], a[44:47], v[188:191], v[124:127]\n"
    "s_waitcnt lgkmcnt(7)\n"
    "ds_read_u16_d16_hi v206, v206\n"
    "v_pk_mul_f16 v232, v192, v232\n"
    "v_smfmac_f32_32x32x32_bf16 v[96:111], a[248:251], v[152:159], v241\n"
    "ds_read_u16 v199, v199\n"
    "v_exp_f16_e64 v234, v232\n"
    "v_lshlrev_b32_e32 v254, 3, v244\n"
    "v_exp_f16_sdwa v234, v232 dst_sel:WORD_1 dst_unused:UNUSED_PRESERVE src0_sel:WORD_1\n"
    "v_mfma_f32_16x16x32_bf16 v[136:139], v[236:239], v[188:191], v[136:139]\n"
    "s_waitcnt lgkmcnt(7)\n"
    "ds_read_u16_d16_hi v207, v207\n"
    "v_pk_fma_f16 v234, v234, s49, s49\n"
    "v_lshlrev_b32_sdwa v200, 1, v169 dst_sel:DWORD dst_unused:UNUSED_PAD src0_sel:DWORD src1_sel:WORD_1\n"
    "v_lshlrev_b32_sdwa v201, 1, v171 dst_sel:DWORD dst_unused:UNUSED_PAD src0_sel:DWORD src1_sel:WORD_1\n"
    "v_lshlrev_b32_sdwa v202, 1, v173 dst_sel:DWORD dst_unused:UNUSED_PAD src0_sel:DWORD src1_sel:WORD_1\n"
    "v_pk_fma_f32 v[64:65], v[64:65], v[224:225], v[228:229]\n"
    "v_pk_fma_f32 v[66:67], v[66:67], v[226:227], v[230:231]\n"
    "v_pk_fma_f32 v[68:69], v[68:69], v[224:225], v[228:229]\n"
    "v_pk_fma_f32 v[70:71], v[70:71], v[226:227], v[230:231]\n"
    "v_pk_fma_f32 v[72:73], v[72:73], v[224:225], v[228:229]\n"
    "v_pk_fma_f32 v[74:75], v[74:75], v[226:227], v[230:231]\n"
    "v_pk_fma_f32 v[76:77], v[76:77], v[224:225], v[228:229]\n"
    "v_pk_fma_f32 v[78:79], v[78:79], v[226:227], v[230:231]\n"
    "v_pk_fma_f32 v[80:81], v[80:81], v[224:225], v[228:229]\n"
    "v_pk_fma_f32 v[82:83], v[82:83], v[226:227], v[230:231]\n"
    "v_pk_fma_f32 v[84:85], v[84:85], v[224:225], v[228:229]\n"
    "v_pk_fma_f32 v[86:87], v[86:87], v[226:227], v[230:231]\n"
    "v_pk_fma_f32 v[88:89], v[88:89], v[224:225], v[228:229]\n"
    "v_pk_fma_f32 v[90:91], v[90:91], v[226:227], v[230:231]\n"
    "v_pk_fma_f32 v[92:93], v[92:93], v[224:225], v[228:229]\n"
    "v_pk_fma_f32 v[94:95], v[94:95], v[226:227], v[230:231]\n"
    "v_pk_fma_f32 v[0:1], v[0:1], v[224:225], v[228:229]\n"
    "v_pk_fma_f32 v[2:3], v[2:3], v[226:227], v[230:231]\n"
    "v_pk_fma_f32 v[4:5], v[4:5], v[224:225], v[228:229]\n"
    "v_pk_fma_f32 v[6:7], v[6:7], v[226:227], v[230:231]\n"
    "v_pk_fma_f32 v[8:9], v[8:9], v[224:225], v[228:229]\n"
    "v_pk_fma_f32 v[10:11], v[10:11], v[226:227], v[230:231]\n"
    "v_pk_fma_f32 v[12:13], v[12:13], v[224:225], v[228:229]\n"
    "v_pk_fma_f32 v[14:15], v[14:15], v[226:227], v[230:231]\n"
    "v_pk_fma_f32 v[16:17], v[16:17], v[224:225], v[228:229]\n"
    "v_pk_fma_f32 v[18:19], v[18:19], v[226:227], v[230:231]\n"
    "v_pk_fma_f32 v[20:21], v[20:21], v[224:225], v[228:229]\n"
    "v_pk_fma_f32 v[22:23], v[22:23], v[226:227], v[230:231]\n"
    "v_pk_fma_f32 v[24:25], v[24:25], v[224:225], v[228:229]\n"
    "v_pk_fma_f32 v[26:27], v[26:27], v[226:227], v[230:231]\n"
    "v_pk_fma_f32 v[28:29], v[28:29], v[224:225], v[228:229]\n"
    "v_pk_fma_f32 v[30:31], v[30:31], v[226:227], v[230:231]\n"
    "v_pk_fma_f32 v[32:33], v[32:33], v[224:225], v[228:229]\n"
    "v_pk_fma_f32 v[34:35], v[34:35], v[226:227], v[230:231]\n"
    "v_pk_fma_f32 v[36:37], v[36:37], v[224:225], v[228:229]\n"
    "v_pk_fma_f32 v[38:39], v[38:39], v[226:227], v[230:231]\n"
    "v_pk_fma_f32 v[40:41], v[40:41], v[224:225], v[228:229]\n"
    "v_pk_fma_f32 v[42:43], v[42:43], v[226:227], v[230:231]\n"
    "v_pk_fma_f32 v[44:45], v[44:45], v[224:225], v[228:229]\n"
    "v_pk_fma_f32 v[46:47], v[46:47], v[226:227], v[230:231]\n"
    "v_pk_fma_f32 v[48:49], v[48:49], v[224:225], v[228:229]\n"
    "v_pk_fma_f32 v[50:51], v[50:51], v[226:227], v[230:231]\n"
    "v_pk_fma_f32 v[52:53], v[52:53], v[224:225], v[228:229]\n"
    "v_pk_fma_f32 v[54:55], v[54:55], v[226:227], v[230:231]\n"
    "v_pk_fma_f32 v[56:57], v[56:57], v[224:225], v[228:229]\n"
    "v_pk_fma_f32 v[58:59], v[58:59], v[226:227], v[230:231]\n"
    "v_pk_fma_f32 v[60:61], v[60:61], v[224:225], v[228:229]\n"
    "v_pk_fma_f32 v[62:63], v[62:63], v[226:227], v[230:231]\n"
    "v_mfma_f32_16x16x32_bf16 v[96:99], a[32:35], v[184:187], v[96:99]\n"
    "s_waitcnt lgkmcnt(0)\n"
    "s_mov_b32 exec_hi, 0\n"
    "buffer_load_dword v233, v254, s[80:83], s85 offen sc1\n"
    "s_mov_b32 exec_hi, -1\n"
    "v_mfma_f32_16x16x32_bf16 v[100:103], a[36:39], v[184:187], v[100:103]\n"
    "s_nop 0\n"
    "ds_read_b128 v[152:155], v242 offset:2048\n"
    "ds_read_b128 v[156:159], v242 offset:3072\n"
    "v_mfma_f32_16x16x32_bf16 v[132:135], v[164:167], v[184:187], v[132:135]\n"
    "ds_read_b128 a[252:255], v244 offset:38912\n"
    "ds_read_u16_d16_hi v200, v200\n"
    "v_mfma_f32_16x16x32_bf16 v[104:107], a[32:35], v[188:191], v[104:107]\n"
    "ds_read_u16_d16_hi v201, v201\n"
    "v_cvt_pk_bf16_f32 v208, v64, v68\n"
    "v_mfma_f32_16x16x32_bf16 v[108:111], a[36:39], v[188:191], v[108:111]\n"
    "ds_read_u16_d16_hi v202, v202\n"
    "v_cvt_pk_bf16_f32 v212, v65, v69\n"
    "v_mfma_f32_16x16x32_bf16 v[140:143], v[164:167], v[188:191], v[140:143]\n"
    "v_cvt_pk_bf16_f32 v216, v66, v70\n"
    "v_cvt_pk_bf16_f32 v220, v67, v71\n"
    "v_cvt_pk_bf16_f32 v209, v72, v76\n"
    "v_cvt_pk_bf16_f32 v213, v73, v77\n"
    "v_cvt_pk_bf16_f32 v217, v74, v78\n"
    "v_cvt_pk_bf16_f32 v221, v75, v79\n"
    "v_cvt_pk_bf16_f32 v210, v80, v84\n"
    "v_cvt_pk_bf16_f32 v214, v81, v85\n"
    "v_cvt_pk_bf16_f32 v218, v82, v86\n"
    "v_cvt_pk_bf16_f32 v222, v83, v87\n"
    "v_cvt_pk_bf16_f32 v211, v88, v92\n"
    "ds_write_b128 v253, v[208:211] offset:26688\n"
    "v_cvt_pk_bf16_f32 v215, v89, v93\n"
    "v_cvt_pk_bf16_f32 v219, v90, v94\n"
    "s_waitcnt lgkmcnt(5)\n"
    "v_smfmac_f32_32x32x32_bf16 v[112:127], a[248:251], v[152:159], v241\n"
    "s_nop 0\n"
    "ds_read_b128 v[236:239], v244 offset:12288\n"
    "ds_write_b128 v253, v[212:215] offset:29760\n"
    "v_cvt_pk_bf16_f32 v223, v91, v95\n"
    "v_cvt_pk_bf16_f32 v80, v0, v4\n"
    "v_cvt_pk_bf16_f32 v84, v1, v5\n"
    "v_cvt_pk_bf16_f32 v88, v2, v6\n"
    "v_or_b32_e64 v195, v195, v203\n"
    "v_cvt_pk_bf16_f32 v92, v3, v7\n"
    "v_cvt_pk_bf16_f32 v81, v8, v12\n"
    "v_cvt_pk_bf16_f32 v85, v9, v13\n"
    "v_cvt_pk_bf16_f32 v89, v10, v14\n"
    "v_cvt_pk_bf16_f32 v93, v11, v15\n"
    "v_cvt_pk_bf16_f32 v82, v16, v20\n"
    "v_or_b32_e64 v197, v197, v205\n"
    "v_cvt_pk_bf16_f32 v86, v17, v21\n"
    "v_cvt_pk_bf16_f32 v90, v18, v22\n"
    "v_cvt_pk_bf16_f32 v94, v19, v23\n"
    "v_cvt_pk_bf16_f32 v83, v24, v28\n"
    "v_lshlrev_b32_sdwa v184, 1, v168 dst_sel:DWORD dst_unused:UNUSED_PAD src0_sel:DWORD src1_sel:WORD_1\n"
    "v_cvt_pk_bf16_f32 v87, v25, v29\n"
    "v_lshlrev_b32_sdwa v185, 1, v170 dst_sel:DWORD dst_unused:UNUSED_PAD src0_sel:DWORD src1_sel:WORD_1\n"
    "v_mfma_f32_16x16x32_bf16 v[168:171], a[120:123], a[224:227], 0\n"
    "ds_read_u16 v184, v184\n"
    "ds_write_b128 v253, v[80:83] offset:24640\n"
    "v_mfma_f32_16x16x32_bf16 v[168:171], a[168:171], a[228:231], v[168:171]\n"
    "v_cvt_pk_bf16_f32 v91, v26, v30\n"
    "v_lshlrev_b32_sdwa v186, 1, v172 dst_sel:DWORD dst_unused:UNUSED_PAD src0_sel:DWORD src1_sel:WORD_1\n"
    "v_or_b32_e64 v198, v198, v206\n"
    "v_lshlrev_b32_sdwa v187, 1, v174 dst_sel:DWORD dst_unused:UNUSED_PAD src0_sel:DWORD src1_sel:WORD_1\n"
    "v_lshlrev_b32_sdwa v203, 1, v175 dst_sel:DWORD dst_unused:UNUSED_PAD src0_sel:DWORD src1_sel:WORD_1\n"
    "v_cvt_pk_bf16_f32 v95, v27, v31\n"
    "v_rcp_f16_e64 v232, v234\n"
    "v_mfma_f32_16x16x32_bf16 v[168:171], a[76:79], a[232:235], v[168:171]\n"
    "ds_read_u16 v185, v185\n"
    "ds_write_b128 v253, v[84:87] offset:27712\n"
    "v_rcp_f16_sdwa v232, v234 dst_sel:WORD_1 dst_unused:UNUSED_PRESERVE src0_sel:WORD_1\n"
    "v_mfma_f32_16x16x32_bf16 v[172:175], a[124:127], a[224:227], 0\n"
    "v_lshlrev_b32_sdwa v188, 1, v176 dst_sel:DWORD dst_unused:UNUSED_PAD src0_sel:DWORD src1_sel:WORD_1\n"
    "v_lshlrev_b32_sdwa v204, 1, v177 dst_sel:DWORD dst_unused:UNUSED_PAD src0_sel:DWORD src1_sel:WORD_1\n"
    "v_mfma_f32_16x16x32_bf16 v[172:175], a[172:175], a[228:231], v[172:175]\n"
    "v_lshlrev_b32_sdwa v189, 1, v178 dst_sel:DWORD dst_unused:UNUSED_PAD src0_sel:DWORD src1_sel:WORD_1\n"
    "v_lshlrev_b32_sdwa v205, 1, v179 dst_sel:DWORD dst_unused:UNUSED_PAD src0_sel:DWORD src1_sel:WORD_1\n"
    "v_lshlrev_b32_sdwa v190, 1, v180 dst_sel:DWORD dst_unused:UNUSED_PAD src0_sel:DWORD src1_sel:WORD_1\n"
    "v_lshlrev_b32_sdwa v206, 1, v181 dst_sel:DWORD dst_unused:UNUSED_PAD src0_sel:DWORD src1_sel:WORD_1\n"
    "v_lshlrev_b32_sdwa v191, 1, v182 dst_sel:DWORD dst_unused:UNUSED_PAD src0_sel:DWORD src1_sel:WORD_1\n"
    "v_fma_mix_f32 v234, v192, v232, 0 op_sel_hi:[1,1,0]\n"
    "s_waitcnt lgkmcnt(4)\n"
    "v_mfma_f32_16x16x32_bf16 v[172:175], a[252:255], a[232:235], v[172:175]\n"
    "s_waitcnt lgkmcnt(2)\n"
    "ds_read_u16 v186, v186\n"
    "ds_read_u16 v187, v187\n"
    "v_cvt_pk_bf16_f32 v64, v32, v36\n"
    "v_or_b32_e64 v199, v199, v207\n"
    "v_lshlrev_b32_sdwa v207, 1, v183 dst_sel:DWORD dst_unused:UNUSED_PAD src0_sel:DWORD src1_sel:WORD_1\n"
    "v_cvt_pk_bf16_f32 v68, v33, v37\n"
    "v_cvt_pk_bf16_f32 v72, v34, v38\n"
    "v_fma_mix_f32 v232, v192, v232, 0 op_sel:[1,1,0] op_sel_hi:[1,1,0]\n"
    "v_mfma_f32_16x16x32_bf16 v[176:179], a[120:123], a[236:239], 0\n"
    "ds_read_u16_d16_hi v203, v203\n"
    "ds_read_b128 v[164:167], v244 offset:14336\n"
    "v_cvt_pk_bf16_f32 v76, v35, v39\n"
    "v_cvt_pk_bf16_f32 v65, v40, v44\n"
    "v_cvt_pk_bf16_f32 v192, v234, v232\n"
    "v_mfma_f32_16x16x32_bf16 v[176:179], a[168:171], a[240:243], v[176:179]\n"
    "ds_read_u16 v188, v188\n"
    "ds_read_u16_d16_hi v204, v204\n"
    "v_mfma_f32_16x16x32_bf16 v[176:179], a[76:79], a[244:247], v[176:179]\n"
    "s_nop 0\n"
    "v_cvt_pk_bf16_f32 v69, v41, v45\n"
    "v_cvt_pk_bf16_f32 v73, v42, v46\n"
    "v_pk_fma_f16 v232, v196, v196, s48\n"
    "v_mfma_f32_16x16x32_bf16 v[180:183], a[124:127], a[236:239], 0\n"
    "s_waitcnt lgkmcnt(6)\n"
    "ds_read_u16 v189, v189\n"
    "v_cvt_pk_bf16_f32 v77, v43, v47\n"
    "v_cvt_pk_bf16_f32 v66, v48, v52\n"
    "v_cvt_pk_bf16_f32 v70, v49, v53\n"
    "v_pk_mul_f16 v232, v196, v232\n"
    "v_mfma_f32_16x16x32_bf16 v[180:183], a[172:175], a[240:243], v[180:183]\n"
    "ds_read_b128 v[152:155], v242 offset:4096\n"
    "ds_read_b128 v[156:159], v242 offset:5120\n"
    "v_cvt_pk_bf16_f32 v74, v50, v54\n"
    "v_cvt_pk_bf16_f32 v78, v51, v55\n"
    "v_exp_f16_e32 v234, v232\n"
    "v_mfma_f32_16x16x32_bf16 v[180:183], a[252:255], a[244:247], v[180:183]\n"
    "s_waitcnt lgkmcnt(8)\n"
    "ds_read_u16_d16_hi v205, v205\n"
    "v_cvt_pk_bf16_f32 v67, v56, v60\n"
    "v_cvt_pk_bf16_f32 v71, v57, v61\n"
    "v_exp_f16_sdwa v234, v232 dst_sel:WORD_1 dst_unused:UNUSED_PRESERVE src0_sel:WORD_1\n"
    "v_mfma_f32_16x16x32_bf16 v[96:99], a[176:179], v[192:195], v[96:99]\n"
    "v_cvt_pk_bf16_f32 v75, v58, v62\n"
    "v_cvt_pk_bf16_f32 v79, v59, v63\n"
    "v_pk_fma_f16 v234, v234, s49, s49\n"
    "v_mfma_f32_16x16x32_bf16 v[100:103], a[180:183], v[192:195], v[100:103]\n"
    "ds_write_b128 v253, v[64:67] offset:25664\n"
    "v_rcp_f16_e64 v232, v234\n"
    "v_mfma_f32_16x16x32_bf16 v[112:115], a[184:187], v[192:195], v[112:115]\n"
    "s_waitcnt lgkmcnt(7)\n"
    "v_rcp_f16_sdwa v232, v234 dst_sel:WORD_1 dst_unused:UNUSED_PRESERVE src0_sel:WORD_1\n"
    "v_mfma_f32_16x16x32_bf16 v[116:119], a[188:191], v[192:195], v[116:119]\n"
    "ds_read_u16 v190, v190\n"
    "ds_write_b128 v253, v[68:71] offset:28736\n"
    "v_mfma_f32_16x16x32_bf16 v[128:131], v[236:239], v[192:195], v[128:131]\n"
    "s_waitcnt lgkmcnt(7)\n"
    "v_fma_mix_f32 v234, v196, v232, 0 op_sel_hi:[1,1,0]\n"
    "v_fma_mix_f32 v232, v196, v232, 0 op_sel:[1,1,0] op_sel_hi:[1,1,0]\n"
    "v_mfma_f32_16x16x32_bf16 v[132:135], v[164:167], v[192:195], v[132:135]\n"
    "v_cvt_pk_bf16_f32 v196, v234, v232\n"
    "v_or_b32_e64 v184, v184, v200\n"
    "v_or_b32_e64 v185, v185, v201\n"
    "v_mfma_f32_16x16x32_bf16 v[104:107], a[176:179], v[196:199], v[104:107]\n"
    "ds_read_u16_d16_hi v206, v206\n"
    "ds_read_u16 v191, v191\n"
    "v_mfma_f32_16x16x32_bf16 v[108:111], a[180:183], v[196:199], v[108:111]\n"
    "ds_read_u16_d16_hi v207, v207\n"
    "v_cvt_pk_f16_f32 v192, v168, v169\n"
    "v_mfma_f32_16x16x32_bf16 v[120:123], a[184:187], v[196:199], v[120:123]\n"
    "s_nop 0\n"
    "ds_read_b128 a[252:255], v244 offset:32768\n"
    "v_lshlrev_b32_sdwa v193, 1, v170 dst_sel:DWORD dst_unused:UNUSED_PAD src0_sel:DWORD src1_sel:WORD_1\n"
    "v_mfma_f32_16x16x32_bf16 v[124:127], a[188:191], v[196:199], v[124:127]\n"
    "s_waitcnt lgkmcnt(6)\n"
    "ds_read_u16 v193, v193\n"
    "v_lshlrev_b32_sdwa v201, 1, v171 dst_sel:DWORD dst_unused:UNUSED_PAD src0_sel:DWORD src1_sel:WORD_1\n"
    "v_mfma_f32_16x16x32_bf16 v[136:139], v[236:239], v[196:199], v[136:139]\n"
    "v_lshlrev_b32_sdwa v194, 1, v172 dst_sel:DWORD dst_unused:UNUSED_PAD src0_sel:DWORD src1_sel:WORD_1\n"
    "v_or_b32_e64 v186, v186, v202\n"
    "v_mfma_f32_16x16x32_bf16 v[140:143], v[164:167], v[196:199], v[140:143]\n"
    "ds_read_u16_d16_hi v201, v201\n"
    "v_lshlrev_b32_sdwa v202, 1, v173 dst_sel:DWORD dst_unused:UNUSED_PAD src0_sel:DWORD src1_sel:WORD_1\n"
    "v_lshlrev_b32_sdwa v195, 1, v174 dst_sel:DWORD dst_unused:UNUSED_PAD src0_sel:DWORD src1_sel:WORD_1\n"
    "v_cvt_pk_f16_f32 v196, v176, v177\n"
    "v_lshlrev_b32_sdwa v197, 1, v178 dst_sel:DWORD dst_unused:UNUSED_PAD src0_sel:DWORD src1_sel:WORD_1\n"
    "v_lshlrev_b32_sdwa v198, 1, v180 dst_sel:DWORD dst_unused:UNUSED_PAD src0_sel:DWORD src1_sel:WORD_1\n"
    "v_smfmac_f32_32x32x32_bf16 v[128:143], a[248:251], v[152:159], v241\n"
    "s_waitcnt lgkmcnt(6)\n"
    "s_barrier\n"
    "v_lshlrev_b32_sdwa v199, 1, v182 dst_sel:DWORD dst_unused:UNUSED_PAD src0_sel:DWORD src1_sel:WORD_1\n"
    "v_or_b32_e64 v187, v187, v203\n"
    "ds_read_b128 a[224:227], v242 offset:24640\n"
    "ds_read_b128 a[228:231], v242 offset:25664\n"
    "v_lshlrev_b32_sdwa v203, 1, v175 dst_sel:DWORD dst_unused:UNUSED_PAD src0_sel:DWORD src1_sel:WORD_1\n"
    "v_or_b32_e64 v188, v188, v204\n"
    "ds_read_b128 a[232:235], v242 offset:26688\n"
    "ds_read_b128 a[236:239], v242 offset:27712\n"
    "ds_read_b128 a[240:243], v242 offset:28736\n"
    "v_or_b32_e64 v189, v189, v205\n"
    "ds_read_b128 a[244:247], v242 offset:29760\n"
    "ds_read_u16 v194, v194\n"
    "v_lshlrev_b32_sdwa v205, 1, v179 dst_sel:DWORD dst_unused:UNUSED_PAD src0_sel:DWORD src1_sel:WORD_1\n"
    "s_waitcnt lgkmcnt(9)\n"
    "v_or_b32_e32 v190, v190, v206\n"
    "v_mfma_f32_16x16x32_bf16 v[96:99], a[192:195], v[184:187], v[96:99]\n"
    "ds_read_u16_d16_hi v202, v202\n"
    "ds_read_u16 v195, v195\n"
    "v_mfma_f32_16x16x32_bf16 v[100:103], a[196:199], v[184:187], v[100:103]\n"
    "v_lshlrev_b32_sdwa v206, 1, v181 dst_sel:DWORD dst_unused:UNUSED_PAD src0_sel:DWORD src1_sel:WORD_1\n"
    "v_or_b32_e32 v191, v191, v207\n"
    "v_mfma_f32_16x16x32_bf16 v[112:115], a[200:203], v[184:187], v[112:115]\n"
    "s_waitcnt lgkmcnt(9)\n"
    "ds_read_b128 v[236:239], v244 offset:16384\n"
    "v_lshlrev_b32_sdwa v207, 1, v183 dst_sel:DWORD dst_unused:UNUSED_PAD src0_sel:DWORD src1_sel:WORD_1\n"
    "v_mfma_f32_16x16x32_bf16 v[116:119], a[204:207], v[184:187], v[116:119]\n"
    "s_waitcnt lgkmcnt(9)\n"
    "ds_read_u16_d16_hi v203, v203\n"
    "v_or_b32_e32 v193, v193, v201\n"
    "v_mfma_f32_16x16x32_bf16 v[168:171], a[80:83], a[224:227], 0\n"
    "s_waitcnt lgkmcnt(9)\n"
    "v_pk_fma_f16 v232, v192, v192, s48\n"
    "v_pk_mul_f16 v232, v192, v232\n"
    "v_mfma_f32_16x16x32_bf16 v[168:171], a[128:131], a[228:231], v[168:171]\n"
    "s_waitcnt lgkmcnt(8)\n"
    "ds_read_b128 v[164:167], v244 offset:18432\n"
    "v_exp_f16_e32 v234, v232\n"
    "v_mfma_f32_16x16x32_bf16 v[168:171], a[48:51], a[232:235], v[168:171]\n"
    "s_waitcnt lgkmcnt(8)\n"
    "ds_read_u16 v197, v197\n"
    "v_exp_f16_sdwa v234, v232 dst_sel:WORD_1 dst_unused:UNUSED_PRESERVE src0_sel:WORD_1\n"
    "v_mfma_f32_16x16x32_bf16 v[172:175], a[84:87], a[224:227], 0\n"
    "ds_read_u16_d16_hi v205, v205\n"
    "v_pk_fma_f16 v234, v234, s49, s49\n"
    "v_mfma_f32_16x16x32_bf16 v[172:175], a[132:135], a[228:231], v[172:175]\n"
    "s_waitcnt lgkmcnt(8)\n"
    "s_barrier\n"
    "v_mfma_f32_16x16x32_bf16 v[172:175], a[252:255], a[232:235], v[172:175]\n"
    "ds_write_b128 v253, v[72:75] offset:1024\n"
    "v_mfma_f32_16x16x32_bf16 v[176:179], a[80:83], a[236:239], 0\n"
    "s_waitcnt lgkmcnt(5)\n"
    "v_or_b32_e32 v194, v194, v202\n"
    "v_rcp_f16_e64 v232, v234\n"
    "v_mfma_f32_16x16x32_bf16 v[176:179], a[128:131], a[240:243], v[176:179]\n"
    "v_rcp_f16_sdwa v232, v234 dst_sel:WORD_1 dst_unused:UNUSED_PRESERVE src0_sel:WORD_1\n"
    "v_mfma_f32_16x16x32_bf16 v[176:179], a[48:51], a[244:247], v[176:179]\n"
    "ds_read_u16 v198, v198\n"
    "ds_write_b128 v253, v[76:79] offset:4096\n"
    "v_mfma_f32_16x16x32_bf16 v[180:183], a[84:87], a[236:239], 0\n"
    "s_waitcnt lgkmcnt(5)\n"
    "v_or_b32_e32 v195, v195, v203\n"
    "v_fma_mix_f32 v234, v192, v232, 0 op_sel_hi:[1,1,0]\n"
    "v_mfma_f32_16x16x32_bf16 v[180:183], a[132:135], a[240:243], v[180:183]\n"
    "v_fma_mix_f32 v232, v192, v232, 0 op_sel:[1,1,0] op_sel_hi:[1,1,0]\n"
    "v_cvt_pk_bf16_f32 v192, v234, v232\n"
    "v_mfma_f32_16x16x32_bf16 v[180:183], a[252:255], a[244:247], v[180:183]\n"
    "ds_read_u16_d16_hi v206, v206\n"
    "ds_write_b128 v253, v[88:91] offset:0\n"
    "v_mfma_f32_16x16x32_bf16 v[128:131], v[236:239], v[184:187], v[128:131]\n"
    "s_waitcnt lgkmcnt(4)\n"
    "v_or_b32_e32 v197, v197, v205\n"
    "v_pk_fma_f16 v232, v196, v196, s48\n"
    "v_mfma_f32_16x16x32_bf16 v[132:135], v[164:167], v[184:187], v[132:135]\n"
    "v_pk_mul_f16 v232, v196, v232\n"
    "v_exp_f16_e64 v234, v232\n"
    "v_mfma_f32_16x16x32_bf16 v[104:107], a[192:195], v[188:191], v[104:107]\n"
    "ds_read_u16 v199, v199\n"
    "ds_write_b128 v253, v[92:95] offset:3072\n"
    "v_mfma_f32_16x16x32_bf16 v[108:111], a[196:199], v[188:191], v[108:111]\n"
    "v_exp_f16_sdwa v234, v232 dst_sel:WORD_1 dst_unused:UNUSED_PRESERVE src0_sel:WORD_1\n"
    "v_mfma_f32_16x16x32_bf16 v[120:123], a[200:203], v[188:191], v[120:123]\n"
    "v_mfma_f32_16x16x32_bf16 v[124:127], a[204:207], v[188:191], v[124:127]\n"
    "ds_read_u16_d16_hi v207, v207\n"
    "v_mfma_f32_16x16x32_bf16 v[136:139], v[236:239], v[188:191], v[136:139]\n"
    "s_mov_b32 s59, s58\n"
    "s_add_u32 s57, s57, 1572864\n"
    "s_add_u32 s58, s58, 1572864\n"
    "s_mov_b32 s70, s46\n"
    "s_add_u32 s88, s57, 98304\n"
    "s_add_u32 s89, s58, 98304\n"
    "s_add_u32 s39, s59, 98304\n"
    "s_mov_b32 s82, 0\n"
    "s_add_u32 s64, s64, 1\n"
    "s_cmp_lt_u32 s64, 2\n"
    "s_cselect_b64 s[72:73], s[72:73], s[76:77]\n"
    "s_cselect_b32 s74, s74, s78\n"
    "s_cselect_b32 s53, s53, s79\n"
    "s_cmp_lg_u32 s64, 3\n"
    "s_cbranch_scc1 1b\n"
    ".Lreplk_last:\n"
    "v_lshlrev_b32_sdwa v184, 1, v168 dst_sel:DWORD dst_unused:UNUSED_PAD src0_sel:DWORD src1_sel:WORD_1\n"
    "v_lshlrev_b32_sdwa v200, 1, v169 dst_sel:DWORD dst_unused:UNUSED_PAD src0_sel:DWORD src1_sel:WORD_1\n"
    "v_lshlrev_b32_sdwa v185, 1, v170 dst_sel:DWORD dst_unused:UNUSED_PAD src0_sel:DWORD src1_sel:WORD_1\n"
    "v_lshlrev_b32_sdwa v201, 1, v171 dst_sel:DWORD dst_unused:UNUSED_PAD src0_sel:DWORD src1_sel:WORD_1\n"
    "v_mfma_f32_16x16x32_bf16 v[140:143], v[164:167], v[188:191], v[140:143]\n"
    "s_waitcnt vmcnt(2) lgkmcnt(3)\n"
    "ds_read_u16 v184, v184\n"
    "ds_read_b128 v[56:59], v244 offset:36864\n"
    "v_mfma_f32_16x16x32_bf16 v[168:171], a[88:91], a[224:227], 0\n"
    "s_nop 0\n"
    "ds_read_u16_d16_hi v200, v200\n"
    "ds_read_u16 v185, v185\n"
    "v_mfma_f32_16x16x32_bf16 v[168:171], a[136:139], a[228:231], v[168:171]\n"
    "v_lshlrev_b32_sdwa v186, 1, v172 dst_sel:DWORD dst_unused:UNUSED_PAD src0_sel:DWORD src1_sel:WORD_1\n"
    "v_lshlrev_b32_sdwa v202, 1, v173 dst_sel:DWORD dst_unused:UNUSED_PAD src0_sel:DWORD src1_sel:WORD_1\n"
    "v_mfma_f32_16x16x32_bf16 v[168:171], a[52:55], a[232:235], v[168:171]\n"
    "ds_read_u16_d16_hi v201, v201\n"
    "v_lshlrev_b32_sdwa v187, 1, v174 dst_sel:DWORD dst_unused:UNUSED_PAD src0_sel:DWORD src1_sel:WORD_1\n"
    "v_smfmac_f32_32x32x32_bf16 v[128:143], a[248:251], v[144:151], v241\n"
    "ds_read_u16 v186, v186\n"
    "v_lshlrev_b32_sdwa v203, 1, v175 dst_sel:DWORD dst_unused:UNUSED_PAD src0_sel:DWORD src1_sel:WORD_1\n"
    "v_lshlrev_b32_sdwa v188, 1, v176 dst_sel:DWORD dst_unused:UNUSED_PAD src0_sel:DWORD src1_sel:WORD_1\n"
    "v_lshlrev_b32_sdwa v204, 1, v177 dst_sel:DWORD dst_unused:UNUSED_PAD src0_sel:DWORD src1_sel:WORD_1\n"
    "v_lshlrev_b32_sdwa v189, 1, v178 dst_sel:DWORD dst_unused:UNUSED_PAD src0_sel:DWORD src1_sel:WORD_1\n"
    "v_mfma_f32_16x16x32_bf16 v[172:175], a[92:95], a[224:227], 0\n"
    "ds_read_u16_d16_hi v202, v202\n"
    "buffer_load_dwordx4 v[152:155], v245, s[40:43], s57 offen offset:64\n"
    "buffer_load_dwordx4 v[144:147], v245, s[40:43], s57 offen offset:0\n"
    "buffer_load_dwordx4 v[208:211], v245, s[40:43], s57 offen offset:128\n"
    "v_mfma_f32_16x16x32_bf16 v[172:175], a[140:143], a[228:231], v[172:175]\n"
    "s_waitcnt lgkmcnt(7)\n"
    "ds_read_b128 v[236:239], v244 offset:20480\n"
    "v_lshlrev_b32_sdwa v205, 1, v179 dst_sel:DWORD dst_unused:UNUSED_PAD src0_sel:DWORD src1_sel:WORD_1\n"
    "v_mfma_f32_16x16x32_bf16 v[176:179], a[88:91], a[236:239], 0\n"
    "s_nop 0\n"
    "ds_read_u16 v187, v187\n"
    "v_lshlrev_b32_sdwa v190, 1, v180 dst_sel:DWORD dst_unused:UNUSED_PAD src0_sel:DWORD src1_sel:WORD_1\n"
    "v_mfma_f32_16x16x32_bf16 v[176:179], a[136:139], a[240:243], v[176:179]\n"
    "v_lshlrev_b32_sdwa v191, 1, v182 dst_sel:DWORD dst_unused:UNUSED_PAD src0_sel:DWORD src1_sel:WORD_1\n"
    "v_pk_fma_f16 v234, v234, s49, s49\n"
    "v_mfma_f32_16x16x32_bf16 v[176:179], a[52:55], a[244:247], v[176:179]\n"
    "s_waitcnt lgkmcnt(7)\n"
    "ds_read_u16_d16_hi v203, v203\n"
    "v_or_b32_e32 v198, v198, v206\n"
    "v_mfma_f32_16x16x32_bf16 v[172:175], v[56:59], a[232:235], v[172:175]\n"
    "v_lshlrev_b32_sdwa v206, 1, v181 dst_sel:DWORD dst_unused:UNUSED_PAD src0_sel:DWORD src1_sel:WORD_1\n"
    "v_or_b32_e64 v199, v199, v207\n"
    "v_mfma_f32_16x16x32_bf16 v[96:99], a[208:211], v[192:195], v[96:99]\n"
    "ds_read_u16 v188, v188\n"
    "v_lshlrev_b32_sdwa v207, 1, v183 dst_sel:DWORD dst_unused:UNUSED_PAD src0_sel:DWORD src1_sel:WORD_1\n"
    "v_mfma_f32_16x16x32_bf16 v[180:183], a[92:95], a[236:239], 0\n"
    "s_waitcnt lgkmcnt(8)\n"
    "v_rcp_f16_e32 v232, v234\n"
    "v_mfma_f32_16x16x32_bf16 v[180:183], a[140:143], a[240:243], v[180:183]\n"
    "v_rcp_f16_sdwa v232, v234 dst_sel:WORD_1 dst_unused:UNUSED_PRESERVE src0_sel:WORD_1\n"
    "v_mfma_f32_16x16x32_bf16 v[180:183], v[56:59], a[244:247], v[180:183]\n"
    "ds_write_b128 v253, v[216:219] offset:2048\n"
    "buffer_load_dwordx4 v[156:159], v245, s[40:43], s88 offen offset:64\n"
    "buffer_load_dwordx4 v[148:151], v245, s[40:43], s88 offen offset:0\n"
    "buffer_load_dwordx4 v[212:215], v245, s[40:43], s88 offen offset:128\n"
    "v_mfma_f32_16x16x32_bf16 v[100:103], a[212:215], v[192:195], v[100:103]\n"
    "s_waitcnt lgkmcnt(6)\n"
    "v_fma_mix_f32 v234, v196, v232, 0 op_sel_hi:[1,1,0]\n"
    "v_fma_mix_f32 v232, v196, v232, 0 op_sel:[1,1,0] op_sel_hi:[1,1,0]\n"
    "v_mfma_f32_16x16x32_bf16 v[112:115], a[216:219], v[192:195], v[112:115]\n"
    "ds_read_b128 v[164:167], v244 offset:22528\n"
    "ds_read_u16_d16_hi v204, v204\n"
    "v_mfma_f32_16x16x32_bf16 v[116:119], a[220:223], v[192:195], v[116:119]\n"
    "s_waitcnt lgkmcnt(6)\n"
    "ds_read_u16 v189, v189\n"
    "ds_read_u16_d16_hi v205, v205\n"
    "v_mfma_f32_16x16x32_bf16 v[128:131], v[236:239], v[192:195], v[128:131]\n"
    "v_cvt_pk_bf16_f32 v196, v234, v232\n"
    "v_or_b32_e64 v184, v184, v200\n"
    "v_or_b32_e64 v185, v185, v201\n"
    "v_mfma_f32_16x16x32_bf16 v[104:107], a[208:211], v[196:199], v[104:107]\n"
    "v_lshlrev_b32_sdwa v201, 1, v171 dst_sel:DWORD dst_unused:UNUSED_PAD src0_sel:DWORD src1_sel:WORD_1\n"
    "v_or_b32_e64 v186, v186, v202\n"
    "v_mfma_f32_16x16x32_bf16 v[108:111], a[212:215], v[196:199], v[108:111]\n"
    "v_lshlrev_b32_sdwa v202, 1, v173 dst_sel:DWORD dst_unused:UNUSED_PAD src0_sel:DWORD src1_sel:WORD_1\n"
    "v_cvt_pk_bf16_f32 v96, v96, v97\n"
    "v_mfma_f32_16x16x32_bf16 v[120:123], a[216:219], v[196:199], v[120:123]\n"
    "s_waitcnt lgkmcnt(6)\n"
    "ds_write_b128 v253, v[220:223] offset:5120\n"
    "v_mfma_f32_16x16x32_bf16 v[124:127], a[220:223], v[196:199], v[124:127]\n"
    "v_or_b32_e32 v187, v187, v203\n"
    "v_lshlrev_b32_sdwa v203, 1, v175 dst_sel:DWORD dst_unused:UNUSED_PAD src0_sel:DWORD src1_sel:WORD_1\n"
    "v_mfma_f32_16x16x32_bf16 v[136:139], v[236:239], v[196:199], v[136:139]\n"
    "v_cvt_pk_bf16_f32 v97, v98, v99\n"
    "v_cvt_pk_bf16_f32 v98, v100, v101\n"
    "ds_read_u16 v190, v190\n"
    "ds_read_u16_d16_hi v206, v206\n"
    "s_waitcnt lgkmcnt(6)\n"
    "v_mfma_f32_16x16x32_bf16 v[132:135], v[164:167], v[192:195], v[132:135]\n"
    "s_nop 0\n"
    "ds_read_u16 v191, v191\n"
    "ds_read_u16_d16_hi v207, v207\n"
    "v_mfma_f32_16x16x32_bf16 v[140:143], v[164:167], v[196:199], v[140:143]\n"
    "s_nop 0\n"
    "v_cvt_pk_f16_f32 v192, v168, v169\n"
    "v_lshlrev_b32_sdwa v193, 1, v170 dst_sel:DWORD dst_unused:UNUSED_PAD src0_sel:DWORD src1_sel:WORD_1\n"
    "v_mfma_f32_16x16x32_bf16 v[168:171], a[96:99], a[224:227], 0\n"
    "s_waitcnt lgkmcnt(7)\n"
    "ds_read_u16 v193, v193\n"
    "v_lshlrev_b32_sdwa v194, 1, v172 dst_sel:DWORD dst_unused:UNUSED_PAD src0_sel:DWORD src1_sel:WORD_1\n"
    "v_mfma_f32_16x16x32_bf16 v[168:171], a[144:147], a[228:231], v[168:171]\n"
    "v_lshlrev_b32_sdwa v195, 1, v174 dst_sel:DWORD dst_unused:UNUSED_PAD src0_sel:DWORD src1_sel:WORD_1\n"
    "v_cvt_pk_f16_f32 v196, v176, v177\n"
    "v_mfma_f32_16x16x32_bf16 v[168:171], a[56:59], a[232:235], v[168:171]\n"
    "s_nop 0\n"
    "ds_read_u16_d16_hi v201, v201\n"
    "v_lshlrev_b32_sdwa v197, 1, v178 dst_sel:DWORD dst_unused:UNUSED_PAD src0_sel:DWORD src1_sel:WORD_1\n"
    "v_mfma_f32_16x16x32_bf16 v[172:175], a[100:103], a[224:227], 0\n"
    "s_waitcnt lgkmcnt(6)\n"
    "v_lshlrev_b32_sdwa v198, 1, v180 dst_sel:DWORD dst_unused:UNUSED_PAD src0_sel:DWORD src1_sel:WORD_1\n"
    "v_lshlrev_b32_sdwa v199, 1, v182 dst_sel:DWORD dst_unused:UNUSED_PAD src0_sel:DWORD src1_sel:WORD_1\n"
    "v_mfma_f32_16x16x32_bf16 v[172:175], a[148:151], a[228:231], v[172:175]\n"
    "ds_read_u16 v194, v194\n"
    "v_or_b32_e64 v188, v188, v204\n"
    "v_mfma_f32_16x16x32_bf16 v[172:175], a[60:63], a[232:235], v[172:175]\n"
    "ds_read_u16_d16_hi v202, v202\n"
    "v_or_b32_e64 v189, v189, v205\n"
    "v_lshlrev_b32_sdwa v205, 1, v179 dst_sel:DWORD dst_unused:UNUSED_PAD src0_sel:DWORD src1_sel:WORD_1\n"
    "v_mfma_f32_16x16x32_bf16 v[176:179], a[96:99], a[236:239], 0\n"
    "v_cvt_pk_bf16_f32 v99, v102, v103\n"
    "v_cvt_pk_bf16_f32 v224, v112, v113\n"
    "v_mfma_f32_16x16x32_bf16 v[176:179], a[144:147], a[240:243], v[176:179]\n"
    "s_nop 0\n"
    "ds_read_u16 v195, v195\n"
    "buffer_store_dwordx4 v[96:99], v245, s[68:71], s59 offen offset:0 sc1\n"
    "v_mfma_f32_16x16x32_bf16 v[176:179], a[56:59], a[244:247], v[176:179]\n"
    "s_waitcnt lgkmcnt(7)\n"
    "ds_read_b128 v[0:3], v244 offset:0\n"
    "ds_read_u16_d16_hi v203, v203\n"
    "v_or_b32_e64 v190, v190, v206\n"
    "v_mfma_f32_16x16x32_bf16 v[96:99], a[0:3], v[184:187], 0\n"
    "v_lshlrev_b32_sdwa v206, 1, v181 dst_sel:DWORD dst_unused:UNUSED_PAD src0_sel:DWORD src1_sel:WORD_1\n"
    "v_cvt_pk_bf16_f32 v225, v114, v115\n"
    "v_mfma_f32_16x16x32_bf16 v[100:103], a[4:7], v[184:187], 0\n"
    "s_nop 0\n"
    "ds_read_b128 v[4:7], v244 offset:2048\n"
    "v_cvt_pk_bf16_f32 v226, v116, v117\n"
    "v_mfma_f32_16x16x32_bf16 v[112:115], a[8:11], v[184:187], 0\n"
    "s_waitcnt lgkmcnt(8)\n"
    "ds_read_u16 v197, v197\n"
    "v_cvt_pk_bf16_f32 v227, v118, v119\n"
    "v_cvt_pk_bf16_f32 v228, v128, v129\n"
    "v_cvt_pk_bf16_f32 v229, v130, v131\n"
    "v_or_b32_e64 v191, v191, v207\n"
    "v_mfma_f32_16x16x32_bf16 v[116:119], a[12:15], v[184:187], 0\n"
    "ds_read_u16_d16_hi v205, v205\n"
    "v_lshlrev_b32_sdwa v207, 1, v183 dst_sel:DWORD dst_unused:UNUSED_PAD src0_sel:DWORD src1_sel:WORD_1\n"
    "v_mfma_f32_16x16x32_bf16 v[180:183], a[100:103], a[236:239], 0\n"
    "v_cvt_pk_bf16_f32 v230, v132, v133\n"
    "v_cvt_pk_bf16_f32 v231, v134, v135\n"
    "v_mfma_f32_16x16x32_bf16 v[180:183], a[148:151], a[240:243], v[180:183]\n"
    "s_waitcnt lgkmcnt(8)\n"
    "ds_read_u16 v198, v198\n"
    "v_or_b32_e32 v193, v193, v201\n"
    "v_mfma_f32_16x16x32_bf16 v[180:183], a[60:63], a[244:247], v[180:183]\n"
    "v_cvt_pk_bf16_f32 v216, v104, v105\n"
    "v_cvt_pk_bf16_f32 v217, v106, v107\n"
    "v_mfma_f32_16x16x32_bf16 v[104:107], a[0:3], v[188:191], 0\n"
    "ds_read_u16_d16_hi v206, v206\n"
    "v_cvt_pk_bf16_f32 v218, v108, v109\n"
    "v_cvt_pk_bf16_f32 v219, v110, v111\n"
    "v_mfma_f32_16x16x32_bf16 v[108:111], a[4:7], v[188:191], 0\n"
    "s_waitcnt lgkmcnt(8)\n"
    "ds_read_u16 v199, v199\n"
    "v_or_b32_e32 v194, v194, v202\n"
    "v_cvt_pk_bf16_f32 v220, v120, v121\n"
    "v_cvt_pk_bf16_f32 v221, v122, v123\n"
    "s_waitcnt lgkmcnt(7)\n"
    "v_mfma_f32_16x16x32_bf16 v[128:131], v[0:3], v[184:187], 0\n"
    "s_nop 0\n"
    "ds_read_u16_d16_hi v207, v207\n"
    "v_cvt_pk_bf16_f32 v222, v124, v125\n"
    "v_cvt_pk_bf16_f32 v223, v126, v127\n"
    "v_pk_fma_f16 v232, v192, v192, s48\n"
    "v_mfma_f32_16x16x32_bf16 v[120:123], a[8:11], v[188:191], 0\n"
    "v_cvt_pk_bf16_f32 v160, v136, v137\n"
    "v_cvt_pk_bf16_f32 v161, v138, v139\n"
    "v_mfma_f32_16x16x32_bf16 v[124:127], a[12:15], v[188:191], 0\n"
    "s_waitcnt lgkmcnt(6)\n"
    "v_or_b32_e32 v195, v195, v203\n"
    "v_cvt_pk_bf16_f32 v162, v140, v141\n"
    "v_mfma_f32_16x16x32_bf16 v[132:135], v[4:7], v[184:187], 0\n"
    "v_cvt_pk_bf16_f32 v163, v142, v143\n"
    "v_pk_mul_f16 v232, v192, v232\n"
    "v_mfma_f32_16x16x32_bf16 v[136:139], v[0:3], v[188:191], 0\n"
    "v_exp_f16_e64 v234, v232\n"
    "v_mfma_f32_16x16x32_bf16 v[140:143], v[4:7], v[188:191], 0\n"
    "v_exp_f16_sdwa v234, v232 dst_sel:WORD_1 dst_unused:UNUSED_PRESERVE src0_sel:WORD_1\n"
    "v_lshlrev_b32_sdwa v184, 1, v168 dst_sel:DWORD dst_unused:UNUSED_PAD src0_sel:DWORD src1_sel:WORD_1\n"
    "v_pk_fma_f16 v234, v234, s49, s49\n"
    "v_lshlrev_b32_sdwa v200, 1, v169 dst_sel:DWORD dst_unused:UNUSED_PAD src0_sel:DWORD src1_sel:WORD_1\n"
    "s_waitcnt lgkmcnt(4)\n"
    "v_or_b32_e32 v197, v197, v205\n"
    "v_lshlrev_b32_sdwa v185, 1, v170 dst_sel:DWORD dst_unused:UNUSED_PAD src0_sel:DWORD src1_sel:WORD_1\n"
    "v_lshlrev_b32_sdwa v201, 1, v171 dst_sel:DWORD dst_unused:UNUSED_PAD src0_sel:DWORD src1_sel:WORD_1\n"
    "v_mfma_f32_16x16x32_bf16 v[168:171], a[104:107], a[224:227], 0\n"
    "ds_read_u16 v184, v184\n"
    "ds_read_u16_d16_hi v200, v200\n"
    "v_mfma_f32_16x16x32_bf16 v[168:171], a[152:155], a[228:231], v[168:171]\n"
    "ds_read_u16 v185, v185\n"
    "ds_read_u16_d16_hi v201, v201\n"
    "v_lshlrev_b32_sdwa v186, 1, v172 dst_sel:DWORD dst_unused:UNUSED_PAD src0_sel:DWORD src1_sel:WORD_1\n"
    "v_lshlrev_b32_sdwa v202, 1, v173 dst_sel:DWORD dst_unused:UNUSED_PAD src0_sel:DWORD src1_sel:WORD_1\n"
    "s_waitcnt lgkmcnt(6)\n"
    "v_or_b32_e32 v198, v198, v206\n"
    "v_lshlrev_b32_sdwa v187, 1, v174 dst_sel:DWORD dst_unused:UNUSED_PAD src0_sel:DWORD src1_sel:WORD_1\n"
    "v_lshlrev_b32_sdwa v203, 1, v175 dst_sel:DWORD dst_unused:UNUSED_PAD src0_sel:DWORD src1_sel:WORD_1\n"
    "v_rcp_f16_e32 v232, v234\n"
    "v_mfma_f32_16x16x32_bf16 v[168:171], a[64:67], a[232:235], v[168:171]\n"
    "ds_read_u16 v186, v186\n"
    "buffer_store_dwordx4 v[224:227], v245, s[68:71], s59 offen offset:64 sc1\n"
    "v_rcp_f16_sdwa v232, v234 dst_sel:WORD_1 dst_unused:UNUSED_PRESERVE src0_sel:WORD_1\n"
    "v_mfma_f32_16x16x32_bf16 v[172:175], a[108:111], a[224:227], 0\n"
    "s_waitcnt lgkmcnt(5)\n"
    "ds_read_u16_d16_hi v202, v202\n"
    "ds_read_b128 v[8:11], v244 offset:4096\n"
    "v_mfma_f32_16x16x32_bf16 v[172:175], a[156:159], a[228:231], v[172:175]\n"
    "ds_read_u16 v187, v187\n"
    "v_lshlrev_b32_sdwa v188, 1, v176 dst_sel:DWORD dst_unused:UNUSED_PAD src0_sel:DWORD src1_sel:WORD_1\n"
    "v_lshlrev_b32_sdwa v204, 1, v177 dst_sel:DWORD dst_unused:UNUSED_PAD src0_sel:DWORD src1_sel:WORD_1\n"
    "v_lshlrev_b32_sdwa v189, 1, v178 dst_sel:DWORD dst_unused:UNUSED_PAD src0_sel:DWORD src1_sel:WORD_1\n"
    "v_lshlrev_b32_sdwa v205, 1, v179 dst_sel:DWORD dst_unused:UNUSED_PAD src0_sel:DWORD src1_sel:WORD_1\n"
    "v_lshlrev_b32_sdwa v190, 1, v180 dst_sel:DWORD dst_unused:UNUSED_PAD src0_sel:DWORD src1_sel:WORD_1\n"
    "v_lshlrev_b32_sdwa v206, 1, v181 dst_sel:DWORD dst_unused:UNUSED_PAD src0_sel:DWORD src1_sel:WORD_1\n"
    "v_lshlrev_b32_sdwa v191, 1, v182 dst_sel:DWORD dst_unused:UNUSED_PAD src0_sel:DWORD src1_sel:WORD_1\n"
    "v_fma_mix_f32 v234, v192, v232, 0 op_sel_hi:[1,1,0]\n"
    "v_mfma_f32_16x16x32_bf16 v[172:175], a[68:71], a[232:235], v[172:175]\n"
    "ds_read_u16_d16_hi v203, v203\n"
    "v_or_b32_e32 v199, v199, v207\n"
    "v_lshlrev_b32_sdwa v207, 1, v183 dst_sel:DWORD dst_unused:UNUSED_PAD src0_sel:DWORD src1_sel:WORD_1\n"
    "v_fma_mix_f32 v232, v192, v232, 0 op_sel:[1,1,0] op_sel_hi:[1,1,0]\n"
    "v_mfma_f32_16x16x32_bf16 v[176:179], a[104:107], a[236:239], 0\n"
    "s_waitcnt lgkmcnt(7)\n"
    "ds_read_b128 v[12:15], v244 offset:6144\n"
    "ds_read_u16 v188, v188\n"
    "v_cvt_pk_bf16_f32 v192, v234, v232\n"
    "v_mfma_f32_16x16x32_bf16 v[176:179], a[152:155], a[240:243], v[176:179]\n"
    "v_pk_fma_f16 v232, v196, v196, s48\n"
    "v_or_b32_e64 v184, v184, v200\n"
    "v_mfma_f32_16x16x32_bf16 v[176:179], a[64:67], a[244:247], v[176:179]\n"
    "ds_read_u16_d16_hi v204, v204\n"
    "v_pk_mul_f16 v232, v196, v232\n"
    "v_mfma_f32_16x16x32_bf16 v[180:183], a[108:111], a[236:239], 0\n"
    "v_exp_f16_e32 v234, v232\n"
    "v_mfma_f32_16x16x32_bf16 v[180:183], a[156:159], a[240:243], v[180:183]\n"
    "s_waitcnt lgkmcnt(8)\n"
    "ds_read_u16 v189, v189\n"
    "v_exp_f16_sdwa v234, v232 dst_sel:WORD_1 dst_unused:UNUSED_PRESERVE src0_sel:WORD_1\n"
    "v_mfma_f32_16x16x32_bf16 v[180:183], a[68:71], a[244:247], v[180:183]\n"
    "ds_read_u16_d16_hi v205, v205\n"
    "v_pk_fma_f16 v234, v234, s49, s49\n"
    "v_mfma_f32_16x16x32_bf16 v[96:99], a[16:19], v[192:195], v[96:99]\n"
    "v_rcp_f16_e32 v232, v234\n"
    "v_mfma_f32_16x16x32_bf16 v[100:103], a[20:23], v[192:195], v[100:103]\n"
    "s_waitcnt lgkmcnt(7)\n"
    "ds_read_u16 v190, v190\n"
    "buffer_store_dwordx4 v[228:231], v245, s[68:71], s59 offen offset:128 sc1\n"
    "v_mfma_f32_16x16x32_bf16 v[112:115], a[24:27], v[192:195], v[112:115]\n"
    "ds_read_u16_d16_hi v206, v206\n"
    "v_rcp_f16_sdwa v232, v234 dst_sel:WORD_1 dst_unused:UNUSED_PRESERVE src0_sel:WORD_1\n"
    "v_mfma_f32_16x16x32_bf16 v[116:119], a[28:31], v[192:195], v[116:119]\n"
    "ds_read_u16 v191, v191\n"
    "v_fma_mix_f32 v234, v196, v232, 0 op_sel_hi:[1,1,0]\n"
    "v_mfma_f32_16x16x32_bf16 v[128:131], v[8:11], v[192:195], v[128:131]\n"
    "s_waitcnt lgkmcnt(7)\n"
    "v_fma_mix_f32 v232, v196, v232, 0 op_sel:[1,1,0] op_sel_hi:[1,1,0]\n"
    "v_or_b32_e32 v185, v185, v201\n"
    "v_mfma_f32_16x16x32_bf16 v[132:135], v[12:15], v[192:195], v[132:135]\n"
    "ds_read_u16_d16_hi v207, v207\n"
    "v_cvt_pk_bf16_f32 v196, v234, v232\n"
    "v_cvt_pk_f16_f32 v192, v168, v169\n"
    "v_lshlrev_b32_sdwa v193, 1, v170 dst_sel:DWORD dst_unused:UNUSED_PAD src0_sel:DWORD src1_sel:WORD_1\n"
    "v_mfma_f32_16x16x32_bf16 v[104:107], a[16:19], v[196:199], v[104:107]\n"
    "ds_read_u16 v193, v193\n"
    "v_lshlrev_b32_sdwa v201, 1, v171 dst_sel:DWORD dst_unused:UNUSED_PAD src0_sel:DWORD src1_sel:WORD_1\n"
    "v_mfma_f32_16x16x32_bf16 v[108:111], a[20:23], v[196:199], v[108:111]\n"
    "v_lshlrev_b32_sdwa v194, 1, v172 dst_sel:DWORD dst_unused:UNUSED_PAD src0_sel:DWORD src1_sel:WORD_1\n"
    "v_or_b32_e32 v186, v186, v202\n"
    "v_mfma_f32_16x16x32_bf16 v[120:123], a[24:27], v[196:199], v[120:123]\n"
    "s_waitcnt lgkmcnt(7)\n"
    "ds_read_b128 v[52:55], v244 offset:34816\n"
    "v_lshlrev_b32_sdwa v202, 1, v173 dst_sel:DWORD dst_unused:UNUSED_PAD src0_sel:DWORD src1_sel:WORD_1\n"
    "v_mfma_f32_16x16x32_bf16 v[124:127], a[28:31], v[196:199], v[124:127]\n"
    "ds_read_u16_d16_hi v201, v201\n"
    "v_lshlrev_b32_sdwa v195, 1, v174 dst_sel:DWORD dst_unused:UNUSED_PAD src0_sel:DWORD src1_sel:WORD_1\n"
    "v_mfma_f32_16x16x32_bf16 v[136:139], v[8:11], v[196:199], v[136:139]\n"
    "v_or_b32_e64 v187, v187, v203\n"
    "v_lshlrev_b32_sdwa v203, 1, v175 dst_sel:DWORD dst_unused:UNUSED_PAD src0_sel:DWORD src1_sel:WORD_1\n"
    "v_mfma_f32_16x16x32_bf16 v[140:143], v[12:15], v[196:199], v[140:143]\n"
    "s_nop 0\n"
    "ds_read_u16 v194, v194\n"
    "v_cvt_pk_f16_f32 v196, v176, v177\n"
    "v_mfma_f32_16x16x32_bf16 v[168:171], a[112:115], a[224:227], 0\n"
    "s_waitcnt lgkmcnt(8)\n"
    "v_lshlrev_b32_sdwa v197, 1, v178 dst_sel:DWORD dst_unused:UNUSED_PAD src0_sel:DWORD src1_sel:WORD_1\n"
    "v_lshlrev_b32_sdwa v198, 1, v180 dst_sel:DWORD dst_unused:UNUSED_PAD src0_sel:DWORD src1_sel:WORD_1\n"
    "v_mfma_f32_16x16x32_bf16 v[168:171], a[160:163], a[228:231], v[168:171]\n"
    "ds_read_u16_d16_hi v202, v202\n"
    "v_lshlrev_b32_sdwa v199, 1, v182 dst_sel:DWORD dst_unused:UNUSED_PAD src0_sel:DWORD src1_sel:WORD_1\n"
    "v_mfma_f32_16x16x32_bf16 v[168:171], a[72:75], a[232:235], v[168:171]\n"
    "ds_read_u16 v195, v195\n"
    "buffer_store_dwordx4 v[216:219], v245, s[68:71], s39 offen offset:0 sc1\n"
    "v_mfma_f32_16x16x32_bf16 v[172:175], a[116:119], a[224:227], 0\n"
    "s_waitcnt lgkmcnt(8)\n"
    "ds_read_b128 v[16:19], v244 offset:8192\n"
    "v_or_b32_e32 v188, v188, v204\n"
    "v_mfma_f32_16x16x32_bf16 v[172:175], a[164:167], a[228:231], v[172:175]\n"
    "ds_read_u16_d16_hi v203, v203\n"
    "v_or_b32_e64 v189, v189, v205\n"
    "v_lshlrev_b32_sdwa v205, 1, v179 dst_sel:DWORD dst_unused:UNUSED_PAD src0_sel:DWORD src1_sel:WORD_1\n"
    "v_mfma_f32_16x16x32_bf16 v[176:179], a[112:115], a[236:239], 0\n"
    "s_waitcnt lgkmcnt(8)\n"
    "v_or_b32_e32 v190, v190, v206\n"
    "v_lshlrev_b32_sdwa v206, 1, v181 dst_sel:DWORD dst_unused:UNUSED_PAD src0_sel:DWORD src1_sel:WORD_1\n"
    "v_mfma_f32_16x16x32_bf16 v[176:179], a[160:163], a[240:243], v[176:179]\n"
    "ds_read_b128 v[20:23], v244 offset:10240\n"
    "ds_read_u16 v197, v197\n"
    "v_mfma_f32_16x16x32_bf16 v[176:179], a[72:75], a[244:247], v[176:179]\n"
    "s_waitcnt lgkmcnt(8)\n"
    "v_or_b32_e32 v191, v191, v207\n"
    "v_lshlrev_b32_sdwa v207, 1, v183 dst_sel:DWORD dst_unused:UNUSED_PAD src0_sel:DWORD src1_sel:WORD_1\n"
    "v_mfma_f32_16x16x32_bf16 v[172:175], v[52:55], a[232:235], v[172:175]\n"
    "ds_read_u16_d16_hi v205, v205\n"
    "v_pk_fma_f16 v232, v192, v192, s48\n"
    "v_mfma_f32_16x16x32_bf16 v[180:183], a[116:119], a[236:239], 0\n"
    "v_pk_mul_f16 v232, v192, v232\n"
    "v_lshlrev_b32_sdwa v200, 1, v169 dst_sel:DWORD dst_unused:UNUSED_PAD src0_sel:DWORD src1_sel:WORD_1\n"
    "v_mfma_f32_16x16x32_bf16 v[180:183], a[164:167], a[240:243], v[180:183]\n"
    "s_waitcnt lgkmcnt(8)\n"
    "ds_read_u16 v198, v198\n"
    "v_or_b32_e32 v193, v193, v201\n"
    "v_mfma_f32_16x16x32_bf16 v[180:183], v[52:55], a[244:247], v[180:183]\n"
    "v_exp_f16_e64 v234, v232\n"
    "v_mfma_f32_16x16x32_bf16 v[96:99], a[32:35], v[184:187], v[96:99]\n"
    "ds_read_u16_d16_hi v206, v206\n"
    "v_exp_f16_sdwa v234, v232 dst_sel:WORD_1 dst_unused:UNUSED_PRESERVE src0_sel:WORD_1\n"
    "v_mfma_f32_16x16x32_bf16 v[100:103], a[36:39], v[184:187], v[100:103]\n"
    "s_waitcnt lgkmcnt(8)\n"
    "ds_read_u16 v199, v199\n"
    "v_or_b32_e32 v194, v194, v202\n"
    "v_mfma_f32_16x16x32_bf16 v[112:115], a[40:43], v[184:187], v[112:115]\n"
    "v_pk_fma_f16 v234, v234, s49, s49\n"
    "v_lshlrev_b32_sdwa v201, 1, v171 dst_sel:DWORD dst_unused:UNUSED_PAD src0_sel:DWORD src1_sel:WORD_1\n"
    "v_mfma_f32_16x16x32_bf16 v[116:119], a[44:47], v[184:187], v[116:119]\n"
    "s_waitcnt lgkmcnt(7)\n"
    "ds_read_u16_d16_hi v207, v207\n"
    "v_lshlrev_b32_sdwa v202, 1, v173 dst_sel:DWORD dst_unused:UNUSED_PAD src0_sel:DWORD src1_sel:WORD_1\n"
    "v_mfma_f32_16x16x32_bf16 v[128:131], v[16:19], v[184:187], v[128:131]\n"
    "v_rcp_f16_e64 v232, v234\n"
    "v_mfma_f32_16x16x32_bf16 v[104:107], a[32:35], v[188:191], v[104:107]\n"
    "s_waitcnt lgkmcnt(6)\n"
    "ds_read_b128 v[60:63], v244 offset:38912\n"
    "v_or_b32_e64 v195, v195, v203\n"
    "v_mfma_f32_16x16x32_bf16 v[132:135], v[20:23], v[184:187], v[132:135]\n"
    "ds_read_u16_d16_hi v200, v200\n"
    "v_lshlrev_b32_sdwa v184, 1, v168 dst_sel:DWORD dst_unused:UNUSED_PAD src0_sel:DWORD src1_sel:WORD_1\n"
    "v_mfma_f32_16x16x32_bf16 v[108:111], a[36:39], v[188:191], v[108:111]\n"
    "v_lshlrev_b32_sdwa v185, 1, v170 dst_sel:DWORD dst_unused:UNUSED_PAD src0_sel:DWORD src1_sel:WORD_1\n"
    "v_lshlrev_b32_sdwa v186, 1, v172 dst_sel:DWORD dst_unused:UNUSED_PAD src0_sel:DWORD src1_sel:WORD_1\n"
    "v_mfma_f32_16x16x32_bf16 v[120:123], a[40:43], v[188:191], v[120:123]\n"
    "ds_read_u16 v184, v184\n"
    "buffer_store_dwordx4 v[220:223], v245, s[68:71], s39 offen offset:64 sc1\n"
    "v_mfma_f32_16x16x32_bf16 v[124:127], a[44:47], v[188:191], v[124:127]\n"
    "s_waitcnt lgkmcnt(7)\n"
    "ds_read_u16 v185, v185\n"
    "v_or_b32_e32 v197, v197, v205\n"
    "v_mfma_f32_16x16x32_bf16 v[136:139], v[16:19], v[188:191], v[136:139]\n"
    "ds_read_u16_d16_hi v201, v201\n"
    "v_lshlrev_b32_sdwa v187, 1, v174 dst_sel:DWORD dst_unused:UNUSED_PAD src0_sel:DWORD src1_sel:WORD_1\n"
    "v_mfma_f32_16x16x32_bf16 v[140:143], v[20:23], v[188:191], v[140:143]\n"
    "v_lshlrev_b32_sdwa v203, 1, v175 dst_sel:DWORD dst_unused:UNUSED_PAD src0_sel:DWORD src1_sel:WORD_1\n"
    "v_rcp_f16_sdwa v232, v234 dst_sel:WORD_1 dst_unused:UNUSED_PRESERVE src0_sel:WORD_1\n"
    "v_mfma_f32_16x16x32_bf16 v[168:171], a[120:123], a[224:227], 0\n"
    "s_waitcnt lgkmcnt(7)\n"
    "ds_read_u16 v186, v186\n"
    "v_or_b32_e32 v198, v198, v206\n"
    "v_mfma_f32_16x16x32_bf16 v[168:171], a[168:171], a[228:231], v[168:171]\n"
    "ds_read_u16_d16_hi v202, v202\n"
    "v_lshlrev_b32_sdwa v188, 1, v176 dst_sel:DWORD dst_unused:UNUSED_PAD src0_sel:DWORD src1_sel:WORD_1\n"
    "v_mfma_f32_16x16x32_bf16 v[168:171], a[76:79], a[232:235], v[168:171]\n"
    "v_lshlrev_b32_sdwa v204, 1, v177 dst_sel:DWORD dst_unused:UNUSED_PAD src0_sel:DWORD src1_sel:WORD_1\n"
    "v_lshlrev_b32_sdwa v189, 1, v178 dst_sel:DWORD dst_unused:UNUSED_PAD src0_sel:DWORD src1_sel:WORD_1\n"
    "v_mfma_f32_16x16x32_bf16 v[172:175], a[124:127], a[224:227], 0\n"
    "s_nop 0\n"
    "ds_read_b128 v[24:27], v244 offset:12288\n"
    "v_lshlrev_b32_sdwa v205, 1, v179 dst_sel:DWORD dst_unused:UNUSED_PAD src0_sel:DWORD src1_sel:WORD_1\n"
    "v_mfma_f32_16x16x32_bf16 v[172:175], a[172:175], a[228:231], v[172:175]\n"
    "s_waitcnt lgkmcnt(7)\n"
    "ds_read_u16 v187, v187\n"
    "v_lshlrev_b32_sdwa v190, 1, v180 dst_sel:DWORD dst_unused:UNUSED_PAD src0_sel:DWORD src1_sel:WORD_1\n"
    "v_lshlrev_b32_sdwa v206, 1, v181 dst_sel:DWORD dst_unused:UNUSED_PAD src0_sel:DWORD src1_sel:WORD_1\n"
    "v_lshlrev_b32_sdwa v191, 1, v182 dst_sel:DWORD dst_unused:UNUSED_PAD src0_sel:DWORD src1_sel:WORD_1\n"
    "v_fma_mix_f32 v234, v192, v232, 0 op_sel_hi:[1,1,0]\n"
    "v_mfma_f32_16x16x32_bf16 v[172:175], v[60:63], a[232:235], v[172:175]\n"
    "ds_read_u16_d16_hi v203, v203\n"
    "v_or_b32_e64 v199, v199, v207\n"
    "v_lshlrev_b32_sdwa v207, 1, v183 dst_sel:DWORD dst_unused:UNUSED_PAD src0_sel:DWORD src1_sel:WORD_1\n"
    "v_fma_mix_f32 v232, v192, v232, 0 op_sel:[1,1,0] op_sel_hi:[1,1,0]\n"
    "v_mfma_f32_16x16x32_bf16 v[176:179], a[120:123], a[236:239], 0\n"
    "s_nop 0\n"
    "ds_read_b128 v[28:31], v244 offset:14336\n"
    "v_cvt_pk_bf16_f32 v192, v234, v232\n"
    "v_mfma_f32_16x16x32_bf16 v[176:179], a[168:171], a[240:243], v[176:179]\n"
    "s_waitcnt lgkmcnt(8)\n"
    "ds_read_u16 v188, v188\n"
    "v_pk_fma_f16 v232, v196, v196, s48\n"
    "v_mfma_f32_16x16x32_bf16 v[176:179], a[76:79], a[244:247], v[176:179]\n"
    "v_pk_mul_f16 v232, v196, v232\n"
    "v_or_b32_e64 v184, v184, v200\n"
    "v_mfma_f32_16x16x32_bf16 v[180:183], a[124:127], a[236:239], 0\n"
    "ds_read_u16_d16_hi v204, v204\n"
    "v_exp_f16_e32 v234, v232\n"
    "v_mfma_f32_16x16x32_bf16 v[180:183], a[172:175], a[240:243], v[180:183]\n"
    "s_waitcnt lgkmcnt(8)\n"
    "v_exp_f16_sdwa v234, v232 dst_sel:WORD_1 dst_unused:UNUSED_PRESERVE src0_sel:WORD_1\n"
    "v_mfma_f32_16x16x32_bf16 v[180:183], v[60:63], a[244:247], v[180:183]\n"
    "ds_read_u16 v189, v189\n"
    "buffer_store_dwordx4 v[160:163], v245, s[68:71], s39 offen offset:128 sc1\n"
    "v_mfma_f32_16x16x32_bf16 v[96:99], a[176:179], v[192:195], v[96:99]\n"
    "ds_read_u16_d16_hi v205, v205\n"
    "v_pk_fma_f16 v234, v234, s49, s49\n"
    "v_mfma_f32_16x16x32_bf16 v[100:103], a[180:183], v[192:195], v[100:103]\n"
    "s_waitcnt lgkmcnt(7)\n"
    "ds_read_u16 v190, v190\n"
    "v_rcp_f16_e32 v232, v234\n"
    "v_mfma_f32_16x16x32_bf16 v[112:115], a[184:187], v[192:195], v[112:115]\n"
    "v_rcp_f16_sdwa v232, v234 dst_sel:WORD_1 dst_unused:UNUSED_PRESERVE src0_sel:WORD_1\n"
    "v_mfma_f32_16x16x32_bf16 v[116:119], a[188:191], v[192:195], v[116:119]\n"
    "ds_read_b128 a[224:227], v243 offset:24640\n"
    "ds_read_u16_d16_hi v206, v206\n"
    "v_fma_mix_f32 v234, v196, v232, 0 op_sel_hi:[1,1,0]\n"
    "v_mfma_f32_16x16x32_bf16 v[128:131], v[24:27], v[192:195], v[128:131]\n"
    "v_fma_mix_f32 v232, v196, v232, 0 op_sel:[1,1,0] op_sel_hi:[1,1,0]\n"
    "v_or_b32_e32 v185, v185, v201\n"
    "s_waitcnt lgkmcnt(7)\n"
    "v_mfma_f32_16x16x32_bf16 v[132:135], v[28:31], v[192:195], v[132:135]\n"
    "ds_read_b128 a[228:231], v243 offset:25664\n"
    "v_cvt_pk_bf16_f32 v196, v234, v232\n"
    "v_cvt_pk_f16_f32 v192, v168, v169\n"
    "v_lshlrev_b32_sdwa v193, 1, v170 dst_sel:DWORD dst_unused:UNUSED_PAD src0_sel:DWORD src1_sel:WORD_1\n"
    "v_mfma_f32_16x16x32_bf16 v[104:107], a[176:179], v[196:199], v[104:107]\n"
    "ds_read_b128 a[232:235], v243 offset:26688\n"
    "ds_read_u16 v191, v191\n"
    "v_mfma_f32_16x16x32_bf16 v[108:111], a[180:183], v[196:199], v[108:111]\n"
    "v_lshlrev_b32_sdwa v201, 1, v171 dst_sel:DWORD dst_unused:UNUSED_PAD src0_sel:DWORD src1_sel:WORD_1\n"
    "v_lshlrev_b32_sdwa v194, 1, v172 dst_sel:DWORD dst_unused:UNUSED_PAD src0_sel:DWORD src1_sel:WORD_1\n"
    "v_mfma_f32_16x16x32_bf16 v[120:123], a[184:187], v[196:199], v[120:123]\n"
    "ds_read_b128 a[236:239], v243 offset:27712\n"
    "v_or_b32_e32 v186, v186, v202\n"
    "v_mfma_f32_16x16x32_bf16 v[124:127], a[188:191], v[196:199], v[124:127]\n"
    "s_waitcnt lgkmcnt(9)\n"
    "ds_read_u16_d16_hi v207, v207\n"
    "v_lshlrev_b32_sdwa v202, 1, v173 dst_sel:DWORD dst_unused:UNUSED_PAD src0_sel:DWORD src1_sel:WORD_1\n"
    "v_mfma_f32_16x16x32_bf16 v[136:139], v[24:27], v[196:199], v[136:139]\n"
    "v_lshlrev_b32_sdwa v195, 1, v174 dst_sel:DWORD dst_unused:UNUSED_PAD src0_sel:DWORD src1_sel:WORD_1\n"
    "v_or_b32_e64 v187, v187, v203\n"
    "v_mfma_f32_16x16x32_bf16 v[140:143], v[28:31], v[196:199], v[140:143]\n"
    "ds_read_b128 a[240:243], v243 offset:28736\n"
    "v_lshlrev_b32_sdwa v203, 1, v175 dst_sel:DWORD dst_unused:UNUSED_PAD src0_sel:DWORD src1_sel:WORD_1\n"
    "v_cvt_pk_f16_f32 v196, v176, v177\n"
    "v_lshlrev_b32_sdwa v197, 1, v178 dst_sel:DWORD dst_unused:UNUSED_PAD src0_sel:DWORD src1_sel:WORD_1\n"
    "v_lshlrev_b32_sdwa v198, 1, v180 dst_sel:DWORD dst_unused:UNUSED_PAD src0_sel:DWORD src1_sel:WORD_1\n"
    "v_lshlrev_b32_sdwa v199, 1, v182 dst_sel:DWORD dst_unused:UNUSED_PAD src0_sel:DWORD src1_sel:WORD_1\n"
    "v_or_b32_e32 v188, v188, v204\n"
    "s_waitcnt lgkmcnt(9)\n"
    "v_or_b32_e32 v189, v189, v205\n"
    "s_waitcnt lgkmcnt(7)\n"
    "v_mfma_f32_16x16x32_bf16 v[168:171], a[80:83], a[224:227], 0\n"
    "ds_read_b128 a[244:247], v243 offset:29760\n"
    "ds_read_u16 v193, v193\n"
    "v_mfma_f32_16x16x32_bf16 v[172:175], a[84:87], a[224:227], 0\n"
    "ds_read_b128 v[48:51], v244 offset:32768\n"
    "ds_read_u16_d16_hi v201, v201\n"
    "v_lshlrev_b32_sdwa v205, 1, v179 dst_sel:DWORD dst_unused:UNUSED_PAD src0_sel:DWORD src1_sel:WORD_1\n"
    "v_pk_fma_f16 v232, v192, v192, s48\n"
    "s_waitcnt lgkmcnt(9)\n"
    "v_mfma_f32_16x16x32_bf16 v[168:171], a[128:131], a[228:231], v[168:171]\n"
    "s_nop 0\n"
    "v_or_b32_e32 v190, v190, v206\n"
    "v_lshlrev_b32_sdwa v206, 1, v181 dst_sel:DWORD dst_unused:UNUSED_PAD src0_sel:DWORD src1_sel:WORD_1\n"
    "v_mfma_f32_16x16x32_bf16 v[172:175], a[132:135], a[228:231], v[172:175]\n"
    "s_waitcnt lgkmcnt(8)\n"
    "ds_read_u16 v194, v194\n"
    "v_pk_mul_f16 v232, v192, v232\n"
    "v_mfma_f32_16x16x32_bf16 v[168:171], a[48:51], a[232:235], v[168:171]\n"
    "ds_read_u16_d16_hi v202, v202\n"
    "v_exp_f16_e32 v234, v232\n"
    "v_mfma_f32_16x16x32_bf16 v[96:99], a[192:195], v[184:187], v[96:99]\n"
    "s_waitcnt lgkmcnt(8)\n"
    "v_exp_f16_sdwa v234, v232 dst_sel:WORD_1 dst_unused:UNUSED_PRESERVE src0_sel:WORD_1\n"
    "v_mfma_f32_16x16x32_bf16 v[176:179], a[80:83], a[236:239], 0\n"
    "ds_read_u16 v195, v195\n"
    "v_pk_fma_f16 v234, v234, s49, s49\n"
    "v_mfma_f32_16x16x32_bf16 v[100:103], a[196:199], v[184:187], v[100:103]\n"
    "v_lshlrev_b32_sdwa v200, 1, v169 dst_sel:DWORD dst_unused:UNUSED_PAD src0_sel:DWORD src1_sel:WORD_1\n"
    "v_rcp_f16_e32 v232, v234\n"
    "v_mfma_f32_16x16x32_bf16 v[112:115], a[200:203], v[184:187], v[112:115]\n"
    "s_waitcnt lgkmcnt(7)\n"
    "ds_read_b128 v[32:35], v244 offset:16384\n"
    "ds_read_u16_d16_hi v203, v203\n"
    "v_mfma_f32_16x16x32_bf16 v[176:179], a[128:131], a[240:243], v[176:179]\n"
    "s_waitcnt lgkmcnt(8)\n"
    "v_or_b32_e32 v191, v191, v207\n"
    "v_lshlrev_b32_sdwa v207, 1, v183 dst_sel:DWORD dst_unused:UNUSED_PAD src0_sel:DWORD src1_sel:WORD_1\n"
    "v_mfma_f32_16x16x32_bf16 v[176:179], a[48:51], a[244:247], v[176:179]\n"
    "s_nop 0\n"
    "ds_read_b128 v[36:39], v244 offset:18432\n"
    "v_rcp_f16_sdwa v232, v234 dst_sel:WORD_1 dst_unused:UNUSED_PRESERVE src0_sel:WORD_1\n"
    "v_mfma_f32_16x16x32_bf16 v[180:183], a[84:87], a[236:239], 0\n"
    "s_waitcnt lgkmcnt(7)\n"
    "ds_read_u16 v197, v197\n"
    "v_fma_mix_f32 v234, v192, v232, 0 op_sel_hi:[1,1,0]\n"
    "v_mfma_f32_16x16x32_bf16 v[172:175], v[48:51], a[232:235], v[172:175]\n"
    "v_fma_mix_f32 v232, v192, v232, 0 op_sel:[1,1,0] op_sel_hi:[1,1,0]\n"
    "v_lshlrev_b32_sdwa v204, 1, v177 dst_sel:DWORD dst_unused:UNUSED_PAD src0_sel:DWORD src1_sel:WORD_1\n"
    "v_mfma_f32_16x16x32_bf16 v[180:183], a[132:135], a[240:243], v[180:183]\n"
    "ds_read_u16_d16_hi v205, v205\n"
    "v_cvt_pk_bf16_f32 v192, v234, v232\n"
    "v_mfma_f32_16x16x32_bf16 v[180:183], v[48:51], a[244:247], v[180:183]\n"
    "s_waitcnt lgkmcnt(8)\n"
    "ds_read_u16 v198, v198\n"
    "v_or_b32_e32 v193, v193, v201\n"
    "v_mfma_f32_16x16x32_bf16 v[116:119], a[204:207], v[184:187], v[116:119]\n"
    "v_lshlrev_b32_sdwa v201, 1, v171 dst_sel:DWORD dst_unused:UNUSED_PAD src0_sel:DWORD src1_sel:WORD_1\n"
    "v_pk_fma_f16 v232, v196, v196, s48\n"
    "v_mfma_f32_16x16x32_bf16 v[104:107], a[192:195], v[188:191], v[104:107]\n"
    "ds_read_u16_d16_hi v206, v206\n"
    "v_pk_mul_f16 v232, v196, v232\n"
    "v_mfma_f32_16x16x32_bf16 v[108:111], a[196:199], v[188:191], v[108:111]\n"
    "v_exp_f16_e32 v234, v232\n"
    "v_mfma_f32_16x16x32_bf16 v[120:123], a[200:203], v[188:191], v[120:123]\n"
    "s_waitcnt lgkmcnt(8)\n"
    "ds_read_u16 v199, v199\n"
    "v_or_b32_e32 v194, v194, v202\n"
    "v_mfma_f32_16x16x32_bf16 v[124:127], a[204:207], v[188:191], v[124:127]\n"
    "s_waitcnt lgkmcnt(7)\n"
    "v_lshlrev_b32_sdwa v202, 1, v173 dst_sel:DWORD dst_unused:UNUSED_PAD src0_sel:DWORD src1_sel:WORD_1\n"
    "v_exp_f16_sdwa v234, v232 dst_sel:WORD_1 dst_unused:UNUSED_PRESERVE src0_sel:WORD_1\n"
    "v_mfma_f32_16x16x32_bf16 v[128:131], v[32:35], v[184:187], v[128:131]\n"
    "ds_read_u16_d16_hi v207, v207\n"
    "v_pk_fma_f16 v234, v234, s49, s49\n"
    "v_mfma_f32_16x16x32_bf16 v[136:139], v[32:35], v[188:191], v[136:139]\n"
    "ds_read_u16_d16_hi v200, v200\n"
    "v_rcp_f16_e32 v232, v234\n"
    "s_waitcnt lgkmcnt(7)\n"
    "v_mfma_f32_16x16x32_bf16 v[132:135], v[36:39], v[184:187], v[132:135]\n"
    "ds_read_u16_d16_hi v201, v201\n"
    "v_or_b32_e64 v195, v195, v203\n"
    "v_mfma_f32_16x16x32_bf16 v[140:143], v[36:39], v[188:191], v[140:143]\n"
    "v_lshlrev_b32_sdwa v184, 1, v168 dst_sel:DWORD dst_unused:UNUSED_PAD src0_sel:DWORD src1_sel:WORD_1\n"
    "v_lshlrev_b32_sdwa v185, 1, v170 dst_sel:DWORD dst_unused:UNUSED_PAD src0_sel:DWORD src1_sel:WORD_1\n"
    "v_mfma_f32_16x16x32_bf16 v[168:171], a[88:91], a[224:227], 0\n"
    "ds_read_u16 v184, v184\n"
    "v_lshlrev_b32_sdwa v186, 1, v172 dst_sel:DWORD dst_unused:UNUSED_PAD src0_sel:DWORD src1_sel:WORD_1\n"
    "v_mfma_f32_16x16x32_bf16 v[168:171], a[136:139], a[228:231], v[168:171]\n"
    "v_lshlrev_b32_sdwa v187, 1, v174 dst_sel:DWORD dst_unused:UNUSED_PAD src0_sel:DWORD src1_sel:WORD_1\n"
    "v_lshlrev_b32_sdwa v203, 1, v175 dst_sel:DWORD dst_unused:UNUSED_PAD src0_sel:DWORD src1_sel:WORD_1\n"
    "v_mfma_f32_16x16x32_bf16 v[168:171], a[52:55], a[232:235], v[168:171]\n"
    "s_waitcnt lgkmcnt(7)\n"
    "ds_read_u16 v185, v185\n"
    "v_or_b32_e32 v197, v197, v205\n"
    "v_mfma_f32_16x16x32_bf16 v[172:175], a[92:95], a[224:227], 0\n"
    "v_lshlrev_b32_sdwa v188, 1, v176 dst_sel:DWORD dst_unused:UNUSED_PAD src0_sel:DWORD src1_sel:WORD_1\n"
    "v_lshlrev_b32_sdwa v189, 1, v178 dst_sel:DWORD dst_unused:UNUSED_PAD src0_sel:DWORD src1_sel:WORD_1\n"
    "v_mfma_f32_16x16x32_bf16 v[172:175], a[140:143], a[228:231], v[172:175]\n"
    "ds_read_u16 v186, v186\n"
    "v_lshlrev_b32_sdwa v205, 1, v179 dst_sel:DWORD dst_unused:UNUSED_PAD src0_sel:DWORD src1_sel:WORD_1\n"
    "v_mfma_f32_16x16x32_bf16 v[172:175], v[56:59], a[232:235], v[172:175]\n"
    "s_waitcnt lgkmcnt(7)\n"
    "ds_read_u16_d16_hi v202, v202\n"
    "v_or_b32_e32 v198, v198, v206\n"
    "v_mfma_f32_16x16x32_bf16 v[176:179], a[88:91], a[236:239], 0\n"
    "v_lshlrev_b32_sdwa v190, 1, v180 dst_sel:DWORD dst_unused:UNUSED_PAD src0_sel:DWORD src1_sel:WORD_1\n"
    "v_lshlrev_b32_sdwa v206, 1, v181 dst_sel:DWORD dst_unused:UNUSED_PAD src0_sel:DWORD src1_sel:WORD_1\n"
    "v_mfma_f32_16x16x32_bf16 v[176:179], a[136:139], a[240:243], v[176:179]\n"
    "ds_read_b128 v[40:43], v244 offset:20480\n"
    "v_lshlrev_b32_sdwa v191, 1, v182 dst_sel:DWORD dst_unused:UNUSED_PAD src0_sel:DWORD src1_sel:WORD_1\n"
    "v_mfma_f32_16x16x32_bf16 v[176:179], a[52:55], a[244:247], v[176:179]\n"
    "ds_read_u16 v187, v187\n"
    "v_rcp_f16_sdwa v232, v234 dst_sel:WORD_1 dst_unused:UNUSED_PRESERVE src0_sel:WORD_1\n"
    "v_mfma_f32_16x16x32_bf16 v[96:99], a[208:211], v[192:195], v[96:99]\n"
    "s_waitcnt lgkmcnt(8)\n"
    "v_or_b32_e32 v199, v199, v207\n"
    "v_lshlrev_b32_sdwa v207, 1, v183 dst_sel:DWORD dst_unused:UNUSED_PAD src0_sel:DWORD src1_sel:WORD_1\n"
    "v_mfma_f32_16x16x32_bf16 v[180:183], a[92:95], a[236:239], 0\n"
    "ds_read_u16_d16_hi v203, v203\n"
    "v_fma_mix_f32 v234, v196, v232, 0 op_sel_hi:[1,1,0]\n"
    "v_mfma_f32_16x16x32_bf16 v[180:183], a[140:143], a[240:243], v[180:183]\n"
    "ds_read_b128 v[44:47], v244 offset:22528\n"
    "v_fma_mix_f32 v232, v196, v232, 0 op_sel:[1,1,0] op_sel_hi:[1,1,0]\n"
    "v_mfma_f32_16x16x32_bf16 v[180:183], v[56:59], a[244:247], v[180:183]\n"
    "ds_read_u16 v188, v188\n"
    "v_cvt_pk_bf16_f32 v196, v234, v232\n"
    "v_mfma_f32_16x16x32_bf16 v[100:103], a[212:215], v[192:195], v[100:103]\n"
    "v_cvt_pk_bf16_f32 v96, v96, v97\n"
    "v_cvt_pk_bf16_f32 v97, v98, v99\n"
    "v_mfma_f32_16x16x32_bf16 v[112:115], a[216:219], v[192:195], v[112:115]\n"
    "s_waitcnt lgkmcnt(8)\n"
    "ds_read_u16_d16_hi v204, v204\n"
    "v_or_b32_e32 v184, v184, v200\n"
    "v_mfma_f32_16x16x32_bf16 v[116:119], a[220:223], v[192:195], v[116:119]\n"
    "s_nop 0\n"
    "v_mfma_f32_16x16x32_bf16 v[104:107], a[208:211], v[196:199], v[104:107]\n"
    "s_waitcnt lgkmcnt(8)\n"
    "ds_read_u16 v189, v189\n"
    "v_or_b32_e64 v185, v185, v201\n"
    "v_mfma_f32_16x16x32_bf16 v[108:111], a[212:215], v[196:199], v[108:111]\n"
    "v_lshlrev_b32_sdwa v201, 1, v171 dst_sel:DWORD dst_unused:UNUSED_PAD src0_sel:DWORD src1_sel:WORD_1\n"
    "v_cvt_pk_bf16_f32 v98, v100, v101\n"
    "v_mfma_f32_16x16x32_bf16 v[120:123], a[216:219], v[196:199], v[120:123]\n"
    "ds_read_u16_d16_hi v205, v205\n"
    "v_cvt_pk_bf16_f32 v99, v102, v103\n"
    "v_mfma_f32_16x16x32_bf16 v[124:127], a[220:223], v[196:199], v[124:127]\n"
    "s_waitcnt lgkmcnt(7)\n"
    "ds_read_u16 v190, v190\n"
    "v_or_b32_e32 v186, v186, v202\n"
    "v_mfma_f32_16x16x32_bf16 v[128:131], v[40:43], v[192:195], v[128:131]\n"
    "v_lshlrev_b32_sdwa v202, 1, v173 dst_sel:DWORD dst_unused:UNUSED_PAD src0_sel:DWORD src1_sel:WORD_1\n"
    "v_cvt_pk_bf16_f32 v112, v112, v113\n"
    "v_mfma_f32_16x16x32_bf16 v[136:139], v[40:43], v[196:199], v[136:139]\n"
    "v_cvt_pk_bf16_f32 v113, v114, v115\n"
    "v_cvt_pk_bf16_f32 v114, v116, v117\n"
    "v_cvt_pk_bf16_f32 v115, v118, v119\n"
    "v_cvt_pk_bf16_f32 v104, v104, v105\n"
    "v_cvt_pk_bf16_f32 v105, v106, v107\n"
    "v_cvt_pk_bf16_f32 v128, v128, v129\n"
    "v_cvt_pk_bf16_f32 v129, v130, v131\n"
    "v_cvt_pk_bf16_f32 v106, v108, v109\n"
    "s_waitcnt lgkmcnt(5)\n"
    "v_mfma_f32_16x16x32_bf16 v[132:135], v[44:47], v[192:195], v[132:135]\n"
    "s_nop 0\n"
    "ds_write_b128 v243, v[96:99] offset:24640\n"
    "v_mfma_f32_16x16x32_bf16 v[140:143], v[44:47], v[196:199], v[140:143]\n"
    "v_cvt_pk_f16_f32 v192, v168, v169\n"
    "v_lshlrev_b32_sdwa v193, 1, v170 dst_sel:DWORD dst_unused:UNUSED_PAD src0_sel:DWORD src1_sel:WORD_1\n"
    "v_mfma_f32_16x16x32_bf16 v[168:171], a[96:99], a[224:227], 0\n"
    "v_lshlrev_b32_sdwa v194, 1, v172 dst_sel:DWORD dst_unused:UNUSED_PAD src0_sel:DWORD src1_sel:WORD_1\n"
    "v_lshlrev_b32_sdwa v195, 1, v174 dst_sel:DWORD dst_unused:UNUSED_PAD src0_sel:DWORD src1_sel:WORD_1\n"
    "v_mfma_f32_16x16x32_bf16 v[168:171], a[144:147], a[228:231], v[168:171]\n"
    "s_nop 0\n"
    "ds_read_u16_d16_hi v206, v206\n"
    "ds_read_u16 v191, v191\n"
    "v_mfma_f32_16x16x32_bf16 v[168:171], a[56:59], a[232:235], v[168:171]\n"
    "s_waitcnt lgkmcnt(6)\n"
    "ds_read_u16_d16_hi v207, v207\n"
    "ds_read_u16 v193, v193\n"
    "v_cvt_pk_f16_f32 v196, v176, v177\n"
    "v_lshlrev_b32_sdwa v197, 1, v178 dst_sel:DWORD dst_unused:UNUSED_PAD src0_sel:DWORD src1_sel:WORD_1\n"
    "v_lshlrev_b32_sdwa v198, 1, v180 dst_sel:DWORD dst_unused:UNUSED_PAD src0_sel:DWORD src1_sel:WORD_1\n"
    "v_lshlrev_b32_sdwa v199, 1, v182 dst_sel:DWORD dst_unused:UNUSED_PAD src0_sel:DWORD src1_sel:WORD_1\n"
    "v_or_b32_e64 v187, v187, v203\n"
    "v_lshlrev_b32_sdwa v203, 1, v175 dst_sel:DWORD dst_unused:UNUSED_PAD src0_sel:DWORD src1_sel:WORD_1\n"
    "v_mfma_f32_16x16x32_bf16 v[172:175], a[100:103], a[224:227], 0\n"
    "v_or_b32_e64 v188, v188, v204\n"
    "v_cvt_pk_bf16_f32 v130, v132, v133\n"
    "v_mfma_f32_16x16x32_bf16 v[172:175], a[148:151], a[228:231], v[172:175]\n"
    "s_waitcnt lgkmcnt(6)\n"
    "ds_write_b128 v243, v[112:115] offset:26688\n"
    "v_mfma_f32_16x16x32_bf16 v[172:175], a[60:63], a[232:235], v[172:175]\n"
    "v_or_b32_e32 v189, v189, v205\n"
    "v_lshlrev_b32_sdwa v205, 1, v179 dst_sel:DWORD dst_unused:UNUSED_PAD src0_sel:DWORD src1_sel:WORD_1\n"
    "v_mfma_f32_16x16x32_bf16 v[176:179], a[96:99], a[236:239], 0\n"
    "v_cvt_pk_bf16_f32 v131, v134, v135\n"
    "v_cvt_pk_bf16_f32 v107, v110, v111\n"
    "v_mfma_f32_16x16x32_bf16 v[176:179], a[144:147], a[240:243], v[176:179]\n"
    "ds_read_u16_d16_hi v201, v201\n"
    "ds_write_b128 v243, v[128:131] offset:28736\n"
    "v_mfma_f32_16x16x32_bf16 v[176:179], a[56:59], a[244:247], v[176:179]\n"
    "s_waitcnt lgkmcnt(6)\n"
    "v_or_b32_e32 v190, v190, v206\n"
    "v_lshlrev_b32_sdwa v206, 1, v181 dst_sel:DWORD dst_unused:UNUSED_PAD src0_sel:DWORD src1_sel:WORD_1\n"
    "v_mfma_f32_16x16x32_bf16 v[96:99], a[0:3], v[184:187], 0\n"
    "v_cvt_pk_bf16_f32 v120, v120, v121\n"
    "v_cvt_pk_bf16_f32 v121, v122, v123\n"
    "v_mfma_f32_16x16x32_bf16 v[100:103], a[4:7], v[184:187], 0\n"
    "ds_read_u16 v194, v194\n"
    "ds_write_b128 v243, v[104:107] offset:25664\n"
    "v_mfma_f32_16x16x32_bf16 v[112:115], a[8:11], v[184:187], 0\n"
    "s_waitcnt lgkmcnt(6)\n"
    "v_or_b32_e32 v191, v191, v207\n"
    "v_lshlrev_b32_sdwa v207, 1, v183 dst_sel:DWORD dst_unused:UNUSED_PAD src0_sel:DWORD src1_sel:WORD_1\n"
    "v_mfma_f32_16x16x32_bf16 v[180:183], a[100:103], a[236:239], 0\n"
    "v_cvt_pk_bf16_f32 v122, v124, v125\n"
    "v_cvt_pk_bf16_f32 v123, v126, v127\n"
    "v_mfma_f32_16x16x32_bf16 v[180:183], a[148:151], a[240:243], v[180:183]\n"
    "ds_read_u16_d16_hi v202, v202\n"
    "ds_write_b128 v243, v[120:123] offset:27712\n"
    "v_mfma_f32_16x16x32_bf16 v[180:183], a[60:63], a[244:247], v[180:183]\n"
    "v_cvt_pk_bf16_f32 v136, v136, v137\n"
    "v_pk_fma_f16 v232, v192, v192, s48\n"
    "v_mfma_f32_16x16x32_bf16 v[116:119], a[12:15], v[184:187], 0\n"
    "s_waitcnt lgkmcnt(4)\n"
    "v_or_b32_e32 v193, v193, v201\n"
    "v_cvt_pk_bf16_f32 v137, v138, v139\n"
    "v_mfma_f32_16x16x32_bf16 v[128:131], v[0:3], v[184:187], 0\n"
    "ds_read_u16 v195, v195\n"
    "ds_read_u16_d16_hi v203, v203\n"
    "v_mfma_f32_16x16x32_bf16 v[132:135], v[4:7], v[184:187], 0\n"
    "ds_read_u16 v197, v197\n"
    "ds_read_u16_d16_hi v205, v205\n"
    "v_mfma_f32_16x16x32_bf16 v[104:107], a[0:3], v[188:191], 0\n"
    "v_cvt_pk_bf16_f32 v138, v140, v141\n"
    "v_cvt_pk_bf16_f32 v139, v142, v143\n"
    "v_mfma_f32_16x16x32_bf16 v[108:111], a[4:7], v[188:191], 0\n"
    "v_pk_mul_f16 v232, v192, v232\n"
    "v_lshlrev_b32_sdwa v184, 1, v168 dst_sel:DWORD dst_unused:UNUSED_PAD src0_sel:DWORD src1_sel:WORD_1\n"
    "v_mfma_f32_16x16x32_bf16 v[120:123], a[8:11], v[188:191], 0\n"
    "v_exp_f16_e64 v234, v232\n"
    "v_mfma_f32_16x16x32_bf16 v[124:127], a[12:15], v[188:191], 0\n"
    "ds_write_b128 v243, v[136:139] offset:29760\n"
    "v_mfma_f32_16x16x32_bf16 v[136:139], v[0:3], v[188:191], 0\n"
    "s_waitcnt lgkmcnt(5)\n"
    "v_or_b32_e32 v194, v194, v202\n"
    "v_exp_f16_sdwa v234, v232 dst_sel:WORD_1 dst_unused:UNUSED_PRESERVE src0_sel:WORD_1\n"
    "v_mfma_f32_16x16x32_bf16 v[140:143], v[4:7], v[188:191], 0\n"
    "v_pk_fma_f16 v234, v234, s49, s49\n"
    "v_lshlrev_b32_sdwa v200, 1, v169 dst_sel:DWORD dst_unused:UNUSED_PAD src0_sel:DWORD src1_sel:WORD_1\n"
    "v_lshlrev_b32_sdwa v185, 1, v170 dst_sel:DWORD dst_unused:UNUSED_PAD src0_sel:DWORD src1_sel:WORD_1\n"
    "v_lshlrev_b32_sdwa v201, 1, v171 dst_sel:DWORD dst_unused:UNUSED_PAD src0_sel:DWORD src1_sel:WORD_1\n"
    "v_mfma_f32_16x16x32_bf16 v[168:171], a[104:107], a[224:227], 0\n"
    "ds_read_u16 v198, v198\n"
    "ds_read_u16_d16_hi v206, v206\n"
    "v_mfma_f32_16x16x32_bf16 v[168:171], a[152:155], a[228:231], v[168:171]\n"
    "ds_read_u16 v199, v199\n"
    "ds_read_u16_d16_hi v207, v207\n"
    "v_lshlrev_b32_sdwa v186, 1, v172 dst_sel:DWORD dst_unused:UNUSED_PAD src0_sel:DWORD src1_sel:WORD_1\n"
    "v_lshlrev_b32_sdwa v202, 1, v173 dst_sel:DWORD dst_unused:UNUSED_PAD src0_sel:DWORD src1_sel:WORD_1\n"
    "v_lshlrev_b32_sdwa v187, 1, v174 dst_sel:DWORD dst_unused:UNUSED_PAD src0_sel:DWORD src1_sel:WORD_1\n"
    "s_waitcnt lgkmcnt(7)\n"
    "v_or_b32_e32 v195, v195, v203\n"
    "v_lshlrev_b32_sdwa v203, 1, v175 dst_sel:DWORD dst_unused:UNUSED_PAD src0_sel:DWORD src1_sel:WORD_1\n"
    "v_rcp_f16_e64 v232, v234\n"
    "v_mfma_f32_16x16x32_bf16 v[168:171], a[64:67], a[232:235], v[168:171]\n"
    "ds_read_u16 v184, v184\n"
    "v_rcp_f16_sdwa v232, v234 dst_sel:WORD_1 dst_unused:UNUSED_PRESERVE src0_sel:WORD_1\n"
    "v_mfma_f32_16x16x32_bf16 v[172:175], a[108:111], a[224:227], 0\n"
    "ds_read_u16_d16_hi v200, v200\n"
    "v_lshlrev_b32_sdwa v188, 1, v176 dst_sel:DWORD dst_unused:UNUSED_PAD src0_sel:DWORD src1_sel:WORD_1\n"
    "v_mfma_f32_16x16x32_bf16 v[172:175], a[156:159], a[228:231], v[172:175]\n"
    "s_waitcnt lgkmcnt(6)\n"
    "v_or_b32_e32 v197, v197, v205\n"
    "v_lshlrev_b32_sdwa v204, 1, v177 dst_sel:DWORD dst_unused:UNUSED_PAD src0_sel:DWORD src1_sel:WORD_1\n"
    "v_lshlrev_b32_sdwa v189, 1, v178 dst_sel:DWORD dst_unused:UNUSED_PAD src0_sel:DWORD src1_sel:WORD_1\n"
    "v_lshlrev_b32_sdwa v205, 1, v179 dst_sel:DWORD dst_unused:UNUSED_PAD src0_sel:DWORD src1_sel:WORD_1\n"
    "v_lshlrev_b32_sdwa v190, 1, v180 dst_sel:DWORD dst_unused:UNUSED_PAD src0_sel:DWORD src1_sel:WORD_1\n"
    "v_lshlrev_b32_sdwa v191, 1, v182 dst_sel:DWORD dst_unused:UNUSED_PAD src0_sel:DWORD src1_sel:WORD_1\n"
    "v_fma_mix_f32 v234, v192, v232, 0 op_sel_hi:[1,1,0]\n"
    "v_mfma_f32_16x16x32_bf16 v[172:175], a[68:71], a[232:235], v[172:175]\n"
    "s_barrier\n"
    "v_fma_mix_f32 v232, v192, v232, 0 op_sel:[1,1,0] op_sel_hi:[1,1,0]\n"
    "v_mfma_f32_16x16x32_bf16 v[176:179], a[104:107], a[236:239], 0\n"
    "s_waitcnt lgkmcnt(4)\n"
    "ds_read_u16 v185, v185\n"
    "ds_read_u16_d16_hi v201, v201\n"
    "v_or_b32_e64 v198, v198, v206\n"
    "v_lshlrev_b32_sdwa v206, 1, v181 dst_sel:DWORD dst_unused:UNUSED_PAD src0_sel:DWORD src1_sel:WORD_1\n"
    "v_cvt_pk_bf16_f32 v192, v234, v232\n"
    "v_mfma_f32_16x16x32_bf16 v[176:179], a[152:155], a[240:243], v[176:179]\n"
    "ds_read_u16 v186, v186\n"
    "ds_read_u16_d16_hi v202, v202\n"
    "v_mfma_f32_16x16x32_bf16 v[176:179], a[64:67], a[244:247], v[176:179]\n"
    "v_pk_fma_f16 v232, v196, v196, s48\n"
    "s_waitcnt lgkmcnt(6)\n"
    "v_or_b32_e32 v199, v199, v207\n"
    "v_lshlrev_b32_sdwa v207, 1, v183 dst_sel:DWORD dst_unused:UNUSED_PAD src0_sel:DWORD src1_sel:WORD_1\n"
    "v_mfma_f32_16x16x32_bf16 v[180:183], a[108:111], a[236:239], 0\n"
    "ds_read_u16 v187, v187\n"
    "v_pk_mul_f16 v232, v196, v232\n"
    "v_mfma_f32_16x16x32_bf16 v[180:183], a[156:159], a[240:243], v[180:183]\n"
    "ds_read_u16_d16_hi v203, v203\n"
    "v_exp_f16_e32 v234, v232\n"
    "v_mfma_f32_16x16x32_bf16 v[180:183], a[68:71], a[244:247], v[180:183]\n"
    "s_waitcnt lgkmcnt(6)\n"
    "ds_read_u16 v188, v188\n"
    "v_exp_f16_sdwa v234, v232 dst_sel:WORD_1 dst_unused:UNUSED_PRESERVE src0_sel:WORD_1\n"
    "v_mfma_f32_16x16x32_bf16 v[96:99], a[16:19], v[192:195], v[96:99]\n"
    "v_pk_fma_f16 v234, v234, s49, s49\n"
    "v_or_b32_e64 v184, v184, v200\n"
    "v_mfma_f32_16x16x32_bf16 v[100:103], a[20:23], v[192:195], v[100:103]\n"
    "ds_read_u16_d16_hi v204, v204\n"
    "v_rcp_f16_e64 v232, v234\n"
    "v_mfma_f32_16x16x32_bf16 v[112:115], a[24:27], v[192:195], v[112:115]\n"
    "ds_read_u16 v189, v189\n"
    "v_rcp_f16_sdwa v232, v234 dst_sel:WORD_1 dst_unused:UNUSED_PRESERVE src0_sel:WORD_1\n"
    "v_mfma_f32_16x16x32_bf16 v[116:119], a[28:31], v[192:195], v[116:119]\n"
    "v_fma_mix_f32 v234, v196, v232, 0 op_sel_hi:[1,1,0]\n"
    "v_fma_mix_f32 v232, v196, v232, 0 op_sel:[1,1,0] op_sel_hi:[1,1,0]\n"
    "v_mfma_f32_16x16x32_bf16 v[128:131], v[8:11], v[192:195], v[128:131]\n"
    "s_waitcnt lgkmcnt(7)\n"
    "ds_read_u16_d16_hi v205, v205\n"
    "v_cvt_pk_bf16_f32 v196, v234, v232\n"
    "v_mfma_f32_16x16x32_bf16 v[132:135], v[12:15], v[192:195], v[132:135]\n"
    "v_or_b32_e32 v185, v185, v201\n"
    "v_cvt_pk_f16_f32 v192, v168, v169\n"
    "v_mfma_f32_16x16x32_bf16 v[104:107], a[16:19], v[196:199], v[104:107]\n"
    "s_nop 0\n"
    "ds_read_u16 v190, v190\n"
    "v_lshlrev_b32_sdwa v193, 1, v170 dst_sel:DWORD dst_unused:UNUSED_PAD src0_sel:DWORD src1_sel:WORD_1\n"
    "v_mfma_f32_16x16x32_bf16 v[108:111], a[20:23], v[196:199], v[108:111]\n"
    "s_waitcnt vmcnt(7) lgkmcnt(7)\n"
    "ds_read_u16_d16_hi v206, v206\n"
    "v_lshlrev_b32_sdwa v201, 1, v171 dst_sel:DWORD dst_unused:UNUSED_PAD src0_sel:DWORD src1_sel:WORD_1\n"
    "v_mfma_f32_16x16x32_bf16 v[120:123], a[24:27], v[196:199], v[120:123]\n"
    "v_lshlrev_b32_sdwa v194, 1, v172 dst_sel:DWORD dst_unused:UNUSED_PAD src0_sel:DWORD src1_sel:WORD_1\n"
    "v_or_b32_e64 v186, v186, v202\n"
    "v_mfma_f32_16x16x32_bf16 v[124:127], a[28:31], v[196:199], v[124:127]\n"
    "ds_read_u16 v191, v191\n"
    "v_lshlrev_b32_sdwa v202, 1, v173 dst_sel:DWORD dst_unused:UNUSED_PAD src0_sel:DWORD src1_sel:WORD_1\n"
    "v_mfma_f32_16x16x32_bf16 v[136:139], v[8:11], v[196:199], v[136:139]\n"
    "s_nop 0\n"
    "v_lshlrev_b32_sdwa v195, 1, v174 dst_sel:DWORD dst_unused:UNUSED_PAD src0_sel:DWORD src1_sel:WORD_1\n"
    "v_pk_fma_f16 v232, v192, v192, s48\n"
    "v_mfma_f32_16x16x32_bf16 v[140:143], v[12:15], v[196:199], v[140:143]\n"
    "s_waitcnt vmcnt(6) lgkmcnt(7)\n"
    "ds_read_u16_d16_hi v207, v207\n"
    "v_cvt_pk_f16_f32 v196, v176, v177\n"
    "v_smfmac_f32_32x32x32_bf16 v[112:127], a[248:251], v[152:159], v241\n"
    "ds_read_u16 v193, v193\n"
    "v_lshlrev_b32_sdwa v197, 1, v178 dst_sel:DWORD dst_unused:UNUSED_PAD src0_sel:DWORD src1_sel:WORD_1\n"
    "v_lshlrev_b32_sdwa v198, 1, v180 dst_sel:DWORD dst_unused:UNUSED_PAD src0_sel:DWORD src1_sel:WORD_1\n"
    "v_lshlrev_b32_sdwa v199, 1, v182 dst_sel:DWORD dst_unused:UNUSED_PAD src0_sel:DWORD src1_sel:WORD_1\n"
    "v_or_b32_e32 v187, v187, v203\n"
    "v_smfmac_f32_32x32x32_bf16 v[96:111], a[248:251], v[144:151], v241\n"
    "s_waitcnt lgkmcnt(7)\n"
    "ds_read_u16_d16_hi v201, v201\n"
    "v_lshlrev_b32_sdwa v203, 1, v175 dst_sel:DWORD dst_unused:UNUSED_PAD src0_sel:DWORD src1_sel:WORD_1\n"
    "v_or_b32_e64 v188, v188, v204\n"
    "v_pk_mul_f16 v232, v192, v232\n"
    "v_exp_f16_e64 v234, v232\n"
    "v_smfmac_f32_32x32x32_bf16 v[128:143], a[248:251], v[208:215], v241\n"
    "ds_read_u16 v194, v194\n"
    "v_exp_f16_sdwa v234, v232 dst_sel:WORD_1 dst_unused:UNUSED_PRESERVE src0_sel:WORD_1\n"
    "v_mfma_f32_16x16x32_bf16 v[168:171], a[112:115], a[224:227], 0\n"
    "s_waitcnt lgkmcnt(7)\n"
    "ds_read_u16_d16_hi v202, v202\n"
    "v_or_b32_e32 v189, v189, v205\n"
    "v_mfma_f32_16x16x32_bf16 v[168:171], a[160:163], a[228:231], v[168:171]\n"
    "ds_read_u16 v195, v195\n"
    "v_lshlrev_b32_sdwa v205, 1, v179 dst_sel:DWORD dst_unused:UNUSED_PAD src0_sel:DWORD src1_sel:WORD_1\n"
    "v_mfma_f32_16x16x32_bf16 v[168:171], a[72:75], a[232:235], v[168:171]\n"
    "v_pk_fma_f16 v234, v234, s49, s49\n"
    "v_rcp_f16_e32 v232, v234\n"
    "v_mfma_f32_16x16x32_bf16 v[172:175], a[116:119], a[224:227], 0\n"
    "s_waitcnt lgkmcnt(7)\n"
    "ds_read_b128 v[152:155], v242 offset:24640\n"
    "v_or_b32_e64 v190, v190, v206\n"
    "v_mfma_f32_16x16x32_bf16 v[172:175], a[164:167], a[228:231], v[172:175]\n"
    "ds_read_b128 v[156:159], v242 offset:25664\n"
    "v_lshlrev_b32_sdwa v206, 1, v181 dst_sel:DWORD dst_unused:UNUSED_PAD src0_sel:DWORD src1_sel:WORD_1\n"
    "v_mfma_f32_16x16x32_bf16 v[172:175], v[52:55], a[232:235], v[172:175]\n"
    "ds_read_u16_d16_hi v203, v203\n"
    "v_lshlrev_b32_sdwa v200, 1, v169 dst_sel:DWORD dst_unused:UNUSED_PAD src0_sel:DWORD src1_sel:WORD_1\n"
    "v_mfma_f32_16x16x32_bf16 v[176:179], a[112:115], a[236:239], 0\n"
    "s_waitcnt lgkmcnt(8)\n"
    "v_or_b32_e32 v191, v191, v207\n"
    "v_lshlrev_b32_sdwa v207, 1, v183 dst_sel:DWORD dst_unused:UNUSED_PAD src0_sel:DWORD src1_sel:WORD_1\n"
    "v_mfma_f32_16x16x32_bf16 v[176:179], a[160:163], a[240:243], v[176:179]\n"
    "ds_read_u16 v197, v197\n"
    "v_rcp_f16_sdwa v232, v234 dst_sel:WORD_1 dst_unused:UNUSED_PRESERVE src0_sel:WORD_1\n"
    "v_mfma_f32_16x16x32_bf16 v[176:179], a[72:75], a[244:247], v[176:179]\n"
    "ds_read_u16_d16_hi v205, v205\n"
    "v_fma_mix_f32 v234, v192, v232, 0 op_sel_hi:[1,1,0]\n"
    "v_mfma_f32_16x16x32_bf16 v[180:183], a[116:119], a[236:239], 0\n"
    "v_fma_mix_f32 v232, v192, v232, 0 op_sel:[1,1,0] op_sel_hi:[1,1,0]\n"
    "v_cvt_pk_bf16_f32 v192, v234, v232\n"
    "v_mfma_f32_16x16x32_bf16 v[180:183], a[164:167], a[240:243], v[180:183]\n"
    "s_waitcnt lgkmcnt(8)\n"
    "ds_read_u16 v198, v198\n"
    "v_or_b32_e32 v193, v193, v201\n"
    "v_mfma_f32_16x16x32_bf16 v[180:183], v[52:55], a[244:247], v[180:183]\n"
    "v_lshlrev_b32_sdwa v201, 1, v171 dst_sel:DWORD dst_unused:UNUSED_PAD src0_sel:DWORD src1_sel:WORD_1\n"
    "v_lshlrev_b32_sdwa v204, 1, v177 dst_sel:DWORD dst_unused:UNUSED_PAD src0_sel:DWORD src1_sel:WORD_1\n"
    "v_mfma_f32_16x16x32_bf16 v[112:115], a[40:43], v[184:187], v[112:115]\n"
    "ds_read_u16_d16_hi v206, v206\n"
    "v_pk_fma_f16 v232, v196, v196, s48\n"
    "v_mfma_f32_16x16x32_bf16 v[116:119], a[44:47], v[184:187], v[116:119]\n"
    "s_waitcnt lgkmcnt(8)\n"
    "ds_read_u16 v199, v199\n"
    "v_or_b32_e32 v194, v194, v202\n"
    "v_mfma_f32_16x16x32_bf16 v[128:131], v[16:19], v[184:187], v[128:131]\n"
    "v_lshlrev_b32_sdwa v202, 1, v173 dst_sel:DWORD dst_unused:UNUSED_PAD src0_sel:DWORD src1_sel:WORD_1\n"
    "v_pk_mul_f16 v232, v196, v232\n"
    "v_mfma_f32_16x16x32_bf16 v[132:135], v[20:23], v[184:187], v[132:135]\n"
    "ds_read_u16_d16_hi v207, v207\n"
    "v_exp_f16_e32 v234, v232\n"
    "v_mfma_f32_16x16x32_bf16 v[120:123], a[40:43], v[188:191], v[120:123]\n"
    "s_waitcnt lgkmcnt(7)\n"
    "v_exp_f16_sdwa v234, v232 dst_sel:WORD_1 dst_unused:UNUSED_PRESERVE src0_sel:WORD_1\n"
    "v_smfmac_f32_32x32x32_bf16 v[96:111], a[248:251], v[152:159], v241\n"
    "ds_read_b128 v[152:155], v242 offset:26688\n"
    "ds_read_b128 v[156:159], v242 offset:27712\n"
    "v_pk_fma_f16 v234, v234, s49, s49\n"
    "v_rcp_f16_e32 v232, v234\n"
    "v_mfma_f32_16x16x32_bf16 v[124:127], a[44:47], v[188:191], v[124:127]\n"
    "s_waitcnt lgkmcnt(8)\n"
    "ds_read_u16_d16_hi v200, v200\n"
    "v_or_b32_e64 v195, v195, v203\n"
    "v_mfma_f32_16x16x32_bf16 v[136:139], v[16:19], v[188:191], v[136:139]\n"
    "ds_read_u16_d16_hi v201, v201\n"
    "v_lshlrev_b32_sdwa v203, 1, v175 dst_sel:DWORD dst_unused:UNUSED_PAD src0_sel:DWORD src1_sel:WORD_1\n"
    "v_mfma_f32_16x16x32_bf16 v[96:99], a[32:35], v[184:187], v[96:99]\n"
    "s_nop 0\n"
    "v_rcp_f16_sdwa v232, v234 dst_sel:WORD_1 dst_unused:UNUSED_PRESERVE src0_sel:WORD_1\n"
    "v_mfma_f32_16x16x32_bf16 v[100:103], a[36:39], v[184:187], v[100:103]\n"
    "s_waitcnt lgkmcnt(8)\n"
    "ds_read_u16_d16_hi v202, v202\n"
    "v_or_b32_e64 v197, v197, v205\n"
    "v_mfma_f32_16x16x32_bf16 v[104:107], a[32:35], v[188:191], v[104:107]\n"
    "v_lshlrev_b32_sdwa v184, 1, v168 dst_sel:DWORD dst_unused:UNUSED_PAD src0_sel:DWORD src1_sel:WORD_1\n"
    "v_lshlrev_b32_sdwa v185, 1, v170 dst_sel:DWORD dst_unused:UNUSED_PAD src0_sel:DWORD src1_sel:WORD_1\n"
    "v_mfma_f32_16x16x32_bf16 v[108:111], a[36:39], v[188:191], v[108:111]\n"
    "ds_read_u16 v184, v184\n"
    "v_lshlrev_b32_sdwa v186, 1, v172 dst_sel:DWORD dst_unused:UNUSED_PAD src0_sel:DWORD src1_sel:WORD_1\n"
    "v_mfma_f32_16x16x32_bf16 v[140:143], v[20:23], v[188:191], v[140:143]\n"
    "s_waitcnt lgkmcnt(8)\n"
    "v_or_b32_e32 v198, v198, v206\n"
    "v_lshlrev_b32_sdwa v187, 1, v174 dst_sel:DWORD dst_unused:UNUSED_PAD src0_sel:DWORD src1_sel:WORD_1\n"
    "v_mfma_f32_16x16x32_bf16 v[168:171], a[120:123], a[224:227], 0\n"
    "ds_read_u16 v185, v185\n"
    "v_lshlrev_b32_sdwa v188, 1, v176 dst_sel:DWORD dst_unused:UNUSED_PAD src0_sel:DWORD src1_sel:WORD_1\n"
    "v_mfma_f32_16x16x32_bf16 v[168:171], a[168:171], a[228:231], v[168:171]\n"
    "ds_read_u16 v186, v186\n"
    "v_lshlrev_b32_sdwa v189, 1, v178 dst_sel:DWORD dst_unused:UNUSED_PAD src0_sel:DWORD src1_sel:WORD_1\n"
    "v_mfma_f32_16x16x32_bf16 v[168:171], a[76:79], a[232:235], v[168:171]\n"
    "v_lshlrev_b32_sdwa v205, 1, v179 dst_sel:DWORD dst_unused:UNUSED_PAD src0_sel:DWORD src1_sel:WORD_1\n"
    "v_lshlrev_b32_sdwa v190, 1, v180 dst_sel:DWORD dst_unused:UNUSED_PAD src0_sel:DWORD src1_sel:WORD_1\n"
    "v_mfma_f32_16x16x32_bf16 v[172:175], a[124:127], a[224:227], 0\n"
    "s_waitcnt lgkmcnt(7)\n"
    "ds_read_u16 v187, v187\n"
    "v_lshlrev_b32_sdwa v206, 1, v181 dst_sel:DWORD dst_unused:UNUSED_PAD src0_sel:DWORD src1_sel:WORD_1\n"
    "v_mfma_f32_16x16x32_bf16 v[172:175], a[172:175], a[228:231], v[172:175]\n"
    "s_waitcnt lgkmcnt(7)\n"
    "v_lshlrev_b32_sdwa v191, 1, v182 dst_sel:DWORD dst_unused:UNUSED_PAD src0_sel:DWORD src1_sel:WORD_1\n"
    "v_or_b32_e64 v199, v199, v207\n"
    "v_smfmac_f32_32x32x32_bf16 v[112:127], a[248:251], v[152:159], v241\n"
    "ds_read_u16_d16_hi v203, v203\n"
    "v_lshlrev_b32_sdwa v207, 1, v183 dst_sel:DWORD dst_unused:UNUSED_PAD src0_sel:DWORD src1_sel:WORD_1\n"
    "v_fma_mix_f32 v234, v196, v232, 0 op_sel_hi:[1,1,0]\n"
    "v_fma_mix_f32 v232, v196, v232, 0 op_sel:[1,1,0] op_sel_hi:[1,1,0]\n"
    "v_cvt_pk_bf16_f32 v196, v234, v232\n"
    "v_mfma_f32_16x16x32_bf16 v[172:175], v[60:63], a[232:235], v[172:175]\n"
    "ds_read_u16 v188, v188\n"
    "v_mfma_f32_16x16x32_bf16 v[176:179], a[120:123], a[236:239], 0\n"
    "ds_read_u16_d16_hi v204, v204\n"
    "v_mfma_f32_16x16x32_bf16 v[176:179], a[168:171], a[240:243], v[176:179]\n"
    "v_mfma_f32_16x16x32_bf16 v[176:179], a[76:79], a[244:247], v[176:179]\n"
    "ds_read_u16 v189, v189\n"
    "v_mfma_f32_16x16x32_bf16 v[180:183], a[124:127], a[236:239], 0\n"
    "s_nop 0\n"
    "v_mfma_f32_16x16x32_bf16 v[180:183], a[172:175], a[240:243], v[180:183]\n"
    "s_waitcnt lgkmcnt(7)\n"
    "ds_read_b128 v[152:155], v242 offset:28736\n"
    "v_or_b32_e32 v184, v184, v200\n"
    "v_mfma_f32_16x16x32_bf16 v[180:183], v[60:63], a[244:247], v[180:183]\n"
    "ds_read_b128 v[156:159], v242 offset:29760\n"
    "v_mfma_f32_16x16x32_bf16 v[96:99], a[176:179], v[192:195], v[96:99]\n"
    "s_waitcnt lgkmcnt(8)\n"
    "ds_read_u16_d16_hi v205, v205\n"
    "v_or_b32_e32 v185, v185, v201\n"
    "v_mfma_f32_16x16x32_bf16 v[100:103], a[180:183], v[192:195], v[100:103]\n"
    "s_waitcnt lgkmcnt(8)\n"
    "v_lshlrev_b32_sdwa v201, 1, v171 dst_sel:DWORD dst_unused:UNUSED_PAD src0_sel:DWORD src1_sel:WORD_1\n"
    "v_or_b32_e64 v186, v186, v202\n"
    "v_mfma_f32_16x16x32_bf16 v[112:115], a[184:187], v[192:195], v[112:115]\n"
    "ds_read_u16 v190, v190\n"
    "v_lshlrev_b32_sdwa v202, 1, v173 dst_sel:DWORD dst_unused:UNUSED_PAD src0_sel:DWORD src1_sel:WORD_1\n"
    "v_mfma_f32_16x16x32_bf16 v[116:119], a[188:191], v[192:195], v[116:119]\n"
    "ds_read_b128 a[224:227], v242 offset:0\n"
    "v_mfma_f32_16x16x32_bf16 v[128:131], v[24:27], v[192:195], v[128:131]\n"
    "s_nop 0\n"
    "ds_read_u16_d16_hi v206, v206\n"
    "v_mfma_f32_16x16x32_bf16 v[132:135], v[28:31], v[192:195], v[132:135]\n"
    "s_waitcnt lgkmcnt(9)\n"
    "v_cvt_pk_f16_f32 v192, v168, v169\n"
    "v_lshlrev_b32_sdwa v193, 1, v170 dst_sel:DWORD dst_unused:UNUSED_PAD src0_sel:DWORD src1_sel:WORD_1\n"
    "v_mfma_f32_16x16x32_bf16 v[104:107], a[176:179], v[196:199], v[104:107]\n"
    "ds_read_b128 a[228:231], v242 offset:1024\n"
    "v_lshlrev_b32_sdwa v194, 1, v172 dst_sel:DWORD dst_unused:UNUSED_PAD src0_sel:DWORD src1_sel:WORD_1\n"
    "v_mfma_f32_16x16x32_bf16 v[108:111], a[180:183], v[196:199], v[108:111]\n"
    "ds_read_u16 v191, v191\n"
    "v_lshlrev_b32_sdwa v195, 1, v174 dst_sel:DWORD dst_unused:UNUSED_PAD src0_sel:DWORD src1_sel:WORD_1\n"
    "v_mfma_f32_16x16x32_bf16 v[120:123], a[184:187], v[196:199], v[120:123]\n"
    "s_waitcnt lgkmcnt(9)\n"
    "v_or_b32_e32 v187, v187, v203\n"
    "v_lshlrev_b32_sdwa v203, 1, v175 dst_sel:DWORD dst_unused:UNUSED_PAD src0_sel:DWORD src1_sel:WORD_1\n"
    "v_mfma_f32_16x16x32_bf16 v[124:127], a[188:191], v[196:199], v[124:127]\n"
    "ds_read_b128 a[232:235], v242 offset:2048\n"
    "v_or_b32_e64 v188, v188, v204\n"
    "v_mfma_f32_16x16x32_bf16 v[136:139], v[24:27], v[196:199], v[136:139]\n"
    "ds_read_u16_d16_hi v207, v207\n"
    "v_pk_fma_f16 v232, v192, v192, s48\n"
    "v_mfma_f32_16x16x32_bf16 v[140:143], v[28:31], v[196:199], v[140:143]\n"
    "s_waitcnt lgkmcnt(8)\n"
    "v_cvt_pk_f16_f32 v196, v176, v177\n"
    "ds_read_b128 a[236:239], v242 offset:3072\n"
    "v_lshlrev_b32_sdwa v197, 1, v178 dst_sel:DWORD dst_unused:UNUSED_PAD src0_sel:DWORD src1_sel:WORD_1\n"
    "ds_read_b128 a[240:243], v242 offset:4096\n"
    "v_lshlrev_b32_sdwa v198, 1, v180 dst_sel:DWORD dst_unused:UNUSED_PAD src0_sel:DWORD src1_sel:WORD_1\n"
    "v_smfmac_f32_32x32x32_bf16 v[128:143], a[248:251], v[152:159], v241\n"
    "s_waitcnt lgkmcnt(9)\n"
    "ds_read_b128 a[244:247], v242 offset:5120\n"
    "ds_read_u16 v193, v193\n"
    "v_lshlrev_b32_sdwa v199, 1, v182 dst_sel:DWORD dst_unused:UNUSED_PAD src0_sel:DWORD src1_sel:WORD_1\n"
    "v_or_b32_e64 v189, v189, v205\n"
    "v_lshlrev_b32_sdwa v205, 1, v179 dst_sel:DWORD dst_unused:UNUSED_PAD src0_sel:DWORD src1_sel:WORD_1\n"
    "s_waitcnt lgkmcnt(9)\n"
    "v_mfma_f32_16x16x32_bf16 v[168:171], a[80:83], a[224:227], 0\n"
    "s_nop 0\n"
    "ds_read_u16_d16_hi v201, v201\n"
    "v_pk_mul_f16 v232, v192, v232\n"
    "v_mfma_f32_16x16x32_bf16 v[172:175], a[84:87], a[224:227], 0\n"
    "v_exp_f16_e64 v234, v232\n"
    "ds_read_u16 v194, v194\n"
    "s_waitcnt lgkmcnt(9)\n"
    "v_mfma_f32_16x16x32_bf16 v[168:171], a[128:131], a[228:231], v[168:171]\n"
    "v_or_b32_e32 v190, v190, v206\n"
    "v_lshlrev_b32_sdwa v206, 1, v181 dst_sel:DWORD dst_unused:UNUSED_PAD src0_sel:DWORD src1_sel:WORD_1\n"
    "v_mfma_f32_16x16x32_bf16 v[172:175], a[132:135], a[228:231], v[172:175]\n"
    "s_nop 0\n"
    "ds_read_u16_d16_hi v202, v202\n"
    "v_exp_f16_sdwa v234, v232 dst_sel:WORD_1 dst_unused:UNUSED_PRESERVE src0_sel:WORD_1\n"
    "v_mfma_f32_16x16x32_bf16 v[96:99], a[192:195], v[184:187], v[96:99]\n"
    "s_waitcnt lgkmcnt(8)\n"
    "ds_read_u16 v195, v195\n"
    "v_pk_fma_f16 v234, v234, s49, s49\n"
    "v_mfma_f32_16x16x32_bf16 v[168:171], a[48:51], a[232:235], v[168:171]\n"
    "v_rcp_f16_e32 v232, v234\n"
    "v_mfma_f32_16x16x32_bf16 v[172:175], v[48:51], a[232:235], v[172:175]\n"
    "s_waitcnt lgkmcnt(7)\n"
    "ds_read_u16_d16_hi v203, v203\n"
    "v_or_b32_e32 v191, v191, v207\n"
    "v_mfma_f32_16x16x32_bf16 v[176:179], a[80:83], a[236:239], 0\n"
    "s_waitcnt lgkmcnt(7)\n"
    "v_lshlrev_b32_sdwa v207, 1, v183 dst_sel:DWORD dst_unused:UNUSED_PAD src0_sel:DWORD src1_sel:WORD_1\n"
    "v_lshlrev_b32_sdwa v200, 1, v169 dst_sel:DWORD dst_unused:UNUSED_PAD src0_sel:DWORD src1_sel:WORD_1\n"
    "v_mfma_f32_16x16x32_bf16 v[176:179], a[128:131], a[240:243], v[176:179]\n"
    "s_waitcnt lgkmcnt(6)\n"
    "ds_read_u16 v197, v197\n"
    "v_rcp_f16_sdwa v232, v234 dst_sel:WORD_1 dst_unused:UNUSED_PRESERVE src0_sel:WORD_1\n"
    "v_mfma_f32_16x16x32_bf16 v[176:179], a[48:51], a[244:247], v[176:179]\n"
    "s_nop 0\n"
    "ds_read_u16_d16_hi v205, v205\n"
    "v_fma_mix_f32 v234, v192, v232, 0 op_sel_hi:[1,1,0]\n"
    "v_mfma_f32_16x16x32_bf16 v[180:183], a[84:87], a[236:239], 0\n"
    "v_fma_mix_f32 v232, v192, v232, 0 op_sel:[1,1,0] op_sel_hi:[1,1,0]\n"
    "v_cvt_pk_bf16_f32 v192, v234, v232\n"
    "v_mfma_f32_16x16x32_bf16 v[180:183], a[132:135], a[240:243], v[180:183]\n"
    "ds_read_u16 v198, v198\n"
    "v_pk_fma_f16 v232, v196, v196, s48\n"
    "v_mfma_f32_16x16x32_bf16 v[180:183], v[48:51], a[244:247], v[180:183]\n"
    "v_lshlrev_b32_sdwa v204, 1, v177 dst_sel:DWORD dst_unused:UNUSED_PAD src0_sel:DWORD src1_sel:WORD_1\n"
    "v_pk_mul_f16 v232, v196, v232\n"
    "v_mfma_f32_16x16x32_bf16 v[100:103], a[196:199], v[184:187], v[100:103]\n"
    "s_waitcnt lgkmcnt(7)\n"
    "ds_read_u16_d16_hi v206, v206\n"
    "v_or_b32_e32 v193, v193, v201\n"
    "v_mfma_f32_16x16x32_bf16 v[112:115], a[200:203], v[184:187], v[112:115]\n"
    "v_lshlrev_b32_sdwa v201, 1, v171 dst_sel:DWORD dst_unused:UNUSED_PAD src0_sel:DWORD src1_sel:WORD_1\n"
    "v_exp_f16_e64 v234, v232\n"
    "v_mfma_f32_16x16x32_bf16 v[116:119], a[204:207], v[184:187], v[116:119]\n"
    "ds_read_u16 v199, v199\n"
    "v_exp_f16_sdwa v234, v232 dst_sel:WORD_1 dst_unused:UNUSED_PRESERVE src0_sel:WORD_1\n"
    "v_mfma_f32_16x16x32_bf16 v[128:131], v[32:35], v[184:187], v[128:131]\n"
    "s_waitcnt lgkmcnt(7)\n"
    "ds_read_u16_d16_hi v207, v207\n"
    "v_or_b32_e32 v194, v194, v202\n"
    "v_mfma_f32_16x16x32_bf16 v[132:135], v[36:39], v[184:187], v[132:135]\n"
    "v_lshlrev_b32_sdwa v184, 1, v168 dst_sel:DWORD dst_unused:UNUSED_PAD src0_sel:DWORD src1_sel:WORD_1\n"
    "v_lshlrev_b32_sdwa v185, 1, v170 dst_sel:DWORD dst_unused:UNUSED_PAD src0_sel:DWORD src1_sel:WORD_1\n"
    "v_mfma_f32_16x16x32_bf16 v[104:107], a[192:195], v[188:191], v[104:107]\n"
    "ds_read_u16 v184, v184\n"
    "v_lshlrev_b32_sdwa v186, 1, v172 dst_sel:DWORD dst_unused:UNUSED_PAD src0_sel:DWORD src1_sel:WORD_1\n"
    "v_mfma_f32_16x16x32_bf16 v[108:111], a[196:199], v[188:191], v[108:111]\n"
    "s_waitcnt lgkmcnt(7)\n"
    "ds_read_u16_d16_hi v200, v200\n"
    "v_or_b32_e32 v195, v195, v203\n"
    "v_mfma_f32_16x16x32_bf16 v[120:123], a[200:203], v[188:191], v[120:123]\n"
    "v_lshlrev_b32_sdwa v202, 1, v173 dst_sel:DWORD dst_unused:UNUSED_PAD src0_sel:DWORD src1_sel:WORD_1\n"
    "v_lshlrev_b32_sdwa v187, 1, v174 dst_sel:DWORD dst_unused:UNUSED_PAD src0_sel:DWORD src1_sel:WORD_1\n"
    "v_mfma_f32_16x16x32_bf16 v[124:127], a[204:207], v[188:191], v[124:127]\n"
    "ds_read_u16 v185, v185\n"
    "v_lshlrev_b32_sdwa v203, 1, v175 dst_sel:DWORD dst_unused:UNUSED_PAD src0_sel:DWORD src1_sel:WORD_1\n"
    "v_mfma_f32_16x16x32_bf16 v[136:139], v[32:35], v[188:191], v[136:139]\n"
    "v_pk_fma_f16 v234, v234, s49, s49\n"
    "v_rcp_f16_e32 v232, v234\n"
    "v_mfma_f32_16x16x32_bf16 v[140:143], v[36:39], v[188:191], v[140:143]\n"
    "s_waitcnt lgkmcnt(7)\n"
    "ds_read_u16_d16_hi v201, v201\n"
    "v_or_b32_e64 v197, v197, v205\n"
    "v_mfma_f32_16x16x32_bf16 v[168:171], a[88:91], a[224:227], 0\n"
    "ds_read_u16 v186, v186\n"
    "v_lshlrev_b32_sdwa v188, 1, v176 dst_sel:DWORD dst_unused:UNUSED_PAD src0_sel:DWORD src1_sel:WORD_1\n"
    "v_mfma_f32_16x16x32_bf16 v[168:171], a[136:139], a[228:231], v[168:171]\n"
    "v_lshlrev_b32_sdwa v189, 1, v178 dst_sel:DWORD dst_unused:UNUSED_PAD src0_sel:DWORD src1_sel:WORD_1\n"
    "v_lshlrev_b32_sdwa v205, 1, v179 dst_sel:DWORD dst_unused:UNUSED_PAD src0_sel:DWORD src1_sel:WORD_1\n"
    "v_mfma_f32_16x16x32_bf16 v[168:171], a[52:55], a[232:235], v[168:171]\n"
    "s_waitcnt lgkmcnt(7)\n"
    "ds_read_u16_d16_hi v202, v202\n"
    "v_or_b32_e32 v198, v198, v206\n"
    "v_mfma_f32_16x16x32_bf16 v[172:175], a[92:95], a[224:227], 0\n"
    "v_lshlrev_b32_sdwa v190, 1, v180 dst_sel:DWORD dst_unused:UNUSED_PAD src0_sel:DWORD src1_sel:WORD_1\n"
    "v_lshlrev_b32_sdwa v206, 1, v181 dst_sel:DWORD dst_unused:UNUSED_PAD src0_sel:DWORD src1_sel:WORD_1\n"
    "v_mfma_f32_16x16x32_bf16 v[172:175], a[140:143], a[228:231], v[172:175]\n"
    "ds_read_u16 v187, v187\n"
    "v_lshlrev_b32_sdwa v191, 1, v182 dst_sel:DWORD dst_unused:UNUSED_PAD src0_sel:DWORD src1_sel:WORD_1\n"
    "v_mfma_f32_16x16x32_bf16 v[172:175], v[56:59], a[232:235], v[172:175]\n"
    "s_nop 0\n"
    "v_rcp_f16_sdwa v232, v234 dst_sel:WORD_1 dst_unused:UNUSED_PRESERVE src0_sel:WORD_1\n"
    "v_mfma_f32_16x16x32_bf16 v[176:179], a[88:91], a[236:239], 0\n"
    "s_waitcnt lgkmcnt(7)\n"
    "ds_read_u16_d16_hi v203, v203\n"
    "v_or_b32_e64 v199, v199, v207\n"
    "v_mfma_f32_16x16x32_bf16 v[176:179], a[136:139], a[240:243], v[176:179]\n"
    "ds_read_u16 v188, v188\n"
    "v_lshlrev_b32_sdwa v207, 1, v183 dst_sel:DWORD dst_unused:UNUSED_PAD src0_sel:DWORD src1_sel:WORD_1\n"
    "v_mfma_f32_16x16x32_bf16 v[176:179], a[52:55], a[244:247], v[176:179]\n"
    "v_fma_mix_f32 v234, v196, v232, 0 op_sel_hi:[1,1,0]\n"
    "v_fma_mix_f32 v232, v196, v232, 0 op_sel:[1,1,0] op_sel_hi:[1,1,0]\n"
    "v_mfma_f32_16x16x32_bf16 v[180:183], a[92:95], a[236:239], 0\n"
    "s_waitcnt lgkmcnt(7)\n"
    "ds_read_u16_d16_hi v204, v204\n"
    "v_cvt_pk_bf16_f32 v196, v234, v232\n"
    "v_mfma_f32_16x16x32_bf16 v[180:183], a[140:143], a[240:243], v[180:183]\n"
    "v_or_b32_e32 v184, v184, v200\n"
    "v_mfma_f32_16x16x32_bf16 v[180:183], v[56:59], a[244:247], v[180:183]\n"
    "ds_read_u16 v189, v189\n"
    "v_mfma_f32_16x16x32_bf16 v[96:99], a[208:211], v[192:195], v[96:99]\n"
    "s_nop 0\n"
    "v_mfma_f32_16x16x32_bf16 v[100:103], a[212:215], v[192:195], v[100:103]\n"
    "s_waitcnt lgkmcnt(7)\n"
    "ds_read_u16_d16_hi v205, v205\n"
    "v_or_b32_e64 v185, v185, v201\n"
    "v_mfma_f32_16x16x32_bf16 v[112:115], a[216:219], v[192:195], v[112:115]\n"
    "s_barrier\n"
    "v_mfma_f32_16x16x32_bf16 v[116:119], a[220:223], v[192:195], v[116:119]\n"
    "s_waitcnt lgkmcnt(5)\n"
    "ds_read_u16 v190, v190\n"
    "ds_read_u16_d16_hi v206, v206\n"
    "v_mfma_f32_16x16x32_bf16 v[128:131], v[40:43], v[192:195], v[128:131]\n"
    "s_nop 0\n"
    "ds_read_u16 v191, v191\n"
    "v_lshlrev_b32_sdwa v201, 1, v171 dst_sel:DWORD dst_unused:UNUSED_PAD src0_sel:DWORD src1_sel:WORD_1\n"
    "v_mfma_f32_16x16x32_bf16 v[132:135], v[44:47], v[192:195], v[132:135]\n"
    "s_waitcnt lgkmcnt(7)\n"
    "buffer_load_dwordx4 v[152:155], v245, s[40:43], s58 offen offset:64\n"
    "buffer_load_dwordx4 v[144:147], v245, s[40:43], s58 offen offset:0\n"
    "buffer_load_dwordx4 v[208:211], v245, s[40:43], s58 offen offset:128\n"
    "ds_read_u16_d16_hi v207, v207\n"
    "v_mfma_f32_16x16x32_bf16 v[104:107], a[208:211], v[196:199], v[104:107]\n"
    "ds_read_u16_d16_hi v201, v201\n"
    "v_cvt_pk_f16_f32 v192, v168, v169\n"
    "v_mfma_f32_16x16x32_bf16 v[108:111], a[212:215], v[196:199], v[108:111]\n"
    "v_lshlrev_b32_sdwa v193, 1, v170 dst_sel:DWORD dst_unused:UNUSED_PAD src0_sel:DWORD src1_sel:WORD_1\n"
    "v_lshlrev_b32_sdwa v194, 1, v172 dst_sel:DWORD dst_unused:UNUSED_PAD src0_sel:DWORD src1_sel:WORD_1\n"
    "v_mfma_f32_16x16x32_bf16 v[120:123], a[216:219], v[196:199], v[120:123]\n"
    "s_waitcnt lgkmcnt(7)\n"
    "ds_read_u16 v193, v193\n"
    "v_or_b32_e32 v186, v186, v202\n"
    "v_mfma_f32_16x16x32_bf16 v[124:127], a[220:223], v[196:199], v[124:127]\n"
    "v_lshlrev_b32_sdwa v202, 1, v173 dst_sel:DWORD dst_unused:UNUSED_PAD src0_sel:DWORD src1_sel:WORD_1\n"
    "v_lshlrev_b32_sdwa v195, 1, v174 dst_sel:DWORD dst_unused:UNUSED_PAD src0_sel:DWORD src1_sel:WORD_1\n"
    "v_mfma_f32_16x16x32_bf16 v[136:139], v[40:43], v[196:199], v[136:139]\n"
    "ds_read_u16 v194, v194\n"
    "v_or_b32_e32 v187, v187, v203\n"
    "v_mfma_f32_16x16x32_bf16 v[140:143], v[44:47], v[196:199], v[140:143]\n"
    "v_lshlrev_b32_sdwa v203, 1, v175 dst_sel:DWORD dst_unused:UNUSED_PAD src0_sel:DWORD src1_sel:WORD_1\n"
    "v_cvt_pk_f16_f32 v196, v176, v177\n"
    "v_mfma_f32_16x16x32_bf16 v[168:171], a[96:99], a[224:227], 0\n"
    "s_waitcnt lgkmcnt(7)\n"
    "ds_read_u16_d16_hi v202, v202\n"
    "v_lshlrev_b32_sdwa v197, 1, v178 dst_sel:DWORD dst_unused:UNUSED_PAD src0_sel:DWORD src1_sel:WORD_1\n"
    "v_mfma_f32_16x16x32_bf16 v[168:171], a[144:147], a[228:231], v[168:171]\n"
    "ds_read_u16 v195, v195\n"
    "v_lshlrev_b32_sdwa v198, 1, v180 dst_sel:DWORD dst_unused:UNUSED_PAD src0_sel:DWORD src1_sel:WORD_1\n"
    "v_mfma_f32_16x16x32_bf16 v[168:171], a[56:59], a[232:235], v[168:171]\n"
    "v_lshlrev_b32_sdwa v199, 1, v182 dst_sel:DWORD dst_unused:UNUSED_PAD src0_sel:DWORD src1_sel:WORD_1\n"
    "v_or_b32_e32 v188, v188, v204\n"
    "v_mfma_f32_16x16x32_bf16 v[172:175], a[100:103], a[224:227], 0\n"
    "s_waitcnt lgkmcnt(7)\n"
    "buffer_load_dwordx4 v[156:159], v245, s[40:43], s89 offen offset:64\n"
    "buffer_load_dwordx4 v[148:151], v245, s[40:43], s89 offen offset:0\n"
    "buffer_load_dwordx4 v[212:215], v245, s[40:43], s89 offen offset:128\n"
    "ds_read_u16_d16_hi v203, v203\n"
    "v_mfma_f32_16x16x32_bf16 v[172:175], a[148:151], a[228:231], v[172:175]\n"
    "ds_read_u16 v197, v197\n"
    "v_or_b32_e32 v189, v189, v205\n"
    "v_mfma_f32_16x16x32_bf16 v[172:175], a[60:63], a[232:235], v[172:175]\n"
    "v_lshlrev_b32_sdwa v205, 1, v179 dst_sel:DWORD dst_unused:UNUSED_PAD src0_sel:DWORD src1_sel:WORD_1\n"
    "v_cvt_pk_bf16_f32 v96, v96, v97\n"
    "v_mfma_f32_16x16x32_bf16 v[176:179], a[96:99], a[236:239], 0\n"
    "s_waitcnt lgkmcnt(7)\n"
    "ds_read_u16_d16_hi v205, v205\n"
    "v_cvt_pk_bf16_f32 v97, v98, v99\n"
    "v_mfma_f32_16x16x32_bf16 v[176:179], a[144:147], a[240:243], v[176:179]\n"
    "v_cvt_pk_bf16_f32 v98, v100, v101\n"
    "v_cvt_pk_bf16_f32 v99, v102, v103\n"
    "v_mfma_f32_16x16x32_bf16 v[176:179], a[56:59], a[244:247], v[176:179]\n"
    "ds_read_u16 v198, v198\n"
    "buffer_store_dwordx4 v[96:99], v245, s[44:47], s57 offen offset:0 sc1\n"
    "v_or_b32_e32 v190, v190, v206\n"
    "v_mfma_f32_16x16x32_bf16 v[100:103], a[4:7], v[184:187], 0\n"
    "s_waitcnt lgkmcnt(7)\n"
    "ds_read_u16 v199, v199\n"
    "v_lshlrev_b32_sdwa v206, 1, v181 dst_sel:DWORD dst_unused:UNUSED_PAD src0_sel:DWORD src1_sel:WORD_1\n"
    "v_mfma_f32_16x16x32_bf16 v[96:99], a[0:3], v[184:187], 0\n"
    "ds_read_u16_d16_hi v206, v206\n"
    "v_cvt_pk_bf16_f32 v224, v112, v113\n"
    "v_cvt_pk_bf16_f32 v225, v114, v115\n"
    "v_mfma_f32_16x16x32_bf16 v[112:115], a[8:11], v[184:187], 0\n"
    "v_or_b32_e64 v193, v193, v201\n"
    "v_cvt_pk_bf16_f32 v226, v116, v117\n"
    "v_cvt_pk_bf16_f32 v227, v118, v119\n"
    "v_cvt_pk_bf16_f32 v228, v128, v129\n"
    "v_cvt_pk_bf16_f32 v229, v130, v131\n"
    "v_or_b32_e32 v191, v191, v207\n"
    "v_mfma_f32_16x16x32_bf16 v[116:119], a[12:15], v[184:187], 0\n"
    "s_waitcnt lgkmcnt(6)\n"
    "v_lshlrev_b32_sdwa v207, 1, v183 dst_sel:DWORD dst_unused:UNUSED_PAD src0_sel:DWORD src1_sel:WORD_1\n"
    "v_cvt_pk_bf16_f32 v230, v132, v133\n"
    "v_mfma_f32_16x16x32_bf16 v[180:183], a[100:103], a[236:239], 0\n"
    "ds_read_u16_d16_hi v207, v207\n"
    "v_cvt_pk_bf16_f32 v231, v134, v135\n"
    "v_mfma_f32_16x16x32_bf16 v[180:183], a[148:151], a[240:243], v[180:183]\n"
    "s_waitcnt lgkmcnt(6)\n"
    "v_or_b32_e32 v194, v194, v202\n"
    "v_cvt_pk_bf16_f32 v216, v104, v105\n"
    "v_mfma_f32_16x16x32_bf16 v[180:183], a[60:63], a[244:247], v[180:183]\n"
    "v_cvt_pk_bf16_f32 v217, v106, v107\n"
    "v_cvt_pk_bf16_f32 v218, v108, v109\n"
    "v_mfma_f32_16x16x32_bf16 v[128:131], v[0:3], v[184:187], 0\n"
    "v_cvt_pk_bf16_f32 v219, v110, v111\n"
    "v_cvt_pk_bf16_f32 v220, v120, v121\n"
    "v_mfma_f32_16x16x32_bf16 v[132:135], v[4:7], v[184:187], 0\n"
    "v_cvt_pk_bf16_f32 v221, v122, v123\n"
    "v_cvt_pk_bf16_f32 v222, v124, v125\n"
    "v_mfma_f32_16x16x32_bf16 v[104:107], a[0:3], v[188:191], 0\n"
    "s_waitcnt lgkmcnt(4)\n"
    "v_cvt_pk_bf16_f32 v223, v126, v127\n"
    "v_or_b32_e32 v195, v195, v203\n"
    "v_mfma_f32_16x16x32_bf16 v[108:111], a[4:7], v[188:191], 0\n"
    "v_pk_fma_f16 v232, v192, v192, s48\n"
    "v_cvt_pk_bf16_f32 v160, v136, v137\n"
    "v_mfma_f32_16x16x32_bf16 v[120:123], a[8:11], v[188:191], 0\n"
    "v_cvt_pk_bf16_f32 v161, v138, v139\n"
    "v_cvt_pk_bf16_f32 v162, v140, v141\n"
    "v_mfma_f32_16x16x32_bf16 v[124:127], a[12:15], v[188:191], 0\n"
    "v_cvt_pk_bf16_f32 v163, v142, v143\n"
    "v_pk_mul_f16 v232, v192, v232\n"
    "v_mfma_f32_16x16x32_bf16 v[136:139], v[0:3], v[188:191], 0\n"
    "v_or_b32_e64 v197, v197, v205\n"
    "v_exp_f16_e32 v234, v232\n"
    "v_mfma_f32_16x16x32_bf16 v[140:143], v[4:7], v[188:191], 0\n"
    "s_waitcnt lgkmcnt(1)\n"
    "v_exp_f16_sdwa v234, v232 dst_sel:WORD_1 dst_unused:UNUSED_PRESERVE src0_sel:WORD_1\n"
    "v_lshlrev_b32_sdwa v184, 1, v168 dst_sel:DWORD dst_unused:UNUSED_PAD src0_sel:DWORD src1_sel:WORD_1\n"
    "v_pk_fma_f16 v234, v234, s49, s49\n"
    "v_lshlrev_b32_sdwa v200, 1, v169 dst_sel:DWORD dst_unused:UNUSED_PAD src0_sel:DWORD src1_sel:WORD_1\n"
    "v_lshlrev_b32_sdwa v185, 1, v170 dst_sel:DWORD dst_unused:UNUSED_PAD src0_sel:DWORD src1_sel:WORD_1\n"
    "v_lshlrev_b32_sdwa v201, 1, v171 dst_sel:DWORD dst_unused:UNUSED_PAD src0_sel:DWORD src1_sel:WORD_1\n"
    "v_mfma_f32_16x16x32_bf16 v[168:171], a[104:107], a[224:227], 0\n"
    "s_waitcnt lgkmcnt(0)\n"
    "ds_read_u16 v184, v184\n"
    "ds_read_u16_d16_hi v200, v200\n"
    "v_mfma_f32_16x16x32_bf16 v[168:171], a[152:155], a[228:231], v[168:171]\n"
    "s_nop 0\n"
    "ds_read_u16 v185, v185\n"
    "ds_read_u16_d16_hi v201, v201\n"
    "v_lshlrev_b32_sdwa v186, 1, v172 dst_sel:DWORD dst_unused:UNUSED_PAD src0_sel:DWORD src1_sel:WORD_1\n"
    "v_lshlrev_b32_sdwa v202, 1, v173 dst_sel:DWORD dst_unused:UNUSED_PAD src0_sel:DWORD src1_sel:WORD_1\n"
    "v_or_b32_e64 v198, v198, v206\n"
    "v_lshlrev_b32_sdwa v187, 1, v174 dst_sel:DWORD dst_unused:UNUSED_PAD src0_sel:DWORD src1_sel:WORD_1\n"
    "v_lshlrev_b32_sdwa v203, 1, v175 dst_sel:DWORD dst_unused:UNUSED_PAD src0_sel:DWORD src1_sel:WORD_1\n"
    "v_rcp_f16_e64 v232, v234\n"
    "v_mfma_f32_16x16x32_bf16 v[168:171], a[64:67], a[232:235], v[168:171]\n"
    "ds_read_u16 v186, v186\n"
    "v_rcp_f16_sdwa v232, v234 dst_sel:WORD_1 dst_unused:UNUSED_PRESERVE src0_sel:WORD_1\n"
    "v_mfma_f32_16x16x32_bf16 v[172:175], a[108:111], a[224:227], 0\n"
    "ds_read_u16_d16_hi v202, v202\n"
    "v_lshlrev_b32_sdwa v188, 1, v176 dst_sel:DWORD dst_unused:UNUSED_PAD src0_sel:DWORD src1_sel:WORD_1\n"
    "v_lshlrev_b32_sdwa v204, 1, v177 dst_sel:DWORD dst_unused:UNUSED_PAD src0_sel:DWORD src1_sel:WORD_1\n"
    "v_lshlrev_b32_sdwa v189, 1, v178 dst_sel:DWORD dst_unused:UNUSED_PAD src0_sel:DWORD src1_sel:WORD_1\n"
    "v_lshlrev_b32_sdwa v205, 1, v179 dst_sel:DWORD dst_unused:UNUSED_PAD src0_sel:DWORD src1_sel:WORD_1\n"
    "v_lshlrev_b32_sdwa v190, 1, v180 dst_sel:DWORD dst_unused:UNUSED_PAD src0_sel:DWORD src1_sel:WORD_1\n"
    "v_lshlrev_b32_sdwa v206, 1, v181 dst_sel:DWORD dst_unused:UNUSED_PAD src0_sel:DWORD src1_sel:WORD_1\n"
    "v_lshlrev_b32_sdwa v191, 1, v182 dst_sel:DWORD dst_unused:UNUSED_PAD src0_sel:DWORD src1_sel:WORD_1\n"
    "v_fma_mix_f32 v234, v192, v232, 0 op_sel_hi:[1,1,0]\n"
    "v_mfma_f32_16x16x32_bf16 v[172:175], a[156:159], a[228:231], v[172:175]\n"
    "ds_read_u16 v187, v187\n"
    "v_or_b32_e64 v199, v199, v207\n"
    "v_mfma_f32_16x16x32_bf16 v[172:175], a[68:71], a[232:235], v[172:175]\n"
    "ds_read_u16_d16_hi v203, v203\n"
    "v_lshlrev_b32_sdwa v207, 1, v183 dst_sel:DWORD dst_unused:UNUSED_PAD src0_sel:DWORD src1_sel:WORD_1\n"
    "v_fma_mix_f32 v232, v192, v232, 0 op_sel:[1,1,0] op_sel_hi:[1,1,0]\n"
    "v_mfma_f32_16x16x32_bf16 v[176:179], a[104:107], a[236:239], 0\n"
    "ds_read_u16 v188, v188\n"
    "v_cvt_pk_bf16_f32 v192, v234, v232\n"
    "v_mfma_f32_16x16x32_bf16 v[176:179], a[152:155], a[240:243], v[176:179]\n"
    "v_pk_fma_f16 v232, v196, v196, s48\n"
    "v_pk_mul_f16 v232, v196, v232\n"
    "v_mfma_f32_16x16x32_bf16 v[176:179], a[64:67], a[244:247], v[176:179]\n"
    "s_waitcnt lgkmcnt(7)\n"
    "ds_read_u16_d16_hi v204, v204\n"
    "v_exp_f16_e32 v234, v232\n"
    "v_mfma_f32_16x16x32_bf16 v[180:183], a[108:111], a[236:239], 0\n"
    "v_exp_f16_sdwa v234, v232 dst_sel:WORD_1 dst_unused:UNUSED_PRESERVE src0_sel:WORD_1\n"
    "v_mfma_f32_16x16x32_bf16 v[180:183], a[156:159], a[240:243], v[180:183]\n"
    "ds_read_u16 v189, v189\n"
    "v_pk_fma_f16 v234, v234, s49, s49\n"
    "v_mfma_f32_16x16x32_bf16 v[180:183], a[68:71], a[244:247], v[180:183]\n"
    "s_waitcnt lgkmcnt(7)\n"
    "ds_read_u16_d16_hi v205, v205\n"
    "buffer_store_dwordx4 v[224:227], v245, s[44:47], s57 offen offset:64 sc1\n"
    "v_mfma_f32_16x16x32_bf16 v[96:99], a[16:19], v[192:195], v[96:99]\n"
    "s_nop 0\n"
    "ds_read_u16 v190, v190\n"
    "v_rcp_f16_e32 v232, v234\n"
    "v_mfma_f32_16x16x32_bf16 v[100:103], a[20:23], v[192:195], v[100:103]\n"
    "v_rcp_f16_sdwa v232, v234 dst_sel:WORD_1 dst_unused:UNUSED_PRESERVE src0_sel:WORD_1\n"
    "v_mfma_f32_16x16x32_bf16 v[112:115], a[24:27], v[192:195], v[112:115]\n"
    "s_waitcnt lgkmcnt(7)\n"
    "ds_read_u16_d16_hi v206, v206\n"
    "v_fma_mix_f32 v234, v196, v232, 0 op_sel_hi:[1,1,0]\n"
    "v_mfma_f32_16x16x32_bf16 v[116:119], a[28:31], v[192:195], v[116:119]\n"
    "ds_read_u16 v191, v191\n"
    "v_fma_mix_f32 v232, v196, v232, 0 op_sel:[1,1,0] op_sel_hi:[1,1,0]\n"
    "v_mfma_f32_16x16x32_bf16 v[128:131], v[8:11], v[192:195], v[128:131]\n"
    "v_cvt_pk_bf16_f32 v196, v234, v232\n"
    "v_or_b32_e32 v184, v184, v200\n"
    "v_mfma_f32_16x16x32_bf16 v[132:135], v[12:15], v[192:195], v[132:135]\n"
    "s_waitcnt lgkmcnt(7)\n"
    "ds_read_u16_d16_hi v207, v207\n"
    "v_or_b32_e64 v185, v185, v201\n"
    "v_mfma_f32_16x16x32_bf16 v[104:107], a[16:19], v[196:199], v[104:107]\n"
    "v_cvt_pk_f16_f32 v192, v168, v169\n"
    "v_lshlrev_b32_sdwa v193, 1, v170 dst_sel:DWORD dst_unused:UNUSED_PAD src0_sel:DWORD src1_sel:WORD_1\n"
    "v_mfma_f32_16x16x32_bf16 v[108:111], a[20:23], v[196:199], v[108:111]\n"
    "ds_read_u16 v193, v193\n"
    "v_lshlrev_b32_sdwa v201, 1, v171 dst_sel:DWORD dst_unused:UNUSED_PAD src0_sel:DWORD src1_sel:WORD_1\n"
    "v_mfma_f32_16x16x32_bf16 v[120:123], a[24:27], v[196:199], v[120:123]\n"
    "v_lshlrev_b32_sdwa v194, 1, v172 dst_sel:DWORD dst_unused:UNUSED_PAD src0_sel:DWORD src1_sel:WORD_1\n"
    "v_or_b32_e32 v186, v186, v202\n"
    "v_mfma_f32_16x16x32_bf16 v[124:127], a[28:31], v[196:199], v[124:127]\n"
    "s_waitcnt lgkmcnt(7)\n"
    "ds_read_u16_d16_hi v201, v201\n"
    "v_lshlrev_b32_sdwa v202, 1, v173 dst_sel:DWORD dst_unused:UNUSED_PAD src0_sel:DWORD src1_sel:WORD_1\n"
    "v_mfma_f32_16x16x32_bf16 v[136:139], v[8:11], v[196:199], v[136:139]\n"
    "ds_read_u16 v194, v194\n"
    "v_lshlrev_b32_sdwa v195, 1, v174 dst_sel:DWORD dst_unused:UNUSED_PAD src0_sel:DWORD src1_sel:WORD_1\n"
    "v_mfma_f32_16x16x32_bf16 v[140:143], v[12:15], v[196:199], v[140:143]\n"
    "s_nop 0\n"
    "v_cvt_pk_f16_f32 v196, v176, v177\n"
    "v_lshlrev_b32_sdwa v197, 1, v178 dst_sel:DWORD dst_unused:UNUSED_PAD src0_sel:DWORD src1_sel:WORD_1\n"
    "v_mfma_f32_16x16x32_bf16 v[168:171], a[112:115], a[224:227], 0\n"
    "s_waitcnt lgkmcnt(7)\n"
    "ds_read_u16_d16_hi v202, v202\n"
    "v_lshlrev_b32_sdwa v198, 1, v180 dst_sel:DWORD dst_unused:UNUSED_PAD src0_sel:DWORD src1_sel:WORD_1\n"
    "v_mfma_f32_16x16x32_bf16 v[168:171], a[160:163], a[228:231], v[168:171]\n"
    "v_lshlrev_b32_sdwa v199, 1, v182 dst_sel:DWORD dst_unused:UNUSED_PAD src0_sel:DWORD src1_sel:WORD_1\n"
    "v_or_b32_e64 v187, v187, v203\n"
    "v_mfma_f32_16x16x32_bf16 v[168:171], a[72:75], a[232:235], v[168:171]\n"
    "ds_read_u16 v195, v195\n"
    "v_lshlrev_b32_sdwa v203, 1, v175 dst_sel:DWORD dst_unused:UNUSED_PAD src0_sel:DWORD src1_sel:WORD_1\n"
    "v_mfma_f32_16x16x32_bf16 v[172:175], a[116:119], a[224:227], 0\n"
    "v_or_b32_e64 v188, v188, v204\n"
    "v_or_b32_e32 v189, v189, v205\n"
    "v_mfma_f32_16x16x32_bf16 v[172:175], a[164:167], a[228:231], v[172:175]\n"
    "s_waitcnt lgkmcnt(7)\n"
    "ds_read_u16_d16_hi v203, v203\n"
    "v_lshlrev_b32_sdwa v205, 1, v179 dst_sel:DWORD dst_unused:UNUSED_PAD src0_sel:DWORD src1_sel:WORD_1\n"
    "v_mfma_f32_16x16x32_bf16 v[172:175], v[52:55], a[232:235], v[172:175]\n"
    "ds_read_u16 v197, v197\n"
    "buffer_store_dwordx4 v[228:231], v245, s[44:47], s57 offen offset:128 sc1\n"
    "v_mfma_f32_16x16x32_bf16 v[176:179], a[112:115], a[236:239], 0\n"
    "s_waitcnt lgkmcnt(7)\n"
    "ds_read_u16_d16_hi v205, v205\n"
    "v_or_b32_e32 v190, v190, v206\n"
    "v_mfma_f32_16x16x32_bf16 v[176:179], a[160:163], a[240:243], v[176:179]\n"
    "v_lshlrev_b32_sdwa v206, 1, v181 dst_sel:DWORD dst_unused:UNUSED_PAD src0_sel:DWORD src1_sel:WORD_1\n"
    "v_or_b32_e64 v191, v191, v207\n"
    "v_mfma_f32_16x16x32_bf16 v[176:179], a[72:75], a[244:247], v[176:179]\n"
    "ds_read_u16 v198, v198\n"
    "v_lshlrev_b32_sdwa v207, 1, v183 dst_sel:DWORD dst_unused:UNUSED_PAD src0_sel:DWORD src1_sel:WORD_1\n"
    "v_mfma_f32_16x16x32_bf16 v[180:183], a[116:119], a[236:239], 0\n"
    "v_pk_fma_f16 v232, v192, v192, s48\n"
    "v_lshlrev_b32_sdwa v200, 1, v169 dst_sel:DWORD dst_unused:UNUSED_PAD src0_sel:DWORD src1_sel:WORD_1\n"
    "v_mfma_f32_16x16x32_bf16 v[180:183], a[164:167], a[240:243], v[180:183]\n"
    "s_waitcnt lgkmcnt(7)\n"
    "ds_read_u16_d16_hi v206, v206\n"
    "v_or_b32_e32 v193, v193, v201\n"
    "v_mfma_f32_16x16x32_bf16 v[180:183], v[52:55], a[244:247], v[180:183]\n"
    "ds_read_u16 v199, v199\n"
    "v_pk_mul_f16 v232, v192, v232\n"
    "v_mfma_f32_16x16x32_bf16 v[96:99], a[32:35], v[184:187], v[96:99]\n"
    "v_exp_f16_e32 v234, v232\n"
    "v_mfma_f32_16x16x32_bf16 v[100:103], a[36:39], v[184:187], v[100:103]\n"
    "s_waitcnt lgkmcnt(7)\n"
    "ds_read_u16_d16_hi v207, v207\n"
    "v_or_b32_e64 v194, v194, v202\n"
    "v_mfma_f32_16x16x32_bf16 v[112:115], a[40:43], v[184:187], v[112:115]\n"
    "v_exp_f16_sdwa v234, v232 dst_sel:WORD_1 dst_unused:UNUSED_PRESERVE src0_sel:WORD_1\n"
    "v_mfma_f32_16x16x32_bf16 v[116:119], a[44:47], v[184:187], v[116:119]\n"
    "ds_read_u16_d16_hi v200, v200\n"
    "v_pk_fma_f16 v234, v234, s49, s49\n"
    "v_mfma_f32_16x16x32_bf16 v[128:131], v[16:19], v[184:187], v[128:131]\n"
    "v_lshlrev_b32_sdwa v201, 1, v171 dst_sel:DWORD dst_unused:UNUSED_PAD src0_sel:DWORD src1_sel:WORD_1\n"
    "v_lshlrev_b32_sdwa v202, 1, v173 dst_sel:DWORD dst_unused:UNUSED_PAD src0_sel:DWORD src1_sel:WORD_1\n"
    "v_mfma_f32_16x16x32_bf16 v[132:135], v[20:23], v[184:187], v[132:135]\n"
    "s_waitcnt lgkmcnt(7)\n"
    "ds_read_u16_d16_hi v201, v201\n"
    "v_or_b32_e32 v195, v195, v203\n"
    "v_mfma_f32_16x16x32_bf16 v[104:107], a[32:35], v[188:191], v[104:107]\n"
    "ds_read_u16_d16_hi v202, v202\n"
    "v_lshlrev_b32_sdwa v184, 1, v168 dst_sel:DWORD dst_unused:UNUSED_PAD src0_sel:DWORD src1_sel:WORD_1\n"
    "v_mfma_f32_16x16x32_bf16 v[108:111], a[36:39], v[188:191], v[108:111]\n"
    "v_lshlrev_b32_sdwa v185, 1, v170 dst_sel:DWORD dst_unused:UNUSED_PAD src0_sel:DWORD src1_sel:WORD_1\n"
    "v_lshlrev_b32_sdwa v186, 1, v172 dst_sel:DWORD dst_unused:UNUSED_PAD src0_sel:DWORD src1_sel:WORD_1\n"
    "v_mfma_f32_16x16x32_bf16 v[120:123], a[40:43], v[188:191], v[120:123]\n"
    "s_waitcnt lgkmcnt(7)\n"
    "ds_read_u16 v184, v184\n"
    "v_or_b32_e32 v197, v197, v205\n"
    "v_mfma_f32_16x16x32_bf16 v[124:127], a[44:47], v[188:191], v[124:127]\n"
    "v_lshlrev_b32_sdwa v187, 1, v174 dst_sel:DWORD dst_unused:UNUSED_PAD src0_sel:DWORD src1_sel:WORD_1\n"
    "v_lshlrev_b32_sdwa v203, 1, v175 dst_sel:DWORD dst_unused:UNUSED_PAD src0_sel:DWORD src1_sel:WORD_1\n"
    "v_mfma_f32_16x16x32_bf16 v[136:139], v[16:19], v[188:191], v[136:139]\n"
    "ds_read_u16 v185, v185\n"
    "buffer_store_dwordx4 v[216:219], v245, s[44:47], s88 offen offset:0 sc1\n"
    "v_mfma_f32_16x16x32_bf16 v[140:143], v[20:23], v[188:191], v[140:143]\n"
    "s_waitcnt lgkmcnt(7)\n"
    "ds_read_u16 v186, v186\n"
    "v_or_b32_e32 v198, v198, v206\n"
    "v_mfma_f32_16x16x32_bf16 v[168:171], a[120:123], a[224:227], 0\n"
    "ds_read_u16 v187, v187\n"
    "v_rcp_f16_e32 v232, v234\n"
    "v_mfma_f32_16x16x32_bf16 v[168:171], a[168:171], a[228:231], v[168:171]\n"
    "v_rcp_f16_sdwa v232, v234 dst_sel:WORD_1 dst_unused:UNUSED_PRESERVE src0_sel:WORD_1\n"
    "v_mfma_f32_16x16x32_bf16 v[168:171], a[76:79], a[232:235], v[168:171]\n"
    "s_waitcnt lgkmcnt(7)\n"
    "ds_read_u16_d16_hi v203, v203\n"
    "v_lshlrev_b32_sdwa v188, 1, v176 dst_sel:DWORD dst_unused:UNUSED_PAD src0_sel:DWORD src1_sel:WORD_1\n"
    "v_mfma_f32_16x16x32_bf16 v[172:175], a[124:127], a[224:227], 0\n"
    "v_lshlrev_b32_sdwa v204, 1, v177 dst_sel:DWORD dst_unused:UNUSED_PAD src0_sel:DWORD src1_sel:WORD_1\n"
    "v_lshlrev_b32_sdwa v189, 1, v178 dst_sel:DWORD dst_unused:UNUSED_PAD src0_sel:DWORD src1_sel:WORD_1\n"
    "v_mfma_f32_16x16x32_bf16 v[172:175], a[172:175], a[228:231], v[172:175]\n"
    "ds_read_u16 v188, v188\n"
    "v_lshlrev_b32_sdwa v205, 1, v179 dst_sel:DWORD dst_unused:UNUSED_PAD src0_sel:DWORD src1_sel:WORD_1\n"
    "v_lshlrev_b32_sdwa v190, 1, v180 dst_sel:DWORD dst_unused:UNUSED_PAD src0_sel:DWORD src1_sel:WORD_1\n"
    "v_lshlrev_b32_sdwa v206, 1, v181 dst_sel:DWORD dst_unused:UNUSED_PAD src0_sel:DWORD src1_sel:WORD_1\n"
    "v_lshlrev_b32_sdwa v191, 1, v182 dst_sel:DWORD dst_unused:UNUSED_PAD src0_sel:DWORD src1_sel:WORD_1\n"
    "v_fma_mix_f32 v234, v192, v232, 0 op_sel_hi:[1,1,0]\n"
    "v_mfma_f32_16x16x32_bf16 v[172:175], v[60:63], a[232:235], v[172:175]\n"
    "ds_read_u16_d16_hi v204, v204\n"
    "v_or_b32_e64 v199, v199, v207\n"
    "v_lshlrev_b32_sdwa v207, 1, v183 dst_sel:DWORD dst_unused:UNUSED_PAD src0_sel:DWORD src1_sel:WORD_1\n"
    "v_fma_mix_f32 v232, v192, v232, 0 op_sel:[1,1,0] op_sel_hi:[1,1,0]\n"
    "v_mfma_f32_16x16x32_bf16 v[176:179], a[120:123], a[236:239], 0\n"
    "s_nop 0\n"
    "ds_read_u16 v189, v189\n"
    "v_cvt_pk_bf16_f32 v192, v234, v232\n"
    "v_mfma_f32_16x16x32_bf16 v[176:179], a[168:171], a[240:243], v[176:179]\n"
    "s_waitcnt lgkmcnt(7)\n"
    "ds_read_u16_d16_hi v205, v205\n"
    "v_pk_fma_f16 v232, v196, v196, s48\n"
    "v_mfma_f32_16x16x32_bf16 v[176:179], a[76:79], a[244:247], v[176:179]\n"
    "v_pk_mul_f16 v232, v196, v232\n"
    "v_or_b32_e32 v184, v184, v200\n"
    "v_mfma_f32_16x16x32_bf16 v[180:183], a[124:127], a[236:239], 0\n"
    "s_waitcnt lgkmcnt(7)\n"
    "ds_read_u16 v190, v190\n"
    "v_exp_f16_e32 v234, v232\n"
    "v_mfma_f32_16x16x32_bf16 v[180:183], a[172:175], a[240:243], v[180:183]\n"
    "v_exp_f16_sdwa v234, v232 dst_sel:WORD_1 dst_unused:UNUSED_PRESERVE src0_sel:WORD_1\n"
    "v_mfma_f32_16x16x32_bf16 v[180:183], v[60:63], a[244:247], v[180:183]\n"
    "s_waitcnt lgkmcnt(7)\n"
    "ds_read_b128 a[224:227], v243 offset:0\n"
    "ds_read_u16_d16_hi v206, v206\n"
    "v_mfma_f32_16x16x32_bf16 v[96:99], a[176:179], v[192:195], v[96:99]\n"
    "v_pk_fma_f16 v234, v234, s49, s49\n"
    "v_or_b32_e64 v185, v185, v201\n"
    "v_mfma_f32_16x16x32_bf16 v[100:103], a[180:183], v[192:195], v[100:103]\n"
    "ds_read_b128 a[228:231], v243 offset:1024\n"
    "buffer_store_dwordx4 v[220:223], v245, s[44:47], s88 offen offset:64 sc1\n"
    "v_rcp_f16_e32 v232, v234\n"
    "v_mfma_f32_16x16x32_bf16 v[112:115], a[184:187], v[192:195], v[112:115]\n"
    "s_waitcnt lgkmcnt(7)\n"
    "ds_read_u16 v191, v191\n"
    "ds_read_b128 a[232:235], v243 offset:2048\n"
    "v_rcp_f16_sdwa v232, v234 dst_sel:WORD_1 dst_unused:UNUSED_PRESERVE src0_sel:WORD_1\n"
    "v_mfma_f32_16x16x32_bf16 v[116:119], a[188:191], v[192:195], v[116:119]\n"
    "s_nop 0\n"
    "ds_read_u16_d16_hi v207, v207\n"
    "v_fma_mix_f32 v234, v196, v232, 0 op_sel_hi:[1,1,0]\n"
    "v_mfma_f32_16x16x32_bf16 v[128:131], v[24:27], v[192:195], v[128:131]\n"
    "s_waitcnt lgkmcnt(9)\n"
    "v_fma_mix_f32 v232, v196, v232, 0 op_sel:[1,1,0] op_sel_hi:[1,1,0]\n"
    "v_lshlrev_b32_sdwa v201, 1, v171 dst_sel:DWORD dst_unused:UNUSED_PAD src0_sel:DWORD src1_sel:WORD_1\n"
    "v_mfma_f32_16x16x32_bf16 v[132:135], v[28:31], v[192:195], v[132:135]\n"
    "ds_read_b128 a[236:239], v243 offset:3072\n"
    "v_cvt_pk_bf16_f32 v196, v234, v232\n"
    "v_cvt_pk_f16_f32 v192, v168, v169\n"
    "v_lshlrev_b32_sdwa v193, 1, v170 dst_sel:DWORD dst_unused:UNUSED_PAD src0_sel:DWORD src1_sel:WORD_1\n"
    "v_mfma_f32_16x16x32_bf16 v[104:107], a[176:179], v[196:199], v[104:107]\n"
    "ds_read_b128 a[240:243], v243 offset:4096\n"
    "ds_read_b128 a[244:247], v243 offset:5120\n"
    "v_mfma_f32_16x16x32_bf16 v[108:111], a[180:183], v[196:199], v[108:111]\n"
    "s_waitcnt lgkmcnt(10)\n"
    "ds_read_u16 v193, v193\n"
    "v_lshlrev_b32_sdwa v194, 1, v172 dst_sel:DWORD dst_unused:UNUSED_PAD src0_sel:DWORD src1_sel:WORD_1\n"
    "v_mfma_f32_16x16x32_bf16 v[120:123], a[184:187], v[196:199], v[120:123]\n"
    "s_waitcnt lgkmcnt(9)\n"
    "v_or_b32_e64 v186, v186, v202\n"
    "v_lshlrev_b32_sdwa v202, 1, v173 dst_sel:DWORD dst_unused:UNUSED_PAD src0_sel:DWORD src1_sel:WORD_1\n"
    "v_mfma_f32_16x16x32_bf16 v[124:127], a[188:191], v[196:199], v[124:127]\n"
    "ds_read_u16_d16_hi v201, v201\n"
    "v_lshlrev_b32_sdwa v195, 1, v174 dst_sel:DWORD dst_unused:UNUSED_PAD src0_sel:DWORD src1_sel:WORD_1\n"
    "v_mfma_f32_16x16x32_bf16 v[136:139], v[24:27], v[196:199], v[136:139]\n"
    "v_or_b32_e32 v187, v187, v203\n"
    "v_lshlrev_b32_sdwa v203, 1, v175 dst_sel:DWORD dst_unused:UNUSED_PAD src0_sel:DWORD src1_sel:WORD_1\n"
    "v_mfma_f32_16x16x32_bf16 v[140:143], v[28:31], v[196:199], v[140:143]\n"
    "s_waitcnt lgkmcnt(8)\n"
    "ds_read_u16 v194, v194\n"
    "v_cvt_pk_f16_f32 v196, v176, v177\n"
    "v_mfma_f32_16x16x32_bf16 v[168:171], a[80:83], a[224:227], 0\n"
    "v_lshlrev_b32_sdwa v197, 1, v178 dst_sel:DWORD dst_unused:UNUSED_PAD src0_sel:DWORD src1_sel:WORD_1\n"
    "v_lshlrev_b32_sdwa v198, 1, v180 dst_sel:DWORD dst_unused:UNUSED_PAD src0_sel:DWORD src1_sel:WORD_1\n"
    "v_mfma_f32_16x16x32_bf16 v[168:171], a[128:131], a[228:231], v[168:171]\n"
    "ds_read_u16_d16_hi v202, v202\n"
    "v_lshlrev_b32_sdwa v199, 1, v182 dst_sel:DWORD dst_unused:UNUSED_PAD src0_sel:DWORD src1_sel:WORD_1\n"
    "v_mfma_f32_16x16x32_bf16 v[172:175], a[84:87], a[224:227], 0\n"
    "s_waitcnt lgkmcnt(8)\n"
    "ds_read_u16 v195, v195\n"
    "v_or_b32_e32 v188, v188, v204\n"
    "v_mfma_f32_16x16x32_bf16 v[168:171], a[48:51], a[232:235], v[168:171]\n"
    "v_or_b32_e64 v189, v189, v205\n"
    "v_lshlrev_b32_sdwa v205, 1, v179 dst_sel:DWORD dst_unused:UNUSED_PAD src0_sel:DWORD src1_sel:WORD_1\n"
    "v_mfma_f32_16x16x32_bf16 v[172:175], a[132:135], a[228:231], v[172:175]\n"
    "s_waitcnt lgkmcnt(7)\n"
    "ds_read_u16_d16_hi v203, v203\n"
    "v_or_b32_e32 v190, v190, v206\n"
    "v_mfma_f32_16x16x32_bf16 v[172:175], v[48:51], a[232:235], v[172:175]\n"
    "buffer_store_dwordx4 v[160:163], v245, s[44:47], s88 offen offset:128 sc1\n"
    "ds_read_u16 v197, v197\n"
    "v_mfma_f32_16x16x32_bf16 v[176:179], a[80:83], a[236:239], 0\n"
    "s_waitcnt lgkmcnt(7)\n"
    "v_lshlrev_b32_sdwa v206, 1, v181 dst_sel:DWORD dst_unused:UNUSED_PAD src0_sel:DWORD src1_sel:WORD_1\n"
    "v_or_b32_e32 v191, v191, v207\n"
    "v_mfma_f32_16x16x32_bf16 v[176:179], a[128:131], a[240:243], v[176:179]\n"
    "ds_read_u16_d16_hi v205, v205\n"
    "v_lshlrev_b32_sdwa v207, 1, v183 dst_sel:DWORD dst_unused:UNUSED_PAD src0_sel:DWORD src1_sel:WORD_1\n"
    "v_mfma_f32_16x16x32_bf16 v[176:179], a[48:51], a[244:247], v[176:179]\n"
    "ds_read_u16 v198, v198\n"
    "v_pk_fma_f16 v232, v192, v192, s48\n"
    "v_mfma_f32_16x16x32_bf16 v[180:183], a[84:87], a[236:239], 0\n"
    "v_pk_mul_f16 v232, v192, v232\n"
    "v_lshlrev_b32_sdwa v200, 1, v169 dst_sel:DWORD dst_unused:UNUSED_PAD src0_sel:DWORD src1_sel:WORD_1\n"
    "v_mfma_f32_16x16x32_bf16 v[180:183], a[132:135], a[240:243], v[180:183]\n"
    "s_waitcnt lgkmcnt(7)\n"
    "ds_read_u16_d16_hi v206, v206\n"
    "v_or_b32_e32 v193, v193, v201\n"
    "v_mfma_f32_16x16x32_bf16 v[180:183], v[48:51], a[244:247], v[180:183]\n"
    "v_exp_f16_e64 v234, v232\n"
    "v_mfma_f32_16x16x32_bf16 v[96:99], a[192:195], v[184:187], v[96:99]\n"
    "ds_read_u16 v199, v199\n"
    "v_exp_f16_sdwa v234, v232 dst_sel:WORD_1 dst_unused:UNUSED_PRESERVE src0_sel:WORD_1\n"
    "v_mfma_f32_16x16x32_bf16 v[100:103], a[196:199], v[184:187], v[100:103]\n"
    "s_waitcnt lgkmcnt(7)\n"
    "ds_read_u16_d16_hi v207, v207\n"
    "v_or_b32_e32 v194, v194, v202\n"
    "v_mfma_f32_16x16x32_bf16 v[112:115], a[200:203], v[184:187], v[112:115]\n"
    "v_pk_fma_f16 v234, v234, s49, s49\n"
    "v_lshlrev_b32_sdwa v201, 1, v171 dst_sel:DWORD dst_unused:UNUSED_PAD src0_sel:DWORD src1_sel:WORD_1\n"
    "v_mfma_f32_16x16x32_bf16 v[116:119], a[204:207], v[184:187], v[116:119]\n"
    "ds_read_u16_d16_hi v200, v200\n"
    "v_lshlrev_b32_sdwa v202, 1, v173 dst_sel:DWORD dst_unused:UNUSED_PAD src0_sel:DWORD src1_sel:WORD_1\n"
    "v_mfma_f32_16x16x32_bf16 v[128:131], v[32:35], v[184:187], v[128:131]\n"
    "v_rcp_f16_e32 v232, v234\n"
    "v_mfma_f32_16x16x32_bf16 v[132:135], v[36:39], v[184:187], v[132:135]\n"
    "s_waitcnt lgkmcnt(7)\n"
    "ds_read_u16_d16_hi v201, v201\n"
    "v_or_b32_e64 v195, v195, v203\n"
    "v_mfma_f32_16x16x32_bf16 v[104:107], a[192:195], v[188:191], v[104:107]\n"
    "v_lshlrev_b32_sdwa v184, 1, v168 dst_sel:DWORD dst_unused:UNUSED_PAD src0_sel:DWORD src1_sel:WORD_1\n"
    "v_lshlrev_b32_sdwa v185, 1, v170 dst_sel:DWORD dst_unused:UNUSED_PAD src0_sel:DWORD src1_sel:WORD_1\n"
    "v_mfma_f32_16x16x32_bf16 v[108:111], a[196:199], v[188:191], v[108:111]\n"
    "ds_read_u16 v184, v184\n"
    "v_lshlrev_b32_sdwa v186, 1, v172 dst_sel:DWORD dst_unused:UNUSED_PAD src0_sel:DWORD src1_sel:WORD_1\n"
    "v_mfma_f32_16x16x32_bf16 v[120:123], a[200:203], v[188:191], v[120:123]\n"
    "s_waitcnt lgkmcnt(7)\n"
    "ds_read_u16 v185, v185\n"
    "v_or_b32_e32 v197, v197, v205\n"
    "v_mfma_f32_16x16x32_bf16 v[124:127], a[204:207], v[188:191], v[124:127]\n"
    "v_lshlrev_b32_sdwa v187, 1, v174 dst_sel:DWORD dst_unused:UNUSED_PAD src0_sel:DWORD src1_sel:WORD_1\n"
    "v_lshlrev_b32_sdwa v203, 1, v175 dst_sel:DWORD dst_unused:UNUSED_PAD src0_sel:DWORD src1_sel:WORD_1\n"
    "v_mfma_f32_16x16x32_bf16 v[136:139], v[32:35], v[188:191], v[136:139]\n"
    "ds_read_u16 v186, v186\n"
    "v_rcp_f16_sdwa v232, v234 dst_sel:WORD_1 dst_unused:UNUSED_PRESERVE src0_sel:WORD_1\n"
    "v_mfma_f32_16x16x32_bf16 v[140:143], v[36:39], v[188:191], v[140:143]\n"
    "v_lshlrev_b32_sdwa v188, 1, v176 dst_sel:DWORD dst_unused:UNUSED_PAD src0_sel:DWORD src1_sel:WORD_1\n"
    "v_lshlrev_b32_sdwa v204, 1, v177 dst_sel:DWORD dst_unused:UNUSED_PAD src0_sel:DWORD src1_sel:WORD_1\n"
    "v_mfma_f32_16x16x32_bf16 v[168:171], a[88:91], a[224:227], 0\n"
    "s_waitcnt lgkmcnt(7)\n"
    "ds_read_u16_d16_hi v202, v202\n"
    "v_or_b32_e32 v198, v198, v206\n"
    "v_mfma_f32_16x16x32_bf16 v[168:171], a[136:139], a[228:231], v[168:171]\n"
    "ds_read_u16 v187, v187\n"
    "v_lshlrev_b32_sdwa v189, 1, v178 dst_sel:DWORD dst_unused:UNUSED_PAD src0_sel:DWORD src1_sel:WORD_1\n"
    "v_mfma_f32_16x16x32_bf16 v[168:171], a[52:55], a[232:235], v[168:171]\n"
    "s_nop 0\n"
    "v_lshlrev_b32_sdwa v205, 1, v179 dst_sel:DWORD dst_unused:UNUSED_PAD src0_sel:DWORD src1_sel:WORD_1\n"
    "v_lshlrev_b32_sdwa v190, 1, v180 dst_sel:DWORD dst_unused:UNUSED_PAD src0_sel:DWORD src1_sel:WORD_1\n"
    "v_mfma_f32_16x16x32_bf16 v[172:175], a[92:95], a[224:227], 0\n"
    "s_waitcnt lgkmcnt(7)\n"
    "ds_read_u16_d16_hi v203, v203\n"
    "v_lshlrev_b32_sdwa v206, 1, v181 dst_sel:DWORD dst_unused:UNUSED_PAD src0_sel:DWORD src1_sel:WORD_1\n"
    "v_mfma_f32_16x16x32_bf16 v[172:175], a[140:143], a[228:231], v[172:175]\n"
    "v_lshlrev_b32_sdwa v191, 1, v182 dst_sel:DWORD dst_unused:UNUSED_PAD src0_sel:DWORD src1_sel:WORD_1\n"
    "v_fma_mix_f32 v234, v192, v232, 0 op_sel_hi:[1,1,0]\n"
    "v_mfma_f32_16x16x32_bf16 v[172:175], v[56:59], a[232:235], v[172:175]\n"
    "ds_read_u16 v188, v188\n"
    "v_or_b32_e64 v199, v199, v207\n"
    "v_lshlrev_b32_sdwa v207, 1, v183 dst_sel:DWORD dst_unused:UNUSED_PAD src0_sel:DWORD src1_sel:WORD_1\n"
    "v_fma_mix_f32 v232, v192, v232, 0 op_sel:[1,1,0] op_sel_hi:[1,1,0]\n"
    "v_mfma_f32_16x16x32_bf16 v[176:179], a[88:91], a[236:239], 0\n"
    "ds_read_u16_d16_hi v204, v204\n"
    "v_cvt_pk_bf16_f32 v192, v234, v232\n"
    "v_mfma_f32_16x16x32_bf16 v[176:179], a[136:139], a[240:243], v[176:179]\n"
    "v_pk_fma_f16 v232, v196, v196, s48\n"
    "v_pk_mul_f16 v232, v196, v232\n"
    "v_mfma_f32_16x16x32_bf16 v[176:179], a[52:55], a[244:247], v[176:179]\n"
    "s_waitcnt lgkmcnt(7)\n"
    "ds_read_u16 v189, v189\n"
    "v_exp_f16_e64 v234, v232\n"
    "v_mfma_f32_16x16x32_bf16 v[180:183], a[92:95], a[236:239], 0\n"
    "s_waitcnt lgkmcnt(7)\n"
    "ds_read_u16_d16_hi v205, v205\n"
    "v_exp_f16_sdwa v234, v232 dst_sel:WORD_1 dst_unused:UNUSED_PRESERVE src0_sel:WORD_1\n"
    "v_mfma_f32_16x16x32_bf16 v[180:183], a[140:143], a[240:243], v[180:183]\n"
    "v_pk_fma_f16 v234, v234, s49, s49\n"
    "v_or_b32_e64 v184, v184, v200\n"
    "v_mfma_f32_16x16x32_bf16 v[180:183], v[56:59], a[244:247], v[180:183]\n"
    "ds_read_u16 v190, v190\n"
    "v_rcp_f16_e32 v232, v234\n"
    "v_mfma_f32_16x16x32_bf16 v[96:99], a[208:211], v[192:195], v[96:99]\n"
    "s_waitcnt lgkmcnt(7)\n"
    "ds_read_u16_d16_hi v206, v206\n"
    "v_rcp_f16_sdwa v232, v234 dst_sel:WORD_1 dst_unused:UNUSED_PRESERVE src0_sel:WORD_1\n"
    "v_mfma_f32_16x16x32_bf16 v[100:103], a[212:215], v[192:195], v[100:103]\n"
    "v_fma_mix_f32 v234, v196, v232, 0 op_sel_hi:[1,1,0]\n"
    "v_fma_mix_f32 v232, v196, v232, 0 op_sel:[1,1,0] op_sel_hi:[1,1,0]\n"
    "v_mfma_f32_16x16x32_bf16 v[112:115], a[216:219], v[192:195], v[112:115]\n"
    "ds_read_u16 v191, v191\n"
    "v_cvt_pk_bf16_f32 v196, v234, v232\n"
    "v_mfma_f32_16x16x32_bf16 v[116:119], a[220:223], v[192:195], v[116:119]\n"
    "v_or_b32_e64 v185, v185, v201\n"
    "v_lshlrev_b32_sdwa v201, 1, v171 dst_sel:DWORD dst_unused:UNUSED_PAD src0_sel:DWORD src1_sel:WORD_1\n"
    "v_mfma_f32_16x16x32_bf16 v[128:131], v[40:43], v[192:195], v[128:131]\n"
    "s_waitcnt lgkmcnt(7)\n"
    "ds_read_u16_d16_hi v207, v207\n"
    "v_or_b32_e32 v186, v186, v202\n"
    "v_mfma_f32_16x16x32_bf16 v[132:135], v[44:47], v[192:195], v[132:135]\n"
    "ds_read_u16_d16_hi v201, v201\n"
    "v_cvt_pk_f16_f32 v192, v168, v169\n"
    "v_mfma_f32_16x16x32_bf16 v[104:107], a[208:211], v[196:199], v[104:107]\n"
    "s_nop 0\n"
    "v_lshlrev_b32_sdwa v193, 1, v170 dst_sel:DWORD dst_unused:UNUSED_PAD src0_sel:DWORD src1_sel:WORD_1\n"
    "v_lshlrev_b32_sdwa v194, 1, v172 dst_sel:DWORD dst_unused:UNUSED_PAD src0_sel:DWORD src1_sel:WORD_1\n"
    "v_mfma_f32_16x16x32_bf16 v[108:111], a[212:215], v[196:199], v[108:111]\n"
    "s_waitcnt lgkmcnt(7)\n"
    "ds_read_u16 v193, v193\n"
    "v_lshlrev_b32_sdwa v202, 1, v173 dst_sel:DWORD dst_unused:UNUSED_PAD src0_sel:DWORD src1_sel:WORD_1\n"
    "v_mfma_f32_16x16x32_bf16 v[120:123], a[216:219], v[196:199], v[120:123]\n"
    "v_lshlrev_b32_sdwa v195, 1, v174 dst_sel:DWORD dst_unused:UNUSED_PAD src0_sel:DWORD src1_sel:WORD_1\n"
    "v_or_b32_e64 v187, v187, v203\n"
    "v_mfma_f32_16x16x32_bf16 v[124:127], a[220:223], v[196:199], v[124:127]\n"
    "ds_read_u16 v194, v194\n"
    "v_lshlrev_b32_sdwa v203, 1, v175 dst_sel:DWORD dst_unused:UNUSED_PAD src0_sel:DWORD src1_sel:WORD_1\n"
    "v_mfma_f32_16x16x32_bf16 v[136:139], v[40:43], v[196:199], v[136:139]\n"
    "v_or_b32_e32 v188, v188, v204\n"
    "v_cvt_pk_bf16_f32 v96, v96, v97\n"
    "v_mfma_f32_16x16x32_bf16 v[140:143], v[44:47], v[196:199], v[140:143]\n"
    "s_waitcnt lgkmcnt(7)\n"
    "ds_read_u16_d16_hi v202, v202\n"
    "v_cvt_pk_f16_f32 v196, v176, v177\n"
    "v_mfma_f32_16x16x32_bf16 v[168:171], a[96:99], a[224:227], 0\n"
    "ds_read_u16 v195, v195\n"
    "v_lshlrev_b32_sdwa v197, 1, v178 dst_sel:DWORD dst_unused:UNUSED_PAD src0_sel:DWORD src1_sel:WORD_1\n"
    "v_mfma_f32_16x16x32_bf16 v[168:171], a[144:147], a[228:231], v[168:171]\n"
    "v_lshlrev_b32_sdwa v198, 1, v180 dst_sel:DWORD dst_unused:UNUSED_PAD src0_sel:DWORD src1_sel:WORD_1\n"
    "v_lshlrev_b32_sdwa v199, 1, v182 dst_sel:DWORD dst_unused:UNUSED_PAD src0_sel:DWORD src1_sel:WORD_1\n"
    "v_mfma_f32_16x16x32_bf16 v[168:171], a[56:59], a[232:235], v[168:171]\n"
    "s_waitcnt lgkmcnt(7)\n"
    "ds_read_u16_d16_hi v203, v203\n"
    "v_or_b32_e32 v189, v189, v205\n"
    "v_mfma_f32_16x16x32_bf16 v[172:175], a[100:103], a[224:227], 0\n"
    "v_lshlrev_b32_sdwa v205, 1, v179 dst_sel:DWORD dst_unused:UNUSED_PAD src0_sel:DWORD src1_sel:WORD_1\n"
    "v_cvt_pk_bf16_f32 v97, v98, v99\n"
    "v_mfma_f32_16x16x32_bf16 v[172:175], a[148:151], a[228:231], v[172:175]\n"
    "ds_read_u16 v197, v197\n"
    "v_cvt_pk_bf16_f32 v98, v100, v101\n"
    "v_mfma_f32_16x16x32_bf16 v[172:175], a[60:63], a[232:235], v[172:175]\n"
    "v_cvt_pk_bf16_f32 v99, v102, v103\n"
    "v_or_b32_e32 v190, v190, v206\n"
    "v_mfma_f32_16x16x32_bf16 v[176:179], a[96:99], a[236:239], 0\n"
    "s_waitcnt lgkmcnt(7)\n"
    "v_lshlrev_b32_sdwa v206, 1, v181 dst_sel:DWORD dst_unused:UNUSED_PAD src0_sel:DWORD src1_sel:WORD_1\n"
    "v_cvt_pk_bf16_f32 v112, v112, v113\n"
    "v_mfma_f32_16x16x32_bf16 v[176:179], a[144:147], a[240:243], v[176:179]\n"
    "s_nop 0\n"
    "ds_write_b128 v243, v[96:99] offset:0\n"
    "v_mfma_f32_16x16x32_bf16 v[176:179], a[56:59], a[244:247], v[176:179]\n"
    "s_waitcnt lgkmcnt(6)\n"
    "v_cvt_pk_bf16_f32 v113, v114, v115\n"
    "v_or_b32_e64 v193, v193, v201\n"
    "v_mfma_f32_16x16x32_bf16 v[96:99], a[0:3], v[184:187], 0\n"
    "v_cvt_pk_bf16_f32 v114, v116, v117\n"
    "v_cvt_pk_bf16_f32 v115, v118, v119\n"
    "v_mfma_f32_16x16x32_bf16 v[100:103], a[4:7], v[184:187], 0\n"
    "s_nop 0\n"
    "ds_read_u16_d16_hi v205, v205\n"
    "ds_write_b128 v243, v[112:115] offset:2048\n"
    "v_mfma_f32_16x16x32_bf16 v[112:115], a[8:11], v[184:187], 0\n"
    "s_waitcnt lgkmcnt(6)\n"
    "v_cvt_pk_bf16_f32 v128, v128, v129\n"
    "v_cvt_pk_bf16_f32 v129, v130, v131\n"
    "v_or_b32_e64 v191, v191, v207\n"
    "v_mfma_f32_16x16x32_bf16 v[116:119], a[12:15], v[184:187], 0\n"
    "v_lshlrev_b32_sdwa v207, 1, v183 dst_sel:DWORD dst_unused:UNUSED_PAD src0_sel:DWORD src1_sel:WORD_1\n"
    "v_cvt_pk_bf16_f32 v130, v132, v133\n"
    "v_mfma_f32_16x16x32_bf16 v[180:183], a[100:103], a[236:239], 0\n"
    "s_nop 0\n"
    "ds_read_u16 v198, v198\n"
    "ds_read_u16_d16_hi v206, v206\n"
    "v_mfma_f32_16x16x32_bf16 v[180:183], a[148:151], a[240:243], v[180:183]\n"
    "s_waitcnt lgkmcnt(6)\n"
    "ds_read_u16 v199, v199\n"
    "ds_read_u16_d16_hi v207, v207\n"
    "v_mfma_f32_16x16x32_bf16 v[180:183], a[60:63], a[244:247], v[180:183]\n"
    "v_cvt_pk_bf16_f32 v131, v134, v135\n"
    "v_or_b32_e64 v194, v194, v202\n"
    "v_mfma_f32_16x16x32_bf16 v[132:135], v[4:7], v[184:187], 0\n"
    "v_cvt_pk_bf16_f32 v104, v104, v105\n"
    "v_cvt_pk_bf16_f32 v105, v106, v107\n"
    "v_cvt_pk_bf16_f32 v106, v108, v109\n"
    "v_cvt_pk_bf16_f32 v107, v110, v111\n"
    "v_mfma_f32_16x16x32_bf16 v[108:111], a[4:7], v[188:191], 0\n"
    "s_nop 0\n"
    "ds_write_b128 v243, v[128:131] offset:4096\n"
    "v_mfma_f32_16x16x32_bf16 v[128:131], v[0:3], v[184:187], 0\n"
    "s_waitcnt lgkmcnt(5)\n"
    "v_cvt_pk_bf16_f32 v120, v120, v121\n"
    "v_cvt_pk_bf16_f32 v121, v122, v123\n"
    "v_cvt_pk_bf16_f32 v122, v124, v125\n"
    "v_cvt_pk_bf16_f32 v123, v126, v127\n"
    "v_or_b32_e64 v195, v195, v203\n"
    "v_cvt_pk_bf16_f32 v136, v136, v137\n"
    "ds_write_b128 v243, v[104:107] offset:1024\n"
    "v_mfma_f32_16x16x32_bf16 v[104:107], a[0:3], v[188:191], 0\n"
    "v_pk_fma_f16 v232, v192, v192, s48\n"
    "v_cvt_pk_bf16_f32 v137, v138, v139\n"
    "v_mfma_f32_16x16x32_bf16 v[124:127], a[12:15], v[188:191], 0\n"
    "s_nop 0\n"
    "ds_write_b128 v243, v[120:123] offset:3072\n"
    "v_mfma_f32_16x16x32_bf16 v[120:123], a[8:11], v[188:191], 0\n"
    "s_waitcnt lgkmcnt(5)\n"
    "v_cvt_pk_bf16_f32 v138, v140, v141\n"
    "v_cvt_pk_bf16_f32 v139, v142, v143\n"
    "v_pk_mul_f16 v232, v192, v232\n"
    "v_or_b32_e64 v197, v197, v205\n"
    "v_exp_f16_e32 v234, v232\n"
    "v_mfma_f32_16x16x32_bf16 v[140:143], v[4:7], v[188:191], 0\n"
    "ds_write_b128 v243, v[136:139] offset:5120\n"
    "v_mfma_f32_16x16x32_bf16 v[136:139], v[0:3], v[188:191], 0\n"
    "s_waitcnt lgkmcnt(3)\n"
    "v_exp_f16_sdwa v234, v232 dst_sel:WORD_1 dst_unused:UNUSED_PRESERVE src0_sel:WORD_1\n"
    "v_lshlrev_b32_sdwa v184, 1, v168 dst_sel:DWORD dst_unused:UNUSED_PAD src0_sel:DWORD src1_sel:WORD_1\n"
    "v_pk_fma_f16 v234, v234, s49, s49\n"
    "v_lshlrev_b32_sdwa v200, 1, v169 dst_sel:DWORD dst_unused:UNUSED_PAD src0_sel:DWORD src1_sel:WORD_1\n"
    "v_lshlrev_b32_sdwa v185, 1, v170 dst_sel:DWORD dst_unused:UNUSED_PAD src0_sel:DWORD src1_sel:WORD_1\n"
    "v_lshlrev_b32_sdwa v201, 1, v171 dst_sel:DWORD dst_unused:UNUSED_PAD src0_sel:DWORD src1_sel:WORD_1\n"
    "v_mfma_f32_16x16x32_bf16 v[168:171], a[104:107], a[224:227], 0\n"
    "ds_read_u16 v184, v184\n"
    "ds_read_u16_d16_hi v200, v200\n"
    "v_mfma_f32_16x16x32_bf16 v[168:171], a[152:155], a[228:231], v[168:171]\n"
    "ds_read_u16 v185, v185\n"
    "ds_read_u16_d16_hi v201, v201\n"
    "v_lshlrev_b32_sdwa v186, 1, v172 dst_sel:DWORD dst_unused:UNUSED_PAD src0_sel:DWORD src1_sel:WORD_1\n"
    "v_lshlrev_b32_sdwa v202, 1, v173 dst_sel:DWORD dst_unused:UNUSED_PAD src0_sel:DWORD src1_sel:WORD_1\n"
    "v_or_b32_e64 v198, v198, v206\n"
    "v_lshlrev_b32_sdwa v187, 1, v174 dst_sel:DWORD dst_unused:UNUSED_PAD src0_sel:DWORD src1_sel:WORD_1\n"
    "v_lshlrev_b32_sdwa v203, 1, v175 dst_sel:DWORD dst_unused:UNUSED_PAD src0_sel:DWORD src1_sel:WORD_1\n"
    "v_rcp_f16_e64 v232, v234\n"
    "v_mfma_f32_16x16x32_bf16 v[168:171], a[64:67], a[232:235], v[168:171]\n"
    "ds_read_u16 v186, v186\n"
    "v_rcp_f16_sdwa v232, v234 dst_sel:WORD_1 dst_unused:UNUSED_PRESERVE src0_sel:WORD_1\n"
    "v_mfma_f32_16x16x32_bf16 v[172:175], a[108:111], a[224:227], 0\n"
    "s_waitcnt lgkmcnt(5)\n"
    "ds_read_u16_d16_hi v202, v202\n"
    "v_lshlrev_b32_sdwa v188, 1, v176 dst_sel:DWORD dst_unused:UNUSED_PAD src0_sel:DWORD src1_sel:WORD_1\n"
    "v_mfma_f32_16x16x32_bf16 v[172:175], a[156:159], a[228:231], v[172:175]\n"
    "s_barrier\n"
    "v_lshlrev_b32_sdwa v204, 1, v177 dst_sel:DWORD dst_unused:UNUSED_PAD src0_sel:DWORD src1_sel:WORD_1\n"
    "v_lshlrev_b32_sdwa v189, 1, v178 dst_sel:DWORD dst_unused:UNUSED_PAD src0_sel:DWORD src1_sel:WORD_1\n"
    "v_lshlrev_b32_sdwa v205, 1, v179 dst_sel:DWORD dst_unused:UNUSED_PAD src0_sel:DWORD src1_sel:WORD_1\n"
    "v_lshlrev_b32_sdwa v190, 1, v180 dst_sel:DWORD dst_unused:UNUSED_PAD src0_sel:DWORD src1_sel:WORD_1\n"
    "v_lshlrev_b32_sdwa v206, 1, v181 dst_sel:DWORD dst_unused:UNUSED_PAD src0_sel:DWORD src1_sel:WORD_1\n"
    "v_lshlrev_b32_sdwa v191, 1, v182 dst_sel:DWORD dst_unused:UNUSED_PAD src0_sel:DWORD src1_sel:WORD_1\n"
    "v_fma_mix_f32 v234, v192, v232, 0 op_sel_hi:[1,1,0]\n"
    "v_mfma_f32_16x16x32_bf16 v[172:175], a[68:71], a[232:235], v[172:175]\n"
    "ds_read_u16 v187, v187\n"
    "ds_read_u16_d16_hi v203, v203\n"
    "v_or_b32_e32 v199, v199, v207\n"
    "v_lshlrev_b32_sdwa v207, 1, v183 dst_sel:DWORD dst_unused:UNUSED_PAD src0_sel:DWORD src1_sel:WORD_1\n"
    "v_fma_mix_f32 v232, v192, v232, 0 op_sel:[1,1,0] op_sel_hi:[1,1,0]\n"
    "v_mfma_f32_16x16x32_bf16 v[176:179], a[104:107], a[236:239], 0\n"
    "s_waitcnt lgkmcnt(6)\n"
    "ds_read_u16 v188, v188\n"
    "ds_read_u16_d16_hi v204, v204\n"
    "v_cvt_pk_bf16_f32 v192, v234, v232\n"
    "v_mfma_f32_16x16x32_bf16 v[176:179], a[152:155], a[240:243], v[176:179]\n"
    "ds_read_u16 v189, v189\n"
    "v_pk_fma_f16 v232, v196, v196, s48\n"
    "v_mfma_f32_16x16x32_bf16 v[176:179], a[64:67], a[244:247], v[176:179]\n"
    "s_waitcnt lgkmcnt(7)\n"
    "v_pk_mul_f16 v232, v196, v232\n"
    "v_or_b32_e32 v184, v184, v200\n"
    "v_mfma_f32_16x16x32_bf16 v[180:183], a[108:111], a[236:239], 0\n"
    "ds_read_u16_d16_hi v205, v205\n"
    "v_exp_f16_e64 v234, v232\n"
    "v_mfma_f32_16x16x32_bf16 v[180:183], a[156:159], a[240:243], v[180:183]\n"
    "v_exp_f16_sdwa v234, v232 dst_sel:WORD_1 dst_unused:UNUSED_PRESERVE src0_sel:WORD_1\n"
    "v_mfma_f32_16x16x32_bf16 v[180:183], a[68:71], a[244:247], v[180:183]\n"
    "ds_read_u16 v190, v190\n"
    "v_pk_fma_f16 v234, v234, s49, s49\n"
    "v_mfma_f32_16x16x32_bf16 v[96:99], a[16:19], v[192:195], v[96:99]\n"
    "s_waitcnt lgkmcnt(7)\n"
    "ds_read_u16_d16_hi v206, v206\n"
    "v_rcp_f16_e32 v232, v234\n"
    "v_mfma_f32_16x16x32_bf16 v[100:103], a[20:23], v[192:195], v[100:103]\n"
    "v_rcp_f16_sdwa v232, v234 dst_sel:WORD_1 dst_unused:UNUSED_PRESERVE src0_sel:WORD_1\n"
    "v_mfma_f32_16x16x32_bf16 v[112:115], a[24:27], v[192:195], v[112:115]\n"
    "ds_read_u16 v191, v191\n"
    "v_fma_mix_f32 v234, v196, v232, 0 op_sel_hi:[1,1,0]\n"
    "v_mfma_f32_16x16x32_bf16 v[116:119], a[28:31], v[192:195], v[116:119]\n"
    "v_fma_mix_f32 v232, v196, v232, 0 op_sel:[1,1,0] op_sel_hi:[1,1,0]\n"
    "v_or_b32_e32 v185, v185, v201\n"
    "v_mfma_f32_16x16x32_bf16 v[128:131], v[8:11], v[192:195], v[128:131]\n"
    "s_waitcnt lgkmcnt(7)\n"
    "ds_read_u16_d16_hi v207, v207\n"
    "v_cvt_pk_bf16_f32 v196, v234, v232\n"
    "v_mfma_f32_16x16x32_bf16 v[132:135], v[12:15], v[192:195], v[132:135]\n"
    "v_cvt_pk_f16_f32 v192, v168, v169\n"
    "v_lshlrev_b32_sdwa v193, 1, v170 dst_sel:DWORD dst_unused:UNUSED_PAD src0_sel:DWORD src1_sel:WORD_1\n"
    "v_mfma_f32_16x16x32_bf16 v[104:107], a[16:19], v[196:199], v[104:107]\n"
    "ds_read_u16 v193, v193\n"
    "v_lshlrev_b32_sdwa v201, 1, v171 dst_sel:DWORD dst_unused:UNUSED_PAD src0_sel:DWORD src1_sel:WORD_1\n"
    "v_mfma_f32_16x16x32_bf16 v[108:111], a[20:23], v[196:199], v[108:111]\n"
    "s_waitcnt vmcnt(7) lgkmcnt(7)\n"
    "ds_read_u16_d16_hi v201, v201\n"
    "v_lshlrev_b32_sdwa v194, 1, v172 dst_sel:DWORD dst_unused:UNUSED_PAD src0_sel:DWORD src1_sel:WORD_1\n"
    "v_mfma_f32_16x16x32_bf16 v[120:123], a[24:27], v[196:199], v[120:123]\n"
    "v_or_b32_e32 v186, v186, v202\n"
    "v_lshlrev_b32_sdwa v202, 1, v173 dst_sel:DWORD dst_unused:UNUSED_PAD src0_sel:DWORD src1_sel:WORD_1\n"
    "v_mfma_f32_16x16x32_bf16 v[124:127], a[28:31], v[196:199], v[124:127]\n"
    "ds_read_u16 v194, v194\n"
    "v_lshlrev_b32_sdwa v195, 1, v174 dst_sel:DWORD dst_unused:UNUSED_PAD src0_sel:DWORD src1_sel:WORD_1\n"
    "v_mfma_f32_16x16x32_bf16 v[136:139], v[8:11], v[196:199], v[136:139]\n"
    "v_or_b32_e32 v187, v187, v203\n"
    "v_lshlrev_b32_sdwa v203, 1, v175 dst_sel:DWORD dst_unused:UNUSED_PAD src0_sel:DWORD src1_sel:WORD_1\n"
    "v_mfma_f32_16x16x32_bf16 v[140:143], v[12:15], v[196:199], v[140:143]\n"
    "s_waitcnt vmcnt(6) lgkmcnt(7)\n"
    "ds_read_u16_d16_hi v202, v202\n"
    "v_cvt_pk_f16_f32 v196, v176, v177\n"
    "v_smfmac_f32_32x32x32_bf16 v[112:127], a[248:251], v[152:159], v241\n"
    "ds_read_u16 v195, v195\n"
    "v_lshlrev_b32_sdwa v197, 1, v178 dst_sel:DWORD dst_unused:UNUSED_PAD src0_sel:DWORD src1_sel:WORD_1\n"
    "v_lshlrev_b32_sdwa v198, 1, v180 dst_sel:DWORD dst_unused:UNUSED_PAD src0_sel:DWORD src1_sel:WORD_1\n"
    "v_lshlrev_b32_sdwa v199, 1, v182 dst_sel:DWORD dst_unused:UNUSED_PAD src0_sel:DWORD src1_sel:WORD_1\n"
    "v_or_b32_e32 v188, v188, v204\n"
    "v_smfmac_f32_32x32x32_bf16 v[96:111], a[248:251], v[144:151], v241\n"
    "s_waitcnt lgkmcnt(7)\n"
    "ds_read_b128 v[152:155], v242 offset:0\n"
    "v_or_b32_e64 v189, v189, v205\n"
    "v_lshlrev_b32_sdwa v205, 1, v179 dst_sel:DWORD dst_unused:UNUSED_PAD src0_sel:DWORD src1_sel:WORD_1\n"
    "v_or_b32_e64 v190, v190, v206\n"
    "v_lshlrev_b32_sdwa v206, 1, v181 dst_sel:DWORD dst_unused:UNUSED_PAD src0_sel:DWORD src1_sel:WORD_1\n"
    "v_smfmac_f32_32x32x32_bf16 v[128:143], a[248:251], v[208:215], v241\n"
    "ds_read_b128 v[156:159], v242 offset:1024\n"
    "ds_read_u16_d16_hi v203, v203\n"
    "v_pk_fma_f16 v232, v192, v192, s48\n"
    "v_pk_mul_f16 v232, v192, v232\n"
    "v_exp_f16_e32 v234, v232\n"
    "v_mfma_f32_16x16x32_bf16 v[168:171], a[112:115], a[224:227], 0\n"
    "s_waitcnt lgkmcnt(8)\n"
    "ds_read_u16 v197, v197\n"
    "v_or_b32_e64 v191, v191, v207\n"
    "v_mfma_f32_16x16x32_bf16 v[168:171], a[160:163], a[228:231], v[168:171]\n"
    "ds_read_u16_d16_hi v205, v205\n"
    "v_lshlrev_b32_sdwa v207, 1, v183 dst_sel:DWORD dst_unused:UNUSED_PAD src0_sel:DWORD src1_sel:WORD_1\n"
    "v_mfma_f32_16x16x32_bf16 v[168:171], a[72:75], a[232:235], v[168:171]\n"
    "s_nop 0\n"
    "v_exp_f16_sdwa v234, v232 dst_sel:WORD_1 dst_unused:UNUSED_PRESERVE src0_sel:WORD_1\n"
    "v_mfma_f32_16x16x32_bf16 v[172:175], a[116:119], a[224:227], 0\n"
    "s_waitcnt lgkmcnt(8)\n"
    "ds_read_u16 v198, v198\n"
    "v_or_b32_e64 v193, v193, v201\n"
    "v_mfma_f32_16x16x32_bf16 v[172:175], a[164:167], a[228:231], v[172:175]\n"
    "v_pk_fma_f16 v234, v234, s49, s49\n"
    "v_rcp_f16_e64 v232, v234\n"
    "v_mfma_f32_16x16x32_bf16 v[172:175], v[52:55], a[232:235], v[172:175]\n"
    "ds_read_u16_d16_hi v206, v206\n"
    "v_lshlrev_b32_sdwa v200, 1, v169 dst_sel:DWORD dst_unused:UNUSED_PAD src0_sel:DWORD src1_sel:WORD_1\n"
    "v_mfma_f32_16x16x32_bf16 v[176:179], a[112:115], a[236:239], 0\n"
    "s_waitcnt lgkmcnt(8)\n"
    "ds_read_u16 v199, v199\n"
    "v_or_b32_e32 v194, v194, v202\n"
    "v_mfma_f32_16x16x32_bf16 v[176:179], a[160:163], a[240:243], v[176:179]\n"
    "v_lshlrev_b32_sdwa v201, 1, v171 dst_sel:DWORD dst_unused:UNUSED_PAD src0_sel:DWORD src1_sel:WORD_1\n"
    "v_lshlrev_b32_sdwa v202, 1, v173 dst_sel:DWORD dst_unused:UNUSED_PAD src0_sel:DWORD src1_sel:WORD_1\n"
    "v_mfma_f32_16x16x32_bf16 v[176:179], a[72:75], a[244:247], v[176:179]\n"
    "s_nop 0\n"
    "ds_read_u16_d16_hi v207, v207\n"
    "v_rcp_f16_sdwa v232, v234 dst_sel:WORD_1 dst_unused:UNUSED_PRESERVE src0_sel:WORD_1\n"
    "v_mfma_f32_16x16x32_bf16 v[180:183], a[116:119], a[236:239], 0\n"
    "s_waitcnt lgkmcnt(7)\n"
    "v_fma_mix_f32 v234, v192, v232, 0 op_sel_hi:[1,1,0]\n"
    "v_fma_mix_f32 v232, v192, v232, 0 op_sel:[1,1,0] op_sel_hi:[1,1,0]\n"
    "v_smfmac_f32_32x32x32_bf16 v[96:111], a[248:251], v[152:159], v241\n"
    "ds_read_b128 v[152:155], v242 offset:2048\n"
    "ds_read_b128 v[156:159], v242 offset:3072\n"
    "v_lshlrev_b32_sdwa v204, 1, v177 dst_sel:DWORD dst_unused:UNUSED_PAD src0_sel:DWORD src1_sel:WORD_1\n"
    "v_cvt_pk_bf16_f32 v192, v234, v232\n"
    "v_pk_fma_f16 v232, v196, v196, s48\n"
    "v_mfma_f32_16x16x32_bf16 v[180:183], a[164:167], a[240:243], v[180:183]\n"
    "s_waitcnt lgkmcnt(8)\n"
    "ds_read_u16_d16_hi v200, v200\n"
    "v_or_b32_e32 v195, v195, v203\n"
    "v_mfma_f32_16x16x32_bf16 v[180:183], v[52:55], a[244:247], v[180:183]\n"
    "ds_read_u16_d16_hi v201, v201\n"
    "v_lshlrev_b32_sdwa v203, 1, v175 dst_sel:DWORD dst_unused:UNUSED_PAD src0_sel:DWORD src1_sel:WORD_1\n"
    "v_mfma_f32_16x16x32_bf16 v[96:99], a[32:35], v[184:187], v[96:99]\n"
    "v_pk_mul_f16 v232, v196, v232\n"
    "v_exp_f16_e32 v234, v232\n"
    "v_mfma_f32_16x16x32_bf16 v[100:103], a[36:39], v[184:187], v[100:103]\n"
    "s_waitcnt lgkmcnt(8)\n"
    "ds_read_u16_d16_hi v202, v202\n"
    "v_or_b32_e64 v197, v197, v205\n"
    "v_mfma_f32_16x16x32_bf16 v[112:115], a[40:43], v[184:187], v[112:115]\n"
    "v_lshlrev_b32_sdwa v205, 1, v179 dst_sel:DWORD dst_unused:UNUSED_PAD src0_sel:DWORD src1_sel:WORD_1\n"
    "v_exp_f16_sdwa v234, v232 dst_sel:WORD_1 dst_unused:UNUSED_PRESERVE src0_sel:WORD_1\n"
    "v_mfma_f32_16x16x32_bf16 v[116:119], a[44:47], v[184:187], v[116:119]\n"
    "ds_read_u16_d16_hi v203, v203\n"
    "v_pk_fma_f16 v234, v234, s49, s49\n"
    "v_mfma_f32_16x16x32_bf16 v[128:131], v[16:19], v[184:187], v[128:131]\n"
    "s_waitcnt lgkmcnt(8)\n"
    "ds_read_u16_d16_hi v204, v204\n"
    "v_or_b32_e32 v198, v198, v206\n"
    "v_mfma_f32_16x16x32_bf16 v[132:135], v[20:23], v[184:187], v[132:135]\n"
    "v_lshlrev_b32_sdwa v184, 1, v168 dst_sel:DWORD dst_unused:UNUSED_PAD src0_sel:DWORD src1_sel:WORD_1\n"
    "v_lshlrev_b32_sdwa v185, 1, v170 dst_sel:DWORD dst_unused:UNUSED_PAD src0_sel:DWORD src1_sel:WORD_1\n"
    "v_mfma_f32_16x16x32_bf16 v[104:107], a[32:35], v[188:191], v[104:107]\n"
    "s_nop 0\n"
    "ds_read_u16 v184, v184\n"
    "v_lshlrev_b32_sdwa v186, 1, v172 dst_sel:DWORD dst_unused:UNUSED_PAD src0_sel:DWORD src1_sel:WORD_1\n"
    "v_mfma_f32_16x16x32_bf16 v[108:111], a[36:39], v[188:191], v[108:111]\n"
    "s_waitcnt lgkmcnt(7)\n"
    "v_lshlrev_b32_sdwa v187, 1, v174 dst_sel:DWORD dst_unused:UNUSED_PAD src0_sel:DWORD src1_sel:WORD_1\n"
    "v_lshlrev_b32_sdwa v206, 1, v181 dst_sel:DWORD dst_unused:UNUSED_PAD src0_sel:DWORD src1_sel:WORD_1\n"
    "v_mfma_f32_16x16x32_bf16 v[120:123], a[40:43], v[188:191], v[120:123]\n"
    "ds_read_u16 v185, v185\n"
    "v_or_b32_e32 v199, v199, v207\n"
    "v_mfma_f32_16x16x32_bf16 v[124:127], a[44:47], v[188:191], v[124:127]\n"
    "s_waitcnt lgkmcnt(7)\n"
    "ds_read_u16 v186, v186\n"
    "v_lshlrev_b32_sdwa v207, 1, v183 dst_sel:DWORD dst_unused:UNUSED_PAD src0_sel:DWORD src1_sel:WORD_1\n"
    "v_mfma_f32_16x16x32_bf16 v[136:139], v[16:19], v[188:191], v[136:139]\n"
    "v_rcp_f16_e64 v232, v234\n"
    "v_mfma_f32_16x16x32_bf16 v[140:143], v[20:23], v[188:191], v[140:143]\n"
    "ds_read_u16 v187, v187\n"
    "v_lshlrev_b32_sdwa v188, 1, v176 dst_sel:DWORD dst_unused:UNUSED_PAD src0_sel:DWORD src1_sel:WORD_1\n"
    "v_smfmac_f32_32x32x32_bf16 v[112:127], a[248:251], v[152:159], v241\n"
    "v_lshlrev_b32_sdwa v189, 1, v178 dst_sel:DWORD dst_unused:UNUSED_PAD src0_sel:DWORD src1_sel:WORD_1\n"
    "v_lshlrev_b32_sdwa v190, 1, v180 dst_sel:DWORD dst_unused:UNUSED_PAD src0_sel:DWORD src1_sel:WORD_1\n"
    "v_lshlrev_b32_sdwa v191, 1, v182 dst_sel:DWORD dst_unused:UNUSED_PAD src0_sel:DWORD src1_sel:WORD_1\n"
    "v_rcp_f16_sdwa v232, v234 dst_sel:WORD_1 dst_unused:UNUSED_PRESERVE src0_sel:WORD_1\n"
    "v_mfma_f32_16x16x32_bf16 v[168:171], a[120:123], a[224:227], 0\n"
    "ds_read_u16 v188, v188\n"
    "ds_read_u16 v189, v189\n"
    "v_mfma_f32_16x16x32_bf16 v[168:171], a[168:171], a[228:231], v[168:171]\n"
    "v_fma_mix_f32 v234, v196, v232, 0 op_sel_hi:[1,1,0]\n"
    "v_fma_mix_f32 v232, v196, v232, 0 op_sel:[1,1,0] op_sel_hi:[1,1,0]\n"
    "v_mfma_f32_16x16x32_bf16 v[168:171], a[76:79], a[232:235], v[168:171]\n"
    "ds_read_b128 v[152:155], v242 offset:4096\n"
    "v_cvt_pk_bf16_f32 v196, v234, v232\n"
    "v_mfma_f32_16x16x32_bf16 v[172:175], a[124:127], a[224:227], 0\n"
    "ds_read_b128 v[156:159], v242 offset:5120\n"
    "v_mfma_f32_16x16x32_bf16 v[172:175], a[172:175], a[228:231], v[172:175]\n"
    "ds_read_u16_d16_hi v205, v205\n"
    "v_mfma_f32_16x16x32_bf16 v[172:175], v[60:63], a[232:235], v[172:175]\n"
    "s_nop 0\n"
    "v_mfma_f32_16x16x32_bf16 v[176:179], a[120:123], a[236:239], 0\n"
    "s_waitcnt lgkmcnt(8)\n"
    "ds_read_u16 v190, v190\n"
    "v_or_b32_e32 v184, v184, v200\n"
    "v_mfma_f32_16x16x32_bf16 v[176:179], a[168:171], a[240:243], v[176:179]\n"
    "v_mfma_f32_16x16x32_bf16 v[176:179], a[76:79], a[244:247], v[176:179]\n"
    "s_waitcnt lgkmcnt(8)\n"
    "ds_read_u16_d16_hi v206, v206\n"
    "v_or_b32_e32 v185, v185, v201\n"
    "v_mfma_f32_16x16x32_bf16 v[180:183], a[124:127], a[236:239], 0\n"
    "s_waitcnt lgkmcnt(8)\n"
    "v_lshlrev_b32_sdwa v201, 1, v171 dst_sel:DWORD dst_unused:UNUSED_PAD src0_sel:DWORD src1_sel:WORD_1\n"
    "v_or_b32_e64 v186, v186, v202\n"
    "v_mfma_f32_16x16x32_bf16 v[180:183], a[172:175], a[240:243], v[180:183]\n"
    "ds_read_u16 v191, v191\n"
    "v_lshlrev_b32_sdwa v202, 1, v173 dst_sel:DWORD dst_unused:UNUSED_PAD src0_sel:DWORD src1_sel:WORD_1\n"
    "v_mfma_f32_16x16x32_bf16 v[180:183], v[60:63], a[244:247], v[180:183]\n"
    "s_waitcnt lgkmcnt(8)\n"
    "ds_read_u16_d16_hi v207, v207\n"
    "v_or_b32_e32 v187, v187, v203\n"
    "v_mfma_f32_16x16x32_bf16 v[96:99], a[176:179], v[192:195], v[96:99]\n"
    "v_lshlrev_b32_sdwa v203, 1, v175 dst_sel:DWORD dst_unused:UNUSED_PAD src0_sel:DWORD src1_sel:WORD_1\n"
    "v_mfma_f32_16x16x32_bf16 v[100:103], a[180:183], v[192:195], v[100:103]\n"
    "s_waitcnt lgkmcnt(8)\n"
    "ds_read_u16_d16_hi v201, v201\n"
    "v_or_b32_e32 v188, v188, v204\n"
    "v_mfma_f32_16x16x32_bf16 v[112:115], a[184:187], v[192:195], v[112:115]\n"
    "v_mfma_f32_16x16x32_bf16 v[116:119], a[188:191], v[192:195], v[116:119]\n"
    "ds_read_u16_d16_hi v202, v202\n"
    "v_mfma_f32_16x16x32_bf16 v[128:131], v[24:27], v[192:195], v[128:131]\n"
    "v_mfma_f32_16x16x32_bf16 v[132:135], v[28:31], v[192:195], v[132:135]\n"
    "s_nop 0\n"
    "ds_read_u16_d16_hi v203, v203\n"
    "v_cvt_pk_f16_f32 v192, v168, v169\n"
    "v_mfma_f32_16x16x32_bf16 v[104:107], a[176:179], v[196:199], v[104:107]\n"
    "s_waitcnt lgkmcnt(7)\n"
    "v_lshlrev_b32_sdwa v193, 1, v170 dst_sel:DWORD dst_unused:UNUSED_PAD src0_sel:DWORD src1_sel:WORD_1\n"
    "v_lshlrev_b32_sdwa v194, 1, v172 dst_sel:DWORD dst_unused:UNUSED_PAD src0_sel:DWORD src1_sel:WORD_1\n"
    "v_mfma_f32_16x16x32_bf16 v[108:111], a[180:183], v[196:199], v[108:111]\n"
    "ds_read_u16 v193, v193\n"
    "v_lshlrev_b32_sdwa v195, 1, v174 dst_sel:DWORD dst_unused:UNUSED_PAD src0_sel:DWORD src1_sel:WORD_1\n"
    "v_mfma_f32_16x16x32_bf16 v[120:123], a[184:187], v[196:199], v[120:123]\n"
    "ds_read_u16 v194, v194\n"
    "v_or_b32_e64 v189, v189, v205\n"
    "v_mfma_f32_16x16x32_bf16 v[124:127], a[188:191], v[196:199], v[124:127]\n"
    "v_lshlrev_b32_sdwa v205, 1, v179 dst_sel:DWORD dst_unused:UNUSED_PAD src0_sel:DWORD src1_sel:WORD_1\n"
    "v_pk_fma_f16 v232, v192, v192, s48\n"
    "v_mfma_f32_16x16x32_bf16 v[136:139], v[24:27], v[196:199], v[136:139]\n"
    "s_waitcnt lgkmcnt(7)\n"
    "ds_read_u16 v195, v195\n"
    "v_or_b32_e32 v190, v190, v206\n"
    "v_mfma_f32_16x16x32_bf16 v[140:143], v[28:31], v[196:199], v[140:143]\n"
    "v_cvt_pk_f16_f32 v196, v176, v177\n"
    "v_lshlrev_b32_sdwa v197, 1, v178 dst_sel:DWORD dst_unused:UNUSED_PAD src0_sel:DWORD src1_sel:WORD_1\n"
    "v_mfma_f32_16x16x32_bf16 v[96:99], a[192:195], v[184:187], v[96:99]\n"
    "s_nop 0\n"
    "ds_read_u16 v197, v197\n"
    "v_lshlrev_b32_sdwa v198, 1, v180 dst_sel:DWORD dst_unused:UNUSED_PAD src0_sel:DWORD src1_sel:WORD_1\n"
    "v_mfma_f32_16x16x32_bf16 v[100:103], a[196:199], v[184:187], v[100:103]\n"
    "s_waitcnt lgkmcnt(7)\n"
    "ds_read_u16_d16_hi v205, v205\n"
    "v_lshlrev_b32_sdwa v206, 1, v181 dst_sel:DWORD dst_unused:UNUSED_PAD src0_sel:DWORD src1_sel:WORD_1\n"
    "v_smfmac_f32_32x32x32_bf16 v[128:143], a[248:251], v[152:159], v241\n"
    "v_lshlrev_b32_sdwa v199, 1, v182 dst_sel:DWORD dst_unused:UNUSED_PAD src0_sel:DWORD src1_sel:WORD_1\n"
    "v_or_b32_e64 v191, v191, v207\n"
    "v_lshlrev_b32_sdwa v207, 1, v183 dst_sel:DWORD dst_unused:UNUSED_PAD src0_sel:DWORD src1_sel:WORD_1\n"
    "v_pk_mul_f16 v232, v192, v232\n"
    "v_exp_f16_e64 v234, v232\n"
    "v_mfma_f32_16x16x32_bf16 v[112:115], a[200:203], v[184:187], v[112:115]\n"
    "ds_read_u16 v198, v198\n"
    "ds_read_u16_d16_hi v206, v206\n"
    "v_mfma_f32_16x16x32_bf16 v[116:119], a[204:207], v[184:187], v[116:119]\n"
    "v_exp_f16_sdwa v234, v232 dst_sel:WORD_1 dst_unused:UNUSED_PRESERVE src0_sel:WORD_1\n"
    "v_mfma_f32_16x16x32_bf16 v[128:131], v[32:35], v[184:187], v[128:131]\n"
    "ds_read_u16 v199, v199\n"
    "v_pk_fma_f16 v234, v234, s49, s49\n"
    "v_mfma_f32_16x16x32_bf16 v[132:135], v[36:39], v[184:187], v[132:135]\n"
    "v_rcp_f16_e32 v232, v234\n"
    "v_mfma_f32_16x16x32_bf16 v[104:107], a[192:195], v[188:191], v[104:107]\n"
    "s_waitcnt lgkmcnt(7)\n"
    "ds_read_u16_d16_hi v207, v207\n"
    "v_or_b32_e64 v193, v193, v201\n"
    "v_mfma_f32_16x16x32_bf16 v[108:111], a[196:199], v[188:191], v[108:111]\n"
    "v_rcp_f16_sdwa v232, v234 dst_sel:WORD_1 dst_unused:UNUSED_PRESERVE src0_sel:WORD_1\n"
    "v_mfma_f32_16x16x32_bf16 v[120:123], a[200:203], v[188:191], v[120:123]\n"
    "s_waitcnt lgkmcnt(7)\n"
    "v_or_b32_e32 v194, v194, v202\n"
    "v_fma_mix_f32 v234, v192, v232, 0 op_sel_hi:[1,1,0]\n"
    "v_mfma_f32_16x16x32_bf16 v[124:127], a[204:207], v[188:191], v[124:127]\n"
    "v_fma_mix_f32 v232, v192, v232, 0 op_sel:[1,1,0] op_sel_hi:[1,1,0]\n"
    "v_cvt_pk_bf16_f32 v192, v234, v232\n"
    "v_mfma_f32_16x16x32_bf16 v[136:139], v[32:35], v[188:191], v[136:139]\n"
    "s_waitcnt lgkmcnt(6)\n"
    "v_or_b32_e32 v195, v195, v203\n"
    "v_pk_fma_f16 v232, v196, v196, s48\n"
    "v_mfma_f32_16x16x32_bf16 v[140:143], v[36:39], v[188:191], v[140:143]\n"
    "v_pk_mul_f16 v232, v196, v232\n"
    "v_exp_f16_e64 v234, v232\n"
    "v_mfma_f32_16x16x32_bf16 v[96:99], a[208:211], v[192:195], v[96:99]\n"
    "v_exp_f16_sdwa v234, v232 dst_sel:WORD_1 dst_unused:UNUSED_PRESERVE src0_sel:WORD_1\n"
    "s_waitcnt lgkmcnt(4)\n"
    "v_or_b32_e32 v197, v197, v205\n"
    "v_pk_fma_f16 v234, v234, s49, s49\n"
    "v_mfma_f32_16x16x32_bf16 v[100:103], a[212:215], v[192:195], v[100:103]\n"
    "v_rcp_f16_e64 v232, v234\n"
    "v_mfma_f32_16x16x32_bf16 v[112:115], a[216:219], v[192:195], v[112:115]\n"
    "v_rcp_f16_sdwa v232, v234 dst_sel:WORD_1 dst_unused:UNUSED_PRESERVE src0_sel:WORD_1\n"
    "v_mfma_f32_16x16x32_bf16 v[116:119], a[220:223], v[192:195], v[116:119]\n"
    "v_fma_mix_f32 v234, v196, v232, 0 op_sel_hi:[1,1,0]\n"
    "v_fma_mix_f32 v232, v196, v232, 0 op_sel:[1,1,0] op_sel_hi:[1,1,0]\n"
    "v_mfma_f32_16x16x32_bf16 v[128:131], v[40:43], v[192:195], v[128:131]\n"
    "s_waitcnt lgkmcnt(2)\n"
    "v_or_b32_e32 v198, v198, v206\n"
    "v_cvt_pk_bf16_f32 v196, v234, v232\n"
    "v_mfma_f32_16x16x32_bf16 v[132:135], v[44:47], v[192:195], v[132:135]\n"
    "v_cvt_pk_bf16_f32 v96, v96, v97\n"
    "v_cvt_pk_bf16_f32 v97, v98, v99\n"
    "v_cvt_pk_bf16_f32 v98, v100, v101\n"
    "v_cvt_pk_bf16_f32 v99, v102, v103\n"
    "buffer_store_dwordx4 v[96:99], v245, s[44:47], s58 offen offset:0 sc1\n"
    "s_waitcnt lgkmcnt(0)\n"
    "v_or_b32_e32 v199, v199, v207\n"
    "v_cvt_pk_bf16_f32 v112, v112, v113\n"
    "v_cvt_pk_bf16_f32 v113, v114, v115\n"
    "v_mfma_f32_16x16x32_bf16 v[104:107], a[208:211], v[196:199], v[104:107]\n"
    "v_cvt_pk_bf16_f32 v114, v116, v117\n"
    "v_cvt_pk_bf16_f32 v115, v118, v119\n"
    "v_mfma_f32_16x16x32_bf16 v[108:111], a[212:215], v[196:199], v[108:111]\n"
    "buffer_store_dwordx4 v[112:115], v245, s[44:47], s58 offen offset:64 sc1\n"
    "v_mfma_f32_16x16x32_bf16 v[120:123], a[216:219], v[196:199], v[120:123]\n"
    "v_cvt_pk_bf16_f32 v128, v128, v129\n"
    "v_cvt_pk_bf16_f32 v129, v130, v131\n"
    "v_mfma_f32_16x16x32_bf16 v[124:127], a[220:223], v[196:199], v[124:127]\n"
    "v_cvt_pk_bf16_f32 v130, v132, v133\n"
    "v_cvt_pk_bf16_f32 v131, v134, v135\n"
    "v_mfma_f32_16x16x32_bf16 v[136:139], v[40:43], v[196:199], v[136:139]\n"
    "buffer_store_dwordx4 v[128:131], v245, s[44:47], s58 offen offset:128 sc1\n"
    "v_mfma_f32_16x16x32_bf16 v[140:143], v[44:47], v[196:199], v[140:143]\n"
    "v_cvt_pk_bf16_f32 v104, v104, v105\n"
    "v_cvt_pk_bf16_f32 v105, v106, v107\n"
    "v_cvt_pk_bf16_f32 v106, v108, v109\n"
    "v_cvt_pk_bf16_f32 v107, v110, v111\n"
    "buffer_store_dwordx4 v[104:107], v245, s[44:47], s89 offen offset:0 sc1\n"
    "v_cvt_pk_bf16_f32 v120, v120, v121\n"
    "v_cvt_pk_bf16_f32 v121, v122, v123\n"
    "v_cvt_pk_bf16_f32 v122, v124, v125\n"
    "v_cvt_pk_bf16_f32 v123, v126, v127\n"
    "buffer_store_dwordx4 v[120:123], v245, s[44:47], s89 offen offset:64 sc1\n"
    "v_cvt_pk_bf16_f32 v136, v136, v137\n"
    "v_cvt_pk_bf16_f32 v137, v138, v139\n"
    "v_cvt_pk_bf16_f32 v138, v140, v141\n"
    "v_cvt_pk_bf16_f32 v139, v142, v143\n"
    "buffer_store_dwordx4 v[136:139], v245, s[44:47], s89 offen offset:128 sc1\n"
    "97:\n"
    "98:\n"
    "s_waitcnt vmcnt(0) lgkmcnt(0)\n"
    ".Lreplk_end:\n"
    : "+{s66}"(flg), "+{v208}"(tid), "+{s65}"(blk), "+{s[98:99]}"(ka)
    : "{s[96:97]}"(CTAB)
    : "memory", "scc", "vcc", "m0", "v0", "v1", "v2", "v3", "v4", "v5", "v6", "v7", "v8", "v9", "v10", "v11", "v12", "v13", "v14", "v15", "v16", "v17", "v18", "v19", "v20", "v21", "v22", "v23", "v24", "v25", "v26", "v27", "v28", "v29", "v30", "v31", "v32", "v33", "v34", "v35", "v36", "v37", "v38", "v39", "v40", "v41", "v42", "v43", "v44", "v45", "v46", "v47", "v48", "v49", "v50", "v51", "v52", "v53", "v54", "v55", "v56", "v57", "v58", "v59", "v60", "v61", "v62", "v63", "v64", "v65", "v66", "v67", "v68", "v69", "v70", "v71", "v72", "v73", "v74", "v75", "v76", "v77", "v78", "v79", "v80", "v81", "v82", "v83", "v84", "v85", "v86", "v87", "v88", "v89", "v90", "v91", "v92", "v93", "v94", "v95", "v96", "v97", "v98", "v99", "v100", "v101", "v102", "v103", "v104", "v105", "v106", "v107", "v108", "v109", "v110", "v111", "v112", "v113", "v114", "v115", "v116", "v117", "v118", "v119", "v120", "v121", "v122", "v123", "v124", "v125", "v126", "v127", "v128", "v129", "v130", "v131", "v132", "v133", "v134", "v135", "v136", "v137", "v138", "v139", "v140", "v141", "v142", "v143", "v144", "v145", "v146", "v147", "v148", "v149", "v150", "v151", "v152", "v153", "v154", "v155", "v156", "v157", "v158", "v159", "v160", "v161", "v162", "v163", "v164", "v165", "v166", "v167", "v168", "v169", "v170", "v171", "v172", "v173", "v174", "v175", "v176", "v177", "v178", "v179", "v180", "v181", "v182", "v183", "v184", "v185", "v186", "v187", "v188", "v189", "v190", "v191", "v192", "v193", "v194", "v195", "v196", "v197", "v198", "v199", "v200", "v201", "v202", "v203", "v204", "v205", "v206", "v207", "v209", "v210", "v211", "v212", "v213", "v214", "v215", "v216", "v217", "v218", "v219", "v220", "v221", "v222", "v223", "v224", "v225", "v226", "v227", "v228", "v229", "v230", "v231", "v232", "v233", "v234", "v235", "v236", "v237", "v238", "v239", "v240", "v241", "v242", "v243", "v244", "v245", "v246", "v247", "v248", "v249", "v250", "v251", "v252", "v253", "v254", "v255", "a0", "a1", "a2", "a3", "a4", "a5", "a6", "a7", "a8", "a9", "a10", "a11", "a12", "a13", "a14", "a15", "a16", "a17", "a18", "a19", "a20", "a21", "a22", "a23", "a24", "a25", "a26", "a27", "a28", "a29", "a30", "a31", "a32", "a33", "a34", "a35", "a36", "a37", "a38", "a39", "a40", "a41", "a42", "a43", "a44", "a45", "a46", "a47", "a48", "a49", "a50", "a51", "a52", "a53", "a54", "a55", "a56", "a57", "a58", "a59", "a60", "a61", "a62", "a63", "a64", "a65", "a66", "a67", "a68", "a69", "a70", "a71", "a72", "a73", "a74", "a75", "a76", "a77", "a78", "a79", "a80", "a81", "a82", "a83", "a84", "a85", "a86", "a87", "a88", "a89", "a90", "a91", "a92", "a93", "a94", "a95", "a96", "a97", "a98", "a99", "a100", "a101", "a102", "a103", "a104", "a105", "a106", "a107", "a108", "a109", "a110", "a111", "a112", "a113", "a114", "a115", "a116", "a117", "a118", "a119", "a120", "a121", "a122", "a123", "a124", "a125", "a126", "a127", "a128", "a129", "a130", "a131", "a132", "a133", "a134", "a135", "a136", "a137", "a138", "a139", "a140", "a141", "a142", "a143", "a144", "a145", "a146", "a147", "a148", "a149", "a150", "a151", "a152", "a153", "a154", "a155", "a156", "a157", "a158", "a159", "a160", "a161", "a162", "a163", "a164", "a165", "a166", "a167", "a168", "a169", "a170", "a171", "a172", "a173", "a174", "a175", "a176", "a177", "a178", "a179", "a180", "a181", "a182", "a183", "a184", "a185", "a186", "a187", "a188", "a189", "a190", "a191", "a192", "a193", "a194", "a195", "a196", "a197", "a198", "a199", "a200", "a201", "a202", "a203", "a204", "a205", "a206", "a207", "a208", "a209", "a210", "a211", "a212", "a213", "a214", "a215", "a216", "a217", "a218", "a219", "a220", "a221", "a222", "a223", "a224", "a225", "a226", "a227", "a228", "a229", "a230", "a231", "a232", "a233", "a234", "a235", "a236", "a237", "a238", "a239", "a240", "a241", "a242", "a243", "a244", "a245", "a246", "a247", "a248", "a249", "a250", "a251", "a252", "a253", "a254", "a255", "s36", "s37", "s38", "s39", "s40", "s41", "s42", "s43", "s44", "s45", "s46", "s47", "s48", "s49", "s50", "s51", "s52", "s53", "s54", "s55", "s56", "s57", "s58", "s59", "s60", "s61", "s62", "s63", "s64", "s67", "s68", "s69", "s70", "s71", "s72", "s73", "s74", "s75", "s76", "s77", "s78", "s79", "s80", "s81", "s82", "s83", "s84", "s85", "s86", "s87", "s88", "s89", "s90", "s91", "s92", "s93", "s94", "s95");
  if (flg != 2u) {
    const int l2 = __builtin_amdgcn_mbcnt_hi(~0u, __builtin_amdgcn_mbcnt_lo(~0u, 0u));
    slow_path(x, dwl, dws, w1, w2, out, (float*)smem, w * 64 + l2, blockIdx.x >> 3, (blockIdx.x & 7) * 4);
  }
}

extern "C" void kernel_launch(void* const* d_in, const int* in_sizes, int n_in, void* d_out, int out_size, void* d_ws,
                              size_t ws_size, hipStream_t stream) {
  replk_kernel<<<256, 256, 0, stream>>>((const u16*)d_in[0], (const u16*)d_in[1], (const u16*)d_in[2], (const u16*)d_in[3],
                                        (const u16*)d_in[4], (u16*)d_out
                                        );
}
```

```cpp
#include <hip/hip_runtime.h>
#include <hip/hip_bf16.h>

typedef unsigned u32;
typedef unsigned short u16;
typedef unsigned long long u64;
typedef __bf16 bf16x2 __attribute__((ext_vector_type(2)));
typedef float f32x2 __attribute__((ext_vector_type(2)));
#define DI __device__ __forceinline__

constexpr int IMG = 512;
constexpr int CH = 96;
constexpr int HID = 384;
constexpr int PXB = 192;
constexpr int ROWB = 98304;
constexpr int TOEP_CS = 336;
constexpr int XT_RS = 320;
constexpr int XT_PP = 1360;
constexpr int XT_CG = 6752;
constexpr int GS = 6160;
constexpr int L_LUT = 0;
constexpr int L_TOEP = 8208;
constexpr int L_W = 40960;
constexpr int L_LUTN = 65536;
constexpr int L_XT = 81920;
constexpr int L_H = 108928;
constexpr int L_EX = 133568;
constexpr int XT_Q = 27024;
constexpr int L_MISC = 162992;
constexpr int L_TOTAL = 163056;
constexpr int LUTK = 106;
constexpr float GC1 = -2.302208198144325f, GC3 = -0.1029432395800235f;
__device__ const u32 CTAB[3 * 1024] = {
671109120,671098880,134490112,673187840,671372952,671229956,145500164,673318996,671636784,671361032,156510216,673450152,671900616,671492108,167520268,673581308,672164452,684206128,178530352,686295376,672428284,684337204,189540404,686426532,672692116,684468280,200550456,686557688,672955948,684599356,211560508,686688844,673219784,697313376,222570592,699402912,673483616,697444452,233580644,699534068,673747448,697575528,244590696,699665224,674011280,697706604,255600748,699796380,674254849,710420624,266610832,712510448,674518681,710551700,277620884,712641604,674782513,710682776,288630936,712772760,675046345,710813852,299640988,712903916,
684223077,723527872,310651072,721423680,684486909,723658948,321661124,721554836,683702165,723790024,332671176,721685992,683965997,723921100,343681228,721817148,685278409,736635120,354691312,734531216,685542241,736766196,365701364,734662372,684757497,736897272,376711416,734793528,685021329,737028348,387721468,734924684,686313474,749742368,398731552,747638752,686577306,749873444,409741604,747769908,685792562,750004520,420751656,747901064,686056394,750135596,431761708,748032220,687368806,762849616,442771792,760746288,687632638,762980692,453781844,760877444,686847894,763111768,464791896,761008600,687111726,763242844,475801948,761139756,
697337034,771762560,486812032,773853824,697600866,771893636,497822084,773984980,697864698,772024712,508832136,774116136,698128530,772155788,519842188,774247292,696274947,784869808,530852272,786961360,696538779,785000884,541862324,787092516,696802611,785131960,552872376,787223672,697066443,785263036,563882428,787354828,699427431,797977056,574892512,800068896,699691263,798108132,585902564,800200052,699955095,798239208,596912616,800331208,700218927,798370284,607922668,800462364,698385611,811084304,618932752,813176432,698649443,811215380,629942804,813307588,698913275,811346456,640952856,813438744,699177107,811477532,651962908,813569900,
710430724,824191552,135539264,822089664,710694556,824322628,146549316,822220820,709909812,824453704,157559368,822351976,710173644,824584780,168569420,822483132,709388904,837298800,179579504,835197200,709652736,837429876,190589556,835328356,708867992,837560952,201599608,835459512,709131824,837692028,212609660,835590668,712541388,850406048,223619744,848304736,712805220,850537124,234629796,848435892,712020476,850668200,245639848,848567048,712284308,850799276,256649900,848698204,711479301,863513296,267659984,861412272,711743133,863644372,278670036,861543428,710958389,863775448,289680088,861674584,711222221,863906524,300690140,861805740,
723544681,677394944,311704064,679484672,723808513,677526020,322714116,679615828,724072345,677657096,333724168,679746984,724336177,677788172,344734220,679878140,724600013,690502192,355744304,692592208,724863845,690633268,366754356,692723364,725127677,690764344,377764408,692854520,725391509,690895420,388774460,692985676,721440774,703609440,399784544,705699744,721704606,703740516,410794596,705830900,721968438,703871592,421804648,705962056,722232270,704002668,432814700,706093212,722496106,716716688,443824784,718807280,722759938,716847764,454834836,718938436,723023770,716978840,465844888,719069592,723287602,717109916,476854940,719200748,
736658638,725629632,487865024,731914816,736922470,725760708,498875076,732045972,736137726,725891784,509885128,732177128,736401558,726022860,520895180,732308284,737693703,738736880,531905264,745022352,737957535,738867956,542915316,745153508,737172791,738999032,553925368,745284664,737436623,739130108,564935420,745415820,734554731,751844128,575945504,758129888,734818563,751975204,586955556,758261044,734033819,752106280,597965608,758392200,734297651,752237356,608975660,758523356,735610063,764951376,619985744,771237424,735873895,765082452,630995796,771368580,735089151,765213528,642005848,771499736,735352983,765344604,653015900,771630892,
749752328,778058624,136592256,780150656,750016160,778189700,147602308,780281812,750279992,778320776,158612360,780443436,750543824,778451852,169622412,780574508,748710508,791165872,180632496,793288492,748974340,791296948,191642548,793419564,749238172,791428024,202652600,793550636,749502004,791559100,213662652,793681708,747668688,804273120,224672736,806395692,747932520,804404196,235682788,806526764,748196352,804535272,246692840,806657836,748460184,804666348,257702892,806788908,746606601,817380368,268712976,819502892,746870433,817511444,279723028,819633964,747134265,817642520,290733080,819765036,747398097,817773596,301743132,819896108,
762866285,826293312,312753216,832610092,763130117,826424388,323763268,832741164,762345373,826555464,334773320,832872236,762609205,826686540,345783372,833003308,761824465,839400560,356793456,845717292,762088297,839531636,367803508,845848364,761303553,839662712,378813560,845979436,761567385,839793788,389823612,846110508,760762378,852507808,400833696,858824492,761026210,852638884,411843748,858955564,760241466,852769960,422853800,859086636,760505298,852901036,433863852,859217708,759720558,865615056,444873936,871931692,759984390,865746132,455883988,872062764,759199646,865877208,466894040,872193836,759463478,866008284,477904092,872324908,
771785938,671098882,488908800,673226540,772049770,671229958,499918852,673357612,772313602,671361034,510928904,673488684,772577434,671492110,521938956,673619756,772821003,684206130,532949040,686333740,773084835,684337206,543959092,686464812,773348667,684468282,554969144,686595884,773612499,684599358,565979196,686726956,773876335,697313378,576989280,699440940,774140167,697444454,587999332,699572012,774403999,697575530,599009384,699703084,774667831,697706606,610019436,699834156,774931667,710420626,621029520,712548140,775195499,710551702,632039572,712679212,775459331,710682778,643049624,712810284,775723163,710813854,654059676,712941356,
784879628,723527874,137636032,721461036,785143460,723658950,148646084,721592108,784358716,723790026,159656136,721723180,784622548,723921102,170666188,721854252,785934960,736635122,181676272,734568236,786198792,736766198,192686324,734699308,785414048,736897274,203696376,734830380,785677880,737028350,214706428,734961452,786990292,749742370,225716512,747675436,787254124,749873446,236726564,747806508,786469380,750004522,247736616,747937580,786733212,750135598,258746668,748068652,788025357,762849618,269756752,760782636,788289189,762980694,280766804,760913708,787504445,763111770,291776856,761044780,787768277,763242846,302786908,761175852,
797993585,771762562,313796992,773889836,798257417,771893638,324807044,774020908,798521249,772024714,335817096,774151980,798785081,772155790,346827148,774283052,796951765,784869810,357837232,786997036,797215597,785000886,368847284,787128108,797479429,785131962,379857336,787259180,797743261,785263038,390867388,787390252,800083982,797977058,401877472,800104236,800347814,798108134,412887524,800235308,800611646,798239210,423897576,800366380,800875478,798370286,434907628,800497452,799042162,811084306,445917712,813211436,799305994,811215382,456927764,813342508,799569826,811346458,467937816,813473580,799833658,811477534,478947868,813604652,
811107542,824191554,489957952,822124332,811371374,824322630,500968004,822255404,810586630,824453706,511978056,822386476,810850462,824584782,522988108,822517548,810045455,837298802,533998192,835231532,810309287,837429878,545008244,835362604,809524543,837560954,556018296,835493676,809788375,837692030,567028348,835624748,813197939,850406050,578038432,848338732,813461771,850537126,589048484,848469804,812677027,850668202,600058536,848600876,812940859,850799278,611068588,848731948,812156119,863513298,622078672,861445932,812419951,863644374,633088724,861577004,811635207,863775450,644098776,861708076,811899039,863906526,655108828,861839148,
824201232,677394946,138689024,679517996,824465064,677526022,149699076,679649068,824728896,677657098,160709128,679780140,824992728,677788174,171719180,679911212,825256564,690502194,182729264,692625196,825520396,690633270,193739316,692756268,825784228,690764346,204749368,692887340,826048060,690895422,215759420,693018412,822117592,703609442,226769504,705732396,822381424,703740518,237779556,705863468,822645256,703871594,248789608,705994540,822909088,704002670,259799660,706125612,823152657,716716690,270809744,718839596,823416489,716847766,281819796,718970668,823680321,716978842,292829848,719101740,823944153,717109918,303839900,719232812,
837315189,725629634,314849984,731946796,837579021,725760710,325860036,732077868,836794277,725891786,336870088,732208940,837058109,726022862,347880140,732340012,838370521,738736882,358890224,745053996,838634353,738867958,369900276,745185068,837849609,738999034,380910328,745316140,838113441,739130110,391920380,745447212,835211282,751844130,402930464,758161196,835475114,751975206,413940516,758292268,834690370,752106282,424950568,758423340,834954202,752237358,435960620,758554412,836266614,764951378,446970704,771268396,836530446,765082454,457980756,771399468,835745702,765213530,468990808,771530540,836009534,765344606,480000860,771661612,
850429146,778058626,491010944,780181292,850692978,778189702,502020996,780312364,850956810,778320778,513031048,780443436,851220642,778451854,524041100,780574508,849367059,791165874,535051184,793288492,849630891,791296950,546061236,793419564,849894723,791428026,557071288,793550636,850158555,791559102,568081340,793681708,848325239,804273122,579091424,806395692,848589071,804404198,590101476,806526764,848852903,804535274,601111528,806657836,849116735,804666350,612121580,806788908,847283419,817380370,623131664,819502892,847547251,817511446,634141716,819633964,847811083,817642522,645151768,819765036,848074915,817773598,656161820,819896108,
863541411,826293314,656161856,832610092,863803555,826424390,656161860,832741164,863017123,826555466,656161864,832872236,863279267,826686542,656161868,833003308,862492835,839400562,656161904,845717292,862754979,839531638,656161908,845848364,861968547,839662714,656161912,845979436,862230691,839793790,656161916,846110508,861444259,852507810,656161952,858824492,861706403,852638886,656161956,858955564,860919971,852769962,656161960,859086636,861182115,852901038,656161964,859217708,860395683,865615058,656162000,871931692,860657827,865746134,656162004,872062764,859871395,865877210,656162008,872193836,860133539,866008286,656162012,872324908,
2151688192u,1885629024,1342187520,1342212212,2151688196u,1885891172,1452812292,1364494564,2151688200u,1886153320,1563437064,1386776836,2218797068u,1886415468,1674061836,1409059156,2165647376u,1886677616,1342253072,1342474360,2165647380u,1886939764,1452877844,1364756712,2164271128u,1887201912,1563502616,1387038984,2231379996u,1887464060,1674127388,1409321304,1082140704,1887726208,1342318624,1342998652,1082140708,1887988356,1452943396,1365281004,1082140712,1888250504,1563568168,1387563276,1149249580,1888512652,1674192940,1409845596,1108682800,1888774800,1342384176,1342736512,1108682804,1889036948,1453008948,1365018864,1107306552,1889299096,1563633720,1387301136,1174415420,1889561244,1674258492,1409583456,
268445760,1892969168,1342449728,1342474372,268445764,1893231316,1453074500,1364756692,268445768,1893493464,1563699272,1387038964,335554636,1893755612,1674324044,1409321316,269822032,1891920576,1342515280,1342736520,269822036,1892182724,1453140052,1365018840,268445784,1892444872,1563764824,1387301112,335554652,1892707020,1674389596,1409583464,1342187616,1890871984,1342580832,1343260812,1342187620,1891134132,1453205604,1365543132,1342187624,1891396280,1563830376,1387825404,1409296492,1891658428,1674455148,1410107756,1343563888,1889823392,1342646384,1342998672,1343563892,1890085540,1453271156,1365280992,1342187640,1890347688,1563895928,1387563264,1409296508,1890609836,1674520700,1409845616,
536881280,1895066352,1342711936,1364494484,536881284,1895328500,1453336708,1386776772,536881288,1895590648,1563961480,1409059108,603990156,1895852796,1674586252,1431341364,538257552,1894017760,1342777488,1364756632,538257556,1894279908,1453402260,1387038920,536881304,1894542056,1564027032,1409321256,603990172,1894804204,1674651804,1431603512,1610623136,1897163536,1342843040,1365280924,1610623140,1897425684,1453467812,1387563212,1610623144,1897687832,1564092584,1409845548,1677732012,1897949980,1674717356,1432127804,1611999408,1896114944,1342908592,1365018784,1611999412,1896377092,1453533364,1387301072,1610623160,1896639240,1564158136,1409583408,1677732028,1896901388,1674782908,1431865664,
809511104,1900309312,1342974144,1364756644,809511108,1900571460,1453598916,1387038900,809511112,1900833608,1564223688,1409321236,876619980,1901095756,1674848460,1431603524,823470288,1901357904,1343039696,1365018792,823470292,1901620052,1453664468,1387301048,822094040,1901882200,1564289240,1409583384,889202908,1902144348,1674914012,1431865672,1887447264,1898212128,1343105248,1365543084,1887447268,1898474276,1453730020,1387825340,1887447272,1898736424,1564354792,1410107676,1954556140,1898998572,1674979564,1432389964,1913989360,1899260720,1343170800,1365280944,1913989364,1899522868,1453795572,1387563200,1912613112,1899785016,1564420344,1409845536,1979721980,1900047164,1675045116,1432127824,
2151688448u,1784705124,1347430656,1452835440,2151688452u,1784967272,1458055428,1475117792,2151688456u,1785229420,1568680200,1497400064,2218797324u,1785491568,1679304972,1519682384,2165647632u,1785753716,1347496208,1453097588,2165647636u,1786015864,1458120980,1475379940,2164271384u,1786278012,1568745752,1497662212,2231380252u,1786540160,1679370524,1519944532,1082140960,1786802308,1347561760,1453621880,1082140964,1787064456,1458186532,1475904232,1082140968,1787326604,1568811304,1498186504,1149249836,1787588752,1679436076,1520468824,1108683056,1787850900,1347627312,1453359740,1108683060,1788113048,1458252084,1475642092,1107306808,1788375196,1568876856,1497924364,1174415676,1788637344,1679501628,1520206684,
268446016,1792045268,1347692864,1453097600,268446020,1792307416,1458317636,1475379920,268446024,1792569564,1568942408,1497662192,335554892,1792831712,1679567180,1519944544,269822288,1790996676,1347758416,1453359748,269822292,1791258824,1458383188,1475642068,268446040,1791520972,1569007960,1497924340,335554908,1791783120,1679632732,1520206692,1342187872,1789948084,1347823968,1453884040,1342187876,1790210232,1458448740,1476166360,1342187880,1790472380,1569073512,1498448632,1409296748,1790734528,1679698284,1520730984,1343564144,1788899492,1347889520,1453621900,1343564148,1789161640,1458514292,1475904220,1342187896,1789423788,1569139064,1498186492,1409296764,1789685936,1679763836,1520468844,
536881536,1794142452,1347955072,1475117712,536881540,1794404600,1458579844,1497400000,536881544,1794666748,1569204616,1519682336,603990412,1794928896,1679829388,1541964592,538257808,1793093860,1348020624,1475379860,538257812,1793356008,1458645396,1497662148,536881560,1793618156,1569270168,1519944484,603990428,1793880304,1679894940,1542226740,1610623392,1796239636,1348086176,1475904152,1610623396,1796501784,1458710948,1498186440,1610623400,1796763932,1569335720,1520468776,1677732268,1797026080,1679960492,1542751032,1611999664,1795191044,1348151728,1475642012,1611999668,1795453192,1458776500,1497924300,1610623416,1795715340,1569401272,1520206636,1677732284,1795977488,1680026044,1542488892,
809511360,1799385412,1348217280,1475379872,809511364,1799647560,1458842052,1497662128,809511368,1799909708,1569466824,1519944464,876620236,1800171856,1680091596,1542226752,823470544,1800434004,1348282832,1475642020,823470548,1800696152,1458907604,1497924276,822094296,1800958300,1569532376,1520206612,889203164,1801220448,1680157148,1542488900,1887447520,1797288228,1348348384,1476166312,1887447524,1797550376,1458973156,1498448568,1887447528,1797812524,1569597928,1520730904,1954556396,1798074672,1680222700,1543013192,1913989616,1798336820,1348413936,1475904172,1913989620,1798598968,1459038708,1498186428,1912613368,1798861116,1569663480,1520468764,1979722236,1799123264,1680288252,1542751052,
2151688704u,2087482984,1352673280,1563464828,2151688708u,2087745132,1463298052,1585747180,2151688712u,2088007280,1573922824,1608029452,2218797580u,2088269428,1684547596,1630311772,2165647888u,2088531576,1352738832,1563726976,2165647892u,2088793724,1463363604,1586009328,2164271640u,2089055872,1573988376,1608291600,2231380508u,2089318020,1684613148,1630573920,1082141216,2089580168,1352804384,1564251268,1082141220,2089842316,1463429156,1586533620,1082141224,2090104464,1574053928,1608815892,1149250092,2090366612,1684678700,1631098212,1108683312,2090628760,1352869936,1563989128,1108683316,2090890908,1463494708,1586271480,1107307064,2091153056,1574119480,1608553752,1174415932,2091415204,1684744252,1630836072,
268446272,2094823128,1352935488,1563726988,268446276,2095085276,1463560260,1586009308,268446280,2095347424,1574185032,1608291580,335555148,2095609572,1684809804,1630573932,269822544,2093774536,1353001040,1563989136,269822548,2094036684,1463625812,1586271456,268446296,2094298832,1574250584,1608553728,335555164,2094560980,1684875356,1630836080,1342188128,2092725944,1353066592,1564513428,1342188132,2092988092,1463691364,1586795748,1342188136,2093250240,1574316136,1609078020,1409297004,2093512388,1684940908,1631360372,1343564400,2091677352,1353132144,1564251288,1343564404,2091939500,1463756916,1586533608,1342188152,2092201648,1574381688,1608815880,1409297020,2092463796,1685006460,1631098232,
536881792,2096920312,1353197696,1585747100,536881796,2097182460,1463822468,1608029388,536881800,2097444608,1574447240,1630311724,603990668,2097706756,1685072012,1652593980,538258064,2095871720,1353263248,1586009248,538258068,2096133868,1463888020,1608291536,536881816,2096396016,1574512792,1630573872,603990684,2096658164,1685137564,1652856128,1610623648,2099017496,1353328800,1586533540,1610623652,2099279644,1463953572,1608815828,1610623656,2099541792,1574578344,1631098164,1677732524,2099803940,1685203116,1653380420,1611999920,2097968904,1353394352,1586271400,1611999924,2098231052,1464019124,1608553688,1610623672,2098493200,1574643896,1630836024,1677732540,2098755348,1685268668,1653118280,
809511616,2102163272,1353459904,1586009260,809511620,2102425420,1464084676,1608291516,809511624,2102687568,1574709448,1630573852,876620492,2102949716,1685334220,1652856140,823470800,2103211864,1353525456,1586271408,823470804,2103474012,1464150228,1608553664,822094552,2103736160,1574775000,1630836000,889203420,2103998308,1685399772,1653118288,1887447776,2100066088,1353591008,1586795700,1887447780,2100328236,1464215780,1609077956,1887447784,2100590384,1574840552,1631360292,1954556652,2100852532,1685465324,1653642580,1913989872,2101114680,1353656560,1586533560,1913989876,2101376828,1464281332,1608815816,1912613624,2101638976,1574906104,1631098152,1979722492,2101901124,1685530876,1653380440,
2151688960u,1986559084,1357916416,1674088056,2151688964u,1986821232,1468541188,1696370408,2151688968u,1987083380,1579165960,1718652680,2218797836u,1987345528,1689790732,1740935000,2165648144u,1987607676,1357981968,1674350204,2165648148u,1987869824,1468606740,1696632556,2164271896u,1988131972,1579231512,1718914828,2231380764u,1988394120,1689856284,1741197148,1082141472,1988656268,1358047520,1674874496,1082141476,1988918416,1468672292,1697156848,1082141480,1989180564,1579297064,1719439120,1149250348,1989442712,1689921836,1741721440,1108683568,1989704860,1358113072,1674612356,1108683572,1989967008,1468737844,1696894708,1107307320,1990229156,1579362616,1719176980,1174416188,1990491304,1689987388,1741459300,
268446528,1993899228,1358178624,1674350216,268446532,1994161376,1468803396,1696632536,268446536,1994423524,1579428168,1718914808,335555404,1994685672,1690052940,1741197160,269822800,1992850636,1358244176,1674612364,269822804,1993112784,1468868948,1696894684,268446552,1993374932,1579493720,1719176956,335555420,1993637080,1690118492,1741459308,1342188384,1991802044,1358309728,1675136656,1342188388,1992064192,1468934500,1697418976,1342188392,1992326340,1579559272,1719701248,1409297260,1992588488,1690184044,1741983600,1343564656,1990753452,1358375280,1674874516,1343564660,1991015600,1469000052,1697156836,1342188408,1991277748,1579624824,1719439108,1409297276,1991539896,1690249596,1741721460,
536882048,1995996412,1358440832,1696370328,536882052,1996258560,1469065604,1718652616,536882056,1996520708,1579690376,1740934952,603990924,1996782856,1690315148,1763217208,538258320,1994947820,1358506384,1696632476,538258324,1995209968,1469131156,1718914764,536882072,1995472116,1579755928,1741197100,603990940,1995734264,1690380700,1763479356,1610623904,1998093596,1358571936,1697156768,1610623908,1998355744,1469196708,1719439056,1610623912,1998617892,1579821480,1741721392,1677732780,1998880040,1690446252,1764003648,1612000176,1997045004,1358637488,1696894628,1612000180,1997307152,1469262260,1719176916,1610623928,1997569300,1579887032,1741459252,1677732796,1997831448,1690511804,1763741508,
809511872,2001239372,1358703040,1696632488,809511876,2001501520,1469327812,1718914744,809511880,2001763668,1579952584,1741197080,876620748,2002025816,1690577356,1763479368,823471056,2002287964,1358768592,1696894636,823471060,2002550112,1469393364,1719176892,822094808,2002812260,1580018136,1741459228,889203676,2003074408,1690642908,1763741516,1887448032,1999142188,1358834144,1697418928,1887448036,1999404336,1469458916,1719701184,1887448040,1999666484,1580083688,1741983520,1954556908,1999928632,1690708460,1764265808,1913990128,2000190780,1358899696,1697156788,1913990132,2000452928,1469524468,1719439044,1912613880,2000715076,1580149240,1741721380,1979722748,2000977224,1690774012,1764003668,
136579076,1343253088,52428,12582912,135530496,1453879396,843229,12648449,134481920,1564505704,1634030,12713986,145229824,1675132012,2411724,12779523,136841224,1343318640,3198156,13369356,135792808,1453944948,3988957,13434893,134744232,1564571256,4779758,13500430,139724968,1675197564,5557452,13565967,137103372,1343384208,6343884,14155800,136054952,1454010516,7134685,14221337,135006376,1564636824,7925486,14286874,139724968,1675263132,8703180,14352411,137365520,1343449728,9489612,14942244,136316928,1454076036,10280413,15007781,135268352,1564702344,11071214,15073318,145229824,1675328652,11848908,15138855,
139724868,1365535332,117964,12582960,139724852,1476161640,908765,12648497,138676260,1586787948,1699566,12714034,137627668,1697414256,2477260,12779571,145229896,1365600884,3263692,13369404,145229880,1476227192,4054493,13434941,138938408,1586853500,4845294,13500478,137889816,1697479808,5622988,13566015,145229900,1365666452,6409420,14155848,145229884,1476292760,7200221,14221385,139200556,1586919068,7991022,14286922,138151964,1697545376,8768716,14352459,139724880,1365731972,9555148,14942292,139724864,1476358280,10345949,15007829,139462704,1586984588,11136750,15073366,138414112,1697610896,11914444,15138903,
137627668,1387817576,183500,12583008,136579076,1498443884,974301,12648545,135530496,1609070192,1765102,12714082,134481920,1719696500,2542796,12779619,137889816,1387883128,3329228,13369452,136841224,1498509436,4120029,13434989,135792808,1609135744,4910830,13500526,134744232,1719762052,5688524,13566063,138151964,1387948696,6474956,14155896,137103372,1498575004,7265757,14221433,136054952,1609201312,8056558,14286970,135006376,1719827620,8834252,14352507,138414112,1388014216,9620684,14942340,137365520,1498640524,10411485,15007877,136316928,1609266832,11202286,15073414,135268352,1719893140,11979980,15138951,
139724968,1410099820,249036,12583056,139724868,1520726128,1039837,12648593,139724852,1631352436,1830638,12714130,138676260,1741978744,2608332,12779667,145229824,1410165372,3394764,13369500,145229896,1520791680,4185565,13435037,145229880,1631417988,4976366,13500574,138938408,1742044296,5754060,13566111,145229824,1410230940,6540492,14155944,145229900,1520857248,7331293,14221481,145229884,1631483556,8122094,14287018,139200556,1742109864,8899788,14352555,139724968,1410296460,9686220,14942388,139724880,1520922768,10477021,15007925,139724864,1631549076,11267822,15073462,139462704,1742175384,12045516,15138999,
180619940,1348496080,52428,12582912,179571360,1459122388,843229,12648449,178522784,1569748696,1634030,12713986,189270688,1680375004,2411724,12779523,180882088,1348561600,3198156,13369356,179833672,1459187908,3988957,13434893,178785096,1569814216,4779758,13500430,183765832,1680440524,5557452,13565967,181144236,1348627104,6343884,14155800,180095816,1459253412,7134685,14221337,179047240,1569879720,7925486,14286874,183765832,1680506028,8703180,14352411,181406384,1348692656,9489612,14942244,180357792,1459318964,10280413,15007781,179309216,1569945272,11071214,15073318,189270688,1680571580,11848908,15138855,
183765732,1370778324,117964,12582960,183765716,1481404632,908765,12648497,182717124,1592030940,1699566,12714034,181668532,1702657248,2477260,12779571,189270760,1370843844,3263692,13369404,189270744,1481470152,4054493,13434941,182979272,1592096460,4845294,13500478,181930680,1702722768,5622988,13566015,189270764,1370909348,6409420,14155848,189270748,1481535656,7200221,14221385,183241420,1592161964,7991022,14286922,182192828,1702788272,8768716,14352459,183765744,1370974900,9555148,14942292,183765728,1481601208,10345949,15007829,183503568,1592227516,11136750,15073366,182454976,1702853824,11914444,15138903,
181668532,1393060568,183500,12583008,180619940,1503686876,974301,12648545,179571360,1614313184,1765102,12714082,178522784,1724939492,2542796,12779619,181930680,1393126088,3329228,13369452,180882088,1503752396,4120029,13434989,179833672,1614378704,4910830,13500526,178785096,1725005012,5688524,13566063,182192828,1393191592,6474956,14155896,181144236,1503817900,7265757,14221433,180095816,1614444208,8056558,14286970,179047240,1725070516,8834252,14352507,182454976,1393257144,9620684,14942340,181406384,1503883452,10411485,15007877,180357792,1614509760,11202286,15073414,179309216,1725136068,11979980,15138951,
183765832,1415342812,249036,12583056,183765732,1525969120,1039837,12648593,183765716,1636595428,1830638,12714130,182717124,1747221736,2608332,12779667,189270688,1415408332,3394764,13369500,189270760,1526034640,4185565,13435037,189270744,1636660948,4976366,13500574,182979272,1747287256,5754060,13566111,189270688,1415473836,6540492,14155944,189270764,1526100144,7331293,14221481,189270748,1636726452,8122094,14287018,183241420,1747352760,8899788,14352555,183765832,1415539388,9686220,14942388,183765744,1526165696,10477021,15007925,183765728,1636792004,11267822,15073462,183503568,1747418312,12045516,15138999,
224660804,1353738992,52428,12582912,223612224,1464365300,843229,12648449,222563648,1574991608,1634030,12713986,233311552,1685617916,2411724,12779523,224922952,1353804512,3198156,13369356,223874536,1464430820,3988957,13434893,222825960,1575057128,4779758,13500430,227806696,1685683436,5557452,13565967,225185100,1353870080,6343884,14155800,224136680,1464496388,7134685,14221337,223088104,1575122696,7925486,14286874,227806696,1685749004,8703180,14352411,225447248,1353935632,9489612,14942244,224398656,1464561940,10280413,15007781,223350080,1575188248,11071214,15073318,233311552,1685814556,11848908,15138855,
227806596,1376021236,117964,12582960,227806580,1486647544,908765,12648497,226757988,1597273852,1699566,12714034,225709396,1707900160,2477260,12779571,233311624,1376086756,3263692,13369404,233311608,1486713064,4054493,13434941,227020136,1597339372,4845294,13500478,225971544,1707965680,5622988,13566015,233311628,1376152324,6409420,14155848,233311612,1486778632,7200221,14221385,227282284,1597404940,7991022,14286922,226233692,1708031248,8768716,14352459,227806608,1376217876,9555148,14942292,227806592,1486844184,10345949,15007829,227544432,1597470492,11136750,15073366,226495840,1708096800,11914444,15138903,
225709396,1398303480,183500,12583008,224660804,1508929788,974301,12648545,223612224,1619556096,1765102,12714082,222563648,1730182404,2542796,12779619,225971544,1398369000,3329228,13369452,224922952,1508995308,4120029,13434989,223874536,1619621616,4910830,13500526,222825960,1730247924,5688524,13566063,226233692,1398434568,6474956,14155896,225185100,1509060876,7265757,14221433,224136680,1619687184,8056558,14286970,223088104,1730313492,8834252,14352507,226495840,1398500120,9620684,14942340,225447248,1509126428,10411485,15007877,224398656,1619752736,11202286,15073414,223350080,1730379044,11979980,15138951,
227806696,1420585724,249036,12583056,227806596,1531212032,1039837,12648593,227806580,1641838340,1830638,12714130,226757988,1752464648,2608332,12779667,233311552,1420651244,3394764,13369500,233311624,1531277552,4185565,13435037,233311608,1641903860,4976366,13500574,227020136,1752530168,5754060,13566111,233311552,1420716812,6540492,14155944,233311628,1531343120,7331293,14221481,233311612,1641969428,8122094,14287018,227282284,1752595736,8899788,14352555,227806696,1420782364,9686220,14942388,227806608,1531408672,10477021,15007925,227806592,1642034980,11267822,15073462,227544432,1752661288,12045516,15138999,
268701668,1358981952,52428,12582912,267653088,1469608260,843229,12648449,266604512,1580234568,1634030,12713986,277352416,1690860876,2411724,12779523,268963816,1359047504,3198156,13369356,267915400,1469673812,3988957,13434893,266866824,1580300120,4779758,13500430,271847560,1690926428,5557452,13565967,269225964,1359113008,6343884,14155800,268177544,1469739316,7134685,14221337,267128968,1580365624,7925486,14286874,271847560,1690991932,8703180,14352411,269488112,1359178528,9489612,14942244,268439520,1469804836,10280413,15007781,267390944,1580431144,11071214,15073318,277352416,1691057452,11848908,15138855,
271847460,1381264196,117964,12582960,271847444,1491890504,908765,12648497,270798852,1602516812,1699566,12714034,269750260,1713143120,2477260,12779571,277352488,1381329748,3263692,13369404,277352472,1491956056,4054493,13434941,271061000,1602582364,4845294,13500478,270012408,1713208672,5622988,13566015,277352492,1381395252,6409420,14155848,277352476,1492021560,7200221,14221385,271323148,1602647868,7991022,14286922,270274556,1713274176,8768716,14352459,271847472,1381460772,9555148,14942292,271847456,1492087080,10345949,15007829,271585296,1602713388,11136750,15073366,270536704,1713339696,11914444,15138903,
269750260,1403546440,183500,12583008,268701668,1514172748,974301,12648545,267653088,1624799056,1765102,12714082,266604512,1735425364,2542796,12779619,270012408,1403611992,3329228,13369452,268963816,1514238300,4120029,13434989,267915400,1624864608,4910830,13500526,266866824,1735490916,5688524,13566063,270274556,1403677496,6474956,14155896,269225964,1514303804,7265757,14221433,268177544,1624930112,8056558,14286970,267128968,1735556420,8834252,14352507,270536704,1403743016,9620684,14942340,269488112,1514369324,10411485,15007877,268439520,1624995632,11202286,15073414,267390944,1735621940,11979980,15138951,
271847560,1425828684,249036,12583056,271847460,1536454992,1039837,12648593,271847444,1647081300,1830638,12714130,270798852,1757707608,2608332,12779667,277352416,1425894236,3394764,13369500,277352488,1536520544,4185565,13435037,277352472,1647146852,4976366,13500574,271061000,1757773160,5754060,13566111,277352416,1425959740,6540492,14155944,277352492,1536586048,7331293,14221481,277352476,1647212356,8122094,14287018,271323148,1757838664,8899788,14352555,271847560,1426025260,9686220,14942388,271847472,1536651568,10477021,15007925,271847456,1647277876,11267822,15073462,271585296,1757904184,12045516,15138999};

DI float bf_lo(u32 v) { return __uint_as_float(v << 16); }
DI u32 pk_bf16(float a, float b) { f32x2 t = {a, b}; return __builtin_bit_cast(u32, __builtin_convertvector(t, bf16x2)); }

DI void slow_path(const u16* __restrict__ x, const u16* __restrict__ dwl, const u16* __restrict__ dws, const u16* __restrict__ w1,
                  const u16* __restrict__ w2, u16* __restrict__ out, float* sm, int t, int tx, int ty0) {
  float* o = sm;
  float* g = sm + 8 * CH;
  float* st = g + 8 * HID;
#pragma unroll 1
  for (int b = 0; b < 128; ++b) {
    const int y = ty0 * 16 + (b >> 1), x0 = tx * 16 + (b & 1) * 8;
    __syncthreads();
#pragma unroll 1
    for (int i = t; i < 8 * CH; i += 256) {
      const int p = i / CH, c = i - p * CH;
      float acc = 0.f;
#pragma unroll 1
      for (int a = 0; a < 5; ++a)
#pragma unroll 1
        for (int e = 0; e < 5; ++e) {
          const int yy = y + a - 2, xc = x0 + p + e - 2;
          if (yy < 0 || yy >= IMG || xc < 0 || xc >= IMG) continue;
          const float xv = bf_lo(x[(size_t)(yy * IMG + xc) * CH + c]);
          acc = __builtin_fmaf(bf_lo(dws[(a * 5 + e) * CH + c]), xv, acc);
          if (a >= 1 && a <= 3 && e >= 1 && e <= 3) acc = __builtin_fmaf(bf_lo(dwl[((a - 1) * 3 + (e - 1)) * CH + c]), xv, acc);
        }
      o[i] = acc;
    }
    __syncthreads();
    if (t < 8) {
      float m = 0.f, v = 0.f;
#pragma unroll 1
      for (int c = 0; c < CH; ++c) m += o[t * CH + c];
      m *= (1.0f / CH);
#pragma unroll 1
      for (int c = 0; c < CH; ++c) { const float d = o[t * CH + c] - m; v = __builtin_fmaf(d, d, v); }
      st[2 * t] = m; st[2 * t + 1] = __builtin_amdgcn_rsqf(v * (1.0f / CH) + 1e-5f);
    }
    __syncthreads();
#pragma unroll 1
    for (int i = t; i < 8 * HID; i += 256) {
      const int p = i / HID, e = i - p * HID;
      const float m = st[2 * p], r = st[2 * p + 1];
      float u = 0.f;
#pragma unroll 1
      for (int c = 0; c < CH; ++c) u = __builtin_fmaf((o[p * CH + c] - m) * r, bf_lo(w1[c * HID + e]), u);
      const float z = u * __builtin_fmaf(u * u, GC3, GC1);
      g[i] = u * __builtin_amdgcn_rcpf(1.0f + __builtin_amdgcn_exp2f(z));
    }
    __syncthreads();
#pragma unroll 1
    for (int i = t; i < 8 * CH; i += 256) {
      const int p = i / CH, c = i - p * CH;
      float acc = 0.f;
#pragma unroll 1
      for (int e = 0; e < HID; ++e) acc = __builtin_fmaf(g[p * HID + e], bf_lo(w2[e * CH + c]), acc);
      const size_t idx = (size_t)(y * IMG + x0 + p) * CH + c;
      out[idx] = (u16)pk_bf16(bf_lo(x[idx]) + acc, 0.f);
    }
  }
}

__global__ __launch_bounds__(256, 1) void replk_kernel(const u16* __restrict__ x, const u16* __restrict__ dwl,
    const u16* __restrict__ dws, const u16* __restrict__ w1, const u16* __restrict__ w2, u16* __restrict__ out
    ) {
  __shared__ __attribute__((aligned(16))) char smem[L_TOTAL];
  __attribute__((address_space(3))) char* smem3 = (__attribute__((address_space(3))) char*)smem;
  const int w = __builtin_amdgcn_readfirstlane(threadIdx.x >> 6);
  u32 tid = threadIdx.x, blk = blockIdx.x;
  u32 flg = (u32)(size_t)smem3 ? 1u : 0u;
  u64 ka = (u64)__builtin_amdgcn_kernarg_segment_ptr();
  asm volatile(
    ".p2align 3\n"
    "s_load_dwordx2 s[40:41], s[98:99], 0x0\n"
    "s_load_dwordx2 s[88:89], s[98:99], 0x10\n"
    "s_load_dwordx2 s[92:93], s[98:99], 0x8\n"
    "s_load_dwordx2 s[84:85], s[98:99], 0x20\n"
    "s_load_dwordx2 s[80:81], s[98:99], 0x18\n"
    "s_load_dwordx2 s[44:45], s[98:99], 0x28\n"
    "v_lshlrev_b32_e32 v213, 4, v208\n"
    "v_lshrrev_b32_e32 v0, 6, v208\n"
    "s_add_u32 vcc_lo, s96, 0\n"
    "s_addc_u32 vcc_hi, s97, 0\n"
    "global_load_dwordx4 v[168:171], v213, vcc\n"
    "s_add_u32 vcc_lo, s96, 4096\n"
    "s_addc_u32 vcc_hi, s97, 0\n"
    "global_load_dwordx4 v[172:175], v213, vcc\n"
    "s_add_u32 vcc_lo, s96, 8192\n"
    "s_addc_u32 vcc_hi, s97, 0\n"
    "global_load_dwordx4 v[176:179], v213, vcc\n"
    "s_getpc_b64 s[76:77]\n"
    ".Lreplk_pc0:\n"
    "s_bfe_u32 s63, s65, 0x30003\n"
    "v_lshrrev_b32_e32 v5, 2, v208\n"
    "s_lshl_b32 s63, s63, 6\n"
    "v_add_lshl_u32 v5, s63, v5, 7\n"
    "v_min_u32_e32 v215, .Lreplk_end-.Lreplk_pc0-4, v5\n"
    "global_load_dword v233, v215, s[76:77]\n"
    "v_readfirstlane_b32 s60, v0\n"
    "v_min_u32_e32 v1, 239, v208\n"
    "v_mul_u32_u24_e32 v2, 1366, v1\n"
    "s_lshr_b32 s61, s65, 3\n"
    "s_and_b32 s62, s65, 7\n"
    "v_lshrrev_b32_e32 v2, 16, v2\n"
    "v_lshlrev_b32_e32 v3, 2, v1\n"
    "s_lshl_b32 s62, s62, 6\n"
    "s_movk_i32 s63, 768\n"
    "v_mad_u32_u24 v211, v2, s63, v3\n"
    "v_mul_u32_u24_e32 v4, 384, v2\n"
    "v_add_u32_e32 v2, -1, v2\n"
    "s_movk_i32 s90, 4800\n"
    "s_mov_b32 s91, 0x20000\n"
    "v_add_u32_e32 v3, -576, v3\n"
    "v_cmp_gt_u32_e32 vcc, 3, v2\n"
    "v_add_u32_e32 v3, v3, v4\n"
    "v_bfrev_b32_e32 v4, 1\n"
    "s_movk_i32 s94, 1728\n"
    "s_mov_b32 s95, 0x20000\n"
    "v_cndmask_b32_e32 v212, v4, v3, vcc\n"
    "s_lshl_b32 s99, s62, 5\n"
    "s_add_u32 s99, s99, s61\n"
    "s_mulk_i32 s99, 3072\n"
    "s_lshr_b32 s51, s60, 1\n"
    "s_mul_i32 s63, s51, 18432\n"
    "s_lshl_b32 s52, s60, 13\n"
    "s_add_u32 s63, s63, s52\n"
    "v_add_u32_e32 v210, s63, v213\n"
    "s_mul_i32 s38, s60, 9216\n"
    "s_add_u32 s38, s38, 40960\n"
    "s_mov_b32 s86, 73728\n"
    "s_mov_b32 s87, 0x20000\n"
    "v_and_b32_e32 v5, 63, v208\n"
    "v_min_u32_e32 v5, 8, v5\n"
    "s_and_b32 s52, s60, 1\n"
    "s_lshl_b32 s53, s61, 1\n"
    "s_add_u32 s52, s52, s53\n"
    "s_mul_i32 s52, s52, 9\n"
    "v_add_lshl_u32 v214, s52, v5, 7\n"
    "s_cmp_eq_u32 s61, 0\n"
    "s_cselect_b32 s52, 24, 0\n"
    "s_cmp_eq_u32 s61, 31\n"
    "s_cselect_b32 s53, 40, s52\n"
    "s_sub_u32 s53, 256, s53\n"
    "v_subrev_u32_e32 v6, s52, v208\n"
    "s_sub_u32 s63, s99, 384\n"
    "v_cmp_gt_u32_e32 vcc, s53, v6\n"
    "v_lshl_add_u32 v7, v1, 4, s63\n"
    "v_bfe_u32 v8, v208, 4, 2\n"
    "v_lshl_or_b32 v8, v0, 3, v8\n"
    "v_and_b32_e32 v9, 0xf0, v213\n"
    "v_cndmask_b32_e32 v208, v4, v7, vcc\n"
    "v_mul_u32_u24_e32 v8, 768, v8\n"
    "s_mov_b32 s42, 50331648\n"
    "s_mov_b32 s43, 0x20000\n"
    "v_or_b32_e32 v209, v8, v9\n"
    "s_mov_b32 s82, 73728\n"
    "s_mov_b32 s83, 0x20000\n"
    "s_cmp_eq_u32 s62, 0\n"
    "s_cselect_b32 s78, 0, s42\n"
    "s_mov_b32 s79, 0x20000\n"
    "s_waitcnt lgkmcnt(0)\n"
    "s_cmp_lt_u32 s60, 2\n"
    "s_nop 0\n"
    "s_cselect_b64 s[70:71], s[80:81], s[84:85]\n"
    "s_sub_u32 s76, s40, 196608\n"
    "s_subb_u32 s77, s41, 0\n"
    "s_mov_b32 s65, 0\n"
    "buffer_load_dwordx4 a[224:227], v208, s[76:79], s65 offen\n"
    "s_mov_b32 s65, 98304\n"
    "buffer_load_dwordx4 a[228:231], v208, s[76:79], s65 offen\n"
    "s_mov_b32 s65, 196608\n"
    "buffer_load_dwordx4 a[232:235], v208, s[40:43], s65 offen\n"
    "s_mov_b32 s65, 294912\n"
    "buffer_load_dwordx4 a[236:239], v208, s[40:43], s65 offen\n"
    "buffer_load_dword v224, v211, s[88:91], 0 offen offset:0\n"
    "buffer_load_dword v225, v211, s[88:91], 0 offen offset:192\n"
    "buffer_load_dword v226, v211, s[88:91], 0 offen offset:384\n"
    "buffer_load_dword v227, v211, s[88:91], 0 offen offset:576\n"
    "buffer_load_dword v228, v211, s[88:91], 0 offen offset:768\n"
    "v_mov_b32_e64 v88, 0\n"
    "v_mov_b32_e32 v240, 0xdddd8888\n"
    "v_mov_b32_e64 v89, 0\n"
    "v_mov_b32_e64 v200, 0\n"
    "v_mov_b32_e64 v90, 0\n"
    "v_mov_b32_e64 v91, 0\n"
    "v_mov_b32_e64 v59, 0\n"
    "v_mov_b32_e64 v60, 0\n"
    "v_mov_b32_e64 v67, 0\n"
    "v_mov_b32_e64 v68, 0\n"
    "v_mov_b32_e64 v75, 0\n"
    "v_mov_b32_e64 v76, 0\n"
    "v_mov_b32_e64 v83, 0\n"
    "v_mov_b32_e64 v84, 0\n"
    "v_mov_b32_e32 v36, 0x75089ba4\n"
    "v_mov_b32_e32 v38, 0xbdd2d3e8\n"
    "v_mov_b32_e32 v32, 0xc0135761\n"
    "v_lshlrev_b32_e64 v34, 16, v213\n"
    "v_lshlrev_b32_e64 v234, 1, v213\n"
    "v_or_b32_e32 v0, 0x8000, v34\n"
    "v_or_b32_e32 v1, 0x18000, v34\n"
    "v_or_b32_e32 v2, 0x28000, v34\n"
    "v_or_b32_e32 v3, 0x38000, v34\n"
    "v_or_b32_e32 v4, 0x48000, v34\n"
    "v_or_b32_e32 v5, 0x58000, v34\n"
    "v_or_b32_e32 v6, 0x68000, v34\n"
    "v_or_b32_e32 v7, 0x78000, v34\n"
    "v_cmp_ne_u32_e64 s[72:73], 0, v34\n"
    "s_nop 0\n"
    "s_nop 1\n"
    "v_cndmask_b32_e64 v0, 0, v0, s[72:73]\n"
    "v_pk_mul_f32 v[0:1], v[0:1], v[36:37] op_sel_hi:[1,0]\n"
    "v_pk_mul_f32 v[2:3], v[2:3], v[36:37] op_sel_hi:[1,0]\n"
    "v_pk_mul_f32 v[4:5], v[4:5], v[36:37] op_sel_hi:[1,0]\n"
    "v_pk_mul_f32 v[6:7], v[6:7], v[36:37] op_sel_hi:[1,0]\n"
    "v_pk_mul_f32 v[8:9], v[0:1], v[0:1]\n"
    "v_pk_mul_f32 v[10:11], v[2:3], v[2:3]\n"
    "v_pk_mul_f32 v[12:13], v[4:5], v[4:5]\n"
    "v_pk_mul_f32 v[14:15], v[6:7], v[6:7]\n"
    "v_pk_fma_f32 v[8:9], v[8:9], v[38:39], v[32:33] op_sel_hi:[1,0,0]\n"
    "v_pk_fma_f32 v[10:11], v[10:11], v[38:39], v[32:33] op_sel_hi:[1,0,0]\n"
    "v_pk_fma_f32 v[12:13], v[12:13], v[38:39], v[32:33] op_sel_hi:[1,0,0]\n"
    "v_pk_fma_f32 v[14:15], v[14:15], v[38:39], v[32:33] op_sel_hi:[1,0,0]\n"
    "v_pk_mul_f32 v[8:9], v[8:9], v[0:1]\n"
    "v_pk_mul_f32 v[10:11], v[10:11], v[2:3]\n"
    "v_pk_mul_f32 v[12:13], v[12:13], v[4:5]\n"
    "v_pk_mul_f32 v[14:15], v[14:15], v[6:7]\n"
    "v_exp_f32_e64 v8, v8\n"
    "v_exp_f32_e64 v9, v9\n"
    "v_exp_f32_e64 v10, v10\n"
    "v_exp_f32_e64 v11, v11\n"
    "v_exp_f32_e64 v12, v12\n"
    "v_exp_f32_e64 v13, v13\n"
    "v_exp_f32_e64 v14, v14\n"
    "v_exp_f32_e64 v15, v15\n"
    "v_pk_add_f32 v[16:17], v[8:9], 1.0 op_sel_hi:[1,0]\n"
    "v_pk_add_f32 v[18:19], v[10:11], 1.0 op_sel_hi:[1,0]\n"
    "v_pk_add_f32 v[20:21], v[12:13], 1.0 op_sel_hi:[1,0]\n"
    "v_pk_add_f32 v[22:23], v[14:15], 1.0 op_sel_hi:[1,0]\n"
    "v_rcp_f32_e64 v16, v16\n"
    "v_rcp_f32_e64 v17, v17\n"
    "v_rcp_f32_e64 v18, v18\n"
    "v_rcp_f32_e64 v19, v19\n"
    "v_rcp_f32_e64 v20, v20\n"
    "v_rcp_f32_e64 v21, v21\n"
    "v_rcp_f32_e64 v22, v22\n"
    "v_rcp_f32_e64 v23, v23\n"
    "v_pk_mul_f32 v[16:17], v[16:17], v[0:1]\n"
    "v_pk_mul_f32 v[18:19], v[18:19], v[2:3]\n"
    "v_pk_mul_f32 v[20:21], v[20:21], v[4:5]\n"
    "v_pk_mul_f32 v[22:23], v[22:23], v[6:7]\n"
    "v_pk_mul_f32 v[8:9], v[8:9], v[16:17]\n"
    "v_pk_mul_f32 v[10:11], v[10:11], v[18:19]\n"
    "v_pk_mul_f32 v[12:13], v[12:13], v[20:21]\n"
    "v_pk_mul_f32 v[14:15], v[14:15], v[22:23]\n"
    "v_cvt_pk_bf16_f32 v188, v16, v17\n"
    "v_cvt_pk_bf16_f32 v189, v18, v19\n"
    "v_cvt_pk_bf16_f32 v190, v20, v21\n"
    "v_cvt_pk_bf16_f32 v191, v22, v23\n"
    "v_cvt_pk_bf16_f32 v24, -v8, -v9\n"
    "v_cvt_pk_bf16_f32 v25, -v10, -v11\n"
    "v_cvt_pk_bf16_f32 v26, -v12, -v13\n"
    "v_cvt_pk_bf16_f32 v27, -v14, -v15\n"
    "ds_write_b128 v234, v[24:27] offset:0\n"
    "v_or_b32_e32 v0, 0x88000, v34\n"
    "v_or_b32_e32 v1, 0x98000, v34\n"
    "v_or_b32_e32 v2, 0xa8000, v34\n"
    "v_or_b32_e32 v3, 0xb8000, v34\n"
    "v_or_b32_e32 v4, 0xc8000, v34\n"
    "v_or_b32_e32 v5, 0xd8000, v34\n"
    "v_or_b32_e32 v6, 0xe8000, v34\n"
    "v_or_b32_e32 v7, 0xf8000, v34\n"
    "v_pk_mul_f32 v[0:1], v[0:1], v[36:37] op_sel_hi:[1,0]\n"
    "v_pk_mul_f32 v[2:3], v[2:3], v[36:37] op_sel_hi:[1,0]\n"
    "v_pk_mul_f32 v[4:5], v[4:5], v[36:37] op_sel_hi:[1,0]\n"
    "v_pk_mul_f32 v[6:7], v[6:7], v[36:37] op_sel_hi:[1,0]\n"
    "v_pk_mul_f32 v[8:9], v[0:1], v[0:1]\n"
    "v_pk_mul_f32 v[10:11], v[2:3], v[2:3]\n"
    "v_pk_mul_f32 v[12:13], v[4:5], v[4:5]\n"
    "v_pk_mul_f32 v[14:15], v[6:7], v[6:7]\n"
    "v_pk_fma_f32 v[8:9], v[8:9], v[38:39], v[32:33] op_sel_hi:[1,0,0]\n"
    "v_pk_fma_f32 v[10:11], v[10:11], v[38:39], v[32:33] op_sel_hi:[1,0,0]\n"
    "v_pk_fma_f32 v[12:13], v[12:13], v[38:39], v[32:33] op_sel_hi:[1,0,0]\n"
    "v_pk_fma_f32 v[14:15], v[14:15], v[38:39], v[32:33] op_sel_hi:[1,0,0]\n"
    "v_pk_mul_f32 v[8:9], v[8:9], v[0:1]\n"
    "v_pk_mul_f32 v[10:11], v[10:11], v[2:3]\n"
    "v_pk_mul_f32 v[12:13], v[12:13], v[4:5]\n"
    "v_pk_mul_f32 v[14:15], v[14:15], v[6:7]\n"
    "v_exp_f32_e64 v8, v8\n"
    "v_exp_f32_e64 v9, v9\n"
    "v_exp_f32_e64 v10, v10\n"
    "v_exp_f32_e64 v11, v11\n"
    "v_exp_f32_e64 v12, v12\n"
    "v_exp_f32_e64 v13, v13\n"
    "v_exp_f32_e64 v14, v14\n"
    "v_exp_f32_e64 v15, v15\n"
    "v_pk_add_f32 v[16:17], v[8:9], 1.0 op_sel_hi:[1,0]\n"
    "v_pk_add_f32 v[18:19], v[10:11], 1.0 op_sel_hi:[1,0]\n"
    "v_pk_add_f32 v[20:21], v[12:13], 1.0 op_sel_hi:[1,0]\n"
    "v_pk_add_f32 v[22:23], v[14:15], 1.0 op_sel_hi:[1,0]\n"
    "v_rcp_f32_e64 v16, v16\n"
    "v_rcp_f32_e64 v17, v17\n"
    "v_rcp_f32_e64 v18, v18\n"
    "v_rcp_f32_e64 v19, v19\n"
    "v_rcp_f32_e64 v20, v20\n"
    "v_rcp_f32_e64 v21, v21\n"
    "v_rcp_f32_e64 v22, v22\n"
    "v_rcp_f32_e64 v23, v23\n"
    "v_pk_mul_f32 v[16:17], v[16:17], v[0:1]\n"
    "v_pk_mul_f32 v[18:19], v[18:19], v[2:3]\n"
    "v_pk_mul_f32 v[20:21], v[20:21], v[4:5]\n"
    "v_pk_mul_f32 v[22:23], v[22:23], v[6:7]\n"
    "v_pk_mul_f32 v[8:9], v[8:9], v[16:17]\n"
    "v_pk_mul_f32 v[10:11], v[10:11], v[18:19]\n"
    "v_pk_mul_f32 v[12:13], v[12:13], v[20:21]\n"
    "v_pk_mul_f32 v[14:15], v[14:15], v[22:23]\n"
    "v_cvt_pk_bf16_f32 v192, v16, v17\n"
    "v_cvt_pk_bf16_f32 v193, v18, v19\n"
    "v_cvt_pk_bf16_f32 v194, v20, v21\n"
    "v_cvt_pk_bf16_f32 v195, v22, v23\n"
    "v_cvt_pk_bf16_f32 v24, -v8, -v9\n"
    "v_cvt_pk_bf16_f32 v25, -v10, -v11\n"
    "v_cvt_pk_bf16_f32 v26, -v12, -v13\n"
    "v_cvt_pk_bf16_f32 v27, -v14, -v15\n"
    "ds_write_b128 v234, v[24:27] offset:16\n"
    "v_add_u32_e32 v234, 65536, v234\n"
    "buffer_load_dword v229, v212, s[92:95], 0 offen offset:0\n"
    "s_nop 0\n"
    "s_waitcnt vmcnt(13)\n"
    "v_lshlrev_b32_sdwa v216, 2, v168 dst_sel:DWORD dst_unused:UNUSED_PAD src0_sel:DWORD src1_sel:WORD_0\n"
    "v_lshlrev_b32_sdwa v217, 2, v168 dst_sel:DWORD dst_unused:UNUSED_PAD src0_sel:DWORD src1_sel:WORD_1\n"
    "v_lshlrev_b32_sdwa v218, 2, v169 dst_sel:DWORD dst_unused:UNUSED_PAD src0_sel:DWORD src1_sel:WORD_0\n"
    "v_lshlrev_b32_sdwa v219, 2, v169 dst_sel:DWORD dst_unused:UNUSED_PAD src0_sel:DWORD src1_sel:WORD_1\n"
    "v_lshlrev_b32_sdwa v220, 2, v170 dst_sel:DWORD dst_unused:UNUSED_PAD src0_sel:DWORD src1_sel:WORD_0\n"
    "v_lshlrev_b32_sdwa v221, 2, v170 dst_sel:DWORD dst_unused:UNUSED_PAD src0_sel:DWORD src1_sel:WORD_1\n"
    "v_lshlrev_b32_sdwa v222, 2, v171 dst_sel:DWORD dst_unused:UNUSED_PAD src0_sel:DWORD src1_sel:WORD_0\n"
    "v_lshlrev_b32_sdwa v223, 2, v171 dst_sel:DWORD dst_unused:UNUSED_PAD src0_sel:DWORD src1_sel:WORD_1\n"
    "ds_write_b128 v222, v[88:91]\n"
    "buffer_load_dword v230, v212, s[92:95], 0 offen offset:192\n"
    "s_nop 0\n"
    "s_waitcnt vmcnt(13)\n"
    "v_lshlrev_b32_sdwa v207, 2, v172 dst_sel:DWORD dst_unused:UNUSED_PAD src0_sel:DWORD src1_sel:WORD_0\n"
    "v_lshlrev_b32_sdwa v242, 2, v173 dst_sel:DWORD dst_unused:UNUSED_PAD src0_sel:DWORD src1_sel:WORD_0\n"
    "v_lshlrev_b32_sdwa v243, 2, v173 dst_sel:DWORD dst_unused:UNUSED_PAD src0_sel:DWORD src1_sel:WORD_1\n"
    "v_lshlrev_b32_sdwa v244, 2, v174 dst_sel:DWORD dst_unused:UNUSED_PAD src0_sel:DWORD src1_sel:WORD_0\n"
    "v_lshlrev_b32_sdwa v248, 2, v174 dst_sel:DWORD dst_unused:UNUSED_PAD src0_sel:DWORD src1_sel:WORD_1\n"
    "v_lshlrev_b32_sdwa v249, 2, v175 dst_sel:DWORD dst_unused:UNUSED_PAD src0_sel:DWORD src1_sel:WORD_0\n"
    "v_lshlrev_b32_sdwa v250, 2, v175 dst_sel:DWORD dst_unused:UNUSED_PAD src0_sel:DWORD src1_sel:WORD_1\n"
    "v_bfe_i32 v201, v172, 16, 1\n"
    "v_and_b32_e32 v201, 0x35003500, v201\n"
    "v_bfe_i32 v203, v172, 18, 1\n"
    "v_and_b32_e32 v203, 0x35003500, v203\n"
    "v_bfe_i32 v205, v172, 20, 1\n"
    "v_and_b32_e32 v205, 0x35003500, v205\n"
    "v_bfe_i32 v92, v172, 26, 1\n"
    "v_xor_b32_e32 v92, 0xffff, v92\n"
    "v_and_b32_e32 v92, 0x3f803f80, v92\n"
    "v_bfe_i32 v236, v172, 22, 1\n"
    "v_and_b32_e64 v236, v92, v236\n"
    "v_bfe_i32 v237, v172, 23, 1\n"
    "v_and_b32_e32 v237, v92, v237\n"
    "buffer_load_dword v231, v212, s[92:95], 0 offen offset:384\n"
    "s_waitcnt vmcnt(13)\n"
    "v_lshlrev_b32_sdwa v251, 2, v176 dst_sel:DWORD dst_unused:UNUSED_PAD src0_sel:DWORD src1_sel:WORD_0\n"
    "v_lshlrev_b32_sdwa v252, 2, v176 dst_sel:DWORD dst_unused:UNUSED_PAD src0_sel:DWORD src1_sel:WORD_1\n"
    "v_lshlrev_b32_sdwa v253, 2, v177 dst_sel:DWORD dst_unused:UNUSED_PAD src0_sel:DWORD src1_sel:WORD_0\n"
    "v_lshlrev_b32_sdwa v254, 2, v177 dst_sel:DWORD dst_unused:UNUSED_PAD src0_sel:DWORD src1_sel:WORD_1\n"
    "v_lshlrev_b32_sdwa v241, 0, v178 dst_sel:DWORD dst_unused:UNUSED_PAD src0_sel:DWORD src1_sel:WORD_0\n"
    "v_lshlrev_b32_sdwa v245, 4, v178 dst_sel:DWORD dst_unused:UNUSED_PAD src0_sel:DWORD src1_sel:WORD_1\n"
    "v_lshlrev_b32_sdwa v255, 4, v179 dst_sel:DWORD dst_unused:UNUSED_PAD src0_sel:DWORD src1_sel:WORD_0\n"
    "v_lshlrev_b32_sdwa v247, 4, v179 dst_sel:DWORD dst_unused:UNUSED_PAD src0_sel:DWORD src1_sel:WORD_1\n"
    "v_bfe_i32 v238, v172, 24, 1\n"
    "v_and_b32_e64 v238, v92, v238\n"
    "v_bfe_i32 v239, v172, 25, 1\n"
    "v_and_b32_e64 v239, v92, v239\n"
    "v_bfe_u32 v93, v172, 28, 2\n"
    "v_mul_u32_u24_e32 v93, 393216, v93\n"
    "v_mov_b32_e64 v222, v254\n"
    "v_mov_b32_e32 v254, 0\n"
    "s_nop 1\n"
    "v_mfma_f32_32x32x1_2b_f32 v[0:31], v254, v254, 0\n"
    "global_load_dword v233, v214, s[70:71]\n"
    "s_mov_b32 s48, 0x44ea44ea\n"
    "s_mov_b32 s49, 0xb77fb77f\n"
    "s_mov_b32 s50, 0x89efde6e\n"
    "s_mov_b32 s46, s42\n"
    "s_mov_b32 s47, s43\n"
    "s_mul_i32 s54, s60, 592\n"
    "s_add_u32 s54, s54, -27008\n"
    "s_mul_i32 s55, s60, -1284\n"
    "s_add_u32 s55, s55, 54688\n"
    "s_mul_i32 s56, s60, -6160\n"
    "s_add_u32 s56, s56, -27008\n"
    "s_add_u32 s98, s99, 1375872\n"
    "s_and_b32 s36, s60, 1\n"
    "s_lshl_b32 s36, s36, 13\n"
    "s_add_u32 s36, s36, 327680\n"
    "s_mul_i32 s53, s60, 98304\n"
    "s_add_u32 s98, s53, s98\n"
    "s_xor_b32 s53, s60, 1\n"
    "s_mul_i32 s53, s53, 393216\n"
    "s_add_u32 s99, s99, s53\n"
    "s_add_u32 s53, s98, 393216\n"
    "s_sub_u32 s53, s53, s36\n"
    "s_and_b32 s37, s61, 1\n"
    "s_lshl_b32 s37, s37, 2\n"
    "s_add_u32 s37, s37, s60\n"
    "s_lshl_b32 s37, s37, 12\n"
    "s_cmp_eq_u32 s61, 0\n"
    "s_cselect_b32 s67, 2, 0\n"
    "s_cmp_eq_u32 s61, 31\n"
    "s_cselect_b32 s67, 1, s67\n"
    "s_lshl_b32 s67, s67, 30\n"
    "s_mov_b64 s[68:69], s[44:45]\n"
    "s_mov_b32 s70, 0\n"
    "s_mov_b32 s71, s47\n"
    "s_mov_b64 s[72:73], s[40:41]\n"
    "s_mov_b64 s[74:75], s[42:43]\n"
    "s_mov_b32 s57, 0\n"
    "s_mov_b32 s58, 196608\n"
    "s_mov_b32 s60, 0\n"
    "s_mov_b32 s61, 393216\n"
    "s_mov_b32 s62, 786432\n"
    "s_mov_b32 s63, 1179648\n"
    "s_mov_b32 s52, 1572864\n"
    "s_mov_b32 s88, 98304\n"
    "s_mov_b32 s89, 294912\n"
    "s_mov_b32 s39, 0\n"
    "s_getpc_b64 s[76:77]\n"
    ".Lreplk_pc1:\n"
    "s_add_u32 s76, s76, .Lreplk_last-.Lreplk_pc1\n"
    "s_addc_u32 s77, s77, 0\n"
    "s_sub_u32 s76, s76, s36\n"
    "s_subb_u32 s77, s77, 0\n"
    "s_add_u32 s78, s36, .Lreplk_end-.Lreplk_last\n"
    "s_mov_b32 s79, s37\n"
    "s_add_u32 m0, s38, 0\n"
    "s_mov_b32 s64, 0\n"
    "buffer_load_dwordx4 v210, s[84:87], s64 offen offset:0 lds\n"
    "s_waitcnt vmcnt(13)\n"
    "ds_write2_b32 v216, a224, a225 offset0:0 offset1:20\n"
    "v_add3_u32 v247, v247, v93, s98\n"
    "v_add_u32_e32 v246, s98, v255\n"
    "v_add_u32_e64 v245, s99, v245\n"
    "v_add_u32_e64 v255, s99, v255\n"
    "ds_write2_b32 v216, a226, a227 offset0:40 offset1:60\n"
    "v_and_b32_e64 v93, s67, v172\n"
    "v_and_b32_e32 v94, 0x80000000, v93\n"
    "s_waitcnt vmcnt(12)\n"
    "ds_write2_b32 v216, a228, a229 offset0:80 offset1:100\n"
    "v_and_b32_e32 v93, 0x40000000, v93\n"
    "v_or_b32_e64 v246, v246, v94\n"
    "v_or_b32_e64 v247, v247, v93\n"
    "ds_write2_b32 v216, a230, a231 offset0:120 offset1:140\n"
    "v_add_u32_e32 v216, 1360, v216\n"
    "buffer_load_dwordx4 v210, s[84:87], s64 offen offset:1024 lds\n"
    "s_waitcnt vmcnt(12)\n"
    "ds_write2_b32 v216, a232, a233 offset0:0 offset1:20\n"
    "ds_write2_b32 v216, a234, a235 offset0:40 offset1:60\n"
    "buffer_load_dwordx4 v210, s[84:87], s64 offen offset:2048 lds\n"
    "s_waitcnt vmcnt(12)\n"
    "ds_write2_b32 v216, a236, a237 offset0:80 offset1:100\n"
    "s_add_u32 m0, s38, 3072\n"
    "s_mov_b32 s64, 3072\n"
    "buffer_load_dwordx4 v210, s[84:87], s64 offen offset:0 lds\n"
    "buffer_load_dwordx4 v210, s[84:87], s64 offen offset:1024 lds\n"
    "ds_write2_b32 v216, a238, a239 offset0:120 offset1:140\n"
    "buffer_load_dwordx4 v210, s[84:87], s64 offen offset:2048 lds\n"
    "s_add_u32 m0, s38, 6144\n"
    "s_mov_b32 s64, 6144\n"
    "buffer_load_dwordx4 v210, s[84:87], s64 offen offset:0 lds\n"
    "buffer_load_dwordx4 v210, s[84:87], s64 offen offset:1024 lds\n"
    "v_add_u32_e32 v216, 1360, v216\n"
    "s_waitcnt vmcnt(16)\n"
    "v_lshlrev_b32_e32 v40, 16, v224\n"
    "buffer_load_dwordx4 v210, s[84:87], s64 offen offset:2048 lds\n"
    "s_mov_b32 s65, 589824\n"
    "buffer_load_dwordx4 a[240:243], v208, s[40:43], s65 offen\n"
    "s_waitcnt vmcnt(17)\n"
    "v_lshlrev_b32_e32 v41, 16, v225\n"
    "s_mov_b32 s65, 688128\n"
    "buffer_load_dwordx4 a[244:247], v208, s[40:43], s65 offen\n"
    "s_mov_b32 s59, 49152\n"
    "buffer_load_dwordx4 v[96:99], v209, s[80:83], s59 offen offset:0\n"
    "s_waitcnt vmcnt(18)\n"
    "v_lshlrev_b32_e32 v42, 16, v226\n"
    "s_waitcnt vmcnt(17)\n"
    "v_lshlrev_b32_e32 v43, 16, v227\n"
    "s_waitcnt vmcnt(16)\n"
    "v_lshlrev_b32_e32 v44, 16, v228\n"
    "s_waitcnt vmcnt(15)\n"
    "v_lshlrev_b32_e32 v45, 16, v229\n"
    "s_waitcnt vmcnt(14)\n"
    "v_lshlrev_b32_e32 v46, 16, v230\n"
    "s_waitcnt vmcnt(13)\n"
    "v_lshlrev_b32_e32 v47, 16, v231\n"
    "v_add_f32_e64 v41, v41, v45\n"
    "v_add_f32_e64 v42, v42, v46\n"
    "v_add_f32_e64 v43, v43, v47\n"
    "v_cvt_pk_bf16_f32 v56, v40, v41\n"
    "v_cvt_pk_bf16_f32 v57, v42, v43\n"
    "v_cvt_pk_bf16_f32 v58, v44, 0\n"
    "v_cvt_pk_bf16_f32 v64, 0, v40\n"
    "buffer_load_dwordx4 v[100:103], v209, s[80:83], s59 offen offset:256\n"
    "ds_write_b128 v221, v[56:59] offset:0\n"
    "v_cvt_pk_bf16_f32 v65, v41, v42\n"
    "v_cvt_pk_bf16_f32 v66, v43, v44\n"
    "v_cvt_pk_bf16_f32 v61, v40, v41\n"
    "v_cvt_pk_bf16_f32 v62, v42, v43\n"
    "v_cvt_pk_bf16_f32 v63, v44, 0\n"
    "v_cvt_pk_bf16_f32 v69, 0, v40\n"
    "v_cvt_pk_bf16_f32 v70, v41, v42\n"
    "v_cvt_pk_bf16_f32 v71, v43, v44\n"
    "ds_write_b128 v221, v[64:67] offset:16\n"
    "buffer_load_dwordx4 v[104:107], v209, s[80:83], s59 offen offset:512\n"
    "v_and_b32_e32 v48, 0xffff0000, v224\n"
    "v_and_b32_e32 v49, 0xffff0000, v225\n"
    "v_and_b32_e32 v50, 0xffff0000, v226\n"
    "v_and_b32_e32 v51, 0xffff0000, v227\n"
    "v_and_b32_e32 v52, 0xffff0000, v228\n"
    "ds_write_b128 v221, v[60:63] offset:32\n"
    "v_and_b32_e32 v53, 0xffff0000, v229\n"
    "v_and_b32_e32 v54, 0xffff0000, v230\n"
    "v_and_b32_e32 v55, 0xffff0000, v231\n"
    "v_add_f32_e64 v49, v49, v53\n"
    "v_add_f32_e64 v50, v50, v54\n"
    "v_add_f32_e64 v51, v51, v55\n"
    "ds_write_b128 v221, v[68:71] offset:48\n"
    "s_mov_b32 s65, 983040\n"
    "buffer_load_dwordx4 a[248:251], v208, s[40:43], s65 offen\n"
    "v_cvt_pk_bf16_f32 v72, v48, v49\n"
    "v_cvt_pk_bf16_f32 v73, v50, v51\n"
    "v_cvt_pk_bf16_f32 v74, v52, 0\n"
    "v_cvt_pk_bf16_f32 v80, 0, v48\n"
    "v_cvt_pk_bf16_f32 v81, v49, v50\n"
    "v_cvt_pk_bf16_f32 v82, v51, v52\n"
    "v_cvt_pk_bf16_f32 v77, v48, v49\n"
    "v_cvt_pk_bf16_f32 v78, v50, v51\n"
    "v_cvt_pk_bf16_f32 v79, v52, 0\n"
    "v_cvt_pk_bf16_f32 v85, 0, v48\n"
    "v_cvt_pk_bf16_f32 v86, v49, v50\n"
    "v_cvt_pk_bf16_f32 v87, v51, v52\n"
    "ds_write_b128 v221, v[72:75] offset:336\n"
    "s_mov_b32 s65, 1081344\n"
    "buffer_load_dwordx4 a[252:255], v208, s[40:43], s65 offen\n"
    "v_xor_b32_e32 v224, 0, v219\n"
    "v_xor_b32_e32 v225, 32, v219\n"
    "v_xor_b32_e32 v226, 64, v219\n"
    "v_xor_b32_e32 v227, 96, v219\n"
    "ds_write_b128 v221, v[80:83] offset:352\n"
    "v_xor_b32_e32 v228, 0, v223\n"
    "v_xor_b32_e32 v229, 32, v223\n"
    "s_mov_b32 s59, 52224\n"
    "buffer_load_dwordx4 v[108:111], v209, s[80:83], s59 offen offset:0\n"
    "v_xor_b32_e32 v230, 64, v223\n"
    "v_xor_b32_e32 v231, 96, v223\n"
    "ds_write_b128 v221, v[76:79] offset:368\n"
    "v_lshl_add_u32 v196, s51, 8, v225\n"
    "v_lshl_add_u32 v197, s51, 8, v227\n"
    "v_mfma_f32_32x32x1_2b_f32 v[32:63], v254, v254, 0\n"
    "ds_write_b128 v221, v[84:87] offset:384\n"
    "buffer_load_dwordx4 v[112:115], v209, s[80:83], s59 offen offset:256\n"
    "v_mfma_f32_32x32x1_2b_f32 v[64:95], v254, v254, 0\n"
    "v_add_u32_e64 v254, s54, v242\n"
    "buffer_load_dwordx4 v[116:119], v209, s[80:83], s59 offen offset:512\n"
    "s_mov_b32 s65, 1376256\n"
    "buffer_load_dwordx4 a[176:179], v208, s[40:43], s65 offen\n"
    "s_mov_b32 s65, 1474560\n"
    "buffer_load_dwordx4 a[180:183], v208, s[40:43], s65 offen\n"
    "s_waitcnt vmcnt(12)\n"
    "s_barrier\n"
    "ds_read_b64_tr_b16 a[0:1], v220 offset:0\n"
    "ds_read_b64_tr_b16 a[2:3], v220 offset:3072\n"
    "ds_read_b64_tr_b16 a[4:5], v220 offset:8\n"
    "ds_read_b64_tr_b16 a[6:7], v220 offset:3080\n"
    "ds_read_b64_tr_b16 a[8:9], v220 offset:64\n"
    "ds_read_b64_tr_b16 a[10:11], v220 offset:3136\n"
    "ds_read_b64_tr_b16 a[12:13], v220 offset:72\n"
    "ds_read_b64_tr_b16 a[14:15], v220 offset:3144\n"
    "ds_read_b64_tr_b16 v[168:169], v218 offset:128\n"
    "ds_read_b64_tr_b16 v[170:171], v218 offset:3200\n"
    "ds_read_b64_tr_b16 a[16:17], v220 offset:6144\n"
    "ds_read_b64_tr_b16 a[18:19], v220 offset:9216\n"
    "ds_read_b64_tr_b16 a[20:21], v220 offset:6152\n"
    "ds_read_b64_tr_b16 a[22:23], v220 offset:9224\n"
    "ds_read_b64_tr_b16 a[24:25], v220 offset:6208\n"
    "ds_read_b64_tr_b16 a[26:27], v220 offset:9280\n"
    "ds_read_b64_tr_b16 a[28:29], v220 offset:6216\n"
    "ds_read_b64_tr_b16 a[30:31], v220 offset:9288\n"
    "ds_read_b64_tr_b16 v[172:173], v218 offset:6272\n"
    "s_nop 0\n"
    "s_mov_b32 s59, 0\n"
    "buffer_load_dwordx4 v[120:123], v209, s[80:83], s59 offen offset:0\n"
    "ds_read_b64_tr_b16 v[174:175], v218 offset:9344\n"
    "ds_read_b64_tr_b16 a[32:33], v220 offset:12288\n"
    "ds_read_b64_tr_b16 a[34:35], v220 offset:15360\n"
    "ds_read_b64_tr_b16 a[36:37], v220 offset:12296\n"
    "ds_read_b64_tr_b16 a[38:39], v220 offset:15368\n"
    "ds_read_b64_tr_b16 a[40:41], v220 offset:12352\n"
    "ds_read_b64_tr_b16 a[42:43], v220 offset:15424\n"
    "ds_read_b64_tr_b16 a[44:45], v220 offset:12360\n"
    "ds_read_b64_tr_b16 a[46:47], v220 offset:15432\n"
    "ds_read_b64_tr_b16 v[176:177], v218 offset:12416\n"
    "ds_read_b64_tr_b16 v[178:179], v218 offset:15488\n"
    "s_waitcnt vmcnt(12)\n"
    "ds_write2_b32 v216, a240, a241 offset0:0 offset1:20\n"
    "ds_write2_b32 v216, a242, a243 offset0:40 offset1:60\n"
    "buffer_load_dwordx4 v[124:127], v209, s[80:83], s59 offen offset:256\n"
    "s_waitcnt vmcnt(12)\n"
    "ds_write2_b32 v216, a244, a245 offset0:80 offset1:100\n"
    "s_waitcnt lgkmcnt(2)\n"
    "s_barrier\n"
    "ds_write2_b32 v216, a246, a247 offset0:120 offset1:140\n"
    "buffer_load_dwordx4 v[128:131], v209, s[80:83], s59 offen offset:512\n"
    "v_add_u32_e32 v216, 1360, v216\n"
    "s_waitcnt vmcnt(12)\n"
    "v_or3_b32 v200, v200, v96, v97\n"
    "v_or3_b32 v200, v200, v98, v99\n"
    "v_and_b32_e32 v232, 0x7fff7fff, v96\n"
    "v_add_u32_e32 v96, v201, v96\n"
    "v_add_u32_e64 v98, v201, v98\n"
    "ds_write_b128 v217, v[96:99] offset:24576\n"
    "s_mov_b32 s65, 0\n"
    "buffer_load_dwordx4 a[184:187], v208, s[40:43], s65 offen\n"
    "s_waitcnt vmcnt(12)\n"
    "v_or3_b32 v200, v200, v100, v101\n"
    "v_or3_b32 v200, v200, v102, v103\n"
    "v_add_u32_e64 v100, v203, v100\n"
    "v_add_u32_e64 v102, v203, v102\n"
    "ds_write_b128 v217, v[100:103] offset:24832\n"
    "s_mov_b32 s65, 98304\n"
    "buffer_load_dwordx4 a[188:191], v208, s[40:43], s65 offen\n"
    "s_waitcnt vmcnt(12)\n"
    "v_or3_b32 v200, v200, v104, v105\n"
    "v_or3_b32 v200, v200, v106, v107\n"
    "v_add_u32_e32 v104, v205, v104\n"
    "v_add_u32_e64 v106, v205, v106\n"
    "ds_write_b128 v217, v[104:107] offset:25088\n"
    "s_waitcnt lgkmcnt(0)\n"
    "s_barrier\n"
    "s_mov_b32 s59, 3072\n"
    "buffer_load_dwordx4 v[132:135], v209, s[80:83], s59 offen offset:0\n"
    "ds_read_b64_tr_b16 a[48:49], v224 offset:24576\n"
    "ds_read_b64_tr_b16 v[180:181], v196 offset:24576\n"
    "ds_read_b64_tr_b16 a[52:53], v226 offset:24576\n"
    "ds_read_b64_tr_b16 v[184:185], v197 offset:24576\n"
    "ds_read_b64_tr_b16 a[56:57], v228 offset:24576\n"
    "ds_read_b64_tr_b16 a[60:61], v229 offset:24576\n"
    "ds_read_b64_tr_b16 a[64:65], v230 offset:24576\n"
    "ds_read_b64_tr_b16 a[68:69], v231 offset:24576\n"
    "ds_read_b64_tr_b16 a[72:73], v224 offset:24832\n"
    "ds_read_b64_tr_b16 a[76:77], v226 offset:24832\n"
    "s_waitcnt vmcnt(12)\n"
    "ds_write2_b32 v216, a248, a249 offset0:0 offset1:20\n"
    "ds_write2_b32 v216, a250, a251 offset0:40 offset1:60\n"
    "buffer_load_dwordx4 v[136:139], v209, s[80:83], s59 offen offset:256\n"
    "s_waitcnt vmcnt(12)\n"
    "ds_write2_b32 v216, a252, a253 offset0:80 offset1:100\n"
    "ds_write2_b32 v216, a254, a255 offset0:120 offset1:140\n"
    "v_add_u32_e32 v216, 1360, v216\n"
    "buffer_load_dwordx4 v[140:143], v209, s[80:83], s59 offen offset:512\n"
    "s_waitcnt vmcnt(12)\n"
    "v_or3_b32 v200, v200, v108, v109\n"
    "v_or3_b32 v200, v200, v110, v111\n"
    "v_add_u32_e32 v108, v201, v108\n"
    "v_add_u32_e64 v110, v201, v110\n"
    "ds_write_b128 v217, v[108:111] offset:0\n"
    "s_mov_b32 s65, 393216\n"
    "buffer_load_dwordx4 a[192:195], v208, s[40:43], s65 offen\n"
    "s_waitcnt vmcnt(12)\n"
    "v_or3_b32 v200, v200, v112, v113\n"
    "v_or3_b32 v200, v200, v114, v115\n"
    "v_add_u32_e32 v112, v203, v112\n"
    "v_add_u32_e64 v114, v203, v114\n"
    "ds_write_b128 v217, v[112:115] offset:256\n"
    "v_mov_b32_e64 v112, v188\n"
    "v_mov_b32_e64 v113, v189\n"
    "v_mov_b32_e64 v114, v190\n"
    "v_mov_b32_e32 v115, v191\n"
    "s_mov_b32 s65, 491520\n"
    "buffer_load_dwordx4 a[196:199], v208, s[40:43], s65 offen\n"
    "s_waitcnt vmcnt(12)\n"
    "v_or3_b32 v200, v200, v116, v117\n"
    "v_or3_b32 v200, v200, v118, v119\n"
    "v_add_u32_e64 v116, v205, v116\n"
    "v_add_u32_e64 v118, v205, v118\n"
    "ds_write_b128 v217, v[116:119] offset:512\n"
    "v_mov_b32_e64 v116, v192\n"
    "v_mov_b32_e64 v117, v193\n"
    "v_mov_b32_e64 v118, v194\n"
    "v_mov_b32_e64 v119, v195\n"
    "s_waitcnt lgkmcnt(0)\n"
    "s_barrier\n"
    "s_mov_b32 s59, 24576\n"
    "buffer_load_dwordx4 v[144:147], v209, s[80:83], s59 offen offset:0\n"
    "ds_read_b64_tr_b16 a[50:51], v224 offset:0\n"
    "ds_read_b64_tr_b16 v[182:183], v196 offset:0\n"
    "ds_read_b64_tr_b16 a[54:55], v226 offset:0\n"
    "ds_read_b64_tr_b16 v[186:187], v197 offset:0\n"
    "ds_read_b64_tr_b16 a[58:59], v228 offset:0\n"
    "ds_read_b64_tr_b16 a[62:63], v229 offset:0\n"
    "ds_read_b64_tr_b16 a[66:67], v230 offset:0\n"
    "ds_read_b64_tr_b16 a[70:71], v231 offset:0\n"
    "ds_read_b64_tr_b16 a[74:75], v224 offset:256\n"
    "ds_read_b64_tr_b16 a[78:79], v226 offset:256\n"
    "s_waitcnt vmcnt(12)\n"
    "ds_write2_b32 v216, a176, a177 offset0:0 offset1:20\n"
    "ds_write2_b32 v216, a178, a179 offset0:40 offset1:60\n"
    "s_waitcnt lgkmcnt(8)\n"
    "ds_write_b128 v207, v[184:187] offset:36864\n"
    "buffer_load_dwordx4 v[148:151], v209, s[80:83], s59 offen offset:256\n"
    "s_waitcnt vmcnt(12)\n"
    "ds_write2_b32 v216, a180, a181 offset0:80 offset1:100\n"
    "ds_write2_b32 v216, a182, a183 offset0:120 offset1:140\n"
    "v_add_u32_e32 v216, -4800, v216\n"
    "buffer_load_dwordx4 v[152:155], v209, s[80:83], s59 offen offset:512\n"
    "s_waitcnt vmcnt(12)\n"
    "v_or3_b32 v200, v200, v120, v121\n"
    "v_or3_b32 v200, v200, v122, v123\n"
    "v_add_u32_e64 v120, v201, v120\n"
    "v_add_u32_e64 v122, v201, v122\n"
    "ds_write_b128 v217, v[120:123] offset:12288\n"
    "s_waitcnt lgkmcnt(1)\n"
    "s_barrier\n"
    "s_mov_b32 s65, 786432\n"
    "buffer_load_dwordx4 a[200:203], v208, s[40:43], s65 offen\n"
    "s_waitcnt vmcnt(12)\n"
    "v_or3_b32 v200, v200, v124, v125\n"
    "v_or3_b32 v200, v200, v126, v127\n"
    "v_add_u32_e32 v124, v203, v124\n"
    "v_add_u32_e64 v126, v203, v126\n"
    "ds_write_b128 v217, v[124:127] offset:12544\n"
    "s_mov_b32 s65, 884736\n"
    "buffer_load_dwordx4 a[204:207], v208, s[40:43], s65 offen\n"
    "s_waitcnt vmcnt(12)\n"
    "v_or3_b32 v200, v200, v128, v129\n"
    "v_or3_b32 v200, v200, v130, v131\n"
    "v_add_u32_e32 v128, v205, v128\n"
    "v_add_u32_e64 v130, v205, v130\n"
    "ds_write_b128 v217, v[128:131] offset:12800\n"
    "ds_read_b128 a[224:227], v250 offset:0\n"
    "ds_read_b128 a[228:231], v250 offset:320\n"
    "ds_read_b128 a[232:235], v251 offset:0\n"
    "ds_read_b128 a[236:239], v251 offset:336\n"
    "ds_read_b128 a[240:243], v250 offset:80\n"
    "ds_read_b128 a[244:247], v250 offset:400\n"
    "ds_read_b128 a[248:251], v251 offset:672\n"
    "ds_read_b128 a[252:255], v251 offset:1008\n"
    "ds_read_b128 v[104:107], v252 offset:1344\n"
    "ds_read_b128 v[108:111], v252 offset:1680\n"
    "ds_read_b128 v[128:131], v252 offset:2016\n"
    "s_waitcnt lgkmcnt(11)\n"
    "s_barrier\n"
    "s_mov_b32 s59, 27648\n"
    "buffer_load_dwordx4 v[156:159], v209, s[80:83], s59 offen offset:0\n"
    "ds_read_b64_tr_b16 a[80:81], v224 offset:12288\n"
    "ds_read_b64_tr_b16 a[84:85], v225 offset:12288\n"
    "ds_read_b64_tr_b16 a[88:89], v226 offset:12288\n"
    "ds_read_b64_tr_b16 a[92:93], v227 offset:12288\n"
    "s_waitcnt lgkmcnt(12)\n"
    "v_smfmac_f32_16x16x64_bf16 v[0:3], a[232:235], a[224:231], v240\n"
    "s_waitcnt lgkmcnt(11)\n"
    "ds_read_b64_tr_b16 a[96:97], v228 offset:12288\n"
    "ds_read_b64_tr_b16 a[100:101], v229 offset:12288\n"
    "v_smfmac_f32_16x16x64_bf16 v[4:7], a[236:239], a[224:231], v240 abid:1\n"
    "ds_read_b64_tr_b16 a[104:105], v230 offset:12288\n"
    "ds_read_b64_tr_b16 a[108:109], v231 offset:12288\n"
    "ds_read_b64_tr_b16 a[112:113], v224 offset:12544\n"
    "ds_read_b64_tr_b16 a[116:117], v225 offset:12544\n"
    "ds_read_b64_tr_b16 a[120:121], v226 offset:12544\n"
    "s_waitcnt lgkmcnt(15)\n"
    "v_smfmac_f32_16x16x64_bf16 v[8:11], a[248:251], a[240:247], v240\n"
    "s_waitcnt lgkmcnt(14)\n"
    "ds_read_b64_tr_b16 a[124:125], v227 offset:12544\n"
    "s_waitcnt vmcnt(12)\n"
    "ds_write2_b32 v216, a184, a185 offset0:0 offset1:20\n"
    "v_smfmac_f32_16x16x64_bf16 v[12:15], a[252:255], a[240:247], v240 abid:1\n"
    "s_nop 0\n"
    "ds_write2_b32 v216, a186, a187 offset0:40 offset1:60\n"
    "ds_read_b128 a[224:227], v250 offset:160\n"
    "ds_read_b128 a[228:231], v250 offset:480\n"
    "ds_read_b128 a[232:235], v251 offset:1344\n"
    "ds_read_b128 a[236:239], v251 offset:1680\n"
    "buffer_load_dwordx4 v[160:163], v209, s[80:83], s59 offen offset:256\n"
    "s_waitcnt vmcnt(12)\n"
    "ds_write2_b32 v216, a188, a189 offset0:80 offset1:100\n"
    "ds_write2_b32 v216, a190, a191 offset0:120 offset1:140\n"
    "v_add_u32_e32 v216, 1360, v216\n"
    "ds_read_b128 a[240:243], v250 offset:240\n"
    "ds_read_b128 a[244:247], v250 offset:560\n"
    "buffer_load_dwordx4 v[164:167], v209, s[80:83], s59 offen offset:512\n"
    "s_waitcnt vmcnt(12)\n"
    "v_or3_b32 v200, v200, v132, v133\n"
    "v_or3_b32 v200, v200, v134, v135\n"
    "v_add_u32_e64 v132, v201, v132\n"
    "v_add_u32_e64 v134, v201, v134\n"
    "ds_write_b128 v217, v[132:135] offset:24576\n"
    "s_waitcnt lgkmcnt(6)\n"
    "v_smfmac_f32_16x16x64_bf16 v[16:19], a[232:235], a[224:231], v240\n"
    "s_waitcnt lgkmcnt(5)\n"
    "v_smfmac_f32_16x16x64_bf16 v[20:23], a[236:239], a[224:231], v240 abid:1\n"
    "s_mov_b32 s65, 1179648\n"
    "buffer_load_dwordx4 a[208:211], v208, s[40:43], s65 offen\n"
    "s_waitcnt vmcnt(12)\n"
    "v_or3_b32 v200, v200, v136, v137\n"
    "v_or3_b32 v200, v200, v138, v139\n"
    "v_add_u32_e32 v136, v203, v136\n"
    "v_add_u32_e64 v138, v203, v138\n"
    "ds_write_b128 v217, v[136:139] offset:24832\n"
    "ds_read_b128 a[248:251], v251 offset:2016\n"
    "ds_read_b128 a[252:255], v251 offset:2352\n"
    "s_mov_b32 s65, 1277952\n"
    "buffer_load_dwordx4 a[212:215], v208, s[40:43], s65 offen\n"
    "s_waitcnt vmcnt(12)\n"
    "v_or3_b32 v200, v200, v140, v141\n"
    "v_or3_b32 v200, v200, v142, v143\n"
    "v_add_u32_e32 v140, v205, v140\n"
    "v_add_u32_e64 v142, v205, v142\n"
    "ds_write_b128 v217, v[140:143] offset:25088\n"
    "ds_read_b128 a[224:227], v250 offset:27024\n"
    "ds_read_b128 a[228:231], v250 offset:27344\n"
    "ds_read_b128 a[232:235], v251 offset:10752\n"
    "ds_read_b128 a[236:239], v251 offset:11088\n"
    "ds_read_b128 v[132:135], v252 offset:2352\n"
    "s_waitcnt lgkmcnt(7)\n"
    "v_smfmac_f32_16x16x64_bf16 v[24:27], a[248:251], a[240:247], v240\n"
    "ds_read_b128 a[248:251], v251 offset:11424\n"
    "s_waitcnt lgkmcnt(7)\n"
    "v_smfmac_f32_16x16x64_bf16 v[28:31], a[252:255], a[240:247], v240 abid:1\n"
    "ds_read_b128 a[240:243], v250 offset:27104\n"
    "ds_read_b128 a[244:247], v250 offset:27424\n"
    "ds_read_b128 a[252:255], v251 offset:11760\n"
    "s_waitcnt lgkmcnt(9)\n"
    "s_barrier\n"
    "s_mov_b32 s65, 1572864\n"
    "buffer_load_dwordx4 a[216:219], v208, s[40:43], s65 offen\n"
    "ds_read_b64_tr_b16 a[82:83], v224 offset:24576\n"
    "ds_read_b64_tr_b16 a[86:87], v225 offset:24576\n"
    "ds_read_b64_tr_b16 a[90:91], v226 offset:24576\n"
    "s_waitcnt lgkmcnt(9)\n"
    "v_smfmac_f32_16x16x64_bf16 v[32:35], a[232:235], a[224:231], v240\n"
    "s_waitcnt lgkmcnt(8)\n"
    "ds_read_b64_tr_b16 a[94:95], v227 offset:24576\n"
    "ds_read_b64_tr_b16 a[98:99], v228 offset:24576\n"
    "v_smfmac_f32_16x16x64_bf16 v[36:39], a[236:239], a[224:231], v240 abid:1\n"
    "ds_read_b64_tr_b16 a[102:103], v229 offset:24576\n"
    "ds_read_b64_tr_b16 a[106:107], v230 offset:24576\n"
    "ds_read_b64_tr_b16 a[110:111], v231 offset:24576\n"
    "ds_read_b64_tr_b16 a[114:115], v224 offset:24832\n"
    "ds_read_b64_tr_b16 a[118:119], v225 offset:24832\n"
    "ds_read_b64_tr_b16 a[122:123], v226 offset:24832\n"
    "ds_read_b64_tr_b16 a[126:127], v227 offset:24832\n"
    "s_waitcnt lgkmcnt(13)\n"
    "v_smfmac_f32_16x16x64_bf16 v[40:43], a[248:251], a[240:247], v240\n"
    "s_waitcnt lgkmcnt(12)\n"
    "s_waitcnt vmcnt(12)\n"
    "ds_write2_b32 v216, a192, a193 offset0:0 offset1:20\n"
    "v_smfmac_f32_16x16x64_bf16 v[44:47], a[252:255], a[240:247], v240 abid:1\n"
    "s_nop 0\n"
    "ds_write2_b32 v216, a194, a195 offset0:40 offset1:60\n"
    "ds_read_b128 a[224:227], v250 offset:27184\n"
    "ds_read_b128 a[228:231], v250 offset:27504\n"
    "ds_read_b128 a[232:235], v251 offset:12096\n"
    "s_mov_b32 s65, 1671168\n"
    "buffer_load_dwordx4 a[220:223], v208, s[40:43], s65 offen\n"
    "s_waitcnt vmcnt(12)\n"
    "ds_write2_b32 v216, a196, a197 offset0:80 offset1:100\n"
    "ds_write2_b32 v216, a198, a199 offset0:120 offset1:140\n"
    "v_add_u32_e32 v216, 1360, v216\n"
    "ds_read_b128 a[236:239], v251 offset:12432\n"
    "ds_read_b128 a[240:243], v250 offset:27264\n"
    "ds_read_b128 a[244:247], v250 offset:27584\n"
    "s_waitcnt vmcnt(11)\n"
    "v_or3_b32 v200, v200, v144, v145\n"
    "v_or3_b32 v200, v200, v146, v147\n"
    "v_add_u32_e64 v144, v201, v144\n"
    "v_add_u32_e32 v146, v201, v146\n"
    "ds_write_b128 v217, v[144:147] offset:0\n"
    "s_waitcnt lgkmcnt(6)\n"
    "v_smfmac_f32_16x16x64_bf16 v[48:51], a[232:235], a[224:231], v240\n"
    "ds_read_b128 a[248:251], v251 offset:12768\n"
    "ds_read_b128 a[252:255], v251 offset:13104\n"
    "ds_read_b128 a[232:235], v251 offset:21504\n"
    "s_waitcnt vmcnt(10)\n"
    "v_or3_b32 v200, v200, v148, v149\n"
    "v_or3_b32 v200, v200, v150, v151\n"
    "v_add_u32_e32 v148, v203, v148\n"
    "v_add_u32_e32 v150, v203, v150\n"
    "ds_write_b128 v217, v[148:151] offset:256\n"
    "s_waitcnt lgkmcnt(6)\n"
    "v_smfmac_f32_16x16x64_bf16 v[52:55], a[236:239], a[224:231], v240 abid:1\n"
    "ds_read_b128 a[224:227], v250 offset:54048\n"
    "ds_read_b128 a[228:231], v250 offset:54368\n"
    "ds_read_b128 a[236:239], v251 offset:21840\n"
    "s_waitcnt vmcnt(9)\n"
    "v_or3_b32 v200, v200, v152, v153\n"
    "v_or3_b32 v200, v200, v154, v155\n"
    "v_add_u32_e32 v152, v205, v152\n"
    "v_add_u32_e32 v154, v205, v154\n"
    "ds_write_b128 v217, v[152:155] offset:512\n"
    "s_waitcnt lgkmcnt(6)\n"
    "v_smfmac_f32_16x16x64_bf16 v[56:59], a[248:251], a[240:247], v240\n"
    "v_smfmac_f32_16x16x64_bf16 v[60:63], a[252:255], a[240:247], v240 abid:1\n"
    "ds_read_b128 a[240:243], v250 offset:54128\n"
    "ds_read_b128 a[244:247], v250 offset:54448\n"
    "ds_read_b128 a[248:251], v251 offset:22176\n"
    "ds_read_b128 a[252:255], v251 offset:22512\n"
    "ds_read_b128 v[144:147], v252 offset:10752\n"
    "ds_read_b128 v[148:151], v252 offset:11088\n"
    "s_waitcnt lgkmcnt(8)\n"
    "v_smfmac_f32_16x16x64_bf16 v[64:67], a[232:235], a[224:231], v240\n"
    "ds_read_b128 a[232:235], v251 offset:22848\n"
    "s_waitcnt lgkmcnt(8)\n"
    "v_smfmac_f32_16x16x64_bf16 v[68:71], a[236:239], a[224:231], v240 abid:1\n"
    "ds_read_b128 a[224:227], v250 offset:54208\n"
    "ds_read_b128 a[228:231], v250 offset:54528\n"
    "s_waitcnt lgkmcnt(9)\n"
    "s_barrier\n"
    "ds_read_b64_tr_b16 a[128:129], v224 offset:0\n"
    "ds_read_b64_tr_b16 a[132:133], v225 offset:0\n"
    "ds_read_b64_tr_b16 a[136:137], v226 offset:0\n"
    "s_waitcnt lgkmcnt(9)\n"
    "v_smfmac_f32_16x16x64_bf16 v[72:75], a[248:251], a[240:247], v240\n"
    "s_waitcnt lgkmcnt(8)\n"
    "ds_read_b64_tr_b16 a[140:141], v227 offset:0\n"
    "ds_read_b64_tr_b16 a[144:145], v228 offset:0\n"
    "v_smfmac_f32_16x16x64_bf16 v[76:79], a[252:255], a[240:247], v240 abid:1\n"
    "ds_read_b64_tr_b16 a[148:149], v229 offset:0\n"
    "ds_read_b64_tr_b16 a[152:153], v230 offset:0\n"
    "ds_read_b64_tr_b16 a[156:157], v231 offset:0\n"
    "ds_read_b64_tr_b16 a[160:161], v224 offset:256\n"
    "ds_read_b64_tr_b16 a[164:165], v225 offset:256\n"
    "ds_read_b64_tr_b16 a[168:169], v226 offset:256\n"
    "ds_read_b64_tr_b16 a[172:173], v227 offset:256\n"
    "s_waitcnt vmcnt(8)\n"
    "ds_write2_b32 v216, a200, a201 offset0:0 offset1:20\n"
    "s_waitcnt lgkmcnt(13)\n"
    "v_smfmac_f32_16x16x64_bf16 v[80:83], a[232:235], a[224:231], v240\n"
    "ds_write2_b32 v216, a202, a203 offset0:40 offset1:60\n"
    "ds_read_b128 a[236:239], v251 offset:23184\n"
    "ds_read_b128 a[240:243], v250 offset:54288\n"
    "ds_read_b128 a[244:247], v250 offset:54608\n"
    "ds_read_b128 a[248:251], v251 offset:23520\n"
    "s_waitcnt vmcnt(7)\n"
    "ds_write2_b32 v216, a204, a205 offset0:80 offset1:100\n"
    "ds_write2_b32 v216, a206, a207 offset0:120 offset1:140\n"
    "v_add_u32_e32 v216, 1360, v216\n"
    "ds_read_b128 a[252:255], v251 offset:23856\n"
    "ds_read_b128 a[232:235], v252 offset:0\n"
    "s_waitcnt vmcnt(6)\n"
    "v_or3_b32 v200, v200, v156, v157\n"
    "v_or3_b32 v200, v200, v158, v159\n"
    "v_add_u32_e64 v156, v201, v156\n"
    "v_add_u32_e64 v158, v201, v158\n"
    "ds_write_b128 v217, v[156:159] offset:12288\n"
    "s_waitcnt lgkmcnt(7)\n"
    "v_smfmac_f32_16x16x64_bf16 v[84:87], a[236:239], a[224:231], v240 abid:1\n"
    "s_waitcnt lgkmcnt(5)\n"
    "v_smfmac_f32_16x16x64_bf16 v[88:91], a[248:251], a[240:247], v240\n"
    "ds_read_b128 a[236:239], v252 offset:336\n"
    "ds_read_b128 a[248:251], v252 offset:672\n"
    "s_waitcnt vmcnt(5)\n"
    "v_or3_b32 v200, v200, v160, v161\n"
    "v_or3_b32 v200, v200, v162, v163\n"
    "v_add_u32_e32 v160, v203, v160\n"
    "v_add_u32_e64 v162, v203, v162\n"
    "ds_write_b128 v217, v[160:163] offset:12544\n"
    "s_waitcnt lgkmcnt(4)\n"
    "v_smfmac_f32_16x16x64_bf16 v[92:95], a[252:255], a[240:247], v240 abid:1\n"
    "ds_read_b128 a[252:255], v252 offset:1008\n"
    "ds_read_b128 v[160:163], v252 offset:11424\n"
    "s_waitcnt vmcnt(4)\n"
    "v_or3_b32 v200, v200, v164, v165\n"
    "v_or3_b32 v200, v200, v166, v167\n"
    "v_add_u32_e64 v164, v205, v164\n"
    "v_add_u32_e64 v166, v205, v166\n"
    "ds_write_b128 v217, v[164:167] offset:12800\n"
    "v_and_b32_e32 v200, 0x40004000, v200\n"
    "v_cmp_le_u32_e32 vcc, 1, v200\n"
    "s_nop 1\n"
    "s_cmp_lg_u64 vcc, 0\n"
    "s_cselect_b32 vcc_lo, 1, 0\n"
    "s_or_b32 s66, s66, vcc_lo\n"
    "v_cmp_le_u32_e32 vcc, 981467136, v232\n"
    "s_nop 1\n"
    "s_cmp_lg_u64 vcc, 0\n"
    "s_cselect_b32 vcc_lo, 2, 0\n"
    "s_or_b32 s66, s66, vcc_lo\n"
    "v_mov_b32_e32 v200, s66\n"
    "v_mov_b32_e32 v232, 162992\n"
    "s_mov_b64 exec, 1\n"
    "ds_or_b32 v232, v200\n"
    "s_mov_b64 exec, -1\n"
    "ds_read_b128 v[164:167], v252 offset:11760\n"
    "s_waitcnt lgkmcnt(2)\n"
    "s_barrier\n"
    "ds_read_b64_tr_b16 a[130:131], v224 offset:12288\n"
    "ds_read_b64_tr_b16 a[134:135], v225 offset:12288\n"
    "ds_read_b64_tr_b16 a[138:139], v226 offset:12288\n"
    "ds_read_b64_tr_b16 a[142:143], v227 offset:12288\n"
    "ds_read_b64_tr_b16 a[146:147], v228 offset:12288\n"
    "ds_read_b64_tr_b16 a[150:151], v229 offset:12288\n"
    "ds_read_b64_tr_b16 a[154:155], v230 offset:12288\n"
    "ds_read_b64_tr_b16 a[158:159], v231 offset:12288\n"
    "ds_read_b64_tr_b16 a[162:163], v224 offset:12544\n"
    "ds_read_b64_tr_b16 a[166:167], v225 offset:12544\n"
    "ds_read_b64_tr_b16 a[170:171], v226 offset:12544\n"
    "ds_read_b64_tr_b16 a[174:175], v227 offset:12544\n"
    "s_nop 0\n"
    "s_waitcnt vmcnt(3)\n"
    "ds_write2_b32 v216, a208, a209 offset0:0 offset1:20\n"
    "ds_write2_b32 v216, a210, a211 offset0:40 offset1:60\n"
    "s_waitcnt vmcnt(2)\n"
    "ds_write2_b32 v216, a212, a213 offset0:80 offset1:100\n"
    "s_waitcnt lgkmcnt(2)\n"
    "s_barrier\n"
    "s_add_u32 m0, s38, 0\n"
    "s_mov_b32 s64, 18432\n"
    "buffer_load_dwordx4 v210, s[84:87], s64 offen offset:0 lds\n"
    "buffer_load_dwordx4 v210, s[84:87], s64 offen offset:1024 lds\n"
    "buffer_load_dwordx4 v210, s[84:87], s64 offen offset:2048 lds\n"
    "s_add_u32 m0, s38, 3072\n"
    "s_mov_b32 s64, 21504\n"
    "buffer_load_dwordx4 v210, s[84:87], s64 offen offset:0 lds\n"
    "buffer_load_dwordx4 v210, s[84:87], s64 offen offset:1024 lds\n"
    "buffer_load_dwordx4 v210, s[84:87], s64 offen offset:2048 lds\n"
    "s_add_u32 m0, s38, 6144\n"
    "s_mov_b32 s64, 24576\n"
    "buffer_load_dwordx4 v210, s[84:87], s64 offen offset:0 lds\n"
    "buffer_load_dwordx4 v210, s[84:87], s64 offen offset:1024 lds\n"
    "buffer_load_dwordx4 v210, s[84:87], s64 offen offset:2048 lds\n"
    "s_sub_u32 s80, s76, .Lreplk_last-.Lreplk_loop\n"
    "s_subb_u32 s81, s77, 0\n"
    "s_add_u32 s82, s36, .Lreplk_last-.Lreplk_loop\n"
    "s_and_b32 s85, s37, 0x3fff\n"
    "s_add_u32 s84, s85, 8192\n"
    "s_lshr_b32 s85, s85, 2\n"
    "v_lshlrev_b32_e64 v235, 3, v244\n"
    "s_mov_b32 exec_hi, 0\n"
    "buffer_load_dword v233, v235, s[40:43], s53 offen sc1\n"
    "s_mov_b32 exec_hi, -1\n"
    "s_add_u32 s53, s53, 393216\n"
    "ds_write2_b32 v216, a214, a215 offset0:120 offset1:140\n"
    "v_add_u32_e32 v216, 1360, v216\n"
    "s_mov_b32 s64, 0\n"
    "s_mov_b32 s59, 0\n"
    "ds_read_b32 v232, v232\n"
    "v_lshlrev_b32_e32 v235, 3, v244\n"
    "s_mov_b32 exec_hi, 0\n"
    "buffer_load_dword v233, v235, s[40:43], s53 offen sc1\n"
    "s_mov_b32 exec_hi, -1\n"
    "s_add_u32 s53, s53, 393216\n"
    "s_waitcnt vmcnt(12)\n"
    "ds_write2_b32 v216, a216, a217 offset0:0 offset1:20\n"
    "ds_write2_b32 v216, a218, a219 offset0:40 offset1:60\n"
    "v_lshlrev_b32_e32 v235, 3, v244\n"
    "s_mov_b32 exec_hi, 0\n"
    "buffer_load_dword v233, v235, s[40:43], s53 offen sc1\n"
    "s_mov_b32 exec_hi, -1\n"
    "s_add_u32 s53, s53, 393216\n"
    "s_waitcnt vmcnt(12)\n"
    "ds_write2_b32 v216, a220, a221 offset0:80 offset1:100\n"
    "ds_write2_b32 v216, a222, a223 offset0:120 offset1:140\n"
    "v_lshlrev_b32_e64 v235, 3, v244\n"
    "s_mov_b32 exec_hi, 0\n"
    "buffer_load_dword v233, v235, s[40:43], s53 offen sc1\n"
    "s_mov_b32 exec_hi, -1\n"
    "s_add_u32 s53, s53, 393216\n"
    "v_mov_b32_e64 v235, v222\n"
    "s_waitcnt lgkmcnt(0)\n"
    "s_barrier\n"
    "s_getpc_b64 vcc\n"
    ".Lreplk_pc2:\n"
    "s_sub_u32 vcc_lo, vcc_lo, .Lreplk_pc2-.Lreplk_pc0\n"
    "s_subb_u32 vcc_hi, vcc_hi, 0\n"
    "global_load_dword v233, v215, vcc\n"
    "ds_read_b128 a[224:227], v250 offset:640\n"
    "ds_read_b128 a[228:231], v250 offset:960\n"
    "ds_read_b128 a[240:243], v250 offset:720\n"
    "ds_read_b128 a[244:247], v250 offset:1040\n"
    "ds_read_b128 v[96:99], v250 offset:800\n"
    "ds_read_b128 v[100:103], v250 offset:1120\n"
    "ds_read_b128 v[120:123], v250 offset:880\n"
    "ds_read_b128 v[124:127], v250 offset:1200\n"
    "ds_read_b128 v[136:139], v250 offset:27664\n"
    "ds_read_b128 v[140:143], v250 offset:27984\n"
    "ds_read_b128 v[152:155], v250 offset:27744\n"
    "ds_read_b128 v[156:159], v250 offset:28064\n"
    "s_waitcnt lgkmcnt(10)\n"
    "v_smfmac_f32_16x16x64_bf16 v[0:3], a[232:235], a[224:231], v240\n"
    "s_nop 0\n"
    "ds_read_b128 a[232:235], v252 offset:12096\n"
    "v_smfmac_f32_16x16x64_bf16 v[4:7], a[236:239], a[224:231], v240 abid:1\n"
    "ds_read_b128 a[224:227], v250 offset:27824\n"
    "s_waitcnt lgkmcnt(10)\n"
    "v_smfmac_f32_16x16x64_bf16 v[8:11], a[248:251], a[240:247], v240\n"
    "ds_read_b128 a[228:231], v250 offset:28144\n"
    "v_smfmac_f32_16x16x64_bf16 v[12:15], a[252:255], a[240:247], v240 abid:1\n"
    "s_waitcnt lgkmcnt(9)\n"
    "ds_read_b128 a[236:239], v252 offset:12432\n"
    "v_smfmac_f32_16x16x64_bf16 v[16:19], v[104:107], v[96:103], v240\n"
    "ds_read_b128 a[240:243], v250 offset:27904\n"
    "v_smfmac_f32_16x16x64_bf16 v[20:23], v[108:111], v[96:103], v240 abid:1\n"
    "s_waitcnt lgkmcnt(9)\n"
    "ds_read_b128 a[244:247], v250 offset:28224\n"
    "v_smfmac_f32_16x16x64_bf16 v[24:27], v[128:131], v[120:127], v240\n"
    "ds_read_b128 a[248:251], v252 offset:12768\n"
    "v_smfmac_f32_16x16x64_bf16 v[28:31], v[132:135], v[120:127], v240 abid:1\n"
    "s_waitcnt lgkmcnt(9)\n"
    "ds_read_b128 a[252:255], v252 offset:13104\n"
    "v_smfmac_f32_16x16x64_bf16 v[32:35], v[144:147], v[136:143], v240\n"
    "s_nop 0\n"
    "ds_read_b128 v[96:99], v250 offset:54688\n"
    "v_smfmac_f32_16x16x64_bf16 v[36:39], v[148:151], v[136:143], v240 abid:1\n"
    "s_waitcnt lgkmcnt(9)\n"
    "ds_read_b128 v[100:103], v250 offset:55008\n"
    "v_smfmac_f32_16x16x64_bf16 v[40:43], v[160:163], v[152:159], v240\n"
    "ds_read_b128 v[104:107], v252 offset:21504\n"
    "v_smfmac_f32_16x16x64_bf16 v[44:47], v[164:167], v[152:159], v240 abid:1\n"
    "ds_read_b128 v[108:111], v252 offset:21840\n"
    "ds_read_b128 v[120:123], v250 offset:54768\n"
    "ds_read_b128 v[124:127], v250 offset:55088\n"
    "v_pk_mov_b32 v[224:225], v[0:1], v[0:1] op_sel:[0,1]\n"
    "v_pk_mov_b32 v[226:227], v[2:3], v[2:3] op_sel:[0,1]\n"
    "v_pk_mul_f32 v[228:229], v[0:1], v[0:1]\n"
    "v_pk_mul_f32 v[230:231], v[2:3], v[2:3]\n"
    "v_pk_add_f32 v[224:225], v[224:225], v[4:5]\n"
    "v_pk_add_f32 v[226:227], v[226:227], v[6:7]\n"
    "v_pk_fma_f32 v[228:229], v[4:5], v[4:5], v[228:229]\n"
    "v_pk_fma_f32 v[230:231], v[6:7], v[6:7], v[230:231]\n"
    "v_pk_add_f32 v[224:225], v[224:225], v[8:9]\n"
    "v_pk_add_f32 v[226:227], v[226:227], v[10:11]\n"
    "v_pk_fma_f32 v[228:229], v[8:9], v[8:9], v[228:229]\n"
    "v_pk_fma_f32 v[230:231], v[10:11], v[10:11], v[230:231]\n"
    "v_pk_add_f32 v[224:225], v[224:225], v[12:13]\n"
    "v_pk_add_f32 v[226:227], v[226:227], v[14:15]\n"
    "v_pk_fma_f32 v[228:229], v[12:13], v[12:13], v[228:229]\n"
    "v_pk_fma_f32 v[230:231], v[14:15], v[14:15], v[230:231]\n"
    "v_pk_add_f32 v[224:225], v[224:225], v[16:17]\n"
    "v_pk_add_f32 v[226:227], v[226:227], v[18:19]\n"
    "v_pk_fma_f32 v[228:229], v[16:17], v[16:17], v[228:229]\n"
    "v_pk_fma_f32 v[230:231], v[18:19], v[18:19], v[230:231]\n"
    "v_pk_add_f32 v[224:225], v[224:225], v[20:21]\n"
    "v_pk_add_f32 v[226:227], v[226:227], v[22:23]\n"
    "v_pk_fma_f32 v[228:229], v[20:21], v[20:21], v[228:229]\n"
    "v_pk_fma_f32 v[230:231], v[22:23], v[22:23], v[230:231]\n"
    "v_pk_add_f32 v[224:225], v[224:225], v[24:25]\n"
    "v_pk_add_f32 v[226:227], v[226:227], v[26:27]\n"
    "v_pk_fma_f32 v[228:229], v[24:25], v[24:25], v[228:229]\n"
    "v_pk_fma_f32 v[230:231], v[26:27], v[26:27], v[230:231]\n"
    "v_pk_add_f32 v[224:225], v[224:225], v[28:29]\n"
    "v_pk_add_f32 v[226:227], v[226:227], v[30:31]\n"
    "v_pk_fma_f32 v[228:229], v[28:29], v[28:29], v[228:229]\n"
    "v_pk_fma_f32 v[230:231], v[30:31], v[30:31], v[230:231]\n"
    "v_pk_add_f32 v[224:225], v[224:225], v[32:33]\n"
    "v_pk_add_f32 v[226:227], v[226:227], v[34:35]\n"
    "v_pk_fma_f32 v[228:229], v[32:33], v[32:33], v[228:229]\n"
    "v_pk_fma_f32 v[230:231], v[34:35], v[34:35], v[230:231]\n"
    "v_pk_add_f32 v[224:225], v[224:225], v[36:37]\n"
    "v_pk_add_f32 v[226:227], v[226:227], v[38:39]\n"
    "v_pk_fma_f32 v[228:229], v[36:37], v[36:37], v[228:229]\n"
    "v_pk_fma_f32 v[230:231], v[38:39], v[38:39], v[230:231]\n"
    "v_pk_add_f32 v[224:225], v[224:225], v[40:41]\n"
    "v_pk_add_f32 v[226:227], v[226:227], v[42:43]\n"
    "v_pk_fma_f32 v[228:229], v[40:41], v[40:41], v[228:229]\n"
    "v_pk_fma_f32 v[230:231], v[42:43], v[42:43], v[230:231]\n"
    "v_pk_add_f32 v[224:225], v[224:225], v[44:45]\n"
    "v_pk_add_f32 v[226:227], v[226:227], v[46:47]\n"
    "v_pk_fma_f32 v[228:229], v[44:45], v[44:45], v[228:229]\n"
    "v_pk_fma_f32 v[230:231], v[46:47], v[46:47], v[230:231]\n"
    "s_waitcnt lgkmcnt(0)\n"
    "v_smfmac_f32_16x16x64_bf16 v[48:51], a[232:235], a[224:231], v240\n"
    "s_nop 0\n"
    "ds_read_b128 v[128:131], v252 offset:22176\n"
    "v_smfmac_f32_16x16x64_bf16 v[52:55], a[236:239], a[224:231], v240 abid:1\n"
    "ds_read_b128 v[132:135], v252 offset:22512\n"
    "v_smfmac_f32_16x16x64_bf16 v[56:59], a[248:251], a[240:247], v240\n"
    "ds_read_b128 v[136:139], v250 offset:54848\n"
    "v_smfmac_f32_16x16x64_bf16 v[60:63], a[252:255], a[240:247], v240 abid:1\n"
    "ds_read_b128 v[140:143], v250 offset:55168\n"
    "v_smfmac_f32_16x16x64_bf16 v[64:67], v[104:107], v[96:103], v240\n"
    "ds_read_b128 v[144:147], v252 offset:22848\n"
    "v_smfmac_f32_16x16x64_bf16 v[68:71], v[108:111], v[96:103], v240 abid:1\n"
    "ds_read_b128 v[148:151], v252 offset:23184\n"
    "ds_read_b128 v[152:155], v250 offset:54928\n"
    "v_accvgpr_write_b32 a248, v236\n"
    "ds_read_b128 v[156:159], v250 offset:55248\n"
    "v_pk_add_f32 v[224:225], v[224:225], v[48:49]\n"
    "v_pk_add_f32 v[226:227], v[226:227], v[50:51]\n"
    "v_pk_fma_f32 v[228:229], v[48:49], v[48:49], v[228:229]\n"
    "v_pk_fma_f32 v[230:231], v[50:51], v[50:51], v[230:231]\n"
    "v_pk_add_f32 v[224:225], v[224:225], v[52:53]\n"
    "s_waitcnt vmcnt(5)\n"
    "s_barrier\n"
    "ds_read_b64_tr_b16 a[176:177], v220 offset:0\n"
    "ds_read_b64_tr_b16 a[178:179], v220 offset:3072\n"
    "v_pk_add_f32 v[226:227], v[226:227], v[54:55]\n"
    "ds_read_b64_tr_b16 a[180:181], v220 offset:8\n"
    "ds_read_b64_tr_b16 a[182:183], v220 offset:3080\n"
    "ds_read_b64_tr_b16 a[184:185], v220 offset:64\n"
    "v_pk_fma_f32 v[228:229], v[52:53], v[52:53], v[228:229]\n"
    "ds_read_b64_tr_b16 a[186:187], v220 offset:3136\n"
    "ds_read_b64_tr_b16 a[188:189], v220 offset:72\n"
    "ds_read_b64_tr_b16 a[190:191], v220 offset:3144\n"
    "v_pk_fma_f32 v[230:231], v[54:55], v[54:55], v[230:231]\n"
    "ds_read_b64_tr_b16 v[188:189], v218 offset:128\n"
    "ds_read_b64_tr_b16 v[190:191], v218 offset:3200\n"
    "ds_read_b64_tr_b16 a[192:193], v220 offset:6144\n"
    "v_pk_add_f32 v[224:225], v[224:225], v[56:57]\n"
    "ds_read_b64_tr_b16 a[194:195], v220 offset:9216\n"
    "ds_read_b64_tr_b16 a[196:197], v220 offset:6152\n"
    "ds_read_b64_tr_b16 a[198:199], v220 offset:9224\n"
    "v_pk_add_f32 v[226:227], v[226:227], v[58:59]\n"
    "ds_read_b64_tr_b16 a[200:201], v220 offset:6208\n"
    "ds_read_b64_tr_b16 a[202:203], v220 offset:9280\n"
    "ds_read_b64_tr_b16 a[204:205], v220 offset:6216\n"
    "v_pk_fma_f32 v[228:229], v[56:57], v[56:57], v[228:229]\n"
    "ds_read_b64_tr_b16 a[206:207], v220 offset:9288\n"
    "ds_read_b64_tr_b16 v[192:193], v218 offset:6272\n"
    "ds_read_b64_tr_b16 v[194:195], v218 offset:9344\n"
    "v_pk_fma_f32 v[230:231], v[58:59], v[58:59], v[230:231]\n"
    "ds_read_b64_tr_b16 a[208:209], v220 offset:12288\n"
    "ds_read_b64_tr_b16 a[210:211], v220 offset:15360\n"
    "ds_read_b64_tr_b16 a[212:213], v220 offset:12296\n"
    "v_pk_add_f32 v[224:225], v[224:225], v[60:61]\n"
    "ds_read_b64_tr_b16 a[214:215], v220 offset:15368\n"
    "ds_read_b64_tr_b16 a[216:217], v220 offset:12352\n"
    "ds_read_b64_tr_b16 a[218:219], v220 offset:15424\n"
    "v_pk_add_f32 v[226:227], v[226:227], v[62:63]\n"
    "ds_read_b64_tr_b16 a[220:221], v220 offset:12360\n"
    "ds_read_b64_tr_b16 a[222:223], v220 offset:15432\n"
    "ds_read_b64_tr_b16 v[196:197], v218 offset:12416\n"
    "v_pk_fma_f32 v[228:229], v[60:61], v[60:61], v[228:229]\n"
    "ds_read_b64_tr_b16 v[198:199], v218 offset:15488\n"
    "v_pk_fma_f32 v[230:231], v[62:63], v[62:63], v[230:231]\n"
    "v_pk_add_f32 v[224:225], v[224:225], v[64:65]\n"
    "v_pk_add_f32 v[226:227], v[226:227], v[66:67]\n"
    "v_pk_fma_f32 v[228:229], v[64:65], v[64:65], v[228:229]\n"
    "v_pk_fma_f32 v[230:231], v[66:67], v[66:67], v[230:231]\n"
    "v_pk_add_f32 v[224:225], v[224:225], v[68:69]\n"
    "v_pk_add_f32 v[226:227], v[226:227], v[70:71]\n"
    "v_pk_fma_f32 v[228:229], v[68:69], v[68:69], v[228:229]\n"
    "v_pk_fma_f32 v[230:231], v[70:71], v[70:71], v[230:231]\n"
    "s_waitcnt lgkmcnt(10)\n"
    "v_smfmac_f32_16x16x64_bf16 v[72:75], v[128:131], v[120:127], v240\n"
    "s_nop 0\n"
    "ds_read_b128 v[160:163], v252 offset:23520\n"
    "v_smfmac_f32_16x16x64_bf16 v[76:79], v[132:135], v[120:127], v240 abid:1\n"
    "ds_read_b128 v[164:167], v252 offset:23856\n"
    "v_smfmac_f32_16x16x64_bf16 v[80:83], v[144:147], v[136:143], v240\n"
    "v_accvgpr_write_b32 a249, v237\n"
    "v_smfmac_f32_16x16x64_bf16 v[84:87], v[148:151], v[136:143], v240 abid:1\n"
    "v_accvgpr_write_b32 a250, v238\n"
    "v_accvgpr_write_b32 a251, v239\n"
    "v_pk_add_f32 v[224:225], v[224:225], v[72:73]\n"
    "v_pk_add_f32 v[226:227], v[226:227], v[74:75]\n"
    "v_pk_fma_f32 v[228:229], v[72:73], v[72:73], v[228:229]\n"
    "v_pk_fma_f32 v[230:231], v[74:75], v[74:75], v[230:231]\n"
    "v_pk_add_f32 v[224:225], v[224:225], v[76:77]\n"
    "v_pk_add_f32 v[226:227], v[226:227], v[78:79]\n"
    "v_pk_fma_f32 v[228:229], v[76:77], v[76:77], v[228:229]\n"
    "v_pk_fma_f32 v[230:231], v[78:79], v[78:79], v[230:231]\n"
    "v_pk_add_f32 v[224:225], v[224:225], v[80:81]\n"
    "v_pk_add_f32 v[226:227], v[226:227], v[82:83]\n"
    "v_pk_fma_f32 v[228:229], v[80:81], v[80:81], v[228:229]\n"
    "v_pk_fma_f32 v[230:231], v[82:83], v[82:83], v[230:231]\n"
    "v_pk_add_f32 v[224:225], v[224:225], v[84:85]\n"
    "v_pk_add_f32 v[226:227], v[226:227], v[86:87]\n"
    "v_pk_fma_f32 v[228:229], v[84:85], v[84:85], v[228:229]\n"
    "v_pk_fma_f32 v[230:231], v[86:87], v[86:87], v[230:231]\n"
    "s_waitcnt lgkmcnt(1)\n"
    "v_smfmac_f32_16x16x64_bf16 v[88:91], v[160:163], v[152:159], v240\n"
    "s_waitcnt lgkmcnt(0)\n"
    "v_smfmac_f32_16x16x64_bf16 v[92:95], v[164:167], v[152:159], v240 abid:1\n"
    "s_nop 0\n"
    "s_nop 5\n"
    "v_pk_add_f32 v[224:225], v[224:225], v[88:89]\n"
    "v_pk_add_f32 v[226:227], v[226:227], v[90:91]\n"
    "v_pk_fma_f32 v[228:229], v[88:89], v[88:89], v[228:229]\n"
    "v_pk_fma_f32 v[230:231], v[90:91], v[90:91], v[230:231]\n"
    "v_pk_add_f32 v[224:225], v[224:225], v[92:93]\n"
    "v_pk_add_f32 v[226:227], v[226:227], v[94:95]\n"
    "v_pk_fma_f32 v[228:229], v[92:93], v[92:93], v[228:229]\n"
    "v_pk_fma_f32 v[230:231], v[94:95], v[94:95], v[230:231]\n"
    "ds_write_b128 v254, v[224:227]\n"
    "ds_write_b128 v254, v[228:231] offset:1024\n"
    "v_add_u32_e64 v254, s56, v242\n"
    "s_waitcnt lgkmcnt(0)\n"
    "s_barrier\n"
    "ds_read_b128 v[128:131], v254 offset:0\n"
    "ds_read_b128 v[132:135], v254 offset:6752\n"
    "ds_read_b128 v[136:139], v254 offset:13504\n"
    "ds_read_b128 v[140:143], v254 offset:20256\n"
    "ds_read_b128 v[144:147], v254 offset:1024\n"
    "ds_read_b128 v[148:151], v254 offset:7776\n"
    "ds_read_b128 v[152:155], v254 offset:14528\n"
    "ds_read_b128 v[156:159], v254 offset:21280\n"
    "ds_write_b128 v234, v[112:115] offset:0\n"
    "ds_write_b128 v234, v[116:119] offset:16\n"
    "s_waitcnt lgkmcnt(8)\n"
    "v_pk_add_f32 v[128:129], v[128:129], v[132:133]\n"
    "v_pk_add_f32 v[130:131], v[130:131], v[134:135]\n"
    "ds_write_b128 v207, v[168:171] offset:0\n"
    "s_waitcnt lgkmcnt(6)\n"
    "v_pk_add_f32 v[136:137], v[136:137], v[140:141]\n"
    "v_pk_add_f32 v[138:139], v[138:139], v[142:143]\n"
    "s_waitcnt lgkmcnt(5)\n"
    "v_pk_add_f32 v[144:145], v[144:145], v[148:149]\n"
    "v_pk_add_f32 v[146:147], v[146:147], v[150:151]\n"
    "v_pk_add_f32 v[128:129], v[128:129], v[136:137]\n"
    "v_pk_add_f32 v[130:131], v[130:131], v[138:139]\n"
    "v_mul_f32_e32 v128, 0x3c2aaaab, v128\n"
    "v_mul_f32_e32 v129, 0x3c2aaaab, v129\n"
    "v_mul_f32_e32 v130, 0x3c2aaaab, v130\n"
    "s_waitcnt lgkmcnt(3)\n"
    "v_pk_add_f32 v[152:153], v[152:153], v[156:157]\n"
    "v_pk_add_f32 v[154:155], v[154:155], v[158:159]\n"
    "v_pk_add_f32 v[144:145], v[144:145], v[152:153]\n"
    "v_pk_add_f32 v[146:147], v[146:147], v[154:155]\n"
    "v_mul_f32_e32 v131, 0x3c2aaaab, v131\n"
    "v_mul_f32_e32 v144, 0x3c2aaaab, v144\n"
    "v_mul_f32_e32 v145, 0x3c2aaaab, v145\n"
    "v_mul_f32_e32 v146, 0x3c2aaaab, v146\n"
    "v_mul_f32_e32 v147, 0x3c2aaaab, v147\n"
    "v_fma_f32 v144, -v128, v128, v144\n"
    "v_fma_f32 v145, -v129, v129, v145\n"
    "v_fma_f32 v146, -v130, v130, v146\n"
    "v_fma_f32 v147, -v131, v131, v147\n"
    "v_max_f32_e64 v144, 0, v144\n"
    "v_max_f32_e64 v145, 0, v145\n"
    "v_max_f32_e64 v146, 0, v146\n"
    "v_max_f32_e64 v147, 0, v147\n"
    "v_add_f32_e32 v144, 0x3727c5ac, v144\n"
    "v_add_f32_e32 v145, 0x3727c5ac, v145\n"
    "v_add_f32_e32 v146, 0x3727c5ac, v146\n"
    "v_add_f32_e32 v147, 0x3727c5ac, v147\n"
    "v_rsq_f32_e64 v144, v144\n"
    "v_rsq_f32_e64 v145, v145\n"
    "v_rsq_f32_e64 v146, v146\n"
    "v_rsq_f32_e64 v147, v147\n"
    "v_mul_f32_e64 v224, s50, v144\n"
    "v_mul_f32_e64 v225, s50, v145\n"
    "v_mul_f32_e64 v226, s50, v146\n"
    "v_mul_f32_e64 v227, s50, v147\n"
    "v_mul_f32_e64 v228, v224, -v128\n"
    "v_mul_f32_e64 v229, v225, -v129\n"
    "v_mul_f32_e64 v230, v226, -v130\n"
    "v_mul_f32_e64 v231, v227, -v131\n"
    "ds_write_b128 v207, v[172:175] offset:4096\n"
    "v_pk_fma_f32 v[64:65], v[64:65], v[224:225], v[228:229]\n"
    "v_pk_fma_f32 v[66:67], v[66:67], v[226:227], v[230:231]\n"
    "v_pk_fma_f32 v[68:69], v[68:69], v[224:225], v[228:229]\n"
    "v_pk_fma_f32 v[70:71], v[70:71], v[226:227], v[230:231]\n"
    "v_pk_fma_f32 v[72:73], v[72:73], v[224:225], v[228:229]\n"
    "v_pk_fma_f32 v[74:75], v[74:75], v[226:227], v[230:231]\n"
    "ds_write_b128 v207, v[176:179] offset:8192\n"
    "v_pk_fma_f32 v[76:77], v[76:77], v[224:225], v[228:229]\n"
    "v_pk_fma_f32 v[78:79], v[78:79], v[226:227], v[230:231]\n"
    "v_pk_fma_f32 v[80:81], v[80:81], v[224:225], v[228:229]\n"
    "v_pk_fma_f32 v[82:83], v[82:83], v[226:227], v[230:231]\n"
    "v_pk_fma_f32 v[84:85], v[84:85], v[224:225], v[228:229]\n"
    "v_pk_fma_f32 v[86:87], v[86:87], v[226:227], v[230:231]\n"
    "ds_write_b128 v207, v[188:191] offset:12288\n"
    "v_pk_fma_f32 v[88:89], v[88:89], v[224:225], v[228:229]\n"
    "v_pk_fma_f32 v[90:91], v[90:91], v[226:227], v[230:231]\n"
    "v_pk_fma_f32 v[92:93], v[92:93], v[224:225], v[228:229]\n"
    "v_pk_fma_f32 v[94:95], v[94:95], v[226:227], v[230:231]\n"
    "v_cvt_pk_bf16_f32 v96, v64, v68\n"
    "v_cvt_pk_bf16_f32 v100, v65, v69\n"
    "ds_write_b128 v207, v[192:195] offset:16384\n"
    "v_cvt_pk_bf16_f32 v104, v66, v70\n"
    "v_cvt_pk_bf16_f32 v108, v67, v71\n"
    "v_cvt_pk_bf16_f32 v97, v72, v76\n"
    "v_cvt_pk_bf16_f32 v101, v73, v77\n"
    "v_cvt_pk_bf16_f32 v105, v74, v78\n"
    "v_cvt_pk_bf16_f32 v109, v75, v79\n"
    "ds_write_b128 v207, v[196:199] offset:20480\n"
    "v_cvt_pk_bf16_f32 v98, v80, v84\n"
    "v_cvt_pk_bf16_f32 v102, v81, v85\n"
    "v_cvt_pk_bf16_f32 v106, v82, v86\n"
    "v_cvt_pk_bf16_f32 v110, v83, v87\n"
    "v_cvt_pk_bf16_f32 v99, v88, v92\n"
    "v_cvt_pk_bf16_f32 v103, v89, v93\n"
    "ds_write_b128 v207, v[180:183] offset:32768\n"
    "v_cvt_pk_bf16_f32 v107, v90, v94\n"
    "v_cvt_pk_bf16_f32 v111, v91, v95\n"
    "v_pk_fma_f32 v[0:1], v[0:1], v[224:225], v[228:229]\n"
    "v_pk_fma_f32 v[2:3], v[2:3], v[226:227], v[230:231]\n"
    "v_pk_fma_f32 v[4:5], v[4:5], v[224:225], v[228:229]\n"
    "v_pk_fma_f32 v[6:7], v[6:7], v[226:227], v[230:231]\n"
    "ds_write_b128 v253, v[96:99] offset:26688\n"
    "v_pk_fma_f32 v[8:9], v[8:9], v[224:225], v[228:229]\n"
    "v_pk_fma_f32 v[10:11], v[10:11], v[226:227], v[230:231]\n"
    "v_pk_fma_f32 v[12:13], v[12:13], v[224:225], v[228:229]\n"
    "v_pk_fma_f32 v[14:15], v[14:15], v[226:227], v[230:231]\n"
    "v_pk_fma_f32 v[16:17], v[16:17], v[224:225], v[228:229]\n"
    "v_pk_fma_f32 v[18:19], v[18:19], v[226:227], v[230:231]\n"
    "ds_write_b128 v253, v[100:103] offset:29760\n"
    "v_pk_fma_f32 v[20:21], v[20:21], v[224:225], v[228:229]\n"
    "v_pk_fma_f32 v[22:23], v[22:23], v[226:227], v[230:231]\n"
    "v_pk_fma_f32 v[24:25], v[24:25], v[224:225], v[228:229]\n"
    "v_pk_fma_f32 v[26:27], v[26:27], v[226:227], v[230:231]\n"
    "v_pk_fma_f32 v[28:29], v[28:29], v[224:225], v[228:229]\n"
    "v_pk_fma_f32 v[30:31], v[30:31], v[226:227], v[230:231]\n"
    "v_cvt_pk_bf16_f32 v80, v0, v4\n"
    "v_cvt_pk_bf16_f32 v84, v1, v5\n"
    "v_cvt_pk_bf16_f32 v88, v2, v6\n"
    "v_cvt_pk_bf16_f32 v92, v3, v7\n"
    "v_cvt_pk_bf16_f32 v81, v8, v12\n"
    "v_cvt_pk_bf16_f32 v85, v9, v13\n"
    "v_cvt_pk_bf16_f32 v89, v10, v14\n"
    "v_cvt_pk_bf16_f32 v93, v11, v15\n"
    "v_cvt_pk_bf16_f32 v82, v16, v20\n"
    "v_cvt_pk_bf16_f32 v86, v17, v21\n"
    "v_cvt_pk_bf16_f32 v90, v18, v22\n"
    "v_cvt_pk_bf16_f32 v94, v19, v23\n"
    "v_cvt_pk_bf16_f32 v83, v24, v28\n"
    "ds_write_b128 v253, v[80:83] offset:24640\n"
    "v_cvt_pk_bf16_f32 v87, v25, v29\n"
    "v_cvt_pk_bf16_f32 v91, v26, v30\n"
    "v_cvt_pk_bf16_f32 v95, v27, v31\n"
    "v_pk_fma_f32 v[32:33], v[32:33], v[224:225], v[228:229]\n"
    "v_pk_fma_f32 v[34:35], v[34:35], v[226:227], v[230:231]\n"
    "v_pk_fma_f32 v[36:37], v[36:37], v[224:225], v[228:229]\n"
    "ds_write_b128 v253, v[84:87] offset:27712\n"
    "v_pk_fma_f32 v[38:39], v[38:39], v[226:227], v[230:231]\n"
    "v_pk_fma_f32 v[40:41], v[40:41], v[224:225], v[228:229]\n"
    "v_pk_fma_f32 v[42:43], v[42:43], v[226:227], v[230:231]\n"
    "v_pk_fma_f32 v[44:45], v[44:45], v[224:225], v[228:229]\n"
    "v_pk_fma_f32 v[46:47], v[46:47], v[226:227], v[230:231]\n"
    "v_pk_fma_f32 v[48:49], v[48:49], v[224:225], v[228:229]\n"
    "v_pk_fma_f32 v[50:51], v[50:51], v[226:227], v[230:231]\n"
    "v_pk_fma_f32 v[52:53], v[52:53], v[224:225], v[228:229]\n"
    "v_pk_fma_f32 v[54:55], v[54:55], v[226:227], v[230:231]\n"
    "v_pk_fma_f32 v[56:57], v[56:57], v[224:225], v[228:229]\n"
    "v_pk_fma_f32 v[58:59], v[58:59], v[226:227], v[230:231]\n"
    "v_pk_fma_f32 v[60:61], v[60:61], v[224:225], v[228:229]\n"
    "v_pk_fma_f32 v[62:63], v[62:63], v[226:227], v[230:231]\n"
    "v_cvt_pk_bf16_f32 v64, v32, v36\n"
    "v_cvt_pk_bf16_f32 v68, v33, v37\n"
    "v_cvt_pk_bf16_f32 v72, v34, v38\n"
    "v_cvt_pk_bf16_f32 v76, v35, v39\n"
    "v_cvt_pk_bf16_f32 v65, v40, v44\n"
    "v_cvt_pk_bf16_f32 v69, v41, v45\n"
    "v_cvt_pk_bf16_f32 v73, v42, v46\n"
    "v_cvt_pk_bf16_f32 v77, v43, v47\n"
    "v_cvt_pk_bf16_f32 v66, v48, v52\n"
    "v_cvt_pk_bf16_f32 v70, v49, v53\n"
    "v_cvt_pk_bf16_f32 v74, v50, v54\n"
    "v_cvt_pk_bf16_f32 v78, v51, v55\n"
    "v_cvt_pk_bf16_f32 v67, v56, v60\n"
    "ds_write_b128 v253, v[64:67] offset:25664\n"
    "v_cvt_pk_bf16_f32 v71, v57, v61\n"
    "v_cvt_pk_bf16_f32 v75, v58, v62\n"
    "v_cvt_pk_bf16_f32 v79, v59, v63\n"
    "ds_write_b128 v253, v[68:71] offset:28736\n"
    "s_waitcnt lgkmcnt(0)\n"
    "s_barrier\n"
    "ds_read_b128 a[224:227], v242 offset:24640\n"
    "ds_read_b128 a[228:231], v242 offset:25664\n"
    "ds_read_b128 a[232:235], v242 offset:26688\n"
    "ds_read_b128 a[236:239], v242 offset:27712\n"
    "ds_read_b128 a[240:243], v242 offset:28736\n"
    "ds_read_b128 a[244:247], v242 offset:29760\n"
    "ds_read_b128 a[252:255], v244 offset:32768\n"
    "s_waitcnt lgkmcnt(6)\n"
    "v_mfma_f32_16x16x32_bf16 v[168:171], a[80:83], a[224:227], 0\n"
    "v_mfma_f32_16x16x32_bf16 v[172:175], a[84:87], a[224:227], 0\n"
    "s_waitcnt lgkmcnt(5)\n"
    "v_mfma_f32_16x16x32_bf16 v[168:171], a[128:131], a[228:231], v[168:171]\n"
    "s_waitcnt lgkmcnt(4)\n"
    "v_mfma_f32_16x16x32_bf16 v[168:171], a[48:51], a[232:235], v[168:171]\n"
    "s_waitcnt lgkmcnt(3)\n"
    "v_mfma_f32_16x16x32_bf16 v[172:175], a[132:135], a[228:231], v[172:175]\n"
    "v_mfma_f32_16x16x32_bf16 v[176:179], a[80:83], a[236:239], 0\n"
    "s_waitcnt lgkmcnt(1)\n"
    "v_mfma_f32_16x16x32_bf16 v[176:179], a[128:131], a[240:243], v[176:179]\n"
    "s_waitcnt lgkmcnt(0)\n"
    "v_mfma_f32_16x16x32_bf16 v[172:175], a[252:255], a[232:235], v[172:175]\n"
    "v_mfma_f32_16x16x32_bf16 v[176:179], a[48:51], a[244:247], v[176:179]\n"
    "v_mfma_f32_16x16x32_bf16 v[180:183], a[84:87], a[236:239], 0\n"
    "v_mfma_f32_16x16x32_bf16 v[180:183], a[132:135], a[240:243], v[180:183]\n"
    "v_mfma_f32_16x16x32_bf16 v[180:183], a[252:255], a[244:247], v[180:183]\n"
    "v_readfirstlane_b32 s66, v232\n"
    "s_nop 1\n"
    "s_cmp_lg_u32 s66, 2\n"
    "s_cbranch_scc1 98f\n"
    "ds_read_b128 a[252:255], v244 offset:36864\n"
    "ds_write_b128 v253, v[72:75] offset:1024\n"
    "v_lshlrev_b32_sdwa v184, 1, v168 dst_sel:DWORD dst_unused:UNUSED_PAD src0_sel:DWORD src1_sel:WORD_1\n"
    "v_lshlrev_b32_sdwa v200, 1, v169 dst_sel:DWORD dst_unused:UNUSED_PAD src0_sel:DWORD src1_sel:WORD_1\n"
    "v_lshlrev_b32_sdwa v185, 1, v170 dst_sel:DWORD dst_unused:UNUSED_PAD src0_sel:DWORD src1_sel:WORD_1\n"
    "v_lshlrev_b32_sdwa v201, 1, v171 dst_sel:DWORD dst_unused:UNUSED_PAD src0_sel:DWORD src1_sel:WORD_1\n"
    "v_lshlrev_b32_sdwa v186, 1, v172 dst_sel:DWORD dst_unused:UNUSED_PAD src0_sel:DWORD src1_sel:WORD_1\n"
    "v_lshlrev_b32_sdwa v202, 1, v173 dst_sel:DWORD dst_unused:UNUSED_PAD src0_sel:DWORD src1_sel:WORD_1\n"
    "v_lshlrev_b32_sdwa v187, 1, v174 dst_sel:DWORD dst_unused:UNUSED_PAD src0_sel:DWORD src1_sel:WORD_1\n"
    "v_mfma_f32_16x16x32_bf16 v[168:171], a[88:91], a[224:227], 0\n"
    "ds_read_u16 v184, v184\n"
    "ds_read_u16_d16_hi v200, v200\n"
    "v_mfma_f32_16x16x32_bf16 v[168:171], a[136:139], a[228:231], v[168:171]\n"
    "ds_read_u16 v185, v185\n"
    "ds_read_u16_d16_hi v201, v201\n"
    "v_mfma_f32_16x16x32_bf16 v[168:171], a[52:55], a[232:235], v[168:171]\n"
    "v_lshlrev_b32_sdwa v203, 1, v175 dst_sel:DWORD dst_unused:UNUSED_PAD src0_sel:DWORD src1_sel:WORD_1\n"
    "v_lshlrev_b32_sdwa v188, 1, v176 dst_sel:DWORD dst_unused:UNUSED_PAD src0_sel:DWORD src1_sel:WORD_1\n"
    "v_mfma_f32_16x16x32_bf16 v[172:175], a[92:95], a[224:227], 0\n"
    "ds_read_u16 v186, v186\n"
    "v_lshlrev_b32_sdwa v204, 1, v177 dst_sel:DWORD dst_unused:UNUSED_PAD src0_sel:DWORD src1_sel:WORD_1\n"
    "v_mfma_f32_16x16x32_bf16 v[172:175], a[140:143], a[228:231], v[172:175]\n"
    "s_nop 0\n"
    "v_lshlrev_b32_sdwa v189, 1, v178 dst_sel:DWORD dst_unused:UNUSED_PAD src0_sel:DWORD src1_sel:WORD_1\n"
    "v_lshlrev_b32_sdwa v205, 1, v179 dst_sel:DWORD dst_unused:UNUSED_PAD src0_sel:DWORD src1_sel:WORD_1\n"
    "v_mfma_f32_16x16x32_bf16 v[176:179], a[88:91], a[236:239], 0\n"
    "s_waitcnt lgkmcnt(6)\n"
    "ds_read_u16_d16_hi v202, v202\n"
    "v_lshlrev_b32_sdwa v190, 1, v180 dst_sel:DWORD dst_unused:UNUSED_PAD src0_sel:DWORD src1_sel:WORD_1\n"
    "v_mfma_f32_16x16x32_bf16 v[176:179], a[136:139], a[240:243], v[176:179]\n"
    "v_lshlrev_b32_sdwa v206, 1, v181 dst_sel:DWORD dst_unused:UNUSED_PAD src0_sel:DWORD src1_sel:WORD_1\n"
    "v_lshlrev_b32_sdwa v191, 1, v182 dst_sel:DWORD dst_unused:UNUSED_PAD src0_sel:DWORD src1_sel:WORD_1\n"
    "v_mfma_f32_16x16x32_bf16 v[176:179], a[52:55], a[244:247], v[176:179]\n"
    "ds_read_u16 v187, v187\n"
    "v_lshlrev_b32_sdwa v207, 1, v183 dst_sel:DWORD dst_unused:UNUSED_PAD src0_sel:DWORD src1_sel:WORD_1\n"
    "v_cvt_pk_f16_f32 v192, v168, v169\n"
    "v_mfma_f32_16x16x32_bf16 v[172:175], a[252:255], a[232:235], v[172:175]\n"
    "ds_read_u16_d16_hi v203, v203\n"
    "v_lshlrev_b32_sdwa v193, 1, v170 dst_sel:DWORD dst_unused:UNUSED_PAD src0_sel:DWORD src1_sel:WORD_1\n"
    "v_pk_fma_f16 v232, v192, v192, s48\n"
    "v_mov_b32_e64 v254, 0\n"
    "v_cvt_pk_f16_f32 v196, v176, v177\n"
    "v_mfma_f32_16x16x32_bf16 v[180:183], a[92:95], a[236:239], 0\n"
    "ds_read_u16 v188, v188\n"
    "v_lshlrev_b32_sdwa v197, 1, v178 dst_sel:DWORD dst_unused:UNUSED_PAD src0_sel:DWORD src1_sel:WORD_1\n"
    "v_mfma_f32_16x16x32_bf16 v[180:183], a[140:143], a[240:243], v[180:183]\n"
    "v_lshlrev_b32_sdwa v194, 1, v172 dst_sel:DWORD dst_unused:UNUSED_PAD src0_sel:DWORD src1_sel:WORD_1\n"
    "v_lshlrev_b32_sdwa v195, 1, v174 dst_sel:DWORD dst_unused:UNUSED_PAD src0_sel:DWORD src1_sel:WORD_1\n"
    "v_mfma_f32_16x16x32_bf16 v[180:183], a[252:255], a[244:247], v[180:183]\n"
    "s_waitcnt lgkmcnt(7)\n"
    "ds_read_u16_d16_hi v204, v204\n"
    "v_or_b32_e32 v184, v184, v200\n"
    "v_pk_mul_f16 v232, v192, v232\n"
    "v_exp_f16_e64 v234, v232\n"
    "ds_read_u16 v189, v189\n"
    "v_exp_f16_sdwa v234, v232 dst_sel:WORD_1 dst_unused:UNUSED_PRESERVE src0_sel:WORD_1\n"
    "ds_write_b128 v253, v[76:79] offset:4096\n"
    "s_waitcnt lgkmcnt(8)\n"
    "v_or_b32_e32 v185, v185, v201\n"
    "v_lshlrev_b32_sdwa v201, 1, v171 dst_sel:DWORD dst_unused:UNUSED_PAD src0_sel:DWORD src1_sel:WORD_1\n"
    "v_lshlrev_b32_sdwa v198, 1, v180 dst_sel:DWORD dst_unused:UNUSED_PAD src0_sel:DWORD src1_sel:WORD_1\n"
    "v_mfma_f32_16x16x32_bf16 v[168:171], a[96:99], a[224:227], 0\n"
    "v_lshlrev_b32_sdwa v199, 1, v182 dst_sel:DWORD dst_unused:UNUSED_PAD src0_sel:DWORD src1_sel:WORD_1\n"
    "v_pk_fma_f16 v234, v234, s49, s49\n"
    "v_rcp_f16_e32 v232, v234\n"
    "s_waitcnt lgkmcnt(6)\n"
    "v_or_b32_e64 v186, v186, v202\n"
    "v_mfma_f32_16x16x32_bf16 v[168:171], a[144:147], a[228:231], v[168:171]\n"
    "v_lshlrev_b32_sdwa v202, 1, v173 dst_sel:DWORD dst_unused:UNUSED_PAD src0_sel:DWORD src1_sel:WORD_1\n"
    "v_mfma_f32_16x16x32_bf16 v[168:171], a[56:59], a[232:235], v[168:171]\n"
    "ds_read_u16_d16_hi v205, v205\n"
    "ds_write_b128 v253, v[88:91] offset:0\n"
    "s_waitcnt lgkmcnt(6)\n"
    "v_or_b32_e32 v187, v187, v203\n"
    "v_lshlrev_b32_sdwa v203, 1, v175 dst_sel:DWORD dst_unused:UNUSED_PAD src0_sel:DWORD src1_sel:WORD_1\n"
    "v_rcp_f16_sdwa v232, v234 dst_sel:WORD_1 dst_unused:UNUSED_PRESERVE src0_sel:WORD_1\n"
    "v_mfma_f32_16x16x32_bf16 v[172:175], a[100:103], a[224:227], 0\n"
    "v_fma_mix_f32 v234, v192, v232, 0 op_sel_hi:[1,1,0]\n"
    "v_lshlrev_b32_sdwa v200, 1, v169 dst_sel:DWORD dst_unused:UNUSED_PAD src0_sel:DWORD src1_sel:WORD_1\n"
    "v_mfma_f32_16x16x32_bf16 v[172:175], a[148:151], a[228:231], v[172:175]\n"
    "ds_read_u16 v190, v190\n"
    "v_fma_mix_f32 v232, v192, v232, 0 op_sel:[1,1,0] op_sel_hi:[1,1,0]\n"
    "v_mfma_f32_16x16x32_bf16 v[172:175], a[60:63], a[232:235], v[172:175]\n"
    "s_waitcnt lgkmcnt(5)\n"
    "ds_read_u16_d16_hi v206, v206\n"
    "ds_write_b128 v253, v[92:95] offset:3072\n"
    "v_or_b32_e32 v188, v188, v204\n"
    "v_cvt_pk_bf16_f32 v192, v234, v232\n"
    "v_pk_fma_f16 v232, v196, v196, s48\n"
    "v_pk_mul_f16 v232, v196, v232\n"
    "v_exp_f16_e64 v234, v232\n"
    "ds_read_u16 v191, v191\n"
    "ds_write_b128 v253, v[104:107] offset:2048\n"
    "s_waitcnt lgkmcnt(5)\n"
    "v_or_b32_e32 v189, v189, v205\n"
    "v_lshlrev_b32_sdwa v205, 1, v179 dst_sel:DWORD dst_unused:UNUSED_PAD src0_sel:DWORD src1_sel:WORD_1\n"
    "v_exp_f16_sdwa v234, v232 dst_sel:WORD_1 dst_unused:UNUSED_PRESERVE src0_sel:WORD_1\n"
    "v_mfma_f32_16x16x32_bf16 v[176:179], a[96:99], a[236:239], 0\n"
    "v_pk_fma_f16 v234, v234, s49, s49\n"
    "v_mfma_f32_16x16x32_bf16 v[176:179], a[144:147], a[240:243], v[176:179]\n"
    "ds_read_u16_d16_hi v207, v207\n"
    "v_mfma_f32_16x16x32_bf16 v[176:179], a[56:59], a[244:247], v[176:179]\n"
    "ds_read_u16 v193, v193\n"
    "ds_write_b128 v253, v[108:111] offset:5120\n"
    "s_waitcnt lgkmcnt(5)\n"
    "v_or_b32_e32 v190, v190, v206\n"
    "v_lshlrev_b32_sdwa v206, 1, v181 dst_sel:DWORD dst_unused:UNUSED_PAD src0_sel:DWORD src1_sel:WORD_1\n"
    "v_rcp_f16_e32 v232, v234\n"
    "v_mfma_f32_16x16x32_bf16 v[96:99], a[0:3], v[184:187], 0\n"
    "s_barrier\n"
    "v_mfma_f32_16x16x32_bf16 v[100:103], a[4:7], v[184:187], 0\n"
    "ds_read_u16_d16_hi v201, v201\n"
    "ds_read_u16 v194, v194\n"
    "v_mfma_f32_16x16x32_bf16 v[112:115], a[8:11], v[184:187], 0\n"
    "ds_read_u16_d16_hi v202, v202\n"
    "ds_read_u16 v195, v195\n"
    "v_lshlrev_b32_sdwa v204, 1, v177 dst_sel:DWORD dst_unused:UNUSED_PAD src0_sel:DWORD src1_sel:WORD_1\n"
    "v_rcp_f16_sdwa v232, v234 dst_sel:WORD_1 dst_unused:UNUSED_PRESERVE src0_sel:WORD_1\n"
    "s_waitcnt lgkmcnt(6)\n"
    "v_or_b32_e32 v191, v191, v207\n"
    "v_mfma_f32_16x16x32_bf16 v[116:119], a[12:15], v[184:187], 0\n"
    "ds_read_b128 v[236:239], v244 offset:0\n"
    "v_lshlrev_b32_sdwa v207, 1, v183 dst_sel:DWORD dst_unused:UNUSED_PAD src0_sel:DWORD src1_sel:WORD_1\n"
    "v_mfma_f32_16x16x32_bf16 v[180:183], a[100:103], a[236:239], 0\n"
    "ds_read_u16_d16_hi v203, v203\n"
    "v_fma_mix_f32 v234, v196, v232, 0 op_sel_hi:[1,1,0]\n"
    "v_mfma_f32_16x16x32_bf16 v[180:183], a[148:151], a[240:243], v[180:183]\n"
    "v_fma_mix_f32 v232, v196, v232, 0 op_sel:[1,1,0] op_sel_hi:[1,1,0]\n"
    "v_cvt_pk_bf16_f32 v196, v234, v232\n"
    "v_mfma_f32_16x16x32_bf16 v[180:183], a[60:63], a[244:247], v[180:183]\n"
    "ds_read_b128 v[164:167], v244 offset:2048\n"
    "v_mfma_f32_16x16x32_bf16 v[104:107], a[0:3], v[188:191], 0\n"
    "ds_read_u16 v197, v197\n"
    "v_mfma_f32_16x16x32_bf16 v[108:111], a[4:7], v[188:191], 0\n"
    "v_mfma_f32_16x16x32_bf16 v[120:123], a[8:11], v[188:191], 0\n"
    "ds_read_u16_d16_hi v205, v205\n"
    "v_mfma_f32_16x16x32_bf16 v[124:127], a[12:15], v[188:191], 0\n"
    "s_waitcnt lgkmcnt(8)\n"
    "ds_read_u16 v198, v198\n"
    "v_or_b32_e32 v193, v193, v201\n"
    "v_lshlrev_b32_sdwa v201, 1, v171 dst_sel:DWORD dst_unused:UNUSED_PAD src0_sel:DWORD src1_sel:WORD_1\n"
    "ds_read_u16_d16_hi v206, v206\n"
    "ds_read_u16 v199, v199\n"
    "ds_read_u16_d16_hi v207, v207\n"
    "ds_read_u16_d16_hi v200, v200\n"
    "ds_read_u16_d16_hi v201, v201\n"
    "ds_read_u16_d16_hi v204, v204\n"
    "ds_read_b128 a[252:255], v244 offset:34816\n"
    "s_waitcnt lgkmcnt(14)\n"
    "v_or_b32_e32 v194, v194, v202\n"
    "v_lshlrev_b32_sdwa v202, 1, v173 dst_sel:DWORD dst_unused:UNUSED_PAD src0_sel:DWORD src1_sel:WORD_1\n"
    "ds_read_u16_d16_hi v202, v202\n"
    "s_waitcnt lgkmcnt(13)\n"
    "v_mfma_f32_16x16x32_bf16 v[128:131], v[236:239], v[184:187], 0\n"
    "s_nop 0\n"
    "v_mfma_f32_16x16x32_bf16 v[136:139], v[236:239], v[188:191], 0\n"
    "ds_read_b128 v[236:239], v244 offset:4096\n"
    "s_waitcnt lgkmcnt(12)\n"
    "v_mfma_f32_16x16x32_bf16 v[132:135], v[164:167], v[184:187], 0\n"
    "v_or_b32_e32 v195, v195, v203\n"
    "v_lshlrev_b32_sdwa v184, 1, v168 dst_sel:DWORD dst_unused:UNUSED_PAD src0_sel:DWORD src1_sel:WORD_1\n"
    "v_mfma_f32_16x16x32_bf16 v[140:143], v[164:167], v[188:191], 0\n"
    "v_lshlrev_b32_sdwa v185, 1, v170 dst_sel:DWORD dst_unused:UNUSED_PAD src0_sel:DWORD src1_sel:WORD_1\n"
    "v_lshlrev_b32_sdwa v186, 1, v172 dst_sel:DWORD dst_unused:UNUSED_PAD src0_sel:DWORD src1_sel:WORD_1\n"
    "v_mfma_f32_16x16x32_bf16 v[168:171], a[104:107], a[224:227], 0\n"
    "v_lshlrev_b32_sdwa v187, 1, v174 dst_sel:DWORD dst_unused:UNUSED_PAD src0_sel:DWORD src1_sel:WORD_1\n"
    "v_lshlrev_b32_sdwa v203, 1, v175 dst_sel:DWORD dst_unused:UNUSED_PAD src0_sel:DWORD src1_sel:WORD_1\n"
    "v_mfma_f32_16x16x32_bf16 v[168:171], a[152:155], a[228:231], v[168:171]\n"
    "v_lshlrev_b32_sdwa v188, 1, v176 dst_sel:DWORD dst_unused:UNUSED_PAD src0_sel:DWORD src1_sel:WORD_1\n"
    "v_lshlrev_b32_sdwa v189, 1, v178 dst_sel:DWORD dst_unused:UNUSED_PAD src0_sel:DWORD src1_sel:WORD_1\n"
    "v_mfma_f32_16x16x32_bf16 v[168:171], a[64:67], a[232:235], v[168:171]\n"
    "v_lshlrev_b32_sdwa v190, 1, v180 dst_sel:DWORD dst_unused:UNUSED_PAD src0_sel:DWORD src1_sel:WORD_1\n"
    "v_lshlrev_b32_sdwa v191, 1, v182 dst_sel:DWORD dst_unused:UNUSED_PAD src0_sel:DWORD src1_sel:WORD_1\n"
    "v_mfma_f32_16x16x32_bf16 v[172:175], a[108:111], a[224:227], 0\n"
    "s_waitcnt lgkmcnt(10)\n"
    "v_or_b32_e32 v197, v197, v205\n"
    "v_lshlrev_b32_sdwa v205, 1, v179 dst_sel:DWORD dst_unused:UNUSED_PAD src0_sel:DWORD src1_sel:WORD_1\n"
    "v_mfma_f32_16x16x32_bf16 v[172:175], a[156:159], a[228:231], v[172:175]\n"
    "v_mfma_f32_16x16x32_bf16 v[172:175], a[68:71], a[232:235], v[172:175]\n"
    "buffer_load_dwordx4 v[0:3], v246, s[40:43], s60 offen offset:0\n"
    "buffer_load_dwordx4 v[32:35], v246, s[40:43], s60 offen offset:64\n"
    "buffer_load_dwordx4 v[64:67], v246, s[40:43], s60 offen offset:128\n"
    "v_mfma_f32_16x16x32_bf16 v[176:179], a[104:107], a[236:239], 0\n"
    "s_waitcnt lgkmcnt(8)\n"
    "ds_read_u16 v184, v184\n"
    "v_or_b32_e32 v198, v198, v206\n"
    "v_mfma_f32_16x16x32_bf16 v[176:179], a[152:155], a[240:243], v[176:179]\n"
    "v_lshlrev_b32_sdwa v206, 1, v181 dst_sel:DWORD dst_unused:UNUSED_PAD src0_sel:DWORD src1_sel:WORD_1\n"
    "v_mfma_f32_16x16x32_bf16 v[176:179], a[64:67], a[244:247], v[176:179]\n"
    "ds_read_u16 v185, v185\n"
    "v_mfma_f32_16x16x32_bf16 v[96:99], a[16:19], v[192:195], v[96:99]\n"
    "s_waitcnt lgkmcnt(8)\n"
    "v_or_b32_e32 v199, v199, v207\n"
    "v_lshlrev_b32_sdwa v207, 1, v183 dst_sel:DWORD dst_unused:UNUSED_PAD src0_sel:DWORD src1_sel:WORD_1\n"
    "v_mfma_f32_16x16x32_bf16 v[180:183], a[108:111], a[236:239], 0\n"
    "ds_read_u16 v186, v186\n"
    "v_mfma_f32_16x16x32_bf16 v[180:183], a[156:159], a[240:243], v[180:183]\n"
    "v_mfma_f32_16x16x32_bf16 v[180:183], a[68:71], a[244:247], v[180:183]\n"
    "ds_read_u16 v187, v187\n"
    "v_mfma_f32_16x16x32_bf16 v[100:103], a[20:23], v[192:195], v[100:103]\n"
    "ds_read_u16_d16_hi v203, v203\n"
    "v_mfma_f32_16x16x32_bf16 v[112:115], a[24:27], v[192:195], v[112:115]\n"
    "v_mfma_f32_16x16x32_bf16 v[116:119], a[28:31], v[192:195], v[116:119]\n"
    "ds_read_b128 v[164:167], v244 offset:6144\n"
    "buffer_load_dwordx4 v[20:23], v247, s[40:43], s60 offen offset:0\n"
    "buffer_load_dwordx4 v[52:55], v247, s[40:43], s60 offen offset:64\n"
    "buffer_load_dwordx4 v[84:87], v247, s[40:43], s60 offen offset:128\n"
    "v_mfma_f32_16x16x32_bf16 v[104:107], a[16:19], v[196:199], v[104:107]\n"
    "s_nop 0\n"
    "ds_read_u16 v188, v188\n"
    "v_mfma_f32_16x16x32_bf16 v[108:111], a[20:23], v[196:199], v[108:111]\n"
    "s_waitcnt lgkmcnt(7)\n"
    "ds_read_u16 v189, v189\n"
    "v_mfma_f32_16x16x32_bf16 v[128:131], v[236:239], v[192:195], v[128:131]\n"
    "v_mfma_f32_16x16x32_bf16 v[120:123], a[24:27], v[196:199], v[120:123]\n"
    "ds_read_u16_d16_hi v205, v205\n"
    "s_waitcnt lgkmcnt(8)\n"
    "v_or_b32_e32 v184, v184, v200\n"
    "v_mfma_f32_16x16x32_bf16 v[124:127], a[28:31], v[196:199], v[124:127]\n"
    "s_nop 0\n"
    "v_mfma_f32_16x16x32_bf16 v[136:139], v[236:239], v[196:199], v[136:139]\n"
    "s_waitcnt lgkmcnt(7)\n"
    "ds_read_u16 v190, v190\n"
    "v_or_b32_e64 v185, v185, v201\n"
    "v_lshlrev_b32_sdwa v201, 1, v171 dst_sel:DWORD dst_unused:UNUSED_PAD src0_sel:DWORD src1_sel:WORD_1\n"
    "ds_read_u16_d16_hi v206, v206\n"
    "s_waitcnt lgkmcnt(8)\n"
    "v_or_b32_e32 v186, v186, v202\n"
    "v_lshlrev_b32_sdwa v202, 1, v173 dst_sel:DWORD dst_unused:UNUSED_PAD src0_sel:DWORD src1_sel:WORD_1\n"
    "ds_read_u16 v191, v191\n"
    "ds_read_u16_d16_hi v207, v207\n"
    "ds_read_u16_d16_hi v201, v201\n"
    "buffer_load_dwordx4 v[4:7], v246, s[40:43], s61 offen offset:0\n"
    "buffer_load_dwordx4 v[36:39], v246, s[40:43], s61 offen offset:64\n"
    "buffer_load_dwordx4 v[68:71], v246, s[40:43], s61 offen offset:128\n"
    "ds_read_u16_d16_hi v202, v202\n"
    "ds_read_b128 v[236:239], v244 offset:8192\n"
    "s_waitcnt lgkmcnt(10)\n"
    "v_mfma_f32_16x16x32_bf16 v[132:135], v[164:167], v[192:195], v[132:135]\n"
    "s_nop 0\n"
    "v_cvt_pk_f16_f32 v192, v168, v169\n"
    "v_lshlrev_b32_sdwa v193, 1, v170 dst_sel:DWORD dst_unused:UNUSED_PAD src0_sel:DWORD src1_sel:WORD_1\n"
    "v_mfma_f32_16x16x32_bf16 v[140:143], v[164:167], v[196:199], v[140:143]\n"
    "v_lshlrev_b32_sdwa v194, 1, v172 dst_sel:DWORD dst_unused:UNUSED_PAD src0_sel:DWORD src1_sel:WORD_1\n"
    "v_lshlrev_b32_sdwa v195, 1, v174 dst_sel:DWORD dst_unused:UNUSED_PAD src0_sel:DWORD src1_sel:WORD_1\n"
    "v_mfma_f32_16x16x32_bf16 v[168:171], a[112:115], a[224:227], 0\n"
    "v_cvt_pk_f16_f32 v196, v176, v177\n"
    "v_lshlrev_b32_sdwa v197, 1, v178 dst_sel:DWORD dst_unused:UNUSED_PAD src0_sel:DWORD src1_sel:WORD_1\n"
    "v_mfma_f32_16x16x32_bf16 v[168:171], a[160:163], a[228:231], v[168:171]\n"
    "s_waitcnt lgkmcnt(9)\n"
    "v_lshlrev_b32_sdwa v198, 1, v180 dst_sel:DWORD dst_unused:UNUSED_PAD src0_sel:DWORD src1_sel:WORD_1\n"
    "v_lshlrev_b32_sdwa v199, 1, v182 dst_sel:DWORD dst_unused:UNUSED_PAD src0_sel:DWORD src1_sel:WORD_1\n"
    "v_mfma_f32_16x16x32_bf16 v[168:171], a[72:75], a[232:235], v[168:171]\n"
    "v_or_b32_e32 v187, v187, v203\n"
    "v_lshlrev_b32_sdwa v203, 1, v175 dst_sel:DWORD dst_unused:UNUSED_PAD src0_sel:DWORD src1_sel:WORD_1\n"
    "v_or_b32_e64 v188, v188, v204\n"
    "v_mfma_f32_16x16x32_bf16 v[172:175], a[116:119], a[224:227], 0\n"
    "ds_read_u16 v193, v193\n"
    "v_pk_fma_f16 v232, v192, v192, s48\n"
    "v_mfma_f32_16x16x32_bf16 v[172:175], a[164:167], a[228:231], v[172:175]\n"
    "s_waitcnt lgkmcnt(8)\n"
    "v_or_b32_e32 v189, v189, v205\n"
    "v_lshlrev_b32_sdwa v205, 1, v179 dst_sel:DWORD dst_unused:UNUSED_PAD src0_sel:DWORD src1_sel:WORD_1\n"
    "v_mfma_f32_16x16x32_bf16 v[172:175], a[252:255], a[232:235], v[172:175]\n"
    "ds_read_u16 v194, v194\n"
    "v_pk_mul_f16 v232, v192, v232\n"
    "v_lshlrev_b32_sdwa v200, 1, v169 dst_sel:DWORD dst_unused:UNUSED_PAD src0_sel:DWORD src1_sel:WORD_1\n"
    "v_exp_f16_e64 v234, v232\n"
    "v_mfma_f32_16x16x32_bf16 v[176:179], a[112:115], a[236:239], 0\n"
    "ds_read_u16 v195, v195\n"
    "v_mfma_f32_16x16x32_bf16 v[176:179], a[160:163], a[240:243], v[176:179]\n"
    "s_waitcnt lgkmcnt(8)\n"
    "v_or_b32_e32 v190, v190, v206\n"
    "v_lshlrev_b32_sdwa v206, 1, v181 dst_sel:DWORD dst_unused:UNUSED_PAD src0_sel:DWORD src1_sel:WORD_1\n"
    "v_mfma_f32_16x16x32_bf16 v[176:179], a[72:75], a[244:247], v[176:179]\n"
    "ds_read_u16_d16_hi v203, v203\n"
    "buffer_load_dwordx4 v[24:27], v247, s[40:43], s61 offen offset:0\n"
    "buffer_load_dwordx4 v[56:59], v247, s[40:43], s61 offen offset:64\n"
    "buffer_load_dwordx4 v[88:91], v247, s[40:43], s61 offen offset:128\n"
    "v_mfma_f32_16x16x32_bf16 v[96:99], a[32:35], v[184:187], v[96:99]\n"
    "ds_read_b128 v[164:167], v244 offset:10240\n"
    "v_exp_f16_sdwa v234, v232 dst_sel:WORD_1 dst_unused:UNUSED_PRESERVE src0_sel:WORD_1\n"
    "v_mfma_f32_16x16x32_bf16 v[100:103], a[36:39], v[184:187], v[100:103]\n"
    "s_waitcnt lgkmcnt(8)\n"
    "ds_read_u16 v197, v197\n"
    "v_or_b32_e32 v191, v191, v207\n"
    "v_lshlrev_b32_sdwa v207, 1, v183 dst_sel:DWORD dst_unused:UNUSED_PAD src0_sel:DWORD src1_sel:WORD_1\n"
    "v_mfma_f32_16x16x32_bf16 v[180:183], a[116:119], a[236:239], 0\n"
    "ds_read_u16_d16_hi v205, v205\n"
    "v_pk_fma_f16 v234, v234, s49, s49\n"
    "v_mfma_f32_16x16x32_bf16 v[180:183], a[164:167], a[240:243], v[180:183]\n"
    "s_waitcnt lgkmcnt(7)\n"
    "v_rcp_f16_e32 v232, v234\n"
    "v_mfma_f32_16x16x32_bf16 v[180:183], a[252:255], a[244:247], v[180:183]\n"
    "ds_read_u16 v198, v198\n"
    "v_rcp_f16_sdwa v232, v234 dst_sel:WORD_1 dst_unused:UNUSED_PRESERVE src0_sel:WORD_1\n"
    "v_mfma_f32_16x16x32_bf16 v[112:115], a[40:43], v[184:187], v[112:115]\n"
    "v_lshlrev_b32_sdwa v204, 1, v177 dst_sel:DWORD dst_unused:UNUSED_PAD src0_sel:DWORD src1_sel:WORD_1\n"
    "v_fma_mix_f32 v234, v192, v232, 0 op_sel_hi:[1,1,0]\n"
    "v_mfma_f32_16x16x32_bf16 v[116:119], a[44:47], v[184:187], v[116:119]\n"
    "s_waitcnt lgkmcnt(7)\n"
    "ds_read_u16_d16_hi v206, v206\n"
    "v_or_b32_e32 v193, v193, v201\n"
    "v_mfma_f32_16x16x32_bf16 v[128:131], v[236:239], v[184:187], v[128:131]\n"
    "s_nop 0\n"
    "ds_read_u16 v199, v199\n"
    "v_lshlrev_b32_sdwa v201, 1, v171 dst_sel:DWORD dst_unused:UNUSED_PAD src0_sel:DWORD src1_sel:WORD_1\n"
    "v_mfma_f32_16x16x32_bf16 v[104:107], a[32:35], v[188:191], v[104:107]\n"
    "s_waitcnt lgkmcnt(8)\n"
    "buffer_load_dwordx4 v[8:11], v246, s[40:43], s62 offen offset:0\n"
    "buffer_load_dwordx4 v[40:43], v246, s[40:43], s62 offen offset:64\n"
    "buffer_load_dwordx4 v[72:75], v246, s[40:43], s62 offen offset:128\n"
    "ds_read_u16_d16_hi v207, v207\n"
    "v_or_b32_e64 v194, v194, v202\n"
    "v_lshlrev_b32_sdwa v202, 1, v173 dst_sel:DWORD dst_unused:UNUSED_PAD src0_sel:DWORD src1_sel:WORD_1\n"
    "v_fma_mix_f32 v232, v192, v232, 0 op_sel:[1,1,0] op_sel_hi:[1,1,0]\n"
    "v_cvt_pk_bf16_f32 v192, v234, v232\n"
    "v_mfma_f32_16x16x32_bf16 v[108:111], a[36:39], v[188:191], v[108:111]\n"
    "ds_read_b128 a[252:255], v244 offset:38912\n"
    "v_pk_fma_f16 v232, v196, v196, s48\n"
    "v_mfma_f32_16x16x32_bf16 v[120:123], a[40:43], v[188:191], v[120:123]\n"
    "s_waitcnt lgkmcnt(7)\n"
    "ds_read_u16_d16_hi v200, v200\n"
    "v_or_b32_e32 v195, v195, v203\n"
    "v_lshlrev_b32_sdwa v203, 1, v175 dst_sel:DWORD dst_unused:UNUSED_PAD src0_sel:DWORD src1_sel:WORD_1\n"
    "v_pk_mul_f16 v232, v196, v232\n"
    "v_exp_f16_e64 v234, v232\n"
    "v_mfma_f32_16x16x32_bf16 v[132:135], v[164:167], v[184:187], v[132:135]\n"
    "ds_read_u16_d16_hi v201, v201\n"
    "v_lshlrev_b32_sdwa v184, 1, v168 dst_sel:DWORD dst_unused:UNUSED_PAD src0_sel:DWORD src1_sel:WORD_1\n"
    "v_lshlrev_b32_sdwa v185, 1, v170 dst_sel:DWORD dst_unused:UNUSED_PAD src0_sel:DWORD src1_sel:WORD_1\n"
    "v_lshlrev_b32_sdwa v186, 1, v172 dst_sel:DWORD dst_unused:UNUSED_PAD src0_sel:DWORD src1_sel:WORD_1\n"
    "v_mfma_f32_16x16x32_bf16 v[124:127], a[44:47], v[188:191], v[124:127]\n"
    "ds_read_u16 v184, v184\n"
    "v_lshlrev_b32_sdwa v187, 1, v174 dst_sel:DWORD dst_unused:UNUSED_PAD src0_sel:DWORD src1_sel:WORD_1\n"
    "v_exp_f16_sdwa v234, v232 dst_sel:WORD_1 dst_unused:UNUSED_PRESERVE src0_sel:WORD_1\n"
    "v_mfma_f32_16x16x32_bf16 v[136:139], v[236:239], v[188:191], v[136:139]\n"
    "s_waitcnt lgkmcnt(8)\n"
    "v_or_b32_e32 v197, v197, v205\n"
    "v_lshlrev_b32_sdwa v205, 1, v179 dst_sel:DWORD dst_unused:UNUSED_PAD src0_sel:DWORD src1_sel:WORD_1\n"
    "v_mfma_f32_16x16x32_bf16 v[140:143], v[164:167], v[188:191], v[140:143]\n"
    "ds_read_u16 v185, v185\n"
    "v_lshlrev_b32_sdwa v188, 1, v176 dst_sel:DWORD dst_unused:UNUSED_PAD src0_sel:DWORD src1_sel:WORD_1\n"
    "v_mfma_f32_16x16x32_bf16 v[168:171], a[120:123], a[224:227], 0\n"
    "s_nop 0\n"
    "ds_read_u16 v186, v186\n"
    "v_lshlrev_b32_sdwa v189, 1, v178 dst_sel:DWORD dst_unused:UNUSED_PAD src0_sel:DWORD src1_sel:WORD_1\n"
    "v_mfma_f32_16x16x32_bf16 v[168:171], a[168:171], a[228:231], v[168:171]\n"
    "s_waitcnt lgkmcnt(8)\n"
    "buffer_load_dwordx4 v[152:155], v245, s[40:43], s57 offen offset:64\n"
    "buffer_load_dwordx4 v[144:147], v245, s[40:43], s57 offen offset:0\n"
    "v_mfma_f32_16x16x32_bf16 v[168:171], a[76:79], a[232:235], v[168:171]\n"
    "ds_read_u16_d16_hi v202, v202\n"
    "v_or_b32_e64 v198, v198, v206\n"
    "v_lshlrev_b32_sdwa v190, 1, v180 dst_sel:DWORD dst_unused:UNUSED_PAD src0_sel:DWORD src1_sel:WORD_1\n"
    "v_lshlrev_b32_sdwa v206, 1, v181 dst_sel:DWORD dst_unused:UNUSED_PAD src0_sel:DWORD src1_sel:WORD_1\n"
    "v_mfma_f32_16x16x32_bf16 v[172:175], a[124:127], a[224:227], 0\n"
    "ds_read_b128 v[236:239], v244 offset:12288\n"
    "v_lshlrev_b32_sdwa v191, 1, v182 dst_sel:DWORD dst_unused:UNUSED_PAD src0_sel:DWORD src1_sel:WORD_1\n"
    "v_mfma_f32_16x16x32_bf16 v[172:175], a[172:175], a[228:231], v[172:175]\n"
    "s_waitcnt lgkmcnt(7)\n"
    "ds_read_u16 v187, v187\n"
    "v_or_b32_e32 v199, v199, v207\n"
    "v_mfma_f32_16x16x32_bf16 v[172:175], a[252:255], a[232:235], v[172:175]\n"
    "v_lshlrev_b32_sdwa v207, 1, v183 dst_sel:DWORD dst_unused:UNUSED_PAD src0_sel:DWORD src1_sel:WORD_1\n"
    "v_pk_fma_f16 v234, v234, s49, s49\n"
    "v_mfma_f32_16x16x32_bf16 v[176:179], a[120:123], a[236:239], 0\n"
    "ds_read_u16_d16_hi v203, v203\n"
    "v_mfma_f32_16x16x32_bf16 v[176:179], a[168:171], a[240:243], v[176:179]\n"
    "v_rcp_f16_e64 v232, v234\n"
    "v_mfma_f32_16x16x32_bf16 v[176:179], a[76:79], a[244:247], v[176:179]\n"
    "ds_read_b128 v[164:167], v244 offset:14336\n"
    "v_rcp_f16_sdwa v232, v234 dst_sel:WORD_1 dst_unused:UNUSED_PRESERVE src0_sel:WORD_1\n"
    "v_mfma_f32_16x16x32_bf16 v[180:183], a[124:127], a[236:239], 0\n"
    "s_nop 0\n"
    "ds_read_u16 v188, v188\n"
    "v_fma_mix_f32 v234, v196, v232, 0 op_sel_hi:[1,1,0]\n"
    "v_mfma_f32_16x16x32_bf16 v[180:183], a[172:175], a[240:243], v[180:183]\n"
    "s_waitcnt lgkmcnt(8)\n"
    "ds_read_u16_d16_hi v204, v204\n"
    "v_fma_mix_f32 v232, v196, v232, 0 op_sel:[1,1,0] op_sel_hi:[1,1,0]\n"
    "v_mfma_f32_16x16x32_bf16 v[180:183], a[252:255], a[244:247], v[180:183]\n"
    "buffer_load_dwordx4 v[12:15], v246, s[40:43], s63 offen offset:0\n"
    "buffer_load_dwordx4 v[44:47], v246, s[40:43], s63 offen offset:64\n"
    "buffer_load_dwordx4 v[76:79], v246, s[40:43], s63 offen offset:128\n"
    "ds_read_u16 v189, v189\n"
    "v_cvt_pk_bf16_f32 v196, v234, v232\n"
    "v_or_b32_e64 v184, v184, v200\n"
    "v_mfma_f32_16x16x32_bf16 v[96:99], a[176:179], v[192:195], v[96:99]\n"
    "s_waitcnt lgkmcnt(8)\n"
    "v_or_b32_e32 v185, v185, v201\n"
    "v_lshlrev_b32_sdwa v201, 1, v171 dst_sel:DWORD dst_unused:UNUSED_PAD src0_sel:DWORD src1_sel:WORD_1\n"
    "v_mfma_f32_16x16x32_bf16 v[100:103], a[180:183], v[192:195], v[100:103]\n"
    "ds_read_u16_d16_hi v205, v205\n"
    "v_mfma_f32_16x16x32_bf16 v[112:115], a[184:187], v[192:195], v[112:115]\n"
    "s_waitcnt lgkmcnt(7)\n"
    "v_or_b32_e32 v186, v186, v202\n"
    "v_lshlrev_b32_sdwa v202, 1, v173 dst_sel:DWORD dst_unused:UNUSED_PAD src0_sel:DWORD src1_sel:WORD_1\n"
    "v_mfma_f32_16x16x32_bf16 v[116:119], a[188:191], v[192:195], v[116:119]\n"
    "ds_read_u16 v190, v190\n"
    "v_mfma_f32_16x16x32_bf16 v[128:131], v[236:239], v[192:195], v[128:131]\n"
    "ds_read_b128 a[224:227], v243 offset:24640\n"
    "v_mfma_f32_16x16x32_bf16 v[104:107], a[176:179], v[196:199], v[104:107]\n"
    "ds_read_u16_d16_hi v206, v206\n"
    "v_mfma_f32_16x16x32_bf16 v[108:111], a[180:183], v[196:199], v[108:111]\n"
    "s_waitcnt lgkmcnt(7)\n"
    "v_or_b32_e32 v187, v187, v203\n"
    "v_lshlrev_b32_sdwa v203, 1, v175 dst_sel:DWORD dst_unused:UNUSED_PAD src0_sel:DWORD src1_sel:WORD_1\n"
    "v_mfma_f32_16x16x32_bf16 v[132:135], v[164:167], v[192:195], v[132:135]\n"
    "ds_read_b128 a[228:231], v243 offset:25664\n"
    "v_cvt_pk_f16_f32 v192, v168, v169\n"
    "v_mfma_f32_16x16x32_bf16 v[120:123], a[184:187], v[196:199], v[120:123]\n"
    "buffer_load_dwordx4 v[16:19], v246, s[40:43], s52 offen offset:0\n"
    "buffer_load_dwordx4 v[48:51], v246, s[40:43], s52 offen offset:64\n"
    "buffer_load_dwordx4 v[80:83], v246, s[40:43], s52 offen offset:128\n"
    "ds_read_b128 a[232:235], v243 offset:26688\n"
    "v_mfma_f32_16x16x32_bf16 v[124:127], a[188:191], v[196:199], v[124:127]\n"
    "ds_read_u16 v191, v191\n"
    "v_lshlrev_b32_sdwa v193, 1, v170 dst_sel:DWORD dst_unused:UNUSED_PAD src0_sel:DWORD src1_sel:WORD_1\n"
    "v_mfma_f32_16x16x32_bf16 v[136:139], v[236:239], v[196:199], v[136:139]\n"
    "s_nop 0\n"
    "ds_read_b128 a[236:239], v243 offset:27712\n"
    "v_lshlrev_b32_sdwa v194, 1, v172 dst_sel:DWORD dst_unused:UNUSED_PAD src0_sel:DWORD src1_sel:WORD_1\n"
    "v_mfma_f32_16x16x32_bf16 v[140:143], v[164:167], v[196:199], v[140:143]\n"
    "s_waitcnt lgkmcnt(9)\n"
    "ds_read_u16_d16_hi v207, v207\n"
    "v_lshlrev_b32_sdwa v195, 1, v174 dst_sel:DWORD dst_unused:UNUSED_PAD src0_sel:DWORD src1_sel:WORD_1\n"
    "v_cvt_pk_f16_f32 v196, v176, v177\n"
    "v_lshlrev_b32_sdwa v197, 1, v178 dst_sel:DWORD dst_unused:UNUSED_PAD src0_sel:DWORD src1_sel:WORD_1\n"
    "v_lshlrev_b32_sdwa v198, 1, v180 dst_sel:DWORD dst_unused:UNUSED_PAD src0_sel:DWORD src1_sel:WORD_1\n"
    "v_lshlrev_b32_sdwa v199, 1, v182 dst_sel:DWORD dst_unused:UNUSED_PAD src0_sel:DWORD src1_sel:WORD_1\n"
    "v_or_b32_e64 v188, v188, v204\n"
    "ds_read_b128 a[240:243], v243 offset:28736\n"
    "v_pk_fma_f16 v232, v192, v192, s48\n"
    "s_waitcnt lgkmcnt(9)\n"
    "v_or_b32_e32 v189, v189, v205\n"
    "ds_read_b128 a[244:247], v243 offset:29760\n"
    "ds_read_u16 v193, v193\n"
    "v_lshlrev_b32_sdwa v205, 1, v179 dst_sel:DWORD dst_unused:UNUSED_PAD src0_sel:DWORD src1_sel:WORD_1\n"
    "v_pk_mul_f16 v232, v192, v232\n"
    "v_exp_f16_e64 v234, v232\n"
    "s_waitcnt lgkmcnt(9)\n"
    "v_mfma_f32_16x16x32_bf16 v[168:171], a[80:83], a[224:227], 0\n"
    "ds_read_b128 a[252:255], v244 offset:32768\n"
    "v_exp_f16_sdwa v234, v232 dst_sel:WORD_1 dst_unused:UNUSED_PRESERVE src0_sel:WORD_1\n"
    "v_mfma_f32_16x16x32_bf16 v[172:175], a[84:87], a[224:227], 0\n"
    "s_waitcnt lgkmcnt(8)\n"
    "buffer_load_dwordx4 v[156:159], v245, s[40:43], s88 offen offset:64\n"
    "buffer_load_dwordx4 v[148:151], v245, s[40:43], s88 offen offset:0\n"
    "v_mfma_f32_16x16x32_bf16 v[172:175], a[132:135], a[228:231], v[172:175]\n"
    "ds_read_u16_d16_hi v201, v201\n"
    "ds_read_u16 v194, v194\n"
    "v_or_b32_e64 v190, v190, v206\n"
    "v_lshlrev_b32_sdwa v206, 1, v181 dst_sel:DWORD dst_unused:UNUSED_PAD src0_sel:DWORD src1_sel:WORD_1\n"
    "v_pk_fma_f16 v234, v234, s49, s49\n"
    "v_mfma_f32_16x16x32_bf16 v[168:171], a[128:131], a[228:231], v[168:171]\n"
    "s_waitcnt lgkmcnt(9)\n"
    "ds_read_u16_d16_hi v202, v202\n"
    "v_mfma_f32_16x16x32_bf16 v[168:171], a[48:51], a[232:235], v[168:171]\n"
    "s_waitcnt lgkmcnt(8)\n"
    "v_rcp_f16_e64 v232, v234\n"
    "v_mfma_f32_16x16x32_bf16 v[176:179], a[80:83], a[236:239], 0\n"
    "ds_read_u16 v195, v195\n"
    "v_rcp_f16_sdwa v232, v234 dst_sel:WORD_1 dst_unused:UNUSED_PRESERVE src0_sel:WORD_1\n"
    "v_mfma_f32_16x16x32_bf16 v[96:99], a[192:195], v[184:187], v[96:99]\n"
    "v_fma_mix_f32 v234, v192, v232, 0 op_sel_hi:[1,1,0]\n"
    "v_fma_mix_f32 v232, v192, v232, 0 op_sel:[1,1,0] op_sel_hi:[1,1,0]\n"
    "v_mfma_f32_16x16x32_bf16 v[100:103], a[196:199], v[184:187], v[100:103]\n"
    "s_waitcnt lgkmcnt(7)\n"
    "ds_read_b128 v[236:239], v244 offset:16384\n"
    "ds_read_u16_d16_hi v203, v203\n"
    "v_or_b32_e32 v191, v191, v207\n"
    "v_lshlrev_b32_sdwa v207, 1, v183 dst_sel:DWORD dst_unused:UNUSED_PAD src0_sel:DWORD src1_sel:WORD_1\n"
    "v_lshlrev_b32_sdwa v200, 1, v169 dst_sel:DWORD dst_unused:UNUSED_PAD src0_sel:DWORD src1_sel:WORD_1\n"
    "v_mfma_f32_16x16x32_bf16 v[176:179], a[128:131], a[240:243], v[176:179]\n"
    "s_waitcnt lgkmcnt(8)\n"
    "v_cvt_pk_bf16_f32 v192, v234, v232\n"
    "v_pk_fma_f16 v232, v196, v196, s48\n"
    "v_mfma_f32_16x16x32_bf16 v[176:179], a[48:51], a[244:247], v[176:179]\n"
    "ds_read_b128 v[164:167], v244 offset:18432\n"
    "ds_read_u16 v197, v197\n"
    "v_mfma_f32_16x16x32_bf16 v[180:183], a[84:87], a[236:239], 0\n"
    "s_waitcnt lgkmcnt(8)\n"
    "v_pk_mul_f16 v232, v196, v232\n"
    "v_mfma_f32_16x16x32_bf16 v[180:183], a[132:135], a[240:243], v[180:183]\n"
    "ds_read_u16_d16_hi v205, v205\n"
    "v_mfma_f32_16x16x32_bf16 v[180:183], a[252:255], a[244:247], v[180:183]\n"
    "v_lshlrev_b32_sdwa v204, 1, v177 dst_sel:DWORD dst_unused:UNUSED_PAD src0_sel:DWORD src1_sel:WORD_1\n"
    "v_exp_f16_e32 v234, v232\n"
    "v_mfma_f32_16x16x32_bf16 v[172:175], a[252:255], a[232:235], v[172:175]\n"
    "s_waitcnt lgkmcnt(8)\n"
    "ds_read_u16 v198, v198\n"
    "v_or_b32_e64 v193, v193, v201\n"
    "v_mfma_f32_16x16x32_bf16 v[112:115], a[200:203], v[184:187], v[112:115]\n"
    "v_lshlrev_b32_sdwa v201, 1, v171 dst_sel:DWORD dst_unused:UNUSED_PAD src0_sel:DWORD src1_sel:WORD_1\n"
    "v_exp_f16_sdwa v234, v232 dst_sel:WORD_1 dst_unused:UNUSED_PRESERVE src0_sel:WORD_1\n"
    "v_mfma_f32_16x16x32_bf16 v[116:119], a[204:207], v[184:187], v[116:119]\n"
    "ds_read_u16_d16_hi v206, v206\n"
    "v_pk_fma_f16 v234, v234, s49, s49\n"
    "v_mfma_f32_16x16x32_bf16 v[104:107], a[192:195], v[188:191], v[104:107]\n"
    "s_waitcnt lgkmcnt(8)\n"
    "ds_read_u16 v199, v199\n"
    "v_or_b32_e32 v194, v194, v202\n"
    "v_lshlrev_b32_sdwa v202, 1, v173 dst_sel:DWORD dst_unused:UNUSED_PAD src0_sel:DWORD src1_sel:WORD_1\n"
    "v_rcp_f16_e32 v232, v234\n"
    "v_mfma_f32_16x16x32_bf16 v[108:111], a[196:199], v[188:191], v[108:111]\n"
    "s_waitcnt lgkmcnt(7)\n"
    "ds_read_u16_d16_hi v207, v207\n"
    "v_mfma_f32_16x16x32_bf16 v[128:131], v[236:239], v[184:187], v[128:131]\n"
    "s_nop 0\n"
    "v_mfma_f32_16x16x32_bf16 v[120:123], a[200:203], v[188:191], v[120:123]\n"
    "s_waitcnt lgkmcnt(6)\n"
    "ds_read_b128 a[252:255], v244 offset:36864\n"
    "v_or_b32_e64 v195, v195, v203\n"
    "v_mfma_f32_16x16x32_bf16 v[132:135], v[164:167], v[184:187], v[132:135]\n"
    "ds_read_u16_d16_hi v200, v200\n"
    "v_lshlrev_b32_sdwa v184, 1, v168 dst_sel:DWORD dst_unused:UNUSED_PAD src0_sel:DWORD src1_sel:WORD_1\n"
    "v_lshlrev_b32_sdwa v185, 1, v170 dst_sel:DWORD dst_unused:UNUSED_PAD src0_sel:DWORD src1_sel:WORD_1\n"
    "v_lshlrev_b32_sdwa v186, 1, v172 dst_sel:DWORD dst_unused:UNUSED_PAD src0_sel:DWORD src1_sel:WORD_1\n"
    "s_branch 2f\n"
    ".p2align 6\n"
    "1:\n"
    ".Lreplk_loop:\n"
    "v_lshlrev_b32_sdwa v184, 1, v168 dst_sel:DWORD dst_unused:UNUSED_PAD src0_sel:DWORD src1_sel:WORD_1\n"
    "v_lshlrev_b32_sdwa v200, 1, v169 dst_sel:DWORD dst_unused:UNUSED_PAD src0_sel:DWORD src1_sel:WORD_1\n"
    "v_lshlrev_b32_sdwa v185, 1, v170 dst_sel:DWORD dst_unused:UNUSED_PAD src0_sel:DWORD src1_sel:WORD_1\n"
    "v_lshlrev_b32_sdwa v201, 1, v171 dst_sel:DWORD dst_unused:UNUSED_PAD src0_sel:DWORD src1_sel:WORD_1\n"
    "v_mfma_f32_16x16x32_bf16 v[140:143], v[164:167], v[188:191], v[140:143]\n"
    "s_waitcnt vmcnt(2) lgkmcnt(3)\n"
    "ds_read_u16 v184, v184\n"
    "ds_read_b128 a[252:255], v244 offset:36864\n"
    "v_mfma_f32_16x16x32_bf16 v[168:171], a[88:91], a[224:227], 0\n"
    "ds_read_u16_d16_hi v200, v200\n"
    "ds_read_u16 v185, v185\n"
    "v_mfma_f32_16x16x32_bf16 v[168:171], a[136:139], a[228:231], v[168:171]\n"
    "s_waitcnt lgkmcnt(5)\n"
    "v_lshlrev_b32_sdwa v186, 1, v172 dst_sel:DWORD dst_unused:UNUSED_PAD src0_sel:DWORD src1_sel:WORD_1\n"
    "v_lshlrev_b32_sdwa v202, 1, v173 dst_sel:DWORD dst_unused:UNUSED_PAD src0_sel:DWORD src1_sel:WORD_1\n"
    "v_mfma_f32_16x16x32_bf16 v[168:171], a[52:55], a[232:235], v[168:171]\n"
    "ds_read_u16_d16_hi v201, v201\n"
    "buffer_load_dwordx4 v[0:3], v246, s[40:43], s60 offen offset:0\n"
    "buffer_load_dwordx4 v[32:35], v246, s[40:43], s60 offen offset:64\n"
    "buffer_load_dwordx4 v[64:67], v246, s[40:43], s60 offen offset:128\n"
    "v_smfmac_f32_32x32x32_bf16 v[128:143], a[248:251], v[144:151], v241\n"
    "ds_read_u16 v186, v186\n"
    "v_lshlrev_b32_sdwa v187, 1, v174 dst_sel:DWORD dst_unused:UNUSED_PAD src0_sel:DWORD src1_sel:WORD_1\n"
    "v_lshlrev_b32_sdwa v203, 1, v175 dst_sel:DWORD dst_unused:UNUSED_PAD src0_sel:DWORD src1_sel:WORD_1\n"
    "v_lshlrev_b32_sdwa v188, 1, v176 dst_sel:DWORD dst_unused:UNUSED_PAD src0_sel:DWORD src1_sel:WORD_1\n"
    "v_lshlrev_b32_sdwa v204, 1, v177 dst_sel:DWORD dst_unused:UNUSED_PAD src0_sel:DWORD src1_sel:WORD_1\n"
    "v_mfma_f32_16x16x32_bf16 v[172:175], a[92:95], a[224:227], 0\n"
    "ds_read_u16_d16_hi v202, v202\n"
    "v_lshlrev_b32_sdwa v189, 1, v178 dst_sel:DWORD dst_unused:UNUSED_PAD src0_sel:DWORD src1_sel:WORD_1\n"
    "v_mfma_f32_16x16x32_bf16 v[172:175], a[140:143], a[228:231], v[172:175]\n"
    "ds_read_b128 v[236:239], v244 offset:20480\n"
    "v_lshlrev_b32_sdwa v205, 1, v179 dst_sel:DWORD dst_unused:UNUSED_PAD src0_sel:DWORD src1_sel:WORD_1\n"
    "v_mfma_f32_16x16x32_bf16 v[176:179], a[88:91], a[236:239], 0\n"
    "ds_read_u16 v187, v187\n"
    "v_lshlrev_b32_sdwa v190, 1, v180 dst_sel:DWORD dst_unused:UNUSED_PAD src0_sel:DWORD src1_sel:WORD_1\n"
    "v_mfma_f32_16x16x32_bf16 v[176:179], a[136:139], a[240:243], v[176:179]\n"
    "v_lshlrev_b32_sdwa v191, 1, v182 dst_sel:DWORD dst_unused:UNUSED_PAD src0_sel:DWORD src1_sel:WORD_1\n"
    "v_pk_fma_f16 v234, v234, s49, s49\n"
    "v_mfma_f32_16x16x32_bf16 v[176:179], a[52:55], a[244:247], v[176:179]\n"
    "s_waitcnt lgkmcnt(7)\n"
    "ds_read_u16_d16_hi v203, v203\n"
    "v_or_b32_e32 v198, v198, v206\n"
    "v_lshlrev_b32_sdwa v206, 1, v181 dst_sel:DWORD dst_unused:UNUSED_PAD src0_sel:DWORD src1_sel:WORD_1\n"
    "v_or_b32_e64 v199, v199, v207\n"
    "v_mfma_f32_16x16x32_bf16 v[172:175], a[252:255], a[232:235], v[172:175]\n"
    "ds_read_u16 v188, v188\n"
    "v_lshlrev_b32_sdwa v207, 1, v183 dst_sel:DWORD dst_unused:UNUSED_PAD src0_sel:DWORD src1_sel:WORD_1\n"
    "v_rcp_f16_e64 v232, v234\n"
    "v_mov_b32_e32 v254, 0\n"
    "v_mfma_f32_16x16x32_bf16 v[180:183], a[92:95], a[236:239], 0\n"
    "s_waitcnt lgkmcnt(8)\n"
    "v_rcp_f16_sdwa v232, v234 dst_sel:WORD_1 dst_unused:UNUSED_PRESERVE src0_sel:WORD_1\n"
    "v_mfma_f32_16x16x32_bf16 v[180:183], a[140:143], a[240:243], v[180:183]\n"
    "v_fma_mix_f32 v234, v196, v232, 0 op_sel_hi:[1,1,0]\n"
    "v_fma_mix_f32 v232, v196, v232, 0 op_sel:[1,1,0] op_sel_hi:[1,1,0]\n"
    "v_mfma_f32_16x16x32_bf16 v[180:183], a[252:255], a[244:247], v[180:183]\n"
    "ds_write_b128 v253, v[216:219] offset:2048\n"
    "buffer_load_dwordx4 v[20:23], v247, s[40:43], s60 offen offset:0\n"
    "buffer_load_dwordx4 v[52:55], v247, s[40:43], s60 offen offset:64\n"
    "buffer_load_dwordx4 v[84:87], v247, s[40:43], s60 offen offset:128\n"
    "v_cvt_pk_bf16_f32 v196, v234, v232\n"
    "v_or_b32_e32 v184, v184, v200\n"
    "v_mfma_f32_16x16x32_bf16 v[96:99], a[208:211], v[192:195], v[96:99]\n"
    "s_waitcnt lgkmcnt(6)\n"
    "v_or_b32_e32 v185, v185, v201\n"
    "v_lshlrev_b32_sdwa v201, 1, v171 dst_sel:DWORD dst_unused:UNUSED_PAD src0_sel:DWORD src1_sel:WORD_1\n"
    "v_mfma_f32_16x16x32_bf16 v[100:103], a[212:215], v[192:195], v[100:103]\n"
    "s_waitcnt lgkmcnt(4)\n"
    "ds_read_b128 v[164:167], v244 offset:22528\n"
    "ds_read_u16_d16_hi v204, v204\n"
    "v_mfma_f32_16x16x32_bf16 v[112:115], a[216:219], v[192:195], v[112:115]\n"
    "ds_write_b128 v253, v[220:223] offset:5120\n"
    "v_mfma_f32_16x16x32_bf16 v[116:119], a[220:223], v[192:195], v[116:119]\n"
    "v_or_b32_e64 v186, v186, v202\n"
    "v_lshlrev_b32_sdwa v202, 1, v173 dst_sel:DWORD dst_unused:UNUSED_PAD src0_sel:DWORD src1_sel:WORD_1\n"
    "v_mfma_f32_16x16x32_bf16 v[128:131], v[236:239], v[192:195], v[128:131]\n"
    "s_nop 0\n"
    "v_cvt_pk_bf16_f32 v208, v96, v97\n"
    "v_cvt_pk_bf16_f32 v209, v98, v99\n"
    "v_mfma_f32_16x16x32_bf16 v[104:107], a[208:211], v[196:199], v[104:107]\n"
    "s_waitcnt lgkmcnt(5)\n"
    "ds_read_u16 v189, v189\n"
    "ds_read_u16_d16_hi v205, v205\n"
    "v_mfma_f32_16x16x32_bf16 v[108:111], a[212:215], v[196:199], v[108:111]\n"
    "ds_read_u16 v190, v190\n"
    "ds_read_u16_d16_hi v206, v206\n"
    "v_or_b32_e64 v187, v187, v203\n"
    "v_lshlrev_b32_sdwa v203, 1, v175 dst_sel:DWORD dst_unused:UNUSED_PAD src0_sel:DWORD src1_sel:WORD_1\n"
    "v_cvt_pk_bf16_f32 v210, v100, v101\n"
    "v_cvt_pk_bf16_f32 v211, v102, v103\n"
    "v_mfma_f32_16x16x32_bf16 v[120:123], a[216:219], v[196:199], v[120:123]\n"
    "ds_read_u16 v191, v191\n"
    "v_cvt_pk_bf16_f32 v212, v112, v113\n"
    "v_cvt_pk_bf16_f32 v213, v114, v115\n"
    "v_cvt_pk_bf16_f32 v214, v116, v117\n"
    "v_cvt_pk_bf16_f32 v215, v118, v119\n"
    "v_mfma_f32_16x16x32_bf16 v[124:127], a[220:223], v[196:199], v[124:127]\n"
    "s_waitcnt lgkmcnt(7)\n"
    "ds_read_u16_d16_hi v207, v207\n"
    "v_cvt_pk_bf16_f32 v216, v128, v129\n"
    "v_cvt_pk_bf16_f32 v217, v130, v131\n"
    "v_cvt_pk_bf16_f32 v220, v104, v105\n"
    "v_cvt_pk_bf16_f32 v221, v106, v107\n"
    "v_mfma_f32_16x16x32_bf16 v[132:135], v[164:167], v[192:195], v[132:135]\n"
    "s_waitcnt lgkmcnt(6)\n"
    "ds_read_u16_d16_hi v201, v201\n"
    "v_cvt_pk_f16_f32 v192, v168, v169\n"
    "v_mfma_f32_16x16x32_bf16 v[136:139], v[236:239], v[196:199], v[136:139]\n"
    "v_lshlrev_b32_sdwa v193, 1, v170 dst_sel:DWORD dst_unused:UNUSED_PAD src0_sel:DWORD src1_sel:WORD_1\n"
    "v_lshlrev_b32_sdwa v194, 1, v172 dst_sel:DWORD dst_unused:UNUSED_PAD src0_sel:DWORD src1_sel:WORD_1\n"
    "v_mfma_f32_16x16x32_bf16 v[140:143], v[164:167], v[196:199], v[140:143]\n"
    "ds_read_u16 v193, v193\n"
    "v_lshlrev_b32_sdwa v195, 1, v174 dst_sel:DWORD dst_unused:UNUSED_PAD src0_sel:DWORD src1_sel:WORD_1\n"
    "v_cvt_pk_f16_f32 v196, v176, v177\n"
    "v_lshlrev_b32_sdwa v197, 1, v178 dst_sel:DWORD dst_unused:UNUSED_PAD src0_sel:DWORD src1_sel:WORD_1\n"
    "v_mfma_f32_16x16x32_bf16 v[168:171], a[96:99], a[224:227], 0\n"
    "ds_read_u16 v194, v194\n"
    "v_lshlrev_b32_sdwa v198, 1, v180 dst_sel:DWORD dst_unused:UNUSED_PAD src0_sel:DWORD src1_sel:WORD_1\n"
    "v_mfma_f32_16x16x32_bf16 v[168:171], a[144:147], a[228:231], v[168:171]\n"
    "v_lshlrev_b32_sdwa v199, 1, v182 dst_sel:DWORD dst_unused:UNUSED_PAD src0_sel:DWORD src1_sel:WORD_1\n"
    "v_or_b32_e32 v188, v188, v204\n"
    "v_mfma_f32_16x16x32_bf16 v[168:171], a[56:59], a[232:235], v[168:171]\n"
    "s_waitcnt lgkmcnt(7)\n"
    "ds_read_u16_d16_hi v202, v202\n"
    "buffer_load_dwordx4 v[4:7], v246, s[40:43], s61 offen offset:0\n"
    "buffer_load_dwordx4 v[36:39], v246, s[40:43], s61 offen offset:64\n"
    "buffer_load_dwordx4 v[68:71], v246, s[40:43], s61 offen offset:128\n"
    "v_mfma_f32_16x16x32_bf16 v[172:175], a[100:103], a[224:227], 0\n"
    "ds_read_u16 v195, v195\n"
    "v_or_b32_e64 v189, v189, v205\n"
    "v_mfma_f32_16x16x32_bf16 v[172:175], a[148:151], a[228:231], v[172:175]\n"
    "v_lshlrev_b32_sdwa v205, 1, v179 dst_sel:DWORD dst_unused:UNUSED_PAD src0_sel:DWORD src1_sel:WORD_1\n"
    "v_cvt_pk_bf16_f32 v218, v132, v133\n"
    "v_mfma_f32_16x16x32_bf16 v[172:175], a[60:63], a[232:235], v[172:175]\n"
    "s_waitcnt lgkmcnt(7)\n"
    "ds_read_b128 v[236:239], v244 offset:0\n"
    "v_or_b32_e32 v190, v190, v206\n"
    "v_mfma_f32_16x16x32_bf16 v[176:179], a[96:99], a[236:239], 0\n"
    "ds_read_u16_d16_hi v203, v203\n"
    "v_lshlrev_b32_sdwa v206, 1, v181 dst_sel:DWORD dst_unused:UNUSED_PAD src0_sel:DWORD src1_sel:WORD_1\n"
    "v_mfma_f32_16x16x32_bf16 v[176:179], a[144:147], a[240:243], v[176:179]\n"
    "v_cvt_pk_bf16_f32 v219, v134, v135\n"
    "v_cvt_pk_bf16_f32 v222, v108, v109\n"
    "v_mfma_f32_16x16x32_bf16 v[176:179], a[56:59], a[244:247], v[176:179]\n"
    "ds_read_b128 v[164:167], v244 offset:2048\n"
    "v_cvt_pk_bf16_f32 v223, v110, v111\n"
    "v_mfma_f32_16x16x32_bf16 v[96:99], a[0:3], v[184:187], 0\n"
    "s_waitcnt lgkmcnt(8)\n"
    "ds_read_u16 v197, v197\n"
    "v_or_b32_e32 v191, v191, v207\n"
    "v_lshlrev_b32_sdwa v207, 1, v183 dst_sel:DWORD dst_unused:UNUSED_PAD src0_sel:DWORD src1_sel:WORD_1\n"
    "v_mfma_f32_16x16x32_bf16 v[180:183], a[100:103], a[236:239], 0\n"
    "ds_read_u16_d16_hi v205, v205\n"
    "v_cvt_pk_bf16_f32 v224, v120, v121\n"
    "v_mfma_f32_16x16x32_bf16 v[180:183], a[148:151], a[240:243], v[180:183]\n"
    "v_cvt_pk_bf16_f32 v225, v122, v123\n"
    "v_cvt_pk_bf16_f32 v226, v124, v125\n"
    "v_mfma_f32_16x16x32_bf16 v[180:183], a[60:63], a[244:247], v[180:183]\n"
    "s_waitcnt lgkmcnt(8)\n"
    "ds_read_u16 v198, v198\n"
    "v_or_b32_e32 v193, v193, v201\n"
    "v_mfma_f32_16x16x32_bf16 v[100:103], a[4:7], v[184:187], 0\n"
    "v_cvt_pk_bf16_f32 v227, v126, v127\n"
    "v_pk_fma_f16 v232, v192, v192, s48\n"
    "v_mfma_f32_16x16x32_bf16 v[112:115], a[8:11], v[184:187], 0\n"
    "ds_read_u16_d16_hi v206, v206\n"
    "v_cvt_pk_bf16_f32 v228, v136, v137\n"
    "v_mfma_f32_16x16x32_bf16 v[116:119], a[12:15], v[184:187], 0\n"
    "s_waitcnt lgkmcnt(8)\n"
    "v_or_b32_e32 v194, v194, v202\n"
    "v_cvt_pk_bf16_f32 v229, v138, v139\n"
    "v_mfma_f32_16x16x32_bf16 v[104:107], a[0:3], v[188:191], 0\n"
    "s_nop 0\n"
    "ds_read_u16 v199, v199\n"
    "v_cvt_pk_bf16_f32 v230, v140, v141\n"
    "v_mfma_f32_16x16x32_bf16 v[108:111], a[4:7], v[188:191], 0\n"
    "s_waitcnt lgkmcnt(7)\n"
    "ds_read_u16_d16_hi v207, v207\n"
    "buffer_load_dwordx4 v[24:27], v247, s[40:43], s61 offen offset:0\n"
    "buffer_load_dwordx4 v[56:59], v247, s[40:43], s61 offen offset:64\n"
    "buffer_load_dwordx4 v[88:91], v247, s[40:43], s61 offen offset:128\n"
    "v_mfma_f32_16x16x32_bf16 v[128:131], v[236:239], v[184:187], 0\n"
    "v_cvt_pk_bf16_f32 v231, v142, v143\n"
    "v_pk_mul_f16 v232, v192, v232\n"
    "v_mfma_f32_16x16x32_bf16 v[120:123], a[8:11], v[188:191], 0\n"
    "s_waitcnt lgkmcnt(6)\n"
    "v_or_b32_e32 v195, v195, v203\n"
    "v_exp_f16_e64 v234, v232\n"
    "v_mfma_f32_16x16x32_bf16 v[132:135], v[164:167], v[184:187], 0\n"
    "v_exp_f16_sdwa v234, v232 dst_sel:WORD_1 dst_unused:UNUSED_PRESERVE src0_sel:WORD_1\n"
    "v_mfma_f32_16x16x32_bf16 v[124:127], a[12:15], v[188:191], 0\n"
    "v_pk_fma_f16 v234, v234, s49, s49\n"
    "v_lshlrev_b32_sdwa v184, 1, v168 dst_sel:DWORD dst_unused:UNUSED_PAD src0_sel:DWORD src1_sel:WORD_1\n"
    "v_mfma_f32_16x16x32_bf16 v[136:139], v[236:239], v[188:191], 0\n"
    "ds_read_u16 v184, v184\n"
    "ds_read_b128 v[236:239], v244 offset:4096\n"
    "v_mfma_f32_16x16x32_bf16 v[140:143], v[164:167], v[188:191], 0\n"
    "v_lshlrev_b32_sdwa v200, 1, v169 dst_sel:DWORD dst_unused:UNUSED_PAD src0_sel:DWORD src1_sel:WORD_1\n"
    "s_waitcnt lgkmcnt(6)\n"
    "v_or_b32_e32 v197, v197, v205\n"
    "v_lshlrev_b32_sdwa v185, 1, v170 dst_sel:DWORD dst_unused:UNUSED_PAD src0_sel:DWORD src1_sel:WORD_1\n"
    "v_lshlrev_b32_sdwa v201, 1, v171 dst_sel:DWORD dst_unused:UNUSED_PAD src0_sel:DWORD src1_sel:WORD_1\n"
    "v_mfma_f32_16x16x32_bf16 v[168:171], a[104:107], a[224:227], 0\n"
    "ds_read_u16_d16_hi v200, v200\n"
    "ds_read_u16 v185, v185\n"
    "v_mfma_f32_16x16x32_bf16 v[168:171], a[152:155], a[228:231], v[168:171]\n"
    "ds_read_u16_d16_hi v201, v201\n"
    "v_lshlrev_b32_sdwa v186, 1, v172 dst_sel:DWORD dst_unused:UNUSED_PAD src0_sel:DWORD src1_sel:WORD_1\n"
    "v_lshlrev_b32_sdwa v202, 1, v173 dst_sel:DWORD dst_unused:UNUSED_PAD src0_sel:DWORD src1_sel:WORD_1\n"
    "v_lshlrev_b32_sdwa v187, 1, v174 dst_sel:DWORD dst_unused:UNUSED_PAD src0_sel:DWORD src1_sel:WORD_1\n"
    "s_waitcnt lgkmcnt(7)\n"
    "v_or_b32_e32 v198, v198, v206\n"
    "v_lshlrev_b32_sdwa v203, 1, v175 dst_sel:DWORD dst_unused:UNUSED_PAD src0_sel:DWORD src1_sel:WORD_1\n"
    "v_rcp_f16_e64 v232, v234\n"
    "v_mfma_f32_16x16x32_bf16 v[168:171], a[64:67], a[232:235], v[168:171]\n"
    "ds_read_u16 v186, v186\n"
    "ds_read_u16_d16_hi v202, v202\n"
    "v_rcp_f16_sdwa v232, v234 dst_sel:WORD_1 dst_unused:UNUSED_PRESERVE src0_sel:WORD_1\n"
    "v_mfma_f32_16x16x32_bf16 v[172:175], a[108:111], a[224:227], 0\n"
    "s_nop 0\n"
    "ds_read_u16 v187, v187\n"
    "v_lshlrev_b32_sdwa v188, 1, v176 dst_sel:DWORD dst_unused:UNUSED_PAD src0_sel:DWORD src1_sel:WORD_1\n"
    "v_mfma_f32_16x16x32_bf16 v[172:175], a[156:159], a[228:231], v[172:175]\n"
    "s_waitcnt lgkmcnt(8)\n"
    "v_lshlrev_b32_sdwa v204, 1, v177 dst_sel:DWORD dst_unused:UNUSED_PAD src0_sel:DWORD src1_sel:WORD_1\n"
    "v_lshlrev_b32_sdwa v189, 1, v178 dst_sel:DWORD dst_unused:UNUSED_PAD src0_sel:DWORD src1_sel:WORD_1\n"
    "v_lshlrev_b32_sdwa v205, 1, v179 dst_sel:DWORD dst_unused:UNUSED_PAD src0_sel:DWORD src1_sel:WORD_1\n"
    "v_lshlrev_b32_sdwa v190, 1, v180 dst_sel:DWORD dst_unused:UNUSED_PAD src0_sel:DWORD src1_sel:WORD_1\n"
    "v_lshlrev_b32_sdwa v206, 1, v181 dst_sel:DWORD dst_unused:UNUSED_PAD src0_sel:DWORD src1_sel:WORD_1\n"
    "v_lshlrev_b32_sdwa v191, 1, v182 dst_sel:DWORD dst_unused:UNUSED_PAD src0_sel:DWORD src1_sel:WORD_1\n"
    "v_fma_mix_f32 v234, v192, v232, 0 op_sel_hi:[1,1,0]\n"
    "v_mfma_f32_16x16x32_bf16 v[172:175], a[68:71], a[232:235], v[172:175]\n"
    "ds_read_u16_d16_hi v203, v203\n"
    "ds_read_b128 v[164:167], v244 offset:6144\n"
    "v_or_b32_e32 v199, v199, v207\n"
    "v_lshlrev_b32_sdwa v207, 1, v183 dst_sel:DWORD dst_unused:UNUSED_PAD src0_sel:DWORD src1_sel:WORD_1\n"
    "v_fma_mix_f32 v232, v192, v232, 0 op_sel:[1,1,0] op_sel_hi:[1,1,0]\n"
    "v_mfma_f32_16x16x32_bf16 v[176:179], a[104:107], a[236:239], 0\n"
    "s_waitcnt lgkmcnt(7)\n"
    "buffer_load_dwordx4 v[8:11], v246, s[40:43], s62 offen offset:0\n"
    "buffer_load_dwordx4 v[40:43], v246, s[40:43], s62 offen offset:64\n"
    "buffer_load_dwordx4 v[72:75], v246, s[40:43], s62 offen offset:128\n"
    "ds_read_u16 v188, v188\n"
    "v_cvt_pk_bf16_f32 v192, v234, v232\n"
    "v_mfma_f32_16x16x32_bf16 v[176:179], a[152:155], a[240:243], v[176:179]\n"
    "ds_read_u16_d16_hi v204, v204\n"
    "v_pk_fma_f16 v232, v196, v196, s48\n"
    "v_mfma_f32_16x16x32_bf16 v[176:179], a[64:67], a[244:247], v[176:179]\n"
    "ds_read_u16 v189, v189\n"
    "v_pk_mul_f16 v232, v196, v232\n"
    "v_mfma_f32_16x16x32_bf16 v[180:183], a[108:111], a[236:239], 0\n"
    "s_waitcnt lgkmcnt(8)\n"
    "v_exp_f16_e32 v234, v232\n"
    "v_mfma_f32_16x16x32_bf16 v[180:183], a[156:159], a[240:243], v[180:183]\n"
    "ds_read_u16_d16_hi v205, v205\n"
    "v_or_b32_e32 v184, v184, v200\n"
    "v_mfma_f32_16x16x32_bf16 v[180:183], a[68:71], a[244:247], v[180:183]\n"
    "v_exp_f16_sdwa v234, v232 dst_sel:WORD_1 dst_unused:UNUSED_PRESERVE src0_sel:WORD_1\n"
    "v_mfma_f32_16x16x32_bf16 v[96:99], a[16:19], v[192:195], v[96:99]\n"
    "s_waitcnt lgkmcnt(7)\n"
    "ds_read_u16 v190, v190\n"
    "v_pk_fma_f16 v234, v234, s49, s49\n"
    "v_mfma_f32_16x16x32_bf16 v[100:103], a[20:23], v[192:195], v[100:103]\n"
    "ds_read_u16_d16_hi v206, v206\n"
    "v_rcp_f16_e64 v232, v234\n"
    "v_mfma_f32_16x16x32_bf16 v[112:115], a[24:27], v[192:195], v[112:115]\n"
    "v_rcp_f16_sdwa v232, v234 dst_sel:WORD_1 dst_unused:UNUSED_PRESERVE src0_sel:WORD_1\n"
    "v_mfma_f32_16x16x32_bf16 v[116:119], a[28:31], v[192:195], v[116:119]\n"
    "ds_read_u16 v191, v191\n"
    "v_fma_mix_f32 v234, v196, v232, 0 op_sel_hi:[1,1,0]\n"
    "v_mfma_f32_16x16x32_bf16 v[128:131], v[236:239], v[192:195], v[128:131]\n"
    "s_waitcnt lgkmcnt(7)\n"
    "v_fma_mix_f32 v232, v196, v232, 0 op_sel:[1,1,0] op_sel_hi:[1,1,0]\n"
    "v_or_b32_e32 v185, v185, v201\n"
    "v_mfma_f32_16x16x32_bf16 v[132:135], v[164:167], v[192:195], v[132:135]\n"
    "ds_read_u16_d16_hi v207, v207\n"
    "v_cvt_pk_bf16_f32 v196, v234, v232\n"
    "v_cvt_pk_f16_f32 v192, v168, v169\n"
    "v_lshlrev_b32_sdwa v193, 1, v170 dst_sel:DWORD dst_unused:UNUSED_PAD src0_sel:DWORD src1_sel:WORD_1\n"
    "v_mfma_f32_16x16x32_bf16 v[104:107], a[16:19], v[196:199], v[104:107]\n"
    "ds_read_u16 v193, v193\n"
    "v_lshlrev_b32_sdwa v201, 1, v171 dst_sel:DWORD dst_unused:UNUSED_PAD src0_sel:DWORD src1_sel:WORD_1\n"
    "v_mfma_f32_16x16x32_bf16 v[108:111], a[20:23], v[196:199], v[108:111]\n"
    "v_lshlrev_b32_sdwa v194, 1, v172 dst_sel:DWORD dst_unused:UNUSED_PAD src0_sel:DWORD src1_sel:WORD_1\n"
    "v_or_b32_e32 v186, v186, v202\n"
    "v_mfma_f32_16x16x32_bf16 v[120:123], a[24:27], v[196:199], v[120:123]\n"
    "s_waitcnt lgkmcnt(7)\n"
    "ds_read_b128 a[252:255], v244 offset:34816\n"
    "ds_read_u16_d16_hi v201, v201\n"
    "v_mfma_f32_16x16x32_bf16 v[124:127], a[28:31], v[196:199], v[124:127]\n"
    "v_lshlrev_b32_sdwa v202, 1, v173 dst_sel:DWORD dst_unused:UNUSED_PAD src0_sel:DWORD src1_sel:WORD_1\n"
    "v_lshlrev_b32_sdwa v195, 1, v174 dst_sel:DWORD dst_unused:UNUSED_PAD src0_sel:DWORD src1_sel:WORD_1\n"
    "v_mfma_f32_16x16x32_bf16 v[136:139], v[236:239], v[196:199], v[136:139]\n"
    "buffer_load_dwordx4 v[12:15], v246, s[40:43], s63 offen offset:0\n"
    "buffer_load_dwordx4 v[44:47], v246, s[40:43], s63 offen offset:64\n"
    "buffer_load_dwordx4 v[76:79], v246, s[40:43], s63 offen offset:128\n"
    "ds_read_u16 v194, v194\n"
    "v_mfma_f32_16x16x32_bf16 v[140:143], v[164:167], v[196:199], v[140:143]\n"
    "s_waitcnt lgkmcnt(8)\n"
    "v_cvt_pk_f16_f32 v196, v176, v177\n"
    "v_lshlrev_b32_sdwa v197, 1, v178 dst_sel:DWORD dst_unused:UNUSED_PAD src0_sel:DWORD src1_sel:WORD_1\n"
    "v_mfma_f32_16x16x32_bf16 v[168:171], a[112:115], a[224:227], 0\n"
    "s_nop 0\n"
    "ds_read_u16_d16_hi v202, v202\n"
    "v_lshlrev_b32_sdwa v198, 1, v180 dst_sel:DWORD dst_unused:UNUSED_PAD src0_sel:DWORD src1_sel:WORD_1\n"
    "v_mfma_f32_16x16x32_bf16 v[168:171], a[160:163], a[228:231], v[168:171]\n"
    "ds_read_u16 v195, v195\n"
    "v_lshlrev_b32_sdwa v199, 1, v182 dst_sel:DWORD dst_unused:UNUSED_PAD src0_sel:DWORD src1_sel:WORD_1\n"
    "v_mfma_f32_16x16x32_bf16 v[168:171], a[72:75], a[232:235], v[168:171]\n"
    "s_waitcnt lgkmcnt(8)\n"
    "v_or_b32_e32 v187, v187, v203\n"
    "v_lshlrev_b32_sdwa v203, 1, v175 dst_sel:DWORD dst_unused:UNUSED_PAD src0_sel:DWORD src1_sel:WORD_1\n"
    "v_mfma_f32_16x16x32_bf16 v[172:175], a[116:119], a[224:227], 0\n"
    "ds_read_b128 v[236:239], v244 offset:8192\n"
    "v_or_b32_e64 v188, v188, v204\n"
    "v_mfma_f32_16x16x32_bf16 v[172:175], a[164:167], a[228:231], v[172:175]\n"
    "ds_read_u16_d16_hi v203, v203\n"
    "v_or_b32_e64 v189, v189, v205\n"
    "v_lshlrev_b32_sdwa v205, 1, v179 dst_sel:DWORD dst_unused:UNUSED_PAD src0_sel:DWORD src1_sel:WORD_1\n"
    "v_mfma_f32_16x16x32_bf16 v[176:179], a[112:115], a[236:239], 0\n"
    "s_waitcnt lgkmcnt(8)\n"
    "v_or_b32_e32 v190, v190, v206\n"
    "v_lshlrev_b32_sdwa v206, 1, v181 dst_sel:DWORD dst_unused:UNUSED_PAD src0_sel:DWORD src1_sel:WORD_1\n"
    "v_mfma_f32_16x16x32_bf16 v[176:179], a[160:163], a[240:243], v[176:179]\n"
    "ds_read_b128 v[164:167], v244 offset:10240\n"
    "ds_read_u16 v197, v197\n"
    "v_mfma_f32_16x16x32_bf16 v[176:179], a[72:75], a[244:247], v[176:179]\n"
    "s_waitcnt lgkmcnt(8)\n"
    "v_or_b32_e32 v191, v191, v207\n"
    "v_lshlrev_b32_sdwa v207, 1, v183 dst_sel:DWORD dst_unused:UNUSED_PAD src0_sel:DWORD src1_sel:WORD_1\n"
    "v_mfma_f32_16x16x32_bf16 v[172:175], a[252:255], a[232:235], v[172:175]\n"
    "ds_read_u16_d16_hi v205, v205\n"
    "v_pk_fma_f16 v232, v192, v192, s48\n"
    "v_mfma_f32_16x16x32_bf16 v[180:183], a[116:119], a[236:239], 0\n"
    "v_pk_mul_f16 v232, v192, v232\n"
    "v_lshlrev_b32_sdwa v200, 1, v169 dst_sel:DWORD dst_unused:UNUSED_PAD src0_sel:DWORD src1_sel:WORD_1\n"
    "v_mfma_f32_16x16x32_bf16 v[180:183], a[164:167], a[240:243], v[180:183]\n"
    "s_waitcnt lgkmcnt(8)\n"
    "ds_read_u16 v198, v198\n"
    "v_or_b32_e32 v193, v193, v201\n"
    "v_mfma_f32_16x16x32_bf16 v[180:183], a[252:255], a[244:247], v[180:183]\n"
    "v_exp_f16_e32 v234, v232\n"
    "v_mfma_f32_16x16x32_bf16 v[96:99], a[32:35], v[184:187], v[96:99]\n"
    "ds_read_u16_d16_hi v206, v206\n"
    "v_exp_f16_sdwa v234, v232 dst_sel:WORD_1 dst_unused:UNUSED_PRESERVE src0_sel:WORD_1\n"
    "v_mfma_f32_16x16x32_bf16 v[100:103], a[36:39], v[184:187], v[100:103]\n"
    "s_waitcnt lgkmcnt(8)\n"
    "ds_read_u16 v199, v199\n"
    "v_or_b32_e32 v194, v194, v202\n"
    "v_mfma_f32_16x16x32_bf16 v[112:115], a[40:43], v[184:187], v[112:115]\n"
    "v_pk_fma_f16 v234, v234, s49, s49\n"
    "v_lshlrev_b32_sdwa v201, 1, v171 dst_sel:DWORD dst_unused:UNUSED_PAD src0_sel:DWORD src1_sel:WORD_1\n"
    "v_mfma_f32_16x16x32_bf16 v[116:119], a[44:47], v[184:187], v[116:119]\n"
    "s_waitcnt lgkmcnt(7)\n"
    "buffer_load_dwordx4 v[16:19], v246, s[40:43], s52 offen offset:0\n"
    "buffer_load_dwordx4 v[48:51], v246, s[40:43], s52 offen offset:64\n"
    "buffer_load_dwordx4 v[80:83], v246, s[40:43], s52 offen offset:128\n"
    "ds_read_u16_d16_hi v207, v207\n"
    "v_mfma_f32_16x16x32_bf16 v[128:131], v[236:239], v[184:187], v[128:131]\n"
    "ds_read_b128 a[252:255], v244 offset:38912\n"
    "v_lshlrev_b32_sdwa v202, 1, v173 dst_sel:DWORD dst_unused:UNUSED_PAD src0_sel:DWORD src1_sel:WORD_1\n"
    "v_mfma_f32_16x16x32_bf16 v[104:107], a[32:35], v[188:191], v[104:107]\n"
    "s_waitcnt lgkmcnt(7)\n"
    "ds_read_u16_d16_hi v200, v200\n"
    "v_or_b32_e32 v195, v195, v203\n"
    "v_mfma_f32_16x16x32_bf16 v[132:135], v[164:167], v[184:187], v[132:135]\n"
    "v_lshlrev_b32_sdwa v184, 1, v168 dst_sel:DWORD dst_unused:UNUSED_PAD src0_sel:DWORD src1_sel:WORD_1\n"
    "v_lshlrev_b32_sdwa v185, 1, v170 dst_sel:DWORD dst_unused:UNUSED_PAD src0_sel:DWORD src1_sel:WORD_1\n"
    "v_mfma_f32_16x16x32_bf16 v[108:111], a[36:39], v[188:191], v[108:111]\n"
    "ds_read_u16 v184, v184\n"
    "v_lshlrev_b32_sdwa v186, 1, v172 dst_sel:DWORD dst_unused:UNUSED_PAD src0_sel:DWORD src1_sel:WORD_1\n"
    "v_mfma_f32_16x16x32_bf16 v[120:123], a[40:43], v[188:191], v[120:123]\n"
    "v_lshlrev_b32_sdwa v187, 1, v174 dst_sel:DWORD dst_unused:UNUSED_PAD src0_sel:DWORD src1_sel:WORD_1\n"
    "v_lshlrev_b32_sdwa v203, 1, v175 dst_sel:DWORD dst_unused:UNUSED_PAD src0_sel:DWORD src1_sel:WORD_1\n"
    "v_mfma_f32_16x16x32_bf16 v[124:127], a[44:47], v[188:191], v[124:127]\n"
    "ds_read_u16 v185, v185\n"
    "v_rcp_f16_e32 v232, v234\n"
    "v_mfma_f32_16x16x32_bf16 v[136:139], v[236:239], v[188:191], v[136:139]\n"
    "s_waitcnt lgkmcnt(8)\n"
    "ds_read_u16_d16_hi v201, v201\n"
    "v_or_b32_e64 v197, v197, v205\n"
    "v_mfma_f32_16x16x32_bf16 v[140:143], v[164:167], v[188:191], v[140:143]\n"
    "v_rcp_f16_sdwa v232, v234 dst_sel:WORD_1 dst_unused:UNUSED_PRESERVE src0_sel:WORD_1\n"
    "v_mfma_f32_16x16x32_bf16 v[168:171], a[120:123], a[224:227], 0\n"
    "ds_read_u16 v186, v186\n"
    "v_lshlrev_b32_sdwa v188, 1, v176 dst_sel:DWORD dst_unused:UNUSED_PAD src0_sel:DWORD src1_sel:WORD_1\n"
    "v_mfma_f32_16x16x32_bf16 v[168:171], a[168:171], a[228:231], v[168:171]\n"
    "s_waitcnt lgkmcnt(8)\n"
    "v_or_b32_e32 v198, v198, v206\n"
    "v_lshlrev_b32_sdwa v204, 1, v177 dst_sel:DWORD dst_unused:UNUSED_PAD src0_sel:DWORD src1_sel:WORD_1\n"
    "v_mfma_f32_16x16x32_bf16 v[168:171], a[76:79], a[232:235], v[168:171]\n"
    "ds_read_u16_d16_hi v202, v202\n"
    "v_lshlrev_b32_sdwa v189, 1, v178 dst_sel:DWORD dst_unused:UNUSED_PAD src0_sel:DWORD src1_sel:WORD_1\n"
    "v_mfma_f32_16x16x32_bf16 v[172:175], a[124:127], a[224:227], 0\n"
    "s_nop 0\n"
    "v_lshlrev_b32_sdwa v205, 1, v179 dst_sel:DWORD dst_unused:UNUSED_PAD src0_sel:DWORD src1_sel:WORD_1\n"
    "v_lshlrev_b32_sdwa v190, 1, v180 dst_sel:DWORD dst_unused:UNUSED_PAD src0_sel:DWORD src1_sel:WORD_1\n"
    "v_mfma_f32_16x16x32_bf16 v[172:175], a[172:175], a[228:231], v[172:175]\n"
    "s_waitcnt lgkmcnt(6)\n"
    "ds_read_b128 v[236:239], v244 offset:12288\n"
    "ds_read_u16 v187, v187\n"
    "v_lshlrev_b32_sdwa v206, 1, v181 dst_sel:DWORD dst_unused:UNUSED_PAD src0_sel:DWORD src1_sel:WORD_1\n"
    "v_lshlrev_b32_sdwa v191, 1, v182 dst_sel:DWORD dst_unused:UNUSED_PAD src0_sel:DWORD src1_sel:WORD_1\n"
    "v_fma_mix_f32 v234, v192, v232, 0 op_sel_hi:[1,1,0]\n"
    "v_mfma_f32_16x16x32_bf16 v[172:175], a[252:255], a[232:235], v[172:175]\n"
    "v_or_b32_e64 v199, v199, v207\n"
    "v_lshlrev_b32_sdwa v207, 1, v183 dst_sel:DWORD dst_unused:UNUSED_PAD src0_sel:DWORD src1_sel:WORD_1\n"
    "v_fma_mix_f32 v232, v192, v232, 0 op_sel:[1,1,0] op_sel_hi:[1,1,0]\n"
    "v_mfma_f32_16x16x32_bf16 v[176:179], a[120:123], a[236:239], 0\n"
    "ds_read_u16_d16_hi v203, v203\n"
    "v_cvt_pk_bf16_f32 v192, v234, v232\n"
    "v_mfma_f32_16x16x32_bf16 v[176:179], a[168:171], a[240:243], v[176:179]\n"
    "s_nop 0\n"
    "buffer_load_dwordx4 v[152:155], v245, s[40:43], s57 offen offset:64\n"
    "buffer_load_dwordx4 v[144:147], v245, s[40:43], s57 offen offset:0\n"
    "v_mfma_f32_16x16x32_bf16 v[176:179], a[76:79], a[244:247], v[176:179]\n"
    "s_waitcnt lgkmcnt(7)\n"
    "ds_read_b128 v[164:167], v244 offset:14336\n"
    "ds_read_u16 v188, v188\n"
    "v_pk_fma_f16 v232, v196, v196, s48\n"
    "v_mfma_f32_16x16x32_bf16 v[180:183], a[124:127], a[236:239], 0\n"
    "v_pk_mul_f16 v232, v196, v232\n"
    "v_or_b32_e64 v184, v184, v200\n"
    "v_mfma_f32_16x16x32_bf16 v[180:183], a[172:175], a[240:243], v[180:183]\n"
    "ds_read_u16_d16_hi v204, v204\n"
    "v_exp_f16_e32 v234, v232\n"
    "v_mfma_f32_16x16x32_bf16 v[180:183], a[252:255], a[244:247], v[180:183]\n"
    "s_waitcnt lgkmcnt(8)\n"
    "ds_read_u16 v189, v189\n"
    "v_exp_f16_sdwa v234, v232 dst_sel:WORD_1 dst_unused:UNUSED_PRESERVE src0_sel:WORD_1\n"
    "v_mfma_f32_16x16x32_bf16 v[96:99], a[176:179], v[192:195], v[96:99]\n"
    "v_pk_fma_f16 v234, v234, s49, s49\n"
    "v_or_b32_e64 v185, v185, v201\n"
    "v_mfma_f32_16x16x32_bf16 v[100:103], a[180:183], v[192:195], v[100:103]\n"
    "ds_read_u16_d16_hi v205, v205\n"
    "v_rcp_f16_e32 v232, v234\n"
    "v_mfma_f32_16x16x32_bf16 v[112:115], a[184:187], v[192:195], v[112:115]\n"
    "s_waitcnt lgkmcnt(7)\n"
    "ds_read_u16 v190, v190\n"
    "v_rcp_f16_sdwa v232, v234 dst_sel:WORD_1 dst_unused:UNUSED_PRESERVE src0_sel:WORD_1\n"
    "v_mfma_f32_16x16x32_bf16 v[116:119], a[188:191], v[192:195], v[116:119]\n"
    "v_fma_mix_f32 v234, v196, v232, 0 op_sel_hi:[1,1,0]\n"
    "v_fma_mix_f32 v232, v196, v232, 0 op_sel:[1,1,0] op_sel_hi:[1,1,0]\n"
    "v_mfma_f32_16x16x32_bf16 v[128:131], v[236:239], v[192:195], v[128:131]\n"
    "ds_read_b128 a[224:227], v243 offset:24640\n"
    "ds_read_u16_d16_hi v206, v206\n"
    "v_cvt_pk_bf16_f32 v196, v234, v232\n"
    "v_lshlrev_b32_sdwa v201, 1, v171 dst_sel:DWORD dst_unused:UNUSED_PAD src0_sel:DWORD src1_sel:WORD_1\n"
    "v_or_b32_e32 v186, v186, v202\n"
    "v_mfma_f32_16x16x32_bf16 v[104:107], a[176:179], v[196:199], v[104:107]\n"
    "s_waitcnt lgkmcnt(7)\n"
    "v_lshlrev_b32_sdwa v202, 1, v173 dst_sel:DWORD dst_unused:UNUSED_PAD src0_sel:DWORD src1_sel:WORD_1\n"
    "v_or_b32_e64 v187, v187, v203\n"
    "v_mfma_f32_16x16x32_bf16 v[132:135], v[164:167], v[192:195], v[132:135]\n"
    "ds_read_b128 a[228:231], v243 offset:25664\n"
    "ds_read_b128 a[232:235], v243 offset:26688\n"
    "v_mfma_f32_16x16x32_bf16 v[108:111], a[180:183], v[196:199], v[108:111]\n"
    "v_cvt_pk_f16_f32 v192, v168, v169\n"
    "v_lshlrev_b32_sdwa v193, 1, v170 dst_sel:DWORD dst_unused:UNUSED_PAD src0_sel:DWORD src1_sel:WORD_1\n"
    "v_mfma_f32_16x16x32_bf16 v[120:123], a[184:187], v[196:199], v[120:123]\n"
    "buffer_load_dwordx4 v[156:159], v245, s[40:43], s88 offen offset:64\n"
    "buffer_load_dwordx4 v[148:151], v245, s[40:43], s88 offen offset:0\n"
    "ds_read_u16 v191, v191\n"
    "v_mfma_f32_16x16x32_bf16 v[124:127], a[188:191], v[196:199], v[124:127]\n"
    "s_nop 0\n"
    "ds_read_b128 a[236:239], v243 offset:27712\n"
    "v_lshlrev_b32_sdwa v194, 1, v172 dst_sel:DWORD dst_unused:UNUSED_PAD src0_sel:DWORD src1_sel:WORD_1\n"
    "v_mfma_f32_16x16x32_bf16 v[136:139], v[236:239], v[196:199], v[136:139]\n"
    "s_waitcnt lgkmcnt(9)\n"
    "ds_read_u16_d16_hi v207, v207\n"
    "v_lshlrev_b32_sdwa v195, 1, v174 dst_sel:DWORD dst_unused:UNUSED_PAD src0_sel:DWORD src1_sel:WORD_1\n"
    "v_lshlrev_b32_sdwa v203, 1, v175 dst_sel:DWORD dst_unused:UNUSED_PAD src0_sel:DWORD src1_sel:WORD_1\n"
    "v_or_b32_e64 v188, v188, v204\n"
    "v_mfma_f32_16x16x32_bf16 v[140:143], v[164:167], v[196:199], v[140:143]\n"
    "ds_read_b128 a[240:243], v243 offset:28736\n"
    "v_cvt_pk_f16_f32 v196, v176, v177\n"
    "v_lshlrev_b32_sdwa v197, 1, v178 dst_sel:DWORD dst_unused:UNUSED_PAD src0_sel:DWORD src1_sel:WORD_1\n"
    "v_lshlrev_b32_sdwa v198, 1, v180 dst_sel:DWORD dst_unused:UNUSED_PAD src0_sel:DWORD src1_sel:WORD_1\n"
    "v_lshlrev_b32_sdwa v199, 1, v182 dst_sel:DWORD dst_unused:UNUSED_PAD src0_sel:DWORD src1_sel:WORD_1\n"
    "s_waitcnt lgkmcnt(9)\n"
    "v_or_b32_e32 v189, v189, v205\n"
    "ds_read_b128 a[244:247], v243 offset:29760\n"
    "ds_read_u16 v193, v193\n"
    "v_lshlrev_b32_sdwa v205, 1, v179 dst_sel:DWORD dst_unused:UNUSED_PAD src0_sel:DWORD src1_sel:WORD_1\n"
    "v_pk_fma_f16 v232, v192, v192, s48\n"
    "s_waitcnt lgkmcnt(9)\n"
    "v_mfma_f32_16x16x32_bf16 v[168:171], a[80:83], a[224:227], 0\n"
    "s_nop 0\n"
    "ds_read_b128 a[252:255], v244 offset:32768\n"
    "v_pk_mul_f16 v232, v192, v232\n"
    "v_mfma_f32_16x16x32_bf16 v[172:175], a[84:87], a[224:227], 0\n"
    "ds_read_u16_d16_hi v201, v201\n"
    "v_exp_f16_e32 v234, v232\n"
    "s_waitcnt lgkmcnt(9)\n"
    "v_or_b32_e32 v190, v190, v206\n"
    "v_mfma_f32_16x16x32_bf16 v[168:171], a[128:131], a[228:231], v[168:171]\n"
    "s_waitcnt lgkmcnt(8)\n"
    "ds_read_u16 v194, v194\n"
    "v_lshlrev_b32_sdwa v206, 1, v181 dst_sel:DWORD dst_unused:UNUSED_PAD src0_sel:DWORD src1_sel:WORD_1\n"
    "v_mfma_f32_16x16x32_bf16 v[168:171], a[48:51], a[232:235], v[168:171]\n"
    "v_exp_f16_sdwa v234, v232 dst_sel:WORD_1 dst_unused:UNUSED_PRESERVE src0_sel:WORD_1\n"
    "v_mfma_f32_16x16x32_bf16 v[172:175], a[132:135], a[228:231], v[172:175]\n"
    "ds_read_u16_d16_hi v202, v202\n"
    "v_pk_fma_f16 v234, v234, s49, s49\n"
    "v_rcp_f16_e32 v232, v234\n"
    "v_mfma_f32_16x16x32_bf16 v[96:99], a[192:195], v[184:187], v[96:99]\n"
    "s_waitcnt lgkmcnt(8)\n"
    "ds_read_u16 v195, v195\n"
    "v_lshlrev_b32_sdwa v200, 1, v169 dst_sel:DWORD dst_unused:UNUSED_PAD src0_sel:DWORD src1_sel:WORD_1\n"
    "v_rcp_f16_sdwa v232, v234 dst_sel:WORD_1 dst_unused:UNUSED_PRESERVE src0_sel:WORD_1\n"
    "v_mfma_f32_16x16x32_bf16 v[176:179], a[80:83], a[236:239], 0\n"
    "ds_read_b128 v[236:239], v244 offset:16384\n"
    "v_fma_mix_f32 v234, v192, v232, 0 op_sel_hi:[1,1,0]\n"
    "v_mfma_f32_16x16x32_bf16 v[100:103], a[196:199], v[184:187], v[100:103]\n"
    "s_waitcnt lgkmcnt(8)\n"
    "ds_read_u16_d16_hi v203, v203\n"
    "v_or_b32_e32 v191, v191, v207\n"
    "v_lshlrev_b32_sdwa v207, 1, v183 dst_sel:DWORD dst_unused:UNUSED_PAD src0_sel:DWORD src1_sel:WORD_1\n"
    "v_fma_mix_f32 v232, v192, v232, 0 op_sel:[1,1,0] op_sel_hi:[1,1,0]\n"
    "v_mfma_f32_16x16x32_bf16 v[176:179], a[128:131], a[240:243], v[176:179]\n"
    "s_waitcnt lgkmcnt(8)\n"
    "v_cvt_pk_bf16_f32 v192, v234, v232\n"
    "v_pk_fma_f16 v232, v196, v196, s48\n"
    "v_mfma_f32_16x16x32_bf16 v[176:179], a[48:51], a[244:247], v[176:179]\n"
    "ds_read_b128 v[164:167], v244 offset:18432\n"
    "ds_read_u16 v197, v197\n"
    "v_mfma_f32_16x16x32_bf16 v[180:183], a[84:87], a[236:239], 0\n"
    "s_waitcnt lgkmcnt(8)\n"
    "v_pk_mul_f16 v232, v196, v232\n"
    "v_mfma_f32_16x16x32_bf16 v[180:183], a[132:135], a[240:243], v[180:183]\n"
    "ds_read_u16_d16_hi v205, v205\n"
    "v_mfma_f32_16x16x32_bf16 v[180:183], a[252:255], a[244:247], v[180:183]\n"
    "v_lshlrev_b32_sdwa v204, 1, v177 dst_sel:DWORD dst_unused:UNUSED_PAD src0_sel:DWORD src1_sel:WORD_1\n"
    "v_exp_f16_e32 v234, v232\n"
    "v_mfma_f32_16x16x32_bf16 v[172:175], a[252:255], a[232:235], v[172:175]\n"
    "s_waitcnt lgkmcnt(8)\n"
    "ds_read_u16 v198, v198\n"
    "buffer_store_dwordx4 v[208:211], v245, s[68:71], s59 offen offset:0 sc1\n"
    "v_mfma_f32_16x16x32_bf16 v[112:115], a[200:203], v[184:187], v[112:115]\n"
    "ds_read_u16_d16_hi v206, v206\n"
    "v_or_b32_e32 v193, v193, v201\n"
    "v_mfma_f32_16x16x32_bf16 v[116:119], a[204:207], v[184:187], v[116:119]\n"
    "s_waitcnt lgkmcnt(8)\n"
    "ds_read_u16 v199, v199\n"
    "v_or_b32_e32 v194, v194, v202\n"
    "v_lshlrev_b32_sdwa v201, 1, v171 dst_sel:DWORD dst_unused:UNUSED_PAD src0_sel:DWORD src1_sel:WORD_1\n"
    "v_lshlrev_b32_sdwa v202, 1, v173 dst_sel:DWORD dst_unused:UNUSED_PAD src0_sel:DWORD src1_sel:WORD_1\n"
    "v_exp_f16_sdwa v234, v232 dst_sel:WORD_1 dst_unused:UNUSED_PRESERVE src0_sel:WORD_1\n"
    "v_mfma_f32_16x16x32_bf16 v[104:107], a[192:195], v[188:191], v[104:107]\n"
    "s_waitcnt lgkmcnt(7)\n"
    "ds_read_u16_d16_hi v207, v207\n"
    "v_pk_fma_f16 v234, v234, s49, s49\n"
    "v_mfma_f32_16x16x32_bf16 v[128:131], v[236:239], v[184:187], v[128:131]\n"
    "v_rcp_f16_e32 v232, v234\n"
    "v_mfma_f32_16x16x32_bf16 v[108:111], a[196:199], v[188:191], v[108:111]\n"
    "s_waitcnt lgkmcnt(6)\n"
    "ds_read_b128 a[252:255], v244 offset:36864\n"
    "v_or_b32_e64 v195, v195, v203\n"
    "v_mfma_f32_16x16x32_bf16 v[132:135], v[164:167], v[184:187], v[132:135]\n"
    "ds_read_u16_d16_hi v200, v200\n"
    "v_lshlrev_b32_sdwa v184, 1, v168 dst_sel:DWORD dst_unused:UNUSED_PAD src0_sel:DWORD src1_sel:WORD_1\n"
    "v_mfma_f32_16x16x32_bf16 v[120:123], a[200:203], v[188:191], v[120:123]\n"
    "v_lshlrev_b32_sdwa v185, 1, v170 dst_sel:DWORD dst_unused:UNUSED_PAD src0_sel:DWORD src1_sel:WORD_1\n"
    "v_lshlrev_b32_sdwa v186, 1, v172 dst_sel:DWORD dst_unused:UNUSED_PAD src0_sel:DWORD src1_sel:WORD_1\n"
    "2:\n"
    "v_mfma_f32_16x16x32_bf16 v[124:127], a[204:207], v[188:191], v[124:127]\n"
    "ds_read_u16 v184, v184\n"
    "v_lshlrev_b32_sdwa v187, 1, v174 dst_sel:DWORD dst_unused:UNUSED_PAD src0_sel:DWORD src1_sel:WORD_1\n"
    "v_mfma_f32_16x16x32_bf16 v[136:139], v[236:239], v[188:191], v[136:139]\n"
    "ds_read_u16 v185, v185\n"
    "v_lshlrev_b32_sdwa v203, 1, v175 dst_sel:DWORD dst_unused:UNUSED_PAD src0_sel:DWORD src1_sel:WORD_1\n"
    "v_mfma_f32_16x16x32_bf16 v[140:143], v[164:167], v[188:191], v[140:143]\n"
    "s_waitcnt lgkmcnt(8)\n"
    "v_or_b32_e32 v197, v197, v205\n"
    "v_lshlrev_b32_sdwa v188, 1, v176 dst_sel:DWORD dst_unused:UNUSED_PAD src0_sel:DWORD src1_sel:WORD_1\n"
    "v_mfma_f32_16x16x32_bf16 v[168:171], a[88:91], a[224:227], 0\n"
    "ds_read_u16_d16_hi v201, v201\n"
    "v_lshlrev_b32_sdwa v189, 1, v178 dst_sel:DWORD dst_unused:UNUSED_PAD src0_sel:DWORD src1_sel:WORD_1\n"
    "v_mfma_f32_16x16x32_bf16 v[168:171], a[136:139], a[228:231], v[168:171]\n"
    "v_lshlrev_b32_sdwa v205, 1, v179 dst_sel:DWORD dst_unused:UNUSED_PAD src0_sel:DWORD src1_sel:WORD_1\n"
    "v_lshlrev_b32_sdwa v190, 1, v180 dst_sel:DWORD dst_unused:UNUSED_PAD src0_sel:DWORD src1_sel:WORD_1\n"
    "v_mfma_f32_16x16x32_bf16 v[168:171], a[52:55], a[232:235], v[168:171]\n"
    "ds_read_u16 v186, v186\n"
    "v_lshlrev_b32_sdwa v191, 1, v182 dst_sel:DWORD dst_unused:UNUSED_PAD src0_sel:DWORD src1_sel:WORD_1\n"
    "v_mfma_f32_16x16x32_bf16 v[172:175], a[92:95], a[224:227], 0\n"
    "s_waitcnt lgkmcnt(8)\n"
    "v_or_b32_e32 v198, v198, v206\n"
    "v_lshlrev_b32_sdwa v206, 1, v181 dst_sel:DWORD dst_unused:UNUSED_PAD src0_sel:DWORD src1_sel:WORD_1\n"
    "v_mfma_f32_16x16x32_bf16 v[172:175], a[140:143], a[228:231], v[172:175]\n"
    "ds_read_u16_d16_hi v202, v202\n"
    "v_rcp_f16_sdwa v232, v234 dst_sel:WORD_1 dst_unused:UNUSED_PRESERVE src0_sel:WORD_1\n"
    "v_mfma_f32_16x16x32_bf16 v[176:179], a[88:91], a[236:239], 0\n"
    "ds_read_b128 v[236:239], v244 offset:20480\n"
    "v_fma_mix_f32 v234, v196, v232, 0 op_sel_hi:[1,1,0]\n"
    "v_mfma_f32_16x16x32_bf16 v[176:179], a[136:139], a[240:243], v[176:179]\n"
    "s_waitcnt lgkmcnt(7)\n"
    "ds_read_u16 v187, v187\n"
    "v_or_b32_e32 v199, v199, v207\n"
    "v_mfma_f32_16x16x32_bf16 v[176:179], a[52:55], a[244:247], v[176:179]\n"
    "buffer_store_dwordx4 v[212:215], v245, s[68:71], s59 offen offset:64 sc1\n"
    "ds_read_u16_d16_hi v203, v203\n"
    "v_mfma_f32_16x16x32_bf16 v[172:175], a[252:255], a[232:235], v[172:175]\n"
    "s_nop 0\n"
    "v_lshlrev_b32_sdwa v207, 1, v183 dst_sel:DWORD dst_unused:UNUSED_PAD src0_sel:DWORD src1_sel:WORD_1\n"
    "v_fma_mix_f32 v232, v196, v232, 0 op_sel:[1,1,0] op_sel_hi:[1,1,0]\n"
    "v_mfma_f32_16x16x32_bf16 v[180:183], a[92:95], a[236:239], 0\n"
    "s_waitcnt lgkmcnt(7)\n"
    "ds_read_b128 v[164:167], v244 offset:22528\n"
    "ds_read_u16 v188, v188\n"
    "v_mfma_f32_16x16x32_bf16 v[180:183], a[140:143], a[240:243], v[180:183]\n"
    "v_cvt_pk_bf16_f32 v196, v234, v232\n"
    "v_or_b32_e64 v184, v184, v200\n"
    "v_mfma_f32_16x16x32_bf16 v[180:183], a[252:255], a[244:247], v[180:183]\n"
    "ds_read_u16_d16_hi v204, v204\n"
    "v_mfma_f32_16x16x32_bf16 v[96:99], a[208:211], v[192:195], v[96:99]\n"
    "s_nop 0\n"
    "v_mfma_f32_16x16x32_bf16 v[100:103], a[212:215], v[192:195], v[100:103]\n"
    "s_waitcnt lgkmcnt(8)\n"
    "ds_read_u16 v189, v189\n"
    "v_or_b32_e64 v185, v185, v201\n"
    "v_mfma_f32_16x16x32_bf16 v[112:115], a[216:219], v[192:195], v[112:115]\n"
    "v_lshlrev_b32_sdwa v201, 1, v171 dst_sel:DWORD dst_unused:UNUSED_PAD src0_sel:DWORD src1_sel:WORD_1\n"
    "v_mfma_f32_16x16x32_bf16 v[116:119], a[220:223], v[192:195], v[116:119]\n"
    "ds_read_u16_d16_hi v205, v205\n"
    "v_cvt_pk_bf16_f32 v96, v96, v97\n"
    "v_cvt_pk_bf16_f32 v97, v98, v99\n"
    "v_mfma_f32_16x16x32_bf16 v[104:107], a[208:211], v[196:199], v[104:107]\n"
    "s_waitcnt lgkmcnt(7)\n"
    "ds_read_u16 v190, v190\n"
    "v_or_b32_e32 v186, v186, v202\n"
    "v_mfma_f32_16x16x32_bf16 v[128:131], v[236:239], v[192:195], v[128:131]\n"
    "v_lshlrev_b32_sdwa v202, 1, v173 dst_sel:DWORD dst_unused:UNUSED_PAD src0_sel:DWORD src1_sel:WORD_1\n"
    "v_cvt_pk_bf16_f32 v98, v100, v101\n"
    "v_mfma_f32_16x16x32_bf16 v[108:111], a[212:215], v[196:199], v[108:111]\n"
    "ds_read_u16_d16_hi v206, v206\n"
    "v_cvt_pk_bf16_f32 v99, v102, v103\n"
    "v_cvt_pk_bf16_f32 v112, v112, v113\n"
    "v_cvt_pk_bf16_f32 v113, v114, v115\n"
    "v_mfma_f32_16x16x32_bf16 v[120:123], a[216:219], v[196:199], v[120:123]\n"
    "v_cvt_pk_bf16_f32 v114, v116, v117\n"
    "v_cvt_pk_bf16_f32 v115, v118, v119\n"
    "v_mfma_f32_16x16x32_bf16 v[124:127], a[220:223], v[196:199], v[124:127]\n"
    "s_waitcnt lgkmcnt(6)\n"
    "v_or_b32_e32 v187, v187, v203\n"
    "v_lshlrev_b32_sdwa v203, 1, v175 dst_sel:DWORD dst_unused:UNUSED_PAD src0_sel:DWORD src1_sel:WORD_1\n"
    "v_mfma_f32_16x16x32_bf16 v[132:135], v[164:167], v[192:195], v[132:135]\n"
    "ds_write_b128 v243, v[96:99] offset:24640\n"
    "v_cvt_pk_f16_f32 v192, v168, v169\n"
    "v_lshlrev_b32_sdwa v193, 1, v170 dst_sel:DWORD dst_unused:UNUSED_PAD src0_sel:DWORD src1_sel:WORD_1\n"
    "v_lshlrev_b32_sdwa v194, 1, v172 dst_sel:DWORD dst_unused:UNUSED_PAD src0_sel:DWORD src1_sel:WORD_1\n"
    "v_lshlrev_b32_sdwa v195, 1, v174 dst_sel:DWORD dst_unused:UNUSED_PAD src0_sel:DWORD src1_sel:WORD_1\n"
    "v_cvt_pk_bf16_f32 v128, v128, v129\n"
    "v_cvt_pk_bf16_f32 v129, v130, v131\n"
    "v_mfma_f32_16x16x32_bf16 v[136:139], v[236:239], v[196:199], v[136:139]\n"
    "s_waitcnt lgkmcnt(5)\n"
    "ds_read_u16 v191, v191\n"
    "buffer_store_dwordx4 v[216:219], v245, s[68:71], s59 offen offset:128 sc1\n"
    "v_mfma_f32_16x16x32_bf16 v[140:143], v[164:167], v[196:199], v[140:143]\n"
    "s_nop 0\n"
    "ds_read_u16_d16_hi v207, v207\n"
    "ds_write_b128 v243, v[112:115] offset:26688\n"
    "v_cvt_pk_f16_f32 v196, v176, v177\n"
    "v_lshlrev_b32_sdwa v197, 1, v178 dst_sel:DWORD dst_unused:UNUSED_PAD src0_sel:DWORD src1_sel:WORD_1\n"
    "v_lshlrev_b32_sdwa v198, 1, v180 dst_sel:DWORD dst_unused:UNUSED_PAD src0_sel:DWORD src1_sel:WORD_1\n"
    "v_lshlrev_b32_sdwa v199, 1, v182 dst_sel:DWORD dst_unused:UNUSED_PAD src0_sel:DWORD src1_sel:WORD_1\n"
    "v_or_b32_e64 v188, v188, v204\n"
    "v_cvt_pk_bf16_f32 v130, v132, v133\n"
    "v_cvt_pk_bf16_f32 v131, v134, v135\n"
    "v_mfma_f32_16x16x32_bf16 v[168:171], a[96:99], a[224:227], 0\n"
    "s_waitcnt lgkmcnt(5)\n"
    "ds_read_u16 v193, v193\n"
    "v_or_b32_e32 v189, v189, v205\n"
    "v_mfma_f32_16x16x32_bf16 v[168:171], a[144:147], a[228:231], v[168:171]\n"
    "s_waitcnt vmcnt(24)\n"
    "v_lshlrev_b32_sdwa v205, 1, v179 dst_sel:DWORD dst_unused:UNUSED_PAD src0_sel:DWORD src1_sel:WORD_1\n"
    "v_cvt_pk_bf16_f32 v104, v104, v105\n"
    "v_mfma_f32_16x16x32_bf16 v[168:171], a[56:59], a[232:235], v[168:171]\n"
    "s_waitcnt lgkmcnt(4)\n"
    "ds_read_u16_d16_hi v201, v201\n"
    "ds_write2_b32 v248, v0, v1 offset0:0 offset1:20\n"
    "v_or_b32_e64 v190, v190, v206\n"
    "v_mfma_f32_16x16x32_bf16 v[172:175], a[100:103], a[224:227], 0\n"
    "v_lshlrev_b32_sdwa v206, 1, v181 dst_sel:DWORD dst_unused:UNUSED_PAD src0_sel:DWORD src1_sel:WORD_1\n"
    "v_cvt_pk_bf16_f32 v105, v106, v107\n"
    "v_mfma_f32_16x16x32_bf16 v[172:175], a[148:151], a[228:231], v[172:175]\n"
    "ds_read_u16 v194, v194\n"
    "v_cvt_pk_bf16_f32 v106, v108, v109\n"
    "v_mfma_f32_16x16x32_bf16 v[172:175], a[60:63], a[232:235], v[172:175]\n"
    "ds_write2_b32 v248, v2, v3 offset0:40 offset1:60\n"
    "v_add_u32_e32 v248, 1360, v248\n"
    "v_cvt_pk_bf16_f32 v107, v110, v111\n"
    "v_mfma_f32_16x16x32_bf16 v[176:179], a[96:99], a[236:239], 0\n"
    "s_waitcnt vmcnt(21) lgkmcnt(5)\n"
    "ds_read_u16_d16_hi v202, v202\n"
    "v_or_b32_e32 v191, v191, v207\n"
    "v_mfma_f32_16x16x32_bf16 v[176:179], a[144:147], a[240:243], v[176:179]\n"
    "v_lshlrev_b32_sdwa v207, 1, v183 dst_sel:DWORD dst_unused:UNUSED_PAD src0_sel:DWORD src1_sel:WORD_1\n"
    "v_cvt_pk_bf16_f32 v120, v120, v121\n"
    "v_mfma_f32_16x16x32_bf16 v[176:179], a[56:59], a[244:247], v[176:179]\n"
    "ds_read_u16 v195, v195\n"
    "ds_write2_b32 v235, v20, v21 offset0:0 offset1:20\n"
    "v_mfma_f32_16x16x32_bf16 v[180:183], a[100:103], a[236:239], 0\n"
    "v_cvt_pk_bf16_f32 v121, v122, v123\n"
    "v_cvt_pk_bf16_f32 v122, v124, v125\n"
    "v_mfma_f32_16x16x32_bf16 v[180:183], a[148:151], a[240:243], v[180:183]\n"
    "ds_read_b128 v[236:239], v244 offset:0\n"
    "v_cvt_pk_bf16_f32 v123, v126, v127\n"
    "v_mfma_f32_16x16x32_bf16 v[180:183], a[60:63], a[244:247], v[180:183]\n"
    "s_waitcnt lgkmcnt(6)\n"
    "ds_read_u16_d16_hi v203, v203\n"
    "ds_write2_b32 v235, v22, v23 offset0:40 offset1:60\n"
    "v_mfma_f32_16x16x32_bf16 v[96:99], a[0:3], v[184:187], 0\n"
    "buffer_store_dwordx4 v[220:223], v245, s[68:71], s39 offen offset:0 sc1\n"
    "s_waitcnt vmcnt(21)\n"
    "ds_write2_b32 v248, v4, v5 offset0:0 offset1:20\n"
    "v_add_u32_e32 v235, 1360, v235\n"
    "v_or_b32_e32 v193, v193, v201\n"
    "v_mfma_f32_16x16x32_bf16 v[100:103], a[4:7], v[184:187], 0\n"
    "s_waitcnt vmcnt(19)\n"
    "ds_read_b128 v[164:167], v244 offset:2048\n"
    "ds_write2_b32 v248, v6, v7 offset0:40 offset1:60\n"
    "v_add_u32_e32 v248, 1360, v248\n"
    "v_cvt_pk_bf16_f32 v136, v136, v137\n"
    "v_pk_fma_f16 v232, v192, v192, s48\n"
    "v_mfma_f32_16x16x32_bf16 v[112:115], a[8:11], v[184:187], 0\n"
    "s_waitcnt vmcnt(16) lgkmcnt(6)\n"
    "ds_read_u16 v197, v197\n"
    "ds_write2_b32 v235, v24, v25 offset0:0 offset1:20\n"
    "v_or_b32_e32 v194, v194, v202\n"
    "v_cvt_pk_bf16_f32 v137, v138, v139\n"
    "v_cvt_pk_bf16_f32 v138, v140, v141\n"
    "v_mfma_f32_16x16x32_bf16 v[116:119], a[12:15], v[184:187], 0\n"
    "ds_read_u16_d16_hi v205, v205\n"
    "ds_write2_b32 v235, v26, v27 offset0:40 offset1:60\n"
    "v_add_u32_e32 v235, -1360, v235\n"
    "v_cvt_pk_bf16_f32 v139, v142, v143\n"
    "v_pk_mul_f16 v232, v192, v232\n"
    "v_mfma_f32_16x16x32_bf16 v[108:111], a[4:7], v[188:191], 0\n"
    "s_waitcnt lgkmcnt(6)\n"
    "ds_write_b128 v243, v[128:131] offset:28736\n"
    "v_or_b32_e32 v195, v195, v203\n"
    "v_exp_f16_e32 v234, v232\n"
    "v_lshlrev_b32_sdwa v200, 1, v169 dst_sel:DWORD dst_unused:UNUSED_PAD src0_sel:DWORD src1_sel:WORD_1\n"
    "v_exp_f16_sdwa v234, v232 dst_sel:WORD_1 dst_unused:UNUSED_PRESERVE src0_sel:WORD_1\n"
    "v_mfma_f32_16x16x32_bf16 v[128:131], v[236:239], v[184:187], 0\n"
    "s_waitcnt lgkmcnt(5)\n"
    "ds_read_u16 v198, v198\n"
    "ds_write_b128 v243, v[104:107] offset:25664\n"
    "v_pk_fma_f16 v234, v234, s49, s49\n"
    "v_lshlrev_b32_sdwa v201, 1, v171 dst_sel:DWORD dst_unused:UNUSED_PAD src0_sel:DWORD src1_sel:WORD_1\n"
    "v_lshlrev_b32_sdwa v202, 1, v173 dst_sel:DWORD dst_unused:UNUSED_PAD src0_sel:DWORD src1_sel:WORD_1\n"
    "v_lshlrev_b32_sdwa v203, 1, v175 dst_sel:DWORD dst_unused:UNUSED_PAD src0_sel:DWORD src1_sel:WORD_1\n"
    "v_rcp_f16_e64 v232, v234\n"
    "v_lshlrev_b32_sdwa v204, 1, v177 dst_sel:DWORD dst_unused:UNUSED_PAD src0_sel:DWORD src1_sel:WORD_1\n"
    "v_mfma_f32_16x16x32_bf16 v[132:135], v[164:167], v[184:187], 0\n"
    "s_nop 0\n"
    "ds_read_u16_d16_hi v206, v206\n"
    "s_waitcnt vmcnt(15)\n"
    "ds_write2_b32 v248, v8, v9 offset0:0 offset1:20\n"
    "v_lshlrev_b32_sdwa v184, 1, v168 dst_sel:DWORD dst_unused:UNUSED_PAD src0_sel:DWORD src1_sel:WORD_1\n"
    "v_lshlrev_b32_sdwa v185, 1, v170 dst_sel:DWORD dst_unused:UNUSED_PAD src0_sel:DWORD src1_sel:WORD_1\n"
    "v_lshlrev_b32_sdwa v186, 1, v172 dst_sel:DWORD dst_unused:UNUSED_PAD src0_sel:DWORD src1_sel:WORD_1\n"
    "v_mfma_f32_16x16x32_bf16 v[104:107], a[0:3], v[188:191], 0\n"
    "s_waitcnt lgkmcnt(4)\n"
    "ds_read_u16 v199, v199\n"
    "ds_write_b128 v243, v[120:123] offset:27712\n"
    "v_or_b32_e32 v197, v197, v205\n"
    "v_lshlrev_b32_sdwa v187, 1, v174 dst_sel:DWORD dst_unused:UNUSED_PAD src0_sel:DWORD src1_sel:WORD_1\n"
    "v_rcp_f16_sdwa v232, v234 dst_sel:WORD_1 dst_unused:UNUSED_PRESERVE src0_sel:WORD_1\n"
    "v_mfma_f32_16x16x32_bf16 v[120:123], a[8:11], v[188:191], 0\n"
    "s_nop 0\n"
    "buffer_store_dwordx4 v[224:227], v245, s[68:71], s39 offen offset:64 sc1\n"
    "ds_write2_b32 v248, v10, v11 offset0:40 offset1:60\n"
    "v_add_u32_e32 v248, 1360, v248\n"
    "v_lshlrev_b32_sdwa v205, 1, v179 dst_sel:DWORD dst_unused:UNUSED_PAD src0_sel:DWORD src1_sel:WORD_1\n"
    "v_mfma_f32_16x16x32_bf16 v[124:127], a[12:15], v[188:191], 0\n"
    "s_waitcnt vmcnt(14)\n"
    "ds_read_u16_d16_hi v207, v207\n"
    "ds_write_b128 v243, v[136:139] offset:29760\n"
    "v_fma_mix_f32 v234, v192, v232, 0 op_sel_hi:[1,1,0]\n"
    "v_fma_mix_f32 v232, v192, v232, 0 op_sel:[1,1,0] op_sel_hi:[1,1,0]\n"
    "v_cvt_pk_bf16_f32 v192, v234, v232\n"
    "v_pk_fma_f16 v232, v196, v196, s48\n"
    "v_pk_mul_f16 v232, v196, v232\n"
    "v_exp_f16_e32 v234, v232\n"
    "v_mfma_f32_16x16x32_bf16 v[136:139], v[236:239], v[188:191], 0\n"
    "s_waitcnt lgkmcnt(5)\n"
    "ds_read_u16 v184, v184\n"
    "s_waitcnt vmcnt(11)\n"
    "ds_write2_b32 v248, v12, v13 offset0:0 offset1:20\n"
    "v_or_b32_e32 v198, v198, v206\n"
    "v_lshlrev_b32_sdwa v206, 1, v181 dst_sel:DWORD dst_unused:UNUSED_PAD src0_sel:DWORD src1_sel:WORD_1\n"
    "v_exp_f16_sdwa v234, v232 dst_sel:WORD_1 dst_unused:UNUSED_PRESERVE src0_sel:WORD_1\n"
    "v_mfma_f32_16x16x32_bf16 v[140:143], v[164:167], v[188:191], 0\n"
    "ds_read_b128 v[216:219], v251 offset:0\n"
    "ds_write2_b32 v248, v14, v15 offset0:40 offset1:60\n"
    "v_add_u32_e32 v248, 1360, v248\n"
    "v_lshlrev_b32_sdwa v188, 1, v176 dst_sel:DWORD dst_unused:UNUSED_PAD src0_sel:DWORD src1_sel:WORD_1\n"
    "v_lshlrev_b32_sdwa v189, 1, v178 dst_sel:DWORD dst_unused:UNUSED_PAD src0_sel:DWORD src1_sel:WORD_1\n"
    "v_mfma_f32_16x16x32_bf16 v[168:171], a[104:107], a[224:227], 0\n"
    "s_waitcnt vmcnt(11)\n"
    "ds_read_b128 v[220:223], v251 offset:336\n"
    "ds_read_u16_d16_hi v200, v200\n"
    "v_mfma_f32_16x16x32_bf16 v[168:171], a[152:155], a[228:231], v[168:171]\n"
    "s_nop 0\n"
    "v_lshlrev_b32_sdwa v190, 1, v180 dst_sel:DWORD dst_unused:UNUSED_PAD src0_sel:DWORD src1_sel:WORD_1\n"
    "v_lshlrev_b32_sdwa v191, 1, v182 dst_sel:DWORD dst_unused:UNUSED_PAD src0_sel:DWORD src1_sel:WORD_1\n"
    "v_mfma_f32_16x16x32_bf16 v[168:171], a[64:67], a[232:235], v[168:171]\n"
    "s_waitcnt lgkmcnt(6)\n"
    "ds_read_u16 v185, v185\n"
    "s_waitcnt vmcnt(8)\n"
    "ds_write2_b32 v248, v16, v17 offset0:0 offset1:20\n"
    "v_or_b32_e64 v199, v199, v207\n"
    "v_lshlrev_b32_sdwa v207, 1, v183 dst_sel:DWORD dst_unused:UNUSED_PAD src0_sel:DWORD src1_sel:WORD_1\n"
    "v_pk_fma_f16 v234, v234, s49, s49\n"
    "v_mfma_f32_16x16x32_bf16 v[172:175], a[108:111], a[224:227], 0\n"
    "v_rcp_f16_e64 v232, v234\n"
    "v_mfma_f32_16x16x32_bf16 v[172:175], a[156:159], a[228:231], v[172:175]\n"
    "ds_read_u16_d16_hi v201, v201\n"
    "v_mfma_f32_16x16x32_bf16 v[172:175], a[68:71], a[232:235], v[172:175]\n"
    "buffer_store_dwordx4 v[228:231], v245, s[68:71], s39 offen offset:128 sc1\n"
    "ds_write2_b32 v248, v18, v19 offset0:40 offset1:60\n"
    "v_mfma_f32_32x32x1_2b_f32 v[0:31], v254, v254, 0\n"
    "v_add_u32_e32 v248, -5440, v248\n"
    "v_rcp_f16_sdwa v232, v234 dst_sel:WORD_1 dst_unused:UNUSED_PRESERVE src0_sel:WORD_1\n"
    "ds_read_u16 v186, v186\n"
    "ds_read_u16_d16_hi v202, v202\n"
    "v_fma_mix_f32 v234, v196, v232, 0 op_sel_hi:[1,1,0]\n"
    "v_fma_mix_f32 v232, v196, v232, 0 op_sel:[1,1,0] op_sel_hi:[1,1,0]\n"
    "v_cvt_pk_bf16_f32 v196, v234, v232\n"
    "ds_read_b128 v[236:239], v244 offset:4096\n"
    "ds_read_u16 v187, v187\n"
    "v_mfma_f32_16x16x32_bf16 v[176:179], a[104:107], a[236:239], 0\n"
    "s_waitcnt lgkmcnt(6)\n"
    "ds_read_u16_d16_hi v203, v203\n"
    "v_or_b32_e32 v184, v184, v200\n"
    "v_mfma_f32_16x16x32_bf16 v[176:179], a[152:155], a[240:243], v[176:179]\n"
    "ds_read_b128 v[164:167], v244 offset:6144\n"
    "v_mfma_f32_16x16x32_bf16 v[176:179], a[64:67], a[244:247], v[176:179]\n"
    "s_waitcnt lgkmcnt(6)\n"
    "s_barrier\n"
    "v_mfma_f32_16x16x32_bf16 v[180:183], a[108:111], a[236:239], 0\n"
    "ds_read_b128 v[208:211], v250 offset:0\n"
    "ds_read_b128 v[212:215], v250 offset:320\n"
    "v_mfma_f32_16x16x32_bf16 v[180:183], a[156:159], a[240:243], v[180:183]\n"
    "ds_read_u16 v188, v188\n"
    "ds_read_u16_d16_hi v204, v204\n"
    "v_mfma_f32_16x16x32_bf16 v[180:183], a[68:71], a[244:247], v[180:183]\n"
    "ds_read_u16 v189, v189\n"
    "v_or_b32_e32 v185, v185, v201\n"
    "v_mfma_f32_16x16x32_bf16 v[96:99], a[16:19], v[192:195], v[96:99]\n"
    "s_waitcnt lgkmcnt(8)\n"
    "v_lshlrev_b32_sdwa v201, 1, v171 dst_sel:DWORD dst_unused:UNUSED_PAD src0_sel:DWORD src1_sel:WORD_1\n"
    "v_or_b32_e64 v186, v186, v202\n"
    "v_mfma_f32_16x16x32_bf16 v[100:103], a[20:23], v[192:195], v[100:103]\n"
    "ds_read_u16_d16_hi v205, v205\n"
    "v_lshlrev_b32_sdwa v202, 1, v173 dst_sel:DWORD dst_unused:UNUSED_PAD src0_sel:DWORD src1_sel:WORD_1\n"
    "v_mfma_f32_16x16x32_bf16 v[112:115], a[24:27], v[192:195], v[112:115]\n"
    "ds_read_u16 v190, v190\n"
    "v_mfma_f32_16x16x32_bf16 v[116:119], a[28:31], v[192:195], v[116:119]\n"
    "s_nop 0\n"
    "v_mfma_f32_16x16x32_bf16 v[128:131], v[236:239], v[192:195], v[128:131]\n"
    "s_waitcnt lgkmcnt(7)\n"
    "ds_read_u16_d16_hi v206, v206\n"
    "v_or_b32_e32 v187, v187, v203\n"
    "v_mfma_f32_16x16x32_bf16 v[132:135], v[164:167], v[192:195], v[132:135]\n"
    "v_cvt_pk_f16_f32 v192, v168, v169\n"
    "v_lshlrev_b32_sdwa v193, 1, v170 dst_sel:DWORD dst_unused:UNUSED_PAD src0_sel:DWORD src1_sel:WORD_1\n"
    "v_mfma_f32_16x16x32_bf16 v[104:107], a[16:19], v[196:199], v[104:107]\n"
    "s_waitcnt lgkmcnt(6)\n"
    "ds_read_u16 v191, v191\n"
    "v_lshlrev_b32_sdwa v194, 1, v172 dst_sel:DWORD dst_unused:UNUSED_PAD src0_sel:DWORD src1_sel:WORD_1\n"
    "v_smfmac_f32_16x16x64_bf16 v[0:3], v[216:219], v[208:215], v240\n"
    "ds_read_b128 v[216:219], v252 offset:0\n"
    "v_smfmac_f32_16x16x64_bf16 v[4:7], v[220:223], v[208:215], v240 abid:1\n"
    "s_nop 0\n"
    "ds_read_b128 v[208:211], v250 offset:640\n"
    "v_mfma_f32_16x16x32_bf16 v[108:111], a[20:23], v[196:199], v[108:111]\n"
    "s_waitcnt vmcnt(6)\n"
    "v_lshlrev_b32_sdwa v195, 1, v174 dst_sel:DWORD dst_unused:UNUSED_PAD src0_sel:DWORD src1_sel:WORD_1\n"
    "v_lshlrev_b32_sdwa v203, 1, v175 dst_sel:DWORD dst_unused:UNUSED_PAD src0_sel:DWORD src1_sel:WORD_1\n"
    "v_mfma_f32_16x16x32_bf16 v[120:123], a[24:27], v[196:199], v[120:123]\n"
    "ds_read_b128 v[212:215], v250 offset:960\n"
    "ds_read_b128 v[220:223], v252 offset:336\n"
    "v_mfma_f32_16x16x32_bf16 v[124:127], a[28:31], v[196:199], v[124:127]\n"
    "s_waitcnt lgkmcnt(9)\n"
    "v_or_b32_e32 v188, v188, v204\n"
    "v_pk_fma_f16 v232, v192, v192, s48\n"
    "v_mfma_f32_16x16x32_bf16 v[136:139], v[236:239], v[196:199], v[136:139]\n"
    "ds_read_u16_d16_hi v207, v207\n"
    "v_pk_mul_f16 v232, v192, v232\n"
    "v_mfma_f32_16x16x32_bf16 v[140:143], v[164:167], v[196:199], v[140:143]\n"
    "ds_read_u16 v193, v193\n"
    "v_cvt_pk_f16_f32 v196, v176, v177\n"
    "v_lshlrev_b32_sdwa v197, 1, v178 dst_sel:DWORD dst_unused:UNUSED_PAD src0_sel:DWORD src1_sel:WORD_1\n"
    "v_lshlrev_b32_sdwa v198, 1, v180 dst_sel:DWORD dst_unused:UNUSED_PAD src0_sel:DWORD src1_sel:WORD_1\n"
    "v_lshlrev_b32_sdwa v199, 1, v182 dst_sel:DWORD dst_unused:UNUSED_PAD src0_sel:DWORD src1_sel:WORD_1\n"
    "v_smfmac_f32_32x32x32_bf16 v[112:127], a[248:251], v[152:159], v241\n"
    "s_waitcnt lgkmcnt(9)\n"
    "ds_read_b128 a[252:255], v244 offset:34816\n"
    "v_or_b32_e32 v189, v189, v205\n"
    "v_lshlrev_b32_sdwa v205, 1, v179 dst_sel:DWORD dst_unused:UNUSED_PAD src0_sel:DWORD src1_sel:WORD_1\n"
    "v_exp_f16_e32 v234, v232\n"
    "s_waitcnt vmcnt(5)\n"
    "v_smfmac_f32_32x32x32_bf16 v[96:111], a[248:251], v[144:151], v241\n"
    "ds_read_u16_d16_hi v201, v201\n"
    "buffer_load_dwordx4 v[144:147], v245, s[40:43], s57 offen offset:128\n"
    "v_exp_f16_sdwa v234, v232 dst_sel:WORD_1 dst_unused:UNUSED_PRESERVE src0_sel:WORD_1\n"
    "ds_read_u16 v194, v194\n"
    "v_mfma_f32_16x16x32_bf16 v[168:171], a[112:115], a[224:227], 0\n"
    "s_waitcnt lgkmcnt(10)\n"
    "ds_read_u16_d16_hi v202, v202\n"
    "v_or_b32_e32 v190, v190, v206\n"
    "v_mfma_f32_16x16x32_bf16 v[168:171], a[160:163], a[228:231], v[168:171]\n"
    "v_lshlrev_b32_sdwa v206, 1, v181 dst_sel:DWORD dst_unused:UNUSED_PAD src0_sel:DWORD src1_sel:WORD_1\n"
    "v_pk_fma_f16 v234, v234, s49, s49\n"
    "v_mfma_f32_16x16x32_bf16 v[168:171], a[72:75], a[232:235], v[168:171]\n"
    "s_waitcnt lgkmcnt(7)\n"
    "ds_read_u16 v195, v195\n"
    "v_rcp_f16_e64 v232, v234\n"
    "v_smfmac_f32_16x16x64_bf16 v[0:3], v[216:219], v[208:215], v240\n"
    "s_waitcnt lgkmcnt(7)\n"
    "ds_read_b128 v[216:219], v251 offset:672\n"
    "v_rcp_f16_sdwa v232, v234 dst_sel:WORD_1 dst_unused:UNUSED_PRESERVE src0_sel:WORD_1\n"
    "v_smfmac_f32_16x16x64_bf16 v[4:7], v[220:223], v[208:215], v240 abid:1\n"
    "ds_read_b128 v[208:211], v250 offset:80\n"
    "v_mfma_f32_16x16x32_bf16 v[172:175], a[116:119], a[224:227], 0\n"
    "ds_read_b128 v[212:215], v250 offset:400\n"
    "v_lshlrev_b32_sdwa v200, 1, v169 dst_sel:DWORD dst_unused:UNUSED_PAD src0_sel:DWORD src1_sel:WORD_1\n"
    "v_fma_mix_f32 v234, v192, v232, 0 op_sel_hi:[1,1,0]\n"
    "ds_read_b128 v[220:223], v251 offset:1008\n"
    "v_pk_mov_b32 v[224:225], v[0:1], v[0:1] op_sel:[0,1]\n"
    "v_pk_mov_b32 v[226:227], v[2:3], v[2:3] op_sel:[0,1]\n"
    "v_pk_mul_f32 v[228:229], v[0:1], v[0:1]\n"
    "v_pk_mul_f32 v[230:231], v[2:3], v[2:3]\n"
    "v_pk_add_f32 v[224:225], v[224:225], v[4:5]\n"
    "v_pk_add_f32 v[226:227], v[226:227], v[6:7]\n"
    "v_pk_fma_f32 v[228:229], v[4:5], v[4:5], v[228:229]\n"
    "v_pk_fma_f32 v[230:231], v[6:7], v[6:7], v[230:231]\n"
    "v_mfma_f32_16x16x32_bf16 v[172:175], a[164:167], a[228:231], v[172:175]\n"
    "s_waitcnt lgkmcnt(8)\n"
    "ds_read_b128 v[236:239], v244 offset:8192\n"
    "ds_read_b128 v[152:155], v242 offset:24640\n"
    "v_mfma_f32_16x16x32_bf16 v[172:175], a[252:255], a[232:235], v[172:175]\n"
    "s_waitcnt lgkmcnt(9)\n"
    "ds_read_b128 v[156:159], v242 offset:25664\n"
    "ds_read_u16_d16_hi v203, v203\n"
    "v_or_b32_e64 v193, v193, v201\n"
    "v_or_b32_e64 v191, v191, v207\n"
    "v_mfma_f32_16x16x32_bf16 v[176:179], a[112:115], a[236:239], 0\n"
    "ds_read_b128 v[164:167], v244 offset:10240\n"
    "buffer_load_dwordx4 v[148:151], v245, s[40:43], s88 offen offset:128\n"
    "v_mfma_f32_16x16x32_bf16 v[176:179], a[160:163], a[240:243], v[176:179]\n"
    "ds_read_u16 v197, v197\n"
    "v_lshlrev_b32_e32 v254, 3, v244\n"
    "v_mfma_f32_16x16x32_bf16 v[176:179], a[72:75], a[244:247], v[176:179]\n"
    "s_waitcnt lgkmcnt(11)\n"
    "v_lshlrev_b32_sdwa v207, 1, v183 dst_sel:DWORD dst_unused:UNUSED_PAD src0_sel:DWORD src1_sel:WORD_1\n"
    "v_or_b32_e64 v194, v194, v202\n"
    "v_mfma_f32_16x16x32_bf16 v[180:183], a[116:119], a[236:239], 0\n"
    "ds_read_u16_d16_hi v205, v205\n"
    "v_lshlrev_b32_sdwa v201, 1, v171 dst_sel:DWORD dst_unused:UNUSED_PAD src0_sel:DWORD src1_sel:WORD_1\n"
    "v_mfma_f32_16x16x32_bf16 v[180:183], a[164:167], a[240:243], v[180:183]\n"
    "v_lshlrev_b32_sdwa v202, 1, v173 dst_sel:DWORD dst_unused:UNUSED_PAD src0_sel:DWORD src1_sel:WORD_1\n"
    "v_lshlrev_b32_sdwa v204, 1, v177 dst_sel:DWORD dst_unused:UNUSED_PAD src0_sel:DWORD src1_sel:WORD_1\n"
    "v_mfma_f32_16x16x32_bf16 v[180:183], a[252:255], a[244:247], v[180:183]\n"
    "ds_read_u16 v198, v198\n"
    "v_fma_mix_f32 v232, v192, v232, 0 op_sel:[1,1,0] op_sel_hi:[1,1,0]\n"
    "v_cvt_pk_bf16_f32 v192, v234, v232\n"
    "v_mfma_f32_16x16x32_bf16 v[112:115], a[40:43], v[184:187], v[112:115]\n"
    "s_waitcnt lgkmcnt(8)\n"
    "ds_read_u16_d16_hi v206, v206\n"
    "v_pk_fma_f16 v232, v196, v196, s48\n"
    "v_smfmac_f32_16x16x64_bf16 v[8:11], v[216:219], v[208:215], v240\n"
    "s_waitcnt lgkmcnt(8)\n"
    "v_pk_mul_f16 v232, v196, v232\n"
    "v_smfmac_f32_16x16x64_bf16 v[12:15], v[220:223], v[208:215], v240 abid:1\n"
    "ds_read_b128 v[208:211], v250 offset:720\n"
    "v_mfma_f32_16x16x32_bf16 v[116:119], a[44:47], v[184:187], v[116:119]\n"
    "s_waitcnt lgkmcnt(7)\n"
    "ds_read_b128 v[212:215], v250 offset:1040\n"
    "v_exp_f16_e32 v234, v232\n"
    "v_smfmac_f32_32x32x32_bf16 v[96:111], a[248:251], v[152:159], v241\n"
    "ds_read_b128 v[216:219], v252 offset:672\n"
    "ds_read_b128 v[220:223], v252 offset:1008\n"
    "v_exp_f16_sdwa v234, v232 dst_sel:WORD_1 dst_unused:UNUSED_PRESERVE src0_sel:WORD_1\n"
    "v_mfma_f32_16x16x32_bf16 v[128:131], v[236:239], v[184:187], v[128:131]\n"
    "s_waitcnt lgkmcnt(8)\n"
    "s_mov_b32 exec_hi, 0\n"
    "buffer_load_dword v233, v254, s[80:83], s84 offen sc1\n"
    "s_mov_b32 exec_hi, -1\n"
    "v_mfma_f32_16x16x32_bf16 v[132:135], v[164:167], v[184:187], v[132:135]\n"
    "s_nop 0\n"
    "ds_read_u16 v199, v199\n"
    "ds_read_u16_d16_hi v207, v207\n"
    "v_mfma_f32_16x16x32_bf16 v[96:99], a[32:35], v[184:187], v[96:99]\n"
    "v_or_b32_e64 v195, v195, v203\n"
    "v_lshlrev_b32_sdwa v203, 1, v175 dst_sel:DWORD dst_unused:UNUSED_PAD src0_sel:DWORD src1_sel:WORD_1\n"
    "v_mfma_f32_16x16x32_bf16 v[100:103], a[36:39], v[184:187], v[100:103]\n"
    "ds_read_b128 v[152:155], v242 offset:26688\n"
    "v_lshlrev_b32_sdwa v184, 1, v168 dst_sel:DWORD dst_unused:UNUSED_PAD src0_sel:DWORD src1_sel:WORD_1\n"
    "v_mfma_f32_16x16x32_bf16 v[104:107], a[32:35], v[188:191], v[104:107]\n"
    "s_waitcnt lgkmcnt(9)\n"
    "ds_read_b128 v[156:159], v242 offset:27712\n"
    "v_or_b32_e32 v197, v197, v205\n"
    "v_mfma_f32_16x16x32_bf16 v[108:111], a[36:39], v[188:191], v[108:111]\n"
    "ds_read_u16 v184, v184\n"
    "v_lshlrev_b32_sdwa v185, 1, v170 dst_sel:DWORD dst_unused:UNUSED_PAD src0_sel:DWORD src1_sel:WORD_1\n"
    "v_mfma_f32_16x16x32_bf16 v[120:123], a[40:43], v[188:191], v[120:123]\n"
    "v_lshlrev_b32_sdwa v186, 1, v172 dst_sel:DWORD dst_unused:UNUSED_PAD src0_sel:DWORD src1_sel:WORD_1\n"
    "v_lshlrev_b32_sdwa v187, 1, v174 dst_sel:DWORD dst_unused:UNUSED_PAD src0_sel:DWORD src1_sel:WORD_1\n"
    "v_mfma_f32_16x16x32_bf16 v[124:127], a[44:47], v[188:191], v[124:127]\n"
    "ds_read_b128 a[252:255], v244 offset:38912\n"
    "v_lshlrev_b32_sdwa v205, 1, v179 dst_sel:DWORD dst_unused:UNUSED_PAD src0_sel:DWORD src1_sel:WORD_1\n"
    "v_mfma_f32_16x16x32_bf16 v[136:139], v[236:239], v[188:191], v[136:139]\n"
    "s_waitcnt lgkmcnt(9)\n"
    "ds_read_u16_d16_hi v200, v200\n"
    "v_or_b32_e64 v198, v198, v206\n"
    "v_lshlrev_b32_sdwa v206, 1, v181 dst_sel:DWORD dst_unused:UNUSED_PAD src0_sel:DWORD src1_sel:WORD_1\n"
    "v_mfma_f32_16x16x32_bf16 v[140:143], v[164:167], v[188:191], v[140:143]\n"
    "s_waitcnt lgkmcnt(8)\n"
    "ds_read_u16 v185, v185\n"
    "v_lshlrev_b32_sdwa v188, 1, v176 dst_sel:DWORD dst_unused:UNUSED_PAD src0_sel:DWORD src1_sel:WORD_1\n"
    "v_lshlrev_b32_sdwa v189, 1, v178 dst_sel:DWORD dst_unused:UNUSED_PAD src0_sel:DWORD src1_sel:WORD_1\n"
    "v_lshlrev_b32_sdwa v190, 1, v180 dst_sel:DWORD dst_unused:UNUSED_PAD src0_sel:DWORD src1_sel:WORD_1\n"
    "v_lshlrev_b32_sdwa v191, 1, v182 dst_sel:DWORD dst_unused:UNUSED_PAD src0_sel:DWORD src1_sel:WORD_1\n"
    "v_smfmac_f32_16x16x64_bf16 v[8:11], v[216:219], v[208:215], v240\n"
    "s_waitcnt lgkmcnt(8)\n"
    "ds_read_b128 v[216:219], v251 offset:1344\n"
    "v_smfmac_f32_16x16x64_bf16 v[12:15], v[220:223], v[208:215], v240 abid:1\n"
    "s_nop 0\n"
    "ds_read_b128 v[208:211], v250 offset:160\n"
    "v_mfma_f32_16x16x32_bf16 v[168:171], a[120:123], a[224:227], 0\n"
    "ds_read_b128 v[212:215], v250 offset:480\n"
    "v_pk_fma_f16 v234, v234, s49, s49\n"
    "v_mfma_f32_16x16x32_bf16 v[168:171], a[168:171], a[228:231], v[168:171]\n"
    "ds_read_b128 v[220:223], v251 offset:1680\n"
    "v_rcp_f16_e32 v232, v234\n"
    "v_mfma_f32_16x16x32_bf16 v[168:171], a[76:79], a[232:235], v[168:171]\n"
    "s_waitcnt lgkmcnt(9)\n"
    "ds_read_u16_d16_hi v201, v201\n"
    "v_or_b32_e64 v199, v199, v207\n"
    "v_lshlrev_b32_sdwa v207, 1, v183 dst_sel:DWORD dst_unused:UNUSED_PAD src0_sel:DWORD src1_sel:WORD_1\n"
    "v_rcp_f16_sdwa v232, v234 dst_sel:WORD_1 dst_unused:UNUSED_PRESERVE src0_sel:WORD_1\n"
    "v_pk_add_f32 v[224:225], v[224:225], v[8:9]\n"
    "v_pk_add_f32 v[226:227], v[226:227], v[10:11]\n"
    "v_pk_fma_f32 v[228:229], v[8:9], v[8:9], v[228:229]\n"
    "v_pk_fma_f32 v[230:231], v[10:11], v[10:11], v[230:231]\n"
    "v_pk_add_f32 v[224:225], v[224:225], v[12:13]\n"
    "v_pk_add_f32 v[226:227], v[226:227], v[14:15]\n"
    "v_pk_fma_f32 v[228:229], v[12:13], v[12:13], v[228:229]\n"
    "v_pk_fma_f32 v[230:231], v[14:15], v[14:15], v[230:231]\n"
    "v_mfma_f32_16x16x32_bf16 v[172:175], a[124:127], a[224:227], 0\n"
    "s_waitcnt lgkmcnt(7)\n"
    "ds_read_u16 v186, v186\n"
    "ds_read_u16_d16_hi v202, v202\n"
    "v_mfma_f32_16x16x32_bf16 v[172:175], a[172:175], a[228:231], v[172:175]\n"
    "s_nop 0\n"
    "v_fma_mix_f32 v234, v196, v232, 0 op_sel_hi:[1,1,0]\n"
    "v_fma_mix_f32 v232, v196, v232, 0 op_sel:[1,1,0] op_sel_hi:[1,1,0]\n"
    "v_mfma_f32_16x16x32_bf16 v[172:175], a[252:255], a[232:235], v[172:175]\n"
    "ds_read_b128 v[236:239], v244 offset:12288\n"
    "ds_read_u16 v187, v187\n"
    "v_smfmac_f32_32x32x32_bf16 v[112:127], a[248:251], v[152:159], v241\n"
    "s_waitcnt lgkmcnt(10)\n"
    "v_cvt_pk_bf16_f32 v196, v234, v232\n"
    "v_or_b32_e32 v184, v184, v200\n"
    "v_mfma_f32_16x16x32_bf16 v[176:179], a[120:123], a[236:239], 0\n"
    "ds_read_u16_d16_hi v203, v203\n"
    "v_mfma_f32_16x16x32_bf16 v[176:179], a[168:171], a[240:243], v[176:179]\n"
    "s_waitcnt lgkmcnt(7)\n"
    "ds_read_b128 v[164:167], v244 offset:14336\n"
    "v_mfma_f32_16x16x32_bf16 v[176:179], a[76:79], a[244:247], v[176:179]\n"
    "s_waitcnt lgkmcnt(7)\n"
    "ds_read_u16 v188, v188\n"
    "v_smfmac_f32_16x16x64_bf16 v[16:19], v[216:219], v[208:215], v240\n"
    "v_smfmac_f32_16x16x64_bf16 v[20:23], v[220:223], v[208:215], v240 abid:1\n"
    "ds_read_b128 v[208:211], v250 offset:800\n"
    "ds_read_b128 v[212:215], v250 offset:1120\n"
    "v_mfma_f32_16x16x32_bf16 v[180:183], a[124:127], a[236:239], 0\n"
    "s_waitcnt lgkmcnt(9)\n"
    "v_or_b32_e32 v185, v185, v201\n"
    "v_lshlrev_b32_sdwa v201, 1, v171 dst_sel:DWORD dst_unused:UNUSED_PAD src0_sel:DWORD src1_sel:WORD_1\n"
    "v_mfma_f32_16x16x32_bf16 v[180:183], a[172:175], a[240:243], v[180:183]\n"
    "ds_read_b128 v[216:219], v252 offset:1344\n"
    "ds_read_b128 v[220:223], v252 offset:1680\n"
    "v_mfma_f32_16x16x32_bf16 v[180:183], a[252:255], a[244:247], v[180:183]\n"
    "s_nop 0\n"
    "v_mfma_f32_16x16x32_bf16 v[96:99], a[176:179], v[192:195], v[96:99]\n"
    "s_waitcnt lgkmcnt(8)\n"
    "ds_read_u16_d16_hi v204, v204\n"
    "v_or_b32_e64 v186, v186, v202\n"
    "v_mfma_f32_16x16x32_bf16 v[100:103], a[180:183], v[192:195], v[100:103]\n"
    "ds_read_u16 v189, v189\n"
    "v_lshlrev_b32_sdwa v202, 1, v173 dst_sel:DWORD dst_unused:UNUSED_PAD src0_sel:DWORD src1_sel:WORD_1\n"
    "v_mfma_f32_16x16x32_bf16 v[112:115], a[184:187], v[192:195], v[112:115]\n"
    "v_mfma_f32_16x16x32_bf16 v[116:119], a[188:191], v[192:195], v[116:119]\n"
    "ds_read_b128 v[152:155], v242 offset:28736\n"
    "v_mfma_f32_16x16x32_bf16 v[128:131], v[236:239], v[192:195], v[128:131]\n"
    "ds_read_b128 v[156:159], v242 offset:29760\n"
    "v_mfma_f32_16x16x32_bf16 v[104:107], a[176:179], v[196:199], v[104:107]\n"
    "s_waitcnt lgkmcnt(9)\n"
    "ds_read_u16_d16_hi v205, v205\n"
    "v_or_b32_e32 v187, v187, v203\n"
    "v_mfma_f32_16x16x32_bf16 v[132:135], v[164:167], v[192:195], v[132:135]\n"
    "v_cvt_pk_f16_f32 v192, v168, v169\n"
    "v_lshlrev_b32_sdwa v193, 1, v170 dst_sel:DWORD dst_unused:UNUSED_PAD src0_sel:DWORD src1_sel:WORD_1\n"
    "v_mfma_f32_16x16x32_bf16 v[108:111], a[180:183], v[196:199], v[108:111]\n"
    "ds_read_u16 v190, v190\n"
    "v_lshlrev_b32_sdwa v194, 1, v172 dst_sel:DWORD dst_unused:UNUSED_PAD src0_sel:DWORD src1_sel:WORD_1\n"
    "v_lshlrev_b32_sdwa v195, 1, v174 dst_sel:DWORD dst_unused:UNUSED_PAD src0_sel:DWORD src1_sel:WORD_1\n"
    "v_mfma_f32_16x16x32_bf16 v[120:123], a[184:187], v[196:199], v[120:123]\n"
    "s_nop 0\n"
    "ds_read_b128 a[224:227], v242 offset:0\n"
    "v_lshlrev_b32_sdwa v203, 1, v175 dst_sel:DWORD dst_unused:UNUSED_PAD src0_sel:DWORD src1_sel:WORD_1\n"
    "v_mfma_f32_16x16x32_bf16 v[124:127], a[188:191], v[196:199], v[124:127]\n"
    "s_waitcnt lgkmcnt(8)\n"
    "ds_read_u16_d16_hi v206, v206\n"
    "v_pk_fma_f16 v232, v192, v192, s48\n"
    "v_pk_mul_f16 v232, v192, v232\n"
    "v_exp_f16_e32 v234, v232\n"
    "v_smfmac_f32_16x16x64_bf16 v[16:19], v[216:219], v[208:215], v240\n"
    "s_waitcnt lgkmcnt(8)\n"
    "ds_read_b128 v[216:219], v251 offset:2016\n"
    "v_smfmac_f32_16x16x64_bf16 v[20:23], v[220:223], v[208:215], v240 abid:1\n"
    "ds_read_b128 v[208:211], v250 offset:240\n"
    "v_mfma_f32_16x16x32_bf16 v[136:139], v[236:239], v[196:199], v[136:139]\n"
    "s_nop 0\n"
    "ds_read_b128 v[212:215], v250 offset:560\n"
    "v_exp_f16_sdwa v234, v232 dst_sel:WORD_1 dst_unused:UNUSED_PRESERVE src0_sel:WORD_1\n"
    "v_mfma_f32_16x16x32_bf16 v[140:143], v[164:167], v[196:199], v[140:143]\n"
    "s_waitcnt lgkmcnt(10)\n"
    "ds_read_b128 v[220:223], v251 offset:2352\n"
    "v_cvt_pk_f16_f32 v196, v176, v177\n"
    "v_lshlrev_b32_sdwa v197, 1, v178 dst_sel:DWORD dst_unused:UNUSED_PAD src0_sel:DWORD src1_sel:WORD_1\n"
    "ds_read_b128 a[228:231], v242 offset:1024\n"
    "v_pk_add_f32 v[224:225], v[224:225], v[16:17]\n"
    "v_pk_add_f32 v[226:227], v[226:227], v[18:19]\n"
    "v_pk_fma_f32 v[228:229], v[16:17], v[16:17], v[228:229]\n"
    "v_pk_fma_f32 v[230:231], v[18:19], v[18:19], v[230:231]\n"
    "v_pk_add_f32 v[224:225], v[224:225], v[20:21]\n"
    "v_pk_add_f32 v[226:227], v[226:227], v[22:23]\n"
    "v_pk_fma_f32 v[228:229], v[20:21], v[20:21], v[228:229]\n"
    "v_pk_fma_f32 v[230:231], v[22:23], v[22:23], v[230:231]\n"
    "s_waitcnt lgkmcnt(9)\n"
    "v_smfmac_f32_32x32x32_bf16 v[128:143], a[248:251], v[152:159], v241\n"
    "s_waitcnt lgkmcnt(8)\n"
    "ds_read_u16 v191, v191\n"
    "ds_read_b128 a[232:235], v242 offset:2048\n"
    "v_lshlrev_b32_e64 v254, 3, v244\n"
    "v_lshlrev_b32_sdwa v198, 1, v180 dst_sel:DWORD dst_unused:UNUSED_PAD src0_sel:DWORD src1_sel:WORD_1\n"
    "v_lshlrev_b32_sdwa v199, 1, v182 dst_sel:DWORD dst_unused:UNUSED_PAD src0_sel:DWORD src1_sel:WORD_1\n"
    "v_or_b32_e32 v188, v188, v204\n"
    "s_waitcnt lgkmcnt(8)\n"
    "v_mfma_f32_16x16x32_bf16 v[168:171], a[80:83], a[224:227], 0\n"
    "ds_read_u16_d16_hi v207, v207\n"
    "ds_read_b128 a[236:239], v242 offset:3072\n"
    "v_mfma_f32_16x16x32_bf16 v[172:175], a[84:87], a[224:227], 0\n"
    "ds_read_b128 a[240:243], v242 offset:4096\n"
    "v_or_b32_e64 v189, v189, v205\n"
    "ds_read_b128 a[244:247], v242 offset:5120\n"
    "v_lshlrev_b32_sdwa v205, 1, v179 dst_sel:DWORD dst_unused:UNUSED_PAD src0_sel:DWORD src1_sel:WORD_1\n"
    "s_waitcnt lgkmcnt(10)\n"
    "v_or_b32_e32 v190, v190, v206\n"
    "v_mfma_f32_16x16x32_bf16 v[96:99], a[192:195], v[184:187], v[96:99]\n"
    "ds_read_u16 v193, v193\n"
    "v_lshlrev_b32_sdwa v206, 1, v181 dst_sel:DWORD dst_unused:UNUSED_PAD src0_sel:DWORD src1_sel:WORD_1\n"
    "v_mfma_f32_16x16x32_bf16 v[100:103], a[196:199], v[184:187], v[100:103]\n"
    "s_waitcnt lgkmcnt(9)\n"
    "ds_read_b128 a[252:255], v244 offset:32768\n"
    "v_pk_fma_f16 v234, v234, s49, s49\n"
    "v_smfmac_f32_16x16x64_bf16 v[24:27], v[216:219], v[208:215], v240\n"
    "s_waitcnt lgkmcnt(9)\n"
    "ds_read_b128 v[216:219], v252 offset:2016\n"
    "v_smfmac_f32_16x16x64_bf16 v[28:31], v[220:223], v[208:215], v240 abid:1\n"
    "s_waitcnt lgkmcnt(9)\n"
    "ds_read_b128 v[208:211], v250 offset:880\n"
    "s_mov_b32 exec_hi, 0\n"
    "buffer_load_dword v233, v254, s[72:75], s53 offen sc1\n"
    "s_mov_b32 exec_hi, -1\n"
    "v_mfma_f32_16x16x32_bf16 v[168:171], a[128:131], a[228:231], v[168:171]\n"
    "s_nop 0\n"
    "ds_read_b128 v[212:215], v250 offset:1200\n"
    "ds_read_b128 v[220:223], v252 offset:2352\n"
    "v_mfma_f32_16x16x32_bf16 v[172:175], a[132:135], a[228:231], v[172:175]\n"
    "s_waitcnt lgkmcnt(10)\n"
    "ds_read_u16_d16_hi v201, v201\n"
    "v_rcp_f16_e32 v232, v234\n"
    "v_mov_b32_e64 v254, 0\n"
    "v_rcp_f16_sdwa v232, v234 dst_sel:WORD_1 dst_unused:UNUSED_PRESERVE src0_sel:WORD_1\n"
    "v_mfma_f32_16x16x32_bf16 v[168:171], a[48:51], a[232:235], v[168:171]\n"
    "ds_read_u16 v194, v194\n"
    "v_fma_mix_f32 v234, v192, v232, 0 op_sel_hi:[1,1,0]\n"
    "v_fma_mix_f32 v232, v192, v232, 0 op_sel:[1,1,0] op_sel_hi:[1,1,0]\n"
    "v_cvt_pk_bf16_f32 v192, v234, v232\n"
    "v_mfma_f32_16x16x32_bf16 v[112:115], a[200:203], v[184:187], v[112:115]\n"
    "s_waitcnt lgkmcnt(9)\n"
    "ds_read_u16_d16_hi v202, v202\n"
    "v_or_b32_e32 v191, v191, v207\n"
    "v_lshlrev_b32_sdwa v207, 1, v183 dst_sel:DWORD dst_unused:UNUSED_PAD src0_sel:DWORD src1_sel:WORD_1\n"
    "v_lshlrev_b32_sdwa v200, 1, v169 dst_sel:DWORD dst_unused:UNUSED_PAD src0_sel:DWORD src1_sel:WORD_1\n"
    "v_mfma_f32_16x16x32_bf16 v[176:179], a[80:83], a[236:239], 0\n"
    "s_add_u32 s60, s60, 1572864\n"
    "v_pk_fma_f16 v232, v196, v196, s48\n"
    "v_pk_mul_f16 v232, v196, v232\n"
    "v_mfma_f32_16x16x32_bf16 v[176:179], a[128:131], a[240:243], v[176:179]\n"
    "s_waitcnt lgkmcnt(9)\n"
    "ds_read_u16 v195, v195\n"
    "v_mfma_f32_16x16x32_bf16 v[176:179], a[48:51], a[244:247], v[176:179]\n"
    "s_waitcnt lgkmcnt(8)\n"
    "ds_read_b128 v[236:239], v244 offset:16384\n"
    "v_exp_f16_e64 v234, v232\n"
    "v_mfma_f32_16x16x32_bf16 v[172:175], a[252:255], a[232:235], v[172:175]\n"
    "s_add_u32 s61, s61, 1572864\n"
    "ds_read_u16_d16_hi v203, v203\n"
    "v_exp_f16_sdwa v234, v232 dst_sel:WORD_1 dst_unused:UNUSED_PRESERVE src0_sel:WORD_1\n"
    "v_mfma_f32_16x16x32_bf16 v[180:183], a[84:87], a[236:239], 0\n"
    "s_add_u32 s62, s62, 1572864\n"
    "v_lshlrev_b32_sdwa v204, 1, v177 dst_sel:DWORD dst_unused:UNUSED_PAD src0_sel:DWORD src1_sel:WORD_1\n"
    "v_pk_fma_f16 v234, v234, s49, s49\n"
    "v_mfma_f32_16x16x32_bf16 v[180:183], a[132:135], a[240:243], v[180:183]\n"
    "s_waitcnt lgkmcnt(7)\n"
    "ds_read_b128 v[164:167], v244 offset:18432\n"
    "ds_read_u16 v197, v197\n"
    "v_mfma_f32_16x16x32_bf16 v[180:183], a[252:255], a[244:247], v[180:183]\n"
    "s_waitcnt lgkmcnt(8)\n"
    "s_barrier\n"
    "v_smfmac_f32_16x16x64_bf16 v[24:27], v[216:219], v[208:215], v240\n"
    "s_waitcnt lgkmcnt(7)\n"
    "ds_write2_b32 v248, v32, v33 offset0:0 offset1:20\n"
    "v_smfmac_f32_16x16x64_bf16 v[28:31], v[220:223], v[208:215], v240 abid:1\n"
    "s_add_u32 s63, s63, 1572864\n"
    "v_or_b32_e32 v193, v193, v201\n"
    "v_mfma_f32_16x16x32_bf16 v[116:119], a[204:207], v[184:187], v[116:119]\n"
    "s_waitcnt lgkmcnt(6)\n"
    "ds_read_u16_d16_hi v205, v205\n"
    "ds_write2_b32 v248, v34, v35 offset0:40 offset1:60\n"
    "v_add_u32_e32 v248, 1360, v248\n"
    "v_pk_add_f32 v[224:225], v[224:225], v[24:25]\n"
    "v_pk_add_f32 v[226:227], v[226:227], v[26:27]\n"
    "v_pk_fma_f32 v[228:229], v[24:25], v[24:25], v[228:229]\n"
    "v_pk_fma_f32 v[230:231], v[26:27], v[26:27], v[230:231]\n"
    "v_pk_add_f32 v[224:225], v[224:225], v[28:29]\n"
    "v_pk_add_f32 v[226:227], v[226:227], v[30:31]\n"
    "v_pk_fma_f32 v[228:229], v[28:29], v[28:29], v[228:229]\n"
    "v_pk_fma_f32 v[230:231], v[30:31], v[30:31], v[230:231]\n"
    "v_mfma_f32_16x16x32_bf16 v[104:107], a[192:195], v[188:191], v[104:107]\n"
    "s_waitcnt lgkmcnt(6)\n"
    "ds_read_u16 v198, v198\n"
    "ds_write2_b32 v235, v52, v53 offset0:0 offset1:20\n"
    "v_mfma_f32_16x16x32_bf16 v[128:131], v[236:239], v[184:187], v[128:131]\n"
    "s_waitcnt lgkmcnt(6)\n"
    "v_or_b32_e64 v194, v194, v202\n"
    "v_or_b32_e64 v195, v195, v203\n"
    "v_mfma_f32_16x16x32_bf16 v[132:135], v[164:167], v[184:187], v[132:135]\n"
    "s_add_u32 s52, s52, 1572864\n"
    "s_add_u32 s53, s53, 393216\n"
    "ds_read_u16_d16_hi v206, v206\n"
    "ds_write2_b32 v235, v54, v55 offset0:40 offset1:60\n"
    "v_mfma_f32_16x16x32_bf16 v[108:111], a[196:199], v[188:191], v[108:111]\n"
    "v_add_u32_e32 v235, 1360, v235\n"
    "v_lshlrev_b32_sdwa v184, 1, v168 dst_sel:DWORD dst_unused:UNUSED_PAD src0_sel:DWORD src1_sel:WORD_1\n"
    "v_mfma_f32_16x16x32_bf16 v[120:123], a[200:203], v[188:191], v[120:123]\n"
    "ds_read_u16 v199, v199\n"
    "ds_write2_b32 v248, v36, v37 offset0:0 offset1:20\n"
    "v_mfma_f32_16x16x32_bf16 v[124:127], a[204:207], v[188:191], v[124:127]\n"
    "s_waitcnt lgkmcnt(6)\n"
    "v_or_b32_e32 v197, v197, v205\n"
    "v_lshlrev_b32_sdwa v185, 1, v170 dst_sel:DWORD dst_unused:UNUSED_PAD src0_sel:DWORD src1_sel:WORD_1\n"
    "v_mfma_f32_16x16x32_bf16 v[136:139], v[236:239], v[188:191], v[136:139]\n"
    "ds_read_u16_d16_hi v207, v207\n"
    "ds_write2_b32 v248, v38, v39 offset0:40 offset1:60\n"
    "v_add_u32_e32 v248, 1360, v248\n"
    "v_lshlrev_b32_sdwa v201, 1, v171 dst_sel:DWORD dst_unused:UNUSED_PAD src0_sel:DWORD src1_sel:WORD_1\n"
    "v_lshlrev_b32_sdwa v186, 1, v172 dst_sel:DWORD dst_unused:UNUSED_PAD src0_sel:DWORD src1_sel:WORD_1\n"
    "v_mfma_f32_16x16x32_bf16 v[140:143], v[164:167], v[188:191], v[140:143]\n"
    "ds_write2_b32 v235, v56, v57 offset0:0 offset1:20\n"
    "v_lshlrev_b32_sdwa v202, 1, v173 dst_sel:DWORD dst_unused:UNUSED_PAD src0_sel:DWORD src1_sel:WORD_1\n"
    "v_lshlrev_b32_sdwa v187, 1, v174 dst_sel:DWORD dst_unused:UNUSED_PAD src0_sel:DWORD src1_sel:WORD_1\n"
    "v_mfma_f32_16x16x32_bf16 v[168:171], a[88:91], a[224:227], 0\n"
    "ds_read_u16 v184, v184\n"
    "v_lshlrev_b32_sdwa v203, 1, v175 dst_sel:DWORD dst_unused:UNUSED_PAD src0_sel:DWORD src1_sel:WORD_1\n"
    "v_mfma_f32_16x16x32_bf16 v[168:171], a[136:139], a[228:231], v[168:171]\n"
    "s_waitcnt lgkmcnt(6)\n"
    "v_or_b32_e32 v198, v198, v206\n"
    "v_lshlrev_b32_sdwa v188, 1, v176 dst_sel:DWORD dst_unused:UNUSED_PAD src0_sel:DWORD src1_sel:WORD_1\n"
    "v_mfma_f32_16x16x32_bf16 v[168:171], a[52:55], a[232:235], v[168:171]\n"
    "ds_read_b128 a[252:255], v244 offset:36864\n"
    "ds_write2_b32 v235, v58, v59 offset0:40 offset1:60\n"
    "v_add_u32_e32 v235, 50288, v235\n"
    "v_lshlrev_b32_sdwa v189, 1, v178 dst_sel:DWORD dst_unused:UNUSED_PAD src0_sel:DWORD src1_sel:WORD_1\n"
    "v_lshlrev_b32_sdwa v205, 1, v179 dst_sel:DWORD dst_unused:UNUSED_PAD src0_sel:DWORD src1_sel:WORD_1\n"
    "v_mfma_f32_16x16x32_bf16 v[172:175], a[92:95], a[224:227], 0\n"
    "ds_read_u16_d16_hi v200, v200\n"
    "v_lshlrev_b32_sdwa v190, 1, v180 dst_sel:DWORD dst_unused:UNUSED_PAD src0_sel:DWORD src1_sel:WORD_1\n"
    "v_mfma_f32_16x16x32_bf16 v[172:175], a[140:143], a[228:231], v[172:175]\n"
    "ds_read_u16 v185, v185\n"
    "ds_write2_b32 v248, v40, v41 offset0:0 offset1:20\n"
    "v_lshlrev_b32_sdwa v206, 1, v181 dst_sel:DWORD dst_unused:UNUSED_PAD src0_sel:DWORD src1_sel:WORD_1\n"
    "v_lshlrev_b32_sdwa v191, 1, v182 dst_sel:DWORD dst_unused:UNUSED_PAD src0_sel:DWORD src1_sel:WORD_1\n"
    "v_rcp_f16_e64 v232, v234\n"
    "v_mfma_f32_16x16x32_bf16 v[176:179], a[88:91], a[236:239], 0\n"
    "s_waitcnt lgkmcnt(6)\n"
    "v_or_b32_e32 v199, v199, v207\n"
    "v_lshlrev_b32_sdwa v207, 1, v183 dst_sel:DWORD dst_unused:UNUSED_PAD src0_sel:DWORD src1_sel:WORD_1\n"
    "v_mfma_f32_16x16x32_bf16 v[176:179], a[136:139], a[240:243], v[176:179]\n"
    "ds_read_u16_d16_hi v201, v201\n"
    "v_mfma_f32_16x16x32_bf16 v[176:179], a[52:55], a[244:247], v[176:179]\n"
    "ds_read_u16 v186, v186\n"
    "ds_write2_b32 v248, v42, v43 offset0:40 offset1:60\n"
    "v_add_u32_e32 v248, 1360, v248\n"
    "v_rcp_f16_sdwa v232, v234 dst_sel:WORD_1 dst_unused:UNUSED_PRESERVE src0_sel:WORD_1\n"
    "v_mfma_f32_16x16x32_bf16 v[180:183], a[92:95], a[236:239], 0\n"
    "s_waitcnt lgkmcnt(6)\n"
    "v_fma_mix_f32 v234, v196, v232, 0 op_sel_hi:[1,1,0]\n"
    "v_fma_mix_f32 v232, v196, v232, 0 op_sel:[1,1,0] op_sel_hi:[1,1,0]\n"
    "v_mfma_f32_16x16x32_bf16 v[180:183], a[140:143], a[240:243], v[180:183]\n"
    "s_nop 0\n"
    "ds_read_u16_d16_hi v202, v202\n"
    "v_cvt_pk_bf16_f32 v196, v234, v232\n"
    "v_mfma_f32_16x16x32_bf16 v[180:183], a[252:255], a[244:247], v[180:183]\n"
    "ds_write2_b32 v248, v44, v45 offset0:0 offset1:20\n"
    "v_mfma_f32_16x16x32_bf16 v[172:175], a[252:255], a[232:235], v[172:175]\n"
    "s_waitcnt lgkmcnt(7)\n"
    "v_or_b32_e32 v184, v184, v200\n"
    "v_mfma_f32_16x16x32_bf16 v[96:99], a[208:211], v[192:195], v[96:99]\n"
    "ds_read_b128 v[236:239], v244 offset:20480\n"
    "ds_write2_b32 v248, v46, v47 offset0:40 offset1:60\n"
    "v_mfma_f32_16x16x32_bf16 v[100:103], a[212:215], v[192:195], v[100:103]\n"
    "v_add_u32_e32 v248, 1360, v248\n"
    "v_mfma_f32_16x16x32_bf16 v[112:115], a[216:219], v[192:195], v[112:115]\n"
    "s_waitcnt lgkmcnt(6)\n"
    "ds_read_u16 v187, v187\n"
    "ds_write2_b32 v248, v48, v49 offset0:0 offset1:20\n"
    "v_mfma_f32_16x16x32_bf16 v[116:119], a[220:223], v[192:195], v[116:119]\n"
    "v_or_b32_e32 v185, v185, v201\n"
    "v_lshlrev_b32_sdwa v201, 1, v171 dst_sel:DWORD dst_unused:UNUSED_PAD src0_sel:DWORD src1_sel:WORD_1\n"
    "v_mfma_f32_16x16x32_bf16 v[104:107], a[208:211], v[196:199], v[104:107]\n"
    "ds_read_u16_d16_hi v203, v203\n"
    "ds_write2_b32 v248, v50, v51 offset0:40 offset1:60\n"
    "v_add_u32_e32 v248, 46208, v248\n"
    "v_cvt_pk_bf16_f32 v96, v96, v97\n"
    "v_cvt_pk_bf16_f32 v97, v98, v99\n"
    "v_mfma_f32_32x32x1_2b_f32 v[32:63], v254, v254, 0\n"
    "s_waitcnt lgkmcnt(6)\n"
    "ds_read_b128 v[164:167], v244 offset:22528\n"
    "ds_write2_b32 v248, v64, v65 offset0:0 offset1:20\n"
    "v_or_b32_e32 v186, v186, v202\n"
    "v_lshlrev_b32_sdwa v202, 1, v173 dst_sel:DWORD dst_unused:UNUSED_PAD src0_sel:DWORD src1_sel:WORD_1\n"
    "v_mov_b32_e64 v254, 0\n"
    "v_cvt_pk_bf16_f32 v98, v100, v101\n"
    "v_cvt_pk_bf16_f32 v99, v102, v103\n"
    "ds_write2_b32 v248, v66, v67 offset0:40 offset1:60\n"
    "buffer_store_dwordx4 v[96:99], v245, s[44:47], s57 offen offset:0 sc1\n"
    "v_mfma_f32_16x16x32_bf16 v[108:111], a[212:215], v[196:199], v[108:111]\n"
    "ds_read_u16 v188, v188\n"
    "ds_write2_b32 v235, v84, v85 offset0:0 offset1:20\n"
    "v_add_u32_e32 v248, 1360, v248\n"
    "v_cvt_pk_bf16_f32 v152, v112, v113\n"
    "v_cvt_pk_bf16_f32 v153, v114, v115\n"
    "v_mfma_f32_16x16x32_bf16 v[120:123], a[216:219], v[196:199], v[120:123]\n"
    "ds_read_u16_d16_hi v204, v204\n"
    "ds_write2_b32 v235, v86, v87 offset0:40 offset1:60\n"
    "v_add_u32_e32 v235, 1360, v235\n"
    "v_cvt_pk_bf16_f32 v154, v116, v117\n"
    "v_cvt_pk_bf16_f32 v155, v118, v119\n"
    "v_mfma_f32_16x16x32_bf16 v[124:127], a[220:223], v[196:199], v[124:127]\n"
    "s_waitcnt vmcnt(3) lgkmcnt(5)\n"
    "ds_write2_b32 v248, v68, v69 offset0:0 offset1:20\n"
    "v_or_b32_e32 v187, v187, v203\n"
    "v_lshlrev_b32_sdwa v203, 1, v175 dst_sel:DWORD dst_unused:UNUSED_PAD src0_sel:DWORD src1_sel:WORD_1\n"
    "v_smfmac_f32_32x32x32_bf16 v[128:143], a[248:251], v[144:151], v241\n"
    "ds_read_u16 v189, v189\n"
    "ds_write2_b32 v248, v70, v71 offset0:40 offset1:60\n"
    "v_add_u32_e32 v248, 1360, v248\n"
    "v_cvt_pk_bf16_f32 v144, v104, v105\n"
    "v_cvt_pk_bf16_f32 v145, v106, v107\n"
    "v_cvt_pk_bf16_f32 v146, v108, v109\n"
    "v_cvt_pk_bf16_f32 v147, v110, v111\n"
    "ds_write2_b32 v235, v88, v89 offset0:0 offset1:20\n"
    "v_cvt_pk_bf16_f32 v148, v120, v121\n"
    "v_cvt_pk_bf16_f32 v149, v122, v123\n"
    "v_mfma_f32_16x16x32_bf16 v[128:131], v[236:239], v[192:195], v[128:131]\n"
    "ds_read_u16_d16_hi v205, v205\n"
    "ds_write2_b32 v235, v90, v91 offset0:40 offset1:60\n"
    "v_add_u32_e32 v235, -53008, v235\n"
    "v_cvt_pk_bf16_f32 v150, v124, v125\n"
    "v_cvt_pk_bf16_f32 v151, v126, v127\n"
    "v_mfma_f32_16x16x32_bf16 v[132:135], v[164:167], v[192:195], v[132:135]\n"
    "s_nop 0\n"
    "s_waitcnt lgkmcnt(5)\n"
    "ds_read_u16 v190, v190\n"
    "ds_write2_b32 v248, v72, v73 offset0:0 offset1:20\n"
    "v_cvt_pk_f16_f32 v192, v168, v169\n"
    "v_lshlrev_b32_sdwa v193, 1, v170 dst_sel:DWORD dst_unused:UNUSED_PAD src0_sel:DWORD src1_sel:WORD_1\n"
    "v_lshlrev_b32_sdwa v194, 1, v172 dst_sel:DWORD dst_unused:UNUSED_PAD src0_sel:DWORD src1_sel:WORD_1\n"
    "v_mfma_f32_16x16x32_bf16 v[136:139], v[236:239], v[196:199], v[136:139]\n"
    "ds_write2_b32 v248, v74, v75 offset0:40 offset1:60\n"
    "v_add_u32_e32 v248, 1360, v248\n"
    "v_lshlrev_b32_sdwa v195, 1, v174 dst_sel:DWORD dst_unused:UNUSED_PAD src0_sel:DWORD src1_sel:WORD_1\n"
    "v_mfma_f32_16x16x32_bf16 v[140:143], v[164:167], v[196:199], v[140:143]\n"
    "ds_read_u16_d16_hi v206, v206\n"
    "ds_write2_b32 v248, v76, v77 offset0:0 offset1:20\n"
    "v_cvt_pk_f16_f32 v196, v176, v177\n"
    "v_lshlrev_b32_sdwa v197, 1, v178 dst_sel:DWORD dst_unused:UNUSED_PAD src0_sel:DWORD src1_sel:WORD_1\n"
    "v_lshlrev_b32_sdwa v198, 1, v180 dst_sel:DWORD dst_unused:UNUSED_PAD src0_sel:DWORD src1_sel:WORD_1\n"
    "v_mfma_f32_16x16x32_bf16 v[168:171], a[96:99], a[224:227], 0\n"
    "ds_read_u16 v191, v191\n"
    "v_lshlrev_b32_sdwa v199, 1, v182 dst_sel:DWORD dst_unused:UNUSED_PAD src0_sel:DWORD src1_sel:WORD_1\n"
    "v_mfma_f32_16x16x32_bf16 v[168:171], a[144:147], a[228:231], v[168:171]\n"
    "v_or_b32_e32 v188, v188, v204\n"
    "v_cvt_pk_bf16_f32 v156, v128, v129\n"
    "v_mfma_f32_16x16x32_bf16 v[168:171], a[56:59], a[232:235], v[168:171]\n"
    "s_waitcnt lgkmcnt(6)\n"
    "ds_read_u16_d16_hi v207, v207\n"
    "ds_write2_b32 v248, v78, v79 offset0:40 offset1:60\n"
    "v_add_u32_e32 v248, 1360, v248\n"
    "v_or_b32_e64 v189, v189, v205\n"
    "v_lshlrev_b32_sdwa v205, 1, v179 dst_sel:DWORD dst_unused:UNUSED_PAD src0_sel:DWORD src1_sel:WORD_1\n"
    "v_mfma_f32_16x16x32_bf16 v[172:175], a[100:103], a[224:227], 0\n"
    "v_cvt_pk_bf16_f32 v157, v130, v131\n"
    "v_cvt_pk_bf16_f32 v158, v132, v133\n"
    "v_mfma_f32_16x16x32_bf16 v[172:175], a[148:151], a[228:231], v[172:175]\n"
    "ds_read_u16 v193, v193\n"
    "v_cvt_pk_bf16_f32 v159, v134, v135\n"
    "v_mfma_f32_16x16x32_bf16 v[172:175], a[60:63], a[232:235], v[172:175]\n"
    "ds_read_u16_d16_hi v201, v201\n"
    "ds_write2_b32 v248, v80, v81 offset0:0 offset1:20\n"
    "v_pk_fma_f16 v232, v192, v192, s48\n"
    "v_cvt_pk_bf16_f32 v160, v136, v137\n"
    "v_cvt_pk_bf16_f32 v161, v138, v139\n"
    "v_mfma_f32_16x16x32_bf16 v[176:179], a[96:99], a[236:239], 0\n"
    "s_waitcnt lgkmcnt(6)\n"
    "v_or_b32_e32 v190, v190, v206\n"
    "v_lshlrev_b32_sdwa v206, 1, v181 dst_sel:DWORD dst_unused:UNUSED_PAD src0_sel:DWORD src1_sel:WORD_1\n"
    "v_mfma_f32_16x16x32_bf16 v[176:179], a[144:147], a[240:243], v[176:179]\n"
    "ds_read_u16 v194, v194\n"
    "v_cvt_pk_bf16_f32 v162, v140, v141\n"
    "v_mfma_f32_16x16x32_bf16 v[176:179], a[56:59], a[244:247], v[176:179]\n"
    "ds_write2_b32 v248, v82, v83 offset0:40 offset1:60\n"
    "v_add_u32_e32 v248, -57088, v248\n"
    "v_cvt_pk_bf16_f32 v163, v142, v143\n"
    "v_mfma_f32_32x32x1_2b_f32 v[64:95], v254, v254, 0\n"
    "ds_read_u16_d16_hi v202, v202\n"
    "ds_read_u16 v195, v195\n"
    "v_pk_mul_f16 v232, v192, v232\n"
    "v_lshlrev_b32_sdwa v200, 1, v169 dst_sel:DWORD dst_unused:UNUSED_PAD src0_sel:DWORD src1_sel:WORD_1\n"
    "v_exp_f16_e64 v234, v232\n"
    "v_lshlrev_b32_sdwa v204, 1, v177 dst_sel:DWORD dst_unused:UNUSED_PAD src0_sel:DWORD src1_sel:WORD_1\n"
    "v_exp_f16_sdwa v234, v232 dst_sel:WORD_1 dst_unused:UNUSED_PRESERVE src0_sel:WORD_1\n"
    "ds_read_b128 v[236:239], v244 offset:0\n"
    "ds_read_u16_d16_hi v203, v203\n"
    "v_pk_fma_f16 v234, v234, s49, s49\n"
    "v_mfma_f32_16x16x32_bf16 v[96:99], a[0:3], v[184:187], 0\n"
    "s_waitcnt lgkmcnt(8)\n"
    "ds_read_b128 v[216:219], v251 offset:10752\n"
    "ds_read_b128 v[220:223], v251 offset:11088\n"
    "v_mfma_f32_16x16x32_bf16 v[100:103], a[4:7], v[184:187], 0\n"
    "s_waitcnt lgkmcnt(8)\n"
    "ds_read_b128 v[164:167], v244 offset:2048\n"
    "v_or_b32_e64 v193, v193, v201\n"
    "v_mfma_f32_16x16x32_bf16 v[112:115], a[8:11], v[184:187], 0\n"
    "ds_read_u16 v197, v197\n"
    "v_or_b32_e32 v191, v191, v207\n"
    "v_lshlrev_b32_sdwa v207, 1, v183 dst_sel:DWORD dst_unused:UNUSED_PAD src0_sel:DWORD src1_sel:WORD_1\n"
    "v_mfma_f32_16x16x32_bf16 v[180:183], a[100:103], a[236:239], 0\n"
    "s_waitcnt lgkmcnt(8)\n"
    "ds_read_u16_d16_hi v205, v205\n"
    "v_lshlrev_b32_sdwa v201, 1, v171 dst_sel:DWORD dst_unused:UNUSED_PAD src0_sel:DWORD src1_sel:WORD_1\n"
    "v_mfma_f32_16x16x32_bf16 v[180:183], a[148:151], a[240:243], v[180:183]\n"
    "v_rcp_f16_e64 v232, v234\n"
    "v_mfma_f32_16x16x32_bf16 v[180:183], a[60:63], a[244:247], v[180:183]\n"
    "s_barrier\n"
    "v_mfma_f32_16x16x32_bf16 v[116:119], a[12:15], v[184:187], 0\n"
    "s_waitcnt lgkmcnt(6)\n"
    "ds_read_b128 v[208:211], v250 offset:0\n"
    "ds_read_b128 v[212:215], v250 offset:320\n"
    "v_mfma_f32_16x16x32_bf16 v[128:131], v[236:239], v[184:187], 0\n"
    "ds_read_u16 v198, v198\n"
    "ds_read_u16_d16_hi v206, v206\n"
    "v_mfma_f32_16x16x32_bf16 v[104:107], a[0:3], v[188:191], 0\n"
    "s_waitcnt lgkmcnt(8)\n"
    "ds_read_u16 v199, v199\n"
    "v_or_b32_e32 v194, v194, v202\n"
    "v_or_b32_e64 v195, v195, v203\n"
    "v_lshlrev_b32_sdwa v202, 1, v173 dst_sel:DWORD dst_unused:UNUSED_PAD src0_sel:DWORD src1_sel:WORD_1\n"
    "v_mfma_f32_16x16x32_bf16 v[108:111], a[4:7], v[188:191], 0\n"
    "ds_read_u16_d16_hi v207, v207\n"
    "v_lshlrev_b32_sdwa v203, 1, v175 dst_sel:DWORD dst_unused:UNUSED_PAD src0_sel:DWORD src1_sel:WORD_1\n"
    "v_mfma_f32_16x16x32_bf16 v[120:123], a[8:11], v[188:191], 0\n"
    "s_waitcnt lgkmcnt(8)\n"
    "v_rcp_f16_sdwa v232, v234 dst_sel:WORD_1 dst_unused:UNUSED_PRESERVE src0_sel:WORD_1\n"
    "v_mfma_f32_16x16x32_bf16 v[132:135], v[164:167], v[184:187], 0\n"
    "s_nop 0\n"
    "ds_read_u16_d16_hi v200, v200\n"
    "v_lshlrev_b32_sdwa v184, 1, v168 dst_sel:DWORD dst_unused:UNUSED_PAD src0_sel:DWORD src1_sel:WORD_1\n"
    "v_lshlrev_b32_sdwa v185, 1, v170 dst_sel:DWORD dst_unused:UNUSED_PAD src0_sel:DWORD src1_sel:WORD_1\n"
    "v_lshlrev_b32_sdwa v186, 1, v172 dst_sel:DWORD dst_unused:UNUSED_PAD src0_sel:DWORD src1_sel:WORD_1\n"
    "v_mfma_f32_16x16x32_bf16 v[124:127], a[12:15], v[188:191], 0\n"
    "ds_read_u16 v184, v184\n"
    "v_lshlrev_b32_sdwa v187, 1, v174 dst_sel:DWORD dst_unused:UNUSED_PAD src0_sel:DWORD src1_sel:WORD_1\n"
    "v_mfma_f32_16x16x32_bf16 v[136:139], v[236:239], v[188:191], 0\n"
    "v_fma_mix_f32 v234, v192, v232, 0 op_sel_hi:[1,1,0]\n"
    "v_fma_mix_f32 v232, v192, v232, 0 op_sel:[1,1,0] op_sel_hi:[1,1,0]\n"
    "v_mfma_f32_16x16x32_bf16 v[140:143], v[164:167], v[188:191], 0\n"
    "s_waitcnt lgkmcnt(7)\n"
    "ds_read_u16 v185, v185\n"
    "v_or_b32_e64 v197, v197, v205\n"
    "v_mfma_f32_16x16x32_bf16 v[168:171], a[104:107], a[224:227], 0\n"
    "s_waitcnt lgkmcnt(7)\n"
    "v_lshlrev_b32_sdwa v188, 1, v176 dst_sel:DWORD dst_unused:UNUSED_PAD src0_sel:DWORD src1_sel:WORD_1\n"
    "v_lshlrev_b32_sdwa v189, 1, v178 dst_sel:DWORD dst_unused:UNUSED_PAD src0_sel:DWORD src1_sel:WORD_1\n"
    "v_mfma_f32_16x16x32_bf16 v[168:171], a[152:155], a[228:231], v[168:171]\n"
    "ds_read_u16_d16_hi v201, v201\n"
    "v_lshlrev_b32_sdwa v205, 1, v179 dst_sel:DWORD dst_unused:UNUSED_PAD src0_sel:DWORD src1_sel:WORD_1\n"
    "v_mfma_f32_16x16x32_bf16 v[168:171], a[64:67], a[232:235], v[168:171]\n"
    "v_lshlrev_b32_sdwa v190, 1, v180 dst_sel:DWORD dst_unused:UNUSED_PAD src0_sel:DWORD src1_sel:WORD_1\n"
    "v_lshlrev_b32_sdwa v191, 1, v182 dst_sel:DWORD dst_unused:UNUSED_PAD src0_sel:DWORD src1_sel:WORD_1\n"
    "v_smfmac_f32_16x16x64_bf16 v[32:35], v[216:219], v[208:215], v240\n"
    "ds_read_b128 v[216:219], v252 offset:10752\n"
    "ds_read_u16 v186, v186\n"
    "v_smfmac_f32_16x16x64_bf16 v[36:39], v[220:223], v[208:215], v240 abid:1\n"
    "s_waitcnt lgkmcnt(8)\n"
    "v_or_b32_e32 v198, v198, v206\n"
    "v_mfma_f32_16x16x32_bf16 v[172:175], a[108:111], a[224:227], 0\n"
    "ds_read_b128 v[208:211], v250 offset:640\n"
    "v_lshlrev_b32_sdwa v206, 1, v181 dst_sel:DWORD dst_unused:UNUSED_PAD src0_sel:DWORD src1_sel:WORD_1\n"
    "v_mfma_f32_16x16x32_bf16 v[172:175], a[156:159], a[228:231], v[172:175]\n"
    "ds_read_b128 v[212:215], v250 offset:960\n"
    "v_cvt_pk_bf16_f32 v192, v234, v232\n"
    "v_mfma_f32_16x16x32_bf16 v[172:175], a[68:71], a[232:235], v[172:175]\n"
    "ds_read_b128 v[220:223], v252 offset:11088\n"
    "v_pk_fma_f16 v232, v196, v196, s48\n"
    "v_mfma_f32_16x16x32_bf16 v[176:179], a[104:107], a[236:239], 0\n"
    "s_waitcnt lgkmcnt(9)\n"
    "ds_read_u16_d16_hi v202, v202\n"
    "v_or_b32_e32 v199, v199, v207\n"
    "v_mfma_f32_16x16x32_bf16 v[176:179], a[152:155], a[240:243], v[176:179]\n"
    "v_lshlrev_b32_sdwa v207, 1, v183 dst_sel:DWORD dst_unused:UNUSED_PAD src0_sel:DWORD src1_sel:WORD_1\n"
    "v_pk_mul_f16 v232, v196, v232\n"
    "v_mfma_f32_16x16x32_bf16 v[176:179], a[64:67], a[244:247], v[176:179]\n"
    "ds_read_b128 v[236:239], v244 offset:4096\n"
    "v_exp_f16_e32 v234, v232\n"
    "v_mfma_f32_16x16x32_bf16 v[180:183], a[108:111], a[236:239], 0\n"
    "s_waitcnt lgkmcnt(9)\n"
    "ds_read_u16 v187, v187\n"
    "v_or_b32_e64 v184, v184, v200\n"
    "v_mfma_f32_16x16x32_bf16 v[180:183], a[156:159], a[240:243], v[180:183]\n"
    "ds_read_u16_d16_hi v203, v203\n"
    "v_mfma_f32_16x16x32_bf16 v[180:183], a[68:71], a[244:247], v[180:183]\n"
    "v_exp_f16_sdwa v234, v232 dst_sel:WORD_1 dst_unused:UNUSED_PRESERVE src0_sel:WORD_1\n"
    "ds_read_b128 v[164:167], v244 offset:6144\n"
    "v_pk_fma_f16 v234, v234, s49, s49\n"
    "v_mfma_f32_16x16x32_bf16 v[96:99], a[16:19], v[192:195], v[96:99]\n"
    "s_waitcnt lgkmcnt(9)\n"
    "ds_read_u16 v188, v188\n"
    "v_rcp_f16_e32 v232, v234\n"
    "v_or_b32_e32 v185, v185, v201\n"
    "v_mfma_f32_16x16x32_bf16 v[100:103], a[20:23], v[192:195], v[100:103]\n"
    "ds_read_u16_d16_hi v204, v204\n"
    "v_rcp_f16_sdwa v232, v234 dst_sel:WORD_1 dst_unused:UNUSED_PRESERVE src0_sel:WORD_1\n"
    "v_lshlrev_b32_sdwa v201, 1, v171 dst_sel:DWORD dst_unused:UNUSED_PAD src0_sel:DWORD src1_sel:WORD_1\n"
    "v_fma_mix_f32 v234, v196, v232, 0 op_sel_hi:[1,1,0]\n"
    "v_mfma_f32_16x16x32_bf16 v[112:115], a[24:27], v[192:195], v[112:115]\n"
    "s_waitcnt lgkmcnt(8)\n"
    "ds_read_u16 v189, v189\n"
    "v_fma_mix_f32 v232, v196, v232, 0 op_sel:[1,1,0] op_sel_hi:[1,1,0]\n"
    "v_cvt_pk_bf16_f32 v196, v234, v232\n"
    "v_smfmac_f32_16x16x64_bf16 v[32:35], v[216:219], v[208:215], v240\n"
    "s_waitcnt lgkmcnt(8)\n"
    "v_smfmac_f32_16x16x64_bf16 v[36:39], v[220:223], v[208:215], v240 abid:1\n"
    "ds_read_b128 v[208:211], v250 offset:80\n"
    "ds_read_b128 v[212:215], v250 offset:400\n"
    "v_mfma_f32_16x16x32_bf16 v[116:119], a[28:31], v[192:195], v[116:119]\n"
    "s_waitcnt lgkmcnt(8)\n"
    "buffer_store_dwordx4 v[152:155], v245, s[44:47], s57 offen offset:64 sc1\n"
    "ds_read_b128 v[216:219], v251 offset:11424\n"
    "v_pk_add_f32 v[224:225], v[224:225], v[32:33]\n"
    "v_pk_add_f32 v[226:227], v[226:227], v[34:35]\n"
    "v_pk_fma_f32 v[228:229], v[32:33], v[32:33], v[228:229]\n"
    "v_pk_fma_f32 v[230:231], v[34:35], v[34:35], v[230:231]\n"
    "v_pk_add_f32 v[224:225], v[224:225], v[36:37]\n"
    "v_pk_add_f32 v[226:227], v[226:227], v[38:39]\n"
    "v_pk_fma_f32 v[228:229], v[36:37], v[36:37], v[228:229]\n"
    "v_pk_fma_f32 v[230:231], v[38:39], v[38:39], v[230:231]\n"
    "v_mfma_f32_16x16x32_bf16 v[128:131], v[236:239], v[192:195], v[128:131]\n"
    "s_waitcnt lgkmcnt(6)\n"
    "ds_read_b128 v[220:223], v251 offset:11760\n"
    "ds_read_u16_d16_hi v205, v205\n"
    "v_mfma_f32_16x16x32_bf16 v[132:135], v[164:167], v[192:195], v[132:135]\n"
    "s_nop 0\n"
    "ds_read_u16 v190, v190\n"
    "v_cvt_pk_f16_f32 v192, v168, v169\n"
    "v_mfma_f32_16x16x32_bf16 v[104:107], a[16:19], v[196:199], v[104:107]\n"
    "ds_read_u16_d16_hi v206, v206\n"
    "v_lshlrev_b32_sdwa v193, 1, v170 dst_sel:DWORD dst_unused:UNUSED_PAD src0_sel:DWORD src1_sel:WORD_1\n"
    "v_mfma_f32_16x16x32_bf16 v[108:111], a[20:23], v[196:199], v[108:111]\n"
    "v_lshlrev_b32_sdwa v194, 1, v172 dst_sel:DWORD dst_unused:UNUSED_PAD src0_sel:DWORD src1_sel:WORD_1\n"
    "v_or_b32_e64 v186, v186, v202\n"
    "v_mfma_f32_16x16x32_bf16 v[120:123], a[24:27], v[196:199], v[120:123]\n"
    "ds_read_u16 v191, v191\n"
    "v_lshlrev_b32_sdwa v202, 1, v173 dst_sel:DWORD dst_unused:UNUSED_PAD src0_sel:DWORD src1_sel:WORD_1\n"
    "v_lshlrev_b32_sdwa v195, 1, v174 dst_sel:DWORD dst_unused:UNUSED_PAD src0_sel:DWORD src1_sel:WORD_1\n"
    "v_mfma_f32_16x16x32_bf16 v[124:127], a[28:31], v[196:199], v[124:127]\n"
    "s_waitcnt lgkmcnt(9)\n"
    "ds_read_u16_d16_hi v207, v207\n"
    "v_or_b32_e32 v187, v187, v203\n"
    "v_lshlrev_b32_sdwa v203, 1, v175 dst_sel:DWORD dst_unused:UNUSED_PAD src0_sel:DWORD src1_sel:WORD_1\n"
    "v_or_b32_e64 v188, v188, v204\n"
    "v_pk_fma_f16 v232, v192, v192, s48\n"
    "v_mfma_f32_16x16x32_bf16 v[136:139], v[236:239], v[196:199], v[136:139]\n"
    "ds_read_u16 v193, v193\n"
    "v_pk_mul_f16 v232, v192, v232\n"
    "v_exp_f16_e64 v234, v232\n"
    "v_mfma_f32_16x16x32_bf16 v[140:143], v[164:167], v[196:199], v[140:143]\n"
    "s_waitcnt lgkmcnt(7)\n"
    "v_cvt_pk_f16_f32 v196, v176, v177\n"
    "v_lshlrev_b32_sdwa v197, 1, v178 dst_sel:DWORD dst_unused:UNUSED_PAD src0_sel:DWORD src1_sel:WORD_1\n"
    "v_smfmac_f32_16x16x64_bf16 v[40:43], v[216:219], v[208:215], v240\n"
    "s_waitcnt lgkmcnt(6)\n"
    "ds_read_b128 v[216:219], v252 offset:11424\n"
    "ds_read_b128 a[252:255], v244 offset:34816\n"
    "v_smfmac_f32_16x16x64_bf16 v[44:47], v[220:223], v[208:215], v240 abid:1\n"
    "ds_read_b128 v[208:211], v250 offset:720\n"
    "v_mfma_f32_16x16x32_bf16 v[168:171], a[112:115], a[224:227], 0\n"
    "s_nop 0\n"
    "ds_read_b128 v[212:215], v250 offset:1040\n"
    "v_lshlrev_b32_sdwa v198, 1, v180 dst_sel:DWORD dst_unused:UNUSED_PAD src0_sel:DWORD src1_sel:WORD_1\n"
    "v_mfma_f32_16x16x32_bf16 v[168:171], a[160:163], a[228:231], v[168:171]\n"
    "s_waitcnt lgkmcnt(9)\n"
    "ds_read_b128 v[220:223], v252 offset:11760\n"
    "v_lshlrev_b32_sdwa v199, 1, v182 dst_sel:DWORD dst_unused:UNUSED_PAD src0_sel:DWORD src1_sel:WORD_1\n"
    "v_mfma_f32_16x16x32_bf16 v[168:171], a[72:75], a[232:235], v[168:171]\n"
    "ds_read_u16_d16_hi v201, v201\n"
    "v_or_b32_e64 v189, v189, v205\n"
    "v_lshlrev_b32_sdwa v205, 1, v179 dst_sel:DWORD dst_unused:UNUSED_PAD src0_sel:DWORD src1_sel:WORD_1\n"
    "v_exp_f16_sdwa v234, v232 dst_sel:WORD_1 dst_unused:UNUSED_PRESERVE src0_sel:WORD_1\n"
    "v_mfma_f32_16x16x32_bf16 v[172:175], a[116:119], a[224:227], 0\n"
    "ds_read_u16 v194, v194\n"
    "v_pk_fma_f16 v234, v234, s49, s49\n"
    "v_mfma_f32_16x16x32_bf16 v[172:175], a[164:167], a[228:231], v[172:175]\n"
    "s_waitcnt lgkmcnt(10)\n"
    "v_or_b32_e32 v190, v190, v206\n"
    "v_lshlrev_b32_sdwa v206, 1, v181 dst_sel:DWORD dst_unused:UNUSED_PAD src0_sel:DWORD src1_sel:WORD_1\n"
    "v_mfma_f32_16x16x32_bf16 v[176:179], a[112:115], a[236:239], 0\n"
    "ds_read_u16_d16_hi v202, v202\n"
    "v_lshlrev_b32_sdwa v200, 1, v169 dst_sel:DWORD dst_unused:UNUSED_PAD src0_sel:DWORD src1_sel:WORD_1\n"
    "v_mfma_f32_16x16x32_bf16 v[176:179], a[160:163], a[240:243], v[176:179]\n"
    "v_rcp_f16_e32 v232, v234\n"
    "v_mfma_f32_16x16x32_bf16 v[176:179], a[72:75], a[244:247], v[176:179]\n"
    "s_waitcnt lgkmcnt(9)\n"
    "ds_read_u16 v195, v195\n"
    "buffer_store_dwordx4 v[156:159], v245, s[44:47], s57 offen offset:128 sc1\n"
    "v_mfma_f32_16x16x32_bf16 v[96:99], a[32:35], v[184:187], v[96:99]\n"
    "ds_read_b128 v[236:239], v244 offset:8192\n"
    "ds_read_u16_d16_hi v203, v203\n"
    "v_or_b32_e32 v191, v191, v207\n"
    "v_lshlrev_b32_sdwa v207, 1, v183 dst_sel:DWORD dst_unused:UNUSED_PAD src0_sel:DWORD src1_sel:WORD_1\n"
    "v_rcp_f16_sdwa v232, v234 dst_sel:WORD_1 dst_unused:UNUSED_PRESERVE src0_sel:WORD_1\n"
    "v_mfma_f32_16x16x32_bf16 v[180:183], a[116:119], a[236:239], 0\n"
    "s_waitcnt lgkmcnt(8)\n"
    "ds_read_b128 v[164:167], v244 offset:10240\n"
    "v_lshlrev_b32_sdwa v204, 1, v177 dst_sel:DWORD dst_unused:UNUSED_PAD src0_sel:DWORD src1_sel:WORD_1\n"
    "v_mfma_f32_16x16x32_bf16 v[180:183], a[164:167], a[240:243], v[180:183]\n"
    "s_waitcnt lgkmcnt(8)\n"
    "ds_read_u16 v197, v197\n"
    "v_fma_mix_f32 v234, v192, v232, 0 op_sel_hi:[1,1,0]\n"
    "v_mfma_f32_16x16x32_bf16 v[180:183], a[252:255], a[244:247], v[180:183]\n"
    "s_waitcnt lgkmcnt(8)\n"
    "v_fma_mix_f32 v232, v192, v232, 0 op_sel:[1,1,0] op_sel_hi:[1,1,0]\n"
    "v_cvt_pk_bf16_f32 v192, v234, v232\n"
    "v_smfmac_f32_16x16x64_bf16 v[40:43], v[216:219], v[208:215], v240\n"
    "s_nop 0\n"
    "ds_read_b128 v[216:219], v251 offset:12096\n"
    "v_smfmac_f32_16x16x64_bf16 v[44:47], v[220:223], v[208:215], v240 abid:1\n"
    "s_waitcnt lgkmcnt(8)\n"
    "ds_read_b128 v[208:211], v250 offset:160\n"
    "v_mfma_f32_16x16x32_bf16 v[172:175], a[252:255], a[232:235], v[172:175]\n"
    "ds_read_b128 v[212:215], v250 offset:480\n"
    "v_or_b32_e64 v193, v193, v201\n"
    "v_mfma_f32_16x16x32_bf16 v[100:103], a[36:39], v[184:187], v[100:103]\n"
    "ds_read_b128 v[220:223], v251 offset:12432\n"
    "v_lshlrev_b32_sdwa v201, 1, v171 dst_sel:DWORD dst_unused:UNUSED_PAD src0_sel:DWORD src1_sel:WORD_1\n"
    "v_pk_fma_f16 v232, v196, v196, s48\n"
    "ds_read_u16_d16_hi v205, v205\n"
    "v_pk_add_f32 v[224:225], v[224:225], v[40:41]\n"
    "v_pk_add_f32 v[226:227], v[226:227], v[42:43]\n"
    "v_pk_fma_f32 v[228:229], v[40:41], v[40:41], v[228:229]\n"
    "v_pk_fma_f32 v[230:231], v[42:43], v[42:43], v[230:231]\n"
    "v_pk_add_f32 v[224:225], v[224:225], v[44:45]\n"
    "v_pk_add_f32 v[226:227], v[226:227], v[46:47]\n"
    "v_pk_fma_f32 v[228:229], v[44:45], v[44:45], v[228:229]\n"
    "v_pk_fma_f32 v[230:231], v[46:47], v[46:47], v[230:231]\n"
    "v_mfma_f32_16x16x32_bf16 v[112:115], a[40:43], v[184:187], v[112:115]\n"
    "s_waitcnt lgkmcnt(8)\n"
    "ds_read_u16 v198, v198\n"
    "ds_read_u16_d16_hi v206, v206\n"
    "v_mfma_f32_16x16x32_bf16 v[116:119], a[44:47], v[184:187], v[116:119]\n"
    "v_or_b32_e32 v194, v194, v202\n"
    "v_lshlrev_b32_sdwa v202, 1, v173 dst_sel:DWORD dst_unused:UNUSED_PAD src0_sel:DWORD src1_sel:WORD_1\n"
    "v_mfma_f32_16x16x32_bf16 v[128:131], v[236:239], v[184:187], v[128:131]\n"
    "s_waitcnt lgkmcnt(8)\n"
    "ds_read_u16 v199, v199\n"
    "v_or_b32_e32 v195, v195, v203\n"
    "v_mfma_f32_16x16x32_bf16 v[132:135], v[164:167], v[184:187], v[132:135]\n"
    "v_lshlrev_b32_sdwa v184, 1, v168 dst_sel:DWORD dst_unused:UNUSED_PAD src0_sel:DWORD src1_sel:WORD_1\n"
    "v_lshlrev_b32_sdwa v185, 1, v170 dst_sel:DWORD dst_unused:UNUSED_PAD src0_sel:DWORD src1_sel:WORD_1\n"
    "v_mfma_f32_16x16x32_bf16 v[104:107], a[32:35], v[188:191], v[104:107]\n"
    "ds_read_u16_d16_hi v207, v207\n"
    "v_lshlrev_b32_sdwa v186, 1, v172 dst_sel:DWORD dst_unused:UNUSED_PAD src0_sel:DWORD src1_sel:WORD_1\n"
    "v_mfma_f32_16x16x32_bf16 v[108:111], a[36:39], v[188:191], v[108:111]\n"
    "v_lshlrev_b32_sdwa v187, 1, v174 dst_sel:DWORD dst_unused:UNUSED_PAD src0_sel:DWORD src1_sel:WORD_1\n"
    "v_lshlrev_b32_sdwa v203, 1, v175 dst_sel:DWORD dst_unused:UNUSED_PAD src0_sel:DWORD src1_sel:WORD_1\n"
    "v_mfma_f32_16x16x32_bf16 v[120:123], a[40:43], v[188:191], v[120:123]\n"
    "ds_read_u16 v184, v184\n"
    "v_pk_mul_f16 v232, v196, v232\n"
    "v_mfma_f32_16x16x32_bf16 v[124:127], a[44:47], v[188:191], v[124:127]\n"
    "s_waitcnt lgkmcnt(7)\n"
    "v_exp_f16_e64 v234, v232\n"
    "v_smfmac_f32_16x16x64_bf16 v[48:51], v[216:219], v[208:215], v240\n"
    "s_waitcnt lgkmcnt(6)\n"
    "ds_read_b128 v[216:219], v252 offset:12096\n"
    "ds_read_b128 a[252:255], v244 offset:38912\n"
    "v_smfmac_f32_16x16x64_bf16 v[52:55], v[220:223], v[208:215], v240 abid:1\n"
    "s_nop 0\n"
    "ds_read_b128 v[208:211], v250 offset:800\n"
    "buffer_store_dwordx4 v[144:147], v245, s[44:47], s88 offen offset:0 sc1\n"
    "v_mfma_f32_16x16x32_bf16 v[136:139], v[236:239], v[188:191], v[136:139]\n"
    "s_waitcnt lgkmcnt(8)\n"
    "ds_read_b128 v[212:215], v250 offset:1120\n"
    "ds_read_b128 v[220:223], v252 offset:12432\n"
    "v_mfma_f32_16x16x32_bf16 v[140:143], v[164:167], v[188:191], v[140:143]\n"
    "ds_read_u16_d16_hi v200, v200\n"
    "v_or_b32_e64 v197, v197, v205\n"
    "v_lshlrev_b32_sdwa v188, 1, v176 dst_sel:DWORD dst_unused:UNUSED_PAD src0_sel:DWORD src1_sel:WORD_1\n"
    "v_lshlrev_b32_sdwa v189, 1, v178 dst_sel:DWORD dst_unused:UNUSED_PAD src0_sel:DWORD src1_sel:WORD_1\n"
    "v_mfma_f32_16x16x32_bf16 v[168:171], a[120:123], a[224:227], 0\n"
    "ds_read_u16 v185, v185\n"
    "v_lshlrev_b32_sdwa v205, 1, v179 dst_sel:DWORD dst_unused:UNUSED_PAD src0_sel:DWORD src1_sel:WORD_1\n"
    "v_mfma_f32_16x16x32_bf16 v[168:171], a[168:171], a[228:231], v[168:171]\n"
    "s_waitcnt lgkmcnt(10)\n"
    "v_or_b32_e32 v198, v198, v206\n"
    "v_lshlrev_b32_sdwa v190, 1, v180 dst_sel:DWORD dst_unused:UNUSED_PAD src0_sel:DWORD src1_sel:WORD_1\n"
    "v_mfma_f32_16x16x32_bf16 v[168:171], a[76:79], a[232:235], v[168:171]\n"
    "ds_read_u16_d16_hi v201, v201\n"
    "v_lshlrev_b32_sdwa v206, 1, v181 dst_sel:DWORD dst_unused:UNUSED_PAD src0_sel:DWORD src1_sel:WORD_1\n"
    "v_lshlrev_b32_sdwa v191, 1, v182 dst_sel:DWORD dst_unused:UNUSED_PAD src0_sel:DWORD src1_sel:WORD_1\n"
    "v_exp_f16_sdwa v234, v232 dst_sel:WORD_1 dst_unused:UNUSED_PRESERVE src0_sel:WORD_1\n"
    "v_mfma_f32_16x16x32_bf16 v[172:175], a[124:127], a[224:227], 0\n"
    "s_waitcnt lgkmcnt(9)\n"
    "ds_read_u16 v186, v186\n"
    "v_or_b32_e32 v199, v199, v207\n"
    "v_mfma_f32_16x16x32_bf16 v[172:175], a[172:175], a[228:231], v[172:175]\n"
    "v_lshlrev_b32_sdwa v207, 1, v183 dst_sel:DWORD dst_unused:UNUSED_PAD src0_sel:DWORD src1_sel:WORD_1\n"
    "v_pk_fma_f16 v234, v234, s49, s49\n"
    "v_mfma_f32_16x16x32_bf16 v[176:179], a[120:123], a[236:239], 0\n"
    "ds_read_u16_d16_hi v202, v202\n"
    "v_mfma_f32_16x16x32_bf16 v[176:179], a[168:171], a[240:243], v[176:179]\n"
    "s_waitcnt lgkmcnt(8)\n"
    "v_rcp_f16_e32 v232, v234\n"
    "v_mfma_f32_16x16x32_bf16 v[176:179], a[76:79], a[244:247], v[176:179]\n"
    "ds_read_b128 v[236:239], v244 offset:12288\n"
    "ds_read_u16 v187, v187\n"
    "v_mfma_f32_16x16x32_bf16 v[172:175], a[252:255], a[232:235], v[172:175]\n"
    "s_waitcnt lgkmcnt(8)\n"
    "v_rcp_f16_sdwa v232, v234 dst_sel:WORD_1 dst_unused:UNUSED_PRESERVE src0_sel:WORD_1\n"
    "v_smfmac_f32_16x16x64_bf16 v[48:51], v[216:219], v[208:215], v240\n"
    "s_waitcnt lgkmcnt(7)\n"
    "ds_read_b128 v[216:219], v251 offset:12768\n"
    "v_smfmac_f32_16x16x64_bf16 v[52:55], v[220:223], v[208:215], v240 abid:1\n"
    "ds_read_b128 v[208:211], v250 offset:240\n"
    "v_mfma_f32_16x16x32_bf16 v[180:183], a[124:127], a[236:239], 0\n"
    "ds_read_b128 v[212:215], v250 offset:560\n"
    "v_fma_mix_f32 v234, v196, v232, 0 op_sel_hi:[1,1,0]\n"
    "v_mfma_f32_16x16x32_bf16 v[180:183], a[172:175], a[240:243], v[180:183]\n"
    "s_waitcnt lgkmcnt(9)\n"
    "ds_read_b128 v[220:223], v251 offset:13104\n"
    "v_fma_mix_f32 v232, v196, v232, 0 op_sel:[1,1,0] op_sel_hi:[1,1,0]\n"
    "v_or_b32_e32 v184, v184, v200\n"
    "v_cvt_pk_bf16_f32 v196, v234, v232\n"
    "v_mfma_f32_16x16x32_bf16 v[180:183], a[252:255], a[244:247], v[180:183]\n"
    "ds_read_u16_d16_hi v203, v203\n"
    "v_pk_add_f32 v[224:225], v[224:225], v[48:49]\n"
    "v_pk_add_f32 v[226:227], v[226:227], v[50:51]\n"
    "v_pk_fma_f32 v[228:229], v[48:49], v[48:49], v[228:229]\n"
    "v_pk_fma_f32 v[230:231], v[50:51], v[50:51], v[230:231]\n"
    "v_pk_add_f32 v[224:225], v[224:225], v[52:53]\n"
    "v_pk_add_f32 v[226:227], v[226:227], v[54:55]\n"
    "v_pk_fma_f32 v[228:229], v[52:53], v[52:53], v[228:229]\n"
    "v_pk_fma_f32 v[230:231], v[54:55], v[54:55], v[230:231]\n"
    "v_mfma_f32_16x16x32_bf16 v[96:99], a[176:179], v[192:195], v[96:99]\n"
    "s_waitcnt lgkmcnt(8)\n"
    "ds_read_b128 v[164:167], v244 offset:14336\n"
    "ds_read_u16 v188, v188\n"
    "v_mfma_f32_16x16x32_bf16 v[100:103], a[180:183], v[192:195], v[100:103]\n"
    "s_waitcnt lgkmcnt(8)\n"
    "ds_read_u16_d16_hi v204, v204\n"
    "v_or_b32_e64 v185, v185, v201\n"
    "v_mfma_f32_16x16x32_bf16 v[112:115], a[184:187], v[192:195], v[112:115]\n"
    "ds_read_u16 v189, v189\n"
    "buffer_store_dwordx4 v[148:151], v245, s[44:47], s88 offen offset:64 sc1\n"
    "v_mfma_f32_16x16x32_bf16 v[116:119], a[188:191], v[192:195], v[116:119]\n"
    "ds_read_u16_d16_hi v205, v205\n"
    "v_lshlrev_b32_sdwa v201, 1, v171 dst_sel:DWORD dst_unused:UNUSED_PAD src0_sel:DWORD src1_sel:WORD_1\n"
    "v_mfma_f32_16x16x32_bf16 v[128:131], v[236:239], v[192:195], v[128:131]\n"
    "v_or_b32_e64 v186, v186, v202\n"
    "v_lshlrev_b32_sdwa v202, 1, v173 dst_sel:DWORD dst_unused:UNUSED_PAD src0_sel:DWORD src1_sel:WORD_1\n"
    "v_mfma_f32_16x16x32_bf16 v[104:107], a[176:179], v[196:199], v[104:107]\n"
    "s_waitcnt lgkmcnt(7)\n"
    "ds_read_u16 v190, v190\n"
    "v_smfmac_f32_16x16x64_bf16 v[56:59], v[216:219], v[208:215], v240\n"
    "s_waitcnt lgkmcnt(7)\n"
    "ds_read_b128 v[216:219], v252 offset:12768\n"
    "v_smfmac_f32_16x16x64_bf16 v[60:63], v[220:223], v[208:215], v240 abid:1\n"
    "ds_read_b128 v[208:211], v250 offset:880\n"
    "v_mfma_f32_16x16x32_bf16 v[108:111], a[180:183], v[196:199], v[108:111]\n"
    "s_nop 0\n"
    "v_mfma_f32_16x16x32_bf16 v[120:123], a[184:187], v[196:199], v[120:123]\n"
    "s_waitcnt lgkmcnt(7)\n"
    "ds_read_b128 v[212:215], v250 offset:1200\n"
    "ds_read_b128 v[220:223], v252 offset:13104\n"
    "v_or_b32_e64 v187, v187, v203\n"
    "v_mfma_f32_16x16x32_bf16 v[132:135], v[164:167], v[192:195], v[132:135]\n"
    "ds_read_b128 a[224:227], v243 offset:0\n"
    "v_cvt_pk_f16_f32 v192, v168, v169\n"
    "v_mfma_f32_16x16x32_bf16 v[124:127], a[188:191], v[196:199], v[124:127]\n"
    "ds_read_u16_d16_hi v206, v206\n"
    "v_lshlrev_b32_sdwa v193, 1, v170 dst_sel:DWORD dst_unused:UNUSED_PAD src0_sel:DWORD src1_sel:WORD_1\n"
    "v_lshlrev_b32_sdwa v194, 1, v172 dst_sel:DWORD dst_unused:UNUSED_PAD src0_sel:DWORD src1_sel:WORD_1\n"
    "v_lshlrev_b32_sdwa v195, 1, v174 dst_sel:DWORD dst_unused:UNUSED_PAD src0_sel:DWORD src1_sel:WORD_1\n"
    "v_lshlrev_b32_sdwa v203, 1, v175 dst_sel:DWORD dst_unused:UNUSED_PAD src0_sel:DWORD src1_sel:WORD_1\n"
    "v_mfma_f32_16x16x32_bf16 v[136:139], v[236:239], v[196:199], v[136:139]\n"
    "s_nop 0\n"
    "ds_read_b128 a[228:231], v243 offset:1024\n"
    "v_pk_fma_f16 v232, v192, v192, s48\n"
    "v_mfma_f32_16x16x32_bf16 v[140:143], v[164:167], v[196:199], v[140:143]\n"
    "s_waitcnt lgkmcnt(10)\n"
    "ds_read_u16 v191, v191\n"
    "v_cvt_pk_f16_f32 v196, v176, v177\n"
    "v_lshlrev_b32_sdwa v197, 1, v178 dst_sel:DWORD dst_unused:UNUSED_PAD src0_sel:DWORD src1_sel:WORD_1\n"
    "v_lshlrev_b32_sdwa v198, 1, v180 dst_sel:DWORD dst_unused:UNUSED_PAD src0_sel:DWORD src1_sel:WORD_1\n"
    "v_lshlrev_b32_sdwa v199, 1, v182 dst_sel:DWORD dst_unused:UNUSED_PAD src0_sel:DWORD src1_sel:WORD_1\n"
    "v_or_b32_e64 v188, v188, v204\n"
    "v_pk_mul_f16 v232, v192, v232\n"
    "ds_read_b128 a[232:235], v243 offset:2048\n"
    "v_exp_f16_e64 v234, v232\n"
    "ds_read_u16_d16_hi v207, v207\n"
    "s_waitcnt lgkmcnt(11)\n"
    "v_or_b32_e32 v189, v189, v205\n"
    "v_lshlrev_b32_sdwa v205, 1, v179 dst_sel:DWORD dst_unused:UNUSED_PAD src0_sel:DWORD src1_sel:WORD_1\n"
    "ds_read_b128 a[236:239], v243 offset:3072\n"
    "v_exp_f16_sdwa v234, v232 dst_sel:WORD_1 dst_unused:UNUSED_PRESERVE src0_sel:WORD_1\n"
    "ds_read_b128 a[240:243], v243 offset:4096\n"
    "ds_read_b128 a[244:247], v243 offset:5120\n"
    "v_pk_fma_f16 v234, v234, s49, s49\n"
    "s_waitcnt lgkmcnt(10)\n"
    "v_smfmac_f32_16x16x64_bf16 v[56:59], v[216:219], v[208:215], v240\n"
    "s_waitcnt lgkmcnt(9)\n"
    "ds_read_b128 v[216:219], v251 offset:21504\n"
    "v_smfmac_f32_16x16x64_bf16 v[60:63], v[220:223], v[208:215], v240 abid:1\n"
    "s_waitcnt lgkmcnt(9)\n"
    "ds_read_b128 v[208:211], v250 offset:51648\n"
    "v_mfma_f32_16x16x32_bf16 v[168:171], a[80:83], a[224:227], 0\n"
    "s_nop 0\n"
    "ds_read_b128 v[212:215], v250 offset:51968\n"
    "v_rcp_f16_e32 v232, v234\n"
    "v_mfma_f32_16x16x32_bf16 v[172:175], a[84:87], a[224:227], 0\n"
    "s_waitcnt lgkmcnt(9)\n"
    "ds_read_b128 v[220:223], v251 offset:21840\n"
    "buffer_store_dwordx4 v[160:163], v245, s[44:47], s88 offen offset:128 sc1\n"
    "v_pk_add_f32 v[224:225], v[224:225], v[56:57]\n"
    "v_pk_add_f32 v[226:227], v[226:227], v[58:59]\n"
    "v_pk_fma_f32 v[228:229], v[56:57], v[56:57], v[228:229]\n"
    "v_pk_fma_f32 v[230:231], v[58:59], v[58:59], v[230:231]\n"
    "v_pk_add_f32 v[224:225], v[224:225], v[60:61]\n"
    "v_pk_add_f32 v[226:227], v[226:227], v[62:63]\n"
    "v_pk_fma_f32 v[228:229], v[60:61], v[60:61], v[228:229]\n"
    "v_pk_fma_f32 v[230:231], v[62:63], v[62:63], v[230:231]\n"
    "v_mfma_f32_16x16x32_bf16 v[168:171], a[128:131], a[228:231], v[168:171]\n"
    "s_waitcnt lgkmcnt(8)\n"
    "ds_read_u16 v193, v193\n"
    "ds_read_b128 a[252:255], v244 offset:32768\n"
    "v_mfma_f32_16x16x32_bf16 v[168:171], a[48:51], a[232:235], v[168:171]\n"
    "s_nop 0\n"
    "ds_read_u16_d16_hi v201, v201\n"
    "ds_read_u16 v194, v194\n"
    "v_or_b32_e32 v190, v190, v206\n"
    "v_lshlrev_b32_sdwa v206, 1, v181 dst_sel:DWORD dst_unused:UNUSED_PAD src0_sel:DWORD src1_sel:WORD_1\n"
    "v_rcp_f16_sdwa v232, v234 dst_sel:WORD_1 dst_unused:UNUSED_PRESERVE src0_sel:WORD_1\n"
    "v_mfma_f32_16x16x32_bf16 v[172:175], a[132:135], a[228:231], v[172:175]\n"
    "s_waitcnt lgkmcnt(9)\n"
    "ds_read_u16_d16_hi v202, v202\n"
    "v_or_b32_e32 v191, v191, v207\n"
    "v_mfma_f32_16x16x32_bf16 v[176:179], a[80:83], a[236:239], 0\n"
    "v_lshlrev_b32_sdwa v207, 1, v183 dst_sel:DWORD dst_unused:UNUSED_PAD src0_sel:DWORD src1_sel:WORD_1\n"
    "v_lshlrev_b32_sdwa v200, 1, v169 dst_sel:DWORD dst_unused:UNUSED_PAD src0_sel:DWORD src1_sel:WORD_1\n"
    "v_mfma_f32_16x16x32_bf16 v[176:179], a[128:131], a[240:243], v[176:179]\n"
    "s_waitcnt lgkmcnt(8)\n"
    "ds_read_u16 v195, v195\n"
    "v_fma_mix_f32 v234, v192, v232, 0 op_sel_hi:[1,1,0]\n"
    "v_mfma_f32_16x16x32_bf16 v[176:179], a[48:51], a[244:247], v[176:179]\n"
    "buffer_load_dwordx4 v[152:155], v245, s[40:43], s58 offen offset:64\n"
    "buffer_load_dwordx4 v[144:147], v245, s[40:43], s58 offen offset:0\n"
    "ds_read_b128 v[236:239], v244 offset:16384\n"
    "v_mfma_f32_16x16x32_bf16 v[180:183], a[84:87], a[236:239], 0\n"
    "s_waitcnt lgkmcnt(8)\n"
    "ds_read_u16_d16_hi v203, v203\n"
    "v_fma_mix_f32 v232, v192, v232, 0 op_sel:[1,1,0] op_sel_hi:[1,1,0]\n"
    "v_mfma_f32_16x16x32_bf16 v[180:183], a[132:135], a[240:243], v[180:183]\n"
    "s_waitcnt lgkmcnt(8)\n"
    "v_lshlrev_b32_sdwa v204, 1, v177 dst_sel:DWORD dst_unused:UNUSED_PAD src0_sel:DWORD src1_sel:WORD_1\n"
    "v_cvt_pk_bf16_f32 v192, v234, v232\n"
    "v_smfmac_f32_16x16x64_bf16 v[64:67], v[216:219], v[208:215], v240\n"
    "s_nop 0\n"
    "ds_read_b128 v[216:219], v252 offset:21504\n"
    "v_smfmac_f32_16x16x64_bf16 v[68:71], v[220:223], v[208:215], v240 abid:1\n"
    "s_waitcnt lgkmcnt(7)\n"
    "ds_read_b128 v[208:211], v250 offset:52288\n"
    "v_mfma_f32_16x16x32_bf16 v[172:175], a[252:255], a[232:235], v[172:175]\n"
    "ds_read_b128 v[212:215], v250 offset:52608\n"
    "v_lshlrev_b32_e64 v254, 3, v244\n"
    "v_mfma_f32_16x16x32_bf16 v[180:183], a[252:255], a[244:247], v[180:183]\n"
    "ds_read_b128 v[220:223], v252 offset:21840\n"
    "v_pk_fma_f16 v232, v196, v196, s48\n"
    "v_mfma_f32_16x16x32_bf16 v[96:99], a[192:195], v[184:187], v[96:99]\n"
    "s_waitcnt lgkmcnt(9)\n"
    "ds_read_b128 v[164:167], v244 offset:18432\n"
    "v_or_b32_e32 v193, v193, v201\n"
    "v_mfma_f32_16x16x32_bf16 v[100:103], a[196:199], v[184:187], v[100:103]\n"
    "ds_read_u16 v197, v197\n"
    "v_lshlrev_b32_sdwa v201, 1, v171 dst_sel:DWORD dst_unused:UNUSED_PAD src0_sel:DWORD src1_sel:WORD_1\n"
    "v_mfma_f32_16x16x32_bf16 v[112:115], a[200:203], v[184:187], v[112:115]\n"
    "v_pk_mul_f16 v232, v196, v232\n"
    "v_exp_f16_e64 v234, v232\n"
    "v_mfma_f32_16x16x32_bf16 v[116:119], a[204:207], v[184:187], v[116:119]\n"
    "ds_read_u16_d16_hi v205, v205\n"
    "v_exp_f16_sdwa v234, v232 dst_sel:WORD_1 dst_unused:UNUSED_PRESERVE src0_sel:WORD_1\n"
    "v_mfma_f32_16x16x32_bf16 v[104:107], a[192:195], v[188:191], v[104:107]\n"
    "s_waitcnt lgkmcnt(10)\n"
    "ds_read_u16 v198, v198\n"
    "v_or_b32_e32 v194, v194, v202\n"
    "v_lshlrev_b32_sdwa v202, 1, v173 dst_sel:DWORD dst_unused:UNUSED_PAD src0_sel:DWORD src1_sel:WORD_1\n"
    "v_pk_fma_f16 v234, v234, s49, s49\n"
    "v_mfma_f32_16x16x32_bf16 v[108:111], a[196:199], v[188:191], v[108:111]\n"
    "s_waitcnt lgkmcnt(9)\n"
    "ds_read_u16_d16_hi v206, v206\n"
    "v_rcp_f16_e32 v232, v234\n"
    "v_mfma_f32_16x16x32_bf16 v[128:131], v[236:239], v[184:187], v[128:131]\n"
    "buffer_load_dwordx4 v[156:159], v245, s[40:43], s89 offen offset:64\n"
    "buffer_load_dwordx4 v[148:151], v245, s[40:43], s89 offen offset:0\n"
    "ds_read_u16 v199, v199\n"
    "v_rcp_f16_sdwa v232, v234 dst_sel:WORD_1 dst_unused:UNUSED_PRESERVE src0_sel:WORD_1\n"
    "v_mfma_f32_16x16x32_bf16 v[120:123], a[200:203], v[188:191], v[120:123]\n"
    "s_waitcnt lgkmcnt(8)\n"
    "v_or_b32_e64 v195, v195, v203\n"
    "v_lshlrev_b32_sdwa v203, 1, v175 dst_sel:DWORD dst_unused:UNUSED_PAD src0_sel:DWORD src1_sel:WORD_1\n"
    "v_mfma_f32_16x16x32_bf16 v[124:127], a[204:207], v[188:191], v[124:127]\n"
    "s_waitcnt lgkmcnt(7)\n"
    "ds_read_u16_d16_hi v207, v207\n"
    "v_fma_mix_f32 v234, v196, v232, 0 op_sel_hi:[1,1,0]\n"
    "v_smfmac_f32_16x16x64_bf16 v[64:67], v[216:219], v[208:215], v240\n"
    "s_waitcnt lgkmcnt(7)\n"
    "ds_read_b128 v[216:219], v251 offset:22176\n"
    "v_smfmac_f32_16x16x64_bf16 v[68:71], v[220:223], v[208:215], v240 abid:1\n"
    "s_waitcnt lgkmcnt(7)\n"
    "ds_read_b128 v[208:211], v250 offset:51728\n"
    "v_mfma_f32_16x16x32_bf16 v[132:135], v[164:167], v[184:187], v[132:135]\n"
    "v_lshlrev_b32_sdwa v184, 1, v168 dst_sel:DWORD dst_unused:UNUSED_PAD src0_sel:DWORD src1_sel:WORD_1\n"
    "v_lshlrev_b32_sdwa v185, 1, v170 dst_sel:DWORD dst_unused:UNUSED_PAD src0_sel:DWORD src1_sel:WORD_1\n"
    "ds_read_b128 v[212:215], v250 offset:52048\n"
    "ds_read_b128 v[220:223], v251 offset:22512\n"
    "v_pk_add_f32 v[224:225], v[224:225], v[64:65]\n"
    "v_pk_add_f32 v[226:227], v[226:227], v[66:67]\n"
    "v_pk_fma_f32 v[228:229], v[64:65], v[64:65], v[228:229]\n"
    "v_pk_fma_f32 v[230:231], v[66:67], v[66:67], v[230:231]\n"
    "v_pk_add_f32 v[224:225], v[224:225], v[68:69]\n"
    "v_pk_add_f32 v[226:227], v[226:227], v[70:71]\n"
    "v_pk_fma_f32 v[228:229], v[68:69], v[68:69], v[228:229]\n"
    "v_pk_fma_f32 v[230:231], v[70:71], v[70:71], v[230:231]\n"
    "v_mfma_f32_16x16x32_bf16 v[136:139], v[236:239], v[188:191], v[136:139]\n"
    "s_waitcnt lgkmcnt(8)\n"
    "s_mov_b32 exec_hi, 0\n"
    "buffer_load_dword v233, v254, s[72:75], s53 offen sc1\n"
    "s_mov_b32 exec_hi, -1\n"
    "s_add_u32 s53, s53, 393216\n"
    "v_mfma_f32_16x16x32_bf16 v[140:143], v[164:167], v[188:191], v[140:143]\n"
    "s_nop 0\n"
    "ds_read_u16 v184, v184\n"
    "ds_read_b128 a[252:255], v244 offset:36864\n"
    "v_mfma_f32_16x16x32_bf16 v[168:171], a[88:91], a[224:227], 0\n"
    "ds_read_u16_d16_hi v200, v200\n"
    "ds_read_u16 v185, v185\n"
    "v_mfma_f32_16x16x32_bf16 v[168:171], a[136:139], a[228:231], v[168:171]\n"
    "s_waitcnt lgkmcnt(10)\n"
    "v_or_b32_e32 v197, v197, v205\n"
    "v_lshlrev_b32_sdwa v186, 1, v172 dst_sel:DWORD dst_unused:UNUSED_PAD src0_sel:DWORD src1_sel:WORD_1\n"
    "v_mfma_f32_16x16x32_bf16 v[168:171], a[52:55], a[232:235], v[168:171]\n"
    "ds_read_u16_d16_hi v201, v201\n"
    "v_or_b32_e64 v198, v198, v206\n"
    "v_lshlrev_b32_sdwa v187, 1, v174 dst_sel:DWORD dst_unused:UNUSED_PAD src0_sel:DWORD src1_sel:WORD_1\n"
    "v_mfma_f32_16x16x32_bf16 v[172:175], a[92:95], a[224:227], 0\n"
    "s_nop 0\n"
    "v_lshlrev_b32_sdwa v188, 1, v176 dst_sel:DWORD dst_unused:UNUSED_PAD src0_sel:DWORD src1_sel:WORD_1\n"
    "v_lshlrev_b32_sdwa v189, 1, v178 dst_sel:DWORD dst_unused:UNUSED_PAD src0_sel:DWORD src1_sel:WORD_1\n"
    "v_mfma_f32_16x16x32_bf16 v[172:175], a[140:143], a[228:231], v[172:175]\n"
    "s_waitcnt lgkmcnt(8)\n"
    "ds_read_u16 v186, v186\n"
    "v_lshlrev_b32_sdwa v205, 1, v179 dst_sel:DWORD dst_unused:UNUSED_PAD src0_sel:DWORD src1_sel:WORD_1\n"
    "v_mfma_f32_16x16x32_bf16 v[176:179], a[88:91], a[236:239], 0\n"
    "v_lshlrev_b32_sdwa v190, 1, v180 dst_sel:DWORD dst_unused:UNUSED_PAD src0_sel:DWORD src1_sel:WORD_1\n"
    "v_lshlrev_b32_sdwa v206, 1, v181 dst_sel:DWORD dst_unused:UNUSED_PAD src0_sel:DWORD src1_sel:WORD_1\n"
    "v_mfma_f32_16x16x32_bf16 v[176:179], a[136:139], a[240:243], v[176:179]\n"
    "s_waitcnt lgkmcnt(7)\n"
    "ds_read_u16_d16_hi v202, v202\n"
    "v_lshlrev_b32_sdwa v191, 1, v182 dst_sel:DWORD dst_unused:UNUSED_PAD src0_sel:DWORD src1_sel:WORD_1\n"
    "v_mfma_f32_16x16x32_bf16 v[176:179], a[52:55], a[244:247], v[176:179]\n"
    "s_waitcnt lgkmcnt(7)\n"
    "ds_read_b128 v[236:239], v244 offset:20480\n"
    "v_or_b32_e32 v199, v199, v207\n"
    "v_smfmac_f32_16x16x64_bf16 v[72:75], v[216:219], v[208:215], v240\n"
    "ds_read_b128 v[216:219], v252 offset:22176\n"
    "v_smfmac_f32_16x16x64_bf16 v[76:79], v[220:223], v[208:215], v240 abid:1\n"
    "s_waitcnt lgkmcnt(7)\n"
    "ds_read_b128 v[208:211], v250 offset:52368\n"
    "v_mfma_f32_16x16x32_bf16 v[172:175], a[252:255], a[232:235], v[172:175]\n"
    "ds_read_b128 v[212:215], v250 offset:52688\n"
    "v_lshlrev_b32_sdwa v207, 1, v183 dst_sel:DWORD dst_unused:UNUSED_PAD src0_sel:DWORD src1_sel:WORD_1\n"
    "v_mfma_f32_16x16x32_bf16 v[180:183], a[92:95], a[236:239], 0\n"
    "ds_read_b128 v[220:223], v252 offset:22512\n"
    "v_fma_mix_f32 v232, v196, v232, 0 op_sel:[1,1,0] op_sel_hi:[1,1,0]\n"
    "v_mfma_f32_16x16x32_bf16 v[180:183], a[140:143], a[240:243], v[180:183]\n"
    "ds_read_u16 v187, v187\n"
    "v_cvt_pk_bf16_f32 v196, v234, v232\n"
    "v_mfma_f32_16x16x32_bf16 v[180:183], a[252:255], a[244:247], v[180:183]\n"
    "s_waitcnt lgkmcnt(10)\n"
    "v_or_b32_e32 v184, v184, v200\n"
    "v_mfma_f32_16x16x32_bf16 v[96:99], a[208:211], v[192:195], v[96:99]\n"
    "ds_read_u16_d16_hi v203, v203\n"
    "v_mfma_f32_16x16x32_bf16 v[100:103], a[212:215], v[192:195], v[100:103]\n"
    "s_nop 0\n"
    "v_mfma_f32_16x16x32_bf16 v[112:115], a[216:219], v[192:195], v[112:115]\n"
    "s_waitcnt lgkmcnt(9)\n"
    "ds_read_b128 v[164:167], v244 offset:22528\n"
    "v_or_b32_e64 v185, v185, v201\n"
    "v_mfma_f32_16x16x32_bf16 v[116:119], a[220:223], v[192:195], v[116:119]\n"
    "ds_read_u16 v188, v188\n"
    "v_lshlrev_b32_sdwa v201, 1, v171 dst_sel:DWORD dst_unused:UNUSED_PAD src0_sel:DWORD src1_sel:WORD_1\n"
    "v_cvt_pk_bf16_f32 v96, v96, v97\n"
    "v_mfma_f32_16x16x32_bf16 v[104:107], a[208:211], v[196:199], v[104:107]\n"
    "ds_read_u16_d16_hi v204, v204\n"
    "v_cvt_pk_bf16_f32 v97, v98, v99\n"
    "v_cvt_pk_bf16_f32 v98, v100, v101\n"
    "v_cvt_pk_bf16_f32 v99, v102, v103\n"
    "v_cvt_pk_bf16_f32 v112, v112, v113\n"
    "v_mfma_f32_16x16x32_bf16 v[108:111], a[212:215], v[196:199], v[108:111]\n"
    "s_waitcnt lgkmcnt(8)\n"
    "ds_read_u16 v189, v189\n"
    "v_or_b32_e32 v186, v186, v202\n"
    "v_lshlrev_b32_sdwa v202, 1, v173 dst_sel:DWORD dst_unused:UNUSED_PAD src0_sel:DWORD src1_sel:WORD_1\n"
    "v_cvt_pk_bf16_f32 v113, v114, v115\n"
    "v_cvt_pk_bf16_f32 v114, v116, v117\n"
    "v_mfma_f32_16x16x32_bf16 v[128:131], v[236:239], v[192:195], v[128:131]\n"
    "v_cvt_pk_bf16_f32 v115, v118, v119\n"
    "v_cvt_pk_bf16_f32 v104, v104, v105\n"
    "v_mfma_f32_16x16x32_bf16 v[120:123], a[216:219], v[196:199], v[120:123]\n"
    "s_waitcnt lgkmcnt(7)\n"
    "v_lshlrev_b32_e64 v254, 3, v244\n"
    "v_cvt_pk_bf16_f32 v105, v106, v107\n"
    "v_smfmac_f32_16x16x64_bf16 v[72:75], v[216:219], v[208:215], v240\n"
    "s_waitcnt lgkmcnt(6)\n"
    "ds_write_b128 v243, v[96:99] offset:0\n"
    "v_cvt_pk_bf16_f32 v128, v128, v129\n"
    "v_cvt_pk_bf16_f32 v129, v130, v131\n"
    "v_cvt_pk_bf16_f32 v106, v108, v109\n"
    "v_cvt_pk_bf16_f32 v107, v110, v111\n"
    "v_cvt_pk_bf16_f32 v120, v120, v121\n"
    "v_cvt_pk_bf16_f32 v121, v122, v123\n"
    "v_pk_add_f32 v[224:225], v[224:225], v[72:73]\n"
    "v_pk_add_f32 v[226:227], v[226:227], v[74:75]\n"
    "v_pk_fma_f32 v[228:229], v[72:73], v[72:73], v[228:229]\n"
    "v_pk_fma_f32 v[230:231], v[74:75], v[74:75], v[230:231]\n"
    "v_smfmac_f32_16x16x64_bf16 v[76:79], v[220:223], v[208:215], v240 abid:1\n"
    "s_waitcnt lgkmcnt(4)\n"
    "ds_read_b128 v[208:211], v250 offset:51808\n"
    "ds_read_b128 v[212:215], v250 offset:52128\n"
    "v_mfma_f32_16x16x32_bf16 v[132:135], v[164:167], v[192:195], v[132:135]\n"
    "s_nop 0\n"
    "ds_read_b128 v[216:219], v251 offset:22848\n"
    "ds_read_b128 v[220:223], v251 offset:23184\n"
    "v_mfma_f32_16x16x32_bf16 v[124:127], a[220:223], v[196:199], v[124:127]\n"
    "ds_read_u16_d16_hi v205, v205\n"
    "ds_read_u16 v190, v190\n"
    "v_cvt_pk_f16_f32 v192, v168, v169\n"
    "v_lshlrev_b32_sdwa v193, 1, v170 dst_sel:DWORD dst_unused:UNUSED_PAD src0_sel:DWORD src1_sel:WORD_1\n"
    "v_lshlrev_b32_sdwa v194, 1, v172 dst_sel:DWORD dst_unused:UNUSED_PAD src0_sel:DWORD src1_sel:WORD_1\n"
    "v_pk_add_f32 v[224:225], v[224:225], v[76:77]\n"
    "v_pk_add_f32 v[226:227], v[226:227], v[78:79]\n"
    "v_pk_fma_f32 v[228:229], v[76:77], v[76:77], v[228:229]\n"
    "v_pk_fma_f32 v[230:231], v[78:79], v[78:79], v[230:231]\n"
    "v_mfma_f32_16x16x32_bf16 v[136:139], v[236:239], v[196:199], v[136:139]\n"
    "s_waitcnt lgkmcnt(8)\n"
    "ds_read_u16_d16_hi v206, v206\n"
    "s_mov_b32 exec_hi, 0\n"
    "buffer_load_dword v233, v254, s[72:75], s53 offen sc1\n"
    "s_mov_b32 exec_hi, -1\n"
    "v_mfma_f32_16x16x32_bf16 v[140:143], v[164:167], v[196:199], v[140:143]\n"
    "s_add_u32 s53, s53, 393216\n"
    "ds_read_u16 v191, v191\n"
    "v_lshlrev_b32_sdwa v195, 1, v174 dst_sel:DWORD dst_unused:UNUSED_PAD src0_sel:DWORD src1_sel:WORD_1\n"
    "v_mfma_f32_16x16x32_bf16 v[168:171], a[96:99], a[224:227], 0\n"
    "s_nop 0\n"
    "ds_read_u16_d16_hi v207, v207\n"
    "v_cvt_pk_f16_f32 v196, v176, v177\n"
    "v_mfma_f32_16x16x32_bf16 v[168:171], a[144:147], a[228:231], v[168:171]\n"
    "v_lshlrev_b32_sdwa v197, 1, v178 dst_sel:DWORD dst_unused:UNUSED_PAD src0_sel:DWORD src1_sel:WORD_1\n"
    "v_lshlrev_b32_sdwa v198, 1, v180 dst_sel:DWORD dst_unused:UNUSED_PAD src0_sel:DWORD src1_sel:WORD_1\n"
    "v_mfma_f32_16x16x32_bf16 v[168:171], a[56:59], a[232:235], v[168:171]\n"
    "ds_read_u16 v193, v193\n"
    "v_lshlrev_b32_sdwa v199, 1, v182 dst_sel:DWORD dst_unused:UNUSED_PAD src0_sel:DWORD src1_sel:WORD_1\n"
    "v_or_b32_e64 v187, v187, v203\n"
    "s_waitcnt lgkmcnt(7)\n"
    "v_smfmac_f32_16x16x64_bf16 v[80:83], v[216:219], v[208:215], v240\n"
    "s_waitcnt lgkmcnt(6)\n"
    "v_lshlrev_b32_sdwa v203, 1, v175 dst_sel:DWORD dst_unused:UNUSED_PAD src0_sel:DWORD src1_sel:WORD_1\n"
    "v_smfmac_f32_16x16x64_bf16 v[84:87], v[220:223], v[208:215], v240 abid:1\n"
    "ds_write_b128 v243, v[112:115] offset:2048\n"
    "v_or_b32_e64 v188, v188, v204\n"
    "v_cvt_pk_bf16_f32 v130, v132, v133\n"
    "v_cvt_pk_bf16_f32 v131, v134, v135\n"
    "v_cvt_pk_bf16_f32 v122, v124, v125\n"
    "v_cvt_pk_bf16_f32 v123, v126, v127\n"
    "v_add_u32_e64 v254, s54, v242\n"
    "v_mfma_f32_16x16x32_bf16 v[172:175], a[100:103], a[224:227], 0\n"
    "s_waitcnt lgkmcnt(6)\n"
    "v_or_b32_e32 v189, v189, v205\n"
    "v_lshlrev_b32_sdwa v205, 1, v179 dst_sel:DWORD dst_unused:UNUSED_PAD src0_sel:DWORD src1_sel:WORD_1\n"
    "v_mfma_f32_16x16x32_bf16 v[172:175], a[148:151], a[228:231], v[172:175]\n"
    "ds_read_b128 v[208:211], v250 offset:52448\n"
    "v_cvt_pk_bf16_f32 v136, v136, v137\n"
    "v_mfma_f32_16x16x32_bf16 v[172:175], a[60:63], a[232:235], v[172:175]\n"
    "ds_read_b128 v[212:215], v250 offset:52768\n"
    "ds_write_b128 v243, v[128:131] offset:4096\n"
    "v_pk_fma_f16 v232, v192, v192, s48\n"
    "v_cvt_pk_bf16_f32 v137, v138, v139\n"
    "v_cvt_pk_bf16_f32 v138, v140, v141\n"
    "v_cvt_pk_bf16_f32 v139, v142, v143\n"
    "v_pk_mul_f16 v232, v192, v232\n"
    "v_lshlrev_b32_sdwa v200, 1, v169 dst_sel:DWORD dst_unused:UNUSED_PAD src0_sel:DWORD src1_sel:WORD_1\n"
    "v_exp_f16_e32 v234, v232\n"
    "v_mfma_f32_16x16x32_bf16 v[176:179], a[96:99], a[236:239], 0\n"
    "s_waitcnt lgkmcnt(5)\n"
    "ds_read_b128 v[216:219], v252 offset:22848\n"
    "ds_read_b128 v[220:223], v252 offset:23184\n"
    "v_mfma_f32_16x16x32_bf16 v[176:179], a[144:147], a[240:243], v[176:179]\n"
    "v_or_b32_e64 v190, v190, v206\n"
    "v_lshlrev_b32_sdwa v206, 1, v181 dst_sel:DWORD dst_unused:UNUSED_PAD src0_sel:DWORD src1_sel:WORD_1\n"
    "v_mfma_f32_16x16x32_bf16 v[176:179], a[56:59], a[244:247], v[176:179]\n"
    "ds_read_u16_d16_hi v201, v201\n"
    "ds_write_b128 v243, v[104:107] offset:1024\n"
    "v_or_b32_e64 v191, v191, v207\n"
    "v_lshlrev_b32_sdwa v207, 1, v183 dst_sel:DWORD dst_unused:UNUSED_PAD src0_sel:DWORD src1_sel:WORD_1\n"
    "v_mfma_f32_16x16x32_bf16 v[180:183], a[100:103], a[236:239], 0\n"
    "v_exp_f16_sdwa v234, v232 dst_sel:WORD_1 dst_unused:UNUSED_PRESERVE src0_sel:WORD_1\n"
    "v_mfma_f32_16x16x32_bf16 v[180:183], a[148:151], a[240:243], v[180:183]\n"
    "v_pk_fma_f16 v234, v234, s49, s49\n"
    "v_lshlrev_b32_sdwa v204, 1, v177 dst_sel:DWORD dst_unused:UNUSED_PAD src0_sel:DWORD src1_sel:WORD_1\n"
    "v_mfma_f32_16x16x32_bf16 v[180:183], a[60:63], a[244:247], v[180:183]\n"
    "ds_read_u16 v194, v194\n"
    "ds_write_b128 v243, v[120:123] offset:3072\n"
    "v_mfma_f32_16x16x32_bf16 v[96:99], a[0:3], v[184:187], 0\n"
    "s_waitcnt lgkmcnt(5)\n"
    "v_rcp_f16_e32 v232, v234\n"
    "v_smfmac_f32_16x16x64_bf16 v[80:83], v[216:219], v[208:215], v240\n"
    "s_waitcnt lgkmcnt(4)\n"
    "v_rcp_f16_sdwa v232, v234 dst_sel:WORD_1 dst_unused:UNUSED_PRESERVE src0_sel:WORD_1\n"
    "v_smfmac_f32_16x16x64_bf16 v[84:87], v[220:223], v[208:215], v240 abid:1\n"
    "ds_read_b128 v[208:211], v250 offset:51888\n"
    "ds_read_b128 v[212:215], v250 offset:52208\n"
    "v_mfma_f32_16x16x32_bf16 v[100:103], a[4:7], v[184:187], 0\n"
    "s_waitcnt lgkmcnt(4)\n"
    "ds_write_b128 v243, v[136:139] offset:5120\n"
    "v_pk_add_f32 v[224:225], v[224:225], v[80:81]\n"
    "v_pk_add_f32 v[226:227], v[226:227], v[82:83]\n"
    "v_pk_fma_f32 v[228:229], v[80:81], v[80:81], v[228:229]\n"
    "v_pk_fma_f32 v[230:231], v[82:83], v[82:83], v[230:231]\n"
    "v_pk_add_f32 v[224:225], v[224:225], v[84:85]\n"
    "v_pk_add_f32 v[226:227], v[226:227], v[86:87]\n"
    "v_pk_fma_f32 v[228:229], v[84:85], v[84:85], v[228:229]\n"
    "v_pk_fma_f32 v[230:231], v[86:87], v[86:87], v[230:231]\n"
    "v_mfma_f32_16x16x32_bf16 v[112:115], a[8:11], v[184:187], 0\n"
    "ds_read_b128 v[216:219], v251 offset:23520\n"
    "ds_read_b128 v[220:223], v251 offset:23856\n"
    "v_mfma_f32_16x16x32_bf16 v[116:119], a[12:15], v[184:187], 0\n"
    "ds_read_u16_d16_hi v202, v202\n"
    "ds_read_u16 v195, v195\n"
    "v_mfma_f32_16x16x32_bf16 v[104:107], a[0:3], v[188:191], 0\n"
    "ds_read_b128 v[236:239], v244 offset:0\n"
    "v_or_b32_e64 v193, v193, v201\n"
    "v_mfma_f32_16x16x32_bf16 v[108:111], a[4:7], v[188:191], 0\n"
    "ds_read_u16_d16_hi v203, v203\n"
    "v_lshlrev_b32_sdwa v201, 1, v171 dst_sel:DWORD dst_unused:UNUSED_PAD src0_sel:DWORD src1_sel:WORD_1\n"
    "v_fma_mix_f32 v234, v192, v232, 0 op_sel_hi:[1,1,0]\n"
    "v_mfma_f32_16x16x32_bf16 v[120:123], a[8:11], v[188:191], 0\n"
    "ds_read_b128 v[164:167], v244 offset:2048\n"
    "v_fma_mix_f32 v232, v192, v232, 0 op_sel:[1,1,0] op_sel_hi:[1,1,0]\n"
    "v_mfma_f32_16x16x32_bf16 v[124:127], a[12:15], v[188:191], 0\n"
    "ds_read_u16 v197, v197\n"
    "v_cvt_pk_bf16_f32 v192, v234, v232\n"
    "v_pk_fma_f16 v232, v196, v196, s48\n"
    "v_pk_mul_f16 v232, v196, v232\n"
    "v_exp_f16_e64 v234, v232\n"
    "ds_read_u16_d16_hi v205, v205\n"
    "v_exp_f16_sdwa v234, v232 dst_sel:WORD_1 dst_unused:UNUSED_PRESERVE src0_sel:WORD_1\n"
    "s_waitcnt lgkmcnt(8)\n"
    "v_smfmac_f32_16x16x64_bf16 v[88:91], v[216:219], v[208:215], v240\n"
    "s_waitcnt lgkmcnt(7)\n"
    "ds_read_b128 v[216:219], v252 offset:23520\n"
    "v_smfmac_f32_16x16x64_bf16 v[92:95], v[220:223], v[208:215], v240 abid:1\n"
    "ds_read_b128 v[208:211], v250 offset:52528\n"
    "v_pk_fma_f16 v234, v234, s49, s49\n"
    "v_rcp_f16_e64 v232, v234\n"
    "ds_read_b128 v[212:215], v250 offset:52848\n"
    "ds_read_b128 v[220:223], v252 offset:23856\n"
    "s_waitcnt lgkmcnt(10)\n"
    "v_or_b32_e32 v194, v194, v202\n"
    "v_lshlrev_b32_sdwa v202, 1, v173 dst_sel:DWORD dst_unused:UNUSED_PAD src0_sel:DWORD src1_sel:WORD_1\n"
    "ds_read_u16 v198, v198\n"
    "v_rcp_f16_sdwa v232, v234 dst_sel:WORD_1 dst_unused:UNUSED_PRESERVE src0_sel:WORD_1\n"
    "s_waitcnt lgkmcnt(9)\n"
    "v_mfma_f32_16x16x32_bf16 v[128:131], v[236:239], v[184:187], 0\n"
    "s_nop 0\n"
    "ds_read_u16_d16_hi v206, v206\n"
    "v_fma_mix_f32 v234, v196, v232, 0 op_sel_hi:[1,1,0]\n"
    "v_fma_mix_f32 v232, v196, v232, 0 op_sel:[1,1,0] op_sel_hi:[1,1,0]\n"
    "v_cvt_pk_bf16_f32 v196, v234, v232\n"
    "v_mfma_f32_16x16x32_bf16 v[136:139], v[236:239], v[188:191], 0\n"
    "s_waitcnt lgkmcnt(8)\n"
    "ds_read_u16 v199, v199\n"
    "v_or_b32_e32 v195, v195, v203\n"
    "v_lshlrev_b32_sdwa v203, 1, v175 dst_sel:DWORD dst_unused:UNUSED_PAD src0_sel:DWORD src1_sel:WORD_1\n"
    "v_mfma_f32_16x16x32_bf16 v[132:135], v[164:167], v[184:187], 0\n"
    "v_lshlrev_b32_sdwa v184, 1, v168 dst_sel:DWORD dst_unused:UNUSED_PAD src0_sel:DWORD src1_sel:WORD_1\n"
    "v_lshlrev_b32_sdwa v185, 1, v170 dst_sel:DWORD dst_unused:UNUSED_PAD src0_sel:DWORD src1_sel:WORD_1\n"
    "v_mfma_f32_16x16x32_bf16 v[140:143], v[164:167], v[188:191], 0\n"
    "ds_read_u16_d16_hi v207, v207\n"
    "v_lshlrev_b32_sdwa v186, 1, v172 dst_sel:DWORD dst_unused:UNUSED_PAD src0_sel:DWORD src1_sel:WORD_1\n"
    "v_mfma_f32_16x16x32_bf16 v[168:171], a[104:107], a[224:227], 0\n"
    "v_lshlrev_b32_sdwa v187, 1, v174 dst_sel:DWORD dst_unused:UNUSED_PAD src0_sel:DWORD src1_sel:WORD_1\n"
    "v_lshlrev_b32_sdwa v188, 1, v176 dst_sel:DWORD dst_unused:UNUSED_PAD src0_sel:DWORD src1_sel:WORD_1\n"
    "v_mfma_f32_16x16x32_bf16 v[168:171], a[152:155], a[228:231], v[168:171]\n"
    "ds_read_u16 v184, v184\n"
    "v_lshlrev_b32_sdwa v189, 1, v178 dst_sel:DWORD dst_unused:UNUSED_PAD src0_sel:DWORD src1_sel:WORD_1\n"
    "v_mfma_f32_16x16x32_bf16 v[168:171], a[64:67], a[232:235], v[168:171]\n"
    "s_waitcnt lgkmcnt(8)\n"
    "ds_read_u16_d16_hi v200, v200\n"
    "v_or_b32_e32 v197, v197, v205\n"
    "v_mfma_f32_16x16x32_bf16 v[172:175], a[108:111], a[224:227], 0\n"
    "v_lshlrev_b32_sdwa v205, 1, v179 dst_sel:DWORD dst_unused:UNUSED_PAD src0_sel:DWORD src1_sel:WORD_1\n"
    "v_lshlrev_b32_sdwa v190, 1, v180 dst_sel:DWORD dst_unused:UNUSED_PAD src0_sel:DWORD src1_sel:WORD_1\n"
    "v_mfma_f32_16x16x32_bf16 v[172:175], a[156:159], a[228:231], v[172:175]\n"
    "s_waitcnt lgkmcnt(7)\n"
    "ds_read_u16 v185, v185\n"
    "v_lshlrev_b32_sdwa v191, 1, v182 dst_sel:DWORD dst_unused:UNUSED_PAD src0_sel:DWORD src1_sel:WORD_1\n"
    "v_mfma_f32_16x16x32_bf16 v[172:175], a[68:71], a[232:235], v[172:175]\n"
    "s_waitcnt lgkmcnt(7)\n"
    "v_smfmac_f32_16x16x64_bf16 v[88:91], v[216:219], v[208:215], v240\n"
    "ds_read_u16_d16_hi v201, v201\n"
    "v_smfmac_f32_16x16x64_bf16 v[92:95], v[220:223], v[208:215], v240 abid:1\n"
    "ds_read_u16 v186, v186\n"
    "v_mfma_f32_16x16x32_bf16 v[176:179], a[104:107], a[236:239], 0\n"
    "s_nop 0\n"
    "v_mfma_f32_16x16x32_bf16 v[176:179], a[152:155], a[240:243], v[176:179]\n"
    "s_waitcnt lgkmcnt(7)\n"
    "ds_read_u16_d16_hi v202, v202\n"
    "v_or_b32_e64 v198, v198, v206\n"
    "v_lshlrev_b32_sdwa v206, 1, v181 dst_sel:DWORD dst_unused:UNUSED_PAD src0_sel:DWORD src1_sel:WORD_1\n"
    "v_mfma_f32_16x16x32_bf16 v[176:179], a[64:67], a[244:247], v[176:179]\n"
    "v_pk_add_f32 v[224:225], v[224:225], v[88:89]\n"
    "v_pk_add_f32 v[226:227], v[226:227], v[90:91]\n"
    "v_pk_fma_f32 v[228:229], v[88:89], v[88:89], v[228:229]\n"
    "v_pk_fma_f32 v[230:231], v[90:91], v[90:91], v[230:231]\n"
    "v_pk_add_f32 v[224:225], v[224:225], v[92:93]\n"
    "v_pk_add_f32 v[226:227], v[226:227], v[94:95]\n"
    "v_pk_fma_f32 v[228:229], v[92:93], v[92:93], v[228:229]\n"
    "v_pk_fma_f32 v[230:231], v[94:95], v[94:95], v[230:231]\n"
    "v_mfma_f32_16x16x32_bf16 v[96:99], a[16:19], v[192:195], v[96:99]\n"
    "s_waitcnt lgkmcnt(6)\n"
    "ds_read_b128 v[236:239], v244 offset:4096\n"
    "ds_write_b128 v254, v[224:227]\n"
    "v_or_b32_e32 v199, v199, v207\n"
    "v_lshlrev_b32_sdwa v207, 1, v183 dst_sel:DWORD dst_unused:UNUSED_PAD src0_sel:DWORD src1_sel:WORD_1\n"
    "v_mfma_f32_16x16x32_bf16 v[180:183], a[108:111], a[236:239], 0\n"
    "s_waitcnt lgkmcnt(6)\n"
    "v_or_b32_e32 v184, v184, v200\n"
    "v_mfma_f32_16x16x32_bf16 v[180:183], a[156:159], a[240:243], v[180:183]\n"
    "v_mfma_f32_16x16x32_bf16 v[180:183], a[68:71], a[244:247], v[180:183]\n"
    "ds_read_u16 v187, v187\n"
    "ds_write_b128 v254, v[228:231] offset:1024\n"
    "v_mfma_f32_16x16x32_bf16 v[100:103], a[20:23], v[192:195], v[100:103]\n"
    "s_waitcnt lgkmcnt(6)\n"
    "v_lshrrev_b32_e32 v254, 2, v242\n"
    "v_or_b32_e64 v185, v185, v201\n"
    "v_mfma_f32_16x16x32_bf16 v[112:115], a[24:27], v[192:195], v[112:115]\n"
    "v_add_u32_e64 v254, s55, v254\n"
    "v_lshlrev_b32_sdwa v201, 1, v171 dst_sel:DWORD dst_unused:UNUSED_PAD src0_sel:DWORD src1_sel:WORD_1\n"
    "v_mfma_f32_16x16x32_bf16 v[116:119], a[28:31], v[192:195], v[116:119]\n"
    "ds_read_u16_d16_hi v203, v203\n"
    "ds_read_b128 v[164:167], v244 offset:6144\n"
    "v_mfma_f32_16x16x32_bf16 v[104:107], a[16:19], v[196:199], v[104:107]\n"
    "s_waitcnt lgkmcnt(5)\n"
    "ds_read_u16 v188, v188\n"
    "ds_read_u16_d16_hi v204, v204\n"
    "v_mfma_f32_16x16x32_bf16 v[128:131], v[236:239], v[192:195], v[128:131]\n"
    "v_or_b32_e32 v186, v186, v202\n"
    "v_lshlrev_b32_sdwa v202, 1, v173 dst_sel:DWORD dst_unused:UNUSED_PAD src0_sel:DWORD src1_sel:WORD_1\n"
    "v_mfma_f32_16x16x32_bf16 v[108:111], a[20:23], v[196:199], v[108:111]\n"
    "ds_read_u16 v189, v189\n"
    "v_mfma_f32_16x16x32_bf16 v[120:123], a[24:27], v[196:199], v[120:123]\n"
    "ds_read_u16_d16_hi v205, v205\n"
    "v_mfma_f32_16x16x32_bf16 v[124:127], a[28:31], v[196:199], v[124:127]\n"
    "s_waitcnt vmcnt(3)\n"
    "v_mfma_f32_16x16x32_bf16 v[136:139], v[236:239], v[196:199], v[136:139]\n"
    "s_waitcnt vmcnt(2) lgkmcnt(6)\n"
    "s_barrier\n"
    "v_smfmac_f32_32x32x32_bf16 v[96:111], a[248:251], v[144:151], v241\n"
    "s_waitcnt lgkmcnt(4)\n"
    "ds_read_b32 v224, v254 offset:0\n"
    "ds_read_b32 v225, v254 offset:6752\n"
    "v_or_b32_e64 v187, v187, v203\n"
    "v_lshlrev_b32_sdwa v203, 1, v175 dst_sel:DWORD dst_unused:UNUSED_PAD src0_sel:DWORD src1_sel:WORD_1\n"
    "ds_read_b32 v226, v254 offset:13504\n"
    "ds_read_b32 v227, v254 offset:20256\n"
    "v_mfma_f32_16x16x32_bf16 v[132:135], v[164:167], v[192:195], v[132:135]\n"
    "ds_read_b32 v228, v254 offset:1024\n"
    "ds_read_b32 v229, v254 offset:7776\n"
    "v_mfma_f32_16x16x32_bf16 v[140:143], v[164:167], v[196:199], v[140:143]\n"
    "s_nop 0\n"
    "ds_read_b32 v230, v254 offset:14528\n"
    "ds_read_b32 v231, v254 offset:21280\n"
    "v_smfmac_f32_32x32x32_bf16 v[112:127], a[248:251], v[152:159], v241\n"
    "s_waitcnt lgkmcnt(10)\n"
    "ds_read_u16 v190, v190\n"
    "ds_read_u16_d16_hi v206, v206\n"
    "v_cvt_pk_f16_f32 v192, v168, v169\n"
    "v_lshlrev_b32_sdwa v193, 1, v170 dst_sel:DWORD dst_unused:UNUSED_PAD src0_sel:DWORD src1_sel:WORD_1\n"
    "v_lshlrev_b32_sdwa v194, 1, v172 dst_sel:DWORD dst_unused:UNUSED_PAD src0_sel:DWORD src1_sel:WORD_1\n"
    "v_mfma_f32_16x16x32_bf16 v[168:171], a[112:115], a[224:227], 0\n"
    "ds_read_u16 v191, v191\n"
    "ds_read_u16_d16_hi v207, v207\n"
    "v_mfma_f32_16x16x32_bf16 v[168:171], a[160:163], a[228:231], v[168:171]\n"
    "buffer_load_dwordx4 v[144:147], v245, s[40:43], s58 offen offset:128\n"
    "ds_read_u16 v193, v193\n"
    "v_mfma_f32_16x16x32_bf16 v[168:171], a[72:75], a[232:235], v[168:171]\n"
    "s_waitcnt lgkmcnt(10)\n"
    "v_add_f32_e32 v224, v224, v225\n"
    "v_lshlrev_b32_sdwa v195, 1, v174 dst_sel:DWORD dst_unused:UNUSED_PAD src0_sel:DWORD src1_sel:WORD_1\n"
    "v_mfma_f32_16x16x32_bf16 v[172:175], a[116:119], a[224:227], 0\n"
    "s_waitcnt lgkmcnt(9)\n"
    "ds_read_b128 a[252:255], v244 offset:34816\n"
    "v_add_f32_e32 v226, v226, v227\n"
    "v_mfma_f32_16x16x32_bf16 v[172:175], a[164:167], a[228:231], v[172:175]\n"
    "s_waitcnt lgkmcnt(8)\n"
    "ds_read_u16_d16_hi v201, v201\n"
    "v_add_f32_e32 v228, v228, v229\n"
    "v_add_f32_e64 v224, v224, v226\n"
    "v_cvt_pk_f16_f32 v196, v176, v177\n"
    "s_waitcnt lgkmcnt(7)\n"
    "v_add_f32_e32 v230, v230, v231\n"
    "v_mul_f32_e32 v224, 0x3c2aaaab, v224\n"
    "v_add_f32_e64 v228, v228, v230\n"
    "v_lshlrev_b32_sdwa v197, 1, v178 dst_sel:DWORD dst_unused:UNUSED_PAD src0_sel:DWORD src1_sel:WORD_1\n"
    "v_mul_f32_e32 v228, 0x3c2aaaab, v228\n"
    "v_lshlrev_b32_sdwa v198, 1, v180 dst_sel:DWORD dst_unused:UNUSED_PAD src0_sel:DWORD src1_sel:WORD_1\n"
    "v_fma_f32 v228, -v224, v224, v228\n"
    "v_lshlrev_b32_sdwa v199, 1, v182 dst_sel:DWORD dst_unused:UNUSED_PAD src0_sel:DWORD src1_sel:WORD_1\n"
    "v_max_f32_e64 v228, 0, v228\n"
    "v_or_b32_e64 v188, v188, v204\n"
    "v_add_f32_e32 v228, 0x3727c5ac, v228\n"
    "v_or_b32_e64 v189, v189, v205\n"
    "v_rsq_f32_e64 v228, v228\n"
    "v_lshlrev_b32_sdwa v205, 1, v179 dst_sel:DWORD dst_unused:UNUSED_PAD src0_sel:DWORD src1_sel:WORD_1\n"
    "v_mfma_f32_16x16x32_bf16 v[176:179], a[112:115], a[236:239], 0\n"
    "s_nop 0\n"
    "ds_read_u16 v194, v194\n"
    "ds_read_u16_d16_hi v202, v202\n"
    "v_mfma_f32_16x16x32_bf16 v[176:179], a[160:163], a[240:243], v[176:179]\n"
    "s_waitcnt lgkmcnt(7)\n"
    "ds_read_u16 v195, v195\n"
    "buffer_load_dwordx4 v[148:151], v245, s[40:43], s89 offen offset:128\n"
    "v_mfma_f32_16x16x32_bf16 v[176:179], a[72:75], a[244:247], v[176:179]\n"
    "ds_read_b128 v[236:239], v244 offset:8192\n"
    "v_mul_f32_e64 v228, s50, v228\n"
    "v_or_b32_e64 v190, v190, v206\n"
    "v_mfma_f32_16x16x32_bf16 v[112:115], a[40:43], v[184:187], v[112:115]\n"
    "v_mul_f32_e64 v224, v228, -v224\n"
    "v_lshlrev_b32_sdwa v206, 1, v181 dst_sel:DWORD dst_unused:UNUSED_PAD src0_sel:DWORD src1_sel:WORD_1\n"
    "v_pk_fma_f16 v232, v192, v192, s48\n"
    "ds_write_b32 v254, v228\n"
    "ds_write_b32 v254, v224 offset:1024\n"
    "s_waitcnt lgkmcnt(7)\n"
    "v_mfma_f32_16x16x32_bf16 v[172:175], a[252:255], a[232:235], v[172:175]\n"
    "s_nop 0\n"
    "ds_read_b128 v[152:155], v242 offset:0\n"
    "ds_read_b128 v[156:159], v242 offset:1024\n"
    "v_add_u32_e64 v254, s56, v242\n"
    "v_or_b32_e64 v191, v191, v207\n"
    "v_mfma_f32_16x16x32_bf16 v[116:119], a[44:47], v[184:187], v[116:119]\n"
    "ds_read_u16_d16_hi v203, v203\n"
    "ds_read_b128 v[164:167], v244 offset:10240\n"
    "v_mfma_f32_16x16x32_bf16 v[120:123], a[40:43], v[188:191], v[120:123]\n"
    "s_waitcnt lgkmcnt(10)\n"
    "ds_read_u16 v197, v197\n"
    "v_lshlrev_b32_sdwa v207, 1, v183 dst_sel:DWORD dst_unused:UNUSED_PAD src0_sel:DWORD src1_sel:WORD_1\n"
    "v_or_b32_e32 v193, v193, v201\n"
    "v_mfma_f32_16x16x32_bf16 v[180:183], a[116:119], a[236:239], 0\n"
    "ds_read_u16_d16_hi v205, v205\n"
    "v_pk_mul_f16 v232, v192, v232\n"
    "v_mfma_f32_16x16x32_bf16 v[180:183], a[164:167], a[240:243], v[180:183]\n"
    "v_exp_f16_e32 v234, v232\n"
    "v_mfma_f32_16x16x32_bf16 v[180:183], a[252:255], a[244:247], v[180:183]\n"
    "s_waitcnt lgkmcnt(10)\n"
    "ds_read_u16 v198, v198\n"
    "v_or_b32_e32 v194, v194, v202\n"
    "v_exp_f16_sdwa v234, v232 dst_sel:WORD_1 dst_unused:UNUSED_PRESERVE src0_sel:WORD_1\n"
    "v_mfma_f32_16x16x32_bf16 v[124:127], a[44:47], v[188:191], v[124:127]\n"
    "s_waitcnt lgkmcnt(8)\n"
    "ds_read_u16_d16_hi v206, v206\n"
    "v_pk_fma_f16 v234, v234, s49, s49\n"
    "v_lshlrev_b32_sdwa v200, 1, v169 dst_sel:DWORD dst_unused:UNUSED_PAD src0_sel:DWORD src1_sel:WORD_1\n"
    "v_lshlrev_b32_sdwa v201, 1, v171 dst_sel:DWORD dst_unused:UNUSED_PAD src0_sel:DWORD src1_sel:WORD_1\n"
    "v_lshlrev_b32_sdwa v202, 1, v173 dst_sel:DWORD dst_unused:UNUSED_PAD src0_sel:DWORD src1_sel:WORD_1\n"
    "v_mfma_f32_16x16x32_bf16 v[128:131], v[236:239], v[184:187], v[128:131]\n"
    "s_waitcnt lgkmcnt(8)\n"
    "s_barrier\n"
    "v_mfma_f32_16x16x32_bf16 v[136:139], v[236:239], v[188:191], v[136:139]\n"
    "s_waitcnt lgkmcnt(6)\n"
    "ds_read_b128 v[224:227], v254\n"
    "ds_read_b128 v[228:231], v254 offset:1024\n"
    "v_smfmac_f32_32x32x32_bf16 v[96:111], a[248:251], v[152:159], v241\n"
    "s_waitcnt lgkmcnt(6)\n"
    "ds_read_u16 v199, v199\n"
    "ds_read_u16_d16_hi v207, v207\n"
    "v_lshlrev_b32_e64 v254, 3, v244\n"
    "v_or_b32_e64 v195, v195, v203\n"
    "v_lshlrev_b32_sdwa v203, 1, v175 dst_sel:DWORD dst_unused:UNUSED_PAD src0_sel:DWORD src1_sel:WORD_1\n"
    "v_mfma_f32_16x16x32_bf16 v[132:135], v[164:167], v[184:187], v[132:135]\n"
    "s_mov_b32 exec_hi, 0\n"
    "buffer_load_dword v233, v254, s[72:75], s53 offen sc1\n"
    "s_mov_b32 exec_hi, -1\n"
    "s_add_u32 s53, s53, 393216\n"
    "v_mfma_f32_16x16x32_bf16 v[96:99], a[32:35], v[184:187], v[96:99]\n"
    "ds_read_b128 v[152:155], v242 offset:2048\n"
    "ds_read_b128 v[156:159], v242 offset:3072\n"
    "v_mfma_f32_16x16x32_bf16 v[100:103], a[36:39], v[184:187], v[100:103]\n"
    "s_waitcnt lgkmcnt(8)\n"
    "ds_read_b128 a[252:255], v244 offset:38912\n"
    "ds_read_u16_d16_hi v200, v200\n"
    "v_mfma_f32_16x16x32_bf16 v[104:107], a[32:35], v[188:191], v[104:107]\n"
    "v_or_b32_e32 v197, v197, v205\n"
    "v_lshlrev_b32_e64 v254, 3, v244\n"
    "v_mfma_f32_16x16x32_bf16 v[108:111], a[36:39], v[188:191], v[108:111]\n"
    "ds_read_u16_d16_hi v201, v201\n"
    "v_lshlrev_b32_sdwa v184, 1, v168 dst_sel:DWORD dst_unused:UNUSED_PAD src0_sel:DWORD src1_sel:WORD_1\n"
    "v_mfma_f32_16x16x32_bf16 v[140:143], v[164:167], v[188:191], v[140:143]\n"
    "ds_read_u16 v184, v184\n"
    "v_lshlrev_b32_sdwa v185, 1, v170 dst_sel:DWORD dst_unused:UNUSED_PAD src0_sel:DWORD src1_sel:WORD_1\n"
    "v_mfma_f32_16x16x32_bf16 v[168:171], a[120:123], a[224:227], 0\n"
    "s_waitcnt lgkmcnt(8)\n"
    "v_lshlrev_b32_sdwa v186, 1, v172 dst_sel:DWORD dst_unused:UNUSED_PAD src0_sel:DWORD src1_sel:WORD_1\n"
    "v_or_b32_e32 v198, v198, v206\n"
    "v_lshlrev_b32_sdwa v187, 1, v174 dst_sel:DWORD dst_unused:UNUSED_PAD src0_sel:DWORD src1_sel:WORD_1\n"
    "v_rcp_f16_e64 v232, v234\n"
    "v_pk_fma_f32 v[64:65], v[64:65], v[224:225], v[228:229]\n"
    "v_pk_fma_f32 v[66:67], v[66:67], v[226:227], v[230:231]\n"
    "v_pk_fma_f32 v[68:69], v[68:69], v[224:225], v[228:229]\n"
    "v_pk_fma_f32 v[70:71], v[70:71], v[226:227], v[230:231]\n"
    "v_pk_fma_f32 v[72:73], v[72:73], v[224:225], v[228:229]\n"
    "v_pk_fma_f32 v[74:75], v[74:75], v[226:227], v[230:231]\n"
    "v_pk_fma_f32 v[76:77], v[76:77], v[224:225], v[228:229]\n"
    "v_pk_fma_f32 v[78:79], v[78:79], v[226:227], v[230:231]\n"
    "v_pk_fma_f32 v[80:81], v[80:81], v[224:225], v[228:229]\n"
    "v_pk_fma_f32 v[82:83], v[82:83], v[226:227], v[230:231]\n"
    "v_pk_fma_f32 v[84:85], v[84:85], v[224:225], v[228:229]\n"
    "v_pk_fma_f32 v[86:87], v[86:87], v[226:227], v[230:231]\n"
    "v_pk_fma_f32 v[88:89], v[88:89], v[224:225], v[228:229]\n"
    "v_pk_fma_f32 v[90:91], v[90:91], v[226:227], v[230:231]\n"
    "v_pk_fma_f32 v[92:93], v[92:93], v[224:225], v[228:229]\n"
    "v_pk_fma_f32 v[94:95], v[94:95], v[226:227], v[230:231]\n"
    "v_pk_fma_f32 v[0:1], v[0:1], v[224:225], v[228:229]\n"
    "v_pk_fma_f32 v[2:3], v[2:3], v[226:227], v[230:231]\n"
    "v_pk_fma_f32 v[4:5], v[4:5], v[224:225], v[228:229]\n"
    "v_pk_fma_f32 v[6:7], v[6:7], v[226:227], v[230:231]\n"
    "v_pk_fma_f32 v[8:9], v[8:9], v[224:225], v[228:229]\n"
    "v_pk_fma_f32 v[10:11], v[10:11], v[226:227], v[230:231]\n"
    "v_pk_fma_f32 v[12:13], v[12:13], v[224:225], v[228:229]\n"
    "v_pk_fma_f32 v[14:15], v[14:15], v[226:227], v[230:231]\n"
    "v_pk_fma_f32 v[16:17], v[16:17], v[224:225], v[228:229]\n"
    "v_pk_fma_f32 v[18:19], v[18:19], v[226:227], v[230:231]\n"
    "v_pk_fma_f32 v[20:21], v[20:21], v[224:225], v[228:229]\n"
    "v_pk_fma_f32 v[22:23], v[22:23], v[226:227], v[230:231]\n"
    "v_pk_fma_f32 v[24:25], v[24:25], v[224:225], v[228:229]\n"
    "v_pk_fma_f32 v[26:27], v[26:27], v[226:227], v[230:231]\n"
    "v_pk_fma_f32 v[28:29], v[28:29], v[224:225], v[228:229]\n"
    "v_pk_fma_f32 v[30:31], v[30:31], v[226:227], v[230:231]\n"
    "v_pk_fma_f32 v[32:33], v[32:33], v[224:225], v[228:229]\n"
    "v_pk_fma_f32 v[34:35], v[34:35], v[226:227], v[230:231]\n"
    "v_pk_fma_f32 v[36:37], v[36:37], v[224:225], v[228:229]\n"
    "v_pk_fma_f32 v[38:39], v[38:39], v[226:227], v[230:231]\n"
    "v_pk_fma_f32 v[40:41], v[40:41], v[224:225], v[228:229]\n"
    "v_pk_fma_f32 v[42:43], v[42:43], v[226:227], v[230:231]\n"
    "v_pk_fma_f32 v[44:45], v[44:45], v[224:225], v[228:229]\n"
    "v_pk_fma_f32 v[46:47], v[46:47], v[226:227], v[230:231]\n"
    "v_pk_fma_f32 v[48:49], v[48:49], v[224:225], v[228:229]\n"
    "v_pk_fma_f32 v[50:51], v[50:51], v[226:227], v[230:231]\n"
    "v_pk_fma_f32 v[52:53], v[52:53], v[224:225], v[228:229]\n"
    "v_pk_fma_f32 v[54:55], v[54:55], v[226:227], v[230:231]\n"
    "v_pk_fma_f32 v[56:57], v[56:57], v[224:225], v[228:229]\n"
    "v_pk_fma_f32 v[58:59], v[58:59], v[226:227], v[230:231]\n"
    "v_pk_fma_f32 v[60:61], v[60:61], v[224:225], v[228:229]\n"
    "v_pk_fma_f32 v[62:63], v[62:63], v[226:227], v[230:231]\n"
    "v_mfma_f32_16x16x32_bf16 v[168:171], a[168:171], a[228:231], v[168:171]\n"
    "s_waitcnt lgkmcnt(0)\n"
    "ds_read_u16 v185, v185\n"
    "ds_read_u16 v186, v186\n"
    "v_mfma_f32_16x16x32_bf16 v[168:171], a[76:79], a[232:235], v[168:171]\n"
    "s_mov_b32 exec_hi, 0\n"
    "buffer_load_dword v233, v254, s[80:83], s85 offen sc1\n"
    "s_mov_b32 exec_hi, -1\n"
    "v_smfmac_f32_32x32x32_bf16 v[112:127], a[248:251], v[152:159], v241\n"
    "s_nop 0\n"
    "ds_read_u16_d16_hi v202, v202\n"
    "ds_read_b128 v[236:239], v244 offset:12288\n"
    "v_cvt_pk_bf16_f32 v208, v64, v68\n"
    "v_cvt_pk_bf16_f32 v212, v65, v69\n"
    "v_cvt_pk_bf16_f32 v216, v66, v70\n"
    "v_cvt_pk_bf16_f32 v220, v67, v71\n"
    "v_cvt_pk_bf16_f32 v209, v72, v76\n"
    "v_cvt_pk_bf16_f32 v213, v73, v77\n"
    "v_cvt_pk_bf16_f32 v217, v74, v78\n"
    "v_cvt_pk_bf16_f32 v221, v75, v79\n"
    "v_cvt_pk_bf16_f32 v210, v80, v84\n"
    "v_cvt_pk_bf16_f32 v214, v81, v85\n"
    "v_cvt_pk_bf16_f32 v218, v82, v86\n"
    "v_cvt_pk_bf16_f32 v222, v83, v87\n"
    "v_cvt_pk_bf16_f32 v211, v88, v92\n"
    "ds_write_b128 v253, v[208:211] offset:26688\n"
    "v_cvt_pk_bf16_f32 v215, v89, v93\n"
    "v_cvt_pk_bf16_f32 v219, v90, v94\n"
    "v_cvt_pk_bf16_f32 v223, v91, v95\n"
    "v_cvt_pk_bf16_f32 v80, v0, v4\n"
    "v_cvt_pk_bf16_f32 v84, v1, v5\n"
    "v_cvt_pk_bf16_f32 v88, v2, v6\n"
    "ds_write_b128 v253, v[212:215] offset:29760\n"
    "v_cvt_pk_bf16_f32 v92, v3, v7\n"
    "v_cvt_pk_bf16_f32 v81, v8, v12\n"
    "v_cvt_pk_bf16_f32 v85, v9, v13\n"
    "v_cvt_pk_bf16_f32 v89, v10, v14\n"
    "v_cvt_pk_bf16_f32 v93, v11, v15\n"
    "v_cvt_pk_bf16_f32 v82, v16, v20\n"
    "v_cvt_pk_bf16_f32 v86, v17, v21\n"
    "v_cvt_pk_bf16_f32 v90, v18, v22\n"
    "v_cvt_pk_bf16_f32 v94, v19, v23\n"
    "v_cvt_pk_bf16_f32 v83, v24, v28\n"
    "v_cvt_pk_bf16_f32 v87, v25, v29\n"
    "v_cvt_pk_bf16_f32 v91, v26, v30\n"
    "v_cvt_pk_bf16_f32 v95, v27, v31\n"
    "v_rcp_f16_sdwa v232, v234 dst_sel:WORD_1 dst_unused:UNUSED_PRESERVE src0_sel:WORD_1\n"
    "v_mfma_f32_16x16x32_bf16 v[172:175], a[124:127], a[224:227], 0\n"
    "ds_read_u16 v187, v187\n"
    "v_lshlrev_b32_sdwa v188, 1, v176 dst_sel:DWORD dst_unused:UNUSED_PAD src0_sel:DWORD src1_sel:WORD_1\n"
    "v_mfma_f32_16x16x32_bf16 v[172:175], a[172:175], a[228:231], v[172:175]\n"
    "ds_read_u16_d16_hi v203, v203\n"
    "ds_write_b128 v253, v[80:83] offset:24640\n"
    "v_lshlrev_b32_sdwa v204, 1, v177 dst_sel:DWORD dst_unused:UNUSED_PAD src0_sel:DWORD src1_sel:WORD_1\n"
    "v_lshlrev_b32_sdwa v189, 1, v178 dst_sel:DWORD dst_unused:UNUSED_PAD src0_sel:DWORD src1_sel:WORD_1\n"
    "v_lshlrev_b32_sdwa v205, 1, v179 dst_sel:DWORD dst_unused:UNUSED_PAD src0_sel:DWORD src1_sel:WORD_1\n"
    "v_lshlrev_b32_sdwa v190, 1, v180 dst_sel:DWORD dst_unused:UNUSED_PAD src0_sel:DWORD src1_sel:WORD_1\n"
    "v_lshlrev_b32_sdwa v206, 1, v181 dst_sel:DWORD dst_unused:UNUSED_PAD src0_sel:DWORD src1_sel:WORD_1\n"
    "v_lshlrev_b32_sdwa v191, 1, v182 dst_sel:DWORD dst_unused:UNUSED_PAD src0_sel:DWORD src1_sel:WORD_1\n"
    "v_fma_mix_f32 v234, v192, v232, 0 op_sel_hi:[1,1,0]\n"
    "v_mfma_f32_16x16x32_bf16 v[172:175], a[252:255], a[232:235], v[172:175]\n"
    "ds_read_b128 v[164:167], v244 offset:14336\n"
    "ds_write_b128 v253, v[84:87] offset:27712\n"
    "v_cvt_pk_bf16_f32 v64, v32, v36\n"
    "v_or_b32_e64 v199, v199, v207\n"
    "v_lshlrev_b32_sdwa v207, 1, v183 dst_sel:DWORD dst_unused:UNUSED_PAD src0_sel:DWORD src1_sel:WORD_1\n"
    "v_cvt_pk_bf16_f32 v68, v33, v37\n"
    "v_cvt_pk_bf16_f32 v72, v34, v38\n"
    "v_fma_mix_f32 v232, v192, v232, 0 op_sel:[1,1,0] op_sel_hi:[1,1,0]\n"
    "v_mfma_f32_16x16x32_bf16 v[176:179], a[120:123], a[236:239], 0\n"
    "v_cvt_pk_bf16_f32 v76, v35, v39\n"
    "v_cvt_pk_bf16_f32 v65, v40, v44\n"
    "v_cvt_pk_bf16_f32 v192, v234, v232\n"
    "v_mfma_f32_16x16x32_bf16 v[176:179], a[168:171], a[240:243], v[176:179]\n"
    "ds_read_u16 v188, v188\n"
    "ds_read_u16_d16_hi v204, v204\n"
    "v_mfma_f32_16x16x32_bf16 v[176:179], a[76:79], a[244:247], v[176:179]\n"
    "ds_read_u16 v189, v189\n"
    "ds_read_b128 v[152:155], v242 offset:4096\n"
    "v_cvt_pk_bf16_f32 v69, v41, v45\n"
    "v_cvt_pk_bf16_f32 v73, v42, v46\n"
    "v_pk_fma_f16 v232, v196, v196, s48\n"
    "v_mfma_f32_16x16x32_bf16 v[180:183], a[124:127], a[236:239], 0\n"
    "s_nop 0\n"
    "s_waitcnt lgkmcnt(6)\n"
    "ds_read_b128 v[156:159], v242 offset:5120\n"
    "v_cvt_pk_bf16_f32 v77, v43, v47\n"
    "v_cvt_pk_bf16_f32 v66, v48, v52\n"
    "v_cvt_pk_bf16_f32 v70, v49, v53\n"
    "v_pk_mul_f16 v232, v196, v232\n"
    "v_mfma_f32_16x16x32_bf16 v[180:183], a[172:175], a[240:243], v[180:183]\n"
    "ds_read_u16_d16_hi v205, v205\n"
    "v_cvt_pk_bf16_f32 v74, v50, v54\n"
    "v_cvt_pk_bf16_f32 v78, v51, v55\n"
    "v_exp_f16_e64 v234, v232\n"
    "v_mfma_f32_16x16x32_bf16 v[180:183], a[252:255], a[244:247], v[180:183]\n"
    "ds_read_u16 v190, v190\n"
    "v_cvt_pk_bf16_f32 v67, v56, v60\n"
    "v_cvt_pk_bf16_f32 v71, v57, v61\n"
    "v_exp_f16_sdwa v234, v232 dst_sel:WORD_1 dst_unused:UNUSED_PRESERVE src0_sel:WORD_1\n"
    "v_cvt_pk_bf16_f32 v75, v58, v62\n"
    "v_mfma_f32_16x16x32_bf16 v[96:99], a[176:179], v[192:195], v[96:99]\n"
    "ds_write_b128 v253, v[64:67] offset:25664\n"
    "v_cvt_pk_bf16_f32 v79, v59, v63\n"
    "v_pk_fma_f16 v234, v234, s49, s49\n"
    "v_or_b32_e64 v184, v184, v200\n"
    "v_rcp_f16_e64 v232, v234\n"
    "v_or_b32_e32 v185, v185, v201\n"
    "v_mfma_f32_16x16x32_bf16 v[100:103], a[180:183], v[192:195], v[100:103]\n"
    "ds_write_b128 v253, v[68:71] offset:28736\n"
    "v_mfma_f32_16x16x32_bf16 v[112:115], a[184:187], v[192:195], v[112:115]\n"
    "v_rcp_f16_sdwa v232, v234 dst_sel:WORD_1 dst_unused:UNUSED_PRESERVE src0_sel:WORD_1\n"
    "v_mfma_f32_16x16x32_bf16 v[116:119], a[188:191], v[192:195], v[116:119]\n"
    "s_waitcnt lgkmcnt(4)\n"
    "v_fma_mix_f32 v234, v196, v232, 0 op_sel_hi:[1,1,0]\n"
    "v_fma_mix_f32 v232, v196, v232, 0 op_sel:[1,1,0] op_sel_hi:[1,1,0]\n"
    "v_mfma_f32_16x16x32_bf16 v[128:131], v[236:239], v[192:195], v[128:131]\n"
    "ds_read_u16_d16_hi v206, v206\n"
    "ds_read_u16 v191, v191\n"
    "v_mfma_f32_16x16x32_bf16 v[132:135], v[164:167], v[192:195], v[132:135]\n"
    "ds_read_u16_d16_hi v207, v207\n"
    "ds_read_b128 a[252:255], v244 offset:32768\n"
    "v_cvt_pk_bf16_f32 v196, v234, v232\n"
    "v_cvt_pk_f16_f32 v192, v168, v169\n"
    "v_lshlrev_b32_sdwa v193, 1, v170 dst_sel:DWORD dst_unused:UNUSED_PAD src0_sel:DWORD src1_sel:WORD_1\n"
    "v_mfma_f32_16x16x32_bf16 v[104:107], a[176:179], v[196:199], v[104:107]\n"
    "s_waitcnt lgkmcnt(7)\n"
    "ds_read_u16 v193, v193\n"
    "v_lshlrev_b32_sdwa v201, 1, v171 dst_sel:DWORD dst_unused:UNUSED_PAD src0_sel:DWORD src1_sel:WORD_1\n"
    "v_mfma_f32_16x16x32_bf16 v[108:111], a[180:183], v[196:199], v[108:111]\n"
    "s_nop 0\n"
    "v_lshlrev_b32_sdwa v194, 1, v172 dst_sel:DWORD dst_unused:UNUSED_PAD src0_sel:DWORD src1_sel:WORD_1\n"
    "v_or_b32_e64 v186, v186, v202\n"
    "v_mfma_f32_16x16x32_bf16 v[120:123], a[184:187], v[196:199], v[120:123]\n"
    "ds_read_u16_d16_hi v201, v201\n"
    "v_lshlrev_b32_sdwa v202, 1, v173 dst_sel:DWORD dst_unused:UNUSED_PAD src0_sel:DWORD src1_sel:WORD_1\n"
    "v_mfma_f32_16x16x32_bf16 v[124:127], a[188:191], v[196:199], v[124:127]\n"
    "s_waitcnt lgkmcnt(6)\n"
    "s_barrier\n"
    "v_mfma_f32_16x16x32_bf16 v[136:139], v[236:239], v[196:199], v[136:139]\n"
    "s_nop 0\n"
    "ds_read_b128 a[224:227], v242 offset:24640\n"
    "ds_read_b128 a[228:231], v242 offset:25664\n"
    "v_mfma_f32_16x16x32_bf16 v[140:143], v[164:167], v[196:199], v[140:143]\n"
    "s_waitcnt lgkmcnt(7)\n"
    "ds_read_b128 a[232:235], v242 offset:26688\n"
    "ds_read_b128 a[236:239], v242 offset:27712\n"
    "v_lshlrev_b32_sdwa v195, 1, v174 dst_sel:DWORD dst_unused:UNUSED_PAD src0_sel:DWORD src1_sel:WORD_1\n"
    "v_cvt_pk_f16_f32 v196, v176, v177\n"
    "v_lshlrev_b32_sdwa v197, 1, v178 dst_sel:DWORD dst_unused:UNUSED_PAD src0_sel:DWORD src1_sel:WORD_1\n"
    "v_smfmac_f32_32x32x32_bf16 v[128:143], a[248:251], v[152:159], v241\n"
    "ds_read_b128 a[240:243], v242 offset:28736\n"
    "ds_read_b128 a[244:247], v242 offset:29760\n"
    "v_lshlrev_b32_sdwa v198, 1, v180 dst_sel:DWORD dst_unused:UNUSED_PAD src0_sel:DWORD src1_sel:WORD_1\n"
    "v_lshlrev_b32_sdwa v199, 1, v182 dst_sel:DWORD dst_unused:UNUSED_PAD src0_sel:DWORD src1_sel:WORD_1\n"
    "v_or_b32_e64 v187, v187, v203\n"
    "v_lshlrev_b32_sdwa v203, 1, v175 dst_sel:DWORD dst_unused:UNUSED_PAD src0_sel:DWORD src1_sel:WORD_1\n"
    "v_or_b32_e64 v188, v188, v204\n"
    "v_or_b32_e64 v189, v189, v205\n"
    "v_lshlrev_b32_sdwa v205, 1, v179 dst_sel:DWORD dst_unused:UNUSED_PAD src0_sel:DWORD src1_sel:WORD_1\n"
    "v_or_b32_e32 v190, v190, v206\n"
    "v_mfma_f32_16x16x32_bf16 v[96:99], a[192:195], v[184:187], v[96:99]\n"
    "s_waitcnt lgkmcnt(8)\n"
    "ds_read_u16 v194, v194\n"
    "ds_read_u16_d16_hi v202, v202\n"
    "v_mfma_f32_16x16x32_bf16 v[100:103], a[196:199], v[184:187], v[100:103]\n"
    "ds_read_u16 v195, v195\n"
    "v_lshlrev_b32_sdwa v206, 1, v181 dst_sel:DWORD dst_unused:UNUSED_PAD src0_sel:DWORD src1_sel:WORD_1\n"
    "v_mfma_f32_16x16x32_bf16 v[112:115], a[200:203], v[184:187], v[112:115]\n"
    "ds_read_b128 v[236:239], v244 offset:16384\n"
    "v_or_b32_e32 v191, v191, v207\n"
    "v_mfma_f32_16x16x32_bf16 v[116:119], a[204:207], v[184:187], v[116:119]\n"
    "s_waitcnt lgkmcnt(9)\n"
    "ds_read_u16_d16_hi v203, v203\n"
    "v_lshlrev_b32_sdwa v207, 1, v183 dst_sel:DWORD dst_unused:UNUSED_PAD src0_sel:DWORD src1_sel:WORD_1\n"
    "v_mfma_f32_16x16x32_bf16 v[168:171], a[80:83], a[224:227], 0\n"
    "s_waitcnt lgkmcnt(9)\n"
    "v_or_b32_e32 v193, v193, v201\n"
    "v_pk_fma_f16 v232, v192, v192, s48\n"
    "v_mfma_f32_16x16x32_bf16 v[168:171], a[128:131], a[228:231], v[168:171]\n"
    "s_waitcnt lgkmcnt(8)\n"
    "ds_read_b128 v[164:167], v244 offset:18432\n"
    "ds_read_u16 v197, v197\n"
    "v_mfma_f32_16x16x32_bf16 v[168:171], a[48:51], a[232:235], v[168:171]\n"
    "s_waitcnt lgkmcnt(9)\n"
    "v_pk_mul_f16 v232, v192, v232\n"
    "v_exp_f16_e32 v234, v232\n"
    "v_mfma_f32_16x16x32_bf16 v[172:175], a[84:87], a[224:227], 0\n"
    "s_waitcnt lgkmcnt(7)\n"
    "ds_read_u16_d16_hi v205, v205\n"
    "v_mfma_f32_16x16x32_bf16 v[172:175], a[132:135], a[228:231], v[172:175]\n"
    "v_exp_f16_sdwa v234, v232 dst_sel:WORD_1 dst_unused:UNUSED_PRESERVE src0_sel:WORD_1\n"
    "v_mfma_f32_16x16x32_bf16 v[172:175], a[252:255], a[232:235], v[172:175]\n"
    "s_barrier\n"
    "v_mfma_f32_16x16x32_bf16 v[176:179], a[80:83], a[236:239], 0\n"
    "s_waitcnt lgkmcnt(6)\n"
    "ds_read_u16 v198, v198\n"
    "v_or_b32_e32 v194, v194, v202\n"
    "v_mfma_f32_16x16x32_bf16 v[176:179], a[128:131], a[240:243], v[176:179]\n"
    "s_waitcnt lgkmcnt(5)\n"
    "ds_read_u16_d16_hi v206, v206\n"
    "v_pk_fma_f16 v234, v234, s49, s49\n"
    "v_mfma_f32_16x16x32_bf16 v[176:179], a[48:51], a[244:247], v[176:179]\n"
    "ds_write_b128 v253, v[72:75] offset:1024\n"
    "v_mfma_f32_16x16x32_bf16 v[180:183], a[84:87], a[236:239], 0\n"
    "s_waitcnt lgkmcnt(5)\n"
    "v_or_b32_e32 v195, v195, v203\n"
    "v_mfma_f32_16x16x32_bf16 v[180:183], a[132:135], a[240:243], v[180:183]\n"
    "v_rcp_f16_e64 v232, v234\n"
    "v_mfma_f32_16x16x32_bf16 v[180:183], a[252:255], a[244:247], v[180:183]\n"
    "ds_read_u16 v199, v199\n"
    "ds_write_b128 v253, v[76:79] offset:4096\n"
    "v_mfma_f32_16x16x32_bf16 v[128:131], v[236:239], v[184:187], v[128:131]\n"
    "v_rcp_f16_sdwa v232, v234 dst_sel:WORD_1 dst_unused:UNUSED_PRESERVE src0_sel:WORD_1\n"
    "v_mfma_f32_16x16x32_bf16 v[132:135], v[164:167], v[184:187], v[132:135]\n"
    "s_waitcnt lgkmcnt(5)\n"
    "v_or_b32_e32 v197, v197, v205\n"
    "v_fma_mix_f32 v234, v192, v232, 0 op_sel_hi:[1,1,0]\n"
    "v_mfma_f32_16x16x32_bf16 v[104:107], a[192:195], v[188:191], v[104:107]\n"
    "ds_read_u16_d16_hi v207, v207\n"
    "ds_write_b128 v253, v[88:91] offset:0\n"
    "v_fma_mix_f32 v232, v192, v232, 0 op_sel:[1,1,0] op_sel_hi:[1,1,0]\n"
    "v_cvt_pk_bf16_f32 v192, v234, v232\n"
    "v_pk_fma_f16 v232, v196, v196, s48\n"
    "v_pk_mul_f16 v232, v196, v232\n"
    "v_exp_f16_e64 v234, v232\n"
    "v_mfma_f32_16x16x32_bf16 v[108:111], a[196:199], v[188:191], v[108:111]\n"
    "v_exp_f16_sdwa v234, v232 dst_sel:WORD_1 dst_unused:UNUSED_PRESERVE src0_sel:WORD_1\n"
    "v_mfma_f32_16x16x32_bf16 v[120:123], a[200:203], v[188:191], v[120:123]\n"
    "ds_write_b128 v253, v[92:95] offset:3072\n"
    "v_mfma_f32_16x16x32_bf16 v[124:127], a[204:207], v[188:191], v[124:127]\n"
    "v_mfma_f32_16x16x32_bf16 v[136:139], v[236:239], v[188:191], v[136:139]\n"
    "s_mov_b32 s59, s58\n"
    "s_add_u32 s57, s57, 1572864\n"
    "s_add_u32 s58, s58, 1572864\n"
    "s_mov_b32 s70, s46\n"
    "s_add_u32 s88, s57, 98304\n"
    "s_add_u32 s89, s58, 98304\n"
    "s_add_u32 s39, s59, 98304\n"
    "s_mov_b32 s82, 0\n"
    "s_add_u32 s64, s64, 1\n"
    "s_cmp_lt_u32 s64, 2\n"
    "s_cselect_b64 s[72:73], s[72:73], s[76:77]\n"
    "s_cselect_b32 s74, s74, s78\n"
    "s_cselect_b32 s53, s53, s79\n"
    "s_cmp_lg_u32 s64, 3\n"
    "s_cbranch_scc1 1b\n"
    ".Lreplk_last:\n"
    "v_lshlrev_b32_sdwa v184, 1, v168 dst_sel:DWORD dst_unused:UNUSED_PAD src0_sel:DWORD src1_sel:WORD_1\n"
    "v_lshlrev_b32_sdwa v200, 1, v169 dst_sel:DWORD dst_unused:UNUSED_PAD src0_sel:DWORD src1_sel:WORD_1\n"
    "v_lshlrev_b32_sdwa v185, 1, v170 dst_sel:DWORD dst_unused:UNUSED_PAD src0_sel:DWORD src1_sel:WORD_1\n"
    "v_lshlrev_b32_sdwa v201, 1, v171 dst_sel:DWORD dst_unused:UNUSED_PAD src0_sel:DWORD src1_sel:WORD_1\n"
    "v_mfma_f32_16x16x32_bf16 v[140:143], v[164:167], v[188:191], v[140:143]\n"
    "s_waitcnt vmcnt(2) lgkmcnt(3)\n"
    "ds_read_u16 v184, v184\n"
    "ds_read_b128 v[56:59], v244 offset:36864\n"
    "v_mfma_f32_16x16x32_bf16 v[168:171], a[88:91], a[224:227], 0\n"
    "ds_read_u16_d16_hi v200, v200\n"
    "ds_read_u16 v185, v185\n"
    "v_mfma_f32_16x16x32_bf16 v[168:171], a[136:139], a[228:231], v[168:171]\n"
    "s_waitcnt lgkmcnt(5)\n"
    "v_lshlrev_b32_sdwa v186, 1, v172 dst_sel:DWORD dst_unused:UNUSED_PAD src0_sel:DWORD src1_sel:WORD_1\n"
    "v_lshlrev_b32_sdwa v202, 1, v173 dst_sel:DWORD dst_unused:UNUSED_PAD src0_sel:DWORD src1_sel:WORD_1\n"
    "v_mfma_f32_16x16x32_bf16 v[168:171], a[52:55], a[232:235], v[168:171]\n"
    "ds_read_u16_d16_hi v201, v201\n"
    "v_lshlrev_b32_sdwa v187, 1, v174 dst_sel:DWORD dst_unused:UNUSED_PAD src0_sel:DWORD src1_sel:WORD_1\n"
    "v_smfmac_f32_32x32x32_bf16 v[128:143], a[248:251], v[144:151], v241\n"
    "ds_read_u16 v186, v186\n"
    "v_lshlrev_b32_sdwa v203, 1, v175 dst_sel:DWORD dst_unused:UNUSED_PAD src0_sel:DWORD src1_sel:WORD_1\n"
    "v_lshlrev_b32_sdwa v188, 1, v176 dst_sel:DWORD dst_unused:UNUSED_PAD src0_sel:DWORD src1_sel:WORD_1\n"
    "v_lshlrev_b32_sdwa v204, 1, v177 dst_sel:DWORD dst_unused:UNUSED_PAD src0_sel:DWORD src1_sel:WORD_1\n"
    "v_lshlrev_b32_sdwa v189, 1, v178 dst_sel:DWORD dst_unused:UNUSED_PAD src0_sel:DWORD src1_sel:WORD_1\n"
    "v_mfma_f32_16x16x32_bf16 v[172:175], a[92:95], a[224:227], 0\n"
    "ds_read_u16_d16_hi v202, v202\n"
    "v_lshlrev_b32_sdwa v205, 1, v179 dst_sel:DWORD dst_unused:UNUSED_PAD src0_sel:DWORD src1_sel:WORD_1\n"
    "v_mfma_f32_16x16x32_bf16 v[172:175], a[140:143], a[228:231], v[172:175]\n"
    "buffer_load_dwordx4 v[152:155], v245, s[40:43], s57 offen offset:64\n"
    "buffer_load_dwordx4 v[144:147], v245, s[40:43], s57 offen offset:0\n"
    "buffer_load_dwordx4 v[208:211], v245, s[40:43], s57 offen offset:128\n"
    "ds_read_b128 v[236:239], v244 offset:20480\n"
    "v_mfma_f32_16x16x32_bf16 v[176:179], a[88:91], a[236:239], 0\n"
    "ds_read_u16 v187, v187\n"
    "v_lshlrev_b32_sdwa v190, 1, v180 dst_sel:DWORD dst_unused:UNUSED_PAD src0_sel:DWORD src1_sel:WORD_1\n"
    "v_mfma_f32_16x16x32_bf16 v[176:179], a[136:139], a[240:243], v[176:179]\n"
    "v_lshlrev_b32_sdwa v191, 1, v182 dst_sel:DWORD dst_unused:UNUSED_PAD src0_sel:DWORD src1_sel:WORD_1\n"
    "v_pk_fma_f16 v234, v234, s49, s49\n"
    "v_mfma_f32_16x16x32_bf16 v[176:179], a[52:55], a[244:247], v[176:179]\n"
    "s_waitcnt lgkmcnt(7)\n"
    "ds_read_u16_d16_hi v203, v203\n"
    "v_or_b32_e32 v198, v198, v206\n"
    "v_lshlrev_b32_sdwa v206, 1, v181 dst_sel:DWORD dst_unused:UNUSED_PAD src0_sel:DWORD src1_sel:WORD_1\n"
    "v_or_b32_e64 v199, v199, v207\n"
    "v_mfma_f32_16x16x32_bf16 v[172:175], v[56:59], a[232:235], v[172:175]\n"
    "ds_read_u16 v188, v188\n"
    "v_lshlrev_b32_sdwa v207, 1, v183 dst_sel:DWORD dst_unused:UNUSED_PAD src0_sel:DWORD src1_sel:WORD_1\n"
    "v_rcp_f16_e32 v232, v234\n"
    "v_mfma_f32_16x16x32_bf16 v[180:183], a[92:95], a[236:239], 0\n"
    "s_waitcnt lgkmcnt(8)\n"
    "v_rcp_f16_sdwa v232, v234 dst_sel:WORD_1 dst_unused:UNUSED_PRESERVE src0_sel:WORD_1\n"
    "v_mfma_f32_16x16x32_bf16 v[180:183], a[140:143], a[240:243], v[180:183]\n"
    "v_fma_mix_f32 v234, v196, v232, 0 op_sel_hi:[1,1,0]\n"
    "v_fma_mix_f32 v232, v196, v232, 0 op_sel:[1,1,0] op_sel_hi:[1,1,0]\n"
    "v_mfma_f32_16x16x32_bf16 v[180:183], v[56:59], a[244:247], v[180:183]\n"
    "ds_write_b128 v253, v[216:219] offset:2048\n"
    "v_cvt_pk_bf16_f32 v196, v234, v232\n"
    "v_or_b32_e32 v184, v184, v200\n"
    "v_mfma_f32_16x16x32_bf16 v[96:99], a[208:211], v[192:195], v[96:99]\n"
    "s_waitcnt lgkmcnt(7)\n"
    "buffer_load_dwordx4 v[156:159], v245, s[40:43], s88 offen offset:64\n"
    "buffer_load_dwordx4 v[148:151], v245, s[40:43], s88 offen offset:0\n"
    "buffer_load_dwordx4 v[212:215], v245, s[40:43], s88 offen offset:128\n"
    "v_mfma_f32_16x16x32_bf16 v[100:103], a[212:215], v[192:195], v[100:103]\n"
    "ds_read_b128 v[164:167], v244 offset:22528\n"
    "ds_write_b128 v253, v[220:223] offset:5120\n"
    "v_mfma_f32_16x16x32_bf16 v[112:115], a[216:219], v[192:195], v[112:115]\n"
    "s_waitcnt lgkmcnt(6)\n"
    "v_or_b32_e32 v185, v185, v201\n"
    "v_lshlrev_b32_sdwa v201, 1, v171 dst_sel:DWORD dst_unused:UNUSED_PAD src0_sel:DWORD src1_sel:WORD_1\n"
    "v_mfma_f32_16x16x32_bf16 v[116:119], a[220:223], v[192:195], v[116:119]\n"
    "v_or_b32_e64 v186, v186, v202\n"
    "v_lshlrev_b32_sdwa v202, 1, v173 dst_sel:DWORD dst_unused:UNUSED_PAD src0_sel:DWORD src1_sel:WORD_1\n"
    "v_mfma_f32_16x16x32_bf16 v[128:131], v[236:239], v[192:195], v[128:131]\n"
    "ds_read_u16_d16_hi v204, v204\n"
    "ds_read_u16 v189, v189\n"
    "v_mfma_f32_16x16x32_bf16 v[104:107], a[208:211], v[196:199], v[104:107]\n"
    "ds_read_u16_d16_hi v205, v205\n"
    "ds_read_u16 v190, v190\n"
    "v_cvt_pk_bf16_f32 v96, v96, v97\n"
    "v_cvt_pk_bf16_f32 v97, v98, v99\n"
    "v_cvt_pk_bf16_f32 v98, v100, v101\n"
    "v_cvt_pk_bf16_f32 v99, v102, v103\n"
    "v_mfma_f32_16x16x32_bf16 v[108:111], a[212:215], v[196:199], v[108:111]\n"
    "s_waitcnt lgkmcnt(6)\n"
    "ds_read_u16_d16_hi v206, v206\n"
    "buffer_store_dwordx4 v[96:99], v245, s[68:71], s59 offen offset:0 sc1\n"
    "v_mfma_f32_16x16x32_bf16 v[120:123], a[216:219], v[196:199], v[120:123]\n"
    "s_waitcnt lgkmcnt(6)\n"
    "ds_read_u16 v191, v191\n"
    "v_or_b32_e64 v187, v187, v203\n"
    "v_lshlrev_b32_sdwa v203, 1, v175 dst_sel:DWORD dst_unused:UNUSED_PAD src0_sel:DWORD src1_sel:WORD_1\n"
    "v_cvt_pk_bf16_f32 v224, v112, v113\n"
    "v_mfma_f32_16x16x32_bf16 v[132:135], v[164:167], v[192:195], v[132:135]\n"
    "ds_read_u16_d16_hi v207, v207\n"
    "v_cvt_pk_f16_f32 v192, v168, v169\n"
    "v_lshlrev_b32_sdwa v193, 1, v170 dst_sel:DWORD dst_unused:UNUSED_PAD src0_sel:DWORD src1_sel:WORD_1\n"
    "v_lshlrev_b32_sdwa v194, 1, v172 dst_sel:DWORD dst_unused:UNUSED_PAD src0_sel:DWORD src1_sel:WORD_1\n"
    "v_lshlrev_b32_sdwa v195, 1, v174 dst_sel:DWORD dst_unused:UNUSED_PAD src0_sel:DWORD src1_sel:WORD_1\n"
    "v_mfma_f32_16x16x32_bf16 v[124:127], a[220:223], v[196:199], v[124:127]\n"
    "ds_read_u16 v193, v193\n"
    "v_cvt_pk_bf16_f32 v225, v114, v115\n"
    "v_cvt_pk_bf16_f32 v226, v116, v117\n"
    "v_cvt_pk_bf16_f32 v227, v118, v119\n"
    "v_cvt_pk_bf16_f32 v228, v128, v129\n"
    "v_mfma_f32_16x16x32_bf16 v[136:139], v[236:239], v[196:199], v[136:139]\n"
    "s_waitcnt lgkmcnt(7)\n"
    "ds_read_u16_d16_hi v201, v201\n"
    "v_or_b32_e32 v188, v188, v204\n"
    "v_cvt_pk_bf16_f32 v229, v130, v131\n"
    "v_cvt_pk_bf16_f32 v230, v132, v133\n"
    "v_cvt_pk_bf16_f32 v231, v134, v135\n"
    "v_mfma_f32_16x16x32_bf16 v[140:143], v[164:167], v[196:199], v[140:143]\n"
    "ds_read_u16 v194, v194\n"
    "v_cvt_pk_f16_f32 v196, v176, v177\n"
    "v_lshlrev_b32_sdwa v197, 1, v178 dst_sel:DWORD dst_unused:UNUSED_PAD src0_sel:DWORD src1_sel:WORD_1\n"
    "v_lshlrev_b32_sdwa v198, 1, v180 dst_sel:DWORD dst_unused:UNUSED_PAD src0_sel:DWORD src1_sel:WORD_1\n"
    "v_lshlrev_b32_sdwa v199, 1, v182 dst_sel:DWORD dst_unused:UNUSED_PAD src0_sel:DWORD src1_sel:WORD_1\n"
    "v_mfma_f32_16x16x32_bf16 v[168:171], a[96:99], a[224:227], 0\n"
    "s_waitcnt lgkmcnt(7)\n"
    "ds_read_u16_d16_hi v202, v202\n"
    "v_or_b32_e32 v189, v189, v205\n"
    "v_mfma_f32_16x16x32_bf16 v[168:171], a[144:147], a[228:231], v[168:171]\n"
    "v_lshlrev_b32_sdwa v205, 1, v179 dst_sel:DWORD dst_unused:UNUSED_PAD src0_sel:DWORD src1_sel:WORD_1\n"
    "v_cvt_pk_bf16_f32 v216, v104, v105\n"
    "v_mfma_f32_16x16x32_bf16 v[168:171], a[56:59], a[232:235], v[168:171]\n"
    "ds_read_u16 v195, v195\n"
    "v_cvt_pk_bf16_f32 v217, v106, v107\n"
    "v_cvt_pk_bf16_f32 v218, v108, v109\n"
    "v_cvt_pk_bf16_f32 v219, v110, v111\n"
    "v_mfma_f32_16x16x32_bf16 v[172:175], a[100:103], a[224:227], 0\n"
    "s_waitcnt lgkmcnt(7)\n"
    "ds_read_b128 v[0:3], v244 offset:0\n"
    "v_or_b32_e32 v190, v190, v206\n"
    "v_mfma_f32_16x16x32_bf16 v[172:175], a[148:151], a[228:231], v[172:175]\n"
    "ds_read_u16_d16_hi v203, v203\n"
    "v_lshlrev_b32_sdwa v206, 1, v181 dst_sel:DWORD dst_unused:UNUSED_PAD src0_sel:DWORD src1_sel:WORD_1\n"
    "v_mfma_f32_16x16x32_bf16 v[172:175], a[60:63], a[232:235], v[172:175]\n"
    "v_cvt_pk_bf16_f32 v220, v120, v121\n"
    "v_cvt_pk_bf16_f32 v221, v122, v123\n"
    "v_mfma_f32_16x16x32_bf16 v[176:179], a[96:99], a[236:239], 0\n"
    "ds_read_b128 v[4:7], v244 offset:2048\n"
    "v_cvt_pk_bf16_f32 v222, v124, v125\n"
    "v_mfma_f32_16x16x32_bf16 v[176:179], a[144:147], a[240:243], v[176:179]\n"
    "s_waitcnt lgkmcnt(8)\n"
    "ds_read_u16 v197, v197\n"
    "v_or_b32_e32 v191, v191, v207\n"
    "v_mfma_f32_16x16x32_bf16 v[176:179], a[56:59], a[244:247], v[176:179]\n"
    "v_lshlrev_b32_sdwa v207, 1, v183 dst_sel:DWORD dst_unused:UNUSED_PAD src0_sel:DWORD src1_sel:WORD_1\n"
    "v_cvt_pk_bf16_f32 v223, v126, v127\n"
    "v_mfma_f32_16x16x32_bf16 v[180:183], a[100:103], a[236:239], 0\n"
    "ds_read_u16_d16_hi v205, v205\n"
    "v_pk_fma_f16 v232, v192, v192, s48\n"
    "v_mfma_f32_16x16x32_bf16 v[180:183], a[148:151], a[240:243], v[180:183]\n"
    "s_waitcnt lgkmcnt(8)\n"
    "v_or_b32_e32 v193, v193, v201\n"
    "v_cvt_pk_bf16_f32 v160, v136, v137\n"
    "v_mfma_f32_16x16x32_bf16 v[180:183], a[60:63], a[244:247], v[180:183]\n"
    "ds_read_u16 v198, v198\n"
    "v_cvt_pk_bf16_f32 v161, v138, v139\n"
    "v_mfma_f32_16x16x32_bf16 v[96:99], a[0:3], v[184:187], 0\n"
    "ds_read_u16_d16_hi v206, v206\n"
    "v_cvt_pk_bf16_f32 v162, v140, v141\n"
    "v_mfma_f32_16x16x32_bf16 v[100:103], a[4:7], v[184:187], 0\n"
    "v_cvt_pk_bf16_f32 v163, v142, v143\n"
    "v_pk_mul_f16 v232, v192, v232\n"
    "v_mfma_f32_16x16x32_bf16 v[112:115], a[8:11], v[184:187], 0\n"
    "s_waitcnt lgkmcnt(8)\n"
    "ds_read_u16 v199, v199\n"
    "v_or_b32_e32 v194, v194, v202\n"
    "v_mfma_f32_16x16x32_bf16 v[116:119], a[12:15], v[184:187], 0\n"
    "s_waitcnt lgkmcnt(7)\n"
    "v_exp_f16_e32 v234, v232\n"
    "v_mfma_f32_16x16x32_bf16 v[128:131], v[0:3], v[184:187], 0\n"
    "ds_read_u16_d16_hi v207, v207\n"
    "v_exp_f16_sdwa v234, v232 dst_sel:WORD_1 dst_unused:UNUSED_PRESERVE src0_sel:WORD_1\n"
    "v_mfma_f32_16x16x32_bf16 v[104:107], a[0:3], v[188:191], 0\n"
    "ds_read_b128 v[8:11], v244 offset:4096\n"
    "v_pk_fma_f16 v234, v234, s49, s49\n"
    "v_mfma_f32_16x16x32_bf16 v[108:111], a[4:7], v[188:191], 0\n"
    "s_waitcnt lgkmcnt(7)\n"
    "v_or_b32_e32 v195, v195, v203\n"
    "v_lshlrev_b32_sdwa v200, 1, v169 dst_sel:DWORD dst_unused:UNUSED_PAD src0_sel:DWORD src1_sel:WORD_1\n"
    "v_mfma_f32_16x16x32_bf16 v[132:135], v[4:7], v[184:187], 0\n"
    "ds_read_u16_d16_hi v200, v200\n"
    "v_lshlrev_b32_sdwa v184, 1, v168 dst_sel:DWORD dst_unused:UNUSED_PAD src0_sel:DWORD src1_sel:WORD_1\n"
    "v_mfma_f32_16x16x32_bf16 v[120:123], a[8:11], v[188:191], 0\n"
    "ds_read_u16 v184, v184\n"
    "v_lshlrev_b32_sdwa v185, 1, v170 dst_sel:DWORD dst_unused:UNUSED_PAD src0_sel:DWORD src1_sel:WORD_1\n"
    "v_mfma_f32_16x16x32_bf16 v[124:127], a[12:15], v[188:191], 0\n"
    "buffer_store_dwordx4 v[224:227], v245, s[68:71], s59 offen offset:64 sc1\n"
    "ds_read_u16 v185, v185\n"
    "v_mfma_f32_16x16x32_bf16 v[136:139], v[0:3], v[188:191], 0\n"
    "s_waitcnt lgkmcnt(8)\n"
    "v_or_b32_e32 v197, v197, v205\n"
    "v_lshlrev_b32_sdwa v201, 1, v171 dst_sel:DWORD dst_unused:UNUSED_PAD src0_sel:DWORD src1_sel:WORD_1\n"
    "v_mfma_f32_16x16x32_bf16 v[140:143], v[4:7], v[188:191], 0\n"
    "ds_read_u16_d16_hi v201, v201\n"
    "v_lshlrev_b32_sdwa v186, 1, v172 dst_sel:DWORD dst_unused:UNUSED_PAD src0_sel:DWORD src1_sel:WORD_1\n"
    "v_mfma_f32_16x16x32_bf16 v[168:171], a[104:107], a[224:227], 0\n"
    "ds_read_u16 v186, v186\n"
    "v_lshlrev_b32_sdwa v202, 1, v173 dst_sel:DWORD dst_unused:UNUSED_PAD src0_sel:DWORD src1_sel:WORD_1\n"
    "v_mfma_f32_16x16x32_bf16 v[168:171], a[152:155], a[228:231], v[168:171]\n"
    "s_waitcnt lgkmcnt(8)\n"
    "v_or_b32_e32 v198, v198, v206\n"
    "v_lshlrev_b32_sdwa v187, 1, v174 dst_sel:DWORD dst_unused:UNUSED_PAD src0_sel:DWORD src1_sel:WORD_1\n"
    "v_lshlrev_b32_sdwa v203, 1, v175 dst_sel:DWORD dst_unused:UNUSED_PAD src0_sel:DWORD src1_sel:WORD_1\n"
    "v_rcp_f16_e64 v232, v234\n"
    "v_mfma_f32_16x16x32_bf16 v[168:171], a[64:67], a[232:235], v[168:171]\n"
    "ds_read_u16_d16_hi v202, v202\n"
    "v_rcp_f16_sdwa v232, v234 dst_sel:WORD_1 dst_unused:UNUSED_PRESERVE src0_sel:WORD_1\n"
    "v_mfma_f32_16x16x32_bf16 v[172:175], a[108:111], a[224:227], 0\n"
    "s_nop 0\n"
    "ds_read_u16 v187, v187\n"
    "v_lshlrev_b32_sdwa v188, 1, v176 dst_sel:DWORD dst_unused:UNUSED_PAD src0_sel:DWORD src1_sel:WORD_1\n"
    "v_mfma_f32_16x16x32_bf16 v[172:175], a[156:159], a[228:231], v[172:175]\n"
    "s_waitcnt lgkmcnt(7)\n"
    "v_lshlrev_b32_sdwa v204, 1, v177 dst_sel:DWORD dst_unused:UNUSED_PAD src0_sel:DWORD src1_sel:WORD_1\n"
    "v_lshlrev_b32_sdwa v189, 1, v178 dst_sel:DWORD dst_unused:UNUSED_PAD src0_sel:DWORD src1_sel:WORD_1\n"
    "v_lshlrev_b32_sdwa v205, 1, v179 dst_sel:DWORD dst_unused:UNUSED_PAD src0_sel:DWORD src1_sel:WORD_1\n"
    "v_lshlrev_b32_sdwa v190, 1, v180 dst_sel:DWORD dst_unused:UNUSED_PAD src0_sel:DWORD src1_sel:WORD_1\n"
    "v_lshlrev_b32_sdwa v206, 1, v181 dst_sel:DWORD dst_unused:UNUSED_PAD src0_sel:DWORD src1_sel:WORD_1\n"
    "v_lshlrev_b32_sdwa v191, 1, v182 dst_sel:DWORD dst_unused:UNUSED_PAD src0_sel:DWORD src1_sel:WORD_1\n"
    "v_fma_mix_f32 v234, v192, v232, 0 op_sel_hi:[1,1,0]\n"
    "v_mfma_f32_16x16x32_bf16 v[172:175], a[68:71], a[232:235], v[172:175]\n"
    "ds_read_u16_d16_hi v203, v203\n"
    "ds_read_b128 v[12:15], v244 offset:6144\n"
    "v_or_b32_e32 v199, v199, v207\n"
    "v_lshlrev_b32_sdwa v207, 1, v183 dst_sel:DWORD dst_unused:UNUSED_PAD src0_sel:DWORD src1_sel:WORD_1\n"
    "v_fma_mix_f32 v232, v192, v232, 0 op_sel:[1,1,0] op_sel_hi:[1,1,0]\n"
    "v_mfma_f32_16x16x32_bf16 v[176:179], a[104:107], a[236:239], 0\n"
    "s_waitcnt lgkmcnt(7)\n"
    "ds_read_u16 v188, v188\n"
    "v_cvt_pk_bf16_f32 v192, v234, v232\n"
    "v_mfma_f32_16x16x32_bf16 v[176:179], a[152:155], a[240:243], v[176:179]\n"
    "ds_read_u16_d16_hi v204, v204\n"
    "v_pk_fma_f16 v232, v196, v196, s48\n"
    "v_mfma_f32_16x16x32_bf16 v[176:179], a[64:67], a[244:247], v[176:179]\n"
    "v_pk_mul_f16 v232, v196, v232\n"
    "v_or_b32_e64 v184, v184, v200\n"
    "v_mfma_f32_16x16x32_bf16 v[180:183], a[108:111], a[236:239], 0\n"
    "ds_read_u16 v189, v189\n"
    "v_mfma_f32_16x16x32_bf16 v[180:183], a[156:159], a[240:243], v[180:183]\n"
    "s_waitcnt lgkmcnt(8)\n"
    "v_exp_f16_e32 v234, v232\n"
    "v_mfma_f32_16x16x32_bf16 v[180:183], a[68:71], a[244:247], v[180:183]\n"
    "ds_read_u16_d16_hi v205, v205\n"
    "v_exp_f16_sdwa v234, v232 dst_sel:WORD_1 dst_unused:UNUSED_PRESERVE src0_sel:WORD_1\n"
    "v_mfma_f32_16x16x32_bf16 v[96:99], a[16:19], v[192:195], v[96:99]\n"
    "ds_read_u16 v190, v190\n"
    "v_pk_fma_f16 v234, v234, s49, s49\n"
    "v_mfma_f32_16x16x32_bf16 v[100:103], a[20:23], v[192:195], v[100:103]\n"
    "s_waitcnt lgkmcnt(8)\n"
    "v_rcp_f16_e64 v232, v234\n"
    "v_mfma_f32_16x16x32_bf16 v[112:115], a[24:27], v[192:195], v[112:115]\n"
    "ds_read_u16_d16_hi v206, v206\n"
    "buffer_store_dwordx4 v[228:231], v245, s[68:71], s59 offen offset:128 sc1\n"
    "v_rcp_f16_sdwa v232, v234 dst_sel:WORD_1 dst_unused:UNUSED_PRESERVE src0_sel:WORD_1\n"
    "v_mfma_f32_16x16x32_bf16 v[116:119], a[28:31], v[192:195], v[116:119]\n"
    "s_waitcnt lgkmcnt(6)\n"
    "ds_read_u16 v191, v191\n"
    "v_fma_mix_f32 v234, v196, v232, 0 op_sel_hi:[1,1,0]\n"
    "v_mfma_f32_16x16x32_bf16 v[128:131], v[8:11], v[192:195], v[128:131]\n"
    "ds_read_u16_d16_hi v207, v207\n"
    "v_fma_mix_f32 v232, v196, v232, 0 op_sel:[1,1,0] op_sel_hi:[1,1,0]\n"
    "v_mfma_f32_16x16x32_bf16 v[132:135], v[12:15], v[192:195], v[132:135]\n"
    "v_cvt_pk_bf16_f32 v196, v234, v232\n"
    "v_or_b32_e64 v185, v185, v201\n"
    "v_cvt_pk_f16_f32 v192, v168, v169\n"
    "v_mfma_f32_16x16x32_bf16 v[104:107], a[16:19], v[196:199], v[104:107]\n"
    "s_nop 0\n"
    "ds_read_b128 v[52:55], v244 offset:34816\n"
    "ds_read_b128 v[16:19], v244 offset:8192\n"
    "v_mfma_f32_16x16x32_bf16 v[108:111], a[20:23], v[196:199], v[108:111]\n"
    "s_waitcnt lgkmcnt(8)\n"
    "v_lshlrev_b32_sdwa v193, 1, v170 dst_sel:DWORD dst_unused:UNUSED_PAD src0_sel:DWORD src1_sel:WORD_1\n"
    "v_lshlrev_b32_sdwa v201, 1, v171 dst_sel:DWORD dst_unused:UNUSED_PAD src0_sel:DWORD src1_sel:WORD_1\n"
    "v_mfma_f32_16x16x32_bf16 v[120:123], a[24:27], v[196:199], v[120:123]\n"
    "ds_read_u16 v193, v193\n"
    "v_lshlrev_b32_sdwa v194, 1, v172 dst_sel:DWORD dst_unused:UNUSED_PAD src0_sel:DWORD src1_sel:WORD_1\n"
    "v_mfma_f32_16x16x32_bf16 v[124:127], a[28:31], v[196:199], v[124:127]\n"
    "ds_read_u16_d16_hi v201, v201\n"
    "v_or_b32_e32 v186, v186, v202\n"
    "v_mfma_f32_16x16x32_bf16 v[136:139], v[8:11], v[196:199], v[136:139]\n"
    "v_lshlrev_b32_sdwa v202, 1, v173 dst_sel:DWORD dst_unused:UNUSED_PAD src0_sel:DWORD src1_sel:WORD_1\n"
    "v_lshlrev_b32_sdwa v195, 1, v174 dst_sel:DWORD dst_unused:UNUSED_PAD src0_sel:DWORD src1_sel:WORD_1\n"
    "v_mfma_f32_16x16x32_bf16 v[140:143], v[12:15], v[196:199], v[140:143]\n"
    "s_waitcnt lgkmcnt(8)\n"
    "ds_read_u16 v194, v194\n"
    "v_cvt_pk_f16_f32 v196, v176, v177\n"
    "v_mfma_f32_16x16x32_bf16 v[168:171], a[112:115], a[224:227], 0\n"
    "v_lshlrev_b32_sdwa v197, 1, v178 dst_sel:DWORD dst_unused:UNUSED_PAD src0_sel:DWORD src1_sel:WORD_1\n"
    "v_lshlrev_b32_sdwa v198, 1, v180 dst_sel:DWORD dst_unused:UNUSED_PAD src0_sel:DWORD src1_sel:WORD_1\n"
    "v_mfma_f32_16x16x32_bf16 v[168:171], a[160:163], a[228:231], v[168:171]\n"
    "ds_read_u16_d16_hi v202, v202\n"
    "v_lshlrev_b32_sdwa v199, 1, v182 dst_sel:DWORD dst_unused:UNUSED_PAD src0_sel:DWORD src1_sel:WORD_1\n"
    "v_mfma_f32_16x16x32_bf16 v[168:171], a[72:75], a[232:235], v[168:171]\n"
    "s_waitcnt lgkmcnt(8)\n"
    "ds_read_u16 v195, v195\n"
    "v_or_b32_e32 v187, v187, v203\n"
    "v_lshlrev_b32_sdwa v203, 1, v175 dst_sel:DWORD dst_unused:UNUSED_PAD src0_sel:DWORD src1_sel:WORD_1\n"
    "v_mfma_f32_16x16x32_bf16 v[172:175], a[116:119], a[224:227], 0\n"
    "v_or_b32_e64 v188, v188, v204\n"
    "v_or_b32_e64 v189, v189, v205\n"
    "v_mfma_f32_16x16x32_bf16 v[172:175], a[164:167], a[228:231], v[172:175]\n"
    "ds_read_u16_d16_hi v203, v203\n"
    "buffer_store_dwordx4 v[216:219], v245, s[68:71], s39 offen offset:0 sc1\n"
    "s_waitcnt lgkmcnt(7)\n"
    "v_mfma_f32_16x16x32_bf16 v[172:175], v[52:55], a[232:235], v[172:175]\n"
    "s_nop 0\n"
    "ds_read_b128 v[20:23], v244 offset:10240\n"
    "ds_read_u16 v197, v197\n"
    "v_lshlrev_b32_sdwa v205, 1, v179 dst_sel:DWORD dst_unused:UNUSED_PAD src0_sel:DWORD src1_sel:WORD_1\n"
    "v_mfma_f32_16x16x32_bf16 v[176:179], a[112:115], a[236:239], 0\n"
    "v_or_b32_e64 v190, v190, v206\n"
    "v_lshlrev_b32_sdwa v206, 1, v181 dst_sel:DWORD dst_unused:UNUSED_PAD src0_sel:DWORD src1_sel:WORD_1\n"
    "v_mfma_f32_16x16x32_bf16 v[176:179], a[160:163], a[240:243], v[176:179]\n"
    "ds_read_u16_d16_hi v205, v205\n"
    "v_or_b32_e32 v191, v191, v207\n"
    "v_mfma_f32_16x16x32_bf16 v[176:179], a[72:75], a[244:247], v[176:179]\n"
    "s_waitcnt lgkmcnt(8)\n"
    "v_lshlrev_b32_sdwa v207, 1, v183 dst_sel:DWORD dst_unused:UNUSED_PAD src0_sel:DWORD src1_sel:WORD_1\n"
    "v_pk_fma_f16 v232, v192, v192, s48\n"
    "v_mfma_f32_16x16x32_bf16 v[180:183], a[116:119], a[236:239], 0\n"
    "s_waitcnt lgkmcnt(7)\n"
    "ds_read_u16 v198, v198\n"
    "v_or_b32_e32 v193, v193, v201\n"
    "v_mfma_f32_16x16x32_bf16 v[180:183], a[164:167], a[240:243], v[180:183]\n"
    "ds_read_u16_d16_hi v206, v206\n"
    "v_pk_mul_f16 v232, v192, v232\n"
    "v_mfma_f32_16x16x32_bf16 v[180:183], v[52:55], a[244:247], v[180:183]\n"
    "v_exp_f16_e64 v234, v232\n"
    "v_mfma_f32_16x16x32_bf16 v[96:99], a[32:35], v[184:187], v[96:99]\n"
    "ds_read_u16 v199, v199\n"
    "v_exp_f16_sdwa v234, v232 dst_sel:WORD_1 dst_unused:UNUSED_PRESERVE src0_sel:WORD_1\n"
    "v_mfma_f32_16x16x32_bf16 v[100:103], a[36:39], v[184:187], v[100:103]\n"
    "s_waitcnt lgkmcnt(8)\n"
    "v_or_b32_e32 v194, v194, v202\n"
    "v_pk_fma_f16 v234, v234, s49, s49\n"
    "v_mfma_f32_16x16x32_bf16 v[112:115], a[40:43], v[184:187], v[112:115]\n"
    "ds_read_u16_d16_hi v207, v207\n"
    "v_lshlrev_b32_sdwa v200, 1, v169 dst_sel:DWORD dst_unused:UNUSED_PAD src0_sel:DWORD src1_sel:WORD_1\n"
    "v_mfma_f32_16x16x32_bf16 v[116:119], a[44:47], v[184:187], v[116:119]\n"
    "ds_read_b128 v[60:63], v244 offset:38912\n"
    "v_lshlrev_b32_sdwa v201, 1, v171 dst_sel:DWORD dst_unused:UNUSED_PAD src0_sel:DWORD src1_sel:WORD_1\n"
    "v_mfma_f32_16x16x32_bf16 v[128:131], v[16:19], v[184:187], v[128:131]\n"
    "s_waitcnt lgkmcnt(7)\n"
    "ds_read_u16_d16_hi v200, v200\n"
    "v_or_b32_e32 v195, v195, v203\n"
    "v_mfma_f32_16x16x32_bf16 v[132:135], v[20:23], v[184:187], v[132:135]\n"
    "v_lshlrev_b32_sdwa v184, 1, v168 dst_sel:DWORD dst_unused:UNUSED_PAD src0_sel:DWORD src1_sel:WORD_1\n"
    "v_lshlrev_b32_sdwa v185, 1, v170 dst_sel:DWORD dst_unused:UNUSED_PAD src0_sel:DWORD src1_sel:WORD_1\n"
    "v_mfma_f32_16x16x32_bf16 v[104:107], a[32:35], v[188:191], v[104:107]\n"
    "ds_read_u16 v184, v184\n"
    "v_lshlrev_b32_sdwa v186, 1, v172 dst_sel:DWORD dst_unused:UNUSED_PAD src0_sel:DWORD src1_sel:WORD_1\n"
    "v_mfma_f32_16x16x32_bf16 v[108:111], a[36:39], v[188:191], v[108:111]\n"
    "v_lshlrev_b32_sdwa v202, 1, v173 dst_sel:DWORD dst_unused:UNUSED_PAD src0_sel:DWORD src1_sel:WORD_1\n"
    "v_lshlrev_b32_sdwa v187, 1, v174 dst_sel:DWORD dst_unused:UNUSED_PAD src0_sel:DWORD src1_sel:WORD_1\n"
    "v_mfma_f32_16x16x32_bf16 v[120:123], a[40:43], v[188:191], v[120:123]\n"
    "ds_read_u16 v185, v185\n"
    "buffer_store_dwordx4 v[220:223], v245, s[68:71], s39 offen offset:64 sc1\n"
    "v_mfma_f32_16x16x32_bf16 v[124:127], a[44:47], v[188:191], v[124:127]\n"
    "s_waitcnt lgkmcnt(7)\n"
    "ds_read_u16_d16_hi v201, v201\n"
    "v_or_b32_e32 v197, v197, v205\n"
    "v_mfma_f32_16x16x32_bf16 v[136:139], v[16:19], v[188:191], v[136:139]\n"
    "ds_read_u16 v186, v186\n"
    "v_lshlrev_b32_sdwa v203, 1, v175 dst_sel:DWORD dst_unused:UNUSED_PAD src0_sel:DWORD src1_sel:WORD_1\n"
    "v_mfma_f32_16x16x32_bf16 v[140:143], v[20:23], v[188:191], v[140:143]\n"
    "s_waitcnt lgkmcnt(8)\n"
    "v_or_b32_e32 v198, v198, v206\n"
    "v_rcp_f16_e64 v232, v234\n"
    "v_mfma_f32_16x16x32_bf16 v[168:171], a[120:123], a[224:227], 0\n"
    "ds_read_u16_d16_hi v202, v202\n"
    "v_lshlrev_b32_sdwa v188, 1, v176 dst_sel:DWORD dst_unused:UNUSED_PAD src0_sel:DWORD src1_sel:WORD_1\n"
    "v_mfma_f32_16x16x32_bf16 v[168:171], a[168:171], a[228:231], v[168:171]\n"
    "s_nop 0\n"
    "v_rcp_f16_sdwa v232, v234 dst_sel:WORD_1 dst_unused:UNUSED_PRESERVE src0_sel:WORD_1\n"
    "v_mfma_f32_16x16x32_bf16 v[168:171], a[76:79], a[232:235], v[168:171]\n"
    "s_waitcnt lgkmcnt(6)\n"
    "ds_read_b128 v[24:27], v244 offset:12288\n"
    "ds_read_u16 v187, v187\n"
    "v_mfma_f32_16x16x32_bf16 v[172:175], a[124:127], a[224:227], 0\n"
    "v_lshlrev_b32_sdwa v204, 1, v177 dst_sel:DWORD dst_unused:UNUSED_PAD src0_sel:DWORD src1_sel:WORD_1\n"
    "v_lshlrev_b32_sdwa v189, 1, v178 dst_sel:DWORD dst_unused:UNUSED_PAD src0_sel:DWORD src1_sel:WORD_1\n"
    "v_mfma_f32_16x16x32_bf16 v[172:175], a[172:175], a[228:231], v[172:175]\n"
    "ds_read_u16_d16_hi v203, v203\n"
    "v_lshlrev_b32_sdwa v205, 1, v179 dst_sel:DWORD dst_unused:UNUSED_PAD src0_sel:DWORD src1_sel:WORD_1\n"
    "v_lshlrev_b32_sdwa v190, 1, v180 dst_sel:DWORD dst_unused:UNUSED_PAD src0_sel:DWORD src1_sel:WORD_1\n"
    "v_lshlrev_b32_sdwa v206, 1, v181 dst_sel:DWORD dst_unused:UNUSED_PAD src0_sel:DWORD src1_sel:WORD_1\n"
    "v_lshlrev_b32_sdwa v191, 1, v182 dst_sel:DWORD dst_unused:UNUSED_PAD src0_sel:DWORD src1_sel:WORD_1\n"
    "v_fma_mix_f32 v234, v192, v232, 0 op_sel_hi:[1,1,0]\n"
    "v_mfma_f32_16x16x32_bf16 v[172:175], v[60:63], a[232:235], v[172:175]\n"
    "ds_read_b128 v[28:31], v244 offset:14336\n"
    "v_or_b32_e32 v199, v199, v207\n"
    "v_lshlrev_b32_sdwa v207, 1, v183 dst_sel:DWORD dst_unused:UNUSED_PAD src0_sel:DWORD src1_sel:WORD_1\n"
    "v_fma_mix_f32 v232, v192, v232, 0 op_sel:[1,1,0] op_sel_hi:[1,1,0]\n"
    "v_mfma_f32_16x16x32_bf16 v[176:179], a[120:123], a[236:239], 0\n"
    "s_waitcnt lgkmcnt(8)\n"
    "ds_read_u16 v188, v188\n"
    "v_cvt_pk_bf16_f32 v192, v234, v232\n"
    "v_mfma_f32_16x16x32_bf16 v[176:179], a[168:171], a[240:243], v[176:179]\n"
    "ds_read_u16_d16_hi v204, v204\n"
    "v_pk_fma_f16 v232, v196, v196, s48\n"
    "v_mfma_f32_16x16x32_bf16 v[176:179], a[76:79], a[244:247], v[176:179]\n"
    "v_pk_mul_f16 v232, v196, v232\n"
    "v_or_b32_e32 v184, v184, v200\n"
    "v_mfma_f32_16x16x32_bf16 v[180:183], a[124:127], a[236:239], 0\n"
    "s_waitcnt lgkmcnt(8)\n"
    "ds_read_u16 v189, v189\n"
    "v_or_b32_e64 v185, v185, v201\n"
    "v_mfma_f32_16x16x32_bf16 v[180:183], a[172:175], a[240:243], v[180:183]\n"
    "v_exp_f16_e32 v234, v232\n"
    "v_mfma_f32_16x16x32_bf16 v[180:183], v[60:63], a[244:247], v[180:183]\n"
    "ds_read_u16_d16_hi v205, v205\n"
    "v_exp_f16_sdwa v234, v232 dst_sel:WORD_1 dst_unused:UNUSED_PRESERVE src0_sel:WORD_1\n"
    "v_mfma_f32_16x16x32_bf16 v[96:99], a[176:179], v[192:195], v[96:99]\n"
    "s_waitcnt lgkmcnt(7)\n"
    "ds_read_u16 v190, v190\n"
    "buffer_store_dwordx4 v[160:163], v245, s[68:71], s39 offen offset:128 sc1\n"
    "v_pk_fma_f16 v234, v234, s49, s49\n"
    "v_mfma_f32_16x16x32_bf16 v[100:103], a[180:183], v[192:195], v[100:103]\n"
    "ds_read_b128 a[224:227], v243 offset:24640\n"
    "v_rcp_f16_e32 v232, v234\n"
    "v_mfma_f32_16x16x32_bf16 v[112:115], a[184:187], v[192:195], v[112:115]\n"
    "ds_read_u16_d16_hi v206, v206\n"
    "v_rcp_f16_sdwa v232, v234 dst_sel:WORD_1 dst_unused:UNUSED_PRESERVE src0_sel:WORD_1\n"
    "v_mfma_f32_16x16x32_bf16 v[116:119], a[188:191], v[192:195], v[116:119]\n"
    "s_waitcnt lgkmcnt(7)\n"
    "ds_read_b128 a[228:231], v243 offset:25664\n"
    "v_fma_mix_f32 v234, v196, v232, 0 op_sel_hi:[1,1,0]\n"
    "v_mfma_f32_16x16x32_bf16 v[128:131], v[24:27], v[192:195], v[128:131]\n"
    "ds_read_b128 a[232:235], v243 offset:26688\n"
    "v_fma_mix_f32 v232, v196, v232, 0 op_sel:[1,1,0] op_sel_hi:[1,1,0]\n"
    "v_mfma_f32_16x16x32_bf16 v[132:135], v[28:31], v[192:195], v[132:135]\n"
    "ds_read_u16 v191, v191\n"
    "v_cvt_pk_bf16_f32 v196, v234, v232\n"
    "v_cvt_pk_f16_f32 v192, v168, v169\n"
    "v_lshlrev_b32_sdwa v193, 1, v170 dst_sel:DWORD dst_unused:UNUSED_PAD src0_sel:DWORD src1_sel:WORD_1\n"
    "v_mfma_f32_16x16x32_bf16 v[104:107], a[176:179], v[196:199], v[104:107]\n"
    "ds_read_b128 a[236:239], v243 offset:27712\n"
    "v_lshlrev_b32_sdwa v201, 1, v171 dst_sel:DWORD dst_unused:UNUSED_PAD src0_sel:DWORD src1_sel:WORD_1\n"
    "v_mfma_f32_16x16x32_bf16 v[108:111], a[180:183], v[196:199], v[108:111]\n"
    "s_waitcnt lgkmcnt(9)\n"
    "ds_read_u16_d16_hi v207, v207\n"
    "v_lshlrev_b32_sdwa v194, 1, v172 dst_sel:DWORD dst_unused:UNUSED_PAD src0_sel:DWORD src1_sel:WORD_1\n"
    "v_or_b32_e32 v186, v186, v202\n"
    "v_lshlrev_b32_sdwa v202, 1, v173 dst_sel:DWORD dst_unused:UNUSED_PAD src0_sel:DWORD src1_sel:WORD_1\n"
    "v_lshlrev_b32_sdwa v195, 1, v174 dst_sel:DWORD dst_unused:UNUSED_PAD src0_sel:DWORD src1_sel:WORD_1\n"
    "v_mfma_f32_16x16x32_bf16 v[120:123], a[184:187], v[196:199], v[120:123]\n"
    "ds_read_b128 a[240:243], v243 offset:28736\n"
    "v_or_b32_e64 v187, v187, v203\n"
    "v_mfma_f32_16x16x32_bf16 v[124:127], a[188:191], v[196:199], v[124:127]\n"
    "ds_read_b128 a[244:247], v243 offset:29760\n"
    "v_lshlrev_b32_sdwa v203, 1, v175 dst_sel:DWORD dst_unused:UNUSED_PAD src0_sel:DWORD src1_sel:WORD_1\n"
    "v_mfma_f32_16x16x32_bf16 v[136:139], v[24:27], v[196:199], v[136:139]\n"
    "s_waitcnt lgkmcnt(10)\n"
    "ds_read_u16 v193, v193\n"
    "v_or_b32_e32 v188, v188, v204\n"
    "v_or_b32_e32 v189, v189, v205\n"
    "v_lshlrev_b32_sdwa v205, 1, v179 dst_sel:DWORD dst_unused:UNUSED_PAD src0_sel:DWORD src1_sel:WORD_1\n"
    "v_pk_fma_f16 v232, v192, v192, s48\n"
    "v_mfma_f32_16x16x32_bf16 v[140:143], v[28:31], v[196:199], v[140:143]\n"
    "s_waitcnt lgkmcnt(9)\n"
    "ds_read_b128 v[48:51], v244 offset:32768\n"
    "v_cvt_pk_f16_f32 v196, v176, v177\n"
    "v_mfma_f32_16x16x32_bf16 v[168:171], a[80:83], a[224:227], 0\n"
    "ds_read_u16_d16_hi v201, v201\n"
    "v_lshlrev_b32_sdwa v197, 1, v178 dst_sel:DWORD dst_unused:UNUSED_PAD src0_sel:DWORD src1_sel:WORD_1\n"
    "v_lshlrev_b32_sdwa v198, 1, v180 dst_sel:DWORD dst_unused:UNUSED_PAD src0_sel:DWORD src1_sel:WORD_1\n"
    "v_lshlrev_b32_sdwa v199, 1, v182 dst_sel:DWORD dst_unused:UNUSED_PAD src0_sel:DWORD src1_sel:WORD_1\n"
    "v_pk_mul_f16 v232, v192, v232\n"
    "v_mfma_f32_16x16x32_bf16 v[172:175], a[84:87], a[224:227], 0\n"
    "s_waitcnt lgkmcnt(8)\n"
    "ds_read_u16 v194, v194\n"
    "v_or_b32_e32 v190, v190, v206\n"
    "v_mfma_f32_16x16x32_bf16 v[172:175], a[132:135], a[228:231], v[172:175]\n"
    "v_lshlrev_b32_sdwa v206, 1, v181 dst_sel:DWORD dst_unused:UNUSED_PAD src0_sel:DWORD src1_sel:WORD_1\n"
    "v_exp_f16_e64 v234, v232\n"
    "v_mfma_f32_16x16x32_bf16 v[168:171], a[128:131], a[228:231], v[168:171]\n"
    "ds_read_u16_d16_hi v202, v202\n"
    "v_mfma_f32_16x16x32_bf16 v[168:171], a[48:51], a[232:235], v[168:171]\n"
    "s_waitcnt lgkmcnt(8)\n"
    "v_exp_f16_sdwa v234, v232 dst_sel:WORD_1 dst_unused:UNUSED_PRESERVE src0_sel:WORD_1\n"
    "v_mfma_f32_16x16x32_bf16 v[176:179], a[80:83], a[236:239], 0\n"
    "s_nop 0\n"
    "ds_read_u16 v195, v195\n"
    "v_pk_fma_f16 v234, v234, s49, s49\n"
    "v_mfma_f32_16x16x32_bf16 v[96:99], a[192:195], v[184:187], v[96:99]\n"
    "ds_read_b128 v[32:35], v244 offset:16384\n"
    "v_rcp_f16_e32 v232, v234\n"
    "v_mfma_f32_16x16x32_bf16 v[100:103], a[196:199], v[184:187], v[100:103]\n"
    "s_waitcnt lgkmcnt(7)\n"
    "ds_read_u16_d16_hi v203, v203\n"
    "v_or_b32_e64 v191, v191, v207\n"
    "v_lshlrev_b32_sdwa v207, 1, v183 dst_sel:DWORD dst_unused:UNUSED_PAD src0_sel:DWORD src1_sel:WORD_1\n"
    "v_lshlrev_b32_sdwa v200, 1, v169 dst_sel:DWORD dst_unused:UNUSED_PAD src0_sel:DWORD src1_sel:WORD_1\n"
    "v_mfma_f32_16x16x32_bf16 v[176:179], a[128:131], a[240:243], v[176:179]\n"
    "ds_read_b128 v[36:39], v244 offset:18432\n"
    "v_mfma_f32_16x16x32_bf16 v[176:179], a[48:51], a[244:247], v[176:179]\n"
    "s_nop 0\n"
    "ds_read_u16 v197, v197\n"
    "v_rcp_f16_sdwa v232, v234 dst_sel:WORD_1 dst_unused:UNUSED_PRESERVE src0_sel:WORD_1\n"
    "v_mfma_f32_16x16x32_bf16 v[180:183], a[84:87], a[236:239], 0\n"
    "s_waitcnt lgkmcnt(8)\n"
    "v_fma_mix_f32 v234, v192, v232, 0 op_sel_hi:[1,1,0]\n"
    "v_fma_mix_f32 v232, v192, v232, 0 op_sel:[1,1,0] op_sel_hi:[1,1,0]\n"
    "v_mfma_f32_16x16x32_bf16 v[180:183], a[132:135], a[240:243], v[180:183]\n"
    "ds_read_u16_d16_hi v205, v205\n"
    "v_lshlrev_b32_sdwa v204, 1, v177 dst_sel:DWORD dst_unused:UNUSED_PAD src0_sel:DWORD src1_sel:WORD_1\n"
    "v_mfma_f32_16x16x32_bf16 v[180:183], v[48:51], a[244:247], v[180:183]\n"
    "s_waitcnt lgkmcnt(8)\n"
    "v_or_b32_e32 v193, v193, v201\n"
    "v_lshlrev_b32_sdwa v201, 1, v171 dst_sel:DWORD dst_unused:UNUSED_PAD src0_sel:DWORD src1_sel:WORD_1\n"
    "v_mfma_f32_16x16x32_bf16 v[172:175], v[48:51], a[232:235], v[172:175]\n"
    "ds_read_u16 v198, v198\n"
    "v_cvt_pk_bf16_f32 v192, v234, v232\n"
    "v_mfma_f32_16x16x32_bf16 v[112:115], a[200:203], v[184:187], v[112:115]\n"
    "ds_read_u16_d16_hi v206, v206\n"
    "v_pk_fma_f16 v232, v196, v196, s48\n"
    "v_mfma_f32_16x16x32_bf16 v[116:119], a[204:207], v[184:187], v[116:119]\n"
    "v_pk_mul_f16 v232, v196, v232\n"
    "v_exp_f16_e32 v234, v232\n"
    "v_mfma_f32_16x16x32_bf16 v[104:107], a[192:195], v[188:191], v[104:107]\n"
    "s_waitcnt lgkmcnt(8)\n"
    "ds_read_u16 v199, v199\n"
    "v_or_b32_e32 v194, v194, v202\n"
    "v_lshlrev_b32_sdwa v202, 1, v173 dst_sel:DWORD dst_unused:UNUSED_PAD src0_sel:DWORD src1_sel:WORD_1\n"
    "v_mfma_f32_16x16x32_bf16 v[108:111], a[196:199], v[188:191], v[108:111]\n"
    "s_waitcnt lgkmcnt(7)\n"
    "ds_read_u16_d16_hi v207, v207\n"
    "v_exp_f16_sdwa v234, v232 dst_sel:WORD_1 dst_unused:UNUSED_PRESERVE src0_sel:WORD_1\n"
    "v_mfma_f32_16x16x32_bf16 v[128:131], v[32:35], v[184:187], v[128:131]\n"
    "v_pk_fma_f16 v234, v234, s49, s49\n"
    "v_rcp_f16_e32 v232, v234\n"
    "v_mfma_f32_16x16x32_bf16 v[120:123], a[200:203], v[188:191], v[120:123]\n"
    "s_waitcnt lgkmcnt(6)\n"
    "ds_read_u16_d16_hi v200, v200\n"
    "v_or_b32_e64 v195, v195, v203\n"
    "v_mfma_f32_16x16x32_bf16 v[132:135], v[36:39], v[184:187], v[132:135]\n"
    "ds_read_u16_d16_hi v201, v201\n"
    "v_lshlrev_b32_sdwa v184, 1, v168 dst_sel:DWORD dst_unused:UNUSED_PAD src0_sel:DWORD src1_sel:WORD_1\n"
    "v_mfma_f32_16x16x32_bf16 v[124:127], a[204:207], v[188:191], v[124:127]\n"
    "v_lshlrev_b32_sdwa v185, 1, v170 dst_sel:DWORD dst_unused:UNUSED_PAD src0_sel:DWORD src1_sel:WORD_1\n"
    "v_lshlrev_b32_sdwa v186, 1, v172 dst_sel:DWORD dst_unused:UNUSED_PAD src0_sel:DWORD src1_sel:WORD_1\n"
    "v_mfma_f32_16x16x32_bf16 v[136:139], v[32:35], v[188:191], v[136:139]\n"
    "ds_read_u16 v184, v184\n"
    "v_lshlrev_b32_sdwa v187, 1, v174 dst_sel:DWORD dst_unused:UNUSED_PAD src0_sel:DWORD src1_sel:WORD_1\n"
    "v_mfma_f32_16x16x32_bf16 v[140:143], v[36:39], v[188:191], v[140:143]\n"
    "v_lshlrev_b32_sdwa v203, 1, v175 dst_sel:DWORD dst_unused:UNUSED_PAD src0_sel:DWORD src1_sel:WORD_1\n"
    "v_lshlrev_b32_sdwa v188, 1, v176 dst_sel:DWORD dst_unused:UNUSED_PAD src0_sel:DWORD src1_sel:WORD_1\n"
    "v_mfma_f32_16x16x32_bf16 v[168:171], a[88:91], a[224:227], 0\n"
    "s_waitcnt lgkmcnt(7)\n"
    "ds_read_u16 v185, v185\n"
    "v_or_b32_e32 v197, v197, v205\n"
    "v_mfma_f32_16x16x32_bf16 v[168:171], a[136:139], a[228:231], v[168:171]\n"
    "v_lshlrev_b32_sdwa v189, 1, v178 dst_sel:DWORD dst_unused:UNUSED_PAD src0_sel:DWORD src1_sel:WORD_1\n"
    "v_lshlrev_b32_sdwa v205, 1, v179 dst_sel:DWORD dst_unused:UNUSED_PAD src0_sel:DWORD src1_sel:WORD_1\n"
    "v_mfma_f32_16x16x32_bf16 v[168:171], a[52:55], a[232:235], v[168:171]\n"
    "ds_read_u16 v186, v186\n"
    "v_lshlrev_b32_sdwa v190, 1, v180 dst_sel:DWORD dst_unused:UNUSED_PAD src0_sel:DWORD src1_sel:WORD_1\n"
    "v_mfma_f32_16x16x32_bf16 v[172:175], a[92:95], a[224:227], 0\n"
    "s_waitcnt lgkmcnt(7)\n"
    "ds_read_u16_d16_hi v202, v202\n"
    "v_or_b32_e32 v198, v198, v206\n"
    "v_mfma_f32_16x16x32_bf16 v[172:175], a[140:143], a[228:231], v[172:175]\n"
    "v_lshlrev_b32_sdwa v206, 1, v181 dst_sel:DWORD dst_unused:UNUSED_PAD src0_sel:DWORD src1_sel:WORD_1\n"
    "v_lshlrev_b32_sdwa v191, 1, v182 dst_sel:DWORD dst_unused:UNUSED_PAD src0_sel:DWORD src1_sel:WORD_1\n"
    "v_mfma_f32_16x16x32_bf16 v[172:175], v[56:59], a[232:235], v[172:175]\n"
    "ds_read_b128 v[40:43], v244 offset:20480\n"
    "v_rcp_f16_sdwa v232, v234 dst_sel:WORD_1 dst_unused:UNUSED_PRESERVE src0_sel:WORD_1\n"
    "v_mfma_f32_16x16x32_bf16 v[176:179], a[88:91], a[236:239], 0\n"
    "ds_read_u16 v187, v187\n"
    "v_fma_mix_f32 v234, v196, v232, 0 op_sel_hi:[1,1,0]\n"
    "v_mfma_f32_16x16x32_bf16 v[176:179], a[136:139], a[240:243], v[176:179]\n"
    "s_waitcnt lgkmcnt(8)\n"
    "v_or_b32_e32 v199, v199, v207\n"
    "v_lshlrev_b32_sdwa v207, 1, v183 dst_sel:DWORD dst_unused:UNUSED_PAD src0_sel:DWORD src1_sel:WORD_1\n"
    "v_mfma_f32_16x16x32_bf16 v[176:179], a[52:55], a[244:247], v[176:179]\n"
    "ds_read_u16_d16_hi v203, v203\n"
    "v_fma_mix_f32 v232, v196, v232, 0 op_sel:[1,1,0] op_sel_hi:[1,1,0]\n"
    "v_mfma_f32_16x16x32_bf16 v[180:183], a[92:95], a[236:239], 0\n"
    "ds_read_b128 v[44:47], v244 offset:22528\n"
    "v_cvt_pk_bf16_f32 v196, v234, v232\n"
    "v_mfma_f32_16x16x32_bf16 v[180:183], a[140:143], a[240:243], v[180:183]\n"
    "ds_read_u16 v188, v188\n"
    "v_mfma_f32_16x16x32_bf16 v[180:183], v[56:59], a[244:247], v[180:183]\n"
    "s_nop 0\n"
    "v_mfma_f32_16x16x32_bf16 v[96:99], a[208:211], v[192:195], v[96:99]\n"
    "s_waitcnt lgkmcnt(8)\n"
    "ds_read_u16_d16_hi v204, v204\n"
    "v_or_b32_e32 v184, v184, v200\n"
    "v_mfma_f32_16x16x32_bf16 v[100:103], a[212:215], v[192:195], v[100:103]\n"
    "v_mfma_f32_16x16x32_bf16 v[112:115], a[216:219], v[192:195], v[112:115]\n"
    "s_waitcnt lgkmcnt(8)\n"
    "ds_read_u16 v189, v189\n"
    "v_or_b32_e64 v185, v185, v201\n"
    "v_mfma_f32_16x16x32_bf16 v[116:119], a[220:223], v[192:195], v[116:119]\n"
    "v_lshlrev_b32_sdwa v201, 1, v171 dst_sel:DWORD dst_unused:UNUSED_PAD src0_sel:DWORD src1_sel:WORD_1\n"
    "v_cvt_pk_bf16_f32 v96, v96, v97\n"
    "v_mfma_f32_16x16x32_bf16 v[104:107], a[208:211], v[196:199], v[104:107]\n"
    "ds_read_u16_d16_hi v205, v205\n"
    "v_cvt_pk_bf16_f32 v97, v98, v99\n"
    "v_mfma_f32_16x16x32_bf16 v[108:111], a[212:215], v[196:199], v[108:111]\n"
    "s_waitcnt lgkmcnt(7)\n"
    "ds_read_u16 v190, v190\n"
    "v_or_b32_e32 v186, v186, v202\n"
    "v_lshlrev_b32_sdwa v202, 1, v173 dst_sel:DWORD dst_unused:UNUSED_PAD src0_sel:DWORD src1_sel:WORD_1\n"
    "v_cvt_pk_bf16_f32 v98, v100, v101\n"
    "v_cvt_pk_bf16_f32 v99, v102, v103\n"
    "v_mfma_f32_16x16x32_bf16 v[128:131], v[40:43], v[192:195], v[128:131]\n"
    "v_cvt_pk_bf16_f32 v112, v112, v113\n"
    "v_cvt_pk_bf16_f32 v113, v114, v115\n"
    "v_mfma_f32_16x16x32_bf16 v[120:123], a[216:219], v[196:199], v[120:123]\n"
    "v_cvt_pk_bf16_f32 v114, v116, v117\n"
    "v_cvt_pk_bf16_f32 v115, v118, v119\n"
    "v_mfma_f32_16x16x32_bf16 v[124:127], a[220:223], v[196:199], v[124:127]\n"
    "ds_write_b128 v243, v[96:99] offset:24640\n"
    "v_cvt_pk_bf16_f32 v104, v104, v105\n"
    "v_cvt_pk_bf16_f32 v128, v128, v129\n"
    "v_cvt_pk_bf16_f32 v129, v130, v131\n"
    "v_cvt_pk_bf16_f32 v105, v106, v107\n"
    "v_cvt_pk_bf16_f32 v106, v108, v109\n"
    "v_cvt_pk_bf16_f32 v107, v110, v111\n"
    "v_mfma_f32_16x16x32_bf16 v[136:139], v[40:43], v[196:199], v[136:139]\n"
    "s_waitcnt lgkmcnt(5)\n"
    "ds_read_u16_d16_hi v206, v206\n"
    "ds_read_u16 v191, v191\n"
    "v_mfma_f32_16x16x32_bf16 v[132:135], v[44:47], v[192:195], v[132:135]\n"
    "ds_read_u16_d16_hi v207, v207\n"
    "v_cvt_pk_f16_f32 v192, v168, v169\n"
    "v_lshlrev_b32_sdwa v193, 1, v170 dst_sel:DWORD dst_unused:UNUSED_PAD src0_sel:DWORD src1_sel:WORD_1\n"
    "v_mfma_f32_16x16x32_bf16 v[140:143], v[44:47], v[196:199], v[140:143]\n"
    "s_waitcnt lgkmcnt(7)\n"
    "ds_read_u16 v193, v193\n"
    "v_lshlrev_b32_sdwa v194, 1, v172 dst_sel:DWORD dst_unused:UNUSED_PAD src0_sel:DWORD src1_sel:WORD_1\n"
    "v_lshlrev_b32_sdwa v195, 1, v174 dst_sel:DWORD dst_unused:UNUSED_PAD src0_sel:DWORD src1_sel:WORD_1\n"
    "v_cvt_pk_f16_f32 v196, v176, v177\n"
    "v_lshlrev_b32_sdwa v197, 1, v178 dst_sel:DWORD dst_unused:UNUSED_PAD src0_sel:DWORD src1_sel:WORD_1\n"
    "v_lshlrev_b32_sdwa v198, 1, v180 dst_sel:DWORD dst_unused:UNUSED_PAD src0_sel:DWORD src1_sel:WORD_1\n"
    "v_lshlrev_b32_sdwa v199, 1, v182 dst_sel:DWORD dst_unused:UNUSED_PAD src0_sel:DWORD src1_sel:WORD_1\n"
    "v_or_b32_e64 v187, v187, v203\n"
    "v_mfma_f32_16x16x32_bf16 v[168:171], a[96:99], a[224:227], 0\n"
    "v_lshlrev_b32_sdwa v203, 1, v175 dst_sel:DWORD dst_unused:UNUSED_PAD src0_sel:DWORD src1_sel:WORD_1\n"
    "v_or_b32_e64 v188, v188, v204\n"
    "v_mfma_f32_16x16x32_bf16 v[168:171], a[144:147], a[228:231], v[168:171]\n"
    "s_waitcnt lgkmcnt(6)\n"
    "v_or_b32_e32 v189, v189, v205\n"
    "v_lshlrev_b32_sdwa v205, 1, v179 dst_sel:DWORD dst_unused:UNUSED_PAD src0_sel:DWORD src1_sel:WORD_1\n"
    "v_mfma_f32_16x16x32_bf16 v[168:171], a[56:59], a[232:235], v[168:171]\n"
    "ds_write_b128 v243, v[112:115] offset:26688\n"
    "v_cvt_pk_bf16_f32 v130, v132, v133\n"
    "v_cvt_pk_bf16_f32 v131, v134, v135\n"
    "v_cvt_pk_bf16_f32 v120, v120, v121\n"
    "v_cvt_pk_bf16_f32 v121, v122, v123\n"
    "v_cvt_pk_bf16_f32 v122, v124, v125\n"
    "v_cvt_pk_bf16_f32 v123, v126, v127\n"
    "v_mfma_f32_16x16x32_bf16 v[172:175], a[100:103], a[224:227], 0\n"
    "ds_read_u16_d16_hi v201, v201\n"
    "v_cvt_pk_bf16_f32 v136, v136, v137\n"
    "v_mfma_f32_16x16x32_bf16 v[172:175], a[148:151], a[228:231], v[172:175]\n"
    "s_waitcnt lgkmcnt(5)\n"
    "ds_read_u16 v194, v194\n"
    "v_or_b32_e32 v190, v190, v206\n"
    "v_mfma_f32_16x16x32_bf16 v[172:175], a[60:63], a[232:235], v[172:175]\n"
    "ds_write_b128 v243, v[128:131] offset:28736\n"
    "v_lshlrev_b32_sdwa v206, 1, v181 dst_sel:DWORD dst_unused:UNUSED_PAD src0_sel:DWORD src1_sel:WORD_1\n"
    "v_pk_fma_f16 v232, v192, v192, s48\n"
    "v_cvt_pk_bf16_f32 v137, v138, v139\n"
    "v_cvt_pk_bf16_f32 v138, v140, v141\n"
    "v_cvt_pk_bf16_f32 v139, v142, v143\n"
    "v_pk_mul_f16 v232, v192, v232\n"
    "v_mfma_f32_16x16x32_bf16 v[176:179], a[96:99], a[236:239], 0\n"
    "s_waitcnt lgkmcnt(5)\n"
    "ds_read_u16_d16_hi v202, v202\n"
    "v_or_b32_e32 v191, v191, v207\n"
    "v_mfma_f32_16x16x32_bf16 v[176:179], a[144:147], a[240:243], v[176:179]\n"
    "v_lshlrev_b32_sdwa v207, 1, v183 dst_sel:DWORD dst_unused:UNUSED_PAD src0_sel:DWORD src1_sel:WORD_1\n"
    "v_lshlrev_b32_sdwa v200, 1, v169 dst_sel:DWORD dst_unused:UNUSED_PAD src0_sel:DWORD src1_sel:WORD_1\n"
    "v_mfma_f32_16x16x32_bf16 v[176:179], a[56:59], a[244:247], v[176:179]\n"
    "ds_read_u16 v195, v195\n"
    "ds_write_b128 v243, v[104:107] offset:25664\n"
    "v_mfma_f32_16x16x32_bf16 v[180:183], a[100:103], a[236:239], 0\n"
    "v_exp_f16_e64 v234, v232\n"
    "v_mfma_f32_16x16x32_bf16 v[180:183], a[148:151], a[240:243], v[180:183]\n"
    "s_waitcnt lgkmcnt(5)\n"
    "v_or_b32_e32 v193, v193, v201\n"
    "v_lshlrev_b32_sdwa v201, 1, v171 dst_sel:DWORD dst_unused:UNUSED_PAD src0_sel:DWORD src1_sel:WORD_1\n"
    "v_mfma_f32_16x16x32_bf16 v[180:183], a[60:63], a[244:247], v[180:183]\n"
    "ds_read_u16_d16_hi v203, v203\n"
    "ds_write_b128 v243, v[120:123] offset:27712\n"
    "v_mfma_f32_16x16x32_bf16 v[96:99], a[0:3], v[184:187], 0\n"
    "v_exp_f16_sdwa v234, v232 dst_sel:WORD_1 dst_unused:UNUSED_PRESERVE src0_sel:WORD_1\n"
    "v_mfma_f32_16x16x32_bf16 v[100:103], a[4:7], v[184:187], 0\n"
    "v_pk_fma_f16 v234, v234, s49, s49\n"
    "v_lshlrev_b32_sdwa v204, 1, v177 dst_sel:DWORD dst_unused:UNUSED_PAD src0_sel:DWORD src1_sel:WORD_1\n"
    "v_mfma_f32_16x16x32_bf16 v[112:115], a[8:11], v[184:187], 0\n"
    "ds_read_u16 v197, v197\n"
    "ds_write_b128 v243, v[136:139] offset:29760\n"
    "v_mfma_f32_16x16x32_bf16 v[116:119], a[12:15], v[184:187], 0\n"
    "s_waitcnt lgkmcnt(4)\n"
    "v_or_b32_e32 v194, v194, v202\n"
    "v_lshlrev_b32_sdwa v202, 1, v173 dst_sel:DWORD dst_unused:UNUSED_PAD src0_sel:DWORD src1_sel:WORD_1\n"
    "v_mfma_f32_16x16x32_bf16 v[128:131], v[0:3], v[184:187], 0\n"
    "v_rcp_f16_e64 v232, v234\n"
    "v_mfma_f32_16x16x32_bf16 v[132:135], v[4:7], v[184:187], 0\n"
    "ds_read_u16_d16_hi v205, v205\n"
    "ds_read_u16 v198, v198\n"
    "v_mfma_f32_16x16x32_bf16 v[104:107], a[0:3], v[188:191], 0\n"
    "ds_read_u16_d16_hi v206, v206\n"
    "ds_read_u16 v199, v199\n"
    "v_mfma_f32_16x16x32_bf16 v[108:111], a[4:7], v[188:191], 0\n"
    "s_waitcnt lgkmcnt(6)\n"
    "v_or_b32_e32 v195, v195, v203\n"
    "v_lshlrev_b32_sdwa v184, 1, v168 dst_sel:DWORD dst_unused:UNUSED_PAD src0_sel:DWORD src1_sel:WORD_1\n"
    "v_mfma_f32_16x16x32_bf16 v[120:123], a[8:11], v[188:191], 0\n"
    "ds_read_u16_d16_hi v207, v207\n"
    "v_lshlrev_b32_sdwa v185, 1, v170 dst_sel:DWORD dst_unused:UNUSED_PAD src0_sel:DWORD src1_sel:WORD_1\n"
    "v_lshlrev_b32_sdwa v186, 1, v172 dst_sel:DWORD dst_unused:UNUSED_PAD src0_sel:DWORD src1_sel:WORD_1\n"
    "v_lshlrev_b32_sdwa v187, 1, v174 dst_sel:DWORD dst_unused:UNUSED_PAD src0_sel:DWORD src1_sel:WORD_1\n"
    "v_lshlrev_b32_sdwa v203, 1, v175 dst_sel:DWORD dst_unused:UNUSED_PAD src0_sel:DWORD src1_sel:WORD_1\n"
    "v_mfma_f32_16x16x32_bf16 v[124:127], a[12:15], v[188:191], 0\n"
    "s_nop 0\n"
    "ds_read_u16 v184, v184\n"
    "v_rcp_f16_sdwa v232, v234 dst_sel:WORD_1 dst_unused:UNUSED_PRESERVE src0_sel:WORD_1\n"
    "v_mfma_f32_16x16x32_bf16 v[136:139], v[0:3], v[188:191], 0\n"
    "s_waitcnt lgkmcnt(6)\n"
    "v_fma_mix_f32 v234, v192, v232, 0 op_sel_hi:[1,1,0]\n"
    "v_fma_mix_f32 v232, v192, v232, 0 op_sel:[1,1,0] op_sel_hi:[1,1,0]\n"
    "v_mfma_f32_16x16x32_bf16 v[140:143], v[4:7], v[188:191], 0\n"
    "s_barrier\n"
    "v_mfma_f32_16x16x32_bf16 v[168:171], a[104:107], a[224:227], 0\n"
    "s_waitcnt lgkmcnt(5)\n"
    "ds_read_u16_d16_hi v200, v200\n"
    "ds_read_u16 v185, v185\n"
    "v_mfma_f32_16x16x32_bf16 v[168:171], a[152:155], a[228:231], v[168:171]\n"
    "ds_read_u16_d16_hi v201, v201\n"
    "ds_read_u16 v186, v186\n"
    "v_mfma_f32_16x16x32_bf16 v[168:171], a[64:67], a[232:235], v[168:171]\n"
    "v_or_b32_e64 v197, v197, v205\n"
    "v_lshlrev_b32_sdwa v188, 1, v176 dst_sel:DWORD dst_unused:UNUSED_PAD src0_sel:DWORD src1_sel:WORD_1\n"
    "v_mfma_f32_16x16x32_bf16 v[172:175], a[108:111], a[224:227], 0\n"
    "s_waitcnt lgkmcnt(7)\n"
    "ds_read_u16_d16_hi v202, v202\n"
    "v_or_b32_e32 v198, v198, v206\n"
    "v_mfma_f32_16x16x32_bf16 v[172:175], a[156:159], a[228:231], v[172:175]\n"
    "v_lshlrev_b32_sdwa v189, 1, v178 dst_sel:DWORD dst_unused:UNUSED_PAD src0_sel:DWORD src1_sel:WORD_1\n"
    "v_lshlrev_b32_sdwa v205, 1, v179 dst_sel:DWORD dst_unused:UNUSED_PAD src0_sel:DWORD src1_sel:WORD_1\n"
    "v_mfma_f32_16x16x32_bf16 v[172:175], a[68:71], a[232:235], v[172:175]\n"
    "ds_read_u16 v187, v187\n"
    "v_lshlrev_b32_sdwa v190, 1, v180 dst_sel:DWORD dst_unused:UNUSED_PAD src0_sel:DWORD src1_sel:WORD_1\n"
    "v_mfma_f32_16x16x32_bf16 v[176:179], a[104:107], a[236:239], 0\n"
    "v_lshlrev_b32_sdwa v206, 1, v181 dst_sel:DWORD dst_unused:UNUSED_PAD src0_sel:DWORD src1_sel:WORD_1\n"
    "v_lshlrev_b32_sdwa v191, 1, v182 dst_sel:DWORD dst_unused:UNUSED_PAD src0_sel:DWORD src1_sel:WORD_1\n"
    "s_waitcnt lgkmcnt(7)\n"
    "v_or_b32_e32 v199, v199, v207\n"
    "v_lshlrev_b32_sdwa v207, 1, v183 dst_sel:DWORD dst_unused:UNUSED_PAD src0_sel:DWORD src1_sel:WORD_1\n"
    "v_cvt_pk_bf16_f32 v192, v234, v232\n"
    "v_mfma_f32_16x16x32_bf16 v[176:179], a[152:155], a[240:243], v[176:179]\n"
    "ds_read_u16_d16_hi v203, v203\n"
    "ds_read_u16 v188, v188\n"
    "v_mfma_f32_16x16x32_bf16 v[176:179], a[64:67], a[244:247], v[176:179]\n"
    "v_pk_fma_f16 v232, v196, v196, s48\n"
    "v_pk_mul_f16 v232, v196, v232\n"
    "v_mfma_f32_16x16x32_bf16 v[180:183], a[108:111], a[236:239], 0\n"
    "s_waitcnt lgkmcnt(7)\n"
    "ds_read_u16_d16_hi v204, v204\n"
    "v_or_b32_e32 v184, v184, v200\n"
    "v_mfma_f32_16x16x32_bf16 v[180:183], a[156:159], a[240:243], v[180:183]\n"
    "v_exp_f16_e64 v234, v232\n"
    "v_mfma_f32_16x16x32_bf16 v[180:183], a[68:71], a[244:247], v[180:183]\n"
    "ds_read_u16 v189, v189\n"
    "v_exp_f16_sdwa v234, v232 dst_sel:WORD_1 dst_unused:UNUSED_PRESERVE src0_sel:WORD_1\n"
    "v_mfma_f32_16x16x32_bf16 v[96:99], a[16:19], v[192:195], v[96:99]\n"
    "v_pk_fma_f16 v234, v234, s49, s49\n"
    "v_rcp_f16_e32 v232, v234\n"
    "v_mfma_f32_16x16x32_bf16 v[100:103], a[20:23], v[192:195], v[100:103]\n"
    "s_waitcnt lgkmcnt(7)\n"
    "ds_read_u16_d16_hi v205, v205\n"
    "v_rcp_f16_sdwa v232, v234 dst_sel:WORD_1 dst_unused:UNUSED_PRESERVE src0_sel:WORD_1\n"
    "v_mfma_f32_16x16x32_bf16 v[112:115], a[24:27], v[192:195], v[112:115]\n"
    "ds_read_u16 v190, v190\n"
    "v_fma_mix_f32 v234, v196, v232, 0 op_sel_hi:[1,1,0]\n"
    "v_mfma_f32_16x16x32_bf16 v[116:119], a[28:31], v[192:195], v[116:119]\n"
    "v_fma_mix_f32 v232, v196, v232, 0 op_sel:[1,1,0] op_sel_hi:[1,1,0]\n"
    "v_or_b32_e32 v185, v185, v201\n"
    "v_mfma_f32_16x16x32_bf16 v[128:131], v[8:11], v[192:195], v[128:131]\n"
    "s_waitcnt lgkmcnt(7)\n"
    "ds_read_u16_d16_hi v206, v206\n"
    "v_cvt_pk_bf16_f32 v196, v234, v232\n"
    "v_mfma_f32_16x16x32_bf16 v[132:135], v[12:15], v[192:195], v[132:135]\n"
    "ds_read_u16 v191, v191\n"
    "v_cvt_pk_f16_f32 v192, v168, v169\n"
    "v_mfma_f32_16x16x32_bf16 v[104:107], a[16:19], v[196:199], v[104:107]\n"
    "v_lshlrev_b32_sdwa v193, 1, v170 dst_sel:DWORD dst_unused:UNUSED_PAD src0_sel:DWORD src1_sel:WORD_1\n"
    "v_lshlrev_b32_sdwa v201, 1, v171 dst_sel:DWORD dst_unused:UNUSED_PAD src0_sel:DWORD src1_sel:WORD_1\n"
    "v_mfma_f32_16x16x32_bf16 v[108:111], a[20:23], v[196:199], v[108:111]\n"
    "s_waitcnt vmcnt(7) lgkmcnt(7)\n"
    "ds_read_u16_d16_hi v207, v207\n"
    "v_lshlrev_b32_sdwa v194, 1, v172 dst_sel:DWORD dst_unused:UNUSED_PAD src0_sel:DWORD src1_sel:WORD_1\n"
    "v_mfma_f32_16x16x32_bf16 v[120:123], a[24:27], v[196:199], v[120:123]\n"
    "v_or_b32_e32 v186, v186, v202\n"
    "v_lshlrev_b32_sdwa v202, 1, v173 dst_sel:DWORD dst_unused:UNUSED_PAD src0_sel:DWORD src1_sel:WORD_1\n"
    "v_mfma_f32_16x16x32_bf16 v[124:127], a[28:31], v[196:199], v[124:127]\n"
    "ds_read_u16 v193, v193\n"
    "v_lshlrev_b32_sdwa v195, 1, v174 dst_sel:DWORD dst_unused:UNUSED_PAD src0_sel:DWORD src1_sel:WORD_1\n"
    "v_mfma_f32_16x16x32_bf16 v[136:139], v[8:11], v[196:199], v[136:139]\n"
    "v_or_b32_e32 v187, v187, v203\n"
    "v_lshlrev_b32_sdwa v203, 1, v175 dst_sel:DWORD dst_unused:UNUSED_PAD src0_sel:DWORD src1_sel:WORD_1\n"
    "v_mfma_f32_16x16x32_bf16 v[140:143], v[12:15], v[196:199], v[140:143]\n"
    "s_waitcnt vmcnt(6) lgkmcnt(7)\n"
    "ds_read_u16_d16_hi v201, v201\n"
    "v_cvt_pk_f16_f32 v196, v176, v177\n"
    "v_smfmac_f32_32x32x32_bf16 v[112:127], a[248:251], v[152:159], v241\n"
    "ds_read_u16 v194, v194\n"
    "v_lshlrev_b32_sdwa v197, 1, v178 dst_sel:DWORD dst_unused:UNUSED_PAD src0_sel:DWORD src1_sel:WORD_1\n"
    "v_lshlrev_b32_sdwa v198, 1, v180 dst_sel:DWORD dst_unused:UNUSED_PAD src0_sel:DWORD src1_sel:WORD_1\n"
    "v_lshlrev_b32_sdwa v199, 1, v182 dst_sel:DWORD dst_unused:UNUSED_PAD src0_sel:DWORD src1_sel:WORD_1\n"
    "v_or_b32_e32 v188, v188, v204\n"
    "v_smfmac_f32_32x32x32_bf16 v[96:111], a[248:251], v[144:151], v241\n"
    "s_waitcnt lgkmcnt(7)\n"
    "ds_read_u16_d16_hi v202, v202\n"
    "v_or_b32_e64 v189, v189, v205\n"
    "v_lshlrev_b32_sdwa v205, 1, v179 dst_sel:DWORD dst_unused:UNUSED_PAD src0_sel:DWORD src1_sel:WORD_1\n"
    "v_pk_fma_f16 v232, v192, v192, s48\n"
    "v_pk_mul_f16 v232, v192, v232\n"
    "v_smfmac_f32_32x32x32_bf16 v[128:143], a[248:251], v[208:215], v241\n"
    "ds_read_u16 v195, v195\n"
    "v_exp_f16_e32 v234, v232\n"
    "v_mfma_f32_16x16x32_bf16 v[168:171], a[112:115], a[224:227], 0\n"
    "s_waitcnt lgkmcnt(7)\n"
    "ds_read_b128 v[152:155], v242 offset:24640\n"
    "ds_read_b128 v[156:159], v242 offset:25664\n"
    "v_mfma_f32_16x16x32_bf16 v[168:171], a[160:163], a[228:231], v[168:171]\n"
    "v_or_b32_e64 v190, v190, v206\n"
    "v_lshlrev_b32_sdwa v206, 1, v181 dst_sel:DWORD dst_unused:UNUSED_PAD src0_sel:DWORD src1_sel:WORD_1\n"
    "v_mfma_f32_16x16x32_bf16 v[168:171], a[72:75], a[232:235], v[168:171]\n"
    "ds_read_u16_d16_hi v203, v203\n"
    "v_exp_f16_sdwa v234, v232 dst_sel:WORD_1 dst_unused:UNUSED_PRESERVE src0_sel:WORD_1\n"
    "v_mfma_f32_16x16x32_bf16 v[172:175], a[116:119], a[224:227], 0\n"
    "s_waitcnt lgkmcnt(8)\n"
    "ds_read_u16 v197, v197\n"
    "v_or_b32_e32 v191, v191, v207\n"
    "v_mfma_f32_16x16x32_bf16 v[172:175], a[164:167], a[228:231], v[172:175]\n"
    "v_lshlrev_b32_sdwa v207, 1, v183 dst_sel:DWORD dst_unused:UNUSED_PAD src0_sel:DWORD src1_sel:WORD_1\n"
    "v_pk_fma_f16 v234, v234, s49, s49\n"
    "v_mfma_f32_16x16x32_bf16 v[172:175], v[52:55], a[232:235], v[172:175]\n"
    "ds_read_u16_d16_hi v205, v205\n"
    "v_lshlrev_b32_sdwa v200, 1, v169 dst_sel:DWORD dst_unused:UNUSED_PAD src0_sel:DWORD src1_sel:WORD_1\n"
    "v_mfma_f32_16x16x32_bf16 v[176:179], a[112:115], a[236:239], 0\n"
    "s_waitcnt lgkmcnt(8)\n"
    "v_or_b32_e32 v193, v193, v201\n"
    "v_lshlrev_b32_sdwa v201, 1, v171 dst_sel:DWORD dst_unused:UNUSED_PAD src0_sel:DWORD src1_sel:WORD_1\n"
    "v_mfma_f32_16x16x32_bf16 v[176:179], a[160:163], a[240:243], v[176:179]\n"
    "ds_read_u16 v198, v198\n"
    "v_mfma_f32_16x16x32_bf16 v[176:179], a[72:75], a[244:247], v[176:179]\n"
    "ds_read_u16_d16_hi v206, v206\n"
    "v_rcp_f16_e32 v232, v234\n"
    "v_mfma_f32_16x16x32_bf16 v[180:183], a[116:119], a[236:239], 0\n"
    "v_rcp_f16_sdwa v232, v234 dst_sel:WORD_1 dst_unused:UNUSED_PRESERVE src0_sel:WORD_1\n"
    "v_mfma_f32_16x16x32_bf16 v[180:183], a[164:167], a[240:243], v[180:183]\n"
    "s_waitcnt lgkmcnt(8)\n"
    "ds_read_u16 v199, v199\n"
    "v_or_b32_e32 v194, v194, v202\n"
    "v_mfma_f32_16x16x32_bf16 v[180:183], v[52:55], a[244:247], v[180:183]\n"
    "v_lshlrev_b32_sdwa v202, 1, v173 dst_sel:DWORD dst_unused:UNUSED_PAD src0_sel:DWORD src1_sel:WORD_1\n"
    "v_lshlrev_b32_sdwa v204, 1, v177 dst_sel:DWORD dst_unused:UNUSED_PAD src0_sel:DWORD src1_sel:WORD_1\n"
    "v_mfma_f32_16x16x32_bf16 v[112:115], a[40:43], v[184:187], v[112:115]\n"
    "s_waitcnt lgkmcnt(6)\n"
    "ds_read_u16_d16_hi v207, v207\n"
    "v_fma_mix_f32 v234, v192, v232, 0 op_sel_hi:[1,1,0]\n"
    "v_smfmac_f32_32x32x32_bf16 v[96:111], a[248:251], v[152:159], v241\n"
    "ds_read_b128 v[152:155], v242 offset:26688\n"
    "v_fma_mix_f32 v232, v192, v232, 0 op_sel:[1,1,0] op_sel_hi:[1,1,0]\n"
    "v_cvt_pk_bf16_f32 v192, v234, v232\n"
    "v_pk_fma_f16 v232, v196, v196, s48\n"
    "v_pk_mul_f16 v232, v196, v232\n"
    "v_mfma_f32_16x16x32_bf16 v[116:119], a[44:47], v[184:187], v[116:119]\n"
    "s_waitcnt lgkmcnt(7)\n"
    "ds_read_b128 v[156:159], v242 offset:27712\n"
    "ds_read_u16_d16_hi v200, v200\n"
    "v_mfma_f32_16x16x32_bf16 v[128:131], v[16:19], v[184:187], v[128:131]\n"
    "v_or_b32_e32 v195, v195, v203\n"
    "v_lshlrev_b32_sdwa v203, 1, v175 dst_sel:DWORD dst_unused:UNUSED_PAD src0_sel:DWORD src1_sel:WORD_1\n"
    "v_mfma_f32_16x16x32_bf16 v[96:99], a[32:35], v[184:187], v[96:99]\n"
    "ds_read_u16_d16_hi v201, v201\n"
    "v_exp_f16_e64 v234, v232\n"
    "v_mfma_f32_16x16x32_bf16 v[100:103], a[36:39], v[184:187], v[100:103]\n"
    "s_waitcnt lgkmcnt(8)\n"
    "v_or_b32_e32 v197, v197, v205\n"
    "v_lshlrev_b32_sdwa v205, 1, v179 dst_sel:DWORD dst_unused:UNUSED_PAD src0_sel:DWORD src1_sel:WORD_1\n"
    "v_mfma_f32_16x16x32_bf16 v[132:135], v[20:23], v[184:187], v[132:135]\n"
    "ds_read_u16_d16_hi v202, v202\n"
    "v_lshlrev_b32_sdwa v184, 1, v168 dst_sel:DWORD dst_unused:UNUSED_PAD src0_sel:DWORD src1_sel:WORD_1\n"
    "v_mfma_f32_16x16x32_bf16 v[104:107], a[32:35], v[188:191], v[104:107]\n"
    "ds_read_u16 v184, v184\n"
    "v_lshlrev_b32_sdwa v185, 1, v170 dst_sel:DWORD dst_unused:UNUSED_PAD src0_sel:DWORD src1_sel:WORD_1\n"
    "v_mfma_f32_16x16x32_bf16 v[108:111], a[36:39], v[188:191], v[108:111]\n"
    "v_lshlrev_b32_sdwa v186, 1, v172 dst_sel:DWORD dst_unused:UNUSED_PAD src0_sel:DWORD src1_sel:WORD_1\n"
    "v_lshlrev_b32_sdwa v187, 1, v174 dst_sel:DWORD dst_unused:UNUSED_PAD src0_sel:DWORD src1_sel:WORD_1\n"
    "v_mfma_f32_16x16x32_bf16 v[120:123], a[40:43], v[188:191], v[120:123]\n"
    "s_waitcnt lgkmcnt(8)\n"
    "ds_read_u16 v185, v185\n"
    "v_or_b32_e32 v198, v198, v206\n"
    "v_mfma_f32_16x16x32_bf16 v[124:127], a[44:47], v[188:191], v[124:127]\n"
    "v_lshlrev_b32_sdwa v206, 1, v181 dst_sel:DWORD dst_unused:UNUSED_PAD src0_sel:DWORD src1_sel:WORD_1\n"
    "v_exp_f16_sdwa v234, v232 dst_sel:WORD_1 dst_unused:UNUSED_PRESERVE src0_sel:WORD_1\n"
    "v_mfma_f32_16x16x32_bf16 v[136:139], v[16:19], v[188:191], v[136:139]\n"
    "ds_read_u16 v186, v186\n"
    "v_pk_fma_f16 v234, v234, s49, s49\n"
    "v_mfma_f32_16x16x32_bf16 v[140:143], v[20:23], v[188:191], v[140:143]\n"
    "s_waitcnt lgkmcnt(7)\n"
    "ds_read_u16 v187, v187\n"
    "v_lshlrev_b32_sdwa v188, 1, v176 dst_sel:DWORD dst_unused:UNUSED_PAD src0_sel:DWORD src1_sel:WORD_1\n"
    "v_lshlrev_b32_sdwa v189, 1, v178 dst_sel:DWORD dst_unused:UNUSED_PAD src0_sel:DWORD src1_sel:WORD_1\n"
    "v_mfma_f32_16x16x32_bf16 v[168:171], a[120:123], a[224:227], 0\n"
    "s_waitcnt lgkmcnt(7)\n"
    "v_lshlrev_b32_sdwa v190, 1, v180 dst_sel:DWORD dst_unused:UNUSED_PAD src0_sel:DWORD src1_sel:WORD_1\n"
    "v_lshlrev_b32_sdwa v191, 1, v182 dst_sel:DWORD dst_unused:UNUSED_PAD src0_sel:DWORD src1_sel:WORD_1\n"
    "v_mfma_f32_16x16x32_bf16 v[168:171], a[168:171], a[228:231], v[168:171]\n"
    "ds_read_u16_d16_hi v203, v203\n"
    "v_or_b32_e64 v199, v199, v207\n"
    "v_mfma_f32_16x16x32_bf16 v[168:171], a[76:79], a[232:235], v[168:171]\n"
    "v_lshlrev_b32_sdwa v207, 1, v183 dst_sel:DWORD dst_unused:UNUSED_PAD src0_sel:DWORD src1_sel:WORD_1\n"
    "v_rcp_f16_e64 v232, v234\n"
    "v_smfmac_f32_32x32x32_bf16 v[112:127], a[248:251], v[152:159], v241\n"
    "ds_read_u16 v188, v188\n"
    "v_rcp_f16_sdwa v232, v234 dst_sel:WORD_1 dst_unused:UNUSED_PRESERVE src0_sel:WORD_1\n"
    "ds_read_u16_d16_hi v204, v204\n"
    "v_mfma_f32_16x16x32_bf16 v[172:175], a[124:127], a[224:227], 0\n"
    "v_fma_mix_f32 v234, v196, v232, 0 op_sel_hi:[1,1,0]\n"
    "v_fma_mix_f32 v232, v196, v232, 0 op_sel:[1,1,0] op_sel_hi:[1,1,0]\n"
    "v_mfma_f32_16x16x32_bf16 v[172:175], a[172:175], a[228:231], v[172:175]\n"
    "ds_read_u16 v189, v189\n"
    "v_cvt_pk_bf16_f32 v196, v234, v232\n"
    "v_mfma_f32_16x16x32_bf16 v[172:175], v[60:63], a[232:235], v[172:175]\n"
    "s_waitcnt lgkmcnt(7)\n"
    "ds_read_b128 v[152:155], v242 offset:28736\n"
    "v_or_b32_e32 v184, v184, v200\n"
    "v_mfma_f32_16x16x32_bf16 v[176:179], a[120:123], a[236:239], 0\n"
    "ds_read_b128 v[156:159], v242 offset:29760\n"
    "v_mfma_f32_16x16x32_bf16 v[176:179], a[168:171], a[240:243], v[176:179]\n"
    "s_waitcnt lgkmcnt(8)\n"
    "ds_read_u16_d16_hi v205, v205\n"
    "v_or_b32_e32 v185, v185, v201\n"
    "v_mfma_f32_16x16x32_bf16 v[176:179], a[76:79], a[244:247], v[176:179]\n"
    "v_lshlrev_b32_sdwa v201, 1, v171 dst_sel:DWORD dst_unused:UNUSED_PAD src0_sel:DWORD src1_sel:WORD_1\n"
    "v_mfma_f32_16x16x32_bf16 v[180:183], a[124:127], a[236:239], 0\n"
    "s_waitcnt lgkmcnt(8)\n"
    "ds_read_u16 v190, v190\n"
    "v_or_b32_e32 v186, v186, v202\n"
    "v_mfma_f32_16x16x32_bf16 v[180:183], a[172:175], a[240:243], v[180:183]\n"
    "v_lshlrev_b32_sdwa v202, 1, v173 dst_sel:DWORD dst_unused:UNUSED_PAD src0_sel:DWORD src1_sel:WORD_1\n"
    "v_mfma_f32_16x16x32_bf16 v[180:183], v[60:63], a[244:247], v[180:183]\n"
    "ds_read_b128 a[224:227], v242 offset:0\n"
    "v_mfma_f32_16x16x32_bf16 v[96:99], a[176:179], v[192:195], v[96:99]\n"
    "s_waitcnt lgkmcnt(8)\n"
    "ds_read_u16_d16_hi v206, v206\n"
    "v_or_b32_e32 v187, v187, v203\n"
    "v_mfma_f32_16x16x32_bf16 v[100:103], a[180:183], v[192:195], v[100:103]\n"
    "v_lshlrev_b32_sdwa v203, 1, v175 dst_sel:DWORD dst_unused:UNUSED_PAD src0_sel:DWORD src1_sel:WORD_1\n"
    "ds_read_b128 a[228:231], v242 offset:1024\n"
    "v_mfma_f32_16x16x32_bf16 v[112:115], a[184:187], v[192:195], v[112:115]\n"
    "ds_read_u16 v191, v191\n"
    "v_mfma_f32_16x16x32_bf16 v[116:119], a[188:191], v[192:195], v[116:119]\n"
    "s_waitcnt lgkmcnt(9)\n"
    "v_or_b32_e32 v188, v188, v204\n"
    "v_mfma_f32_16x16x32_bf16 v[128:131], v[24:27], v[192:195], v[128:131]\n"
    "ds_read_b128 a[232:235], v242 offset:2048\n"
    "v_mfma_f32_16x16x32_bf16 v[132:135], v[28:31], v[192:195], v[132:135]\n"
    "ds_read_u16_d16_hi v207, v207\n"
    "v_cvt_pk_f16_f32 v192, v168, v169\n"
    "v_mfma_f32_16x16x32_bf16 v[104:107], a[176:179], v[196:199], v[104:107]\n"
    "v_lshlrev_b32_sdwa v193, 1, v170 dst_sel:DWORD dst_unused:UNUSED_PAD src0_sel:DWORD src1_sel:WORD_1\n"
    "v_lshlrev_b32_sdwa v194, 1, v172 dst_sel:DWORD dst_unused:UNUSED_PAD src0_sel:DWORD src1_sel:WORD_1\n"
    "v_mfma_f32_16x16x32_bf16 v[108:111], a[180:183], v[196:199], v[108:111]\n"
    "ds_read_b128 a[236:239], v242 offset:3072\n"
    "ds_read_b128 a[240:243], v242 offset:4096\n"
    "v_lshlrev_b32_sdwa v195, 1, v174 dst_sel:DWORD dst_unused:UNUSED_PAD src0_sel:DWORD src1_sel:WORD_1\n"
    "v_mfma_f32_16x16x32_bf16 v[120:123], a[184:187], v[196:199], v[120:123]\n"
    "ds_read_b128 a[244:247], v242 offset:5120\n"
    "v_pk_fma_f16 v232, v192, v192, s48\n"
    "v_mfma_f32_16x16x32_bf16 v[124:127], a[188:191], v[196:199], v[124:127]\n"
    "s_waitcnt lgkmcnt(10)\n"
    "ds_read_u16 v193, v193\n"
    "v_or_b32_e32 v189, v189, v205\n"
    "v_lshlrev_b32_sdwa v205, 1, v179 dst_sel:DWORD dst_unused:UNUSED_PAD src0_sel:DWORD src1_sel:WORD_1\n"
    "v_pk_mul_f16 v232, v192, v232\n"
    "v_exp_f16_e32 v234, v232\n"
    "v_mfma_f32_16x16x32_bf16 v[136:139], v[24:27], v[196:199], v[136:139]\n"
    "s_waitcnt lgkmcnt(9)\n"
    "ds_read_u16_d16_hi v201, v201\n"
    "v_exp_f16_sdwa v234, v232 dst_sel:WORD_1 dst_unused:UNUSED_PRESERVE src0_sel:WORD_1\n"
    "v_mfma_f32_16x16x32_bf16 v[140:143], v[28:31], v[196:199], v[140:143]\n"
    "s_nop 0\n"
    "v_cvt_pk_f16_f32 v196, v176, v177\n"
    "v_lshlrev_b32_sdwa v197, 1, v178 dst_sel:DWORD dst_unused:UNUSED_PAD src0_sel:DWORD src1_sel:WORD_1\n"
    "v_mfma_f32_16x16x32_bf16 v[168:171], a[80:83], a[224:227], 0\n"
    "s_waitcnt lgkmcnt(8)\n"
    "ds_read_u16 v194, v194\n"
    "v_lshlrev_b32_sdwa v198, 1, v180 dst_sel:DWORD dst_unused:UNUSED_PAD src0_sel:DWORD src1_sel:WORD_1\n"
    "v_mfma_f32_16x16x32_bf16 v[168:171], a[128:131], a[228:231], v[168:171]\n"
    "ds_read_u16_d16_hi v202, v202\n"
    "v_lshlrev_b32_sdwa v199, 1, v182 dst_sel:DWORD dst_unused:UNUSED_PAD src0_sel:DWORD src1_sel:WORD_1\n"
    "v_or_b32_e32 v190, v190, v206\n"
    "v_smfmac_f32_32x32x32_bf16 v[128:143], a[248:251], v[152:159], v241\n"
    "s_waitcnt lgkmcnt(8)\n"
    "v_lshlrev_b32_sdwa v206, 1, v181 dst_sel:DWORD dst_unused:UNUSED_PAD src0_sel:DWORD src1_sel:WORD_1\n"
    "v_pk_fma_f16 v234, v234, s49, s49\n"
    "v_rcp_f16_e64 v232, v234\n"
    "ds_read_u16 v195, v195\n"
    "v_mfma_f32_16x16x32_bf16 v[168:171], a[48:51], a[232:235], v[168:171]\n"
    "s_waitcnt lgkmcnt(7)\n"
    "ds_read_u16_d16_hi v203, v203\n"
    "v_or_b32_e32 v191, v191, v207\n"
    "v_mfma_f32_16x16x32_bf16 v[172:175], a[84:87], a[224:227], 0\n"
    "v_lshlrev_b32_sdwa v207, 1, v183 dst_sel:DWORD dst_unused:UNUSED_PAD src0_sel:DWORD src1_sel:WORD_1\n"
    "v_mfma_f32_16x16x32_bf16 v[172:175], a[132:135], a[228:231], v[172:175]\n"
    "ds_read_u16 v197, v197\n"
    "v_mfma_f32_16x16x32_bf16 v[172:175], v[48:51], a[232:235], v[172:175]\n"
    "s_nop 0\n"
    "v_lshlrev_b32_sdwa v200, 1, v169 dst_sel:DWORD dst_unused:UNUSED_PAD src0_sel:DWORD src1_sel:WORD_1\n"
    "v_rcp_f16_sdwa v232, v234 dst_sel:WORD_1 dst_unused:UNUSED_PRESERVE src0_sel:WORD_1\n"
    "v_mfma_f32_16x16x32_bf16 v[176:179], a[80:83], a[236:239], 0\n"
    "s_waitcnt lgkmcnt(7)\n"
    "ds_read_u16_d16_hi v205, v205\n"
    "v_fma_mix_f32 v234, v192, v232, 0 op_sel_hi:[1,1,0]\n"
    "v_mfma_f32_16x16x32_bf16 v[176:179], a[128:131], a[240:243], v[176:179]\n"
    "ds_read_u16 v198, v198\n"
    "v_fma_mix_f32 v232, v192, v232, 0 op_sel:[1,1,0] op_sel_hi:[1,1,0]\n"
    "v_mfma_f32_16x16x32_bf16 v[176:179], a[48:51], a[244:247], v[176:179]\n"
    "v_cvt_pk_bf16_f32 v192, v234, v232\n"
    "v_pk_fma_f16 v232, v196, v196, s48\n"
    "v_mfma_f32_16x16x32_bf16 v[180:183], a[84:87], a[236:239], 0\n"
    "s_waitcnt lgkmcnt(7)\n"
    "ds_read_u16_d16_hi v206, v206\n"
    "v_or_b32_e32 v193, v193, v201\n"
    "v_mfma_f32_16x16x32_bf16 v[180:183], a[132:135], a[240:243], v[180:183]\n"
    "v_lshlrev_b32_sdwa v201, 1, v171 dst_sel:DWORD dst_unused:UNUSED_PAD src0_sel:DWORD src1_sel:WORD_1\n"
    "v_lshlrev_b32_sdwa v204, 1, v177 dst_sel:DWORD dst_unused:UNUSED_PAD src0_sel:DWORD src1_sel:WORD_1\n"
    "v_mfma_f32_16x16x32_bf16 v[180:183], v[48:51], a[244:247], v[180:183]\n"
    "ds_read_u16 v199, v199\n"
    "v_pk_mul_f16 v232, v196, v232\n"
    "v_mfma_f32_16x16x32_bf16 v[96:99], a[192:195], v[184:187], v[96:99]\n"
    "s_waitcnt lgkmcnt(7)\n"
    "ds_read_u16_d16_hi v207, v207\n"
    "v_or_b32_e32 v194, v194, v202\n"
    "v_mfma_f32_16x16x32_bf16 v[100:103], a[196:199], v[184:187], v[100:103]\n"
    "v_lshlrev_b32_sdwa v202, 1, v173 dst_sel:DWORD dst_unused:UNUSED_PAD src0_sel:DWORD src1_sel:WORD_1\n"
    "v_exp_f16_e64 v234, v232\n"
    "v_mfma_f32_16x16x32_bf16 v[112:115], a[200:203], v[184:187], v[112:115]\n"
    "ds_read_u16_d16_hi v200, v200\n"
    "v_exp_f16_sdwa v234, v232 dst_sel:WORD_1 dst_unused:UNUSED_PRESERVE src0_sel:WORD_1\n"
    "v_mfma_f32_16x16x32_bf16 v[116:119], a[204:207], v[184:187], v[116:119]\n"
    "s_waitcnt lgkmcnt(7)\n"
    "ds_read_u16_d16_hi v201, v201\n"
    "v_or_b32_e32 v195, v195, v203\n"
    "v_mfma_f32_16x16x32_bf16 v[128:131], v[32:35], v[184:187], v[128:131]\n"
    "v_lshlrev_b32_sdwa v203, 1, v175 dst_sel:DWORD dst_unused:UNUSED_PAD src0_sel:DWORD src1_sel:WORD_1\n"
    "v_pk_fma_f16 v234, v234, s49, s49\n"
    "v_mfma_f32_16x16x32_bf16 v[132:135], v[36:39], v[184:187], v[132:135]\n"
    "ds_read_u16_d16_hi v202, v202\n"
    "v_lshlrev_b32_sdwa v184, 1, v168 dst_sel:DWORD dst_unused:UNUSED_PAD src0_sel:DWORD src1_sel:WORD_1\n"
    "v_mfma_f32_16x16x32_bf16 v[104:107], a[192:195], v[188:191], v[104:107]\n"
    "v_lshlrev_b32_sdwa v185, 1, v170 dst_sel:DWORD dst_unused:UNUSED_PAD src0_sel:DWORD src1_sel:WORD_1\n"
    "v_lshlrev_b32_sdwa v186, 1, v172 dst_sel:DWORD dst_unused:UNUSED_PAD src0_sel:DWORD src1_sel:WORD_1\n"
    "v_mfma_f32_16x16x32_bf16 v[108:111], a[196:199], v[188:191], v[108:111]\n"
    "s_waitcnt lgkmcnt(7)\n"
    "ds_read_u16 v184, v184\n"
    "v_or_b32_e32 v197, v197, v205\n"
    "v_mfma_f32_16x16x32_bf16 v[120:123], a[200:203], v[188:191], v[120:123]\n"
    "v_lshlrev_b32_sdwa v187, 1, v174 dst_sel:DWORD dst_unused:UNUSED_PAD src0_sel:DWORD src1_sel:WORD_1\n"
    "v_lshlrev_b32_sdwa v205, 1, v179 dst_sel:DWORD dst_unused:UNUSED_PAD src0_sel:DWORD src1_sel:WORD_1\n"
    "v_mfma_f32_16x16x32_bf16 v[124:127], a[204:207], v[188:191], v[124:127]\n"
    "ds_read_u16 v185, v185\n"
    "v_rcp_f16_e32 v232, v234\n"
    "v_mfma_f32_16x16x32_bf16 v[136:139], v[32:35], v[188:191], v[136:139]\n"
    "s_waitcnt lgkmcnt(7)\n"
    "ds_read_u16 v186, v186\n"
    "v_or_b32_e64 v198, v198, v206\n"
    "v_mfma_f32_16x16x32_bf16 v[140:143], v[36:39], v[188:191], v[140:143]\n"
    "v_lshlrev_b32_sdwa v188, 1, v176 dst_sel:DWORD dst_unused:UNUSED_PAD src0_sel:DWORD src1_sel:WORD_1\n"
    "v_lshlrev_b32_sdwa v189, 1, v178 dst_sel:DWORD dst_unused:UNUSED_PAD src0_sel:DWORD src1_sel:WORD_1\n"
    "v_mfma_f32_16x16x32_bf16 v[168:171], a[88:91], a[224:227], 0\n"
    "ds_read_u16 v187, v187\n"
    "v_lshlrev_b32_sdwa v190, 1, v180 dst_sel:DWORD dst_unused:UNUSED_PAD src0_sel:DWORD src1_sel:WORD_1\n"
    "v_mfma_f32_16x16x32_bf16 v[168:171], a[136:139], a[228:231], v[168:171]\n"
    "v_lshlrev_b32_sdwa v206, 1, v181 dst_sel:DWORD dst_unused:UNUSED_PAD src0_sel:DWORD src1_sel:WORD_1\n"
    "v_lshlrev_b32_sdwa v191, 1, v182 dst_sel:DWORD dst_unused:UNUSED_PAD src0_sel:DWORD src1_sel:WORD_1\n"
    "v_mfma_f32_16x16x32_bf16 v[168:171], a[52:55], a[232:235], v[168:171]\n"
    "s_waitcnt lgkmcnt(7)\n"
    "ds_read_u16_d16_hi v203, v203\n"
    "v_or_b32_e32 v199, v199, v207\n"
    "v_mfma_f32_16x16x32_bf16 v[172:175], a[92:95], a[224:227], 0\n"
    "ds_read_u16 v188, v188\n"
    "v_lshlrev_b32_sdwa v207, 1, v183 dst_sel:DWORD dst_unused:UNUSED_PAD src0_sel:DWORD src1_sel:WORD_1\n"
    "v_mfma_f32_16x16x32_bf16 v[172:175], a[140:143], a[228:231], v[172:175]\n"
    "v_rcp_f16_sdwa v232, v234 dst_sel:WORD_1 dst_unused:UNUSED_PRESERVE src0_sel:WORD_1\n"
    "v_mfma_f32_16x16x32_bf16 v[172:175], v[56:59], a[232:235], v[172:175]\n"
    "ds_read_u16_d16_hi v204, v204\n"
    "v_fma_mix_f32 v234, v196, v232, 0 op_sel_hi:[1,1,0]\n"
    "v_mfma_f32_16x16x32_bf16 v[176:179], a[88:91], a[236:239], 0\n"
    "v_fma_mix_f32 v232, v196, v232, 0 op_sel:[1,1,0] op_sel_hi:[1,1,0]\n"
    "v_cvt_pk_bf16_f32 v196, v234, v232\n"
    "v_mfma_f32_16x16x32_bf16 v[176:179], a[136:139], a[240:243], v[176:179]\n"
    "ds_read_u16 v189, v189\n"
    "v_mfma_f32_16x16x32_bf16 v[176:179], a[52:55], a[244:247], v[176:179]\n"
    "s_nop 0\n"
    "v_mfma_f32_16x16x32_bf16 v[180:183], a[92:95], a[236:239], 0\n"
    "s_waitcnt lgkmcnt(7)\n"
    "ds_read_u16_d16_hi v205, v205\n"
    "v_or_b32_e32 v184, v184, v200\n"
    "v_mfma_f32_16x16x32_bf16 v[180:183], a[140:143], a[240:243], v[180:183]\n"
    "s_waitcnt lgkmcnt(7)\n"
    "ds_read_u16 v190, v190\n"
    "v_or_b32_e32 v185, v185, v201\n"
    "v_mfma_f32_16x16x32_bf16 v[180:183], v[56:59], a[244:247], v[180:183]\n"
    "s_barrier\n"
    "v_mfma_f32_16x16x32_bf16 v[96:99], a[208:211], v[192:195], v[96:99]\n"
    "s_waitcnt lgkmcnt(6)\n"
    "ds_read_u16_d16_hi v206, v206\n"
    "ds_read_u16 v191, v191\n"
    "v_mfma_f32_16x16x32_bf16 v[100:103], a[212:215], v[192:195], v[100:103]\n"
    "s_waitcnt lgkmcnt(7)\n"
    "ds_read_u16_d16_hi v207, v207\n"
    "v_lshlrev_b32_sdwa v201, 1, v171 dst_sel:DWORD dst_unused:UNUSED_PAD src0_sel:DWORD src1_sel:WORD_1\n"
    "v_mfma_f32_16x16x32_bf16 v[112:115], a[216:219], v[192:195], v[112:115]\n"
    "v_or_b32_e64 v186, v186, v202\n"
    "v_lshlrev_b32_sdwa v202, 1, v173 dst_sel:DWORD dst_unused:UNUSED_PAD src0_sel:DWORD src1_sel:WORD_1\n"
    "v_mfma_f32_16x16x32_bf16 v[116:119], a[220:223], v[192:195], v[116:119]\n"
    "ds_read_u16_d16_hi v201, v201\n"
    "v_or_b32_e32 v187, v187, v203\n"
    "v_mfma_f32_16x16x32_bf16 v[128:131], v[40:43], v[192:195], v[128:131]\n"
    "v_lshlrev_b32_sdwa v203, 1, v175 dst_sel:DWORD dst_unused:UNUSED_PAD src0_sel:DWORD src1_sel:WORD_1\n"
    "v_cvt_pk_bf16_f32 v96, v96, v97\n"
    "v_mfma_f32_16x16x32_bf16 v[132:135], v[44:47], v[192:195], v[132:135]\n"
    "s_waitcnt lgkmcnt(7)\n"
    "buffer_load_dwordx4 v[152:155], v245, s[40:43], s58 offen offset:64\n"
    "buffer_load_dwordx4 v[144:147], v245, s[40:43], s58 offen offset:0\n"
    "buffer_load_dwordx4 v[208:211], v245, s[40:43], s58 offen offset:128\n"
    "ds_read_u16_d16_hi v202, v202\n"
    "v_mfma_f32_16x16x32_bf16 v[104:107], a[208:211], v[196:199], v[104:107]\n"
    "ds_read_u16_d16_hi v203, v203\n"
    "v_cvt_pk_f16_f32 v192, v168, v169\n"
    "v_mfma_f32_16x16x32_bf16 v[108:111], a[212:215], v[196:199], v[108:111]\n"
    "s_nop 0\n"
    "v_lshlrev_b32_sdwa v193, 1, v170 dst_sel:DWORD dst_unused:UNUSED_PAD src0_sel:DWORD src1_sel:WORD_1\n"
    "v_lshlrev_b32_sdwa v194, 1, v172 dst_sel:DWORD dst_unused:UNUSED_PAD src0_sel:DWORD src1_sel:WORD_1\n"
    "v_mfma_f32_16x16x32_bf16 v[120:123], a[216:219], v[196:199], v[120:123]\n"
    "s_waitcnt lgkmcnt(7)\n"
    "ds_read_u16 v193, v193\n"
    "v_lshlrev_b32_sdwa v195, 1, v174 dst_sel:DWORD dst_unused:UNUSED_PAD src0_sel:DWORD src1_sel:WORD_1\n"
    "v_mfma_f32_16x16x32_bf16 v[124:127], a[220:223], v[196:199], v[124:127]\n"
    "ds_read_u16 v194, v194\n"
    "v_or_b32_e64 v188, v188, v204\n"
    "v_mfma_f32_16x16x32_bf16 v[136:139], v[40:43], v[196:199], v[136:139]\n"
    "v_or_b32_e32 v189, v189, v205\n"
    "v_lshlrev_b32_sdwa v205, 1, v179 dst_sel:DWORD dst_unused:UNUSED_PAD src0_sel:DWORD src1_sel:WORD_1\n"
    "v_mfma_f32_16x16x32_bf16 v[140:143], v[44:47], v[196:199], v[140:143]\n"
    "s_waitcnt lgkmcnt(7)\n"
    "ds_read_u16 v195, v195\n"
    "v_cvt_pk_f16_f32 v196, v176, v177\n"
    "v_mfma_f32_16x16x32_bf16 v[168:171], a[96:99], a[224:227], 0\n"
    "v_lshlrev_b32_sdwa v197, 1, v178 dst_sel:DWORD dst_unused:UNUSED_PAD src0_sel:DWORD src1_sel:WORD_1\n"
    "v_lshlrev_b32_sdwa v198, 1, v180 dst_sel:DWORD dst_unused:UNUSED_PAD src0_sel:DWORD src1_sel:WORD_1\n"
    "v_mfma_f32_16x16x32_bf16 v[168:171], a[144:147], a[228:231], v[168:171]\n"
    "ds_read_u16 v197, v197\n"
    "v_lshlrev_b32_sdwa v199, 1, v182 dst_sel:DWORD dst_unused:UNUSED_PAD src0_sel:DWORD src1_sel:WORD_1\n"
    "v_mfma_f32_16x16x32_bf16 v[168:171], a[56:59], a[232:235], v[168:171]\n"
    "s_nop 0\n"
    "v_cvt_pk_bf16_f32 v97, v98, v99\n"
    "v_cvt_pk_bf16_f32 v98, v100, v101\n"
    "v_mfma_f32_16x16x32_bf16 v[172:175], a[100:103], a[224:227], 0\n"
    "s_waitcnt lgkmcnt(7)\n"
    "ds_read_u16_d16_hi v205, v205\n"
    "v_cvt_pk_bf16_f32 v99, v102, v103\n"
    "v_mfma_f32_16x16x32_bf16 v[172:175], a[148:151], a[228:231], v[172:175]\n"
    "ds_read_u16 v198, v198\n"
    "v_or_b32_e64 v190, v190, v206\n"
    "v_mfma_f32_16x16x32_bf16 v[172:175], a[60:63], a[232:235], v[172:175]\n"
    "buffer_load_dwordx4 v[156:159], v245, s[40:43], s89 offen offset:64\n"
    "buffer_load_dwordx4 v[148:151], v245, s[40:43], s89 offen offset:0\n"
    "buffer_load_dwordx4 v[212:215], v245, s[40:43], s89 offen offset:128\n"
    "ds_read_u16 v199, v199\n"
    "v_mfma_f32_16x16x32_bf16 v[176:179], a[96:99], a[236:239], 0\n"
    "v_lshlrev_b32_sdwa v206, 1, v181 dst_sel:DWORD dst_unused:UNUSED_PAD src0_sel:DWORD src1_sel:WORD_1\n"
    "v_cvt_pk_bf16_f32 v224, v112, v113\n"
    "v_mfma_f32_16x16x32_bf16 v[176:179], a[144:147], a[240:243], v[176:179]\n"
    "ds_read_u16_d16_hi v206, v206\n"
    "v_cvt_pk_bf16_f32 v225, v114, v115\n"
    "v_mfma_f32_16x16x32_bf16 v[176:179], a[56:59], a[244:247], v[176:179]\n"
    "s_nop 0\n"
    "buffer_store_dwordx4 v[96:99], v245, s[44:47], s57 offen offset:0 sc1\n"
    "v_mfma_f32_16x16x32_bf16 v[100:103], a[4:7], v[184:187], 0\n"
    "s_waitcnt lgkmcnt(7)\n"
    "v_or_b32_e32 v193, v193, v201\n"
    "v_cvt_pk_bf16_f32 v226, v116, v117\n"
    "v_mfma_f32_16x16x32_bf16 v[96:99], a[0:3], v[184:187], 0\n"
    "s_waitcnt lgkmcnt(6)\n"
    "v_cvt_pk_bf16_f32 v227, v118, v119\n"
    "v_cvt_pk_bf16_f32 v228, v128, v129\n"
    "v_mfma_f32_16x16x32_bf16 v[112:115], a[8:11], v[184:187], 0\n"
    "v_cvt_pk_bf16_f32 v229, v130, v131\n"
    "v_or_b32_e32 v191, v191, v207\n"
    "v_mfma_f32_16x16x32_bf16 v[116:119], a[12:15], v[184:187], 0\n"
    "s_waitcnt lgkmcnt(5)\n"
    "v_lshlrev_b32_sdwa v207, 1, v183 dst_sel:DWORD dst_unused:UNUSED_PAD src0_sel:DWORD src1_sel:WORD_1\n"
    "v_cvt_pk_bf16_f32 v230, v132, v133\n"
    "v_mfma_f32_16x16x32_bf16 v[180:183], a[100:103], a[236:239], 0\n"
    "ds_read_u16_d16_hi v207, v207\n"
    "v_cvt_pk_bf16_f32 v231, v134, v135\n"
    "v_mfma_f32_16x16x32_bf16 v[180:183], a[148:151], a[240:243], v[180:183]\n"
    "v_or_b32_e32 v194, v194, v202\n"
    "v_cvt_pk_bf16_f32 v216, v104, v105\n"
    "v_mfma_f32_16x16x32_bf16 v[180:183], a[60:63], a[244:247], v[180:183]\n"
    "s_waitcnt lgkmcnt(4)\n"
    "v_cvt_pk_bf16_f32 v217, v106, v107\n"
    "v_cvt_pk_bf16_f32 v218, v108, v109\n"
    "v_mfma_f32_16x16x32_bf16 v[128:131], v[0:3], v[184:187], 0\n"
    "v_cvt_pk_bf16_f32 v219, v110, v111\n"
    "v_cvt_pk_bf16_f32 v220, v120, v121\n"
    "v_mfma_f32_16x16x32_bf16 v[132:135], v[4:7], v[184:187], 0\n"
    "v_cvt_pk_bf16_f32 v221, v122, v123\n"
    "v_cvt_pk_bf16_f32 v222, v124, v125\n"
    "v_mfma_f32_16x16x32_bf16 v[104:107], a[0:3], v[188:191], 0\n"
    "v_cvt_pk_bf16_f32 v223, v126, v127\n"
    "v_or_b32_e64 v195, v195, v203\n"
    "v_mfma_f32_16x16x32_bf16 v[108:111], a[4:7], v[188:191], 0\n"
    "v_pk_fma_f16 v232, v192, v192, s48\n"
    "v_cvt_pk_bf16_f32 v160, v136, v137\n"
    "v_mfma_f32_16x16x32_bf16 v[120:123], a[8:11], v[188:191], 0\n"
    "v_cvt_pk_bf16_f32 v161, v138, v139\n"
    "v_cvt_pk_bf16_f32 v162, v140, v141\n"
    "v_mfma_f32_16x16x32_bf16 v[124:127], a[12:15], v[188:191], 0\n"
    "s_waitcnt lgkmcnt(1)\n"
    "v_cvt_pk_bf16_f32 v163, v142, v143\n"
    "v_pk_mul_f16 v232, v192, v232\n"
    "v_mfma_f32_16x16x32_bf16 v[136:139], v[0:3], v[188:191], 0\n"
    "v_or_b32_e32 v197, v197, v205\n"
    "v_exp_f16_e32 v234, v232\n"
    "v_mfma_f32_16x16x32_bf16 v[140:143], v[4:7], v[188:191], 0\n"
    "s_waitcnt lgkmcnt(0)\n"
    "v_exp_f16_sdwa v234, v232 dst_sel:WORD_1 dst_unused:UNUSED_PRESERVE src0_sel:WORD_1\n"
    "v_lshlrev_b32_sdwa v184, 1, v168 dst_sel:DWORD dst_unused:UNUSED_PAD src0_sel:DWORD src1_sel:WORD_1\n"
    "v_pk_fma_f16 v234, v234, s49, s49\n"
    "v_lshlrev_b32_sdwa v200, 1, v169 dst_sel:DWORD dst_unused:UNUSED_PAD src0_sel:DWORD src1_sel:WORD_1\n"
    "v_lshlrev_b32_sdwa v185, 1, v170 dst_sel:DWORD dst_unused:UNUSED_PAD src0_sel:DWORD src1_sel:WORD_1\n"
    "v_lshlrev_b32_sdwa v201, 1, v171 dst_sel:DWORD dst_unused:UNUSED_PAD src0_sel:DWORD src1_sel:WORD_1\n"
    "v_mfma_f32_16x16x32_bf16 v[168:171], a[104:107], a[224:227], 0\n"
    "ds_read_u16 v184, v184\n"
    "ds_read_u16_d16_hi v200, v200\n"
    "v_mfma_f32_16x16x32_bf16 v[168:171], a[152:155], a[228:231], v[168:171]\n"
    "ds_read_u16 v185, v185\n"
    "ds_read_u16_d16_hi v201, v201\n"
    "v_lshlrev_b32_sdwa v186, 1, v172 dst_sel:DWORD dst_unused:UNUSED_PAD src0_sel:DWORD src1_sel:WORD_1\n"
    "v_lshlrev_b32_sdwa v202, 1, v173 dst_sel:DWORD dst_unused:UNUSED_PAD src0_sel:DWORD src1_sel:WORD_1\n"
    "v_or_b32_e64 v198, v198, v206\n"
    "v_lshlrev_b32_sdwa v187, 1, v174 dst_sel:DWORD dst_unused:UNUSED_PAD src0_sel:DWORD src1_sel:WORD_1\n"
    "v_lshlrev_b32_sdwa v203, 1, v175 dst_sel:DWORD dst_unused:UNUSED_PAD src0_sel:DWORD src1_sel:WORD_1\n"
    "v_rcp_f16_e64 v232, v234\n"
    "v_mfma_f32_16x16x32_bf16 v[168:171], a[64:67], a[232:235], v[168:171]\n"
    "ds_read_u16 v186, v186\n"
    "v_rcp_f16_sdwa v232, v234 dst_sel:WORD_1 dst_unused:UNUSED_PRESERVE src0_sel:WORD_1\n"
    "v_mfma_f32_16x16x32_bf16 v[172:175], a[108:111], a[224:227], 0\n"
    "ds_read_u16_d16_hi v202, v202\n"
    "v_lshlrev_b32_sdwa v188, 1, v176 dst_sel:DWORD dst_unused:UNUSED_PAD src0_sel:DWORD src1_sel:WORD_1\n"
    "v_lshlrev_b32_sdwa v204, 1, v177 dst_sel:DWORD dst_unused:UNUSED_PAD src0_sel:DWORD src1_sel:WORD_1\n"
    "v_lshlrev_b32_sdwa v189, 1, v178 dst_sel:DWORD dst_unused:UNUSED_PAD src0_sel:DWORD src1_sel:WORD_1\n"
    "v_lshlrev_b32_sdwa v205, 1, v179 dst_sel:DWORD dst_unused:UNUSED_PAD src0_sel:DWORD src1_sel:WORD_1\n"
    "v_lshlrev_b32_sdwa v190, 1, v180 dst_sel:DWORD dst_unused:UNUSED_PAD src0_sel:DWORD src1_sel:WORD_1\n"
    "v_lshlrev_b32_sdwa v206, 1, v181 dst_sel:DWORD dst_unused:UNUSED_PAD src0_sel:DWORD src1_sel:WORD_1\n"
    "v_lshlrev_b32_sdwa v191, 1, v182 dst_sel:DWORD dst_unused:UNUSED_PAD src0_sel:DWORD src1_sel:WORD_1\n"
    "v_fma_mix_f32 v234, v192, v232, 0 op_sel_hi:[1,1,0]\n"
    "v_mfma_f32_16x16x32_bf16 v[172:175], a[156:159], a[228:231], v[172:175]\n"
    "ds_read_u16 v187, v187\n"
    "v_or_b32_e64 v199, v199, v207\n"
    "v_mfma_f32_16x16x32_bf16 v[172:175], a[68:71], a[232:235], v[172:175]\n"
    "ds_read_u16_d16_hi v203, v203\n"
    "v_lshlrev_b32_sdwa v207, 1, v183 dst_sel:DWORD dst_unused:UNUSED_PAD src0_sel:DWORD src1_sel:WORD_1\n"
    "v_fma_mix_f32 v232, v192, v232, 0 op_sel:[1,1,0] op_sel_hi:[1,1,0]\n"
    "v_mfma_f32_16x16x32_bf16 v[176:179], a[104:107], a[236:239], 0\n"
    "ds_read_u16 v188, v188\n"
    "v_cvt_pk_bf16_f32 v192, v234, v232\n"
    "v_mfma_f32_16x16x32_bf16 v[176:179], a[152:155], a[240:243], v[176:179]\n"
    "v_pk_fma_f16 v232, v196, v196, s48\n"
    "v_pk_mul_f16 v232, v196, v232\n"
    "v_mfma_f32_16x16x32_bf16 v[176:179], a[64:67], a[244:247], v[176:179]\n"
    "s_waitcnt lgkmcnt(7)\n"
    "ds_read_u16_d16_hi v204, v204\n"
    "v_exp_f16_e32 v234, v232\n"
    "v_mfma_f32_16x16x32_bf16 v[180:183], a[108:111], a[236:239], 0\n"
    "v_exp_f16_sdwa v234, v232 dst_sel:WORD_1 dst_unused:UNUSED_PRESERVE src0_sel:WORD_1\n"
    "v_mfma_f32_16x16x32_bf16 v[180:183], a[156:159], a[240:243], v[180:183]\n"
    "ds_read_u16 v189, v189\n"
    "v_pk_fma_f16 v234, v234, s49, s49\n"
    "v_mfma_f32_16x16x32_bf16 v[180:183], a[68:71], a[244:247], v[180:183]\n"
    "s_waitcnt lgkmcnt(7)\n"
    "ds_read_u16_d16_hi v205, v205\n"
    "v_rcp_f16_e64 v232, v234\n"
    "v_mfma_f32_16x16x32_bf16 v[96:99], a[16:19], v[192:195], v[96:99]\n"
    "v_rcp_f16_sdwa v232, v234 dst_sel:WORD_1 dst_unused:UNUSED_PRESERVE src0_sel:WORD_1\n"
    "v_mfma_f32_16x16x32_bf16 v[100:103], a[20:23], v[192:195], v[100:103]\n"
    "ds_read_u16 v190, v190\n"
    "buffer_store_dwordx4 v[224:227], v245, s[44:47], s57 offen offset:64 sc1\n"
    "v_mfma_f32_16x16x32_bf16 v[112:115], a[24:27], v[192:195], v[112:115]\n"
    "s_waitcnt lgkmcnt(7)\n"
    "ds_read_u16_d16_hi v206, v206\n"
    "v_fma_mix_f32 v234, v196, v232, 0 op_sel_hi:[1,1,0]\n"
    "v_mfma_f32_16x16x32_bf16 v[116:119], a[28:31], v[192:195], v[116:119]\n"
    "ds_read_u16 v191, v191\n"
    "v_fma_mix_f32 v232, v196, v232, 0 op_sel:[1,1,0] op_sel_hi:[1,1,0]\n"
    "v_mfma_f32_16x16x32_bf16 v[128:131], v[8:11], v[192:195], v[128:131]\n"
    "v_cvt_pk_bf16_f32 v196, v234, v232\n"
    "v_or_b32_e32 v184, v184, v200\n"
    "v_mfma_f32_16x16x32_bf16 v[132:135], v[12:15], v[192:195], v[132:135]\n"
    "s_waitcnt lgkmcnt(7)\n"
    "ds_read_u16_d16_hi v207, v207\n"
    "v_or_b32_e64 v185, v185, v201\n"
    "v_mfma_f32_16x16x32_bf16 v[104:107], a[16:19], v[196:199], v[104:107]\n"
    "v_cvt_pk_f16_f32 v192, v168, v169\n"
    "v_lshlrev_b32_sdwa v193, 1, v170 dst_sel:DWORD dst_unused:UNUSED_PAD src0_sel:DWORD src1_sel:WORD_1\n"
    "v_mfma_f32_16x16x32_bf16 v[108:111], a[20:23], v[196:199], v[108:111]\n"
    "ds_read_u16 v193, v193\n"
    "v_lshlrev_b32_sdwa v201, 1, v171 dst_sel:DWORD dst_unused:UNUSED_PAD src0_sel:DWORD src1_sel:WORD_1\n"
    "v_mfma_f32_16x16x32_bf16 v[120:123], a[24:27], v[196:199], v[120:123]\n"
    "v_lshlrev_b32_sdwa v194, 1, v172 dst_sel:DWORD dst_unused:UNUSED_PAD src0_sel:DWORD src1_sel:WORD_1\n"
    "v_or_b32_e32 v186, v186, v202\n"
    "v_mfma_f32_16x16x32_bf16 v[124:127], a[28:31], v[196:199], v[124:127]\n"
    "s_waitcnt lgkmcnt(7)\n"
    "ds_read_u16_d16_hi v201, v201\n"
    "v_lshlrev_b32_sdwa v202, 1, v173 dst_sel:DWORD dst_unused:UNUSED_PAD src0_sel:DWORD src1_sel:WORD_1\n"
    "v_mfma_f32_16x16x32_bf16 v[136:139], v[8:11], v[196:199], v[136:139]\n"
    "ds_read_u16 v194, v194\n"
    "v_lshlrev_b32_sdwa v195, 1, v174 dst_sel:DWORD dst_unused:UNUSED_PAD src0_sel:DWORD src1_sel:WORD_1\n"
    "v_mfma_f32_16x16x32_bf16 v[140:143], v[12:15], v[196:199], v[140:143]\n"
    "s_nop 0\n"
    "v_cvt_pk_f16_f32 v196, v176, v177\n"
    "v_lshlrev_b32_sdwa v197, 1, v178 dst_sel:DWORD dst_unused:UNUSED_PAD src0_sel:DWORD src1_sel:WORD_1\n"
    "v_mfma_f32_16x16x32_bf16 v[168:171], a[112:115], a[224:227], 0\n"
    "s_waitcnt lgkmcnt(7)\n"
    "ds_read_u16_d16_hi v202, v202\n"
    "v_lshlrev_b32_sdwa v198, 1, v180 dst_sel:DWORD dst_unused:UNUSED_PAD src0_sel:DWORD src1_sel:WORD_1\n"
    "v_mfma_f32_16x16x32_bf16 v[168:171], a[160:163], a[228:231], v[168:171]\n"
    "v_lshlrev_b32_sdwa v199, 1, v182 dst_sel:DWORD dst_unused:UNUSED_PAD src0_sel:DWORD src1_sel:WORD_1\n"
    "v_or_b32_e64 v187, v187, v203\n"
    "v_mfma_f32_16x16x32_bf16 v[168:171], a[72:75], a[232:235], v[168:171]\n"
    "ds_read_u16 v195, v195\n"
    "v_lshlrev_b32_sdwa v203, 1, v175 dst_sel:DWORD dst_unused:UNUSED_PAD src0_sel:DWORD src1_sel:WORD_1\n"
    "v_mfma_f32_16x16x32_bf16 v[172:175], a[116:119], a[224:227], 0\n"
    "v_or_b32_e64 v188, v188, v204\n"
    "v_or_b32_e32 v189, v189, v205\n"
    "v_mfma_f32_16x16x32_bf16 v[172:175], a[164:167], a[228:231], v[172:175]\n"
    "s_waitcnt lgkmcnt(7)\n"
    "ds_read_u16_d16_hi v203, v203\n"
    "v_lshlrev_b32_sdwa v205, 1, v179 dst_sel:DWORD dst_unused:UNUSED_PAD src0_sel:DWORD src1_sel:WORD_1\n"
    "v_mfma_f32_16x16x32_bf16 v[172:175], v[52:55], a[232:235], v[172:175]\n"
    "ds_read_u16 v197, v197\n"
    "v_or_b32_e64 v190, v190, v206\n"
    "v_lshlrev_b32_sdwa v206, 1, v181 dst_sel:DWORD dst_unused:UNUSED_PAD src0_sel:DWORD src1_sel:WORD_1\n"
    "v_pk_fma_f16 v232, v192, v192, s48\n"
    "v_lshlrev_b32_sdwa v200, 1, v169 dst_sel:DWORD dst_unused:UNUSED_PAD src0_sel:DWORD src1_sel:WORD_1\n"
    "v_mfma_f32_16x16x32_bf16 v[176:179], a[112:115], a[236:239], 0\n"
    "s_waitcnt lgkmcnt(7)\n"
    "ds_read_u16_d16_hi v205, v205\n"
    "v_or_b32_e32 v191, v191, v207\n"
    "v_mfma_f32_16x16x32_bf16 v[176:179], a[160:163], a[240:243], v[176:179]\n"
    "v_lshlrev_b32_sdwa v207, 1, v183 dst_sel:DWORD dst_unused:UNUSED_PAD src0_sel:DWORD src1_sel:WORD_1\n"
    "v_pk_mul_f16 v232, v192, v232\n"
    "v_mfma_f32_16x16x32_bf16 v[176:179], a[72:75], a[244:247], v[176:179]\n"
    "ds_read_u16 v198, v198\n"
    "buffer_store_dwordx4 v[228:231], v245, s[44:47], s57 offen offset:128 sc1\n"
    "v_mfma_f32_16x16x32_bf16 v[180:183], a[116:119], a[236:239], 0\n"
    "s_waitcnt lgkmcnt(7)\n"
    "ds_read_u16_d16_hi v206, v206\n"
    "v_or_b32_e32 v193, v193, v201\n"
    "v_mfma_f32_16x16x32_bf16 v[180:183], a[164:167], a[240:243], v[180:183]\n"
    "v_exp_f16_e64 v234, v232\n"
    "v_mfma_f32_16x16x32_bf16 v[180:183], v[52:55], a[244:247], v[180:183]\n"
    "ds_read_u16 v199, v199\n"
    "v_exp_f16_sdwa v234, v232 dst_sel:WORD_1 dst_unused:UNUSED_PRESERVE src0_sel:WORD_1\n"
    "v_mfma_f32_16x16x32_bf16 v[96:99], a[32:35], v[184:187], v[96:99]\n"
    "s_waitcnt lgkmcnt(7)\n"
    "ds_read_u16_d16_hi v207, v207\n"
    "v_or_b32_e32 v194, v194, v202\n"
    "v_mfma_f32_16x16x32_bf16 v[100:103], a[36:39], v[184:187], v[100:103]\n"
    "v_pk_fma_f16 v234, v234, s49, s49\n"
    "v_lshlrev_b32_sdwa v201, 1, v171 dst_sel:DWORD dst_unused:UNUSED_PAD src0_sel:DWORD src1_sel:WORD_1\n"
    "v_mfma_f32_16x16x32_bf16 v[112:115], a[40:43], v[184:187], v[112:115]\n"
    "ds_read_u16_d16_hi v200, v200\n"
    "v_lshlrev_b32_sdwa v202, 1, v173 dst_sel:DWORD dst_unused:UNUSED_PAD src0_sel:DWORD src1_sel:WORD_1\n"
    "v_mfma_f32_16x16x32_bf16 v[116:119], a[44:47], v[184:187], v[116:119]\n"
    "v_rcp_f16_e32 v232, v234\n"
    "v_mfma_f32_16x16x32_bf16 v[128:131], v[16:19], v[184:187], v[128:131]\n"
    "s_waitcnt lgkmcnt(7)\n"
    "ds_read_u16_d16_hi v201, v201\n"
    "v_or_b32_e64 v195, v195, v203\n"
    "v_mfma_f32_16x16x32_bf16 v[132:135], v[20:23], v[184:187], v[132:135]\n"
    "ds_read_u16_d16_hi v202, v202\n"
    "v_lshlrev_b32_sdwa v184, 1, v168 dst_sel:DWORD dst_unused:UNUSED_PAD src0_sel:DWORD src1_sel:WORD_1\n"
    "v_mfma_f32_16x16x32_bf16 v[104:107], a[32:35], v[188:191], v[104:107]\n"
    "v_lshlrev_b32_sdwa v185, 1, v170 dst_sel:DWORD dst_unused:UNUSED_PAD src0_sel:DWORD src1_sel:WORD_1\n"
    "v_lshlrev_b32_sdwa v186, 1, v172 dst_sel:DWORD dst_unused:UNUSED_PAD src0_sel:DWORD src1_sel:WORD_1\n"
    "v_mfma_f32_16x16x32_bf16 v[108:111], a[36:39], v[188:191], v[108:111]\n"
    "s_waitcnt lgkmcnt(7)\n"
    "ds_read_u16 v184, v184\n"
    "v_or_b32_e32 v197, v197, v205\n"
    "v_mfma_f32_16x16x32_bf16 v[120:123], a[40:43], v[188:191], v[120:123]\n"
    "v_lshlrev_b32_sdwa v187, 1, v174 dst_sel:DWORD dst_unused:UNUSED_PAD src0_sel:DWORD src1_sel:WORD_1\n"
    "v_lshlrev_b32_sdwa v203, 1, v175 dst_sel:DWORD dst_unused:UNUSED_PAD src0_sel:DWORD src1_sel:WORD_1\n"
    "v_mfma_f32_16x16x32_bf16 v[124:127], a[44:47], v[188:191], v[124:127]\n"
    "ds_read_u16 v185, v185\n"
    "v_rcp_f16_sdwa v232, v234 dst_sel:WORD_1 dst_unused:UNUSED_PRESERVE src0_sel:WORD_1\n"
    "v_mfma_f32_16x16x32_bf16 v[136:139], v[16:19], v[188:191], v[136:139]\n"
    "s_waitcnt lgkmcnt(7)\n"
    "ds_read_u16 v186, v186\n"
    "v_or_b32_e32 v198, v198, v206\n"
    "v_mfma_f32_16x16x32_bf16 v[140:143], v[20:23], v[188:191], v[140:143]\n"
    "buffer_store_dwordx4 v[216:219], v245, s[44:47], s88 offen offset:0 sc1\n"
    "ds_read_u16 v187, v187\n"
    "v_mfma_f32_16x16x32_bf16 v[168:171], a[120:123], a[224:227], 0\n"
    "s_nop 0\n"
    "v_lshlrev_b32_sdwa v188, 1, v176 dst_sel:DWORD dst_unused:UNUSED_PAD src0_sel:DWORD src1_sel:WORD_1\n"
    "v_lshlrev_b32_sdwa v204, 1, v177 dst_sel:DWORD dst_unused:UNUSED_PAD src0_sel:DWORD src1_sel:WORD_1\n"
    "v_mfma_f32_16x16x32_bf16 v[168:171], a[168:171], a[228:231], v[168:171]\n"
    "s_waitcnt lgkmcnt(7)\n"
    "ds_read_u16_d16_hi v203, v203\n"
    "v_lshlrev_b32_sdwa v189, 1, v178 dst_sel:DWORD dst_unused:UNUSED_PAD src0_sel:DWORD src1_sel:WORD_1\n"
    "v_mfma_f32_16x16x32_bf16 v[168:171], a[76:79], a[232:235], v[168:171]\n"
    "v_lshlrev_b32_sdwa v205, 1, v179 dst_sel:DWORD dst_unused:UNUSED_PAD src0_sel:DWORD src1_sel:WORD_1\n"
    "v_lshlrev_b32_sdwa v190, 1, v180 dst_sel:DWORD dst_unused:UNUSED_PAD src0_sel:DWORD src1_sel:WORD_1\n"
    "v_mfma_f32_16x16x32_bf16 v[172:175], a[124:127], a[224:227], 0\n"
    "ds_read_u16 v188, v188\n"
    "v_lshlrev_b32_sdwa v206, 1, v181 dst_sel:DWORD dst_unused:UNUSED_PAD src0_sel:DWORD src1_sel:WORD_1\n"
    "v_mfma_f32_16x16x32_bf16 v[172:175], a[172:175], a[228:231], v[172:175]\n"
    "ds_read_u16_d16_hi v204, v204\n"
    "v_lshlrev_b32_sdwa v191, 1, v182 dst_sel:DWORD dst_unused:UNUSED_PAD src0_sel:DWORD src1_sel:WORD_1\n"
    "v_fma_mix_f32 v234, v192, v232, 0 op_sel_hi:[1,1,0]\n"
    "v_mfma_f32_16x16x32_bf16 v[172:175], v[60:63], a[232:235], v[172:175]\n"
    "v_or_b32_e64 v199, v199, v207\n"
    "v_lshlrev_b32_sdwa v207, 1, v183 dst_sel:DWORD dst_unused:UNUSED_PAD src0_sel:DWORD src1_sel:WORD_1\n"
    "v_fma_mix_f32 v232, v192, v232, 0 op_sel:[1,1,0] op_sel_hi:[1,1,0]\n"
    "v_mfma_f32_16x16x32_bf16 v[176:179], a[120:123], a[236:239], 0\n"
    "s_nop 0\n"
    "ds_read_u16 v189, v189\n"
    "v_cvt_pk_bf16_f32 v192, v234, v232\n"
    "v_mfma_f32_16x16x32_bf16 v[176:179], a[168:171], a[240:243], v[176:179]\n"
    "s_waitcnt lgkmcnt(7)\n"
    "ds_read_u16_d16_hi v205, v205\n"
    "v_pk_fma_f16 v232, v196, v196, s48\n"
    "v_mfma_f32_16x16x32_bf16 v[176:179], a[76:79], a[244:247], v[176:179]\n"
    "v_pk_mul_f16 v232, v196, v232\n"
    "v_or_b32_e32 v184, v184, v200\n"
    "v_mfma_f32_16x16x32_bf16 v[180:183], a[124:127], a[236:239], 0\n"
    "s_waitcnt lgkmcnt(7)\n"
    "ds_read_u16 v190, v190\n"
    "v_or_b32_e64 v185, v185, v201\n"
    "v_mfma_f32_16x16x32_bf16 v[180:183], a[172:175], a[240:243], v[180:183]\n"
    "v_exp_f16_e32 v234, v232\n"
    "v_mfma_f32_16x16x32_bf16 v[180:183], v[60:63], a[244:247], v[180:183]\n"
    "s_waitcnt lgkmcnt(7)\n"
    "ds_read_b128 a[224:227], v243 offset:0\n"
    "v_exp_f16_sdwa v234, v232 dst_sel:WORD_1 dst_unused:UNUSED_PRESERVE src0_sel:WORD_1\n"
    "v_mfma_f32_16x16x32_bf16 v[96:99], a[176:179], v[192:195], v[96:99]\n"
    "ds_read_u16_d16_hi v206, v206\n"
    "v_pk_fma_f16 v234, v234, s49, s49\n"
    "v_mfma_f32_16x16x32_bf16 v[100:103], a[180:183], v[192:195], v[100:103]\n"
    "ds_read_b128 a[228:231], v243 offset:1024\n"
    "v_rcp_f16_e32 v232, v234\n"
    "v_mfma_f32_16x16x32_bf16 v[112:115], a[184:187], v[192:195], v[112:115]\n"
    "s_waitcnt lgkmcnt(8)\n"
    "ds_read_u16 v191, v191\n"
    "v_rcp_f16_sdwa v232, v234 dst_sel:WORD_1 dst_unused:UNUSED_PRESERVE src0_sel:WORD_1\n"
    "v_mfma_f32_16x16x32_bf16 v[116:119], a[188:191], v[192:195], v[116:119]\n"
    "s_nop 0\n"
    "buffer_store_dwordx4 v[220:223], v245, s[44:47], s88 offen offset:64 sc1\n"
    "ds_read_b128 a[232:235], v243 offset:2048\n"
    "v_mfma_f32_16x16x32_bf16 v[128:131], v[24:27], v[192:195], v[128:131]\n"
    "s_waitcnt lgkmcnt(8)\n"
    "ds_read_u16_d16_hi v207, v207\n"
    "v_fma_mix_f32 v234, v196, v232, 0 op_sel_hi:[1,1,0]\n"
    "v_mfma_f32_16x16x32_bf16 v[132:135], v[28:31], v[192:195], v[132:135]\n"
    "ds_read_b128 a[236:239], v243 offset:3072\n"
    "v_fma_mix_f32 v232, v196, v232, 0 op_sel:[1,1,0] op_sel_hi:[1,1,0]\n"
    "v_cvt_pk_f16_f32 v192, v168, v169\n"
    "v_cvt_pk_bf16_f32 v196, v234, v232\n"
    "v_lshlrev_b32_sdwa v193, 1, v170 dst_sel:DWORD dst_unused:UNUSED_PAD src0_sel:DWORD src1_sel:WORD_1\n"
    "v_lshlrev_b32_sdwa v201, 1, v171 dst_sel:DWORD dst_unused:UNUSED_PAD src0_sel:DWORD src1_sel:WORD_1\n"
    "v_mfma_f32_16x16x32_bf16 v[104:107], a[176:179], v[196:199], v[104:107]\n"
    "ds_read_b128 a[240:243], v243 offset:4096\n"
    "ds_read_b128 a[244:247], v243 offset:5120\n"
    "v_mfma_f32_16x16x32_bf16 v[108:111], a[180:183], v[196:199], v[108:111]\n"
    "s_waitcnt lgkmcnt(10)\n"
    "ds_read_u16 v193, v193\n"
    "v_lshlrev_b32_sdwa v194, 1, v172 dst_sel:DWORD dst_unused:UNUSED_PAD src0_sel:DWORD src1_sel:WORD_1\n"
    "v_mfma_f32_16x16x32_bf16 v[120:123], a[184:187], v[196:199], v[120:123]\n"
    "s_waitcnt lgkmcnt(9)\n"
    "v_or_b32_e64 v186, v186, v202\n"
    "v_lshlrev_b32_sdwa v202, 1, v173 dst_sel:DWORD dst_unused:UNUSED_PAD src0_sel:DWORD src1_sel:WORD_1\n"
    "v_mfma_f32_16x16x32_bf16 v[124:127], a[188:191], v[196:199], v[124:127]\n"
    "ds_read_u16_d16_hi v201, v201\n"
    "v_lshlrev_b32_sdwa v195, 1, v174 dst_sel:DWORD dst_unused:UNUSED_PAD src0_sel:DWORD src1_sel:WORD_1\n"
    "v_mfma_f32_16x16x32_bf16 v[136:139], v[24:27], v[196:199], v[136:139]\n"
    "v_or_b32_e32 v187, v187, v203\n"
    "v_lshlrev_b32_sdwa v203, 1, v175 dst_sel:DWORD dst_unused:UNUSED_PAD src0_sel:DWORD src1_sel:WORD_1\n"
    "v_mfma_f32_16x16x32_bf16 v[140:143], v[28:31], v[196:199], v[140:143]\n"
    "s_waitcnt lgkmcnt(8)\n"
    "ds_read_u16 v194, v194\n"
    "v_cvt_pk_f16_f32 v196, v176, v177\n"
    "v_mfma_f32_16x16x32_bf16 v[168:171], a[80:83], a[224:227], 0\n"
    "v_lshlrev_b32_sdwa v197, 1, v178 dst_sel:DWORD dst_unused:UNUSED_PAD src0_sel:DWORD src1_sel:WORD_1\n"
    "v_lshlrev_b32_sdwa v198, 1, v180 dst_sel:DWORD dst_unused:UNUSED_PAD src0_sel:DWORD src1_sel:WORD_1\n"
    "v_mfma_f32_16x16x32_bf16 v[168:171], a[128:131], a[228:231], v[168:171]\n"
    "ds_read_u16_d16_hi v202, v202\n"
    "v_lshlrev_b32_sdwa v199, 1, v182 dst_sel:DWORD dst_unused:UNUSED_PAD src0_sel:DWORD src1_sel:WORD_1\n"
    "v_mfma_f32_16x16x32_bf16 v[172:175], a[84:87], a[224:227], 0\n"
    "s_waitcnt lgkmcnt(8)\n"
    "ds_read_u16 v195, v195\n"
    "v_or_b32_e32 v188, v188, v204\n"
    "v_mfma_f32_16x16x32_bf16 v[172:175], a[132:135], a[228:231], v[172:175]\n"
    "v_or_b32_e64 v189, v189, v205\n"
    "v_lshlrev_b32_sdwa v205, 1, v179 dst_sel:DWORD dst_unused:UNUSED_PAD src0_sel:DWORD src1_sel:WORD_1\n"
    "v_mfma_f32_16x16x32_bf16 v[172:175], v[48:51], a[232:235], v[172:175]\n"
    "s_waitcnt lgkmcnt(7)\n"
    "ds_read_u16_d16_hi v203, v203\n"
    "v_or_b32_e32 v190, v190, v206\n"
    "v_mfma_f32_16x16x32_bf16 v[168:171], a[48:51], a[232:235], v[168:171]\n"
    "v_lshlrev_b32_sdwa v206, 1, v181 dst_sel:DWORD dst_unused:UNUSED_PAD src0_sel:DWORD src1_sel:WORD_1\n"
    "v_or_b32_e32 v191, v191, v207\n"
    "v_mfma_f32_16x16x32_bf16 v[176:179], a[80:83], a[236:239], 0\n"
    "s_waitcnt lgkmcnt(6)\n"
    "ds_read_u16 v197, v197\n"
    "v_lshlrev_b32_sdwa v207, 1, v183 dst_sel:DWORD dst_unused:UNUSED_PAD src0_sel:DWORD src1_sel:WORD_1\n"
    "v_mfma_f32_16x16x32_bf16 v[176:179], a[128:131], a[240:243], v[176:179]\n"
    "v_pk_fma_f16 v232, v192, v192, s48\n"
    "v_lshlrev_b32_sdwa v200, 1, v169 dst_sel:DWORD dst_unused:UNUSED_PAD src0_sel:DWORD src1_sel:WORD_1\n"
    "v_mfma_f32_16x16x32_bf16 v[176:179], a[48:51], a[244:247], v[176:179]\n"
    "ds_read_u16_d16_hi v205, v205\n"
    "buffer_store_dwordx4 v[160:163], v245, s[44:47], s88 offen offset:128 sc1\n"
    "v_mfma_f32_16x16x32_bf16 v[180:183], a[84:87], a[236:239], 0\n"
    "ds_read_u16 v198, v198\n"
    "v_pk_mul_f16 v232, v192, v232\n"
    "v_mfma_f32_16x16x32_bf16 v[180:183], a[132:135], a[240:243], v[180:183]\n"
    "s_waitcnt lgkmcnt(7)\n"
    "ds_read_u16_d16_hi v206, v206\n"
    "v_or_b32_e32 v193, v193, v201\n"
    "v_mfma_f32_16x16x32_bf16 v[180:183], v[48:51], a[244:247], v[180:183]\n"
    "v_exp_f16_e64 v234, v232\n"
    "v_mfma_f32_16x16x32_bf16 v[96:99], a[192:195], v[184:187], v[96:99]\n"
    "ds_read_u16 v199, v199\n"
    "v_exp_f16_sdwa v234, v232 dst_sel:WORD_1 dst_unused:UNUSED_PRESERVE src0_sel:WORD_1\n"
    "v_mfma_f32_16x16x32_bf16 v[100:103], a[196:199], v[184:187], v[100:103]\n"
    "s_waitcnt lgkmcnt(7)\n"
    "ds_read_u16_d16_hi v207, v207\n"
    "v_or_b32_e32 v194, v194, v202\n"
    "v_mfma_f32_16x16x32_bf16 v[112:115], a[200:203], v[184:187], v[112:115]\n"
    "v_pk_fma_f16 v234, v234, s49, s49\n"
    "v_lshlrev_b32_sdwa v201, 1, v171 dst_sel:DWORD dst_unused:UNUSED_PAD src0_sel:DWORD src1_sel:WORD_1\n"
    "v_mfma_f32_16x16x32_bf16 v[116:119], a[204:207], v[184:187], v[116:119]\n"
    "ds_read_u16_d16_hi v200, v200\n"
    "v_lshlrev_b32_sdwa v202, 1, v173 dst_sel:DWORD dst_unused:UNUSED_PAD src0_sel:DWORD src1_sel:WORD_1\n"
    "v_mfma_f32_16x16x32_bf16 v[128:131], v[32:35], v[184:187], v[128:131]\n"
    "v_rcp_f16_e32 v232, v234\n"
    "v_mfma_f32_16x16x32_bf16 v[132:135], v[36:39], v[184:187], v[132:135]\n"
    "s_waitcnt lgkmcnt(7)\n"
    "ds_read_u16_d16_hi v201, v201\n"
    "v_or_b32_e64 v195, v195, v203\n"
    "v_mfma_f32_16x16x32_bf16 v[104:107], a[192:195], v[188:191], v[104:107]\n"
    "v_lshlrev_b32_sdwa v184, 1, v168 dst_sel:DWORD dst_unused:UNUSED_PAD src0_sel:DWORD src1_sel:WORD_1\n"
    "v_lshlrev_b32_sdwa v185, 1, v170 dst_sel:DWORD dst_unused:UNUSED_PAD src0_sel:DWORD src1_sel:WORD_1\n"
    "v_mfma_f32_16x16x32_bf16 v[108:111], a[196:199], v[188:191], v[108:111]\n"
    "ds_read_u16 v184, v184\n"
    "v_lshlrev_b32_sdwa v186, 1, v172 dst_sel:DWORD dst_unused:UNUSED_PAD src0_sel:DWORD src1_sel:WORD_1\n"
    "v_mfma_f32_16x16x32_bf16 v[120:123], a[200:203], v[188:191], v[120:123]\n"
    "s_waitcnt lgkmcnt(7)\n"
    "ds_read_u16 v185, v185\n"
    "v_or_b32_e32 v197, v197, v205\n"
    "v_mfma_f32_16x16x32_bf16 v[124:127], a[204:207], v[188:191], v[124:127]\n"
    "v_lshlrev_b32_sdwa v187, 1, v174 dst_sel:DWORD dst_unused:UNUSED_PAD src0_sel:DWORD src1_sel:WORD_1\n"
    "v_lshlrev_b32_sdwa v203, 1, v175 dst_sel:DWORD dst_unused:UNUSED_PAD src0_sel:DWORD src1_sel:WORD_1\n"
    "v_mfma_f32_16x16x32_bf16 v[136:139], v[32:35], v[188:191], v[136:139]\n"
    "ds_read_u16 v186, v186\n"
    "v_rcp_f16_sdwa v232, v234 dst_sel:WORD_1 dst_unused:UNUSED_PRESERVE src0_sel:WORD_1\n"
    "v_mfma_f32_16x16x32_bf16 v[140:143], v[36:39], v[188:191], v[140:143]\n"
    "v_lshlrev_b32_sdwa v188, 1, v176 dst_sel:DWORD dst_unused:UNUSED_PAD src0_sel:DWORD src1_sel:WORD_1\n"
    "v_lshlrev_b32_sdwa v204, 1, v177 dst_sel:DWORD dst_unused:UNUSED_PAD src0_sel:DWORD src1_sel:WORD_1\n"
    "v_mfma_f32_16x16x32_bf16 v[168:171], a[88:91], a[224:227], 0\n"
    "s_waitcnt lgkmcnt(7)\n"
    "ds_read_u16_d16_hi v202, v202\n"
    "v_or_b32_e32 v198, v198, v206\n"
    "v_mfma_f32_16x16x32_bf16 v[168:171], a[136:139], a[228:231], v[168:171]\n"
    "ds_read_u16 v187, v187\n"
    "v_lshlrev_b32_sdwa v189, 1, v178 dst_sel:DWORD dst_unused:UNUSED_PAD src0_sel:DWORD src1_sel:WORD_1\n"
    "v_mfma_f32_16x16x32_bf16 v[168:171], a[52:55], a[232:235], v[168:171]\n"
    "s_nop 0\n"
    "v_lshlrev_b32_sdwa v205, 1, v179 dst_sel:DWORD dst_unused:UNUSED_PAD src0_sel:DWORD src1_sel:WORD_1\n"
    "v_lshlrev_b32_sdwa v190, 1, v180 dst_sel:DWORD dst_unused:UNUSED_PAD src0_sel:DWORD src1_sel:WORD_1\n"
    "v_mfma_f32_16x16x32_bf16 v[172:175], a[92:95], a[224:227], 0\n"
    "s_waitcnt lgkmcnt(7)\n"
    "ds_read_u16_d16_hi v203, v203\n"
    "v_lshlrev_b32_sdwa v206, 1, v181 dst_sel:DWORD dst_unused:UNUSED_PAD src0_sel:DWORD src1_sel:WORD_1\n"
    "v_mfma_f32_16x16x32_bf16 v[172:175], a[140:143], a[228:231], v[172:175]\n"
    "v_lshlrev_b32_sdwa v191, 1, v182 dst_sel:DWORD dst_unused:UNUSED_PAD src0_sel:DWORD src1_sel:WORD_1\n"
    "v_fma_mix_f32 v234, v192, v232, 0 op_sel_hi:[1,1,0]\n"
    "v_mfma_f32_16x16x32_bf16 v[172:175], v[56:59], a[232:235], v[172:175]\n"
    "ds_read_u16 v188, v188\n"
    "v_or_b32_e64 v199, v199, v207\n"
    "v_lshlrev_b32_sdwa v207, 1, v183 dst_sel:DWORD dst_unused:UNUSED_PAD src0_sel:DWORD src1_sel:WORD_1\n"
    "v_fma_mix_f32 v232, v192, v232, 0 op_sel:[1,1,0] op_sel_hi:[1,1,0]\n"
    "v_mfma_f32_16x16x32_bf16 v[176:179], a[88:91], a[236:239], 0\n"
    "ds_read_u16_d16_hi v204, v204\n"
    "v_cvt_pk_bf16_f32 v192, v234, v232\n"
    "v_mfma_f32_16x16x32_bf16 v[176:179], a[136:139], a[240:243], v[176:179]\n"
    "v_pk_fma_f16 v232, v196, v196, s48\n"
    "v_pk_mul_f16 v232, v196, v232\n"
    "v_mfma_f32_16x16x32_bf16 v[176:179], a[52:55], a[244:247], v[176:179]\n"
    "s_waitcnt lgkmcnt(7)\n"
    "ds_read_u16 v189, v189\n"
    "v_exp_f16_e64 v234, v232\n"
    "v_mfma_f32_16x16x32_bf16 v[180:183], a[92:95], a[236:239], 0\n"
    "s_waitcnt lgkmcnt(7)\n"
    "ds_read_u16_d16_hi v205, v205\n"
    "v_or_b32_e64 v184, v184, v200\n"
    "v_mfma_f32_16x16x32_bf16 v[180:183], a[140:143], a[240:243], v[180:183]\n"
    "v_exp_f16_sdwa v234, v232 dst_sel:WORD_1 dst_unused:UNUSED_PRESERVE src0_sel:WORD_1\n"
    "v_mfma_f32_16x16x32_bf16 v[180:183], v[56:59], a[244:247], v[180:183]\n"
    "ds_read_u16 v190, v190\n"
    "v_pk_fma_f16 v234, v234, s49, s49\n"
    "v_mfma_f32_16x16x32_bf16 v[96:99], a[208:211], v[192:195], v[96:99]\n"
    "v_rcp_f16_e32 v232, v234\n"
    "v_mfma_f32_16x16x32_bf16 v[100:103], a[212:215], v[192:195], v[100:103]\n"
    "s_waitcnt lgkmcnt(7)\n"
    "ds_read_u16_d16_hi v206, v206\n"
    "v_rcp_f16_sdwa v232, v234 dst_sel:WORD_1 dst_unused:UNUSED_PRESERVE src0_sel:WORD_1\n"
    "v_mfma_f32_16x16x32_bf16 v[112:115], a[216:219], v[192:195], v[112:115]\n"
    "ds_read_u16 v191, v191\n"
    "v_fma_mix_f32 v234, v196, v232, 0 op_sel_hi:[1,1,0]\n"
    "v_mfma_f32_16x16x32_bf16 v[116:119], a[220:223], v[192:195], v[116:119]\n"
    "v_fma_mix_f32 v232, v196, v232, 0 op_sel:[1,1,0] op_sel_hi:[1,1,0]\n"
    "v_or_b32_e32 v185, v185, v201\n"
    "v_mfma_f32_16x16x32_bf16 v[128:131], v[40:43], v[192:195], v[128:131]\n"
    "s_waitcnt lgkmcnt(7)\n"
    "ds_read_u16_d16_hi v207, v207\n"
    "v_cvt_pk_bf16_f32 v196, v234, v232\n"
    "v_mfma_f32_16x16x32_bf16 v[132:135], v[44:47], v[192:195], v[132:135]\n"
    "v_cvt_pk_f16_f32 v192, v168, v169\n"
    "v_lshlrev_b32_sdwa v193, 1, v170 dst_sel:DWORD dst_unused:UNUSED_PAD src0_sel:DWORD src1_sel:WORD_1\n"
    "v_mfma_f32_16x16x32_bf16 v[104:107], a[208:211], v[196:199], v[104:107]\n"
    "ds_read_u16 v193, v193\n"
    "v_lshlrev_b32_sdwa v201, 1, v171 dst_sel:DWORD dst_unused:UNUSED_PAD src0_sel:DWORD src1_sel:WORD_1\n"
    "v_mfma_f32_16x16x32_bf16 v[108:111], a[212:215], v[196:199], v[108:111]\n"
    "v_lshlrev_b32_sdwa v194, 1, v172 dst_sel:DWORD dst_unused:UNUSED_PAD src0_sel:DWORD src1_sel:WORD_1\n"
    "v_or_b32_e32 v186, v186, v202\n"
    "v_mfma_f32_16x16x32_bf16 v[120:123], a[216:219], v[196:199], v[120:123]\n"
    "s_waitcnt lgkmcnt(7)\n"
    "ds_read_u16_d16_hi v201, v201\n"
    "v_lshlrev_b32_sdwa v202, 1, v173 dst_sel:DWORD dst_unused:UNUSED_PAD src0_sel:DWORD src1_sel:WORD_1\n"
    "v_mfma_f32_16x16x32_bf16 v[124:127], a[220:223], v[196:199], v[124:127]\n"
    "ds_read_u16 v194, v194\n"
    "v_lshlrev_b32_sdwa v195, 1, v174 dst_sel:DWORD dst_unused:UNUSED_PAD src0_sel:DWORD src1_sel:WORD_1\n"
    "v_mfma_f32_16x16x32_bf16 v[136:139], v[40:43], v[196:199], v[136:139]\n"
    "v_or_b32_e32 v187, v187, v203\n"
    "v_lshlrev_b32_sdwa v203, 1, v175 dst_sel:DWORD dst_unused:UNUSED_PAD src0_sel:DWORD src1_sel:WORD_1\n"
    "v_mfma_f32_16x16x32_bf16 v[140:143], v[44:47], v[196:199], v[140:143]\n"
    "s_waitcnt lgkmcnt(7)\n"
    "ds_read_u16_d16_hi v202, v202\n"
    "v_cvt_pk_f16_f32 v196, v176, v177\n"
    "v_mfma_f32_16x16x32_bf16 v[168:171], a[96:99], a[224:227], 0\n"
    "v_lshlrev_b32_sdwa v197, 1, v178 dst_sel:DWORD dst_unused:UNUSED_PAD src0_sel:DWORD src1_sel:WORD_1\n"
    "v_lshlrev_b32_sdwa v198, 1, v180 dst_sel:DWORD dst_unused:UNUSED_PAD src0_sel:DWORD src1_sel:WORD_1\n"
    "v_mfma_f32_16x16x32_bf16 v[168:171], a[144:147], a[228:231], v[168:171]\n"
    "ds_read_u16 v195, v195\n"
    "v_lshlrev_b32_sdwa v199, 1, v182 dst_sel:DWORD dst_unused:UNUSED_PAD src0_sel:DWORD src1_sel:WORD_1\n"
    "v_mfma_f32_16x16x32_bf16 v[168:171], a[56:59], a[232:235], v[168:171]\n"
    "v_or_b32_e64 v188, v188, v204\n"
    "v_or_b32_e32 v189, v189, v205\n"
    "v_mfma_f32_16x16x32_bf16 v[172:175], a[100:103], a[224:227], 0\n"
    "s_waitcnt lgkmcnt(7)\n"
    "ds_read_u16_d16_hi v203, v203\n"
    "v_lshlrev_b32_sdwa v205, 1, v179 dst_sel:DWORD dst_unused:UNUSED_PAD src0_sel:DWORD src1_sel:WORD_1\n"
    "v_mfma_f32_16x16x32_bf16 v[172:175], a[148:151], a[228:231], v[172:175]\n"
    "ds_read_u16 v197, v197\n"
    "v_cvt_pk_bf16_f32 v96, v96, v97\n"
    "v_mfma_f32_16x16x32_bf16 v[172:175], a[60:63], a[232:235], v[172:175]\n"
    "v_cvt_pk_bf16_f32 v97, v98, v99\n"
    "v_cvt_pk_bf16_f32 v98, v100, v101\n"
    "v_mfma_f32_16x16x32_bf16 v[176:179], a[96:99], a[236:239], 0\n"
    "s_waitcnt lgkmcnt(7)\n"
    "ds_read_u16_d16_hi v205, v205\n"
    "v_cvt_pk_bf16_f32 v99, v102, v103\n"
    "v_mfma_f32_16x16x32_bf16 v[176:179], a[144:147], a[240:243], v[176:179]\n"
    "v_or_b32_e32 v190, v190, v206\n"
    "v_lshlrev_b32_sdwa v206, 1, v181 dst_sel:DWORD dst_unused:UNUSED_PAD src0_sel:DWORD src1_sel:WORD_1\n"
    "v_mfma_f32_16x16x32_bf16 v[176:179], a[56:59], a[244:247], v[176:179]\n"
    "v_cvt_pk_bf16_f32 v112, v112, v113\n"
    "v_cvt_pk_bf16_f32 v113, v114, v115\n"
    "v_mfma_f32_16x16x32_bf16 v[100:103], a[4:7], v[184:187], 0\n"
    "ds_write_b128 v243, v[96:99] offset:0\n"
    "v_mfma_f32_16x16x32_bf16 v[96:99], a[0:3], v[184:187], 0\n"
    "s_waitcnt lgkmcnt(7)\n"
    "v_or_b32_e32 v193, v193, v201\n"
    "v_cvt_pk_bf16_f32 v114, v116, v117\n"
    "v_cvt_pk_bf16_f32 v115, v118, v119\n"
    "v_cvt_pk_bf16_f32 v128, v128, v129\n"
    "v_cvt_pk_bf16_f32 v129, v130, v131\n"
    "v_or_b32_e32 v191, v191, v207\n"
    "v_mfma_f32_16x16x32_bf16 v[116:119], a[12:15], v[184:187], 0\n"
    "s_waitcnt lgkmcnt(5)\n"
    "ds_read_u16 v198, v198\n"
    "ds_write_b128 v243, v[112:115] offset:2048\n"
    "v_lshlrev_b32_sdwa v207, 1, v183 dst_sel:DWORD dst_unused:UNUSED_PAD src0_sel:DWORD src1_sel:WORD_1\n"
    "v_cvt_pk_bf16_f32 v130, v132, v133\n"
    "v_cvt_pk_bf16_f32 v131, v134, v135\n"
    "v_or_b32_e64 v194, v194, v202\n"
    "v_cvt_pk_bf16_f32 v104, v104, v105\n"
    "v_cvt_pk_bf16_f32 v105, v106, v107\n"
    "v_cvt_pk_bf16_f32 v106, v108, v109\n"
    "v_mfma_f32_16x16x32_bf16 v[180:183], a[100:103], a[236:239], 0\n"
    "s_waitcnt lgkmcnt(5)\n"
    "ds_read_u16_d16_hi v206, v206\n"
    "v_cvt_pk_bf16_f32 v107, v110, v111\n"
    "v_mfma_f32_16x16x32_bf16 v[180:183], a[148:151], a[240:243], v[180:183]\n"
    "s_nop 0\n"
    "v_cvt_pk_bf16_f32 v120, v120, v121\n"
    "v_cvt_pk_bf16_f32 v121, v122, v123\n"
    "v_mfma_f32_16x16x32_bf16 v[180:183], a[60:63], a[244:247], v[180:183]\n"
    "ds_read_u16 v199, v199\n"
    "ds_write_b128 v243, v[128:131] offset:4096\n"
    "v_cvt_pk_bf16_f32 v122, v124, v125\n"
    "v_cvt_pk_bf16_f32 v123, v126, v127\n"
    "v_or_b32_e32 v195, v195, v203\n"
    "v_cvt_pk_bf16_f32 v136, v136, v137\n"
    "v_pk_fma_f16 v232, v192, v192, s48\n"
    "v_mfma_f32_16x16x32_bf16 v[112:115], a[8:11], v[184:187], 0\n"
    "s_waitcnt lgkmcnt(5)\n"
    "v_cvt_pk_bf16_f32 v137, v138, v139\n"
    "v_cvt_pk_bf16_f32 v138, v140, v141\n"
    "v_mfma_f32_16x16x32_bf16 v[128:131], v[0:3], v[184:187], 0\n"
    "ds_read_u16_d16_hi v207, v207\n"
    "ds_write_b128 v243, v[104:107] offset:1024\n"
    "v_cvt_pk_bf16_f32 v139, v142, v143\n"
    "v_pk_mul_f16 v232, v192, v232\n"
    "v_or_b32_e64 v197, v197, v205\n"
    "v_exp_f16_e64 v234, v232\n"
    "v_mfma_f32_16x16x32_bf16 v[132:135], v[4:7], v[184:187], 0\n"
    "v_exp_f16_sdwa v234, v232 dst_sel:WORD_1 dst_unused:UNUSED_PRESERVE src0_sel:WORD_1\n"
    "v_mfma_f32_16x16x32_bf16 v[104:107], a[0:3], v[188:191], 0\n"
    "ds_write_b128 v243, v[120:123] offset:3072\n"
    "v_pk_fma_f16 v234, v234, s49, s49\n"
    "v_lshlrev_b32_sdwa v184, 1, v168 dst_sel:DWORD dst_unused:UNUSED_PAD src0_sel:DWORD src1_sel:WORD_1\n"
    "v_lshlrev_b32_sdwa v200, 1, v169 dst_sel:DWORD dst_unused:UNUSED_PAD src0_sel:DWORD src1_sel:WORD_1\n"
    "v_lshlrev_b32_sdwa v185, 1, v170 dst_sel:DWORD dst_unused:UNUSED_PAD src0_sel:DWORD src1_sel:WORD_1\n"
    "v_lshlrev_b32_sdwa v201, 1, v171 dst_sel:DWORD dst_unused:UNUSED_PAD src0_sel:DWORD src1_sel:WORD_1\n"
    "v_mfma_f32_16x16x32_bf16 v[108:111], a[4:7], v[188:191], 0\n"
    "s_waitcnt lgkmcnt(3)\n"
    "v_lshlrev_b32_sdwa v186, 1, v172 dst_sel:DWORD dst_unused:UNUSED_PAD src0_sel:DWORD src1_sel:WORD_1\n"
    "v_lshlrev_b32_sdwa v202, 1, v173 dst_sel:DWORD dst_unused:UNUSED_PAD src0_sel:DWORD src1_sel:WORD_1\n"
    "v_mfma_f32_16x16x32_bf16 v[120:123], a[8:11], v[188:191], 0\n"
    "s_nop 0\n"
    "ds_read_u16 v184, v184\n"
    "ds_write_b128 v243, v[136:139] offset:5120\n"
    "v_mfma_f32_16x16x32_bf16 v[124:127], a[12:15], v[188:191], 0\n"
    "s_waitcnt lgkmcnt(3)\n"
    "v_or_b32_e32 v198, v198, v206\n"
    "v_lshlrev_b32_sdwa v187, 1, v174 dst_sel:DWORD dst_unused:UNUSED_PAD src0_sel:DWORD src1_sel:WORD_1\n"
    "v_mfma_f32_16x16x32_bf16 v[136:139], v[0:3], v[188:191], 0\n"
    "v_lshlrev_b32_sdwa v203, 1, v175 dst_sel:DWORD dst_unused:UNUSED_PAD src0_sel:DWORD src1_sel:WORD_1\n"
    "v_rcp_f16_e64 v232, v234\n"
    "v_mfma_f32_16x16x32_bf16 v[140:143], v[4:7], v[188:191], 0\n"
    "ds_read_u16_d16_hi v200, v200\n"
    "ds_read_u16 v185, v185\n"
    "v_mfma_f32_16x16x32_bf16 v[168:171], a[104:107], a[224:227], 0\n"
    "ds_read_u16_d16_hi v201, v201\n"
    "ds_read_u16 v186, v186\n"
    "v_mfma_f32_16x16x32_bf16 v[168:171], a[152:155], a[228:231], v[168:171]\n"
    "v_rcp_f16_sdwa v232, v234 dst_sel:WORD_1 dst_unused:UNUSED_PRESERVE src0_sel:WORD_1\n"
    "v_mfma_f32_16x16x32_bf16 v[168:171], a[64:67], a[232:235], v[168:171]\n"
    "ds_read_u16_d16_hi v202, v202\n"
    "v_lshlrev_b32_sdwa v188, 1, v176 dst_sel:DWORD dst_unused:UNUSED_PAD src0_sel:DWORD src1_sel:WORD_1\n"
    "v_mfma_f32_16x16x32_bf16 v[172:175], a[108:111], a[224:227], 0\n"
    "v_lshlrev_b32_sdwa v204, 1, v177 dst_sel:DWORD dst_unused:UNUSED_PAD src0_sel:DWORD src1_sel:WORD_1\n"
    "v_lshlrev_b32_sdwa v189, 1, v178 dst_sel:DWORD dst_unused:UNUSED_PAD src0_sel:DWORD src1_sel:WORD_1\n"
    "v_mfma_f32_16x16x32_bf16 v[172:175], a[156:159], a[228:231], v[172:175]\n"
    "ds_read_u16 v187, v187\n"
    "v_lshlrev_b32_sdwa v205, 1, v179 dst_sel:DWORD dst_unused:UNUSED_PAD src0_sel:DWORD src1_sel:WORD_1\n"
    "v_lshlrev_b32_sdwa v190, 1, v180 dst_sel:DWORD dst_unused:UNUSED_PAD src0_sel:DWORD src1_sel:WORD_1\n"
    "v_lshlrev_b32_sdwa v206, 1, v181 dst_sel:DWORD dst_unused:UNUSED_PAD src0_sel:DWORD src1_sel:WORD_1\n"
    "v_lshlrev_b32_sdwa v191, 1, v182 dst_sel:DWORD dst_unused:UNUSED_PAD src0_sel:DWORD src1_sel:WORD_1\n"
    "v_fma_mix_f32 v234, v192, v232, 0 op_sel_hi:[1,1,0]\n"
    "v_mfma_f32_16x16x32_bf16 v[172:175], a[68:71], a[232:235], v[172:175]\n"
    "s_waitcnt lgkmcnt(6)\n"
    "ds_read_u16_d16_hi v203, v203\n"
    "v_or_b32_e32 v199, v199, v207\n"
    "v_lshlrev_b32_sdwa v207, 1, v183 dst_sel:DWORD dst_unused:UNUSED_PAD src0_sel:DWORD src1_sel:WORD_1\n"
    "v_fma_mix_f32 v232, v192, v232, 0 op_sel:[1,1,0] op_sel_hi:[1,1,0]\n"
    "v_mfma_f32_16x16x32_bf16 v[176:179], a[104:107], a[236:239], 0\n"
    "s_barrier\n"
    "v_cvt_pk_bf16_f32 v192, v234, v232\n"
    "v_mfma_f32_16x16x32_bf16 v[176:179], a[152:155], a[240:243], v[176:179]\n"
    "s_waitcnt lgkmcnt(5)\n"
    "ds_read_u16 v188, v188\n"
    "ds_read_u16_d16_hi v204, v204\n"
    "v_mfma_f32_16x16x32_bf16 v[176:179], a[64:67], a[244:247], v[176:179]\n"
    "ds_read_u16 v189, v189\n"
    "ds_read_u16_d16_hi v205, v205\n"
    "v_pk_fma_f16 v232, v196, v196, s48\n"
    "v_mfma_f32_16x16x32_bf16 v[180:183], a[108:111], a[236:239], 0\n"
    "s_waitcnt lgkmcnt(8)\n"
    "v_pk_mul_f16 v232, v196, v232\n"
    "v_or_b32_e32 v184, v184, v200\n"
    "v_mfma_f32_16x16x32_bf16 v[180:183], a[156:159], a[240:243], v[180:183]\n"
    "ds_read_u16 v190, v190\n"
    "v_exp_f16_e32 v234, v232\n"
    "v_mfma_f32_16x16x32_bf16 v[180:183], a[68:71], a[244:247], v[180:183]\n"
    "v_exp_f16_sdwa v234, v232 dst_sel:WORD_1 dst_unused:UNUSED_PRESERVE src0_sel:WORD_1\n"
    "v_mfma_f32_16x16x32_bf16 v[96:99], a[16:19], v[192:195], v[96:99]\n"
    "s_waitcnt lgkmcnt(7)\n"
    "ds_read_u16_d16_hi v206, v206\n"
    "v_pk_fma_f16 v234, v234, s49, s49\n"
    "v_mfma_f32_16x16x32_bf16 v[100:103], a[20:23], v[192:195], v[100:103]\n"
    "ds_read_u16 v191, v191\n"
    "v_rcp_f16_e32 v232, v234\n"
    "v_mfma_f32_16x16x32_bf16 v[112:115], a[24:27], v[192:195], v[112:115]\n"
    "v_rcp_f16_sdwa v232, v234 dst_sel:WORD_1 dst_unused:UNUSED_PRESERVE src0_sel:WORD_1\n"
    "v_mfma_f32_16x16x32_bf16 v[116:119], a[28:31], v[192:195], v[116:119]\n"
    "s_waitcnt lgkmcnt(7)\n"
    "ds_read_u16_d16_hi v207, v207\n"
    "v_fma_mix_f32 v234, v196, v232, 0 op_sel_hi:[1,1,0]\n"
    "v_mfma_f32_16x16x32_bf16 v[128:131], v[8:11], v[192:195], v[128:131]\n"
    "v_fma_mix_f32 v232, v196, v232, 0 op_sel:[1,1,0] op_sel_hi:[1,1,0]\n"
    "v_or_b32_e64 v185, v185, v201\n"
    "v_mfma_f32_16x16x32_bf16 v[132:135], v[12:15], v[192:195], v[132:135]\n"
    "v_cvt_pk_bf16_f32 v196, v234, v232\n"
    "v_cvt_pk_f16_f32 v192, v168, v169\n"
    "v_lshlrev_b32_sdwa v193, 1, v170 dst_sel:DWORD dst_unused:UNUSED_PAD src0_sel:DWORD src1_sel:WORD_1\n"
    "v_mfma_f32_16x16x32_bf16 v[104:107], a[16:19], v[196:199], v[104:107]\n"
    "s_waitcnt lgkmcnt(6)\n"
    "ds_read_u16 v193, v193\n"
    "v_lshlrev_b32_sdwa v201, 1, v171 dst_sel:DWORD dst_unused:UNUSED_PAD src0_sel:DWORD src1_sel:WORD_1\n"
    "v_mfma_f32_16x16x32_bf16 v[108:111], a[20:23], v[196:199], v[108:111]\n"
    "s_waitcnt vmcnt(7)\n"
    "ds_read_u16_d16_hi v201, v201\n"
    "v_lshlrev_b32_sdwa v194, 1, v172 dst_sel:DWORD dst_unused:UNUSED_PAD src0_sel:DWORD src1_sel:WORD_1\n"
    "v_mfma_f32_16x16x32_bf16 v[120:123], a[24:27], v[196:199], v[120:123]\n"
    "ds_read_u16 v194, v194\n"
    "v_or_b32_e64 v186, v186, v202\n"
    "v_mfma_f32_16x16x32_bf16 v[124:127], a[28:31], v[196:199], v[124:127]\n"
    "v_lshlrev_b32_sdwa v202, 1, v173 dst_sel:DWORD dst_unused:UNUSED_PAD src0_sel:DWORD src1_sel:WORD_1\n"
    "v_lshlrev_b32_sdwa v195, 1, v174 dst_sel:DWORD dst_unused:UNUSED_PAD src0_sel:DWORD src1_sel:WORD_1\n"
    "v_mfma_f32_16x16x32_bf16 v[136:139], v[8:11], v[196:199], v[136:139]\n"
    "s_waitcnt lgkmcnt(7)\n"
    "ds_read_u16_d16_hi v202, v202\n"
    "v_or_b32_e64 v187, v187, v203\n"
    "v_mfma_f32_16x16x32_bf16 v[140:143], v[12:15], v[196:199], v[140:143]\n"
    "s_waitcnt vmcnt(6)\n"
    "ds_read_u16 v195, v195\n"
    "v_lshlrev_b32_sdwa v203, 1, v175 dst_sel:DWORD dst_unused:UNUSED_PAD src0_sel:DWORD src1_sel:WORD_1\n"
    "v_smfmac_f32_32x32x32_bf16 v[112:127], a[248:251], v[152:159], v241\n"
    "v_cvt_pk_f16_f32 v196, v176, v177\n"
    "v_lshlrev_b32_sdwa v197, 1, v178 dst_sel:DWORD dst_unused:UNUSED_PAD src0_sel:DWORD src1_sel:WORD_1\n"
    "v_lshlrev_b32_sdwa v198, 1, v180 dst_sel:DWORD dst_unused:UNUSED_PAD src0_sel:DWORD src1_sel:WORD_1\n"
    "v_lshlrev_b32_sdwa v199, 1, v182 dst_sel:DWORD dst_unused:UNUSED_PAD src0_sel:DWORD src1_sel:WORD_1\n"
    "v_or_b32_e32 v188, v188, v204\n"
    "v_smfmac_f32_32x32x32_bf16 v[96:111], a[248:251], v[144:151], v241\n"
    "s_waitcnt lgkmcnt(7)\n"
    "ds_read_b128 v[152:155], v242 offset:0\n"
    "ds_read_b128 v[156:159], v242 offset:1024\n"
    "v_or_b32_e64 v189, v189, v205\n"
    "v_lshlrev_b32_sdwa v205, 1, v179 dst_sel:DWORD dst_unused:UNUSED_PAD src0_sel:DWORD src1_sel:WORD_1\n"
    "v_or_b32_e32 v190, v190, v206\n"
    "v_smfmac_f32_32x32x32_bf16 v[128:143], a[248:251], v[208:215], v241\n"
    "s_waitcnt lgkmcnt(7)\n"
    "ds_read_u16_d16_hi v203, v203\n"
    "ds_read_u16 v197, v197\n"
    "v_lshlrev_b32_sdwa v206, 1, v181 dst_sel:DWORD dst_unused:UNUSED_PAD src0_sel:DWORD src1_sel:WORD_1\n"
    "v_or_b32_e64 v191, v191, v207\n"
    "v_lshlrev_b32_sdwa v207, 1, v183 dst_sel:DWORD dst_unused:UNUSED_PAD src0_sel:DWORD src1_sel:WORD_1\n"
    "v_mfma_f32_16x16x32_bf16 v[168:171], a[112:115], a[224:227], 0\n"
    "v_pk_fma_f16 v232, v192, v192, s48\n"
    "v_pk_mul_f16 v232, v192, v232\n"
    "v_mfma_f32_16x16x32_bf16 v[168:171], a[160:163], a[228:231], v[168:171]\n"
    "ds_read_u16_d16_hi v205, v205\n"
    "v_mfma_f32_16x16x32_bf16 v[168:171], a[72:75], a[232:235], v[168:171]\n"
    "s_waitcnt lgkmcnt(8)\n"
    "ds_read_u16 v198, v198\n"
    "v_or_b32_e32 v193, v193, v201\n"
    "v_mfma_f32_16x16x32_bf16 v[172:175], a[116:119], a[224:227], 0\n"
    "v_exp_f16_e64 v234, v232\n"
    "v_mfma_f32_16x16x32_bf16 v[172:175], a[164:167], a[228:231], v[172:175]\n"
    "ds_read_u16_d16_hi v206, v206\n"
    "v_mfma_f32_16x16x32_bf16 v[172:175], v[52:55], a[232:235], v[172:175]\n"
    "s_nop 0\n"
    "v_exp_f16_sdwa v234, v232 dst_sel:WORD_1 dst_unused:UNUSED_PRESERVE src0_sel:WORD_1\n"
    "v_mfma_f32_16x16x32_bf16 v[176:179], a[112:115], a[236:239], 0\n"
    "s_waitcnt lgkmcnt(8)\n"
    "ds_read_u16 v199, v199\n"
    "v_or_b32_e32 v194, v194, v202\n"
    "v_mfma_f32_16x16x32_bf16 v[176:179], a[160:163], a[240:243], v[176:179]\n"
    "v_pk_fma_f16 v234, v234, s49, s49\n"
    "v_lshlrev_b32_sdwa v200, 1, v169 dst_sel:DWORD dst_unused:UNUSED_PAD src0_sel:DWORD src1_sel:WORD_1\n"
    "v_mfma_f32_16x16x32_bf16 v[176:179], a[72:75], a[244:247], v[176:179]\n"
    "s_waitcnt lgkmcnt(6)\n"
    "ds_read_u16_d16_hi v207, v207\n"
    "v_lshlrev_b32_sdwa v201, 1, v171 dst_sel:DWORD dst_unused:UNUSED_PAD src0_sel:DWORD src1_sel:WORD_1\n"
    "v_smfmac_f32_32x32x32_bf16 v[96:111], a[248:251], v[152:159], v241\n"
    "ds_read_b128 v[152:155], v242 offset:2048\n"
    "v_lshlrev_b32_sdwa v202, 1, v173 dst_sel:DWORD dst_unused:UNUSED_PAD src0_sel:DWORD src1_sel:WORD_1\n"
    "v_rcp_f16_e64 v232, v234\n"
    "ds_read_b128 v[156:159], v242 offset:3072\n"
    "ds_read_u16_d16_hi v200, v200\n"
    "v_mfma_f32_16x16x32_bf16 v[180:183], a[116:119], a[236:239], 0\n"
    "s_waitcnt lgkmcnt(9)\n"
    "v_or_b32_e32 v195, v195, v203\n"
    "v_lshlrev_b32_sdwa v203, 1, v175 dst_sel:DWORD dst_unused:UNUSED_PAD src0_sel:DWORD src1_sel:WORD_1\n"
    "v_mfma_f32_16x16x32_bf16 v[180:183], a[164:167], a[240:243], v[180:183]\n"
    "ds_read_u16_d16_hi v201, v201\n"
    "v_lshlrev_b32_sdwa v204, 1, v177 dst_sel:DWORD dst_unused:UNUSED_PAD src0_sel:DWORD src1_sel:WORD_1\n"
    "v_mfma_f32_16x16x32_bf16 v[180:183], v[52:55], a[244:247], v[180:183]\n"
    "s_nop 0\n"
    "v_rcp_f16_sdwa v232, v234 dst_sel:WORD_1 dst_unused:UNUSED_PRESERVE src0_sel:WORD_1\n"
    "v_mfma_f32_16x16x32_bf16 v[96:99], a[32:35], v[184:187], v[96:99]\n"
    "s_waitcnt lgkmcnt(8)\n"
    "ds_read_u16_d16_hi v202, v202\n"
    "v_or_b32_e64 v197, v197, v205\n"
    "v_mfma_f32_16x16x32_bf16 v[100:103], a[36:39], v[184:187], v[100:103]\n"
    "v_lshlrev_b32_sdwa v205, 1, v179 dst_sel:DWORD dst_unused:UNUSED_PAD src0_sel:DWORD src1_sel:WORD_1\n"
    "v_fma_mix_f32 v234, v192, v232, 0 op_sel_hi:[1,1,0]\n"
    "v_mfma_f32_16x16x32_bf16 v[112:115], a[40:43], v[184:187], v[112:115]\n"
    "ds_read_u16_d16_hi v203, v203\n"
    "v_fma_mix_f32 v232, v192, v232, 0 op_sel:[1,1,0] op_sel_hi:[1,1,0]\n"
    "v_mfma_f32_16x16x32_bf16 v[116:119], a[44:47], v[184:187], v[116:119]\n"
    "s_waitcnt lgkmcnt(8)\n"
    "v_or_b32_e32 v198, v198, v206\n"
    "v_lshlrev_b32_sdwa v206, 1, v181 dst_sel:DWORD dst_unused:UNUSED_PAD src0_sel:DWORD src1_sel:WORD_1\n"
    "v_mfma_f32_16x16x32_bf16 v[128:131], v[16:19], v[184:187], v[128:131]\n"
    "ds_read_u16_d16_hi v204, v204\n"
    "v_cvt_pk_bf16_f32 v192, v234, v232\n"
    "v_mfma_f32_16x16x32_bf16 v[132:135], v[20:23], v[184:187], v[132:135]\n"
    "ds_read_u16_d16_hi v205, v205\n"
    "v_lshlrev_b32_sdwa v184, 1, v168 dst_sel:DWORD dst_unused:UNUSED_PAD src0_sel:DWORD src1_sel:WORD_1\n"
    "v_mfma_f32_16x16x32_bf16 v[104:107], a[32:35], v[188:191], v[104:107]\n"
    "s_nop 0\n"
    "v_lshlrev_b32_sdwa v185, 1, v170 dst_sel:DWORD dst_unused:UNUSED_PAD src0_sel:DWORD src1_sel:WORD_1\n"
    "v_lshlrev_b32_sdwa v186, 1, v172 dst_sel:DWORD dst_unused:UNUSED_PAD src0_sel:DWORD src1_sel:WORD_1\n"
    "v_mfma_f32_16x16x32_bf16 v[108:111], a[36:39], v[188:191], v[108:111]\n"
    "s_waitcnt lgkmcnt(7)\n"
    "ds_read_u16 v184, v184\n"
    "v_lshlrev_b32_sdwa v187, 1, v174 dst_sel:DWORD dst_unused:UNUSED_PAD src0_sel:DWORD src1_sel:WORD_1\n"
    "v_mfma_f32_16x16x32_bf16 v[120:123], a[40:43], v[188:191], v[120:123]\n"
    "v_or_b32_e32 v199, v199, v207\n"
    "v_lshlrev_b32_sdwa v207, 1, v183 dst_sel:DWORD dst_unused:UNUSED_PAD src0_sel:DWORD src1_sel:WORD_1\n"
    "v_mfma_f32_16x16x32_bf16 v[124:127], a[44:47], v[188:191], v[124:127]\n"
    "s_waitcnt lgkmcnt(7)\n"
    "ds_read_u16 v185, v185\n"
    "v_pk_fma_f16 v232, v196, v196, s48\n"
    "v_pk_mul_f16 v232, v196, v232\n"
    "v_mfma_f32_16x16x32_bf16 v[136:139], v[16:19], v[188:191], v[136:139]\n"
    "ds_read_u16 v186, v186\n"
    "v_exp_f16_e64 v234, v232\n"
    "v_mfma_f32_16x16x32_bf16 v[140:143], v[20:23], v[188:191], v[140:143]\n"
    "v_lshlrev_b32_sdwa v188, 1, v176 dst_sel:DWORD dst_unused:UNUSED_PAD src0_sel:DWORD src1_sel:WORD_1\n"
    "v_lshlrev_b32_sdwa v189, 1, v178 dst_sel:DWORD dst_unused:UNUSED_PAD src0_sel:DWORD src1_sel:WORD_1\n"
    "v_smfmac_f32_32x32x32_bf16 v[112:127], a[248:251], v[152:159], v241\n"
    "ds_read_u16 v187, v187\n"
    "v_lshlrev_b32_sdwa v190, 1, v180 dst_sel:DWORD dst_unused:UNUSED_PAD src0_sel:DWORD src1_sel:WORD_1\n"
    "v_lshlrev_b32_sdwa v191, 1, v182 dst_sel:DWORD dst_unused:UNUSED_PAD src0_sel:DWORD src1_sel:WORD_1\n"
    "v_exp_f16_sdwa v234, v232 dst_sel:WORD_1 dst_unused:UNUSED_PRESERVE src0_sel:WORD_1\n"
    "v_mfma_f32_16x16x32_bf16 v[168:171], a[120:123], a[224:227], 0\n"
    "ds_read_u16 v188, v188\n"
    "v_pk_fma_f16 v234, v234, s49, s49\n"
    "v_mfma_f32_16x16x32_bf16 v[168:171], a[168:171], a[228:231], v[168:171]\n"
    "ds_read_u16 v189, v189\n"
    "v_mfma_f32_16x16x32_bf16 v[168:171], a[76:79], a[232:235], v[168:171]\n"
    "v_rcp_f16_e64 v232, v234\n"
    "v_mfma_f32_16x16x32_bf16 v[172:175], a[124:127], a[224:227], 0\n"
    "ds_read_b128 v[152:155], v242 offset:4096\n"
    "v_mfma_f32_16x16x32_bf16 v[172:175], a[172:175], a[228:231], v[172:175]\n"
    "ds_read_b128 v[156:159], v242 offset:5120\n"
    "v_mfma_f32_16x16x32_bf16 v[172:175], v[60:63], a[232:235], v[172:175]\n"
    "s_nop 0\n"
    "ds_read_u16 v190, v190\n"
    "v_rcp_f16_sdwa v232, v234 dst_sel:WORD_1 dst_unused:UNUSED_PRESERVE src0_sel:WORD_1\n"
    "v_mfma_f32_16x16x32_bf16 v[176:179], a[120:123], a[236:239], 0\n"
    "s_waitcnt lgkmcnt(8)\n"
    "v_fma_mix_f32 v234, v196, v232, 0 op_sel_hi:[1,1,0]\n"
    "v_fma_mix_f32 v232, v196, v232, 0 op_sel:[1,1,0] op_sel_hi:[1,1,0]\n"
    "v_mfma_f32_16x16x32_bf16 v[176:179], a[168:171], a[240:243], v[176:179]\n"
    "ds_read_u16_d16_hi v206, v206\n"
    "v_cvt_pk_bf16_f32 v196, v234, v232\n"
    "v_mfma_f32_16x16x32_bf16 v[176:179], a[76:79], a[244:247], v[176:179]\n"
    "s_waitcnt lgkmcnt(8)\n"
    "ds_read_u16 v191, v191\n"
    "v_or_b32_e32 v184, v184, v200\n"
    "v_mfma_f32_16x16x32_bf16 v[180:183], a[124:127], a[236:239], 0\n"
    "v_or_b32_e32 v185, v185, v201\n"
    "v_lshlrev_b32_sdwa v201, 1, v171 dst_sel:DWORD dst_unused:UNUSED_PAD src0_sel:DWORD src1_sel:WORD_1\n"
    "v_mfma_f32_16x16x32_bf16 v[180:183], a[172:175], a[240:243], v[180:183]\n"
    "s_waitcnt lgkmcnt(8)\n"
    "ds_read_u16_d16_hi v207, v207\n"
    "v_or_b32_e32 v186, v186, v202\n"
    "v_mfma_f32_16x16x32_bf16 v[180:183], v[60:63], a[244:247], v[180:183]\n"
    "v_lshlrev_b32_sdwa v202, 1, v173 dst_sel:DWORD dst_unused:UNUSED_PAD src0_sel:DWORD src1_sel:WORD_1\n"
    "v_mfma_f32_16x16x32_bf16 v[96:99], a[176:179], v[192:195], v[96:99]\n"
    "s_waitcnt lgkmcnt(8)\n"
    "ds_read_u16_d16_hi v201, v201\n"
    "v_or_b32_e32 v187, v187, v203\n"
    "v_mfma_f32_16x16x32_bf16 v[100:103], a[180:183], v[192:195], v[100:103]\n"
    "s_waitcnt lgkmcnt(8)\n"
    "v_lshlrev_b32_sdwa v203, 1, v175 dst_sel:DWORD dst_unused:UNUSED_PAD src0_sel:DWORD src1_sel:WORD_1\n"
    "v_or_b32_e64 v188, v188, v204\n"
    "v_mfma_f32_16x16x32_bf16 v[112:115], a[184:187], v[192:195], v[112:115]\n"
    "ds_read_u16_d16_hi v202, v202\n"
    "v_mfma_f32_16x16x32_bf16 v[116:119], a[188:191], v[192:195], v[116:119]\n"
    "s_waitcnt lgkmcnt(7)\n"
    "ds_read_u16_d16_hi v203, v203\n"
    "v_or_b32_e32 v189, v189, v205\n"
    "v_mfma_f32_16x16x32_bf16 v[128:131], v[24:27], v[192:195], v[128:131]\n"
    "v_lshlrev_b32_sdwa v205, 1, v179 dst_sel:DWORD dst_unused:UNUSED_PAD src0_sel:DWORD src1_sel:WORD_1\n"
    "v_mfma_f32_16x16x32_bf16 v[132:135], v[28:31], v[192:195], v[132:135]\n"
    "ds_read_u16_d16_hi v205, v205\n"
    "v_cvt_pk_f16_f32 v192, v168, v169\n"
    "v_mfma_f32_16x16x32_bf16 v[104:107], a[176:179], v[196:199], v[104:107]\n"
    "v_lshlrev_b32_sdwa v193, 1, v170 dst_sel:DWORD dst_unused:UNUSED_PAD src0_sel:DWORD src1_sel:WORD_1\n"
    "v_lshlrev_b32_sdwa v194, 1, v172 dst_sel:DWORD dst_unused:UNUSED_PAD src0_sel:DWORD src1_sel:WORD_1\n"
    "v_mfma_f32_16x16x32_bf16 v[108:111], a[180:183], v[196:199], v[108:111]\n"
    "ds_read_u16 v193, v193\n"
    "v_lshlrev_b32_sdwa v195, 1, v174 dst_sel:DWORD dst_unused:UNUSED_PAD src0_sel:DWORD src1_sel:WORD_1\n"
    "v_pk_fma_f16 v232, v192, v192, s48\n"
    "v_mfma_f32_16x16x32_bf16 v[120:123], a[184:187], v[196:199], v[120:123]\n"
    "s_waitcnt lgkmcnt(7)\n"
    "ds_read_u16 v194, v194\n"
    "v_or_b32_e32 v190, v190, v206\n"
    "v_mfma_f32_16x16x32_bf16 v[124:127], a[188:191], v[196:199], v[124:127]\n"
    "v_lshlrev_b32_sdwa v206, 1, v181 dst_sel:DWORD dst_unused:UNUSED_PAD src0_sel:DWORD src1_sel:WORD_1\n"
    "v_pk_mul_f16 v232, v192, v232\n"
    "v_mfma_f32_16x16x32_bf16 v[136:139], v[24:27], v[196:199], v[136:139]\n"
    "ds_read_u16 v195, v195\n"
    "v_exp_f16_e32 v234, v232\n"
    "v_mfma_f32_16x16x32_bf16 v[140:143], v[28:31], v[196:199], v[140:143]\n"
    "v_cvt_pk_f16_f32 v196, v176, v177\n"
    "v_lshlrev_b32_sdwa v197, 1, v178 dst_sel:DWORD dst_unused:UNUSED_PAD src0_sel:DWORD src1_sel:WORD_1\n"
    "v_mfma_f32_16x16x32_bf16 v[96:99], a[192:195], v[184:187], v[96:99]\n"
    "s_waitcnt lgkmcnt(7)\n"
    "ds_read_u16 v197, v197\n"
    "v_lshlrev_b32_sdwa v198, 1, v180 dst_sel:DWORD dst_unused:UNUSED_PAD src0_sel:DWORD src1_sel:WORD_1\n"
    "v_smfmac_f32_32x32x32_bf16 v[128:143], a[248:251], v[152:159], v241\n"
    "ds_read_u16 v198, v198\n"
    "v_lshlrev_b32_sdwa v199, 1, v182 dst_sel:DWORD dst_unused:UNUSED_PAD src0_sel:DWORD src1_sel:WORD_1\n"
    "v_or_b32_e64 v191, v191, v207\n"
    "v_lshlrev_b32_sdwa v207, 1, v183 dst_sel:DWORD dst_unused:UNUSED_PAD src0_sel:DWORD src1_sel:WORD_1\n"
    "v_exp_f16_sdwa v234, v232 dst_sel:WORD_1 dst_unused:UNUSED_PRESERVE src0_sel:WORD_1\n"
    "v_mfma_f32_16x16x32_bf16 v[100:103], a[196:199], v[184:187], v[100:103]\n"
    "ds_read_u16_d16_hi v206, v206\n"
    "v_pk_fma_f16 v234, v234, s49, s49\n"
    "v_mfma_f32_16x16x32_bf16 v[112:115], a[200:203], v[184:187], v[112:115]\n"
    "v_rcp_f16_e64 v232, v234\n"
    "v_mfma_f32_16x16x32_bf16 v[116:119], a[204:207], v[184:187], v[116:119]\n"
    "ds_read_u16 v199, v199\n"
    "v_rcp_f16_sdwa v232, v234 dst_sel:WORD_1 dst_unused:UNUSED_PRESERVE src0_sel:WORD_1\n"
    "v_mfma_f32_16x16x32_bf16 v[128:131], v[32:35], v[184:187], v[128:131]\n"
    "ds_read_u16_d16_hi v207, v207\n"
    "v_fma_mix_f32 v234, v192, v232, 0 op_sel_hi:[1,1,0]\n"
    "v_mfma_f32_16x16x32_bf16 v[132:135], v[36:39], v[184:187], v[132:135]\n"
    "v_fma_mix_f32 v232, v192, v232, 0 op_sel:[1,1,0] op_sel_hi:[1,1,0]\n"
    "v_cvt_pk_bf16_f32 v192, v234, v232\n"
    "v_mfma_f32_16x16x32_bf16 v[104:107], a[192:195], v[188:191], v[104:107]\n"
    "s_waitcnt lgkmcnt(7)\n"
    "v_or_b32_e32 v193, v193, v201\n"
    "v_pk_fma_f16 v232, v196, v196, s48\n"
    "v_mfma_f32_16x16x32_bf16 v[108:111], a[196:199], v[188:191], v[108:111]\n"
    "v_pk_mul_f16 v232, v196, v232\n"
    "v_exp_f16_e64 v234, v232\n"
    "v_mfma_f32_16x16x32_bf16 v[120:123], a[200:203], v[188:191], v[120:123]\n"
    "s_waitcnt lgkmcnt(6)\n"
    "v_or_b32_e32 v194, v194, v202\n"
    "v_exp_f16_sdwa v234, v232 dst_sel:WORD_1 dst_unused:UNUSED_PRESERVE src0_sel:WORD_1\n"
    "v_mfma_f32_16x16x32_bf16 v[124:127], a[204:207], v[188:191], v[124:127]\n"
    "s_waitcnt lgkmcnt(5)\n"
    "v_or_b32_e32 v195, v195, v203\n"
    "v_pk_fma_f16 v234, v234, s49, s49\n"
    "v_mfma_f32_16x16x32_bf16 v[136:139], v[32:35], v[188:191], v[136:139]\n"
    "v_rcp_f16_e64 v232, v234\n"
    "v_mfma_f32_16x16x32_bf16 v[140:143], v[36:39], v[188:191], v[140:143]\n"
    "s_waitcnt lgkmcnt(4)\n"
    "v_or_b32_e32 v197, v197, v205\n"
    "v_rcp_f16_sdwa v232, v234 dst_sel:WORD_1 dst_unused:UNUSED_PRESERVE src0_sel:WORD_1\n"
    "v_mfma_f32_16x16x32_bf16 v[96:99], a[208:211], v[192:195], v[96:99]\n"
    "v_fma_mix_f32 v234, v196, v232, 0 op_sel_hi:[1,1,0]\n"
    "v_fma_mix_f32 v232, v196, v232, 0 op_sel:[1,1,0] op_sel_hi:[1,1,0]\n"
    "v_mfma_f32_16x16x32_bf16 v[100:103], a[212:215], v[192:195], v[100:103]\n"
    "v_cvt_pk_bf16_f32 v196, v234, v232\n"
    "v_mfma_f32_16x16x32_bf16 v[112:115], a[216:219], v[192:195], v[112:115]\n"
    "s_waitcnt lgkmcnt(2)\n"
    "v_or_b32_e32 v198, v198, v206\n"
    "v_mfma_f32_16x16x32_bf16 v[116:119], a[220:223], v[192:195], v[116:119]\n"
    "v_cvt_pk_bf16_f32 v96, v96, v97\n"
    "v_cvt_pk_bf16_f32 v97, v98, v99\n"
    "v_mfma_f32_16x16x32_bf16 v[128:131], v[40:43], v[192:195], v[128:131]\n"
    "v_cvt_pk_bf16_f32 v98, v100, v101\n"
    "v_cvt_pk_bf16_f32 v99, v102, v103\n"
    "v_mfma_f32_16x16x32_bf16 v[132:135], v[44:47], v[192:195], v[132:135]\n"
    "s_waitcnt lgkmcnt(0)\n"
    "buffer_store_dwordx4 v[96:99], v245, s[44:47], s58 offen offset:0 sc1\n"
    "v_or_b32_e32 v199, v199, v207\n"
    "v_cvt_pk_bf16_f32 v112, v112, v113\n"
    "v_cvt_pk_bf16_f32 v113, v114, v115\n"
    "v_mfma_f32_16x16x32_bf16 v[104:107], a[208:211], v[196:199], v[104:107]\n"
    "v_cvt_pk_bf16_f32 v114, v116, v117\n"
    "v_cvt_pk_bf16_f32 v115, v118, v119\n"
    "v_mfma_f32_16x16x32_bf16 v[108:111], a[212:215], v[196:199], v[108:111]\n"
    "buffer_store_dwordx4 v[112:115], v245, s[44:47], s58 offen offset:64 sc1\n"
    "v_mfma_f32_16x16x32_bf16 v[120:123], a[216:219], v[196:199], v[120:123]\n"
    "v_cvt_pk_bf16_f32 v128, v128, v129\n"
    "v_cvt_pk_bf16_f32 v129, v130, v131\n"
    "v_mfma_f32_16x16x32_bf16 v[124:127], a[220:223], v[196:199], v[124:127]\n"
    "v_cvt_pk_bf16_f32 v130, v132, v133\n"
    "v_cvt_pk_bf16_f32 v131, v134, v135\n"
    "v_mfma_f32_16x16x32_bf16 v[136:139], v[40:43], v[196:199], v[136:139]\n"
    "buffer_store_dwordx4 v[128:131], v245, s[44:47], s58 offen offset:128 sc1\n"
    "v_mfma_f32_16x16x32_bf16 v[140:143], v[44:47], v[196:199], v[140:143]\n"
    "v_cvt_pk_bf16_f32 v104, v104, v105\n"
    "v_cvt_pk_bf16_f32 v105, v106, v107\n"
    "v_cvt_pk_bf16_f32 v106, v108, v109\n"
    "v_cvt_pk_bf16_f32 v107, v110, v111\n"
    "buffer_store_dwordx4 v[104:107], v245, s[44:47], s89 offen offset:0 sc1\n"
    "v_cvt_pk_bf16_f32 v120, v120, v121\n"
    "v_cvt_pk_bf16_f32 v121, v122, v123\n"
    "v_cvt_pk_bf16_f32 v122, v124, v125\n"
    "v_cvt_pk_bf16_f32 v123, v126, v127\n"
    "buffer_store_dwordx4 v[120:123], v245, s[44:47], s89 offen offset:64 sc1\n"
    "v_cvt_pk_bf16_f32 v136, v136, v137\n"
    "v_cvt_pk_bf16_f32 v137, v138, v139\n"
    "v_cvt_pk_bf16_f32 v138, v140, v141\n"
    "v_cvt_pk_bf16_f32 v139, v142, v143\n"
    "buffer_store_dwordx4 v[136:139], v245, s[44:47], s89 offen offset:128 sc1\n"
    "97:\n"
    "98:\n"
    "s_waitcnt vmcnt(0) lgkmcnt(0)\n"
    ".Lreplk_end:\n"
    : "+{s66}"(flg), "+{v208}"(tid), "+{s65}"(blk), "+{s[98:99]}"(ka)
    : "{s[96:97]}"(CTAB)
    : "memory", "scc", "vcc", "m0", "v0", "v1", "v2", "v3", "v4", "v5", "v6", "v7", "v8", "v9", "v10", "v11", "v12", "v13", "v14", "v15", "v16", "v17", "v18", "v19", "v20", "v21", "v22", "v23", "v24", "v25", "v26", "v27", "v28", "v29", "v30", "v31", "v32", "v33", "v34", "v35", "v36", "v37", "v38", "v39", "v40", "v41", "v42", "v43", "v44", "v45", "v46", "v47", "v48", "v49", "v50", "v51", "v52", "v53", "v54", "v55", "v56", "v57", "v58", "v59", "v60", "v61", "v62", "v63", "v64", "v65", "v66", "v67", "v68", "v69", "v70", "v71", "v72", "v73", "v74", "v75", "v76", "v77", "v78", "v79", "v80", "v81", "v82", "v83", "v84", "v85", "v86", "v87", "v88", "v89", "v90", "v91", "v92", "v93", "v94", "v95", "v96", "v97", "v98", "v99", "v100", "v101", "v102", "v103", "v104", "v105", "v106", "v107", "v108", "v109", "v110", "v111", "v112", "v113", "v114", "v115", "v116", "v117", "v118", "v119", "v120", "v121", "v122", "v123", "v124", "v125", "v126", "v127", "v128", "v129", "v130", "v131", "v132", "v133", "v134", "v135", "v136", "v137", "v138", "v139", "v140", "v141", "v142", "v143", "v144", "v145", "v146", "v147", "v148", "v149", "v150", "v151", "v152", "v153", "v154", "v155", "v156", "v157", "v158", "v159", "v160", "v161", "v162", "v163", "v164", "v165", "v166", "v167", "v168", "v169", "v170", "v171", "v172", "v173", "v174", "v175", "v176", "v177", "v178", "v179", "v180", "v181", "v182", "v183", "v184", "v185", "v186", "v187", "v188", "v189", "v190", "v191", "v192", "v193", "v194", "v195", "v196", "v197", "v198", "v199", "v200", "v201", "v202", "v203", "v204", "v205", "v206", "v207", "v209", "v210", "v211", "v212", "v213", "v214", "v215", "v216", "v217", "v218", "v219", "v220", "v221", "v222", "v223", "v224", "v225", "v226", "v227", "v228", "v229", "v230", "v231", "v232", "v233", "v234", "v235", "v236", "v237", "v238", "v239", "v240", "v241", "v242", "v243", "v244", "v245", "v246", "v247", "v248", "v249", "v250", "v251", "v252", "v253", "v254", "v255", "a0", "a1", "a2", "a3", "a4", "a5", "a6", "a7", "a8", "a9", "a10", "a11", "a12", "a13", "a14", "a15", "a16", "a17", "a18", "a19", "a20", "a21", "a22", "a23", "a24", "a25", "a26", "a27", "a28", "a29", "a30", "a31", "a32", "a33", "a34", "a35", "a36", "a37", "a38", "a39", "a40", "a41", "a42", "a43", "a44", "a45", "a46", "a47", "a48", "a49", "a50", "a51", "a52", "a53", "a54", "a55", "a56", "a57", "a58", "a59", "a60", "a61", "a62", "a63", "a64", "a65", "a66", "a67", "a68", "a69", "a70", "a71", "a72", "a73", "a74", "a75", "a76", "a77", "a78", "a79", "a80", "a81", "a82", "a83", "a84", "a85", "a86", "a87", "a88", "a89", "a90", "a91", "a92", "a93", "a94", "a95", "a96", "a97", "a98", "a99", "a100", "a101", "a102", "a103", "a104", "a105", "a106", "a107", "a108", "a109", "a110", "a111", "a112", "a113", "a114", "a115", "a116", "a117", "a118", "a119", "a120", "a121", "a122", "a123", "a124", "a125", "a126", "a127", "a128", "a129", "a130", "a131", "a132", "a133", "a134", "a135", "a136", "a137", "a138", "a139", "a140", "a141", "a142", "a143", "a144", "a145", "a146", "a147", "a148", "a149", "a150", "a151", "a152", "a153", "a154", "a155", "a156", "a157", "a158", "a159", "a160", "a161", "a162", "a163", "a164", "a165", "a166", "a167", "a168", "a169", "a170", "a171", "a172", "a173", "a174", "a175", "a176", "a177", "a178", "a179", "a180", "a181", "a182", "a183", "a184", "a185", "a186", "a187", "a188", "a189", "a190", "a191", "a192", "a193", "a194", "a195", "a196", "a197", "a198", "a199", "a200", "a201", "a202", "a203", "a204", "a205", "a206", "a207", "a208", "a209", "a210", "a211", "a212", "a213", "a214", "a215", "a216", "a217", "a218", "a219", "a220", "a221", "a222", "a223", "a224", "a225", "a226", "a227", "a228", "a229", "a230", "a231", "a232", "a233", "a234", "a235", "a236", "a237", "a238", "a239", "a240", "a241", "a242", "a243", "a244", "a245", "a246", "a247", "a248", "a249", "a250", "a251", "a252", "a253", "a254", "a255", "s36", "s37", "s38", "s39", "s40", "s41", "s42", "s43", "s44", "s45", "s46", "s47", "s48", "s49", "s50", "s51", "s52", "s53", "s54", "s55", "s56", "s57", "s58", "s59", "s60", "s61", "s62", "s63", "s64", "s67", "s68", "s69", "s70", "s71", "s72", "s73", "s74", "s75", "s76", "s77", "s78", "s79", "s80", "s81", "s82", "s83", "s84", "s85", "s86", "s87", "s88", "s89", "s90", "s91", "s92", "s93", "s94", "s95");
  if (flg != 2u) {
    const int l2 = __builtin_amdgcn_mbcnt_hi(~0u, __builtin_amdgcn_mbcnt_lo(~0u, 0u));
    slow_path(x, dwl, dws, w1, w2, out, (float*)smem, w * 64 + l2, blockIdx.x >> 3, (blockIdx.x & 7) * 4);
  }
}

extern "C" void kernel_launch(void* const* d_in, const int* in_sizes, int n_in, void* d_out, int out_size, void* d_ws,
                              size_t ws_size, hipStream_t stream) {
  replk_kernel<<<256, 256, 0, stream>>>((const u16*)d_in[0], (const u16*)d_in[1], (const u16*)d_in[2], (const u16*)d_in[3],
                                        (const u16*)d_in[4], (u16*)d_out
                                        );
}
```

```cpp
#include <hip/hip_runtime.h>
#include <hip/hip_bf16.h>

typedef unsigned u32;
typedef unsigned short u16;
typedef unsigned long long u64;
typedef __bf16 bf16x2 __attribute__((ext_vector_type(2)));
typedef float f32x2 __attribute__((ext_vector_type(2)));
#define DI __device__ __forceinline__

constexpr int IMG = 512;
constexpr int CH = 96;
constexpr int HID = 384;
constexpr int PXB = 192;
constexpr int ROWB = 98304;
constexpr int TOEP_CS = 336;
constexpr int XT_RS = 320;
constexpr int XT_PP = 1360;
constexpr int XT_CG = 6752;
constexpr int GS = 6160;
constexpr int L_LUT = 0;
constexpr int L_TOEP = 8208;
constexpr int L_W = 40960;
constexpr int L_LUTN = 65536;
constexpr int L_XT = 81920;
constexpr int L_H = 108928;
constexpr int L_EX = 133568;
constexpr int XT_Q = 27024;
constexpr int L_MISC = 162992;
constexpr int L_TOTAL = 163056;
constexpr int LUTK = 106;
constexpr float GC1 = -2.302208198144325f, GC3 = -0.1029432395800235f;
__device__ const u32 CTAB[3 * 1024] = {
671109120,671098880,134490112,673187840,671372952,671229956,145500164,673318996,671636784,671361032,156510216,673450152,671900616,671492108,167520268,673581308,672164452,684206128,178530352,686295376,672428284,684337204,189540404,686426532,672692116,684468280,200550456,686557688,672955948,684599356,211560508,686688844,673219784,697313376,222570592,699402912,673483616,697444452,233580644,699534068,673747448,697575528,244590696,699665224,674011280,697706604,255600748,699796380,674254849,710420624,266610832,712510448,674518681,710551700,277620884,712641604,674782513,710682776,288630936,712772760,675046345,710813852,299640988,712903916,
684223077,723527872,310651072,721423680,684486909,723658948,321661124,721554836,683702165,723790024,332671176,721685992,683965997,723921100,343681228,721817148,685278409,736635120,354691312,734531216,685542241,736766196,365701364,734662372,684757497,736897272,376711416,734793528,685021329,737028348,387721468,734924684,686313474,749742368,398731552,747638752,686577306,749873444,409741604,747769908,685792562,750004520,420751656,747901064,686056394,750135596,431761708,748032220,687368806,762849616,442771792,760746288,687632638,762980692,453781844,760877444,686847894,763111768,464791896,761008600,687111726,763242844,475801948,761139756,
697337034,771762560,486812032,773853824,697600866,771893636,497822084,773984980,697864698,772024712,508832136,774116136,698128530,772155788,519842188,774247292,696274947,784869808,530852272,786961360,696538779,785000884,541862324,787092516,696802611,785131960,552872376,787223672,697066443,785263036,563882428,787354828,699427431,797977056,574892512,800068896,699691263,798108132,585902564,800200052,699955095,798239208,596912616,800331208,700218927,798370284,607922668,800462364,698385611,811084304,618932752,813176432,698649443,811215380,629942804,813307588,698913275,811346456,640952856,813438744,699177107,811477532,651962908,813569900,
710430724,824191552,135539264,822089664,710694556,824322628,146549316,822220820,709909812,824453704,157559368,822351976,710173644,824584780,168569420,822483132,709388904,837298800,179579504,835197200,709652736,837429876,190589556,835328356,708867992,837560952,201599608,835459512,709131824,837692028,212609660,835590668,712541388,850406048,223619744,848304736,712805220,850537124,234629796,848435892,712020476,850668200,245639848,848567048,712284308,850799276,256649900,848698204,711479301,863513296,267659984,861412272,711743133,863644372,278670036,861543428,710958389,863775448,289680088,861674584,711222221,863906524,300690140,861805740,
723544681,677394944,311704064,679484672,723808513,677526020,322714116,679615828,724072345,677657096,333724168,679746984,724336177,677788172,344734220,679878140,724600013,690502192,355744304,692592208,724863845,690633268,366754356,692723364,725127677,690764344,377764408,692854520,725391509,690895420,388774460,692985676,721440774,703609440,399784544,705699744,721704606,703740516,410794596,705830900,721968438,703871592,421804648,705962056,722232270,704002668,432814700,706093212,722496106,716716688,443824784,718807280,722759938,716847764,454834836,718938436,723023770,716978840,465844888,719069592,723287602,717109916,476854940,719200748,
736658638,725629632,487865024,731914816,736922470,725760708,498875076,732045972,736137726,725891784,509885128,732177128,736401558,726022860,520895180,732308284,737693703,738736880,531905264,745022352,737957535,738867956,542915316,745153508,737172791,738999032,553925368,745284664,737436623,739130108,564935420,745415820,734554731,751844128,575945504,758129888,734818563,751975204,586955556,758261044,734033819,752106280,597965608,758392200,734297651,752237356,608975660,758523356,735610063,764951376,619985744,771237424,735873895,765082452,630995796,771368580,735089151,765213528,642005848,771499736,735352983,765344604,653015900,771630892,
749752328,778058624,136592256,780150656,750016160,778189700,147602308,780281812,750279992,778320776,158612360,780443436,750543824,778451852,169622412,780574508,748710508,791165872,180632496,793288492,748974340,791296948,191642548,793419564,749238172,791428024,202652600,793550636,749502004,791559100,213662652,793681708,747668688,804273120,224672736,806395692,747932520,804404196,235682788,806526764,748196352,804535272,246692840,806657836,748460184,804666348,257702892,806788908,746606601,817380368,268712976,819502892,746870433,817511444,279723028,819633964,747134265,817642520,290733080,819765036,747398097,817773596,301743132,819896108,
762866285,826293312,312753216,832610092,763130117,826424388,323763268,832741164,762345373,826555464,334773320,832872236,762609205,826686540,345783372,833003308,761824465,839400560,356793456,845717292,762088297,839531636,367803508,845848364,761303553,839662712,378813560,845979436,761567385,839793788,389823612,846110508,760762378,852507808,400833696,858824492,761026210,852638884,411843748,858955564,760241466,852769960,422853800,859086636,760505298,852901036,433863852,859217708,759720558,865615056,444873936,871931692,759984390,865746132,455883988,872062764,759199646,865877208,466894040,872193836,759463478,866008284,477904092,872324908,
771785938,671098882,488908800,673226540,772049770,671229958,499918852,673357612,772313602,671361034,510928904,673488684,772577434,671492110,521938956,673619756,772821003,684206130,532949040,686333740,773084835,684337206,543959092,686464812,773348667,684468282,554969144,686595884,773612499,684599358,565979196,686726956,773876335,697313378,576989280,699440940,774140167,697444454,587999332,699572012,774403999,697575530,599009384,699703084,774667831,697706606,610019436,699834156,774931667,710420626,621029520,712548140,775195499,710551702,632039572,712679212,775459331,710682778,643049624,712810284,775723163,710813854,654059676,712941356,
784879628,723527874,137636032,721461036,785143460,723658950,148646084,721592108,784358716,723790026,159656136,721723180,784622548,723921102,170666188,721854252,785934960,736635122,181676272,734568236,786198792,736766198,192686324,734699308,785414048,736897274,203696376,734830380,785677880,737028350,214706428,734961452,786990292,749742370,225716512,747675436,787254124,749873446,236726564,747806508,786469380,750004522,247736616,747937580,786733212,750135598,258746668,748068652,788025357,762849618,269756752,760782636,788289189,762980694,280766804,760913708,787504445,763111770,291776856,761044780,787768277,763242846,302786908,761175852,
797993585,771762562,313796992,773889836,798257417,771893638,324807044,774020908,798521249,772024714,335817096,774151980,798785081,772155790,346827148,774283052,796951765,784869810,357837232,786997036,797215597,785000886,368847284,787128108,797479429,785131962,379857336,787259180,797743261,785263038,390867388,787390252,800083982,797977058,401877472,800104236,800347814,798108134,412887524,800235308,800611646,798239210,423897576,800366380,800875478,798370286,434907628,800497452,799042162,811084306,445917712,813211436,799305994,811215382,456927764,813342508,799569826,811346458,467937816,813473580,799833658,811477534,478947868,813604652,
811107542,824191554,489957952,822124332,811371374,824322630,500968004,822255404,810586630,824453706,511978056,822386476,810850462,824584782,522988108,822517548,810045455,837298802,533998192,835231532,810309287,837429878,545008244,835362604,809524543,837560954,556018296,835493676,809788375,837692030,567028348,835624748,813197939,850406050,578038432,848338732,813461771,850537126,589048484,848469804,812677027,850668202,600058536,848600876,812940859,850799278,611068588,848731948,812156119,863513298,622078672,861445932,812419951,863644374,633088724,861577004,811635207,863775450,644098776,861708076,811899039,863906526,655108828,861839148,
824201232,677394946,138689024,679517996,824465064,677526022,149699076,679649068,824728896,677657098,160709128,679780140,824992728,677788174,171719180,679911212,825256564,690502194,182729264,692625196,825520396,690633270,193739316,692756268,825784228,690764346,204749368,692887340,826048060,690895422,215759420,693018412,822117592,703609442,226769504,705732396,822381424,703740518,237779556,705863468,822645256,703871594,248789608,705994540,822909088,704002670,259799660,706125612,823152657,716716690,270809744,718839596,823416489,716847766,281819796,718970668,823680321,716978842,292829848,719101740,823944153,717109918,303839900,719232812,
837315189,725629634,314849984,731946796,837579021,725760710,325860036,732077868,836794277,725891786,336870088,732208940,837058109,726022862,347880140,732340012,838370521,738736882,358890224,745053996,838634353,738867958,369900276,745185068,837849609,738999034,380910328,745316140,838113441,739130110,391920380,745447212,835211282,751844130,402930464,758161196,835475114,751975206,413940516,758292268,834690370,752106282,424950568,758423340,834954202,752237358,435960620,758554412,836266614,764951378,446970704,771268396,836530446,765082454,457980756,771399468,835745702,765213530,468990808,771530540,836009534,765344606,480000860,771661612,
850429146,778058626,491010944,780181292,850692978,778189702,502020996,780312364,850956810,778320778,513031048,780443436,851220642,778451854,524041100,780574508,849367059,791165874,535051184,793288492,849630891,791296950,546061236,793419564,849894723,791428026,557071288,793550636,850158555,791559102,568081340,793681708,848325239,804273122,579091424,806395692,848589071,804404198,590101476,806526764,848852903,804535274,601111528,806657836,849116735,804666350,612121580,806788908,847283419,817380370,623131664,819502892,847547251,817511446,634141716,819633964,847811083,817642522,645151768,819765036,848074915,817773598,656161820,819896108,
863541411,826293314,656161856,832610092,863803555,826424390,656161860,832741164,863017123,826555466,656161864,832872236,863279267,826686542,656161868,833003308,862492835,839400562,656161904,845717292,862754979,839531638,656161908,845848364,861968547,839662714,656161912,845979436,862230691,839793790,656161916,846110508,861444259,852507810,656161952,858824492,861706403,852638886,656161956,858955564,860919971,852769962,656161960,859086636,861182115,852901038,656161964,859217708,860395683,865615058,656162000,871931692,860657827,865746134,656162004,872062764,859871395,865877210,656162008,872193836,860133539,866008286,656162012,872324908,
2151688192u,1885629024,1342187520,1342212212,2151688196u,1885891172,1452812292,1364494564,2151688200u,1886153320,1563437064,1386776836,2218797068u,1886415468,1674061836,1409059156,2165647376u,1886677616,1342253072,1342474360,2165647380u,1886939764,1452877844,1364756712,2164271128u,1887201912,1563502616,1387038984,2231379996u,1887464060,1674127388,1409321304,1082140704,1887726208,1342318624,1342998652,1082140708,1887988356,1452943396,1365281004,1082140712,1888250504,1563568168,1387563276,1149249580,1888512652,1674192940,1409845596,1108682800,1888774800,1342384176,1342736512,1108682804,1889036948,1453008948,1365018864,1107306552,1889299096,1563633720,1387301136,1174415420,1889561244,1674258492,1409583456,
268445760,1892969168,1342449728,1342474372,268445764,1893231316,1453074500,1364756692,268445768,1893493464,1563699272,1387038964,335554636,1893755612,1674324044,1409321316,269822032,1891920576,1342515280,1342736520,269822036,1892182724,1453140052,1365018840,268445784,1892444872,1563764824,1387301112,335554652,1892707020,1674389596,1409583464,1342187616,1890871984,1342580832,1343260812,1342187620,1891134132,1453205604,1365543132,1342187624,1891396280,1563830376,1387825404,1409296492,1891658428,1674455148,1410107756,1343563888,1889823392,1342646384,1342998672,1343563892,1890085540,1453271156,1365280992,1342187640,1890347688,1563895928,1387563264,1409296508,1890609836,1674520700,1409845616,
536881280,1895066352,1342711936,1364494484,536881284,1895328500,1453336708,1386776772,536881288,1895590648,1563961480,1409059108,603990156,1895852796,1674586252,1431341364,538257552,1894017760,1342777488,1364756632,538257556,1894279908,1453402260,1387038920,536881304,1894542056,1564027032,1409321256,603990172,1894804204,1674651804,1431603512,1610623136,1897163536,1342843040,1365280924,1610623140,1897425684,1453467812,1387563212,1610623144,1897687832,1564092584,1409845548,1677732012,1897949980,1674717356,1432127804,1611999408,1896114944,1342908592,1365018784,1611999412,1896377092,1453533364,1387301072,1610623160,1896639240,1564158136,1409583408,1677732028,1896901388,1674782908,1431865664,
809511104,1900309312,1342974144,1364756644,809511108,1900571460,1453598916,1387038900,809511112,1900833608,1564223688,1409321236,876619980,1901095756,1674848460,1431603524,823470288,1901357904,1343039696,1365018792,823470292,1901620052,1453664468,1387301048,822094040,1901882200,1564289240,1409583384,889202908,1902144348,1674914012,1431865672,1887447264,1898212128,1343105248,1365543084,1887447268,1898474276,1453730020,1387825340,1887447272,1898736424,1564354792,1410107676,1954556140,1898998572,1674979564,1432389964,1913989360,1899260720,1343170800,1365280944,1913989364,1899522868,1453795572,1387563200,1912613112,1899785016,1564420344,1409845536,1979721980,1900047164,1675045116,1432127824,
2151688448u,1784705124,1347430656,1452835440,2151688452u,1784967272,1458055428,1475117792,2151688456u,1785229420,1568680200,1497400064,2218797324u,1785491568,1679304972,1519682384,2165647632u,1785753716,1347496208,1453097588,2165647636u,1786015864,1458120980,1475379940,2164271384u,1786278012,1568745752,1497662212,2231380252u,1786540160,1679370524,1519944532,1082140960,1786802308,1347561760,1453621880,1082140964,1787064456,1458186532,1475904232,1082140968,1787326604,1568811304,1498186504,1149249836,1787588752,1679436076,1520468824,1108683056,1787850900,1347627312,1453359740,1108683060,1788113048,1458252084,1475642092,1107306808,1788375196,1568876856,1497924364,1174415676,1788637344,1679501628,1520206684,
268446016,1792045268,1347692864,1453097600,268446020,1792307416,1458317636,1475379920,268446024,1792569564,1568942408,1497662192,335554892,1792831712,1679567180,1519944544,269822288,1790996676,1347758416,1453359748,269822292,1791258824,1458383188,1475642068,268446040,1791520972,1569007960,1497924340,335554908,1791783120,1679632732,1520206692,1342187872,1789948084,1347823968,1453884040,1342187876,1790210232,1458448740,1476166360,1342187880,1790472380,1569073512,1498448632,1409296748,1790734528,1679698284,1520730984,1343564144,1788899492,1347889520,1453621900,1343564148,1789161640,1458514292,1475904220,1342187896,1789423788,1569139064,1498186492,1409296764,1789685936,1679763836,1520468844,
536881536,1794142452,1347955072,1475117712,536881540,1794404600,1458579844,1497400000,536881544,1794666748,1569204616,1519682336,603990412,1794928896,1679829388,1541964592,538257808,1793093860,1348020624,1475379860,538257812,1793356008,1458645396,1497662148,536881560,1793618156,1569270168,1519944484,603990428,1793880304,1679894940,1542226740,1610623392,1796239636,1348086176,1475904152,1610623396,1796501784,1458710948,1498186440,1610623400,1796763932,1569335720,1520468776,1677732268,1797026080,1679960492,1542751032,1611999664,1795191044,1348151728,1475642012,1611999668,1795453192,1458776500,1497924300,1610623416,1795715340,1569401272,1520206636,1677732284,1795977488,1680026044,1542488892,
809511360,1799385412,1348217280,1475379872,809511364,1799647560,1458842052,1497662128,809511368,1799909708,1569466824,1519944464,876620236,1800171856,1680091596,1542226752,823470544,1800434004,1348282832,1475642020,823470548,1800696152,1458907604,1497924276,822094296,1800958300,1569532376,1520206612,889203164,1801220448,1680157148,1542488900,1887447520,1797288228,1348348384,1476166312,1887447524,1797550376,1458973156,1498448568,1887447528,1797812524,1569597928,1520730904,1954556396,1798074672,1680222700,1543013192,1913989616,1798336820,1348413936,1475904172,1913989620,1798598968,1459038708,1498186428,1912613368,1798861116,1569663480,1520468764,1979722236,1799123264,1680288252,1542751052,
2151688704u,2087482984,1352673280,1563464828,2151688708u,2087745132,1463298052,1585747180,2151688712u,2088007280,1573922824,1608029452,2218797580u,2088269428,1684547596,1630311772,2165647888u,2088531576,1352738832,1563726976,2165647892u,2088793724,1463363604,1586009328,2164271640u,2089055872,1573988376,1608291600,2231380508u,2089318020,1684613148,1630573920,1082141216,2089580168,1352804384,1564251268,1082141220,2089842316,1463429156,1586533620,1082141224,2090104464,1574053928,1608815892,1149250092,2090366612,1684678700,1631098212,1108683312,2090628760,1352869936,1563989128,1108683316,2090890908,1463494708,1586271480,1107307064,2091153056,1574119480,1608553752,1174415932,2091415204,1684744252,1630836072,
268446272,2094823128,1352935488,1563726988,268446276,2095085276,1463560260,1586009308,268446280,2095347424,1574185032,1608291580,335555148,2095609572,1684809804,1630573932,269822544,2093774536,1353001040,1563989136,269822548,2094036684,1463625812,1586271456,268446296,2094298832,1574250584,1608553728,335555164,2094560980,1684875356,1630836080,1342188128,2092725944,1353066592,1564513428,1342188132,2092988092,1463691364,1586795748,1342188136,2093250240,1574316136,1609078020,1409297004,2093512388,1684940908,1631360372,1343564400,2091677352,1353132144,1564251288,1343564404,2091939500,1463756916,1586533608,1342188152,2092201648,1574381688,1608815880,1409297020,2092463796,1685006460,1631098232,
536881792,2096920312,1353197696,1585747100,536881796,2097182460,1463822468,1608029388,536881800,2097444608,1574447240,1630311724,603990668,2097706756,1685072012,1652593980,538258064,2095871720,1353263248,1586009248,538258068,2096133868,1463888020,1608291536,536881816,2096396016,1574512792,1630573872,603990684,2096658164,1685137564,1652856128,1610623648,2099017496,1353328800,1586533540,1610623652,2099279644,1463953572,1608815828,1610623656,2099541792,1574578344,1631098164,1677732524,2099803940,1685203116,1653380420,1611999920,2097968904,1353394352,1586271400,1611999924,2098231052,1464019124,1608553688,1610623672,2098493200,1574643896,1630836024,1677732540,2098755348,1685268668,1653118280,
809511616,2102163272,1353459904,1586009260,809511620,2102425420,1464084676,1608291516,809511624,2102687568,1574709448,1630573852,876620492,2102949716,1685334220,1652856140,823470800,2103211864,1353525456,1586271408,823470804,2103474012,1464150228,1608553664,822094552,2103736160,1574775000,1630836000,889203420,2103998308,1685399772,1653118288,1887447776,2100066088,1353591008,1586795700,1887447780,2100328236,1464215780,1609077956,1887447784,2100590384,1574840552,1631360292,1954556652,2100852532,1685465324,1653642580,1913989872,2101114680,1353656560,1586533560,1913989876,2101376828,1464281332,1608815816,1912613624,2101638976,1574906104,1631098152,1979722492,2101901124,1685530876,1653380440,
2151688960u,1986559084,1357916416,1674088056,2151688964u,1986821232,1468541188,1696370408,2151688968u,1987083380,1579165960,1718652680,2218797836u,1987345528,1689790732,1740935000,2165648144u,1987607676,1357981968,1674350204,2165648148u,1987869824,1468606740,1696632556,2164271896u,1988131972,1579231512,1718914828,2231380764u,1988394120,1689856284,1741197148,1082141472,1988656268,1358047520,1674874496,1082141476,1988918416,1468672292,1697156848,1082141480,1989180564,1579297064,1719439120,1149250348,1989442712,1689921836,1741721440,1108683568,1989704860,1358113072,1674612356,1108683572,1989967008,1468737844,1696894708,1107307320,1990229156,1579362616,1719176980,1174416188,1990491304,1689987388,1741459300,
268446528,1993899228,1358178624,1674350216,268446532,1994161376,1468803396,1696632536,268446536,1994423524,1579428168,1718914808,335555404,1994685672,1690052940,1741197160,269822800,1992850636,1358244176,1674612364,269822804,1993112784,1468868948,1696894684,268446552,1993374932,1579493720,1719176956,335555420,1993637080,1690118492,1741459308,1342188384,1991802044,1358309728,1675136656,1342188388,1992064192,1468934500,1697418976,1342188392,1992326340,1579559272,1719701248,1409297260,1992588488,1690184044,1741983600,1343564656,1990753452,1358375280,1674874516,1343564660,1991015600,1469000052,1697156836,1342188408,1991277748,1579624824,1719439108,1409297276,1991539896,1690249596,1741721460,
536882048,1995996412,1358440832,1696370328,536882052,1996258560,1469065604,1718652616,536882056,1996520708,1579690376,1740934952,603990924,1996782856,1690315148,1763217208,538258320,1994947820,1358506384,1696632476,538258324,1995209968,1469131156,1718914764,536882072,1995472116,1579755928,1741197100,603990940,1995734264,1690380700,1763479356,1610623904,1998093596,1358571936,1697156768,1610623908,1998355744,1469196708,1719439056,1610623912,1998617892,1579821480,1741721392,1677732780,1998880040,1690446252,1764003648,1612000176,1997045004,1358637488,1696894628,1612000180,1997307152,1469262260,1719176916,1610623928,1997569300,1579887032,1741459252,1677732796,1997831448,1690511804,1763741508,
809511872,2001239372,1358703040,1696632488,809511876,2001501520,1469327812,1718914744,809511880,2001763668,1579952584,1741197080,876620748,2002025816,1690577356,1763479368,823471056,2002287964,1358768592,1696894636,823471060,2002550112,1469393364,1719176892,822094808,2002812260,1580018136,1741459228,889203676,2003074408,1690642908,1763741516,1887448032,1999142188,1358834144,1697418928,1887448036,1999404336,1469458916,1719701184,1887448040,1999666484,1580083688,1741983520,1954556908,1999928632,1690708460,1764265808,1913990128,2000190780,1358899696,1697156788,1913990132,2000452928,1469524468,1719439044,1912613880,2000715076,1580149240,1741721380,1979722748,2000977224,1690774012,1764003668,
136579076,1343253088,52428,12582912,135530496,1453879396,843229,12648449,134481920,1564505704,1634030,12713986,145229824,1675132012,2411724,12779523,136841224,1343318640,3198156,13369356,135792808,1453944948,3988957,13434893,134744232,1564571256,4779758,13500430,139724968,1675197564,5557452,13565967,137103372,1343384208,6343884,14155800,136054952,1454010516,7134685,14221337,135006376,1564636824,7925486,14286874,139724968,1675263132,8703180,14352411,137365520,1343449728,9489612,14942244,136316928,1454076036,10280413,15007781,135268352,1564702344,11071214,15073318,145229824,1675328652,11848908,15138855,
139724868,1365535332,117964,12582960,139724852,1476161640,908765,12648497,138676260,1586787948,1699566,12714034,137627668,1697414256,2477260,12779571,145229896,1365600884,3263692,13369404,145229880,1476227192,4054493,13434941,138938408,1586853500,4845294,13500478,137889816,1697479808,5622988,13566015,145229900,1365666452,6409420,14155848,145229884,1476292760,7200221,14221385,139200556,1586919068,7991022,14286922,138151964,1697545376,8768716,14352459,139724880,1365731972,9555148,14942292,139724864,1476358280,10345949,15007829,139462704,1586984588,11136750,15073366,138414112,1697610896,11914444,15138903,
137627668,1387817576,183500,12583008,136579076,1498443884,974301,12648545,135530496,1609070192,1765102,12714082,134481920,1719696500,2542796,12779619,137889816,1387883128,3329228,13369452,136841224,1498509436,4120029,13434989,135792808,1609135744,4910830,13500526,134744232,1719762052,5688524,13566063,138151964,1387948696,6474956,14155896,137103372,1498575004,7265757,14221433,136054952,1609201312,8056558,14286970,135006376,1719827620,8834252,14352507,138414112,1388014216,9620684,14942340,137365520,1498640524,10411485,15007877,136316928,1609266832,11202286,15073414,135268352,1719893140,11979980,15138951,
139724968,1410099820,249036,12583056,139724868,1520726128,1039837,12648593,139724852,1631352436,1830638,12714130,138676260,1741978744,2608332,12779667,145229824,1410165372,3394764,13369500,145229896,1520791680,4185565,13435037,145229880,1631417988,4976366,13500574,138938408,1742044296,5754060,13566111,145229824,1410230940,6540492,14155944,145229900,1520857248,7331293,14221481,145229884,1631483556,8122094,14287018,139200556,1742109864,8899788,14352555,139724968,1410296460,9686220,14942388,139724880,1520922768,10477021,15007925,139724864,1631549076,11267822,15073462,139462704,1742175384,12045516,15138999,
180619940,1348496080,52428,12582912,179571360,1459122388,843229,12648449,178522784,1569748696,1634030,12713986,189270688,1680375004,2411724,12779523,180882088,1348561600,3198156,13369356,179833672,1459187908,3988957,13434893,178785096,1569814216,4779758,13500430,183765832,1680440524,5557452,13565967,181144236,1348627104,6343884,14155800,180095816,1459253412,7134685,14221337,179047240,1569879720,7925486,14286874,183765832,1680506028,8703180,14352411,181406384,1348692656,9489612,14942244,180357792,1459318964,10280413,15007781,179309216,1569945272,11071214,15073318,189270688,1680571580,11848908,15138855,
183765732,1370778324,117964,12582960,183765716,1481404632,908765,12648497,182717124,1592030940,1699566,12714034,181668532,1702657248,2477260,12779571,189270760,1370843844,3263692,13369404,189270744,1481470152,4054493,13434941,182979272,1592096460,4845294,13500478,181930680,1702722768,5622988,13566015,189270764,1370909348,6409420,14155848,189270748,1481535656,7200221,14221385,183241420,1592161964,7991022,14286922,182192828,1702788272,8768716,14352459,183765744,1370974900,9555148,14942292,183765728,1481601208,10345949,15007829,183503568,1592227516,11136750,15073366,182454976,1702853824,11914444,15138903,
181668532,1393060568,183500,12583008,180619940,1503686876,974301,12648545,179571360,1614313184,1765102,12714082,178522784,1724939492,2542796,12779619,181930680,1393126088,3329228,13369452,180882088,1503752396,4120029,13434989,179833672,1614378704,4910830,13500526,178785096,1725005012,5688524,13566063,182192828,1393191592,6474956,14155896,181144236,1503817900,7265757,14221433,180095816,1614444208,8056558,14286970,179047240,1725070516,8834252,14352507,182454976,1393257144,9620684,14942340,181406384,1503883452,10411485,15007877,180357792,1614509760,11202286,15073414,179309216,1725136068,11979980,15138951,
183765832,1415342812,249036,12583056,183765732,1525969120,1039837,12648593,183765716,1636595428,1830638,12714130,182717124,1747221736,2608332,12779667,189270688,1415408332,3394764,13369500,189270760,1526034640,4185565,13435037,189270744,1636660948,4976366,13500574,182979272,1747287256,5754060,13566111,189270688,1415473836,6540492,14155944,189270764,1526100144,7331293,14221481,189270748,1636726452,8122094,14287018,183241420,1747352760,8899788,14352555,183765832,1415539388,9686220,14942388,183765744,1526165696,10477021,15007925,183765728,1636792004,11267822,15073462,183503568,1747418312,12045516,15138999,
224660804,1353738992,52428,12582912,223612224,1464365300,843229,12648449,222563648,1574991608,1634030,12713986,233311552,1685617916,2411724,12779523,224922952,1353804512,3198156,13369356,223874536,1464430820,3988957,13434893,222825960,1575057128,4779758,13500430,227806696,1685683436,5557452,13565967,225185100,1353870080,6343884,14155800,224136680,1464496388,7134685,14221337,223088104,1575122696,7925486,14286874,227806696,1685749004,8703180,14352411,225447248,1353935632,9489612,14942244,224398656,1464561940,10280413,15007781,223350080,1575188248,11071214,15073318,233311552,1685814556,11848908,15138855,
227806596,1376021236,117964,12582960,227806580,1486647544,908765,12648497,226757988,1597273852,1699566,12714034,225709396,1707900160,2477260,12779571,233311624,1376086756,3263692,13369404,233311608,1486713064,4054493,13434941,227020136,1597339372,4845294,13500478,225971544,1707965680,5622988,13566015,233311628,1376152324,6409420,14155848,233311612,1486778632,7200221,14221385,227282284,1597404940,7991022,14286922,226233692,1708031248,8768716,14352459,227806608,1376217876,9555148,14942292,227806592,1486844184,10345949,15007829,227544432,1597470492,11136750,15073366,226495840,1708096800,11914444,15138903,
225709396,1398303480,183500,12583008,224660804,1508929788,974301,12648545,223612224,1619556096,1765102,12714082,222563648,1730182404,2542796,12779619,225971544,1398369000,3329228,13369452,224922952,1508995308,4120029,13434989,223874536,1619621616,4910830,13500526,222825960,1730247924,5688524,13566063,226233692,1398434568,6474956,14155896,225185100,1509060876,7265757,14221433,224136680,1619687184,8056558,14286970,223088104,1730313492,8834252,14352507,226495840,1398500120,9620684,14942340,225447248,1509126428,10411485,15007877,224398656,1619752736,11202286,15073414,223350080,1730379044,11979980,15138951,
227806696,1420585724,249036,12583056,227806596,1531212032,1039837,12648593,227806580,1641838340,1830638,12714130,226757988,1752464648,2608332,12779667,233311552,1420651244,3394764,13369500,233311624,1531277552,4185565,13435037,233311608,1641903860,4976366,13500574,227020136,1752530168,5754060,13566111,233311552,1420716812,6540492,14155944,233311628,1531343120,7331293,14221481,233311612,1641969428,8122094,14287018,227282284,1752595736,8899788,14352555,227806696,1420782364,9686220,14942388,227806608,1531408672,10477021,15007925,227806592,1642034980,11267822,15073462,227544432,1752661288,12045516,15138999,
268701668,1358981952,52428,12582912,267653088,1469608260,843229,12648449,266604512,1580234568,1634030,12713986,277352416,1690860876,2411724,12779523,268963816,1359047504,3198156,13369356,267915400,1469673812,3988957,13434893,266866824,1580300120,4779758,13500430,271847560,1690926428,5557452,13565967,269225964,1359113008,6343884,14155800,268177544,1469739316,7134685,14221337,267128968,1580365624,7925486,14286874,271847560,1690991932,8703180,14352411,269488112,1359178528,9489612,14942244,268439520,1469804836,10280413,15007781,267390944,1580431144,11071214,15073318,277352416,1691057452,11848908,15138855,
271847460,1381264196,117964,12582960,271847444,1491890504,908765,12648497,270798852,1602516812,1699566,12714034,269750260,1713143120,2477260,12779571,277352488,1381329748,3263692,13369404,277352472,1491956056,4054493,13434941,271061000,1602582364,4845294,13500478,270012408,1713208672,5622988,13566015,277352492,1381395252,6409420,14155848,277352476,1492021560,7200221,14221385,271323148,1602647868,7991022,14286922,270274556,1713274176,8768716,14352459,271847472,1381460772,9555148,14942292,271847456,1492087080,10345949,15007829,271585296,1602713388,11136750,15073366,270536704,1713339696,11914444,15138903,
269750260,1403546440,183500,12583008,268701668,1514172748,974301,12648545,267653088,1624799056,1765102,12714082,266604512,1735425364,2542796,12779619,270012408,1403611992,3329228,13369452,268963816,1514238300,4120029,13434989,267915400,1624864608,4910830,13500526,266866824,1735490916,5688524,13566063,270274556,1403677496,6474956,14155896,269225964,1514303804,7265757,14221433,268177544,1624930112,8056558,14286970,267128968,1735556420,8834252,14352507,270536704,1403743016,9620684,14942340,269488112,1514369324,10411485,15007877,268439520,1624995632,11202286,15073414,267390944,1735621940,11979980,15138951,
271847560,1425828684,249036,12583056,271847460,1536454992,1039837,12648593,271847444,1647081300,1830638,12714130,270798852,1757707608,2608332,12779667,277352416,1425894236,3394764,13369500,277352488,1536520544,4185565,13435037,277352472,1647146852,4976366,13500574,271061000,1757773160,5754060,13566111,277352416,1425959740,6540492,14155944,277352492,1536586048,7331293,14221481,277352476,1647212356,8122094,14287018,271323148,1757838664,8899788,14352555,271847560,1426025260,9686220,14942388,271847472,1536651568,10477021,15007925,271847456,1647277876,11267822,15073462,271585296,1757904184,12045516,15138999};

DI float bf_lo(u32 v) { return __uint_as_float(v << 16); }
DI u32 pk_bf16(float a, float b) { f32x2 t = {a, b}; return __builtin_bit_cast(u32, __builtin_convertvector(t, bf16x2)); }

DI void slow_path(const u16* __restrict__ x, const u16* __restrict__ dwl, const u16* __restrict__ dws, const u16* __restrict__ w1,
                  const u16* __restrict__ w2, u16* __restrict__ out, float* sm, int t, int tx, int ty0) {
  float* o = sm;
  float* g = sm + 8 * CH;
  float* st = g + 8 * HID;
#pragma unroll 1
  for (int b = 0; b < 128; ++b) {
    const int y = ty0 * 16 + (b >> 1), x0 = tx * 16 + (b & 1) * 8;
    __syncthreads();
#pragma unroll 1
    for (int i = t; i < 8 * CH; i += 256) {
      const int p = i / CH, c = i - p * CH;
      float acc = 0.f;
#pragma unroll 1
      for (int a = 0; a < 5; ++a)
#pragma unroll 1
        for (int e = 0; e < 5; ++e) {
          const int yy = y + a - 2, xc = x0 + p + e - 2;
          if (yy < 0 || yy >= IMG || xc < 0 || xc >= IMG) continue;
          const float xv = bf_lo(x[(size_t)(yy * IMG + xc) * CH + c]);
          acc = __builtin_fmaf(bf_lo(dws[(a * 5 + e) * CH + c]), xv, acc);
          if (a >= 1 && a <= 3 && e >= 1 && e <= 3) acc = __builtin_fmaf(bf_lo(dwl[((a - 1) * 3 + (e - 1)) * CH + c]), xv, acc);
        }
      o[i] = acc;
    }
    __syncthreads();
    if (t < 8) {
      float m = 0.f, v = 0.f;
#pragma unroll 1
      for (int c = 0; c < CH; ++c) m += o[t * CH + c];
      m *= (1.0f / CH);
#pragma unroll 1
      for (int c = 0; c < CH; ++c) { const float d = o[t * CH + c] - m; v = __builtin_fmaf(d, d, v); }
      st[2 * t] = m; st[2 * t + 1] = __builtin_amdgcn_rsqf(v * (1.0f / CH) + 1e-5f);
    }
    __syncthreads();
#pragma unroll 1
    for (int i = t; i < 8 * HID; i += 256) {
      const int p = i / HID, e = i - p * HID;
      const float m = st[2 * p], r = st[2 * p + 1];
      float u = 0.f;
#pragma unroll 1
      for (int c = 0; c < CH; ++c) u = __builtin_fmaf((o[p * CH + c] - m) * r, bf_lo(w1[c * HID + e]), u);
      const float z = u * __builtin_fmaf(u * u, GC3, GC1);
      g[i] = u * __builtin_amdgcn_rcpf(1.0f + __builtin_amdgcn_exp2f(z));
    }
    __syncthreads();
#pragma unroll 1
    for (int i = t; i < 8 * CH; i += 256) {
      const int p = i / CH, c = i - p * CH;
      float acc = 0.f;
#pragma unroll 1
      for (int e = 0; e < HID; ++e) acc = __builtin_fmaf(g[p * HID + e], bf_lo(w2[e * CH + c]), acc);
      const size_t idx = (size_t)(y * IMG + x0 + p) * CH + c;
      out[idx] = (u16)pk_bf16(bf_lo(x[idx]) + acc, 0.f);
    }
  }
}

__global__ __launch_bounds__(256, 1) void replk_kernel(const u16* __restrict__ x, const u16* __restrict__ dwl,
    const u16* __restrict__ dws, const u16* __restrict__ w1, const u16* __restrict__ w2, u16* __restrict__ out
    ) {
  __shared__ __attribute__((aligned(16))) char smem[L_TOTAL];
  __attribute__((address_space(3))) char* smem3 = (__attribute__((address_space(3))) char*)smem;
  const int w = __builtin_amdgcn_readfirstlane(threadIdx.x >> 6);
  u32 tid = threadIdx.x, blk = blockIdx.x;
  u32 flg = (u32)(size_t)smem3 ? 1u : 0u;
  u64 ka = (u64)__builtin_amdgcn_kernarg_segment_ptr();
  asm volatile(
    ".p2align 3\n"
    "s_load_dwordx2 s[40:41], s[98:99], 0x0\n"
    "s_load_dwordx2 s[88:89], s[98:99], 0x10\n"
    "s_load_dwordx2 s[92:93], s[98:99], 0x8\n"
    "s_load_dwordx2 s[84:85], s[98:99], 0x20\n"
    "s_load_dwordx2 s[80:81], s[98:99], 0x18\n"
    "s_load_dwordx2 s[44:45], s[98:99], 0x28\n"
    "v_lshlrev_b32_e32 v213, 4, v208\n"
    "v_lshrrev_b32_e32 v0, 6, v208\n"
    "s_add_u32 vcc_lo, s96, 0\n"
    "s_addc_u32 vcc_hi, s97, 0\n"
    "global_load_dwordx4 v[168:171], v213, vcc\n"
    "s_add_u32 vcc_lo, s96, 4096\n"
    "s_addc_u32 vcc_hi, s97, 0\n"
    "global_load_dwordx4 v[172:175], v213, vcc\n"
    "s_add_u32 vcc_lo, s96, 8192\n"
    "s_addc_u32 vcc_hi, s97, 0\n"
    "global_load_dwordx4 v[176:179], v213, vcc\n"
    "s_getpc_b64 s[76:77]\n"
    ".Lreplk_pc0:\n"
    "s_bfe_u32 s63, s65, 0x30003\n"
    "v_lshrrev_b32_e32 v5, 2, v208\n"
    "s_lshl_b32 s63, s63, 6\n"
    "v_add_lshl_u32 v5, s63, v5, 7\n"
    "v_min_u32_e32 v215, .Lreplk_end-.Lreplk_pc0-4, v5\n"
    "global_load_dword v233, v215, s[76:77]\n"
    "v_readfirstlane_b32 s60, v0\n"
    "v_min_u32_e32 v1, 239, v208\n"
    "v_mul_u32_u24_e32 v2, 1366, v1\n"
    "s_lshr_b32 s61, s65, 3\n"
    "s_and_b32 s62, s65, 7\n"
    "v_lshrrev_b32_e32 v2, 16, v2\n"
    "v_lshlrev_b32_e32 v3, 2, v1\n"
    "s_lshl_b32 s62, s62, 6\n"
    "s_movk_i32 s63, 768\n"
    "v_mad_u32_u24 v211, v2, s63, v3\n"
    "v_mul_u32_u24_e32 v4, 384, v2\n"
    "v_add_u32_e32 v2, -1, v2\n"
    "s_movk_i32 s90, 4800\n"
    "s_mov_b32 s91, 0x20000\n"
    "v_add_u32_e32 v3, -576, v3\n"
    "v_cmp_gt_u32_e32 vcc, 3, v2\n"
    "v_add_u32_e32 v3, v3, v4\n"
    "v_bfrev_b32_e32 v4, 1\n"
    "s_movk_i32 s94, 1728\n"
    "s_mov_b32 s95, 0x20000\n"
    "v_cndmask_b32_e32 v212, v4, v3, vcc\n"
    "s_lshl_b32 s99, s62, 5\n"
    "s_add_u32 s99, s99, s61\n"
    "s_mulk_i32 s99, 3072\n"
    "s_lshr_b32 s51, s60, 1\n"
    "s_mul_i32 s63, s51, 18432\n"
    "s_lshl_b32 s52, s60, 13\n"
    "s_add_u32 s63, s63, s52\n"
    "v_add_u32_e32 v210, s63, v213\n"
    "s_mul_i32 s38, s60, 9216\n"
    "s_add_u32 s38, s38, 40960\n"
    "s_mov_b32 s86, 73728\n"
    "s_mov_b32 s87, 0x20000\n"
    "v_and_b32_e32 v5, 63, v208\n"
    "v_min_u32_e32 v5, 8, v5\n"
    "s_and_b32 s52, s60, 1\n"
    "s_lshl_b32 s53, s61, 1\n"
    "s_add_u32 s52, s52, s53\n"
    "s_mul_i32 s52, s52, 9\n"
    "v_add_lshl_u32 v214, s52, v5, 7\n"
    "s_cmp_eq_u32 s61, 0\n"
    "s_cselect_b32 s52, 24, 0\n"
    "s_cmp_eq_u32 s61, 31\n"
    "s_cselect_b32 s53, 40, s52\n"
    "s_sub_u32 s53, 256, s53\n"
    "v_subrev_u32_e32 v6, s52, v208\n"
    "s_sub_u32 s63, s99, 384\n"
    "v_cmp_gt_u32_e32 vcc, s53, v6\n"
    "v_lshl_add_u32 v7, v1, 4, s63\n"
    "v_bfe_u32 v8, v208, 4, 2\n"
    "v_lshl_or_b32 v8, v0, 3, v8\n"
    "v_and_b32_e32 v9, 0xf0, v213\n"
    "v_cndmask_b32_e32 v208, v4, v7, vcc\n"
    "v_mul_u32_u24_e32 v8, 768, v8\n"
    "s_mov_b32 s42, 50331648\n"
    "s_mov_b32 s43, 0x20000\n"
    "v_or_b32_e32 v209, v8, v9\n"
    "s_mov_b32 s82, 73728\n"
    "s_mov_b32 s83, 0x20000\n"
    "s_cmp_eq_u32 s62, 0\n"
    "s_cselect_b32 s78, 0, s42\n"
    "s_mov_b32 s79, 0x20000\n"
    "s_waitcnt lgkmcnt(0)\n"
    "s_cmp_lt_u32 s60, 2\n"
    "s_nop 0\n"
    "s_cselect_b64 s[70:71], s[80:81], s[84:85]\n"
    "s_sub_u32 s76, s40, 196608\n"
    "s_subb_u32 s77, s41, 0\n"
    "s_mov_b32 s65, 0\n"
    "buffer_load_dwordx4 a[224:227], v208, s[76:79], s65 offen\n"
    "s_mov_b32 s65, 98304\n"
    "buffer_load_dwordx4 a[228:231], v208, s[76:79], s65 offen\n"
    "s_mov_b32 s65, 196608\n"
    "buffer_load_dwordx4 a[232:235], v208, s[40:43], s65 offen\n"
    "s_mov_b32 s65, 294912\n"
    "buffer_load_dwordx4 a[236:239], v208, s[40:43], s65 offen\n"
    "buffer_load_dword v224, v211, s[88:91], 0 offen offset:0\n"
    "buffer_load_dword v225, v211, s[88:91], 0 offen offset:192\n"
    "buffer_load_dword v226, v211, s[88:91], 0 offen offset:384\n"
    "buffer_load_dword v227, v211, s[88:91], 0 offen offset:576\n"
    "buffer_load_dword v228, v211, s[88:91], 0 offen offset:768\n"
    "v_mov_b32_e64 v88, 0\n"
    "v_mov_b32_e32 v240, 0xdddd8888\n"
    "v_mov_b32_e64 v89, 0\n"
    "v_mov_b32_e64 v200, 0\n"
    "v_mov_b32_e64 v90, 0\n"
    "v_mov_b32_e64 v91, 0\n"
    "v_mov_b32_e64 v59, 0\n"
    "v_mov_b32_e64 v60, 0\n"
    "v_mov_b32_e64 v67, 0\n"
    "v_mov_b32_e64 v68, 0\n"
    "v_mov_b32_e64 v75, 0\n"
    "v_mov_b32_e64 v76, 0\n"
    "v_mov_b32_e64 v83, 0\n"
    "v_mov_b32_e64 v84, 0\n"
    "v_mov_b32_e32 v36, 0x75089ba4\n"
    "v_mov_b32_e32 v38, 0xbdd2d3e8\n"
    "v_mov_b32_e32 v32, 0xc0135761\n"
    "v_lshlrev_b32_e64 v34, 16, v213\n"
    "v_lshlrev_b32_e64 v234, 1, v213\n"
    "v_or_b32_e32 v0, 0x8000, v34\n"
    "v_or_b32_e32 v1, 0x18000, v34\n"
    "v_or_b32_e32 v2, 0x28000, v34\n"
    "v_or_b32_e32 v3, 0x38000, v34\n"
    "v_or_b32_e32 v4, 0x48000, v34\n"
    "v_or_b32_e32 v5, 0x58000, v34\n"
    "v_or_b32_e32 v6, 0x68000, v34\n"
    "v_or_b32_e32 v7, 0x78000, v34\n"
    "v_cmp_ne_u32_e64 s[72:73], 0, v34\n"
    "s_nop 0\n"
    "s_nop 1\n"
    "v_cndmask_b32_e64 v0, 0, v0, s[72:73]\n"
    "v_pk_mul_f32 v[0:1], v[0:1], v[36:37] op_sel_hi:[1,0]\n"
    "v_pk_mul_f32 v[2:3], v[2:3], v[36:37] op_sel_hi:[1,0]\n"
    "v_pk_mul_f32 v[4:5], v[4:5], v[36:37] op_sel_hi:[1,0]\n"
    "v_pk_mul_f32 v[6:7], v[6:7], v[36:37] op_sel_hi:[1,0]\n"
    "v_pk_mul_f32 v[8:9], v[0:1], v[0:1]\n"
    "v_pk_mul_f32 v[10:11], v[2:3], v[2:3]\n"
    "v_pk_mul_f32 v[12:13], v[4:5], v[4:5]\n"
    "v_pk_mul_f32 v[14:15], v[6:7], v[6:7]\n"
    "v_pk_fma_f32 v[8:9], v[8:9], v[38:39], v[32:33] op_sel_hi:[1,0,0]\n"
    "v_pk_fma_f32 v[10:11], v[10:11], v[38:39], v[32:33] op_sel_hi:[1,0,0]\n"
    "v_pk_fma_f32 v[12:13], v[12:13], v[38:39], v[32:33] op_sel_hi:[1,0,0]\n"
    "v_pk_fma_f32 v[14:15], v[14:15], v[38:39], v[32:33] op_sel_hi:[1,0,0]\n"
    "v_pk_mul_f32 v[8:9], v[8:9], v[0:1]\n"
    "v_pk_mul_f32 v[10:11], v[10:11], v[2:3]\n"
    "v_pk_mul_f32 v[12:13], v[12:13], v[4:5]\n"
    "v_pk_mul_f32 v[14:15], v[14:15], v[6:7]\n"
    "v_exp_f32_e64 v8, v8\n"
    "v_exp_f32_e64 v9, v9\n"
    "v_exp_f32_e64 v10, v10\n"
    "v_exp_f32_e64 v11, v11\n"
    "v_exp_f32_e64 v12, v12\n"
    "v_exp_f32_e64 v13, v13\n"
    "v_exp_f32_e64 v14, v14\n"
    "v_exp_f32_e64 v15, v15\n"
    "v_pk_add_f32 v[16:17], v[8:9], 1.0 op_sel_hi:[1,0]\n"
    "v_pk_add_f32 v[18:19], v[10:11], 1.0 op_sel_hi:[1,0]\n"
    "v_pk_add_f32 v[20:21], v[12:13], 1.0 op_sel_hi:[1,0]\n"
    "v_pk_add_f32 v[22:23], v[14:15], 1.0 op_sel_hi:[1,0]\n"
    "v_rcp_f32_e64 v16, v16\n"
    "v_rcp_f32_e64 v17, v17\n"
    "v_rcp_f32_e64 v18, v18\n"
    "v_rcp_f32_e64 v19, v19\n"
    "v_rcp_f32_e64 v20, v20\n"
    "v_rcp_f32_e64 v21, v21\n"
    "v_rcp_f32_e64 v22, v22\n"
    "v_rcp_f32_e64 v23, v23\n"
    "v_pk_mul_f32 v[16:17], v[16:17], v[0:1]\n"
    "v_pk_mul_f32 v[18:19], v[18:19], v[2:3]\n"
    "v_pk_mul_f32 v[20:21], v[20:21], v[4:5]\n"
    "v_pk_mul_f32 v[22:23], v[22:23], v[6:7]\n"
    "v_pk_mul_f32 v[8:9], v[8:9], v[16:17]\n"
    "v_pk_mul_f32 v[10:11], v[10:11], v[18:19]\n"
    "v_pk_mul_f32 v[12:13], v[12:13], v[20:21]\n"
    "v_pk_mul_f32 v[14:15], v[14:15], v[22:23]\n"
    "v_cvt_pk_bf16_f32 v188, v16, v17\n"
    "v_cvt_pk_bf16_f32 v189, v18, v19\n"
    "v_cvt_pk_bf16_f32 v190, v20, v21\n"
    "v_cvt_pk_bf16_f32 v191, v22, v23\n"
    "v_cvt_pk_bf16_f32 v24, -v8, -v9\n"
    "v_cvt_pk_bf16_f32 v25, -v10, -v11\n"
    "v_cvt_pk_bf16_f32 v26, -v12, -v13\n"
    "v_cvt_pk_bf16_f32 v27, -v14, -v15\n"
    "ds_write_b128 v234, v[24:27] offset:0\n"
    "v_or_b32_e32 v0, 0x88000, v34\n"
    "v_or_b32_e32 v1, 0x98000, v34\n"
    "v_or_b32_e32 v2, 0xa8000, v34\n"
    "v_or_b32_e32 v3, 0xb8000, v34\n"
    "v_or_b32_e32 v4, 0xc8000, v34\n"
    "v_or_b32_e32 v5, 0xd8000, v34\n"
    "v_or_b32_e32 v6, 0xe8000, v34\n"
    "v_or_b32_e32 v7, 0xf8000, v34\n"
    "v_pk_mul_f32 v[0:1], v[0:1], v[36:37] op_sel_hi:[1,0]\n"
    "v_pk_mul_f32 v[2:3], v[2:3], v[36:37] op_sel_hi:[1,0]\n"
    "v_pk_mul_f32 v[4:5], v[4:5], v[36:37] op_sel_hi:[1,0]\n"
    "v_pk_mul_f32 v[6:7], v[6:7], v[36:37] op_sel_hi:[1,0]\n"
    "v_pk_mul_f32 v[8:9], v[0:1], v[0:1]\n"
    "v_pk_mul_f32 v[10:11], v[2:3], v[2:3]\n"
    "v_pk_mul_f32 v[12:13], v[4:5], v[4:5]\n"
    "v_pk_mul_f32 v[14:15], v[6:7], v[6:7]\n"
    "v_pk_fma_f32 v[8:9], v[8:9], v[38:39], v[32:33] op_sel_hi:[1,0,0]\n"
    "v_pk_fma_f32 v[10:11], v[10:11], v[38:39], v[32:33] op_sel_hi:[1,0,0]\n"
    "v_pk_fma_f32 v[12:13], v[12:13], v[38:39], v[32:33] op_sel_hi:[1,0,0]\n"
    "v_pk_fma_f32 v[14:15], v[14:15], v[38:39], v[32:33] op_sel_hi:[1,0,0]\n"
    "v_pk_mul_f32 v[8:9], v[8:9], v[0:1]\n"
    "v_pk_mul_f32 v[10:11], v[10:11], v[2:3]\n"
    "v_pk_mul_f32 v[12:13], v[12:13], v[4:5]\n"
    "v_pk_mul_f32 v[14:15], v[14:15], v[6:7]\n"
    "v_exp_f32_e64 v8, v8\n"
    "v_exp_f32_e64 v9, v9\n"
    "v_exp_f32_e64 v10, v10\n"
    "v_exp_f32_e64 v11, v11\n"
    "v_exp_f32_e64 v12, v12\n"
    "v_exp_f32_e64 v13, v13\n"
    "v_exp_f32_e64 v14, v14\n"
    "v_exp_f32_e64 v15, v15\n"
    "v_pk_add_f32 v[16:17], v[8:9], 1.0 op_sel_hi:[1,0]\n"
    "v_pk_add_f32 v[18:19], v[10:11], 1.0 op_sel_hi:[1,0]\n"
    "v_pk_add_f32 v[20:21], v[12:13], 1.0 op_sel_hi:[1,0]\n"
    "v_pk_add_f32 v[22:23], v[14:15], 1.0 op_sel_hi:[1,0]\n"
    "v_rcp_f32_e64 v16, v16\n"
    "v_rcp_f32_e64 v17, v17\n"
    "v_rcp_f32_e64 v18, v18\n"
    "v_rcp_f32_e64 v19, v19\n"
    "v_rcp_f32_e64 v20, v20\n"
    "v_rcp_f32_e64 v21, v21\n"
    "v_rcp_f32_e64 v22, v22\n"
    "v_rcp_f32_e64 v23, v23\n"
    "v_pk_mul_f32 v[16:17], v[16:17], v[0:1]\n"
    "v_pk_mul_f32 v[18:19], v[18:19], v[2:3]\n"
    "v_pk_mul_f32 v[20:21], v[20:21], v[4:5]\n"
    "v_pk_mul_f32 v[22:23], v[22:23], v[6:7]\n"
    "v_pk_mul_f32 v[8:9], v[8:9], v[16:17]\n"
    "v_pk_mul_f32 v[10:11], v[10:11], v[18:19]\n"
    "v_pk_mul_f32 v[12:13], v[12:13], v[20:21]\n"
    "v_pk_mul_f32 v[14:15], v[14:15], v[22:23]\n"
    "v_cvt_pk_bf16_f32 v192, v16, v17\n"
    "v_cvt_pk_bf16_f32 v193, v18, v19\n"
    "v_cvt_pk_bf16_f32 v194, v20, v21\n"
    "v_cvt_pk_bf16_f32 v195, v22, v23\n"
    "v_cvt_pk_bf16_f32 v24, -v8, -v9\n"
    "v_cvt_pk_bf16_f32 v25, -v10, -v11\n"
    "v_cvt_pk_bf16_f32 v26, -v12, -v13\n"
    "v_cvt_pk_bf16_f32 v27, -v14, -v15\n"
    "ds_write_b128 v234, v[24:27] offset:16\n"
    "v_add_u32_e32 v234, 65536, v234\n"
    "buffer_load_dword v229, v212, s[92:95], 0 offen offset:0\n"
    "s_nop 0\n"
    "s_waitcnt vmcnt(13)\n"
    "v_lshlrev_b32_sdwa v216, 2, v168 dst_sel:DWORD dst_unused:UNUSED_PAD src0_sel:DWORD src1_sel:WORD_0\n"
    "v_lshlrev_b32_sdwa v217, 2, v168 dst_sel:DWORD dst_unused:UNUSED_PAD src0_sel:DWORD src1_sel:WORD_1\n"
    "v_lshlrev_b32_sdwa v218, 2, v169 dst_sel:DWORD dst_unused:UNUSED_PAD src0_sel:DWORD src1_sel:WORD_0\n"
    "v_lshlrev_b32_sdwa v219, 2, v169 dst_sel:DWORD dst_unused:UNUSED_PAD src0_sel:DWORD src1_sel:WORD_1\n"
    "v_lshlrev_b32_sdwa v220, 2, v170 dst_sel:DWORD dst_unused:UNUSED_PAD src0_sel:DWORD src1_sel:WORD_0\n"
    "v_lshlrev_b32_sdwa v221, 2, v170 dst_sel:DWORD dst_unused:UNUSED_PAD src0_sel:DWORD src1_sel:WORD_1\n"
    "v_lshlrev_b32_sdwa v222, 2, v171 dst_sel:DWORD dst_unused:UNUSED_PAD src0_sel:DWORD src1_sel:WORD_0\n"
    "v_lshlrev_b32_sdwa v223, 2, v171 dst_sel:DWORD dst_unused:UNUSED_PAD src0_sel:DWORD src1_sel:WORD_1\n"
    "ds_write_b128 v222, v[88:91]\n"
    "buffer_load_dword v230, v212, s[92:95], 0 offen offset:192\n"
    "s_nop 0\n"
    "s_waitcnt vmcnt(13)\n"
    "v_lshlrev_b32_sdwa v207, 2, v172 dst_sel:DWORD dst_unused:UNUSED_PAD src0_sel:DWORD src1_sel:WORD_0\n"
    "v_lshlrev_b32_sdwa v242, 2, v173 dst_sel:DWORD dst_unused:UNUSED_PAD src0_sel:DWORD src1_sel:WORD_0\n"
    "v_lshlrev_b32_sdwa v243, 2, v173 dst_sel:DWORD dst_unused:UNUSED_PAD src0_sel:DWORD src1_sel:WORD_1\n"
    "v_lshlrev_b32_sdwa v244, 2, v174 dst_sel:DWORD dst_unused:UNUSED_PAD src0_sel:DWORD src1_sel:WORD_0\n"
    "v_lshlrev_b32_sdwa v248, 2, v174 dst_sel:DWORD dst_unused:UNUSED_PAD src0_sel:DWORD src1_sel:WORD_1\n"
    "v_lshlrev_b32_sdwa v249, 2, v175 dst_sel:DWORD dst_unused:UNUSED_PAD src0_sel:DWORD src1_sel:WORD_0\n"
    "v_lshlrev_b32_sdwa v250, 2, v175 dst_sel:DWORD dst_unused:UNUSED_PAD src0_sel:DWORD src1_sel:WORD_1\n"
    "v_bfe_i32 v201, v172, 16, 1\n"
    "v_and_b32_e32 v201, 0x35003500, v201\n"
    "v_bfe_i32 v203, v172, 18, 1\n"
    "v_and_b32_e32 v203, 0x35003500, v203\n"
    "v_bfe_i32 v205, v172, 20, 1\n"
    "v_and_b32_e32 v205, 0x35003500, v205\n"
    "v_bfe_i32 v92, v172, 26, 1\n"
    "v_xor_b32_e32 v92, 0xffff, v92\n"
    "v_and_b32_e32 v92, 0x3f803f80, v92\n"
    "v_bfe_i32 v236, v172, 22, 1\n"
    "v_and_b32_e64 v236, v92, v236\n"
    "v_bfe_i32 v237, v172, 23, 1\n"
    "v_and_b32_e32 v237, v92, v237\n"
    "buffer_load_dword v231, v212, s[92:95], 0 offen offset:384\n"
    "s_waitcnt vmcnt(13)\n"
    "v_lshlrev_b32_sdwa v251, 2, v176 dst_sel:DWORD dst_unused:UNUSED_PAD src0_sel:DWORD src1_sel:WORD_0\n"
    "v_lshlrev_b32_sdwa v252, 2, v176 dst_sel:DWORD dst_unused:UNUSED_PAD src0_sel:DWORD src1_sel:WORD_1\n"
    "v_lshlrev_b32_sdwa v253, 2, v177 dst_sel:DWORD dst_unused:UNUSED_PAD src0_sel:DWORD src1_sel:WORD_0\n"
    "v_lshlrev_b32_sdwa v254, 2, v177 dst_sel:DWORD dst_unused:UNUSED_PAD src0_sel:DWORD src1_sel:WORD_1\n"
    "v_lshlrev_b32_sdwa v241, 0, v178 dst_sel:DWORD dst_unused:UNUSED_PAD src0_sel:DWORD src1_sel:WORD_0\n"
    "v_lshlrev_b32_sdwa v245, 4, v178 dst_sel:DWORD dst_unused:UNUSED_PAD src0_sel:DWORD src1_sel:WORD_1\n"
    "v_lshlrev_b32_sdwa v255, 4, v179 dst_sel:DWORD dst_unused:UNUSED_PAD src0_sel:DWORD src1_sel:WORD_0\n"
    "v_lshlrev_b32_sdwa v247, 4, v179 dst_sel:DWORD dst_unused:UNUSED_PAD src0_sel:DWORD src1_sel:WORD_1\n"
    "v_bfe_i32 v238, v172, 24, 1\n"
    "v_and_b32_e64 v238, v92, v238\n"
    "v_bfe_i32 v239, v172, 25, 1\n"
    "v_and_b32_e64 v239, v92, v239\n"
    "v_bfe_u32 v93, v172, 28, 2\n"
    "v_mul_u32_u24_e32 v93, 393216, v93\n"
    "v_mov_b32_e64 v222, v254\n"
    "v_mov_b32_e32 v254, 0\n"
    "s_nop 1\n"
    "v_mfma_f32_32x32x1_2b_f32 v[0:31], v254, v254, 0\n"
    "global_load_dword v233, v214, s[70:71]\n"
    "s_mov_b32 s48, 0x44ea44ea\n"
    "s_mov_b32 s49, 0xb77fb77f\n"
    "s_mov_b32 s50, 0x89efde6e\n"
    "s_mov_b32 s46, s42\n"
    "s_mov_b32 s47, s43\n"
    "s_mul_i32 s54, s60, 592\n"
    "s_add_u32 s54, s54, -27008\n"
    "s_mul_i32 s55, s60, -1284\n"
    "s_add_u32 s55, s55, 54688\n"
    "s_mul_i32 s56, s60, -6160\n"
    "s_add_u32 s56, s56, -27008\n"
    "s_add_u32 s98, s99, 1375872\n"
    "s_and_b32 s36, s60, 1\n"
    "s_lshl_b32 s36, s36, 13\n"
    "s_add_u32 s36, s36, 327680\n"
    "s_mul_i32 s53, s60, 98304\n"
    "s_add_u32 s98, s53, s98\n"
    "s_xor_b32 s53, s60, 1\n"
    "s_mul_i32 s53, s53, 393216\n"
    "s_add_u32 s99, s99, s53\n"
    "s_add_u32 s53, s98, 393216\n"
    "s_sub_u32 s53, s53, s36\n"
    "s_and_b32 s37, s61, 1\n"
    "s_lshl_b32 s37, s37, 2\n"
    "s_add_u32 s37, s37, s60\n"
    "s_lshl_b32 s37, s37, 12\n"
    "s_cmp_eq_u32 s61, 0\n"
    "s_cselect_b32 s67, 2, 0\n"
    "s_cmp_eq_u32 s61, 31\n"
    "s_cselect_b32 s67, 1, s67\n"
    "s_lshl_b32 s67, s67, 30\n"
    "s_mov_b64 s[68:69], s[44:45]\n"
    "s_mov_b32 s70, 0\n"
    "s_mov_b32 s71, s47\n"
    "s_mov_b64 s[72:73], s[40:41]\n"
    "s_mov_b64 s[74:75], s[42:43]\n"
    "s_mov_b32 s57, 0\n"
    "s_mov_b32 s58, 196608\n"
    "s_mov_b32 s60, 0\n"
    "s_mov_b32 s61, 393216\n"
    "s_mov_b32 s62, 786432\n"
    "s_mov_b32 s63, 1179648\n"
    "s_mov_b32 s52, 1572864\n"
    "s_mov_b32 s88, 98304\n"
    "s_mov_b32 s89, 294912\n"
    "s_mov_b32 s39, 0\n"
    "s_getpc_b64 s[76:77]\n"
    ".Lreplk_pc1:\n"
    "s_add_u32 s76, s76, .Lreplk_last-.Lreplk_pc1\n"
    "s_addc_u32 s77, s77, 0\n"
    "s_sub_u32 s76, s76, s36\n"
    "s_subb_u32 s77, s77, 0\n"
    "s_add_u32 s78, s36, .Lreplk_end-.Lreplk_last\n"
    "s_mov_b32 s79, s37\n"
    "s_add_u32 m0, s38, 0\n"
    "s_mov_b32 s64, 0\n"
    "buffer_load_dwordx4 v210, s[84:87], s64 offen offset:0 lds\n"
    "s_waitcnt vmcnt(13)\n"
    "ds_write2_b32 v216, a224, a225 offset0:0 offset1:20\n"
    "v_add3_u32 v247, v247, v93, s98\n"
    "v_add_u32_e32 v246, s98, v255\n"
    "v_add_u32_e64 v245, s99, v245\n"
    "v_add_u32_e64 v255, s99, v255\n"
    "ds_write2_b32 v216, a226, a227 offset0:40 offset1:60\n"
    "v_and_b32_e64 v93, s67, v172\n"
    "v_and_b32_e32 v94, 0x80000000, v93\n"
    "s_waitcnt vmcnt(12)\n"
    "ds_write2_b32 v216, a228, a229 offset0:80 offset1:100\n"
    "v_and_b32_e32 v93, 0x40000000, v93\n"
    "v_or_b32_e64 v246, v246, v94\n"
    "v_or_b32_e64 v247, v247, v93\n"
    "ds_write2_b32 v216, a230, a231 offset0:120 offset1:140\n"
    "v_add_u32_e32 v216, 1360, v216\n"
    "buffer_load_dwordx4 v210, s[84:87], s64 offen offset:1024 lds\n"
    "s_waitcnt vmcnt(12)\n"
    "ds_write2_b32 v216, a232, a233 offset0:0 offset1:20\n"
    "ds_write2_b32 v216, a234, a235 offset0:40 offset1:60\n"
    "buffer_load_dwordx4 v210, s[84:87], s64 offen offset:2048 lds\n"
    "s_waitcnt vmcnt(12)\n"
    "ds_write2_b32 v216, a236, a237 offset0:80 offset1:100\n"
    "s_add_u32 m0, s38, 3072\n"
    "s_mov_b32 s64, 3072\n"
    "buffer_load_dwordx4 v210, s[84:87], s64 offen offset:0 lds\n"
    "buffer_load_dwordx4 v210, s[84:87], s64 offen offset:1024 lds\n"
    "ds_write2_b32 v216, a238, a239 offset0:120 offset1:140\n"
    "buffer_load_dwordx4 v210, s[84:87], s64 offen offset:2048 lds\n"
    "s_add_u32 m0, s38, 6144\n"
    "s_mov_b32 s64, 6144\n"
    "buffer_load_dwordx4 v210, s[84:87], s64 offen offset:0 lds\n"
    "buffer_load_dwordx4 v210, s[84:87], s64 offen offset:1024 lds\n"
    "v_add_u32_e32 v216, 1360, v216\n"
    "s_waitcnt vmcnt(16)\n"
    "v_lshlrev_b32_e32 v40, 16, v224\n"
    "buffer_load_dwordx4 v210, s[84:87], s64 offen offset:2048 lds\n"
    "s_mov_b32 s65, 589824\n"
    "buffer_load_dwordx4 a[240:243], v208, s[40:43], s65 offen\n"
    "s_waitcnt vmcnt(17)\n"
    "v_lshlrev_b32_e32 v41, 16, v225\n"
    "s_mov_b32 s65, 688128\n"
    "buffer_load_dwordx4 a[244:247], v208, s[40:43], s65 offen\n"
    "s_mov_b32 s59, 49152\n"
    "buffer_load_dwordx4 v[96:99], v209, s[80:83], s59 offen offset:0\n"
    "s_waitcnt vmcnt(18)\n"
    "v_lshlrev_b32_e32 v42, 16, v226\n"
    "s_waitcnt vmcnt(17)\n"
    "v_lshlrev_b32_e32 v43, 16, v227\n"
    "s_waitcnt vmcnt(16)\n"
    "v_lshlrev_b32_e32 v44, 16, v228\n"
    "s_waitcnt vmcnt(15)\n"
    "v_lshlrev_b32_e32 v45, 16, v229\n"
    "s_waitcnt vmcnt(14)\n"
    "v_lshlrev_b32_e32 v46, 16, v230\n"
    "s_waitcnt vmcnt(13)\n"
    "v_lshlrev_b32_e32 v47, 16, v231\n"
    "v_add_f32_e64 v41, v41, v45\n"
    "v_add_f32_e64 v42, v42, v46\n"
    "v_add_f32_e64 v43, v43, v47\n"
    "v_cvt_pk_bf16_f32 v56, v40, v41\n"
    "v_cvt_pk_bf16_f32 v57, v42, v43\n"
    "v_cvt_pk_bf16_f32 v58, v44, 0\n"
    "v_cvt_pk_bf16_f32 v64, 0, v40\n"
    "buffer_load_dwordx4 v[100:103], v209, s[80:83], s59 offen offset:256\n"
    "ds_write_b128 v221, v[56:59] offset:0\n"
    "v_cvt_pk_bf16_f32 v65, v41, v42\n"
    "v_cvt_pk_bf16_f32 v66, v43, v44\n"
    "v_cvt_pk_bf16_f32 v61, v40, v41\n"
    "v_cvt_pk_bf16_f32 v62, v42, v43\n"
    "v_cvt_pk_bf16_f32 v63, v44, 0\n"
    "v_cvt_pk_bf16_f32 v69, 0, v40\n"
    "v_cvt_pk_bf16_f32 v70, v41, v42\n"
    "v_cvt_pk_bf16_f32 v71, v43, v44\n"
    "ds_write_b128 v221, v[64:67] offset:16\n"
    "buffer_load_dwordx4 v[104:107], v209, s[80:83], s59 offen offset:512\n"
    "v_and_b32_e32 v48, 0xffff0000, v224\n"
    "v_and_b32_e32 v49, 0xffff0000, v225\n"
    "v_and_b32_e32 v50, 0xffff0000, v226\n"
    "v_and_b32_e32 v51, 0xffff0000, v227\n"
    "v_and_b32_e32 v52, 0xffff0000, v228\n"
    "ds_write_b128 v221, v[60:63] offset:32\n"
    "v_and_b32_e32 v53, 0xffff0000, v229\n"
    "v_and_b32_e32 v54, 0xffff0000, v230\n"
    "v_and_b32_e32 v55, 0xffff0000, v231\n"
    "v_add_f32_e64 v49, v49, v53\n"
    "v_add_f32_e64 v50, v50, v54\n"
    "v_add_f32_e64 v51, v51, v55\n"
    "ds_write_b128 v221, v[68:71] offset:48\n"
    "s_mov_b32 s65, 983040\n"
    "buffer_load_dwordx4 a[248:251], v208, s[40:43], s65 offen\n"
    "v_cvt_pk_bf16_f32 v72, v48, v49\n"
    "v_cvt_pk_bf16_f32 v73, v50, v51\n"
    "v_cvt_pk_bf16_f32 v74, v52, 0\n"
    "v_cvt_pk_bf16_f32 v80, 0, v48\n"
    "v_cvt_pk_bf16_f32 v81, v49, v50\n"
    "v_cvt_pk_bf16_f32 v82, v51, v52\n"
    "v_cvt_pk_bf16_f32 v77, v48, v49\n"
    "v_cvt_pk_bf16_f32 v78, v50, v51\n"
    "v_cvt_pk_bf16_f32 v79, v52, 0\n"
    "v_cvt_pk_bf16_f32 v85, 0, v48\n"
    "v_cvt_pk_bf16_f32 v86, v49, v50\n"
    "v_cvt_pk_bf16_f32 v87, v51, v52\n"
    "ds_write_b128 v221, v[72:75] offset:336\n"
    "s_mov_b32 s65, 1081344\n"
    "buffer_load_dwordx4 a[252:255], v208, s[40:43], s65 offen\n"
    "v_xor_b32_e32 v224, 0, v219\n"
    "v_xor_b32_e32 v225, 32, v219\n"
    "v_xor_b32_e32 v226, 64, v219\n"
    "v_xor_b32_e32 v227, 96, v219\n"
    "ds_write_b128 v221, v[80:83] offset:352\n"
    "v_xor_b32_e32 v228, 0, v223\n"
    "v_xor_b32_e32 v229, 32, v223\n"
    "s_mov_b32 s59, 52224\n"
    "buffer_load_dwordx4 v[108:111], v209, s[80:83], s59 offen offset:0\n"
    "v_xor_b32_e32 v230, 64, v223\n"
    "v_xor_b32_e32 v231, 96, v223\n"
    "ds_write_b128 v221, v[76:79] offset:368\n"
    "v_lshl_add_u32 v196, s51, 8, v225\n"
    "v_lshl_add_u32 v197, s51, 8, v227\n"
    "v_mfma_f32_32x32x1_2b_f32 v[32:63], v254, v254, 0\n"
    "ds_write_b128 v221, v[84:87] offset:384\n"
    "buffer_load_dwordx4 v[112:115], v209, s[80:83], s59 offen offset:256\n"
    "v_mfma_f32_32x32x1_2b_f32 v[64:95], v254, v254, 0\n"
    "v_add_u32_e64 v254, s54, v242\n"
    "buffer_load_dwordx4 v[116:119], v209, s[80:83], s59 offen offset:512\n"
    "s_mov_b32 s65, 1376256\n"
    "buffer_load_dwordx4 a[176:179], v208, s[40:43], s65 offen\n"
    "s_mov_b32 s65, 1474560\n"
    "buffer_load_dwordx4 a[180:183], v208, s[40:43], s65 offen\n"
    "s_waitcnt vmcnt(12)\n"
    "s_barrier\n"
    "ds_read_b64_tr_b16 a[0:1], v220 offset:0\n"
    "ds_read_b64_tr_b16 a[2:3], v220 offset:3072\n"
    "ds_read_b64_tr_b16 a[4:5], v220 offset:8\n"
    "ds_read_b64_tr_b16 a[6:7], v220 offset:3080\n"
    "ds_read_b64_tr_b16 a[8:9], v220 offset:64\n"
    "ds_read_b64_tr_b16 a[10:11], v220 offset:3136\n"
    "ds_read_b64_tr_b16 a[12:13], v220 offset:72\n"
    "ds_read_b64_tr_b16 a[14:15], v220 offset:3144\n"
    "ds_read_b64_tr_b16 v[168:169], v218 offset:128\n"
    "ds_read_b64_tr_b16 v[170:171], v218 offset:3200\n"
    "ds_read_b64_tr_b16 a[16:17], v220 offset:6144\n"
    "ds_read_b64_tr_b16 a[18:19], v220 offset:9216\n"
    "ds_read_b64_tr_b16 a[20:21], v220 offset:6152\n"
    "ds_read_b64_tr_b16 a[22:23], v220 offset:9224\n"
    "ds_read_b64_tr_b16 a[24:25], v220 offset:6208\n"
    "ds_read_b64_tr_b16 a[26:27], v220 offset:9280\n"
    "ds_read_b64_tr_b16 a[28:29], v220 offset:6216\n"
    "ds_read_b64_tr_b16 a[30:31], v220 offset:9288\n"
    "ds_read_b64_tr_b16 v[172:173], v218 offset:6272\n"
    "s_nop 0\n"
    "s_mov_b32 s59, 0\n"
    "buffer_load_dwordx4 v[120:123], v209, s[80:83], s59 offen offset:0\n"
    "ds_read_b64_tr_b16 v[174:175], v218 offset:9344\n"
    "ds_read_b64_tr_b16 a[32:33], v220 offset:12288\n"
    "ds_read_b64_tr_b16 a[34:35], v220 offset:15360\n"
    "ds_read_b64_tr_b16 a[36:37], v220 offset:12296\n"
    "ds_read_b64_tr_b16 a[38:39], v220 offset:15368\n"
    "ds_read_b64_tr_b16 a[40:41], v220 offset:12352\n"
    "ds_read_b64_tr_b16 a[42:43], v220 offset:15424\n"
    "ds_read_b64_tr_b16 a[44:45], v220 offset:12360\n"
    "ds_read_b64_tr_b16 a[46:47], v220 offset:15432\n"
    "ds_read_b64_tr_b16 v[176:177], v218 offset:12416\n"
    "ds_read_b64_tr_b16 v[178:179], v218 offset:15488\n"
    "s_waitcnt vmcnt(12)\n"
    "ds_write2_b32 v216, a240, a241 offset0:0 offset1:20\n"
    "ds_write2_b32 v216, a242, a243 offset0:40 offset1:60\n"
    "buffer_load_dwordx4 v[124:127], v209, s[80:83], s59 offen offset:256\n"
    "s_waitcnt vmcnt(12)\n"
    "ds_write2_b32 v216, a244, a245 offset0:80 offset1:100\n"
    "s_waitcnt lgkmcnt(2)\n"
    "s_barrier\n"
    "ds_write2_b32 v216, a246, a247 offset0:120 offset1:140\n"
    "buffer_load_dwordx4 v[128:131], v209, s[80:83], s59 offen offset:512\n"
    "v_add_u32_e32 v216, 1360, v216\n"
    "s_waitcnt vmcnt(12)\n"
    "v_or3_b32 v200, v200, v96, v97\n"
    "v_or3_b32 v200, v200, v98, v99\n"
    "v_and_b32_e32 v232, 0x7fff7fff, v96\n"
    "v_add_u32_e32 v96, v201, v96\n"
    "v_add_u32_e64 v98, v201, v98\n"
    "ds_write_b128 v217, v[96:99] offset:24576\n"
    "s_mov_b32 s65, 0\n"
    "buffer_load_dwordx4 a[184:187], v208, s[40:43], s65 offen\n"
    "s_waitcnt vmcnt(12)\n"
    "v_or3_b32 v200, v200, v100, v101\n"
    "v_or3_b32 v200, v200, v102, v103\n"
    "v_add_u32_e64 v100, v203, v100\n"
    "v_add_u32_e64 v102, v203, v102\n"
    "ds_write_b128 v217, v[100:103] offset:24832\n"
    "s_mov_b32 s65, 98304\n"
    "buffer_load_dwordx4 a[188:191], v208, s[40:43], s65 offen\n"
    "s_waitcnt vmcnt(12)\n"
    "v_or3_b32 v200, v200, v104, v105\n"
    "v_or3_b32 v200, v200, v106, v107\n"
    "v_add_u32_e32 v104, v205, v104\n"
    "v_add_u32_e64 v106, v205, v106\n"
    "ds_write_b128 v217, v[104:107] offset:25088\n"
    "s_waitcnt lgkmcnt(0)\n"
    "s_barrier\n"
    "s_mov_b32 s59, 3072\n"
    "buffer_load_dwordx4 v[132:135], v209, s[80:83], s59 offen offset:0\n"
    "ds_read_b64_tr_b16 a[48:49], v224 offset:24576\n"
    "ds_read_b64_tr_b16 v[180:181], v196 offset:24576\n"
    "ds_read_b64_tr_b16 a[52:53], v226 offset:24576\n"
    "ds_read_b64_tr_b16 v[184:185], v197 offset:24576\n"
    "ds_read_b64_tr_b16 a[56:57], v228 offset:24576\n"
    "ds_read_b64_tr_b16 a[60:61], v229 offset:24576\n"
    "ds_read_b64_tr_b16 a[64:65], v230 offset:24576\n"
    "ds_read_b64_tr_b16 a[68:69], v231 offset:24576\n"
    "ds_read_b64_tr_b16 a[72:73], v224 offset:24832\n"
    "ds_read_b64_tr_b16 a[76:77], v226 offset:24832\n"
    "s_waitcnt vmcnt(12)\n"
    "ds_write2_b32 v216, a248, a249 offset0:0 offset1:20\n"
    "ds_write2_b32 v216, a250, a251 offset0:40 offset1:60\n"
    "buffer_load_dwordx4 v[136:139], v209, s[80:83], s59 offen offset:256\n"
    "s_waitcnt vmcnt(12)\n"
    "ds_write2_b32 v216, a252, a253 offset0:80 offset1:100\n"
    "ds_write2_b32 v216, a254, a255 offset0:120 offset1:140\n"
    "v_add_u32_e32 v216, 1360, v216\n"
    "buffer_load_dwordx4 v[140:143], v209, s[80:83], s59 offen offset:512\n"
    "s_waitcnt vmcnt(12)\n"
    "v_or3_b32 v200, v200, v108, v109\n"
    "v_or3_b32 v200, v200, v110, v111\n"
    "v_add_u32_e32 v108, v201, v108\n"
    "v_add_u32_e64 v110, v201, v110\n"
    "ds_write_b128 v217, v[108:111] offset:0\n"
    "s_mov_b32 s65, 393216\n"
    "buffer_load_dwordx4 a[192:195], v208, s[40:43], s65 offen\n"
    "s_waitcnt vmcnt(12)\n"
    "v_or3_b32 v200, v200, v112, v113\n"
    "v_or3_b32 v200, v200, v114, v115\n"
    "v_add_u32_e32 v112, v203, v112\n"
    "v_add_u32_e64 v114, v203, v114\n"
    "ds_write_b128 v217, v[112:115] offset:256\n"
    "v_mov_b32_e64 v112, v188\n"
    "v_mov_b32_e64 v113, v189\n"
    "v_mov_b32_e64 v114, v190\n"
    "v_mov_b32_e32 v115, v191\n"
    "s_mov_b32 s65, 491520\n"
    "buffer_load_dwordx4 a[196:199], v208, s[40:43], s65 offen\n"
    "s_waitcnt vmcnt(12)\n"
    "v_or3_b32 v200, v200, v116, v117\n"
    "v_or3_b32 v200, v200, v118, v119\n"
    "v_add_u32_e64 v116, v205, v116\n"
    "v_add_u32_e64 v118, v205, v118\n"
    "ds_write_b128 v217, v[116:119] offset:512\n"
    "v_mov_b32_e64 v116, v192\n"
    "v_mov_b32_e64 v117, v193\n"
    "v_mov_b32_e64 v118, v194\n"
    "v_mov_b32_e64 v119, v195\n"
    "s_waitcnt lgkmcnt(0)\n"
    "s_barrier\n"
    "s_mov_b32 s59, 24576\n"
    "buffer_load_dwordx4 v[144:147], v209, s[80:83], s59 offen offset:0\n"
    "ds_read_b64_tr_b16 a[50:51], v224 offset:0\n"
    "ds_read_b64_tr_b16 v[182:183], v196 offset:0\n"
    "ds_read_b64_tr_b16 a[54:55], v226 offset:0\n"
    "ds_read_b64_tr_b16 v[186:187], v197 offset:0\n"
    "ds_read_b64_tr_b16 a[58:59], v228 offset:0\n"
    "ds_read_b64_tr_b16 a[62:63], v229 offset:0\n"
    "ds_read_b64_tr_b16 a[66:67], v230 offset:0\n"
    "ds_read_b64_tr_b16 a[70:71], v231 offset:0\n"
    "ds_read_b64_tr_b16 a[74:75], v224 offset:256\n"
    "ds_read_b64_tr_b16 a[78:79], v226 offset:256\n"
    "s_waitcnt vmcnt(12)\n"
    "ds_write2_b32 v216, a176, a177 offset0:0 offset1:20\n"
    "ds_write2_b32 v216, a178, a179 offset0:40 offset1:60\n"
    "s_waitcnt lgkmcnt(8)\n"
    "ds_write_b128 v207, v[184:187] offset:36864\n"
    "buffer_load_dwordx4 v[148:151], v209, s[80:83], s59 offen offset:256\n"
    "s_waitcnt vmcnt(12)\n"
    "ds_write2_b32 v216, a180, a181 offset0:80 offset1:100\n"
    "ds_write2_b32 v216, a182, a183 offset0:120 offset1:140\n"
    "v_add_u32_e32 v216, -4800, v216\n"
    "buffer_load_dwordx4 v[152:155], v209, s[80:83], s59 offen offset:512\n"
    "s_waitcnt vmcnt(12)\n"
    "v_or3_b32 v200, v200, v120, v121\n"
    "v_or3_b32 v200, v200, v122, v123\n"
    "v_add_u32_e64 v120, v201, v120\n"
    "v_add_u32_e64 v122, v201, v122\n"
    "ds_write_b128 v217, v[120:123] offset:12288\n"
    "s_waitcnt lgkmcnt(1)\n"
    "s_barrier\n"
    "s_mov_b32 s65, 786432\n"
    "buffer_load_dwordx4 a[200:203], v208, s[40:43], s65 offen\n"
    "s_waitcnt vmcnt(12)\n"
    "v_or3_b32 v200, v200, v124, v125\n"
    "v_or3_b32 v200, v200, v126, v127\n"
    "v_add_u32_e32 v124, v203, v124\n"
    "v_add_u32_e64 v126, v203, v126\n"
    "ds_write_b128 v217, v[124:127] offset:12544\n"
    "s_mov_b32 s65, 884736\n"
    "buffer_load_dwordx4 a[204:207], v208, s[40:43], s65 offen\n"
    "s_waitcnt vmcnt(12)\n"
    "v_or3_b32 v200, v200, v128, v129\n"
    "v_or3_b32 v200, v200, v130, v131\n"
    "v_add_u32_e32 v128, v205, v128\n"
    "v_add_u32_e64 v130, v205, v130\n"
    "ds_write_b128 v217, v[128:131] offset:12800\n"
    "ds_read_b128 a[224:227], v250 offset:0\n"
    "ds_read_b128 a[228:231], v250 offset:320\n"
    "ds_read_b128 a[232:235], v251 offset:0\n"
    "ds_read_b128 a[236:239], v251 offset:336\n"
    "ds_read_b128 a[240:243], v250 offset:80\n"
    "ds_read_b128 a[244:247], v250 offset:400\n"
    "ds_read_b128 a[248:251], v251 offset:672\n"
    "ds_read_b128 a[252:255], v251 offset:1008\n"
    "ds_read_b128 v[104:107], v252 offset:1344\n"
    "ds_read_b128 v[108:111], v252 offset:1680\n"
    "ds_read_b128 v[128:131], v252 offset:2016\n"
    "s_waitcnt lgkmcnt(11)\n"
    "s_barrier\n"
    "s_mov_b32 s59, 27648\n"
    "buffer_load_dwordx4 v[156:159], v209, s[80:83], s59 offen offset:0\n"
    "ds_read_b64_tr_b16 a[80:81], v224 offset:12288\n"
    "ds_read_b64_tr_b16 a[84:85], v225 offset:12288\n"
    "ds_read_b64_tr_b16 a[88:89], v226 offset:12288\n"
    "ds_read_b64_tr_b16 a[92:93], v227 offset:12288\n"
    "s_waitcnt lgkmcnt(12)\n"
    "v_smfmac_f32_16x16x64_bf16 v[0:3], a[232:235], a[224:231], v240\n"
    "s_waitcnt lgkmcnt(11)\n"
    "ds_read_b64_tr_b16 a[96:97], v228 offset:12288\n"
    "ds_read_b64_tr_b16 a[100:101], v229 offset:12288\n"
    "v_smfmac_f32_16x16x64_bf16 v[4:7], a[236:239], a[224:231], v240 abid:1\n"
    "ds_read_b64_tr_b16 a[104:105], v230 offset:12288\n"
    "ds_read_b64_tr_b16 a[108:109], v231 offset:12288\n"
    "ds_read_b64_tr_b16 a[112:113], v224 offset:12544\n"
    "ds_read_b64_tr_b16 a[116:117], v225 offset:12544\n"
    "ds_read_b64_tr_b16 a[120:121], v226 offset:12544\n"
    "s_waitcnt lgkmcnt(15)\n"
    "v_smfmac_f32_16x16x64_bf16 v[8:11], a[248:251], a[240:247], v240\n"
    "s_waitcnt lgkmcnt(14)\n"
    "ds_read_b64_tr_b16 a[124:125], v227 offset:12544\n"
    "s_waitcnt vmcnt(12)\n"
    "ds_write2_b32 v216, a184, a185 offset0:0 offset1:20\n"
    "v_smfmac_f32_16x16x64_bf16 v[12:15], a[252:255], a[240:247], v240 abid:1\n"
    "s_nop 0\n"
    "ds_write2_b32 v216, a186, a187 offset0:40 offset1:60\n"
    "ds_read_b128 a[224:227], v250 offset:160\n"
    "ds_read_b128 a[228:231], v250 offset:480\n"
    "ds_read_b128 a[232:235], v251 offset:1344\n"
    "ds_read_b128 a[236:239], v251 offset:1680\n"
    "buffer_load_dwordx4 v[160:163], v209, s[80:83], s59 offen offset:256\n"
    "s_waitcnt vmcnt(12)\n"
    "ds_write2_b32 v216, a188, a189 offset0:80 offset1:100\n"
    "ds_write2_b32 v216, a190, a191 offset0:120 offset1:140\n"
    "v_add_u32_e32 v216, 1360, v216\n"
    "ds_read_b128 a[240:243], v250 offset:240\n"
    "ds_read_b128 a[244:247], v250 offset:560\n"
    "buffer_load_dwordx4 v[164:167], v209, s[80:83], s59 offen offset:512\n"
    "s_waitcnt vmcnt(12)\n"
    "v_or3_b32 v200, v200, v132, v133\n"
    "v_or3_b32 v200, v200, v134, v135\n"
    "v_add_u32_e64 v132, v201, v132\n"
    "v_add_u32_e64 v134, v201, v134\n"
    "ds_write_b128 v217, v[132:135] offset:24576\n"
    "s_waitcnt lgkmcnt(6)\n"
    "v_smfmac_f32_16x16x64_bf16 v[16:19], a[232:235], a[224:231], v240\n"
    "s_waitcnt lgkmcnt(5)\n"
    "v_smfmac_f32_16x16x64_bf16 v[20:23], a[236:239], a[224:231], v240 abid:1\n"
    "s_mov_b32 s65, 1179648\n"
    "buffer_load_dwordx4 a[208:211], v208, s[40:43], s65 offen\n"
    "s_waitcnt vmcnt(12)\n"
    "v_or3_b32 v200, v200, v136, v137\n"
    "v_or3_b32 v200, v200, v138, v139\n"
    "v_add_u32_e32 v136, v203, v136\n"
    "v_add_u32_e64 v138, v203, v138\n"
    "ds_write_b128 v217, v[136:139] offset:24832\n"
    "ds_read_b128 a[248:251], v251 offset:2016\n"
    "ds_read_b128 a[252:255], v251 offset:2352\n"
    "s_mov_b32 s65, 1277952\n"
    "buffer_load_dwordx4 a[212:215], v208, s[40:43], s65 offen\n"
    "s_waitcnt vmcnt(12)\n"
    "v_or3_b32 v200, v200, v140, v141\n"
    "v_or3_b32 v200, v200, v142, v143\n"
    "v_add_u32_e32 v140, v205, v140\n"
    "v_add_u32_e64 v142, v205, v142\n"
    "ds_write_b128 v217, v[140:143] offset:25088\n"
    "ds_read_b128 a[224:227], v250 offset:27024\n"
    "ds_read_b128 a[228:231], v250 offset:27344\n"
    "ds_read_b128 a[232:235], v251 offset:10752\n"
    "ds_read_b128 a[236:239], v251 offset:11088\n"
    "ds_read_b128 v[132:135], v252 offset:2352\n"
    "s_waitcnt lgkmcnt(7)\n"
    "v_smfmac_f32_16x16x64_bf16 v[24:27], a[248:251], a[240:247], v240\n"
    "ds_read_b128 a[248:251], v251 offset:11424\n"
    "s_waitcnt lgkmcnt(7)\n"
    "v_smfmac_f32_16x16x64_bf16 v[28:31], a[252:255], a[240:247], v240 abid:1\n"
    "ds_read_b128 a[240:243], v250 offset:27104\n"
    "ds_read_b128 a[244:247], v250 offset:27424\n"
    "ds_read_b128 a[252:255], v251 offset:11760\n"
    "s_waitcnt lgkmcnt(9)\n"
    "s_barrier\n"
    "s_mov_b32 s65, 1572864\n"
    "buffer_load_dwordx4 a[216:219], v208, s[40:43], s65 offen\n"
    "ds_read_b64_tr_b16 a[82:83], v224 offset:24576\n"
    "ds_read_b64_tr_b16 a[86:87], v225 offset:24576\n"
    "ds_read_b64_tr_b16 a[90:91], v226 offset:24576\n"
    "s_waitcnt lgkmcnt(9)\n"
    "v_smfmac_f32_16x16x64_bf16 v[32:35], a[232:235], a[224:231], v240\n"
    "s_waitcnt lgkmcnt(8)\n"
    "ds_read_b64_tr_b16 a[94:95], v227 offset:24576\n"
    "ds_read_b64_tr_b16 a[98:99], v228 offset:24576\n"
    "v_smfmac_f32_16x16x64_bf16 v[36:39], a[236:239], a[224:231], v240 abid:1\n"
    "ds_read_b64_tr_b16 a[102:103], v229 offset:24576\n"
    "ds_read_b64_tr_b16 a[106:107], v230 offset:24576\n"
    "ds_read_b64_tr_b16 a[110:111], v231 offset:24576\n"
    "ds_read_b64_tr_b16 a[114:115], v224 offset:24832\n"
    "ds_read_b64_tr_b16 a[118:119], v225 offset:24832\n"
    "ds_read_b64_tr_b16 a[122:123], v226 offset:24832\n"
    "ds_read_b64_tr_b16 a[126:127], v227 offset:24832\n"
    "s_waitcnt lgkmcnt(13)\n"
    "v_smfmac_f32_16x16x64_bf16 v[40:43], a[248:251], a[240:247], v240\n"
    "s_waitcnt lgkmcnt(12)\n"
    "s_waitcnt vmcnt(12)\n"
    "ds_write2_b32 v216, a192, a193 offset0:0 offset1:20\n"
    "v_smfmac_f32_16x16x64_bf16 v[44:47], a[252:255], a[240:247], v240 abid:1\n"
    "s_nop 0\n"
    "ds_write2_b32 v216, a194, a195 offset0:40 offset1:60\n"
    "ds_read_b128 a[224:227], v250 offset:27184\n"
    "ds_read_b128 a[228:231], v250 offset:27504\n"
    "ds_read_b128 a[232:235], v251 offset:12096\n"
    "s_mov_b32 s65, 1671168\n"
    "buffer_load_dwordx4 a[220:223], v208, s[40:43], s65 offen\n"
    "s_waitcnt vmcnt(12)\n"
    "ds_write2_b32 v216, a196, a197 offset0:80 offset1:100\n"
    "ds_write2_b32 v216, a198, a199 offset0:120 offset1:140\n"
    "v_add_u32_e32 v216, 1360, v216\n"
    "ds_read_b128 a[236:239], v251 offset:12432\n"
    "ds_read_b128 a[240:243], v250 offset:27264\n"
    "ds_read_b128 a[244:247], v250 offset:27584\n"
    "s_waitcnt vmcnt(11)\n"
    "v_or3_b32 v200, v200, v144, v145\n"
    "v_or3_b32 v200, v200, v146, v147\n"
    "v_add_u32_e64 v144, v201, v144\n"
    "v_add_u32_e32 v146, v201, v146\n"
    "ds_write_b128 v217, v[144:147] offset:0\n"
    "s_waitcnt lgkmcnt(6)\n"
    "v_smfmac_f32_16x16x64_bf16 v[48:51], a[232:235], a[224:231], v240\n"
    "ds_read_b128 a[248:251], v251 offset:12768\n"
    "ds_read_b128 a[252:255], v251 offset:13104\n"
    "ds_read_b128 a[232:235], v251 offset:21504\n"
    "s_waitcnt vmcnt(10)\n"
    "v_or3_b32 v200, v200, v148, v149\n"
    "v_or3_b32 v200, v200, v150, v151\n"
    "v_add_u32_e32 v148, v203, v148\n"
    "v_add_u32_e32 v150, v203, v150\n"
    "ds_write_b128 v217, v[148:151] offset:256\n"
    "s_waitcnt lgkmcnt(6)\n"
    "v_smfmac_f32_16x16x64_bf16 v[52:55], a[236:239], a[224:231], v240 abid:1\n"
    "ds_read_b128 a[224:227], v250 offset:54048\n"
    "ds_read_b128 a[228:231], v250 offset:54368\n"
    "ds_read_b128 a[236:239], v251 offset:21840\n"
    "s_waitcnt vmcnt(9)\n"
    "v_or3_b32 v200, v200, v152, v153\n"
    "v_or3_b32 v200, v200, v154, v155\n"
    "v_add_u32_e32 v152, v205, v152\n"
    "v_add_u32_e32 v154, v205, v154\n"
    "ds_write_b128 v217, v[152:155] offset:512\n"
    "s_waitcnt lgkmcnt(6)\n"
    "v_smfmac_f32_16x16x64_bf16 v[56:59], a[248:251], a[240:247], v240\n"
    "v_smfmac_f32_16x16x64_bf16 v[60:63], a[252:255], a[240:247], v240 abid:1\n"
    "ds_read_b128 a[240:243], v250 offset:54128\n"
    "ds_read_b128 a[244:247], v250 offset:54448\n"
    "ds_read_b128 a[248:251], v251 offset:22176\n"
    "ds_read_b128 a[252:255], v251 offset:22512\n"
    "ds_read_b128 v[144:147], v252 offset:10752\n"
    "ds_read_b128 v[148:151], v252 offset:11088\n"
    "s_waitcnt lgkmcnt(8)\n"
    "v_smfmac_f32_16x16x64_bf16 v[64:67], a[232:235], a[224:231], v240\n"
    "ds_read_b128 a[232:235], v251 offset:22848\n"
    "s_waitcnt lgkmcnt(8)\n"
    "v_smfmac_f32_16x16x64_bf16 v[68:71], a[236:239], a[224:231], v240 abid:1\n"
    "ds_read_b128 a[224:227], v250 offset:54208\n"
    "ds_read_b128 a[228:231], v250 offset:54528\n"
    "s_waitcnt lgkmcnt(9)\n"
    "s_barrier\n"
    "ds_read_b64_tr_b16 a[128:129], v224 offset:0\n"
    "ds_read_b64_tr_b16 a[132:133], v225 offset:0\n"
    "ds_read_b64_tr_b16 a[136:137], v226 offset:0\n"
    "s_waitcnt lgkmcnt(9)\n"
    "v_smfmac_f32_16x16x64_bf16 v[72:75], a[248:251], a[240:247], v240\n"
    "s_waitcnt lgkmcnt(8)\n"
    "ds_read_b64_tr_b16 a[140:141], v227 offset:0\n"
    "ds_read_b64_tr_b16 a[144:145], v228 offset:0\n"
    "v_smfmac_f32_16x16x64_bf16 v[76:79], a[252:255], a[240:247], v240 abid:1\n"
    "ds_read_b64_tr_b16 a[148:149], v229 offset:0\n"
    "ds_read_b64_tr_b16 a[152:153], v230 offset:0\n"
    "ds_read_b64_tr_b16 a[156:157], v231 offset:0\n"
    "ds_read_b64_tr_b16 a[160:161], v224 offset:256\n"
    "ds_read_b64_tr_b16 a[164:165], v225 offset:256\n"
    "ds_read_b64_tr_b16 a[168:169], v226 offset:256\n"
    "ds_read_b64_tr_b16 a[172:173], v227 offset:256\n"
    "s_waitcnt vmcnt(8)\n"
    "ds_write2_b32 v216, a200, a201 offset0:0 offset1:20\n"
    "s_waitcnt lgkmcnt(13)\n"
    "v_smfmac_f32_16x16x64_bf16 v[80:83], a[232:235], a[224:231], v240\n"
    "ds_write2_b32 v216, a202, a203 offset0:40 offset1:60\n"
    "ds_read_b128 a[236:239], v251 offset:23184\n"
    "ds_read_b128 a[240:243], v250 offset:54288\n"
    "ds_read_b128 a[244:247], v250 offset:54608\n"
    "ds_read_b128 a[248:251], v251 offset:23520\n"
    "s_waitcnt vmcnt(7)\n"
    "ds_write2_b32 v216, a204, a205 offset0:80 offset1:100\n"
    "ds_write2_b32 v216, a206, a207 offset0:120 offset1:140\n"
    "v_add_u32_e32 v216, 1360, v216\n"
    "ds_read_b128 a[252:255], v251 offset:23856\n"
    "ds_read_b128 a[232:235], v252 offset:0\n"
    "s_waitcnt vmcnt(6)\n"
    "v_or3_b32 v200, v200, v156, v157\n"
    "v_or3_b32 v200, v200, v158, v159\n"
    "v_add_u32_e64 v156, v201, v156\n"
    "v_add_u32_e64 v158, v201, v158\n"
    "ds_write_b128 v217, v[156:159] offset:12288\n"
    "s_waitcnt lgkmcnt(7)\n"
    "v_smfmac_f32_16x16x64_bf16 v[84:87], a[236:239], a[224:231], v240 abid:1\n"
    "s_waitcnt lgkmcnt(5)\n"
    "v_smfmac_f32_16x16x64_bf16 v[88:91], a[248:251], a[240:247], v240\n"
    "ds_read_b128 a[236:239], v252 offset:336\n"
    "ds_read_b128 a[248:251], v252 offset:672\n"
    "s_waitcnt vmcnt(5)\n"
    "v_or3_b32 v200, v200, v160, v161\n"
    "v_or3_b32 v200, v200, v162, v163\n"
    "v_add_u32_e32 v160, v203, v160\n"
    "v_add_u32_e64 v162, v203, v162\n"
    "ds_write_b128 v217, v[160:163] offset:12544\n"
    "s_waitcnt lgkmcnt(4)\n"
    "v_smfmac_f32_16x16x64_bf16 v[92:95], a[252:255], a[240:247], v240 abid:1\n"
    "ds_read_b128 a[252:255], v252 offset:1008\n"
    "ds_read_b128 v[160:163], v252 offset:11424\n"
    "s_waitcnt vmcnt(4)\n"
    "v_or3_b32 v200, v200, v164, v165\n"
    "v_or3_b32 v200, v200, v166, v167\n"
    "v_add_u32_e64 v164, v205, v164\n"
    "v_add_u32_e64 v166, v205, v166\n"
    "ds_write_b128 v217, v[164:167] offset:12800\n"
    "v_and_b32_e32 v200, 0x40004000, v200\n"
    "v_cmp_le_u32_e32 vcc, 1, v200\n"
    "s_nop 1\n"
    "s_cmp_lg_u64 vcc, 0\n"
    "s_cselect_b32 vcc_lo, 1, 0\n"
    "s_or_b32 s66, s66, vcc_lo\n"
    "v_cmp_le_u32_e32 vcc, 981467136, v232\n"
    "s_nop 1\n"
    "s_cmp_lg_u64 vcc, 0\n"
    "s_cselect_b32 vcc_lo, 2, 0\n"
    "s_or_b32 s66, s66, vcc_lo\n"
    "v_mov_b32_e32 v200, s66\n"
    "v_mov_b32_e32 v232, 162992\n"
    "s_mov_b64 exec, 1\n"
    "ds_or_b32 v232, v200\n"
    "s_mov_b64 exec, -1\n"
    "ds_read_b128 v[164:167], v252 offset:11760\n"
    "s_waitcnt lgkmcnt(2)\n"
    "s_barrier\n"
    "ds_read_b64_tr_b16 a[130:131], v224 offset:12288\n"
    "ds_read_b64_tr_b16 a[134:135], v225 offset:12288\n"
    "ds_read_b64_tr_b16 a[138:139], v226 offset:12288\n"
    "ds_read_b64_tr_b16 a[142:143], v227 offset:12288\n"
    "ds_read_b64_tr_b16 a[146:147], v228 offset:12288\n"
    "ds_read_b64_tr_b16 a[150:151], v229 offset:12288\n"
    "ds_read_b64_tr_b16 a[154:155], v230 offset:12288\n"
    "ds_read_b64_tr_b16 a[158:159], v231 offset:12288\n"
    "ds_read_b64_tr_b16 a[162:163], v224 offset:12544\n"
    "ds_read_b64_tr_b16 a[166:167], v225 offset:12544\n"
    "ds_read_b64_tr_b16 a[170:171], v226 offset:12544\n"
    "ds_read_b64_tr_b16 a[174:175], v227 offset:12544\n"
    "s_nop 0\n"
    "s_waitcnt vmcnt(3)\n"
    "ds_write2_b32 v216, a208, a209 offset0:0 offset1:20\n"
    "ds_write2_b32 v216, a210, a211 offset0:40 offset1:60\n"
    "s_waitcnt vmcnt(2)\n"
    "ds_write2_b32 v216, a212, a213 offset0:80 offset1:100\n"
    "s_waitcnt lgkmcnt(2)\n"
    "s_barrier\n"
    "s_add_u32 m0, s38, 0\n"
    "s_mov_b32 s64, 18432\n"
    "buffer_load_dwordx4 v210, s[84:87], s64 offen offset:0 lds\n"
    "buffer_load_dwordx4 v210, s[84:87], s64 offen offset:1024 lds\n"
    "buffer_load_dwordx4 v210, s[84:87], s64 offen offset:2048 lds\n"
    "s_add_u32 m0, s38, 3072\n"
    "s_mov_b32 s64, 21504\n"
    "buffer_load_dwordx4 v210, s[84:87], s64 offen offset:0 lds\n"
    "buffer_load_dwordx4 v210, s[84:87], s64 offen offset:1024 lds\n"
    "buffer_load_dwordx4 v210, s[84:87], s64 offen offset:2048 lds\n"
    "s_add_u32 m0, s38, 6144\n"
    "s_mov_b32 s64, 24576\n"
    "buffer_load_dwordx4 v210, s[84:87], s64 offen offset:0 lds\n"
    "buffer_load_dwordx4 v210, s[84:87], s64 offen offset:1024 lds\n"
    "buffer_load_dwordx4 v210, s[84:87], s64 offen offset:2048 lds\n"
    "s_sub_u32 s80, s76, .Lreplk_last-.Lreplk_loop\n"
    "s_subb_u32 s81, s77, 0\n"
    "s_add_u32 s82, s36, .Lreplk_last-.Lreplk_loop\n"
    "s_and_b32 s85, s37, 0x3fff\n"
    "s_add_u32 s84, s85, 8192\n"
    "s_lshr_b32 s85, s85, 2\n"
    "v_lshlrev_b32_e64 v235, 3, v244\n"
    "s_mov_b32 exec_hi, 0\n"
    "buffer_load_dword v233, v235, s[40:43], s53 offen sc1\n"
    "s_mov_b32 exec_hi, -1\n"
    "s_add_u32 s53, s53, 393216\n"
    "ds_write2_b32 v216, a214, a215 offset0:120 offset1:140\n"
    "v_add_u32_e32 v216, 1360, v216\n"
    "s_mov_b32 s64, 0\n"
    "s_mov_b32 s59, 0\n"
    "ds_read_b32 v232, v232\n"
    "v_lshlrev_b32_e32 v235, 3, v244\n"
    "s_mov_b32 exec_hi, 0\n"
    "buffer_load_dword v233, v235, s[40:43], s53 offen sc1\n"
    "s_mov_b32 exec_hi, -1\n"
    "s_add_u32 s53, s53, 393216\n"
    "s_waitcnt vmcnt(12)\n"
    "ds_write2_b32 v216, a216, a217 offset0:0 offset1:20\n"
    "ds_write2_b32 v216, a218, a219 offset0:40 offset1:60\n"
    "v_lshlrev_b32_e32 v235, 3, v244\n"
    "s_mov_b32 exec_hi, 0\n"
    "buffer_load_dword v233, v235, s[40:43], s53 offen sc1\n"
    "s_mov_b32 exec_hi, -1\n"
    "s_add_u32 s53, s53, 393216\n"
    "s_waitcnt vmcnt(12)\n"
    "ds_write2_b32 v216, a220, a221 offset0:80 offset1:100\n"
    "ds_write2_b32 v216, a222, a223 offset0:120 offset1:140\n"
    "v_lshlrev_b32_e64 v235, 3, v244\n"
    "s_mov_b32 exec_hi, 0\n"
    "buffer_load_dword v233, v235, s[40:43], s53 offen sc1\n"
    "s_mov_b32 exec_hi, -1\n"
    "s_add_u32 s53, s53, 393216\n"
    "v_mov_b32_e64 v235, v222\n"
    "s_waitcnt lgkmcnt(0)\n"
    "s_barrier\n"
    "s_getpc_b64 vcc\n"
    ".Lreplk_pc2:\n"
    "s_sub_u32 vcc_lo, vcc_lo, .Lreplk_pc2-.Lreplk_pc0\n"
    "s_subb_u32 vcc_hi, vcc_hi, 0\n"
    "global_load_dword v233, v215, vcc\n"
    "ds_read_b128 a[224:227], v250 offset:640\n"
    "ds_read_b128 a[228:231], v250 offset:960\n"
    "ds_read_b128 a[240:243], v250 offset:720\n"
    "ds_read_b128 a[244:247], v250 offset:1040\n"
    "ds_read_b128 v[96:99], v250 offset:800\n"
    "ds_read_b128 v[100:103], v250 offset:1120\n"
    "ds_read_b128 v[120:123], v250 offset:880\n"
    "ds_read_b128 v[124:127], v250 offset:1200\n"
    "ds_read_b128 v[136:139], v250 offset:27664\n"
    "ds_read_b128 v[140:143], v250 offset:27984\n"
    "ds_read_b128 v[152:155], v250 offset:27744\n"
    "ds_read_b128 v[156:159], v250 offset:28064\n"
    "s_waitcnt lgkmcnt(10)\n"
    "v_smfmac_f32_16x16x64_bf16 v[0:3], a[232:235], a[224:231], v240\n"
    "s_nop 0\n"
    "ds_read_b128 a[232:235], v252 offset:12096\n"
    "v_smfmac_f32_16x16x64_bf16 v[4:7], a[236:239], a[224:231], v240 abid:1\n"
    "ds_read_b128 a[224:227], v250 offset:27824\n"
    "s_waitcnt lgkmcnt(10)\n"
    "v_smfmac_f32_16x16x64_bf16 v[8:11], a[248:251], a[240:247], v240\n"
    "ds_read_b128 a[228:231], v250 offset:28144\n"
    "v_smfmac_f32_16x16x64_bf16 v[12:15], a[252:255], a[240:247], v240 abid:1\n"
    "s_waitcnt lgkmcnt(9)\n"
    "ds_read_b128 a[236:239], v252 offset:12432\n"
    "v_smfmac_f32_16x16x64_bf16 v[16:19], v[104:107], v[96:103], v240\n"
    "ds_read_b128 a[240:243], v250 offset:27904\n"
    "v_smfmac_f32_16x16x64_bf16 v[20:23], v[108:111], v[96:103], v240 abid:1\n"
    "s_waitcnt lgkmcnt(9)\n"
    "ds_read_b128 a[244:247], v250 offset:28224\n"
    "v_smfmac_f32_16x16x64_bf16 v[24:27], v[128:131], v[120:127], v240\n"
    "ds_read_b128 a[248:251], v252 offset:12768\n"
    "v_smfmac_f32_16x16x64_bf16 v[28:31], v[132:135], v[120:127], v240 abid:1\n"
    "s_waitcnt lgkmcnt(9)\n"
    "ds_read_b128 a[252:255], v252 offset:13104\n"
    "v_smfmac_f32_16x16x64_bf16 v[32:35], v[144:147], v[136:143], v240\n"
    "s_nop 0\n"
    "ds_read_b128 v[96:99], v250 offset:54688\n"
    "v_smfmac_f32_16x16x64_bf16 v[36:39], v[148:151], v[136:143], v240 abid:1\n"
    "s_waitcnt lgkmcnt(9)\n"
    "ds_read_b128 v[100:103], v250 offset:55008\n"
    "v_smfmac_f32_16x16x64_bf16 v[40:43], v[160:163], v[152:159], v240\n"
    "ds_read_b128 v[104:107], v252 offset:21504\n"
    "v_smfmac_f32_16x16x64_bf16 v[44:47], v[164:167], v[152:159], v240 abid:1\n"
    "ds_read_b128 v[108:111], v252 offset:21840\n"
    "ds_read_b128 v[120:123], v250 offset:54768\n"
    "ds_read_b128 v[124:127], v250 offset:55088\n"
    "v_pk_mov_b32 v[224:225], v[0:1], v[0:1] op_sel:[0,1]\n"
    "v_pk_mov_b32 v[226:227], v[2:3], v[2:3] op_sel:[0,1]\n"
    "v_pk_mul_f32 v[228:229], v[0:1], v[0:1]\n"
    "v_pk_mul_f32 v[230:231], v[2:3], v[2:3]\n"
    "v_pk_add_f32 v[224:225], v[224:225], v[4:5]\n"
    "v_pk_add_f32 v[226:227], v[226:227], v[6:7]\n"
    "v_pk_fma_f32 v[228:229], v[4:5], v[4:5], v[228:229]\n"
    "v_pk_fma_f32 v[230:231], v[6:7], v[6:7], v[230:231]\n"
    "v_pk_add_f32 v[224:225], v[224:225], v[8:9]\n"
    "v_pk_add_f32 v[226:227], v[226:227], v[10:11]\n"
    "v_pk_fma_f32 v[228:229], v[8:9], v[8:9], v[228:229]\n"
    "v_pk_fma_f32 v[230:231], v[10:11], v[10:11], v[230:231]\n"
    "v_pk_add_f32 v[224:225], v[224:225], v[12:13]\n"
    "v_pk_add_f32 v[226:227], v[226:227], v[14:15]\n"
    "v_pk_fma_f32 v[228:229], v[12:13], v[12:13], v[228:229]\n"
    "v_pk_fma_f32 v[230:231], v[14:15], v[14:15], v[230:231]\n"
    "v_pk_add_f32 v[224:225], v[224:225], v[16:17]\n"
    "v_pk_add_f32 v[226:227], v[226:227], v[18:19]\n"
    "v_pk_fma_f32 v[228:229], v[16:17], v[16:17], v[228:229]\n"
    "v_pk_fma_f32 v[230:231], v[18:19], v[18:19], v[230:231]\n"
    "v_pk_add_f32 v[224:225], v[224:225], v[20:21]\n"
    "v_pk_add_f32 v[226:227], v[226:227], v[22:23]\n"
    "v_pk_fma_f32 v[228:229], v[20:21], v[20:21], v[228:229]\n"
    "v_pk_fma_f32 v[230:231], v[22:23], v[22:23], v[230:231]\n"
    "v_pk_add_f32 v[224:225], v[224:225], v[24:25]\n"
    "v_pk_add_f32 v[226:227], v[226:227], v[26:27]\n"
    "v_pk_fma_f32 v[228:229], v[24:25], v[24:25], v[228:229]\n"
    "v_pk_fma_f32 v[230:231], v[26:27], v[26:27], v[230:231]\n"
    "v_pk_add_f32 v[224:225], v[224:225], v[28:29]\n"
    "v_pk_add_f32 v[226:227], v[226:227], v[30:31]\n"
    "v_pk_fma_f32 v[228:229], v[28:29], v[28:29], v[228:229]\n"
    "v_pk_fma_f32 v[230:231], v[30:31], v[30:31], v[230:231]\n"
    "v_pk_add_f32 v[224:225], v[224:225], v[32:33]\n"
    "v_pk_add_f32 v[226:227], v[226:227], v[34:35]\n"
    "v_pk_fma_f32 v[228:229], v[32:33], v[32:33], v[228:229]\n"
    "v_pk_fma_f32 v[230:231], v[34:35], v[34:35], v[230:231]\n"
    "v_pk_add_f32 v[224:225], v[224:225], v[36:37]\n"
    "v_pk_add_f32 v[226:227], v[226:227], v[38:39]\n"
    "v_pk_fma_f32 v[228:229], v[36:37], v[36:37], v[228:229]\n"
    "v_pk_fma_f32 v[230:231], v[38:39], v[38:39], v[230:231]\n"
    "v_pk_add_f32 v[224:225], v[224:225], v[40:41]\n"
    "v_pk_add_f32 v[226:227], v[226:227], v[42:43]\n"
    "v_pk_fma_f32 v[228:229], v[40:41], v[40:41], v[228:229]\n"
    "v_pk_fma_f32 v[230:231], v[42:43], v[42:43], v[230:231]\n"
    "v_pk_add_f32 v[224:225], v[224:225], v[44:45]\n"
    "v_pk_add_f32 v[226:227], v[226:227], v[46:47]\n"
    "v_pk_fma_f32 v[228:229], v[44:45], v[44:45], v[228:229]\n"
    "v_pk_fma_f32 v[230:231], v[46:47], v[46:47], v[230:231]\n"
    "s_waitcnt lgkmcnt(0)\n"
    "v_smfmac_f32_16x16x64_bf16 v[48:51], a[232:235], a[224:231], v240\n"
    "s_nop 0\n"
    "ds_read_b128 v[128:131], v252 offset:22176\n"
    "v_smfmac_f32_16x16x64_bf16 v[52:55], a[236:239], a[224:231], v240 abid:1\n"
    "ds_read_b128 v[132:135], v252 offset:22512\n"
    "v_smfmac_f32_16x16x64_bf16 v[56:59], a[248:251], a[240:247], v240\n"
    "ds_read_b128 v[136:139], v250 offset:54848\n"
    "v_smfmac_f32_16x16x64_bf16 v[60:63], a[252:255], a[240:247], v240 abid:1\n"
    "ds_read_b128 v[140:143], v250 offset:55168\n"
    "v_smfmac_f32_16x16x64_bf16 v[64:67], v[104:107], v[96:103], v240\n"
    "ds_read_b128 v[144:147], v252 offset:22848\n"
    "v_smfmac_f32_16x16x64_bf16 v[68:71], v[108:111], v[96:103], v240 abid:1\n"
    "ds_read_b128 v[148:151], v252 offset:23184\n"
    "ds_read_b128 v[152:155], v250 offset:54928\n"
    "v_accvgpr_write_b32 a248, v236\n"
    "ds_read_b128 v[156:159], v250 offset:55248\n"
    "v_pk_add_f32 v[224:225], v[224:225], v[48:49]\n"
    "v_pk_add_f32 v[226:227], v[226:227], v[50:51]\n"
    "v_pk_fma_f32 v[228:229], v[48:49], v[48:49], v[228:229]\n"
    "v_pk_fma_f32 v[230:231], v[50:51], v[50:51], v[230:231]\n"
    "v_pk_add_f32 v[224:225], v[224:225], v[52:53]\n"
    "s_waitcnt vmcnt(5)\n"
    "s_barrier\n"
    "ds_read_b64_tr_b16 a[176:177], v220 offset:0\n"
    "ds_read_b64_tr_b16 a[178:179], v220 offset:3072\n"
    "v_pk_add_f32 v[226:227], v[226:227], v[54:55]\n"
    "ds_read_b64_tr_b16 a[180:181], v220 offset:8\n"
    "ds_read_b64_tr_b16 a[182:183], v220 offset:3080\n"
    "ds_read_b64_tr_b16 a[184:185], v220 offset:64\n"
    "v_pk_fma_f32 v[228:229], v[52:53], v[52:53], v[228:229]\n"
    "ds_read_b64_tr_b16 a[186:187], v220 offset:3136\n"
    "ds_read_b64_tr_b16 a[188:189], v220 offset:72\n"
    "ds_read_b64_tr_b16 a[190:191], v220 offset:3144\n"
    "v_pk_fma_f32 v[230:231], v[54:55], v[54:55], v[230:231]\n"
    "ds_read_b64_tr_b16 v[188:189], v218 offset:128\n"
    "ds_read_b64_tr_b16 v[190:191], v218 offset:3200\n"
    "ds_read_b64_tr_b16 a[192:193], v220 offset:6144\n"
    "v_pk_add_f32 v[224:225], v[224:225], v[56:57]\n"
    "ds_read_b64_tr_b16 a[194:195], v220 offset:9216\n"
    "ds_read_b64_tr_b16 a[196:197], v220 offset:6152\n"
    "ds_read_b64_tr_b16 a[198:199], v220 offset:9224\n"
    "v_pk_add_f32 v[226:227], v[226:227], v[58:59]\n"
    "ds_read_b64_tr_b16 a[200:201], v220 offset:6208\n"
    "ds_read_b64_tr_b16 a[202:203], v220 offset:9280\n"
    "ds_read_b64_tr_b16 a[204:205], v220 offset:6216\n"
    "v_pk_fma_f32 v[228:229], v[56:57], v[56:57], v[228:229]\n"
    "ds_read_b64_tr_b16 a[206:207], v220 offset:9288\n"
    "ds_read_b64_tr_b16 v[192:193], v218 offset:6272\n"
    "ds_read_b64_tr_b16 v[194:195], v218 offset:9344\n"
    "v_pk_fma_f32 v[230:231], v[58:59], v[58:59], v[230:231]\n"
    "ds_read_b64_tr_b16 a[208:209], v220 offset:12288\n"
    "ds_read_b64_tr_b16 a[210:211], v220 offset:15360\n"
    "ds_read_b64_tr_b16 a[212:213], v220 offset:12296\n"
    "v_pk_add_f32 v[224:225], v[224:225], v[60:61]\n"
    "ds_read_b64_tr_b16 a[214:215], v220 offset:15368\n"
    "ds_read_b64_tr_b16 a[216:217], v220 offset:12352\n"
    "ds_read_b64_tr_b16 a[218:219], v220 offset:15424\n"
    "v_pk_add_f32 v[226:227], v[226:227], v[62:63]\n"
    "ds_read_b64_tr_b16 a[220:221], v220 offset:12360\n"
    "ds_read_b64_tr_b16 a[222:223], v220 offset:15432\n"
    "ds_read_b64_tr_b16 v[196:197], v218 offset:12416\n"
    "v_pk_fma_f32 v[228:229], v[60:61], v[60:61], v[228:229]\n"
    "ds_read_b64_tr_b16 v[198:199], v218 offset:15488\n"
    "v_pk_fma_f32 v[230:231], v[62:63], v[62:63], v[230:231]\n"
    "v_pk_add_f32 v[224:225], v[224:225], v[64:65]\n"
    "v_pk_add_f32 v[226:227], v[226:227], v[66:67]\n"
    "v_pk_fma_f32 v[228:229], v[64:65], v[64:65], v[228:229]\n"
    "v_pk_fma_f32 v[230:231], v[66:67], v[66:67], v[230:231]\n"
    "v_pk_add_f32 v[224:225], v[224:225], v[68:69]\n"
    "v_pk_add_f32 v[226:227], v[226:227], v[70:71]\n"
    "v_pk_fma_f32 v[228:229], v[68:69], v[68:69], v[228:229]\n"
    "v_pk_fma_f32 v[230:231], v[70:71], v[70:71], v[230:231]\n"
    "s_waitcnt lgkmcnt(10)\n"
    "v_smfmac_f32_16x16x64_bf16 v[72:75], v[128:131], v[120:127], v240\n"
    "s_nop 0\n"
    "ds_read_b128 v[160:163], v252 offset:23520\n"
    "v_smfmac_f32_16x16x64_bf16 v[76:79], v[132:135], v[120:127], v240 abid:1\n"
    "ds_read_b128 v[164:167], v252 offset:23856\n"
    "v_smfmac_f32_16x16x64_bf16 v[80:83], v[144:147], v[136:143], v240\n"
    "v_accvgpr_write_b32 a249, v237\n"
    "v_smfmac_f32_16x16x64_bf16 v[84:87], v[148:151], v[136:143], v240 abid:1\n"
    "v_accvgpr_write_b32 a250, v238\n"
    "v_accvgpr_write_b32 a251, v239\n"
    "v_pk_add_f32 v[224:225], v[224:225], v[72:73]\n"
    "v_pk_add_f32 v[226:227], v[226:227], v[74:75]\n"
    "v_pk_fma_f32 v[228:229], v[72:73], v[72:73], v[228:229]\n"
    "v_pk_fma_f32 v[230:231], v[74:75], v[74:75], v[230:231]\n"
    "v_pk_add_f32 v[224:225], v[224:225], v[76:77]\n"
    "v_pk_add_f32 v[226:227], v[226:227], v[78:79]\n"
    "v_pk_fma_f32 v[228:229], v[76:77], v[76:77], v[228:229]\n"
    "v_pk_fma_f32 v[230:231], v[78:79], v[78:79], v[230:231]\n"
    "v_pk_add_f32 v[224:225], v[224:225], v[80:81]\n"
    "v_pk_add_f32 v[226:227], v[226:227], v[82:83]\n"
    "v_pk_fma_f32 v[228:229], v[80:81], v[80:81], v[228:229]\n"
    "v_pk_fma_f32 v[230:231], v[82:83], v[82:83], v[230:231]\n"
    "v_pk_add_f32 v[224:225], v[224:225], v[84:85]\n"
    "v_pk_add_f32 v[226:227], v[226:227], v[86:87]\n"
    "v_pk_fma_f32 v[228:229], v[84:85], v[84:85], v[228:229]\n"
    "v_pk_fma_f32 v[230:231], v[86:87], v[86:87], v[230:231]\n"
    "s_waitcnt lgkmcnt(1)\n"
    "v_smfmac_f32_16x16x64_bf16 v[88:91], v[160:163], v[152:159], v240\n"
    "s_waitcnt lgkmcnt(0)\n"
    "v_smfmac_f32_16x16x64_bf16 v[92:95], v[164:167], v[152:159], v240 abid:1\n"
    "s_nop 0\n"
    "s_nop 5\n"
    "v_pk_add_f32 v[224:225], v[224:225], v[88:89]\n"
    "v_pk_add_f32 v[226:227], v[226:227], v[90:91]\n"
    "v_pk_fma_f32 v[228:229], v[88:89], v[88:89], v[228:229]\n"
    "v_pk_fma_f32 v[230:231], v[90:91], v[90:91], v[230:231]\n"
    "v_pk_add_f32 v[224:225], v[224:225], v[92:93]\n"
    "v_pk_add_f32 v[226:227], v[226:227], v[94:95]\n"
    "v_pk_fma_f32 v[228:229], v[92:93], v[92:93], v[228:229]\n"
    "v_pk_fma_f32 v[230:231], v[94:95], v[94:95], v[230:231]\n"
    "ds_write_b128 v254, v[224:227]\n"
    "ds_write_b128 v254, v[228:231] offset:1024\n"
    "v_add_u32_e64 v254, s56, v242\n"
    "s_waitcnt lgkmcnt(0)\n"
    "s_barrier\n"
    "ds_read_b128 v[128:131], v254 offset:0\n"
    "ds_read_b128 v[132:135], v254 offset:6752\n"
    "ds_read_b128 v[136:139], v254 offset:13504\n"
    "ds_read_b128 v[140:143], v254 offset:20256\n"
    "ds_read_b128 v[144:147], v254 offset:1024\n"
    "ds_read_b128 v[148:151], v254 offset:7776\n"
    "ds_read_b128 v[152:155], v254 offset:14528\n"
    "ds_read_b128 v[156:159], v254 offset:21280\n"
    "ds_write_b128 v234, v[112:115] offset:0\n"
    "ds_write_b128 v234, v[116:119] offset:16\n"
    "s_waitcnt lgkmcnt(8)\n"
    "v_pk_add_f32 v[128:129], v[128:129], v[132:133]\n"
    "v_pk_add_f32 v[130:131], v[130:131], v[134:135]\n"
    "ds_write_b128 v207, v[168:171] offset:0\n"
    "s_waitcnt lgkmcnt(6)\n"
    "v_pk_add_f32 v[136:137], v[136:137], v[140:141]\n"
    "v_pk_add_f32 v[138:139], v[138:139], v[142:143]\n"
    "s_waitcnt lgkmcnt(5)\n"
    "v_pk_add_f32 v[144:145], v[144:145], v[148:149]\n"
    "v_pk_add_f32 v[146:147], v[146:147], v[150:151]\n"
    "v_pk_add_f32 v[128:129], v[128:129], v[136:137]\n"
    "v_pk_add_f32 v[130:131], v[130:131], v[138:139]\n"
    "v_mul_f32_e32 v128, 0x3c2aaaab, v128\n"
    "v_mul_f32_e32 v129, 0x3c2aaaab, v129\n"
    "v_mul_f32_e32 v130, 0x3c2aaaab, v130\n"
    "s_waitcnt lgkmcnt(3)\n"
    "v_pk_add_f32 v[152:153], v[152:153], v[156:157]\n"
    "v_pk_add_f32 v[154:155], v[154:155], v[158:159]\n"
    "v_pk_add_f32 v[144:145], v[144:145], v[152:153]\n"
    "v_pk_add_f32 v[146:147], v[146:147], v[154:155]\n"
    "v_mul_f32_e32 v131, 0x3c2aaaab, v131\n"
    "v_mul_f32_e32 v144, 0x3c2aaaab, v144\n"
    "v_mul_f32_e32 v145, 0x3c2aaaab, v145\n"
    "v_mul_f32_e32 v146, 0x3c2aaaab, v146\n"
    "v_mul_f32_e32 v147, 0x3c2aaaab, v147\n"
    "v_fma_f32 v144, -v128, v128, v144\n"
    "v_fma_f32 v145, -v129, v129, v145\n"
    "v_fma_f32 v146, -v130, v130, v146\n"
    "v_fma_f32 v147, -v131, v131, v147\n"
    "v_max_f32_e64 v144, 0, v144\n"
    "v_max_f32_e64 v145, 0, v145\n"
    "v_max_f32_e64 v146, 0, v146\n"
    "v_max_f32_e64 v147, 0, v147\n"
    "v_add_f32_e32 v144, 0x3727c5ac, v144\n"
    "v_add_f32_e32 v145, 0x3727c5ac, v145\n"
    "v_add_f32_e32 v146, 0x3727c5ac, v146\n"
    "v_add_f32_e32 v147, 0x3727c5ac, v147\n"
    "v_rsq_f32_e64 v144, v144\n"
    "v_rsq_f32_e64 v145, v145\n"
    "v_rsq_f32_e64 v146, v146\n"
    "v_rsq_f32_e64 v147, v147\n"
    "v_mul_f32_e64 v224, s50, v144\n"
    "v_mul_f32_e64 v225, s50, v145\n"
    "v_mul_f32_e64 v226, s50, v146\n"
    "v_mul_f32_e64 v227, s50, v147\n"
    "v_mul_f32_e64 v228, v224, -v128\n"
    "v_mul_f32_e64 v229, v225, -v129\n"
    "v_mul_f32_e64 v230, v226, -v130\n"
    "v_mul_f32_e64 v231, v227, -v131\n"
    "ds_write_b128 v207, v[172:175] offset:4096\n"
    "v_pk_fma_f32 v[64:65], v[64:65], v[224:225], v[228:229]\n"
    "v_pk_fma_f32 v[66:67], v[66:67], v[226:227], v[230:231]\n"
    "v_pk_fma_f32 v[68:69], v[68:69], v[224:225], v[228:229]\n"
    "v_pk_fma_f32 v[70:71], v[70:71], v[226:227], v[230:231]\n"
    "v_pk_fma_f32 v[72:73], v[72:73], v[224:225], v[228:229]\n"
    "v_pk_fma_f32 v[74:75], v[74:75], v[226:227], v[230:231]\n"
    "ds_write_b128 v207, v[176:179] offset:8192\n"
    "v_pk_fma_f32 v[76:77], v[76:77], v[224:225], v[228:229]\n"
    "v_pk_fma_f32 v[78:79], v[78:79], v[226:227], v[230:231]\n"
    "v_pk_fma_f32 v[80:81], v[80:81], v[224:225], v[228:229]\n"
    "v_pk_fma_f32 v[82:83], v[82:83], v[226:227], v[230:231]\n"
    "v_pk_fma_f32 v[84:85], v[84:85], v[224:225], v[228:229]\n"
    "v_pk_fma_f32 v[86:87], v[86:87], v[226:227], v[230:231]\n"
    "ds_write_b128 v207, v[188:191] offset:12288\n"
    "v_pk_fma_f32 v[88:89], v[88:89], v[224:225], v[228:229]\n"
    "v_pk_fma_f32 v[90:91], v[90:91], v[226:227], v[230:231]\n"
    "v_pk_fma_f32 v[92:93], v[92:93], v[224:225], v[228:229]\n"
    "v_pk_fma_f32 v[94:95], v[94:95], v[226:227], v[230:231]\n"
    "v_cvt_pk_bf16_f32 v96, v64, v68\n"
    "v_cvt_pk_bf16_f32 v100, v65, v69\n"
    "ds_write_b128 v207, v[192:195] offset:16384\n"
    "v_cvt_pk_bf16_f32 v104, v66, v70\n"
    "v_cvt_pk_bf16_f32 v108, v67, v71\n"
    "v_cvt_pk_bf16_f32 v97, v72, v76\n"
    "v_cvt_pk_bf16_f32 v101, v73, v77\n"
    "v_cvt_pk_bf16_f32 v105, v74, v78\n"
    "v_cvt_pk_bf16_f32 v109, v75, v79\n"
    "ds_write_b128 v207, v[196:199] offset:20480\n"
    "v_cvt_pk_bf16_f32 v98, v80, v84\n"
    "v_cvt_pk_bf16_f32 v102, v81, v85\n"
    "v_cvt_pk_bf16_f32 v106, v82, v86\n"
    "v_cvt_pk_bf16_f32 v110, v83, v87\n"
    "v_cvt_pk_bf16_f32 v99, v88, v92\n"
    "v_cvt_pk_bf16_f32 v103, v89, v93\n"
    "ds_write_b128 v207, v[180:183] offset:32768\n"
    "v_cvt_pk_bf16_f32 v107, v90, v94\n"
    "v_cvt_pk_bf16_f32 v111, v91, v95\n"
    "v_pk_fma_f32 v[0:1], v[0:1], v[224:225], v[228:229]\n"
    "v_pk_fma_f32 v[2:3], v[2:3], v[226:227], v[230:231]\n"
    "v_pk_fma_f32 v[4:5], v[4:5], v[224:225], v[228:229]\n"
    "v_pk_fma_f32 v[6:7], v[6:7], v[226:227], v[230:231]\n"
    "ds_write_b128 v253, v[96:99] offset:26688\n"
    "v_pk_fma_f32 v[8:9], v[8:9], v[224:225], v[228:229]\n"
    "v_pk_fma_f32 v[10:11], v[10:11], v[226:227], v[230:231]\n"
    "v_pk_fma_f32 v[12:13], v[12:13], v[224:225], v[228:229]\n"
    "v_pk_fma_f32 v[14:15], v[14:15], v[226:227], v[230:231]\n"
    "v_pk_fma_f32 v[16:17], v[16:17], v[224:225], v[228:229]\n"
    "v_pk_fma_f32 v[18:19], v[18:19], v[226:227], v[230:231]\n"
    "ds_write_b128 v253, v[100:103] offset:29760\n"
    "v_pk_fma_f32 v[20:21], v[20:21], v[224:225], v[228:229]\n"
    "v_pk_fma_f32 v[22:23], v[22:23], v[226:227], v[230:231]\n"
    "v_pk_fma_f32 v[24:25], v[24:25], v[224:225], v[228:229]\n"
    "v_pk_fma_f32 v[26:27], v[26:27], v[226:227], v[230:231]\n"
    "v_pk_fma_f32 v[28:29], v[28:29], v[224:225], v[228:229]\n"
    "v_pk_fma_f32 v[30:31], v[30:31], v[226:227], v[230:231]\n"
    "v_cvt_pk_bf16_f32 v80, v0, v4\n"
    "v_cvt_pk_bf16_f32 v84, v1, v5\n"
    "v_cvt_pk_bf16_f32 v88, v2, v6\n"
    "v_cvt_pk_bf16_f32 v92, v3, v7\n"
    "v_cvt_pk_bf16_f32 v81, v8, v12\n"
    "v_cvt_pk_bf16_f32 v85, v9, v13\n"
    "v_cvt_pk_bf16_f32 v89, v10, v14\n"
    "v_cvt_pk_bf16_f32 v93, v11, v15\n"
    "v_cvt_pk_bf16_f32 v82, v16, v20\n"
    "v_cvt_pk_bf16_f32 v86, v17, v21\n"
    "v_cvt_pk_bf16_f32 v90, v18, v22\n"
    "v_cvt_pk_bf16_f32 v94, v19, v23\n"
    "v_cvt_pk_bf16_f32 v83, v24, v28\n"
    "ds_write_b128 v253, v[80:83] offset:24640\n"
    "v_cvt_pk_bf16_f32 v87, v25, v29\n"
    "v_cvt_pk_bf16_f32 v91, v26, v30\n"
    "v_cvt_pk_bf16_f32 v95, v27, v31\n"
    "v_pk_fma_f32 v[32:33], v[32:33], v[224:225], v[228:229]\n"
    "v_pk_fma_f32 v[34:35], v[34:35], v[226:227], v[230:231]\n"
    "v_pk_fma_f32 v[36:37], v[36:37], v[224:225], v[228:229]\n"
    "ds_write_b128 v253, v[84:87] offset:27712\n"
    "v_pk_fma_f32 v[38:39], v[38:39], v[226:227], v[230:231]\n"
    "v_pk_fma_f32 v[40:41], v[40:41], v[224:225], v[228:229]\n"
    "v_pk_fma_f32 v[42:43], v[42:43], v[226:227], v[230:231]\n"
    "v_pk_fma_f32 v[44:45], v[44:45], v[224:225], v[228:229]\n"
    "v_pk_fma_f32 v[46:47], v[46:47], v[226:227], v[230:231]\n"
    "v_pk_fma_f32 v[48:49], v[48:49], v[224:225], v[228:229]\n"
    "v_pk_fma_f32 v[50:51], v[50:51], v[226:227], v[230:231]\n"
    "v_pk_fma_f32 v[52:53], v[52:53], v[224:225], v[228:229]\n"
    "v_pk_fma_f32 v[54:55], v[54:55], v[226:227], v[230:231]\n"
    "v_pk_fma_f32 v[56:57], v[56:57], v[224:225], v[228:229]\n"
    "v_pk_fma_f32 v[58:59], v[58:59], v[226:227], v[230:231]\n"
    "v_pk_fma_f32 v[60:61], v[60:61], v[224:225], v[228:229]\n"
    "v_pk_fma_f32 v[62:63], v[62:63], v[226:227], v[230:231]\n"
    "v_cvt_pk_bf16_f32 v64, v32, v36\n"
    "v_cvt_pk_bf16_f32 v68, v33, v37\n"
    "v_cvt_pk_bf16_f32 v72, v34, v38\n"
    "v_cvt_pk_bf16_f32 v76, v35, v39\n"
    "v_cvt_pk_bf16_f32 v65, v40, v44\n"
    "v_cvt_pk_bf16_f32 v69, v41, v45\n"
    "v_cvt_pk_bf16_f32 v73, v42, v46\n"
    "v_cvt_pk_bf16_f32 v77, v43, v47\n"
    "v_cvt_pk_bf16_f32 v66, v48, v52\n"
    "v_cvt_pk_bf16_f32 v70, v49, v53\n"
    "v_cvt_pk_bf16_f32 v74, v50, v54\n"
    "v_cvt_pk_bf16_f32 v78, v51, v55\n"
    "v_cvt_pk_bf16_f32 v67, v56, v60\n"
    "ds_write_b128 v253, v[64:67] offset:25664\n"
    "v_cvt_pk_bf16_f32 v71, v57, v61\n"
    "v_cvt_pk_bf16_f32 v75, v58, v62\n"
    "v_cvt_pk_bf16_f32 v79, v59, v63\n"
    "ds_write_b128 v253, v[68:71] offset:28736\n"
    "s_waitcnt lgkmcnt(0)\n"
    "s_barrier\n"
    "ds_read_b128 a[224:227], v242 offset:24640\n"
    "ds_read_b128 a[228:231], v242 offset:25664\n"
    "ds_read_b128 a[232:235], v242 offset:26688\n"
    "ds_read_b128 a[236:239], v242 offset:27712\n"
    "ds_read_b128 a[240:243], v242 offset:28736\n"
    "ds_read_b128 a[244:247], v242 offset:29760\n"
    "ds_read_b128 a[252:255], v244 offset:32768\n"
    "s_waitcnt lgkmcnt(6)\n"
    "v_mfma_f32_16x16x32_bf16 v[168:171], a[80:83], a[224:227], 0\n"
    "v_mfma_f32_16x16x32_bf16 v[172:175], a[84:87], a[224:227], 0\n"
    "s_waitcnt lgkmcnt(5)\n"
    "v_mfma_f32_16x16x32_bf16 v[168:171], a[128:131], a[228:231], v[168:171]\n"
    "s_waitcnt lgkmcnt(4)\n"
    "v_mfma_f32_16x16x32_bf16 v[168:171], a[48:51], a[232:235], v[168:171]\n"
    "s_waitcnt lgkmcnt(3)\n"
    "v_mfma_f32_16x16x32_bf16 v[172:175], a[132:135], a[228:231], v[172:175]\n"
    "v_mfma_f32_16x16x32_bf16 v[176:179], a[80:83], a[236:239], 0\n"
    "s_waitcnt lgkmcnt(1)\n"
    "v_mfma_f32_16x16x32_bf16 v[176:179], a[128:131], a[240:243], v[176:179]\n"
    "s_waitcnt lgkmcnt(0)\n"
    "v_mfma_f32_16x16x32_bf16 v[172:175], a[252:255], a[232:235], v[172:175]\n"
    "v_mfma_f32_16x16x32_bf16 v[176:179], a[48:51], a[244:247], v[176:179]\n"
    "v_mfma_f32_16x16x32_bf16 v[180:183], a[84:87], a[236:239], 0\n"
    "v_mfma_f32_16x16x32_bf16 v[180:183], a[132:135], a[240:243], v[180:183]\n"
    "v_mfma_f32_16x16x32_bf16 v[180:183], a[252:255], a[244:247], v[180:183]\n"
    "v_readfirstlane_b32 s66, v232\n"
    "s_nop 1\n"
    "s_cmp_lg_u32 s66, 2\n"
    "s_cbranch_scc1 98f\n"
    "ds_read_b128 a[252:255], v244 offset:36864\n"
    "ds_write_b128 v253, v[72:75] offset:1024\n"
    "v_lshlrev_b32_sdwa v184, 1, v168 dst_sel:DWORD dst_unused:UNUSED_PAD src0_sel:DWORD src1_sel:WORD_1\n"
    "v_lshlrev_b32_sdwa v200, 1, v169 dst_sel:DWORD dst_unused:UNUSED_PAD src0_sel:DWORD src1_sel:WORD_1\n"
    "v_lshlrev_b32_sdwa v185, 1, v170 dst_sel:DWORD dst_unused:UNUSED_PAD src0_sel:DWORD src1_sel:WORD_1\n"
    "v_lshlrev_b32_sdwa v201, 1, v171 dst_sel:DWORD dst_unused:UNUSED_PAD src0_sel:DWORD src1_sel:WORD_1\n"
    "v_lshlrev_b32_sdwa v186, 1, v172 dst_sel:DWORD dst_unused:UNUSED_PAD src0_sel:DWORD src1_sel:WORD_1\n"
    "v_lshlrev_b32_sdwa v202, 1, v173 dst_sel:DWORD dst_unused:UNUSED_PAD src0_sel:DWORD src1_sel:WORD_1\n"
    "v_lshlrev_b32_sdwa v187, 1, v174 dst_sel:DWORD dst_unused:UNUSED_PAD src0_sel:DWORD src1_sel:WORD_1\n"
    "v_mfma_f32_16x16x32_bf16 v[168:171], a[88:91], a[224:227], 0\n"
    "ds_read_u16 v184, v184\n"
    "ds_read_u16_d16_hi v200, v200\n"
    "v_mfma_f32_16x16x32_bf16 v[168:171], a[136:139], a[228:231], v[168:171]\n"
    "ds_read_u16 v185, v185\n"
    "ds_read_u16_d16_hi v201, v201\n"
    "v_mfma_f32_16x16x32_bf16 v[168:171], a[52:55], a[232:235], v[168:171]\n"
    "v_lshlrev_b32_sdwa v203, 1, v175 dst_sel:DWORD dst_unused:UNUSED_PAD src0_sel:DWORD src1_sel:WORD_1\n"
    "v_lshlrev_b32_sdwa v188, 1, v176 dst_sel:DWORD dst_unused:UNUSED_PAD src0_sel:DWORD src1_sel:WORD_1\n"
    "v_mfma_f32_16x16x32_bf16 v[172:175], a[92:95], a[224:227], 0\n"
    "ds_read_u16 v186, v186\n"
    "v_lshlrev_b32_sdwa v204, 1, v177 dst_sel:DWORD dst_unused:UNUSED_PAD src0_sel:DWORD src1_sel:WORD_1\n"
    "v_mfma_f32_16x16x32_bf16 v[172:175], a[140:143], a[228:231], v[172:175]\n"
    "s_nop 0\n"
    "v_lshlrev_b32_sdwa v189, 1, v178 dst_sel:DWORD dst_unused:UNUSED_PAD src0_sel:DWORD src1_sel:WORD_1\n"
    "v_lshlrev_b32_sdwa v205, 1, v179 dst_sel:DWORD dst_unused:UNUSED_PAD src0_sel:DWORD src1_sel:WORD_1\n"
    "v_mfma_f32_16x16x32_bf16 v[176:179], a[88:91], a[236:239], 0\n"
    "s_waitcnt lgkmcnt(6)\n"
    "ds_read_u16_d16_hi v202, v202\n"
    "v_lshlrev_b32_sdwa v190, 1, v180 dst_sel:DWORD dst_unused:UNUSED_PAD src0_sel:DWORD src1_sel:WORD_1\n"
    "v_mfma_f32_16x16x32_bf16 v[176:179], a[136:139], a[240:243], v[176:179]\n"
    "v_lshlrev_b32_sdwa v206, 1, v181 dst_sel:DWORD dst_unused:UNUSED_PAD src0_sel:DWORD src1_sel:WORD_1\n"
    "v_lshlrev_b32_sdwa v191, 1, v182 dst_sel:DWORD dst_unused:UNUSED_PAD src0_sel:DWORD src1_sel:WORD_1\n"
    "v_mfma_f32_16x16x32_bf16 v[176:179], a[52:55], a[244:247], v[176:179]\n"
    "ds_read_u16 v187, v187\n"
    "v_lshlrev_b32_sdwa v207, 1, v183 dst_sel:DWORD dst_unused:UNUSED_PAD src0_sel:DWORD src1_sel:WORD_1\n"
    "v_cvt_pk_f16_f32 v192, v168, v169\n"
    "v_mfma_f32_16x16x32_bf16 v[172:175], a[252:255], a[232:235], v[172:175]\n"
    "ds_read_u16_d16_hi v203, v203\n"
    "v_lshlrev_b32_sdwa v193, 1, v170 dst_sel:DWORD dst_unused:UNUSED_PAD src0_sel:DWORD src1_sel:WORD_1\n"
    "v_lshlrev_b32_sdwa v249, 1, v171 dst_sel:DWORD dst_unused:UNUSED_PAD src0_sel:DWORD src1_sel:WORD_1\n"
    "v_pk_fma_f16 v232, v192, v192, s48\n"
    "v_cvt_pk_f16_f32 v196, v176, v177\n"
    "v_mfma_f32_16x16x32_bf16 v[180:183], a[92:95], a[236:239], 0\n"
    "ds_read_u16 v188, v188\n"
    "v_lshlrev_b32_sdwa v197, 1, v178 dst_sel:DWORD dst_unused:UNUSED_PAD src0_sel:DWORD src1_sel:WORD_1\n"
    "v_mfma_f32_16x16x32_bf16 v[180:183], a[140:143], a[240:243], v[180:183]\n"
    "v_lshlrev_b32_sdwa v194, 1, v172 dst_sel:DWORD dst_unused:UNUSED_PAD src0_sel:DWORD src1_sel:WORD_1\n"
    "v_lshlrev_b32_sdwa v255, 1, v173 dst_sel:DWORD dst_unused:UNUSED_PAD src0_sel:DWORD src1_sel:WORD_1\n"
    "v_mfma_f32_16x16x32_bf16 v[180:183], a[252:255], a[244:247], v[180:183]\n"
    "s_waitcnt lgkmcnt(7)\n"
    "ds_read_u16_d16_hi v204, v204\n"
    "v_or_b32_e32 v184, v184, v200\n"
    "v_lshlrev_b32_sdwa v195, 1, v174 dst_sel:DWORD dst_unused:UNUSED_PAD src0_sel:DWORD src1_sel:WORD_1\n"
    "v_lshlrev_b32_sdwa v160, 1, v175 dst_sel:DWORD dst_unused:UNUSED_PAD src0_sel:DWORD src1_sel:WORD_1\n"
    "v_lshlrev_b32_sdwa v161, 1, v179 dst_sel:DWORD dst_unused:UNUSED_PAD src0_sel:DWORD src1_sel:WORD_1\n"
    "v_pk_mul_f16 v232, v192, v232\n"
    "v_mov_b32_e64 v254, 0\n"
    "v_lshlrev_b32_sdwa v198, 1, v180 dst_sel:DWORD dst_unused:UNUSED_PAD src0_sel:DWORD src1_sel:WORD_1\n"
    "v_lshlrev_b32_sdwa v162, 1, v181 dst_sel:DWORD dst_unused:UNUSED_PAD src0_sel:DWORD src1_sel:WORD_1\n"
    "v_lshlrev_b32_sdwa v199, 1, v182 dst_sel:DWORD dst_unused:UNUSED_PAD src0_sel:DWORD src1_sel:WORD_1\n"
    "v_lshlrev_b32_sdwa v163, 1, v183 dst_sel:DWORD dst_unused:UNUSED_PAD src0_sel:DWORD src1_sel:WORD_1\n"
    "v_exp_f16_e32 v234, v232\n"
    "s_waitcnt lgkmcnt(6)\n"
    "v_or_b32_e64 v185, v185, v201\n"
    "v_mfma_f32_16x16x32_bf16 v[168:171], a[96:99], a[224:227], 0\n"
    "ds_write_b128 v253, v[76:79] offset:4096\n"
    "v_exp_f16_sdwa v234, v232 dst_sel:WORD_1 dst_unused:UNUSED_PRESERVE src0_sel:WORD_1\n"
    "ds_read_u16 v189, v189\n"
    "ds_write_b128 v253, v[88:91] offset:0\n"
    "s_waitcnt lgkmcnt(6)\n"
    "v_or_b32_e32 v186, v186, v202\n"
    "v_pk_fma_f16 v234, v234, s49, s49\n"
    "v_rcp_f16_e64 v232, v234\n"
    "v_mfma_f32_16x16x32_bf16 v[168:171], a[144:147], a[228:231], v[168:171]\n"
    "s_waitcnt lgkmcnt(5)\n"
    "v_or_b32_e32 v187, v187, v203\n"
    "v_mfma_f32_16x16x32_bf16 v[168:171], a[56:59], a[232:235], v[168:171]\n"
    "ds_read_u16_d16_hi v205, v205\n"
    "ds_write_b128 v253, v[92:95] offset:3072\n"
    "v_rcp_f16_sdwa v232, v234 dst_sel:WORD_1 dst_unused:UNUSED_PRESERVE src0_sel:WORD_1\n"
    "v_mfma_f32_16x16x32_bf16 v[172:175], a[100:103], a[224:227], 0\n"
    "s_waitcnt lgkmcnt(4)\n"
    "v_or_b32_e32 v188, v188, v204\n"
    "v_fma_mix_f32 v234, v192, v232, 0 op_sel_hi:[1,1,0]\n"
    "v_mfma_f32_16x16x32_bf16 v[172:175], a[148:151], a[228:231], v[172:175]\n"
    "v_lshlrev_b32_sdwa v200, 1, v169 dst_sel:DWORD dst_unused:UNUSED_PAD src0_sel:DWORD src1_sel:WORD_1\n"
    "v_lshlrev_b32_sdwa v201, 1, v171 dst_sel:DWORD dst_unused:UNUSED_PAD src0_sel:DWORD src1_sel:WORD_1\n"
    "v_mfma_f32_16x16x32_bf16 v[172:175], a[60:63], a[232:235], v[172:175]\n"
    "ds_read_u16 v190, v190\n"
    "ds_write_b128 v253, v[104:107] offset:2048\n"
    "v_fma_mix_f32 v232, v192, v232, 0 op_sel:[1,1,0] op_sel_hi:[1,1,0]\n"
    "v_cvt_pk_bf16_f32 v192, v234, v232\n"
    "v_pk_fma_f16 v232, v196, v196, s48\n"
    "v_pk_mul_f16 v232, v196, v232\n"
    "v_exp_f16_e64 v234, v232\n"
    "v_mfma_f32_16x16x32_bf16 v[176:179], a[96:99], a[236:239], 0\n"
    "v_lshlrev_b32_sdwa v202, 1, v173 dst_sel:DWORD dst_unused:UNUSED_PAD src0_sel:DWORD src1_sel:WORD_1\n"
    "v_lshlrev_b32_sdwa v203, 1, v175 dst_sel:DWORD dst_unused:UNUSED_PAD src0_sel:DWORD src1_sel:WORD_1\n"
    "v_mfma_f32_16x16x32_bf16 v[176:179], a[144:147], a[240:243], v[176:179]\n"
    "ds_read_u16_d16_hi v206, v206\n"
    "ds_read_u16 v191, v191\n"
    "v_mfma_f32_16x16x32_bf16 v[176:179], a[56:59], a[244:247], v[176:179]\n"
    "s_waitcnt lgkmcnt(4)\n"
    "ds_read_u16_d16_hi v207, v207\n"
    "ds_read_u16 v193, v193\n"
    "v_or_b32_e32 v189, v189, v205\n"
    "v_exp_f16_sdwa v234, v232 dst_sel:WORD_1 dst_unused:UNUSED_PRESERVE src0_sel:WORD_1\n"
    "v_mfma_f32_16x16x32_bf16 v[180:183], a[100:103], a[236:239], 0\n"
    "v_pk_fma_f16 v234, v234, s49, s49\n"
    "v_mfma_f32_16x16x32_bf16 v[180:183], a[148:151], a[240:243], v[180:183]\n"
    "v_lshlrev_b32_sdwa v204, 1, v177 dst_sel:DWORD dst_unused:UNUSED_PAD src0_sel:DWORD src1_sel:WORD_1\n"
    "v_lshlrev_b32_sdwa v205, 1, v179 dst_sel:DWORD dst_unused:UNUSED_PAD src0_sel:DWORD src1_sel:WORD_1\n"
    "v_mfma_f32_16x16x32_bf16 v[180:183], a[60:63], a[244:247], v[180:183]\n"
    "ds_write_b128 v253, v[108:111] offset:5120\n"
    "v_rcp_f16_e64 v232, v234\n"
    "ds_read_u16_d16_hi v249, v249\n"
    "ds_read_u16 v194, v194\n"
    "v_rcp_f16_sdwa v232, v234 dst_sel:WORD_1 dst_unused:UNUSED_PRESERVE src0_sel:WORD_1\n"
    "ds_read_u16_d16_hi v255, v255\n"
    "ds_read_u16 v195, v195\n"
    "s_waitcnt lgkmcnt(8)\n"
    "v_or_b32_e32 v190, v190, v206\n"
    "v_lshlrev_b32_sdwa v206, 1, v181 dst_sel:DWORD dst_unused:UNUSED_PAD src0_sel:DWORD src1_sel:WORD_1\n"
    "v_fma_mix_f32 v234, v196, v232, 0 op_sel_hi:[1,1,0]\n"
    "v_mfma_f32_16x16x32_bf16 v[96:99], a[0:3], v[184:187], 0\n"
    "s_barrier\n"
    "v_mfma_f32_16x16x32_bf16 v[100:103], a[4:7], v[184:187], 0\n"
    "s_waitcnt lgkmcnt(4)\n"
    "ds_read_b128 v[236:239], v244 offset:0\n"
    "ds_read_u16_d16_hi v160, v160\n"
    "v_mfma_f32_16x16x32_bf16 v[112:115], a[8:11], v[184:187], 0\n"
    "ds_read_b128 v[164:167], v244 offset:2048\n"
    "ds_read_u16 v197, v197\n"
    "v_or_b32_e64 v191, v191, v207\n"
    "v_mfma_f32_16x16x32_bf16 v[116:119], a[12:15], v[184:187], 0\n"
    "ds_read_u16_d16_hi v161, v161\n"
    "v_lshlrev_b32_sdwa v207, 1, v183 dst_sel:DWORD dst_unused:UNUSED_PAD src0_sel:DWORD src1_sel:WORD_1\n"
    "v_fma_mix_f32 v232, v196, v232, 0 op_sel:[1,1,0] op_sel_hi:[1,1,0]\n"
    "v_cvt_pk_bf16_f32 v196, v234, v232\n"
    "v_mfma_f32_16x16x32_bf16 v[104:107], a[0:3], v[188:191], 0\n"
    "s_waitcnt lgkmcnt(8)\n"
    "ds_read_u16 v198, v198\n"
    "v_or_b32_e32 v193, v193, v249\n"
    "v_mfma_f32_16x16x32_bf16 v[108:111], a[4:7], v[188:191], 0\n"
    "v_mfma_f32_16x16x32_bf16 v[120:123], a[8:11], v[188:191], 0\n"
    "ds_read_u16_d16_hi v162, v162\n"
    "v_mfma_f32_16x16x32_bf16 v[124:127], a[12:15], v[188:191], 0\n"
    "ds_read_u16 v199, v199\n"
    "s_waitcnt lgkmcnt(9)\n"
    "v_or_b32_e32 v194, v194, v255\n"
    "ds_read_u16_d16_hi v163, v163\n"
    "ds_read_u16_d16_hi v200, v200\n"
    "s_waitcnt lgkmcnt(9)\n"
    "v_mfma_f32_16x16x32_bf16 v[128:131], v[236:239], v[184:187], 0\n"
    "s_nop 0\n"
    "v_mfma_f32_16x16x32_bf16 v[136:139], v[236:239], v[188:191], 0\n"
    "ds_read_u16_d16_hi v201, v201\n"
    "ds_read_u16_d16_hi v202, v202\n"
    "ds_read_b128 v[236:239], v244 offset:4096\n"
    "ds_read_u16_d16_hi v203, v203\n"
    "s_waitcnt lgkmcnt(11)\n"
    "v_mfma_f32_16x16x32_bf16 v[132:135], v[164:167], v[184:187], 0\n"
    "v_or_b32_e32 v195, v195, v160\n"
    "v_lshlrev_b32_sdwa v184, 1, v168 dst_sel:DWORD dst_unused:UNUSED_PAD src0_sel:DWORD src1_sel:WORD_1\n"
    "v_mfma_f32_16x16x32_bf16 v[140:143], v[164:167], v[188:191], 0\n"
    "v_lshlrev_b32_sdwa v185, 1, v170 dst_sel:DWORD dst_unused:UNUSED_PAD src0_sel:DWORD src1_sel:WORD_1\n"
    "v_lshlrev_b32_sdwa v186, 1, v172 dst_sel:DWORD dst_unused:UNUSED_PAD src0_sel:DWORD src1_sel:WORD_1\n"
    "v_mfma_f32_16x16x32_bf16 v[168:171], a[104:107], a[224:227], 0\n"
    "v_lshlrev_b32_sdwa v187, 1, v174 dst_sel:DWORD dst_unused:UNUSED_PAD src0_sel:DWORD src1_sel:WORD_1\n"
    "v_lshlrev_b32_sdwa v188, 1, v176 dst_sel:DWORD dst_unused:UNUSED_PAD src0_sel:DWORD src1_sel:WORD_1\n"
    "v_mfma_f32_16x16x32_bf16 v[168:171], a[152:155], a[228:231], v[168:171]\n"
    "v_lshlrev_b32_sdwa v189, 1, v178 dst_sel:DWORD dst_unused:UNUSED_PAD src0_sel:DWORD src1_sel:WORD_1\n"
    "v_lshlrev_b32_sdwa v190, 1, v180 dst_sel:DWORD dst_unused:UNUSED_PAD src0_sel:DWORD src1_sel:WORD_1\n"
    "v_mfma_f32_16x16x32_bf16 v[168:171], a[64:67], a[232:235], v[168:171]\n"
    "s_waitcnt lgkmcnt(9)\n"
    "v_or_b32_e32 v197, v197, v161\n"
    "v_lshlrev_b32_sdwa v191, 1, v182 dst_sel:DWORD dst_unused:UNUSED_PAD src0_sel:DWORD src1_sel:WORD_1\n"
    "v_mfma_f32_16x16x32_bf16 v[172:175], a[108:111], a[224:227], 0\n"
    "v_mfma_f32_16x16x32_bf16 v[172:175], a[156:159], a[228:231], v[172:175]\n"
    "v_mfma_f32_16x16x32_bf16 v[172:175], a[68:71], a[232:235], v[172:175]\n"
    "ds_read_u16 v184, v184\n"
    "buffer_load_dwordx4 v[0:3], v246, s[40:43], s60 offen offset:0\n"
    "buffer_load_dwordx4 v[32:35], v246, s[40:43], s60 offen offset:64\n"
    "buffer_load_dwordx4 v[64:67], v246, s[40:43], s60 offen offset:128\n"
    "v_mfma_f32_16x16x32_bf16 v[176:179], a[104:107], a[236:239], 0\n"
    "s_waitcnt lgkmcnt(8)\n"
    "ds_read_u16 v185, v185\n"
    "v_or_b32_e32 v198, v198, v162\n"
    "v_mfma_f32_16x16x32_bf16 v[176:179], a[152:155], a[240:243], v[176:179]\n"
    "v_lshlrev_b32_sdwa v249, 1, v171 dst_sel:DWORD dst_unused:UNUSED_PAD src0_sel:DWORD src1_sel:WORD_1\n"
    "v_lshlrev_b32_sdwa v255, 1, v173 dst_sel:DWORD dst_unused:UNUSED_PAD src0_sel:DWORD src1_sel:WORD_1\n"
    "v_mfma_f32_16x16x32_bf16 v[176:179], a[64:67], a[244:247], v[176:179]\n"
    "ds_read_u16 v186, v186\n"
    "v_lshlrev_b32_sdwa v160, 1, v175 dst_sel:DWORD dst_unused:UNUSED_PAD src0_sel:DWORD src1_sel:WORD_1\n"
    "v_mfma_f32_16x16x32_bf16 v[180:183], a[108:111], a[236:239], 0\n"
    "s_waitcnt lgkmcnt(8)\n"
    "v_or_b32_e32 v199, v199, v163\n"
    "v_mfma_f32_16x16x32_bf16 v[180:183], a[156:159], a[240:243], v[180:183]\n"
    "ds_read_u16 v187, v187\n"
    "v_mfma_f32_16x16x32_bf16 v[180:183], a[68:71], a[244:247], v[180:183]\n"
    "v_lshlrev_b32_sdwa v161, 1, v179 dst_sel:DWORD dst_unused:UNUSED_PAD src0_sel:DWORD src1_sel:WORD_1\n"
    "ds_read_b128 v[164:167], v244 offset:6144\n"
    "v_mfma_f32_16x16x32_bf16 v[96:99], a[16:19], v[192:195], v[96:99]\n"
    "ds_read_u16 v188, v188\n"
    "v_mfma_f32_16x16x32_bf16 v[100:103], a[20:23], v[192:195], v[100:103]\n"
    "s_waitcnt lgkmcnt(7)\n"
    "v_mfma_f32_16x16x32_bf16 v[112:115], a[24:27], v[192:195], v[112:115]\n"
    "s_nop 0\n"
    "ds_read_u16_d16_hi v204, v204\n"
    "buffer_load_dwordx4 v[20:23], v247, s[40:43], s60 offen offset:0\n"
    "buffer_load_dwordx4 v[52:55], v247, s[40:43], s60 offen offset:64\n"
    "buffer_load_dwordx4 v[84:87], v247, s[40:43], s60 offen offset:128\n"
    "v_mfma_f32_16x16x32_bf16 v[116:119], a[28:31], v[192:195], v[116:119]\n"
    "ds_read_u16 v189, v189\n"
    "v_lshlrev_b32_sdwa v162, 1, v181 dst_sel:DWORD dst_unused:UNUSED_PAD src0_sel:DWORD src1_sel:WORD_1\n"
    "v_lshlrev_b32_sdwa v163, 1, v183 dst_sel:DWORD dst_unused:UNUSED_PAD src0_sel:DWORD src1_sel:WORD_1\n"
    "v_mfma_f32_16x16x32_bf16 v[128:131], v[236:239], v[192:195], v[128:131]\n"
    "s_nop 0\n"
    "v_mfma_f32_16x16x32_bf16 v[104:107], a[16:19], v[196:199], v[104:107]\n"
    "s_waitcnt lgkmcnt(7)\n"
    "ds_read_u16_d16_hi v205, v205\n"
    "v_or_b32_e64 v184, v184, v200\n"
    "v_mfma_f32_16x16x32_bf16 v[108:111], a[20:23], v[196:199], v[108:111]\n"
    "ds_read_u16 v190, v190\n"
    "v_mfma_f32_16x16x32_bf16 v[120:123], a[24:27], v[196:199], v[120:123]\n"
    "s_waitcnt lgkmcnt(8)\n"
    "v_or_b32_e32 v185, v185, v201\n"
    "v_mfma_f32_16x16x32_bf16 v[124:127], a[28:31], v[196:199], v[124:127]\n"
    "ds_read_u16_d16_hi v206, v206\n"
    "v_mfma_f32_16x16x32_bf16 v[136:139], v[236:239], v[196:199], v[136:139]\n"
    "s_waitcnt lgkmcnt(8)\n"
    "v_or_b32_e32 v186, v186, v202\n"
    "ds_read_u16 v191, v191\n"
    "s_waitcnt lgkmcnt(7)\n"
    "v_mfma_f32_16x16x32_bf16 v[132:135], v[164:167], v[192:195], v[132:135]\n"
    "s_nop 0\n"
    "v_cvt_pk_f16_f32 v192, v168, v169\n"
    "v_lshlrev_b32_sdwa v193, 1, v170 dst_sel:DWORD dst_unused:UNUSED_PAD src0_sel:DWORD src1_sel:WORD_1\n"
    "v_mfma_f32_16x16x32_bf16 v[140:143], v[164:167], v[196:199], v[140:143]\n"
    "ds_read_u16_d16_hi v207, v207\n"
    "buffer_load_dwordx4 v[4:7], v246, s[40:43], s61 offen offset:0\n"
    "buffer_load_dwordx4 v[36:39], v246, s[40:43], s61 offen offset:64\n"
    "buffer_load_dwordx4 v[68:71], v246, s[40:43], s61 offen offset:128\n"
    "v_mfma_f32_16x16x32_bf16 v[168:171], a[112:115], a[224:227], 0\n"
    "ds_read_u16 v193, v193\n"
    "v_lshlrev_b32_sdwa v194, 1, v172 dst_sel:DWORD dst_unused:UNUSED_PAD src0_sel:DWORD src1_sel:WORD_1\n"
    "v_mfma_f32_16x16x32_bf16 v[168:171], a[160:163], a[228:231], v[168:171]\n"
    "s_nop 0\n"
    "v_lshlrev_b32_sdwa v195, 1, v174 dst_sel:DWORD dst_unused:UNUSED_PAD src0_sel:DWORD src1_sel:WORD_1\n"
    "v_cvt_pk_f16_f32 v196, v176, v177\n"
    "v_mfma_f32_16x16x32_bf16 v[168:171], a[72:75], a[232:235], v[168:171]\n"
    "s_waitcnt lgkmcnt(7)\n"
    "ds_read_b128 a[252:255], v244 offset:34816\n"
    "v_lshlrev_b32_sdwa v197, 1, v178 dst_sel:DWORD dst_unused:UNUSED_PAD src0_sel:DWORD src1_sel:WORD_1\n"
    "v_mfma_f32_16x16x32_bf16 v[172:175], a[116:119], a[224:227], 0\n"
    "ds_read_u16_d16_hi v249, v249\n"
    "v_lshlrev_b32_sdwa v198, 1, v180 dst_sel:DWORD dst_unused:UNUSED_PAD src0_sel:DWORD src1_sel:WORD_1\n"
    "v_lshlrev_b32_sdwa v199, 1, v182 dst_sel:DWORD dst_unused:UNUSED_PAD src0_sel:DWORD src1_sel:WORD_1\n"
    "v_or_b32_e64 v187, v187, v203\n"
    "v_mfma_f32_16x16x32_bf16 v[172:175], a[164:167], a[228:231], v[172:175]\n"
    "ds_read_u16 v194, v194\n"
    "v_or_b32_e64 v188, v188, v204\n"
    "v_pk_fma_f16 v232, v192, v192, s48\n"
    "v_lshlrev_b32_sdwa v200, 1, v169 dst_sel:DWORD dst_unused:UNUSED_PAD src0_sel:DWORD src1_sel:WORD_1\n"
    "v_mfma_f32_16x16x32_bf16 v[176:179], a[112:115], a[236:239], 0\n"
    "s_waitcnt lgkmcnt(8)\n"
    "ds_read_u16_d16_hi v255, v255\n"
    "v_or_b32_e32 v189, v189, v205\n"
    "v_mfma_f32_16x16x32_bf16 v[176:179], a[160:163], a[240:243], v[176:179]\n"
    "v_pk_mul_f16 v232, v192, v232\n"
    "v_lshlrev_b32_sdwa v201, 1, v171 dst_sel:DWORD dst_unused:UNUSED_PAD src0_sel:DWORD src1_sel:WORD_1\n"
    "v_mfma_f32_16x16x32_bf16 v[176:179], a[72:75], a[244:247], v[176:179]\n"
    "ds_read_u16 v195, v195\n"
    "v_exp_f16_e64 v234, v232\n"
    "v_mfma_f32_16x16x32_bf16 v[180:183], a[116:119], a[236:239], 0\n"
    "s_waitcnt lgkmcnt(8)\n"
    "v_or_b32_e32 v190, v190, v206\n"
    "v_mfma_f32_16x16x32_bf16 v[180:183], a[164:167], a[240:243], v[180:183]\n"
    "ds_read_b128 v[236:239], v244 offset:8192\n"
    "ds_read_u16_d16_hi v160, v160\n"
    "v_exp_f16_sdwa v234, v232 dst_sel:WORD_1 dst_unused:UNUSED_PRESERVE src0_sel:WORD_1\n"
    "v_lshlrev_b32_sdwa v204, 1, v177 dst_sel:DWORD dst_unused:UNUSED_PAD src0_sel:DWORD src1_sel:WORD_1\n"
    "v_pk_fma_f16 v234, v234, s49, s49\n"
    "v_mfma_f32_16x16x32_bf16 v[96:99], a[32:35], v[184:187], v[96:99]\n"
    "s_waitcnt lgkmcnt(8)\n"
    "buffer_load_dwordx4 v[24:27], v247, s[40:43], s61 offen offset:0\n"
    "buffer_load_dwordx4 v[56:59], v247, s[40:43], s61 offen offset:64\n"
    "buffer_load_dwordx4 v[88:91], v247, s[40:43], s61 offen offset:128\n"
    "ds_read_b128 v[164:167], v244 offset:10240\n"
    "v_mfma_f32_16x16x32_bf16 v[100:103], a[36:39], v[184:187], v[100:103]\n"
    "s_waitcnt lgkmcnt(7)\n"
    "ds_read_u16 v197, v197\n"
    "v_or_b32_e64 v191, v191, v207\n"
    "v_rcp_f16_e64 v232, v234\n"
    "v_mfma_f32_16x16x32_bf16 v[172:175], a[252:255], a[232:235], v[172:175]\n"
    "ds_read_u16_d16_hi v161, v161\n"
    "v_rcp_f16_sdwa v232, v234 dst_sel:WORD_1 dst_unused:UNUSED_PRESERVE src0_sel:WORD_1\n"
    "v_mfma_f32_16x16x32_bf16 v[180:183], a[252:255], a[244:247], v[180:183]\n"
    "s_waitcnt lgkmcnt(8)\n"
    "v_or_b32_e32 v193, v193, v249\n"
    "v_lshlrev_b32_sdwa v205, 1, v179 dst_sel:DWORD dst_unused:UNUSED_PAD src0_sel:DWORD src1_sel:WORD_1\n"
    "v_mfma_f32_16x16x32_bf16 v[112:115], a[40:43], v[184:187], v[112:115]\n"
    "ds_read_u16 v198, v198\n"
    "v_lshlrev_b32_sdwa v202, 1, v173 dst_sel:DWORD dst_unused:UNUSED_PAD src0_sel:DWORD src1_sel:WORD_1\n"
    "v_mfma_f32_16x16x32_bf16 v[116:119], a[44:47], v[184:187], v[116:119]\n"
    "ds_read_u16_d16_hi v162, v162\n"
    "v_lshlrev_b32_sdwa v203, 1, v175 dst_sel:DWORD dst_unused:UNUSED_PAD src0_sel:DWORD src1_sel:WORD_1\n"
    "v_lshlrev_b32_sdwa v206, 1, v181 dst_sel:DWORD dst_unused:UNUSED_PAD src0_sel:DWORD src1_sel:WORD_1\n"
    "v_lshlrev_b32_sdwa v207, 1, v183 dst_sel:DWORD dst_unused:UNUSED_PAD src0_sel:DWORD src1_sel:WORD_1\n"
    "v_fma_mix_f32 v234, v192, v232, 0 op_sel_hi:[1,1,0]\n"
    "v_mfma_f32_16x16x32_bf16 v[104:107], a[32:35], v[188:191], v[104:107]\n"
    "s_waitcnt lgkmcnt(8)\n"
    "ds_read_u16 v199, v199\n"
    "v_or_b32_e32 v194, v194, v255\n"
    "v_fma_mix_f32 v232, v192, v232, 0 op_sel:[1,1,0] op_sel_hi:[1,1,0]\n"
    "v_cvt_pk_bf16_f32 v192, v234, v232\n"
    "v_pk_fma_f16 v232, v196, v196, s48\n"
    "v_mfma_f32_16x16x32_bf16 v[108:111], a[36:39], v[188:191], v[108:111]\n"
    "s_nop 0\n"
    "s_waitcnt lgkmcnt(7)\n"
    "ds_read_u16_d16_hi v163, v163\n"
    "buffer_load_dwordx4 v[8:11], v246, s[40:43], s62 offen offset:0\n"
    "buffer_load_dwordx4 v[40:43], v246, s[40:43], s62 offen offset:64\n"
    "buffer_load_dwordx4 v[72:75], v246, s[40:43], s62 offen offset:128\n"
    "v_pk_mul_f16 v232, v196, v232\n"
    "v_mfma_f32_16x16x32_bf16 v[128:131], v[236:239], v[184:187], v[128:131]\n"
    "ds_read_b128 a[252:255], v244 offset:38912\n"
    "v_exp_f16_e32 v234, v232\n"
    "v_mfma_f32_16x16x32_bf16 v[120:123], a[40:43], v[188:191], v[120:123]\n"
    "s_waitcnt lgkmcnt(7)\n"
    "ds_read_u16_d16_hi v200, v200\n"
    "v_or_b32_e64 v195, v195, v160\n"
    "v_exp_f16_sdwa v234, v232 dst_sel:WORD_1 dst_unused:UNUSED_PRESERVE src0_sel:WORD_1\n"
    "v_mfma_f32_16x16x32_bf16 v[132:135], v[164:167], v[184:187], v[132:135]\n"
    "ds_read_u16_d16_hi v201, v201\n"
    "v_lshlrev_b32_sdwa v184, 1, v168 dst_sel:DWORD dst_unused:UNUSED_PAD src0_sel:DWORD src1_sel:WORD_1\n"
    "v_lshlrev_b32_sdwa v185, 1, v170 dst_sel:DWORD dst_unused:UNUSED_PAD src0_sel:DWORD src1_sel:WORD_1\n"
    "v_mfma_f32_16x16x32_bf16 v[124:127], a[44:47], v[188:191], v[124:127]\n"
    "v_lshlrev_b32_sdwa v186, 1, v172 dst_sel:DWORD dst_unused:UNUSED_PAD src0_sel:DWORD src1_sel:WORD_1\n"
    "v_lshlrev_b32_sdwa v187, 1, v174 dst_sel:DWORD dst_unused:UNUSED_PAD src0_sel:DWORD src1_sel:WORD_1\n"
    "v_mfma_f32_16x16x32_bf16 v[136:139], v[236:239], v[188:191], v[136:139]\n"
    "ds_read_u16 v184, v184\n"
    "v_pk_fma_f16 v234, v234, s49, s49\n"
    "v_mfma_f32_16x16x32_bf16 v[140:143], v[164:167], v[188:191], v[140:143]\n"
    "s_waitcnt lgkmcnt(8)\n"
    "v_or_b32_e32 v197, v197, v161\n"
    "v_lshlrev_b32_sdwa v188, 1, v176 dst_sel:DWORD dst_unused:UNUSED_PAD src0_sel:DWORD src1_sel:WORD_1\n"
    "v_mfma_f32_16x16x32_bf16 v[168:171], a[120:123], a[224:227], 0\n"
    "ds_read_u16 v185, v185\n"
    "v_lshlrev_b32_sdwa v189, 1, v178 dst_sel:DWORD dst_unused:UNUSED_PAD src0_sel:DWORD src1_sel:WORD_1\n"
    "v_mfma_f32_16x16x32_bf16 v[168:171], a[168:171], a[228:231], v[168:171]\n"
    "s_nop 0\n"
    "ds_read_u16 v186, v186\n"
    "v_lshlrev_b32_sdwa v190, 1, v180 dst_sel:DWORD dst_unused:UNUSED_PAD src0_sel:DWORD src1_sel:WORD_1\n"
    "v_mfma_f32_16x16x32_bf16 v[168:171], a[76:79], a[232:235], v[168:171]\n"
    "s_waitcnt lgkmcnt(8)\n"
    "buffer_load_dwordx4 v[12:15], v246, s[40:43], s63 offen offset:0\n"
    "buffer_load_dwordx4 v[44:47], v246, s[40:43], s63 offen offset:64\n"
    "buffer_load_dwordx4 v[76:79], v246, s[40:43], s63 offen offset:128\n"
    "ds_read_u16_d16_hi v202, v202\n"
    "v_or_b32_e64 v198, v198, v162\n"
    "v_lshlrev_b32_sdwa v191, 1, v182 dst_sel:DWORD dst_unused:UNUSED_PAD src0_sel:DWORD src1_sel:WORD_1\n"
    "v_mfma_f32_16x16x32_bf16 v[172:175], a[124:127], a[224:227], 0\n"
    "v_rcp_f16_e32 v232, v234\n"
    "v_mfma_f32_16x16x32_bf16 v[172:175], a[172:175], a[228:231], v[172:175]\n"
    "s_waitcnt lgkmcnt(6)\n"
    "ds_read_b128 v[236:239], v244 offset:12288\n"
    "v_or_b32_e64 v199, v199, v163\n"
    "v_mfma_f32_16x16x32_bf16 v[172:175], a[252:255], a[232:235], v[172:175]\n"
    "ds_read_u16 v187, v187\n"
    "v_rcp_f16_sdwa v232, v234 dst_sel:WORD_1 dst_unused:UNUSED_PRESERVE src0_sel:WORD_1\n"
    "v_mfma_f32_16x16x32_bf16 v[176:179], a[120:123], a[236:239], 0\n"
    "ds_read_u16_d16_hi v203, v203\n"
    "v_fma_mix_f32 v234, v196, v232, 0 op_sel_hi:[1,1,0]\n"
    "v_mfma_f32_16x16x32_bf16 v[176:179], a[168:171], a[240:243], v[176:179]\n"
    "v_fma_mix_f32 v232, v196, v232, 0 op_sel:[1,1,0] op_sel_hi:[1,1,0]\n"
    "v_lshlrev_b32_sdwa v249, 1, v171 dst_sel:DWORD dst_unused:UNUSED_PAD src0_sel:DWORD src1_sel:WORD_1\n"
    "v_mfma_f32_16x16x32_bf16 v[176:179], a[76:79], a[244:247], v[176:179]\n"
    "ds_read_b128 v[164:167], v244 offset:14336\n"
    "v_cvt_pk_bf16_f32 v196, v234, v232\n"
    "v_mfma_f32_16x16x32_bf16 v[180:183], a[124:127], a[236:239], 0\n"
    "ds_read_u16 v188, v188\n"
    "v_lshlrev_b32_sdwa v255, 1, v173 dst_sel:DWORD dst_unused:UNUSED_PAD src0_sel:DWORD src1_sel:WORD_1\n"
    "v_mfma_f32_16x16x32_bf16 v[180:183], a[172:175], a[240:243], v[180:183]\n"
    "s_nop 0\n"
    "v_lshlrev_b32_sdwa v160, 1, v175 dst_sel:DWORD dst_unused:UNUSED_PAD src0_sel:DWORD src1_sel:WORD_1\n"
    "v_mfma_f32_16x16x32_bf16 v[180:183], a[252:255], a[244:247], v[180:183]\n"
    "s_waitcnt lgkmcnt(8)\n"
    "buffer_load_dwordx4 v[16:19], v246, s[40:43], s52 offen offset:0\n"
    "buffer_load_dwordx4 v[48:51], v246, s[40:43], s52 offen offset:64\n"
    "buffer_load_dwordx4 v[80:83], v246, s[40:43], s52 offen offset:128\n"
    "ds_read_u16_d16_hi v204, v204\n"
    "v_mfma_f32_16x16x32_bf16 v[96:99], a[176:179], v[192:195], v[96:99]\n"
    "s_waitcnt lgkmcnt(8)\n"
    "ds_read_u16 v189, v189\n"
    "v_or_b32_e32 v184, v184, v200\n"
    "v_mfma_f32_16x16x32_bf16 v[100:103], a[180:183], v[192:195], v[100:103]\n"
    "v_or_b32_e64 v185, v185, v201\n"
    "v_lshlrev_b32_sdwa v161, 1, v179 dst_sel:DWORD dst_unused:UNUSED_PAD src0_sel:DWORD src1_sel:WORD_1\n"
    "v_mfma_f32_16x16x32_bf16 v[112:115], a[184:187], v[192:195], v[112:115]\n"
    "ds_read_u16_d16_hi v205, v205\n"
    "v_lshlrev_b32_sdwa v162, 1, v181 dst_sel:DWORD dst_unused:UNUSED_PAD src0_sel:DWORD src1_sel:WORD_1\n"
    "v_mfma_f32_16x16x32_bf16 v[116:119], a[188:191], v[192:195], v[116:119]\n"
    "s_waitcnt lgkmcnt(7)\n"
    "v_or_b32_e32 v186, v186, v202\n"
    "v_lshlrev_b32_sdwa v163, 1, v183 dst_sel:DWORD dst_unused:UNUSED_PAD src0_sel:DWORD src1_sel:WORD_1\n"
    "v_mfma_f32_16x16x32_bf16 v[128:131], v[236:239], v[192:195], v[128:131]\n"
    "ds_read_u16 v190, v190\n"
    "v_mfma_f32_16x16x32_bf16 v[104:107], a[176:179], v[196:199], v[104:107]\n"
    "ds_read_b128 a[224:227], v243 offset:24640\n"
    "v_mfma_f32_16x16x32_bf16 v[108:111], a[180:183], v[196:199], v[108:111]\n"
    "ds_read_u16_d16_hi v206, v206\n"
    "v_mfma_f32_16x16x32_bf16 v[120:123], a[184:187], v[196:199], v[120:123]\n"
    "s_nop 0\n"
    "v_mfma_f32_16x16x32_bf16 v[124:127], a[188:191], v[196:199], v[124:127]\n"
    "s_waitcnt lgkmcnt(7)\n"
    "ds_read_b128 a[228:231], v243 offset:25664\n"
    "v_or_b32_e64 v187, v187, v203\n"
    "v_mfma_f32_16x16x32_bf16 v[132:135], v[164:167], v[192:195], v[132:135]\n"
    "buffer_load_dwordx4 v[152:155], v245, s[40:43], s57 offen offset:64\n"
    "buffer_load_dwordx4 v[144:147], v245, s[40:43], s57 offen offset:0\n"
    "ds_read_b128 a[232:235], v243 offset:26688\n"
    "v_mfma_f32_16x16x32_bf16 v[136:139], v[236:239], v[196:199], v[136:139]\n"
    "ds_read_u16 v191, v191\n"
    "v_cvt_pk_f16_f32 v192, v168, v169\n"
    "v_lshlrev_b32_sdwa v193, 1, v170 dst_sel:DWORD dst_unused:UNUSED_PAD src0_sel:DWORD src1_sel:WORD_1\n"
    "v_mfma_f32_16x16x32_bf16 v[140:143], v[164:167], v[196:199], v[140:143]\n"
    "ds_read_b128 a[236:239], v243 offset:27712\n"
    "v_lshlrev_b32_sdwa v194, 1, v172 dst_sel:DWORD dst_unused:UNUSED_PAD src0_sel:DWORD src1_sel:WORD_1\n"
    "v_lshlrev_b32_sdwa v195, 1, v174 dst_sel:DWORD dst_unused:UNUSED_PAD src0_sel:DWORD src1_sel:WORD_1\n"
    "v_cvt_pk_f16_f32 v196, v176, v177\n"
    "v_lshlrev_b32_sdwa v197, 1, v178 dst_sel:DWORD dst_unused:UNUSED_PAD src0_sel:DWORD src1_sel:WORD_1\n"
    "v_lshlrev_b32_sdwa v198, 1, v180 dst_sel:DWORD dst_unused:UNUSED_PAD src0_sel:DWORD src1_sel:WORD_1\n"
    "v_lshlrev_b32_sdwa v199, 1, v182 dst_sel:DWORD dst_unused:UNUSED_PAD src0_sel:DWORD src1_sel:WORD_1\n"
    "s_waitcnt lgkmcnt(9)\n"
    "v_or_b32_e32 v188, v188, v204\n"
    "ds_read_u16_d16_hi v207, v207\n"
    "ds_read_b128 a[240:243], v243 offset:28736\n"
    "v_pk_fma_f16 v232, v192, v192, s48\n"
    "v_pk_mul_f16 v232, v192, v232\n"
    "v_exp_f16_e32 v234, v232\n"
    "s_waitcnt lgkmcnt(9)\n"
    "v_or_b32_e64 v189, v189, v205\n"
    "ds_read_b128 a[244:247], v243 offset:29760\n"
    "ds_read_u16 v193, v193\n"
    "v_exp_f16_sdwa v234, v232 dst_sel:WORD_1 dst_unused:UNUSED_PRESERVE src0_sel:WORD_1\n"
    "s_waitcnt lgkmcnt(9)\n"
    "v_mfma_f32_16x16x32_bf16 v[168:171], a[80:83], a[224:227], 0\n"
    "s_nop 0\n"
    "ds_read_b128 a[252:255], v244 offset:32768\n"
    "buffer_load_dwordx4 v[156:159], v245, s[40:43], s88 offen offset:64\n"
    "buffer_load_dwordx4 v[148:151], v245, s[40:43], s88 offen offset:0\n"
    "v_mfma_f32_16x16x32_bf16 v[172:175], a[84:87], a[224:227], 0\n"
    "s_waitcnt lgkmcnt(8)\n"
    "ds_read_u16_d16_hi v249, v249\n"
    "v_or_b32_e32 v190, v190, v206\n"
    "v_mfma_f32_16x16x32_bf16 v[172:175], a[132:135], a[228:231], v[172:175]\n"
    "ds_read_u16 v194, v194\n"
    "v_pk_fma_f16 v234, v234, s49, s49\n"
    "v_rcp_f16_e64 v232, v234\n"
    "v_mfma_f32_16x16x32_bf16 v[168:171], a[128:131], a[228:231], v[168:171]\n"
    "s_waitcnt lgkmcnt(9)\n"
    "v_rcp_f16_sdwa v232, v234 dst_sel:WORD_1 dst_unused:UNUSED_PRESERVE src0_sel:WORD_1\n"
    "v_mfma_f32_16x16x32_bf16 v[168:171], a[48:51], a[232:235], v[168:171]\n"
    "ds_read_u16_d16_hi v255, v255\n"
    "v_fma_mix_f32 v234, v192, v232, 0 op_sel_hi:[1,1,0]\n"
    "v_mfma_f32_16x16x32_bf16 v[96:99], a[192:195], v[184:187], v[96:99]\n"
    "s_waitcnt lgkmcnt(8)\n"
    "ds_read_u16 v195, v195\n"
    "v_fma_mix_f32 v232, v192, v232, 0 op_sel:[1,1,0] op_sel_hi:[1,1,0]\n"
    "v_cvt_pk_bf16_f32 v192, v234, v232\n"
    "v_pk_fma_f16 v232, v196, v196, s48\n"
    "v_lshlrev_b32_sdwa v200, 1, v169 dst_sel:DWORD dst_unused:UNUSED_PAD src0_sel:DWORD src1_sel:WORD_1\n"
    "v_mfma_f32_16x16x32_bf16 v[176:179], a[80:83], a[236:239], 0\n"
    "ds_read_b128 v[236:239], v244 offset:16384\n"
    "v_lshlrev_b32_sdwa v201, 1, v171 dst_sel:DWORD dst_unused:UNUSED_PAD src0_sel:DWORD src1_sel:WORD_1\n"
    "v_mfma_f32_16x16x32_bf16 v[180:183], a[84:87], a[236:239], 0\n"
    "s_waitcnt lgkmcnt(8)\n"
    "ds_read_u16_d16_hi v160, v160\n"
    "v_or_b32_e32 v191, v191, v207\n"
    "v_mfma_f32_16x16x32_bf16 v[180:183], a[132:135], a[240:243], v[180:183]\n"
    "v_pk_mul_f16 v232, v196, v232\n"
    "v_exp_f16_e64 v234, v232\n"
    "v_mfma_f32_16x16x32_bf16 v[176:179], a[128:131], a[240:243], v[176:179]\n"
    "s_waitcnt lgkmcnt(8)\n"
    "ds_read_b128 v[164:167], v244 offset:18432\n"
    "v_mfma_f32_16x16x32_bf16 v[176:179], a[48:51], a[244:247], v[176:179]\n"
    "s_waitcnt lgkmcnt(7)\n"
    "ds_read_u16 v197, v197\n"
    "v_exp_f16_sdwa v234, v232 dst_sel:WORD_1 dst_unused:UNUSED_PRESERVE src0_sel:WORD_1\n"
    "v_mfma_f32_16x16x32_bf16 v[172:175], a[252:255], a[232:235], v[172:175]\n"
    "ds_read_u16_d16_hi v161, v161\n"
    "v_pk_fma_f16 v234, v234, s49, s49\n"
    "v_mfma_f32_16x16x32_bf16 v[180:183], a[252:255], a[244:247], v[180:183]\n"
    "v_rcp_f16_e32 v232, v234\n"
    "v_mfma_f32_16x16x32_bf16 v[100:103], a[196:199], v[184:187], v[100:103]\n"
    "s_waitcnt lgkmcnt(8)\n"
    "ds_read_u16 v198, v198\n"
    "v_or_b32_e64 v193, v193, v249\n"
    "v_mfma_f32_16x16x32_bf16 v[112:115], a[200:203], v[184:187], v[112:115]\n"
    "v_lshlrev_b32_sdwa v202, 1, v173 dst_sel:DWORD dst_unused:UNUSED_PAD src0_sel:DWORD src1_sel:WORD_1\n"
    "v_lshlrev_b32_sdwa v203, 1, v175 dst_sel:DWORD dst_unused:UNUSED_PAD src0_sel:DWORD src1_sel:WORD_1\n"
    "v_mfma_f32_16x16x32_bf16 v[116:119], a[204:207], v[184:187], v[116:119]\n"
    "ds_read_u16_d16_hi v162, v162\n"
    "v_lshlrev_b32_sdwa v204, 1, v177 dst_sel:DWORD dst_unused:UNUSED_PAD src0_sel:DWORD src1_sel:WORD_1\n"
    "v_lshlrev_b32_sdwa v205, 1, v179 dst_sel:DWORD dst_unused:UNUSED_PAD src0_sel:DWORD src1_sel:WORD_1\n"
    "v_mfma_f32_16x16x32_bf16 v[104:107], a[192:195], v[188:191], v[104:107]\n"
    "s_waitcnt lgkmcnt(8)\n"
    "ds_read_u16 v199, v199\n"
    "v_or_b32_e32 v194, v194, v255\n"
    "v_lshlrev_b32_sdwa v206, 1, v181 dst_sel:DWORD dst_unused:UNUSED_PAD src0_sel:DWORD src1_sel:WORD_1\n"
    "v_lshlrev_b32_sdwa v207, 1, v183 dst_sel:DWORD dst_unused:UNUSED_PAD src0_sel:DWORD src1_sel:WORD_1\n"
    "v_mfma_f32_16x16x32_bf16 v[108:111], a[196:199], v[188:191], v[108:111]\n"
    "s_waitcnt lgkmcnt(7)\n"
    "v_mfma_f32_16x16x32_bf16 v[128:131], v[236:239], v[184:187], v[128:131]\n"
    "s_nop 0\n"
    "ds_read_u16_d16_hi v163, v163\n"
    "v_mfma_f32_16x16x32_bf16 v[120:123], a[200:203], v[188:191], v[120:123]\n"
    "ds_read_b128 a[252:255], v244 offset:36864\n"
    "ds_read_u16_d16_hi v200, v200\n"
    "s_waitcnt lgkmcnt(8)\n"
    "v_or_b32_e32 v195, v195, v160\n"
    "s_branch 2f\n"
    ".p2align 6\n"
    "1:\n"
    ".Lreplk_loop:\n"
    "v_lshlrev_b32_sdwa v184, 1, v168 dst_sel:DWORD dst_unused:UNUSED_PAD src0_sel:DWORD src1_sel:WORD_1\n"
    "v_lshlrev_b32_sdwa v200, 1, v169 dst_sel:DWORD dst_unused:UNUSED_PAD src0_sel:DWORD src1_sel:WORD_1\n"
    "v_lshlrev_b32_sdwa v185, 1, v170 dst_sel:DWORD dst_unused:UNUSED_PAD src0_sel:DWORD src1_sel:WORD_1\n"
    "v_lshlrev_b32_sdwa v201, 1, v171 dst_sel:DWORD dst_unused:UNUSED_PAD src0_sel:DWORD src1_sel:WORD_1\n"
    "v_mfma_f32_16x16x32_bf16 v[140:143], v[164:167], v[188:191], v[140:143]\n"
    "s_waitcnt vmcnt(2) lgkmcnt(3)\n"
    "ds_read_u16 v184, v184\n"
    "ds_read_b128 a[252:255], v244 offset:36864\n"
    "v_mfma_f32_16x16x32_bf16 v[168:171], a[88:91], a[224:227], 0\n"
    "ds_read_u16_d16_hi v200, v200\n"
    "ds_read_u16 v185, v185\n"
    "v_mfma_f32_16x16x32_bf16 v[168:171], a[136:139], a[228:231], v[168:171]\n"
    "s_waitcnt lgkmcnt(5)\n"
    "v_lshlrev_b32_sdwa v186, 1, v172 dst_sel:DWORD dst_unused:UNUSED_PAD src0_sel:DWORD src1_sel:WORD_1\n"
    "v_lshlrev_b32_sdwa v202, 1, v173 dst_sel:DWORD dst_unused:UNUSED_PAD src0_sel:DWORD src1_sel:WORD_1\n"
    "v_mfma_f32_16x16x32_bf16 v[168:171], a[52:55], a[232:235], v[168:171]\n"
    "ds_read_u16_d16_hi v201, v201\n"
    "buffer_load_dwordx4 v[0:3], v246, s[40:43], s60 offen offset:0\n"
    "buffer_load_dwordx4 v[32:35], v246, s[40:43], s60 offen offset:64\n"
    "buffer_load_dwordx4 v[64:67], v246, s[40:43], s60 offen offset:128\n"
    "v_smfmac_f32_32x32x32_bf16 v[128:143], a[248:251], v[144:151], v241\n"
    "ds_read_u16 v186, v186\n"
    "v_lshlrev_b32_sdwa v187, 1, v174 dst_sel:DWORD dst_unused:UNUSED_PAD src0_sel:DWORD src1_sel:WORD_1\n"
    "v_lshlrev_b32_sdwa v203, 1, v175 dst_sel:DWORD dst_unused:UNUSED_PAD src0_sel:DWORD src1_sel:WORD_1\n"
    "v_lshlrev_b32_sdwa v188, 1, v176 dst_sel:DWORD dst_unused:UNUSED_PAD src0_sel:DWORD src1_sel:WORD_1\n"
    "v_lshlrev_b32_sdwa v204, 1, v177 dst_sel:DWORD dst_unused:UNUSED_PAD src0_sel:DWORD src1_sel:WORD_1\n"
    "v_mfma_f32_16x16x32_bf16 v[172:175], a[92:95], a[224:227], 0\n"
    "ds_read_u16_d16_hi v202, v202\n"
    "v_lshlrev_b32_sdwa v189, 1, v178 dst_sel:DWORD dst_unused:UNUSED_PAD src0_sel:DWORD src1_sel:WORD_1\n"
    "v_mfma_f32_16x16x32_bf16 v[172:175], a[140:143], a[228:231], v[172:175]\n"
    "ds_read_b128 v[236:239], v244 offset:20480\n"
    "v_lshlrev_b32_sdwa v205, 1, v179 dst_sel:DWORD dst_unused:UNUSED_PAD src0_sel:DWORD src1_sel:WORD_1\n"
    "v_mfma_f32_16x16x32_bf16 v[176:179], a[88:91], a[236:239], 0\n"
    "ds_read_u16 v187, v187\n"
    "v_lshlrev_b32_sdwa v190, 1, v180 dst_sel:DWORD dst_unused:UNUSED_PAD src0_sel:DWORD src1_sel:WORD_1\n"
    "v_mfma_f32_16x16x32_bf16 v[176:179], a[136:139], a[240:243], v[176:179]\n"
    "s_nop 0\n"
    "v_lshlrev_b32_sdwa v206, 1, v181 dst_sel:DWORD dst_unused:UNUSED_PAD src0_sel:DWORD src1_sel:WORD_1\n"
    "v_lshlrev_b32_sdwa v191, 1, v182 dst_sel:DWORD dst_unused:UNUSED_PAD src0_sel:DWORD src1_sel:WORD_1\n"
    "v_mfma_f32_16x16x32_bf16 v[176:179], a[52:55], a[244:247], v[176:179]\n"
    "s_waitcnt lgkmcnt(7)\n"
    "ds_read_u16_d16_hi v203, v203\n"
    "v_lshlrev_b32_sdwa v207, 1, v183 dst_sel:DWORD dst_unused:UNUSED_PAD src0_sel:DWORD src1_sel:WORD_1\n"
    "v_pk_fma_f16 v234, v234, s49, s49\n"
    "v_or_b32_e64 v198, v198, v162\n"
    "v_mfma_f32_16x16x32_bf16 v[172:175], a[252:255], a[232:235], v[172:175]\n"
    "ds_read_u16 v188, v188\n"
    "v_or_b32_e64 v199, v199, v163\n"
    "v_rcp_f16_e64 v232, v234\n"
    "v_lshlrev_b32_sdwa v249, 1, v171 dst_sel:DWORD dst_unused:UNUSED_PAD src0_sel:DWORD src1_sel:WORD_1\n"
    "v_mfma_f32_16x16x32_bf16 v[180:183], a[92:95], a[236:239], 0\n"
    "s_waitcnt lgkmcnt(8)\n"
    "v_rcp_f16_sdwa v232, v234 dst_sel:WORD_1 dst_unused:UNUSED_PRESERVE src0_sel:WORD_1\n"
    "v_mfma_f32_16x16x32_bf16 v[180:183], a[140:143], a[240:243], v[180:183]\n"
    "s_nop 0\n"
    "v_fma_mix_f32 v234, v196, v232, 0 op_sel_hi:[1,1,0]\n"
    "v_fma_mix_f32 v232, v196, v232, 0 op_sel:[1,1,0] op_sel_hi:[1,1,0]\n"
    "v_mfma_f32_16x16x32_bf16 v[180:183], a[252:255], a[244:247], v[180:183]\n"
    "ds_write_b128 v253, v[216:219] offset:2048\n"
    "buffer_load_dwordx4 v[20:23], v247, s[40:43], s60 offen offset:0\n"
    "buffer_load_dwordx4 v[52:55], v247, s[40:43], s60 offen offset:64\n"
    "buffer_load_dwordx4 v[84:87], v247, s[40:43], s60 offen offset:128\n"
    "v_cvt_pk_bf16_f32 v196, v234, v232\n"
    "v_or_b32_e32 v184, v184, v200\n"
    "v_lshlrev_b32_sdwa v255, 1, v173 dst_sel:DWORD dst_unused:UNUSED_PAD src0_sel:DWORD src1_sel:WORD_1\n"
    "v_mfma_f32_16x16x32_bf16 v[96:99], a[208:211], v[192:195], v[96:99]\n"
    "s_waitcnt lgkmcnt(6)\n"
    "ds_read_b128 v[164:167], v244 offset:22528\n"
    "ds_write_b128 v253, v[220:223] offset:5120\n"
    "v_mfma_f32_16x16x32_bf16 v[100:103], a[212:215], v[192:195], v[100:103]\n"
    "s_waitcnt lgkmcnt(6)\n"
    "v_or_b32_e32 v185, v185, v201\n"
    "v_or_b32_e64 v186, v186, v202\n"
    "v_mfma_f32_16x16x32_bf16 v[112:115], a[216:219], v[192:195], v[112:115]\n"
    "v_lshlrev_b32_sdwa v160, 1, v175 dst_sel:DWORD dst_unused:UNUSED_PAD src0_sel:DWORD src1_sel:WORD_1\n"
    "v_lshlrev_b32_sdwa v161, 1, v179 dst_sel:DWORD dst_unused:UNUSED_PAD src0_sel:DWORD src1_sel:WORD_1\n"
    "v_mfma_f32_16x16x32_bf16 v[116:119], a[220:223], v[192:195], v[116:119]\n"
    "ds_read_u16_d16_hi v204, v204\n"
    "ds_read_u16 v189, v189\n"
    "v_mfma_f32_16x16x32_bf16 v[128:131], v[236:239], v[192:195], v[128:131]\n"
    "ds_read_u16_d16_hi v205, v205\n"
    "ds_read_u16 v190, v190\n"
    "v_mfma_f32_16x16x32_bf16 v[104:107], a[208:211], v[196:199], v[104:107]\n"
    "s_waitcnt lgkmcnt(8)\n"
    "v_lshlrev_b32_sdwa v162, 1, v181 dst_sel:DWORD dst_unused:UNUSED_PAD src0_sel:DWORD src1_sel:WORD_1\n"
    "v_or_b32_e32 v187, v187, v203\n"
    "v_mfma_f32_16x16x32_bf16 v[108:111], a[212:215], v[196:199], v[108:111]\n"
    "ds_read_u16_d16_hi v206, v206\n"
    "v_lshlrev_b32_sdwa v163, 1, v183 dst_sel:DWORD dst_unused:UNUSED_PAD src0_sel:DWORD src1_sel:WORD_1\n"
    "v_cvt_pk_bf16_f32 v208, v96, v97\n"
    "v_cvt_pk_bf16_f32 v209, v98, v99\n"
    "v_cvt_pk_bf16_f32 v210, v100, v101\n"
    "v_mfma_f32_16x16x32_bf16 v[120:123], a[216:219], v[196:199], v[120:123]\n"
    "s_nop 0\n"
    "s_waitcnt lgkmcnt(6)\n"
    "ds_read_u16 v191, v191\n"
    "v_cvt_pk_bf16_f32 v211, v102, v103\n"
    "v_cvt_pk_bf16_f32 v212, v112, v113\n"
    "v_cvt_pk_bf16_f32 v213, v114, v115\n"
    "v_cvt_pk_bf16_f32 v214, v116, v117\n"
    "v_mfma_f32_16x16x32_bf16 v[132:135], v[164:167], v[192:195], v[132:135]\n"
    "ds_read_u16_d16_hi v207, v207\n"
    "v_cvt_pk_f16_f32 v192, v168, v169\n"
    "v_lshlrev_b32_sdwa v193, 1, v170 dst_sel:DWORD dst_unused:UNUSED_PAD src0_sel:DWORD src1_sel:WORD_1\n"
    "v_lshlrev_b32_sdwa v194, 1, v172 dst_sel:DWORD dst_unused:UNUSED_PAD src0_sel:DWORD src1_sel:WORD_1\n"
    "v_lshlrev_b32_sdwa v195, 1, v174 dst_sel:DWORD dst_unused:UNUSED_PAD src0_sel:DWORD src1_sel:WORD_1\n"
    "v_mfma_f32_16x16x32_bf16 v[124:127], a[220:223], v[196:199], v[124:127]\n"
    "ds_read_u16 v193, v193\n"
    "v_cvt_pk_bf16_f32 v215, v118, v119\n"
    "v_cvt_pk_bf16_f32 v216, v128, v129\n"
    "v_cvt_pk_bf16_f32 v217, v130, v131\n"
    "v_cvt_pk_bf16_f32 v218, v132, v133\n"
    "v_mfma_f32_16x16x32_bf16 v[136:139], v[236:239], v[196:199], v[136:139]\n"
    "s_waitcnt lgkmcnt(7)\n"
    "ds_read_u16_d16_hi v249, v249\n"
    "v_or_b32_e32 v188, v188, v204\n"
    "v_cvt_pk_bf16_f32 v219, v134, v135\n"
    "v_cvt_pk_bf16_f32 v220, v104, v105\n"
    "v_cvt_pk_bf16_f32 v221, v106, v107\n"
    "v_mfma_f32_16x16x32_bf16 v[140:143], v[164:167], v[196:199], v[140:143]\n"
    "ds_read_u16 v194, v194\n"
    "buffer_load_dwordx4 v[4:7], v246, s[40:43], s61 offen offset:0\n"
    "buffer_load_dwordx4 v[36:39], v246, s[40:43], s61 offen offset:64\n"
    "buffer_load_dwordx4 v[68:71], v246, s[40:43], s61 offen offset:128\n"
    "v_cvt_pk_f16_f32 v196, v176, v177\n"
    "v_mfma_f32_16x16x32_bf16 v[168:171], a[96:99], a[224:227], 0\n"
    "s_waitcnt lgkmcnt(7)\n"
    "ds_read_u16_d16_hi v255, v255\n"
    "v_lshlrev_b32_sdwa v197, 1, v178 dst_sel:DWORD dst_unused:UNUSED_PAD src0_sel:DWORD src1_sel:WORD_1\n"
    "v_mfma_f32_16x16x32_bf16 v[168:171], a[144:147], a[228:231], v[168:171]\n"
    "s_nop 0\n"
    "v_lshlrev_b32_sdwa v198, 1, v180 dst_sel:DWORD dst_unused:UNUSED_PAD src0_sel:DWORD src1_sel:WORD_1\n"
    "v_lshlrev_b32_sdwa v199, 1, v182 dst_sel:DWORD dst_unused:UNUSED_PAD src0_sel:DWORD src1_sel:WORD_1\n"
    "v_mfma_f32_16x16x32_bf16 v[168:171], a[56:59], a[232:235], v[168:171]\n"
    "ds_read_u16 v195, v195\n"
    "v_or_b32_e64 v189, v189, v205\n"
    "v_cvt_pk_bf16_f32 v222, v108, v109\n"
    "v_cvt_pk_bf16_f32 v223, v110, v111\n"
    "v_mfma_f32_16x16x32_bf16 v[172:175], a[100:103], a[224:227], 0\n"
    "s_waitcnt lgkmcnt(7)\n"
    "ds_read_b128 v[236:239], v244 offset:0\n"
    "v_or_b32_e32 v190, v190, v206\n"
    "v_mfma_f32_16x16x32_bf16 v[172:175], a[148:151], a[228:231], v[172:175]\n"
    "ds_read_u16_d16_hi v160, v160\n"
    "v_cvt_pk_bf16_f32 v224, v120, v121\n"
    "v_mfma_f32_16x16x32_bf16 v[172:175], a[60:63], a[232:235], v[172:175]\n"
    "v_cvt_pk_bf16_f32 v225, v122, v123\n"
    "v_cvt_pk_bf16_f32 v226, v124, v125\n"
    "v_mfma_f32_16x16x32_bf16 v[176:179], a[96:99], a[236:239], 0\n"
    "ds_read_b128 v[164:167], v244 offset:2048\n"
    "v_cvt_pk_bf16_f32 v227, v126, v127\n"
    "v_mfma_f32_16x16x32_bf16 v[176:179], a[144:147], a[240:243], v[176:179]\n"
    "s_waitcnt lgkmcnt(8)\n"
    "ds_read_u16 v197, v197\n"
    "v_or_b32_e32 v191, v191, v207\n"
    "v_mfma_f32_16x16x32_bf16 v[176:179], a[56:59], a[244:247], v[176:179]\n"
    "v_pk_fma_f16 v232, v192, v192, s48\n"
    "v_cvt_pk_bf16_f32 v228, v136, v137\n"
    "v_mfma_f32_16x16x32_bf16 v[180:183], a[100:103], a[236:239], 0\n"
    "ds_read_u16_d16_hi v161, v161\n"
    "v_cvt_pk_bf16_f32 v229, v138, v139\n"
    "v_mfma_f32_16x16x32_bf16 v[180:183], a[148:151], a[240:243], v[180:183]\n"
    "s_waitcnt lgkmcnt(8)\n"
    "v_or_b32_e32 v193, v193, v249\n"
    "v_cvt_pk_bf16_f32 v230, v140, v141\n"
    "v_mfma_f32_16x16x32_bf16 v[180:183], a[60:63], a[244:247], v[180:183]\n"
    "ds_read_u16 v198, v198\n"
    "v_cvt_pk_bf16_f32 v231, v142, v143\n"
    "v_mfma_f32_16x16x32_bf16 v[96:99], a[0:3], v[184:187], 0\n"
    "ds_read_u16_d16_hi v162, v162\n"
    "v_pk_mul_f16 v232, v192, v232\n"
    "v_mfma_f32_16x16x32_bf16 v[100:103], a[4:7], v[184:187], 0\n"
    "v_exp_f16_e32 v234, v232\n"
    "v_mfma_f32_16x16x32_bf16 v[112:115], a[8:11], v[184:187], 0\n"
    "s_waitcnt lgkmcnt(8)\n"
    "ds_read_u16 v199, v199\n"
    "v_or_b32_e32 v194, v194, v255\n"
    "v_mfma_f32_16x16x32_bf16 v[116:119], a[12:15], v[184:187], 0\n"
    "s_waitcnt lgkmcnt(7)\n"
    "v_exp_f16_sdwa v234, v232 dst_sel:WORD_1 dst_unused:UNUSED_PRESERVE src0_sel:WORD_1\n"
    "v_mfma_f32_16x16x32_bf16 v[128:131], v[236:239], v[184:187], 0\n"
    "ds_read_u16_d16_hi v163, v163\n"
    "buffer_load_dwordx4 v[24:27], v247, s[40:43], s61 offen offset:0\n"
    "buffer_load_dwordx4 v[56:59], v247, s[40:43], s61 offen offset:64\n"
    "buffer_load_dwordx4 v[88:91], v247, s[40:43], s61 offen offset:128\n"
    "v_mfma_f32_16x16x32_bf16 v[104:107], a[0:3], v[188:191], 0\n"
    "v_pk_fma_f16 v234, v234, s49, s49\n"
    "v_lshlrev_b32_sdwa v200, 1, v169 dst_sel:DWORD dst_unused:UNUSED_PAD src0_sel:DWORD src1_sel:WORD_1\n"
    "v_mfma_f32_16x16x32_bf16 v[108:111], a[4:7], v[188:191], 0\n"
    "s_waitcnt lgkmcnt(6)\n"
    "ds_read_u16_d16_hi v200, v200\n"
    "v_or_b32_e32 v195, v195, v160\n"
    "v_mfma_f32_16x16x32_bf16 v[132:135], v[164:167], v[184:187], 0\n"
    "v_lshlrev_b32_sdwa v184, 1, v168 dst_sel:DWORD dst_unused:UNUSED_PAD src0_sel:DWORD src1_sel:WORD_1\n"
    "v_lshlrev_b32_sdwa v185, 1, v170 dst_sel:DWORD dst_unused:UNUSED_PAD src0_sel:DWORD src1_sel:WORD_1\n"
    "v_mfma_f32_16x16x32_bf16 v[120:123], a[8:11], v[188:191], 0\n"
    "ds_read_u16 v184, v184\n"
    "ds_read_u16 v185, v185\n"
    "v_mfma_f32_16x16x32_bf16 v[124:127], a[12:15], v[188:191], 0\n"
    "v_lshlrev_b32_sdwa v201, 1, v171 dst_sel:DWORD dst_unused:UNUSED_PAD src0_sel:DWORD src1_sel:WORD_1\n"
    "v_lshlrev_b32_sdwa v186, 1, v172 dst_sel:DWORD dst_unused:UNUSED_PAD src0_sel:DWORD src1_sel:WORD_1\n"
    "v_mfma_f32_16x16x32_bf16 v[136:139], v[236:239], v[188:191], 0\n"
    "s_waitcnt lgkmcnt(7)\n"
    "ds_read_u16_d16_hi v201, v201\n"
    "v_or_b32_e32 v197, v197, v161\n"
    "v_mfma_f32_16x16x32_bf16 v[140:143], v[164:167], v[188:191], 0\n"
    "v_lshlrev_b32_sdwa v202, 1, v173 dst_sel:DWORD dst_unused:UNUSED_PAD src0_sel:DWORD src1_sel:WORD_1\n"
    "v_lshlrev_b32_sdwa v187, 1, v174 dst_sel:DWORD dst_unused:UNUSED_PAD src0_sel:DWORD src1_sel:WORD_1\n"
    "v_mfma_f32_16x16x32_bf16 v[168:171], a[104:107], a[224:227], 0\n"
    "ds_read_u16 v186, v186\n"
    "v_lshlrev_b32_sdwa v203, 1, v175 dst_sel:DWORD dst_unused:UNUSED_PAD src0_sel:DWORD src1_sel:WORD_1\n"
    "v_mfma_f32_16x16x32_bf16 v[168:171], a[152:155], a[228:231], v[168:171]\n"
    "v_rcp_f16_e32 v232, v234\n"
    "v_mfma_f32_16x16x32_bf16 v[168:171], a[64:67], a[232:235], v[168:171]\n"
    "s_waitcnt lgkmcnt(7)\n"
    "ds_read_u16_d16_hi v202, v202\n"
    "v_or_b32_e64 v198, v198, v162\n"
    "v_rcp_f16_sdwa v232, v234 dst_sel:WORD_1 dst_unused:UNUSED_PRESERVE src0_sel:WORD_1\n"
    "v_mfma_f32_16x16x32_bf16 v[172:175], a[108:111], a[224:227], 0\n"
    "ds_read_b128 v[236:239], v244 offset:4096\n"
    "v_lshlrev_b32_sdwa v188, 1, v176 dst_sel:DWORD dst_unused:UNUSED_PAD src0_sel:DWORD src1_sel:WORD_1\n"
    "v_mfma_f32_16x16x32_bf16 v[172:175], a[156:159], a[228:231], v[172:175]\n"
    "ds_read_u16 v187, v187\n"
    "v_lshlrev_b32_sdwa v204, 1, v177 dst_sel:DWORD dst_unused:UNUSED_PAD src0_sel:DWORD src1_sel:WORD_1\n"
    "v_lshlrev_b32_sdwa v189, 1, v178 dst_sel:DWORD dst_unused:UNUSED_PAD src0_sel:DWORD src1_sel:WORD_1\n"
    "v_lshlrev_b32_sdwa v205, 1, v179 dst_sel:DWORD dst_unused:UNUSED_PAD src0_sel:DWORD src1_sel:WORD_1\n"
    "v_lshlrev_b32_sdwa v190, 1, v180 dst_sel:DWORD dst_unused:UNUSED_PAD src0_sel:DWORD src1_sel:WORD_1\n"
    "v_lshlrev_b32_sdwa v206, 1, v181 dst_sel:DWORD dst_unused:UNUSED_PAD src0_sel:DWORD src1_sel:WORD_1\n"
    "v_lshlrev_b32_sdwa v191, 1, v182 dst_sel:DWORD dst_unused:UNUSED_PAD src0_sel:DWORD src1_sel:WORD_1\n"
    "v_lshlrev_b32_sdwa v207, 1, v183 dst_sel:DWORD dst_unused:UNUSED_PAD src0_sel:DWORD src1_sel:WORD_1\n"
    "v_fma_mix_f32 v234, v192, v232, 0 op_sel_hi:[1,1,0]\n"
    "v_mfma_f32_16x16x32_bf16 v[172:175], a[68:71], a[232:235], v[172:175]\n"
    "s_waitcnt lgkmcnt(7)\n"
    "ds_read_u16_d16_hi v203, v203\n"
    "v_or_b32_e64 v199, v199, v163\n"
    "v_fma_mix_f32 v232, v192, v232, 0 op_sel:[1,1,0] op_sel_hi:[1,1,0]\n"
    "v_mfma_f32_16x16x32_bf16 v[176:179], a[104:107], a[236:239], 0\n"
    "s_waitcnt lgkmcnt(7)\n"
    "ds_read_b128 v[164:167], v244 offset:6144\n"
    "ds_read_u16 v188, v188\n"
    "v_cvt_pk_bf16_f32 v192, v234, v232\n"
    "v_mfma_f32_16x16x32_bf16 v[176:179], a[152:155], a[240:243], v[176:179]\n"
    "v_pk_fma_f16 v232, v196, v196, s48\n"
    "v_or_b32_e64 v184, v184, v200\n"
    "v_mfma_f32_16x16x32_bf16 v[176:179], a[64:67], a[244:247], v[176:179]\n"
    "buffer_load_dwordx4 v[8:11], v246, s[40:43], s62 offen offset:0\n"
    "buffer_load_dwordx4 v[40:43], v246, s[40:43], s62 offen offset:64\n"
    "buffer_load_dwordx4 v[72:75], v246, s[40:43], s62 offen offset:128\n"
    "ds_read_u16_d16_hi v204, v204\n"
    "v_mfma_f32_16x16x32_bf16 v[180:183], a[108:111], a[236:239], 0\n"
    "s_waitcnt lgkmcnt(8)\n"
    "ds_read_u16 v189, v189\n"
    "v_pk_mul_f16 v232, v196, v232\n"
    "v_mfma_f32_16x16x32_bf16 v[180:183], a[156:159], a[240:243], v[180:183]\n"
    "v_exp_f16_e32 v234, v232\n"
    "v_mfma_f32_16x16x32_bf16 v[180:183], a[68:71], a[244:247], v[180:183]\n"
    "ds_read_u16_d16_hi v205, v205\n"
    "v_exp_f16_sdwa v234, v232 dst_sel:WORD_1 dst_unused:UNUSED_PRESERVE src0_sel:WORD_1\n"
    "v_mfma_f32_16x16x32_bf16 v[96:99], a[16:19], v[192:195], v[96:99]\n"
    "s_waitcnt lgkmcnt(7)\n"
    "v_pk_fma_f16 v234, v234, s49, s49\n"
    "v_or_b32_e32 v185, v185, v201\n"
    "v_mfma_f32_16x16x32_bf16 v[100:103], a[20:23], v[192:195], v[100:103]\n"
    "ds_read_u16 v190, v190\n"
    "v_rcp_f16_e64 v232, v234\n"
    "v_mfma_f32_16x16x32_bf16 v[112:115], a[24:27], v[192:195], v[112:115]\n"
    "ds_read_u16_d16_hi v206, v206\n"
    "v_rcp_f16_sdwa v232, v234 dst_sel:WORD_1 dst_unused:UNUSED_PRESERVE src0_sel:WORD_1\n"
    "v_mfma_f32_16x16x32_bf16 v[116:119], a[28:31], v[192:195], v[116:119]\n"
    "s_nop 0\n"
    "v_fma_mix_f32 v234, v196, v232, 0 op_sel_hi:[1,1,0]\n"
    "v_fma_mix_f32 v232, v196, v232, 0 op_sel:[1,1,0] op_sel_hi:[1,1,0]\n"
    "v_mfma_f32_16x16x32_bf16 v[128:131], v[236:239], v[192:195], v[128:131]\n"
    "s_waitcnt lgkmcnt(6)\n"
    "ds_read_u16 v191, v191\n"
    "v_cvt_pk_bf16_f32 v196, v234, v232\n"
    "v_mfma_f32_16x16x32_bf16 v[132:135], v[164:167], v[192:195], v[132:135]\n"
    "ds_read_u16_d16_hi v207, v207\n"
    "v_cvt_pk_f16_f32 v192, v168, v169\n"
    "v_mfma_f32_16x16x32_bf16 v[104:107], a[16:19], v[196:199], v[104:107]\n"
    "v_lshlrev_b32_sdwa v193, 1, v170 dst_sel:DWORD dst_unused:UNUSED_PAD src0_sel:DWORD src1_sel:WORD_1\n"
    "v_lshlrev_b32_sdwa v249, 1, v171 dst_sel:DWORD dst_unused:UNUSED_PAD src0_sel:DWORD src1_sel:WORD_1\n"
    "v_mfma_f32_16x16x32_bf16 v[108:111], a[20:23], v[196:199], v[108:111]\n"
    "ds_read_u16 v193, v193\n"
    "v_lshlrev_b32_sdwa v194, 1, v172 dst_sel:DWORD dst_unused:UNUSED_PAD src0_sel:DWORD src1_sel:WORD_1\n"
    "v_mfma_f32_16x16x32_bf16 v[120:123], a[24:27], v[196:199], v[120:123]\n"
    "v_or_b32_e32 v186, v186, v202\n"
    "v_lshlrev_b32_sdwa v255, 1, v173 dst_sel:DWORD dst_unused:UNUSED_PAD src0_sel:DWORD src1_sel:WORD_1\n"
    "v_mfma_f32_16x16x32_bf16 v[124:127], a[28:31], v[196:199], v[124:127]\n"
    "s_waitcnt lgkmcnt(7)\n"
    "ds_read_b128 a[252:255], v244 offset:34816\n"
    "v_lshlrev_b32_sdwa v195, 1, v174 dst_sel:DWORD dst_unused:UNUSED_PAD src0_sel:DWORD src1_sel:WORD_1\n"
    "v_mfma_f32_16x16x32_bf16 v[136:139], v[236:239], v[196:199], v[136:139]\n"
    "ds_read_u16_d16_hi v249, v249\n"
    "v_lshlrev_b32_sdwa v160, 1, v175 dst_sel:DWORD dst_unused:UNUSED_PAD src0_sel:DWORD src1_sel:WORD_1\n"
    "v_mfma_f32_16x16x32_bf16 v[140:143], v[164:167], v[196:199], v[140:143]\n"
    "buffer_load_dwordx4 v[12:15], v246, s[40:43], s63 offen offset:0\n"
    "buffer_load_dwordx4 v[44:47], v246, s[40:43], s63 offen offset:64\n"
    "buffer_load_dwordx4 v[76:79], v246, s[40:43], s63 offen offset:128\n"
    "ds_read_u16 v194, v194\n"
    "v_mfma_f32_16x16x32_bf16 v[168:171], a[112:115], a[224:227], 0\n"
    "s_waitcnt lgkmcnt(8)\n"
    "v_cvt_pk_f16_f32 v196, v176, v177\n"
    "v_lshlrev_b32_sdwa v197, 1, v178 dst_sel:DWORD dst_unused:UNUSED_PAD src0_sel:DWORD src1_sel:WORD_1\n"
    "v_mfma_f32_16x16x32_bf16 v[168:171], a[160:163], a[228:231], v[168:171]\n"
    "s_nop 0\n"
    "ds_read_u16_d16_hi v255, v255\n"
    "v_lshlrev_b32_sdwa v161, 1, v179 dst_sel:DWORD dst_unused:UNUSED_PAD src0_sel:DWORD src1_sel:WORD_1\n"
    "v_mfma_f32_16x16x32_bf16 v[168:171], a[72:75], a[232:235], v[168:171]\n"
    "v_lshlrev_b32_sdwa v198, 1, v180 dst_sel:DWORD dst_unused:UNUSED_PAD src0_sel:DWORD src1_sel:WORD_1\n"
    "v_lshlrev_b32_sdwa v162, 1, v181 dst_sel:DWORD dst_unused:UNUSED_PAD src0_sel:DWORD src1_sel:WORD_1\n"
    "v_mfma_f32_16x16x32_bf16 v[172:175], a[116:119], a[224:227], 0\n"
    "s_waitcnt lgkmcnt(7)\n"
    "ds_read_u16 v195, v195\n"
    "v_lshlrev_b32_sdwa v199, 1, v182 dst_sel:DWORD dst_unused:UNUSED_PAD src0_sel:DWORD src1_sel:WORD_1\n"
    "v_or_b32_e32 v187, v187, v203\n"
    "v_mfma_f32_16x16x32_bf16 v[172:175], a[164:167], a[228:231], v[172:175]\n"
    "ds_read_b128 v[236:239], v244 offset:8192\n"
    "v_lshlrev_b32_sdwa v163, 1, v183 dst_sel:DWORD dst_unused:UNUSED_PAD src0_sel:DWORD src1_sel:WORD_1\n"
    "v_mfma_f32_16x16x32_bf16 v[176:179], a[112:115], a[236:239], 0\n"
    "ds_read_u16_d16_hi v160, v160\n"
    "v_or_b32_e64 v188, v188, v204\n"
    "v_mfma_f32_16x16x32_bf16 v[176:179], a[160:163], a[240:243], v[176:179]\n"
    "v_or_b32_e64 v189, v189, v205\n"
    "v_or_b32_e32 v190, v190, v206\n"
    "v_mfma_f32_16x16x32_bf16 v[176:179], a[72:75], a[244:247], v[176:179]\n"
    "s_waitcnt lgkmcnt(8)\n"
    "ds_read_b128 v[164:167], v244 offset:10240\n"
    "v_or_b32_e32 v191, v191, v207\n"
    "v_mfma_f32_16x16x32_bf16 v[180:183], a[116:119], a[236:239], 0\n"
    "s_waitcnt lgkmcnt(7)\n"
    "ds_read_u16 v197, v197\n"
    "v_pk_fma_f16 v232, v192, v192, s48\n"
    "v_mfma_f32_16x16x32_bf16 v[180:183], a[164:167], a[240:243], v[180:183]\n"
    "v_pk_mul_f16 v232, v192, v232\n"
    "v_lshlrev_b32_sdwa v200, 1, v169 dst_sel:DWORD dst_unused:UNUSED_PAD src0_sel:DWORD src1_sel:WORD_1\n"
    "v_mfma_f32_16x16x32_bf16 v[180:183], a[252:255], a[244:247], v[180:183]\n"
    "ds_read_u16_d16_hi v161, v161\n"
    "v_exp_f16_e32 v234, v232\n"
    "v_mfma_f32_16x16x32_bf16 v[172:175], a[252:255], a[232:235], v[172:175]\n"
    "s_waitcnt lgkmcnt(8)\n"
    "ds_read_u16 v198, v198\n"
    "v_or_b32_e64 v193, v193, v249\n"
    "v_mfma_f32_16x16x32_bf16 v[96:99], a[32:35], v[184:187], v[96:99]\n"
    "v_exp_f16_sdwa v234, v232 dst_sel:WORD_1 dst_unused:UNUSED_PRESERVE src0_sel:WORD_1\n"
    "v_mfma_f32_16x16x32_bf16 v[100:103], a[36:39], v[184:187], v[100:103]\n"
    "ds_read_u16_d16_hi v162, v162\n"
    "v_pk_fma_f16 v234, v234, s49, s49\n"
    "v_mfma_f32_16x16x32_bf16 v[112:115], a[40:43], v[184:187], v[112:115]\n"
    "s_waitcnt lgkmcnt(8)\n"
    "v_or_b32_e32 v194, v194, v255\n"
    "v_lshlrev_b32_sdwa v201, 1, v171 dst_sel:DWORD dst_unused:UNUSED_PAD src0_sel:DWORD src1_sel:WORD_1\n"
    "v_mfma_f32_16x16x32_bf16 v[116:119], a[44:47], v[184:187], v[116:119]\n"
    "s_nop 0\n"
    "ds_read_u16 v199, v199\n"
    "v_lshlrev_b32_sdwa v202, 1, v173 dst_sel:DWORD dst_unused:UNUSED_PAD src0_sel:DWORD src1_sel:WORD_1\n"
    "v_mfma_f32_16x16x32_bf16 v[104:107], a[32:35], v[188:191], v[104:107]\n"
    "s_waitcnt lgkmcnt(7)\n"
    "buffer_load_dwordx4 v[16:19], v246, s[40:43], s52 offen offset:0\n"
    "buffer_load_dwordx4 v[48:51], v246, s[40:43], s52 offen offset:64\n"
    "buffer_load_dwordx4 v[80:83], v246, s[40:43], s52 offen offset:128\n"
    "ds_read_u16_d16_hi v163, v163\n"
    "v_mfma_f32_16x16x32_bf16 v[128:131], v[236:239], v[184:187], v[128:131]\n"
    "v_lshlrev_b32_sdwa v203, 1, v175 dst_sel:DWORD dst_unused:UNUSED_PAD src0_sel:DWORD src1_sel:WORD_1\n"
    "v_rcp_f16_e32 v232, v234\n"
    "v_mfma_f32_16x16x32_bf16 v[108:111], a[36:39], v[188:191], v[108:111]\n"
    "s_waitcnt lgkmcnt(6)\n"
    "ds_read_b128 a[252:255], v244 offset:38912\n"
    "ds_read_u16_d16_hi v200, v200\n"
    "v_mfma_f32_16x16x32_bf16 v[132:135], v[164:167], v[184:187], v[132:135]\n"
    "v_or_b32_e64 v195, v195, v160\n"
    "v_lshlrev_b32_sdwa v184, 1, v168 dst_sel:DWORD dst_unused:UNUSED_PAD src0_sel:DWORD src1_sel:WORD_1\n"
    "v_mfma_f32_16x16x32_bf16 v[120:123], a[40:43], v[188:191], v[120:123]\n"
    "ds_read_u16 v184, v184\n"
    "v_lshlrev_b32_sdwa v185, 1, v170 dst_sel:DWORD dst_unused:UNUSED_PAD src0_sel:DWORD src1_sel:WORD_1\n"
    "v_mfma_f32_16x16x32_bf16 v[124:127], a[44:47], v[188:191], v[124:127]\n"
    "v_lshlrev_b32_sdwa v186, 1, v172 dst_sel:DWORD dst_unused:UNUSED_PAD src0_sel:DWORD src1_sel:WORD_1\n"
    "v_lshlrev_b32_sdwa v187, 1, v174 dst_sel:DWORD dst_unused:UNUSED_PAD src0_sel:DWORD src1_sel:WORD_1\n"
    "v_mfma_f32_16x16x32_bf16 v[136:139], v[236:239], v[188:191], v[136:139]\n"
    "ds_read_u16 v185, v185\n"
    "v_rcp_f16_sdwa v232, v234 dst_sel:WORD_1 dst_unused:UNUSED_PRESERVE src0_sel:WORD_1\n"
    "v_mfma_f32_16x16x32_bf16 v[140:143], v[164:167], v[188:191], v[140:143]\n"
    "s_waitcnt lgkmcnt(8)\n"
    "ds_read_u16_d16_hi v201, v201\n"
    "v_or_b32_e32 v197, v197, v161\n"
    "v_mfma_f32_16x16x32_bf16 v[168:171], a[120:123], a[224:227], 0\n"
    "v_lshlrev_b32_sdwa v188, 1, v176 dst_sel:DWORD dst_unused:UNUSED_PAD src0_sel:DWORD src1_sel:WORD_1\n"
    "v_lshlrev_b32_sdwa v204, 1, v177 dst_sel:DWORD dst_unused:UNUSED_PAD src0_sel:DWORD src1_sel:WORD_1\n"
    "v_mfma_f32_16x16x32_bf16 v[168:171], a[168:171], a[228:231], v[168:171]\n"
    "ds_read_u16 v186, v186\n"
    "v_lshlrev_b32_sdwa v189, 1, v178 dst_sel:DWORD dst_unused:UNUSED_PAD src0_sel:DWORD src1_sel:WORD_1\n"
    "v_mfma_f32_16x16x32_bf16 v[168:171], a[76:79], a[232:235], v[168:171]\n"
    "s_waitcnt lgkmcnt(8)\n"
    "v_or_b32_e32 v198, v198, v162\n"
    "v_lshlrev_b32_sdwa v205, 1, v179 dst_sel:DWORD dst_unused:UNUSED_PAD src0_sel:DWORD src1_sel:WORD_1\n"
    "v_mfma_f32_16x16x32_bf16 v[172:175], a[124:127], a[224:227], 0\n"
    "ds_read_u16_d16_hi v202, v202\n"
    "v_lshlrev_b32_sdwa v190, 1, v180 dst_sel:DWORD dst_unused:UNUSED_PAD src0_sel:DWORD src1_sel:WORD_1\n"
    "v_mfma_f32_16x16x32_bf16 v[172:175], a[172:175], a[228:231], v[172:175]\n"
    "v_lshlrev_b32_sdwa v206, 1, v181 dst_sel:DWORD dst_unused:UNUSED_PAD src0_sel:DWORD src1_sel:WORD_1\n"
    "v_lshlrev_b32_sdwa v191, 1, v182 dst_sel:DWORD dst_unused:UNUSED_PAD src0_sel:DWORD src1_sel:WORD_1\n"
    "v_lshlrev_b32_sdwa v207, 1, v183 dst_sel:DWORD dst_unused:UNUSED_PAD src0_sel:DWORD src1_sel:WORD_1\n"
    "v_fma_mix_f32 v234, v192, v232, 0 op_sel_hi:[1,1,0]\n"
    "s_waitcnt lgkmcnt(6)\n"
    "v_mfma_f32_16x16x32_bf16 v[172:175], a[252:255], a[232:235], v[172:175]\n"
    "s_nop 0\n"
    "ds_read_b128 v[236:239], v244 offset:12288\n"
    "ds_read_u16 v187, v187\n"
    "v_or_b32_e64 v199, v199, v163\n"
    "v_fma_mix_f32 v232, v192, v232, 0 op_sel:[1,1,0] op_sel_hi:[1,1,0]\n"
    "v_mfma_f32_16x16x32_bf16 v[176:179], a[120:123], a[236:239], 0\n"
    "ds_read_u16_d16_hi v203, v203\n"
    "v_cvt_pk_bf16_f32 v192, v234, v232\n"
    "v_mfma_f32_16x16x32_bf16 v[176:179], a[168:171], a[240:243], v[176:179]\n"
    "v_pk_fma_f16 v232, v196, v196, s48\n"
    "v_lshlrev_b32_sdwa v249, 1, v171 dst_sel:DWORD dst_unused:UNUSED_PAD src0_sel:DWORD src1_sel:WORD_1\n"
    "v_mfma_f32_16x16x32_bf16 v[176:179], a[76:79], a[244:247], v[176:179]\n"
    "buffer_load_dwordx4 v[152:155], v245, s[40:43], s57 offen offset:64\n"
    "buffer_load_dwordx4 v[144:147], v245, s[40:43], s57 offen offset:0\n"
    "ds_read_b128 v[164:167], v244 offset:14336\n"
    "v_mfma_f32_16x16x32_bf16 v[180:183], a[124:127], a[236:239], 0\n"
    "s_waitcnt lgkmcnt(8)\n"
    "ds_read_u16 v188, v188\n"
    "v_pk_mul_f16 v232, v196, v232\n"
    "v_mfma_f32_16x16x32_bf16 v[180:183], a[172:175], a[240:243], v[180:183]\n"
    "s_nop 0\n"
    "ds_read_u16_d16_hi v204, v204\n"
    "v_exp_f16_e32 v234, v232\n"
    "v_mfma_f32_16x16x32_bf16 v[180:183], a[252:255], a[244:247], v[180:183]\n"
    "v_exp_f16_sdwa v234, v232 dst_sel:WORD_1 dst_unused:UNUSED_PRESERVE src0_sel:WORD_1\n"
    "v_mfma_f32_16x16x32_bf16 v[96:99], a[176:179], v[192:195], v[96:99]\n"
    "s_waitcnt lgkmcnt(8)\n"
    "ds_read_u16 v189, v189\n"
    "v_pk_fma_f16 v234, v234, s49, s49\n"
    "v_mfma_f32_16x16x32_bf16 v[100:103], a[180:183], v[192:195], v[100:103]\n"
    "v_rcp_f16_e32 v232, v234\n"
    "v_mfma_f32_16x16x32_bf16 v[112:115], a[184:187], v[192:195], v[112:115]\n"
    "ds_read_u16_d16_hi v205, v205\n"
    "v_rcp_f16_sdwa v232, v234 dst_sel:WORD_1 dst_unused:UNUSED_PRESERVE src0_sel:WORD_1\n"
    "v_mfma_f32_16x16x32_bf16 v[116:119], a[188:191], v[192:195], v[116:119]\n"
    "s_waitcnt lgkmcnt(7)\n"
    "ds_read_u16 v190, v190\n"
    "v_fma_mix_f32 v234, v196, v232, 0 op_sel_hi:[1,1,0]\n"
    "v_fma_mix_f32 v232, v196, v232, 0 op_sel:[1,1,0] op_sel_hi:[1,1,0]\n"
    "v_or_b32_e64 v184, v184, v200\n"
    "v_mfma_f32_16x16x32_bf16 v[128:131], v[236:239], v[192:195], v[128:131]\n"
    "ds_read_b128 a[224:227], v243 offset:24640\n"
    "v_cvt_pk_bf16_f32 v196, v234, v232\n"
    "v_or_b32_e64 v185, v185, v201\n"
    "v_or_b32_e64 v186, v186, v202\n"
    "v_mfma_f32_16x16x32_bf16 v[104:107], a[176:179], v[196:199], v[104:107]\n"
    "ds_read_u16_d16_hi v206, v206\n"
    "v_lshlrev_b32_sdwa v255, 1, v173 dst_sel:DWORD dst_unused:UNUSED_PAD src0_sel:DWORD src1_sel:WORD_1\n"
    "v_lshlrev_b32_sdwa v160, 1, v175 dst_sel:DWORD dst_unused:UNUSED_PAD src0_sel:DWORD src1_sel:WORD_1\n"
    "v_mfma_f32_16x16x32_bf16 v[108:111], a[180:183], v[196:199], v[108:111]\n"
    "s_waitcnt lgkmcnt(7)\n"
    "ds_read_b128 a[228:231], v243 offset:25664\n"
    "v_lshlrev_b32_sdwa v161, 1, v179 dst_sel:DWORD dst_unused:UNUSED_PAD src0_sel:DWORD src1_sel:WORD_1\n"
    "v_mfma_f32_16x16x32_bf16 v[132:135], v[164:167], v[192:195], v[132:135]\n"
    "s_nop 0\n"
    "ds_read_b128 a[232:235], v243 offset:26688\n"
    "v_cvt_pk_f16_f32 v192, v168, v169\n"
    "v_mfma_f32_16x16x32_bf16 v[120:123], a[184:187], v[196:199], v[120:123]\n"
    "ds_read_u16 v191, v191\n"
    "v_lshlrev_b32_sdwa v193, 1, v170 dst_sel:DWORD dst_unused:UNUSED_PAD src0_sel:DWORD src1_sel:WORD_1\n"
    "v_lshlrev_b32_sdwa v194, 1, v172 dst_sel:DWORD dst_unused:UNUSED_PAD src0_sel:DWORD src1_sel:WORD_1\n"
    "v_lshlrev_b32_sdwa v195, 1, v174 dst_sel:DWORD dst_unused:UNUSED_PAD src0_sel:DWORD src1_sel:WORD_1\n"
    "v_lshlrev_b32_sdwa v162, 1, v181 dst_sel:DWORD dst_unused:UNUSED_PAD src0_sel:DWORD src1_sel:WORD_1\n"
    "v_mfma_f32_16x16x32_bf16 v[124:127], a[188:191], v[196:199], v[124:127]\n"
    "buffer_load_dwordx4 v[156:159], v245, s[40:43], s88 offen offset:64\n"
    "buffer_load_dwordx4 v[148:151], v245, s[40:43], s88 offen offset:0\n"
    "ds_read_b128 a[236:239], v243 offset:27712\n"
    "v_mfma_f32_16x16x32_bf16 v[136:139], v[236:239], v[196:199], v[136:139]\n"
    "s_waitcnt lgkmcnt(9)\n"
    "ds_read_u16_d16_hi v207, v207\n"
    "v_or_b32_e32 v187, v187, v203\n"
    "v_lshlrev_b32_sdwa v163, 1, v183 dst_sel:DWORD dst_unused:UNUSED_PAD src0_sel:DWORD src1_sel:WORD_1\n"
    "v_or_b32_e64 v188, v188, v204\n"
    "v_mfma_f32_16x16x32_bf16 v[140:143], v[164:167], v[196:199], v[140:143]\n"
    "ds_read_b128 a[240:243], v243 offset:28736\n"
    "v_cvt_pk_f16_f32 v196, v176, v177\n"
    "v_lshlrev_b32_sdwa v197, 1, v178 dst_sel:DWORD dst_unused:UNUSED_PAD src0_sel:DWORD src1_sel:WORD_1\n"
    "v_lshlrev_b32_sdwa v198, 1, v180 dst_sel:DWORD dst_unused:UNUSED_PAD src0_sel:DWORD src1_sel:WORD_1\n"
    "v_lshlrev_b32_sdwa v199, 1, v182 dst_sel:DWORD dst_unused:UNUSED_PAD src0_sel:DWORD src1_sel:WORD_1\n"
    "s_waitcnt lgkmcnt(9)\n"
    "v_or_b32_e32 v189, v189, v205\n"
    "ds_read_b128 a[244:247], v243 offset:29760\n"
    "ds_read_u16 v193, v193\n"
    "v_pk_fma_f16 v232, v192, v192, s48\n"
    "v_mov_b32_e32 v254, 0\n"
    "s_waitcnt lgkmcnt(9)\n"
    "v_mfma_f32_16x16x32_bf16 v[168:171], a[80:83], a[224:227], 0\n"
    "ds_read_b128 a[252:255], v244 offset:32768\n"
    "v_pk_mul_f16 v232, v192, v232\n"
    "v_mfma_f32_16x16x32_bf16 v[172:175], a[84:87], a[224:227], 0\n"
    "ds_read_u16_d16_hi v249, v249\n"
    "v_exp_f16_e32 v234, v232\n"
    "s_waitcnt lgkmcnt(9)\n"
    "v_or_b32_e32 v190, v190, v206\n"
    "v_mfma_f32_16x16x32_bf16 v[168:171], a[128:131], a[228:231], v[168:171]\n"
    "s_waitcnt lgkmcnt(8)\n"
    "ds_read_u16 v194, v194\n"
    "v_mfma_f32_16x16x32_bf16 v[168:171], a[48:51], a[232:235], v[168:171]\n"
    "v_exp_f16_sdwa v234, v232 dst_sel:WORD_1 dst_unused:UNUSED_PRESERVE src0_sel:WORD_1\n"
    "v_mfma_f32_16x16x32_bf16 v[172:175], a[132:135], a[228:231], v[172:175]\n"
    "ds_read_u16_d16_hi v255, v255\n"
    "v_pk_fma_f16 v234, v234, s49, s49\n"
    "v_rcp_f16_e32 v232, v234\n"
    "v_mfma_f32_16x16x32_bf16 v[96:99], a[192:195], v[184:187], v[96:99]\n"
    "s_waitcnt lgkmcnt(8)\n"
    "ds_read_u16 v195, v195\n"
    "v_lshlrev_b32_sdwa v200, 1, v169 dst_sel:DWORD dst_unused:UNUSED_PAD src0_sel:DWORD src1_sel:WORD_1\n"
    "v_lshlrev_b32_sdwa v201, 1, v171 dst_sel:DWORD dst_unused:UNUSED_PAD src0_sel:DWORD src1_sel:WORD_1\n"
    "v_rcp_f16_sdwa v232, v234 dst_sel:WORD_1 dst_unused:UNUSED_PRESERVE src0_sel:WORD_1\n"
    "v_mfma_f32_16x16x32_bf16 v[176:179], a[80:83], a[236:239], 0\n"
    "s_waitcnt lgkmcnt(7)\n"
    "ds_read_b128 v[236:239], v244 offset:16384\n"
    "v_or_b32_e64 v191, v191, v207\n"
    "v_mfma_f32_16x16x32_bf16 v[176:179], a[128:131], a[240:243], v[176:179]\n"
    "s_waitcnt lgkmcnt(7)\n"
    "ds_read_u16_d16_hi v160, v160\n"
    "v_fma_mix_f32 v234, v192, v232, 0 op_sel_hi:[1,1,0]\n"
    "v_mfma_f32_16x16x32_bf16 v[176:179], a[48:51], a[244:247], v[176:179]\n"
    "v_fma_mix_f32 v232, v192, v232, 0 op_sel:[1,1,0] op_sel_hi:[1,1,0]\n"
    "v_cvt_pk_bf16_f32 v192, v234, v232\n"
    "v_mfma_f32_16x16x32_bf16 v[180:183], a[84:87], a[236:239], 0\n"
    "s_nop 0\n"
    "ds_read_b128 v[164:167], v244 offset:18432\n"
    "v_pk_fma_f16 v232, v196, v196, s48\n"
    "v_mfma_f32_16x16x32_bf16 v[180:183], a[132:135], a[240:243], v[180:183]\n"
    "s_waitcnt lgkmcnt(7)\n"
    "ds_read_u16 v197, v197\n"
    "v_lshlrev_b32_sdwa v204, 1, v177 dst_sel:DWORD dst_unused:UNUSED_PAD src0_sel:DWORD src1_sel:WORD_1\n"
    "v_mfma_f32_16x16x32_bf16 v[180:183], a[252:255], a[244:247], v[180:183]\n"
    "v_lshlrev_b32_sdwa v205, 1, v179 dst_sel:DWORD dst_unused:UNUSED_PAD src0_sel:DWORD src1_sel:WORD_1\n"
    "v_pk_mul_f16 v232, v196, v232\n"
    "v_mfma_f32_16x16x32_bf16 v[172:175], a[252:255], a[232:235], v[172:175]\n"
    "ds_read_u16_d16_hi v161, v161\n"
    "v_exp_f16_e32 v234, v232\n"
    "v_mfma_f32_16x16x32_bf16 v[100:103], a[196:199], v[184:187], v[100:103]\n"
    "s_waitcnt lgkmcnt(8)\n"
    "ds_read_u16 v198, v198\n"
    "buffer_store_dwordx4 v[208:211], v245, s[68:71], s59 offen offset:0 sc1\n"
    "v_mfma_f32_16x16x32_bf16 v[112:115], a[200:203], v[184:187], v[112:115]\n"
    "ds_read_u16_d16_hi v162, v162\n"
    "v_or_b32_e64 v193, v193, v249\n"
    "v_mfma_f32_16x16x32_bf16 v[116:119], a[204:207], v[184:187], v[116:119]\n"
    "s_waitcnt lgkmcnt(8)\n"
    "v_or_b32_e32 v194, v194, v255\n"
    "v_lshlrev_b32_sdwa v202, 1, v173 dst_sel:DWORD dst_unused:UNUSED_PAD src0_sel:DWORD src1_sel:WORD_1\n"
    "v_mfma_f32_16x16x32_bf16 v[104:107], a[192:195], v[188:191], v[104:107]\n"
    "s_nop 0\n"
    "ds_read_u16 v199, v199\n"
    "v_lshlrev_b32_sdwa v203, 1, v175 dst_sel:DWORD dst_unused:UNUSED_PAD src0_sel:DWORD src1_sel:WORD_1\n"
    "v_mfma_f32_16x16x32_bf16 v[108:111], a[196:199], v[188:191], v[108:111]\n"
    "s_waitcnt lgkmcnt(7)\n"
    "ds_read_u16_d16_hi v163, v163\n"
    "v_lshlrev_b32_sdwa v206, 1, v181 dst_sel:DWORD dst_unused:UNUSED_PAD src0_sel:DWORD src1_sel:WORD_1\n"
    "v_lshlrev_b32_sdwa v207, 1, v183 dst_sel:DWORD dst_unused:UNUSED_PAD src0_sel:DWORD src1_sel:WORD_1\n"
    "v_exp_f16_sdwa v234, v232 dst_sel:WORD_1 dst_unused:UNUSED_PRESERVE src0_sel:WORD_1\n"
    "v_mfma_f32_16x16x32_bf16 v[128:131], v[236:239], v[184:187], v[128:131]\n"
    "ds_read_b128 a[252:255], v244 offset:36864\n"
    "v_pk_fma_f16 v234, v234, s49, s49\n"
    "v_mfma_f32_16x16x32_bf16 v[120:123], a[200:203], v[188:191], v[120:123]\n"
    "s_waitcnt lgkmcnt(7)\n"
    "ds_read_u16_d16_hi v200, v200\n"
    "v_or_b32_e32 v195, v195, v160\n"
    "v_rcp_f16_e64 v232, v234\n"
    "2:\n"
    "v_mfma_f32_16x16x32_bf16 v[132:135], v[164:167], v[184:187], v[132:135]\n"
    "v_lshlrev_b32_sdwa v184, 1, v168 dst_sel:DWORD dst_unused:UNUSED_PAD src0_sel:DWORD src1_sel:WORD_1\n"
    "v_lshlrev_b32_sdwa v185, 1, v170 dst_sel:DWORD dst_unused:UNUSED_PAD src0_sel:DWORD src1_sel:WORD_1\n"
    "v_mfma_f32_16x16x32_bf16 v[124:127], a[204:207], v[188:191], v[124:127]\n"
    "ds_read_u16 v184, v184\n"
    "v_lshlrev_b32_sdwa v186, 1, v172 dst_sel:DWORD dst_unused:UNUSED_PAD src0_sel:DWORD src1_sel:WORD_1\n"
    "v_mfma_f32_16x16x32_bf16 v[136:139], v[236:239], v[188:191], v[136:139]\n"
    "ds_read_u16 v185, v185\n"
    "v_lshlrev_b32_sdwa v187, 1, v174 dst_sel:DWORD dst_unused:UNUSED_PAD src0_sel:DWORD src1_sel:WORD_1\n"
    "v_mfma_f32_16x16x32_bf16 v[140:143], v[164:167], v[188:191], v[140:143]\n"
    "s_waitcnt lgkmcnt(8)\n"
    "v_or_b32_e32 v197, v197, v161\n"
    "v_lshlrev_b32_sdwa v188, 1, v176 dst_sel:DWORD dst_unused:UNUSED_PAD src0_sel:DWORD src1_sel:WORD_1\n"
    "v_mfma_f32_16x16x32_bf16 v[168:171], a[88:91], a[224:227], 0\n"
    "ds_read_u16_d16_hi v201, v201\n"
    "v_lshlrev_b32_sdwa v189, 1, v178 dst_sel:DWORD dst_unused:UNUSED_PAD src0_sel:DWORD src1_sel:WORD_1\n"
    "v_mfma_f32_16x16x32_bf16 v[168:171], a[136:139], a[228:231], v[168:171]\n"
    "v_lshlrev_b32_sdwa v190, 1, v180 dst_sel:DWORD dst_unused:UNUSED_PAD src0_sel:DWORD src1_sel:WORD_1\n"
    "v_lshlrev_b32_sdwa v191, 1, v182 dst_sel:DWORD dst_unused:UNUSED_PAD src0_sel:DWORD src1_sel:WORD_1\n"
    "v_mfma_f32_16x16x32_bf16 v[168:171], a[52:55], a[232:235], v[168:171]\n"
    "ds_read_u16 v186, v186\n"
    "v_rcp_f16_sdwa v232, v234 dst_sel:WORD_1 dst_unused:UNUSED_PRESERVE src0_sel:WORD_1\n"
    "v_mfma_f32_16x16x32_bf16 v[172:175], a[92:95], a[224:227], 0\n"
    "s_waitcnt lgkmcnt(8)\n"
    "ds_read_u16_d16_hi v202, v202\n"
    "v_or_b32_e32 v198, v198, v162\n"
    "v_mfma_f32_16x16x32_bf16 v[172:175], a[140:143], a[228:231], v[172:175]\n"
    "v_fma_mix_f32 v234, v196, v232, 0 op_sel_hi:[1,1,0]\n"
    "v_fma_mix_f32 v232, v196, v232, 0 op_sel:[1,1,0] op_sel_hi:[1,1,0]\n"
    "v_mfma_f32_16x16x32_bf16 v[176:179], a[88:91], a[236:239], 0\n"
    "ds_read_b128 v[236:239], v244 offset:20480\n"
    "v_cvt_pk_bf16_f32 v196, v234, v232\n"
    "v_mfma_f32_16x16x32_bf16 v[176:179], a[136:139], a[240:243], v[176:179]\n"
    "s_waitcnt lgkmcnt(7)\n"
    "ds_read_u16 v187, v187\n"
    "v_or_b32_e32 v199, v199, v163\n"
    "v_mfma_f32_16x16x32_bf16 v[176:179], a[52:55], a[244:247], v[176:179]\n"
    "buffer_store_dwordx4 v[212:215], v245, s[68:71], s59 offen offset:64 sc1\n"
    "ds_read_u16_d16_hi v203, v203\n"
    "v_mfma_f32_16x16x32_bf16 v[172:175], a[252:255], a[232:235], v[172:175]\n"
    "v_lshlrev_b32_sdwa v249, 1, v171 dst_sel:DWORD dst_unused:UNUSED_PAD src0_sel:DWORD src1_sel:WORD_1\n"
    "ds_read_b128 v[164:167], v244 offset:22528\n"
    "v_mfma_f32_16x16x32_bf16 v[180:183], a[92:95], a[236:239], 0\n"
    "s_waitcnt lgkmcnt(8)\n"
    "ds_read_u16 v188, v188\n"
    "v_or_b32_e32 v184, v184, v200\n"
    "v_mfma_f32_16x16x32_bf16 v[180:183], a[140:143], a[240:243], v[180:183]\n"
    "v_lshlrev_b32_sdwa v161, 1, v179 dst_sel:DWORD dst_unused:UNUSED_PAD src0_sel:DWORD src1_sel:WORD_1\n"
    "v_lshlrev_b32_sdwa v255, 1, v173 dst_sel:DWORD dst_unused:UNUSED_PAD src0_sel:DWORD src1_sel:WORD_1\n"
    "v_mfma_f32_16x16x32_bf16 v[180:183], a[252:255], a[244:247], v[180:183]\n"
    "ds_read_u16_d16_hi v204, v204\n"
    "v_lshlrev_b32_sdwa v160, 1, v175 dst_sel:DWORD dst_unused:UNUSED_PAD src0_sel:DWORD src1_sel:WORD_1\n"
    "v_mfma_f32_16x16x32_bf16 v[96:99], a[208:211], v[192:195], v[96:99]\n"
    "s_waitcnt lgkmcnt(8)\n"
    "v_or_b32_e32 v185, v185, v201\n"
    "ds_read_u16 v189, v189\n"
    "v_mfma_f32_16x16x32_bf16 v[100:103], a[212:215], v[192:195], v[100:103]\n"
    "v_mfma_f32_16x16x32_bf16 v[112:115], a[216:219], v[192:195], v[112:115]\n"
    "ds_read_u16_d16_hi v205, v205\n"
    "v_lshlrev_b32_sdwa v162, 1, v181 dst_sel:DWORD dst_unused:UNUSED_PAD src0_sel:DWORD src1_sel:WORD_1\n"
    "v_mfma_f32_16x16x32_bf16 v[116:119], a[220:223], v[192:195], v[116:119]\n"
    "s_waitcnt lgkmcnt(7)\n"
    "v_or_b32_e32 v186, v186, v202\n"
    "v_lshlrev_b32_sdwa v163, 1, v183 dst_sel:DWORD dst_unused:UNUSED_PAD src0_sel:DWORD src1_sel:WORD_1\n"
    "v_mfma_f32_16x16x32_bf16 v[128:131], v[236:239], v[192:195], v[128:131]\n"
    "ds_read_u16 v190, v190\n"
    "v_cvt_pk_bf16_f32 v96, v96, v97\n"
    "v_mfma_f32_16x16x32_bf16 v[104:107], a[208:211], v[196:199], v[104:107]\n"
    "ds_read_u16_d16_hi v206, v206\n"
    "v_cvt_pk_bf16_f32 v97, v98, v99\n"
    "v_cvt_pk_bf16_f32 v98, v100, v101\n"
    "v_cvt_pk_bf16_f32 v99, v102, v103\n"
    "v_cvt_pk_bf16_f32 v112, v112, v113\n"
    "v_mfma_f32_16x16x32_bf16 v[108:111], a[212:215], v[196:199], v[108:111]\n"
    "v_cvt_pk_bf16_f32 v113, v114, v115\n"
    "v_cvt_pk_bf16_f32 v114, v116, v117\n"
    "v_mfma_f32_16x16x32_bf16 v[120:123], a[216:219], v[196:199], v[120:123]\n"
    "s_waitcnt lgkmcnt(6)\n"
    "v_or_b32_e32 v187, v187, v203\n"
    "v_cvt_pk_bf16_f32 v115, v118, v119\n"
    "v_mfma_f32_16x16x32_bf16 v[132:135], v[164:167], v[192:195], v[132:135]\n"
    "ds_write_b128 v243, v[96:99] offset:24640\n"
    "v_cvt_pk_f16_f32 v192, v168, v169\n"
    "v_lshlrev_b32_sdwa v193, 1, v170 dst_sel:DWORD dst_unused:UNUSED_PAD src0_sel:DWORD src1_sel:WORD_1\n"
    "v_lshlrev_b32_sdwa v194, 1, v172 dst_sel:DWORD dst_unused:UNUSED_PAD src0_sel:DWORD src1_sel:WORD_1\n"
    "v_lshlrev_b32_sdwa v195, 1, v174 dst_sel:DWORD dst_unused:UNUSED_PAD src0_sel:DWORD src1_sel:WORD_1\n"
    "v_mfma_f32_16x16x32_bf16 v[124:127], a[220:223], v[196:199], v[124:127]\n"
    "v_cvt_pk_bf16_f32 v128, v128, v129\n"
    "v_cvt_pk_bf16_f32 v129, v130, v131\n"
    "v_mfma_f32_16x16x32_bf16 v[136:139], v[236:239], v[196:199], v[136:139]\n"
    "s_waitcnt lgkmcnt(5)\n"
    "ds_read_u16 v191, v191\n"
    "buffer_store_dwordx4 v[216:219], v245, s[68:71], s59 offen offset:128 sc1\n"
    "v_mfma_f32_16x16x32_bf16 v[140:143], v[164:167], v[196:199], v[140:143]\n"
    "s_nop 0\n"
    "ds_read_u16_d16_hi v207, v207\n"
    "ds_write_b128 v243, v[112:115] offset:26688\n"
    "v_cvt_pk_f16_f32 v196, v176, v177\n"
    "v_lshlrev_b32_sdwa v197, 1, v178 dst_sel:DWORD dst_unused:UNUSED_PAD src0_sel:DWORD src1_sel:WORD_1\n"
    "v_lshlrev_b32_sdwa v198, 1, v180 dst_sel:DWORD dst_unused:UNUSED_PAD src0_sel:DWORD src1_sel:WORD_1\n"
    "v_lshlrev_b32_sdwa v199, 1, v182 dst_sel:DWORD dst_unused:UNUSED_PAD src0_sel:DWORD src1_sel:WORD_1\n"
    "v_or_b32_e64 v188, v188, v204\n"
    "v_cvt_pk_bf16_f32 v130, v132, v133\n"
    "v_cvt_pk_bf16_f32 v131, v134, v135\n"
    "v_mfma_f32_16x16x32_bf16 v[168:171], a[96:99], a[224:227], 0\n"
    "s_waitcnt lgkmcnt(5)\n"
    "ds_read_u16 v193, v193\n"
    "v_or_b32_e32 v189, v189, v205\n"
    "v_mfma_f32_16x16x32_bf16 v[168:171], a[144:147], a[228:231], v[168:171]\n"
    "s_waitcnt vmcnt(24) lgkmcnt(4)\n"
    "ds_read_u16_d16_hi v249, v249\n"
    "v_or_b32_e32 v190, v190, v206\n"
    "v_mfma_f32_16x16x32_bf16 v[168:171], a[56:59], a[232:235], v[168:171]\n"
    "ds_write2_b32 v248, v0, v1 offset0:0 offset1:20\n"
    "v_mfma_f32_16x16x32_bf16 v[172:175], a[100:103], a[224:227], 0\n"
    "v_cvt_pk_bf16_f32 v104, v104, v105\n"
    "v_cvt_pk_bf16_f32 v105, v106, v107\n"
    "v_mfma_f32_16x16x32_bf16 v[172:175], a[148:151], a[228:231], v[172:175]\n"
    "ds_read_u16 v194, v194\n"
    "v_cvt_pk_bf16_f32 v106, v108, v109\n"
    "v_mfma_f32_16x16x32_bf16 v[172:175], a[60:63], a[232:235], v[172:175]\n"
    "s_nop 0\n"
    "ds_write2_b32 v248, v2, v3 offset0:40 offset1:60\n"
    "v_mfma_f32_16x16x32_bf16 v[176:179], a[96:99], a[236:239], 0\n"
    "s_waitcnt lgkmcnt(5)\n"
    "v_add_u32_e32 v248, 1360, v248\n"
    "v_or_b32_e32 v191, v191, v207\n"
    "v_mfma_f32_16x16x32_bf16 v[176:179], a[144:147], a[240:243], v[176:179]\n"
    "s_waitcnt vmcnt(21)\n"
    "ds_read_u16_d16_hi v255, v255\n"
    "v_cvt_pk_bf16_f32 v107, v110, v111\n"
    "v_mfma_f32_16x16x32_bf16 v[176:179], a[56:59], a[244:247], v[176:179]\n"
    "ds_read_u16 v195, v195\n"
    "ds_write2_b32 v235, v20, v21 offset0:0 offset1:20\n"
    "v_mfma_f32_16x16x32_bf16 v[180:183], a[100:103], a[236:239], 0\n"
    "v_cvt_pk_bf16_f32 v120, v120, v121\n"
    "v_cvt_pk_bf16_f32 v121, v122, v123\n"
    "v_mfma_f32_16x16x32_bf16 v[180:183], a[148:151], a[240:243], v[180:183]\n"
    "v_cvt_pk_bf16_f32 v122, v124, v125\n"
    "v_cvt_pk_bf16_f32 v123, v126, v127\n"
    "v_mfma_f32_16x16x32_bf16 v[180:183], a[60:63], a[244:247], v[180:183]\n"
    "s_waitcnt lgkmcnt(5)\n"
    "ds_read_b128 v[236:239], v244 offset:0\n"
    "ds_write2_b32 v235, v22, v23 offset0:40 offset1:60\n"
    "v_mfma_f32_16x16x32_bf16 v[96:99], a[0:3], v[184:187], 0\n"
    "buffer_store_dwordx4 v[220:223], v245, s[68:71], s39 offen offset:0 sc1\n"
    "s_waitcnt vmcnt(21)\n"
    "ds_write2_b32 v248, v4, v5 offset0:0 offset1:20\n"
    "v_add_u32_e32 v235, 1360, v235\n"
    "v_or_b32_e32 v193, v193, v249\n"
    "v_mfma_f32_16x16x32_bf16 v[100:103], a[4:7], v[184:187], 0\n"
    "s_waitcnt vmcnt(19)\n"
    "ds_read_u16_d16_hi v160, v160\n"
    "ds_write2_b32 v248, v6, v7 offset0:40 offset1:60\n"
    "v_add_u32_e32 v248, 1360, v248\n"
    "v_cvt_pk_bf16_f32 v136, v136, v137\n"
    "v_pk_fma_f16 v232, v192, v192, s48\n"
    "v_mfma_f32_16x16x32_bf16 v[112:115], a[8:11], v[184:187], 0\n"
    "s_waitcnt vmcnt(16) lgkmcnt(7)\n"
    "ds_read_b128 v[164:167], v244 offset:2048\n"
    "ds_write2_b32 v235, v24, v25 offset0:0 offset1:20\n"
    "v_or_b32_e32 v194, v194, v255\n"
    "v_cvt_pk_bf16_f32 v137, v138, v139\n"
    "v_cvt_pk_bf16_f32 v138, v140, v141\n"
    "v_mfma_f32_16x16x32_bf16 v[116:119], a[12:15], v[184:187], 0\n"
    "ds_read_u16 v197, v197\n"
    "ds_write2_b32 v235, v26, v27 offset0:40 offset1:60\n"
    "v_add_u32_e32 v235, -1360, v235\n"
    "v_cvt_pk_bf16_f32 v139, v142, v143\n"
    "v_pk_mul_f16 v232, v192, v232\n"
    "v_mfma_f32_16x16x32_bf16 v[108:111], a[4:7], v[188:191], 0\n"
    "ds_read_u16_d16_hi v161, v161\n"
    "ds_write_b128 v243, v[128:131] offset:28736\n"
    "v_exp_f16_e64 v234, v232\n"
    "v_lshlrev_b32_sdwa v200, 1, v169 dst_sel:DWORD dst_unused:UNUSED_PAD src0_sel:DWORD src1_sel:WORD_1\n"
    "v_exp_f16_sdwa v234, v232 dst_sel:WORD_1 dst_unused:UNUSED_PRESERVE src0_sel:WORD_1\n"
    "v_lshlrev_b32_sdwa v201, 1, v171 dst_sel:DWORD dst_unused:UNUSED_PAD src0_sel:DWORD src1_sel:WORD_1\n"
    "v_pk_fma_f16 v234, v234, s49, s49\n"
    "v_mfma_f32_16x16x32_bf16 v[124:127], a[12:15], v[188:191], 0\n"
    "s_waitcnt lgkmcnt(6)\n"
    "ds_read_u16 v198, v198\n"
    "ds_write_b128 v243, v[104:107] offset:25664\n"
    "v_or_b32_e32 v195, v195, v160\n"
    "v_lshlrev_b32_sdwa v202, 1, v173 dst_sel:DWORD dst_unused:UNUSED_PAD src0_sel:DWORD src1_sel:WORD_1\n"
    "v_lshlrev_b32_sdwa v203, 1, v175 dst_sel:DWORD dst_unused:UNUSED_PAD src0_sel:DWORD src1_sel:WORD_1\n"
    "v_rcp_f16_e64 v232, v234\n"
    "v_lshlrev_b32_sdwa v204, 1, v177 dst_sel:DWORD dst_unused:UNUSED_PAD src0_sel:DWORD src1_sel:WORD_1\n"
    "v_rcp_f16_sdwa v232, v234 dst_sel:WORD_1 dst_unused:UNUSED_PRESERVE src0_sel:WORD_1\n"
    "v_mfma_f32_16x16x32_bf16 v[128:131], v[236:239], v[184:187], 0\n"
    "s_waitcnt lgkmcnt(6)\n"
    "ds_read_u16_d16_hi v162, v162\n"
    "s_waitcnt vmcnt(15)\n"
    "ds_write2_b32 v248, v8, v9 offset0:0 offset1:20\n"
    "v_lshlrev_b32_sdwa v205, 1, v179 dst_sel:DWORD dst_unused:UNUSED_PAD src0_sel:DWORD src1_sel:WORD_1\n"
    "v_lshlrev_b32_sdwa v206, 1, v181 dst_sel:DWORD dst_unused:UNUSED_PAD src0_sel:DWORD src1_sel:WORD_1\n"
    "v_lshlrev_b32_sdwa v207, 1, v183 dst_sel:DWORD dst_unused:UNUSED_PAD src0_sel:DWORD src1_sel:WORD_1\n"
    "v_mfma_f32_16x16x32_bf16 v[132:135], v[164:167], v[184:187], 0\n"
    "s_nop 0\n"
    "ds_write_b128 v243, v[120:123] offset:27712\n"
    "buffer_store_dwordx4 v[224:227], v245, s[68:71], s39 offen offset:64 sc1\n"
    "v_lshlrev_b32_sdwa v184, 1, v168 dst_sel:DWORD dst_unused:UNUSED_PAD src0_sel:DWORD src1_sel:WORD_1\n"
    "v_mfma_f32_16x16x32_bf16 v[104:107], a[0:3], v[188:191], 0\n"
    "s_waitcnt lgkmcnt(5)\n"
    "ds_read_u16 v199, v199\n"
    "ds_write2_b32 v248, v10, v11 offset0:40 offset1:60\n"
    "v_add_u32_e32 v248, 1360, v248\n"
    "v_or_b32_e32 v197, v197, v161\n"
    "v_lshlrev_b32_sdwa v185, 1, v170 dst_sel:DWORD dst_unused:UNUSED_PAD src0_sel:DWORD src1_sel:WORD_1\n"
    "v_mfma_f32_16x16x32_bf16 v[120:123], a[8:11], v[188:191], 0\n"
    "s_waitcnt vmcnt(14)\n"
    "ds_read_u16_d16_hi v163, v163\n"
    "ds_write_b128 v243, v[136:139] offset:29760\n"
    "v_lshlrev_b32_sdwa v186, 1, v172 dst_sel:DWORD dst_unused:UNUSED_PAD src0_sel:DWORD src1_sel:WORD_1\n"
    "v_lshlrev_b32_sdwa v187, 1, v174 dst_sel:DWORD dst_unused:UNUSED_PAD src0_sel:DWORD src1_sel:WORD_1\n"
    "v_fma_mix_f32 v234, v192, v232, 0 op_sel_hi:[1,1,0]\n"
    "v_fma_mix_f32 v232, v192, v232, 0 op_sel:[1,1,0] op_sel_hi:[1,1,0]\n"
    "v_cvt_pk_bf16_f32 v192, v234, v232\n"
    "v_pk_fma_f16 v232, v196, v196, s48\n"
    "v_pk_mul_f16 v232, v196, v232\n"
    "v_mfma_f32_16x16x32_bf16 v[136:139], v[236:239], v[188:191], 0\n"
    "s_waitcnt lgkmcnt(4)\n"
    "ds_read_u16 v184, v184\n"
    "s_waitcnt vmcnt(13)\n"
    "ds_write2_b32 v248, v12, v13 offset0:0 offset1:20\n"
    "v_or_b32_e64 v198, v198, v162\n"
    "v_exp_f16_e32 v234, v232\n"
    "v_mfma_f32_16x16x32_bf16 v[140:143], v[164:167], v[188:191], 0\n"
    "ds_write2_b32 v248, v14, v15 offset0:40 offset1:60\n"
    "v_add_u32_e32 v248, 1360, v248\n"
    "v_lshlrev_b32_sdwa v188, 1, v176 dst_sel:DWORD dst_unused:UNUSED_PAD src0_sel:DWORD src1_sel:WORD_1\n"
    "v_mfma_f32_16x16x32_bf16 v[168:171], a[104:107], a[224:227], 0\n"
    "s_waitcnt vmcnt(11)\n"
    "ds_read_b128 v[216:219], v251 offset:0\n"
    "v_lshlrev_b32_sdwa v189, 1, v178 dst_sel:DWORD dst_unused:UNUSED_PAD src0_sel:DWORD src1_sel:WORD_1\n"
    "v_mfma_f32_16x16x32_bf16 v[168:171], a[152:155], a[228:231], v[168:171]\n"
    "ds_read_b128 v[220:223], v251 offset:336\n"
    "v_lshlrev_b32_sdwa v190, 1, v180 dst_sel:DWORD dst_unused:UNUSED_PAD src0_sel:DWORD src1_sel:WORD_1\n"
    "v_mfma_f32_16x16x32_bf16 v[168:171], a[64:67], a[232:235], v[168:171]\n"
    "ds_read_u16_d16_hi v200, v200\n"
    "s_waitcnt vmcnt(10)\n"
    "ds_write2_b32 v248, v16, v17 offset0:0 offset1:20\n"
    "v_lshlrev_b32_sdwa v191, 1, v182 dst_sel:DWORD dst_unused:UNUSED_PAD src0_sel:DWORD src1_sel:WORD_1\n"
    "v_exp_f16_sdwa v234, v232 dst_sel:WORD_1 dst_unused:UNUSED_PRESERVE src0_sel:WORD_1\n"
    "v_mfma_f32_16x16x32_bf16 v[172:175], a[108:111], a[224:227], 0\n"
    "s_waitcnt lgkmcnt(7)\n"
    "ds_read_u16 v185, v185\n"
    "v_or_b32_e64 v199, v199, v163\n"
    "v_mfma_f32_16x16x32_bf16 v[172:175], a[156:159], a[228:231], v[172:175]\n"
    "v_pk_fma_f16 v234, v234, s49, s49\n"
    "v_lshlrev_b32_sdwa v249, 1, v171 dst_sel:DWORD dst_unused:UNUSED_PAD src0_sel:DWORD src1_sel:WORD_1\n"
    "v_mfma_f32_16x16x32_bf16 v[172:175], a[68:71], a[232:235], v[172:175]\n"
    "ds_read_u16_d16_hi v201, v201\n"
    "ds_write2_b32 v248, v18, v19 offset0:40 offset1:60\n"
    "v_mfma_f32_32x32x1_2b_f32 v[0:31], v254, v254, 0\n"
    "buffer_store_dwordx4 v[228:231], v245, s[68:71], s39 offen offset:128 sc1\n"
    "ds_read_u16 v186, v186\n"
    "v_add_u32_e32 v248, -5440, v248\n"
    "v_rcp_f16_e64 v232, v234\n"
    "ds_read_u16_d16_hi v202, v202\n"
    "ds_read_b128 v[236:239], v244 offset:4096\n"
    "v_rcp_f16_sdwa v232, v234 dst_sel:WORD_1 dst_unused:UNUSED_PRESERVE src0_sel:WORD_1\n"
    "v_mfma_f32_16x16x32_bf16 v[176:179], a[104:107], a[236:239], 0\n"
    "s_nop 0\n"
    "ds_read_u16 v187, v187\n"
    "ds_read_u16_d16_hi v203, v203\n"
    "v_mfma_f32_16x16x32_bf16 v[176:179], a[152:155], a[240:243], v[176:179]\n"
    "s_waitcnt lgkmcnt(8)\n"
    "v_fma_mix_f32 v234, v196, v232, 0 op_sel_hi:[1,1,0]\n"
    "v_fma_mix_f32 v232, v196, v232, 0 op_sel:[1,1,0] op_sel_hi:[1,1,0]\n"
    "v_mfma_f32_16x16x32_bf16 v[176:179], a[64:67], a[244:247], v[176:179]\n"
    "ds_read_b128 v[164:167], v244 offset:6144\n"
    "v_cvt_pk_bf16_f32 v196, v234, v232\n"
    "v_mfma_f32_16x16x32_bf16 v[180:183], a[108:111], a[236:239], 0\n"
    "ds_read_u16 v188, v188\n"
    "v_or_b32_e64 v184, v184, v200\n"
    "v_mfma_f32_16x16x32_bf16 v[180:183], a[156:159], a[240:243], v[180:183]\n"
    "s_waitcnt lgkmcnt(7)\n"
    "v_or_b32_e32 v185, v185, v201\n"
    "v_lshlrev_b32_sdwa v255, 1, v173 dst_sel:DWORD dst_unused:UNUSED_PAD src0_sel:DWORD src1_sel:WORD_1\n"
    "v_mfma_f32_16x16x32_bf16 v[180:183], a[68:71], a[244:247], v[180:183]\n"
    "s_barrier\n"
    "v_mfma_f32_16x16x32_bf16 v[96:99], a[16:19], v[192:195], v[96:99]\n"
    "ds_read_b128 v[208:211], v250 offset:0\n"
    "ds_read_b128 v[212:215], v250 offset:320\n"
    "v_mfma_f32_16x16x32_bf16 v[100:103], a[20:23], v[192:195], v[100:103]\n"
    "s_waitcnt lgkmcnt(6)\n"
    "ds_read_u16_d16_hi v204, v204\n"
    "ds_read_u16 v189, v189\n"
    "v_mfma_f32_16x16x32_bf16 v[112:115], a[24:27], v[192:195], v[112:115]\n"
    "ds_read_u16_d16_hi v205, v205\n"
    "v_or_b32_e64 v186, v186, v202\n"
    "v_mfma_f32_16x16x32_bf16 v[116:119], a[28:31], v[192:195], v[116:119]\n"
    "v_lshlrev_b32_sdwa v160, 1, v175 dst_sel:DWORD dst_unused:UNUSED_PAD src0_sel:DWORD src1_sel:WORD_1\n"
    "v_lshlrev_b32_sdwa v161, 1, v179 dst_sel:DWORD dst_unused:UNUSED_PAD src0_sel:DWORD src1_sel:WORD_1\n"
    "v_mfma_f32_16x16x32_bf16 v[128:131], v[236:239], v[192:195], v[128:131]\n"
    "ds_read_u16 v190, v190\n"
    "v_lshlrev_b32_sdwa v162, 1, v181 dst_sel:DWORD dst_unused:UNUSED_PAD src0_sel:DWORD src1_sel:WORD_1\n"
    "v_mfma_f32_16x16x32_bf16 v[104:107], a[16:19], v[196:199], v[104:107]\n"
    "s_waitcnt lgkmcnt(7)\n"
    "ds_read_u16_d16_hi v206, v206\n"
    "v_or_b32_e32 v187, v187, v203\n"
    "v_mfma_f32_16x16x32_bf16 v[132:135], v[164:167], v[192:195], v[132:135]\n"
    "v_cvt_pk_f16_f32 v192, v168, v169\n"
    "v_lshlrev_b32_sdwa v193, 1, v170 dst_sel:DWORD dst_unused:UNUSED_PAD src0_sel:DWORD src1_sel:WORD_1\n"
    "v_mfma_f32_16x16x32_bf16 v[108:111], a[20:23], v[196:199], v[108:111]\n"
    "s_waitcnt vmcnt(6)\n"
    "ds_read_u16 v191, v191\n"
    "v_lshlrev_b32_sdwa v194, 1, v172 dst_sel:DWORD dst_unused:UNUSED_PAD src0_sel:DWORD src1_sel:WORD_1\n"
    "v_mfma_f32_16x16x32_bf16 v[120:123], a[24:27], v[196:199], v[120:123]\n"
    "v_lshlrev_b32_sdwa v195, 1, v174 dst_sel:DWORD dst_unused:UNUSED_PAD src0_sel:DWORD src1_sel:WORD_1\n"
    "v_lshlrev_b32_sdwa v163, 1, v183 dst_sel:DWORD dst_unused:UNUSED_PAD src0_sel:DWORD src1_sel:WORD_1\n"
    "v_mfma_f32_16x16x32_bf16 v[124:127], a[28:31], v[196:199], v[124:127]\n"
    "s_waitcnt lgkmcnt(6)\n"
    "ds_read_u16_d16_hi v207, v207\n"
    "v_pk_fma_f16 v232, v192, v192, s48\n"
    "v_smfmac_f32_16x16x64_bf16 v[0:3], v[216:219], v[208:215], v240\n"
    "ds_read_b128 v[216:219], v252 offset:0\n"
    "v_smfmac_f32_16x16x64_bf16 v[4:7], v[220:223], v[208:215], v240 abid:1\n"
    "ds_read_b128 v[208:211], v250 offset:640\n"
    "v_mfma_f32_16x16x32_bf16 v[136:139], v[236:239], v[196:199], v[136:139]\n"
    "s_waitcnt lgkmcnt(8)\n"
    "v_or_b32_e32 v188, v188, v204\n"
    "v_pk_mul_f16 v232, v192, v232\n"
    "v_mfma_f32_16x16x32_bf16 v[140:143], v[164:167], v[196:199], v[140:143]\n"
    "ds_read_b128 v[212:215], v250 offset:960\n"
    "ds_read_b128 v[220:223], v252 offset:336\n"
    "v_cvt_pk_f16_f32 v196, v176, v177\n"
    "v_smfmac_f32_32x32x32_bf16 v[112:127], a[248:251], v[152:159], v241\n"
    "ds_read_u16 v193, v193\n"
    "v_lshlrev_b32_sdwa v197, 1, v178 dst_sel:DWORD dst_unused:UNUSED_PAD src0_sel:DWORD src1_sel:WORD_1\n"
    "v_lshlrev_b32_sdwa v198, 1, v180 dst_sel:DWORD dst_unused:UNUSED_PAD src0_sel:DWORD src1_sel:WORD_1\n"
    "v_lshlrev_b32_sdwa v199, 1, v182 dst_sel:DWORD dst_unused:UNUSED_PAD src0_sel:DWORD src1_sel:WORD_1\n"
    "v_exp_f16_e64 v234, v232\n"
    "s_waitcnt vmcnt(5)\n"
    "v_smfmac_f32_32x32x32_bf16 v[96:111], a[248:251], v[144:151], v241\n"
    "s_waitcnt lgkmcnt(9)\n"
    "ds_read_b128 a[252:255], v244 offset:34816\n"
    "ds_read_u16_d16_hi v249, v249\n"
    "v_or_b32_e64 v189, v189, v205\n"
    "v_exp_f16_sdwa v234, v232 dst_sel:WORD_1 dst_unused:UNUSED_PRESERVE src0_sel:WORD_1\n"
    "v_mfma_f32_16x16x32_bf16 v[168:171], a[112:115], a[224:227], 0\n"
    "buffer_load_dwordx4 v[144:147], v245, s[40:43], s57 offen offset:128\n"
    "ds_read_u16 v194, v194\n"
    "v_mfma_f32_16x16x32_bf16 v[168:171], a[160:163], a[228:231], v[168:171]\n"
    "s_waitcnt lgkmcnt(10)\n"
    "v_or_b32_e32 v190, v190, v206\n"
    "v_pk_fma_f16 v234, v234, s49, s49\n"
    "v_mfma_f32_16x16x32_bf16 v[168:171], a[72:75], a[232:235], v[168:171]\n"
    "ds_read_u16_d16_hi v255, v255\n"
    "v_rcp_f16_e32 v232, v234\n"
    "v_mfma_f32_16x16x32_bf16 v[172:175], a[116:119], a[224:227], 0\n"
    "v_rcp_f16_sdwa v232, v234 dst_sel:WORD_1 dst_unused:UNUSED_PRESERVE src0_sel:WORD_1\n"
    "v_mfma_f32_16x16x32_bf16 v[172:175], a[164:167], a[228:231], v[172:175]\n"
    "s_waitcnt lgkmcnt(8)\n"
    "ds_read_u16 v195, v195\n"
    "v_or_b32_e64 v191, v191, v207\n"
    "v_lshlrev_b32_sdwa v200, 1, v169 dst_sel:DWORD dst_unused:UNUSED_PAD src0_sel:DWORD src1_sel:WORD_1\n"
    "v_mfma_f32_16x16x32_bf16 v[176:179], a[112:115], a[236:239], 0\n"
    "ds_read_b128 v[236:239], v244 offset:8192\n"
    "v_lshlrev_b32_sdwa v201, 1, v171 dst_sel:DWORD dst_unused:UNUSED_PAD src0_sel:DWORD src1_sel:WORD_1\n"
    "v_mfma_f32_16x16x32_bf16 v[176:179], a[160:163], a[240:243], v[176:179]\n"
    "s_waitcnt lgkmcnt(8)\n"
    "ds_read_b128 v[152:155], v242 offset:24640\n"
    "v_fma_mix_f32 v234, v192, v232, 0 op_sel_hi:[1,1,0]\n"
    "v_mfma_f32_16x16x32_bf16 v[176:179], a[72:75], a[244:247], v[176:179]\n"
    "s_waitcnt lgkmcnt(8)\n"
    "ds_read_b128 v[156:159], v242 offset:25664\n"
    "v_fma_mix_f32 v232, v192, v232, 0 op_sel:[1,1,0] op_sel_hi:[1,1,0]\n"
    "v_smfmac_f32_16x16x64_bf16 v[0:3], v[216:219], v[208:215], v240\n"
    "s_nop 0\n"
    "ds_read_b128 v[216:219], v251 offset:672\n"
    "v_smfmac_f32_16x16x64_bf16 v[4:7], v[220:223], v[208:215], v240 abid:1\n"
    "s_waitcnt lgkmcnt(8)\n"
    "ds_read_b128 v[208:211], v250 offset:80\n"
    "buffer_load_dwordx4 v[148:151], v245, s[40:43], s88 offen offset:128\n"
    "v_mfma_f32_16x16x32_bf16 v[172:175], a[252:255], a[232:235], v[172:175]\n"
    "ds_read_b128 v[212:215], v250 offset:400\n"
    "v_lshlrev_b32_sdwa v204, 1, v177 dst_sel:DWORD dst_unused:UNUSED_PAD src0_sel:DWORD src1_sel:WORD_1\n"
    "v_lshlrev_b32_sdwa v205, 1, v179 dst_sel:DWORD dst_unused:UNUSED_PAD src0_sel:DWORD src1_sel:WORD_1\n"
    "ds_read_b128 v[220:223], v251 offset:1008\n"
    "v_pk_mov_b32 v[224:225], v[0:1], v[0:1] op_sel:[0,1]\n"
    "v_pk_mov_b32 v[226:227], v[2:3], v[2:3] op_sel:[0,1]\n"
    "v_pk_mul_f32 v[228:229], v[0:1], v[0:1]\n"
    "v_pk_mul_f32 v[230:231], v[2:3], v[2:3]\n"
    "v_pk_add_f32 v[224:225], v[224:225], v[4:5]\n"
    "v_pk_add_f32 v[226:227], v[226:227], v[6:7]\n"
    "v_pk_fma_f32 v[228:229], v[4:5], v[4:5], v[228:229]\n"
    "v_pk_fma_f32 v[230:231], v[6:7], v[6:7], v[230:231]\n"
    "v_mfma_f32_16x16x32_bf16 v[180:183], a[116:119], a[236:239], 0\n"
    "s_waitcnt lgkmcnt(9)\n"
    "ds_read_u16_d16_hi v160, v160\n"
    "ds_read_b128 v[164:167], v244 offset:10240\n"
    "v_mfma_f32_16x16x32_bf16 v[180:183], a[164:167], a[240:243], v[180:183]\n"
    "s_nop 0\n"
    "ds_read_u16 v197, v197\n"
    "v_lshlrev_b32_e64 v254, 3, v244\n"
    "v_mfma_f32_16x16x32_bf16 v[180:183], a[252:255], a[244:247], v[180:183]\n"
    "s_waitcnt lgkmcnt(11)\n"
    "v_or_b32_e32 v193, v193, v249\n"
    "v_or_b32_e32 v194, v194, v255\n"
    "v_mfma_f32_16x16x32_bf16 v[112:115], a[40:43], v[184:187], v[112:115]\n"
    "s_waitcnt lgkmcnt(9)\n"
    "ds_read_u16_d16_hi v161, v161\n"
    "v_lshlrev_b32_sdwa v202, 1, v173 dst_sel:DWORD dst_unused:UNUSED_PAD src0_sel:DWORD src1_sel:WORD_1\n"
    "v_mfma_f32_16x16x32_bf16 v[116:119], a[44:47], v[184:187], v[116:119]\n"
    "ds_read_u16 v198, v198\n"
    "v_lshlrev_b32_sdwa v203, 1, v175 dst_sel:DWORD dst_unused:UNUSED_PAD src0_sel:DWORD src1_sel:WORD_1\n"
    "v_lshlrev_b32_sdwa v206, 1, v181 dst_sel:DWORD dst_unused:UNUSED_PAD src0_sel:DWORD src1_sel:WORD_1\n"
    "v_lshlrev_b32_sdwa v207, 1, v183 dst_sel:DWORD dst_unused:UNUSED_PAD src0_sel:DWORD src1_sel:WORD_1\n"
    "v_cvt_pk_bf16_f32 v192, v234, v232\n"
    "v_mfma_f32_16x16x32_bf16 v[128:131], v[236:239], v[184:187], v[128:131]\n"
    "s_waitcnt lgkmcnt(8)\n"
    "ds_read_u16_d16_hi v162, v162\n"
    "v_pk_fma_f16 v232, v196, v196, s48\n"
    "v_pk_mul_f16 v232, v196, v232\n"
    "v_exp_f16_e64 v234, v232\n"
    "v_smfmac_f32_32x32x32_bf16 v[96:111], a[248:251], v[152:159], v241\n"
    "s_waitcnt lgkmcnt(7)\n"
    "ds_read_u16 v199, v199\n"
    "v_exp_f16_sdwa v234, v232 dst_sel:WORD_1 dst_unused:UNUSED_PRESERVE src0_sel:WORD_1\n"
    "v_smfmac_f32_16x16x64_bf16 v[8:11], v[216:219], v[208:215], v240\n"
    "s_waitcnt lgkmcnt(7)\n"
    "ds_read_b128 v[216:219], v252 offset:672\n"
    "v_smfmac_f32_16x16x64_bf16 v[12:15], v[220:223], v[208:215], v240 abid:1\n"
    "ds_read_b128 v[208:211], v250 offset:720\n"
    "v_mfma_f32_16x16x32_bf16 v[120:123], a[40:43], v[188:191], v[120:123]\n"
    "s_waitcnt lgkmcnt(7)\n"
    "ds_read_b128 v[212:215], v250 offset:1040\n"
    "v_or_b32_e32 v195, v195, v160\n"
    "v_mfma_f32_16x16x32_bf16 v[96:99], a[32:35], v[184:187], v[96:99]\n"
    "s_mov_b32 exec_hi, 0\n"
    "buffer_load_dword v233, v254, s[80:83], s84 offen sc1\n"
    "s_mov_b32 exec_hi, -1\n"
    "v_mfma_f32_16x16x32_bf16 v[100:103], a[36:39], v[184:187], v[100:103]\n"
    "s_nop 0\n"
    "ds_read_b128 v[220:223], v252 offset:1008\n"
    "ds_read_u16_d16_hi v163, v163\n"
    "v_mfma_f32_16x16x32_bf16 v[132:135], v[164:167], v[184:187], v[132:135]\n"
    "v_lshlrev_b32_sdwa v184, 1, v168 dst_sel:DWORD dst_unused:UNUSED_PAD src0_sel:DWORD src1_sel:WORD_1\n"
    "v_lshlrev_b32_sdwa v185, 1, v170 dst_sel:DWORD dst_unused:UNUSED_PAD src0_sel:DWORD src1_sel:WORD_1\n"
    "v_mfma_f32_16x16x32_bf16 v[104:107], a[32:35], v[188:191], v[104:107]\n"
    "ds_read_b128 v[152:155], v242 offset:26688\n"
    "v_lshlrev_b32_sdwa v186, 1, v172 dst_sel:DWORD dst_unused:UNUSED_PAD src0_sel:DWORD src1_sel:WORD_1\n"
    "v_mfma_f32_16x16x32_bf16 v[108:111], a[36:39], v[188:191], v[108:111]\n"
    "s_waitcnt lgkmcnt(9)\n"
    "ds_read_b128 v[156:159], v242 offset:27712\n"
    "v_or_b32_e32 v197, v197, v161\n"
    "v_mfma_f32_16x16x32_bf16 v[124:127], a[44:47], v[188:191], v[124:127]\n"
    "ds_read_u16 v184, v184\n"
    "v_lshlrev_b32_sdwa v187, 1, v174 dst_sel:DWORD dst_unused:UNUSED_PAD src0_sel:DWORD src1_sel:WORD_1\n"
    "v_pk_fma_f16 v234, v234, s49, s49\n"
    "v_mfma_f32_16x16x32_bf16 v[136:139], v[236:239], v[188:191], v[136:139]\n"
    "ds_read_b128 a[252:255], v244 offset:38912\n"
    "v_rcp_f16_e32 v232, v234\n"
    "v_mfma_f32_16x16x32_bf16 v[140:143], v[164:167], v[188:191], v[140:143]\n"
    "s_waitcnt lgkmcnt(10)\n"
    "ds_read_u16_d16_hi v200, v200\n"
    "v_or_b32_e64 v198, v198, v162\n"
    "v_lshlrev_b32_sdwa v188, 1, v176 dst_sel:DWORD dst_unused:UNUSED_PAD src0_sel:DWORD src1_sel:WORD_1\n"
    "v_lshlrev_b32_sdwa v189, 1, v178 dst_sel:DWORD dst_unused:UNUSED_PAD src0_sel:DWORD src1_sel:WORD_1\n"
    "v_mfma_f32_16x16x32_bf16 v[168:171], a[120:123], a[224:227], 0\n"
    "ds_read_u16 v185, v185\n"
    "v_lshlrev_b32_sdwa v190, 1, v180 dst_sel:DWORD dst_unused:UNUSED_PAD src0_sel:DWORD src1_sel:WORD_1\n"
    "v_mfma_f32_16x16x32_bf16 v[168:171], a[168:171], a[228:231], v[168:171]\n"
    "v_lshlrev_b32_sdwa v191, 1, v182 dst_sel:DWORD dst_unused:UNUSED_PAD src0_sel:DWORD src1_sel:WORD_1\n"
    "v_mfma_f32_16x16x32_bf16 v[168:171], a[76:79], a[232:235], v[168:171]\n"
    "s_waitcnt lgkmcnt(8)\n"
    "ds_read_u16_d16_hi v201, v201\n"
    "v_rcp_f16_sdwa v232, v234 dst_sel:WORD_1 dst_unused:UNUSED_PRESERVE src0_sel:WORD_1\n"
    "v_smfmac_f32_16x16x64_bf16 v[8:11], v[216:219], v[208:215], v240\n"
    "s_waitcnt lgkmcnt(8)\n"
    "ds_read_b128 v[216:219], v251 offset:1344\n"
    "v_smfmac_f32_16x16x64_bf16 v[12:15], v[220:223], v[208:215], v240 abid:1\n"
    "ds_read_b128 v[208:211], v250 offset:160\n"
    "v_mfma_f32_16x16x32_bf16 v[172:175], a[124:127], a[224:227], 0\n"
    "v_fma_mix_f32 v234, v196, v232, 0 op_sel_hi:[1,1,0]\n"
    "v_fma_mix_f32 v232, v196, v232, 0 op_sel:[1,1,0] op_sel_hi:[1,1,0]\n"
    "v_mfma_f32_16x16x32_bf16 v[172:175], a[172:175], a[228:231], v[172:175]\n"
    "s_waitcnt lgkmcnt(8)\n"
    "ds_read_b128 v[212:215], v250 offset:480\n"
    "ds_read_b128 v[220:223], v251 offset:1680\n"
    "v_or_b32_e32 v199, v199, v163\n"
    "v_cvt_pk_bf16_f32 v196, v234, v232\n"
    "v_lshlrev_b32_sdwa v249, 1, v171 dst_sel:DWORD dst_unused:UNUSED_PAD src0_sel:DWORD src1_sel:WORD_1\n"
    "v_pk_add_f32 v[224:225], v[224:225], v[8:9]\n"
    "v_pk_add_f32 v[226:227], v[226:227], v[10:11]\n"
    "v_pk_fma_f32 v[228:229], v[8:9], v[8:9], v[228:229]\n"
    "v_pk_fma_f32 v[230:231], v[10:11], v[10:11], v[230:231]\n"
    "v_pk_add_f32 v[224:225], v[224:225], v[12:13]\n"
    "v_pk_add_f32 v[226:227], v[226:227], v[14:15]\n"
    "v_pk_fma_f32 v[228:229], v[12:13], v[12:13], v[228:229]\n"
    "v_pk_fma_f32 v[230:231], v[14:15], v[14:15], v[230:231]\n"
    "v_mfma_f32_16x16x32_bf16 v[176:179], a[120:123], a[236:239], 0\n"
    "s_waitcnt lgkmcnt(7)\n"
    "ds_read_u16 v186, v186\n"
    "ds_read_u16_d16_hi v202, v202\n"
    "v_mfma_f32_16x16x32_bf16 v[176:179], a[168:171], a[240:243], v[176:179]\n"
    "s_nop 0\n"
    "ds_read_b128 v[236:239], v244 offset:12288\n"
    "v_mfma_f32_16x16x32_bf16 v[176:179], a[76:79], a[244:247], v[176:179]\n"
    "ds_read_u16 v187, v187\n"
    "v_smfmac_f32_32x32x32_bf16 v[112:127], a[248:251], v[152:159], v241\n"
    "s_waitcnt lgkmcnt(10)\n"
    "v_or_b32_e32 v184, v184, v200\n"
    "v_mfma_f32_16x16x32_bf16 v[172:175], a[252:255], a[232:235], v[172:175]\n"
    "ds_read_u16_d16_hi v203, v203\n"
    "v_mfma_f32_16x16x32_bf16 v[180:183], a[124:127], a[236:239], 0\n"
    "s_waitcnt lgkmcnt(8)\n"
    "ds_read_b128 v[164:167], v244 offset:14336\n"
    "v_or_b32_e32 v185, v185, v201\n"
    "v_mfma_f32_16x16x32_bf16 v[180:183], a[172:175], a[240:243], v[180:183]\n"
    "ds_read_u16 v188, v188\n"
    "v_lshlrev_b32_sdwa v161, 1, v179 dst_sel:DWORD dst_unused:UNUSED_PAD src0_sel:DWORD src1_sel:WORD_1\n"
    "v_mfma_f32_16x16x32_bf16 v[180:183], a[252:255], a[244:247], v[180:183]\n"
    "s_waitcnt lgkmcnt(8)\n"
    "v_lshlrev_b32_sdwa v255, 1, v173 dst_sel:DWORD dst_unused:UNUSED_PAD src0_sel:DWORD src1_sel:WORD_1\n"
    "v_lshlrev_b32_sdwa v160, 1, v175 dst_sel:DWORD dst_unused:UNUSED_PAD src0_sel:DWORD src1_sel:WORD_1\n"
    "v_smfmac_f32_16x16x64_bf16 v[16:19], v[216:219], v[208:215], v240\n"
    "s_waitcnt lgkmcnt(7)\n"
    "ds_read_b128 v[216:219], v252 offset:1344\n"
    "ds_read_u16_d16_hi v204, v204\n"
    "v_smfmac_f32_16x16x64_bf16 v[20:23], v[220:223], v[208:215], v240 abid:1\n"
    "v_lshlrev_b32_sdwa v162, 1, v181 dst_sel:DWORD dst_unused:UNUSED_PAD src0_sel:DWORD src1_sel:WORD_1\n"
    "v_mfma_f32_16x16x32_bf16 v[96:99], a[176:179], v[192:195], v[96:99]\n"
    "ds_read_b128 v[208:211], v250 offset:800\n"
    "v_lshlrev_b32_sdwa v163, 1, v183 dst_sel:DWORD dst_unused:UNUSED_PAD src0_sel:DWORD src1_sel:WORD_1\n"
    "v_mfma_f32_16x16x32_bf16 v[100:103], a[180:183], v[192:195], v[100:103]\n"
    "ds_read_b128 v[212:215], v250 offset:1120\n"
    "v_mfma_f32_16x16x32_bf16 v[112:115], a[184:187], v[192:195], v[112:115]\n"
    "s_waitcnt lgkmcnt(8)\n"
    "ds_read_b128 v[220:223], v252 offset:1680\n"
    "v_or_b32_e32 v186, v186, v202\n"
    "v_mfma_f32_16x16x32_bf16 v[116:119], a[188:191], v[192:195], v[116:119]\n"
    "ds_read_u16 v189, v189\n"
    "v_mfma_f32_16x16x32_bf16 v[128:131], v[236:239], v[192:195], v[128:131]\n"
    "v_mfma_f32_16x16x32_bf16 v[104:107], a[176:179], v[196:199], v[104:107]\n"
    "ds_read_b128 v[152:155], v242 offset:28736\n"
    "v_mfma_f32_16x16x32_bf16 v[108:111], a[180:183], v[196:199], v[108:111]\n"
    "ds_read_b128 v[156:159], v242 offset:29760\n"
    "v_mfma_f32_16x16x32_bf16 v[120:123], a[184:187], v[196:199], v[120:123]\n"
    "s_waitcnt lgkmcnt(9)\n"
    "ds_read_u16_d16_hi v205, v205\n"
    "v_or_b32_e32 v187, v187, v203\n"
    "v_mfma_f32_16x16x32_bf16 v[132:135], v[164:167], v[192:195], v[132:135]\n"
    "v_cvt_pk_f16_f32 v192, v168, v169\n"
    "v_lshlrev_b32_sdwa v193, 1, v170 dst_sel:DWORD dst_unused:UNUSED_PAD src0_sel:DWORD src1_sel:WORD_1\n"
    "v_mfma_f32_16x16x32_bf16 v[124:127], a[188:191], v[196:199], v[124:127]\n"
    "ds_read_u16 v190, v190\n"
    "v_lshlrev_b32_sdwa v194, 1, v172 dst_sel:DWORD dst_unused:UNUSED_PAD src0_sel:DWORD src1_sel:WORD_1\n"
    "v_mfma_f32_16x16x32_bf16 v[136:139], v[236:239], v[196:199], v[136:139]\n"
    "ds_read_b128 a[224:227], v242 offset:0\n"
    "v_lshlrev_b32_sdwa v195, 1, v174 dst_sel:DWORD dst_unused:UNUSED_PAD src0_sel:DWORD src1_sel:WORD_1\n"
    "v_mfma_f32_16x16x32_bf16 v[140:143], v[164:167], v[196:199], v[140:143]\n"
    "ds_read_u16_d16_hi v206, v206\n"
    "v_cvt_pk_f16_f32 v196, v176, v177\n"
    "v_lshlrev_b32_sdwa v197, 1, v178 dst_sel:DWORD dst_unused:UNUSED_PAD src0_sel:DWORD src1_sel:WORD_1\n"
    "v_lshlrev_b32_sdwa v198, 1, v180 dst_sel:DWORD dst_unused:UNUSED_PAD src0_sel:DWORD src1_sel:WORD_1\n"
    "v_lshlrev_b32_sdwa v199, 1, v182 dst_sel:DWORD dst_unused:UNUSED_PAD src0_sel:DWORD src1_sel:WORD_1\n"
    "s_waitcnt lgkmcnt(9)\n"
    "v_or_b32_e32 v188, v188, v204\n"
    "s_waitcnt lgkmcnt(8)\n"
    "v_smfmac_f32_16x16x64_bf16 v[16:19], v[216:219], v[208:215], v240\n"
    "s_waitcnt lgkmcnt(7)\n"
    "ds_read_b128 v[216:219], v251 offset:2016\n"
    "ds_read_b128 a[228:231], v242 offset:1024\n"
    "v_smfmac_f32_16x16x64_bf16 v[20:23], v[220:223], v[208:215], v240 abid:1\n"
    "ds_read_b128 v[208:211], v250 offset:240\n"
    "v_pk_fma_f16 v232, v192, v192, s48\n"
    "v_pk_mul_f16 v232, v192, v232\n"
    "ds_read_b128 v[212:215], v250 offset:560\n"
    "v_pk_add_f32 v[224:225], v[224:225], v[16:17]\n"
    "v_pk_add_f32 v[226:227], v[226:227], v[18:19]\n"
    "v_pk_fma_f32 v[228:229], v[16:17], v[16:17], v[228:229]\n"
    "v_pk_fma_f32 v[230:231], v[18:19], v[18:19], v[230:231]\n"
    "v_pk_add_f32 v[224:225], v[224:225], v[20:21]\n"
    "v_pk_add_f32 v[226:227], v[226:227], v[22:23]\n"
    "v_pk_fma_f32 v[228:229], v[20:21], v[20:21], v[228:229]\n"
    "v_pk_fma_f32 v[230:231], v[22:23], v[22:23], v[230:231]\n"
    "s_waitcnt lgkmcnt(8)\n"
    "v_smfmac_f32_32x32x32_bf16 v[128:143], a[248:251], v[152:159], v241\n"
    "s_nop 0\n"
    "ds_read_b128 v[220:223], v251 offset:2352\n"
    "ds_read_u16 v191, v191\n"
    "v_lshlrev_b32_e64 v254, 3, v244\n"
    "ds_read_b128 a[232:235], v242 offset:2048\n"
    "ds_read_u16_d16_hi v207, v207\n"
    "s_waitcnt lgkmcnt(11)\n"
    "v_or_b32_e32 v189, v189, v205\n"
    "v_exp_f16_e32 v234, v232\n"
    "s_waitcnt lgkmcnt(9)\n"
    "v_mfma_f32_16x16x32_bf16 v[168:171], a[80:83], a[224:227], 0\n"
    "ds_read_b128 a[236:239], v242 offset:3072\n"
    "ds_read_b128 a[240:243], v242 offset:4096\n"
    "v_exp_f16_sdwa v234, v232 dst_sel:WORD_1 dst_unused:UNUSED_PRESERVE src0_sel:WORD_1\n"
    "v_mfma_f32_16x16x32_bf16 v[172:175], a[84:87], a[224:227], 0\n"
    "ds_read_b128 a[244:247], v242 offset:5120\n"
    "v_pk_fma_f16 v234, v234, s49, s49\n"
    "s_waitcnt lgkmcnt(10)\n"
    "v_or_b32_e64 v190, v190, v206\n"
    "v_mfma_f32_16x16x32_bf16 v[96:99], a[192:195], v[184:187], v[96:99]\n"
    "s_waitcnt lgkmcnt(9)\n"
    "ds_read_u16 v193, v193\n"
    "v_rcp_f16_e32 v232, v234\n"
    "v_mfma_f32_16x16x32_bf16 v[168:171], a[128:131], a[228:231], v[168:171]\n"
    "ds_read_b128 a[252:255], v244 offset:32768\n"
    "v_rcp_f16_sdwa v232, v234 dst_sel:WORD_1 dst_unused:UNUSED_PRESERVE src0_sel:WORD_1\n"
    "v_mfma_f32_16x16x32_bf16 v[172:175], a[132:135], a[228:231], v[172:175]\n"
    "s_waitcnt lgkmcnt(9)\n"
    "ds_read_u16_d16_hi v249, v249\n"
    "v_fma_mix_f32 v234, v192, v232, 0 op_sel_hi:[1,1,0]\n"
    "v_fma_mix_f32 v232, v192, v232, 0 op_sel:[1,1,0] op_sel_hi:[1,1,0]\n"
    "v_smfmac_f32_16x16x64_bf16 v[24:27], v[216:219], v[208:215], v240\n"
    "s_waitcnt lgkmcnt(9)\n"
    "ds_read_b128 v[216:219], v252 offset:2016\n"
    "s_mov_b32 exec_hi, 0\n"
    "buffer_load_dword v233, v254, s[72:75], s53 offen sc1\n"
    "s_mov_b32 exec_hi, -1\n"
    "v_smfmac_f32_16x16x64_bf16 v[28:31], v[220:223], v[208:215], v240 abid:1\n"
    "s_waitcnt lgkmcnt(8)\n"
    "ds_read_b128 v[208:211], v250 offset:880\n"
    "ds_read_b128 v[212:215], v250 offset:1200\n"
    "v_mfma_f32_16x16x32_bf16 v[168:171], a[48:51], a[232:235], v[168:171]\n"
    "ds_read_b128 v[220:223], v252 offset:2352\n"
    "v_mov_b32_e64 v254, 0\n"
    "v_mfma_f32_16x16x32_bf16 v[100:103], a[196:199], v[184:187], v[100:103]\n"
    "ds_read_u16 v194, v194\n"
    "v_cvt_pk_bf16_f32 v192, v234, v232\n"
    "v_pk_fma_f16 v232, v196, v196, s48\n"
    "v_pk_mul_f16 v232, v196, v232\n"
    "v_exp_f16_e32 v234, v232\n"
    "v_mfma_f32_16x16x32_bf16 v[112:115], a[200:203], v[184:187], v[112:115]\n"
    "s_waitcnt lgkmcnt(9)\n"
    "ds_read_u16_d16_hi v255, v255\n"
    "v_or_b32_e64 v191, v191, v207\n"
    "v_lshlrev_b32_sdwa v200, 1, v169 dst_sel:DWORD dst_unused:UNUSED_PAD src0_sel:DWORD src1_sel:WORD_1\n"
    "v_lshlrev_b32_sdwa v201, 1, v171 dst_sel:DWORD dst_unused:UNUSED_PAD src0_sel:DWORD src1_sel:WORD_1\n"
    "v_mfma_f32_16x16x32_bf16 v[176:179], a[80:83], a[236:239], 0\n"
    "s_add_u32 s60, s60, 1572864\n"
    "ds_read_u16 v195, v195\n"
    "v_mfma_f32_16x16x32_bf16 v[176:179], a[128:131], a[240:243], v[176:179]\n"
    "s_waitcnt lgkmcnt(10)\n"
    "v_exp_f16_sdwa v234, v232 dst_sel:WORD_1 dst_unused:UNUSED_PRESERVE src0_sel:WORD_1\n"
    "v_mfma_f32_16x16x32_bf16 v[176:179], a[48:51], a[244:247], v[176:179]\n"
    "s_waitcnt lgkmcnt(8)\n"
    "ds_read_b128 v[236:239], v244 offset:16384\n"
    "v_pk_fma_f16 v234, v234, s49, s49\n"
    "v_mfma_f32_16x16x32_bf16 v[172:175], a[252:255], a[232:235], v[172:175]\n"
    "s_add_u32 s61, s61, 1572864\n"
    "ds_read_u16_d16_hi v160, v160\n"
    "v_rcp_f16_e64 v232, v234\n"
    "v_mfma_f32_16x16x32_bf16 v[180:183], a[84:87], a[236:239], 0\n"
    "s_add_u32 s62, s62, 1572864\n"
    "v_lshlrev_b32_sdwa v204, 1, v177 dst_sel:DWORD dst_unused:UNUSED_PAD src0_sel:DWORD src1_sel:WORD_1\n"
    "v_lshlrev_b32_sdwa v205, 1, v179 dst_sel:DWORD dst_unused:UNUSED_PAD src0_sel:DWORD src1_sel:WORD_1\n"
    "v_mfma_f32_16x16x32_bf16 v[180:183], a[132:135], a[240:243], v[180:183]\n"
    "s_waitcnt lgkmcnt(8)\n"
    "ds_read_b128 v[164:167], v244 offset:18432\n"
    "ds_read_u16 v197, v197\n"
    "v_mfma_f32_16x16x32_bf16 v[180:183], a[252:255], a[244:247], v[180:183]\n"
    "s_add_u32 s63, s63, 1572864\n"
    "v_or_b32_e32 v193, v193, v249\n"
    "v_lshlrev_b32_sdwa v202, 1, v173 dst_sel:DWORD dst_unused:UNUSED_PAD src0_sel:DWORD src1_sel:WORD_1\n"
    "v_mfma_f32_16x16x32_bf16 v[116:119], a[204:207], v[184:187], v[116:119]\n"
    "s_waitcnt lgkmcnt(8)\n"
    "s_barrier\n"
    "v_smfmac_f32_16x16x64_bf16 v[24:27], v[216:219], v[208:215], v240\n"
    "s_waitcnt lgkmcnt(6)\n"
    "ds_read_u16_d16_hi v161, v161\n"
    "ds_write2_b32 v248, v32, v33 offset0:0 offset1:20\n"
    "v_smfmac_f32_16x16x64_bf16 v[28:31], v[220:223], v[208:215], v240 abid:1\n"
    "s_waitcnt lgkmcnt(7)\n"
    "v_or_b32_e32 v194, v194, v255\n"
    "v_mfma_f32_16x16x32_bf16 v[104:107], a[192:195], v[188:191], v[104:107]\n"
    "s_waitcnt lgkmcnt(5)\n"
    "ds_read_u16 v198, v198\n"
    "ds_write2_b32 v248, v34, v35 offset0:40 offset1:60\n"
    "v_add_u32_e32 v248, 1360, v248\n"
    "v_pk_add_f32 v[224:225], v[224:225], v[24:25]\n"
    "v_pk_add_f32 v[226:227], v[226:227], v[26:27]\n"
    "v_pk_fma_f32 v[228:229], v[24:25], v[24:25], v[228:229]\n"
    "v_pk_fma_f32 v[230:231], v[26:27], v[26:27], v[230:231]\n"
    "v_pk_add_f32 v[224:225], v[224:225], v[28:29]\n"
    "v_pk_add_f32 v[226:227], v[226:227], v[30:31]\n"
    "v_pk_fma_f32 v[228:229], v[28:29], v[28:29], v[228:229]\n"
    "v_pk_fma_f32 v[230:231], v[30:31], v[30:31], v[230:231]\n"
    "v_mfma_f32_16x16x32_bf16 v[128:131], v[236:239], v[184:187], v[128:131]\n"
    "s_waitcnt lgkmcnt(5)\n"
    "ds_read_u16_d16_hi v162, v162\n"
    "ds_write2_b32 v235, v52, v53 offset0:0 offset1:20\n"
    "v_mfma_f32_16x16x32_bf16 v[132:135], v[164:167], v[184:187], v[132:135]\n"
    "s_add_u32 s52, s52, 1572864\n"
    "s_add_u32 s53, s53, 393216\n"
    "v_or_b32_e32 v195, v195, v160\n"
    "v_mfma_f32_16x16x32_bf16 v[108:111], a[196:199], v[188:191], v[108:111]\n"
    "ds_read_u16 v199, v199\n"
    "ds_write2_b32 v235, v54, v55 offset0:40 offset1:60\n"
    "v_mfma_f32_16x16x32_bf16 v[120:123], a[200:203], v[188:191], v[120:123]\n"
    "s_waitcnt lgkmcnt(6)\n"
    "v_add_u32_e32 v235, 1360, v235\n"
    "v_or_b32_e32 v197, v197, v161\n"
    "v_mfma_f32_16x16x32_bf16 v[124:127], a[204:207], v[188:191], v[124:127]\n"
    "ds_read_u16_d16_hi v163, v163\n"
    "ds_write2_b32 v248, v36, v37 offset0:0 offset1:20\n"
    "v_mfma_f32_16x16x32_bf16 v[136:139], v[236:239], v[188:191], v[136:139]\n"
    "v_lshlrev_b32_sdwa v184, 1, v168 dst_sel:DWORD dst_unused:UNUSED_PAD src0_sel:DWORD src1_sel:WORD_1\n"
    "v_lshlrev_b32_sdwa v185, 1, v170 dst_sel:DWORD dst_unused:UNUSED_PAD src0_sel:DWORD src1_sel:WORD_1\n"
    "v_mfma_f32_16x16x32_bf16 v[140:143], v[164:167], v[188:191], v[140:143]\n"
    "ds_read_u16 v184, v184\n"
    "ds_write2_b32 v248, v38, v39 offset0:40 offset1:60\n"
    "v_add_u32_e32 v248, 1360, v248\n"
    "v_lshlrev_b32_sdwa v186, 1, v172 dst_sel:DWORD dst_unused:UNUSED_PAD src0_sel:DWORD src1_sel:WORD_1\n"
    "v_lshlrev_b32_sdwa v187, 1, v174 dst_sel:DWORD dst_unused:UNUSED_PAD src0_sel:DWORD src1_sel:WORD_1\n"
    "v_mfma_f32_16x16x32_bf16 v[168:171], a[88:91], a[224:227], 0\n"
    "v_lshlrev_b32_sdwa v203, 1, v175 dst_sel:DWORD dst_unused:UNUSED_PAD src0_sel:DWORD src1_sel:WORD_1\n"
    "v_lshlrev_b32_sdwa v188, 1, v176 dst_sel:DWORD dst_unused:UNUSED_PAD src0_sel:DWORD src1_sel:WORD_1\n"
    "v_mfma_f32_16x16x32_bf16 v[168:171], a[136:139], a[228:231], v[168:171]\n"
    "s_waitcnt lgkmcnt(6)\n"
    "ds_read_b128 a[252:255], v244 offset:36864\n"
    "v_or_b32_e32 v198, v198, v162\n"
    "v_mfma_f32_16x16x32_bf16 v[168:171], a[52:55], a[232:235], v[168:171]\n"
    "ds_read_u16_d16_hi v200, v200\n"
    "ds_write2_b32 v235, v56, v57 offset0:0 offset1:20\n"
    "v_lshlrev_b32_sdwa v189, 1, v178 dst_sel:DWORD dst_unused:UNUSED_PAD src0_sel:DWORD src1_sel:WORD_1\n"
    "v_mfma_f32_16x16x32_bf16 v[172:175], a[92:95], a[224:227], 0\n"
    "v_lshlrev_b32_sdwa v190, 1, v180 dst_sel:DWORD dst_unused:UNUSED_PAD src0_sel:DWORD src1_sel:WORD_1\n"
    "v_lshlrev_b32_sdwa v206, 1, v181 dst_sel:DWORD dst_unused:UNUSED_PAD src0_sel:DWORD src1_sel:WORD_1\n"
    "v_mfma_f32_16x16x32_bf16 v[172:175], a[140:143], a[228:231], v[172:175]\n"
    "ds_read_u16 v185, v185\n"
    "ds_write2_b32 v235, v58, v59 offset0:40 offset1:60\n"
    "v_add_u32_e32 v235, 50288, v235\n"
    "v_lshlrev_b32_sdwa v191, 1, v182 dst_sel:DWORD dst_unused:UNUSED_PAD src0_sel:DWORD src1_sel:WORD_1\n"
    "v_lshlrev_b32_sdwa v207, 1, v183 dst_sel:DWORD dst_unused:UNUSED_PAD src0_sel:DWORD src1_sel:WORD_1\n"
    "v_mfma_f32_16x16x32_bf16 v[176:179], a[88:91], a[236:239], 0\n"
    "s_waitcnt lgkmcnt(7)\n"
    "ds_read_u16_d16_hi v201, v201\n"
    "v_or_b32_e32 v199, v199, v163\n"
    "v_mfma_f32_16x16x32_bf16 v[176:179], a[136:139], a[240:243], v[176:179]\n"
    "v_rcp_f16_sdwa v232, v234 dst_sel:WORD_1 dst_unused:UNUSED_PRESERVE src0_sel:WORD_1\n"
    "v_mfma_f32_16x16x32_bf16 v[176:179], a[52:55], a[244:247], v[176:179]\n"
    "s_nop 0\n"
    "ds_read_u16 v186, v186\n"
    "ds_write2_b32 v248, v40, v41 offset0:0 offset1:20\n"
    "v_mfma_f32_16x16x32_bf16 v[180:183], a[92:95], a[236:239], 0\n"
    "s_waitcnt lgkmcnt(7)\n"
    "v_fma_mix_f32 v234, v196, v232, 0 op_sel_hi:[1,1,0]\n"
    "v_fma_mix_f32 v232, v196, v232, 0 op_sel:[1,1,0] op_sel_hi:[1,1,0]\n"
    "v_mfma_f32_16x16x32_bf16 v[180:183], a[140:143], a[240:243], v[180:183]\n"
    "ds_read_u16_d16_hi v202, v202\n"
    "v_cvt_pk_bf16_f32 v196, v234, v232\n"
    "v_mfma_f32_16x16x32_bf16 v[180:183], a[252:255], a[244:247], v[180:183]\n"
    "s_waitcnt lgkmcnt(6)\n"
    "ds_write2_b32 v248, v42, v43 offset0:40 offset1:60\n"
    "v_mfma_f32_16x16x32_bf16 v[172:175], a[252:255], a[232:235], v[172:175]\n"
    "s_nop 0\n"
    "v_add_u32_e32 v248, 1360, v248\n"
    "v_or_b32_e64 v184, v184, v200\n"
    "v_mfma_f32_16x16x32_bf16 v[96:99], a[208:211], v[192:195], v[96:99]\n"
    "ds_read_b128 v[236:239], v244 offset:20480\n"
    "ds_write2_b32 v248, v44, v45 offset0:0 offset1:20\n"
    "v_mfma_f32_16x16x32_bf16 v[100:103], a[212:215], v[192:195], v[100:103]\n"
    "v_lshlrev_b32_sdwa v249, 1, v171 dst_sel:DWORD dst_unused:UNUSED_PAD src0_sel:DWORD src1_sel:WORD_1\n"
    "v_lshlrev_b32_sdwa v161, 1, v179 dst_sel:DWORD dst_unused:UNUSED_PAD src0_sel:DWORD src1_sel:WORD_1\n"
    "v_mfma_f32_16x16x32_bf16 v[112:115], a[216:219], v[192:195], v[112:115]\n"
    "ds_read_u16 v187, v187\n"
    "ds_write2_b32 v248, v46, v47 offset0:40 offset1:60\n"
    "v_mfma_f32_16x16x32_bf16 v[116:119], a[220:223], v[192:195], v[116:119]\n"
    "s_waitcnt lgkmcnt(8)\n"
    "v_add_u32_e32 v248, 1360, v248\n"
    "v_or_b32_e32 v185, v185, v201\n"
    "v_mfma_f32_16x16x32_bf16 v[104:107], a[208:211], v[196:199], v[104:107]\n"
    "ds_read_u16_d16_hi v203, v203\n"
    "ds_write2_b32 v248, v48, v49 offset0:0 offset1:20\n"
    "v_lshlrev_b32_sdwa v255, 1, v173 dst_sel:DWORD dst_unused:UNUSED_PAD src0_sel:DWORD src1_sel:WORD_1\n"
    "v_lshlrev_b32_sdwa v160, 1, v175 dst_sel:DWORD dst_unused:UNUSED_PAD src0_sel:DWORD src1_sel:WORD_1\n"
    "v_lshlrev_b32_sdwa v162, 1, v181 dst_sel:DWORD dst_unused:UNUSED_PAD src0_sel:DWORD src1_sel:WORD_1\n"
    "v_mfma_f32_16x16x32_bf16 v[108:111], a[212:215], v[196:199], v[108:111]\n"
    "ds_read_b128 v[164:167], v244 offset:22528\n"
    "ds_write2_b32 v248, v50, v51 offset0:40 offset1:60\n"
    "v_add_u32_e32 v248, 46208, v248\n"
    "v_lshlrev_b32_sdwa v163, 1, v183 dst_sel:DWORD dst_unused:UNUSED_PAD src0_sel:DWORD src1_sel:WORD_1\n"
    "v_cvt_pk_bf16_f32 v96, v96, v97\n"
    "v_mfma_f32_32x32x1_2b_f32 v[32:63], v254, v254, 0\n"
    "s_waitcnt lgkmcnt(6)\n"
    "ds_read_u16 v188, v188\n"
    "ds_write2_b32 v248, v64, v65 offset0:0 offset1:20\n"
    "v_or_b32_e32 v186, v186, v202\n"
    "v_mov_b32_e64 v254, 0\n"
    "v_cvt_pk_bf16_f32 v97, v98, v99\n"
    "v_cvt_pk_bf16_f32 v98, v100, v101\n"
    "v_cvt_pk_bf16_f32 v99, v102, v103\n"
    "ds_write2_b32 v248, v66, v67 offset0:40 offset1:60\n"
    "buffer_store_dwordx4 v[96:99], v245, s[44:47], s57 offen offset:0 sc1\n"
    "v_mfma_f32_16x16x32_bf16 v[120:123], a[216:219], v[196:199], v[120:123]\n"
    "ds_read_u16_d16_hi v204, v204\n"
    "ds_write2_b32 v235, v84, v85 offset0:0 offset1:20\n"
    "v_add_u32_e32 v248, 1360, v248\n"
    "v_cvt_pk_bf16_f32 v152, v112, v113\n"
    "v_cvt_pk_bf16_f32 v153, v114, v115\n"
    "v_mfma_f32_16x16x32_bf16 v[124:127], a[220:223], v[196:199], v[124:127]\n"
    "s_waitcnt lgkmcnt(5)\n"
    "ds_read_u16 v189, v189\n"
    "ds_write2_b32 v235, v86, v87 offset0:40 offset1:60\n"
    "v_add_u32_e32 v235, 1360, v235\n"
    "v_or_b32_e32 v187, v187, v203\n"
    "v_cvt_pk_bf16_f32 v154, v116, v117\n"
    "s_waitcnt vmcnt(3)\n"
    "v_smfmac_f32_32x32x32_bf16 v[128:143], a[248:251], v[144:151], v241\n"
    "s_nop 0\n"
    "ds_read_u16_d16_hi v205, v205\n"
    "ds_write2_b32 v248, v68, v69 offset0:0 offset1:20\n"
    "v_cvt_pk_bf16_f32 v155, v118, v119\n"
    "v_cvt_pk_bf16_f32 v144, v104, v105\n"
    "v_cvt_pk_bf16_f32 v145, v106, v107\n"
    "v_cvt_pk_bf16_f32 v146, v108, v109\n"
    "v_cvt_pk_bf16_f32 v147, v110, v111\n"
    "ds_write2_b32 v248, v70, v71 offset0:40 offset1:60\n"
    "v_add_u32_e32 v248, 1360, v248\n"
    "v_cvt_pk_bf16_f32 v148, v120, v121\n"
    "v_mfma_f32_16x16x32_bf16 v[128:131], v[236:239], v[192:195], v[128:131]\n"
    "ds_read_u16 v190, v190\n"
    "ds_write2_b32 v235, v88, v89 offset0:0 offset1:20\n"
    "v_cvt_pk_bf16_f32 v149, v122, v123\n"
    "v_cvt_pk_bf16_f32 v150, v124, v125\n"
    "v_cvt_pk_bf16_f32 v151, v126, v127\n"
    "v_mfma_f32_16x16x32_bf16 v[132:135], v[164:167], v[192:195], v[132:135]\n"
    "s_waitcnt lgkmcnt(7)\n"
    "ds_read_u16_d16_hi v206, v206\n"
    "ds_write2_b32 v235, v90, v91 offset0:40 offset1:60\n"
    "v_add_u32_e32 v235, -53008, v235\n"
    "v_cvt_pk_f16_f32 v192, v168, v169\n"
    "v_lshlrev_b32_sdwa v193, 1, v170 dst_sel:DWORD dst_unused:UNUSED_PAD src0_sel:DWORD src1_sel:WORD_1\n"
    "v_mfma_f32_16x16x32_bf16 v[136:139], v[236:239], v[196:199], v[136:139]\n"
    "ds_write2_b32 v248, v72, v73 offset0:0 offset1:20\n"
    "v_lshlrev_b32_sdwa v194, 1, v172 dst_sel:DWORD dst_unused:UNUSED_PAD src0_sel:DWORD src1_sel:WORD_1\n"
    "v_lshlrev_b32_sdwa v195, 1, v174 dst_sel:DWORD dst_unused:UNUSED_PAD src0_sel:DWORD src1_sel:WORD_1\n"
    "v_mfma_f32_16x16x32_bf16 v[140:143], v[164:167], v[196:199], v[140:143]\n"
    "s_waitcnt lgkmcnt(5)\n"
    "ds_read_u16 v191, v191\n"
    "ds_write2_b32 v248, v74, v75 offset0:40 offset1:60\n"
    "v_add_u32_e32 v248, 1360, v248\n"
    "v_cvt_pk_f16_f32 v196, v176, v177\n"
    "v_lshlrev_b32_sdwa v197, 1, v178 dst_sel:DWORD dst_unused:UNUSED_PAD src0_sel:DWORD src1_sel:WORD_1\n"
    "v_mfma_f32_16x16x32_bf16 v[168:171], a[96:99], a[224:227], 0\n"
    "ds_read_u16_d16_hi v207, v207\n"
    "v_lshlrev_b32_sdwa v198, 1, v180 dst_sel:DWORD dst_unused:UNUSED_PAD src0_sel:DWORD src1_sel:WORD_1\n"
    "v_mfma_f32_16x16x32_bf16 v[168:171], a[144:147], a[228:231], v[168:171]\n"
    "v_lshlrev_b32_sdwa v199, 1, v182 dst_sel:DWORD dst_unused:UNUSED_PAD src0_sel:DWORD src1_sel:WORD_1\n"
    "v_or_b32_e64 v188, v188, v204\n"
    "v_mfma_f32_16x16x32_bf16 v[168:171], a[56:59], a[232:235], v[168:171]\n"
    "ds_read_u16 v193, v193\n"
    "ds_write2_b32 v248, v76, v77 offset0:0 offset1:20\n"
    "v_or_b32_e64 v189, v189, v205\n"
    "v_cvt_pk_bf16_f32 v156, v128, v129\n"
    "v_cvt_pk_bf16_f32 v157, v130, v131\n"
    "v_mfma_f32_16x16x32_bf16 v[172:175], a[100:103], a[224:227], 0\n"
    "s_waitcnt lgkmcnt(5)\n"
    "v_or_b32_e32 v190, v190, v206\n"
    "v_cvt_pk_bf16_f32 v158, v132, v133\n"
    "v_mfma_f32_16x16x32_bf16 v[172:175], a[148:151], a[228:231], v[172:175]\n"
    "ds_read_u16_d16_hi v249, v249\n"
    "v_cvt_pk_bf16_f32 v159, v134, v135\n"
    "v_mfma_f32_16x16x32_bf16 v[172:175], a[60:63], a[232:235], v[172:175]\n"
    "ds_read_u16 v194, v194\n"
    "ds_write2_b32 v248, v78, v79 offset0:40 offset1:60\n"
    "v_mfma_f32_16x16x32_bf16 v[176:179], a[96:99], a[236:239], 0\n"
    "v_add_u32_e32 v248, 1360, v248\n"
    "v_pk_fma_f16 v232, v192, v192, s48\n"
    "v_mfma_f32_16x16x32_bf16 v[176:179], a[144:147], a[240:243], v[176:179]\n"
    "v_cvt_pk_bf16_f32 v136, v136, v137\n"
    "v_cvt_pk_bf16_f32 v137, v138, v139\n"
    "v_mfma_f32_16x16x32_bf16 v[176:179], a[56:59], a[244:247], v[176:179]\n"
    "ds_read_u16_d16_hi v255, v255\n"
    "ds_write2_b32 v248, v80, v81 offset0:0 offset1:20\n"
    "v_cvt_pk_bf16_f32 v138, v140, v141\n"
    "v_cvt_pk_bf16_f32 v139, v142, v143\n"
    "v_pk_mul_f16 v232, v192, v232\n"
    "v_mfma_f32_16x16x32_bf16 v[180:183], a[100:103], a[236:239], 0\n"
    "s_waitcnt lgkmcnt(7)\n"
    "ds_read_u16 v195, v195\n"
    "v_or_b32_e32 v191, v191, v207\n"
    "v_mfma_f32_16x16x32_bf16 v[180:183], a[148:151], a[240:243], v[180:183]\n"
    "v_exp_f16_e32 v234, v232\n"
    "v_mfma_f32_16x16x32_bf16 v[180:183], a[60:63], a[244:247], v[180:183]\n"
    "ds_read_b128 v[236:239], v244 offset:0\n"
    "ds_write2_b32 v248, v82, v83 offset0:40 offset1:60\n"
    "v_mfma_f32_32x32x1_2b_f32 v[64:95], v254, v254, 0\n"
    "s_waitcnt lgkmcnt(7)\n"
    "buffer_store_dwordx4 v[136:139], v245, s[44:47], s88 offen offset:128 sc1\n"
    "ds_read_u16_d16_hi v160, v160\n"
    "ds_read_b128 v[216:219], v251 offset:10752\n"
    "v_add_u32_e32 v248, -57088, v248\n"
    "ds_read_b128 v[220:223], v251 offset:11088\n"
    "ds_read_b128 v[164:167], v244 offset:2048\n"
    "v_or_b32_e32 v193, v193, v249\n"
    "v_exp_f16_sdwa v234, v232 dst_sel:WORD_1 dst_unused:UNUSED_PRESERVE src0_sel:WORD_1\n"
    "v_mfma_f32_16x16x32_bf16 v[96:99], a[0:3], v[184:187], 0\n"
    "s_waitcnt lgkmcnt(7)\n"
    "ds_read_u16 v197, v197\n"
    "ds_read_u16_d16_hi v161, v161\n"
    "v_mfma_f32_16x16x32_bf16 v[100:103], a[4:7], v[184:187], 0\n"
    "v_or_b32_e64 v194, v194, v255\n"
    "v_pk_fma_f16 v234, v234, s49, s49\n"
    "v_mfma_f32_16x16x32_bf16 v[112:115], a[8:11], v[184:187], 0\n"
    "ds_read_u16 v198, v198\n"
    "v_lshlrev_b32_sdwa v200, 1, v169 dst_sel:DWORD dst_unused:UNUSED_PAD src0_sel:DWORD src1_sel:WORD_1\n"
    "v_mfma_f32_16x16x32_bf16 v[116:119], a[12:15], v[184:187], 0\n"
    "s_waitcnt lgkmcnt(7)\n"
    "s_barrier\n"
    "v_mfma_f32_16x16x32_bf16 v[128:131], v[236:239], v[184:187], 0\n"
    "ds_read_b128 v[208:211], v250 offset:0\n"
    "ds_read_b128 v[212:215], v250 offset:320\n"
    "v_mfma_f32_16x16x32_bf16 v[104:107], a[0:3], v[188:191], 0\n"
    "s_waitcnt lgkmcnt(6)\n"
    "ds_read_u16_d16_hi v162, v162\n"
    "ds_read_u16 v199, v199\n"
    "v_mfma_f32_16x16x32_bf16 v[108:111], a[4:7], v[188:191], 0\n"
    "s_waitcnt lgkmcnt(7)\n"
    "ds_read_u16_d16_hi v163, v163\n"
    "v_or_b32_e64 v195, v195, v160\n"
    "v_mfma_f32_16x16x32_bf16 v[132:135], v[164:167], v[184:187], 0\n"
    "v_lshlrev_b32_sdwa v184, 1, v168 dst_sel:DWORD dst_unused:UNUSED_PAD src0_sel:DWORD src1_sel:WORD_1\n"
    "v_lshlrev_b32_sdwa v185, 1, v170 dst_sel:DWORD dst_unused:UNUSED_PAD src0_sel:DWORD src1_sel:WORD_1\n"
    "v_mfma_f32_16x16x32_bf16 v[120:123], a[8:11], v[188:191], 0\n"
    "ds_read_u16 v184, v184\n"
    "v_lshlrev_b32_sdwa v201, 1, v171 dst_sel:DWORD dst_unused:UNUSED_PAD src0_sel:DWORD src1_sel:WORD_1\n"
    "v_mfma_f32_16x16x32_bf16 v[124:127], a[12:15], v[188:191], 0\n"
    "v_lshlrev_b32_sdwa v186, 1, v172 dst_sel:DWORD dst_unused:UNUSED_PAD src0_sel:DWORD src1_sel:WORD_1\n"
    "v_lshlrev_b32_sdwa v202, 1, v173 dst_sel:DWORD dst_unused:UNUSED_PAD src0_sel:DWORD src1_sel:WORD_1\n"
    "v_mfma_f32_16x16x32_bf16 v[136:139], v[236:239], v[188:191], 0\n"
    "ds_read_u16_d16_hi v200, v200\n"
    "v_lshlrev_b32_sdwa v187, 1, v174 dst_sel:DWORD dst_unused:UNUSED_PAD src0_sel:DWORD src1_sel:WORD_1\n"
    "v_mfma_f32_16x16x32_bf16 v[140:143], v[164:167], v[188:191], 0\n"
    "s_waitcnt lgkmcnt(8)\n"
    "ds_read_u16 v185, v185\n"
    "v_or_b32_e32 v197, v197, v161\n"
    "v_mfma_f32_16x16x32_bf16 v[168:171], a[104:107], a[224:227], 0\n"
    "v_lshlrev_b32_sdwa v203, 1, v175 dst_sel:DWORD dst_unused:UNUSED_PAD src0_sel:DWORD src1_sel:WORD_1\n"
    "v_lshlrev_b32_sdwa v188, 1, v176 dst_sel:DWORD dst_unused:UNUSED_PAD src0_sel:DWORD src1_sel:WORD_1\n"
    "v_mfma_f32_16x16x32_bf16 v[168:171], a[152:155], a[228:231], v[168:171]\n"
    "ds_read_u16_d16_hi v201, v201\n"
    "v_rcp_f16_e32 v232, v234\n"
    "v_mfma_f32_16x16x32_bf16 v[168:171], a[64:67], a[232:235], v[168:171]\n"
    "s_waitcnt lgkmcnt(7)\n"
    "v_rcp_f16_sdwa v232, v234 dst_sel:WORD_1 dst_unused:UNUSED_PRESERVE src0_sel:WORD_1\n"
    "v_smfmac_f32_16x16x64_bf16 v[32:35], v[216:219], v[208:215], v240\n"
    "ds_read_b128 v[216:219], v252 offset:10752\n"
    "ds_read_u16 v186, v186\n"
    "v_smfmac_f32_16x16x64_bf16 v[36:39], v[220:223], v[208:215], v240 abid:1\n"
    "s_waitcnt lgkmcnt(8)\n"
    "v_or_b32_e32 v198, v198, v162\n"
    "v_mfma_f32_16x16x32_bf16 v[172:175], a[108:111], a[224:227], 0\n"
    "ds_read_b128 v[208:211], v250 offset:640\n"
    "v_lshlrev_b32_sdwa v204, 1, v177 dst_sel:DWORD dst_unused:UNUSED_PAD src0_sel:DWORD src1_sel:WORD_1\n"
    "v_lshlrev_b32_sdwa v189, 1, v178 dst_sel:DWORD dst_unused:UNUSED_PAD src0_sel:DWORD src1_sel:WORD_1\n"
    "v_lshlrev_b32_sdwa v205, 1, v179 dst_sel:DWORD dst_unused:UNUSED_PAD src0_sel:DWORD src1_sel:WORD_1\n"
    "v_lshlrev_b32_sdwa v190, 1, v180 dst_sel:DWORD dst_unused:UNUSED_PAD src0_sel:DWORD src1_sel:WORD_1\n"
    "v_lshlrev_b32_sdwa v206, 1, v181 dst_sel:DWORD dst_unused:UNUSED_PAD src0_sel:DWORD src1_sel:WORD_1\n"
    "v_lshlrev_b32_sdwa v191, 1, v182 dst_sel:DWORD dst_unused:UNUSED_PAD src0_sel:DWORD src1_sel:WORD_1\n"
    "v_lshlrev_b32_sdwa v207, 1, v183 dst_sel:DWORD dst_unused:UNUSED_PAD src0_sel:DWORD src1_sel:WORD_1\n"
    "v_fma_mix_f32 v234, v192, v232, 0 op_sel_hi:[1,1,0]\n"
    "v_mfma_f32_16x16x32_bf16 v[172:175], a[156:159], a[228:231], v[172:175]\n"
    "s_waitcnt lgkmcnt(7)\n"
    "ds_read_b128 v[212:215], v250 offset:960\n"
    "ds_read_b128 v[220:223], v252 offset:11088\n"
    "v_mfma_f32_16x16x32_bf16 v[172:175], a[68:71], a[232:235], v[172:175]\n"
    "s_nop 0\n"
    "ds_read_u16_d16_hi v202, v202\n"
    "v_or_b32_e64 v199, v199, v163\n"
    "v_fma_mix_f32 v232, v192, v232, 0 op_sel:[1,1,0] op_sel_hi:[1,1,0]\n"
    "v_mfma_f32_16x16x32_bf16 v[176:179], a[104:107], a[236:239], 0\n"
    "s_nop 0\n"
    "ds_read_b128 v[236:239], v244 offset:4096\n"
    "v_cvt_pk_bf16_f32 v192, v234, v232\n"
    "v_mfma_f32_16x16x32_bf16 v[176:179], a[152:155], a[240:243], v[176:179]\n"
    "s_waitcnt lgkmcnt(9)\n"
    "ds_read_u16 v187, v187\n"
    "v_pk_fma_f16 v232, v196, v196, s48\n"
    "v_mfma_f32_16x16x32_bf16 v[176:179], a[64:67], a[244:247], v[176:179]\n"
    "v_pk_mul_f16 v232, v196, v232\n"
    "v_or_b32_e64 v184, v184, v200\n"
    "v_mfma_f32_16x16x32_bf16 v[180:183], a[108:111], a[236:239], 0\n"
    "ds_read_u16_d16_hi v203, v203\n"
    "v_lshlrev_b32_sdwa v249, 1, v171 dst_sel:DWORD dst_unused:UNUSED_PAD src0_sel:DWORD src1_sel:WORD_1\n"
    "v_mfma_f32_16x16x32_bf16 v[180:183], a[156:159], a[240:243], v[180:183]\n"
    "ds_read_b128 v[164:167], v244 offset:6144\n"
    "v_exp_f16_e32 v234, v232\n"
    "v_mfma_f32_16x16x32_bf16 v[180:183], a[68:71], a[244:247], v[180:183]\n"
    "s_waitcnt lgkmcnt(9)\n"
    "ds_read_u16 v188, v188\n"
    "v_exp_f16_sdwa v234, v232 dst_sel:WORD_1 dst_unused:UNUSED_PRESERVE src0_sel:WORD_1\n"
    "v_or_b32_e64 v185, v185, v201\n"
    "v_pk_fma_f16 v234, v234, s49, s49\n"
    "v_mfma_f32_16x16x32_bf16 v[96:99], a[16:19], v[192:195], v[96:99]\n"
    "ds_read_u16_d16_hi v204, v204\n"
    "v_rcp_f16_e64 v232, v234\n"
    "v_lshlrev_b32_sdwa v255, 1, v173 dst_sel:DWORD dst_unused:UNUSED_PAD src0_sel:DWORD src1_sel:WORD_1\n"
    "v_mfma_f32_16x16x32_bf16 v[100:103], a[20:23], v[192:195], v[100:103]\n"
    "s_waitcnt lgkmcnt(8)\n"
    "ds_read_u16 v189, v189\n"
    "v_rcp_f16_sdwa v232, v234 dst_sel:WORD_1 dst_unused:UNUSED_PRESERVE src0_sel:WORD_1\n"
    "v_smfmac_f32_16x16x64_bf16 v[32:35], v[216:219], v[208:215], v240\n"
    "s_waitcnt lgkmcnt(8)\n"
    "v_fma_mix_f32 v234, v196, v232, 0 op_sel_hi:[1,1,0]\n"
    "v_smfmac_f32_16x16x64_bf16 v[36:39], v[220:223], v[208:215], v240 abid:1\n"
    "ds_read_b128 v[208:211], v250 offset:80\n"
    "ds_read_b128 v[212:215], v250 offset:400\n"
    "v_fma_mix_f32 v232, v196, v232, 0 op_sel:[1,1,0] op_sel_hi:[1,1,0]\n"
    "v_mfma_f32_16x16x32_bf16 v[112:115], a[24:27], v[192:195], v[112:115]\n"
    "s_waitcnt lgkmcnt(8)\n"
    "ds_read_b128 v[216:219], v251 offset:11424\n"
    "v_cvt_pk_bf16_f32 v196, v234, v232\n"
    "v_or_b32_e32 v186, v186, v202\n"
    "v_lshlrev_b32_sdwa v160, 1, v175 dst_sel:DWORD dst_unused:UNUSED_PAD src0_sel:DWORD src1_sel:WORD_1\n"
    "ds_read_b128 v[220:223], v251 offset:11760\n"
    "buffer_store_dwordx4 v[152:155], v245, s[44:47], s57 offen offset:64 sc1\n"
    "v_pk_add_f32 v[224:225], v[224:225], v[32:33]\n"
    "v_pk_add_f32 v[226:227], v[226:227], v[34:35]\n"
    "v_pk_fma_f32 v[228:229], v[32:33], v[32:33], v[228:229]\n"
    "v_pk_fma_f32 v[230:231], v[34:35], v[34:35], v[230:231]\n"
    "v_pk_add_f32 v[224:225], v[224:225], v[36:37]\n"
    "v_pk_add_f32 v[226:227], v[226:227], v[38:39]\n"
    "v_pk_fma_f32 v[228:229], v[36:37], v[36:37], v[228:229]\n"
    "v_pk_fma_f32 v[230:231], v[38:39], v[38:39], v[230:231]\n"
    "v_mfma_f32_16x16x32_bf16 v[116:119], a[28:31], v[192:195], v[116:119]\n"
    "s_waitcnt lgkmcnt(7)\n"
    "ds_read_u16_d16_hi v205, v205\n"
    "ds_read_u16 v190, v190\n"
    "v_mfma_f32_16x16x32_bf16 v[128:131], v[236:239], v[192:195], v[128:131]\n"
    "s_nop 0\n"
    "ds_read_u16_d16_hi v206, v206\n"
    "v_lshlrev_b32_sdwa v161, 1, v179 dst_sel:DWORD dst_unused:UNUSED_PAD src0_sel:DWORD src1_sel:WORD_1\n"
    "v_mfma_f32_16x16x32_bf16 v[132:135], v[164:167], v[192:195], v[132:135]\n"
    "s_nop 0\n"
    "ds_read_u16 v191, v191\n"
    "v_cvt_pk_f16_f32 v192, v168, v169\n"
    "v_mfma_f32_16x16x32_bf16 v[104:107], a[16:19], v[196:199], v[104:107]\n"
    "s_waitcnt lgkmcnt(9)\n"
    "v_lshlrev_b32_sdwa v193, 1, v170 dst_sel:DWORD dst_unused:UNUSED_PAD src0_sel:DWORD src1_sel:WORD_1\n"
    "v_lshlrev_b32_sdwa v194, 1, v172 dst_sel:DWORD dst_unused:UNUSED_PAD src0_sel:DWORD src1_sel:WORD_1\n"
    "v_mfma_f32_16x16x32_bf16 v[108:111], a[20:23], v[196:199], v[108:111]\n"
    "ds_read_u16_d16_hi v207, v207\n"
    "v_lshlrev_b32_sdwa v195, 1, v174 dst_sel:DWORD dst_unused:UNUSED_PAD src0_sel:DWORD src1_sel:WORD_1\n"
    "v_mfma_f32_16x16x32_bf16 v[120:123], a[24:27], v[196:199], v[120:123]\n"
    "v_lshlrev_b32_sdwa v162, 1, v181 dst_sel:DWORD dst_unused:UNUSED_PAD src0_sel:DWORD src1_sel:WORD_1\n"
    "v_or_b32_e64 v187, v187, v203\n"
    "v_mfma_f32_16x16x32_bf16 v[124:127], a[28:31], v[196:199], v[124:127]\n"
    "ds_read_u16 v193, v193\n"
    "v_lshlrev_b32_sdwa v163, 1, v183 dst_sel:DWORD dst_unused:UNUSED_PAD src0_sel:DWORD src1_sel:WORD_1\n"
    "v_mfma_f32_16x16x32_bf16 v[136:139], v[236:239], v[196:199], v[136:139]\n"
    "s_waitcnt lgkmcnt(7)\n"
    "ds_read_b128 a[252:255], v244 offset:34816\n"
    "v_or_b32_e64 v188, v188, v204\n"
    "v_smfmac_f32_16x16x64_bf16 v[40:43], v[216:219], v[208:215], v240\n"
    "s_waitcnt lgkmcnt(7)\n"
    "ds_read_b128 v[216:219], v252 offset:11424\n"
    "v_smfmac_f32_16x16x64_bf16 v[44:47], v[220:223], v[208:215], v240 abid:1\n"
    "ds_read_b128 v[208:211], v250 offset:720\n"
    "v_mfma_f32_16x16x32_bf16 v[140:143], v[164:167], v[196:199], v[140:143]\n"
    "s_nop 0\n"
    "ds_read_b128 v[212:215], v250 offset:1040\n"
    "v_cvt_pk_f16_f32 v196, v176, v177\n"
    "v_mfma_f32_16x16x32_bf16 v[168:171], a[112:115], a[224:227], 0\n"
    "s_waitcnt lgkmcnt(9)\n"
    "ds_read_b128 v[220:223], v252 offset:11760\n"
    "v_lshlrev_b32_sdwa v197, 1, v178 dst_sel:DWORD dst_unused:UNUSED_PAD src0_sel:DWORD src1_sel:WORD_1\n"
    "v_mfma_f32_16x16x32_bf16 v[168:171], a[160:163], a[228:231], v[168:171]\n"
    "ds_read_u16_d16_hi v249, v249\n"
    "v_lshlrev_b32_sdwa v198, 1, v180 dst_sel:DWORD dst_unused:UNUSED_PAD src0_sel:DWORD src1_sel:WORD_1\n"
    "v_mfma_f32_16x16x32_bf16 v[168:171], a[72:75], a[232:235], v[168:171]\n"
    "v_lshlrev_b32_sdwa v199, 1, v182 dst_sel:DWORD dst_unused:UNUSED_PAD src0_sel:DWORD src1_sel:WORD_1\n"
    "v_or_b32_e32 v189, v189, v205\n"
    "v_mfma_f32_16x16x32_bf16 v[172:175], a[116:119], a[224:227], 0\n"
    "s_waitcnt lgkmcnt(9)\n"
    "ds_read_u16 v194, v194\n"
    "v_or_b32_e64 v190, v190, v206\n"
    "v_mfma_f32_16x16x32_bf16 v[172:175], a[164:167], a[228:231], v[172:175]\n"
    "v_pk_fma_f16 v232, v192, v192, s48\n"
    "v_lshlrev_b32_sdwa v200, 1, v169 dst_sel:DWORD dst_unused:UNUSED_PAD src0_sel:DWORD src1_sel:WORD_1\n"
    "v_mfma_f32_16x16x32_bf16 v[176:179], a[112:115], a[236:239], 0\n"
    "ds_read_u16_d16_hi v255, v255\n"
    "v_pk_mul_f16 v232, v192, v232\n"
    "v_mfma_f32_16x16x32_bf16 v[176:179], a[160:163], a[240:243], v[176:179]\n"
    "v_exp_f16_e32 v234, v232\n"
    "v_mfma_f32_16x16x32_bf16 v[176:179], a[72:75], a[244:247], v[176:179]\n"
    "s_waitcnt lgkmcnt(9)\n"
    "ds_read_u16 v195, v195\n"
    "v_or_b32_e32 v191, v191, v207\n"
    "v_mfma_f32_16x16x32_bf16 v[180:183], a[116:119], a[236:239], 0\n"
    "s_waitcnt lgkmcnt(8)\n"
    "ds_read_b128 v[236:239], v244 offset:8192\n"
    "v_lshlrev_b32_sdwa v201, 1, v171 dst_sel:DWORD dst_unused:UNUSED_PAD src0_sel:DWORD src1_sel:WORD_1\n"
    "v_mfma_f32_16x16x32_bf16 v[180:183], a[164:167], a[240:243], v[180:183]\n"
    "ds_read_u16_d16_hi v160, v160\n"
    "v_lshlrev_b32_sdwa v204, 1, v177 dst_sel:DWORD dst_unused:UNUSED_PAD src0_sel:DWORD src1_sel:WORD_1\n"
    "v_mfma_f32_16x16x32_bf16 v[180:183], a[252:255], a[244:247], v[180:183]\n"
    "v_exp_f16_sdwa v234, v232 dst_sel:WORD_1 dst_unused:UNUSED_PRESERVE src0_sel:WORD_1\n"
    "v_mfma_f32_16x16x32_bf16 v[172:175], a[252:255], a[232:235], v[172:175]\n"
    "s_waitcnt lgkmcnt(7)\n"
    "ds_read_b128 v[164:167], v244 offset:10240\n"
    "v_pk_fma_f16 v234, v234, s49, s49\n"
    "v_smfmac_f32_16x16x64_bf16 v[40:43], v[216:219], v[208:215], v240\n"
    "s_waitcnt lgkmcnt(7)\n"
    "ds_read_b128 v[216:219], v251 offset:12096\n"
    "v_smfmac_f32_16x16x64_bf16 v[44:47], v[220:223], v[208:215], v240 abid:1\n"
    "ds_read_b128 v[208:211], v250 offset:160\n"
    "v_mfma_f32_16x16x32_bf16 v[96:99], a[32:35], v[184:187], v[96:99]\n"
    "s_nop 0\n"
    "ds_read_b128 v[212:215], v250 offset:480\n"
    "v_lshlrev_b32_sdwa v202, 1, v173 dst_sel:DWORD dst_unused:UNUSED_PAD src0_sel:DWORD src1_sel:WORD_1\n"
    "v_mfma_f32_16x16x32_bf16 v[100:103], a[36:39], v[184:187], v[100:103]\n"
    "s_waitcnt lgkmcnt(9)\n"
    "ds_read_b128 v[220:223], v251 offset:12432\n"
    "buffer_store_dwordx4 v[156:159], v245, s[44:47], s57 offen offset:128 sc1\n"
    "v_pk_add_f32 v[224:225], v[224:225], v[40:41]\n"
    "v_pk_add_f32 v[226:227], v[226:227], v[42:43]\n"
    "v_pk_fma_f32 v[228:229], v[40:41], v[40:41], v[228:229]\n"
    "v_pk_fma_f32 v[230:231], v[42:43], v[42:43], v[230:231]\n"
    "v_pk_add_f32 v[224:225], v[224:225], v[44:45]\n"
    "v_pk_add_f32 v[226:227], v[226:227], v[46:47]\n"
    "v_pk_fma_f32 v[228:229], v[44:45], v[44:45], v[228:229]\n"
    "v_pk_fma_f32 v[230:231], v[46:47], v[46:47], v[230:231]\n"
    "v_mfma_f32_16x16x32_bf16 v[112:115], a[40:43], v[184:187], v[112:115]\n"
    "s_waitcnt lgkmcnt(8)\n"
    "ds_read_u16 v197, v197\n"
    "ds_read_u16_d16_hi v161, v161\n"
    "v_mfma_f32_16x16x32_bf16 v[116:119], a[44:47], v[184:187], v[116:119]\n"
    "s_waitcnt lgkmcnt(8)\n"
    "ds_read_u16 v198, v198\n"
    "v_or_b32_e64 v193, v193, v249\n"
    "v_mfma_f32_16x16x32_bf16 v[128:131], v[236:239], v[184:187], v[128:131]\n"
    "ds_read_u16_d16_hi v162, v162\n"
    "v_or_b32_e64 v194, v194, v255\n"
    "v_mfma_f32_16x16x32_bf16 v[104:107], a[32:35], v[188:191], v[104:107]\n"
    "v_lshlrev_b32_sdwa v203, 1, v175 dst_sel:DWORD dst_unused:UNUSED_PAD src0_sel:DWORD src1_sel:WORD_1\n"
    "v_rcp_f16_e32 v232, v234\n"
    "v_mfma_f32_16x16x32_bf16 v[108:111], a[36:39], v[188:191], v[108:111]\n"
    "s_waitcnt lgkmcnt(8)\n"
    "ds_read_u16 v199, v199\n"
    "v_or_b32_e64 v195, v195, v160\n"
    "v_mfma_f32_16x16x32_bf16 v[132:135], v[164:167], v[184:187], v[132:135]\n"
    "v_lshlrev_b32_sdwa v184, 1, v168 dst_sel:DWORD dst_unused:UNUSED_PAD src0_sel:DWORD src1_sel:WORD_1\n"
    "v_lshlrev_b32_sdwa v185, 1, v170 dst_sel:DWORD dst_unused:UNUSED_PAD src0_sel:DWORD src1_sel:WORD_1\n"
    "v_mfma_f32_16x16x32_bf16 v[120:123], a[40:43], v[188:191], v[120:123]\n"
    "ds_read_u16_d16_hi v163, v163\n"
    "v_lshlrev_b32_sdwa v186, 1, v172 dst_sel:DWORD dst_unused:UNUSED_PAD src0_sel:DWORD src1_sel:WORD_1\n"
    "v_mfma_f32_16x16x32_bf16 v[124:127], a[44:47], v[188:191], v[124:127]\n"
    "s_waitcnt lgkmcnt(7)\n"
    "ds_read_u16 v184, v184\n"
    "v_lshlrev_b32_sdwa v187, 1, v174 dst_sel:DWORD dst_unused:UNUSED_PAD src0_sel:DWORD src1_sel:WORD_1\n"
    "v_rcp_f16_sdwa v232, v234 dst_sel:WORD_1 dst_unused:UNUSED_PRESERVE src0_sel:WORD_1\n"
    "v_smfmac_f32_16x16x64_bf16 v[48:51], v[216:219], v[208:215], v240\n"
    "s_waitcnt lgkmcnt(7)\n"
    "ds_read_b128 v[216:219], v252 offset:12096\n"
    "v_smfmac_f32_16x16x64_bf16 v[52:55], v[220:223], v[208:215], v240 abid:1\n"
    "ds_read_b128 v[208:211], v250 offset:800\n"
    "v_lshlrev_b32_sdwa v205, 1, v179 dst_sel:DWORD dst_unused:UNUSED_PAD src0_sel:DWORD src1_sel:WORD_1\n"
    "v_mfma_f32_16x16x32_bf16 v[136:139], v[236:239], v[188:191], v[136:139]\n"
    "ds_read_b128 v[212:215], v250 offset:1120\n"
    "v_lshlrev_b32_sdwa v206, 1, v181 dst_sel:DWORD dst_unused:UNUSED_PAD src0_sel:DWORD src1_sel:WORD_1\n"
    "v_mfma_f32_16x16x32_bf16 v[140:143], v[164:167], v[188:191], v[140:143]\n"
    "ds_read_b128 v[220:223], v252 offset:12432\n"
    "v_lshlrev_b32_sdwa v188, 1, v176 dst_sel:DWORD dst_unused:UNUSED_PAD src0_sel:DWORD src1_sel:WORD_1\n"
    "v_mfma_f32_16x16x32_bf16 v[168:171], a[120:123], a[224:227], 0\n"
    "s_waitcnt lgkmcnt(9)\n"
    "ds_read_b128 a[252:255], v244 offset:38912\n"
    "v_or_b32_e32 v197, v197, v161\n"
    "v_mfma_f32_16x16x32_bf16 v[168:171], a[168:171], a[228:231], v[168:171]\n"
    "ds_read_u16_d16_hi v200, v200\n"
    "v_lshlrev_b32_sdwa v189, 1, v178 dst_sel:DWORD dst_unused:UNUSED_PAD src0_sel:DWORD src1_sel:WORD_1\n"
    "v_mfma_f32_16x16x32_bf16 v[168:171], a[76:79], a[232:235], v[168:171]\n"
    "v_lshlrev_b32_sdwa v190, 1, v180 dst_sel:DWORD dst_unused:UNUSED_PAD src0_sel:DWORD src1_sel:WORD_1\n"
    "v_lshlrev_b32_sdwa v191, 1, v182 dst_sel:DWORD dst_unused:UNUSED_PAD src0_sel:DWORD src1_sel:WORD_1\n"
    "v_mfma_f32_16x16x32_bf16 v[172:175], a[124:127], a[224:227], 0\n"
    "s_waitcnt lgkmcnt(9)\n"
    "ds_read_u16 v185, v185\n"
    "v_or_b32_e32 v198, v198, v162\n"
    "v_mfma_f32_16x16x32_bf16 v[172:175], a[172:175], a[228:231], v[172:175]\n"
    "v_lshlrev_b32_sdwa v207, 1, v183 dst_sel:DWORD dst_unused:UNUSED_PAD src0_sel:DWORD src1_sel:WORD_1\n"
    "v_fma_mix_f32 v234, v192, v232, 0 op_sel_hi:[1,1,0]\n"
    "v_fma_mix_f32 v232, v192, v232, 0 op_sel:[1,1,0] op_sel_hi:[1,1,0]\n"
    "v_mfma_f32_16x16x32_bf16 v[176:179], a[120:123], a[236:239], 0\n"
    "ds_read_u16_d16_hi v201, v201\n"
    "v_cvt_pk_bf16_f32 v192, v234, v232\n"
    "v_mfma_f32_16x16x32_bf16 v[176:179], a[168:171], a[240:243], v[176:179]\n"
    "ds_read_u16 v186, v186\n"
    "v_pk_fma_f16 v232, v196, v196, s48\n"
    "v_mfma_f32_16x16x32_bf16 v[176:179], a[76:79], a[244:247], v[176:179]\n"
    "s_waitcnt lgkmcnt(10)\n"
    "v_or_b32_e32 v199, v199, v163\n"
    "v_pk_mul_f16 v232, v196, v232\n"
    "v_mfma_f32_16x16x32_bf16 v[180:183], a[124:127], a[236:239], 0\n"
    "ds_read_u16_d16_hi v202, v202\n"
    "v_lshlrev_b32_sdwa v249, 1, v171 dst_sel:DWORD dst_unused:UNUSED_PAD src0_sel:DWORD src1_sel:WORD_1\n"
    "v_mfma_f32_16x16x32_bf16 v[180:183], a[172:175], a[240:243], v[180:183]\n"
    "v_exp_f16_e64 v234, v232\n"
    "v_lshlrev_b32_sdwa v161, 1, v179 dst_sel:DWORD dst_unused:UNUSED_PAD src0_sel:DWORD src1_sel:WORD_1\n"
    "s_waitcnt lgkmcnt(7)\n"
    "v_smfmac_f32_16x16x64_bf16 v[48:51], v[216:219], v[208:215], v240\n"
    "s_waitcnt lgkmcnt(6)\n"
    "ds_read_b128 v[216:219], v251 offset:12768\n"
    "ds_read_b128 v[236:239], v244 offset:12288\n"
    "v_smfmac_f32_16x16x64_bf16 v[52:55], v[220:223], v[208:215], v240 abid:1\n"
    "s_waitcnt lgkmcnt(7)\n"
    "ds_read_b128 v[208:211], v250 offset:240\n"
    "v_mfma_f32_16x16x32_bf16 v[172:175], a[252:255], a[232:235], v[172:175]\n"
    "s_nop 0\n"
    "ds_read_b128 v[212:215], v250 offset:560\n"
    "buffer_store_dwordx4 v[144:147], v245, s[44:47], s88 offen offset:0 sc1\n"
    "v_pk_add_f32 v[224:225], v[224:225], v[48:49]\n"
    "v_pk_add_f32 v[226:227], v[226:227], v[50:51]\n"
    "v_pk_fma_f32 v[228:229], v[48:49], v[48:49], v[228:229]\n"
    "v_pk_fma_f32 v[230:231], v[50:51], v[50:51], v[230:231]\n"
    "v_pk_add_f32 v[224:225], v[224:225], v[52:53]\n"
    "v_pk_add_f32 v[226:227], v[226:227], v[54:55]\n"
    "v_pk_fma_f32 v[228:229], v[52:53], v[52:53], v[228:229]\n"
    "v_pk_fma_f32 v[230:231], v[54:55], v[54:55], v[230:231]\n"
    "v_mfma_f32_16x16x32_bf16 v[180:183], a[252:255], a[244:247], v[180:183]\n"
    "s_waitcnt lgkmcnt(7)\n"
    "ds_read_b128 v[220:223], v251 offset:13104\n"
    "ds_read_u16 v187, v187\n"
    "v_exp_f16_sdwa v234, v232 dst_sel:WORD_1 dst_unused:UNUSED_PRESERVE src0_sel:WORD_1\n"
    "v_mfma_f32_16x16x32_bf16 v[96:99], a[176:179], v[192:195], v[96:99]\n"
    "s_waitcnt lgkmcnt(8)\n"
    "ds_read_u16_d16_hi v203, v203\n"
    "ds_read_b128 v[164:167], v244 offset:14336\n"
    "v_pk_fma_f16 v234, v234, s49, s49\n"
    "v_mfma_f32_16x16x32_bf16 v[100:103], a[180:183], v[192:195], v[100:103]\n"
    "ds_read_u16 v188, v188\n"
    "v_rcp_f16_e64 v232, v234\n"
    "v_mfma_f32_16x16x32_bf16 v[112:115], a[184:187], v[192:195], v[112:115]\n"
    "ds_read_u16_d16_hi v204, v204\n"
    "v_rcp_f16_sdwa v232, v234 dst_sel:WORD_1 dst_unused:UNUSED_PRESERVE src0_sel:WORD_1\n"
    "v_or_b32_e32 v184, v184, v200\n"
    "v_fma_mix_f32 v234, v196, v232, 0 op_sel_hi:[1,1,0]\n"
    "v_mfma_f32_16x16x32_bf16 v[116:119], a[188:191], v[192:195], v[116:119]\n"
    "s_waitcnt lgkmcnt(8)\n"
    "ds_read_u16 v189, v189\n"
    "v_fma_mix_f32 v232, v196, v232, 0 op_sel:[1,1,0] op_sel_hi:[1,1,0]\n"
    "v_or_b32_e64 v185, v185, v201\n"
    "v_cvt_pk_bf16_f32 v196, v234, v232\n"
    "v_or_b32_e64 v186, v186, v202\n"
    "v_mfma_f32_16x16x32_bf16 v[128:131], v[236:239], v[192:195], v[128:131]\n"
    "ds_read_u16_d16_hi v205, v205\n"
    "v_lshlrev_b32_sdwa v255, 1, v173 dst_sel:DWORD dst_unused:UNUSED_PAD src0_sel:DWORD src1_sel:WORD_1\n"
    "v_mfma_f32_16x16x32_bf16 v[104:107], a[176:179], v[196:199], v[104:107]\n"
    "s_waitcnt lgkmcnt(8)\n"
    "v_lshlrev_b32_sdwa v160, 1, v175 dst_sel:DWORD dst_unused:UNUSED_PAD src0_sel:DWORD src1_sel:WORD_1\n"
    "v_lshlrev_b32_sdwa v162, 1, v181 dst_sel:DWORD dst_unused:UNUSED_PAD src0_sel:DWORD src1_sel:WORD_1\n"
    "v_smfmac_f32_16x16x64_bf16 v[56:59], v[216:219], v[208:215], v240\n"
    "s_waitcnt lgkmcnt(7)\n"
    "ds_read_b128 v[216:219], v252 offset:12768\n"
    "v_smfmac_f32_16x16x64_bf16 v[60:63], v[220:223], v[208:215], v240 abid:1\n"
    "ds_read_b128 v[208:211], v250 offset:880\n"
    "v_mfma_f32_16x16x32_bf16 v[108:111], a[180:183], v[196:199], v[108:111]\n"
    "ds_read_b128 v[212:215], v250 offset:1200\n"
    "v_lshlrev_b32_sdwa v163, 1, v183 dst_sel:DWORD dst_unused:UNUSED_PAD src0_sel:DWORD src1_sel:WORD_1\n"
    "v_mfma_f32_16x16x32_bf16 v[120:123], a[184:187], v[196:199], v[120:123]\n"
    "ds_read_b128 v[220:223], v252 offset:13104\n"
    "v_mfma_f32_16x16x32_bf16 v[124:127], a[188:191], v[196:199], v[124:127]\n"
    "s_waitcnt lgkmcnt(8)\n"
    "ds_read_u16 v190, v190\n"
    "v_or_b32_e32 v187, v187, v203\n"
    "v_mfma_f32_16x16x32_bf16 v[132:135], v[164:167], v[192:195], v[132:135]\n"
    "v_cvt_pk_f16_f32 v192, v168, v169\n"
    "v_lshlrev_b32_sdwa v193, 1, v170 dst_sel:DWORD dst_unused:UNUSED_PAD src0_sel:DWORD src1_sel:WORD_1\n"
    "v_mfma_f32_16x16x32_bf16 v[136:139], v[236:239], v[196:199], v[136:139]\n"
    "ds_read_b128 a[224:227], v243 offset:0\n"
    "v_lshlrev_b32_sdwa v194, 1, v172 dst_sel:DWORD dst_unused:UNUSED_PAD src0_sel:DWORD src1_sel:WORD_1\n"
    "v_mfma_f32_16x16x32_bf16 v[140:143], v[164:167], v[196:199], v[140:143]\n"
    "ds_read_u16_d16_hi v206, v206\n"
    "v_lshlrev_b32_sdwa v195, 1, v174 dst_sel:DWORD dst_unused:UNUSED_PAD src0_sel:DWORD src1_sel:WORD_1\n"
    "v_cvt_pk_f16_f32 v196, v176, v177\n"
    "v_lshlrev_b32_sdwa v197, 1, v178 dst_sel:DWORD dst_unused:UNUSED_PAD src0_sel:DWORD src1_sel:WORD_1\n"
    "v_lshlrev_b32_sdwa v198, 1, v180 dst_sel:DWORD dst_unused:UNUSED_PAD src0_sel:DWORD src1_sel:WORD_1\n"
    "ds_read_b128 a[228:231], v243 offset:1024\n"
    "v_lshlrev_b32_sdwa v199, 1, v182 dst_sel:DWORD dst_unused:UNUSED_PAD src0_sel:DWORD src1_sel:WORD_1\n"
    "s_waitcnt lgkmcnt(10)\n"
    "v_or_b32_e32 v188, v188, v204\n"
    "ds_read_u16 v191, v191\n"
    "v_pk_fma_f16 v232, v192, v192, s48\n"
    "v_pk_mul_f16 v232, v192, v232\n"
    "ds_read_b128 a[232:235], v243 offset:2048\n"
    "v_exp_f16_e64 v234, v232\n"
    "ds_read_u16_d16_hi v207, v207\n"
    "s_waitcnt lgkmcnt(9)\n"
    "v_or_b32_e32 v189, v189, v205\n"
    "v_exp_f16_sdwa v234, v232 dst_sel:WORD_1 dst_unused:UNUSED_PRESERVE src0_sel:WORD_1\n"
    "s_waitcnt lgkmcnt(8)\n"
    "v_smfmac_f32_16x16x64_bf16 v[56:59], v[216:219], v[208:215], v240\n"
    "s_waitcnt lgkmcnt(7)\n"
    "ds_read_b128 v[216:219], v251 offset:21504\n"
    "ds_read_b128 a[236:239], v243 offset:3072\n"
    "v_smfmac_f32_16x16x64_bf16 v[60:63], v[220:223], v[208:215], v240 abid:1\n"
    "ds_read_b128 v[208:211], v250 offset:51648\n"
    "buffer_store_dwordx4 v[148:151], v245, s[44:47], s88 offen offset:64 sc1\n"
    "s_waitcnt lgkmcnt(8)\n"
    "v_mfma_f32_16x16x32_bf16 v[168:171], a[80:83], a[224:227], 0\n"
    "s_nop 0\n"
    "ds_read_b128 v[212:215], v250 offset:51968\n"
    "ds_read_b128 v[220:223], v251 offset:21840\n"
    "v_pk_fma_f16 v234, v234, s49, s49\n"
    "ds_read_b128 a[240:243], v243 offset:4096\n"
    "ds_read_b128 a[244:247], v243 offset:5120\n"
    "v_pk_add_f32 v[224:225], v[224:225], v[56:57]\n"
    "v_pk_add_f32 v[226:227], v[226:227], v[58:59]\n"
    "v_pk_fma_f32 v[228:229], v[56:57], v[56:57], v[228:229]\n"
    "v_pk_fma_f32 v[230:231], v[58:59], v[58:59], v[230:231]\n"
    "v_pk_add_f32 v[224:225], v[224:225], v[60:61]\n"
    "v_pk_add_f32 v[226:227], v[226:227], v[62:63]\n"
    "v_pk_fma_f32 v[228:229], v[60:61], v[60:61], v[228:229]\n"
    "v_pk_fma_f32 v[230:231], v[62:63], v[62:63], v[230:231]\n"
    "v_mfma_f32_16x16x32_bf16 v[172:175], a[84:87], a[224:227], 0\n"
    "s_waitcnt lgkmcnt(10)\n"
    "ds_read_u16 v193, v193\n"
    "ds_read_b128 a[252:255], v244 offset:32768\n"
    "v_mfma_f32_16x16x32_bf16 v[172:175], a[132:135], a[228:231], v[172:175]\n"
    "s_nop 0\n"
    "ds_read_u16_d16_hi v249, v249\n"
    "v_or_b32_e64 v190, v190, v206\n"
    "v_rcp_f16_e64 v232, v234\n"
    "v_mfma_f32_16x16x32_bf16 v[168:171], a[128:131], a[228:231], v[168:171]\n"
    "s_waitcnt lgkmcnt(11)\n"
    "ds_read_u16 v194, v194\n"
    "v_mfma_f32_16x16x32_bf16 v[168:171], a[48:51], a[232:235], v[168:171]\n"
    "s_waitcnt lgkmcnt(10)\n"
    "v_or_b32_e32 v191, v191, v207\n"
    "v_rcp_f16_sdwa v232, v234 dst_sel:WORD_1 dst_unused:UNUSED_PRESERVE src0_sel:WORD_1\n"
    "v_mfma_f32_16x16x32_bf16 v[96:99], a[192:195], v[184:187], v[96:99]\n"
    "s_waitcnt lgkmcnt(9)\n"
    "ds_read_u16_d16_hi v255, v255\n"
    "v_fma_mix_f32 v234, v192, v232, 0 op_sel_hi:[1,1,0]\n"
    "v_mfma_f32_16x16x32_bf16 v[176:179], a[80:83], a[236:239], 0\n"
    "v_lshlrev_b32_sdwa v200, 1, v169 dst_sel:DWORD dst_unused:UNUSED_PAD src0_sel:DWORD src1_sel:WORD_1\n"
    "v_lshlrev_b32_sdwa v201, 1, v171 dst_sel:DWORD dst_unused:UNUSED_PAD src0_sel:DWORD src1_sel:WORD_1\n"
    "v_mfma_f32_16x16x32_bf16 v[180:183], a[84:87], a[236:239], 0\n"
    "s_waitcnt lgkmcnt(8)\n"
    "ds_read_u16 v195, v195\n"
    "v_fma_mix_f32 v232, v192, v232, 0 op_sel:[1,1,0] op_sel_hi:[1,1,0]\n"
    "v_smfmac_f32_16x16x64_bf16 v[64:67], v[216:219], v[208:215], v240\n"
    "s_waitcnt lgkmcnt(7)\n"
    "ds_read_b128 v[216:219], v252 offset:21504\n"
    "v_smfmac_f32_16x16x64_bf16 v[68:71], v[220:223], v[208:215], v240 abid:1\n"
    "ds_read_b128 v[208:211], v250 offset:52288\n"
    "buffer_load_dwordx4 v[152:155], v245, s[40:43], s58 offen offset:64\n"
    "buffer_load_dwordx4 v[144:147], v245, s[40:43], s58 offen offset:0\n"
    "v_mfma_f32_16x16x32_bf16 v[176:179], a[128:131], a[240:243], v[176:179]\n"
    "s_waitcnt lgkmcnt(8)\n"
    "ds_read_b128 v[212:215], v250 offset:52608\n"
    "v_cvt_pk_bf16_f32 v192, v234, v232\n"
    "v_mfma_f32_16x16x32_bf16 v[176:179], a[48:51], a[244:247], v[176:179]\n"
    "ds_read_b128 v[220:223], v252 offset:21840\n"
    "v_pk_fma_f16 v232, v196, v196, s48\n"
    "v_mfma_f32_16x16x32_bf16 v[180:183], a[132:135], a[240:243], v[180:183]\n"
    "s_waitcnt lgkmcnt(8)\n"
    "ds_read_b128 v[236:239], v244 offset:16384\n"
    "v_pk_mul_f16 v232, v196, v232\n"
    "v_mfma_f32_16x16x32_bf16 v[180:183], a[252:255], a[244:247], v[180:183]\n"
    "ds_read_u16_d16_hi v160, v160\n"
    "v_lshlrev_b32_sdwa v204, 1, v177 dst_sel:DWORD dst_unused:UNUSED_PAD src0_sel:DWORD src1_sel:WORD_1\n"
    "v_mfma_f32_16x16x32_bf16 v[172:175], a[252:255], a[232:235], v[172:175]\n"
    "v_lshlrev_b32_e32 v254, 3, v244\n"
    "v_lshlrev_b32_sdwa v205, 1, v179 dst_sel:DWORD dst_unused:UNUSED_PAD src0_sel:DWORD src1_sel:WORD_1\n"
    "v_mfma_f32_16x16x32_bf16 v[100:103], a[196:199], v[184:187], v[100:103]\n"
    "s_waitcnt lgkmcnt(9)\n"
    "ds_read_b128 v[164:167], v244 offset:18432\n"
    "ds_read_u16 v197, v197\n"
    "v_mfma_f32_16x16x32_bf16 v[112:115], a[200:203], v[184:187], v[112:115]\n"
    "v_or_b32_e64 v193, v193, v249\n"
    "v_lshlrev_b32_sdwa v202, 1, v173 dst_sel:DWORD dst_unused:UNUSED_PAD src0_sel:DWORD src1_sel:WORD_1\n"
    "v_mfma_f32_16x16x32_bf16 v[116:119], a[204:207], v[184:187], v[116:119]\n"
    "ds_read_u16_d16_hi v161, v161\n"
    "v_lshlrev_b32_sdwa v203, 1, v175 dst_sel:DWORD dst_unused:UNUSED_PAD src0_sel:DWORD src1_sel:WORD_1\n"
    "v_lshlrev_b32_sdwa v206, 1, v181 dst_sel:DWORD dst_unused:UNUSED_PAD src0_sel:DWORD src1_sel:WORD_1\n"
    "v_lshlrev_b32_sdwa v207, 1, v183 dst_sel:DWORD dst_unused:UNUSED_PAD src0_sel:DWORD src1_sel:WORD_1\n"
    "v_exp_f16_e32 v234, v232\n"
    "v_mfma_f32_16x16x32_bf16 v[104:107], a[192:195], v[188:191], v[104:107]\n"
    "s_waitcnt lgkmcnt(10)\n"
    "ds_read_u16 v198, v198\n"
    "v_or_b32_e64 v194, v194, v255\n"
    "v_exp_f16_sdwa v234, v232 dst_sel:WORD_1 dst_unused:UNUSED_PRESERVE src0_sel:WORD_1\n"
    "v_mfma_f32_16x16x32_bf16 v[108:111], a[196:199], v[188:191], v[108:111]\n"
    "ds_read_u16_d16_hi v162, v162\n"
    "v_pk_fma_f16 v234, v234, s49, s49\n"
    "v_rcp_f16_e32 v232, v234\n"
    "v_mfma_f32_16x16x32_bf16 v[120:123], a[200:203], v[188:191], v[120:123]\n"
    "s_waitcnt lgkmcnt(8)\n"
    "v_rcp_f16_sdwa v232, v234 dst_sel:WORD_1 dst_unused:UNUSED_PRESERVE src0_sel:WORD_1\n"
    "v_smfmac_f32_16x16x64_bf16 v[64:67], v[216:219], v[208:215], v240\n"
    "s_waitcnt lgkmcnt(7)\n"
    "ds_read_b128 v[216:219], v251 offset:22176\n"
    "buffer_load_dwordx4 v[156:159], v245, s[40:43], s89 offen offset:64\n"
    "buffer_load_dwordx4 v[148:151], v245, s[40:43], s89 offen offset:0\n"
    "v_smfmac_f32_16x16x64_bf16 v[68:71], v[220:223], v[208:215], v240 abid:1\n"
    "s_waitcnt lgkmcnt(7)\n"
    "ds_read_b128 v[208:211], v250 offset:51728\n"
    "ds_read_b128 v[212:215], v250 offset:52048\n"
    "v_fma_mix_f32 v234, v196, v232, 0 op_sel_hi:[1,1,0]\n"
    "ds_read_b128 v[220:223], v251 offset:22512\n"
    "ds_read_u16 v199, v199\n"
    "v_pk_add_f32 v[224:225], v[224:225], v[64:65]\n"
    "v_pk_add_f32 v[226:227], v[226:227], v[66:67]\n"
    "v_pk_fma_f32 v[228:229], v[64:65], v[64:65], v[228:229]\n"
    "v_pk_fma_f32 v[230:231], v[66:67], v[66:67], v[230:231]\n"
    "v_pk_add_f32 v[224:225], v[224:225], v[68:69]\n"
    "v_pk_add_f32 v[226:227], v[226:227], v[70:71]\n"
    "v_pk_fma_f32 v[228:229], v[68:69], v[68:69], v[228:229]\n"
    "v_pk_fma_f32 v[230:231], v[70:71], v[70:71], v[230:231]\n"
    "v_mfma_f32_16x16x32_bf16 v[128:131], v[236:239], v[184:187], v[128:131]\n"
    "s_waitcnt lgkmcnt(9)\n"
    "ds_read_u16_d16_hi v163, v163\n"
    "v_or_b32_e32 v195, v195, v160\n"
    "v_mfma_f32_16x16x32_bf16 v[132:135], v[164:167], v[184:187], v[132:135]\n"
    "ds_read_b128 a[252:255], v244 offset:36864\n"
    "v_lshlrev_b32_sdwa v184, 1, v168 dst_sel:DWORD dst_unused:UNUSED_PAD src0_sel:DWORD src1_sel:WORD_1\n"
    "v_mfma_f32_16x16x32_bf16 v[124:127], a[204:207], v[188:191], v[124:127]\n"
    "s_waitcnt lgkmcnt(9)\n"
    "ds_read_u16 v184, v184\n"
    "v_or_b32_e32 v197, v197, v161\n"
    "v_mfma_f32_16x16x32_bf16 v[136:139], v[236:239], v[188:191], v[136:139]\n"
    "v_lshlrev_b32_sdwa v185, 1, v170 dst_sel:DWORD dst_unused:UNUSED_PAD src0_sel:DWORD src1_sel:WORD_1\n"
    "v_lshlrev_b32_sdwa v186, 1, v172 dst_sel:DWORD dst_unused:UNUSED_PAD src0_sel:DWORD src1_sel:WORD_1\n"
    "v_mfma_f32_16x16x32_bf16 v[140:143], v[164:167], v[188:191], v[140:143]\n"
    "ds_read_u16_d16_hi v200, v200\n"
    "s_mov_b32 exec_hi, 0\n"
    "buffer_load_dword v233, v254, s[72:75], s53 offen sc1\n"
    "s_mov_b32 exec_hi, -1\n"
    "s_add_u32 s53, s53, 393216\n"
    "v_mfma_f32_16x16x32_bf16 v[168:171], a[88:91], a[224:227], 0\n"
    "s_waitcnt lgkmcnt(8)\n"
    "ds_read_u16 v185, v185\n"
    "v_or_b32_e32 v198, v198, v162\n"
    "v_mfma_f32_16x16x32_bf16 v[168:171], a[136:139], a[228:231], v[168:171]\n"
    "v_lshlrev_b32_sdwa v187, 1, v174 dst_sel:DWORD dst_unused:UNUSED_PAD src0_sel:DWORD src1_sel:WORD_1\n"
    "v_lshlrev_b32_sdwa v188, 1, v176 dst_sel:DWORD dst_unused:UNUSED_PAD src0_sel:DWORD src1_sel:WORD_1\n"
    "v_mfma_f32_16x16x32_bf16 v[168:171], a[52:55], a[232:235], v[168:171]\n"
    "s_waitcnt lgkmcnt(7)\n"
    "ds_read_u16_d16_hi v201, v201\n"
    "v_lshlrev_b32_sdwa v189, 1, v178 dst_sel:DWORD dst_unused:UNUSED_PAD src0_sel:DWORD src1_sel:WORD_1\n"
    "v_smfmac_f32_16x16x64_bf16 v[72:75], v[216:219], v[208:215], v240\n"
    "s_waitcnt lgkmcnt(7)\n"
    "ds_read_b128 v[216:219], v252 offset:22176\n"
    "v_smfmac_f32_16x16x64_bf16 v[76:79], v[220:223], v[208:215], v240 abid:1\n"
    "ds_read_b128 v[208:211], v250 offset:52368\n"
    "v_mfma_f32_16x16x32_bf16 v[172:175], a[92:95], a[224:227], 0\n"
    "ds_read_b128 v[212:215], v250 offset:52688\n"
    "v_lshlrev_b32_sdwa v190, 1, v180 dst_sel:DWORD dst_unused:UNUSED_PAD src0_sel:DWORD src1_sel:WORD_1\n"
    "v_mfma_f32_16x16x32_bf16 v[172:175], a[140:143], a[228:231], v[172:175]\n"
    "ds_read_b128 v[220:223], v252 offset:22512\n"
    "v_lshlrev_b32_sdwa v191, 1, v182 dst_sel:DWORD dst_unused:UNUSED_PAD src0_sel:DWORD src1_sel:WORD_1\n"
    "v_mfma_f32_16x16x32_bf16 v[176:179], a[88:91], a[236:239], 0\n"
    "s_waitcnt lgkmcnt(8)\n"
    "ds_read_u16 v186, v186\n"
    "v_or_b32_e32 v199, v199, v163\n"
    "v_mfma_f32_16x16x32_bf16 v[176:179], a[136:139], a[240:243], v[176:179]\n"
    "v_fma_mix_f32 v232, v196, v232, 0 op_sel:[1,1,0] op_sel_hi:[1,1,0]\n"
    "v_lshlrev_b32_sdwa v249, 1, v171 dst_sel:DWORD dst_unused:UNUSED_PAD src0_sel:DWORD src1_sel:WORD_1\n"
    "v_mfma_f32_16x16x32_bf16 v[176:179], a[52:55], a[244:247], v[176:179]\n"
    "ds_read_u16_d16_hi v202, v202\n"
    "v_cvt_pk_bf16_f32 v196, v234, v232\n"
    "v_mfma_f32_16x16x32_bf16 v[172:175], a[252:255], a[232:235], v[172:175]\n"
    "v_mfma_f32_16x16x32_bf16 v[180:183], a[92:95], a[236:239], 0\n"
    "ds_read_b128 v[236:239], v244 offset:20480\n"
    "v_mfma_f32_16x16x32_bf16 v[180:183], a[140:143], a[240:243], v[180:183]\n"
    "s_waitcnt lgkmcnt(9)\n"
    "ds_read_u16 v187, v187\n"
    "v_or_b32_e32 v184, v184, v200\n"
    "v_mfma_f32_16x16x32_bf16 v[180:183], a[252:255], a[244:247], v[180:183]\n"
    "v_lshlrev_b32_sdwa v161, 1, v179 dst_sel:DWORD dst_unused:UNUSED_PAD src0_sel:DWORD src1_sel:WORD_1\n"
    "v_lshlrev_b32_sdwa v255, 1, v173 dst_sel:DWORD dst_unused:UNUSED_PAD src0_sel:DWORD src1_sel:WORD_1\n"
    "v_mfma_f32_16x16x32_bf16 v[96:99], a[208:211], v[192:195], v[96:99]\n"
    "ds_read_u16_d16_hi v203, v203\n"
    "v_lshlrev_b32_sdwa v160, 1, v175 dst_sel:DWORD dst_unused:UNUSED_PAD src0_sel:DWORD src1_sel:WORD_1\n"
    "v_mfma_f32_16x16x32_bf16 v[100:103], a[212:215], v[192:195], v[100:103]\n"
    "ds_read_b128 v[164:167], v244 offset:22528\n"
    "v_mfma_f32_16x16x32_bf16 v[112:115], a[216:219], v[192:195], v[112:115]\n"
    "s_waitcnt lgkmcnt(8)\n"
    "ds_read_u16 v188, v188\n"
    "v_or_b32_e32 v185, v185, v201\n"
    "v_mfma_f32_16x16x32_bf16 v[116:119], a[220:223], v[192:195], v[116:119]\n"
    "s_waitcnt lgkmcnt(8)\n"
    "v_lshlrev_b32_sdwa v162, 1, v181 dst_sel:DWORD dst_unused:UNUSED_PAD src0_sel:DWORD src1_sel:WORD_1\n"
    "v_lshlrev_b32_sdwa v163, 1, v183 dst_sel:DWORD dst_unused:UNUSED_PAD src0_sel:DWORD src1_sel:WORD_1\n"
    "v_smfmac_f32_16x16x64_bf16 v[72:75], v[216:219], v[208:215], v240\n"
    "s_waitcnt lgkmcnt(7)\n"
    "ds_read_b128 v[216:219], v251 offset:22848\n"
    "v_smfmac_f32_16x16x64_bf16 v[76:79], v[220:223], v[208:215], v240 abid:1\n"
    "ds_read_b128 v[208:211], v250 offset:51808\n"
    "v_mfma_f32_16x16x32_bf16 v[104:107], a[208:211], v[196:199], v[104:107]\n"
    "ds_read_b128 v[212:215], v250 offset:52128\n"
    "v_cvt_pk_bf16_f32 v96, v96, v97\n"
    "v_mfma_f32_16x16x32_bf16 v[108:111], a[212:215], v[196:199], v[108:111]\n"
    "ds_read_b128 v[220:223], v251 offset:23184\n"
    "v_cvt_pk_bf16_f32 v97, v98, v99\n"
    "v_cvt_pk_bf16_f32 v98, v100, v101\n"
    "ds_read_u16_d16_hi v204, v204\n"
    "v_pk_add_f32 v[224:225], v[224:225], v[72:73]\n"
    "v_pk_add_f32 v[226:227], v[226:227], v[74:75]\n"
    "v_pk_fma_f32 v[228:229], v[72:73], v[72:73], v[228:229]\n"
    "v_pk_fma_f32 v[230:231], v[74:75], v[74:75], v[230:231]\n"
    "v_pk_add_f32 v[224:225], v[224:225], v[76:77]\n"
    "v_pk_add_f32 v[226:227], v[226:227], v[78:79]\n"
    "v_pk_fma_f32 v[228:229], v[76:77], v[76:77], v[228:229]\n"
    "v_pk_fma_f32 v[230:231], v[78:79], v[78:79], v[230:231]\n"
    "v_mfma_f32_16x16x32_bf16 v[120:123], a[216:219], v[196:199], v[120:123]\n"
    "s_waitcnt lgkmcnt(9)\n"
    "ds_read_u16 v189, v189\n"
    "ds_read_u16_d16_hi v205, v205\n"
    "v_lshlrev_b32_e32 v254, 3, v244\n"
    "v_or_b32_e64 v186, v186, v202\n"
    "v_cvt_pk_bf16_f32 v99, v102, v103\n"
    "v_cvt_pk_bf16_f32 v112, v112, v113\n"
    "v_mfma_f32_16x16x32_bf16 v[128:131], v[236:239], v[192:195], v[128:131]\n"
    "s_waitcnt lgkmcnt(8)\n"
    "v_or_b32_e32 v187, v187, v203\n"
    "v_cvt_pk_bf16_f32 v113, v114, v115\n"
    "v_mfma_f32_16x16x32_bf16 v[132:135], v[164:167], v[192:195], v[132:135]\n"
    "v_cvt_pk_f16_f32 v192, v168, v169\n"
    "v_lshlrev_b32_sdwa v193, 1, v170 dst_sel:DWORD dst_unused:UNUSED_PAD src0_sel:DWORD src1_sel:WORD_1\n"
    "v_mfma_f32_16x16x32_bf16 v[124:127], a[220:223], v[196:199], v[124:127]\n"
    "ds_write_b128 v243, v[96:99] offset:0\n"
    "v_lshlrev_b32_sdwa v194, 1, v172 dst_sel:DWORD dst_unused:UNUSED_PAD src0_sel:DWORD src1_sel:WORD_1\n"
    "v_lshlrev_b32_sdwa v195, 1, v174 dst_sel:DWORD dst_unused:UNUSED_PAD src0_sel:DWORD src1_sel:WORD_1\n"
    "v_cvt_pk_bf16_f32 v114, v116, v117\n"
    "v_cvt_pk_bf16_f32 v115, v118, v119\n"
    "v_cvt_pk_bf16_f32 v128, v128, v129\n"
    "v_cvt_pk_bf16_f32 v129, v130, v131\n"
    "v_mfma_f32_16x16x32_bf16 v[136:139], v[236:239], v[196:199], v[136:139]\n"
    "s_nop 0\n"
    "s_waitcnt lgkmcnt(5)\n"
    "ds_read_u16 v190, v190\n"
    "ds_write_b128 v243, v[112:115] offset:2048\n"
    "v_cvt_pk_bf16_f32 v130, v132, v133\n"
    "v_cvt_pk_bf16_f32 v131, v134, v135\n"
    "v_cvt_pk_bf16_f32 v104, v104, v105\n"
    "v_cvt_pk_bf16_f32 v105, v106, v107\n"
    "v_cvt_pk_bf16_f32 v106, v108, v109\n"
    "v_cvt_pk_bf16_f32 v107, v110, v111\n"
    "v_cvt_pk_bf16_f32 v120, v120, v121\n"
    "v_smfmac_f32_16x16x64_bf16 v[80:83], v[216:219], v[208:215], v240\n"
    "s_waitcnt lgkmcnt(5)\n"
    "ds_read_b128 v[216:219], v252 offset:22848\n"
    "ds_read_u16_d16_hi v206, v206\n"
    "v_smfmac_f32_16x16x64_bf16 v[84:87], v[220:223], v[208:215], v240 abid:1\n"
    "s_nop 0\n"
    "ds_read_b128 v[208:211], v250 offset:52448\n"
    "ds_read_b128 v[212:215], v250 offset:52768\n"
    "v_mfma_f32_16x16x32_bf16 v[140:143], v[164:167], v[196:199], v[140:143]\n"
    "s_nop 0\n"
    "ds_read_b128 v[220:223], v252 offset:23184\n"
    "s_mov_b32 exec_hi, 0\n"
    "buffer_load_dword v233, v254, s[72:75], s53 offen sc1\n"
    "s_mov_b32 exec_hi, -1\n"
    "v_mfma_f32_16x16x32_bf16 v[168:171], a[96:99], a[224:227], 0\n"
    "s_waitcnt lgkmcnt(7)\n"
    "ds_read_u16 v191, v191\n"
    "ds_read_u16_d16_hi v207, v207\n"
    "v_mfma_f32_16x16x32_bf16 v[168:171], a[144:147], a[228:231], v[168:171]\n"
    "s_add_u32 s53, s53, 393216\n"
    "v_cvt_pk_f16_f32 v196, v176, v177\n"
    "v_lshlrev_b32_sdwa v197, 1, v178 dst_sel:DWORD dst_unused:UNUSED_PAD src0_sel:DWORD src1_sel:WORD_1\n"
    "v_mfma_f32_16x16x32_bf16 v[168:171], a[56:59], a[232:235], v[168:171]\n"
    "ds_read_u16 v193, v193\n"
    "v_lshlrev_b32_sdwa v198, 1, v180 dst_sel:DWORD dst_unused:UNUSED_PAD src0_sel:DWORD src1_sel:WORD_1\n"
    "v_lshlrev_b32_sdwa v199, 1, v182 dst_sel:DWORD dst_unused:UNUSED_PAD src0_sel:DWORD src1_sel:WORD_1\n"
    "v_or_b32_e64 v188, v188, v204\n"
    "v_or_b32_e64 v189, v189, v205\n"
    "v_mfma_f32_16x16x32_bf16 v[172:175], a[100:103], a[224:227], 0\n"
    "v_cvt_pk_bf16_f32 v121, v122, v123\n"
    "v_cvt_pk_bf16_f32 v122, v124, v125\n"
    "v_mfma_f32_16x16x32_bf16 v[172:175], a[148:151], a[228:231], v[172:175]\n"
    "v_cvt_pk_bf16_f32 v123, v126, v127\n"
    "v_add_u32_e64 v254, s54, v242\n"
    "v_mfma_f32_16x16x32_bf16 v[172:175], a[60:63], a[232:235], v[172:175]\n"
    "ds_write_b128 v243, v[128:131] offset:4096\n"
    "v_cvt_pk_bf16_f32 v136, v136, v137\n"
    "v_pk_fma_f16 v232, v192, v192, s48\n"
    "v_cvt_pk_bf16_f32 v137, v138, v139\n"
    "v_cvt_pk_bf16_f32 v138, v140, v141\n"
    "v_cvt_pk_bf16_f32 v139, v142, v143\n"
    "v_pk_mul_f16 v232, v192, v232\n"
    "v_mfma_f32_16x16x32_bf16 v[176:179], a[96:99], a[236:239], 0\n"
    "s_waitcnt lgkmcnt(5)\n"
    "ds_read_u16_d16_hi v249, v249\n"
    "v_or_b32_e32 v190, v190, v206\n"
    "v_mfma_f32_16x16x32_bf16 v[176:179], a[144:147], a[240:243], v[176:179]\n"
    "s_waitcnt lgkmcnt(5)\n"
    "v_exp_f16_e32 v234, v232\n"
    "v_mfma_f32_16x16x32_bf16 v[176:179], a[56:59], a[244:247], v[176:179]\n"
    "ds_read_u16 v194, v194\n"
    "ds_write_b128 v243, v[104:107] offset:1024\n"
    "v_smfmac_f32_16x16x64_bf16 v[80:83], v[216:219], v[208:215], v240\n"
    "v_exp_f16_sdwa v234, v232 dst_sel:WORD_1 dst_unused:UNUSED_PRESERVE src0_sel:WORD_1\n"
    "v_smfmac_f32_16x16x64_bf16 v[84:87], v[220:223], v[208:215], v240 abid:1\n"
    "s_waitcnt lgkmcnt(5)\n"
    "v_or_b32_e32 v191, v191, v207\n"
    "v_mfma_f32_16x16x32_bf16 v[180:183], a[100:103], a[236:239], 0\n"
    "ds_read_b128 v[208:211], v250 offset:51888\n"
    "ds_read_b128 v[212:215], v250 offset:52208\n"
    "v_mfma_f32_16x16x32_bf16 v[180:183], a[148:151], a[240:243], v[180:183]\n"
    "ds_write_b128 v243, v[120:123] offset:3072\n"
    "v_pk_add_f32 v[224:225], v[224:225], v[80:81]\n"
    "v_pk_add_f32 v[226:227], v[226:227], v[82:83]\n"
    "v_pk_fma_f32 v[228:229], v[80:81], v[80:81], v[228:229]\n"
    "v_pk_fma_f32 v[230:231], v[82:83], v[82:83], v[230:231]\n"
    "v_pk_add_f32 v[224:225], v[224:225], v[84:85]\n"
    "v_pk_add_f32 v[226:227], v[226:227], v[86:87]\n"
    "v_pk_fma_f32 v[228:229], v[84:85], v[84:85], v[228:229]\n"
    "v_pk_fma_f32 v[230:231], v[86:87], v[86:87], v[230:231]\n"
    "v_mfma_f32_16x16x32_bf16 v[180:183], a[60:63], a[244:247], v[180:183]\n"
    "s_waitcnt lgkmcnt(5)\n"
    "ds_read_b128 v[216:219], v251 offset:23520\n"
    "ds_read_b128 v[220:223], v251 offset:23856\n"
    "v_mfma_f32_16x16x32_bf16 v[96:99], a[0:3], v[184:187], 0\n"
    "ds_write_b128 v243, v[136:139] offset:5120\n"
    "v_mfma_f32_16x16x32_bf16 v[100:103], a[4:7], v[184:187], 0\n"
    "v_or_b32_e32 v193, v193, v249\n"
    "v_pk_fma_f16 v234, v234, s49, s49\n"
    "v_mfma_f32_16x16x32_bf16 v[112:115], a[8:11], v[184:187], 0\n"
    "v_lshlrev_b32_sdwa v200, 1, v169 dst_sel:DWORD dst_unused:UNUSED_PAD src0_sel:DWORD src1_sel:WORD_1\n"
    "v_lshlrev_b32_sdwa v201, 1, v171 dst_sel:DWORD dst_unused:UNUSED_PAD src0_sel:DWORD src1_sel:WORD_1\n"
    "v_mfma_f32_16x16x32_bf16 v[116:119], a[12:15], v[184:187], 0\n"
    "ds_read_u16_d16_hi v255, v255\n"
    "ds_read_u16 v195, v195\n"
    "v_mfma_f32_16x16x32_bf16 v[104:107], a[0:3], v[188:191], 0\n"
    "ds_read_b128 v[236:239], v244 offset:0\n"
    "ds_read_u16_d16_hi v160, v160\n"
    "v_lshlrev_b32_sdwa v202, 1, v173 dst_sel:DWORD dst_unused:UNUSED_PAD src0_sel:DWORD src1_sel:WORD_1\n"
    "v_mfma_f32_16x16x32_bf16 v[108:111], a[4:7], v[188:191], 0\n"
    "ds_read_b128 v[164:167], v244 offset:2048\n"
    "v_lshlrev_b32_sdwa v203, 1, v175 dst_sel:DWORD dst_unused:UNUSED_PAD src0_sel:DWORD src1_sel:WORD_1\n"
    "v_mfma_f32_16x16x32_bf16 v[120:123], a[8:11], v[188:191], 0\n"
    "s_waitcnt lgkmcnt(7)\n"
    "ds_read_u16 v197, v197\n"
    "v_rcp_f16_e64 v232, v234\n"
    "v_lshlrev_b32_sdwa v204, 1, v177 dst_sel:DWORD dst_unused:UNUSED_PAD src0_sel:DWORD src1_sel:WORD_1\n"
    "v_rcp_f16_sdwa v232, v234 dst_sel:WORD_1 dst_unused:UNUSED_PRESERVE src0_sel:WORD_1\n"
    "v_smfmac_f32_16x16x64_bf16 v[88:91], v[216:219], v[208:215], v240\n"
    "s_waitcnt lgkmcnt(6)\n"
    "ds_read_b128 v[216:219], v252 offset:23520\n"
    "v_smfmac_f32_16x16x64_bf16 v[92:95], v[220:223], v[208:215], v240 abid:1\n"
    "ds_read_b128 v[208:211], v250 offset:52528\n"
    "v_mfma_f32_16x16x32_bf16 v[124:127], a[12:15], v[188:191], 0\n"
    "ds_read_b128 v[212:215], v250 offset:52848\n"
    "v_lshlrev_b32_sdwa v205, 1, v179 dst_sel:DWORD dst_unused:UNUSED_PAD src0_sel:DWORD src1_sel:WORD_1\n"
    "v_lshlrev_b32_sdwa v206, 1, v181 dst_sel:DWORD dst_unused:UNUSED_PAD src0_sel:DWORD src1_sel:WORD_1\n"
    "ds_read_b128 v[220:223], v252 offset:23856\n"
    "v_lshlrev_b32_sdwa v207, 1, v183 dst_sel:DWORD dst_unused:UNUSED_PAD src0_sel:DWORD src1_sel:WORD_1\n"
    "ds_read_u16_d16_hi v161, v161\n"
    "v_fma_mix_f32 v234, v192, v232, 0 op_sel_hi:[1,1,0]\n"
    "s_waitcnt lgkmcnt(10)\n"
    "v_or_b32_e32 v194, v194, v255\n"
    "v_fma_mix_f32 v232, v192, v232, 0 op_sel:[1,1,0] op_sel_hi:[1,1,0]\n"
    "ds_read_u16 v198, v198\n"
    "v_cvt_pk_bf16_f32 v192, v234, v232\n"
    "v_pk_fma_f16 v232, v196, v196, s48\n"
    "s_waitcnt lgkmcnt(9)\n"
    "v_mfma_f32_16x16x32_bf16 v[128:131], v[236:239], v[184:187], 0\n"
    "s_nop 0\n"
    "ds_read_u16_d16_hi v162, v162\n"
    "v_pk_mul_f16 v232, v196, v232\n"
    "v_exp_f16_e32 v234, v232\n"
    "v_mfma_f32_16x16x32_bf16 v[136:139], v[236:239], v[188:191], 0\n"
    "s_waitcnt lgkmcnt(8)\n"
    "ds_read_u16 v199, v199\n"
    "v_or_b32_e64 v195, v195, v160\n"
    "v_exp_f16_sdwa v234, v232 dst_sel:WORD_1 dst_unused:UNUSED_PRESERVE src0_sel:WORD_1\n"
    "v_mfma_f32_16x16x32_bf16 v[132:135], v[164:167], v[184:187], 0\n"
    "v_lshlrev_b32_sdwa v184, 1, v168 dst_sel:DWORD dst_unused:UNUSED_PAD src0_sel:DWORD src1_sel:WORD_1\n"
    "v_lshlrev_b32_sdwa v185, 1, v170 dst_sel:DWORD dst_unused:UNUSED_PAD src0_sel:DWORD src1_sel:WORD_1\n"
    "v_mfma_f32_16x16x32_bf16 v[140:143], v[164:167], v[188:191], 0\n"
    "ds_read_u16_d16_hi v163, v163\n"
    "v_lshlrev_b32_sdwa v186, 1, v172 dst_sel:DWORD dst_unused:UNUSED_PAD src0_sel:DWORD src1_sel:WORD_1\n"
    "v_mfma_f32_16x16x32_bf16 v[168:171], a[104:107], a[224:227], 0\n"
    "ds_read_u16 v184, v184\n"
    "v_lshlrev_b32_sdwa v187, 1, v174 dst_sel:DWORD dst_unused:UNUSED_PAD src0_sel:DWORD src1_sel:WORD_1\n"
    "v_mfma_f32_16x16x32_bf16 v[168:171], a[152:155], a[228:231], v[168:171]\n"
    "v_lshlrev_b32_sdwa v188, 1, v176 dst_sel:DWORD dst_unused:UNUSED_PAD src0_sel:DWORD src1_sel:WORD_1\n"
    "v_lshlrev_b32_sdwa v189, 1, v178 dst_sel:DWORD dst_unused:UNUSED_PAD src0_sel:DWORD src1_sel:WORD_1\n"
    "v_mfma_f32_16x16x32_bf16 v[168:171], a[64:67], a[232:235], v[168:171]\n"
    "s_waitcnt lgkmcnt(7)\n"
    "ds_read_u16_d16_hi v200, v200\n"
    "v_lshlrev_b32_sdwa v190, 1, v180 dst_sel:DWORD dst_unused:UNUSED_PAD src0_sel:DWORD src1_sel:WORD_1\n"
    "v_lshlrev_b32_sdwa v191, 1, v182 dst_sel:DWORD dst_unused:UNUSED_PAD src0_sel:DWORD src1_sel:WORD_1\n"
    "v_smfmac_f32_16x16x64_bf16 v[88:91], v[216:219], v[208:215], v240\n"
    "s_waitcnt lgkmcnt(7)\n"
    "ds_read_u16 v185, v185\n"
    "v_smfmac_f32_16x16x64_bf16 v[92:95], v[220:223], v[208:215], v240 abid:1\n"
    "v_pk_fma_f16 v234, v234, s49, s49\n"
    "v_mfma_f32_16x16x32_bf16 v[172:175], a[108:111], a[224:227], 0\n"
    "s_waitcnt lgkmcnt(7)\n"
    "ds_read_u16_d16_hi v201, v201\n"
    "v_or_b32_e32 v197, v197, v161\n"
    "v_rcp_f16_e64 v232, v234\n"
    "v_lshlrev_b32_sdwa v249, 1, v171 dst_sel:DWORD dst_unused:UNUSED_PAD src0_sel:DWORD src1_sel:WORD_1\n"
    "v_pk_add_f32 v[224:225], v[224:225], v[88:89]\n"
    "v_pk_add_f32 v[226:227], v[226:227], v[90:91]\n"
    "v_pk_fma_f32 v[228:229], v[88:89], v[88:89], v[228:229]\n"
    "v_pk_fma_f32 v[230:231], v[90:91], v[90:91], v[230:231]\n"
    "v_pk_add_f32 v[224:225], v[224:225], v[92:93]\n"
    "v_pk_add_f32 v[226:227], v[226:227], v[94:95]\n"
    "v_pk_fma_f32 v[228:229], v[92:93], v[92:93], v[228:229]\n"
    "v_pk_fma_f32 v[230:231], v[94:95], v[94:95], v[230:231]\n"
    "v_mfma_f32_16x16x32_bf16 v[172:175], a[156:159], a[228:231], v[172:175]\n"
    "s_waitcnt lgkmcnt(6)\n"
    "v_or_b32_e32 v198, v198, v162\n"
    "v_mfma_f32_16x16x32_bf16 v[172:175], a[68:71], a[232:235], v[172:175]\n"
    "ds_write_b128 v254, v[224:227]\n"
    "v_mfma_f32_16x16x32_bf16 v[176:179], a[104:107], a[236:239], 0\n"
    "v_rcp_f16_sdwa v232, v234 dst_sel:WORD_1 dst_unused:UNUSED_PRESERVE src0_sel:WORD_1\n"
    "v_mfma_f32_16x16x32_bf16 v[176:179], a[152:155], a[240:243], v[176:179]\n"
    "s_waitcnt lgkmcnt(5)\n"
    "v_or_b32_e32 v199, v199, v163\n"
    "v_fma_mix_f32 v234, v196, v232, 0 op_sel_hi:[1,1,0]\n"
    "v_mfma_f32_16x16x32_bf16 v[176:179], a[64:67], a[244:247], v[176:179]\n"
    "ds_read_u16 v186, v186\n"
    "ds_write_b128 v254, v[228:231] offset:1024\n"
    "v_mfma_f32_16x16x32_bf16 v[180:183], a[108:111], a[236:239], 0\n"
    "s_waitcnt lgkmcnt(5)\n"
    "v_lshrrev_b32_e32 v254, 2, v242\n"
    "v_fma_mix_f32 v232, v196, v232, 0 op_sel:[1,1,0] op_sel_hi:[1,1,0]\n"
    "v_mfma_f32_16x16x32_bf16 v[180:183], a[156:159], a[240:243], v[180:183]\n"
    "v_add_u32_e64 v254, s55, v254\n"
    "v_cvt_pk_bf16_f32 v196, v234, v232\n"
    "v_mfma_f32_16x16x32_bf16 v[180:183], a[68:71], a[244:247], v[180:183]\n"
    "ds_read_u16_d16_hi v202, v202\n"
    "ds_read_b128 v[236:239], v244 offset:4096\n"
    "v_mfma_f32_16x16x32_bf16 v[96:99], a[16:19], v[192:195], v[96:99]\n"
    "ds_read_u16 v187, v187\n"
    "ds_read_u16_d16_hi v203, v203\n"
    "v_mfma_f32_16x16x32_bf16 v[100:103], a[20:23], v[192:195], v[100:103]\n"
    "s_waitcnt lgkmcnt(6)\n"
    "v_or_b32_e32 v184, v184, v200\n"
    "v_or_b32_e64 v185, v185, v201\n"
    "v_mfma_f32_16x16x32_bf16 v[112:115], a[24:27], v[192:195], v[112:115]\n"
    "ds_read_b128 v[164:167], v244 offset:6144\n"
    "ds_read_u16 v188, v188\n"
    "v_mfma_f32_16x16x32_bf16 v[116:119], a[28:31], v[192:195], v[116:119]\n"
    "v_lshlrev_b32_sdwa v255, 1, v173 dst_sel:DWORD dst_unused:UNUSED_PAD src0_sel:DWORD src1_sel:WORD_1\n"
    "v_lshlrev_b32_sdwa v160, 1, v175 dst_sel:DWORD dst_unused:UNUSED_PAD src0_sel:DWORD src1_sel:WORD_1\n"
    "v_mfma_f32_16x16x32_bf16 v[104:107], a[16:19], v[196:199], v[104:107]\n"
    "ds_read_u16_d16_hi v204, v204\n"
    "v_lshlrev_b32_sdwa v161, 1, v179 dst_sel:DWORD dst_unused:UNUSED_PAD src0_sel:DWORD src1_sel:WORD_1\n"
    "v_lshlrev_b32_sdwa v162, 1, v181 dst_sel:DWORD dst_unused:UNUSED_PAD src0_sel:DWORD src1_sel:WORD_1\n"
    "v_lshlrev_b32_sdwa v163, 1, v183 dst_sel:DWORD dst_unused:UNUSED_PAD src0_sel:DWORD src1_sel:WORD_1\n"
    "v_mfma_f32_16x16x32_bf16 v[108:111], a[20:23], v[196:199], v[108:111]\n"
    "s_waitcnt lgkmcnt(7)\n"
    "s_barrier\n"
    "v_mfma_f32_16x16x32_bf16 v[120:123], a[24:27], v[196:199], v[120:123]\n"
    "s_waitcnt lgkmcnt(5)\n"
    "ds_read_b32 v224, v254 offset:0\n"
    "ds_read_b32 v225, v254 offset:6752\n"
    "v_mfma_f32_16x16x32_bf16 v[128:131], v[236:239], v[192:195], v[128:131]\n"
    "s_nop 0\n"
    "ds_read_b32 v226, v254 offset:13504\n"
    "ds_read_b32 v227, v254 offset:20256\n"
    "v_mfma_f32_16x16x32_bf16 v[124:127], a[28:31], v[196:199], v[124:127]\n"
    "ds_read_b32 v228, v254 offset:1024\n"
    "ds_read_b32 v229, v254 offset:7776\n"
    "v_mfma_f32_16x16x32_bf16 v[136:139], v[236:239], v[196:199], v[136:139]\n"
    "ds_read_b32 v230, v254 offset:14528\n"
    "ds_read_b32 v231, v254 offset:21280\n"
    "s_waitcnt vmcnt(2)\n"
    "v_smfmac_f32_32x32x32_bf16 v[96:111], a[248:251], v[144:151], v241\n"
    "s_waitcnt lgkmcnt(10)\n"
    "ds_read_u16 v189, v189\n"
    "ds_read_u16_d16_hi v205, v205\n"
    "v_or_b32_e64 v186, v186, v202\n"
    "v_or_b32_e64 v187, v187, v203\n"
    "ds_read_u16 v190, v190\n"
    "ds_read_u16_d16_hi v206, v206\n"
    "v_mfma_f32_16x16x32_bf16 v[132:135], v[164:167], v[192:195], v[132:135]\n"
    "buffer_load_dwordx4 v[144:147], v245, s[40:43], s58 offen offset:128\n"
    "v_cvt_pk_f16_f32 v192, v168, v169\n"
    "v_mfma_f32_16x16x32_bf16 v[140:143], v[164:167], v[196:199], v[140:143]\n"
    "ds_read_u16 v191, v191\n"
    "v_lshlrev_b32_sdwa v193, 1, v170 dst_sel:DWORD dst_unused:UNUSED_PAD src0_sel:DWORD src1_sel:WORD_1\n"
    "v_lshlrev_b32_sdwa v194, 1, v172 dst_sel:DWORD dst_unused:UNUSED_PAD src0_sel:DWORD src1_sel:WORD_1\n"
    "v_smfmac_f32_32x32x32_bf16 v[112:127], a[248:251], v[152:159], v241\n"
    "s_waitcnt lgkmcnt(9)\n"
    "ds_read_u16_d16_hi v207, v207\n"
    "v_add_f32_e32 v224, v224, v225\n"
    "v_add_f32_e64 v226, v226, v227\n"
    "v_lshlrev_b32_sdwa v195, 1, v174 dst_sel:DWORD dst_unused:UNUSED_PAD src0_sel:DWORD src1_sel:WORD_1\n"
    "v_add_f32_e32 v224, v224, v226\n"
    "v_mfma_f32_16x16x32_bf16 v[168:171], a[112:115], a[224:227], 0\n"
    "s_waitcnt lgkmcnt(7)\n"
    "ds_read_u16 v193, v193\n"
    "v_add_f32_e64 v228, v228, v229\n"
    "v_mfma_f32_16x16x32_bf16 v[168:171], a[160:163], a[228:231], v[168:171]\n"
    "s_waitcnt lgkmcnt(7)\n"
    "v_add_f32_e32 v230, v230, v231\n"
    "v_mul_f32_e32 v224, 0x3c2aaaab, v224\n"
    "v_mfma_f32_16x16x32_bf16 v[168:171], a[72:75], a[232:235], v[168:171]\n"
    "ds_read_b128 a[252:255], v244 offset:34816\n"
    "v_add_f32_e64 v228, v228, v230\n"
    "v_mfma_f32_16x16x32_bf16 v[172:175], a[116:119], a[224:227], 0\n"
    "ds_read_u16_d16_hi v249, v249\n"
    "v_mul_f32_e32 v228, 0x3c2aaaab, v228\n"
    "v_mfma_f32_16x16x32_bf16 v[172:175], a[164:167], a[228:231], v[172:175]\n"
    "v_fma_f32 v228, -v224, v224, v228\n"
    "v_cvt_pk_f16_f32 v196, v176, v177\n"
    "v_max_f32_e32 v228, 0, v228\n"
    "v_lshlrev_b32_sdwa v197, 1, v178 dst_sel:DWORD dst_unused:UNUSED_PAD src0_sel:DWORD src1_sel:WORD_1\n"
    "v_mfma_f32_16x16x32_bf16 v[176:179], a[112:115], a[236:239], 0\n"
    "s_waitcnt lgkmcnt(7)\n"
    "ds_read_u16 v194, v194\n"
    "buffer_load_dwordx4 v[148:151], v245, s[40:43], s89 offen offset:128\n"
    "v_add_f32_e32 v228, 0x3727c5ac, v228\n"
    "v_lshlrev_b32_sdwa v198, 1, v180 dst_sel:DWORD dst_unused:UNUSED_PAD src0_sel:DWORD src1_sel:WORD_1\n"
    "v_rsq_f32_e64 v228, v228\n"
    "v_lshlrev_b32_sdwa v199, 1, v182 dst_sel:DWORD dst_unused:UNUSED_PAD src0_sel:DWORD src1_sel:WORD_1\n"
    "v_mul_f32_e64 v228, s50, v228\n"
    "ds_write_b32 v254, v228\n"
    "ds_read_u16_d16_hi v255, v255\n"
    "v_mul_f32_e64 v224, v228, -v224\n"
    "v_or_b32_e32 v188, v188, v204\n"
    "v_mfma_f32_16x16x32_bf16 v[176:179], a[160:163], a[240:243], v[176:179]\n"
    "s_waitcnt lgkmcnt(8)\n"
    "ds_write_b32 v254, v224 offset:1024\n"
    "v_mfma_f32_16x16x32_bf16 v[176:179], a[72:75], a[244:247], v[176:179]\n"
    "ds_read_u16 v195, v195\n"
    "ds_read_b128 v[236:239], v244 offset:8192\n"
    "v_mfma_f32_16x16x32_bf16 v[180:183], a[116:119], a[236:239], 0\n"
    "ds_read_b128 v[152:155], v242 offset:0\n"
    "v_add_u32_e64 v254, s56, v242\n"
    "v_or_b32_e32 v189, v189, v205\n"
    "v_mfma_f32_16x16x32_bf16 v[180:183], a[164:167], a[240:243], v[180:183]\n"
    "s_waitcnt lgkmcnt(10)\n"
    "ds_read_b128 v[156:159], v242 offset:1024\n"
    "v_or_b32_e32 v190, v190, v206\n"
    "v_mfma_f32_16x16x32_bf16 v[112:115], a[40:43], v[184:187], v[112:115]\n"
    "s_waitcnt lgkmcnt(9)\n"
    "ds_read_u16_d16_hi v160, v160\n"
    "v_or_b32_e64 v191, v191, v207\n"
    "v_pk_fma_f16 v232, v192, v192, s48\n"
    "v_mfma_f32_16x16x32_bf16 v[172:175], a[252:255], a[232:235], v[172:175]\n"
    "ds_read_b128 v[164:167], v244 offset:10240\n"
    "v_pk_mul_f16 v232, v192, v232\n"
    "v_mfma_f32_16x16x32_bf16 v[180:183], a[252:255], a[244:247], v[180:183]\n"
    "ds_read_u16 v197, v197\n"
    "v_exp_f16_e64 v234, v232\n"
    "v_lshlrev_b32_sdwa v200, 1, v169 dst_sel:DWORD dst_unused:UNUSED_PAD src0_sel:DWORD src1_sel:WORD_1\n"
    "v_exp_f16_sdwa v234, v232 dst_sel:WORD_1 dst_unused:UNUSED_PRESERVE src0_sel:WORD_1\n"
    "v_mfma_f32_16x16x32_bf16 v[116:119], a[44:47], v[184:187], v[116:119]\n"
    "s_waitcnt lgkmcnt(9)\n"
    "ds_read_u16_d16_hi v161, v161\n"
    "v_or_b32_e32 v193, v193, v249\n"
    "v_pk_fma_f16 v234, v234, s49, s49\n"
    "v_lshlrev_b32_sdwa v201, 1, v171 dst_sel:DWORD dst_unused:UNUSED_PAD src0_sel:DWORD src1_sel:WORD_1\n"
    "v_mfma_f32_16x16x32_bf16 v[120:123], a[40:43], v[188:191], v[120:123]\n"
    "s_waitcnt lgkmcnt(8)\n"
    "s_barrier\n"
    "v_mfma_f32_16x16x32_bf16 v[124:127], a[44:47], v[188:191], v[124:127]\n"
    "s_waitcnt lgkmcnt(5)\n"
    "ds_read_b128 v[224:227], v254\n"
    "ds_read_b128 v[228:231], v254 offset:1024\n"
    "v_mfma_f32_16x16x32_bf16 v[128:131], v[236:239], v[184:187], v[128:131]\n"
    "s_waitcnt lgkmcnt(6)\n"
    "ds_read_u16 v198, v198\n"
    "ds_read_u16_d16_hi v162, v162\n"
    "v_smfmac_f32_32x32x32_bf16 v[96:111], a[248:251], v[152:159], v241\n"
    "ds_read_u16 v199, v199\n"
    "v_lshlrev_b32_e64 v254, 3, v244\n"
    "v_or_b32_e64 v194, v194, v255\n"
    "v_lshlrev_b32_sdwa v202, 1, v173 dst_sel:DWORD dst_unused:UNUSED_PAD src0_sel:DWORD src1_sel:WORD_1\n"
    "v_lshlrev_b32_sdwa v203, 1, v175 dst_sel:DWORD dst_unused:UNUSED_PAD src0_sel:DWORD src1_sel:WORD_1\n"
    "v_mfma_f32_16x16x32_bf16 v[136:139], v[236:239], v[188:191], v[136:139]\n"
    "s_waitcnt lgkmcnt(7)\n"
    "s_mov_b32 exec_hi, 0\n"
    "buffer_load_dword v233, v254, s[72:75], s53 offen sc1\n"
    "s_mov_b32 exec_hi, -1\n"
    "s_add_u32 s53, s53, 393216\n"
    "v_mfma_f32_16x16x32_bf16 v[96:99], a[32:35], v[184:187], v[96:99]\n"
    "s_nop 0\n"
    "ds_read_u16_d16_hi v163, v163\n"
    "ds_read_b128 v[152:155], v242 offset:2048\n"
    "v_mfma_f32_16x16x32_bf16 v[100:103], a[36:39], v[184:187], v[100:103]\n"
    "ds_read_b128 v[156:159], v242 offset:3072\n"
    "v_or_b32_e64 v195, v195, v160\n"
    "v_mfma_f32_16x16x32_bf16 v[132:135], v[164:167], v[184:187], v[132:135]\n"
    "ds_read_b128 a[252:255], v244 offset:38912\n"
    "v_lshlrev_b32_e32 v254, 3, v244\n"
    "v_mfma_f32_16x16x32_bf16 v[104:107], a[32:35], v[188:191], v[104:107]\n"
    "s_waitcnt lgkmcnt(8)\n"
    "ds_read_u16_d16_hi v200, v200\n"
    "v_or_b32_e32 v197, v197, v161\n"
    "v_mfma_f32_16x16x32_bf16 v[108:111], a[36:39], v[188:191], v[108:111]\n"
    "s_waitcnt lgkmcnt(8)\n"
    "v_lshlrev_b32_sdwa v184, 1, v168 dst_sel:DWORD dst_unused:UNUSED_PAD src0_sel:DWORD src1_sel:WORD_1\n"
    "v_lshlrev_b32_sdwa v185, 1, v170 dst_sel:DWORD dst_unused:UNUSED_PAD src0_sel:DWORD src1_sel:WORD_1\n"
    "v_lshlrev_b32_sdwa v186, 1, v172 dst_sel:DWORD dst_unused:UNUSED_PAD src0_sel:DWORD src1_sel:WORD_1\n"
    "v_lshlrev_b32_sdwa v187, 1, v174 dst_sel:DWORD dst_unused:UNUSED_PAD src0_sel:DWORD src1_sel:WORD_1\n"
    "v_rcp_f16_e64 v232, v234\n"
    "v_pk_fma_f32 v[64:65], v[64:65], v[224:225], v[228:229]\n"
    "v_pk_fma_f32 v[66:67], v[66:67], v[226:227], v[230:231]\n"
    "v_pk_fma_f32 v[68:69], v[68:69], v[224:225], v[228:229]\n"
    "v_pk_fma_f32 v[70:71], v[70:71], v[226:227], v[230:231]\n"
    "v_pk_fma_f32 v[72:73], v[72:73], v[224:225], v[228:229]\n"
    "v_pk_fma_f32 v[74:75], v[74:75], v[226:227], v[230:231]\n"
    "v_pk_fma_f32 v[76:77], v[76:77], v[224:225], v[228:229]\n"
    "v_pk_fma_f32 v[78:79], v[78:79], v[226:227], v[230:231]\n"
    "v_pk_fma_f32 v[80:81], v[80:81], v[224:225], v[228:229]\n"
    "v_pk_fma_f32 v[82:83], v[82:83], v[226:227], v[230:231]\n"
    "v_pk_fma_f32 v[84:85], v[84:85], v[224:225], v[228:229]\n"
    "v_pk_fma_f32 v[86:87], v[86:87], v[226:227], v[230:231]\n"
    "v_pk_fma_f32 v[88:89], v[88:89], v[224:225], v[228:229]\n"
    "v_pk_fma_f32 v[90:91], v[90:91], v[226:227], v[230:231]\n"
    "v_pk_fma_f32 v[92:93], v[92:93], v[224:225], v[228:229]\n"
    "v_pk_fma_f32 v[94:95], v[94:95], v[226:227], v[230:231]\n"
    "v_pk_fma_f32 v[0:1], v[0:1], v[224:225], v[228:229]\n"
    "v_pk_fma_f32 v[2:3], v[2:3], v[226:227], v[230:231]\n"
    "v_pk_fma_f32 v[4:5], v[4:5], v[224:225], v[228:229]\n"
    "v_pk_fma_f32 v[6:7], v[6:7], v[226:227], v[230:231]\n"
    "v_pk_fma_f32 v[8:9], v[8:9], v[224:225], v[228:229]\n"
    "v_pk_fma_f32 v[10:11], v[10:11], v[226:227], v[230:231]\n"
    "v_pk_fma_f32 v[12:13], v[12:13], v[224:225], v[228:229]\n"
    "v_pk_fma_f32 v[14:15], v[14:15], v[226:227], v[230:231]\n"
    "v_pk_fma_f32 v[16:17], v[16:17], v[224:225], v[228:229]\n"
    "v_pk_fma_f32 v[18:19], v[18:19], v[226:227], v[230:231]\n"
    "v_pk_fma_f32 v[20:21], v[20:21], v[224:225], v[228:229]\n"
    "v_pk_fma_f32 v[22:23], v[22:23], v[226:227], v[230:231]\n"
    "v_pk_fma_f32 v[24:25], v[24:25], v[224:225], v[228:229]\n"
    "v_pk_fma_f32 v[26:27], v[26:27], v[226:227], v[230:231]\n"
    "v_pk_fma_f32 v[28:29], v[28:29], v[224:225], v[228:229]\n"
    "v_pk_fma_f32 v[30:31], v[30:31], v[226:227], v[230:231]\n"
    "v_pk_fma_f32 v[32:33], v[32:33], v[224:225], v[228:229]\n"
    "v_pk_fma_f32 v[34:35], v[34:35], v[226:227], v[230:231]\n"
    "v_pk_fma_f32 v[36:37], v[36:37], v[224:225], v[228:229]\n"
    "v_pk_fma_f32 v[38:39], v[38:39], v[226:227], v[230:231]\n"
    "v_pk_fma_f32 v[40:41], v[40:41], v[224:225], v[228:229]\n"
    "v_pk_fma_f32 v[42:43], v[42:43], v[226:227], v[230:231]\n"
    "v_pk_fma_f32 v[44:45], v[44:45], v[224:225], v[228:229]\n"
    "v_pk_fma_f32 v[46:47], v[46:47], v[226:227], v[230:231]\n"
    "v_pk_fma_f32 v[48:49], v[48:49], v[224:225], v[228:229]\n"
    "v_pk_fma_f32 v[50:51], v[50:51], v[226:227], v[230:231]\n"
    "v_pk_fma_f32 v[52:53], v[52:53], v[224:225], v[228:229]\n"
    "v_pk_fma_f32 v[54:55], v[54:55], v[226:227], v[230:231]\n"
    "v_pk_fma_f32 v[56:57], v[56:57], v[224:225], v[228:229]\n"
    "v_pk_fma_f32 v[58:59], v[58:59], v[226:227], v[230:231]\n"
    "v_pk_fma_f32 v[60:61], v[60:61], v[224:225], v[228:229]\n"
    "v_pk_fma_f32 v[62:63], v[62:63], v[226:227], v[230:231]\n"
    "v_mfma_f32_16x16x32_bf16 v[140:143], v[164:167], v[188:191], v[140:143]\n"
    "s_waitcnt lgkmcnt(0)\n"
    "s_mov_b32 exec_hi, 0\n"
    "buffer_load_dword v233, v254, s[80:83], s85 offen sc1\n"
    "s_mov_b32 exec_hi, -1\n"
    "v_smfmac_f32_32x32x32_bf16 v[112:127], a[248:251], v[152:159], v241\n"
    "s_nop 0\n"
    "ds_read_u16 v184, v184\n"
    "ds_read_u16 v185, v185\n"
    "v_cvt_pk_bf16_f32 v208, v64, v68\n"
    "v_cvt_pk_bf16_f32 v212, v65, v69\n"
    "v_cvt_pk_bf16_f32 v216, v66, v70\n"
    "v_mfma_f32_16x16x32_bf16 v[168:171], a[120:123], a[224:227], 0\n"
    "ds_read_u16_d16_hi v201, v201\n"
    "ds_read_u16 v186, v186\n"
    "v_mfma_f32_16x16x32_bf16 v[168:171], a[168:171], a[228:231], v[168:171]\n"
    "v_cvt_pk_bf16_f32 v220, v67, v71\n"
    "v_cvt_pk_bf16_f32 v209, v72, v76\n"
    "v_mfma_f32_16x16x32_bf16 v[168:171], a[76:79], a[232:235], v[168:171]\n"
    "ds_read_u16_d16_hi v202, v202\n"
    "v_cvt_pk_bf16_f32 v213, v73, v77\n"
    "v_cvt_pk_bf16_f32 v217, v74, v78\n"
    "v_cvt_pk_bf16_f32 v221, v75, v79\n"
    "v_cvt_pk_bf16_f32 v210, v80, v84\n"
    "v_cvt_pk_bf16_f32 v214, v81, v85\n"
    "v_cvt_pk_bf16_f32 v218, v82, v86\n"
    "v_cvt_pk_bf16_f32 v222, v83, v87\n"
    "v_cvt_pk_bf16_f32 v211, v88, v92\n"
    "v_cvt_pk_bf16_f32 v215, v89, v93\n"
    "ds_write_b128 v253, v[208:211] offset:26688\n"
    "v_cvt_pk_bf16_f32 v219, v90, v94\n"
    "v_cvt_pk_bf16_f32 v223, v91, v95\n"
    "v_cvt_pk_bf16_f32 v80, v0, v4\n"
    "v_cvt_pk_bf16_f32 v84, v1, v5\n"
    "v_cvt_pk_bf16_f32 v88, v2, v6\n"
    "v_cvt_pk_bf16_f32 v92, v3, v7\n"
    "ds_write_b128 v253, v[212:215] offset:29760\n"
    "v_cvt_pk_bf16_f32 v81, v8, v12\n"
    "v_cvt_pk_bf16_f32 v85, v9, v13\n"
    "v_cvt_pk_bf16_f32 v89, v10, v14\n"
    "v_cvt_pk_bf16_f32 v93, v11, v15\n"
    "v_cvt_pk_bf16_f32 v82, v16, v20\n"
    "v_cvt_pk_bf16_f32 v86, v17, v21\n"
    "v_cvt_pk_bf16_f32 v90, v18, v22\n"
    "v_cvt_pk_bf16_f32 v94, v19, v23\n"
    "v_cvt_pk_bf16_f32 v83, v24, v28\n"
    "v_cvt_pk_bf16_f32 v87, v25, v29\n"
    "v_cvt_pk_bf16_f32 v91, v26, v30\n"
    "v_or_b32_e64 v198, v198, v162\n"
    "v_cvt_pk_bf16_f32 v95, v27, v31\n"
    "v_rcp_f16_sdwa v232, v234 dst_sel:WORD_1 dst_unused:UNUSED_PRESERVE src0_sel:WORD_1\n"
    "v_mfma_f32_16x16x32_bf16 v[172:175], a[124:127], a[224:227], 0\n"
    "ds_read_b128 v[236:239], v244 offset:12288\n"
    "ds_read_u16 v187, v187\n"
    "v_mfma_f32_16x16x32_bf16 v[172:175], a[172:175], a[228:231], v[172:175]\n"
    "ds_write_b128 v253, v[80:83] offset:24640\n"
    "v_lshlrev_b32_sdwa v188, 1, v176 dst_sel:DWORD dst_unused:UNUSED_PAD src0_sel:DWORD src1_sel:WORD_1\n"
    "v_lshlrev_b32_sdwa v204, 1, v177 dst_sel:DWORD dst_unused:UNUSED_PAD src0_sel:DWORD src1_sel:WORD_1\n"
    "v_lshlrev_b32_sdwa v189, 1, v178 dst_sel:DWORD dst_unused:UNUSED_PAD src0_sel:DWORD src1_sel:WORD_1\n"
    "v_lshlrev_b32_sdwa v205, 1, v179 dst_sel:DWORD dst_unused:UNUSED_PAD src0_sel:DWORD src1_sel:WORD_1\n"
    "v_lshlrev_b32_sdwa v190, 1, v180 dst_sel:DWORD dst_unused:UNUSED_PAD src0_sel:DWORD src1_sel:WORD_1\n"
    "v_lshlrev_b32_sdwa v206, 1, v181 dst_sel:DWORD dst_unused:UNUSED_PAD src0_sel:DWORD src1_sel:WORD_1\n"
    "v_lshlrev_b32_sdwa v191, 1, v182 dst_sel:DWORD dst_unused:UNUSED_PAD src0_sel:DWORD src1_sel:WORD_1\n"
    "v_lshlrev_b32_sdwa v207, 1, v183 dst_sel:DWORD dst_unused:UNUSED_PAD src0_sel:DWORD src1_sel:WORD_1\n"
    "v_fma_mix_f32 v234, v192, v232, 0 op_sel_hi:[1,1,0]\n"
    "v_mfma_f32_16x16x32_bf16 v[172:175], a[252:255], a[232:235], v[172:175]\n"
    "s_waitcnt lgkmcnt(3)\n"
    "ds_read_u16_d16_hi v203, v203\n"
    "ds_write_b128 v253, v[84:87] offset:27712\n"
    "v_cvt_pk_bf16_f32 v64, v32, v36\n"
    "v_or_b32_e32 v199, v199, v163\n"
    "v_cvt_pk_bf16_f32 v68, v33, v37\n"
    "v_cvt_pk_bf16_f32 v72, v34, v38\n"
    "v_fma_mix_f32 v232, v192, v232, 0 op_sel:[1,1,0] op_sel_hi:[1,1,0]\n"
    "v_mfma_f32_16x16x32_bf16 v[176:179], a[120:123], a[236:239], 0\n"
    "v_cvt_pk_bf16_f32 v76, v35, v39\n"
    "v_cvt_pk_bf16_f32 v65, v40, v44\n"
    "v_cvt_pk_bf16_f32 v192, v234, v232\n"
    "v_mfma_f32_16x16x32_bf16 v[176:179], a[168:171], a[240:243], v[176:179]\n"
    "ds_read_b128 v[164:167], v244 offset:14336\n"
    "ds_read_u16 v188, v188\n"
    "v_mfma_f32_16x16x32_bf16 v[176:179], a[76:79], a[244:247], v[176:179]\n"
    "ds_read_u16_d16_hi v204, v204\n"
    "ds_read_u16 v189, v189\n"
    "v_cvt_pk_bf16_f32 v69, v41, v45\n"
    "v_cvt_pk_bf16_f32 v73, v42, v46\n"
    "v_pk_fma_f16 v232, v196, v196, s48\n"
    "v_mfma_f32_16x16x32_bf16 v[180:183], a[124:127], a[236:239], 0\n"
    "ds_read_b128 v[152:155], v242 offset:4096\n"
    "v_cvt_pk_bf16_f32 v77, v43, v47\n"
    "v_cvt_pk_bf16_f32 v66, v48, v52\n"
    "v_cvt_pk_bf16_f32 v70, v49, v53\n"
    "v_pk_mul_f16 v232, v196, v232\n"
    "v_mfma_f32_16x16x32_bf16 v[180:183], a[172:175], a[240:243], v[180:183]\n"
    "ds_read_b128 v[156:159], v242 offset:5120\n"
    "ds_read_u16_d16_hi v205, v205\n"
    "v_cvt_pk_bf16_f32 v74, v50, v54\n"
    "v_cvt_pk_bf16_f32 v78, v51, v55\n"
    "v_exp_f16_e64 v234, v232\n"
    "v_mfma_f32_16x16x32_bf16 v[180:183], a[252:255], a[244:247], v[180:183]\n"
    "ds_read_u16 v190, v190\n"
    "v_cvt_pk_bf16_f32 v67, v56, v60\n"
    "v_cvt_pk_bf16_f32 v71, v57, v61\n"
    "v_exp_f16_sdwa v234, v232 dst_sel:WORD_1 dst_unused:UNUSED_PRESERVE src0_sel:WORD_1\n"
    "v_cvt_pk_bf16_f32 v75, v58, v62\n"
    "v_cvt_pk_bf16_f32 v79, v59, v63\n"
    "v_pk_fma_f16 v234, v234, s49, s49\n"
    "v_or_b32_e32 v184, v184, v200\n"
    "v_mfma_f32_16x16x32_bf16 v[96:99], a[176:179], v[192:195], v[96:99]\n"
    "s_waitcnt lgkmcnt(7)\n"
    "ds_write_b128 v253, v[64:67] offset:25664\n"
    "v_rcp_f16_e64 v232, v234\n"
    "v_or_b32_e64 v185, v185, v201\n"
    "v_rcp_f16_sdwa v232, v234 dst_sel:WORD_1 dst_unused:UNUSED_PRESERVE src0_sel:WORD_1\n"
    "v_lshlrev_b32_sdwa v249, 1, v171 dst_sel:DWORD dst_unused:UNUSED_PAD src0_sel:DWORD src1_sel:WORD_1\n"
    "v_mfma_f32_16x16x32_bf16 v[100:103], a[180:183], v[192:195], v[100:103]\n"
    "s_nop 0\n"
    "ds_write_b128 v253, v[68:71] offset:28736\n"
    "v_mfma_f32_16x16x32_bf16 v[112:115], a[184:187], v[192:195], v[112:115]\n"
    "s_waitcnt lgkmcnt(7)\n"
    "v_fma_mix_f32 v234, v196, v232, 0 op_sel_hi:[1,1,0]\n"
    "v_fma_mix_f32 v232, v196, v232, 0 op_sel:[1,1,0] op_sel_hi:[1,1,0]\n"
    "v_mfma_f32_16x16x32_bf16 v[116:119], a[188:191], v[192:195], v[116:119]\n"
    "v_cvt_pk_bf16_f32 v196, v234, v232\n"
    "v_or_b32_e64 v186, v186, v202\n"
    "v_mfma_f32_16x16x32_bf16 v[128:131], v[236:239], v[192:195], v[128:131]\n"
    "ds_read_u16_d16_hi v206, v206\n"
    "ds_read_u16 v191, v191\n"
    "v_mfma_f32_16x16x32_bf16 v[132:135], v[164:167], v[192:195], v[132:135]\n"
    "s_nop 0\n"
    "ds_read_u16_d16_hi v207, v207\n"
    "ds_read_b128 a[252:255], v244 offset:32768\n"
    "v_mfma_f32_16x16x32_bf16 v[104:107], a[176:179], v[196:199], v[104:107]\n"
    "s_waitcnt lgkmcnt(7)\n"
    "v_cvt_pk_f16_f32 v192, v168, v169\n"
    "v_lshlrev_b32_sdwa v193, 1, v170 dst_sel:DWORD dst_unused:UNUSED_PAD src0_sel:DWORD src1_sel:WORD_1\n"
    "v_mfma_f32_16x16x32_bf16 v[108:111], a[180:183], v[196:199], v[108:111]\n"
    "ds_read_u16 v193, v193\n"
    "v_lshlrev_b32_sdwa v194, 1, v172 dst_sel:DWORD dst_unused:UNUSED_PAD src0_sel:DWORD src1_sel:WORD_1\n"
    "v_mfma_f32_16x16x32_bf16 v[120:123], a[184:187], v[196:199], v[120:123]\n"
    "ds_read_u16_d16_hi v249, v249\n"
    "v_lshlrev_b32_sdwa v255, 1, v173 dst_sel:DWORD dst_unused:UNUSED_PAD src0_sel:DWORD src1_sel:WORD_1\n"
    "v_mfma_f32_16x16x32_bf16 v[124:127], a[188:191], v[196:199], v[124:127]\n"
    "v_lshlrev_b32_sdwa v195, 1, v174 dst_sel:DWORD dst_unused:UNUSED_PAD src0_sel:DWORD src1_sel:WORD_1\n"
    "v_lshlrev_b32_sdwa v160, 1, v175 dst_sel:DWORD dst_unused:UNUSED_PAD src0_sel:DWORD src1_sel:WORD_1\n"
    "v_mfma_f32_16x16x32_bf16 v[136:139], v[236:239], v[196:199], v[136:139]\n"
    "s_waitcnt lgkmcnt(6)\n"
    "s_barrier\n"
    "v_mfma_f32_16x16x32_bf16 v[140:143], v[164:167], v[196:199], v[140:143]\n"
    "ds_read_b128 a[224:227], v242 offset:24640\n"
    "ds_read_b128 a[228:231], v242 offset:25664\n"
    "v_cvt_pk_f16_f32 v196, v176, v177\n"
    "v_lshlrev_b32_sdwa v197, 1, v178 dst_sel:DWORD dst_unused:UNUSED_PAD src0_sel:DWORD src1_sel:WORD_1\n"
    "v_lshlrev_b32_sdwa v161, 1, v179 dst_sel:DWORD dst_unused:UNUSED_PAD src0_sel:DWORD src1_sel:WORD_1\n"
    "v_lshlrev_b32_sdwa v198, 1, v180 dst_sel:DWORD dst_unused:UNUSED_PAD src0_sel:DWORD src1_sel:WORD_1\n"
    "v_smfmac_f32_32x32x32_bf16 v[128:143], a[248:251], v[152:159], v241\n"
    "s_nop 0\n"
    "s_waitcnt lgkmcnt(6)\n"
    "ds_read_b128 a[232:235], v242 offset:26688\n"
    "ds_read_b128 a[236:239], v242 offset:27712\n"
    "v_lshlrev_b32_sdwa v162, 1, v181 dst_sel:DWORD dst_unused:UNUSED_PAD src0_sel:DWORD src1_sel:WORD_1\n"
    "v_lshlrev_b32_sdwa v199, 1, v182 dst_sel:DWORD dst_unused:UNUSED_PAD src0_sel:DWORD src1_sel:WORD_1\n"
    "v_or_b32_e64 v187, v187, v203\n"
    "v_lshlrev_b32_sdwa v163, 1, v183 dst_sel:DWORD dst_unused:UNUSED_PAD src0_sel:DWORD src1_sel:WORD_1\n"
    "v_or_b32_e64 v188, v188, v204\n"
    "ds_read_b128 a[240:243], v242 offset:28736\n"
    "ds_read_b128 a[244:247], v242 offset:29760\n"
    "v_or_b32_e64 v189, v189, v205\n"
    "v_or_b32_e32 v190, v190, v206\n"
    "v_mfma_f32_16x16x32_bf16 v[96:99], a[192:195], v[184:187], v[96:99]\n"
    "s_waitcnt lgkmcnt(8)\n"
    "ds_read_u16 v194, v194\n"
    "ds_read_u16_d16_hi v255, v255\n"
    "v_mfma_f32_16x16x32_bf16 v[100:103], a[196:199], v[184:187], v[100:103]\n"
    "ds_read_u16 v195, v195\n"
    "v_or_b32_e32 v191, v191, v207\n"
    "v_mfma_f32_16x16x32_bf16 v[112:115], a[200:203], v[184:187], v[112:115]\n"
    "s_waitcnt lgkmcnt(8)\n"
    "ds_read_b128 v[236:239], v244 offset:16384\n"
    "v_or_b32_e32 v193, v193, v249\n"
    "v_mfma_f32_16x16x32_bf16 v[168:171], a[80:83], a[224:227], 0\n"
    "s_waitcnt lgkmcnt(8)\n"
    "ds_read_u16_d16_hi v160, v160\n"
    "v_pk_fma_f16 v232, v192, v192, s48\n"
    "v_mfma_f32_16x16x32_bf16 v[168:171], a[128:131], a[228:231], v[168:171]\n"
    "v_pk_mul_f16 v232, v192, v232\n"
    "v_exp_f16_e32 v234, v232\n"
    "v_mfma_f32_16x16x32_bf16 v[172:175], a[84:87], a[224:227], 0\n"
    "s_waitcnt lgkmcnt(7)\n"
    "ds_read_b128 v[164:167], v244 offset:18432\n"
    "ds_read_u16 v197, v197\n"
    "v_mfma_f32_16x16x32_bf16 v[172:175], a[132:135], a[228:231], v[172:175]\n"
    "v_exp_f16_sdwa v234, v232 dst_sel:WORD_1 dst_unused:UNUSED_PRESERVE src0_sel:WORD_1\n"
    "v_mfma_f32_16x16x32_bf16 v[172:175], a[252:255], a[232:235], v[172:175]\n"
    "s_waitcnt lgkmcnt(7)\n"
    "s_barrier\n"
    "v_mfma_f32_16x16x32_bf16 v[168:171], a[48:51], a[232:235], v[168:171]\n"
    "ds_read_u16_d16_hi v161, v161\n"
    "ds_write_b128 v253, v[72:75] offset:1024\n"
    "v_mfma_f32_16x16x32_bf16 v[176:179], a[80:83], a[236:239], 0\n"
    "s_waitcnt lgkmcnt(7)\n"
    "v_or_b32_e32 v194, v194, v255\n"
    "v_pk_fma_f16 v234, v234, s49, s49\n"
    "v_mfma_f32_16x16x32_bf16 v[176:179], a[128:131], a[240:243], v[176:179]\n"
    "s_waitcnt lgkmcnt(5)\n"
    "v_rcp_f16_e32 v232, v234\n"
    "v_mfma_f32_16x16x32_bf16 v[176:179], a[48:51], a[244:247], v[176:179]\n"
    "ds_read_u16 v198, v198\n"
    "ds_write_b128 v253, v[76:79] offset:4096\n"
    "v_mfma_f32_16x16x32_bf16 v[180:183], a[84:87], a[236:239], 0\n"
    "s_waitcnt lgkmcnt(5)\n"
    "v_or_b32_e32 v195, v195, v160\n"
    "v_mfma_f32_16x16x32_bf16 v[180:183], a[132:135], a[240:243], v[180:183]\n"
    "v_rcp_f16_sdwa v232, v234 dst_sel:WORD_1 dst_unused:UNUSED_PRESERVE src0_sel:WORD_1\n"
    "v_mfma_f32_16x16x32_bf16 v[180:183], a[252:255], a[244:247], v[180:183]\n"
    "ds_read_u16_d16_hi v162, v162\n"
    "ds_read_u16 v199, v199\n"
    "v_mfma_f32_16x16x32_bf16 v[116:119], a[204:207], v[184:187], v[116:119]\n"
    "ds_read_u16_d16_hi v163, v163\n"
    "v_fma_mix_f32 v234, v192, v232, 0 op_sel_hi:[1,1,0]\n"
    "v_mfma_f32_16x16x32_bf16 v[128:131], v[236:239], v[184:187], v[128:131]\n"
    "s_waitcnt lgkmcnt(5)\n"
    "v_or_b32_e32 v197, v197, v161\n"
    "v_fma_mix_f32 v232, v192, v232, 0 op_sel:[1,1,0] op_sel_hi:[1,1,0]\n"
    "v_mfma_f32_16x16x32_bf16 v[132:135], v[164:167], v[184:187], v[132:135]\n"
    "ds_write_b128 v253, v[88:91] offset:0\n"
    "v_mfma_f32_16x16x32_bf16 v[104:107], a[192:195], v[188:191], v[104:107]\n"
    "v_cvt_pk_bf16_f32 v192, v234, v232\n"
    "v_pk_fma_f16 v232, v196, v196, s48\n"
    "v_pk_mul_f16 v232, v196, v232\n"
    "v_exp_f16_e64 v234, v232\n"
    "v_mfma_f32_16x16x32_bf16 v[108:111], a[196:199], v[188:191], v[108:111]\n"
    "ds_write_b128 v253, v[92:95] offset:3072\n"
    "v_mfma_f32_16x16x32_bf16 v[120:123], a[200:203], v[188:191], v[120:123]\n"
    "v_exp_f16_sdwa v234, v232 dst_sel:WORD_1 dst_unused:UNUSED_PRESERVE src0_sel:WORD_1\n"
    "v_mfma_f32_16x16x32_bf16 v[124:127], a[204:207], v[188:191], v[124:127]\n"
    "v_mfma_f32_16x16x32_bf16 v[136:139], v[236:239], v[188:191], v[136:139]\n"
    "s_mov_b32 s59, s58\n"
    "s_add_u32 s57, s57, 1572864\n"
    "s_add_u32 s58, s58, 1572864\n"
    "s_mov_b32 s70, s46\n"
    "s_add_u32 s88, s57, 98304\n"
    "s_add_u32 s89, s58, 98304\n"
    "s_add_u32 s39, s59, 98304\n"
    "s_mov_b32 s82, 0\n"
    "s_add_u32 s64, s64, 1\n"
    "s_cmp_lt_u32 s64, 2\n"
    "s_cselect_b64 s[72:73], s[72:73], s[76:77]\n"
    "s_cselect_b32 s74, s74, s78\n"
    "s_cselect_b32 s53, s53, s79\n"
    "s_cmp_lg_u32 s64, 3\n"
    "s_cbranch_scc1 1b\n"
    ".Lreplk_last:\n"
    "v_lshlrev_b32_sdwa v184, 1, v168 dst_sel:DWORD dst_unused:UNUSED_PAD src0_sel:DWORD src1_sel:WORD_1\n"
    "v_lshlrev_b32_sdwa v200, 1, v169 dst_sel:DWORD dst_unused:UNUSED_PAD src0_sel:DWORD src1_sel:WORD_1\n"
    "v_lshlrev_b32_sdwa v185, 1, v170 dst_sel:DWORD dst_unused:UNUSED_PAD src0_sel:DWORD src1_sel:WORD_1\n"
    "v_lshlrev_b32_sdwa v201, 1, v171 dst_sel:DWORD dst_unused:UNUSED_PAD src0_sel:DWORD src1_sel:WORD_1\n"
    "v_mfma_f32_16x16x32_bf16 v[140:143], v[164:167], v[188:191], v[140:143]\n"
    "s_waitcnt vmcnt(2) lgkmcnt(3)\n"
    "ds_read_u16 v184, v184\n"
    "ds_read_b128 v[56:59], v244 offset:36864\n"
    "v_mfma_f32_16x16x32_bf16 v[168:171], a[88:91], a[224:227], 0\n"
    "ds_read_u16_d16_hi v200, v200\n"
    "ds_read_u16 v185, v185\n"
    "v_mfma_f32_16x16x32_bf16 v[168:171], a[136:139], a[228:231], v[168:171]\n"
    "s_waitcnt lgkmcnt(5)\n"
    "v_lshlrev_b32_sdwa v186, 1, v172 dst_sel:DWORD dst_unused:UNUSED_PAD src0_sel:DWORD src1_sel:WORD_1\n"
    "v_lshlrev_b32_sdwa v202, 1, v173 dst_sel:DWORD dst_unused:UNUSED_PAD src0_sel:DWORD src1_sel:WORD_1\n"
    "v_mfma_f32_16x16x32_bf16 v[168:171], a[52:55], a[232:235], v[168:171]\n"
    "ds_read_u16_d16_hi v201, v201\n"
    "v_lshlrev_b32_sdwa v187, 1, v174 dst_sel:DWORD dst_unused:UNUSED_PAD src0_sel:DWORD src1_sel:WORD_1\n"
    "v_smfmac_f32_32x32x32_bf16 v[128:143], a[248:251], v[144:151], v241\n"
    "ds_read_u16 v186, v186\n"
    "v_lshlrev_b32_sdwa v203, 1, v175 dst_sel:DWORD dst_unused:UNUSED_PAD src0_sel:DWORD src1_sel:WORD_1\n"
    "v_lshlrev_b32_sdwa v188, 1, v176 dst_sel:DWORD dst_unused:UNUSED_PAD src0_sel:DWORD src1_sel:WORD_1\n"
    "v_lshlrev_b32_sdwa v204, 1, v177 dst_sel:DWORD dst_unused:UNUSED_PAD src0_sel:DWORD src1_sel:WORD_1\n"
    "v_lshlrev_b32_sdwa v189, 1, v178 dst_sel:DWORD dst_unused:UNUSED_PAD src0_sel:DWORD src1_sel:WORD_1\n"
    "v_mfma_f32_16x16x32_bf16 v[172:175], a[92:95], a[224:227], 0\n"
    "ds_read_b128 v[236:239], v244 offset:20480\n"
    "v_lshlrev_b32_sdwa v205, 1, v179 dst_sel:DWORD dst_unused:UNUSED_PAD src0_sel:DWORD src1_sel:WORD_1\n"
    "v_mfma_f32_16x16x32_bf16 v[172:175], a[140:143], a[228:231], v[172:175]\n"
    "ds_read_u16_d16_hi v202, v202\n"
    "buffer_load_dwordx4 v[152:155], v245, s[40:43], s57 offen offset:64\n"
    "buffer_load_dwordx4 v[144:147], v245, s[40:43], s57 offen offset:0\n"
    "buffer_load_dwordx4 v[208:211], v245, s[40:43], s57 offen offset:128\n"
    "v_mfma_f32_16x16x32_bf16 v[176:179], a[88:91], a[236:239], 0\n"
    "ds_read_u16 v187, v187\n"
    "v_lshlrev_b32_sdwa v190, 1, v180 dst_sel:DWORD dst_unused:UNUSED_PAD src0_sel:DWORD src1_sel:WORD_1\n"
    "v_mfma_f32_16x16x32_bf16 v[176:179], a[136:139], a[240:243], v[176:179]\n"
    "s_nop 0\n"
    "v_lshlrev_b32_sdwa v206, 1, v181 dst_sel:DWORD dst_unused:UNUSED_PAD src0_sel:DWORD src1_sel:WORD_1\n"
    "v_lshlrev_b32_sdwa v191, 1, v182 dst_sel:DWORD dst_unused:UNUSED_PAD src0_sel:DWORD src1_sel:WORD_1\n"
    "v_mfma_f32_16x16x32_bf16 v[176:179], a[52:55], a[244:247], v[176:179]\n"
    "s_waitcnt lgkmcnt(7)\n"
    "ds_read_b128 v[164:167], v244 offset:22528\n"
    "v_lshlrev_b32_sdwa v207, 1, v183 dst_sel:DWORD dst_unused:UNUSED_PAD src0_sel:DWORD src1_sel:WORD_1\n"
    "v_pk_fma_f16 v234, v234, s49, s49\n"
    "v_mfma_f32_16x16x32_bf16 v[172:175], v[56:59], a[232:235], v[172:175]\n"
    "ds_read_u16_d16_hi v203, v203\n"
    "v_or_b32_e64 v198, v198, v162\n"
    "v_or_b32_e64 v199, v199, v163\n"
    "v_rcp_f16_e32 v232, v234\n"
    "v_mfma_f32_16x16x32_bf16 v[180:183], a[92:95], a[236:239], 0\n"
    "s_waitcnt lgkmcnt(8)\n"
    "v_rcp_f16_sdwa v232, v234 dst_sel:WORD_1 dst_unused:UNUSED_PRESERVE src0_sel:WORD_1\n"
    "v_mfma_f32_16x16x32_bf16 v[180:183], a[140:143], a[240:243], v[180:183]\n"
    "v_fma_mix_f32 v234, v196, v232, 0 op_sel_hi:[1,1,0]\n"
    "v_fma_mix_f32 v232, v196, v232, 0 op_sel:[1,1,0] op_sel_hi:[1,1,0]\n"
    "v_mfma_f32_16x16x32_bf16 v[180:183], v[56:59], a[244:247], v[180:183]\n"
    "ds_write_b128 v253, v[216:219] offset:2048\n"
    "v_cvt_pk_bf16_f32 v196, v234, v232\n"
    "v_or_b32_e32 v184, v184, v200\n"
    "v_lshlrev_b32_sdwa v249, 1, v171 dst_sel:DWORD dst_unused:UNUSED_PAD src0_sel:DWORD src1_sel:WORD_1\n"
    "v_mfma_f32_16x16x32_bf16 v[96:99], a[208:211], v[192:195], v[96:99]\n"
    "s_waitcnt lgkmcnt(5)\n"
    "buffer_load_dwordx4 v[156:159], v245, s[40:43], s88 offen offset:64\n"
    "buffer_load_dwordx4 v[148:151], v245, s[40:43], s88 offen offset:0\n"
    "buffer_load_dwordx4 v[212:215], v245, s[40:43], s88 offen offset:128\n"
    "ds_write_b128 v253, v[220:223] offset:5120\n"
    "v_mfma_f32_16x16x32_bf16 v[100:103], a[212:215], v[192:195], v[100:103]\n"
    "v_or_b32_e64 v185, v185, v201\n"
    "v_lshlrev_b32_sdwa v255, 1, v173 dst_sel:DWORD dst_unused:UNUSED_PAD src0_sel:DWORD src1_sel:WORD_1\n"
    "v_mfma_f32_16x16x32_bf16 v[112:115], a[216:219], v[192:195], v[112:115]\n"
    "s_waitcnt lgkmcnt(5)\n"
    "v_or_b32_e64 v186, v186, v202\n"
    "v_lshlrev_b32_sdwa v160, 1, v175 dst_sel:DWORD dst_unused:UNUSED_PAD src0_sel:DWORD src1_sel:WORD_1\n"
    "v_mfma_f32_16x16x32_bf16 v[116:119], a[220:223], v[192:195], v[116:119]\n"
    "s_waitcnt lgkmcnt(3)\n"
    "ds_read_u16 v188, v188\n"
    "ds_read_u16_d16_hi v204, v204\n"
    "v_mfma_f32_16x16x32_bf16 v[128:131], v[236:239], v[192:195], v[128:131]\n"
    "ds_read_u16 v189, v189\n"
    "ds_read_u16_d16_hi v205, v205\n"
    "v_mfma_f32_16x16x32_bf16 v[132:135], v[164:167], v[192:195], v[132:135]\n"
    "s_nop 0\n"
    "v_cvt_pk_f16_f32 v192, v168, v169\n"
    "v_lshlrev_b32_sdwa v193, 1, v170 dst_sel:DWORD dst_unused:UNUSED_PAD src0_sel:DWORD src1_sel:WORD_1\n"
    "v_mfma_f32_16x16x32_bf16 v[104:107], a[208:211], v[196:199], v[104:107]\n"
    "s_waitcnt lgkmcnt(5)\n"
    "ds_read_u16 v190, v190\n"
    "v_lshlrev_b32_sdwa v194, 1, v172 dst_sel:DWORD dst_unused:UNUSED_PAD src0_sel:DWORD src1_sel:WORD_1\n"
    "v_mfma_f32_16x16x32_bf16 v[108:111], a[212:215], v[196:199], v[108:111]\n"
    "v_lshlrev_b32_sdwa v195, 1, v174 dst_sel:DWORD dst_unused:UNUSED_PAD src0_sel:DWORD src1_sel:WORD_1\n"
    "v_lshlrev_b32_sdwa v161, 1, v179 dst_sel:DWORD dst_unused:UNUSED_PAD src0_sel:DWORD src1_sel:WORD_1\n"
    "v_mfma_f32_16x16x32_bf16 v[120:123], a[216:219], v[196:199], v[120:123]\n"
    "ds_read_u16_d16_hi v206, v206\n"
    "v_lshlrev_b32_sdwa v162, 1, v181 dst_sel:DWORD dst_unused:UNUSED_PAD src0_sel:DWORD src1_sel:WORD_1\n"
    "v_mfma_f32_16x16x32_bf16 v[124:127], a[220:223], v[196:199], v[124:127]\n"
    "v_or_b32_e64 v187, v187, v203\n"
    "v_lshlrev_b32_sdwa v163, 1, v183 dst_sel:DWORD dst_unused:UNUSED_PAD src0_sel:DWORD src1_sel:WORD_1\n"
    "v_mfma_f32_16x16x32_bf16 v[136:139], v[236:239], v[196:199], v[136:139]\n"
    "ds_read_u16 v191, v191\n"
    "v_cvt_pk_bf16_f32 v96, v96, v97\n"
    "v_cvt_pk_bf16_f32 v97, v98, v99\n"
    "v_mfma_f32_16x16x32_bf16 v[140:143], v[164:167], v[196:199], v[140:143]\n"
    "ds_read_u16_d16_hi v207, v207\n"
    "v_cvt_pk_f16_f32 v196, v176, v177\n"
    "v_lshlrev_b32_sdwa v197, 1, v178 dst_sel:DWORD dst_unused:UNUSED_PAD src0_sel:DWORD src1_sel:WORD_1\n"
    "v_lshlrev_b32_sdwa v198, 1, v180 dst_sel:DWORD dst_unused:UNUSED_PAD src0_sel:DWORD src1_sel:WORD_1\n"
    "v_lshlrev_b32_sdwa v199, 1, v182 dst_sel:DWORD dst_unused:UNUSED_PAD src0_sel:DWORD src1_sel:WORD_1\n"
    "v_mfma_f32_16x16x32_bf16 v[168:171], a[96:99], a[224:227], 0\n"
    "ds_read_u16 v193, v193\n"
    "v_cvt_pk_bf16_f32 v98, v100, v101\n"
    "v_mfma_f32_16x16x32_bf16 v[168:171], a[144:147], a[228:231], v[168:171]\n"
    "v_cvt_pk_bf16_f32 v99, v102, v103\n"
    "v_cvt_pk_bf16_f32 v224, v112, v113\n"
    "v_mfma_f32_16x16x32_bf16 v[168:171], a[56:59], a[232:235], v[168:171]\n"
    "s_waitcnt lgkmcnt(7)\n"
    "ds_read_u16_d16_hi v249, v249\n"
    "buffer_store_dwordx4 v[96:99], v245, s[68:71], s59 offen offset:0 sc1\n"
    "v_mfma_f32_16x16x32_bf16 v[172:175], a[100:103], a[224:227], 0\n"
    "s_nop 0\n"
    "ds_read_u16 v194, v194\n"
    "v_or_b32_e64 v188, v188, v204\n"
    "v_mfma_f32_16x16x32_bf16 v[172:175], a[148:151], a[228:231], v[172:175]\n"
    "v_cvt_pk_bf16_f32 v225, v114, v115\n"
    "v_cvt_pk_bf16_f32 v226, v116, v117\n"
    "v_mfma_f32_16x16x32_bf16 v[172:175], a[60:63], a[232:235], v[172:175]\n"
    "s_waitcnt lgkmcnt(7)\n"
    "ds_read_u16_d16_hi v255, v255\n"
    "v_or_b32_e32 v189, v189, v205\n"
    "v_mfma_f32_16x16x32_bf16 v[176:179], a[96:99], a[236:239], 0\n"
    "ds_read_b128 v[0:3], v244 offset:0\n"
    "v_cvt_pk_bf16_f32 v227, v118, v119\n"
    "v_mfma_f32_16x16x32_bf16 v[176:179], a[144:147], a[240:243], v[176:179]\n"
    "ds_read_u16 v195, v195\n"
    "v_cvt_pk_bf16_f32 v228, v128, v129\n"
    "v_mfma_f32_16x16x32_bf16 v[176:179], a[56:59], a[244:247], v[176:179]\n"
    "s_waitcnt lgkmcnt(8)\n"
    "v_or_b32_e32 v190, v190, v206\n"
    "v_cvt_pk_bf16_f32 v229, v130, v131\n"
    "v_mfma_f32_16x16x32_bf16 v[180:183], a[100:103], a[236:239], 0\n"
    "ds_read_b128 v[4:7], v244 offset:2048\n"
    "v_cvt_pk_bf16_f32 v230, v132, v133\n"
    "v_mfma_f32_16x16x32_bf16 v[180:183], a[148:151], a[240:243], v[180:183]\n"
    "ds_read_u16_d16_hi v160, v160\n"
    "v_cvt_pk_bf16_f32 v231, v134, v135\n"
    "v_mfma_f32_16x16x32_bf16 v[180:183], a[60:63], a[244:247], v[180:183]\n"
    "v_cvt_pk_bf16_f32 v216, v104, v105\n"
    "v_cvt_pk_bf16_f32 v217, v106, v107\n"
    "v_mfma_f32_16x16x32_bf16 v[96:99], a[0:3], v[184:187], 0\n"
    "s_waitcnt lgkmcnt(8)\n"
    "ds_read_u16 v197, v197\n"
    "v_or_b32_e32 v191, v191, v207\n"
    "v_cvt_pk_bf16_f32 v218, v108, v109\n"
    "v_mfma_f32_16x16x32_bf16 v[100:103], a[4:7], v[184:187], 0\n"
    "ds_read_u16_d16_hi v161, v161\n"
    "v_cvt_pk_bf16_f32 v219, v110, v111\n"
    "v_cvt_pk_bf16_f32 v220, v120, v121\n"
    "v_cvt_pk_bf16_f32 v221, v122, v123\n"
    "v_cvt_pk_bf16_f32 v222, v124, v125\n"
    "v_mfma_f32_16x16x32_bf16 v[112:115], a[8:11], v[184:187], 0\n"
    "s_waitcnt lgkmcnt(8)\n"
    "ds_read_u16 v198, v198\n"
    "v_or_b32_e32 v193, v193, v249\n"
    "v_mfma_f32_16x16x32_bf16 v[116:119], a[12:15], v[184:187], 0\n"
    "v_cvt_pk_bf16_f32 v223, v126, v127\n"
    "v_pk_fma_f16 v232, v192, v192, s48\n"
    "v_mfma_f32_16x16x32_bf16 v[104:107], a[0:3], v[188:191], 0\n"
    "ds_read_u16_d16_hi v162, v162\n"
    "v_cvt_pk_bf16_f32 v64, v136, v137\n"
    "v_mfma_f32_16x16x32_bf16 v[108:111], a[4:7], v[188:191], 0\n"
    "s_waitcnt lgkmcnt(7)\n"
    "v_or_b32_e32 v194, v194, v255\n"
    "v_cvt_pk_bf16_f32 v65, v138, v139\n"
    "v_mfma_f32_16x16x32_bf16 v[128:131], v[0:3], v[184:187], 0\n"
    "ds_read_u16 v199, v199\n"
    "v_cvt_pk_bf16_f32 v66, v140, v141\n"
    "v_mfma_f32_16x16x32_bf16 v[120:123], a[8:11], v[188:191], 0\n"
    "v_cvt_pk_bf16_f32 v67, v142, v143\n"
    "v_pk_mul_f16 v232, v192, v232\n"
    "v_mfma_f32_16x16x32_bf16 v[124:127], a[12:15], v[188:191], 0\n"
    "s_waitcnt lgkmcnt(6)\n"
    "ds_read_u16_d16_hi v163, v163\n"
    "v_exp_f16_e32 v234, v232\n"
    "v_mfma_f32_16x16x32_bf16 v[132:135], v[4:7], v[184:187], 0\n"
    "v_exp_f16_sdwa v234, v232 dst_sel:WORD_1 dst_unused:UNUSED_PRESERVE src0_sel:WORD_1\n"
    "v_mfma_f32_16x16x32_bf16 v[136:139], v[0:3], v[188:191], 0\n"
    "ds_read_b128 v[8:11], v244 offset:4096\n"
    "v_pk_fma_f16 v234, v234, s49, s49\n"
    "v_mfma_f32_16x16x32_bf16 v[140:143], v[4:7], v[188:191], 0\n"
    "s_waitcnt lgkmcnt(7)\n"
    "v_or_b32_e32 v195, v195, v160\n"
    "v_lshlrev_b32_sdwa v184, 1, v168 dst_sel:DWORD dst_unused:UNUSED_PAD src0_sel:DWORD src1_sel:WORD_1\n"
    "v_lshlrev_b32_sdwa v200, 1, v169 dst_sel:DWORD dst_unused:UNUSED_PAD src0_sel:DWORD src1_sel:WORD_1\n"
    "v_lshlrev_b32_sdwa v185, 1, v170 dst_sel:DWORD dst_unused:UNUSED_PAD src0_sel:DWORD src1_sel:WORD_1\n"
    "v_lshlrev_b32_sdwa v201, 1, v171 dst_sel:DWORD dst_unused:UNUSED_PAD src0_sel:DWORD src1_sel:WORD_1\n"
    "v_mfma_f32_16x16x32_bf16 v[168:171], a[104:107], a[224:227], 0\n"
    "ds_read_u16 v184, v184\n"
    "ds_read_u16_d16_hi v200, v200\n"
    "v_mfma_f32_16x16x32_bf16 v[168:171], a[152:155], a[228:231], v[168:171]\n"
    "ds_read_u16 v185, v185\n"
    "buffer_store_dwordx4 v[224:227], v245, s[68:71], s59 offen offset:64 sc1\n"
    "s_waitcnt lgkmcnt(8)\n"
    "v_or_b32_e32 v197, v197, v161\n"
    "v_lshlrev_b32_sdwa v186, 1, v172 dst_sel:DWORD dst_unused:UNUSED_PAD src0_sel:DWORD src1_sel:WORD_1\n"
    "v_lshlrev_b32_sdwa v202, 1, v173 dst_sel:DWORD dst_unused:UNUSED_PAD src0_sel:DWORD src1_sel:WORD_1\n"
    "v_lshlrev_b32_sdwa v187, 1, v174 dst_sel:DWORD dst_unused:UNUSED_PAD src0_sel:DWORD src1_sel:WORD_1\n"
    "v_lshlrev_b32_sdwa v203, 1, v175 dst_sel:DWORD dst_unused:UNUSED_PAD src0_sel:DWORD src1_sel:WORD_1\n"
    "v_rcp_f16_e32 v232, v234\n"
    "v_mfma_f32_16x16x32_bf16 v[168:171], a[64:67], a[232:235], v[168:171]\n"
    "s_waitcnt lgkmcnt(6)\n"
    "ds_read_u16_d16_hi v201, v201\n"
    "ds_read_u16 v186, v186\n"
    "v_rcp_f16_sdwa v232, v234 dst_sel:WORD_1 dst_unused:UNUSED_PRESERVE src0_sel:WORD_1\n"
    "v_mfma_f32_16x16x32_bf16 v[172:175], a[108:111], a[224:227], 0\n"
    "ds_read_u16_d16_hi v202, v202\n"
    "v_lshlrev_b32_sdwa v188, 1, v176 dst_sel:DWORD dst_unused:UNUSED_PAD src0_sel:DWORD src1_sel:WORD_1\n"
    "v_mfma_f32_16x16x32_bf16 v[172:175], a[156:159], a[228:231], v[172:175]\n"
    "ds_read_u16 v187, v187\n"
    "v_lshlrev_b32_sdwa v204, 1, v177 dst_sel:DWORD dst_unused:UNUSED_PAD src0_sel:DWORD src1_sel:WORD_1\n"
    "v_lshlrev_b32_sdwa v189, 1, v178 dst_sel:DWORD dst_unused:UNUSED_PAD src0_sel:DWORD src1_sel:WORD_1\n"
    "v_lshlrev_b32_sdwa v205, 1, v179 dst_sel:DWORD dst_unused:UNUSED_PAD src0_sel:DWORD src1_sel:WORD_1\n"
    "v_lshlrev_b32_sdwa v190, 1, v180 dst_sel:DWORD dst_unused:UNUSED_PAD src0_sel:DWORD src1_sel:WORD_1\n"
    "v_lshlrev_b32_sdwa v206, 1, v181 dst_sel:DWORD dst_unused:UNUSED_PAD src0_sel:DWORD src1_sel:WORD_1\n"
    "v_lshlrev_b32_sdwa v191, 1, v182 dst_sel:DWORD dst_unused:UNUSED_PAD src0_sel:DWORD src1_sel:WORD_1\n"
    "v_lshlrev_b32_sdwa v207, 1, v183 dst_sel:DWORD dst_unused:UNUSED_PAD src0_sel:DWORD src1_sel:WORD_1\n"
    "v_fma_mix_f32 v234, v192, v232, 0 op_sel_hi:[1,1,0]\n"
    "v_mfma_f32_16x16x32_bf16 v[172:175], a[68:71], a[232:235], v[172:175]\n"
    "s_waitcnt lgkmcnt(7)\n"
    "ds_read_b128 v[12:15], v244 offset:6144\n"
    "ds_read_u16_d16_hi v203, v203\n"
    "v_or_b32_e32 v198, v198, v162\n"
    "v_fma_mix_f32 v232, v192, v232, 0 op_sel:[1,1,0] op_sel_hi:[1,1,0]\n"
    "v_mfma_f32_16x16x32_bf16 v[176:179], a[104:107], a[236:239], 0\n"
    "v_cvt_pk_bf16_f32 v192, v234, v232\n"
    "v_pk_fma_f16 v232, v196, v196, s48\n"
    "v_mfma_f32_16x16x32_bf16 v[176:179], a[152:155], a[240:243], v[176:179]\n"
    "s_waitcnt lgkmcnt(7)\n"
    "ds_read_u16 v188, v188\n"
    "v_or_b32_e32 v199, v199, v163\n"
    "v_mfma_f32_16x16x32_bf16 v[176:179], a[64:67], a[244:247], v[176:179]\n"
    "ds_read_u16_d16_hi v204, v204\n"
    "v_pk_mul_f16 v232, v196, v232\n"
    "v_mfma_f32_16x16x32_bf16 v[180:183], a[108:111], a[236:239], 0\n"
    "v_exp_f16_e64 v234, v232\n"
    "v_mfma_f32_16x16x32_bf16 v[180:183], a[156:159], a[240:243], v[180:183]\n"
    "ds_read_u16 v189, v189\n"
    "v_or_b32_e32 v184, v184, v200\n"
    "v_mfma_f32_16x16x32_bf16 v[180:183], a[68:71], a[244:247], v[180:183]\n"
    "s_waitcnt lgkmcnt(8)\n"
    "v_exp_f16_sdwa v234, v232 dst_sel:WORD_1 dst_unused:UNUSED_PRESERVE src0_sel:WORD_1\n"
    "v_mfma_f32_16x16x32_bf16 v[96:99], a[16:19], v[192:195], v[96:99]\n"
    "ds_read_u16_d16_hi v205, v205\n"
    "v_pk_fma_f16 v234, v234, s49, s49\n"
    "v_mfma_f32_16x16x32_bf16 v[100:103], a[20:23], v[192:195], v[100:103]\n"
    "v_rcp_f16_e32 v232, v234\n"
    "v_mfma_f32_16x16x32_bf16 v[112:115], a[24:27], v[192:195], v[112:115]\n"
    "s_waitcnt lgkmcnt(7)\n"
    "ds_read_u16 v190, v190\n"
    "buffer_store_dwordx4 v[228:231], v245, s[68:71], s59 offen offset:128 sc1\n"
    "v_rcp_f16_sdwa v232, v234 dst_sel:WORD_1 dst_unused:UNUSED_PRESERVE src0_sel:WORD_1\n"
    "v_mfma_f32_16x16x32_bf16 v[116:119], a[28:31], v[192:195], v[116:119]\n"
    "ds_read_u16_d16_hi v206, v206\n"
    "ds_read_u16 v191, v191\n"
    "v_mfma_f32_16x16x32_bf16 v[128:131], v[8:11], v[192:195], v[128:131]\n"
    "v_fma_mix_f32 v234, v196, v232, 0 op_sel_hi:[1,1,0]\n"
    "v_fma_mix_f32 v232, v196, v232, 0 op_sel:[1,1,0] op_sel_hi:[1,1,0]\n"
    "s_waitcnt lgkmcnt(8)\n"
    "v_mfma_f32_16x16x32_bf16 v[132:135], v[12:15], v[192:195], v[132:135]\n"
    "s_waitcnt lgkmcnt(7)\n"
    "ds_read_u16_d16_hi v207, v207\n"
    "v_cvt_pk_bf16_f32 v196, v234, v232\n"
    "v_or_b32_e64 v185, v185, v201\n"
    "v_cvt_pk_f16_f32 v192, v168, v169\n"
    "v_mfma_f32_16x16x32_bf16 v[104:107], a[16:19], v[196:199], v[104:107]\n"
    "ds_read_b128 v[52:55], v244 offset:34816\n"
    "v_lshlrev_b32_sdwa v193, 1, v170 dst_sel:DWORD dst_unused:UNUSED_PAD src0_sel:DWORD src1_sel:WORD_1\n"
    "v_mfma_f32_16x16x32_bf16 v[108:111], a[20:23], v[196:199], v[108:111]\n"
    "ds_read_u16 v193, v193\n"
    "v_lshlrev_b32_sdwa v249, 1, v171 dst_sel:DWORD dst_unused:UNUSED_PAD src0_sel:DWORD src1_sel:WORD_1\n"
    "v_mfma_f32_16x16x32_bf16 v[120:123], a[24:27], v[196:199], v[120:123]\n"
    "s_waitcnt lgkmcnt(8)\n"
    "v_lshlrev_b32_sdwa v194, 1, v172 dst_sel:DWORD dst_unused:UNUSED_PAD src0_sel:DWORD src1_sel:WORD_1\n"
    "v_or_b32_e32 v186, v186, v202\n"
    "v_mfma_f32_16x16x32_bf16 v[124:127], a[28:31], v[196:199], v[124:127]\n"
    "ds_read_u16_d16_hi v249, v249\n"
    "v_lshlrev_b32_sdwa v255, 1, v173 dst_sel:DWORD dst_unused:UNUSED_PAD src0_sel:DWORD src1_sel:WORD_1\n"
    "v_mfma_f32_16x16x32_bf16 v[136:139], v[8:11], v[196:199], v[136:139]\n"
    "v_lshlrev_b32_sdwa v195, 1, v174 dst_sel:DWORD dst_unused:UNUSED_PAD src0_sel:DWORD src1_sel:WORD_1\n"
    "v_lshlrev_b32_sdwa v160, 1, v175 dst_sel:DWORD dst_unused:UNUSED_PAD src0_sel:DWORD src1_sel:WORD_1\n"
    "v_mfma_f32_16x16x32_bf16 v[140:143], v[12:15], v[196:199], v[140:143]\n"
    "s_nop 0\n"
    "ds_read_u16 v194, v194\n"
    "v_cvt_pk_f16_f32 v196, v176, v177\n"
    "v_mfma_f32_16x16x32_bf16 v[168:171], a[112:115], a[224:227], 0\n"
    "s_waitcnt lgkmcnt(8)\n"
    "v_lshlrev_b32_sdwa v197, 1, v178 dst_sel:DWORD dst_unused:UNUSED_PAD src0_sel:DWORD src1_sel:WORD_1\n"
    "v_lshlrev_b32_sdwa v161, 1, v179 dst_sel:DWORD dst_unused:UNUSED_PAD src0_sel:DWORD src1_sel:WORD_1\n"
    "v_mfma_f32_16x16x32_bf16 v[168:171], a[160:163], a[228:231], v[168:171]\n"
    "ds_read_u16_d16_hi v255, v255\n"
    "v_lshlrev_b32_sdwa v198, 1, v180 dst_sel:DWORD dst_unused:UNUSED_PAD src0_sel:DWORD src1_sel:WORD_1\n"
    "v_mfma_f32_16x16x32_bf16 v[168:171], a[72:75], a[232:235], v[168:171]\n"
    "ds_read_b128 v[16:19], v244 offset:8192\n"
    "v_lshlrev_b32_sdwa v162, 1, v181 dst_sel:DWORD dst_unused:UNUSED_PAD src0_sel:DWORD src1_sel:WORD_1\n"
    "v_mfma_f32_16x16x32_bf16 v[172:175], a[116:119], a[224:227], 0\n"
    "s_waitcnt lgkmcnt(8)\n"
    "ds_read_u16 v195, v195\n"
    "v_lshlrev_b32_sdwa v199, 1, v182 dst_sel:DWORD dst_unused:UNUSED_PAD src0_sel:DWORD src1_sel:WORD_1\n"
    "v_or_b32_e32 v187, v187, v203\n"
    "v_mfma_f32_16x16x32_bf16 v[172:175], a[164:167], a[228:231], v[172:175]\n"
    "s_nop 0\n"
    "buffer_store_dwordx4 v[216:219], v245, s[68:71], s39 offen offset:0 sc1\n"
    "ds_read_b128 v[20:23], v244 offset:10240\n"
    "v_mfma_f32_16x16x32_bf16 v[176:179], a[112:115], a[236:239], 0\n"
    "s_waitcnt lgkmcnt(7)\n"
    "ds_read_u16_d16_hi v160, v160\n"
    "v_lshlrev_b32_sdwa v163, 1, v183 dst_sel:DWORD dst_unused:UNUSED_PAD src0_sel:DWORD src1_sel:WORD_1\n"
    "v_mfma_f32_16x16x32_bf16 v[172:175], v[52:55], a[232:235], v[172:175]\n"
    "v_or_b32_e64 v188, v188, v204\n"
    "v_or_b32_e64 v189, v189, v205\n"
    "v_mfma_f32_16x16x32_bf16 v[176:179], a[160:163], a[240:243], v[176:179]\n"
    "ds_read_u16 v197, v197\n"
    "v_or_b32_e64 v190, v190, v206\n"
    "v_mfma_f32_16x16x32_bf16 v[176:179], a[72:75], a[244:247], v[176:179]\n"
    "ds_read_u16_d16_hi v161, v161\n"
    "v_or_b32_e64 v191, v191, v207\n"
    "v_mfma_f32_16x16x32_bf16 v[180:183], a[116:119], a[236:239], 0\n"
    "v_pk_fma_f16 v232, v192, v192, s48\n"
    "v_lshlrev_b32_sdwa v200, 1, v169 dst_sel:DWORD dst_unused:UNUSED_PAD src0_sel:DWORD src1_sel:WORD_1\n"
    "v_mfma_f32_16x16x32_bf16 v[180:183], a[164:167], a[240:243], v[180:183]\n"
    "s_waitcnt lgkmcnt(8)\n"
    "ds_read_u16 v198, v198\n"
    "v_or_b32_e32 v193, v193, v249\n"
    "v_mfma_f32_16x16x32_bf16 v[180:183], v[52:55], a[244:247], v[180:183]\n"
    "v_pk_mul_f16 v232, v192, v232\n"
    "v_lshlrev_b32_sdwa v201, 1, v171 dst_sel:DWORD dst_unused:UNUSED_PAD src0_sel:DWORD src1_sel:WORD_1\n"
    "v_mfma_f32_16x16x32_bf16 v[96:99], a[32:35], v[184:187], v[96:99]\n"
    "ds_read_u16_d16_hi v162, v162\n"
    "v_exp_f16_e32 v234, v232\n"
    "v_mfma_f32_16x16x32_bf16 v[100:103], a[36:39], v[184:187], v[100:103]\n"
    "s_waitcnt lgkmcnt(7)\n"
    "ds_read_u16 v199, v199\n"
    "v_or_b32_e32 v194, v194, v255\n"
    "v_mfma_f32_16x16x32_bf16 v[112:115], a[40:43], v[184:187], v[112:115]\n"
    "v_exp_f16_sdwa v234, v232 dst_sel:WORD_1 dst_unused:UNUSED_PRESERVE src0_sel:WORD_1\n"
    "v_mfma_f32_16x16x32_bf16 v[116:119], a[44:47], v[184:187], v[116:119]\n"
    "ds_read_u16_d16_hi v163, v163\n"
    "v_pk_fma_f16 v234, v234, s49, s49\n"
    "v_mfma_f32_16x16x32_bf16 v[128:131], v[16:19], v[184:187], v[128:131]\n"
    "s_waitcnt lgkmcnt(7)\n"
    "v_lshlrev_b32_sdwa v202, 1, v173 dst_sel:DWORD dst_unused:UNUSED_PAD src0_sel:DWORD src1_sel:WORD_1\n"
    "v_lshlrev_b32_sdwa v203, 1, v175 dst_sel:DWORD dst_unused:UNUSED_PAD src0_sel:DWORD src1_sel:WORD_1\n"
    "v_mfma_f32_16x16x32_bf16 v[132:135], v[20:23], v[184:187], v[132:135]\n"
    "ds_read_b128 v[60:63], v244 offset:38912\n"
    "v_lshlrev_b32_sdwa v184, 1, v168 dst_sel:DWORD dst_unused:UNUSED_PAD src0_sel:DWORD src1_sel:WORD_1\n"
    "v_mfma_f32_16x16x32_bf16 v[104:107], a[32:35], v[188:191], v[104:107]\n"
    "ds_read_u16 v184, v184\n"
    "v_lshlrev_b32_sdwa v185, 1, v170 dst_sel:DWORD dst_unused:UNUSED_PAD src0_sel:DWORD src1_sel:WORD_1\n"
    "v_mfma_f32_16x16x32_bf16 v[108:111], a[36:39], v[188:191], v[108:111]\n"
    "s_waitcnt lgkmcnt(8)\n"
    "v_or_b32_e32 v195, v195, v160\n"
    "v_lshlrev_b32_sdwa v186, 1, v172 dst_sel:DWORD dst_unused:UNUSED_PAD src0_sel:DWORD src1_sel:WORD_1\n"
    "v_mfma_f32_16x16x32_bf16 v[120:123], a[40:43], v[188:191], v[120:123]\n"
    "ds_read_u16_d16_hi v200, v200\n"
    "v_lshlrev_b32_sdwa v187, 1, v174 dst_sel:DWORD dst_unused:UNUSED_PAD src0_sel:DWORD src1_sel:WORD_1\n"
    "v_mfma_f32_16x16x32_bf16 v[124:127], a[44:47], v[188:191], v[124:127]\n"
    "ds_read_u16 v185, v185\n"
    "buffer_store_dwordx4 v[220:223], v245, s[68:71], s39 offen offset:64 sc1\n"
    "v_mfma_f32_16x16x32_bf16 v[136:139], v[16:19], v[188:191], v[136:139]\n"
    "s_waitcnt lgkmcnt(8)\n"
    "ds_read_u16_d16_hi v201, v201\n"
    "v_or_b32_e32 v197, v197, v161\n"
    "v_mfma_f32_16x16x32_bf16 v[140:143], v[20:23], v[188:191], v[140:143]\n"
    "v_rcp_f16_e64 v232, v234\n"
    "v_mfma_f32_16x16x32_bf16 v[168:171], a[120:123], a[224:227], 0\n"
    "ds_read_u16 v186, v186\n"
    "v_lshlrev_b32_sdwa v188, 1, v176 dst_sel:DWORD dst_unused:UNUSED_PAD src0_sel:DWORD src1_sel:WORD_1\n"
    "v_mfma_f32_16x16x32_bf16 v[168:171], a[168:171], a[228:231], v[168:171]\n"
    "s_waitcnt lgkmcnt(8)\n"
    "v_rcp_f16_sdwa v232, v234 dst_sel:WORD_1 dst_unused:UNUSED_PRESERVE src0_sel:WORD_1\n"
    "v_mfma_f32_16x16x32_bf16 v[168:171], a[76:79], a[232:235], v[168:171]\n"
    "s_nop 0\n"
    "ds_read_b128 v[24:27], v244 offset:12288\n"
    "ds_read_u16_d16_hi v202, v202\n"
    "v_mfma_f32_16x16x32_bf16 v[172:175], a[124:127], a[224:227], 0\n"
    "s_nop 0\n"
    "v_lshlrev_b32_sdwa v204, 1, v177 dst_sel:DWORD dst_unused:UNUSED_PAD src0_sel:DWORD src1_sel:WORD_1\n"
    "v_lshlrev_b32_sdwa v189, 1, v178 dst_sel:DWORD dst_unused:UNUSED_PAD src0_sel:DWORD src1_sel:WORD_1\n"
    "v_mfma_f32_16x16x32_bf16 v[172:175], a[172:175], a[228:231], v[172:175]\n"
    "s_waitcnt lgkmcnt(7)\n"
    "ds_read_u16 v187, v187\n"
    "v_lshlrev_b32_sdwa v205, 1, v179 dst_sel:DWORD dst_unused:UNUSED_PAD src0_sel:DWORD src1_sel:WORD_1\n"
    "v_lshlrev_b32_sdwa v190, 1, v180 dst_sel:DWORD dst_unused:UNUSED_PAD src0_sel:DWORD src1_sel:WORD_1\n"
    "v_lshlrev_b32_sdwa v206, 1, v181 dst_sel:DWORD dst_unused:UNUSED_PAD src0_sel:DWORD src1_sel:WORD_1\n"
    "v_lshlrev_b32_sdwa v191, 1, v182 dst_sel:DWORD dst_unused:UNUSED_PAD src0_sel:DWORD src1_sel:WORD_1\n"
    "v_lshlrev_b32_sdwa v207, 1, v183 dst_sel:DWORD dst_unused:UNUSED_PAD src0_sel:DWORD src1_sel:WORD_1\n"
    "v_fma_mix_f32 v234, v192, v232, 0 op_sel_hi:[1,1,0]\n"
    "v_mfma_f32_16x16x32_bf16 v[172:175], v[60:63], a[232:235], v[172:175]\n"
    "ds_read_b128 v[28:31], v244 offset:14336\n"
    "v_or_b32_e64 v198, v198, v162\n"
    "v_fma_mix_f32 v232, v192, v232, 0 op_sel:[1,1,0] op_sel_hi:[1,1,0]\n"
    "v_mfma_f32_16x16x32_bf16 v[176:179], a[120:123], a[236:239], 0\n"
    "ds_read_u16_d16_hi v203, v203\n"
    "v_cvt_pk_bf16_f32 v192, v234, v232\n"
    "v_mfma_f32_16x16x32_bf16 v[176:179], a[168:171], a[240:243], v[176:179]\n"
    "s_waitcnt lgkmcnt(8)\n"
    "ds_read_u16 v188, v188\n"
    "v_pk_fma_f16 v232, v196, v196, s48\n"
    "v_mfma_f32_16x16x32_bf16 v[176:179], a[76:79], a[244:247], v[176:179]\n"
    "v_or_b32_e32 v199, v199, v163\n"
    "v_pk_mul_f16 v232, v196, v232\n"
    "v_mfma_f32_16x16x32_bf16 v[180:183], a[124:127], a[236:239], 0\n"
    "ds_read_u16_d16_hi v204, v204\n"
    "v_or_b32_e64 v184, v184, v200\n"
    "v_mfma_f32_16x16x32_bf16 v[180:183], a[172:175], a[240:243], v[180:183]\n"
    "v_exp_f16_e32 v234, v232\n"
    "v_mfma_f32_16x16x32_bf16 v[180:183], v[60:63], a[244:247], v[180:183]\n"
    "s_waitcnt lgkmcnt(8)\n"
    "ds_read_b128 a[224:227], v243 offset:24640\n"
    "v_exp_f16_sdwa v234, v232 dst_sel:WORD_1 dst_unused:UNUSED_PRESERVE src0_sel:WORD_1\n"
    "v_mfma_f32_16x16x32_bf16 v[96:99], a[176:179], v[192:195], v[96:99]\n"
    "ds_read_u16 v189, v189\n"
    "buffer_store_dwordx4 v[64:67], v245, s[68:71], s39 offen offset:128 sc1\n"
    "v_pk_fma_f16 v234, v234, s49, s49\n"
    "v_mfma_f32_16x16x32_bf16 v[100:103], a[180:183], v[192:195], v[100:103]\n"
    "ds_read_b128 a[228:231], v243 offset:25664\n"
    "ds_read_u16_d16_hi v205, v205\n"
    "v_rcp_f16_e32 v232, v234\n"
    "v_mfma_f32_16x16x32_bf16 v[112:115], a[184:187], v[192:195], v[112:115]\n"
    "s_waitcnt lgkmcnt(9)\n"
    "ds_read_b128 a[232:235], v243 offset:26688\n"
    "v_rcp_f16_sdwa v232, v234 dst_sel:WORD_1 dst_unused:UNUSED_PRESERVE src0_sel:WORD_1\n"
    "v_mfma_f32_16x16x32_bf16 v[116:119], a[188:191], v[192:195], v[116:119]\n"
    "ds_read_b128 a[236:239], v243 offset:27712\n"
    "v_fma_mix_f32 v234, v196, v232, 0 op_sel_hi:[1,1,0]\n"
    "v_mfma_f32_16x16x32_bf16 v[128:131], v[24:27], v[192:195], v[128:131]\n"
    "s_waitcnt lgkmcnt(9)\n"
    "ds_read_u16 v190, v190\n"
    "v_fma_mix_f32 v232, v196, v232, 0 op_sel:[1,1,0] op_sel_hi:[1,1,0]\n"
    "v_mfma_f32_16x16x32_bf16 v[132:135], v[28:31], v[192:195], v[132:135]\n"
    "s_waitcnt lgkmcnt(9)\n"
    "v_cvt_pk_bf16_f32 v196, v234, v232\n"
    "v_or_b32_e64 v185, v185, v201\n"
    "v_cvt_pk_f16_f32 v192, v168, v169\n"
    "v_mfma_f32_16x16x32_bf16 v[104:107], a[176:179], v[196:199], v[104:107]\n"
    "ds_read_b128 a[240:243], v243 offset:28736\n"
    "ds_read_u16_d16_hi v206, v206\n"
    "v_mfma_f32_16x16x32_bf16 v[108:111], a[180:183], v[196:199], v[108:111]\n"
    "s_nop 0\n"
    "v_lshlrev_b32_sdwa v193, 1, v170 dst_sel:DWORD dst_unused:UNUSED_PAD src0_sel:DWORD src1_sel:WORD_1\n"
    "v_lshlrev_b32_sdwa v249, 1, v171 dst_sel:DWORD dst_unused:UNUSED_PAD src0_sel:DWORD src1_sel:WORD_1\n"
    "v_mfma_f32_16x16x32_bf16 v[120:123], a[184:187], v[196:199], v[120:123]\n"
    "s_waitcnt lgkmcnt(8)\n"
    "ds_read_b128 a[244:247], v243 offset:29760\n"
    "v_lshlrev_b32_sdwa v194, 1, v172 dst_sel:DWORD dst_unused:UNUSED_PAD src0_sel:DWORD src1_sel:WORD_1\n"
    "v_mfma_f32_16x16x32_bf16 v[124:127], a[188:191], v[196:199], v[124:127]\n"
    "ds_read_u16 v191, v191\n"
    "v_or_b32_e64 v186, v186, v202\n"
    "v_lshlrev_b32_sdwa v255, 1, v173 dst_sel:DWORD dst_unused:UNUSED_PAD src0_sel:DWORD src1_sel:WORD_1\n"
    "v_lshlrev_b32_sdwa v195, 1, v174 dst_sel:DWORD dst_unused:UNUSED_PAD src0_sel:DWORD src1_sel:WORD_1\n"
    "v_lshlrev_b32_sdwa v160, 1, v175 dst_sel:DWORD dst_unused:UNUSED_PAD src0_sel:DWORD src1_sel:WORD_1\n"
    "v_mfma_f32_16x16x32_bf16 v[136:139], v[24:27], v[196:199], v[136:139]\n"
    "ds_read_u16_d16_hi v207, v207\n"
    "v_lshlrev_b32_sdwa v161, 1, v179 dst_sel:DWORD dst_unused:UNUSED_PAD src0_sel:DWORD src1_sel:WORD_1\n"
    "v_lshlrev_b32_sdwa v162, 1, v181 dst_sel:DWORD dst_unused:UNUSED_PAD src0_sel:DWORD src1_sel:WORD_1\n"
    "v_or_b32_e64 v187, v187, v203\n"
    "v_lshlrev_b32_sdwa v163, 1, v183 dst_sel:DWORD dst_unused:UNUSED_PAD src0_sel:DWORD src1_sel:WORD_1\n"
    "v_mfma_f32_16x16x32_bf16 v[140:143], v[28:31], v[196:199], v[140:143]\n"
    "ds_read_u16 v193, v193\n"
    "v_cvt_pk_f16_f32 v196, v176, v177\n"
    "v_lshlrev_b32_sdwa v197, 1, v178 dst_sel:DWORD dst_unused:UNUSED_PAD src0_sel:DWORD src1_sel:WORD_1\n"
    "v_lshlrev_b32_sdwa v198, 1, v180 dst_sel:DWORD dst_unused:UNUSED_PAD src0_sel:DWORD src1_sel:WORD_1\n"
    "v_lshlrev_b32_sdwa v199, 1, v182 dst_sel:DWORD dst_unused:UNUSED_PAD src0_sel:DWORD src1_sel:WORD_1\n"
    "v_mfma_f32_16x16x32_bf16 v[168:171], a[80:83], a[224:227], 0\n"
    "s_waitcnt lgkmcnt(8)\n"
    "ds_read_b128 v[48:51], v244 offset:32768\n"
    "v_or_b32_e32 v188, v188, v204\n"
    "v_mfma_f32_16x16x32_bf16 v[168:171], a[128:131], a[228:231], v[168:171]\n"
    "s_waitcnt lgkmcnt(8)\n"
    "ds_read_u16_d16_hi v249, v249\n"
    "v_or_b32_e32 v189, v189, v205\n"
    "v_mfma_f32_16x16x32_bf16 v[168:171], a[48:51], a[232:235], v[168:171]\n"
    "v_pk_fma_f16 v232, v192, v192, s48\n"
    "v_pk_mul_f16 v232, v192, v232\n"
    "v_mfma_f32_16x16x32_bf16 v[172:175], a[84:87], a[224:227], 0\n"
    "ds_read_u16 v194, v194\n"
    "v_mfma_f32_16x16x32_bf16 v[172:175], a[132:135], a[228:231], v[172:175]\n"
    "v_exp_f16_e32 v234, v232\n"
    "v_mfma_f32_16x16x32_bf16 v[176:179], a[80:83], a[236:239], 0\n"
    "s_waitcnt lgkmcnt(8)\n"
    "ds_read_u16_d16_hi v255, v255\n"
    "v_lshlrev_b32_sdwa v200, 1, v169 dst_sel:DWORD dst_unused:UNUSED_PAD src0_sel:DWORD src1_sel:WORD_1\n"
    "v_mfma_f32_16x16x32_bf16 v[176:179], a[128:131], a[240:243], v[176:179]\n"
    "s_waitcnt lgkmcnt(7)\n"
    "v_or_b32_e32 v190, v190, v206\n"
    "v_lshlrev_b32_sdwa v201, 1, v171 dst_sel:DWORD dst_unused:UNUSED_PAD src0_sel:DWORD src1_sel:WORD_1\n"
    "v_mfma_f32_16x16x32_bf16 v[176:179], a[48:51], a[244:247], v[176:179]\n"
    "ds_read_b128 v[32:35], v244 offset:16384\n"
    "ds_read_u16 v195, v195\n"
    "v_exp_f16_sdwa v234, v232 dst_sel:WORD_1 dst_unused:UNUSED_PRESERVE src0_sel:WORD_1\n"
    "v_mfma_f32_16x16x32_bf16 v[180:183], a[84:87], a[236:239], 0\n"
    "v_pk_fma_f16 v234, v234, s49, s49\n"
    "v_mfma_f32_16x16x32_bf16 v[180:183], a[132:135], a[240:243], v[180:183]\n"
    "ds_read_b128 v[36:39], v244 offset:18432\n"
    "v_rcp_f16_e32 v232, v234\n"
    "v_mfma_f32_16x16x32_bf16 v[96:99], a[192:195], v[184:187], v[96:99]\n"
    "s_waitcnt lgkmcnt(8)\n"
    "ds_read_u16_d16_hi v160, v160\n"
    "v_or_b32_e32 v191, v191, v207\n"
    "v_mfma_f32_16x16x32_bf16 v[100:103], a[196:199], v[184:187], v[100:103]\n"
    "ds_read_u16 v197, v197\n"
    "v_rcp_f16_sdwa v232, v234 dst_sel:WORD_1 dst_unused:UNUSED_PRESERVE src0_sel:WORD_1\n"
    "v_mfma_f32_16x16x32_bf16 v[112:115], a[200:203], v[184:187], v[112:115]\n"
    "s_waitcnt lgkmcnt(8)\n"
    "v_lshlrev_b32_sdwa v204, 1, v177 dst_sel:DWORD dst_unused:UNUSED_PAD src0_sel:DWORD src1_sel:WORD_1\n"
    "v_lshlrev_b32_sdwa v205, 1, v179 dst_sel:DWORD dst_unused:UNUSED_PAD src0_sel:DWORD src1_sel:WORD_1\n"
    "v_mfma_f32_16x16x32_bf16 v[172:175], v[48:51], a[232:235], v[172:175]\n"
    "ds_read_u16_d16_hi v161, v161\n"
    "v_fma_mix_f32 v234, v192, v232, 0 op_sel_hi:[1,1,0]\n"
    "v_mfma_f32_16x16x32_bf16 v[180:183], v[48:51], a[244:247], v[180:183]\n"
    "s_waitcnt lgkmcnt(8)\n"
    "v_or_b32_e32 v193, v193, v249\n"
    "v_fma_mix_f32 v232, v192, v232, 0 op_sel:[1,1,0] op_sel_hi:[1,1,0]\n"
    "v_mfma_f32_16x16x32_bf16 v[116:119], a[204:207], v[184:187], v[116:119]\n"
    "ds_read_u16 v198, v198\n"
    "v_lshlrev_b32_sdwa v202, 1, v173 dst_sel:DWORD dst_unused:UNUSED_PAD src0_sel:DWORD src1_sel:WORD_1\n"
    "v_mfma_f32_16x16x32_bf16 v[104:107], a[192:195], v[188:191], v[104:107]\n"
    "ds_read_u16_d16_hi v162, v162\n"
    "v_lshlrev_b32_sdwa v203, 1, v175 dst_sel:DWORD dst_unused:UNUSED_PAD src0_sel:DWORD src1_sel:WORD_1\n"
    "v_lshlrev_b32_sdwa v206, 1, v181 dst_sel:DWORD dst_unused:UNUSED_PAD src0_sel:DWORD src1_sel:WORD_1\n"
    "v_lshlrev_b32_sdwa v207, 1, v183 dst_sel:DWORD dst_unused:UNUSED_PAD src0_sel:DWORD src1_sel:WORD_1\n"
    "v_cvt_pk_bf16_f32 v192, v234, v232\n"
    "v_mfma_f32_16x16x32_bf16 v[108:111], a[196:199], v[188:191], v[108:111]\n"
    "s_waitcnt lgkmcnt(7)\n"
    "ds_read_u16 v199, v199\n"
    "v_or_b32_e32 v194, v194, v255\n"
    "v_pk_fma_f16 v232, v196, v196, s48\n"
    "v_pk_mul_f16 v232, v196, v232\n"
    "v_exp_f16_e64 v234, v232\n"
    "v_mfma_f32_16x16x32_bf16 v[128:131], v[32:35], v[184:187], v[128:131]\n"
    "ds_read_u16_d16_hi v163, v163\n"
    "v_exp_f16_sdwa v234, v232 dst_sel:WORD_1 dst_unused:UNUSED_PRESERVE src0_sel:WORD_1\n"
    "v_mfma_f32_16x16x32_bf16 v[120:123], a[200:203], v[188:191], v[120:123]\n"
    "s_waitcnt lgkmcnt(7)\n"
    "v_pk_fma_f16 v234, v234, s49, s49\n"
    "v_rcp_f16_e32 v232, v234\n"
    "v_mfma_f32_16x16x32_bf16 v[132:135], v[36:39], v[184:187], v[132:135]\n"
    "ds_read_u16_d16_hi v200, v200\n"
    "v_lshlrev_b32_sdwa v184, 1, v168 dst_sel:DWORD dst_unused:UNUSED_PAD src0_sel:DWORD src1_sel:WORD_1\n"
    "v_mfma_f32_16x16x32_bf16 v[124:127], a[204:207], v[188:191], v[124:127]\n"
    "s_waitcnt lgkmcnt(7)\n"
    "ds_read_u16 v184, v184\n"
    "v_or_b32_e32 v195, v195, v160\n"
    "v_lshlrev_b32_sdwa v185, 1, v170 dst_sel:DWORD dst_unused:UNUSED_PAD src0_sel:DWORD src1_sel:WORD_1\n"
    "v_lshlrev_b32_sdwa v186, 1, v172 dst_sel:DWORD dst_unused:UNUSED_PAD src0_sel:DWORD src1_sel:WORD_1\n"
    "v_mfma_f32_16x16x32_bf16 v[136:139], v[32:35], v[188:191], v[136:139]\n"
    "ds_read_u16 v185, v185\n"
    "v_lshlrev_b32_sdwa v187, 1, v174 dst_sel:DWORD dst_unused:UNUSED_PAD src0_sel:DWORD src1_sel:WORD_1\n"
    "v_mfma_f32_16x16x32_bf16 v[140:143], v[36:39], v[188:191], v[140:143]\n"
    "v_lshlrev_b32_sdwa v188, 1, v176 dst_sel:DWORD dst_unused:UNUSED_PAD src0_sel:DWORD src1_sel:WORD_1\n"
    "v_lshlrev_b32_sdwa v189, 1, v178 dst_sel:DWORD dst_unused:UNUSED_PAD src0_sel:DWORD src1_sel:WORD_1\n"
    "v_mfma_f32_16x16x32_bf16 v[168:171], a[88:91], a[224:227], 0\n"
    "s_waitcnt lgkmcnt(7)\n"
    "ds_read_u16_d16_hi v201, v201\n"
    "v_or_b32_e32 v197, v197, v161\n"
    "v_mfma_f32_16x16x32_bf16 v[168:171], a[136:139], a[228:231], v[168:171]\n"
    "v_lshlrev_b32_sdwa v190, 1, v180 dst_sel:DWORD dst_unused:UNUSED_PAD src0_sel:DWORD src1_sel:WORD_1\n"
    "v_lshlrev_b32_sdwa v191, 1, v182 dst_sel:DWORD dst_unused:UNUSED_PAD src0_sel:DWORD src1_sel:WORD_1\n"
    "v_mfma_f32_16x16x32_bf16 v[168:171], a[52:55], a[232:235], v[168:171]\n"
    "ds_read_u16 v186, v186\n"
    "v_rcp_f16_sdwa v232, v234 dst_sel:WORD_1 dst_unused:UNUSED_PRESERVE src0_sel:WORD_1\n"
    "v_mfma_f32_16x16x32_bf16 v[172:175], a[92:95], a[224:227], 0\n"
    "s_waitcnt lgkmcnt(7)\n"
    "ds_read_b128 v[40:43], v244 offset:20480\n"
    "v_or_b32_e32 v198, v198, v162\n"
    "v_mfma_f32_16x16x32_bf16 v[172:175], a[140:143], a[228:231], v[172:175]\n"
    "ds_read_u16_d16_hi v202, v202\n"
    "v_fma_mix_f32 v234, v196, v232, 0 op_sel_hi:[1,1,0]\n"
    "v_mfma_f32_16x16x32_bf16 v[172:175], v[56:59], a[232:235], v[172:175]\n"
    "v_fma_mix_f32 v232, v196, v232, 0 op_sel:[1,1,0] op_sel_hi:[1,1,0]\n"
    "v_lshlrev_b32_sdwa v249, 1, v171 dst_sel:DWORD dst_unused:UNUSED_PAD src0_sel:DWORD src1_sel:WORD_1\n"
    "v_mfma_f32_16x16x32_bf16 v[176:179], a[88:91], a[236:239], 0\n"
    "ds_read_u16 v187, v187\n"
    "v_cvt_pk_bf16_f32 v196, v234, v232\n"
    "v_mfma_f32_16x16x32_bf16 v[176:179], a[136:139], a[240:243], v[176:179]\n"
    "s_waitcnt lgkmcnt(8)\n"
    "v_or_b32_e32 v199, v199, v163\n"
    "v_lshlrev_b32_sdwa v255, 1, v173 dst_sel:DWORD dst_unused:UNUSED_PAD src0_sel:DWORD src1_sel:WORD_1\n"
    "v_mfma_f32_16x16x32_bf16 v[176:179], a[52:55], a[244:247], v[176:179]\n"
    "ds_read_b128 v[44:47], v244 offset:22528\n"
    "v_lshlrev_b32_sdwa v160, 1, v175 dst_sel:DWORD dst_unused:UNUSED_PAD src0_sel:DWORD src1_sel:WORD_1\n"
    "v_mfma_f32_16x16x32_bf16 v[180:183], a[92:95], a[236:239], 0\n"
    "ds_read_u16_d16_hi v203, v203\n"
    "v_mfma_f32_16x16x32_bf16 v[180:183], a[140:143], a[240:243], v[180:183]\n"
    "s_waitcnt lgkmcnt(8)\n"
    "v_or_b32_e32 v184, v184, v200\n"
    "v_mfma_f32_16x16x32_bf16 v[180:183], v[56:59], a[244:247], v[180:183]\n"
    "ds_read_u16 v188, v188\n"
    "v_lshlrev_b32_sdwa v161, 1, v179 dst_sel:DWORD dst_unused:UNUSED_PAD src0_sel:DWORD src1_sel:WORD_1\n"
    "v_mfma_f32_16x16x32_bf16 v[96:99], a[208:211], v[192:195], v[96:99]\n"
    "ds_read_u16_d16_hi v204, v204\n"
    "v_mfma_f32_16x16x32_bf16 v[100:103], a[212:215], v[192:195], v[100:103]\n"
    "s_nop 0\n"
    "v_mfma_f32_16x16x32_bf16 v[112:115], a[216:219], v[192:195], v[112:115]\n"
    "s_waitcnt lgkmcnt(8)\n"
    "ds_read_u16 v189, v189\n"
    "v_or_b32_e32 v185, v185, v201\n"
    "v_mfma_f32_16x16x32_bf16 v[116:119], a[220:223], v[192:195], v[116:119]\n"
    "s_waitcnt lgkmcnt(7)\n"
    "v_lshlrev_b32_sdwa v162, 1, v181 dst_sel:DWORD dst_unused:UNUSED_PAD src0_sel:DWORD src1_sel:WORD_1\n"
    "v_lshlrev_b32_sdwa v163, 1, v183 dst_sel:DWORD dst_unused:UNUSED_PAD src0_sel:DWORD src1_sel:WORD_1\n"
    "v_mfma_f32_16x16x32_bf16 v[128:131], v[40:43], v[192:195], v[128:131]\n"
    "ds_read_u16_d16_hi v205, v205\n"
    "v_cvt_pk_bf16_f32 v96, v96, v97\n"
    "v_mfma_f32_16x16x32_bf16 v[104:107], a[208:211], v[196:199], v[104:107]\n"
    "v_cvt_pk_bf16_f32 v97, v98, v99\n"
    "v_cvt_pk_bf16_f32 v98, v100, v101\n"
    "v_mfma_f32_16x16x32_bf16 v[108:111], a[212:215], v[196:199], v[108:111]\n"
    "s_waitcnt lgkmcnt(7)\n"
    "ds_read_u16 v190, v190\n"
    "v_or_b32_e32 v186, v186, v202\n"
    "v_mfma_f32_16x16x32_bf16 v[120:123], a[216:219], v[196:199], v[120:123]\n"
    "ds_read_u16_d16_hi v206, v206\n"
    "v_cvt_pk_bf16_f32 v99, v102, v103\n"
    "v_cvt_pk_bf16_f32 v112, v112, v113\n"
    "v_cvt_pk_bf16_f32 v113, v114, v115\n"
    "v_cvt_pk_bf16_f32 v114, v116, v117\n"
    "v_mfma_f32_16x16x32_bf16 v[124:127], a[220:223], v[196:199], v[124:127]\n"
    "s_waitcnt lgkmcnt(7)\n"
    "v_cvt_pk_bf16_f32 v115, v118, v119\n"
    "v_cvt_pk_bf16_f32 v128, v128, v129\n"
    "v_mfma_f32_16x16x32_bf16 v[132:135], v[44:47], v[192:195], v[132:135]\n"
    "v_cvt_pk_f16_f32 v192, v168, v169\n"
    "v_lshlrev_b32_sdwa v193, 1, v170 dst_sel:DWORD dst_unused:UNUSED_PAD src0_sel:DWORD src1_sel:WORD_1\n"
    "v_mfma_f32_16x16x32_bf16 v[136:139], v[40:43], v[196:199], v[136:139]\n"
    "s_waitcnt lgkmcnt(6)\n"
    "ds_write_b128 v243, v[96:99] offset:24640\n"
    "v_lshlrev_b32_sdwa v194, 1, v172 dst_sel:DWORD dst_unused:UNUSED_PAD src0_sel:DWORD src1_sel:WORD_1\n"
    "v_lshlrev_b32_sdwa v195, 1, v174 dst_sel:DWORD dst_unused:UNUSED_PAD src0_sel:DWORD src1_sel:WORD_1\n"
    "v_or_b32_e32 v187, v187, v203\n"
    "v_cvt_pk_bf16_f32 v129, v130, v131\n"
    "v_cvt_pk_bf16_f32 v130, v132, v133\n"
    "v_cvt_pk_bf16_f32 v131, v134, v135\n"
    "v_mfma_f32_16x16x32_bf16 v[140:143], v[44:47], v[196:199], v[140:143]\n"
    "s_waitcnt lgkmcnt(5)\n"
    "ds_read_u16 v191, v191\n"
    "ds_write_b128 v243, v[112:115] offset:26688\n"
    "v_cvt_pk_f16_f32 v196, v176, v177\n"
    "v_lshlrev_b32_sdwa v197, 1, v178 dst_sel:DWORD dst_unused:UNUSED_PAD src0_sel:DWORD src1_sel:WORD_1\n"
    "v_lshlrev_b32_sdwa v198, 1, v180 dst_sel:DWORD dst_unused:UNUSED_PAD src0_sel:DWORD src1_sel:WORD_1\n"
    "v_lshlrev_b32_sdwa v199, 1, v182 dst_sel:DWORD dst_unused:UNUSED_PAD src0_sel:DWORD src1_sel:WORD_1\n"
    "v_or_b32_e64 v188, v188, v204\n"
    "v_cvt_pk_bf16_f32 v104, v104, v105\n"
    "v_cvt_pk_bf16_f32 v105, v106, v107\n"
    "v_mfma_f32_16x16x32_bf16 v[168:171], a[96:99], a[224:227], 0\n"
    "s_waitcnt lgkmcnt(5)\n"
    "ds_read_u16_d16_hi v207, v207\n"
    "v_or_b32_e32 v189, v189, v205\n"
    "v_mfma_f32_16x16x32_bf16 v[168:171], a[144:147], a[228:231], v[168:171]\n"
    "v_cvt_pk_bf16_f32 v106, v108, v109\n"
    "v_cvt_pk_bf16_f32 v107, v110, v111\n"
    "v_mfma_f32_16x16x32_bf16 v[168:171], a[56:59], a[232:235], v[168:171]\n"
    "ds_read_u16 v193, v193\n"
    "ds_write_b128 v243, v[128:131] offset:28736\n"
    "v_cvt_pk_bf16_f32 v120, v120, v121\n"
    "v_cvt_pk_bf16_f32 v121, v122, v123\n"
    "v_cvt_pk_bf16_f32 v122, v124, v125\n"
    "v_cvt_pk_bf16_f32 v123, v126, v127\n"
    "v_cvt_pk_bf16_f32 v136, v136, v137\n"
    "v_pk_fma_f16 v232, v192, v192, s48\n"
    "v_cvt_pk_bf16_f32 v137, v138, v139\n"
    "v_mfma_f32_16x16x32_bf16 v[172:175], a[100:103], a[224:227], 0\n"
    "s_waitcnt lgkmcnt(5)\n"
    "ds_read_u16_d16_hi v249, v249\n"
    "v_or_b32_e32 v190, v190, v206\n"
    "v_mfma_f32_16x16x32_bf16 v[172:175], a[148:151], a[228:231], v[172:175]\n"
    "v_cvt_pk_bf16_f32 v138, v140, v141\n"
    "v_cvt_pk_bf16_f32 v139, v142, v143\n"
    "v_mfma_f32_16x16x32_bf16 v[172:175], a[60:63], a[232:235], v[172:175]\n"
    "ds_read_u16 v194, v194\n"
    "ds_write_b128 v243, v[104:107] offset:25664\n"
    "v_mfma_f32_16x16x32_bf16 v[176:179], a[96:99], a[236:239], 0\n"
    "v_pk_mul_f16 v232, v192, v232\n"
    "v_lshlrev_b32_sdwa v200, 1, v169 dst_sel:DWORD dst_unused:UNUSED_PAD src0_sel:DWORD src1_sel:WORD_1\n"
    "v_mfma_f32_16x16x32_bf16 v[176:179], a[144:147], a[240:243], v[176:179]\n"
    "s_waitcnt lgkmcnt(5)\n"
    "v_or_b32_e32 v191, v191, v207\n"
    "v_lshlrev_b32_sdwa v201, 1, v171 dst_sel:DWORD dst_unused:UNUSED_PAD src0_sel:DWORD src1_sel:WORD_1\n"
    "v_mfma_f32_16x16x32_bf16 v[176:179], a[56:59], a[244:247], v[176:179]\n"
    "ds_read_u16_d16_hi v255, v255\n"
    "ds_write_b128 v243, v[120:123] offset:27712\n"
    "v_mfma_f32_16x16x32_bf16 v[180:183], a[100:103], a[236:239], 0\n"
    "v_exp_f16_e64 v234, v232\n"
    "v_mfma_f32_16x16x32_bf16 v[180:183], a[148:151], a[240:243], v[180:183]\n"
    "v_exp_f16_sdwa v234, v232 dst_sel:WORD_1 dst_unused:UNUSED_PRESERVE src0_sel:WORD_1\n"
    "v_mfma_f32_16x16x32_bf16 v[180:183], a[60:63], a[244:247], v[180:183]\n"
    "ds_read_u16 v195, v195\n"
    "ds_write_b128 v243, v[136:139] offset:29760\n"
    "v_mfma_f32_16x16x32_bf16 v[96:99], a[0:3], v[184:187], 0\n"
    "s_waitcnt lgkmcnt(6)\n"
    "v_or_b32_e32 v193, v193, v249\n"
    "v_pk_fma_f16 v234, v234, s49, s49\n"
    "v_mfma_f32_16x16x32_bf16 v[100:103], a[4:7], v[184:187], 0\n"
    "v_lshlrev_b32_sdwa v202, 1, v173 dst_sel:DWORD dst_unused:UNUSED_PAD src0_sel:DWORD src1_sel:WORD_1\n"
    "v_lshlrev_b32_sdwa v203, 1, v175 dst_sel:DWORD dst_unused:UNUSED_PAD src0_sel:DWORD src1_sel:WORD_1\n"
    "v_mfma_f32_16x16x32_bf16 v[112:115], a[8:11], v[184:187], 0\n"
    "ds_read_u16_d16_hi v160, v160\n"
    "ds_read_u16 v197, v197\n"
    "v_mfma_f32_16x16x32_bf16 v[116:119], a[12:15], v[184:187], 0\n"
    "ds_read_u16_d16_hi v161, v161\n"
    "ds_read_u16 v198, v198\n"
    "v_mfma_f32_16x16x32_bf16 v[128:131], v[0:3], v[184:187], 0\n"
    "s_waitcnt lgkmcnt(6)\n"
    "v_or_b32_e32 v194, v194, v255\n"
    "v_rcp_f16_e64 v232, v234\n"
    "v_mfma_f32_16x16x32_bf16 v[132:135], v[4:7], v[184:187], 0\n"
    "ds_read_u16_d16_hi v162, v162\n"
    "v_lshlrev_b32_sdwa v184, 1, v168 dst_sel:DWORD dst_unused:UNUSED_PAD src0_sel:DWORD src1_sel:WORD_1\n"
    "v_mfma_f32_16x16x32_bf16 v[104:107], a[0:3], v[188:191], 0\n"
    "v_lshlrev_b32_sdwa v185, 1, v170 dst_sel:DWORD dst_unused:UNUSED_PAD src0_sel:DWORD src1_sel:WORD_1\n"
    "v_lshlrev_b32_sdwa v186, 1, v172 dst_sel:DWORD dst_unused:UNUSED_PAD src0_sel:DWORD src1_sel:WORD_1\n"
    "v_mfma_f32_16x16x32_bf16 v[108:111], a[4:7], v[188:191], 0\n"
    "s_nop 0\n"
    "ds_read_u16 v199, v199\n"
    "v_lshlrev_b32_sdwa v187, 1, v174 dst_sel:DWORD dst_unused:UNUSED_PAD src0_sel:DWORD src1_sel:WORD_1\n"
    "v_mfma_f32_16x16x32_bf16 v[120:123], a[8:11], v[188:191], 0\n"
    "s_waitcnt lgkmcnt(6)\n"
    "v_rcp_f16_sdwa v232, v234 dst_sel:WORD_1 dst_unused:UNUSED_PRESERVE src0_sel:WORD_1\n"
    "v_mfma_f32_16x16x32_bf16 v[124:127], a[12:15], v[188:191], 0\n"
    "s_barrier\n"
    "v_mfma_f32_16x16x32_bf16 v[136:139], v[0:3], v[188:191], 0\n"
    "s_waitcnt lgkmcnt(5)\n"
    "ds_read_u16_d16_hi v163, v163\n"
    "ds_read_u16 v184, v184\n"
    "v_mfma_f32_16x16x32_bf16 v[140:143], v[4:7], v[188:191], 0\n"
    "ds_read_u16_d16_hi v200, v200\n"
    "ds_read_u16 v185, v185\n"
    "v_mfma_f32_16x16x32_bf16 v[168:171], a[104:107], a[224:227], 0\n"
    "v_or_b32_e64 v195, v195, v160\n"
    "v_lshlrev_b32_sdwa v188, 1, v176 dst_sel:DWORD dst_unused:UNUSED_PAD src0_sel:DWORD src1_sel:WORD_1\n"
    "v_mfma_f32_16x16x32_bf16 v[168:171], a[152:155], a[228:231], v[168:171]\n"
    "s_waitcnt lgkmcnt(7)\n"
    "ds_read_u16_d16_hi v201, v201\n"
    "v_or_b32_e32 v197, v197, v161\n"
    "v_mfma_f32_16x16x32_bf16 v[168:171], a[64:67], a[232:235], v[168:171]\n"
    "v_lshlrev_b32_sdwa v204, 1, v177 dst_sel:DWORD dst_unused:UNUSED_PAD src0_sel:DWORD src1_sel:WORD_1\n"
    "v_lshlrev_b32_sdwa v189, 1, v178 dst_sel:DWORD dst_unused:UNUSED_PAD src0_sel:DWORD src1_sel:WORD_1\n"
    "v_mfma_f32_16x16x32_bf16 v[172:175], a[108:111], a[224:227], 0\n"
    "ds_read_u16 v186, v186\n"
    "v_lshlrev_b32_sdwa v205, 1, v179 dst_sel:DWORD dst_unused:UNUSED_PAD src0_sel:DWORD src1_sel:WORD_1\n"
    "v_mfma_f32_16x16x32_bf16 v[172:175], a[156:159], a[228:231], v[172:175]\n"
    "v_lshlrev_b32_sdwa v190, 1, v180 dst_sel:DWORD dst_unused:UNUSED_PAD src0_sel:DWORD src1_sel:WORD_1\n"
    "v_lshlrev_b32_sdwa v206, 1, v181 dst_sel:DWORD dst_unused:UNUSED_PAD src0_sel:DWORD src1_sel:WORD_1\n"
    "v_lshlrev_b32_sdwa v191, 1, v182 dst_sel:DWORD dst_unused:UNUSED_PAD src0_sel:DWORD src1_sel:WORD_1\n"
    "v_lshlrev_b32_sdwa v207, 1, v183 dst_sel:DWORD dst_unused:UNUSED_PAD src0_sel:DWORD src1_sel:WORD_1\n"
    "v_fma_mix_f32 v234, v192, v232, 0 op_sel_hi:[1,1,0]\n"
    "v_mfma_f32_16x16x32_bf16 v[172:175], a[68:71], a[232:235], v[172:175]\n"
    "s_waitcnt lgkmcnt(7)\n"
    "ds_read_u16_d16_hi v202, v202\n"
    "v_or_b32_e32 v198, v198, v162\n"
    "v_fma_mix_f32 v232, v192, v232, 0 op_sel:[1,1,0] op_sel_hi:[1,1,0]\n"
    "v_mfma_f32_16x16x32_bf16 v[176:179], a[104:107], a[236:239], 0\n"
    "ds_read_u16 v187, v187\n"
    "v_cvt_pk_bf16_f32 v192, v234, v232\n"
    "v_mfma_f32_16x16x32_bf16 v[176:179], a[152:155], a[240:243], v[176:179]\n"
    "s_waitcnt lgkmcnt(7)\n"
    "ds_read_u16_d16_hi v203, v203\n"
    "v_pk_fma_f16 v232, v196, v196, s48\n"
    "v_mfma_f32_16x16x32_bf16 v[176:179], a[64:67], a[244:247], v[176:179]\n"
    "v_or_b32_e32 v199, v199, v163\n"
    "v_pk_mul_f16 v232, v196, v232\n"
    "v_mfma_f32_16x16x32_bf16 v[180:183], a[108:111], a[236:239], 0\n"
    "ds_read_u16 v188, v188\n"
    "v_lshlrev_b32_sdwa v249, 1, v171 dst_sel:DWORD dst_unused:UNUSED_PAD src0_sel:DWORD src1_sel:WORD_1\n"
    "v_mfma_f32_16x16x32_bf16 v[180:183], a[156:159], a[240:243], v[180:183]\n"
    "v_exp_f16_e32 v234, v232\n"
    "v_mfma_f32_16x16x32_bf16 v[180:183], a[68:71], a[244:247], v[180:183]\n"
    "s_waitcnt lgkmcnt(7)\n"
    "ds_read_u16_d16_hi v204, v204\n"
    "v_exp_f16_sdwa v234, v232 dst_sel:WORD_1 dst_unused:UNUSED_PRESERVE src0_sel:WORD_1\n"
    "v_mfma_f32_16x16x32_bf16 v[96:99], a[16:19], v[192:195], v[96:99]\n"
    "ds_read_u16 v189, v189\n"
    "v_pk_fma_f16 v234, v234, s49, s49\n"
    "v_mfma_f32_16x16x32_bf16 v[100:103], a[20:23], v[192:195], v[100:103]\n"
    "v_rcp_f16_e32 v232, v234\n"
    "v_mfma_f32_16x16x32_bf16 v[112:115], a[24:27], v[192:195], v[112:115]\n"
    "s_waitcnt lgkmcnt(7)\n"
    "ds_read_u16_d16_hi v205, v205\n"
    "v_rcp_f16_sdwa v232, v234 dst_sel:WORD_1 dst_unused:UNUSED_PRESERVE src0_sel:WORD_1\n"
    "v_mfma_f32_16x16x32_bf16 v[116:119], a[28:31], v[192:195], v[116:119]\n"
    "v_fma_mix_f32 v234, v196, v232, 0 op_sel_hi:[1,1,0]\n"
    "v_fma_mix_f32 v232, v196, v232, 0 op_sel:[1,1,0] op_sel_hi:[1,1,0]\n"
    "v_mfma_f32_16x16x32_bf16 v[128:131], v[8:11], v[192:195], v[128:131]\n"
    "ds_read_u16 v190, v190\n"
    "v_cvt_pk_bf16_f32 v196, v234, v232\n"
    "v_mfma_f32_16x16x32_bf16 v[132:135], v[12:15], v[192:195], v[132:135]\n"
    "s_waitcnt lgkmcnt(7)\n"
    "ds_read_u16_d16_hi v206, v206\n"
    "v_or_b32_e32 v184, v184, v200\n"
    "v_mfma_f32_16x16x32_bf16 v[104:107], a[16:19], v[196:199], v[104:107]\n"
    "v_or_b32_e32 v185, v185, v201\n"
    "v_cvt_pk_f16_f32 v192, v168, v169\n"
    "v_mfma_f32_16x16x32_bf16 v[108:111], a[20:23], v[196:199], v[108:111]\n"
    "s_waitcnt vmcnt(7)\n"
    "ds_read_u16 v191, v191\n"
    "v_lshlrev_b32_sdwa v193, 1, v170 dst_sel:DWORD dst_unused:UNUSED_PAD src0_sel:DWORD src1_sel:WORD_1\n"
    "v_mfma_f32_16x16x32_bf16 v[120:123], a[24:27], v[196:199], v[120:123]\n"
    "v_lshlrev_b32_sdwa v194, 1, v172 dst_sel:DWORD dst_unused:UNUSED_PAD src0_sel:DWORD src1_sel:WORD_1\n"
    "v_or_b32_e32 v186, v186, v202\n"
    "v_mfma_f32_16x16x32_bf16 v[124:127], a[28:31], v[196:199], v[124:127]\n"
    "s_waitcnt lgkmcnt(7)\n"
    "ds_read_u16_d16_hi v207, v207\n"
    "v_lshlrev_b32_sdwa v255, 1, v173 dst_sel:DWORD dst_unused:UNUSED_PAD src0_sel:DWORD src1_sel:WORD_1\n"
    "v_lshlrev_b32_sdwa v195, 1, v174 dst_sel:DWORD dst_unused:UNUSED_PAD src0_sel:DWORD src1_sel:WORD_1\n"
    "v_mfma_f32_16x16x32_bf16 v[136:139], v[8:11], v[196:199], v[136:139]\n"
    "ds_read_u16 v193, v193\n"
    "v_lshlrev_b32_sdwa v160, 1, v175 dst_sel:DWORD dst_unused:UNUSED_PAD src0_sel:DWORD src1_sel:WORD_1\n"
    "v_mfma_f32_16x16x32_bf16 v[140:143], v[12:15], v[196:199], v[140:143]\n"
    "s_waitcnt vmcnt(6)\n"
    "v_cvt_pk_f16_f32 v196, v176, v177\n"
    "v_lshlrev_b32_sdwa v197, 1, v178 dst_sel:DWORD dst_unused:UNUSED_PAD src0_sel:DWORD src1_sel:WORD_1\n"
    "v_smfmac_f32_32x32x32_bf16 v[112:127], a[248:251], v[152:159], v241\n"
    "s_waitcnt lgkmcnt(7)\n"
    "ds_read_u16_d16_hi v249, v249\n"
    "v_lshlrev_b32_sdwa v161, 1, v179 dst_sel:DWORD dst_unused:UNUSED_PAD src0_sel:DWORD src1_sel:WORD_1\n"
    "v_lshlrev_b32_sdwa v198, 1, v180 dst_sel:DWORD dst_unused:UNUSED_PAD src0_sel:DWORD src1_sel:WORD_1\n"
    "v_lshlrev_b32_sdwa v162, 1, v181 dst_sel:DWORD dst_unused:UNUSED_PAD src0_sel:DWORD src1_sel:WORD_1\n"
    "v_lshlrev_b32_sdwa v199, 1, v182 dst_sel:DWORD dst_unused:UNUSED_PAD src0_sel:DWORD src1_sel:WORD_1\n"
    "v_smfmac_f32_32x32x32_bf16 v[96:111], a[248:251], v[144:151], v241\n"
    "ds_read_u16 v194, v194\n"
    "v_or_b32_e64 v187, v187, v203\n"
    "v_lshlrev_b32_sdwa v163, 1, v183 dst_sel:DWORD dst_unused:UNUSED_PAD src0_sel:DWORD src1_sel:WORD_1\n"
    "v_or_b32_e64 v188, v188, v204\n"
    "v_pk_fma_f16 v232, v192, v192, s48\n"
    "v_smfmac_f32_32x32x32_bf16 v[128:143], a[248:251], v[208:215], v241\n"
    "s_waitcnt lgkmcnt(7)\n"
    "ds_read_u16_d16_hi v255, v255\n"
    "v_or_b32_e32 v189, v189, v205\n"
    "v_pk_mul_f16 v232, v192, v232\n"
    "v_exp_f16_e64 v234, v232\n"
    "v_mfma_f32_16x16x32_bf16 v[168:171], a[112:115], a[224:227], 0\n"
    "ds_read_b128 v[152:155], v242 offset:24640\n"
    "ds_read_b128 v[156:159], v242 offset:25664\n"
    "v_mfma_f32_16x16x32_bf16 v[168:171], a[160:163], a[228:231], v[168:171]\n"
    "s_waitcnt lgkmcnt(8)\n"
    "ds_read_u16 v195, v195\n"
    "v_or_b32_e32 v190, v190, v206\n"
    "v_mfma_f32_16x16x32_bf16 v[168:171], a[72:75], a[232:235], v[168:171]\n"
    "v_exp_f16_sdwa v234, v232 dst_sel:WORD_1 dst_unused:UNUSED_PRESERVE src0_sel:WORD_1\n"
    "v_mfma_f32_16x16x32_bf16 v[172:175], a[116:119], a[224:227], 0\n"
    "ds_read_u16_d16_hi v160, v160\n"
    "v_pk_fma_f16 v234, v234, s49, s49\n"
    "v_mfma_f32_16x16x32_bf16 v[172:175], a[164:167], a[228:231], v[172:175]\n"
    "v_rcp_f16_e32 v232, v234\n"
    "v_mfma_f32_16x16x32_bf16 v[172:175], v[52:55], a[232:235], v[172:175]\n"
    "s_waitcnt lgkmcnt(8)\n"
    "ds_read_u16 v197, v197\n"
    "v_or_b32_e64 v191, v191, v207\n"
    "v_mfma_f32_16x16x32_bf16 v[176:179], a[112:115], a[236:239], 0\n"
    "v_lshlrev_b32_sdwa v200, 1, v169 dst_sel:DWORD dst_unused:UNUSED_PAD src0_sel:DWORD src1_sel:WORD_1\n"
    "v_lshlrev_b32_sdwa v201, 1, v171 dst_sel:DWORD dst_unused:UNUSED_PAD src0_sel:DWORD src1_sel:WORD_1\n"
    "v_mfma_f32_16x16x32_bf16 v[176:179], a[160:163], a[240:243], v[176:179]\n"
    "ds_read_u16_d16_hi v161, v161\n"
    "v_lshlrev_b32_sdwa v202, 1, v173 dst_sel:DWORD dst_unused:UNUSED_PAD src0_sel:DWORD src1_sel:WORD_1\n"
    "v_mfma_f32_16x16x32_bf16 v[176:179], a[72:75], a[244:247], v[176:179]\n"
    "s_waitcnt lgkmcnt(8)\n"
    "ds_read_u16 v198, v198\n"
    "v_or_b32_e32 v193, v193, v249\n"
    "v_mfma_f32_16x16x32_bf16 v[180:183], a[116:119], a[236:239], 0\n"
    "v_lshlrev_b32_sdwa v203, 1, v175 dst_sel:DWORD dst_unused:UNUSED_PAD src0_sel:DWORD src1_sel:WORD_1\n"
    "v_mfma_f32_16x16x32_bf16 v[180:183], a[164:167], a[240:243], v[180:183]\n"
    "ds_read_u16_d16_hi v162, v162\n"
    "v_mfma_f32_16x16x32_bf16 v[180:183], v[52:55], a[244:247], v[180:183]\n"
    "s_nop 0\n"
    "v_rcp_f16_sdwa v232, v234 dst_sel:WORD_1 dst_unused:UNUSED_PRESERVE src0_sel:WORD_1\n"
    "v_mfma_f32_16x16x32_bf16 v[112:115], a[40:43], v[184:187], v[112:115]\n"
    "s_waitcnt lgkmcnt(6)\n"
    "ds_read_u16 v199, v199\n"
    "v_or_b32_e64 v194, v194, v255\n"
    "v_smfmac_f32_32x32x32_bf16 v[96:111], a[248:251], v[152:159], v241\n"
    "ds_read_b128 v[152:155], v242 offset:26688\n"
    "v_lshlrev_b32_sdwa v204, 1, v177 dst_sel:DWORD dst_unused:UNUSED_PAD src0_sel:DWORD src1_sel:WORD_1\n"
    "v_lshlrev_b32_sdwa v205, 1, v179 dst_sel:DWORD dst_unused:UNUSED_PAD src0_sel:DWORD src1_sel:WORD_1\n"
    "v_lshlrev_b32_sdwa v206, 1, v181 dst_sel:DWORD dst_unused:UNUSED_PAD src0_sel:DWORD src1_sel:WORD_1\n"
    "v_lshlrev_b32_sdwa v207, 1, v183 dst_sel:DWORD dst_unused:UNUSED_PAD src0_sel:DWORD src1_sel:WORD_1\n"
    "v_mfma_f32_16x16x32_bf16 v[116:119], a[44:47], v[184:187], v[116:119]\n"
    "ds_read_b128 v[156:159], v242 offset:27712\n"
    "ds_read_u16_d16_hi v163, v163\n"
    "v_mfma_f32_16x16x32_bf16 v[128:131], v[16:19], v[184:187], v[128:131]\n"
    "v_fma_mix_f32 v234, v192, v232, 0 op_sel_hi:[1,1,0]\n"
    "v_fma_mix_f32 v232, v192, v232, 0 op_sel:[1,1,0] op_sel_hi:[1,1,0]\n"
    "v_mfma_f32_16x16x32_bf16 v[96:99], a[32:35], v[184:187], v[96:99]\n"
    "s_waitcnt lgkmcnt(8)\n"
    "ds_read_u16_d16_hi v200, v200\n"
    "v_or_b32_e32 v195, v195, v160\n"
    "v_mfma_f32_16x16x32_bf16 v[100:103], a[36:39], v[184:187], v[100:103]\n"
    "v_cvt_pk_bf16_f32 v192, v234, v232\n"
    "v_pk_fma_f16 v232, v196, v196, s48\n"
    "v_mfma_f32_16x16x32_bf16 v[132:135], v[20:23], v[184:187], v[132:135]\n"
    "ds_read_u16_d16_hi v201, v201\n"
    "v_lshlrev_b32_sdwa v184, 1, v168 dst_sel:DWORD dst_unused:UNUSED_PAD src0_sel:DWORD src1_sel:WORD_1\n"
    "v_mfma_f32_16x16x32_bf16 v[104:107], a[32:35], v[188:191], v[104:107]\n"
    "v_lshlrev_b32_sdwa v185, 1, v170 dst_sel:DWORD dst_unused:UNUSED_PAD src0_sel:DWORD src1_sel:WORD_1\n"
    "v_lshlrev_b32_sdwa v186, 1, v172 dst_sel:DWORD dst_unused:UNUSED_PAD src0_sel:DWORD src1_sel:WORD_1\n"
    "v_mfma_f32_16x16x32_bf16 v[108:111], a[36:39], v[188:191], v[108:111]\n"
    "s_waitcnt lgkmcnt(8)\n"
    "ds_read_u16 v184, v184\n"
    "v_or_b32_e32 v197, v197, v161\n"
    "v_mfma_f32_16x16x32_bf16 v[120:123], a[40:43], v[188:191], v[120:123]\n"
    "v_lshlrev_b32_sdwa v187, 1, v174 dst_sel:DWORD dst_unused:UNUSED_PAD src0_sel:DWORD src1_sel:WORD_1\n"
    "v_pk_mul_f16 v232, v196, v232\n"
    "v_mfma_f32_16x16x32_bf16 v[124:127], a[44:47], v[188:191], v[124:127]\n"
    "ds_read_u16 v185, v185\n"
    "v_exp_f16_e32 v234, v232\n"
    "v_mfma_f32_16x16x32_bf16 v[136:139], v[16:19], v[188:191], v[136:139]\n"
    "s_waitcnt lgkmcnt(8)\n"
    "ds_read_u16 v186, v186\n"
    "v_or_b32_e64 v198, v198, v162\n"
    "v_exp_f16_sdwa v234, v232 dst_sel:WORD_1 dst_unused:UNUSED_PRESERVE src0_sel:WORD_1\n"
    "v_mfma_f32_16x16x32_bf16 v[140:143], v[20:23], v[188:191], v[140:143]\n"
    "ds_read_u16_d16_hi v202, v202\n"
    "v_lshlrev_b32_sdwa v188, 1, v176 dst_sel:DWORD dst_unused:UNUSED_PAD src0_sel:DWORD src1_sel:WORD_1\n"
    "v_lshlrev_b32_sdwa v189, 1, v178 dst_sel:DWORD dst_unused:UNUSED_PAD src0_sel:DWORD src1_sel:WORD_1\n"
    "v_lshlrev_b32_sdwa v190, 1, v180 dst_sel:DWORD dst_unused:UNUSED_PAD src0_sel:DWORD src1_sel:WORD_1\n"
    "v_lshlrev_b32_sdwa v191, 1, v182 dst_sel:DWORD dst_unused:UNUSED_PAD src0_sel:DWORD src1_sel:WORD_1\n"
    "v_mfma_f32_16x16x32_bf16 v[168:171], a[120:123], a[224:227], 0\n"
    "s_waitcnt lgkmcnt(7)\n"
    "ds_read_u16 v187, v187\n"
    "v_pk_fma_f16 v234, v234, s49, s49\n"
    "v_mfma_f32_16x16x32_bf16 v[168:171], a[168:171], a[228:231], v[168:171]\n"
    "v_rcp_f16_e64 v232, v234\n"
    "v_mfma_f32_16x16x32_bf16 v[168:171], a[76:79], a[232:235], v[168:171]\n"
    "s_waitcnt lgkmcnt(7)\n"
    "ds_read_u16_d16_hi v203, v203\n"
    "v_or_b32_e64 v199, v199, v163\n"
    "v_smfmac_f32_32x32x32_bf16 v[112:127], a[248:251], v[152:159], v241\n"
    "v_rcp_f16_sdwa v232, v234 dst_sel:WORD_1 dst_unused:UNUSED_PRESERVE src0_sel:WORD_1\n"
    "ds_read_u16 v188, v188\n"
    "v_mfma_f32_16x16x32_bf16 v[172:175], a[124:127], a[224:227], 0\n"
    "v_fma_mix_f32 v234, v196, v232, 0 op_sel_hi:[1,1,0]\n"
    "v_fma_mix_f32 v232, v196, v232, 0 op_sel:[1,1,0] op_sel_hi:[1,1,0]\n"
    "v_mfma_f32_16x16x32_bf16 v[172:175], a[172:175], a[228:231], v[172:175]\n"
    "ds_read_b128 v[152:155], v242 offset:28736\n"
    "ds_read_b128 v[156:159], v242 offset:29760\n"
    "v_mfma_f32_16x16x32_bf16 v[172:175], v[60:63], a[232:235], v[172:175]\n"
    "s_waitcnt lgkmcnt(8)\n"
    "v_cvt_pk_bf16_f32 v196, v234, v232\n"
    "v_or_b32_e32 v184, v184, v200\n"
    "v_mfma_f32_16x16x32_bf16 v[176:179], a[120:123], a[236:239], 0\n"
    "ds_read_u16_d16_hi v204, v204\n"
    "v_lshlrev_b32_sdwa v249, 1, v171 dst_sel:DWORD dst_unused:UNUSED_PAD src0_sel:DWORD src1_sel:WORD_1\n"
    "v_mfma_f32_16x16x32_bf16 v[176:179], a[168:171], a[240:243], v[176:179]\n"
    "s_waitcnt lgkmcnt(8)\n"
    "ds_read_b128 a[224:227], v242 offset:0\n"
    "v_or_b32_e32 v185, v185, v201\n"
    "v_mfma_f32_16x16x32_bf16 v[176:179], a[76:79], a[244:247], v[176:179]\n"
    "ds_read_u16 v189, v189\n"
    "v_lshlrev_b32_sdwa v255, 1, v173 dst_sel:DWORD dst_unused:UNUSED_PAD src0_sel:DWORD src1_sel:WORD_1\n"
    "v_mfma_f32_16x16x32_bf16 v[180:183], a[124:127], a[236:239], 0\n"
    "v_lshlrev_b32_sdwa v160, 1, v175 dst_sel:DWORD dst_unused:UNUSED_PAD src0_sel:DWORD src1_sel:WORD_1\n"
    "v_mfma_f32_16x16x32_bf16 v[180:183], a[172:175], a[240:243], v[180:183]\n"
    "ds_read_b128 a[228:231], v242 offset:1024\n"
    "v_mfma_f32_16x16x32_bf16 v[180:183], v[60:63], a[244:247], v[180:183]\n"
    "s_waitcnt lgkmcnt(9)\n"
    "ds_read_u16_d16_hi v205, v205\n"
    "v_or_b32_e32 v186, v186, v202\n"
    "v_mfma_f32_16x16x32_bf16 v[96:99], a[176:179], v[192:195], v[96:99]\n"
    "v_lshlrev_b32_sdwa v161, 1, v179 dst_sel:DWORD dst_unused:UNUSED_PAD src0_sel:DWORD src1_sel:WORD_1\n"
    "v_mfma_f32_16x16x32_bf16 v[100:103], a[180:183], v[192:195], v[100:103]\n"
    "s_nop 0\n"
    "ds_read_b128 a[232:235], v242 offset:2048\n"
    "v_mfma_f32_16x16x32_bf16 v[112:115], a[184:187], v[192:195], v[112:115]\n"
    "s_waitcnt lgkmcnt(9)\n"
    "ds_read_u16 v190, v190\n"
    "v_lshlrev_b32_sdwa v162, 1, v181 dst_sel:DWORD dst_unused:UNUSED_PAD src0_sel:DWORD src1_sel:WORD_1\n"
    "v_mfma_f32_16x16x32_bf16 v[116:119], a[188:191], v[192:195], v[116:119]\n"
    "v_or_b32_e64 v187, v187, v203\n"
    "v_lshlrev_b32_sdwa v163, 1, v183 dst_sel:DWORD dst_unused:UNUSED_PAD src0_sel:DWORD src1_sel:WORD_1\n"
    "v_mfma_f32_16x16x32_bf16 v[128:131], v[24:27], v[192:195], v[128:131]\n"
    "ds_read_b128 a[236:239], v242 offset:3072\n"
    "ds_read_b128 a[240:243], v242 offset:4096\n"
    "v_mfma_f32_16x16x32_bf16 v[132:135], v[28:31], v[192:195], v[132:135]\n"
    "v_cvt_pk_f16_f32 v192, v168, v169\n"
    "v_lshlrev_b32_sdwa v193, 1, v170 dst_sel:DWORD dst_unused:UNUSED_PAD src0_sel:DWORD src1_sel:WORD_1\n"
    "v_mfma_f32_16x16x32_bf16 v[104:107], a[176:179], v[196:199], v[104:107]\n"
    "s_nop 0\n"
    "ds_read_u16_d16_hi v206, v206\n"
    "v_lshlrev_b32_sdwa v194, 1, v172 dst_sel:DWORD dst_unused:UNUSED_PAD src0_sel:DWORD src1_sel:WORD_1\n"
    "v_mfma_f32_16x16x32_bf16 v[108:111], a[180:183], v[196:199], v[108:111]\n"
    "s_waitcnt lgkmcnt(8)\n"
    "ds_read_b128 a[244:247], v242 offset:5120\n"
    "v_lshlrev_b32_sdwa v195, 1, v174 dst_sel:DWORD dst_unused:UNUSED_PAD src0_sel:DWORD src1_sel:WORD_1\n"
    "v_mfma_f32_16x16x32_bf16 v[120:123], a[184:187], v[196:199], v[120:123]\n"
    "ds_read_u16 v191, v191\n"
    "v_or_b32_e64 v188, v188, v204\n"
    "v_mfma_f32_16x16x32_bf16 v[124:127], a[188:191], v[196:199], v[124:127]\n"
    "v_pk_fma_f16 v232, v192, v192, s48\n"
    "v_pk_mul_f16 v232, v192, v232\n"
    "v_mfma_f32_16x16x32_bf16 v[136:139], v[24:27], v[196:199], v[136:139]\n"
    "ds_read_u16_d16_hi v207, v207\n"
    "v_exp_f16_e64 v234, v232\n"
    "v_mfma_f32_16x16x32_bf16 v[140:143], v[28:31], v[196:199], v[140:143]\n"
    "v_cvt_pk_f16_f32 v196, v176, v177\n"
    "v_lshlrev_b32_sdwa v197, 1, v178 dst_sel:DWORD dst_unused:UNUSED_PAD src0_sel:DWORD src1_sel:WORD_1\n"
    "v_mfma_f32_16x16x32_bf16 v[168:171], a[80:83], a[224:227], 0\n"
    "s_waitcnt lgkmcnt(9)\n"
    "ds_read_u16 v193, v193\n"
    "v_lshlrev_b32_sdwa v198, 1, v180 dst_sel:DWORD dst_unused:UNUSED_PAD src0_sel:DWORD src1_sel:WORD_1\n"
    "v_mfma_f32_16x16x32_bf16 v[168:171], a[128:131], a[228:231], v[168:171]\n"
    "s_waitcnt lgkmcnt(8)\n"
    "ds_read_u16_d16_hi v249, v249\n"
    "v_lshlrev_b32_sdwa v199, 1, v182 dst_sel:DWORD dst_unused:UNUSED_PAD src0_sel:DWORD src1_sel:WORD_1\n"
    "v_mfma_f32_16x16x32_bf16 v[168:171], a[48:51], a[232:235], v[168:171]\n"
    "v_or_b32_e32 v189, v189, v205\n"
    "v_exp_f16_sdwa v234, v232 dst_sel:WORD_1 dst_unused:UNUSED_PRESERVE src0_sel:WORD_1\n"
    "v_smfmac_f32_32x32x32_bf16 v[128:143], a[248:251], v[152:159], v241\n"
    "s_waitcnt lgkmcnt(7)\n"
    "ds_read_u16 v194, v194\n"
    "v_pk_fma_f16 v234, v234, s49, s49\n"
    "v_rcp_f16_e64 v232, v234\n"
    "ds_read_u16_d16_hi v255, v255\n"
    "v_lshlrev_b32_sdwa v200, 1, v169 dst_sel:DWORD dst_unused:UNUSED_PAD src0_sel:DWORD src1_sel:WORD_1\n"
    "v_lshlrev_b32_sdwa v201, 1, v171 dst_sel:DWORD dst_unused:UNUSED_PAD src0_sel:DWORD src1_sel:WORD_1\n"
    "v_mfma_f32_16x16x32_bf16 v[172:175], a[84:87], a[224:227], 0\n"
    "ds_read_u16 v195, v195\n"
    "v_mfma_f32_16x16x32_bf16 v[172:175], a[132:135], a[228:231], v[172:175]\n"
    "s_waitcnt lgkmcnt(7)\n"
    "v_or_b32_e32 v190, v190, v206\n"
    "v_mfma_f32_16x16x32_bf16 v[172:175], v[48:51], a[232:235], v[172:175]\n"
    "ds_read_u16_d16_hi v160, v160\n"
    "v_rcp_f16_sdwa v232, v234 dst_sel:WORD_1 dst_unused:UNUSED_PRESERVE src0_sel:WORD_1\n"
    "v_mfma_f32_16x16x32_bf16 v[176:179], a[80:83], a[236:239], 0\n"
    "v_fma_mix_f32 v234, v192, v232, 0 op_sel_hi:[1,1,0]\n"
    "v_fma_mix_f32 v232, v192, v232, 0 op_sel:[1,1,0] op_sel_hi:[1,1,0]\n"
    "v_mfma_f32_16x16x32_bf16 v[176:179], a[128:131], a[240:243], v[176:179]\n"
    "ds_read_u16 v197, v197\n"
    "v_cvt_pk_bf16_f32 v192, v234, v232\n"
    "v_mfma_f32_16x16x32_bf16 v[176:179], a[48:51], a[244:247], v[176:179]\n"
    "s_waitcnt lgkmcnt(7)\n"
    "ds_read_u16_d16_hi v161, v161\n"
    "v_or_b32_e32 v191, v191, v207\n"
    "v_mfma_f32_16x16x32_bf16 v[180:183], a[84:87], a[236:239], 0\n"
    "v_lshlrev_b32_sdwa v202, 1, v173 dst_sel:DWORD dst_unused:UNUSED_PAD src0_sel:DWORD src1_sel:WORD_1\n"
    "v_lshlrev_b32_sdwa v203, 1, v175 dst_sel:DWORD dst_unused:UNUSED_PAD src0_sel:DWORD src1_sel:WORD_1\n"
    "v_mfma_f32_16x16x32_bf16 v[180:183], a[132:135], a[240:243], v[180:183]\n"
    "ds_read_u16 v198, v198\n"
    "v_lshlrev_b32_sdwa v204, 1, v177 dst_sel:DWORD dst_unused:UNUSED_PAD src0_sel:DWORD src1_sel:WORD_1\n"
    "v_mfma_f32_16x16x32_bf16 v[180:183], v[48:51], a[244:247], v[180:183]\n"
    "v_lshlrev_b32_sdwa v205, 1, v179 dst_sel:DWORD dst_unused:UNUSED_PAD src0_sel:DWORD src1_sel:WORD_1\n"
    "v_pk_fma_f16 v232, v196, v196, s48\n"
    "v_mfma_f32_16x16x32_bf16 v[96:99], a[192:195], v[184:187], v[96:99]\n"
    "s_waitcnt lgkmcnt(7)\n"
    "ds_read_u16_d16_hi v162, v162\n"
    "v_or_b32_e32 v193, v193, v249\n"
    "v_mfma_f32_16x16x32_bf16 v[100:103], a[196:199], v[184:187], v[100:103]\n"
    "v_pk_mul_f16 v232, v196, v232\n"
    "v_lshlrev_b32_sdwa v206, 1, v181 dst_sel:DWORD dst_unused:UNUSED_PAD src0_sel:DWORD src1_sel:WORD_1\n"
    "v_mfma_f32_16x16x32_bf16 v[112:115], a[200:203], v[184:187], v[112:115]\n"
    "ds_read_u16 v199, v199\n"
    "v_lshlrev_b32_sdwa v207, 1, v183 dst_sel:DWORD dst_unused:UNUSED_PAD src0_sel:DWORD src1_sel:WORD_1\n"
    "v_mfma_f32_16x16x32_bf16 v[116:119], a[204:207], v[184:187], v[116:119]\n"
    "s_waitcnt lgkmcnt(7)\n"
    "ds_read_u16_d16_hi v163, v163\n"
    "v_or_b32_e32 v194, v194, v255\n"
    "v_mfma_f32_16x16x32_bf16 v[128:131], v[32:35], v[184:187], v[128:131]\n"
    "v_exp_f16_e64 v234, v232\n"
    "v_mfma_f32_16x16x32_bf16 v[132:135], v[36:39], v[184:187], v[132:135]\n"
    "ds_read_u16_d16_hi v200, v200\n"
    "v_lshlrev_b32_sdwa v184, 1, v168 dst_sel:DWORD dst_unused:UNUSED_PAD src0_sel:DWORD src1_sel:WORD_1\n"
    "v_mfma_f32_16x16x32_bf16 v[104:107], a[192:195], v[188:191], v[104:107]\n"
    "v_lshlrev_b32_sdwa v185, 1, v170 dst_sel:DWORD dst_unused:UNUSED_PAD src0_sel:DWORD src1_sel:WORD_1\n"
    "v_lshlrev_b32_sdwa v186, 1, v172 dst_sel:DWORD dst_unused:UNUSED_PAD src0_sel:DWORD src1_sel:WORD_1\n"
    "v_mfma_f32_16x16x32_bf16 v[108:111], a[196:199], v[188:191], v[108:111]\n"
    "s_waitcnt lgkmcnt(7)\n"
    "ds_read_u16 v184, v184\n"
    "v_or_b32_e32 v195, v195, v160\n"
    "v_mfma_f32_16x16x32_bf16 v[120:123], a[200:203], v[188:191], v[120:123]\n"
    "v_lshlrev_b32_sdwa v187, 1, v174 dst_sel:DWORD dst_unused:UNUSED_PAD src0_sel:DWORD src1_sel:WORD_1\n"
    "v_exp_f16_sdwa v234, v232 dst_sel:WORD_1 dst_unused:UNUSED_PRESERVE src0_sel:WORD_1\n"
    "v_mfma_f32_16x16x32_bf16 v[124:127], a[204:207], v[188:191], v[124:127]\n"
    "ds_read_u16 v185, v185\n"
    "v_pk_fma_f16 v234, v234, s49, s49\n"
    "v_mfma_f32_16x16x32_bf16 v[136:139], v[32:35], v[188:191], v[136:139]\n"
    "s_waitcnt lgkmcnt(7)\n"
    "ds_read_u16_d16_hi v201, v201\n"
    "v_or_b32_e32 v197, v197, v161\n"
    "v_mfma_f32_16x16x32_bf16 v[140:143], v[36:39], v[188:191], v[140:143]\n"
    "v_lshlrev_b32_sdwa v188, 1, v176 dst_sel:DWORD dst_unused:UNUSED_PAD src0_sel:DWORD src1_sel:WORD_1\n"
    "v_lshlrev_b32_sdwa v189, 1, v178 dst_sel:DWORD dst_unused:UNUSED_PAD src0_sel:DWORD src1_sel:WORD_1\n"
    "v_mfma_f32_16x16x32_bf16 v[168:171], a[88:91], a[224:227], 0\n"
    "ds_read_u16 v186, v186\n"
    "v_lshlrev_b32_sdwa v190, 1, v180 dst_sel:DWORD dst_unused:UNUSED_PAD src0_sel:DWORD src1_sel:WORD_1\n"
    "v_mfma_f32_16x16x32_bf16 v[168:171], a[136:139], a[228:231], v[168:171]\n"
    "v_lshlrev_b32_sdwa v191, 1, v182 dst_sel:DWORD dst_unused:UNUSED_PAD src0_sel:DWORD src1_sel:WORD_1\n"
    "v_mfma_f32_16x16x32_bf16 v[168:171], a[52:55], a[232:235], v[168:171]\n"
    "s_waitcnt lgkmcnt(7)\n"
    "ds_read_u16_d16_hi v202, v202\n"
    "v_or_b32_e32 v198, v198, v162\n"
    "v_mfma_f32_16x16x32_bf16 v[172:175], a[92:95], a[224:227], 0\n"
    "ds_read_u16 v187, v187\n"
    "v_mfma_f32_16x16x32_bf16 v[172:175], a[140:143], a[228:231], v[172:175]\n"
    "v_rcp_f16_e32 v232, v234\n"
    "v_mfma_f32_16x16x32_bf16 v[172:175], v[56:59], a[232:235], v[172:175]\n"
    "s_waitcnt lgkmcnt(7)\n"
    "ds_read_u16_d16_hi v203, v203\n"
    "v_or_b32_e64 v199, v199, v163\n"
    "v_mfma_f32_16x16x32_bf16 v[176:179], a[88:91], a[236:239], 0\n"
    "v_rcp_f16_sdwa v232, v234 dst_sel:WORD_1 dst_unused:UNUSED_PRESERVE src0_sel:WORD_1\n"
    "v_mfma_f32_16x16x32_bf16 v[176:179], a[136:139], a[240:243], v[176:179]\n"
    "ds_read_u16 v188, v188\n"
    "v_fma_mix_f32 v234, v196, v232, 0 op_sel_hi:[1,1,0]\n"
    "v_mfma_f32_16x16x32_bf16 v[176:179], a[52:55], a[244:247], v[176:179]\n"
    "s_nop 0\n"
    "v_fma_mix_f32 v232, v196, v232, 0 op_sel:[1,1,0] op_sel_hi:[1,1,0]\n"
    "v_lshlrev_b32_sdwa v249, 1, v171 dst_sel:DWORD dst_unused:UNUSED_PAD src0_sel:DWORD src1_sel:WORD_1\n"
    "v_mfma_f32_16x16x32_bf16 v[180:183], a[92:95], a[236:239], 0\n"
    "s_waitcnt lgkmcnt(7)\n"
    "ds_read_u16_d16_hi v204, v204\n"
    "v_cvt_pk_bf16_f32 v196, v234, v232\n"
    "v_mfma_f32_16x16x32_bf16 v[180:183], a[140:143], a[240:243], v[180:183]\n"
    "ds_read_u16 v189, v189\n"
    "v_or_b32_e64 v184, v184, v200\n"
    "v_mfma_f32_16x16x32_bf16 v[180:183], v[56:59], a[244:247], v[180:183]\n"
    "v_lshlrev_b32_sdwa v255, 1, v173 dst_sel:DWORD dst_unused:UNUSED_PAD src0_sel:DWORD src1_sel:WORD_1\n"
    "v_lshlrev_b32_sdwa v160, 1, v175 dst_sel:DWORD dst_unused:UNUSED_PAD src0_sel:DWORD src1_sel:WORD_1\n"
    "v_mfma_f32_16x16x32_bf16 v[96:99], a[208:211], v[192:195], v[96:99]\n"
    "s_waitcnt lgkmcnt(7)\n"
    "ds_read_u16_d16_hi v205, v205\n"
    "v_or_b32_e32 v185, v185, v201\n"
    "v_mfma_f32_16x16x32_bf16 v[100:103], a[212:215], v[192:195], v[100:103]\n"
    "s_barrier\n"
    "v_mfma_f32_16x16x32_bf16 v[112:115], a[216:219], v[192:195], v[112:115]\n"
    "s_waitcnt lgkmcnt(6)\n"
    "ds_read_u16 v190, v190\n"
    "ds_read_u16_d16_hi v206, v206\n"
    "v_mfma_f32_16x16x32_bf16 v[116:119], a[220:223], v[192:195], v[116:119]\n"
    "ds_read_u16 v191, v191\n"
    "v_or_b32_e32 v186, v186, v202\n"
    "v_mfma_f32_16x16x32_bf16 v[128:131], v[40:43], v[192:195], v[128:131]\n"
    "v_lshlrev_b32_sdwa v161, 1, v179 dst_sel:DWORD dst_unused:UNUSED_PAD src0_sel:DWORD src1_sel:WORD_1\n"
    "v_lshlrev_b32_sdwa v162, 1, v181 dst_sel:DWORD dst_unused:UNUSED_PAD src0_sel:DWORD src1_sel:WORD_1\n"
    "v_mfma_f32_16x16x32_bf16 v[132:135], v[44:47], v[192:195], v[132:135]\n"
    "s_waitcnt lgkmcnt(7)\n"
    "ds_read_u16_d16_hi v207, v207\n"
    "v_cvt_pk_f16_f32 v192, v168, v169\n"
    "v_mfma_f32_16x16x32_bf16 v[104:107], a[208:211], v[196:199], v[104:107]\n"
    "ds_read_u16_d16_hi v249, v249\n"
    "v_lshlrev_b32_sdwa v193, 1, v170 dst_sel:DWORD dst_unused:UNUSED_PAD src0_sel:DWORD src1_sel:WORD_1\n"
    "v_mfma_f32_16x16x32_bf16 v[108:111], a[212:215], v[196:199], v[108:111]\n"
    "s_nop 0\n"
    "v_lshlrev_b32_sdwa v194, 1, v172 dst_sel:DWORD dst_unused:UNUSED_PAD src0_sel:DWORD src1_sel:WORD_1\n"
    "v_lshlrev_b32_sdwa v195, 1, v174 dst_sel:DWORD dst_unused:UNUSED_PAD src0_sel:DWORD src1_sel:WORD_1\n"
    "v_mfma_f32_16x16x32_bf16 v[120:123], a[216:219], v[196:199], v[120:123]\n"
    "s_waitcnt lgkmcnt(7)\n"
    "buffer_load_dwordx4 v[152:155], v245, s[40:43], s58 offen offset:64\n"
    "buffer_load_dwordx4 v[144:147], v245, s[40:43], s58 offen offset:0\n"
    "buffer_load_dwordx4 v[208:211], v245, s[40:43], s58 offen offset:128\n"
    "ds_read_u16 v193, v193\n"
    "v_mfma_f32_16x16x32_bf16 v[124:127], a[220:223], v[196:199], v[124:127]\n"
    "ds_read_u16 v194, v194\n"
    "v_or_b32_e64 v187, v187, v203\n"
    "v_mfma_f32_16x16x32_bf16 v[136:139], v[40:43], v[196:199], v[136:139]\n"
    "v_lshlrev_b32_sdwa v163, 1, v183 dst_sel:DWORD dst_unused:UNUSED_PAD src0_sel:DWORD src1_sel:WORD_1\n"
    "v_or_b32_e32 v188, v188, v204\n"
    "v_mfma_f32_16x16x32_bf16 v[140:143], v[44:47], v[196:199], v[140:143]\n"
    "s_waitcnt lgkmcnt(7)\n"
    "ds_read_u16_d16_hi v255, v255\n"
    "v_cvt_pk_f16_f32 v196, v176, v177\n"
    "v_mfma_f32_16x16x32_bf16 v[168:171], a[96:99], a[224:227], 0\n"
    "v_lshlrev_b32_sdwa v197, 1, v178 dst_sel:DWORD dst_unused:UNUSED_PAD src0_sel:DWORD src1_sel:WORD_1\n"
    "v_lshlrev_b32_sdwa v198, 1, v180 dst_sel:DWORD dst_unused:UNUSED_PAD src0_sel:DWORD src1_sel:WORD_1\n"
    "v_mfma_f32_16x16x32_bf16 v[168:171], a[144:147], a[228:231], v[168:171]\n"
    "ds_read_u16 v195, v195\n"
    "v_lshlrev_b32_sdwa v199, 1, v182 dst_sel:DWORD dst_unused:UNUSED_PAD src0_sel:DWORD src1_sel:WORD_1\n"
    "v_mfma_f32_16x16x32_bf16 v[168:171], a[56:59], a[232:235], v[168:171]\n"
    "v_or_b32_e32 v189, v189, v205\n"
    "v_cvt_pk_bf16_f32 v96, v96, v97\n"
    "v_mfma_f32_16x16x32_bf16 v[172:175], a[100:103], a[224:227], 0\n"
    "s_waitcnt lgkmcnt(7)\n"
    "ds_read_u16_d16_hi v160, v160\n"
    "v_cvt_pk_bf16_f32 v97, v98, v99\n"
    "v_mfma_f32_16x16x32_bf16 v[172:175], a[148:151], a[228:231], v[172:175]\n"
    "ds_read_u16 v197, v197\n"
    "v_cvt_pk_bf16_f32 v98, v100, v101\n"
    "v_mfma_f32_16x16x32_bf16 v[172:175], a[60:63], a[232:235], v[172:175]\n"
    "v_cvt_pk_bf16_f32 v99, v102, v103\n"
    "v_or_b32_e32 v190, v190, v206\n"
    "v_mfma_f32_16x16x32_bf16 v[176:179], a[96:99], a[236:239], 0\n"
    "s_waitcnt lgkmcnt(7)\n"
    "ds_read_u16_d16_hi v161, v161\n"
    "v_cvt_pk_bf16_f32 v224, v112, v113\n"
    "v_mfma_f32_16x16x32_bf16 v[176:179], a[144:147], a[240:243], v[176:179]\n"
    "v_cvt_pk_bf16_f32 v225, v114, v115\n"
    "v_cvt_pk_bf16_f32 v226, v116, v117\n"
    "v_mfma_f32_16x16x32_bf16 v[176:179], a[56:59], a[244:247], v[176:179]\n"
    "buffer_load_dwordx4 v[156:159], v245, s[40:43], s89 offen offset:64\n"
    "buffer_load_dwordx4 v[148:151], v245, s[40:43], s89 offen offset:0\n"
    "buffer_load_dwordx4 v[212:215], v245, s[40:43], s89 offen offset:128\n"
    "ds_read_u16 v198, v198\n"
    "v_mfma_f32_16x16x32_bf16 v[180:183], a[100:103], a[236:239], 0\n"
    "s_waitcnt lgkmcnt(7)\n"
    "ds_read_u16_d16_hi v162, v162\n"
    "v_or_b32_e32 v193, v193, v249\n"
    "v_mfma_f32_16x16x32_bf16 v[180:183], a[148:151], a[240:243], v[180:183]\n"
    "v_cvt_pk_bf16_f32 v227, v118, v119\n"
    "v_cvt_pk_bf16_f32 v228, v128, v129\n"
    "v_mfma_f32_16x16x32_bf16 v[180:183], a[60:63], a[244:247], v[180:183]\n"
    "ds_read_u16 v199, v199\n"
    "buffer_store_dwordx4 v[96:99], v245, s[44:47], s57 offen offset:0 sc1\n"
    "v_mfma_f32_16x16x32_bf16 v[100:103], a[4:7], v[184:187], 0\n"
    "s_waitcnt lgkmcnt(7)\n"
    "v_cvt_pk_bf16_f32 v229, v130, v131\n"
    "v_or_b32_e32 v191, v191, v207\n"
    "v_mfma_f32_16x16x32_bf16 v[96:99], a[0:3], v[184:187], 0\n"
    "ds_read_u16_d16_hi v163, v163\n"
    "v_cvt_pk_bf16_f32 v230, v132, v133\n"
    "v_mfma_f32_16x16x32_bf16 v[112:115], a[8:11], v[184:187], 0\n"
    "v_cvt_pk_bf16_f32 v231, v134, v135\n"
    "v_cvt_pk_bf16_f32 v216, v104, v105\n"
    "v_mfma_f32_16x16x32_bf16 v[116:119], a[12:15], v[184:187], 0\n"
    "s_waitcnt lgkmcnt(6)\n"
    "v_cvt_pk_bf16_f32 v217, v106, v107\n"
    "v_or_b32_e32 v194, v194, v255\n"
    "v_mfma_f32_16x16x32_bf16 v[128:131], v[0:3], v[184:187], 0\n"
    "v_cvt_pk_bf16_f32 v218, v108, v109\n"
    "v_cvt_pk_bf16_f32 v219, v110, v111\n"
    "v_mfma_f32_16x16x32_bf16 v[132:135], v[4:7], v[184:187], 0\n"
    "v_cvt_pk_bf16_f32 v220, v120, v121\n"
    "v_cvt_pk_bf16_f32 v221, v122, v123\n"
    "v_mfma_f32_16x16x32_bf16 v[104:107], a[0:3], v[188:191], 0\n"
    "s_nop 0\n"
    "v_cvt_pk_bf16_f32 v222, v124, v125\n"
    "v_cvt_pk_bf16_f32 v223, v126, v127\n"
    "v_mfma_f32_16x16x32_bf16 v[108:111], a[4:7], v[188:191], 0\n"
    "s_waitcnt lgkmcnt(4)\n"
    "v_pk_fma_f16 v232, v192, v192, s48\n"
    "v_cvt_pk_bf16_f32 v64, v136, v137\n"
    "v_mfma_f32_16x16x32_bf16 v[120:123], a[8:11], v[188:191], 0\n"
    "v_cvt_pk_bf16_f32 v65, v138, v139\n"
    "v_cvt_pk_bf16_f32 v66, v140, v141\n"
    "v_mfma_f32_16x16x32_bf16 v[124:127], a[12:15], v[188:191], 0\n"
    "v_or_b32_e64 v195, v195, v160\n"
    "v_cvt_pk_bf16_f32 v67, v142, v143\n"
    "v_pk_mul_f16 v232, v192, v232\n"
    "v_mfma_f32_16x16x32_bf16 v[136:139], v[0:3], v[188:191], 0\n"
    "s_waitcnt lgkmcnt(2)\n"
    "v_exp_f16_e32 v234, v232\n"
    "v_mfma_f32_16x16x32_bf16 v[140:143], v[4:7], v[188:191], 0\n"
    "v_exp_f16_sdwa v234, v232 dst_sel:WORD_1 dst_unused:UNUSED_PRESERVE src0_sel:WORD_1\n"
    "v_or_b32_e64 v197, v197, v161\n"
    "v_pk_fma_f16 v234, v234, s49, s49\n"
    "v_lshlrev_b32_sdwa v184, 1, v168 dst_sel:DWORD dst_unused:UNUSED_PAD src0_sel:DWORD src1_sel:WORD_1\n"
    "v_lshlrev_b32_sdwa v200, 1, v169 dst_sel:DWORD dst_unused:UNUSED_PAD src0_sel:DWORD src1_sel:WORD_1\n"
    "v_lshlrev_b32_sdwa v185, 1, v170 dst_sel:DWORD dst_unused:UNUSED_PAD src0_sel:DWORD src1_sel:WORD_1\n"
    "v_lshlrev_b32_sdwa v201, 1, v171 dst_sel:DWORD dst_unused:UNUSED_PAD src0_sel:DWORD src1_sel:WORD_1\n"
    "v_mfma_f32_16x16x32_bf16 v[168:171], a[104:107], a[224:227], 0\n"
    "s_waitcnt lgkmcnt(0)\n"
    "ds_read_u16 v184, v184\n"
    "ds_read_u16_d16_hi v200, v200\n"
    "v_mfma_f32_16x16x32_bf16 v[168:171], a[152:155], a[228:231], v[168:171]\n"
    "s_nop 0\n"
    "ds_read_u16 v185, v185\n"
    "ds_read_u16_d16_hi v201, v201\n"
    "v_lshlrev_b32_sdwa v186, 1, v172 dst_sel:DWORD dst_unused:UNUSED_PAD src0_sel:DWORD src1_sel:WORD_1\n"
    "v_lshlrev_b32_sdwa v202, 1, v173 dst_sel:DWORD dst_unused:UNUSED_PAD src0_sel:DWORD src1_sel:WORD_1\n"
    "v_lshlrev_b32_sdwa v187, 1, v174 dst_sel:DWORD dst_unused:UNUSED_PAD src0_sel:DWORD src1_sel:WORD_1\n"
    "v_lshlrev_b32_sdwa v203, 1, v175 dst_sel:DWORD dst_unused:UNUSED_PAD src0_sel:DWORD src1_sel:WORD_1\n"
    "v_rcp_f16_e64 v232, v234\n"
    "v_mfma_f32_16x16x32_bf16 v[168:171], a[64:67], a[232:235], v[168:171]\n"
    "ds_read_u16 v186, v186\n"
    "v_rcp_f16_sdwa v232, v234 dst_sel:WORD_1 dst_unused:UNUSED_PRESERVE src0_sel:WORD_1\n"
    "v_mfma_f32_16x16x32_bf16 v[172:175], a[108:111], a[224:227], 0\n"
    "ds_read_u16_d16_hi v202, v202\n"
    "v_lshlrev_b32_sdwa v188, 1, v176 dst_sel:DWORD dst_unused:UNUSED_PAD src0_sel:DWORD src1_sel:WORD_1\n"
    "v_lshlrev_b32_sdwa v204, 1, v177 dst_sel:DWORD dst_unused:UNUSED_PAD src0_sel:DWORD src1_sel:WORD_1\n"
    "v_lshlrev_b32_sdwa v189, 1, v178 dst_sel:DWORD dst_unused:UNUSED_PAD src0_sel:DWORD src1_sel:WORD_1\n"
    "v_lshlrev_b32_sdwa v205, 1, v179 dst_sel:DWORD dst_unused:UNUSED_PAD src0_sel:DWORD src1_sel:WORD_1\n"
    "v_lshlrev_b32_sdwa v190, 1, v180 dst_sel:DWORD dst_unused:UNUSED_PAD src0_sel:DWORD src1_sel:WORD_1\n"
    "v_lshlrev_b32_sdwa v206, 1, v181 dst_sel:DWORD dst_unused:UNUSED_PAD src0_sel:DWORD src1_sel:WORD_1\n"
    "v_lshlrev_b32_sdwa v191, 1, v182 dst_sel:DWORD dst_unused:UNUSED_PAD src0_sel:DWORD src1_sel:WORD_1\n"
    "v_lshlrev_b32_sdwa v207, 1, v183 dst_sel:DWORD dst_unused:UNUSED_PAD src0_sel:DWORD src1_sel:WORD_1\n"
    "v_fma_mix_f32 v234, v192, v232, 0 op_sel_hi:[1,1,0]\n"
    "v_mfma_f32_16x16x32_bf16 v[172:175], a[156:159], a[228:231], v[172:175]\n"
    "ds_read_u16 v187, v187\n"
    "v_or_b32_e64 v198, v198, v162\n"
    "v_mfma_f32_16x16x32_bf16 v[172:175], a[68:71], a[232:235], v[172:175]\n"
    "ds_read_u16_d16_hi v203, v203\n"
    "v_fma_mix_f32 v232, v192, v232, 0 op_sel:[1,1,0] op_sel_hi:[1,1,0]\n"
    "v_mfma_f32_16x16x32_bf16 v[176:179], a[104:107], a[236:239], 0\n"
    "v_cvt_pk_bf16_f32 v192, v234, v232\n"
    "v_pk_fma_f16 v232, v196, v196, s48\n"
    "v_mfma_f32_16x16x32_bf16 v[176:179], a[152:155], a[240:243], v[176:179]\n"
    "ds_read_u16 v188, v188\n"
    "v_or_b32_e32 v199, v199, v163\n"
    "v_mfma_f32_16x16x32_bf16 v[176:179], a[64:67], a[244:247], v[176:179]\n"
    "s_waitcnt lgkmcnt(7)\n"
    "ds_read_u16_d16_hi v204, v204\n"
    "v_pk_mul_f16 v232, v196, v232\n"
    "v_mfma_f32_16x16x32_bf16 v[180:183], a[108:111], a[236:239], 0\n"
    "v_exp_f16_e64 v234, v232\n"
    "v_mfma_f32_16x16x32_bf16 v[180:183], a[156:159], a[240:243], v[180:183]\n"
    "ds_read_u16 v189, v189\n"
    "v_or_b32_e64 v184, v184, v200\n"
    "v_mfma_f32_16x16x32_bf16 v[180:183], a[68:71], a[244:247], v[180:183]\n"
    "v_exp_f16_sdwa v234, v232 dst_sel:WORD_1 dst_unused:UNUSED_PRESERVE src0_sel:WORD_1\n"
    "s_waitcnt lgkmcnt(7)\n"
    "v_or_b32_e32 v185, v185, v201\n"
    "v_pk_fma_f16 v234, v234, s49, s49\n"
    "v_mfma_f32_16x16x32_bf16 v[96:99], a[16:19], v[192:195], v[96:99]\n"
    "ds_read_u16_d16_hi v205, v205\n"
    "v_rcp_f16_e32 v232, v234\n"
    "v_mfma_f32_16x16x32_bf16 v[100:103], a[20:23], v[192:195], v[100:103]\n"
    "ds_read_u16 v190, v190\n"
    "v_rcp_f16_sdwa v232, v234 dst_sel:WORD_1 dst_unused:UNUSED_PRESERVE src0_sel:WORD_1\n"
    "v_mfma_f32_16x16x32_bf16 v[112:115], a[24:27], v[192:195], v[112:115]\n"
    "s_waitcnt lgkmcnt(7)\n"
    "ds_read_u16_d16_hi v206, v206\n"
    "v_fma_mix_f32 v234, v196, v232, 0 op_sel_hi:[1,1,0]\n"
    "v_mfma_f32_16x16x32_bf16 v[116:119], a[28:31], v[192:195], v[116:119]\n"
    "buffer_store_dwordx4 v[224:227], v245, s[44:47], s57 offen offset:64 sc1\n"
    "ds_read_u16 v191, v191\n"
    "v_mfma_f32_16x16x32_bf16 v[128:131], v[8:11], v[192:195], v[128:131]\n"
    "s_nop 0\n"
    "v_fma_mix_f32 v232, v196, v232, 0 op_sel:[1,1,0] op_sel_hi:[1,1,0]\n"
    "v_lshlrev_b32_sdwa v249, 1, v171 dst_sel:DWORD dst_unused:UNUSED_PAD src0_sel:DWORD src1_sel:WORD_1\n"
    "v_mfma_f32_16x16x32_bf16 v[132:135], v[12:15], v[192:195], v[132:135]\n"
    "s_waitcnt lgkmcnt(7)\n"
    "ds_read_u16_d16_hi v207, v207\n"
    "v_cvt_pk_bf16_f32 v196, v234, v232\n"
    "v_cvt_pk_f16_f32 v192, v168, v169\n"
    "v_lshlrev_b32_sdwa v193, 1, v170 dst_sel:DWORD dst_unused:UNUSED_PAD src0_sel:DWORD src1_sel:WORD_1\n"
    "v_mfma_f32_16x16x32_bf16 v[104:107], a[16:19], v[196:199], v[104:107]\n"
    "ds_read_u16 v193, v193\n"
    "v_lshlrev_b32_sdwa v194, 1, v172 dst_sel:DWORD dst_unused:UNUSED_PAD src0_sel:DWORD src1_sel:WORD_1\n"
    "v_mfma_f32_16x16x32_bf16 v[108:111], a[20:23], v[196:199], v[108:111]\n"
    "v_or_b32_e32 v186, v186, v202\n"
    "v_lshlrev_b32_sdwa v255, 1, v173 dst_sel:DWORD dst_unused:UNUSED_PAD src0_sel:DWORD src1_sel:WORD_1\n"
    "v_mfma_f32_16x16x32_bf16 v[120:123], a[24:27], v[196:199], v[120:123]\n"
    "s_waitcnt lgkmcnt(7)\n"
    "ds_read_u16_d16_hi v249, v249\n"
    "v_lshlrev_b32_sdwa v195, 1, v174 dst_sel:DWORD dst_unused:UNUSED_PAD src0_sel:DWORD src1_sel:WORD_1\n"
    "v_mfma_f32_16x16x32_bf16 v[124:127], a[28:31], v[196:199], v[124:127]\n"
    "ds_read_u16 v194, v194\n"
    "v_lshlrev_b32_sdwa v160, 1, v175 dst_sel:DWORD dst_unused:UNUSED_PAD src0_sel:DWORD src1_sel:WORD_1\n"
    "v_mfma_f32_16x16x32_bf16 v[136:139], v[8:11], v[196:199], v[136:139]\n"
    "s_nop 0\n"
    "v_lshlrev_b32_sdwa v161, 1, v179 dst_sel:DWORD dst_unused:UNUSED_PAD src0_sel:DWORD src1_sel:WORD_1\n"
    "v_lshlrev_b32_sdwa v162, 1, v181 dst_sel:DWORD dst_unused:UNUSED_PAD src0_sel:DWORD src1_sel:WORD_1\n"
    "v_mfma_f32_16x16x32_bf16 v[140:143], v[12:15], v[196:199], v[140:143]\n"
    "s_waitcnt lgkmcnt(7)\n"
    "ds_read_u16_d16_hi v255, v255\n"
    "v_cvt_pk_f16_f32 v196, v176, v177\n"
    "v_mfma_f32_16x16x32_bf16 v[168:171], a[112:115], a[224:227], 0\n"
    "v_lshlrev_b32_sdwa v197, 1, v178 dst_sel:DWORD dst_unused:UNUSED_PAD src0_sel:DWORD src1_sel:WORD_1\n"
    "v_lshlrev_b32_sdwa v198, 1, v180 dst_sel:DWORD dst_unused:UNUSED_PAD src0_sel:DWORD src1_sel:WORD_1\n"
    "v_mfma_f32_16x16x32_bf16 v[168:171], a[160:163], a[228:231], v[168:171]\n"
    "ds_read_u16 v195, v195\n"
    "v_lshlrev_b32_sdwa v199, 1, v182 dst_sel:DWORD dst_unused:UNUSED_PAD src0_sel:DWORD src1_sel:WORD_1\n"
    "v_mfma_f32_16x16x32_bf16 v[168:171], a[72:75], a[232:235], v[168:171]\n"
    "v_or_b32_e64 v187, v187, v203\n"
    "v_lshlrev_b32_sdwa v163, 1, v183 dst_sel:DWORD dst_unused:UNUSED_PAD src0_sel:DWORD src1_sel:WORD_1\n"
    "v_mfma_f32_16x16x32_bf16 v[172:175], a[116:119], a[224:227], 0\n"
    "s_waitcnt lgkmcnt(7)\n"
    "ds_read_u16_d16_hi v160, v160\n"
    "v_or_b32_e32 v188, v188, v204\n"
    "v_mfma_f32_16x16x32_bf16 v[172:175], a[164:167], a[228:231], v[172:175]\n"
    "ds_read_u16 v197, v197\n"
    "v_or_b32_e64 v189, v189, v205\n"
    "v_mfma_f32_16x16x32_bf16 v[172:175], v[52:55], a[232:235], v[172:175]\n"
    "v_or_b32_e64 v190, v190, v206\n"
    "v_pk_fma_f16 v232, v192, v192, s48\n"
    "v_mfma_f32_16x16x32_bf16 v[176:179], a[112:115], a[236:239], 0\n"
    "s_waitcnt lgkmcnt(7)\n"
    "ds_read_u16_d16_hi v161, v161\n"
    "v_or_b32_e32 v191, v191, v207\n"
    "v_mfma_f32_16x16x32_bf16 v[176:179], a[160:163], a[240:243], v[176:179]\n"
    "v_pk_mul_f16 v232, v192, v232\n"
    "v_lshlrev_b32_sdwa v200, 1, v169 dst_sel:DWORD dst_unused:UNUSED_PAD src0_sel:DWORD src1_sel:WORD_1\n"
    "v_mfma_f32_16x16x32_bf16 v[176:179], a[72:75], a[244:247], v[176:179]\n"
    "ds_read_u16 v198, v198\n"
    "buffer_store_dwordx4 v[228:231], v245, s[44:47], s57 offen offset:128 sc1\n"
    "v_mfma_f32_16x16x32_bf16 v[180:183], a[116:119], a[236:239], 0\n"
    "s_waitcnt lgkmcnt(7)\n"
    "ds_read_u16_d16_hi v162, v162\n"
    "v_or_b32_e32 v193, v193, v249\n"
    "v_mfma_f32_16x16x32_bf16 v[180:183], a[164:167], a[240:243], v[180:183]\n"
    "ds_read_u16 v199, v199\n"
    "v_lshlrev_b32_sdwa v201, 1, v171 dst_sel:DWORD dst_unused:UNUSED_PAD src0_sel:DWORD src1_sel:WORD_1\n"
    "v_mfma_f32_16x16x32_bf16 v[180:183], v[52:55], a[244:247], v[180:183]\n"
    "v_exp_f16_e32 v234, v232\n"
    "v_mfma_f32_16x16x32_bf16 v[96:99], a[32:35], v[184:187], v[96:99]\n"
    "s_waitcnt lgkmcnt(7)\n"
    "ds_read_u16_d16_hi v163, v163\n"
    "v_or_b32_e64 v194, v194, v255\n"
    "v_mfma_f32_16x16x32_bf16 v[100:103], a[36:39], v[184:187], v[100:103]\n"
    "v_exp_f16_sdwa v234, v232 dst_sel:WORD_1 dst_unused:UNUSED_PRESERVE src0_sel:WORD_1\n"
    "v_mfma_f32_16x16x32_bf16 v[112:115], a[40:43], v[184:187], v[112:115]\n"
    "ds_read_u16_d16_hi v200, v200\n"
    "v_pk_fma_f16 v234, v234, s49, s49\n"
    "v_mfma_f32_16x16x32_bf16 v[116:119], a[44:47], v[184:187], v[116:119]\n"
    "v_lshlrev_b32_sdwa v202, 1, v173 dst_sel:DWORD dst_unused:UNUSED_PAD src0_sel:DWORD src1_sel:WORD_1\n"
    "v_lshlrev_b32_sdwa v203, 1, v175 dst_sel:DWORD dst_unused:UNUSED_PAD src0_sel:DWORD src1_sel:WORD_1\n"
    "v_mfma_f32_16x16x32_bf16 v[128:131], v[16:19], v[184:187], v[128:131]\n"
    "s_waitcnt lgkmcnt(7)\n"
    "ds_read_u16_d16_hi v201, v201\n"
    "v_or_b32_e32 v195, v195, v160\n"
    "v_mfma_f32_16x16x32_bf16 v[132:135], v[20:23], v[184:187], v[132:135]\n"
    "ds_read_u16_d16_hi v202, v202\n"
    "v_lshlrev_b32_sdwa v184, 1, v168 dst_sel:DWORD dst_unused:UNUSED_PAD src0_sel:DWORD src1_sel:WORD_1\n"
    "v_mfma_f32_16x16x32_bf16 v[104:107], a[32:35], v[188:191], v[104:107]\n"
    "v_lshlrev_b32_sdwa v185, 1, v170 dst_sel:DWORD dst_unused:UNUSED_PAD src0_sel:DWORD src1_sel:WORD_1\n"
    "v_lshlrev_b32_sdwa v186, 1, v172 dst_sel:DWORD dst_unused:UNUSED_PAD src0_sel:DWORD src1_sel:WORD_1\n"
    "v_mfma_f32_16x16x32_bf16 v[108:111], a[36:39], v[188:191], v[108:111]\n"
    "s_waitcnt lgkmcnt(7)\n"
    "ds_read_u16 v184, v184\n"
    "v_or_b32_e32 v197, v197, v161\n"
    "v_mfma_f32_16x16x32_bf16 v[120:123], a[40:43], v[188:191], v[120:123]\n"
    "v_lshlrev_b32_sdwa v187, 1, v174 dst_sel:DWORD dst_unused:UNUSED_PAD src0_sel:DWORD src1_sel:WORD_1\n"
    "v_rcp_f16_e32 v232, v234\n"
    "v_mfma_f32_16x16x32_bf16 v[124:127], a[44:47], v[188:191], v[124:127]\n"
    "ds_read_u16 v185, v185\n"
    "v_rcp_f16_sdwa v232, v234 dst_sel:WORD_1 dst_unused:UNUSED_PRESERVE src0_sel:WORD_1\n"
    "v_mfma_f32_16x16x32_bf16 v[136:139], v[16:19], v[188:191], v[136:139]\n"
    "s_waitcnt lgkmcnt(7)\n"
    "ds_read_u16 v186, v186\n"
    "v_lshlrev_b32_sdwa v204, 1, v177 dst_sel:DWORD dst_unused:UNUSED_PAD src0_sel:DWORD src1_sel:WORD_1\n"
    "v_mfma_f32_16x16x32_bf16 v[140:143], v[20:23], v[188:191], v[140:143]\n"
    "v_lshlrev_b32_sdwa v188, 1, v176 dst_sel:DWORD dst_unused:UNUSED_PAD src0_sel:DWORD src1_sel:WORD_1\n"
    "v_lshlrev_b32_sdwa v189, 1, v178 dst_sel:DWORD dst_unused:UNUSED_PAD src0_sel:DWORD src1_sel:WORD_1\n"
    "v_mfma_f32_16x16x32_bf16 v[168:171], a[120:123], a[224:227], 0\n"
    "ds_read_u16 v187, v187\n"
    "v_lshlrev_b32_sdwa v205, 1, v179 dst_sel:DWORD dst_unused:UNUSED_PAD src0_sel:DWORD src1_sel:WORD_1\n"
    "v_mfma_f32_16x16x32_bf16 v[168:171], a[168:171], a[228:231], v[168:171]\n"
    "v_lshlrev_b32_sdwa v190, 1, v180 dst_sel:DWORD dst_unused:UNUSED_PAD src0_sel:DWORD src1_sel:WORD_1\n"
    "v_lshlrev_b32_sdwa v206, 1, v181 dst_sel:DWORD dst_unused:UNUSED_PAD src0_sel:DWORD src1_sel:WORD_1\n"
    "v_mfma_f32_16x16x32_bf16 v[168:171], a[76:79], a[232:235], v[168:171]\n"
    "s_waitcnt lgkmcnt(7)\n"
    "ds_read_u16_d16_hi v203, v203\n"
    "buffer_store_dwordx4 v[216:219], v245, s[44:47], s88 offen offset:0 sc1\n"
    "v_mfma_f32_16x16x32_bf16 v[172:175], a[124:127], a[224:227], 0\n"
    "s_nop 0\n"
    "v_lshlrev_b32_sdwa v191, 1, v182 dst_sel:DWORD dst_unused:UNUSED_PAD src0_sel:DWORD src1_sel:WORD_1\n"
    "v_lshlrev_b32_sdwa v207, 1, v183 dst_sel:DWORD dst_unused:UNUSED_PAD src0_sel:DWORD src1_sel:WORD_1\n"
    "v_mfma_f32_16x16x32_bf16 v[172:175], a[172:175], a[228:231], v[172:175]\n"
    "v_fma_mix_f32 v234, v192, v232, 0 op_sel_hi:[1,1,0]\n"
    "v_or_b32_e64 v198, v198, v162\n"
    "v_mfma_f32_16x16x32_bf16 v[172:175], v[60:63], a[232:235], v[172:175]\n"
    "ds_read_u16 v188, v188\n"
    "v_fma_mix_f32 v232, v192, v232, 0 op_sel:[1,1,0] op_sel_hi:[1,1,0]\n"
    "v_mfma_f32_16x16x32_bf16 v[176:179], a[120:123], a[236:239], 0\n"
    "v_cvt_pk_bf16_f32 v192, v234, v232\n"
    "v_pk_fma_f16 v232, v196, v196, s48\n"
    "v_mfma_f32_16x16x32_bf16 v[176:179], a[168:171], a[240:243], v[176:179]\n"
    "ds_read_u16_d16_hi v204, v204\n"
    "ds_read_b128 a[224:227], v243 offset:0\n"
    "v_mfma_f32_16x16x32_bf16 v[176:179], a[76:79], a[244:247], v[176:179]\n"
    "s_waitcnt lgkmcnt(7)\n"
    "ds_read_u16 v189, v189\n"
    "ds_read_b128 a[228:231], v243 offset:1024\n"
    "v_mfma_f32_16x16x32_bf16 v[180:183], a[124:127], a[236:239], 0\n"
    "s_waitcnt lgkmcnt(8)\n"
    "v_or_b32_e64 v199, v199, v163\n"
    "v_pk_mul_f16 v232, v196, v232\n"
    "v_mfma_f32_16x16x32_bf16 v[180:183], a[172:175], a[240:243], v[180:183]\n"
    "ds_read_u16_d16_hi v205, v205\n"
    "v_exp_f16_e32 v234, v232\n"
    "v_mfma_f32_16x16x32_bf16 v[180:183], v[60:63], a[244:247], v[180:183]\n"
    "s_waitcnt lgkmcnt(8)\n"
    "ds_read_b128 a[232:235], v243 offset:2048\n"
    "v_exp_f16_sdwa v234, v232 dst_sel:WORD_1 dst_unused:UNUSED_PRESERVE src0_sel:WORD_1\n"
    "v_mfma_f32_16x16x32_bf16 v[96:99], a[176:179], v[192:195], v[96:99]\n"
    "ds_read_u16 v190, v190\n"
    "v_pk_fma_f16 v234, v234, s49, s49\n"
    "v_mfma_f32_16x16x32_bf16 v[100:103], a[180:183], v[192:195], v[100:103]\n"
    "v_rcp_f16_e32 v232, v234\n"
    "v_mfma_f32_16x16x32_bf16 v[112:115], a[184:187], v[192:195], v[112:115]\n"
    "s_waitcnt lgkmcnt(8)\n"
    "ds_read_b128 a[236:239], v243 offset:3072\n"
    "ds_read_b128 a[240:243], v243 offset:4096\n"
    "v_rcp_f16_sdwa v232, v234 dst_sel:WORD_1 dst_unused:UNUSED_PRESERVE src0_sel:WORD_1\n"
    "v_mfma_f32_16x16x32_bf16 v[116:119], a[188:191], v[192:195], v[116:119]\n"
    "ds_read_u16_d16_hi v206, v206\n"
    "v_fma_mix_f32 v234, v196, v232, 0 op_sel_hi:[1,1,0]\n"
    "v_mfma_f32_16x16x32_bf16 v[128:131], v[24:27], v[192:195], v[128:131]\n"
    "v_fma_mix_f32 v232, v196, v232, 0 op_sel:[1,1,0] op_sel_hi:[1,1,0]\n"
    "v_or_b32_e32 v184, v184, v200\n"
    "v_mfma_f32_16x16x32_bf16 v[132:135], v[28:31], v[192:195], v[132:135]\n"
    "s_waitcnt lgkmcnt(8)\n"
    "ds_read_b128 a[244:247], v243 offset:5120\n"
    "buffer_store_dwordx4 v[220:223], v245, s[44:47], s88 offen offset:64 sc1\n"
    "v_cvt_pk_bf16_f32 v196, v234, v232\n"
    "v_or_b32_e64 v185, v185, v201\n"
    "v_cvt_pk_f16_f32 v192, v168, v169\n"
    "v_mfma_f32_16x16x32_bf16 v[104:107], a[176:179], v[196:199], v[104:107]\n"
    "ds_read_u16 v191, v191\n"
    "ds_read_u16_d16_hi v207, v207\n"
    "v_mfma_f32_16x16x32_bf16 v[108:111], a[180:183], v[196:199], v[108:111]\n"
    "v_lshlrev_b32_sdwa v193, 1, v170 dst_sel:DWORD dst_unused:UNUSED_PAD src0_sel:DWORD src1_sel:WORD_1\n"
    "v_lshlrev_b32_sdwa v249, 1, v171 dst_sel:DWORD dst_unused:UNUSED_PAD src0_sel:DWORD src1_sel:WORD_1\n"
    "v_mfma_f32_16x16x32_bf16 v[120:123], a[184:187], v[196:199], v[120:123]\n"
    "ds_read_u16 v193, v193\n"
    "v_lshlrev_b32_sdwa v194, 1, v172 dst_sel:DWORD dst_unused:UNUSED_PAD src0_sel:DWORD src1_sel:WORD_1\n"
    "v_mfma_f32_16x16x32_bf16 v[124:127], a[188:191], v[196:199], v[124:127]\n"
    "s_waitcnt lgkmcnt(8)\n"
    "ds_read_u16_d16_hi v249, v249\n"
    "v_or_b32_e32 v186, v186, v202\n"
    "v_mfma_f32_16x16x32_bf16 v[136:139], v[24:27], v[196:199], v[136:139]\n"
    "v_lshlrev_b32_sdwa v255, 1, v173 dst_sel:DWORD dst_unused:UNUSED_PAD src0_sel:DWORD src1_sel:WORD_1\n"
    "v_lshlrev_b32_sdwa v195, 1, v174 dst_sel:DWORD dst_unused:UNUSED_PAD src0_sel:DWORD src1_sel:WORD_1\n"
    "v_mfma_f32_16x16x32_bf16 v[140:143], v[28:31], v[196:199], v[140:143]\n"
    "ds_read_u16 v194, v194\n"
    "v_lshlrev_b32_sdwa v160, 1, v175 dst_sel:DWORD dst_unused:UNUSED_PAD src0_sel:DWORD src1_sel:WORD_1\n"
    "v_mfma_f32_16x16x32_bf16 v[168:171], a[80:83], a[224:227], 0\n"
    "v_cvt_pk_f16_f32 v196, v176, v177\n"
    "v_lshlrev_b32_sdwa v197, 1, v178 dst_sel:DWORD dst_unused:UNUSED_PAD src0_sel:DWORD src1_sel:WORD_1\n"
    "v_mfma_f32_16x16x32_bf16 v[168:171], a[128:131], a[228:231], v[168:171]\n"
    "ds_read_u16_d16_hi v255, v255\n"
    "v_lshlrev_b32_sdwa v161, 1, v179 dst_sel:DWORD dst_unused:UNUSED_PAD src0_sel:DWORD src1_sel:WORD_1\n"
    "v_mfma_f32_16x16x32_bf16 v[168:171], a[48:51], a[232:235], v[168:171]\n"
    "v_lshlrev_b32_sdwa v198, 1, v180 dst_sel:DWORD dst_unused:UNUSED_PAD src0_sel:DWORD src1_sel:WORD_1\n"
    "v_lshlrev_b32_sdwa v162, 1, v181 dst_sel:DWORD dst_unused:UNUSED_PAD src0_sel:DWORD src1_sel:WORD_1\n"
    "v_mfma_f32_16x16x32_bf16 v[172:175], a[84:87], a[224:227], 0\n"
    "s_waitcnt lgkmcnt(6)\n"
    "ds_read_u16 v195, v195\n"
    "v_lshlrev_b32_sdwa v199, 1, v182 dst_sel:DWORD dst_unused:UNUSED_PAD src0_sel:DWORD src1_sel:WORD_1\n"
    "v_or_b32_e32 v187, v187, v203\n"
    "v_mfma_f32_16x16x32_bf16 v[172:175], a[132:135], a[228:231], v[172:175]\n"
    "ds_read_u16_d16_hi v160, v160\n"
    "v_lshlrev_b32_sdwa v163, 1, v183 dst_sel:DWORD dst_unused:UNUSED_PAD src0_sel:DWORD src1_sel:WORD_1\n"
    "v_mfma_f32_16x16x32_bf16 v[172:175], v[48:51], a[232:235], v[172:175]\n"
    "v_or_b32_e64 v188, v188, v204\n"
    "v_or_b32_e64 v189, v189, v205\n"
    "v_mfma_f32_16x16x32_bf16 v[176:179], a[80:83], a[236:239], 0\n"
    "ds_read_u16 v197, v197\n"
    "v_or_b32_e64 v190, v190, v206\n"
    "v_mfma_f32_16x16x32_bf16 v[176:179], a[128:131], a[240:243], v[176:179]\n"
    "v_pk_fma_f16 v232, v192, v192, s48\n"
    "v_lshlrev_b32_sdwa v200, 1, v169 dst_sel:DWORD dst_unused:UNUSED_PAD src0_sel:DWORD src1_sel:WORD_1\n"
    "v_mfma_f32_16x16x32_bf16 v[176:179], a[48:51], a[244:247], v[176:179]\n"
    "s_waitcnt lgkmcnt(7)\n"
    "ds_read_u16_d16_hi v161, v161\n"
    "v_or_b32_e32 v191, v191, v207\n"
    "v_mfma_f32_16x16x32_bf16 v[180:183], a[84:87], a[236:239], 0\n"
    "ds_read_u16 v198, v198\n"
    "v_pk_mul_f16 v232, v192, v232\n"
    "v_mfma_f32_16x16x32_bf16 v[180:183], a[132:135], a[240:243], v[180:183]\n"
    "v_exp_f16_e32 v234, v232\n"
    "v_mfma_f32_16x16x32_bf16 v[180:183], v[48:51], a[244:247], v[180:183]\n"
    "s_waitcnt lgkmcnt(7)\n"
    "ds_read_u16_d16_hi v162, v162\n"
    "buffer_store_dwordx4 v[64:67], v245, s[44:47], s88 offen offset:128 sc1\n"
    "v_mfma_f32_16x16x32_bf16 v[96:99], a[192:195], v[184:187], v[96:99]\n"
    "ds_read_u16 v199, v199\n"
    "v_or_b32_e32 v193, v193, v249\n"
    "v_mfma_f32_16x16x32_bf16 v[100:103], a[196:199], v[184:187], v[100:103]\n"
    "v_exp_f16_sdwa v234, v232 dst_sel:WORD_1 dst_unused:UNUSED_PRESERVE src0_sel:WORD_1\n"
    "v_mfma_f32_16x16x32_bf16 v[112:115], a[200:203], v[184:187], v[112:115]\n"
    "s_waitcnt lgkmcnt(7)\n"
    "ds_read_u16_d16_hi v163, v163\n"
    "v_or_b32_e64 v194, v194, v255\n"
    "v_mfma_f32_16x16x32_bf16 v[116:119], a[204:207], v[184:187], v[116:119]\n"
    "ds_read_u16_d16_hi v200, v200\n"
    "v_pk_fma_f16 v234, v234, s49, s49\n"
    "v_mfma_f32_16x16x32_bf16 v[128:131], v[32:35], v[184:187], v[128:131]\n"
    "v_lshlrev_b32_sdwa v201, 1, v171 dst_sel:DWORD dst_unused:UNUSED_PAD src0_sel:DWORD src1_sel:WORD_1\n"
    "v_lshlrev_b32_sdwa v202, 1, v173 dst_sel:DWORD dst_unused:UNUSED_PAD src0_sel:DWORD src1_sel:WORD_1\n"
    "v_mfma_f32_16x16x32_bf16 v[132:135], v[36:39], v[184:187], v[132:135]\n"
    "s_waitcnt lgkmcnt(7)\n"
    "ds_read_u16_d16_hi v201, v201\n"
    "v_or_b32_e32 v195, v195, v160\n"
    "v_mfma_f32_16x16x32_bf16 v[104:107], a[192:195], v[188:191], v[104:107]\n"
    "v_lshlrev_b32_sdwa v184, 1, v168 dst_sel:DWORD dst_unused:UNUSED_PAD src0_sel:DWORD src1_sel:WORD_1\n"
    "v_lshlrev_b32_sdwa v185, 1, v170 dst_sel:DWORD dst_unused:UNUSED_PAD src0_sel:DWORD src1_sel:WORD_1\n"
    "v_mfma_f32_16x16x32_bf16 v[108:111], a[196:199], v[188:191], v[108:111]\n"
    "ds_read_u16 v184, v184\n"
    "v_lshlrev_b32_sdwa v186, 1, v172 dst_sel:DWORD dst_unused:UNUSED_PAD src0_sel:DWORD src1_sel:WORD_1\n"
    "v_mfma_f32_16x16x32_bf16 v[120:123], a[200:203], v[188:191], v[120:123]\n"
    "v_lshlrev_b32_sdwa v187, 1, v174 dst_sel:DWORD dst_unused:UNUSED_PAD src0_sel:DWORD src1_sel:WORD_1\n"
    "v_lshlrev_b32_sdwa v203, 1, v175 dst_sel:DWORD dst_unused:UNUSED_PAD src0_sel:DWORD src1_sel:WORD_1\n"
    "v_mfma_f32_16x16x32_bf16 v[124:127], a[204:207], v[188:191], v[124:127]\n"
    "s_waitcnt lgkmcnt(7)\n"
    "ds_read_u16 v185, v185\n"
    "v_or_b32_e32 v197, v197, v161\n"
    "v_mfma_f32_16x16x32_bf16 v[136:139], v[32:35], v[188:191], v[136:139]\n"
    "ds_read_u16 v186, v186\n"
    "v_rcp_f16_e32 v232, v234\n"
    "v_mfma_f32_16x16x32_bf16 v[140:143], v[36:39], v[188:191], v[140:143]\n"
    "v_rcp_f16_sdwa v232, v234 dst_sel:WORD_1 dst_unused:UNUSED_PRESERVE src0_sel:WORD_1\n"
    "v_mfma_f32_16x16x32_bf16 v[168:171], a[88:91], a[224:227], 0\n"
    "s_waitcnt lgkmcnt(7)\n"
    "ds_read_u16_d16_hi v202, v202\n"
    "v_lshlrev_b32_sdwa v188, 1, v176 dst_sel:DWORD dst_unused:UNUSED_PAD src0_sel:DWORD src1_sel:WORD_1\n"
    "v_mfma_f32_16x16x32_bf16 v[168:171], a[136:139], a[228:231], v[168:171]\n"
    "v_lshlrev_b32_sdwa v204, 1, v177 dst_sel:DWORD dst_unused:UNUSED_PAD src0_sel:DWORD src1_sel:WORD_1\n"
    "v_lshlrev_b32_sdwa v189, 1, v178 dst_sel:DWORD dst_unused:UNUSED_PAD src0_sel:DWORD src1_sel:WORD_1\n"
    "v_mfma_f32_16x16x32_bf16 v[168:171], a[52:55], a[232:235], v[168:171]\n"
    "s_nop 0\n"
    "ds_read_u16 v187, v187\n"
    "v_lshlrev_b32_sdwa v205, 1, v179 dst_sel:DWORD dst_unused:UNUSED_PAD src0_sel:DWORD src1_sel:WORD_1\n"
    "v_mfma_f32_16x16x32_bf16 v[172:175], a[92:95], a[224:227], 0\n"
    "s_waitcnt lgkmcnt(7)\n"
    "ds_read_u16_d16_hi v203, v203\n"
    "v_lshlrev_b32_sdwa v190, 1, v180 dst_sel:DWORD dst_unused:UNUSED_PAD src0_sel:DWORD src1_sel:WORD_1\n"
    "v_mfma_f32_16x16x32_bf16 v[172:175], a[140:143], a[228:231], v[172:175]\n"
    "v_lshlrev_b32_sdwa v206, 1, v181 dst_sel:DWORD dst_unused:UNUSED_PAD src0_sel:DWORD src1_sel:WORD_1\n"
    "v_lshlrev_b32_sdwa v191, 1, v182 dst_sel:DWORD dst_unused:UNUSED_PAD src0_sel:DWORD src1_sel:WORD_1\n"
    "v_lshlrev_b32_sdwa v207, 1, v183 dst_sel:DWORD dst_unused:UNUSED_PAD src0_sel:DWORD src1_sel:WORD_1\n"
    "v_fma_mix_f32 v234, v192, v232, 0 op_sel_hi:[1,1,0]\n"
    "v_mfma_f32_16x16x32_bf16 v[172:175], v[56:59], a[232:235], v[172:175]\n"
    "ds_read_u16 v188, v188\n"
    "v_or_b32_e64 v198, v198, v162\n"
    "v_fma_mix_f32 v232, v192, v232, 0 op_sel:[1,1,0] op_sel_hi:[1,1,0]\n"
    "v_mfma_f32_16x16x32_bf16 v[176:179], a[88:91], a[236:239], 0\n"
    "v_cvt_pk_bf16_f32 v192, v234, v232\n"
    "v_pk_fma_f16 v232, v196, v196, s48\n"
    "v_mfma_f32_16x16x32_bf16 v[176:179], a[136:139], a[240:243], v[176:179]\n"
    "ds_read_u16_d16_hi v204, v204\n"
    "v_or_b32_e32 v199, v199, v163\n"
    "v_mfma_f32_16x16x32_bf16 v[176:179], a[52:55], a[244:247], v[176:179]\n"
    "s_waitcnt lgkmcnt(7)\n"
    "ds_read_u16 v189, v189\n"
    "v_pk_mul_f16 v232, v196, v232\n"
    "v_mfma_f32_16x16x32_bf16 v[180:183], a[92:95], a[236:239], 0\n"
    "v_exp_f16_e32 v234, v232\n"
    "v_mfma_f32_16x16x32_bf16 v[180:183], a[140:143], a[240:243], v[180:183]\n"
    "s_waitcnt lgkmcnt(7)\n"
    "ds_read_u16_d16_hi v205, v205\n"
    "v_or_b32_e64 v184, v184, v200\n"
    "v_mfma_f32_16x16x32_bf16 v[180:183], v[56:59], a[244:247], v[180:183]\n"
    "ds_read_u16 v190, v190\n"
    "v_exp_f16_sdwa v234, v232 dst_sel:WORD_1 dst_unused:UNUSED_PRESERVE src0_sel:WORD_1\n"
    "v_mfma_f32_16x16x32_bf16 v[96:99], a[208:211], v[192:195], v[96:99]\n"
    "v_pk_fma_f16 v234, v234, s49, s49\n"
    "v_or_b32_e32 v185, v185, v201\n"
    "v_mfma_f32_16x16x32_bf16 v[100:103], a[212:215], v[192:195], v[100:103]\n"
    "s_waitcnt lgkmcnt(7)\n"
    "ds_read_u16_d16_hi v206, v206\n"
    "v_rcp_f16_e64 v232, v234\n"
    "v_mfma_f32_16x16x32_bf16 v[112:115], a[216:219], v[192:195], v[112:115]\n"
    "ds_read_u16 v191, v191\n"
    "v_rcp_f16_sdwa v232, v234 dst_sel:WORD_1 dst_unused:UNUSED_PRESERVE src0_sel:WORD_1\n"
    "v_mfma_f32_16x16x32_bf16 v[116:119], a[220:223], v[192:195], v[116:119]\n"
    "s_nop 0\n"
    "v_fma_mix_f32 v234, v196, v232, 0 op_sel_hi:[1,1,0]\n"
    "v_fma_mix_f32 v232, v196, v232, 0 op_sel:[1,1,0] op_sel_hi:[1,1,0]\n"
    "v_mfma_f32_16x16x32_bf16 v[128:131], v[40:43], v[192:195], v[128:131]\n"
    "s_waitcnt lgkmcnt(7)\n"
    "ds_read_u16_d16_hi v207, v207\n"
    "v_cvt_pk_bf16_f32 v196, v234, v232\n"
    "v_mfma_f32_16x16x32_bf16 v[132:135], v[44:47], v[192:195], v[132:135]\n"
    "v_cvt_pk_f16_f32 v192, v168, v169\n"
    "v_lshlrev_b32_sdwa v193, 1, v170 dst_sel:DWORD dst_unused:UNUSED_PAD src0_sel:DWORD src1_sel:WORD_1\n"
    "v_mfma_f32_16x16x32_bf16 v[104:107], a[208:211], v[196:199], v[104:107]\n"
    "ds_read_u16 v193, v193\n"
    "v_lshlrev_b32_sdwa v249, 1, v171 dst_sel:DWORD dst_unused:UNUSED_PAD src0_sel:DWORD src1_sel:WORD_1\n"
    "v_mfma_f32_16x16x32_bf16 v[108:111], a[212:215], v[196:199], v[108:111]\n"
    "s_waitcnt lgkmcnt(7)\n"
    "ds_read_u16_d16_hi v249, v249\n"
    "v_lshlrev_b32_sdwa v194, 1, v172 dst_sel:DWORD dst_unused:UNUSED_PAD src0_sel:DWORD src1_sel:WORD_1\n"
    "v_mfma_f32_16x16x32_bf16 v[120:123], a[216:219], v[196:199], v[120:123]\n"
    "v_or_b32_e32 v186, v186, v202\n"
    "v_lshlrev_b32_sdwa v255, 1, v173 dst_sel:DWORD dst_unused:UNUSED_PAD src0_sel:DWORD src1_sel:WORD_1\n"
    "v_mfma_f32_16x16x32_bf16 v[124:127], a[220:223], v[196:199], v[124:127]\n"
    "ds_read_u16 v194, v194\n"
    "v_lshlrev_b32_sdwa v195, 1, v174 dst_sel:DWORD dst_unused:UNUSED_PAD src0_sel:DWORD src1_sel:WORD_1\n"
    "v_mfma_f32_16x16x32_bf16 v[136:139], v[40:43], v[196:199], v[136:139]\n"
    "s_nop 0\n"
    "v_lshlrev_b32_sdwa v160, 1, v175 dst_sel:DWORD dst_unused:UNUSED_PAD src0_sel:DWORD src1_sel:WORD_1\n"
    "v_lshlrev_b32_sdwa v161, 1, v179 dst_sel:DWORD dst_unused:UNUSED_PAD src0_sel:DWORD src1_sel:WORD_1\n"
    "v_mfma_f32_16x16x32_bf16 v[140:143], v[44:47], v[196:199], v[140:143]\n"
    "s_waitcnt lgkmcnt(7)\n"
    "ds_read_u16_d16_hi v255, v255\n"
    "v_cvt_pk_f16_f32 v196, v176, v177\n"
    "v_mfma_f32_16x16x32_bf16 v[168:171], a[96:99], a[224:227], 0\n"
    "v_lshlrev_b32_sdwa v197, 1, v178 dst_sel:DWORD dst_unused:UNUSED_PAD src0_sel:DWORD src1_sel:WORD_1\n"
    "v_lshlrev_b32_sdwa v198, 1, v180 dst_sel:DWORD dst_unused:UNUSED_PAD src0_sel:DWORD src1_sel:WORD_1\n"
    "v_mfma_f32_16x16x32_bf16 v[168:171], a[144:147], a[228:231], v[168:171]\n"
    "ds_read_u16 v195, v195\n"
    "v_lshlrev_b32_sdwa v162, 1, v181 dst_sel:DWORD dst_unused:UNUSED_PAD src0_sel:DWORD src1_sel:WORD_1\n"
    "v_mfma_f32_16x16x32_bf16 v[168:171], a[56:59], a[232:235], v[168:171]\n"
    "s_waitcnt lgkmcnt(7)\n"
    "ds_read_u16_d16_hi v160, v160\n"
    "v_lshlrev_b32_sdwa v199, 1, v182 dst_sel:DWORD dst_unused:UNUSED_PAD src0_sel:DWORD src1_sel:WORD_1\n"
    "v_mfma_f32_16x16x32_bf16 v[172:175], a[100:103], a[224:227], 0\n"
    "v_or_b32_e32 v187, v187, v203\n"
    "v_lshlrev_b32_sdwa v163, 1, v183 dst_sel:DWORD dst_unused:UNUSED_PAD src0_sel:DWORD src1_sel:WORD_1\n"
    "v_mfma_f32_16x16x32_bf16 v[172:175], a[148:151], a[228:231], v[172:175]\n"
    "ds_read_u16 v197, v197\n"
    "v_or_b32_e64 v188, v188, v204\n"
    "v_mfma_f32_16x16x32_bf16 v[172:175], a[60:63], a[232:235], v[172:175]\n"
    "v_or_b32_e32 v189, v189, v205\n"
    "v_cvt_pk_bf16_f32 v96, v96, v97\n"
    "v_mfma_f32_16x16x32_bf16 v[176:179], a[96:99], a[236:239], 0\n"
    "s_waitcnt lgkmcnt(7)\n"
    "ds_read_u16_d16_hi v161, v161\n"
    "v_cvt_pk_bf16_f32 v97, v98, v99\n"
    "v_mfma_f32_16x16x32_bf16 v[176:179], a[144:147], a[240:243], v[176:179]\n"
    "v_cvt_pk_bf16_f32 v98, v100, v101\n"
    "v_cvt_pk_bf16_f32 v99, v102, v103\n"
    "v_mfma_f32_16x16x32_bf16 v[176:179], a[56:59], a[244:247], v[176:179]\n"
    "v_or_b32_e64 v190, v190, v206\n"
    "v_cvt_pk_bf16_f32 v112, v112, v113\n"
    "v_mfma_f32_16x16x32_bf16 v[180:183], a[100:103], a[236:239], 0\n"
    "s_waitcnt lgkmcnt(6)\n"
    "v_cvt_pk_bf16_f32 v113, v114, v115\n"
    "v_or_b32_e32 v193, v193, v249\n"
    "v_mfma_f32_16x16x32_bf16 v[180:183], a[148:151], a[240:243], v[180:183]\n"
    "v_cvt_pk_bf16_f32 v114, v116, v117\n"
    "v_cvt_pk_bf16_f32 v115, v118, v119\n"
    "v_mfma_f32_16x16x32_bf16 v[180:183], a[60:63], a[244:247], v[180:183]\n"
    "ds_read_u16 v198, v198\n"
    "ds_write_b128 v243, v[96:99] offset:0\n"
    "v_cvt_pk_bf16_f32 v128, v128, v129\n"
    "v_cvt_pk_bf16_f32 v129, v130, v131\n"
    "v_or_b32_e64 v191, v191, v207\n"
    "v_cvt_pk_bf16_f32 v130, v132, v133\n"
    "v_cvt_pk_bf16_f32 v131, v134, v135\n"
    "v_cvt_pk_bf16_f32 v104, v104, v105\n"
    "v_cvt_pk_bf16_f32 v105, v106, v107\n"
    "v_mfma_f32_16x16x32_bf16 v[96:99], a[0:3], v[184:187], 0\n"
    "s_waitcnt lgkmcnt(5)\n"
    "ds_read_u16_d16_hi v162, v162\n"
    "ds_write_b128 v243, v[112:115] offset:2048\n"
    "v_or_b32_e32 v194, v194, v255\n"
    "v_cvt_pk_bf16_f32 v106, v108, v109\n"
    "v_cvt_pk_bf16_f32 v107, v110, v111\n"
    "v_mfma_f32_16x16x32_bf16 v[100:103], a[4:7], v[184:187], 0\n"
    "s_waitcnt lgkmcnt(6)\n"
    "v_cvt_pk_bf16_f32 v120, v120, v121\n"
    "v_cvt_pk_bf16_f32 v121, v122, v123\n"
    "v_mfma_f32_16x16x32_bf16 v[112:115], a[8:11], v[184:187], 0\n"
    "s_nop 0\n"
    "ds_read_u16 v199, v199\n"
    "ds_write_b128 v243, v[128:131] offset:4096\n"
    "v_cvt_pk_bf16_f32 v122, v124, v125\n"
    "v_cvt_pk_bf16_f32 v123, v126, v127\n"
    "v_cvt_pk_bf16_f32 v136, v136, v137\n"
    "v_pk_fma_f16 v232, v192, v192, s48\n"
    "v_mfma_f32_16x16x32_bf16 v[116:119], a[12:15], v[184:187], 0\n"
    "s_waitcnt lgkmcnt(4)\n"
    "v_cvt_pk_bf16_f32 v137, v138, v139\n"
    "v_cvt_pk_bf16_f32 v138, v140, v141\n"
    "v_mfma_f32_16x16x32_bf16 v[128:131], v[0:3], v[184:187], 0\n"
    "s_nop 0\n"
    "ds_read_u16_d16_hi v163, v163\n"
    "ds_write_b128 v243, v[104:107] offset:1024\n"
    "v_or_b32_e64 v195, v195, v160\n"
    "v_cvt_pk_bf16_f32 v139, v142, v143\n"
    "v_pk_mul_f16 v232, v192, v232\n"
    "v_or_b32_e64 v197, v197, v161\n"
    "v_exp_f16_e32 v234, v232\n"
    "v_mfma_f32_16x16x32_bf16 v[132:135], v[4:7], v[184:187], 0\n"
    "s_waitcnt lgkmcnt(4)\n"
    "v_exp_f16_sdwa v234, v232 dst_sel:WORD_1 dst_unused:UNUSED_PRESERVE src0_sel:WORD_1\n"
    "v_mfma_f32_16x16x32_bf16 v[104:107], a[0:3], v[188:191], 0\n"
    "ds_write_b128 v243, v[120:123] offset:3072\n"
    "v_pk_fma_f16 v234, v234, s49, s49\n"
    "v_lshlrev_b32_sdwa v184, 1, v168 dst_sel:DWORD dst_unused:UNUSED_PAD src0_sel:DWORD src1_sel:WORD_1\n"
    "v_lshlrev_b32_sdwa v200, 1, v169 dst_sel:DWORD dst_unused:UNUSED_PAD src0_sel:DWORD src1_sel:WORD_1\n"
    "v_lshlrev_b32_sdwa v185, 1, v170 dst_sel:DWORD dst_unused:UNUSED_PAD src0_sel:DWORD src1_sel:WORD_1\n"
    "v_lshlrev_b32_sdwa v201, 1, v171 dst_sel:DWORD dst_unused:UNUSED_PAD src0_sel:DWORD src1_sel:WORD_1\n"
    "v_mfma_f32_16x16x32_bf16 v[108:111], a[4:7], v[188:191], 0\n"
    "v_lshlrev_b32_sdwa v186, 1, v172 dst_sel:DWORD dst_unused:UNUSED_PAD src0_sel:DWORD src1_sel:WORD_1\n"
    "v_lshlrev_b32_sdwa v202, 1, v173 dst_sel:DWORD dst_unused:UNUSED_PAD src0_sel:DWORD src1_sel:WORD_1\n"
    "v_mfma_f32_16x16x32_bf16 v[120:123], a[8:11], v[188:191], 0\n"
    "s_nop 0\n"
    "ds_read_u16 v184, v184\n"
    "ds_write_b128 v243, v[136:139] offset:5120\n"
    "v_mfma_f32_16x16x32_bf16 v[124:127], a[12:15], v[188:191], 0\n"
    "s_waitcnt lgkmcnt(3)\n"
    "v_lshlrev_b32_sdwa v187, 1, v174 dst_sel:DWORD dst_unused:UNUSED_PAD src0_sel:DWORD src1_sel:WORD_1\n"
    "v_lshlrev_b32_sdwa v203, 1, v175 dst_sel:DWORD dst_unused:UNUSED_PAD src0_sel:DWORD src1_sel:WORD_1\n"
    "v_mfma_f32_16x16x32_bf16 v[136:139], v[0:3], v[188:191], 0\n"
    "v_rcp_f16_e64 v232, v234\n"
    "v_mfma_f32_16x16x32_bf16 v[140:143], v[4:7], v[188:191], 0\n"
    "ds_read_u16_d16_hi v200, v200\n"
    "ds_read_u16 v185, v185\n"
    "v_mfma_f32_16x16x32_bf16 v[168:171], a[104:107], a[224:227], 0\n"
    "ds_read_u16_d16_hi v201, v201\n"
    "ds_read_u16 v186, v186\n"
    "v_mfma_f32_16x16x32_bf16 v[168:171], a[152:155], a[228:231], v[168:171]\n"
    "v_rcp_f16_sdwa v232, v234 dst_sel:WORD_1 dst_unused:UNUSED_PRESERVE src0_sel:WORD_1\n"
    "v_mfma_f32_16x16x32_bf16 v[168:171], a[64:67], a[232:235], v[168:171]\n"
    "ds_read_u16_d16_hi v202, v202\n"
    "v_lshlrev_b32_sdwa v188, 1, v176 dst_sel:DWORD dst_unused:UNUSED_PAD src0_sel:DWORD src1_sel:WORD_1\n"
    "v_mfma_f32_16x16x32_bf16 v[172:175], a[108:111], a[224:227], 0\n"
    "v_lshlrev_b32_sdwa v204, 1, v177 dst_sel:DWORD dst_unused:UNUSED_PAD src0_sel:DWORD src1_sel:WORD_1\n"
    "v_lshlrev_b32_sdwa v189, 1, v178 dst_sel:DWORD dst_unused:UNUSED_PAD src0_sel:DWORD src1_sel:WORD_1\n"
    "v_mfma_f32_16x16x32_bf16 v[172:175], a[156:159], a[228:231], v[172:175]\n"
    "ds_read_u16 v187, v187\n"
    "v_lshlrev_b32_sdwa v205, 1, v179 dst_sel:DWORD dst_unused:UNUSED_PAD src0_sel:DWORD src1_sel:WORD_1\n"
    "v_lshlrev_b32_sdwa v190, 1, v180 dst_sel:DWORD dst_unused:UNUSED_PAD src0_sel:DWORD src1_sel:WORD_1\n"
    "v_lshlrev_b32_sdwa v206, 1, v181 dst_sel:DWORD dst_unused:UNUSED_PAD src0_sel:DWORD src1_sel:WORD_1\n"
    "v_lshlrev_b32_sdwa v191, 1, v182 dst_sel:DWORD dst_unused:UNUSED_PAD src0_sel:DWORD src1_sel:WORD_1\n"
    "v_lshlrev_b32_sdwa v207, 1, v183 dst_sel:DWORD dst_unused:UNUSED_PAD src0_sel:DWORD src1_sel:WORD_1\n"
    "v_fma_mix_f32 v234, v192, v232, 0 op_sel_hi:[1,1,0]\n"
    "v_mfma_f32_16x16x32_bf16 v[172:175], a[68:71], a[232:235], v[172:175]\n"
    "s_waitcnt lgkmcnt(6)\n"
    "ds_read_u16_d16_hi v203, v203\n"
    "v_or_b32_e32 v198, v198, v162\n"
    "v_fma_mix_f32 v232, v192, v232, 0 op_sel:[1,1,0] op_sel_hi:[1,1,0]\n"
    "v_mfma_f32_16x16x32_bf16 v[176:179], a[104:107], a[236:239], 0\n"
    "s_barrier\n"
    "v_cvt_pk_bf16_f32 v192, v234, v232\n"
    "v_mfma_f32_16x16x32_bf16 v[176:179], a[152:155], a[240:243], v[176:179]\n"
    "s_waitcnt lgkmcnt(5)\n"
    "ds_read_u16 v188, v188\n"
    "ds_read_u16_d16_hi v204, v204\n"
    "v_mfma_f32_16x16x32_bf16 v[176:179], a[64:67], a[244:247], v[176:179]\n"
    "v_pk_fma_f16 v232, v196, v196, s48\n"
    "v_or_b32_e32 v199, v199, v163\n"
    "v_mfma_f32_16x16x32_bf16 v[180:183], a[108:111], a[236:239], 0\n"
    "s_waitcnt lgkmcnt(6)\n"
    "ds_read_u16 v189, v189\n"
    "v_pk_mul_f16 v232, v196, v232\n"
    "v_mfma_f32_16x16x32_bf16 v[180:183], a[156:159], a[240:243], v[180:183]\n"
    "ds_read_u16_d16_hi v205, v205\n"
    "v_exp_f16_e64 v234, v232\n"
    "v_mfma_f32_16x16x32_bf16 v[180:183], a[68:71], a[244:247], v[180:183]\n"
    "ds_read_u16 v190, v190\n"
    "v_exp_f16_sdwa v234, v232 dst_sel:WORD_1 dst_unused:UNUSED_PRESERVE src0_sel:WORD_1\n"
    "v_mfma_f32_16x16x32_bf16 v[96:99], a[16:19], v[192:195], v[96:99]\n"
    "s_waitcnt lgkmcnt(7)\n"
    "v_pk_fma_f16 v234, v234, s49, s49\n"
    "v_or_b32_e32 v184, v184, v200\n"
    "v_mfma_f32_16x16x32_bf16 v[100:103], a[20:23], v[192:195], v[100:103]\n"
    "ds_read_u16_d16_hi v206, v206\n"
    "v_rcp_f16_e32 v232, v234\n"
    "v_mfma_f32_16x16x32_bf16 v[112:115], a[24:27], v[192:195], v[112:115]\n"
    "ds_read_u16 v191, v191\n"
    "v_rcp_f16_sdwa v232, v234 dst_sel:WORD_1 dst_unused:UNUSED_PRESERVE src0_sel:WORD_1\n"
    "v_mfma_f32_16x16x32_bf16 v[116:119], a[28:31], v[192:195], v[116:119]\n"
    "s_waitcnt lgkmcnt(7)\n"
    "ds_read_u16_d16_hi v207, v207\n"
    "v_fma_mix_f32 v234, v196, v232, 0 op_sel_hi:[1,1,0]\n"
    "v_mfma_f32_16x16x32_bf16 v[128:131], v[8:11], v[192:195], v[128:131]\n"
    "v_fma_mix_f32 v232, v196, v232, 0 op_sel:[1,1,0] op_sel_hi:[1,1,0]\n"
    "v_or_b32_e64 v185, v185, v201\n"
    "v_mfma_f32_16x16x32_bf16 v[132:135], v[12:15], v[192:195], v[132:135]\n"
    "v_cvt_pk_bf16_f32 v196, v234, v232\n"
    "v_cvt_pk_f16_f32 v192, v168, v169\n"
    "v_lshlrev_b32_sdwa v193, 1, v170 dst_sel:DWORD dst_unused:UNUSED_PAD src0_sel:DWORD src1_sel:WORD_1\n"
    "v_mfma_f32_16x16x32_bf16 v[104:107], a[16:19], v[196:199], v[104:107]\n"
    "s_waitcnt lgkmcnt(6)\n"
    "ds_read_u16 v193, v193\n"
    "v_lshlrev_b32_sdwa v249, 1, v171 dst_sel:DWORD dst_unused:UNUSED_PAD src0_sel:DWORD src1_sel:WORD_1\n"
    "v_mfma_f32_16x16x32_bf16 v[108:111], a[20:23], v[196:199], v[108:111]\n"
    "s_waitcnt vmcnt(7)\n"
    "ds_read_u16_d16_hi v249, v249\n"
    "v_lshlrev_b32_sdwa v194, 1, v172 dst_sel:DWORD dst_unused:UNUSED_PAD src0_sel:DWORD src1_sel:WORD_1\n"
    "v_mfma_f32_16x16x32_bf16 v[120:123], a[24:27], v[196:199], v[120:123]\n"
    "ds_read_u16 v194, v194\n"
    "v_or_b32_e32 v186, v186, v202\n"
    "v_mfma_f32_16x16x32_bf16 v[124:127], a[28:31], v[196:199], v[124:127]\n"
    "s_waitcnt lgkmcnt(7)\n"
    "v_lshlrev_b32_sdwa v255, 1, v173 dst_sel:DWORD dst_unused:UNUSED_PAD src0_sel:DWORD src1_sel:WORD_1\n"
    "v_lshlrev_b32_sdwa v195, 1, v174 dst_sel:DWORD dst_unused:UNUSED_PAD src0_sel:DWORD src1_sel:WORD_1\n"
    "v_mfma_f32_16x16x32_bf16 v[136:139], v[8:11], v[196:199], v[136:139]\n"
    "s_nop 0\n"
    "ds_read_u16_d16_hi v255, v255\n"
    "v_lshlrev_b32_sdwa v160, 1, v175 dst_sel:DWORD dst_unused:UNUSED_PAD src0_sel:DWORD src1_sel:WORD_1\n"
    "v_mfma_f32_16x16x32_bf16 v[140:143], v[12:15], v[196:199], v[140:143]\n"
    "s_waitcnt vmcnt(6)\n"
    "ds_read_u16 v195, v195\n"
    "v_cvt_pk_f16_f32 v196, v176, v177\n"
    "v_smfmac_f32_32x32x32_bf16 v[112:127], a[248:251], v[152:159], v241\n"
    "v_lshlrev_b32_sdwa v197, 1, v178 dst_sel:DWORD dst_unused:UNUSED_PAD src0_sel:DWORD src1_sel:WORD_1\n"
    "v_lshlrev_b32_sdwa v161, 1, v179 dst_sel:DWORD dst_unused:UNUSED_PAD src0_sel:DWORD src1_sel:WORD_1\n"
    "v_lshlrev_b32_sdwa v198, 1, v180 dst_sel:DWORD dst_unused:UNUSED_PAD src0_sel:DWORD src1_sel:WORD_1\n"
    "v_lshlrev_b32_sdwa v162, 1, v181 dst_sel:DWORD dst_unused:UNUSED_PAD src0_sel:DWORD src1_sel:WORD_1\n"
    "v_lshlrev_b32_sdwa v199, 1, v182 dst_sel:DWORD dst_unused:UNUSED_PAD src0_sel:DWORD src1_sel:WORD_1\n"
    "v_smfmac_f32_32x32x32_bf16 v[96:111], a[248:251], v[144:151], v241\n"
    "s_waitcnt lgkmcnt(6)\n"
    "ds_read_b128 v[152:155], v242 offset:0\n"
    "ds_read_b128 v[156:159], v242 offset:1024\n"
    "v_or_b32_e32 v187, v187, v203\n"
    "v_lshlrev_b32_sdwa v163, 1, v183 dst_sel:DWORD dst_unused:UNUSED_PAD src0_sel:DWORD src1_sel:WORD_1\n"
    "v_or_b32_e32 v188, v188, v204\n"
    "v_smfmac_f32_32x32x32_bf16 v[128:143], a[248:251], v[208:215], v241\n"
    "s_waitcnt lgkmcnt(7)\n"
    "ds_read_u16_d16_hi v160, v160\n"
    "ds_read_u16 v197, v197\n"
    "v_or_b32_e64 v189, v189, v205\n"
    "v_or_b32_e64 v190, v190, v206\n"
    "v_or_b32_e64 v191, v191, v207\n"
    "v_mfma_f32_16x16x32_bf16 v[168:171], a[112:115], a[224:227], 0\n"
    "ds_read_u16_d16_hi v161, v161\n"
    "v_pk_fma_f16 v232, v192, v192, s48\n"
    "v_mfma_f32_16x16x32_bf16 v[168:171], a[160:163], a[228:231], v[168:171]\n"
    "v_pk_mul_f16 v232, v192, v232\n"
    "v_mfma_f32_16x16x32_bf16 v[168:171], a[72:75], a[232:235], v[168:171]\n"
    "s_waitcnt lgkmcnt(8)\n"
    "ds_read_u16 v198, v198\n"
    "v_or_b32_e32 v193, v193, v249\n"
    "v_mfma_f32_16x16x32_bf16 v[172:175], a[116:119], a[224:227], 0\n"
    "v_exp_f16_e64 v234, v232\n"
    "v_mfma_f32_16x16x32_bf16 v[172:175], a[164:167], a[228:231], v[172:175]\n"
    "ds_read_u16_d16_hi v162, v162\n"
    "v_mfma_f32_16x16x32_bf16 v[172:175], v[52:55], a[232:235], v[172:175]\n"
    "s_waitcnt lgkmcnt(8)\n"
    "v_or_b32_e32 v194, v194, v255\n"
    "v_exp_f16_sdwa v234, v232 dst_sel:WORD_1 dst_unused:UNUSED_PRESERVE src0_sel:WORD_1\n"
    "v_mfma_f32_16x16x32_bf16 v[176:179], a[112:115], a[236:239], 0\n"
    "ds_read_u16 v199, v199\n"
    "v_pk_fma_f16 v234, v234, s49, s49\n"
    "v_mfma_f32_16x16x32_bf16 v[176:179], a[160:163], a[240:243], v[176:179]\n"
    "s_nop 0\n"
    "ds_read_u16_d16_hi v163, v163\n"
    "v_lshlrev_b32_sdwa v200, 1, v169 dst_sel:DWORD dst_unused:UNUSED_PAD src0_sel:DWORD src1_sel:WORD_1\n"
    "v_mfma_f32_16x16x32_bf16 v[176:179], a[72:75], a[244:247], v[176:179]\n"
    "s_waitcnt lgkmcnt(7)\n"
    "v_lshlrev_b32_sdwa v201, 1, v171 dst_sel:DWORD dst_unused:UNUSED_PAD src0_sel:DWORD src1_sel:WORD_1\n"
    "v_lshlrev_b32_sdwa v202, 1, v173 dst_sel:DWORD dst_unused:UNUSED_PAD src0_sel:DWORD src1_sel:WORD_1\n"
    "v_smfmac_f32_32x32x32_bf16 v[96:111], a[248:251], v[152:159], v241\n"
    "ds_read_b128 v[152:155], v242 offset:2048\n"
    "ds_read_b128 v[156:159], v242 offset:3072\n"
    "v_lshlrev_b32_sdwa v203, 1, v175 dst_sel:DWORD dst_unused:UNUSED_PAD src0_sel:DWORD src1_sel:WORD_1\n"
    "v_rcp_f16_e32 v232, v234\n"
    "v_mfma_f32_16x16x32_bf16 v[180:183], a[116:119], a[236:239], 0\n"
    "s_waitcnt lgkmcnt(8)\n"
    "ds_read_u16_d16_hi v200, v200\n"
    "v_or_b32_e64 v195, v195, v160\n"
    "v_mfma_f32_16x16x32_bf16 v[180:183], a[164:167], a[240:243], v[180:183]\n"
    "v_rcp_f16_sdwa v232, v234 dst_sel:WORD_1 dst_unused:UNUSED_PRESERVE src0_sel:WORD_1\n"
    "v_mfma_f32_16x16x32_bf16 v[180:183], v[52:55], a[244:247], v[180:183]\n"
    "ds_read_u16_d16_hi v201, v201\n"
    "v_lshlrev_b32_sdwa v204, 1, v177 dst_sel:DWORD dst_unused:UNUSED_PAD src0_sel:DWORD src1_sel:WORD_1\n"
    "v_mfma_f32_16x16x32_bf16 v[96:99], a[32:35], v[184:187], v[96:99]\n"
    "s_waitcnt lgkmcnt(8)\n"
    "ds_read_u16_d16_hi v202, v202\n"
    "v_or_b32_e32 v197, v197, v161\n"
    "v_mfma_f32_16x16x32_bf16 v[100:103], a[36:39], v[184:187], v[100:103]\n"
    "v_lshlrev_b32_sdwa v205, 1, v179 dst_sel:DWORD dst_unused:UNUSED_PAD src0_sel:DWORD src1_sel:WORD_1\n"
    "v_lshlrev_b32_sdwa v206, 1, v181 dst_sel:DWORD dst_unused:UNUSED_PAD src0_sel:DWORD src1_sel:WORD_1\n"
    "v_mfma_f32_16x16x32_bf16 v[112:115], a[40:43], v[184:187], v[112:115]\n"
    "ds_read_u16_d16_hi v203, v203\n"
    "v_lshlrev_b32_sdwa v207, 1, v183 dst_sel:DWORD dst_unused:UNUSED_PAD src0_sel:DWORD src1_sel:WORD_1\n"
    "v_mfma_f32_16x16x32_bf16 v[116:119], a[44:47], v[184:187], v[116:119]\n"
    "v_fma_mix_f32 v234, v192, v232, 0 op_sel_hi:[1,1,0]\n"
    "v_fma_mix_f32 v232, v192, v232, 0 op_sel:[1,1,0] op_sel_hi:[1,1,0]\n"
    "v_mfma_f32_16x16x32_bf16 v[128:131], v[16:19], v[184:187], v[128:131]\n"
    "s_waitcnt lgkmcnt(8)\n"
    "v_or_b32_e32 v198, v198, v162\n"
    "v_cvt_pk_bf16_f32 v192, v234, v232\n"
    "v_mfma_f32_16x16x32_bf16 v[132:135], v[20:23], v[184:187], v[132:135]\n"
    "ds_read_u16_d16_hi v204, v204\n"
    "v_lshlrev_b32_sdwa v184, 1, v168 dst_sel:DWORD dst_unused:UNUSED_PAD src0_sel:DWORD src1_sel:WORD_1\n"
    "v_mfma_f32_16x16x32_bf16 v[104:107], a[32:35], v[188:191], v[104:107]\n"
    "s_nop 0\n"
    "ds_read_u16 v184, v184\n"
    "v_lshlrev_b32_sdwa v185, 1, v170 dst_sel:DWORD dst_unused:UNUSED_PAD src0_sel:DWORD src1_sel:WORD_1\n"
    "v_mfma_f32_16x16x32_bf16 v[108:111], a[36:39], v[188:191], v[108:111]\n"
    "s_waitcnt lgkmcnt(7)\n"
    "ds_read_u16 v185, v185\n"
    "v_lshlrev_b32_sdwa v186, 1, v172 dst_sel:DWORD dst_unused:UNUSED_PAD src0_sel:DWORD src1_sel:WORD_1\n"
    "v_mfma_f32_16x16x32_bf16 v[120:123], a[40:43], v[188:191], v[120:123]\n"
    "v_lshlrev_b32_sdwa v187, 1, v174 dst_sel:DWORD dst_unused:UNUSED_PAD src0_sel:DWORD src1_sel:WORD_1\n"
    "v_pk_fma_f16 v232, v196, v196, s48\n"
    "v_mfma_f32_16x16x32_bf16 v[124:127], a[44:47], v[188:191], v[124:127]\n"
    "s_waitcnt lgkmcnt(7)\n"
    "ds_read_u16 v186, v186\n"
    "v_or_b32_e32 v199, v199, v163\n"
    "v_mfma_f32_16x16x32_bf16 v[136:139], v[16:19], v[188:191], v[136:139]\n"
    "v_pk_mul_f16 v232, v196, v232\n"
    "v_exp_f16_e64 v234, v232\n"
    "v_mfma_f32_16x16x32_bf16 v[140:143], v[20:23], v[188:191], v[140:143]\n"
    "ds_read_u16 v187, v187\n"
    "v_lshlrev_b32_sdwa v188, 1, v176 dst_sel:DWORD dst_unused:UNUSED_PAD src0_sel:DWORD src1_sel:WORD_1\n"
    "v_smfmac_f32_32x32x32_bf16 v[112:127], a[248:251], v[152:159], v241\n"
    "ds_read_u16 v188, v188\n"
    "v_lshlrev_b32_sdwa v189, 1, v178 dst_sel:DWORD dst_unused:UNUSED_PAD src0_sel:DWORD src1_sel:WORD_1\n"
    "v_lshlrev_b32_sdwa v190, 1, v180 dst_sel:DWORD dst_unused:UNUSED_PAD src0_sel:DWORD src1_sel:WORD_1\n"
    "v_lshlrev_b32_sdwa v191, 1, v182 dst_sel:DWORD dst_unused:UNUSED_PAD src0_sel:DWORD src1_sel:WORD_1\n"
    "v_exp_f16_sdwa v234, v232 dst_sel:WORD_1 dst_unused:UNUSED_PRESERVE src0_sel:WORD_1\n"
    "v_mfma_f32_16x16x32_bf16 v[168:171], a[120:123], a[224:227], 0\n"
    "ds_read_b128 v[152:155], v242 offset:4096\n"
    "v_pk_fma_f16 v234, v234, s49, s49\n"
    "v_mfma_f32_16x16x32_bf16 v[168:171], a[168:171], a[228:231], v[168:171]\n"
    "ds_read_b128 v[156:159], v242 offset:5120\n"
    "v_mfma_f32_16x16x32_bf16 v[168:171], a[76:79], a[232:235], v[168:171]\n"
    "ds_read_u16 v189, v189\n"
    "v_rcp_f16_e64 v232, v234\n"
    "v_mfma_f32_16x16x32_bf16 v[172:175], a[124:127], a[224:227], 0\n"
    "ds_read_u16_d16_hi v205, v205\n"
    "v_mfma_f32_16x16x32_bf16 v[172:175], a[172:175], a[228:231], v[172:175]\n"
    "s_nop 0\n"
    "v_rcp_f16_sdwa v232, v234 dst_sel:WORD_1 dst_unused:UNUSED_PRESERVE src0_sel:WORD_1\n"
    "v_mfma_f32_16x16x32_bf16 v[172:175], v[60:63], a[232:235], v[172:175]\n"
    "s_waitcnt lgkmcnt(8)\n"
    "ds_read_u16 v190, v190\n"
    "v_fma_mix_f32 v234, v196, v232, 0 op_sel_hi:[1,1,0]\n"
    "v_mfma_f32_16x16x32_bf16 v[176:179], a[120:123], a[236:239], 0\n"
    "v_fma_mix_f32 v232, v196, v232, 0 op_sel:[1,1,0] op_sel_hi:[1,1,0]\n"
    "v_or_b32_e32 v184, v184, v200\n"
    "v_mfma_f32_16x16x32_bf16 v[176:179], a[168:171], a[240:243], v[176:179]\n"
    "s_waitcnt lgkmcnt(8)\n"
    "ds_read_u16_d16_hi v206, v206\n"
    "v_cvt_pk_bf16_f32 v196, v234, v232\n"
    "v_mfma_f32_16x16x32_bf16 v[176:179], a[76:79], a[244:247], v[176:179]\n"
    "s_waitcnt lgkmcnt(8)\n"
    "v_or_b32_e32 v185, v185, v201\n"
    "v_lshlrev_b32_sdwa v249, 1, v171 dst_sel:DWORD dst_unused:UNUSED_PAD src0_sel:DWORD src1_sel:WORD_1\n"
    "v_mfma_f32_16x16x32_bf16 v[180:183], a[124:127], a[236:239], 0\n"
    "ds_read_u16 v191, v191\n"
    "v_or_b32_e32 v186, v186, v202\n"
    "v_mfma_f32_16x16x32_bf16 v[180:183], a[172:175], a[240:243], v[180:183]\n"
    "s_waitcnt lgkmcnt(8)\n"
    "ds_read_u16_d16_hi v207, v207\n"
    "v_lshlrev_b32_sdwa v255, 1, v173 dst_sel:DWORD dst_unused:UNUSED_PAD src0_sel:DWORD src1_sel:WORD_1\n"
    "v_mfma_f32_16x16x32_bf16 v[180:183], v[60:63], a[244:247], v[180:183]\n"
    "v_lshlrev_b32_sdwa v160, 1, v175 dst_sel:DWORD dst_unused:UNUSED_PAD src0_sel:DWORD src1_sel:WORD_1\n"
    "v_lshlrev_b32_sdwa v161, 1, v179 dst_sel:DWORD dst_unused:UNUSED_PAD src0_sel:DWORD src1_sel:WORD_1\n"
    "v_mfma_f32_16x16x32_bf16 v[96:99], a[176:179], v[192:195], v[96:99]\n"
    "s_waitcnt lgkmcnt(7)\n"
    "ds_read_u16_d16_hi v249, v249\n"
    "v_or_b32_e32 v187, v187, v203\n"
    "v_mfma_f32_16x16x32_bf16 v[100:103], a[180:183], v[192:195], v[100:103]\n"
    "v_or_b32_e64 v188, v188, v204\n"
    "v_lshlrev_b32_sdwa v162, 1, v181 dst_sel:DWORD dst_unused:UNUSED_PAD src0_sel:DWORD src1_sel:WORD_1\n"
    "v_mfma_f32_16x16x32_bf16 v[112:115], a[184:187], v[192:195], v[112:115]\n"
    "ds_read_u16_d16_hi v255, v255\n"
    "v_lshlrev_b32_sdwa v163, 1, v183 dst_sel:DWORD dst_unused:UNUSED_PAD src0_sel:DWORD src1_sel:WORD_1\n"
    "v_mfma_f32_16x16x32_bf16 v[116:119], a[188:191], v[192:195], v[116:119]\n"
    "v_mfma_f32_16x16x32_bf16 v[128:131], v[24:27], v[192:195], v[128:131]\n"
    "s_nop 0\n"
    "ds_read_u16_d16_hi v160, v160\n"
    "v_mfma_f32_16x16x32_bf16 v[132:135], v[28:31], v[192:195], v[132:135]\n"
    "s_waitcnt lgkmcnt(7)\n"
    "ds_read_u16_d16_hi v161, v161\n"
    "v_cvt_pk_f16_f32 v192, v168, v169\n"
    "v_mfma_f32_16x16x32_bf16 v[104:107], a[176:179], v[196:199], v[104:107]\n"
    "v_lshlrev_b32_sdwa v193, 1, v170 dst_sel:DWORD dst_unused:UNUSED_PAD src0_sel:DWORD src1_sel:WORD_1\n"
    "v_lshlrev_b32_sdwa v194, 1, v172 dst_sel:DWORD dst_unused:UNUSED_PAD src0_sel:DWORD src1_sel:WORD_1\n"
    "v_mfma_f32_16x16x32_bf16 v[108:111], a[180:183], v[196:199], v[108:111]\n"
    "ds_read_u16 v193, v193\n"
    "v_lshlrev_b32_sdwa v195, 1, v174 dst_sel:DWORD dst_unused:UNUSED_PAD src0_sel:DWORD src1_sel:WORD_1\n"
    "v_or_b32_e64 v189, v189, v205\n"
    "v_pk_fma_f16 v232, v192, v192, s48\n"
    "v_mfma_f32_16x16x32_bf16 v[120:123], a[184:187], v[196:199], v[120:123]\n"
    "s_waitcnt lgkmcnt(7)\n"
    "ds_read_u16 v194, v194\n"
    "v_or_b32_e32 v190, v190, v206\n"
    "v_mfma_f32_16x16x32_bf16 v[124:127], a[188:191], v[196:199], v[124:127]\n"
    "v_pk_mul_f16 v232, v192, v232\n"
    "v_exp_f16_e32 v234, v232\n"
    "v_mfma_f32_16x16x32_bf16 v[136:139], v[24:27], v[196:199], v[136:139]\n"
    "ds_read_u16 v195, v195\n"
    "v_exp_f16_sdwa v234, v232 dst_sel:WORD_1 dst_unused:UNUSED_PRESERVE src0_sel:WORD_1\n"
    "v_mfma_f32_16x16x32_bf16 v[140:143], v[28:31], v[196:199], v[140:143]\n"
    "s_waitcnt lgkmcnt(7)\n"
    "ds_read_u16_d16_hi v162, v162\n"
    "v_cvt_pk_f16_f32 v196, v176, v177\n"
    "v_mfma_f32_16x16x32_bf16 v[96:99], a[192:195], v[184:187], v[96:99]\n"
    "v_lshlrev_b32_sdwa v197, 1, v178 dst_sel:DWORD dst_unused:UNUSED_PAD src0_sel:DWORD src1_sel:WORD_1\n"
    "v_lshlrev_b32_sdwa v198, 1, v180 dst_sel:DWORD dst_unused:UNUSED_PAD src0_sel:DWORD src1_sel:WORD_1\n"
    "v_smfmac_f32_32x32x32_bf16 v[128:143], a[248:251], v[152:159], v241\n"
    "ds_read_u16 v197, v197\n"
    "v_lshlrev_b32_sdwa v199, 1, v182 dst_sel:DWORD dst_unused:UNUSED_PAD src0_sel:DWORD src1_sel:WORD_1\n"
    "v_or_b32_e64 v191, v191, v207\n"
    "v_pk_fma_f16 v234, v234, s49, s49\n"
    "v_rcp_f16_e64 v232, v234\n"
    "v_mfma_f32_16x16x32_bf16 v[100:103], a[196:199], v[184:187], v[100:103]\n"
    "ds_read_u16 v198, v198\n"
    "v_rcp_f16_sdwa v232, v234 dst_sel:WORD_1 dst_unused:UNUSED_PRESERVE src0_sel:WORD_1\n"
    "v_mfma_f32_16x16x32_bf16 v[112:115], a[200:203], v[184:187], v[112:115]\n"
    "ds_read_u16 v199, v199\n"
    "v_fma_mix_f32 v234, v192, v232, 0 op_sel_hi:[1,1,0]\n"
    "v_mfma_f32_16x16x32_bf16 v[116:119], a[204:207], v[184:187], v[116:119]\n"
    "v_fma_mix_f32 v232, v192, v232, 0 op_sel:[1,1,0] op_sel_hi:[1,1,0]\n"
    "v_cvt_pk_bf16_f32 v192, v234, v232\n"
    "v_mfma_f32_16x16x32_bf16 v[128:131], v[32:35], v[184:187], v[128:131]\n"
    "ds_read_u16_d16_hi v163, v163\n"
    "v_pk_fma_f16 v232, v196, v196, s48\n"
    "v_mfma_f32_16x16x32_bf16 v[132:135], v[36:39], v[184:187], v[132:135]\n"
    "v_pk_mul_f16 v232, v196, v232\n"
    "v_exp_f16_e64 v234, v232\n"
    "v_mfma_f32_16x16x32_bf16 v[104:107], a[192:195], v[188:191], v[104:107]\n"
    "s_waitcnt lgkmcnt(7)\n"
    "v_or_b32_e32 v193, v193, v249\n"
    "v_exp_f16_sdwa v234, v232 dst_sel:WORD_1 dst_unused:UNUSED_PRESERVE src0_sel:WORD_1\n"
    "v_mfma_f32_16x16x32_bf16 v[108:111], a[196:199], v[188:191], v[108:111]\n"
    "s_waitcnt lgkmcnt(6)\n"
    "v_or_b32_e32 v194, v194, v255\n"
    "v_pk_fma_f16 v234, v234, s49, s49\n"
    "v_mfma_f32_16x16x32_bf16 v[120:123], a[200:203], v[188:191], v[120:123]\n"
    "v_rcp_f16_e64 v232, v234\n"
    "v_mfma_f32_16x16x32_bf16 v[124:127], a[204:207], v[188:191], v[124:127]\n"
    "s_waitcnt lgkmcnt(5)\n"
    "v_or_b32_e32 v195, v195, v160\n"
    "v_rcp_f16_sdwa v232, v234 dst_sel:WORD_1 dst_unused:UNUSED_PRESERVE src0_sel:WORD_1\n"
    "v_mfma_f32_16x16x32_bf16 v[136:139], v[32:35], v[188:191], v[136:139]\n"
    "v_fma_mix_f32 v234, v196, v232, 0 op_sel_hi:[1,1,0]\n"
    "v_fma_mix_f32 v232, v196, v232, 0 op_sel:[1,1,0] op_sel_hi:[1,1,0]\n"
    "v_mfma_f32_16x16x32_bf16 v[140:143], v[36:39], v[188:191], v[140:143]\n"
    "v_cvt_pk_bf16_f32 v196, v234, v232\n"
    "v_mfma_f32_16x16x32_bf16 v[96:99], a[208:211], v[192:195], v[96:99]\n"
    "s_waitcnt lgkmcnt(3)\n"
    "v_or_b32_e32 v197, v197, v161\n"
    "v_mfma_f32_16x16x32_bf16 v[100:103], a[212:215], v[192:195], v[100:103]\n"
    "v_mfma_f32_16x16x32_bf16 v[112:115], a[216:219], v[192:195], v[112:115]\n"
    "s_waitcnt lgkmcnt(2)\n"
    "v_or_b32_e32 v198, v198, v162\n"
    "v_mfma_f32_16x16x32_bf16 v[116:119], a[220:223], v[192:195], v[116:119]\n"
    "v_mfma_f32_16x16x32_bf16 v[128:131], v[40:43], v[192:195], v[128:131]\n"
    "v_cvt_pk_bf16_f32 v96, v96, v97\n"
    "v_cvt_pk_bf16_f32 v97, v98, v99\n"
    "v_mfma_f32_16x16x32_bf16 v[132:135], v[44:47], v[192:195], v[132:135]\n"
    "s_waitcnt lgkmcnt(0)\n"
    "v_or_b32_e32 v199, v199, v163\n"
    "v_cvt_pk_bf16_f32 v98, v100, v101\n"
    "v_cvt_pk_bf16_f32 v99, v102, v103\n"
    "v_mfma_f32_16x16x32_bf16 v[104:107], a[208:211], v[196:199], v[104:107]\n"
    "buffer_store_dwordx4 v[96:99], v245, s[44:47], s58 offen offset:0 sc1\n"
    "v_mfma_f32_16x16x32_bf16 v[108:111], a[212:215], v[196:199], v[108:111]\n"
    "v_cvt_pk_bf16_f32 v112, v112, v113\n"
    "v_cvt_pk_bf16_f32 v113, v114, v115\n"
    "v_mfma_f32_16x16x32_bf16 v[120:123], a[216:219], v[196:199], v[120:123]\n"
    "v_cvt_pk_bf16_f32 v114, v116, v117\n"
    "v_cvt_pk_bf16_f32 v115, v118, v119\n"
    "v_mfma_f32_16x16x32_bf16 v[124:127], a[220:223], v[196:199], v[124:127]\n"
    "buffer_store_dwordx4 v[112:115], v245, s[44:47], s58 offen offset:64 sc1\n"
    "v_mfma_f32_16x16x32_bf16 v[136:139], v[40:43], v[196:199], v[136:139]\n"
    "v_cvt_pk_bf16_f32 v128, v128, v129\n"
    "v_cvt_pk_bf16_f32 v129, v130, v131\n"
    "v_mfma_f32_16x16x32_bf16 v[140:143], v[44:47], v[196:199], v[140:143]\n"
    "v_cvt_pk_bf16_f32 v130, v132, v133\n"
    "v_cvt_pk_bf16_f32 v131, v134, v135\n"
    "buffer_store_dwordx4 v[128:131], v245, s[44:47], s58 offen offset:128 sc1\n"
    "v_cvt_pk_bf16_f32 v104, v104, v105\n"
    "v_cvt_pk_bf16_f32 v105, v106, v107\n"
    "v_cvt_pk_bf16_f32 v106, v108, v109\n"
    "v_cvt_pk_bf16_f32 v107, v110, v111\n"
    "buffer_store_dwordx4 v[104:107], v245, s[44:47], s89 offen offset:0 sc1\n"
    "v_cvt_pk_bf16_f32 v120, v120, v121\n"
    "v_cvt_pk_bf16_f32 v121, v122, v123\n"
    "v_cvt_pk_bf16_f32 v122, v124, v125\n"
    "v_cvt_pk_bf16_f32 v123, v126, v127\n"
    "buffer_store_dwordx4 v[120:123], v245, s[44:47], s89 offen offset:64 sc1\n"
    "v_cvt_pk_bf16_f32 v136, v136, v137\n"
    "v_cvt_pk_bf16_f32 v137, v138, v139\n"
    "v_cvt_pk_bf16_f32 v138, v140, v141\n"
    "v_cvt_pk_bf16_f32 v139, v142, v143\n"
    "buffer_store_dwordx4 v[136:139], v245, s[44:47], s89 offen offset:128 sc1\n"
    "97:\n"
    "98:\n"
    "s_waitcnt vmcnt(0) lgkmcnt(0)\n"
    ".Lreplk_end:\n"
    : "+{s66}"(flg), "+{v208}"(tid), "+{s65}"(blk), "+{s[98:99]}"(ka)
    : "{s[96:97]}"(CTAB)
    : "memory", "scc", "vcc", "m0", "v0", "v1", "v2", "v3", "v4", "v5", "v6", "v7", "v8", "v9", "v10", "v11", "v12", "v13", "v14", "v15", "v16", "v17", "v18", "v19", "v20", "v21", "v22", "v23", "v24", "v25", "v26", "v27", "v28", "v29", "v30", "v31", "v32", "v33", "v34", "v35", "v36", "v37", "v38", "v39", "v40", "v41", "v42", "v43", "v44", "v45", "v46", "v47", "v48", "v49", "v50", "v51", "v52", "v53", "v54", "v55", "v56", "v57", "v58", "v59", "v60", "v61", "v62", "v63", "v64", "v65", "v66", "v67", "v68", "v69", "v70", "v71", "v72", "v73", "v74", "v75", "v76", "v77", "v78", "v79", "v80", "v81", "v82", "v83", "v84", "v85", "v86", "v87", "v88", "v89", "v90", "v91", "v92", "v93", "v94", "v95", "v96", "v97", "v98", "v99", "v100", "v101", "v102", "v103", "v104", "v105", "v106", "v107", "v108", "v109", "v110", "v111", "v112", "v113", "v114", "v115", "v116", "v117", "v118", "v119", "v120", "v121", "v122", "v123", "v124", "v125", "v126", "v127", "v128", "v129", "v130", "v131", "v132", "v133", "v134", "v135", "v136", "v137", "v138", "v139", "v140", "v141", "v142", "v143", "v144", "v145", "v146", "v147", "v148", "v149", "v150", "v151", "v152", "v153", "v154", "v155", "v156", "v157", "v158", "v159", "v160", "v161", "v162", "v163", "v164", "v165", "v166", "v167", "v168", "v169", "v170", "v171", "v172", "v173", "v174", "v175", "v176", "v177", "v178", "v179", "v180", "v181", "v182", "v183", "v184", "v185", "v186", "v187", "v188", "v189", "v190", "v191", "v192", "v193", "v194", "v195", "v196", "v197", "v198", "v199", "v200", "v201", "v202", "v203", "v204", "v205", "v206", "v207", "v209", "v210", "v211", "v212", "v213", "v214", "v215", "v216", "v217", "v218", "v219", "v220", "v221", "v222", "v223", "v224", "v225", "v226", "v227", "v228", "v229", "v230", "v231", "v232", "v233", "v234", "v235", "v236", "v237", "v238", "v239", "v240", "v241", "v242", "v243", "v244", "v245", "v246", "v247", "v248", "v249", "v250", "v251", "v252", "v253", "v254", "v255", "a0", "a1", "a2", "a3", "a4", "a5", "a6", "a7", "a8", "a9", "a10", "a11", "a12", "a13", "a14", "a15", "a16", "a17", "a18", "a19", "a20", "a21", "a22", "a23", "a24", "a25", "a26", "a27", "a28", "a29", "a30", "a31", "a32", "a33", "a34", "a35", "a36", "a37", "a38", "a39", "a40", "a41", "a42", "a43", "a44", "a45", "a46", "a47", "a48", "a49", "a50", "a51", "a52", "a53", "a54", "a55", "a56", "a57", "a58", "a59", "a60", "a61", "a62", "a63", "a64", "a65", "a66", "a67", "a68", "a69", "a70", "a71", "a72", "a73", "a74", "a75", "a76", "a77", "a78", "a79", "a80", "a81", "a82", "a83", "a84", "a85", "a86", "a87", "a88", "a89", "a90", "a91", "a92", "a93", "a94", "a95", "a96", "a97", "a98", "a99", "a100", "a101", "a102", "a103", "a104", "a105", "a106", "a107", "a108", "a109", "a110", "a111", "a112", "a113", "a114", "a115", "a116", "a117", "a118", "a119", "a120", "a121", "a122", "a123", "a124", "a125", "a126", "a127", "a128", "a129", "a130", "a131", "a132", "a133", "a134", "a135", "a136", "a137", "a138", "a139", "a140", "a141", "a142", "a143", "a144", "a145", "a146", "a147", "a148", "a149", "a150", "a151", "a152", "a153", "a154", "a155", "a156", "a157", "a158", "a159", "a160", "a161", "a162", "a163", "a164", "a165", "a166", "a167", "a168", "a169", "a170", "a171", "a172", "a173", "a174", "a175", "a176", "a177", "a178", "a179", "a180", "a181", "a182", "a183", "a184", "a185", "a186", "a187", "a188", "a189", "a190", "a191", "a192", "a193", "a194", "a195", "a196", "a197", "a198", "a199", "a200", "a201", "a202", "a203", "a204", "a205", "a206", "a207", "a208", "a209", "a210", "a211", "a212", "a213", "a214", "a215", "a216", "a217", "a218", "a219", "a220", "a221", "a222", "a223", "a224", "a225", "a226", "a227", "a228", "a229", "a230", "a231", "a232", "a233", "a234", "a235", "a236", "a237", "a238", "a239", "a240", "a241", "a242", "a243", "a244", "a245", "a246", "a247", "a248", "a249", "a250", "a251", "a252", "a253", "a254", "a255", "s36", "s37", "s38", "s39", "s40", "s41", "s42", "s43", "s44", "s45", "s46", "s47", "s48", "s49", "s50", "s51", "s52", "s53", "s54", "s55", "s56", "s57", "s58", "s59", "s60", "s61", "s62", "s63", "s64", "s67", "s68", "s69", "s70", "s71", "s72", "s73", "s74", "s75", "s76", "s77", "s78", "s79", "s80", "s81", "s82", "s83", "s84", "s85", "s86", "s87", "s88", "s89", "s90", "s91", "s92", "s93", "s94", "s95");
  if (flg != 2u) {
    const int l2 = __builtin_amdgcn_mbcnt_hi(~0u, __builtin_amdgcn_mbcnt_lo(~0u, 0u));
    slow_path(x, dwl, dws, w1, w2, out, (float*)smem, w * 64 + l2, blockIdx.x >> 3, (blockIdx.x & 7) * 4);
  }
}

extern "C" void kernel_launch(void* const* d_in, const int* in_sizes, int n_in, void* d_out, int out_size, void* d_ws,
                              size_t ws_size, hipStream_t stream) {
  replk_kernel<<<256, 256, 0, stream>>>((const u16*)d_in[0], (const u16*)d_in[1], (const u16*)d_in[2], (const u16*)d_in[3],
                                        (const u16*)d_in[4], (u16*)d_out
                                        );
}
```
